```python
import torch

def _softmax(x, axis=-1):
    return torch.softmax(x, dim=axis)
def _gelu(x):
    return torch.nn.functional.gelu(x, approximate="tanh")
def _silu(x):
    return torch.nn.functional.silu(x)
def _sigmoid(x):
    return torch.sigmoid(x)
def _layernorm(x, axis=-1, eps=1e-5):
    m = x.mean(dim=axis, keepdim=True)
    v = x.var(dim=axis, keepdim=True, unbiased=False)
    return (x - m) / torch.sqrt(v + eps)
def _scatter_add(tgt, idx, src, axis=0):
    return tgt.clone().index_add_(axis, idx, src)
def _segment_sum(src, seg, n):
    out = torch.zeros((n,) + src.shape[1:], dtype=src.dtype)
    return out.index_add_(0, seg, src)
def _pad(x, pw):
    flat = tuple(v for pair in reversed(pw) for v in pair)
    return torch.nn.functional.pad(x, flat)

kernel_name = 'interleave_layers__sliding_local_dilated__glu_gated_geglu__prompt'

def setup_inputs():

    _w = (lambda a: (lambda t: t.to(torch.bfloat16) if t.is_floating_point() else t)(torch.as_tensor(a)))
    def _A_setup():
        B, S, D = 1, 2048, 576
        return {
            "x": _w((lambda *s: torch.randn(*s, dtype=torch.bfloat16) * 0.05)(B, S, D)),
            "wqkv": _w((lambda *s: torch.randn(*s, dtype=torch.bfloat16) * 0.05)(D, 3 * D)),
            "wo": _w((lambda *s: torch.randn(*s, dtype=torch.bfloat16) * 0.05)(D, D)),
        }
    _A_in = _A_setup()

    def _B_setup():
        B, S, Dm, Df = 1, 2048, 576, 4608
        return {
            "x": _w((lambda *s: torch.randn(*s, dtype=torch.bfloat16) * 0.05)(B, S, Dm)),
            "w_gate": _w((lambda *s: torch.randn(*s, dtype=torch.bfloat16) * 0.8)(Dm, Df) * (Dm ** -0.5)),
            "w_up": _w((lambda *s: torch.randn(*s, dtype=torch.bfloat16) * 0.8)(Dm, Df) * (Dm ** -0.5)),
            "w_down": _w((lambda *s: torch.randn(*s, dtype=torch.bfloat16) * 0.8)(Df, Dm) * (Df ** -0.5)),
        }
    _B_in = _B_setup()

    return {
        "A_x": _A_in["x"],
        "A_wqkv": _A_in["wqkv"],
        "A_wo": _A_in["wo"],
        "B_x": _B_in["x"],
        "B_w_gate": _B_in["w_gate"],
        "B_w_up": _B_in["w_up"],
        "B_w_down": _B_in["w_down"],
    }

def reference(A_x, A_wqkv, A_wo, B_x, B_w_gate, B_w_up, B_w_down):
    def _A_ref(x, wqkv, wo):
        B, S, D = x.shape
        H = 32; Dh = D // H
        W, dil = 8, 2
        qkv = (lambda x, *d: x.permute(*d))((x @ wqkv).reshape(B, S, 3, H, Dh), 2, 0, 3, 1, 4)
        q, k, v = qkv[0], qkv[1], qkv[2]
        scale = Dh ** -0.5
        pos = torch.arange(S)
        off = torch.arange(W) * dil
        idx = pos[:, None] - off[None, :]
        mask = idx >= 0
        idx_c = torch.where(mask, idx, 0).reshape(-1)
        kg = k[:, :, idx_c, :].reshape(B, H, S, W, Dh)
        vg = v[:, :, idx_c, :].reshape(B, H, S, W, Dh)
        logits = torch.einsum("bhsd,bhswd->bhsw", q, kg) * scale
        logits = torch.where(mask[None, None, :, :], logits, -1e9)
        attn = _softmax(logits, axis=-1)
        o = torch.einsum("bhsw,bhswd->bhsd", attn, vg)
        return (lambda x, *d: x.permute(*d))(o, 0, 2, 1, 3).reshape(B, S, D) @ wo

    def _B_ref(x, w_gate, w_up, w_down):
        g = torch.einsum("bsd,df->bsf", x, w_gate)
        u = torch.einsum("bsd,df->bsf", x, w_up)
        h = _gelu(g) * u
        return torch.einsum("bsf,fd->bsd", h, w_down)

    x = A_x
    x = x + _A_ref(_layernorm(x, axis=-1), A_wqkv, A_wo)
    x = x + _B_ref(_layernorm(x, axis=-1), B_w_gate, B_w_up, B_w_down)
    x = x + _A_ref(_layernorm(x, axis=-1), A_wqkv, A_wo)
    x = x + _B_ref(_layernorm(x, axis=-1), B_w_gate, B_w_up, B_w_down)
    return x
```

```cpp
#include <hip/hip_runtime.h>
#include <stdint.h>

typedef unsigned short u16;
typedef __attribute__((ext_vector_type(4))) unsigned u32x4;
constexpr unsigned WS_DBG = 65536u;
constexpr unsigned WS_XIN = 1310720u;
constexpr unsigned WS_OB = 9175040u;
constexpr unsigned WS_X1 = 17039360u;
constexpr unsigned WS_H = 26034176u;
constexpr unsigned WS_PS = 78589952u;
constexpr unsigned WS_END = 109740032u;
constexpr unsigned QMAP = 2u;
constexpr unsigned UMAP = 0u;
constexpr unsigned DSH = 4u;
constexpr unsigned PW = 1664u;
constexpr unsigned REC = 24576u;
constexpr unsigned HREC = 165888u;
constexpr unsigned PREC = 99840u;
constexpr unsigned NREC = 80u;
constexpr unsigned OOB = 0x40000000u, NEG = 0xff61b1e6u;
constexpr int LDS_BYTES = 160 * 1024;

__device__ __forceinline__ u32x4 srd(const void* p, unsigned bytes) {
  const unsigned long long a = (unsigned long long)p;
  return u32x4{(unsigned)a, (unsigned)(a >> 32) & 0xffffu, bytes, 0x00020000u};
}

__device__ __forceinline__ unsigned unit_ks(unsigned t) { return UMAP == 0 ? t >> 2 : UMAP == 1 ? t % 12 >> 1 : t % 6; }
__device__ __forceinline__ unsigned unit_gr(unsigned t) { return UMAP == 0 ? t & 3 : UMAP == 1 ? t / 12 * 2 + (t & 1) : t / 6; }

extern "C" __global__ __launch_bounds__(256, 1) __attribute__((amdgpu_waves_per_eu(1, 1))) void k_all(
    const u16* X, const u16* Wqkv, const u16* Wo, const u16* Wg, const u16* Wu, const u16* Wd, char* ws, u16* out) {
  const unsigned b = blockIdx.x, xcd = b & 7, slot = b >> 3;
  const unsigned w = __builtin_amdgcn_readfirstlane(threadIdx.x >> 6), lane = threadIdx.x & 63;
  const unsigned i16 = lane & 15, g = lane >> 4, q = i16 >> 2, p = lane & 3;
  const u32x4 rsWS = srd(ws, WS_END);
  const unsigned v_l16 = lane * 16, v_dbg = WS_DBG + (b * 4 + w) * 1024 + lane * 4;
  const unsigned yb = slot >= 14 || slot < 5 ? 61440 : 149760, v_yw = yb + w * 4, v_yr = yb + p * 4;
  const unsigned half = (i16 + 16 * (g >> 1)) * 16 + (g & 1) * 8;
  const unsigned line = lane < 32 ? lane * 4 : OOB, v_al = w ? OOB : line + 128 * (1 + b);
  const unsigned l8 = lane >> 3, l7 = (lane & 7) * 16;
  (void)v_dbg;
  if (slot < 4) {
    const unsigned gr = QMAP == 2 ? 3 - (xcd & 3) : QMAP ? xcd >> 1 : slot, hg = QMAP == 2 ? (xcd >> 2) * 4 + slot : QMAP ? (xcd & 1) * 4 + slot : xcd, h = 4 * hg + w, par = i16 & 1;
    const u32x4 rsA = srd(Wqkv, 576 * 1728 * 2);
    const unsigned v_l5 = w < 3 || lane < 32 ? v_l16 + 16384 - w * 3072 : OOB;
    const unsigned v_fa = 20480 - w * 4096 + (lane < 60 ? lane / 5 * 128 + lane % 5 * 4 : 0), v_ra = (lane >> 5) * 8 * REC + (lane & 31) * 4 + 20480 + h * 128, v_hi = lane < 32 ? 0u : ~0u;
    const unsigned v_st = 18432 + i16 * 32 + q * 8, v_tw = 61568 + w * 128 + i16 * 8, v_tr = 61568 + w * 128 + g * 32;
    const unsigned v_om = h * 512 + half, v_oe = g ? OOB : 16384 + h * 128 + i16 * 8;
    const unsigned v_pw = 20480 + h * 128 + l8 * REC + l7;
    unsigned v_vo[4], v_bc[4], v_bp[4], v_mk[4], v_sel[4] = {0, 0, 0, 0}, v_ci[4];
#pragma unroll
    for (unsigned i = 0; i < 4; ++i) {
      const unsigned c = 64 * (4 * w + i) + lane, cc = c % 27, kk = 4 * g + i, ka = 4 * g + 2 * (i & 1) + par;
      v_vo[i] = c < 864 ? c / 27 * 3456 + cc / 9 * 1152 + 144 * hg + cc % 9 * 16 - 1024 * i : OOB;
      v_bc[i] = (i & 1) == par && kk <= i16 ? 0u : NEG;
      v_bp[i] = (i & 1) == par && kk >= i16 + 2 ? 0u : NEG;
      v_mk[i] = (i < 2 ? ka > i16 : ka <= i16) ? 0xffffu << (16 * par) : 0u;
      v_ci[i] = p == 2 ? 0x3f800000u : 0u;
    }
    v_sel[3] = lane == 16 ? 0x3f80u : lane == 17 ? 0x3f800000u : 0u;
    const unsigned v_at = 65536 + (8 * g + q) * 432 + 36 * w + 4 * (w & 1) + 8 * p;
    const unsigned v_ax = 65536 + (8 * g + q) * 432 + (p < 2 ? p : 2) * 144 + 72 * (w >> 1) + 32;
    const unsigned v_pm = (16 * g + 4 * (i16 >> 1 & 3) + 2 * (w & 1) + par) * 4, v_ms = i16 < 6 ? ~0u : 0u;
    const unsigned s_sx = WS_XIN + gr * NREC * REC + w * 4096, s_ev = s_sx + 32 * REC, s_del = 0u - (gr ? NREC + 32 : 32) * REC;
    const unsigned s_kil = gr ? 0x7f7fffffu : 0xff7fffffu, s_so = WS_OB + gr * NREC * REC;
    const unsigned s_m4 = w * 4096, s_m5 = 16384 + w * 1024, s_ms = w * 4096, s_up = b + w;
    asm volatile(
      "s_buffer_load_dword s21, s[8:11], 0\n"
      "s_mov_b32 s23, 65536\n"
      "s_mov_b32 s31, 0x40000000\n"
      "s_mov_b32 s54, 0\n"
      "s_add_u32 m0, s43, 65536\n"
      "s_nop 0\n"
      "buffer_load_dwordx4 v52, s[12:15], s54 offen offset:0 lds\n"
      "buffer_load_dwordx4 v53, s[12:15], s54 offen offset:1024 lds\n"
      "buffer_load_dwordx4 v54, s[12:15], s54 offen offset:2048 lds\n"
      "buffer_load_dwordx4 v55, s[12:15], s54 offen offset:3072 lds\n"
      "s_add_u32 s54, s54, 110592\n"
      "s_add_u32 m0, s43, 81920\n"
      "s_nop 0\n"
      "buffer_load_dwordx4 v52, s[12:15], s54 offen offset:0 lds\n"
      "buffer_load_dwordx4 v53, s[12:15], s54 offen offset:1024 lds\n"
      "buffer_load_dwordx4 v54, s[12:15], s54 offen offset:2048 lds\n"
      "buffer_load_dwordx4 v55, s[12:15], s54 offen offset:3072 lds\n"
      "s_add_u32 s54, s54, 110592\n"
      "s_add_u32 m0, s43, 98304\n"
      "s_nop 0\n"
      "buffer_load_dwordx4 v52, s[12:15], s54 offen offset:0 lds\n"
      "buffer_load_dwordx4 v53, s[12:15], s54 offen offset:1024 lds\n"
      "buffer_load_dwordx4 v54, s[12:15], s54 offen offset:2048 lds\n"
      "buffer_load_dwordx4 v55, s[12:15], s54 offen offset:3072 lds\n"
      "s_add_u32 s54, s54, 110592\n"
      "s_add_u32 m0, s43, 114688\n"
      "s_nop 0\n"
      "buffer_load_dwordx4 v52, s[12:15], s54 offen offset:0 lds\n"
      "buffer_load_dwordx4 v53, s[12:15], s54 offen offset:1024 lds\n"
      "buffer_load_dwordx4 v54, s[12:15], s54 offen offset:2048 lds\n"
      "buffer_load_dwordx4 v55, s[12:15], s54 offen offset:3072 lds\n"
      "s_add_u32 s54, s54, 110592\n"
      "s_add_u32 m0, s43, 131072\n"
      "s_nop 0\n"
      "buffer_load_dwordx4 v52, s[12:15], s54 offen offset:0 lds\n"
      "buffer_load_dwordx4 v53, s[12:15], s54 offen offset:1024 lds\n"
      "buffer_load_dwordx4 v54, s[12:15], s54 offen offset:2048 lds\n"
      "buffer_load_dwordx4 v55, s[12:15], s54 offen offset:3072 lds\n"
      "s_add_u32 s54, s54, 110592\n"
      "s_add_u32 s36, s36, 4294942720\n"
      "s_add_u32 s48, s37, 811008\n"
      "s_mov_b32 s53, 40960\n"
      "s_mov_b32 s56, 0x7f7fffff\n"
      "v_mov_b32 v183, 0\n"
      "s_mov_b32 s58, 0xffff\n"
      "s_mov_b32 s59, 0\n"
      "v_mov_b32 v9, 0\n"
      "ds_write_b32 v11, v9\n"
      "v_mov_b32 v60, 0x3f803f80\n"
      "v_mov_b32 v61, 0x3f803f80\n"
      "v_mov_b32 v62, 0x3f803f80\n"
      "v_mov_b32 v63, 0x3f803f80\n"
      "s_waitcnt vmcnt(16) lgkmcnt(0)\n"
      "s_barrier\n"
      "ds_read_b64_tr_b16 v[64:65], v57 offset:0\n"
      "ds_read_b64_tr_b16 v[66:67], v57 offset:1728\n"
      "ds_read_b64_tr_b16 a[0:1], v56 offset:144\n"
      "ds_read_b64_tr_b16 a[2:3], v56 offset:1872\n"
      "ds_read_b64_tr_b16 a[72:73], v56 offset:288\n"
      "ds_read_b64_tr_b16 a[74:75], v56 offset:2016\n"
      "ds_read_b64_tr_b16 a[144:145], v56 offset:0\n"
      "ds_read_b64_tr_b16 a[146:147], v56 offset:1728\n"
      "v_add_u32 v56, 16384, v56\n"
      "v_add_u32 v57, 16384, v57\n"
      "s_add_u32 m0, s43, 147456\n"
      "s_nop 0\n"
      "buffer_load_dwordx4 v52, s[12:15], s54 offen offset:0 lds\n"
      "buffer_load_dwordx4 v53, s[12:15], s54 offen offset:1024 lds\n"
      "buffer_load_dwordx4 v54, s[12:15], s54 offen offset:2048 lds\n"
      "buffer_load_dwordx4 v55, s[12:15], s54 offen offset:3072 lds\n"
      "s_add_u32 s54, s54, 110592\n"
      "s_waitcnt vmcnt(16) lgkmcnt(0)\n"
      "s_barrier\n"
      "ds_read_b64_tr_b16 v[72:73], v57 offset:0\n"
      "ds_read_b64_tr_b16 v[74:75], v57 offset:1728\n"
      "ds_read_b64_tr_b16 a[4:5], v56 offset:144\n"
      "ds_read_b64_tr_b16 a[6:7], v56 offset:1872\n"
      "ds_read_b64_tr_b16 a[76:77], v56 offset:288\n"
      "ds_read_b64_tr_b16 a[78:79], v56 offset:2016\n"
      "ds_read_b64_tr_b16 a[148:149], v56 offset:0\n"
      "ds_read_b64_tr_b16 a[150:151], v56 offset:1728\n"
      "v_add_u32 v56, 16384, v56\n"
      "v_add_u32 v57, 16384, v57\n"
      "ds_bpermute_b32 v68, v58, v64\n"
      "ds_bpermute_b32 v69, v58, v65\n"
      "ds_bpermute_b32 v70, v58, v66\n"
      "ds_bpermute_b32 v71, v58, v67\n"
      "v_mfma_f32_16x16x32_bf16 v[140:143], a[0:3], v[60:63], 0\n"
      "v_mfma_f32_16x16x32_bf16 v[148:151], v[60:63], a[72:75], 0\n"
      "v_mfma_f32_16x16x32_bf16 v[152:155], a[144:147], v[60:63], 0\n"
      "s_add_u32 m0, s43, 65536\n"
      "s_nop 0\n"
      "buffer_load_dwordx4 v52, s[12:15], s54 offen offset:0 lds\n"
      "buffer_load_dwordx4 v53, s[12:15], s54 offen offset:1024 lds\n"
      "buffer_load_dwordx4 v54, s[12:15], s54 offen offset:2048 lds\n"
      "buffer_load_dwordx4 v55, s[12:15], s54 offen offset:3072 lds\n"
      "s_add_u32 s54, s54, 110592\n"
      "s_waitcnt vmcnt(16) lgkmcnt(0)\n"
      "s_barrier\n"
      "ds_read_b64_tr_b16 v[64:65], v57 offset:0\n"
      "ds_read_b64_tr_b16 v[66:67], v57 offset:1728\n"
      "ds_read_b64_tr_b16 a[8:9], v56 offset:144\n"
      "ds_read_b64_tr_b16 a[10:11], v56 offset:1872\n"
      "ds_read_b64_tr_b16 a[80:81], v56 offset:288\n"
      "ds_read_b64_tr_b16 a[82:83], v56 offset:2016\n"
      "ds_read_b64_tr_b16 a[152:153], v56 offset:0\n"
      "ds_read_b64_tr_b16 a[154:155], v56 offset:1728\n"
      "v_add_u32 v56, 16384, v56\n"
      "v_add_u32 v57, 16384, v57\n"
      "ds_bpermute_b32 v76, v58, v72\n"
      "ds_bpermute_b32 v77, v58, v73\n"
      "ds_bpermute_b32 v78, v58, v74\n"
      "ds_bpermute_b32 v79, v58, v75\n"
      "v_mfma_f32_16x16x32_bf16 v[140:143], a[4:7], v[60:63], v[140:143]\n"
      "v_mfma_f32_16x16x32_bf16 v[148:151], v[60:63], a[76:79], v[148:151]\n"
      "v_mfma_f32_16x16x32_bf16 v[152:155], a[148:151], v[60:63], v[152:155]\n"
      "s_add_u32 m0, s43, 81920\n"
      "s_nop 0\n"
      "buffer_load_dwordx4 v52, s[12:15], s54 offen offset:0 lds\n"
      "buffer_load_dwordx4 v53, s[12:15], s54 offen offset:1024 lds\n"
      "buffer_load_dwordx4 v54, s[12:15], s54 offen offset:2048 lds\n"
      "buffer_load_dwordx4 v55, s[12:15], s54 offen offset:3072 lds\n"
      "s_add_u32 s54, s54, 110592\n"
      "v_and_b32 v68, v59, v68\n"
      "v_and_b32 v69, v59, v69\n"
      "v_and_b32 v70, v59, v70\n"
      "v_and_b32 v71, v59, v71\n"
      "v_accvgpr_write_b32 a216, v68\n"
      "v_accvgpr_write_b32 a217, v69\n"
      "v_accvgpr_write_b32 a218, v70\n"
      "v_accvgpr_write_b32 a219, v71\n"
      "v_mfma_f32_16x16x32_bf16 v[144:147], v[68:71], v[60:63], 0\n"
      "s_waitcnt vmcnt(16) lgkmcnt(0)\n"
      "s_barrier\n"
      "ds_read_b64_tr_b16 v[72:73], v57 offset:0\n"
      "ds_read_b64_tr_b16 v[74:75], v57 offset:1728\n"
      "ds_read_b64_tr_b16 a[12:13], v56 offset:144\n"
      "ds_read_b64_tr_b16 a[14:15], v56 offset:1872\n"
      "ds_read_b64_tr_b16 a[84:85], v56 offset:288\n"
      "ds_read_b64_tr_b16 a[86:87], v56 offset:2016\n"
      "ds_read_b64_tr_b16 a[156:157], v56 offset:0\n"
      "ds_read_b64_tr_b16 a[158:159], v56 offset:1728\n"
      "v_add_u32 v56, 16384, v56\n"
      "v_add_u32 v57, 16384, v57\n"
      "ds_bpermute_b32 v68, v58, v64\n"
      "ds_bpermute_b32 v69, v58, v65\n"
      "ds_bpermute_b32 v70, v58, v66\n"
      "ds_bpermute_b32 v71, v58, v67\n"
      "v_mfma_f32_16x16x32_bf16 v[140:143], a[8:11], v[60:63], v[140:143]\n"
      "v_mfma_f32_16x16x32_bf16 v[148:151], v[60:63], a[80:83], v[148:151]\n"
      "v_mfma_f32_16x16x32_bf16 v[152:155], a[152:155], v[60:63], v[152:155]\n"
      "s_add_u32 m0, s43, 98304\n"
      "s_nop 0\n"
      "buffer_load_dwordx4 v52, s[12:15], s54 offen offset:0 lds\n"
      "buffer_load_dwordx4 v53, s[12:15], s54 offen offset:1024 lds\n"
      "buffer_load_dwordx4 v54, s[12:15], s54 offen offset:2048 lds\n"
      "buffer_load_dwordx4 v55, s[12:15], s54 offen offset:3072 lds\n"
      "s_add_u32 s54, s54, 110592\n"
      "v_and_b32 v76, v59, v76\n"
      "v_and_b32 v77, v59, v77\n"
      "v_and_b32 v78, v59, v78\n"
      "v_and_b32 v79, v59, v79\n"
      "v_accvgpr_write_b32 a220, v76\n"
      "v_accvgpr_write_b32 a221, v77\n"
      "v_accvgpr_write_b32 a222, v78\n"
      "v_accvgpr_write_b32 a223, v79\n"
      "v_mfma_f32_16x16x32_bf16 v[144:147], v[76:79], v[60:63], v[144:147]\n"
      "s_waitcnt vmcnt(16) lgkmcnt(0)\n"
      "s_barrier\n"
      "ds_read_b64_tr_b16 v[64:65], v57 offset:0\n"
      "ds_read_b64_tr_b16 v[66:67], v57 offset:1728\n"
      "ds_read_b64_tr_b16 a[16:17], v56 offset:144\n"
      "ds_read_b64_tr_b16 a[18:19], v56 offset:1872\n"
      "ds_read_b64_tr_b16 a[88:89], v56 offset:288\n"
      "ds_read_b64_tr_b16 a[90:91], v56 offset:2016\n"
      "ds_read_b64_tr_b16 a[160:161], v56 offset:0\n"
      "ds_read_b64_tr_b16 a[162:163], v56 offset:1728\n"
      "v_add_u32 v56, 16384, v56\n"
      "v_add_u32 v57, 16384, v57\n"
      "ds_bpermute_b32 v76, v58, v72\n"
      "ds_bpermute_b32 v77, v58, v73\n"
      "ds_bpermute_b32 v78, v58, v74\n"
      "ds_bpermute_b32 v79, v58, v75\n"
      "v_mfma_f32_16x16x32_bf16 v[140:143], a[12:15], v[60:63], v[140:143]\n"
      "v_mfma_f32_16x16x32_bf16 v[148:151], v[60:63], a[84:87], v[148:151]\n"
      "v_mfma_f32_16x16x32_bf16 v[152:155], a[156:159], v[60:63], v[152:155]\n"
      "s_add_u32 m0, s43, 114688\n"
      "s_nop 0\n"
      "buffer_load_dwordx4 v52, s[12:15], s54 offen offset:0 lds\n"
      "buffer_load_dwordx4 v53, s[12:15], s54 offen offset:1024 lds\n"
      "buffer_load_dwordx4 v54, s[12:15], s54 offen offset:2048 lds\n"
      "buffer_load_dwordx4 v55, s[12:15], s54 offen offset:3072 lds\n"
      "s_add_u32 s54, s54, 110592\n"
      "v_and_b32 v68, v59, v68\n"
      "v_and_b32 v69, v59, v69\n"
      "v_and_b32 v70, v59, v70\n"
      "v_and_b32 v71, v59, v71\n"
      "v_accvgpr_write_b32 a224, v68\n"
      "v_accvgpr_write_b32 a225, v69\n"
      "v_accvgpr_write_b32 a226, v70\n"
      "v_accvgpr_write_b32 a227, v71\n"
      "v_mfma_f32_16x16x32_bf16 v[144:147], v[68:71], v[60:63], v[144:147]\n"
      "s_waitcnt vmcnt(16) lgkmcnt(0)\n"
      "s_barrier\n"
      "ds_read_b64_tr_b16 v[72:73], v57 offset:0\n"
      "ds_read_b64_tr_b16 v[74:75], v57 offset:1728\n"
      "ds_read_b64_tr_b16 a[20:21], v56 offset:144\n"
      "ds_read_b64_tr_b16 a[22:23], v56 offset:1872\n"
      "ds_read_b64_tr_b16 a[92:93], v56 offset:288\n"
      "ds_read_b64_tr_b16 a[94:95], v56 offset:2016\n"
      "ds_read_b64_tr_b16 a[164:165], v56 offset:0\n"
      "ds_read_b64_tr_b16 a[166:167], v56 offset:1728\n"
      "v_add_u32 v56, 4294885376, v56\n"
      "v_add_u32 v57, 4294885376, v57\n"
      "ds_bpermute_b32 v68, v58, v64\n"
      "ds_bpermute_b32 v69, v58, v65\n"
      "ds_bpermute_b32 v70, v58, v66\n"
      "ds_bpermute_b32 v71, v58, v67\n"
      "v_mfma_f32_16x16x32_bf16 v[140:143], a[16:19], v[60:63], v[140:143]\n"
      "v_mfma_f32_16x16x32_bf16 v[148:151], v[60:63], a[88:91], v[148:151]\n"
      "v_mfma_f32_16x16x32_bf16 v[152:155], a[160:163], v[60:63], v[152:155]\n"
      "s_add_u32 m0, s43, 131072\n"
      "s_nop 0\n"
      "buffer_load_dwordx4 v52, s[12:15], s54 offen offset:0 lds\n"
      "buffer_load_dwordx4 v53, s[12:15], s54 offen offset:1024 lds\n"
      "buffer_load_dwordx4 v54, s[12:15], s54 offen offset:2048 lds\n"
      "buffer_load_dwordx4 v55, s[12:15], s54 offen offset:3072 lds\n"
      "s_add_u32 s54, s54, 110592\n"
      "v_and_b32 v76, v59, v76\n"
      "v_and_b32 v77, v59, v77\n"
      "v_and_b32 v78, v59, v78\n"
      "v_and_b32 v79, v59, v79\n"
      "v_accvgpr_write_b32 a228, v76\n"
      "v_accvgpr_write_b32 a229, v77\n"
      "v_accvgpr_write_b32 a230, v78\n"
      "v_accvgpr_write_b32 a231, v79\n"
      "v_mfma_f32_16x16x32_bf16 v[144:147], v[76:79], v[60:63], v[144:147]\n"
      "s_waitcnt vmcnt(16) lgkmcnt(0)\n"
      "s_barrier\n"
      "ds_read_b64_tr_b16 v[64:65], v57 offset:0\n"
      "ds_read_b64_tr_b16 v[66:67], v57 offset:1728\n"
      "ds_read_b64_tr_b16 a[24:25], v56 offset:144\n"
      "ds_read_b64_tr_b16 a[26:27], v56 offset:1872\n"
      "ds_read_b64_tr_b16 a[96:97], v56 offset:288\n"
      "ds_read_b64_tr_b16 a[98:99], v56 offset:2016\n"
      "ds_read_b64_tr_b16 a[168:169], v56 offset:0\n"
      "ds_read_b64_tr_b16 a[170:171], v56 offset:1728\n"
      "v_add_u32 v56, 16384, v56\n"
      "v_add_u32 v57, 16384, v57\n"
      "ds_bpermute_b32 v76, v58, v72\n"
      "ds_bpermute_b32 v77, v58, v73\n"
      "ds_bpermute_b32 v78, v58, v74\n"
      "ds_bpermute_b32 v79, v58, v75\n"
      "v_mfma_f32_16x16x32_bf16 v[140:143], a[20:23], v[60:63], v[140:143]\n"
      "v_mfma_f32_16x16x32_bf16 v[148:151], v[60:63], a[92:95], v[148:151]\n"
      "v_mfma_f32_16x16x32_bf16 v[152:155], a[164:167], v[60:63], v[152:155]\n"
      "s_add_u32 m0, s43, 147456\n"
      "s_nop 0\n"
      "buffer_load_dwordx4 v52, s[12:15], s54 offen offset:0 lds\n"
      "buffer_load_dwordx4 v53, s[12:15], s54 offen offset:1024 lds\n"
      "buffer_load_dwordx4 v54, s[12:15], s54 offen offset:2048 lds\n"
      "buffer_load_dwordx4 v55, s[12:15], s54 offen offset:3072 lds\n"
      "s_add_u32 s54, s54, 110592\n"
      "v_and_b32 v68, v59, v68\n"
      "v_and_b32 v69, v59, v69\n"
      "v_and_b32 v70, v59, v70\n"
      "v_and_b32 v71, v59, v71\n"
      "v_accvgpr_write_b32 a232, v68\n"
      "v_accvgpr_write_b32 a233, v69\n"
      "v_accvgpr_write_b32 a234, v70\n"
      "v_accvgpr_write_b32 a235, v71\n"
      "v_mfma_f32_16x16x32_bf16 v[144:147], v[68:71], v[60:63], v[144:147]\n"
      "s_waitcnt vmcnt(16) lgkmcnt(0)\n"
      "s_barrier\n"
      "ds_read_b64_tr_b16 v[72:73], v57 offset:0\n"
      "ds_read_b64_tr_b16 v[74:75], v57 offset:1728\n"
      "ds_read_b64_tr_b16 a[28:29], v56 offset:144\n"
      "ds_read_b64_tr_b16 a[30:31], v56 offset:1872\n"
      "ds_read_b64_tr_b16 a[100:101], v56 offset:288\n"
      "ds_read_b64_tr_b16 a[102:103], v56 offset:2016\n"
      "ds_read_b64_tr_b16 a[172:173], v56 offset:0\n"
      "ds_read_b64_tr_b16 a[174:175], v56 offset:1728\n"
      "v_add_u32 v56, 16384, v56\n"
      "v_add_u32 v57, 16384, v57\n"
      "ds_bpermute_b32 v68, v58, v64\n"
      "ds_bpermute_b32 v69, v58, v65\n"
      "ds_bpermute_b32 v70, v58, v66\n"
      "ds_bpermute_b32 v71, v58, v67\n"
      "v_mfma_f32_16x16x32_bf16 v[140:143], a[24:27], v[60:63], v[140:143]\n"
      "v_mfma_f32_16x16x32_bf16 v[148:151], v[60:63], a[96:99], v[148:151]\n"
      "v_mfma_f32_16x16x32_bf16 v[152:155], a[168:171], v[60:63], v[152:155]\n"
      "s_add_u32 m0, s43, 65536\n"
      "s_nop 0\n"
      "buffer_load_dwordx4 v52, s[12:15], s54 offen offset:0 lds\n"
      "buffer_load_dwordx4 v53, s[12:15], s54 offen offset:1024 lds\n"
      "buffer_load_dwordx4 v54, s[12:15], s54 offen offset:2048 lds\n"
      "buffer_load_dwordx4 v55, s[12:15], s54 offen offset:3072 lds\n"
      "s_add_u32 s54, s54, 110592\n"
      "v_and_b32 v76, v59, v76\n"
      "v_and_b32 v77, v59, v77\n"
      "v_and_b32 v78, v59, v78\n"
      "v_and_b32 v79, v59, v79\n"
      "v_accvgpr_write_b32 a236, v76\n"
      "v_accvgpr_write_b32 a237, v77\n"
      "v_accvgpr_write_b32 a238, v78\n"
      "v_accvgpr_write_b32 a239, v79\n"
      "v_mfma_f32_16x16x32_bf16 v[144:147], v[76:79], v[60:63], v[144:147]\n"
      "s_add_u32 s30, s36, 24576\n"
      "buffer_load_dword v82, v2, s[8:11], s30 offen sc1\n"
      "s_add_u32 s30, s36, 49152\n"
      "buffer_load_dword v83, v2, s[8:11], s30 offen sc1\n"
      "s_waitcnt vmcnt(18) lgkmcnt(0)\n"
      "s_barrier\n"
      "ds_read_b64_tr_b16 v[64:65], v57 offset:0\n"
      "ds_read_b64_tr_b16 v[66:67], v57 offset:1728\n"
      "ds_read_b64_tr_b16 a[32:33], v56 offset:144\n"
      "ds_read_b64_tr_b16 a[34:35], v56 offset:1872\n"
      "ds_read_b64_tr_b16 a[104:105], v56 offset:288\n"
      "ds_read_b64_tr_b16 a[106:107], v56 offset:2016\n"
      "ds_read_b64_tr_b16 a[176:177], v56 offset:0\n"
      "ds_read_b64_tr_b16 a[178:179], v56 offset:1728\n"
      "v_add_u32 v56, 16384, v56\n"
      "v_add_u32 v57, 16384, v57\n"
      "ds_bpermute_b32 v76, v58, v72\n"
      "ds_bpermute_b32 v77, v58, v73\n"
      "ds_bpermute_b32 v78, v58, v74\n"
      "ds_bpermute_b32 v79, v58, v75\n"
      "v_mfma_f32_16x16x32_bf16 v[140:143], a[28:31], v[60:63], v[140:143]\n"
      "v_mfma_f32_16x16x32_bf16 v[148:151], v[60:63], a[100:103], v[148:151]\n"
      "v_mfma_f32_16x16x32_bf16 v[152:155], a[172:175], v[60:63], v[152:155]\n"
      "s_add_u32 m0, s43, 81920\n"
      "s_nop 0\n"
      "buffer_load_dwordx4 v52, s[12:15], s54 offen offset:0 lds\n"
      "buffer_load_dwordx4 v53, s[12:15], s54 offen offset:1024 lds\n"
      "buffer_load_dwordx4 v54, s[12:15], s54 offen offset:2048 lds\n"
      "buffer_load_dwordx4 v55, s[12:15], s54 offen offset:3072 lds\n"
      "s_add_u32 s54, s54, 110592\n"
      "v_and_b32 v68, v59, v68\n"
      "v_and_b32 v69, v59, v69\n"
      "v_and_b32 v70, v59, v70\n"
      "v_and_b32 v71, v59, v71\n"
      "v_accvgpr_write_b32 a240, v68\n"
      "v_accvgpr_write_b32 a241, v69\n"
      "v_accvgpr_write_b32 a242, v70\n"
      "v_accvgpr_write_b32 a243, v71\n"
      "v_mfma_f32_16x16x32_bf16 v[144:147], v[68:71], v[60:63], v[144:147]\n"
      "s_waitcnt vmcnt(18) lgkmcnt(0)\n"
      "s_barrier\n"
      "ds_read_b64_tr_b16 v[72:73], v57 offset:0\n"
      "ds_read_b64_tr_b16 v[74:75], v57 offset:1728\n"
      "ds_read_b64_tr_b16 a[36:37], v56 offset:144\n"
      "ds_read_b64_tr_b16 a[38:39], v56 offset:1872\n"
      "ds_read_b64_tr_b16 a[108:109], v56 offset:288\n"
      "ds_read_b64_tr_b16 a[110:111], v56 offset:2016\n"
      "ds_read_b64_tr_b16 a[180:181], v56 offset:0\n"
      "ds_read_b64_tr_b16 a[182:183], v56 offset:1728\n"
      "v_add_u32 v56, 16384, v56\n"
      "v_add_u32 v57, 16384, v57\n"
      "ds_bpermute_b32 v68, v58, v64\n"
      "ds_bpermute_b32 v69, v58, v65\n"
      "ds_bpermute_b32 v70, v58, v66\n"
      "ds_bpermute_b32 v71, v58, v67\n"
      "v_mfma_f32_16x16x32_bf16 v[140:143], a[32:35], v[60:63], v[140:143]\n"
      "v_mfma_f32_16x16x32_bf16 v[148:151], v[60:63], a[104:107], v[148:151]\n"
      "v_mfma_f32_16x16x32_bf16 v[152:155], a[176:179], v[60:63], v[152:155]\n"
      "s_add_u32 m0, s43, 98304\n"
      "s_nop 0\n"
      "buffer_load_dwordx4 v52, s[12:15], s54 offen offset:0 lds\n"
      "buffer_load_dwordx4 v53, s[12:15], s54 offen offset:1024 lds\n"
      "buffer_load_dwordx4 v54, s[12:15], s54 offen offset:2048 lds\n"
      "buffer_load_dwordx4 v55, s[12:15], s54 offen offset:3072 lds\n"
      "s_add_u32 s54, s54, 110592\n"
      "v_and_b32 v76, v59, v76\n"
      "v_and_b32 v77, v59, v77\n"
      "v_and_b32 v78, v59, v78\n"
      "v_and_b32 v79, v59, v79\n"
      "v_accvgpr_write_b32 a244, v76\n"
      "v_accvgpr_write_b32 a245, v77\n"
      "v_accvgpr_write_b32 a246, v78\n"
      "v_accvgpr_write_b32 a247, v79\n"
      "v_mfma_f32_16x16x32_bf16 v[144:147], v[76:79], v[60:63], v[144:147]\n"
      "s_waitcnt vmcnt(18) lgkmcnt(0)\n"
      "s_barrier\n"
      "ds_read_b64_tr_b16 v[64:65], v57 offset:0\n"
      "ds_read_b64_tr_b16 v[66:67], v57 offset:1728\n"
      "ds_read_b64_tr_b16 a[40:41], v56 offset:144\n"
      "ds_read_b64_tr_b16 a[42:43], v56 offset:1872\n"
      "ds_read_b64_tr_b16 a[112:113], v56 offset:288\n"
      "ds_read_b64_tr_b16 a[114:115], v56 offset:2016\n"
      "ds_read_b64_tr_b16 a[184:185], v56 offset:0\n"
      "ds_read_b64_tr_b16 a[186:187], v56 offset:1728\n"
      "v_add_u32 v56, 16384, v56\n"
      "v_add_u32 v57, 16384, v57\n"
      "ds_bpermute_b32 v76, v58, v72\n"
      "ds_bpermute_b32 v77, v58, v73\n"
      "ds_bpermute_b32 v78, v58, v74\n"
      "ds_bpermute_b32 v79, v58, v75\n"
      "v_mfma_f32_16x16x32_bf16 v[140:143], a[36:39], v[60:63], v[140:143]\n"
      "v_mfma_f32_16x16x32_bf16 v[148:151], v[60:63], a[108:111], v[148:151]\n"
      "v_mfma_f32_16x16x32_bf16 v[152:155], a[180:183], v[60:63], v[152:155]\n"
      "s_add_u32 m0, s43, 114688\n"
      "s_nop 0\n"
      "buffer_load_dwordx4 v52, s[12:15], s54 offen offset:0 lds\n"
      "buffer_load_dwordx4 v53, s[12:15], s54 offen offset:1024 lds\n"
      "buffer_load_dwordx4 v54, s[12:15], s54 offen offset:2048 lds\n"
      "buffer_load_dwordx4 v55, s[12:15], s54 offen offset:3072 lds\n"
      "s_add_u32 s54, s54, 110592\n"
      "v_and_b32 v68, v59, v68\n"
      "v_and_b32 v69, v59, v69\n"
      "v_and_b32 v70, v59, v70\n"
      "v_and_b32 v71, v59, v71\n"
      "v_accvgpr_write_b32 a248, v68\n"
      "v_accvgpr_write_b32 a249, v69\n"
      "v_accvgpr_write_b32 a250, v70\n"
      "v_accvgpr_write_b32 a251, v71\n"
      "v_mfma_f32_16x16x32_bf16 v[144:147], v[68:71], v[60:63], v[144:147]\n"
      "s_waitcnt vmcnt(18) lgkmcnt(0)\n"
      "s_barrier\n"
      "ds_read_b64_tr_b16 v[72:73], v57 offset:0\n"
      "ds_read_b64_tr_b16 v[74:75], v57 offset:1728\n"
      "ds_read_b64_tr_b16 a[44:45], v56 offset:144\n"
      "ds_read_b64_tr_b16 a[46:47], v56 offset:1872\n"
      "ds_read_b64_tr_b16 a[116:117], v56 offset:288\n"
      "ds_read_b64_tr_b16 a[118:119], v56 offset:2016\n"
      "ds_read_b64_tr_b16 a[188:189], v56 offset:0\n"
      "ds_read_b64_tr_b16 a[190:191], v56 offset:1728\n"
      "v_add_u32 v56, 4294885376, v56\n"
      "v_add_u32 v57, 4294885376, v57\n"
      "ds_bpermute_b32 v68, v58, v64\n"
      "ds_bpermute_b32 v69, v58, v65\n"
      "ds_bpermute_b32 v70, v58, v66\n"
      "ds_bpermute_b32 v71, v58, v67\n"
      "v_mfma_f32_16x16x32_bf16 v[140:143], a[40:43], v[60:63], v[140:143]\n"
      "v_mfma_f32_16x16x32_bf16 v[148:151], v[60:63], a[112:115], v[148:151]\n"
      "v_mfma_f32_16x16x32_bf16 v[152:155], a[184:187], v[60:63], v[152:155]\n"
      "s_add_u32 m0, s43, 131072\n"
      "s_nop 0\n"
      "buffer_load_dwordx4 v52, s[12:15], s54 offen offset:0 lds\n"
      "buffer_load_dwordx4 v53, s[12:15], s54 offen offset:1024 lds\n"
      "buffer_load_dwordx4 v54, s[12:15], s54 offen offset:2048 lds\n"
      "buffer_load_dwordx4 v55, s[12:15], s54 offen offset:3072 lds\n"
      "s_add_u32 s54, s54, 110592\n"
      "v_and_b32 v76, v59, v76\n"
      "v_and_b32 v77, v59, v77\n"
      "v_and_b32 v78, v59, v78\n"
      "v_and_b32 v79, v59, v79\n"
      "v_accvgpr_write_b32 a252, v76\n"
      "v_accvgpr_write_b32 a253, v77\n"
      "v_accvgpr_write_b32 a254, v78\n"
      "v_accvgpr_write_b32 a255, v79\n"
      "v_mfma_f32_16x16x32_bf16 v[144:147], v[76:79], v[60:63], v[144:147]\n"
      "s_waitcnt vmcnt(18) lgkmcnt(0)\n"
      "s_barrier\n"
      "ds_read_b64_tr_b16 v[64:65], v57 offset:0\n"
      "ds_read_b64_tr_b16 v[66:67], v57 offset:1728\n"
      "ds_read_b64_tr_b16 a[48:49], v56 offset:144\n"
      "ds_read_b64_tr_b16 a[50:51], v56 offset:1872\n"
      "ds_read_b64_tr_b16 a[120:121], v56 offset:288\n"
      "ds_read_b64_tr_b16 a[122:123], v56 offset:2016\n"
      "ds_read_b64_tr_b16 a[192:193], v56 offset:0\n"
      "ds_read_b64_tr_b16 a[194:195], v56 offset:1728\n"
      "v_add_u32 v56, 16384, v56\n"
      "v_add_u32 v57, 16384, v57\n"
      "ds_bpermute_b32 v76, v58, v72\n"
      "ds_bpermute_b32 v77, v58, v73\n"
      "ds_bpermute_b32 v78, v58, v74\n"
      "ds_bpermute_b32 v79, v58, v75\n"
      "v_mfma_f32_16x16x32_bf16 v[140:143], a[44:47], v[60:63], v[140:143]\n"
      "v_mfma_f32_16x16x32_bf16 v[148:151], v[60:63], a[116:119], v[148:151]\n"
      "v_mfma_f32_16x16x32_bf16 v[152:155], a[188:191], v[60:63], v[152:155]\n"
      "s_add_u32 m0, s43, 147456\n"
      "s_nop 0\n"
      "buffer_load_dwordx4 v52, s[12:15], s54 offen offset:0 lds\n"
      "buffer_load_dwordx4 v53, s[12:15], s54 offen offset:1024 lds\n"
      "buffer_load_dwordx4 v54, s[12:15], s54 offen offset:2048 lds\n"
      "buffer_load_dwordx4 v55, s[12:15], s54 offen offset:3072 lds\n"
      "s_add_u32 s54, s54, 110592\n"
      "v_and_b32 v68, v59, v68\n"
      "v_and_b32 v69, v59, v69\n"
      "v_and_b32 v70, v59, v70\n"
      "v_and_b32 v71, v59, v71\n"
      "v_mov_b32 v20, v68\n"
      "v_mov_b32 v21, v69\n"
      "v_mov_b32 v22, v70\n"
      "v_mov_b32 v23, v71\n"
      "v_mfma_f32_16x16x32_bf16 v[144:147], v[68:71], v[60:63], v[144:147]\n"
      "s_mul_i32 s21, s21, 1664525\n"
      "s_add_u32 s21, s21, 1013904223\n"
      "v_mov_b32 v13, s21\n"
      "buffer_store_dword v13, v10, s[8:11], 0 offen sc1\n"
      "v_not_b32 v84, v13\n"
      "v_not_b32 v85, v13\n"
      "v_not_b32 v86, v13\n"
      "v_not_b32 v87, v13\n"
      "s_add_u32 s30, s40, 0\n"
      "buffer_store_dwordx4 v[84:87], v80, s[8:11], s30 offen sc1\n"
      "v_xor_b32 v13, v13, v81\n"
      "s_add_u32 s57, s40, 1597440\n"
      "s_add_u32 s55, s40, 4294893568\n"
      "s_add_u32 s40, s40, 4294918144\n"
      "s_add_u32 s36, s36, 24576\n"
      "s_cmp_eq_u32 s36, s37\n"
      "s_cselect_b32 s47, s38, 0\n"
      "s_cselect_b32 s37, s48, s37\n"
      "s_cselect_b32 s49, s39, s56\n"
      "s_add_u32 s45, s36, s47\n"
      "s_mov_b32 s50, s49\n"
      "s_add_u32 s36, s36, 24576\n"
      "s_cmp_eq_u32 s36, s37\n"
      "s_cselect_b32 s47, s38, 0\n"
      "s_cselect_b32 s37, s48, s37\n"
      "s_cselect_b32 s49, s39, s56\n"
      "s_mov_b32 s46, s45\n"
      "s_add_u32 s53, s53, 20480\n"
      "s_cmp_eq_u32 s53, 61440\n"
      "s_cselect_b32 s53, 0, s53\n"
      "s_waitcnt vmcnt(23)\n"
      "v_cmp_ne_u32 vcc, s21, v82\n"
      "s_add_u32 m0, s53, s41\n"
      "s_add_u32 s45, s36, s47\n"
      "s_cbranch_vccnz .Lsp_q_1\n"
      ".Lbk_q_1:\n"
      "buffer_load_dwordx4 v0, s[8:11], s46 offen offset:0 sc1 lds\n"
      "buffer_load_dwordx4 v0, s[8:11], s46 offen offset:1024 sc1 lds\n"
      "buffer_load_dwordx4 v0, s[8:11], s46 offen offset:2048 sc1 lds\n"
      "buffer_load_dwordx4 v0, s[8:11], s46 offen offset:3072 sc1 lds\n"
      "s_add_u32 m0, s53, s42\n"
      "s_nop 0\n"
      "buffer_load_dwordx4 v1, s[8:11], s46 offen offset:0 sc1 lds\n"
      "s_mov_b32 s51, s50\n"
      "s_mov_b32 s50, s49\n"
      "s_add_u32 s36, s36, 24576\n"
      "v_add_u32 v16, s53, v4\n"
      "s_cmp_eq_u32 s36, s37\n"
      "s_cselect_b32 s47, s38, 0\n"
      "s_cselect_b32 s37, s48, s37\n"
      "s_cselect_b32 s49, s39, s56\n"
      "v_add_u32 v15, s53, v0\n"
      "s_mov_b32 s46, s45\n"
      "s_add_u32 s53, s53, 20480\n"
      "s_cmp_eq_u32 s53, 61440\n"
      "s_cselect_b32 s53, 0, s53\n"
      "s_waitcnt vmcnt(27)\n"
      "v_cmp_ne_u32 vcc, s21, v83\n"
      "s_add_u32 m0, s53, s41\n"
      "s_add_u32 s45, s36, s47\n"
      "s_cbranch_vccnz .Lsp_q_2\n"
      ".Lbk_q_2:\n"
      "buffer_load_dwordx4 v0, s[8:11], s46 offen offset:0 sc1 lds\n"
      "buffer_load_dwordx4 v0, s[8:11], s46 offen offset:1024 sc1 lds\n"
      "buffer_load_dword v17, v2, s[8:11], s45 offen sc1\n"
      "buffer_load_dwordx4 v0, s[8:11], s46 offen offset:2048 sc1 lds\n"
      "buffer_load_dwordx4 v0, s[8:11], s46 offen offset:3072 sc1 lds\n"
      "s_mov_b32 s52, s51\n"
      "s_add_u32 m0, s53, s42\n"
      "s_nop 0\n"
      "buffer_load_dwordx4 v1, s[8:11], s46 offen offset:0 sc1 lds\n"
      "s_waitcnt vmcnt(29) lgkmcnt(0)\n"
      "s_barrier\n"
      "ds_read_b64_tr_b16 v[72:73], v57 offset:0\n"
      "ds_read_b64_tr_b16 v[74:75], v57 offset:1728\n"
      "ds_read_b64_tr_b16 a[52:53], v56 offset:144\n"
      "ds_read_b64_tr_b16 a[54:55], v56 offset:1872\n"
      "ds_read_b64_tr_b16 a[124:125], v56 offset:288\n"
      "ds_read_b64_tr_b16 a[126:127], v56 offset:2016\n"
      "ds_read_b64_tr_b16 a[196:197], v56 offset:0\n"
      "ds_read_b64_tr_b16 a[198:199], v56 offset:1728\n"
      "v_add_u32 v56, 16384, v56\n"
      "v_add_u32 v57, 16384, v57\n"
      "ds_bpermute_b32 v68, v58, v64\n"
      "ds_bpermute_b32 v69, v58, v65\n"
      "ds_bpermute_b32 v70, v58, v66\n"
      "ds_bpermute_b32 v71, v58, v67\n"
      "v_mfma_f32_16x16x32_bf16 v[140:143], a[48:51], v[60:63], v[140:143]\n"
      "v_mfma_f32_16x16x32_bf16 v[148:151], v[60:63], a[120:123], v[148:151]\n"
      "v_mfma_f32_16x16x32_bf16 v[152:155], a[192:195], v[60:63], v[152:155]\n"
      "v_and_b32 v76, v59, v76\n"
      "v_and_b32 v77, v59, v77\n"
      "v_and_b32 v78, v59, v78\n"
      "v_and_b32 v79, v59, v79\n"
      "v_mov_b32 v24, v76\n"
      "v_mov_b32 v25, v77\n"
      "v_mov_b32 v26, v78\n"
      "v_mov_b32 v27, v79\n"
      "v_mfma_f32_16x16x32_bf16 v[144:147], v[76:79], v[60:63], v[144:147]\n"
      "s_waitcnt vmcnt(25) lgkmcnt(0)\n"
      "s_barrier\n"
      "ds_read_b64_tr_b16 v[64:65], v57 offset:0\n"
      "ds_read_b64_tr_b16 v[66:67], v57 offset:1728\n"
      "ds_read_b64_tr_b16 a[56:57], v56 offset:144\n"
      "ds_read_b64_tr_b16 a[58:59], v56 offset:1872\n"
      "ds_read_b64_tr_b16 a[128:129], v56 offset:288\n"
      "ds_read_b64_tr_b16 a[130:131], v56 offset:2016\n"
      "ds_read_b64_tr_b16 a[200:201], v56 offset:0\n"
      "ds_read_b64_tr_b16 a[202:203], v56 offset:1728\n"
      "v_add_u32 v56, 16384, v56\n"
      "v_add_u32 v57, 16384, v57\n"
      "ds_bpermute_b32 v76, v58, v72\n"
      "ds_bpermute_b32 v77, v58, v73\n"
      "ds_bpermute_b32 v78, v58, v74\n"
      "ds_bpermute_b32 v79, v58, v75\n"
      "v_mfma_f32_16x16x32_bf16 v[140:143], a[52:55], v[60:63], v[140:143]\n"
      "v_mfma_f32_16x16x32_bf16 v[148:151], v[60:63], a[124:127], v[148:151]\n"
      "v_mfma_f32_16x16x32_bf16 v[152:155], a[196:199], v[60:63], v[152:155]\n"
      "v_and_b32 v68, v59, v68\n"
      "v_and_b32 v69, v59, v69\n"
      "v_and_b32 v70, v59, v70\n"
      "v_and_b32 v71, v59, v71\n"
      "v_mov_b32 v28, v68\n"
      "v_mov_b32 v29, v69\n"
      "v_mov_b32 v30, v70\n"
      "v_mov_b32 v31, v71\n"
      "v_mfma_f32_16x16x32_bf16 v[144:147], v[68:71], v[60:63], v[144:147]\n"
      "s_waitcnt vmcnt(21) lgkmcnt(0)\n"
      "s_barrier\n"
      "ds_read_b64_tr_b16 v[72:73], v57 offset:0\n"
      "ds_read_b64_tr_b16 v[74:75], v57 offset:1728\n"
      "ds_read_b64_tr_b16 a[60:61], v56 offset:144\n"
      "ds_read_b64_tr_b16 a[62:63], v56 offset:1872\n"
      "ds_read_b64_tr_b16 a[132:133], v56 offset:288\n"
      "ds_read_b64_tr_b16 a[134:135], v56 offset:2016\n"
      "ds_read_b64_tr_b16 a[204:205], v56 offset:0\n"
      "ds_read_b64_tr_b16 a[206:207], v56 offset:1728\n"
      "v_add_u32 v56, 16384, v56\n"
      "v_add_u32 v57, 16384, v57\n"
      "ds_bpermute_b32 v68, v58, v64\n"
      "ds_bpermute_b32 v69, v58, v65\n"
      "ds_bpermute_b32 v70, v58, v66\n"
      "ds_bpermute_b32 v71, v58, v67\n"
      "v_mfma_f32_16x16x32_bf16 v[140:143], a[56:59], v[60:63], v[140:143]\n"
      "v_mfma_f32_16x16x32_bf16 v[148:151], v[60:63], a[128:131], v[148:151]\n"
      "v_mfma_f32_16x16x32_bf16 v[152:155], a[200:203], v[60:63], v[152:155]\n"
      "v_and_b32 v76, v59, v76\n"
      "v_and_b32 v77, v59, v77\n"
      "v_and_b32 v78, v59, v78\n"
      "v_and_b32 v79, v59, v79\n"
      "v_mov_b32 v32, v76\n"
      "v_mov_b32 v33, v77\n"
      "v_mov_b32 v34, v78\n"
      "v_mov_b32 v35, v79\n"
      "v_mfma_f32_16x16x32_bf16 v[144:147], v[76:79], v[60:63], v[144:147]\n"
      "s_waitcnt vmcnt(17) lgkmcnt(0)\n"
      "s_barrier\n"
      "ds_read_b64_tr_b16 v[64:65], v57 offset:0\n"
      "ds_read_b64_tr_b16 v[66:67], v57 offset:1728\n"
      "ds_read_b64_tr_b16 a[64:65], v56 offset:144\n"
      "ds_read_b64_tr_b16 a[66:67], v56 offset:1872\n"
      "ds_read_b64_tr_b16 a[136:137], v56 offset:288\n"
      "ds_read_b64_tr_b16 a[138:139], v56 offset:2016\n"
      "ds_read_b64_tr_b16 a[208:209], v56 offset:0\n"
      "ds_read_b64_tr_b16 a[210:211], v56 offset:1728\n"
      "v_add_u32 v56, 16384, v56\n"
      "v_add_u32 v57, 16384, v57\n"
      "ds_bpermute_b32 v76, v58, v72\n"
      "ds_bpermute_b32 v77, v58, v73\n"
      "ds_bpermute_b32 v78, v58, v74\n"
      "ds_bpermute_b32 v79, v58, v75\n"
      "v_mfma_f32_16x16x32_bf16 v[140:143], a[60:63], v[60:63], v[140:143]\n"
      "v_mfma_f32_16x16x32_bf16 v[148:151], v[60:63], a[132:135], v[148:151]\n"
      "v_mfma_f32_16x16x32_bf16 v[152:155], a[204:207], v[60:63], v[152:155]\n"
      "v_and_b32 v68, v59, v68\n"
      "v_and_b32 v69, v59, v69\n"
      "v_and_b32 v70, v59, v70\n"
      "v_and_b32 v71, v59, v71\n"
      "v_mov_b32 v36, v68\n"
      "v_mov_b32 v37, v69\n"
      "v_mov_b32 v38, v70\n"
      "v_mov_b32 v39, v71\n"
      "v_mfma_f32_16x16x32_bf16 v[144:147], v[68:71], v[60:63], v[144:147]\n"
      "s_waitcnt vmcnt(13) lgkmcnt(0)\n"
      "s_barrier\n"
      "ds_read_b64_tr_b16 v[72:73], v57 offset:0\n"
      "ds_read_b64_tr_b16 v[74:75], v57 offset:1728\n"
      "ds_read_b64_tr_b16 a[68:69], v56 offset:144\n"
      "ds_read_b64_tr_b16 a[70:71], v56 offset:1872\n"
      "ds_read_b64_tr_b16 a[140:141], v56 offset:288\n"
      "ds_read_b64_tr_b16 a[142:143], v56 offset:2016\n"
      "ds_read_b64_tr_b16 a[212:213], v56 offset:0\n"
      "ds_read_b64_tr_b16 a[214:215], v56 offset:1728\n"
      "v_add_u32 v56, 4294885376, v56\n"
      "v_add_u32 v57, 4294885376, v57\n"
      "ds_bpermute_b32 v68, v58, v64\n"
      "ds_bpermute_b32 v69, v58, v65\n"
      "ds_bpermute_b32 v70, v58, v66\n"
      "ds_bpermute_b32 v71, v58, v67\n"
      "v_mfma_f32_16x16x32_bf16 v[140:143], a[64:67], v[60:63], v[140:143]\n"
      "v_mfma_f32_16x16x32_bf16 v[148:151], v[60:63], a[136:139], v[148:151]\n"
      "v_mfma_f32_16x16x32_bf16 v[152:155], a[208:211], v[60:63], v[152:155]\n"
      "v_and_b32 v76, v59, v76\n"
      "v_and_b32 v77, v59, v77\n"
      "v_and_b32 v78, v59, v78\n"
      "v_and_b32 v79, v59, v79\n"
      "v_mov_b32 v40, v76\n"
      "v_mov_b32 v41, v77\n"
      "v_mov_b32 v42, v78\n"
      "v_mov_b32 v43, v79\n"
      "v_mfma_f32_16x16x32_bf16 v[144:147], v[76:79], v[60:63], v[144:147]\n"
      "s_waitcnt lgkmcnt(0)\n"
      "ds_bpermute_b32 v76, v58, v72\n"
      "ds_bpermute_b32 v77, v58, v73\n"
      "ds_bpermute_b32 v78, v58, v74\n"
      "ds_bpermute_b32 v79, v58, v75\n"
      "v_mfma_f32_16x16x32_bf16 v[140:143], a[68:71], v[60:63], v[140:143]\n"
      "v_mfma_f32_16x16x32_bf16 v[148:151], v[60:63], a[140:143], v[148:151]\n"
      "v_mfma_f32_16x16x32_bf16 v[152:155], a[212:215], v[60:63], v[152:155]\n"
      "v_and_b32 v68, v59, v68\n"
      "v_and_b32 v69, v59, v69\n"
      "v_and_b32 v70, v59, v70\n"
      "v_and_b32 v71, v59, v71\n"
      "v_mov_b32 v44, v68\n"
      "v_mov_b32 v45, v69\n"
      "v_mov_b32 v46, v70\n"
      "v_mov_b32 v47, v71\n"
      "v_mfma_f32_16x16x32_bf16 v[144:147], v[68:71], v[60:63], v[144:147]\n"
      "s_waitcnt lgkmcnt(0)\n"
      "v_and_b32 v76, v59, v76\n"
      "v_and_b32 v77, v59, v77\n"
      "v_and_b32 v78, v59, v78\n"
      "v_and_b32 v79, v59, v79\n"
      "v_mov_b32 v48, v76\n"
      "v_mov_b32 v49, v77\n"
      "v_mov_b32 v50, v78\n"
      "v_mov_b32 v51, v79\n"
      "v_mfma_f32_16x16x32_bf16 v[144:147], v[76:79], v[60:63], v[144:147]\n"
      "v_add_u32 v9, 1, v9\n"
      "s_waitcnt vmcnt(6)\n"
      "ds_write_b32 v11, v9\n"
      "ds_read_b32 v10, v12\n"
      "s_waitcnt lgkmcnt(0)\n"
      "v_cmp_gt_i32 vcc, v9, v10\n"
      "s_cbranch_vccnz .Lsp_qy_3\n"
      ".Lbk_qy_3:\n"
      "ds_read_b128 v[56:59], v15 offset:1024\n"
      "ds_read_b128 v[80:83], v15 offset:7168\n"
      "ds_read_b128 v[84:87], v15 offset:8192\n"
      "ds_read_b64 v[220:221], v16 offset:1024\n"
      "s_mov_b32 s51, s50\n"
      "ds_read_b128 v[52:55], v15 offset:0\n"
      "ds_read_b128 v[60:63], v15 offset:2048\n"
      "ds_read_b128 v[68:71], v15 offset:4096\n"
      "ds_read_b128 v[76:79], v15 offset:6144\n"
      "s_mov_b32 s50, s49\n"
      "ds_read_b128 v[64:67], v15 offset:3072\n"
      "ds_read_b128 v[88:91], v15 offset:9216\n"
      "ds_read_b128 v[92:95], v15 offset:10240\n"
      "ds_read_b128 v[100:103], v15 offset:12288\n"
      "ds_read2_b64 v[216:219], v16 offset1:64\n"
      "ds_read_b128 v[72:75], v15 offset:5120\n"
      "ds_read_b128 v[108:111], v15 offset:14336\n"
      "s_mov_b32 s20, 32\n"
      "s_waitcnt lgkmcnt(0)\n"
      "s_nop 4\n"
      ".p2align 6\n"
      ".Lq_loop:\n"
      "v_mfma_f32_16x16x32_bf16 v[124:127], a[0:3], v[52:55], 0\n"
      "ds_read_b128 v[104:107], v15 offset:13312\n"
      "v_mul_f32 v209, v209, v214\n"
      "v_mfma_f32_16x16x32_bf16 v[124:127], a[4:7], v[56:59], v[124:127]\n"
      "s_waitcnt vmcnt(6) lgkmcnt(6)\n"
      "buffer_store_dword v13, v3, s[8:11], s55 offen sc1\n"
      "v_mfma_f32_16x16x32_bf16 v[124:127], a[8:11], v[60:63], v[124:127]\n"
      "ds_read_b128 v[120:123], v15 offset:17408\n"
      "v_fma_f32 v137, -v222, v153, v137\n"
      "v_mfma_f32_16x16x32_bf16 v[124:127], a[12:15], v[64:67], v[124:127]\n"
      "s_add_u32 s36, s36, 24576\n"
      "ds_read_b128 v[96:99], v15 offset:11264\n"
      "v_fma_f32 v139, -v222, v155, v139\n"
      "v_mfma_f32_16x16x32_bf16 v[124:127], a[16:19], v[68:71], v[124:127]\n"
      "s_waitcnt lgkmcnt(4)\n"
      "v_fma_f32 v138, -v222, v154, v138\n"
      "v_add_u32 v16, s53, v4\n"
      "v_mfma_f32_16x16x32_bf16 v[124:127], a[20:23], v[72:75], v[124:127]\n"
      "v_cvt_pk_bf16_f32 v232, v208, v209\n"
      "s_cmp_eq_u32 s36, s37\n"
      "v_mfma_f32_16x16x32_bf16 v[124:127], a[24:27], v[76:79], v[124:127]\n"
      "s_cselect_b32 s47, s38, 0\n"
      "v_fma_f32 v136, -v222, v152, v136\n"
      "v_cvt_pk_bf16_f32 v181, v138, v139\n"
      "v_mfma_f32_16x16x32_bf16 v[124:127], a[28:31], v[80:83], v[124:127]\n"
      "s_cselect_b32 s37, s48, s37\n"
      "ds_read_b128 v[112:115], v15 offset:15360\n"
      "v_cvt_pk_bf16_f32 v180, v136, v137\n"
      "v_mfma_f32_16x16x32_bf16 v[124:127], a[32:35], v[84:87], v[124:127]\n"
      "s_cselect_b32 s49, s39, s56\n"
      "ds_read_b128 v[116:119], v15 offset:16384\n"
      "v_add_u32 v15, s53, v0\n"
      "v_mfma_f32_16x16x32_bf16 v[192:195], v[176:179], v[180:183], v[156:159]\n"
      "v_cvt_pk_bf16_f32 v234, v212, v213\n"
      "v_add_f32 v216, v216, v218\n"
      "v_mfma_f32_16x16x32_bf16 v[124:127], a[36:39], v[88:91], v[124:127]\n"
      "v_mfma_f32_16x16x32_bf16 v[124:127], a[40:43], v[92:95], v[124:127]\n"
      "s_waitcnt lgkmcnt(2)\n"
      "buffer_store_dwordx2 v[234:235], v8, s[8:11], s40 offen sc1\n"
      "v_mfma_f32_16x16x32_bf16 v[124:127], a[44:47], v[96:99], v[124:127]\n"
      "v_cvt_pk_bf16_f32 v233, v210, v211\n"
      "v_add_f32 v222, v216, v220\n"
      "v_mfma_f32_16x16x32_bf16 v[124:127], a[48:51], v[100:103], v[124:127]\n"
      "buffer_store_dwordx2 v[232:233], v7, s[8:11], s40 offen sc1\n"
      "v_mfma_f32_16x16x32_bf16 v[124:127], a[52:55], v[104:107], v[124:127]\n"
      "s_mov_b32 s46, s45\n"
      "v_mfma_f32_16x16x32_bf16 v[124:127], a[56:59], v[108:111], v[124:127]\n"
      "s_waitcnt lgkmcnt(0)\n"
      "v_mfma_f32_16x16x32_bf16 v[124:127], a[60:63], v[112:115], v[124:127]\n"
      "v_mul_f32 v236, 0x3eae1a93, v223\n"
      "v_mfma_f32_16x16x32_bf16 v[124:127], a[64:67], v[116:119], v[124:127]\n"
      "v_add_f32 v217, v217, v219\n"
      "v_add_f32 v217, v217, v221\n"
      "v_mfma_f32_16x16x32_bf16 v[124:127], a[68:71], v[120:123], v[124:127]\n"
      "v_add_u32 v9, 1, v9\n"
      "v_mfma_f32_16x16x32_bf16 v[128:131], a[216:219], v[52:55], 0\n"
      "v_fma_f32 v217, -v222, v222, v217\n"
      "s_add_u32 s53, s53, 20480\n"
      "v_mfma_f32_16x16x32_bf16 v[128:131], a[220:223], v[56:59], v[128:131]\n"
      "s_cmp_eq_u32 s53, 61440\n"
      "s_cselect_b32 s53, 0, s53\n"
      "v_mfma_f32_16x16x32_bf16 v[128:131], a[224:227], v[60:63], v[128:131]\n"
      "v_max_f32 v217, 0x3727c5ac, v217\n"
      "v_mfma_f32_16x16x32_bf16 v[128:131], a[228:231], v[64:67], v[128:131]\n"
      "v_fma_f32 v125, -v222, v141, v125\n"
      "v_mfma_f32_16x16x32_bf16 v[128:131], a[232:235], v[68:71], v[128:131]\n"
      "v_fma_f32 v126, -v222, v142, v126\n"
      "v_mfma_f32_16x16x32_bf16 v[128:131], a[236:239], v[72:75], v[128:131]\n"
      "s_waitcnt vmcnt(6)\n"
      "v_fma_f32 v124, -v222, v140, v124\n"
      "v_cmp_ne_u32 vcc, s21, v17\n"
      "v_mfma_f32_16x16x32_bf16 v[128:131], a[240:243], v[76:79], v[128:131]\n"
      "s_add_u32 m0, s53, s41\n"
      "v_rsq_f32 v223, v217\n"
      "v_mfma_f32_16x16x32_bf16 v[128:131], a[244:247], v[80:83], v[128:131]\n"
      "s_add_u32 s45, s36, s47\n"
      "v_mul_f32 v124, v223, v124\n"
      "v_mfma_f32_16x16x32_bf16 v[128:131], a[248:251], v[84:87], v[128:131]\n"
      "v_fma_f32 v127, -v222, v143, v127\n"
      "v_mfma_f32_16x16x32_bf16 v[128:131], a[252:255], v[88:91], v[128:131]\n"
      "v_cndmask_b32 v237, v223, 1.0, s[58:59]\n"
      "v_mfma_f32_16x16x32_bf16 v[128:131], v[20:23], v[92:95], v[128:131]\n"
      "ds_write_b64 v5, v[222:223]\n"
      "v_mfma_f32_16x16x32_bf16 v[128:131], v[24:27], v[96:99], v[128:131]\n"
      "ds_read_b128 v[224:227], v6\n"
      "v_mfma_f32_16x16x32_bf16 v[128:131], v[28:31], v[100:103], v[128:131]\n"
      "s_cbranch_vccnz .Lsp_q_4\n"
      ".Lbk_q_4:\n"
      "v_mul_f32 v125, v223, v125\n"
      "v_mfma_f32_16x16x32_bf16 v[128:131], v[32:35], v[104:107], v[128:131]\n"
      "v_mul_f32 v126, v223, v126\n"
      "v_mul_f32 v127, v223, v127\n"
      "v_mfma_f32_16x16x32_bf16 v[128:131], v[36:39], v[108:111], v[128:131]\n"
      "v_mov_b32 v186, v184\n"
      "v_mfma_f32_16x16x32_bf16 v[128:131], v[40:43], v[112:115], v[128:131]\n"
      "buffer_load_dwordx4 v0, s[8:11], s46 offen offset:0 sc1 lds\n"
      "v_mfma_f32_16x16x32_bf16 v[128:131], v[44:47], v[116:119], v[128:131]\n"
      "v_cvt_pk_bf16_f32 v176, v124, v125\n"
      "v_mfma_f32_16x16x32_bf16 v[128:131], v[48:51], v[120:123], v[128:131]\n"
      "v_mov_b32 v190, v188\n"
      "v_cvt_pk_bf16_f32 v177, v126, v127\n"
      "v_mfma_f32_16x16x32_bf16 v[132:135], v[52:55], a[72:75], 0\n"
      "s_waitcnt vmcnt(4) lgkmcnt(0)\n"
      "ds_write_b32 v11, v9\n"
      "v_mfma_f32_16x16x32_bf16 v[132:135], v[56:59], a[76:79], v[132:135]\n"
      "buffer_load_dwordx4 v0, s[8:11], s46 offen offset:1024 sc1 lds\n"
      "v_mfma_f32_16x16x32_bf16 v[132:135], v[60:63], a[80:83], v[132:135]\n"
      "v_fma_f32 v129, -v222, v145, v129\n"
      "v_fma_f32 v131, -v222, v147, v131\n"
      "v_mfma_f32_16x16x32_bf16 v[132:135], v[64:67], a[84:87], v[132:135]\n"
      "v_fma_f32 v128, -v222, v144, v128\n"
      "v_mul_f32 v129, v237, v129\n"
      "v_mfma_f32_16x16x32_bf16 v[132:135], v[68:71], a[88:91], v[132:135]\n"
      "v_mul_f32 v128, v237, v128\n"
      "v_mul_f32 v131, v223, v131\n"
      "v_mfma_f32_16x16x32_bf16 v[132:135], v[72:75], a[92:95], v[132:135]\n"
      "v_fma_f32 v130, -v222, v146, v130\n"
      "v_mul_f32 v130, v223, v130\n"
      "v_mfma_f32_16x16x32_bf16 v[132:135], v[76:79], a[96:99], v[132:135]\n"
      "buffer_load_dword v17, v2, s[8:11], s45 offen sc1\n"
      "v_cvt_pk_bf16_f32 v178, v130, v131\n"
      "v_cvt_pk_bf16_f32 v179, v128, v129\n"
      "v_mfma_f32_16x16x32_bf16 v[132:135], v[80:83], a[100:103], v[132:135]\n"
      "buffer_load_dwordx4 v0, s[8:11], s46 offen offset:2048 sc1 lds\n"
      "v_mfma_f32_16x16x32_bf16 v[196:199], v[176:179], v[180:183], v[160:163]\n"
      "ds_read_b128 v[228:231], v6 offset:16\n"
      "v_mov_b32 v187, v185\n"
      "v_mfma_f32_16x16x32_bf16 v[200:203], v[176:179], v[168:171], v[172:175]\n"
      "ds_read_b32 v10, v12\n"
      "v_mfma_f32_16x16x32_bf16 v[132:135], v[84:87], a[104:107], v[132:135]\n"
      "v_mov_b32 v191, v189\n"
      "v_mfma_f32_16x16x32_bf16 v[132:135], v[88:91], a[108:111], v[132:135]\n"
      "s_add_u32 s55, s55, 24576\n"
      "v_med3_f32 v239, v198, v199, s52\n"
      "v_med3_f32 v238, v196, v197, s52\n"
      "v_mfma_f32_16x16x32_bf16 v[132:135], v[92:95], a[112:115], v[132:135]\n"
      "buffer_load_dwordx4 v0, s[8:11], s46 offen offset:3072 sc1 lds\n"
      "v_mfma_f32_16x16x32_bf16 v[132:135], v[96:99], a[116:119], v[132:135]\n"
      "s_mov_b32 s52, s51\n"
      "v_max3_f32 v238, v192, v193, v238\n"
      "v_max3_f32 v239, v194, v195, v239\n"
      "v_mfma_f32_16x16x32_bf16 v[132:135], v[100:103], a[120:123], v[132:135]\n"
      "v_max_f32 v240, v238, v239\n"
      "v_cvt_pk_bf16_f32 v189, v202, v203\n"
      "v_mov_b32 v241, v240\n"
      "v_mfma_f32_16x16x32_bf16 v[132:135], v[104:107], a[124:127], v[132:135]\n"
      "s_add_u32 m0, s53, s42\n"
      "v_permlane16_swap_b32 v240, v241\n"
      "v_mfma_f32_16x16x32_bf16 v[132:135], v[108:111], a[128:131], v[132:135]\n"
      "s_waitcnt lgkmcnt(0)\n"
      "v_max_f32 v240, v240, v241\n"
      "v_mov_b32 v241, v240\n"
      "v_cmp_gt_i32 vcc, v9, v10\n"
      "v_mfma_f32_16x16x32_bf16 v[132:135], v[112:115], a[132:135], v[132:135]\n"
      "v_permlane32_swap_b32 v240, v241\n"
      "v_mfma_f32_16x16x32_bf16 v[132:135], v[116:119], a[136:139], v[132:135]\n"
      "buffer_load_dwordx4 v1, s[8:11], s46 offen offset:0 sc1 lds\n"
      "v_max_f32 v240, v240, v241\n"
      "v_cvt_pk_bf16_f32 v188, v200, v201\n"
      "v_mfma_f32_16x16x32_bf16 v[132:135], v[120:123], a[140:143], v[132:135]\n"
      "v_mul_f32 v240, v240, v236\n"
      "v_fma_f32 v238, v238, v236, -v240\n"
      "v_mfma_f32_16x16x32_bf16 v[136:139], a[144:147], v[52:55], 0\n"
      "s_cbranch_vccnz .Lsp_qy_5\n"
      ".Lbk_qy_5:\n"
      "v_fma_f32 v239, v239, v236, -v240\n"
      "v_exp_f32 v238, v238\n"
      "v_mfma_f32_16x16x32_bf16 v[136:139], a[148:151], v[56:59], v[136:139]\n"
      "ds_read_b128 v[56:59], v15 offset:1024\n"
      "v_cvt_pk_bf16_f32 v238, v238, v238\n"
      "v_mfma_f32_16x16x32_bf16 v[136:139], a[152:155], v[60:63], v[136:139]\n"
      "v_exp_f32 v239, v239\n"
      "v_mfma_f32_16x16x32_bf16 v[136:139], a[156:159], v[64:67], v[136:139]\n"
      "v_cvt_pk_bf16_f32 v239, v239, v239\n"
      "v_fma_f32 v132, -v224, v149, v132\n"
      "v_mul_f32 v132, v225, v132\n"
      "v_mfma_f32_16x16x32_bf16 v[136:139], a[160:163], v[68:71], v[136:139]\n"
      "v_fma_f32 v133, -v226, v150, v133\n"
      "v_fma_f32 v134, -v228, v151, v134\n"
      "v_mfma_f32_16x16x32_bf16 v[136:139], a[164:167], v[72:75], v[136:139]\n"
      "v_and_b32 v207, v167, v239\n"
      "v_fma_f32 v135, -v230, v148, v135\n"
      "v_mul_f32 v135, v231, v135\n"
      "v_mfma_f32_16x16x32_bf16 v[136:139], a[168:171], v[76:79], v[136:139]\n"
      "v_mul_f32 v134, v229, v134\n"
      "v_and_b32 v204, v164, v238\n"
      "v_and_b32 v205, v165, v239\n"
      "v_and_b32 v206, v166, v238\n"
      "v_mfma_f32_16x16x32_bf16 v[136:139], a[172:175], v[80:83], v[136:139]\n"
      "v_cvt_pk_bf16_f32 v185, v134, v135\n"
      "v_mul_f32 v133, v227, v133\n"
      "v_mfma_f32_16x16x32_bf16 v[136:139], a[176:179], v[84:87], v[136:139]\n"
      "ds_read_b128 v[80:83], v15 offset:7168\n"
      "v_cvt_pk_bf16_f32 v184, v132, v133\n"
      "v_mfma_f32_16x16x32_bf16 v[212:215], v[188:191], v[204:207], 0\n"
      "ds_read_b128 v[84:87], v15 offset:8192\n"
      "ds_read_b64 v[220:221], v16 offset:1024\n"
      "v_mfma_f32_16x16x32_bf16 v[208:211], v[184:187], v[204:207], 0\n"
      "s_mov_b32 s51, s50\n"
      "ds_read_b128 v[52:55], v15 offset:0\n"
      "ds_read_b128 v[60:63], v15 offset:2048\n"
      "v_mfma_f32_16x16x32_bf16 v[136:139], a[180:183], v[88:91], v[136:139]\n"
      "ds_read_b128 v[68:71], v15 offset:4096\n"
      "v_mov_b32 v182, v179\n"
      "v_mfma_f32_16x16x32_bf16 v[136:139], a[184:187], v[92:95], v[136:139]\n"
      "ds_read_b128 v[76:79], v15 offset:6144\n"
      "v_rcp_f32 v214, v214\n"
      "v_mfma_f32_16x16x32_bf16 v[136:139], a[188:191], v[96:99], v[136:139]\n"
      "s_mov_b32 s50, s49\n"
      "ds_read_b128 v[64:67], v15 offset:3072\n"
      "v_mul_f32 v213, v213, v214\n"
      "v_mfma_f32_16x16x32_bf16 v[136:139], a[192:195], v[100:103], v[136:139]\n"
      "ds_read_b128 v[88:91], v15 offset:9216\n"
      "v_mul_f32 v210, v210, v214\n"
      "v_mfma_f32_16x16x32_bf16 v[136:139], a[196:199], v[104:107], v[136:139]\n"
      "ds_read_b128 v[92:95], v15 offset:10240\n"
      "v_mul_f32 v208, v208, v214\n"
      "v_mfma_f32_16x16x32_bf16 v[136:139], a[200:203], v[108:111], v[136:139]\n"
      "ds_read_b128 v[100:103], v15 offset:12288\n"
      "ds_read2_b64 v[216:219], v16 offset1:64\n"
      "v_mfma_f32_16x16x32_bf16 v[136:139], a[204:207], v[112:115], v[136:139]\n"
      "s_add_u32 s40, s40, 24576\n"
      "ds_read_b128 v[72:75], v15 offset:5120\n"
      "v_mul_f32 v211, v211, v214\n"
      "v_mfma_f32_16x16x32_bf16 v[136:139], a[208:211], v[116:119], v[136:139]\n"
      "s_waitcnt lgkmcnt(7)\n"
      "ds_read_b128 v[108:111], v15 offset:14336\n"
      "v_mul_f32 v212, v212, v214\n"
      "v_mfma_f32_16x16x32_bf16 v[136:139], a[212:215], v[120:123], v[136:139]\n"
      "v_mfma_f32_16x16x32_bf16 v[124:127], a[0:3], v[52:55], 0\n"
      "ds_read_b128 v[104:107], v15 offset:13312\n"
      "v_mul_f32 v209, v209, v214\n"
      "v_mfma_f32_16x16x32_bf16 v[124:127], a[4:7], v[56:59], v[124:127]\n"
      "s_waitcnt vmcnt(6) lgkmcnt(6)\n"
      "buffer_store_dword v13, v3, s[8:11], s55 offen sc1\n"
      "v_mfma_f32_16x16x32_bf16 v[124:127], a[8:11], v[60:63], v[124:127]\n"
      "ds_read_b128 v[120:123], v15 offset:17408\n"
      "v_fma_f32 v137, -v222, v153, v137\n"
      "v_mfma_f32_16x16x32_bf16 v[124:127], a[12:15], v[64:67], v[124:127]\n"
      "s_add_u32 s36, s36, 24576\n"
      "ds_read_b128 v[96:99], v15 offset:11264\n"
      "v_fma_f32 v139, -v222, v155, v139\n"
      "v_mfma_f32_16x16x32_bf16 v[124:127], a[16:19], v[68:71], v[124:127]\n"
      "s_waitcnt lgkmcnt(4)\n"
      "v_fma_f32 v138, -v222, v154, v138\n"
      "v_add_u32 v16, s53, v4\n"
      "v_mfma_f32_16x16x32_bf16 v[124:127], a[20:23], v[72:75], v[124:127]\n"
      "v_cvt_pk_bf16_f32 v232, v208, v209\n"
      "s_cmp_eq_u32 s36, s37\n"
      "v_mfma_f32_16x16x32_bf16 v[124:127], a[24:27], v[76:79], v[124:127]\n"
      "s_cselect_b32 s47, s38, 0\n"
      "v_fma_f32 v136, -v222, v152, v136\n"
      "v_cvt_pk_bf16_f32 v181, v138, v139\n"
      "v_mfma_f32_16x16x32_bf16 v[124:127], a[28:31], v[80:83], v[124:127]\n"
      "s_cselect_b32 s37, s48, s37\n"
      "ds_read_b128 v[112:115], v15 offset:15360\n"
      "v_cvt_pk_bf16_f32 v180, v136, v137\n"
      "v_mfma_f32_16x16x32_bf16 v[124:127], a[32:35], v[84:87], v[124:127]\n"
      "s_cselect_b32 s49, s39, s56\n"
      "ds_read_b128 v[116:119], v15 offset:16384\n"
      "v_add_u32 v15, s53, v0\n"
      "v_mfma_f32_16x16x32_bf16 v[192:195], v[176:179], v[180:183], v[156:159]\n"
      "v_cvt_pk_bf16_f32 v234, v212, v213\n"
      "v_add_f32 v216, v216, v218\n"
      "v_mfma_f32_16x16x32_bf16 v[124:127], a[36:39], v[88:91], v[124:127]\n"
      "v_mfma_f32_16x16x32_bf16 v[124:127], a[40:43], v[92:95], v[124:127]\n"
      "s_waitcnt lgkmcnt(2)\n"
      "buffer_store_dwordx2 v[234:235], v8, s[8:11], s40 offen sc1\n"
      "v_mfma_f32_16x16x32_bf16 v[124:127], a[44:47], v[96:99], v[124:127]\n"
      "v_cvt_pk_bf16_f32 v233, v210, v211\n"
      "v_add_f32 v222, v216, v220\n"
      "v_mfma_f32_16x16x32_bf16 v[124:127], a[48:51], v[100:103], v[124:127]\n"
      "buffer_store_dwordx2 v[232:233], v7, s[8:11], s40 offen sc1\n"
      "v_mfma_f32_16x16x32_bf16 v[124:127], a[52:55], v[104:107], v[124:127]\n"
      "s_mov_b32 s46, s45\n"
      "v_mfma_f32_16x16x32_bf16 v[124:127], a[56:59], v[108:111], v[124:127]\n"
      "s_waitcnt lgkmcnt(0)\n"
      "v_mfma_f32_16x16x32_bf16 v[124:127], a[60:63], v[112:115], v[124:127]\n"
      "v_mul_f32 v236, 0x3eae1a93, v223\n"
      "v_mfma_f32_16x16x32_bf16 v[124:127], a[64:67], v[116:119], v[124:127]\n"
      "v_add_f32 v217, v217, v219\n"
      "v_add_f32 v217, v217, v221\n"
      "v_mfma_f32_16x16x32_bf16 v[124:127], a[68:71], v[120:123], v[124:127]\n"
      "v_add_u32 v9, 1, v9\n"
      "v_mfma_f32_16x16x32_bf16 v[128:131], a[216:219], v[52:55], 0\n"
      "v_fma_f32 v217, -v222, v222, v217\n"
      "s_add_u32 s53, s53, 20480\n"
      "v_mfma_f32_16x16x32_bf16 v[128:131], a[220:223], v[56:59], v[128:131]\n"
      "s_cmp_eq_u32 s53, 61440\n"
      "s_cselect_b32 s53, 0, s53\n"
      "v_mfma_f32_16x16x32_bf16 v[128:131], a[224:227], v[60:63], v[128:131]\n"
      "v_max_f32 v217, 0x3727c5ac, v217\n"
      "v_mfma_f32_16x16x32_bf16 v[128:131], a[228:231], v[64:67], v[128:131]\n"
      "v_fma_f32 v125, -v222, v141, v125\n"
      "v_mfma_f32_16x16x32_bf16 v[128:131], a[232:235], v[68:71], v[128:131]\n"
      "v_fma_f32 v126, -v222, v142, v126\n"
      "v_mfma_f32_16x16x32_bf16 v[128:131], a[236:239], v[72:75], v[128:131]\n"
      "s_waitcnt vmcnt(6)\n"
      "v_fma_f32 v124, -v222, v140, v124\n"
      "v_cmp_ne_u32 vcc, s21, v17\n"
      "v_mfma_f32_16x16x32_bf16 v[128:131], a[240:243], v[76:79], v[128:131]\n"
      "s_add_u32 m0, s53, s41\n"
      "v_rsq_f32 v223, v217\n"
      "v_mfma_f32_16x16x32_bf16 v[128:131], a[244:247], v[80:83], v[128:131]\n"
      "s_add_u32 s45, s36, s47\n"
      "v_mul_f32 v124, v223, v124\n"
      "v_mfma_f32_16x16x32_bf16 v[128:131], a[248:251], v[84:87], v[128:131]\n"
      "v_fma_f32 v127, -v222, v143, v127\n"
      "v_mfma_f32_16x16x32_bf16 v[128:131], a[252:255], v[88:91], v[128:131]\n"
      "v_cndmask_b32 v237, v223, 1.0, s[58:59]\n"
      "v_mfma_f32_16x16x32_bf16 v[128:131], v[20:23], v[92:95], v[128:131]\n"
      "ds_write_b64 v5, v[222:223]\n"
      "v_mfma_f32_16x16x32_bf16 v[128:131], v[24:27], v[96:99], v[128:131]\n"
      "ds_read_b128 v[224:227], v6\n"
      "v_mfma_f32_16x16x32_bf16 v[128:131], v[28:31], v[100:103], v[128:131]\n"
      "s_cbranch_vccnz .Lsp_q_6\n"
      ".Lbk_q_6:\n"
      "v_mul_f32 v125, v223, v125\n"
      "v_mfma_f32_16x16x32_bf16 v[128:131], v[32:35], v[104:107], v[128:131]\n"
      "v_mul_f32 v126, v223, v126\n"
      "v_mul_f32 v127, v223, v127\n"
      "v_mfma_f32_16x16x32_bf16 v[128:131], v[36:39], v[108:111], v[128:131]\n"
      "v_mov_b32 v186, v184\n"
      "v_mfma_f32_16x16x32_bf16 v[128:131], v[40:43], v[112:115], v[128:131]\n"
      "buffer_load_dwordx4 v0, s[8:11], s46 offen offset:0 sc1 lds\n"
      "v_mfma_f32_16x16x32_bf16 v[128:131], v[44:47], v[116:119], v[128:131]\n"
      "v_cvt_pk_bf16_f32 v176, v124, v125\n"
      "v_mfma_f32_16x16x32_bf16 v[128:131], v[48:51], v[120:123], v[128:131]\n"
      "v_mov_b32 v190, v188\n"
      "v_cvt_pk_bf16_f32 v177, v126, v127\n"
      "v_mfma_f32_16x16x32_bf16 v[132:135], v[52:55], a[72:75], 0\n"
      "s_waitcnt vmcnt(4) lgkmcnt(0)\n"
      "ds_write_b32 v11, v9\n"
      "v_mfma_f32_16x16x32_bf16 v[132:135], v[56:59], a[76:79], v[132:135]\n"
      "buffer_load_dwordx4 v0, s[8:11], s46 offen offset:1024 sc1 lds\n"
      "v_mfma_f32_16x16x32_bf16 v[132:135], v[60:63], a[80:83], v[132:135]\n"
      "v_fma_f32 v129, -v222, v145, v129\n"
      "v_fma_f32 v131, -v222, v147, v131\n"
      "v_mfma_f32_16x16x32_bf16 v[132:135], v[64:67], a[84:87], v[132:135]\n"
      "v_fma_f32 v128, -v222, v144, v128\n"
      "v_mul_f32 v129, v237, v129\n"
      "v_mfma_f32_16x16x32_bf16 v[132:135], v[68:71], a[88:91], v[132:135]\n"
      "v_mul_f32 v128, v237, v128\n"
      "v_mul_f32 v131, v223, v131\n"
      "v_mfma_f32_16x16x32_bf16 v[132:135], v[72:75], a[92:95], v[132:135]\n"
      "v_fma_f32 v130, -v222, v146, v130\n"
      "v_mul_f32 v130, v223, v130\n"
      "v_mfma_f32_16x16x32_bf16 v[132:135], v[76:79], a[96:99], v[132:135]\n"
      "buffer_load_dword v17, v2, s[8:11], s45 offen sc1\n"
      "v_cvt_pk_bf16_f32 v178, v130, v131\n"
      "v_cvt_pk_bf16_f32 v179, v128, v129\n"
      "v_mfma_f32_16x16x32_bf16 v[132:135], v[80:83], a[100:103], v[132:135]\n"
      "buffer_load_dwordx4 v0, s[8:11], s46 offen offset:2048 sc1 lds\n"
      "v_mfma_f32_16x16x32_bf16 v[196:199], v[176:179], v[180:183], v[160:163]\n"
      "ds_read_b128 v[228:231], v6 offset:16\n"
      "v_mov_b32 v187, v185\n"
      "v_mfma_f32_16x16x32_bf16 v[200:203], v[176:179], v[168:171], v[172:175]\n"
      "ds_read_b32 v10, v12\n"
      "v_mfma_f32_16x16x32_bf16 v[132:135], v[84:87], a[104:107], v[132:135]\n"
      "v_mov_b32 v191, v189\n"
      "v_mfma_f32_16x16x32_bf16 v[132:135], v[88:91], a[108:111], v[132:135]\n"
      "s_add_u32 s55, s55, 24576\n"
      "v_med3_f32 v239, v198, v199, s52\n"
      "v_med3_f32 v238, v196, v197, s52\n"
      "v_mfma_f32_16x16x32_bf16 v[132:135], v[92:95], a[112:115], v[132:135]\n"
      "buffer_load_dwordx4 v0, s[8:11], s46 offen offset:3072 sc1 lds\n"
      "v_mfma_f32_16x16x32_bf16 v[132:135], v[96:99], a[116:119], v[132:135]\n"
      "s_mov_b32 s52, s51\n"
      "v_max3_f32 v238, v192, v193, v238\n"
      "v_max3_f32 v239, v194, v195, v239\n"
      "v_mfma_f32_16x16x32_bf16 v[132:135], v[100:103], a[120:123], v[132:135]\n"
      "v_max_f32 v240, v238, v239\n"
      "v_cvt_pk_bf16_f32 v189, v202, v203\n"
      "v_mov_b32 v241, v240\n"
      "v_mfma_f32_16x16x32_bf16 v[132:135], v[104:107], a[124:127], v[132:135]\n"
      "s_add_u32 m0, s53, s42\n"
      "v_permlane16_swap_b32 v240, v241\n"
      "v_mfma_f32_16x16x32_bf16 v[132:135], v[108:111], a[128:131], v[132:135]\n"
      "s_waitcnt lgkmcnt(0)\n"
      "v_max_f32 v240, v240, v241\n"
      "v_mov_b32 v241, v240\n"
      "v_cmp_gt_i32 vcc, v9, v10\n"
      "v_mfma_f32_16x16x32_bf16 v[132:135], v[112:115], a[132:135], v[132:135]\n"
      "v_permlane32_swap_b32 v240, v241\n"
      "v_mfma_f32_16x16x32_bf16 v[132:135], v[116:119], a[136:139], v[132:135]\n"
      "buffer_load_dwordx4 v1, s[8:11], s46 offen offset:0 sc1 lds\n"
      "v_max_f32 v240, v240, v241\n"
      "v_cvt_pk_bf16_f32 v188, v200, v201\n"
      "v_mfma_f32_16x16x32_bf16 v[132:135], v[120:123], a[140:143], v[132:135]\n"
      "v_mul_f32 v240, v240, v236\n"
      "v_fma_f32 v238, v238, v236, -v240\n"
      "v_mfma_f32_16x16x32_bf16 v[136:139], a[144:147], v[52:55], 0\n"
      "s_cbranch_vccnz .Lsp_qy_7\n"
      ".Lbk_qy_7:\n"
      "v_fma_f32 v239, v239, v236, -v240\n"
      "v_exp_f32 v238, v238\n"
      "v_mfma_f32_16x16x32_bf16 v[136:139], a[148:151], v[56:59], v[136:139]\n"
      "ds_read_b128 v[56:59], v15 offset:1024\n"
      "v_cvt_pk_bf16_f32 v238, v238, v238\n"
      "v_mfma_f32_16x16x32_bf16 v[136:139], a[152:155], v[60:63], v[136:139]\n"
      "v_exp_f32 v239, v239\n"
      "v_mfma_f32_16x16x32_bf16 v[136:139], a[156:159], v[64:67], v[136:139]\n"
      "v_cvt_pk_bf16_f32 v239, v239, v239\n"
      "v_fma_f32 v132, -v224, v149, v132\n"
      "v_mul_f32 v132, v225, v132\n"
      "v_mfma_f32_16x16x32_bf16 v[136:139], a[160:163], v[68:71], v[136:139]\n"
      "v_fma_f32 v133, -v226, v150, v133\n"
      "v_fma_f32 v134, -v228, v151, v134\n"
      "v_mfma_f32_16x16x32_bf16 v[136:139], a[164:167], v[72:75], v[136:139]\n"
      "v_and_b32 v207, v167, v239\n"
      "v_fma_f32 v135, -v230, v148, v135\n"
      "v_mul_f32 v135, v231, v135\n"
      "v_mfma_f32_16x16x32_bf16 v[136:139], a[168:171], v[76:79], v[136:139]\n"
      "v_mul_f32 v134, v229, v134\n"
      "v_and_b32 v204, v164, v238\n"
      "v_and_b32 v205, v165, v239\n"
      "v_and_b32 v206, v166, v238\n"
      "v_mfma_f32_16x16x32_bf16 v[136:139], a[172:175], v[80:83], v[136:139]\n"
      "v_cvt_pk_bf16_f32 v185, v134, v135\n"
      "v_mul_f32 v133, v227, v133\n"
      "v_mfma_f32_16x16x32_bf16 v[136:139], a[176:179], v[84:87], v[136:139]\n"
      "ds_read_b128 v[80:83], v15 offset:7168\n"
      "v_cvt_pk_bf16_f32 v184, v132, v133\n"
      "v_mfma_f32_16x16x32_bf16 v[212:215], v[188:191], v[204:207], 0\n"
      "ds_read_b128 v[84:87], v15 offset:8192\n"
      "ds_read_b64 v[220:221], v16 offset:1024\n"
      "v_mfma_f32_16x16x32_bf16 v[208:211], v[184:187], v[204:207], 0\n"
      "s_mov_b32 s51, s50\n"
      "ds_read_b128 v[52:55], v15 offset:0\n"
      "ds_read_b128 v[60:63], v15 offset:2048\n"
      "v_mfma_f32_16x16x32_bf16 v[136:139], a[180:183], v[88:91], v[136:139]\n"
      "ds_read_b128 v[68:71], v15 offset:4096\n"
      "v_mov_b32 v182, v179\n"
      "v_mfma_f32_16x16x32_bf16 v[136:139], a[184:187], v[92:95], v[136:139]\n"
      "ds_read_b128 v[76:79], v15 offset:6144\n"
      "v_rcp_f32 v214, v214\n"
      "v_mfma_f32_16x16x32_bf16 v[136:139], a[188:191], v[96:99], v[136:139]\n"
      "s_mov_b32 s50, s49\n"
      "ds_read_b128 v[64:67], v15 offset:3072\n"
      "v_mul_f32 v213, v213, v214\n"
      "v_mfma_f32_16x16x32_bf16 v[136:139], a[192:195], v[100:103], v[136:139]\n"
      "ds_read_b128 v[88:91], v15 offset:9216\n"
      "v_mul_f32 v210, v210, v214\n"
      "v_mfma_f32_16x16x32_bf16 v[136:139], a[196:199], v[104:107], v[136:139]\n"
      "ds_read_b128 v[92:95], v15 offset:10240\n"
      "v_mul_f32 v208, v208, v214\n"
      "v_mfma_f32_16x16x32_bf16 v[136:139], a[200:203], v[108:111], v[136:139]\n"
      "ds_read_b128 v[100:103], v15 offset:12288\n"
      "ds_read2_b64 v[216:219], v16 offset1:64\n"
      "v_mfma_f32_16x16x32_bf16 v[136:139], a[204:207], v[112:115], v[136:139]\n"
      "s_add_u32 s40, s40, 24576\n"
      "ds_read_b128 v[72:75], v15 offset:5120\n"
      "v_mul_f32 v211, v211, v214\n"
      "v_mfma_f32_16x16x32_bf16 v[136:139], a[208:211], v[116:119], v[136:139]\n"
      "s_waitcnt lgkmcnt(7)\n"
      "ds_read_b128 v[108:111], v15 offset:14336\n"
      "v_mul_f32 v212, v212, v214\n"
      "v_mfma_f32_16x16x32_bf16 v[136:139], a[212:215], v[120:123], v[136:139]\n"
      "s_sub_u32 s20, s20, 1\n"
      "s_cbranch_scc0 .Lq_loop\n"
      "v_mul_f32 v209, v209, v214\n"
      "s_waitcnt vmcnt(6)\n"
      "buffer_store_dword v13, v3, s[8:11], s55 offen sc1\n"
      "s_nop 5\n"
      "v_fma_f32 v137, -v222, v153, v137\n"
      "v_fma_f32 v139, -v222, v155, v139\n"
      "v_fma_f32 v138, -v222, v154, v138\n"
      "v_cvt_pk_bf16_f32 v232, v208, v209\n"
      "v_fma_f32 v136, -v222, v152, v136\n"
      "v_cvt_pk_bf16_f32 v181, v138, v139\n"
      "v_cvt_pk_bf16_f32 v180, v136, v137\n"
      "s_nop 1\n"
      "v_mfma_f32_16x16x32_bf16 v[192:195], v[176:179], v[180:183], v[156:159]\n"
      "v_cvt_pk_bf16_f32 v234, v212, v213\n"
      "buffer_store_dwordx2 v[234:235], v8, s[8:11], s40 offen sc1\n"
      "v_cvt_pk_bf16_f32 v233, v210, v211\n"
      "buffer_store_dwordx2 v[232:233], v7, s[8:11], s40 offen sc1\n"
      "v_mul_f32 v236, 0x3eae1a93, v223\n"
      "v_mov_b32 v186, v184\n"
      "v_mov_b32 v190, v188\n"
      "v_mov_b32 v187, v185\n"
      "v_mov_b32 v191, v189\n"
      "s_add_u32 s55, s55, 24576\n"
      "s_add_u32 s40, s40, 24576\n"
      "s_waitcnt vmcnt(0)\n"
      "buffer_store_dword v13, v3, s[8:11], s55 offen sc1\n"
      "s_add_u32 s55, s55, 24576\n"
      "buffer_store_dword v13, v3, s[8:11], s57 offen sc1\n"
      "s_add_u32 s57, s57, 24576\n"
      "buffer_store_dword v13, v3, s[8:11], s57 offen sc1\n"
      "s_add_u32 s57, s57, 24576\n"
      "buffer_store_dword v13, v3, s[8:11], s57 offen sc1\n"
      "s_add_u32 s57, s57, 24576\n"
      "s_cmp_lg_u32 s44, 0\n"
      "s_cbranch_scc1 .Lq_done\n"
      "v_lshlrev_b32 v52, 3, v0\n"
      "s_mov_b32 s22, s23\n"
      ".Lq_up:\n"
      "s_mov_b32 s30, 128\n"
      "buffer_load_dword v53, v52, s[8:11], s30 offen sc1\n"
      "s_mov_b32 s30, 8320\n"
      "buffer_load_dword v54, v52, s[8:11], s30 offen sc1\n"
      "s_mov_b32 s30, 16512\n"
      "buffer_load_dword v55, v52, s[8:11], s30 offen sc1\n"
      "s_mov_b32 s30, 24704\n"
      "buffer_load_dword v56, v52, s[8:11], s30 offen sc1\n"
      "s_waitcnt vmcnt(0)\n"
      "v_xor_b32 v53, s21, v53\n"
      "v_xor_b32 v54, s21, v54\n"
      "v_xor_b32 v55, s21, v55\n"
      "v_xor_b32 v56, s21, v56\n"
      "v_or3_b32 v53, v53, v54, v55\n"
      "v_or_b32 v53, v53, v56\n"
      "v_cmp_ne_u32 vcc, 0, v53\n"
      "s_cbranch_vccz .Lq_ok\n"
      "s_sleep 4\n"
      "s_sub_u32 s22, s22, 1\n"
      "s_cbranch_scc0 .Lq_up\n"
      ".Lq_ok:\n"
      "v_lshrrev_b32 v52, 2, v0\n"
      "v_and_b32 v52, 0x7c, v52\n"
      "v_mov_b32 v53, s21\n"
      "buffer_store_dword v53, v52, s[8:11], 0 offen sc1\n"
      ".Lq_done:\n"
      "s_endpgm\n"
      ".Lsp_q_1:\n"
      "s_mov_b32 s22, s23\n"
      ".Lsl_q_1:\n"
      "buffer_load_dword v17, v2, s[8:11], s46 offen sc1\n"
      "s_waitcnt vmcnt(0)\n"
      "v_cmp_ne_u32 vcc, s21, v17\n"
      "s_cbranch_vccz .Lbk_q_1\n"
      "s_sleep 4\n"
      "s_sub_u32 s22, s22, 1\n"
      "s_cbranch_scc0 .Lsl_q_1\n"
      "s_mov_b32 s23, 0\n"
      "s_branch .Lbk_q_1\n"
      ".Lsp_q_2:\n"
      "s_mov_b32 s22, s23\n"
      ".Lsl_q_2:\n"
      "buffer_load_dword v17, v2, s[8:11], s46 offen sc1\n"
      "s_waitcnt vmcnt(0)\n"
      "v_cmp_ne_u32 vcc, s21, v17\n"
      "s_cbranch_vccz .Lbk_q_2\n"
      "s_sleep 4\n"
      "s_sub_u32 s22, s22, 1\n"
      "s_cbranch_scc0 .Lsl_q_2\n"
      "s_mov_b32 s23, 0\n"
      "s_branch .Lbk_q_2\n"
      ".Lsp_qy_3:\n"
      "s_lshl_b32 s22, s23, 4\n"
      ".Lsl_qy_3:\n"
      "ds_read_b32 v10, v12\n"
      "s_waitcnt lgkmcnt(0)\n"
      "v_cmp_gt_i32 vcc, v9, v10\n"
      "s_cbranch_vccz .Lbk_qy_3\n"
      "s_sub_u32 s22, s22, 1\n"
      "s_cbranch_scc0 .Lsl_qy_3\n"
      "s_mov_b32 s23, 0\n"
      "s_branch .Lbk_qy_3\n"
      ".Lsp_q_4:\n"
      "s_mov_b32 s22, s23\n"
      "buffer_load_dword v17, v2, s[8:11], s46 offen sc1\n"
      "s_waitcnt vmcnt(0)\n"
      "v_cmp_ne_u32 vcc, s21, v17\n"
      "s_add_u32 s30, s55, 24576\n"
      "buffer_store_dword v13, v3, s[8:11], s30 offen sc1\n"
      "s_cbranch_vccz .Lbk_q_4\n"
      ".Lsl_q_4:\n"
      "buffer_load_dword v17, v2, s[8:11], s46 offen sc1\n"
      "s_waitcnt vmcnt(0)\n"
      "v_cmp_ne_u32 vcc, s21, v17\n"
      "s_cbranch_vccz .Lbk_q_4\n"
      "s_sleep 4\n"
      "s_sub_u32 s22, s22, 1\n"
      "s_cbranch_scc0 .Lsl_q_4\n"
      "s_mov_b32 s23, 0\n"
      "s_branch .Lbk_q_4\n"
      ".Lsp_qy_5:\n"
      "s_lshl_b32 s22, s23, 4\n"
      ".Lsl_qy_5:\n"
      "ds_read_b32 v10, v12\n"
      "s_waitcnt lgkmcnt(0)\n"
      "v_cmp_gt_i32 vcc, v9, v10\n"
      "s_cbranch_vccz .Lbk_qy_5\n"
      "s_sub_u32 s22, s22, 1\n"
      "s_cbranch_scc0 .Lsl_qy_5\n"
      "s_mov_b32 s23, 0\n"
      "s_branch .Lbk_qy_5\n"
      ".Lsp_q_6:\n"
      "s_mov_b32 s22, s23\n"
      "buffer_load_dword v17, v2, s[8:11], s46 offen sc1\n"
      "s_waitcnt vmcnt(0)\n"
      "v_cmp_ne_u32 vcc, s21, v17\n"
      "s_add_u32 s30, s55, 24576\n"
      "buffer_store_dword v13, v3, s[8:11], s30 offen sc1\n"
      "s_cbranch_vccz .Lbk_q_6\n"
      ".Lsl_q_6:\n"
      "buffer_load_dword v17, v2, s[8:11], s46 offen sc1\n"
      "s_waitcnt vmcnt(0)\n"
      "v_cmp_ne_u32 vcc, s21, v17\n"
      "s_cbranch_vccz .Lbk_q_6\n"
      "s_sleep 4\n"
      "s_sub_u32 s22, s22, 1\n"
      "s_cbranch_scc0 .Lsl_q_6\n"
      "s_mov_b32 s23, 0\n"
      "s_branch .Lbk_q_6\n"
      ".Lsp_qy_7:\n"
      "s_lshl_b32 s22, s23, 4\n"
      ".Lsl_qy_7:\n"
      "ds_read_b32 v10, v12\n"
      "s_waitcnt lgkmcnt(0)\n"
      "v_cmp_gt_i32 vcc, v9, v10\n"
      "s_cbranch_vccz .Lbk_qy_7\n"
      "s_sub_u32 s22, s22, 1\n"
      "s_cbranch_scc0 .Lsl_qy_7\n"
      "s_mov_b32 s23, 0\n"
      "s_branch .Lbk_qy_7\n"
      ".Lq_last:\n"
      :
      : "{s[8:11]}"(rsWS), "{s[12:15]}"(rsA), "{v0}"(v_l16), "{v1}"(v_l5), "{v2}"(v_fa), "{v3}"(v_ra), "{v4}"(v_st), "{v5}"(v_tw), "{v6}"(v_tr), "{v7}"(v_om), "{v8}"(v_oe), "{v10}"(v_al), "{v11}"(v_yw), "{v12}"(v_yr), "{v156}"(v_bc[0]), "{v157}"(v_bc[1]), "{v158}"(v_bc[2]), "{v159}"(v_bc[3]), "{v160}"(v_bp[0]), "{v161}"(v_bp[1]), "{v162}"(v_bp[2]), "{v163}"(v_bp[3]), "{v164}"(v_mk[0]), "{v165}"(v_mk[1]), "{v166}"(v_mk[2]), "{v167}"(v_mk[3]), "{v168}"(v_sel[0]), "{v169}"(v_sel[1]), "{v170}"(v_sel[2]), "{v171}"(v_sel[3]), "{v172}"(v_ci[0]), "{v173}"(v_ci[1]), "{v174}"(v_ci[2]), "{v175}"(v_ci[3]), "{v52}"(v_vo[0]), "{v53}"(v_vo[1]), "{v54}"(v_vo[2]), "{v55}"(v_vo[3]), "{v56}"(v_at), "{v57}"(v_ax), "{v58}"(v_pm), "{v59}"(v_ms), "{v80}"(v_pw), "{v81}"(v_hi), "{s36}"(s_sx), "{s37}"(s_ev), "{s38}"(s_del), "{s39}"(s_kil), "{s40}"(s_so), "{s41}"(s_m4), "{s42}"(s_m5), "{s43}"(s_ms), "{s44}"(s_up)
      : "v255", "a255", "s99", "vcc", "scc", "memory");

  } else if (slot == 4) {
    const unsigned gr = QMAP == 2 ? 3 - (xcd & 3) : xcd >> 1, c = QMAP == 2 ? xcd >> 2 : xcd & 1, o = w & 1;
    const u32x4 rsA = srd(Wo, 576 * 576 * 2);
    const unsigned v_l5 = 16384 - w * 4096 + w * 1024 + p * 128 + (lane >> 2) * 8, v_l6 = v_l5 + 256;
    const unsigned v_fa = 20480 - w * 4096 + (8 * w + (lane >> 3)) * 128 + (lane & 7) * 4, v_ra = line + 19456 + (4 * c + w) * 128;
    const unsigned v_b0 = v_l16 + o * 9216, v_b1 = v_l16 + 9216 - o * 9216;
    const unsigned v_xp = (9 * c + 2 * w) * 1024 + v_l16, v_xs = o ? OOB : (9 * c + 8) * 1024 + (w >> 1) * 512 + half;
    const unsigned v_rp = v_xp + WS_XIN, v_rs = o ? OOB : v_xs + WS_XIN;
    const unsigned v_ew = 65536 + w * 1024 + v_l16, v_er = 65536 + (w ^ 1) * 1024 + v_l16;
    const unsigned v_sw = 69632 + i16 * 136 + (4 * w + g) * 8, v_sr = 69632 + (4 * w + g) * 136 + i16 * 8;
    const unsigned v_ss = 18432 + c * 512 + w * 128 + g * 32 + p * 8;
    const unsigned v_pw = 19456 + (4 * c + w) * 128 + l8 * REC + l7;
    unsigned v_vo[9], v_vp[9];
#pragma unroll
    for (unsigned j = 0; j < 9; ++j) {
      const unsigned ch = 64 * j + lane, r = ch / 36, x = 576 * c + ch % 36 * 16 - 1024 * (j & 3);
      v_vo[j] = (20 * o + r) * 1152 + x;
      v_vp[j] = w < 2 ? v_vo[j] : (36 * (4 * o + (r >> 2)) + 16 + (r & 3)) * 1152 + x;
    }
    const unsigned ro = (8 * g + q) * 576;
    const unsigned v_t0 = ro + o * 18432 + 128 * w + 16 * p, v_t1 = v_t0 + 18432 - 2 * o * 18432, v_ts = ro + o * 18432 + 512 + 32 * (w >> 1) + 8 * p;
    const unsigned s_sx = WS_OB + gr * NREC * REC + w * 4096, s_ev = s_sx + 31 * REC, s_sy = WS_X1 + gr * NREC * REC;
    const unsigned s_m4 = w * 4096, s_m5 = 16384 + w * 512, s_m9 = w * 9216, s_so = (w >> 1) * 9 * 41472;
    const unsigned s_s7 = w < 2 ? 7 * 41472 : 0, s_s8 = 8 * 41472, s_wg = o ? 0u : 0x3f800000u, s_rb = WS_OB + w * 4096;
    asm volatile(
      "s_buffer_load_dword s21, s[8:11], 0\n"
      "s_mov_b32 s23, 65536\n"
      "s_mov_b32 s31, 0x40000000\n"
      "s_add_u32 m0, s41, 0\n"
      "s_nop 0\n"
      "buffer_load_dwordx4 v96, s[12:15], s42 offen offset:0 lds\n"
      "buffer_load_dwordx4 v97, s[12:15], s42 offen offset:1024 lds\n"
      "buffer_load_dwordx4 v98, s[12:15], s42 offen offset:2048 lds\n"
      "buffer_load_dwordx4 v99, s[12:15], s42 offen offset:3072 lds\n"
      "s_add_u32 m0, s41, 4096\n"
      "s_nop 0\n"
      "buffer_load_dwordx4 v100, s[12:15], s42 offen offset:0 lds\n"
      "buffer_load_dwordx4 v101, s[12:15], s42 offen offset:1024 lds\n"
      "buffer_load_dwordx4 v102, s[12:15], s42 offen offset:2048 lds\n"
      "buffer_load_dwordx4 v103, s[12:15], s42 offen offset:3072 lds\n"
      "s_add_u32 m0, s41, 8192\n"
      "s_nop 0\n"
      "buffer_load_dwordx4 v104, s[12:15], s42 offen offset:0 lds\n"
      "s_add_u32 s42, s42, 41472\n"
      "s_add_u32 m0, s41, 36864\n"
      "s_nop 0\n"
      "buffer_load_dwordx4 v96, s[12:15], s42 offen offset:0 lds\n"
      "buffer_load_dwordx4 v97, s[12:15], s42 offen offset:1024 lds\n"
      "buffer_load_dwordx4 v98, s[12:15], s42 offen offset:2048 lds\n"
      "buffer_load_dwordx4 v99, s[12:15], s42 offen offset:3072 lds\n"
      "s_add_u32 m0, s41, 40960\n"
      "s_nop 0\n"
      "buffer_load_dwordx4 v100, s[12:15], s42 offen offset:0 lds\n"
      "buffer_load_dwordx4 v101, s[12:15], s42 offen offset:1024 lds\n"
      "buffer_load_dwordx4 v102, s[12:15], s42 offen offset:2048 lds\n"
      "buffer_load_dwordx4 v103, s[12:15], s42 offen offset:3072 lds\n"
      "s_add_u32 m0, s41, 45056\n"
      "s_nop 0\n"
      "buffer_load_dwordx4 v104, s[12:15], s42 offen offset:0 lds\n"
      "s_add_u32 s42, s42, 41472\n"
      "s_add_u32 m0, s41, 73728\n"
      "s_nop 0\n"
      "buffer_load_dwordx4 v96, s[12:15], s42 offen offset:0 lds\n"
      "buffer_load_dwordx4 v97, s[12:15], s42 offen offset:1024 lds\n"
      "buffer_load_dwordx4 v98, s[12:15], s42 offen offset:2048 lds\n"
      "buffer_load_dwordx4 v99, s[12:15], s42 offen offset:3072 lds\n"
      "s_add_u32 m0, s41, 77824\n"
      "s_nop 0\n"
      "buffer_load_dwordx4 v100, s[12:15], s42 offen offset:0 lds\n"
      "buffer_load_dwordx4 v101, s[12:15], s42 offen offset:1024 lds\n"
      "buffer_load_dwordx4 v102, s[12:15], s42 offen offset:2048 lds\n"
      "buffer_load_dwordx4 v103, s[12:15], s42 offen offset:3072 lds\n"
      "s_add_u32 m0, s41, 81920\n"
      "s_nop 0\n"
      "buffer_load_dwordx4 v104, s[12:15], s42 offen offset:0 lds\n"
      "s_add_u32 s42, s42, 41472\n"
      "s_waitcnt vmcnt(18)\n"
      "s_barrier\n"
      "ds_read_b64_tr_b16 v[60:61], v116 offset:0\n"
      "ds_read_b64_tr_b16 v[62:63], v116 offset:2304\n"
      "ds_read_b64_tr_b16 a[0:1], v114 offset:0\n"
      "ds_read_b64_tr_b16 a[2:3], v114 offset:2304\n"
      "ds_read_b64_tr_b16 a[72:73], v114 offset:8\n"
      "ds_read_b64_tr_b16 a[74:75], v114 offset:2312\n"
      "ds_read_b64_tr_b16 a[144:145], v114 offset:64\n"
      "ds_read_b64_tr_b16 a[146:147], v114 offset:2368\n"
      "ds_read_b64_tr_b16 a[216:217], v114 offset:72\n"
      "ds_read_b64_tr_b16 a[218:219], v114 offset:2376\n"
      "ds_read_b64_tr_b16 a[36:37], v115 offset:0\n"
      "ds_read_b64_tr_b16 a[38:39], v115 offset:2304\n"
      "ds_read_b64_tr_b16 a[108:109], v115 offset:8\n"
      "ds_read_b64_tr_b16 a[110:111], v115 offset:2312\n"
      "ds_read_b64_tr_b16 a[180:181], v115 offset:64\n"
      "ds_read_b64_tr_b16 a[182:183], v115 offset:2368\n"
      "ds_read_b64_tr_b16 a[252:253], v115 offset:72\n"
      "ds_read_b64_tr_b16 a[254:255], v115 offset:2376\n"
      "v_add_u32 v114, 36864, v114\n"
      "v_add_u32 v115, 36864, v115\n"
      "v_add_u32 v116, 36864, v116\n"
      "s_add_u32 m0, s41, 110592\n"
      "s_nop 0\n"
      "buffer_load_dwordx4 v96, s[12:15], s42 offen offset:0 lds\n"
      "buffer_load_dwordx4 v97, s[12:15], s42 offen offset:1024 lds\n"
      "buffer_load_dwordx4 v98, s[12:15], s42 offen offset:2048 lds\n"
      "buffer_load_dwordx4 v99, s[12:15], s42 offen offset:3072 lds\n"
      "s_add_u32 m0, s41, 114688\n"
      "s_nop 0\n"
      "buffer_load_dwordx4 v100, s[12:15], s42 offen offset:0 lds\n"
      "buffer_load_dwordx4 v101, s[12:15], s42 offen offset:1024 lds\n"
      "buffer_load_dwordx4 v102, s[12:15], s42 offen offset:2048 lds\n"
      "buffer_load_dwordx4 v103, s[12:15], s42 offen offset:3072 lds\n"
      "s_add_u32 m0, s41, 118784\n"
      "s_nop 0\n"
      "buffer_load_dwordx4 v104, s[12:15], s42 offen offset:0 lds\n"
      "s_add_u32 s42, s42, 41472\n"
      "s_waitcnt vmcnt(18) lgkmcnt(0)\n"
      "s_barrier\n"
      "ds_read_b64_tr_b16 v[64:65], v116 offset:0\n"
      "ds_read_b64_tr_b16 v[66:67], v116 offset:2304\n"
      "ds_read_b64_tr_b16 a[4:5], v114 offset:0\n"
      "ds_read_b64_tr_b16 a[6:7], v114 offset:2304\n"
      "ds_read_b64_tr_b16 a[76:77], v114 offset:8\n"
      "ds_read_b64_tr_b16 a[78:79], v114 offset:2312\n"
      "ds_read_b64_tr_b16 a[148:149], v114 offset:64\n"
      "ds_read_b64_tr_b16 a[150:151], v114 offset:2368\n"
      "ds_read_b64_tr_b16 a[220:221], v114 offset:72\n"
      "ds_read_b64_tr_b16 a[222:223], v114 offset:2376\n"
      "ds_read_b64_tr_b16 a[40:41], v115 offset:0\n"
      "ds_read_b64_tr_b16 a[42:43], v115 offset:2304\n"
      "ds_read_b64_tr_b16 a[112:113], v115 offset:8\n"
      "ds_read_b64_tr_b16 a[114:115], v115 offset:2312\n"
      "ds_read_b64_tr_b16 a[184:185], v115 offset:64\n"
      "ds_read_b64_tr_b16 a[186:187], v115 offset:2368\n"
      "ds_read_b64_tr_b16 v[28:29], v115 offset:72\n"
      "ds_read_b64_tr_b16 v[30:31], v115 offset:2376\n"
      "v_add_u32 v114, 36864, v114\n"
      "v_add_u32 v115, 36864, v115\n"
      "v_add_u32 v116, 36864, v116\n"
      "s_add_u32 m0, s41, 0\n"
      "s_nop 0\n"
      "buffer_load_dwordx4 v96, s[12:15], s42 offen offset:0 lds\n"
      "buffer_load_dwordx4 v97, s[12:15], s42 offen offset:1024 lds\n"
      "buffer_load_dwordx4 v98, s[12:15], s42 offen offset:2048 lds\n"
      "buffer_load_dwordx4 v99, s[12:15], s42 offen offset:3072 lds\n"
      "s_add_u32 m0, s41, 4096\n"
      "s_nop 0\n"
      "buffer_load_dwordx4 v100, s[12:15], s42 offen offset:0 lds\n"
      "buffer_load_dwordx4 v101, s[12:15], s42 offen offset:1024 lds\n"
      "buffer_load_dwordx4 v102, s[12:15], s42 offen offset:2048 lds\n"
      "buffer_load_dwordx4 v103, s[12:15], s42 offen offset:3072 lds\n"
      "s_add_u32 m0, s41, 8192\n"
      "s_nop 0\n"
      "buffer_load_dwordx4 v104, s[12:15], s42 offen offset:0 lds\n"
      "s_add_u32 s42, s42, 41472\n"
      "s_waitcnt vmcnt(18) lgkmcnt(0)\n"
      "s_barrier\n"
      "ds_read_b64_tr_b16 v[68:69], v116 offset:0\n"
      "ds_read_b64_tr_b16 v[70:71], v116 offset:2304\n"
      "ds_read_b64_tr_b16 a[8:9], v114 offset:0\n"
      "ds_read_b64_tr_b16 a[10:11], v114 offset:2304\n"
      "ds_read_b64_tr_b16 a[80:81], v114 offset:8\n"
      "ds_read_b64_tr_b16 a[82:83], v114 offset:2312\n"
      "ds_read_b64_tr_b16 a[152:153], v114 offset:64\n"
      "ds_read_b64_tr_b16 a[154:155], v114 offset:2368\n"
      "ds_read_b64_tr_b16 a[224:225], v114 offset:72\n"
      "ds_read_b64_tr_b16 a[226:227], v114 offset:2376\n"
      "ds_read_b64_tr_b16 a[44:45], v115 offset:0\n"
      "ds_read_b64_tr_b16 a[46:47], v115 offset:2304\n"
      "ds_read_b64_tr_b16 a[116:117], v115 offset:8\n"
      "ds_read_b64_tr_b16 a[118:119], v115 offset:2312\n"
      "ds_read_b64_tr_b16 a[188:189], v115 offset:64\n"
      "ds_read_b64_tr_b16 a[190:191], v115 offset:2368\n"
      "ds_read_b64_tr_b16 v[32:33], v115 offset:72\n"
      "ds_read_b64_tr_b16 v[34:35], v115 offset:2376\n"
      "v_add_u32 v114, 36864, v114\n"
      "v_add_u32 v115, 36864, v115\n"
      "v_add_u32 v116, 36864, v116\n"
      "s_add_u32 m0, s41, 36864\n"
      "s_nop 0\n"
      "buffer_load_dwordx4 v96, s[12:15], s42 offen offset:0 lds\n"
      "buffer_load_dwordx4 v97, s[12:15], s42 offen offset:1024 lds\n"
      "buffer_load_dwordx4 v98, s[12:15], s42 offen offset:2048 lds\n"
      "buffer_load_dwordx4 v99, s[12:15], s42 offen offset:3072 lds\n"
      "s_add_u32 m0, s41, 40960\n"
      "s_nop 0\n"
      "buffer_load_dwordx4 v100, s[12:15], s42 offen offset:0 lds\n"
      "buffer_load_dwordx4 v101, s[12:15], s42 offen offset:1024 lds\n"
      "buffer_load_dwordx4 v102, s[12:15], s42 offen offset:2048 lds\n"
      "buffer_load_dwordx4 v103, s[12:15], s42 offen offset:3072 lds\n"
      "s_add_u32 m0, s41, 45056\n"
      "s_nop 0\n"
      "buffer_load_dwordx4 v104, s[12:15], s42 offen offset:0 lds\n"
      "s_add_u32 s42, s42, 41472\n"
      "s_waitcnt vmcnt(18) lgkmcnt(0)\n"
      "s_barrier\n"
      "ds_read_b64_tr_b16 v[72:73], v116 offset:0\n"
      "ds_read_b64_tr_b16 v[74:75], v116 offset:2304\n"
      "ds_read_b64_tr_b16 a[12:13], v114 offset:0\n"
      "ds_read_b64_tr_b16 a[14:15], v114 offset:2304\n"
      "ds_read_b64_tr_b16 a[84:85], v114 offset:8\n"
      "ds_read_b64_tr_b16 a[86:87], v114 offset:2312\n"
      "ds_read_b64_tr_b16 a[156:157], v114 offset:64\n"
      "ds_read_b64_tr_b16 a[158:159], v114 offset:2368\n"
      "ds_read_b64_tr_b16 a[228:229], v114 offset:72\n"
      "ds_read_b64_tr_b16 a[230:231], v114 offset:2376\n"
      "ds_read_b64_tr_b16 a[48:49], v115 offset:0\n"
      "ds_read_b64_tr_b16 a[50:51], v115 offset:2304\n"
      "ds_read_b64_tr_b16 a[120:121], v115 offset:8\n"
      "ds_read_b64_tr_b16 a[122:123], v115 offset:2312\n"
      "ds_read_b64_tr_b16 a[192:193], v115 offset:64\n"
      "ds_read_b64_tr_b16 a[194:195], v115 offset:2368\n"
      "ds_read_b64_tr_b16 v[36:37], v115 offset:72\n"
      "ds_read_b64_tr_b16 v[38:39], v115 offset:2376\n"
      "v_add_u32 v114, 4294856704, v114\n"
      "v_add_u32 v115, 4294856704, v115\n"
      "v_add_u32 v116, 4294856704, v116\n"
      "s_add_u32 m0, s41, 73728\n"
      "s_nop 0\n"
      "buffer_load_dwordx4 v96, s[12:15], s42 offen offset:0 lds\n"
      "buffer_load_dwordx4 v97, s[12:15], s42 offen offset:1024 lds\n"
      "buffer_load_dwordx4 v98, s[12:15], s42 offen offset:2048 lds\n"
      "buffer_load_dwordx4 v99, s[12:15], s42 offen offset:3072 lds\n"
      "s_add_u32 m0, s41, 77824\n"
      "s_nop 0\n"
      "buffer_load_dwordx4 v100, s[12:15], s42 offen offset:0 lds\n"
      "buffer_load_dwordx4 v101, s[12:15], s42 offen offset:1024 lds\n"
      "buffer_load_dwordx4 v102, s[12:15], s42 offen offset:2048 lds\n"
      "buffer_load_dwordx4 v103, s[12:15], s42 offen offset:3072 lds\n"
      "s_add_u32 m0, s41, 81920\n"
      "s_nop 0\n"
      "buffer_load_dwordx4 v104, s[12:15], s42 offen offset:0 lds\n"
      "s_add_u32 s42, s42, 41472\n"
      "s_waitcnt vmcnt(18) lgkmcnt(0)\n"
      "s_barrier\n"
      "ds_read_b64_tr_b16 v[76:77], v116 offset:0\n"
      "ds_read_b64_tr_b16 v[78:79], v116 offset:2304\n"
      "ds_read_b64_tr_b16 a[16:17], v114 offset:0\n"
      "ds_read_b64_tr_b16 a[18:19], v114 offset:2304\n"
      "ds_read_b64_tr_b16 a[88:89], v114 offset:8\n"
      "ds_read_b64_tr_b16 a[90:91], v114 offset:2312\n"
      "ds_read_b64_tr_b16 a[160:161], v114 offset:64\n"
      "ds_read_b64_tr_b16 a[162:163], v114 offset:2368\n"
      "ds_read_b64_tr_b16 a[232:233], v114 offset:72\n"
      "ds_read_b64_tr_b16 a[234:235], v114 offset:2376\n"
      "ds_read_b64_tr_b16 a[52:53], v115 offset:0\n"
      "ds_read_b64_tr_b16 a[54:55], v115 offset:2304\n"
      "ds_read_b64_tr_b16 a[124:125], v115 offset:8\n"
      "ds_read_b64_tr_b16 a[126:127], v115 offset:2312\n"
      "ds_read_b64_tr_b16 a[196:197], v115 offset:64\n"
      "ds_read_b64_tr_b16 a[198:199], v115 offset:2368\n"
      "ds_read_b64_tr_b16 v[40:41], v115 offset:72\n"
      "ds_read_b64_tr_b16 v[42:43], v115 offset:2376\n"
      "v_add_u32 v114, 36864, v114\n"
      "v_add_u32 v115, 36864, v115\n"
      "v_add_u32 v116, 36864, v116\n"
      "s_add_u32 m0, s41, 110592\n"
      "s_nop 0\n"
      "buffer_load_dwordx4 v105, s[12:15], s43 offen offset:0 lds\n"
      "buffer_load_dwordx4 v106, s[12:15], s43 offen offset:1024 lds\n"
      "buffer_load_dwordx4 v107, s[12:15], s43 offen offset:2048 lds\n"
      "buffer_load_dwordx4 v108, s[12:15], s43 offen offset:3072 lds\n"
      "s_add_u32 m0, s41, 114688\n"
      "s_nop 0\n"
      "buffer_load_dwordx4 v109, s[12:15], s43 offen offset:0 lds\n"
      "buffer_load_dwordx4 v110, s[12:15], s43 offen offset:1024 lds\n"
      "buffer_load_dwordx4 v111, s[12:15], s43 offen offset:2048 lds\n"
      "buffer_load_dwordx4 v112, s[12:15], s43 offen offset:3072 lds\n"
      "s_add_u32 m0, s41, 118784\n"
      "s_nop 0\n"
      "buffer_load_dwordx4 v113, s[12:15], s43 offen offset:0 lds\n"
      "s_add_u32 s42, s42, 41472\n"
      "s_waitcnt vmcnt(18) lgkmcnt(0)\n"
      "s_barrier\n"
      "ds_read_b64_tr_b16 v[80:81], v116 offset:0\n"
      "ds_read_b64_tr_b16 v[82:83], v116 offset:2304\n"
      "ds_read_b64_tr_b16 a[20:21], v114 offset:0\n"
      "ds_read_b64_tr_b16 a[22:23], v114 offset:2304\n"
      "ds_read_b64_tr_b16 a[92:93], v114 offset:8\n"
      "ds_read_b64_tr_b16 a[94:95], v114 offset:2312\n"
      "ds_read_b64_tr_b16 a[164:165], v114 offset:64\n"
      "ds_read_b64_tr_b16 a[166:167], v114 offset:2368\n"
      "ds_read_b64_tr_b16 a[236:237], v114 offset:72\n"
      "ds_read_b64_tr_b16 a[238:239], v114 offset:2376\n"
      "ds_read_b64_tr_b16 a[56:57], v115 offset:0\n"
      "ds_read_b64_tr_b16 a[58:59], v115 offset:2304\n"
      "ds_read_b64_tr_b16 a[128:129], v115 offset:8\n"
      "ds_read_b64_tr_b16 a[130:131], v115 offset:2312\n"
      "ds_read_b64_tr_b16 a[200:201], v115 offset:64\n"
      "ds_read_b64_tr_b16 a[202:203], v115 offset:2368\n"
      "ds_read_b64_tr_b16 v[44:45], v115 offset:72\n"
      "ds_read_b64_tr_b16 v[46:47], v115 offset:2376\n"
      "v_add_u32 v114, 36864, v114\n"
      "v_add_u32 v115, 36864, v115\n"
      "v_add_u32 v116, 36864, v116\n"
      "s_add_u32 m0, s41, 0\n"
      "s_nop 0\n"
      "buffer_load_dwordx4 v105, s[12:15], s44 offen offset:0 lds\n"
      "buffer_load_dwordx4 v106, s[12:15], s44 offen offset:1024 lds\n"
      "buffer_load_dwordx4 v107, s[12:15], s44 offen offset:2048 lds\n"
      "buffer_load_dwordx4 v108, s[12:15], s44 offen offset:3072 lds\n"
      "s_add_u32 m0, s41, 4096\n"
      "s_nop 0\n"
      "buffer_load_dwordx4 v109, s[12:15], s44 offen offset:0 lds\n"
      "buffer_load_dwordx4 v110, s[12:15], s44 offen offset:1024 lds\n"
      "buffer_load_dwordx4 v111, s[12:15], s44 offen offset:2048 lds\n"
      "buffer_load_dwordx4 v112, s[12:15], s44 offen offset:3072 lds\n"
      "s_add_u32 m0, s41, 8192\n"
      "s_nop 0\n"
      "buffer_load_dwordx4 v113, s[12:15], s44 offen offset:0 lds\n"
      "s_add_u32 s42, s42, 41472\n"
      "s_waitcnt vmcnt(18) lgkmcnt(0)\n"
      "s_barrier\n"
      "ds_read_b64_tr_b16 v[84:85], v116 offset:0\n"
      "ds_read_b64_tr_b16 v[86:87], v116 offset:2304\n"
      "ds_read_b64_tr_b16 a[24:25], v114 offset:0\n"
      "ds_read_b64_tr_b16 a[26:27], v114 offset:2304\n"
      "ds_read_b64_tr_b16 a[96:97], v114 offset:8\n"
      "ds_read_b64_tr_b16 a[98:99], v114 offset:2312\n"
      "ds_read_b64_tr_b16 a[168:169], v114 offset:64\n"
      "ds_read_b64_tr_b16 a[170:171], v114 offset:2368\n"
      "ds_read_b64_tr_b16 a[240:241], v114 offset:72\n"
      "ds_read_b64_tr_b16 a[242:243], v114 offset:2376\n"
      "ds_read_b64_tr_b16 a[60:61], v115 offset:0\n"
      "ds_read_b64_tr_b16 a[62:63], v115 offset:2304\n"
      "ds_read_b64_tr_b16 a[132:133], v115 offset:8\n"
      "ds_read_b64_tr_b16 a[134:135], v115 offset:2312\n"
      "ds_read_b64_tr_b16 a[204:205], v115 offset:64\n"
      "ds_read_b64_tr_b16 a[206:207], v115 offset:2368\n"
      "ds_read_b64_tr_b16 v[48:49], v115 offset:72\n"
      "ds_read_b64_tr_b16 v[50:51], v115 offset:2376\n"
      "v_add_u32 v114, 36864, v114\n"
      "v_add_u32 v115, 36864, v115\n"
      "v_add_u32 v116, 36864, v116\n"
      "s_waitcnt vmcnt(9) lgkmcnt(0)\n"
      "s_barrier\n"
      "ds_read_b64_tr_b16 v[88:89], v116 offset:0\n"
      "ds_read_b64_tr_b16 v[90:91], v116 offset:2304\n"
      "ds_read_b64_tr_b16 a[28:29], v114 offset:0\n"
      "ds_read_b64_tr_b16 a[30:31], v114 offset:2304\n"
      "ds_read_b64_tr_b16 a[100:101], v114 offset:8\n"
      "ds_read_b64_tr_b16 a[102:103], v114 offset:2312\n"
      "ds_read_b64_tr_b16 a[172:173], v114 offset:64\n"
      "ds_read_b64_tr_b16 a[174:175], v114 offset:2368\n"
      "ds_read_b64_tr_b16 a[244:245], v114 offset:72\n"
      "ds_read_b64_tr_b16 a[246:247], v114 offset:2376\n"
      "ds_read_b64_tr_b16 a[64:65], v115 offset:0\n"
      "ds_read_b64_tr_b16 a[66:67], v115 offset:2304\n"
      "ds_read_b64_tr_b16 a[136:137], v115 offset:8\n"
      "ds_read_b64_tr_b16 a[138:139], v115 offset:2312\n"
      "ds_read_b64_tr_b16 a[208:209], v115 offset:64\n"
      "ds_read_b64_tr_b16 a[210:211], v115 offset:2368\n"
      "ds_read_b64_tr_b16 v[52:53], v115 offset:72\n"
      "ds_read_b64_tr_b16 v[54:55], v115 offset:2376\n"
      "v_add_u32 v114, 4294856704, v114\n"
      "v_add_u32 v115, 4294856704, v115\n"
      "v_add_u32 v116, 4294856704, v116\n"
      "s_waitcnt vmcnt(0) lgkmcnt(0)\n"
      "s_barrier\n"
      "ds_read_b64_tr_b16 v[92:93], v116 offset:0\n"
      "ds_read_b64_tr_b16 v[94:95], v116 offset:2304\n"
      "ds_read_b64_tr_b16 a[32:33], v114 offset:0\n"
      "ds_read_b64_tr_b16 a[34:35], v114 offset:2304\n"
      "ds_read_b64_tr_b16 a[104:105], v114 offset:8\n"
      "ds_read_b64_tr_b16 a[106:107], v114 offset:2312\n"
      "ds_read_b64_tr_b16 a[176:177], v114 offset:64\n"
      "ds_read_b64_tr_b16 a[178:179], v114 offset:2368\n"
      "ds_read_b64_tr_b16 a[248:249], v114 offset:72\n"
      "ds_read_b64_tr_b16 a[250:251], v114 offset:2376\n"
      "ds_read_b64_tr_b16 a[68:69], v115 offset:0\n"
      "ds_read_b64_tr_b16 a[70:71], v115 offset:2304\n"
      "ds_read_b64_tr_b16 a[140:141], v115 offset:8\n"
      "ds_read_b64_tr_b16 a[142:143], v115 offset:2312\n"
      "ds_read_b64_tr_b16 a[212:213], v115 offset:64\n"
      "ds_read_b64_tr_b16 a[214:215], v115 offset:2368\n"
      "ds_read_b64_tr_b16 v[56:57], v115 offset:72\n"
      "ds_read_b64_tr_b16 v[58:59], v115 offset:2376\n"
      "v_add_u32 v114, 36864, v114\n"
      "v_add_u32 v115, 36864, v115\n"
      "v_add_u32 v116, 36864, v116\n"
      "s_waitcnt lgkmcnt(0)\n"
      "s_mul_i32 s21, s21, 1664525\n"
      "s_add_u32 s21, s21, 1013904223\n"
      "v_mov_b32 v20, s21\n"
      "buffer_store_dword v20, v17, s[8:11], 0 offen sc1\n"
      "v_not_b32 v120, v20\n"
      "v_not_b32 v121, v20\n"
      "v_not_b32 v122, v20\n"
      "v_not_b32 v123, v20\n"
      "s_add_u32 s30, s38, 0\n"
      "buffer_store_dwordx4 v[120:123], v117, s[8:11], s30 offen sc1\n"
      "s_add_u32 s30, s38, 196608\n"
      "buffer_store_dwordx4 v[120:123], v117, s[8:11], s30 offen sc1\n"
      "s_add_u32 s30, s38, 393216\n"
      "buffer_store_dwordx4 v[120:123], v117, s[8:11], s30 offen sc1\n"
      "s_add_u32 s30, s38, 589824\n"
      "buffer_store_dwordx4 v[120:123], v117, s[8:11], s30 offen sc1\n"
      "s_add_u32 s30, s38, 786432\n"
      "buffer_store_dwordx4 v[120:123], v117, s[8:11], s30 offen sc1\n"
      "s_add_u32 s30, s38, 983040\n"
      "buffer_store_dwordx4 v[120:123], v117, s[8:11], s30 offen sc1\n"
      "s_add_u32 s30, s38, 1179648\n"
      "buffer_store_dwordx4 v[120:123], v117, s[8:11], s30 offen sc1\n"
      "s_add_u32 s30, s38, 1376256\n"
      "buffer_store_dwordx4 v[120:123], v117, s[8:11], s30 offen sc1\n"
      "s_add_u32 s30, s38, 1572864\n"
      "buffer_store_dword v20, v4, s[8:11], s30 offen sc1\n"
      "s_add_u32 s30, s38, 1597440\n"
      "buffer_store_dword v20, v4, s[8:11], s30 offen sc1\n"
      "s_add_u32 s30, s38, 1622016\n"
      "buffer_store_dword v20, v4, s[8:11], s30 offen sc1\n"
      "s_barrier\n"
      "v_mov_b32 v16, 0\n"
      "ds_write_b32 v18, v16\n"
      "s_waitcnt lgkmcnt(0)\n"
      "s_barrier\n"
      "s_add_u32 s36, s36, 4294942720\n"
      "s_mov_b32 s50, 40960\n"
      "s_mov_b32 s53, 24576\n"
      "s_add_u32 s52, s38, 4294918144\n"
      "s_add_u32 s51, s38, 4294918144\n"
      "s_add_u32 s38, s38, 4294942720\n"
      "s_cmp_eq_u32 s36, s37\n"
      "s_cselect_b32 s49, 49152, s53\n"
      "s_add_u32 s36, s36, s49\n"
      "buffer_load_dword v24, v3, s[8:11], s36 offen sc1\n"
      "s_mov_b32 s47, s36\n"
      "s_add_u32 s50, s50, 20480\n"
      "s_cmp_eq_u32 s36, s37\n"
      "s_cselect_b32 s49, 49152, s53\n"
      "s_add_u32 s36, s36, s49\n"
      "s_waitcnt vmcnt(0)\n"
      "v_cmp_ne_u32 vcc, s21, v24\n"
      "s_cmp_eq_u32 s50, 61440\n"
      "s_cselect_b32 s50, 0, s50\n"
      "s_add_u32 m0, s50, s39\n"
      "s_cbranch_vccnz .Lsp_w_1\n"
      ".Lbk_w_1:\n"
      "buffer_load_dwordx4 v0, s[8:11], s47 offen offset:0 sc1 lds\n"
      "buffer_load_dwordx4 v0, s[8:11], s47 offen offset:3072 sc1 lds\n"
      "buffer_load_dwordx4 v0, s[8:11], s47 offen offset:2048 sc1 lds\n"
      "buffer_load_dword v24, v3, s[8:11], s36 offen sc1\n"
      "buffer_load_dwordx4 v0, s[8:11], s47 offen offset:1024 sc1 lds\n"
      "s_add_u32 m0, s50, s40\n"
      "s_nop 0\n"
      "buffer_load_dword v1, s[8:11], s47 offen offset:0 sc1 lds\n"
      "buffer_load_dword v2, s[8:11], s47 offen offset:256 sc1 lds\n"
      "s_sub_u32 s48, s47, s46\n"
      "buffer_load_dwordx2 v[196:197], v8, s[8:11], s48 offen sc1\n"
      "v_add_u32 v22, s50, v5\n"
      "s_mov_b32 s47, s36\n"
      "v_add_u32 v23, s50, v6\n"
      "buffer_load_dwordx4 v[188:191], v7, s[8:11], s48 offen sc1\n"
      "s_add_u32 s50, s50, 20480\n"
      "buffer_load_dword v21, v18, s[8:11], s31 offen\n"
      "s_cmp_eq_u32 s36, s37\n"
      "s_cselect_b32 s49, 49152, s53\n"
      "s_add_u32 s36, s36, s49\n"
      "buffer_load_dwordx4 v[192:195], v7, s[8:11], s48 offen offset:1024 sc1\n"
      "s_waitcnt vmcnt(7)\n"
      "v_cmp_ne_u32 vcc, s21, v24\n"
      "s_cmp_eq_u32 s50, 61440\n"
      "s_cselect_b32 s50, 0, s50\n"
      "s_add_u32 m0, s50, s39\n"
      "s_cbranch_vccnz .Lsp_w_2\n"
      ".Lbk_w_2:\n"
      "buffer_load_dwordx4 v0, s[8:11], s47 offen offset:0 sc1 lds\n"
      "buffer_load_dwordx4 v0, s[8:11], s47 offen offset:3072 sc1 lds\n"
      "v_add_u32 v16, 1, v16\n"
      "buffer_load_dwordx4 v0, s[8:11], s47 offen offset:2048 sc1 lds\n"
      "s_waitcnt vmcnt(7)\n"
      "ds_write_b32 v18, v16\n"
      "buffer_load_dword v24, v3, s[8:11], s36 offen sc1\n"
      "buffer_load_dword v21, v18, s[8:11], s31 offen\n"
      "ds_read_b32 v17, v19\n"
      "buffer_load_dwordx4 v0, s[8:11], s47 offen offset:1024 sc1 lds\n"
      "s_add_u32 m0, s50, s40\n"
      "s_waitcnt vmcnt(9)\n"
      "v_and_b32 v185, 0xffff0000, v196\n"
      "s_waitcnt lgkmcnt(0)\n"
      "v_cmp_gt_i32 vcc, v16, v17\n"
      "buffer_load_dword v21, v18, s[8:11], s31 offen\n"
      "v_lshlrev_b32 v186, 16, v197\n"
      "v_lshlrev_b32 v184, 16, v196\n"
      "s_cbranch_vccnz .Lsp_wy_3\n"
      ".Lbk_wy_3:\n"
      "buffer_load_dword v1, s[8:11], s47 offen offset:0 sc1 lds\n"
      "ds_read_b128 v[96:99], v22 offset:0\n"
      "buffer_load_dword v2, s[8:11], s47 offen offset:256 sc1 lds\n"
      "v_and_b32 v187, 0xffff0000, v197\n"
      "s_waitcnt vmcnt(11)\n"
      "v_lshlrev_b32 v168, 16, v188\n"
      "v_lshlrev_b32 v174, 16, v191\n"
      "ds_read_b128 v[112:115], v22 offset:4096\n"
      "v_and_b32 v171, 0xffff0000, v189\n"
      "buffer_load_dword v21, v18, s[8:11], s31 offen\n"
      "ds_read_b128 v[108:111], v22 offset:3072\n"
      "ds_read_b128 v[120:123], v22 offset:6144\n"
      "s_sub_u32 s48, s47, s46\n"
      "ds_read_b128 v[100:103], v22 offset:1024\n"
      "ds_read_b128 v[104:107], v22 offset:2048\n"
      "ds_read_b128 v[124:127], v22 offset:7168\n"
      "v_lshlrev_b32 v170, 16, v189\n"
      "v_add_u32 v16, 1, v16\n"
      "ds_read_b128 v[144:147], v23 offset:3072\n"
      "v_lshlrev_b32 v172, 16, v190\n"
      "buffer_load_dwordx2 v[196:197], v8, s[8:11], s48 offen sc1\n"
      "ds_read_b128 v[140:143], v23 offset:2048\n"
      "ds_read_b128 v[116:119], v22 offset:5120\n"
      "buffer_load_dword v21, v18, s[8:11], s31 offen\n"
      "ds_read_b128 v[128:131], v22 offset:8192\n"
      "v_and_b32 v169, 0xffff0000, v188\n"
      "ds_read_b128 v[152:155], v23 offset:5120\n"
      "s_mov_b32 s20, 31\n"
      "s_waitcnt lgkmcnt(0)\n"
      "s_nop 4\n"
      ".p2align 6\n"
      ".Lw_loop:\n"
      "v_mfma_f32_16x16x32_bf16 v[184:187], v[60:63], v[96:99], v[184:187]\n"
      "ds_read_b128 v[156:159], v23 offset:6144\n"
      "v_and_b32 v175, 0xffff0000, v191\n"
      "v_mfma_f32_16x16x32_bf16 v[184:187], v[64:67], v[100:103], v[184:187]\n"
      "s_waitcnt vmcnt(12) lgkmcnt(5)\n"
      "v_fmac_f32 v215, v177, v177\n"
      "v_lshlrev_b32 v176, 16, v192\n"
      "v_mfma_f32_16x16x32_bf16 v[184:187], v[68:71], v[104:107], v[184:187]\n"
      "ds_read_b128 v[164:167], v23 offset:8192\n"
      "v_add_u32 v22, s50, v5\n"
      "v_mfma_f32_16x16x32_bf16 v[184:187], v[72:75], v[108:111], v[184:187]\n"
      "ds_read_b128 v[132:135], v23 offset:0\n"
      "v_fmac_f32 v215, v178, v178\n"
      "v_mfma_f32_16x16x32_bf16 v[184:187], v[76:79], v[112:115], v[184:187]\n"
      "s_waitcnt lgkmcnt(4)\n"
      "ds_read_b128 v[136:139], v23 offset:1024\n"
      "v_fmac_f32 v215, v179, v179\n"
      "v_mfma_f32_16x16x32_bf16 v[184:187], v[80:83], v[116:119], v[184:187]\n"
      "s_mov_b32 s47, s36\n"
      "ds_read_b128 v[148:151], v23 offset:4096\n"
      "v_and_b32 v173, 0xffff0000, v190\n"
      "v_fmac_f32 v215, s45, v217\n"
      "v_mfma_f32_16x16x32_bf16 v[184:187], v[84:87], v[120:123], v[184:187]\n"
      "v_fmac_f32 v214, s45, v216\n"
      "v_add_f32 v214, v214, v180\n"
      "v_mfma_f32_16x16x32_bf16 v[184:187], v[88:91], v[124:127], v[184:187]\n"
      "ds_read_b128 v[160:163], v23 offset:7168\n"
      "v_fmac_f32 v215, v180, v180\n"
      "v_add_u32 v23, s50, v6\n"
      "v_mfma_f32_16x16x32_bf16 v[184:187], v[92:95], v[128:131], v[184:187]\n"
      "v_add_f32 v214, v214, v181\n"
      "v_add_f32 v214, v214, v182\n"
      "v_mfma_f32_16x16x32_bf16 v[168:171], a[0:3], v[96:99], v[168:171]\n"
      "buffer_load_dwordx4 v[188:191], v7, s[8:11], s48 offen sc1\n"
      "v_mfma_f32_16x16x32_bf16 v[168:171], a[4:7], v[100:103], v[168:171]\n"
      "s_add_u32 s51, s51, 24576\n"
      "v_fmac_f32 v215, v181, v181\n"
      "v_fmac_f32 v215, v182, v182\n"
      "v_mfma_f32_16x16x32_bf16 v[168:171], a[8:11], v[104:107], v[168:171]\n"
      "v_add_f32 v214, v214, v183\n"
      "v_fmac_f32 v215, v183, v183\n"
      "v_mfma_f32_16x16x32_bf16 v[168:171], a[12:15], v[108:111], v[168:171]\n"
      "ds_write_b64 v13, v[214:215]\n"
      "v_mfma_f32_16x16x32_bf16 v[168:171], a[16:19], v[112:115], v[168:171]\n"
      "ds_write_b128 v11, v[184:187]\n"
      "v_mfma_f32_16x16x32_bf16 v[168:171], a[20:23], v[116:119], v[168:171]\n"
      "v_cvt_pk_bf16_f32 v210, v180, v181\n"
      "s_add_u32 s50, s50, 20480\n"
      "v_mfma_f32_16x16x32_bf16 v[168:171], a[24:27], v[120:123], v[168:171]\n"
      "v_cvt_pk_bf16_f32 v211, v182, v183\n"
      "v_cvt_pk_bf16_f32 v209, v178, v179\n"
      "v_mfma_f32_16x16x32_bf16 v[168:171], a[28:31], v[124:127], v[168:171]\n"
      "ds_write_b32 v18, v16\n"
      "v_mfma_f32_16x16x32_bf16 v[168:171], a[32:35], v[128:131], v[168:171]\n"
      "s_waitcnt lgkmcnt(3)\n"
      "v_and_b32 v181, 0xffff0000, v194\n"
      "v_lshlrev_b32 v182, 16, v195\n"
      "v_mfma_f32_16x16x32_bf16 v[168:171], a[36:39], v[132:135], v[168:171]\n"
      "buffer_store_dwordx4 v[208:211], v9, s[8:11], s38 offen offset:1024 sc1\n"
      "v_mfma_f32_16x16x32_bf16 v[168:171], a[40:43], v[136:139], v[168:171]\n"
      "v_mfma_f32_16x16x32_bf16 v[168:171], a[44:47], v[140:143], v[168:171]\n"
      "s_cmp_eq_u32 s36, s37\n"
      "s_cselect_b32 s49, 49152, s53\n"
      "v_and_b32 v183, 0xffff0000, v195\n"
      "v_mfma_f32_16x16x32_bf16 v[168:171], a[48:51], v[144:147], v[168:171]\n"
      "v_and_b32 v177, 0xffff0000, v192\n"
      "s_add_u32 s38, s38, 24576\n"
      "v_mfma_f32_16x16x32_bf16 v[168:171], a[52:55], v[148:151], v[168:171]\n"
      "v_lshlrev_b32 v180, 16, v194\n"
      "v_mfma_f32_16x16x32_bf16 v[168:171], a[56:59], v[152:155], v[168:171]\n"
      "s_add_u32 s36, s36, s49\n"
      "v_and_b32 v179, 0xffff0000, v193\n"
      "v_lshlrev_b32 v178, 16, v193\n"
      "v_mfma_f32_16x16x32_bf16 v[168:171], a[60:63], v[156:159], v[168:171]\n"
      "buffer_load_dwordx4 v[192:195], v7, s[8:11], s48 offen offset:1024 sc1\n"
      "v_mfma_f32_16x16x32_bf16 v[168:171], a[64:67], v[160:163], v[168:171]\n"
      "s_waitcnt vmcnt(11) lgkmcnt(0)\n"
      "ds_read_b32 v17, v19\n"
      "v_cmp_ne_u32 vcc, s21, v24\n"
      "v_mfma_f32_16x16x32_bf16 v[168:171], a[68:71], v[164:167], v[168:171]\n"
      "v_mfma_f32_16x16x32_bf16 v[172:175], a[72:75], v[96:99], v[172:175]\n"
      "s_cmp_eq_u32 s50, 61440\n"
      "s_cselect_b32 s50, 0, s50\n"
      "v_mfma_f32_16x16x32_bf16 v[172:175], a[76:79], v[100:103], v[172:175]\n"
      "s_add_u32 m0, s50, s39\n"
      "s_cbranch_vccnz .Lsp_w_4\n"
      ".Lbk_w_4:\n"
      "v_mfma_f32_16x16x32_bf16 v[172:175], a[80:83], v[104:107], v[172:175]\n"
      "buffer_load_dwordx4 v0, s[8:11], s47 offen offset:0 sc1 lds\n"
      "v_mfma_f32_16x16x32_bf16 v[172:175], a[84:87], v[108:111], v[172:175]\n"
      "s_waitcnt lgkmcnt(0)\n"
      "v_cmp_gt_i32 vcc, v16, v17\n"
      "v_add_f32 v214, v168, v169\n"
      "v_mfma_f32_16x16x32_bf16 v[172:175], a[88:91], v[112:115], v[172:175]\n"
      "v_add_f32 v214, v214, v170\n"
      "v_add_f32 v214, v214, v171\n"
      "v_mfma_f32_16x16x32_bf16 v[172:175], a[92:95], v[116:119], v[172:175]\n"
      "v_mul_f32 v215, v168, v168\n"
      "v_mfma_f32_16x16x32_bf16 v[172:175], a[96:99], v[120:123], v[172:175]\n"
      "s_cbranch_vccnz .Lsp_wz_5\n"
      ".Lbk_wz_5:\n"
      "ds_read_b64 v[218:219], v14\n"
      "v_mfma_f32_16x16x32_bf16 v[172:175], a[100:103], v[124:127], v[172:175]\n"
      "buffer_load_dwordx4 v0, s[8:11], s47 offen offset:3072 sc1 lds\n"
      "v_mfma_f32_16x16x32_bf16 v[172:175], a[104:107], v[128:131], v[172:175]\n"
      "v_fmac_f32 v215, v169, v169\n"
      "v_cvt_pk_bf16_f32 v205, v170, v171\n"
      "v_mfma_f32_16x16x32_bf16 v[172:175], a[108:111], v[132:135], v[172:175]\n"
      "ds_read_b128 v[200:203], v12\n"
      "v_fmac_f32 v215, v170, v170\n"
      "v_mfma_f32_16x16x32_bf16 v[172:175], a[112:115], v[136:139], v[172:175]\n"
      "v_mfma_f32_16x16x32_bf16 v[172:175], a[116:119], v[140:143], v[172:175]\n"
      "v_add_u32 v16, 1, v16\n"
      "v_fmac_f32 v215, v171, v171\n"
      "v_mfma_f32_16x16x32_bf16 v[172:175], a[120:123], v[144:147], v[172:175]\n"
      "buffer_load_dwordx4 v0, s[8:11], s47 offen offset:2048 sc1 lds\n"
      "v_mfma_f32_16x16x32_bf16 v[172:175], a[124:127], v[148:151], v[172:175]\n"
      "s_waitcnt lgkmcnt(1)\n"
      "v_add_f32_dpp v218, v218, v218 row_ror:8 row_mask:0xf bank_mask:0xf\n"
      "v_add_f32_dpp v219, v219, v219 row_ror:8 row_mask:0xf bank_mask:0xf\n"
      "v_mfma_f32_16x16x32_bf16 v[172:175], a[128:131], v[152:155], v[172:175]\n"
      "s_waitcnt vmcnt(9)\n"
      "ds_write_b32 v18, v16\n"
      "v_mfma_f32_16x16x32_bf16 v[172:175], a[132:135], v[156:159], v[172:175]\n"
      "v_add_f32_dpp v218, v218, v218 row_ror:4 row_mask:0xf bank_mask:0xf\n"
      "v_add_f32_dpp v219, v219, v219 row_ror:4 row_mask:0xf bank_mask:0xf\n"
      "v_mfma_f32_16x16x32_bf16 v[172:175], a[136:139], v[160:163], v[172:175]\n"
      "s_waitcnt lgkmcnt(1)\n"
      "v_add_f32_dpp v219, v219, v219 row_ror:2 row_mask:0xf bank_mask:0xf\n"
      "v_add_f32 v203, v203, v187\n"
      "v_mfma_f32_16x16x32_bf16 v[172:175], a[140:143], v[164:167], v[172:175]\n"
      "v_add_f32 v200, v200, v184\n"
      "v_add_f32 v201, v201, v185\n"
      "v_mfma_f32_16x16x32_bf16 v[176:179], a[144:147], v[96:99], v[176:179]\n"
      "v_add_f32_dpp v218, v218, v218 row_ror:2 row_mask:0xf bank_mask:0xf\n"
      "v_add_f32_dpp v219, v219, v219 row_ror:1 row_mask:0xf bank_mask:0xf\n"
      "v_mfma_f32_16x16x32_bf16 v[176:179], a[148:151], v[100:103], v[176:179]\n"
      "buffer_load_dword v24, v3, s[8:11], s36 offen sc1\n"
      "v_mfma_f32_16x16x32_bf16 v[176:179], a[152:155], v[104:107], v[176:179]\n"
      "buffer_store_dword v20, v4, s[8:11], s52 offen sc1\n"
      "v_mfma_f32_16x16x32_bf16 v[176:179], a[156:159], v[108:111], v[176:179]\n"
      "v_fmac_f32 v215, v172, v172\n"
      "v_add_f32 v216, v200, v201\n"
      "v_mfma_f32_16x16x32_bf16 v[176:179], a[160:163], v[112:115], v[176:179]\n"
      "v_add_f32_dpp v218, v218, v218 row_ror:1 row_mask:0xf bank_mask:0xf\n"
      "v_add_f32 v214, v214, v172\n"
      "v_mfma_f32_16x16x32_bf16 v[176:179], a[164:167], v[116:119], v[176:179]\n"
      "ds_read_b32 v17, v19\n"
      "v_cvt_pk_bf16_f32 v207, v174, v175\n"
      "v_mfma_f32_16x16x32_bf16 v[176:179], a[168:171], v[120:123], v[176:179]\n"
      "buffer_load_dwordx4 v0, s[8:11], s47 offen offset:1024 sc1 lds\n"
      "v_mfma_f32_16x16x32_bf16 v[176:179], a[172:175], v[124:127], v[176:179]\n"
      "v_add_f32 v219, 0x3b3cbe62, v219\n"
      "v_mul_f32 v219, 0x3ae38e39, v219\n"
      "v_mfma_f32_16x16x32_bf16 v[176:179], a[176:179], v[128:131], v[176:179]\n"
      "s_add_u32 m0, s50, s40\n"
      "v_add_f32 v214, v214, v173\n"
      "v_fmac_f32 v215, v173, v173\n"
      "v_mfma_f32_16x16x32_bf16 v[176:179], a[180:183], v[132:135], v[176:179]\n"
      "v_add_f32 v202, v202, v186\n"
      "v_cvt_pk_bf16_f32 v204, v168, v169\n"
      "v_mfma_f32_16x16x32_bf16 v[176:179], a[184:187], v[136:139], v[176:179]\n"
      "v_fmac_f32 v215, v174, v174\n"
      "v_fmac_f32 v215, v175, v175\n"
      "v_mfma_f32_16x16x32_bf16 v[176:179], a[188:191], v[140:143], v[176:179]\n"
      "s_waitcnt vmcnt(10) lgkmcnt(1)\n"
      "v_mul_f32 v218, 0x3ae38e39, v218\n"
      "v_and_b32 v185, 0xffff0000, v196\n"
      "v_mfma_f32_16x16x32_bf16 v[176:179], a[192:195], v[144:147], v[176:179]\n"
      "s_waitcnt lgkmcnt(0)\n"
      "v_cmp_gt_i32 vcc, v16, v17\n"
      "v_add_f32 v214, v214, v174\n"
      "v_mfma_f32_16x16x32_bf16 v[176:179], a[196:199], v[148:151], v[176:179]\n"
      "buffer_store_dwordx2 v[218:219], v15, s[8:11], s51 offen sc1\n"
      "v_mfma_f32_16x16x32_bf16 v[176:179], a[200:203], v[152:155], v[176:179]\n"
      "v_lshlrev_b32 v186, 16, v197\n"
      "v_cvt_pk_bf16_f32 v212, v200, v201\n"
      "v_mfma_f32_16x16x32_bf16 v[176:179], a[204:207], v[156:159], v[176:179]\n"
      "v_mul_f32 v217, v200, v200\n"
      "v_lshlrev_b32 v184, 16, v196\n"
      "v_mfma_f32_16x16x32_bf16 v[176:179], a[208:211], v[160:163], v[176:179]\n"
      "s_cbranch_vccnz .Lsp_wy_6\n"
      ".Lbk_wy_6:\n"
      "v_cvt_pk_bf16_f32 v206, v172, v173\n"
      "v_mfma_f32_16x16x32_bf16 v[176:179], a[212:215], v[164:167], v[176:179]\n"
      "buffer_load_dword v1, s[8:11], s47 offen offset:0 sc1 lds\n"
      "v_mfma_f32_16x16x32_bf16 v[180:183], a[216:219], v[96:99], v[180:183]\n"
      "ds_read_b128 v[96:99], v22 offset:0\n"
      "v_fmac_f32 v217, v201, v201\n"
      "v_fmac_f32 v217, v202, v202\n"
      "v_mfma_f32_16x16x32_bf16 v[180:183], a[220:223], v[100:103], v[180:183]\n"
      "buffer_load_dword v2, s[8:11], s47 offen offset:256 sc1 lds\n"
      "v_add_f32 v216, v216, v202\n"
      "v_mfma_f32_16x16x32_bf16 v[180:183], a[224:227], v[104:107], v[180:183]\n"
      "v_and_b32 v187, 0xffff0000, v197\n"
      "v_add_f32 v216, v216, v203\n"
      "v_mfma_f32_16x16x32_bf16 v[180:183], a[228:231], v[108:111], v[180:183]\n"
      "s_waitcnt vmcnt(11)\n"
      "v_lshlrev_b32 v168, 16, v188\n"
      "v_lshlrev_b32 v174, 16, v191\n"
      "v_mfma_f32_16x16x32_bf16 v[180:183], a[232:235], v[112:115], v[180:183]\n"
      "ds_read_b128 v[112:115], v22 offset:4096\n"
      "v_and_b32 v171, 0xffff0000, v189\n"
      "v_mfma_f32_16x16x32_bf16 v[180:183], a[236:239], v[116:119], v[180:183]\n"
      "buffer_store_dwordx4 v[204:207], v9, s[8:11], s38 offen sc1\n"
      "ds_read_b128 v[108:111], v22 offset:3072\n"
      "v_add_f32 v214, v214, v175\n"
      "v_mfma_f32_16x16x32_bf16 v[180:183], a[240:243], v[120:123], v[180:183]\n"
      "ds_read_b128 v[120:123], v22 offset:6144\n"
      "v_add_f32 v214, v214, v176\n"
      "v_mfma_f32_16x16x32_bf16 v[180:183], a[244:247], v[124:127], v[180:183]\n"
      "s_sub_u32 s48, s47, s46\n"
      "ds_read_b128 v[100:103], v22 offset:1024\n"
      "v_add_f32 v214, v214, v177\n"
      "v_mfma_f32_16x16x32_bf16 v[180:183], a[248:251], v[128:131], v[180:183]\n"
      "s_add_u32 s52, s52, 24576\n"
      "ds_read_b128 v[104:107], v22 offset:2048\n"
      "v_cvt_pk_bf16_f32 v213, v202, v203\n"
      "v_mfma_f32_16x16x32_bf16 v[180:183], a[252:255], v[132:135], v[180:183]\n"
      "v_fmac_f32 v217, v203, v203\n"
      "v_add_f32 v214, v214, v178\n"
      "v_mfma_f32_16x16x32_bf16 v[180:183], v[28:31], v[136:139], v[180:183]\n"
      "ds_read_b128 v[124:127], v22 offset:7168\n"
      "v_lshlrev_b32 v170, 16, v189\n"
      "v_mfma_f32_16x16x32_bf16 v[180:183], v[32:35], v[140:143], v[180:183]\n"
      "v_cvt_pk_bf16_f32 v208, v176, v177\n"
      "v_add_u32 v16, 1, v16\n"
      "v_mfma_f32_16x16x32_bf16 v[180:183], v[36:39], v[144:147], v[180:183]\n"
      "ds_read_b128 v[144:147], v23 offset:3072\n"
      "v_lshlrev_b32 v172, 16, v190\n"
      "v_mfma_f32_16x16x32_bf16 v[180:183], v[40:43], v[148:151], v[180:183]\n"
      "buffer_load_dwordx2 v[196:197], v8, s[8:11], s48 offen sc1\n"
      "ds_read_b128 v[140:143], v23 offset:2048\n"
      "v_mfma_f32_16x16x32_bf16 v[180:183], v[44:47], v[152:155], v[180:183]\n"
      "ds_read_b128 v[116:119], v22 offset:5120\n"
      "v_fmac_f32 v215, v176, v176\n"
      "v_mfma_f32_16x16x32_bf16 v[180:183], v[48:51], v[156:159], v[180:183]\n"
      "buffer_store_dwordx2 v[212:213], v10, s[8:11], s38 offen sc1\n"
      "ds_read_b128 v[128:131], v22 offset:8192\n"
      "v_and_b32 v169, 0xffff0000, v188\n"
      "v_mfma_f32_16x16x32_bf16 v[180:183], v[52:55], v[160:163], v[180:183]\n"
      "s_waitcnt lgkmcnt(5)\n"
      "ds_read_b128 v[152:155], v23 offset:5120\n"
      "v_add_f32 v214, v214, v179\n"
      "v_mfma_f32_16x16x32_bf16 v[180:183], v[56:59], v[164:167], v[180:183]\n"
      "v_mfma_f32_16x16x32_bf16 v[184:187], v[60:63], v[96:99], v[184:187]\n"
      "ds_read_b128 v[156:159], v23 offset:6144\n"
      "v_and_b32 v175, 0xffff0000, v191\n"
      "v_mfma_f32_16x16x32_bf16 v[184:187], v[64:67], v[100:103], v[184:187]\n"
      "s_waitcnt vmcnt(12) lgkmcnt(5)\n"
      "v_fmac_f32 v215, v177, v177\n"
      "v_lshlrev_b32 v176, 16, v192\n"
      "v_mfma_f32_16x16x32_bf16 v[184:187], v[68:71], v[104:107], v[184:187]\n"
      "ds_read_b128 v[164:167], v23 offset:8192\n"
      "v_add_u32 v22, s50, v5\n"
      "v_mfma_f32_16x16x32_bf16 v[184:187], v[72:75], v[108:111], v[184:187]\n"
      "ds_read_b128 v[132:135], v23 offset:0\n"
      "v_fmac_f32 v215, v178, v178\n"
      "v_mfma_f32_16x16x32_bf16 v[184:187], v[76:79], v[112:115], v[184:187]\n"
      "s_waitcnt lgkmcnt(4)\n"
      "ds_read_b128 v[136:139], v23 offset:1024\n"
      "v_fmac_f32 v215, v179, v179\n"
      "v_mfma_f32_16x16x32_bf16 v[184:187], v[80:83], v[116:119], v[184:187]\n"
      "s_mov_b32 s47, s36\n"
      "ds_read_b128 v[148:151], v23 offset:4096\n"
      "v_and_b32 v173, 0xffff0000, v190\n"
      "v_fmac_f32 v215, s45, v217\n"
      "v_mfma_f32_16x16x32_bf16 v[184:187], v[84:87], v[120:123], v[184:187]\n"
      "v_fmac_f32 v214, s45, v216\n"
      "v_add_f32 v214, v214, v180\n"
      "v_mfma_f32_16x16x32_bf16 v[184:187], v[88:91], v[124:127], v[184:187]\n"
      "ds_read_b128 v[160:163], v23 offset:7168\n"
      "v_fmac_f32 v215, v180, v180\n"
      "v_add_u32 v23, s50, v6\n"
      "v_mfma_f32_16x16x32_bf16 v[184:187], v[92:95], v[128:131], v[184:187]\n"
      "v_add_f32 v214, v214, v181\n"
      "v_add_f32 v214, v214, v182\n"
      "v_mfma_f32_16x16x32_bf16 v[168:171], a[0:3], v[96:99], v[168:171]\n"
      "buffer_load_dwordx4 v[188:191], v7, s[8:11], s48 offen sc1\n"
      "v_mfma_f32_16x16x32_bf16 v[168:171], a[4:7], v[100:103], v[168:171]\n"
      "s_add_u32 s51, s51, 24576\n"
      "v_fmac_f32 v215, v181, v181\n"
      "v_fmac_f32 v215, v182, v182\n"
      "v_mfma_f32_16x16x32_bf16 v[168:171], a[8:11], v[104:107], v[168:171]\n"
      "v_add_f32 v214, v214, v183\n"
      "v_fmac_f32 v215, v183, v183\n"
      "v_mfma_f32_16x16x32_bf16 v[168:171], a[12:15], v[108:111], v[168:171]\n"
      "ds_write_b64 v13, v[214:215]\n"
      "v_mfma_f32_16x16x32_bf16 v[168:171], a[16:19], v[112:115], v[168:171]\n"
      "ds_write_b128 v11, v[184:187]\n"
      "v_mfma_f32_16x16x32_bf16 v[168:171], a[20:23], v[116:119], v[168:171]\n"
      "v_cvt_pk_bf16_f32 v210, v180, v181\n"
      "s_add_u32 s50, s50, 20480\n"
      "v_mfma_f32_16x16x32_bf16 v[168:171], a[24:27], v[120:123], v[168:171]\n"
      "v_cvt_pk_bf16_f32 v211, v182, v183\n"
      "v_cvt_pk_bf16_f32 v209, v178, v179\n"
      "v_mfma_f32_16x16x32_bf16 v[168:171], a[28:31], v[124:127], v[168:171]\n"
      "ds_write_b32 v18, v16\n"
      "v_mfma_f32_16x16x32_bf16 v[168:171], a[32:35], v[128:131], v[168:171]\n"
      "s_waitcnt lgkmcnt(3)\n"
      "v_and_b32 v181, 0xffff0000, v194\n"
      "v_lshlrev_b32 v182, 16, v195\n"
      "v_mfma_f32_16x16x32_bf16 v[168:171], a[36:39], v[132:135], v[168:171]\n"
      "buffer_store_dwordx4 v[208:211], v9, s[8:11], s38 offen offset:1024 sc1\n"
      "v_mfma_f32_16x16x32_bf16 v[168:171], a[40:43], v[136:139], v[168:171]\n"
      "v_mfma_f32_16x16x32_bf16 v[168:171], a[44:47], v[140:143], v[168:171]\n"
      "s_cmp_eq_u32 s36, s37\n"
      "s_cselect_b32 s49, 49152, s53\n"
      "v_and_b32 v183, 0xffff0000, v195\n"
      "v_mfma_f32_16x16x32_bf16 v[168:171], a[48:51], v[144:147], v[168:171]\n"
      "v_and_b32 v177, 0xffff0000, v192\n"
      "s_add_u32 s38, s38, 24576\n"
      "v_mfma_f32_16x16x32_bf16 v[168:171], a[52:55], v[148:151], v[168:171]\n"
      "v_lshlrev_b32 v180, 16, v194\n"
      "v_mfma_f32_16x16x32_bf16 v[168:171], a[56:59], v[152:155], v[168:171]\n"
      "s_add_u32 s36, s36, s49\n"
      "v_and_b32 v179, 0xffff0000, v193\n"
      "v_lshlrev_b32 v178, 16, v193\n"
      "v_mfma_f32_16x16x32_bf16 v[168:171], a[60:63], v[156:159], v[168:171]\n"
      "buffer_load_dwordx4 v[192:195], v7, s[8:11], s48 offen offset:1024 sc1\n"
      "v_mfma_f32_16x16x32_bf16 v[168:171], a[64:67], v[160:163], v[168:171]\n"
      "s_waitcnt vmcnt(11) lgkmcnt(0)\n"
      "ds_read_b32 v17, v19\n"
      "v_cmp_ne_u32 vcc, s21, v24\n"
      "v_mfma_f32_16x16x32_bf16 v[168:171], a[68:71], v[164:167], v[168:171]\n"
      "v_mfma_f32_16x16x32_bf16 v[172:175], a[72:75], v[96:99], v[172:175]\n"
      "s_cmp_eq_u32 s50, 61440\n"
      "s_cselect_b32 s50, 0, s50\n"
      "v_mfma_f32_16x16x32_bf16 v[172:175], a[76:79], v[100:103], v[172:175]\n"
      "s_add_u32 m0, s50, s39\n"
      "s_cbranch_vccnz .Lsp_w_7\n"
      ".Lbk_w_7:\n"
      "v_mfma_f32_16x16x32_bf16 v[172:175], a[80:83], v[104:107], v[172:175]\n"
      "buffer_load_dwordx4 v0, s[8:11], s47 offen offset:0 sc1 lds\n"
      "v_mfma_f32_16x16x32_bf16 v[172:175], a[84:87], v[108:111], v[172:175]\n"
      "s_waitcnt lgkmcnt(0)\n"
      "v_cmp_gt_i32 vcc, v16, v17\n"
      "v_add_f32 v214, v168, v169\n"
      "v_mfma_f32_16x16x32_bf16 v[172:175], a[88:91], v[112:115], v[172:175]\n"
      "v_add_f32 v214, v214, v170\n"
      "v_add_f32 v214, v214, v171\n"
      "v_mfma_f32_16x16x32_bf16 v[172:175], a[92:95], v[116:119], v[172:175]\n"
      "v_mul_f32 v215, v168, v168\n"
      "v_mfma_f32_16x16x32_bf16 v[172:175], a[96:99], v[120:123], v[172:175]\n"
      "s_cbranch_vccnz .Lsp_wz_8\n"
      ".Lbk_wz_8:\n"
      "ds_read_b64 v[218:219], v14\n"
      "v_mfma_f32_16x16x32_bf16 v[172:175], a[100:103], v[124:127], v[172:175]\n"
      "buffer_load_dwordx4 v0, s[8:11], s47 offen offset:3072 sc1 lds\n"
      "v_mfma_f32_16x16x32_bf16 v[172:175], a[104:107], v[128:131], v[172:175]\n"
      "v_fmac_f32 v215, v169, v169\n"
      "v_cvt_pk_bf16_f32 v205, v170, v171\n"
      "v_mfma_f32_16x16x32_bf16 v[172:175], a[108:111], v[132:135], v[172:175]\n"
      "ds_read_b128 v[200:203], v12\n"
      "v_fmac_f32 v215, v170, v170\n"
      "v_mfma_f32_16x16x32_bf16 v[172:175], a[112:115], v[136:139], v[172:175]\n"
      "v_mfma_f32_16x16x32_bf16 v[172:175], a[116:119], v[140:143], v[172:175]\n"
      "v_add_u32 v16, 1, v16\n"
      "v_fmac_f32 v215, v171, v171\n"
      "v_mfma_f32_16x16x32_bf16 v[172:175], a[120:123], v[144:147], v[172:175]\n"
      "buffer_load_dwordx4 v0, s[8:11], s47 offen offset:2048 sc1 lds\n"
      "v_mfma_f32_16x16x32_bf16 v[172:175], a[124:127], v[148:151], v[172:175]\n"
      "s_waitcnt lgkmcnt(1)\n"
      "v_add_f32_dpp v218, v218, v218 row_ror:8 row_mask:0xf bank_mask:0xf\n"
      "v_add_f32_dpp v219, v219, v219 row_ror:8 row_mask:0xf bank_mask:0xf\n"
      "v_mfma_f32_16x16x32_bf16 v[172:175], a[128:131], v[152:155], v[172:175]\n"
      "s_waitcnt vmcnt(9)\n"
      "ds_write_b32 v18, v16\n"
      "v_mfma_f32_16x16x32_bf16 v[172:175], a[132:135], v[156:159], v[172:175]\n"
      "v_add_f32_dpp v218, v218, v218 row_ror:4 row_mask:0xf bank_mask:0xf\n"
      "v_add_f32_dpp v219, v219, v219 row_ror:4 row_mask:0xf bank_mask:0xf\n"
      "v_mfma_f32_16x16x32_bf16 v[172:175], a[136:139], v[160:163], v[172:175]\n"
      "s_waitcnt lgkmcnt(1)\n"
      "v_add_f32_dpp v219, v219, v219 row_ror:2 row_mask:0xf bank_mask:0xf\n"
      "v_add_f32 v203, v203, v187\n"
      "v_mfma_f32_16x16x32_bf16 v[172:175], a[140:143], v[164:167], v[172:175]\n"
      "v_add_f32 v200, v200, v184\n"
      "v_add_f32 v201, v201, v185\n"
      "v_mfma_f32_16x16x32_bf16 v[176:179], a[144:147], v[96:99], v[176:179]\n"
      "v_add_f32_dpp v218, v218, v218 row_ror:2 row_mask:0xf bank_mask:0xf\n"
      "v_add_f32_dpp v219, v219, v219 row_ror:1 row_mask:0xf bank_mask:0xf\n"
      "v_mfma_f32_16x16x32_bf16 v[176:179], a[148:151], v[100:103], v[176:179]\n"
      "buffer_load_dword v24, v3, s[8:11], s36 offen sc1\n"
      "v_mfma_f32_16x16x32_bf16 v[176:179], a[152:155], v[104:107], v[176:179]\n"
      "buffer_store_dword v20, v4, s[8:11], s52 offen sc1\n"
      "v_mfma_f32_16x16x32_bf16 v[176:179], a[156:159], v[108:111], v[176:179]\n"
      "v_fmac_f32 v215, v172, v172\n"
      "v_add_f32 v216, v200, v201\n"
      "v_mfma_f32_16x16x32_bf16 v[176:179], a[160:163], v[112:115], v[176:179]\n"
      "v_add_f32_dpp v218, v218, v218 row_ror:1 row_mask:0xf bank_mask:0xf\n"
      "v_add_f32 v214, v214, v172\n"
      "v_mfma_f32_16x16x32_bf16 v[176:179], a[164:167], v[116:119], v[176:179]\n"
      "ds_read_b32 v17, v19\n"
      "v_cvt_pk_bf16_f32 v207, v174, v175\n"
      "v_mfma_f32_16x16x32_bf16 v[176:179], a[168:171], v[120:123], v[176:179]\n"
      "buffer_load_dwordx4 v0, s[8:11], s47 offen offset:1024 sc1 lds\n"
      "v_mfma_f32_16x16x32_bf16 v[176:179], a[172:175], v[124:127], v[176:179]\n"
      "v_add_f32 v219, 0x3b3cbe62, v219\n"
      "v_mul_f32 v219, 0x3ae38e39, v219\n"
      "v_mfma_f32_16x16x32_bf16 v[176:179], a[176:179], v[128:131], v[176:179]\n"
      "s_add_u32 m0, s50, s40\n"
      "v_add_f32 v214, v214, v173\n"
      "v_fmac_f32 v215, v173, v173\n"
      "v_mfma_f32_16x16x32_bf16 v[176:179], a[180:183], v[132:135], v[176:179]\n"
      "v_add_f32 v202, v202, v186\n"
      "v_cvt_pk_bf16_f32 v204, v168, v169\n"
      "v_mfma_f32_16x16x32_bf16 v[176:179], a[184:187], v[136:139], v[176:179]\n"
      "v_fmac_f32 v215, v174, v174\n"
      "v_fmac_f32 v215, v175, v175\n"
      "v_mfma_f32_16x16x32_bf16 v[176:179], a[188:191], v[140:143], v[176:179]\n"
      "s_waitcnt vmcnt(10) lgkmcnt(1)\n"
      "v_mul_f32 v218, 0x3ae38e39, v218\n"
      "v_and_b32 v185, 0xffff0000, v196\n"
      "v_mfma_f32_16x16x32_bf16 v[176:179], a[192:195], v[144:147], v[176:179]\n"
      "s_waitcnt lgkmcnt(0)\n"
      "v_cmp_gt_i32 vcc, v16, v17\n"
      "v_add_f32 v214, v214, v174\n"
      "v_mfma_f32_16x16x32_bf16 v[176:179], a[196:199], v[148:151], v[176:179]\n"
      "buffer_store_dwordx2 v[218:219], v15, s[8:11], s51 offen sc1\n"
      "v_mfma_f32_16x16x32_bf16 v[176:179], a[200:203], v[152:155], v[176:179]\n"
      "v_lshlrev_b32 v186, 16, v197\n"
      "v_cvt_pk_bf16_f32 v212, v200, v201\n"
      "v_mfma_f32_16x16x32_bf16 v[176:179], a[204:207], v[156:159], v[176:179]\n"
      "v_mul_f32 v217, v200, v200\n"
      "v_lshlrev_b32 v184, 16, v196\n"
      "v_mfma_f32_16x16x32_bf16 v[176:179], a[208:211], v[160:163], v[176:179]\n"
      "s_cbranch_vccnz .Lsp_wy_9\n"
      ".Lbk_wy_9:\n"
      "v_cvt_pk_bf16_f32 v206, v172, v173\n"
      "v_mfma_f32_16x16x32_bf16 v[176:179], a[212:215], v[164:167], v[176:179]\n"
      "buffer_load_dword v1, s[8:11], s47 offen offset:0 sc1 lds\n"
      "v_mfma_f32_16x16x32_bf16 v[180:183], a[216:219], v[96:99], v[180:183]\n"
      "ds_read_b128 v[96:99], v22 offset:0\n"
      "v_fmac_f32 v217, v201, v201\n"
      "v_fmac_f32 v217, v202, v202\n"
      "v_mfma_f32_16x16x32_bf16 v[180:183], a[220:223], v[100:103], v[180:183]\n"
      "buffer_load_dword v2, s[8:11], s47 offen offset:256 sc1 lds\n"
      "v_add_f32 v216, v216, v202\n"
      "v_mfma_f32_16x16x32_bf16 v[180:183], a[224:227], v[104:107], v[180:183]\n"
      "v_and_b32 v187, 0xffff0000, v197\n"
      "v_add_f32 v216, v216, v203\n"
      "v_mfma_f32_16x16x32_bf16 v[180:183], a[228:231], v[108:111], v[180:183]\n"
      "s_waitcnt vmcnt(11)\n"
      "v_lshlrev_b32 v168, 16, v188\n"
      "v_lshlrev_b32 v174, 16, v191\n"
      "v_mfma_f32_16x16x32_bf16 v[180:183], a[232:235], v[112:115], v[180:183]\n"
      "ds_read_b128 v[112:115], v22 offset:4096\n"
      "v_and_b32 v171, 0xffff0000, v189\n"
      "v_mfma_f32_16x16x32_bf16 v[180:183], a[236:239], v[116:119], v[180:183]\n"
      "buffer_store_dwordx4 v[204:207], v9, s[8:11], s38 offen sc1\n"
      "ds_read_b128 v[108:111], v22 offset:3072\n"
      "v_add_f32 v214, v214, v175\n"
      "v_mfma_f32_16x16x32_bf16 v[180:183], a[240:243], v[120:123], v[180:183]\n"
      "ds_read_b128 v[120:123], v22 offset:6144\n"
      "v_add_f32 v214, v214, v176\n"
      "v_mfma_f32_16x16x32_bf16 v[180:183], a[244:247], v[124:127], v[180:183]\n"
      "s_sub_u32 s48, s47, s46\n"
      "ds_read_b128 v[100:103], v22 offset:1024\n"
      "v_add_f32 v214, v214, v177\n"
      "v_mfma_f32_16x16x32_bf16 v[180:183], a[248:251], v[128:131], v[180:183]\n"
      "s_add_u32 s52, s52, 24576\n"
      "ds_read_b128 v[104:107], v22 offset:2048\n"
      "v_cvt_pk_bf16_f32 v213, v202, v203\n"
      "v_mfma_f32_16x16x32_bf16 v[180:183], a[252:255], v[132:135], v[180:183]\n"
      "v_fmac_f32 v217, v203, v203\n"
      "v_add_f32 v214, v214, v178\n"
      "v_mfma_f32_16x16x32_bf16 v[180:183], v[28:31], v[136:139], v[180:183]\n"
      "ds_read_b128 v[124:127], v22 offset:7168\n"
      "v_lshlrev_b32 v170, 16, v189\n"
      "v_mfma_f32_16x16x32_bf16 v[180:183], v[32:35], v[140:143], v[180:183]\n"
      "v_cvt_pk_bf16_f32 v208, v176, v177\n"
      "v_add_u32 v16, 1, v16\n"
      "v_mfma_f32_16x16x32_bf16 v[180:183], v[36:39], v[144:147], v[180:183]\n"
      "ds_read_b128 v[144:147], v23 offset:3072\n"
      "v_lshlrev_b32 v172, 16, v190\n"
      "v_mfma_f32_16x16x32_bf16 v[180:183], v[40:43], v[148:151], v[180:183]\n"
      "buffer_load_dwordx2 v[196:197], v8, s[8:11], s48 offen sc1\n"
      "ds_read_b128 v[140:143], v23 offset:2048\n"
      "v_mfma_f32_16x16x32_bf16 v[180:183], v[44:47], v[152:155], v[180:183]\n"
      "ds_read_b128 v[116:119], v22 offset:5120\n"
      "v_fmac_f32 v215, v176, v176\n"
      "v_mfma_f32_16x16x32_bf16 v[180:183], v[48:51], v[156:159], v[180:183]\n"
      "buffer_store_dwordx2 v[212:213], v10, s[8:11], s38 offen sc1\n"
      "ds_read_b128 v[128:131], v22 offset:8192\n"
      "v_and_b32 v169, 0xffff0000, v188\n"
      "v_mfma_f32_16x16x32_bf16 v[180:183], v[52:55], v[160:163], v[180:183]\n"
      "s_waitcnt lgkmcnt(5)\n"
      "ds_read_b128 v[152:155], v23 offset:5120\n"
      "v_add_f32 v214, v214, v179\n"
      "v_mfma_f32_16x16x32_bf16 v[180:183], v[56:59], v[164:167], v[180:183]\n"
      "s_sub_u32 s20, s20, 1\n"
      "s_cbranch_scc0 .Lw_loop\n"
      "v_fmac_f32 v215, v177, v177\n"
      "v_fmac_f32 v215, v178, v178\n"
      "v_fmac_f32 v215, v179, v179\n"
      "v_fmac_f32 v215, s45, v217\n"
      "v_fmac_f32 v214, s45, v216\n"
      "s_nop 3\n"
      "v_add_f32 v214, v214, v180\n"
      "v_fmac_f32 v215, v180, v180\n"
      "v_add_f32 v214, v214, v181\n"
      "v_add_f32 v214, v214, v182\n"
      "s_add_u32 s51, s51, 24576\n"
      "v_fmac_f32 v215, v181, v181\n"
      "v_fmac_f32 v215, v182, v182\n"
      "v_add_f32 v214, v214, v183\n"
      "v_fmac_f32 v215, v183, v183\n"
      "ds_write_b64 v13, v[214:215]\n"
      "v_cvt_pk_bf16_f32 v210, v180, v181\n"
      "v_cvt_pk_bf16_f32 v211, v182, v183\n"
      "v_cvt_pk_bf16_f32 v209, v178, v179\n"
      "buffer_store_dwordx4 v[208:211], v9, s[8:11], s38 offen offset:1024 sc1\n"
      "s_add_u32 s38, s38, 24576\n"
      "s_waitcnt lgkmcnt(0)\n"
      "s_barrier\n"
      "ds_read_b64 v[218:219], v14\n"
      "s_waitcnt lgkmcnt(0)\n"
      "v_add_f32_dpp v218, v218, v218 row_ror:8 row_mask:0xf bank_mask:0xf\n"
      "v_add_f32_dpp v219, v219, v219 row_ror:8 row_mask:0xf bank_mask:0xf\n"
      "s_nop 0\n"
      "v_add_f32_dpp v218, v218, v218 row_ror:4 row_mask:0xf bank_mask:0xf\n"
      "v_add_f32_dpp v219, v219, v219 row_ror:4 row_mask:0xf bank_mask:0xf\n"
      "s_nop 1\n"
      "v_add_f32_dpp v219, v219, v219 row_ror:2 row_mask:0xf bank_mask:0xf\n"
      "v_add_f32_dpp v218, v218, v218 row_ror:2 row_mask:0xf bank_mask:0xf\n"
      "s_nop 0\n"
      "v_add_f32_dpp v219, v219, v219 row_ror:1 row_mask:0xf bank_mask:0xf\n"
      "s_waitcnt vmcnt(6)\n"
      "buffer_store_dword v20, v4, s[8:11], s52 offen sc1\n"
      "v_add_f32_dpp v218, v218, v218 row_ror:1 row_mask:0xf bank_mask:0xf\n"
      "v_add_f32 v219, 0x3b3cbe62, v219\n"
      "v_mul_f32 v219, 0x3ae38e39, v219\n"
      "v_mul_f32 v218, 0x3ae38e39, v218\n"
      "buffer_store_dwordx2 v[218:219], v15, s[8:11], s51 offen sc1\n"
      "s_add_u32 s52, s52, 24576\n"
      "s_add_u32 s51, s51, 24576\n"
      "s_waitcnt vmcnt(0)\n"
      "buffer_store_dword v20, v4, s[8:11], s52 offen sc1\n"
      "s_add_u32 s52, s52, 24576\n"
      "s_endpgm\n"
      ".Lsp_w_1:\n"
      "s_mov_b32 s22, s23\n"
      ".Lsl_w_1:\n"
      "buffer_load_dword v24, v3, s[8:11], s47 offen sc1\n"
      "s_waitcnt vmcnt(0)\n"
      "v_cmp_ne_u32 vcc, s21, v24\n"
      "s_cbranch_vccz .Lbk_w_1\n"
      "s_sleep 4\n"
      "s_sub_u32 s22, s22, 1\n"
      "s_cbranch_scc0 .Lsl_w_1\n"
      "s_mov_b32 s23, 0\n"
      "s_branch .Lbk_w_1\n"
      ".Lsp_w_2:\n"
      "s_mov_b32 s22, s23\n"
      ".Lsl_w_2:\n"
      "buffer_load_dword v24, v3, s[8:11], s47 offen sc1\n"
      "s_waitcnt vmcnt(0)\n"
      "v_cmp_ne_u32 vcc, s21, v24\n"
      "s_cbranch_vccz .Lbk_w_2\n"
      "s_sleep 4\n"
      "s_sub_u32 s22, s22, 1\n"
      "s_cbranch_scc0 .Lsl_w_2\n"
      "s_mov_b32 s23, 0\n"
      "s_branch .Lbk_w_2\n"
      ".Lsp_wy_3:\n"
      "s_lshl_b32 s22, s23, 4\n"
      ".Lsl_wy_3:\n"
      "ds_read_b32 v17, v19\n"
      "s_waitcnt lgkmcnt(0)\n"
      "v_cmp_gt_i32 vcc, v16, v17\n"
      "s_cbranch_vccz .Lbk_wy_3\n"
      "s_sub_u32 s22, s22, 1\n"
      "s_cbranch_scc0 .Lsl_wy_3\n"
      "s_mov_b32 s23, 0\n"
      "s_branch .Lbk_wy_3\n"
      ".Lsp_w_4:\n"
      "s_mov_b32 s22, s23\n"
      "buffer_load_dword v24, v3, s[8:11], s47 offen sc1\n"
      "s_waitcnt vmcnt(0)\n"
      "v_cmp_ne_u32 vcc, s21, v24\n"
      "s_add_u32 s30, s52, 0\n"
      "buffer_store_dword v20, v4, s[8:11], s30 offen sc1\n"
      "s_cbranch_vccz .Lbk_w_4\n"
      ".Lsl_w_4:\n"
      "buffer_load_dword v24, v3, s[8:11], s47 offen sc1\n"
      "s_waitcnt vmcnt(0)\n"
      "v_cmp_ne_u32 vcc, s21, v24\n"
      "s_cbranch_vccz .Lbk_w_4\n"
      "s_sleep 4\n"
      "s_sub_u32 s22, s22, 1\n"
      "s_cbranch_scc0 .Lsl_w_4\n"
      "s_mov_b32 s23, 0\n"
      "s_branch .Lbk_w_4\n"
      ".Lsp_wz_5:\n"
      "s_lshl_b32 s22, s23, 4\n"
      ".Lsl_wz_5:\n"
      "ds_read_b32 v17, v19\n"
      "s_waitcnt lgkmcnt(0)\n"
      "v_cmp_gt_i32 vcc, v16, v17\n"
      "s_cbranch_vccz .Lbk_wz_5\n"
      "s_sub_u32 s22, s22, 1\n"
      "s_cbranch_scc0 .Lsl_wz_5\n"
      "s_mov_b32 s23, 0\n"
      "s_branch .Lbk_wz_5\n"
      ".Lsp_wy_6:\n"
      "s_lshl_b32 s22, s23, 4\n"
      ".Lsl_wy_6:\n"
      "ds_read_b32 v17, v19\n"
      "s_waitcnt lgkmcnt(0)\n"
      "v_cmp_gt_i32 vcc, v16, v17\n"
      "s_cbranch_vccz .Lbk_wy_6\n"
      "s_sub_u32 s22, s22, 1\n"
      "s_cbranch_scc0 .Lsl_wy_6\n"
      "s_mov_b32 s23, 0\n"
      "s_branch .Lbk_wy_6\n"
      ".Lsp_w_7:\n"
      "s_mov_b32 s22, s23\n"
      "buffer_load_dword v24, v3, s[8:11], s47 offen sc1\n"
      "s_waitcnt vmcnt(0)\n"
      "v_cmp_ne_u32 vcc, s21, v24\n"
      "s_add_u32 s30, s52, 0\n"
      "buffer_store_dword v20, v4, s[8:11], s30 offen sc1\n"
      "s_cbranch_vccz .Lbk_w_7\n"
      ".Lsl_w_7:\n"
      "buffer_load_dword v24, v3, s[8:11], s47 offen sc1\n"
      "s_waitcnt vmcnt(0)\n"
      "v_cmp_ne_u32 vcc, s21, v24\n"
      "s_cbranch_vccz .Lbk_w_7\n"
      "s_sleep 4\n"
      "s_sub_u32 s22, s22, 1\n"
      "s_cbranch_scc0 .Lsl_w_7\n"
      "s_mov_b32 s23, 0\n"
      "s_branch .Lbk_w_7\n"
      ".Lsp_wz_8:\n"
      "s_lshl_b32 s22, s23, 4\n"
      ".Lsl_wz_8:\n"
      "ds_read_b32 v17, v19\n"
      "s_waitcnt lgkmcnt(0)\n"
      "v_cmp_gt_i32 vcc, v16, v17\n"
      "s_cbranch_vccz .Lbk_wz_8\n"
      "s_sub_u32 s22, s22, 1\n"
      "s_cbranch_scc0 .Lsl_wz_8\n"
      "s_mov_b32 s23, 0\n"
      "s_branch .Lbk_wz_8\n"
      ".Lsp_wy_9:\n"
      "s_lshl_b32 s22, s23, 4\n"
      ".Lsl_wy_9:\n"
      "ds_read_b32 v17, v19\n"
      "s_waitcnt lgkmcnt(0)\n"
      "v_cmp_gt_i32 vcc, v16, v17\n"
      "s_cbranch_vccz .Lbk_wy_9\n"
      "s_sub_u32 s22, s22, 1\n"
      "s_cbranch_scc0 .Lsl_wy_9\n"
      "s_mov_b32 s23, 0\n"
      "s_branch .Lbk_wy_9\n"
      ".Lw_last:\n"
      :
      : "{s[8:11]}"(rsWS), "{s[12:15]}"(rsA), "{v0}"(v_l16), "{v1}"(v_l5), "{v2}"(v_l6), "{v3}"(v_fa), "{v4}"(v_ra), "{v5}"(v_b0), "{v6}"(v_b1), "{v7}"(v_rp), "{v8}"(v_rs), "{v9}"(v_xp), "{v10}"(v_xs), "{v11}"(v_ew), "{v12}"(v_er), "{v13}"(v_sw), "{v14}"(v_sr), "{v15}"(v_ss), "{v17}"(v_al), "{v18}"(v_yw), "{v19}"(v_yr), "{v96}"(v_vo[0]), "{v97}"(v_vo[1]), "{v98}"(v_vo[2]), "{v99}"(v_vo[3]), "{v100}"(v_vo[4]), "{v101}"(v_vo[5]), "{v102}"(v_vo[6]), "{v103}"(v_vo[7]), "{v104}"(v_vo[8]), "{v105}"(v_vp[0]), "{v106}"(v_vp[1]), "{v107}"(v_vp[2]), "{v108}"(v_vp[3]), "{v109}"(v_vp[4]), "{v110}"(v_vp[5]), "{v111}"(v_vp[6]), "{v112}"(v_vp[7]), "{v113}"(v_vp[8]), "{v114}"(v_t0), "{v115}"(v_t1), "{v116}"(v_ts), "{v117}"(v_pw), "{s36}"(s_sx), "{s37}"(s_ev), "{s38}"(s_sy), "{s39}"(s_m4), "{s40}"(s_m5), "{s41}"(s_m9), "{s42}"(s_so), "{s43}"(s_s7), "{s44}"(s_s8), "{s45}"(s_wg), "{s46}"(s_rb)
      : "v255", "a255", "s99", "vcc", "scc", "memory");

  } else if (slot < 14) {
    const unsigned tr = ((xcd + DSH) & 7) * 3 + (slot - 5) / 3, n3 = (slot - 5) % 3, ks = unit_ks(tr), gr = unit_gr(tr);
    const u32x4 rsA = srd(Wd, 4608 * 576 * 2), rsB = srd(out, 2048 * 576 * 2), rsC = srd(X, 2048 * 576 * 2);
    const unsigned v_l16b = v_l16 + 4096, v_l8 = 1024 + lane * 8, f0 = 24 * ks + 6 * w;
    const unsigned v_fa = 147456 - f0 * 1024 + (f0 + lane / 11) * 128 + lane % 11 * 4, v_ga = lane / 13 * 4 * PW + 1536 + lane % 13 * 4;
    const unsigned v_ra = line + 1536, v_rf = line + 20480 + (4 * n3 + w) * 128;
    const unsigned v_pw = l8 < 5 ? (l8 + 7) * PREC + l8 * 4 * PW + 1536 + l7 : OOB, v_pu = ks && l8 + ks < 5 ? OOB : v_pw - 6 * PREC;
    const unsigned v_pv = ks + 6 * l8 < 32 ? (33 + 6 * l8) * REC + 20480 + (4 * n3 + w) * 128 + l7 : OOB;
    const unsigned pf = 3 * (w >> 1) + 2 * (w & 1), sf = 3 * (w >> 1) + 1;
    const unsigned v_at = (8 * g + q) * 384 + 64 * pf + 16 * p, v_as = (8 * g + q) * 384 + 64 * sf + 32 * (w & 1) + 8 * p;
    auto vo = [&](unsigned i) { const unsigned c = 64 * i + lane; return (8 * w + c / 24) * 1152 + 384 * n3 + c % 24 * 16 - 1024 * i; };
    const unsigned v_vo0 = vo(0), v_vo1 = vo(1), v_vo2 = vo(2);
    const unsigned v_xp = (6 * n3 + pf) * 1024 + v_l16, v_xs = (6 * n3 + sf) * 1024 + (w & 1) * 512 + half;
    const unsigned v_op = i16 * 1152 + 384 * n3 + 64 * pf + 16 * g, v_os = i16 * 1152 + 384 * n3 + 64 * sf + 32 * (w & 1) + 8 * g;
    const unsigned v_sw = 147456 + i16 * 136 + (4 * w + g) * 8, v_sr = 147456 + (4 * w + g) * 136 + i16 * 8;
    const unsigned v_ss = 18432 + n3 * 512 + w * 128 + g * 32 + p * 8;
    const unsigned s_ph = ks ? 6 - ks : 0, s_p0 = 0u - s_ph, r0 = gr * NREC + s_p0;
    const unsigned s_sh = WS_H + r0 * HREC + f0 * 1024, s_ps = WS_PS + r0 * PREC + (n3 * 20 + w) * PW;
    const unsigned s_x1 = WS_X1 + r0 * REC, s_xr = WS_XIN + (r0 + 33) * REC, s_or = (32 * gr + 63 - s_p0) * 18432;
    const unsigned s_m3 = w * 3072, s_m6 = w * 6144, s_so = ks * 768 * 1152;
    const unsigned s_i0 = (32 * gr + 31 - ks) * 18432, s_i5 = ks < 2 ? s_i0 - 30 * 18432 : OOB;
    const unsigned s_j0 = WS_XIN + (gr * NREC + ks) * REC, s_j5 = ks < 2 ? s_j0 + 30 * REC : OOB, s_jx = s_j0 + 66 * REC;
    asm volatile(
      "s_buffer_load_dword s21, s[8:11], 0\n"
      "s_mov_b32 s23, 65536\n"
      "s_mov_b32 s31, 0x40000000\n"
      "buffer_load_dwordx4 v[56:59], v8, s[92:95], s46 offen\n"
      "buffer_load_dwordx2 v[60:61], v9, s[92:95], s46 offen\n"
      "s_add_u32 s30, s46, 4294856704\n"
      "buffer_load_dwordx4 v[64:67], v8, s[92:95], s30 offen\n"
      "buffer_load_dwordx2 v[68:69], v9, s[92:95], s30 offen\n"
      "s_add_u32 s30, s46, 4294746112\n"
      "buffer_load_dwordx4 v[72:75], v8, s[92:95], s30 offen\n"
      "buffer_load_dwordx2 v[76:77], v9, s[92:95], s30 offen\n"
      "s_add_u32 s30, s46, 4294635520\n"
      "buffer_load_dwordx4 v[80:83], v8, s[92:95], s30 offen\n"
      "buffer_load_dwordx2 v[84:85], v9, s[92:95], s30 offen\n"
      "s_add_u32 s30, s46, 4294524928\n"
      "buffer_load_dwordx4 v[88:91], v8, s[92:95], s30 offen\n"
      "buffer_load_dwordx2 v[92:93], v9, s[92:95], s30 offen\n"
      "buffer_load_dwordx4 v[96:99], v8, s[92:95], s47 offen\n"
      "buffer_load_dwordx2 v[100:101], v9, s[92:95], s47 offen\n"
      "s_waitcnt lgkmcnt(0)\n"
      "s_mul_i32 s21, s21, 1664525\n"
      "s_add_u32 s21, s21, 1013904223\n"
      "v_mov_b32 v18, s21\n"
      "buffer_store_dword v18, v15, s[8:11], 0 offen sc1\n"
      "s_waitcnt vmcnt(11)\n"
      "buffer_store_dwordx4 v[56:59], v6, s[8:11], s48 offen sc1\n"
      "buffer_store_dwordx2 v[60:61], v7, s[8:11], s48 offen sc1\n"
      "v_lshlrev_b32 v164, 16, v56\n"
      "v_and_b32 v165, 0xffff0000, v56\n"
      "v_lshlrev_b32 v166, 16, v57\n"
      "v_and_b32 v167, 0xffff0000, v57\n"
      "v_lshlrev_b32 v168, 16, v58\n"
      "v_and_b32 v169, 0xffff0000, v58\n"
      "v_lshlrev_b32 v170, 16, v59\n"
      "v_and_b32 v171, 0xffff0000, v59\n"
      "v_lshlrev_b32 v172, 16, v60\n"
      "v_and_b32 v173, 0xffff0000, v60\n"
      "v_lshlrev_b32 v174, 16, v61\n"
      "v_and_b32 v175, 0xffff0000, v61\n"
      "v_add_f32 v62, v164, v165\n"
      "v_mul_f32 v63, v164, v164\n"
      "v_fmac_f32 v63, v165, v165\n"
      "v_add_f32 v62, v62, v166\n"
      "v_fmac_f32 v63, v166, v166\n"
      "v_add_f32 v62, v62, v167\n"
      "v_fmac_f32 v63, v167, v167\n"
      "v_add_f32 v62, v62, v168\n"
      "v_fmac_f32 v63, v168, v168\n"
      "v_add_f32 v62, v62, v169\n"
      "v_fmac_f32 v63, v169, v169\n"
      "v_add_f32 v62, v62, v170\n"
      "v_fmac_f32 v63, v170, v170\n"
      "v_add_f32 v62, v62, v171\n"
      "v_fmac_f32 v63, v171, v171\n"
      "v_add_f32 v62, v62, v172\n"
      "v_fmac_f32 v63, v172, v172\n"
      "v_add_f32 v62, v62, v173\n"
      "v_fmac_f32 v63, v173, v173\n"
      "v_add_f32 v62, v62, v174\n"
      "v_fmac_f32 v63, v174, v174\n"
      "v_add_f32 v62, v62, v175\n"
      "v_fmac_f32 v63, v175, v175\n"
      "ds_write_b64 v10, v[62:63] offset:0\n"
      "s_waitcnt lgkmcnt(0)\n"
      "s_barrier\n"
      "ds_read_b64 v[152:153], v11 offset:0\n"
      "s_waitcnt lgkmcnt(0)\n"
      "v_add_f32_dpp v152, v152, v152 row_ror:8 row_mask:0xf bank_mask:0xf\n"
      "v_add_f32_dpp v153, v153, v153 row_ror:8 row_mask:0xf bank_mask:0xf\n"
      "s_nop 0\n"
      "v_add_f32_dpp v152, v152, v152 row_ror:4 row_mask:0xf bank_mask:0xf\n"
      "v_add_f32_dpp v153, v153, v153 row_ror:4 row_mask:0xf bank_mask:0xf\n"
      "s_nop 0\n"
      "v_add_f32_dpp v152, v152, v152 row_ror:2 row_mask:0xf bank_mask:0xf\n"
      "v_add_f32_dpp v153, v153, v153 row_ror:2 row_mask:0xf bank_mask:0xf\n"
      "s_nop 0\n"
      "v_add_f32_dpp v152, v152, v152 row_ror:1 row_mask:0xf bank_mask:0xf\n"
      "v_add_f32_dpp v153, v153, v153 row_ror:1 row_mask:0xf bank_mask:0xf\n"
      "v_mul_f32 v152, 0x3ae38e39, v152\n"
      "v_add_f32 v153, 0x3afba882, v153\n"
      "v_mul_f32 v153, 0x3ae38e39, v153\n"
      "buffer_store_dwordx2 v[152:153], v12, s[8:11], s48 offen sc1\n"
      "s_waitcnt vmcnt(12)\n"
      "s_add_u32 s30, s48, 147456\n"
      "buffer_store_dwordx4 v[64:67], v6, s[8:11], s30 offen sc1\n"
      "buffer_store_dwordx2 v[68:69], v7, s[8:11], s30 offen sc1\n"
      "v_lshlrev_b32 v164, 16, v64\n"
      "v_and_b32 v165, 0xffff0000, v64\n"
      "v_lshlrev_b32 v166, 16, v65\n"
      "v_and_b32 v167, 0xffff0000, v65\n"
      "v_lshlrev_b32 v168, 16, v66\n"
      "v_and_b32 v169, 0xffff0000, v66\n"
      "v_lshlrev_b32 v170, 16, v67\n"
      "v_and_b32 v171, 0xffff0000, v67\n"
      "v_lshlrev_b32 v172, 16, v68\n"
      "v_and_b32 v173, 0xffff0000, v68\n"
      "v_lshlrev_b32 v174, 16, v69\n"
      "v_and_b32 v175, 0xffff0000, v69\n"
      "v_add_f32 v70, v164, v165\n"
      "v_mul_f32 v71, v164, v164\n"
      "v_fmac_f32 v71, v165, v165\n"
      "v_add_f32 v70, v70, v166\n"
      "v_fmac_f32 v71, v166, v166\n"
      "v_add_f32 v70, v70, v167\n"
      "v_fmac_f32 v71, v167, v167\n"
      "v_add_f32 v70, v70, v168\n"
      "v_fmac_f32 v71, v168, v168\n"
      "v_add_f32 v70, v70, v169\n"
      "v_fmac_f32 v71, v169, v169\n"
      "v_add_f32 v70, v70, v170\n"
      "v_fmac_f32 v71, v170, v170\n"
      "v_add_f32 v70, v70, v171\n"
      "v_fmac_f32 v71, v171, v171\n"
      "v_add_f32 v70, v70, v172\n"
      "v_fmac_f32 v71, v172, v172\n"
      "v_add_f32 v70, v70, v173\n"
      "v_fmac_f32 v71, v173, v173\n"
      "v_add_f32 v70, v70, v174\n"
      "v_fmac_f32 v71, v174, v174\n"
      "v_add_f32 v70, v70, v175\n"
      "v_fmac_f32 v71, v175, v175\n"
      "ds_write_b64 v10, v[70:71] offset:2176\n"
      "s_waitcnt vmcnt(2)\n"
      "buffer_store_dword v18, v5, s[8:11], s48 offen sc1\n"
      "s_add_u32 s30, s48, 294912\n"
      "buffer_store_dwordx4 v[72:75], v6, s[8:11], s30 offen sc1\n"
      "buffer_store_dwordx2 v[76:77], v7, s[8:11], s30 offen sc1\n"
      "v_lshlrev_b32 v164, 16, v72\n"
      "v_and_b32 v165, 0xffff0000, v72\n"
      "v_lshlrev_b32 v166, 16, v73\n"
      "v_and_b32 v167, 0xffff0000, v73\n"
      "v_lshlrev_b32 v168, 16, v74\n"
      "v_and_b32 v169, 0xffff0000, v74\n"
      "v_lshlrev_b32 v170, 16, v75\n"
      "v_and_b32 v171, 0xffff0000, v75\n"
      "v_lshlrev_b32 v172, 16, v76\n"
      "v_and_b32 v173, 0xffff0000, v76\n"
      "v_lshlrev_b32 v174, 16, v77\n"
      "v_and_b32 v175, 0xffff0000, v77\n"
      "v_add_f32 v78, v164, v165\n"
      "v_mul_f32 v79, v164, v164\n"
      "v_fmac_f32 v79, v165, v165\n"
      "v_add_f32 v78, v78, v166\n"
      "v_fmac_f32 v79, v166, v166\n"
      "v_add_f32 v78, v78, v167\n"
      "v_fmac_f32 v79, v167, v167\n"
      "v_add_f32 v78, v78, v168\n"
      "v_fmac_f32 v79, v168, v168\n"
      "v_add_f32 v78, v78, v169\n"
      "v_fmac_f32 v79, v169, v169\n"
      "v_add_f32 v78, v78, v170\n"
      "v_fmac_f32 v79, v170, v170\n"
      "v_add_f32 v78, v78, v171\n"
      "v_fmac_f32 v79, v171, v171\n"
      "v_add_f32 v78, v78, v172\n"
      "v_fmac_f32 v79, v172, v172\n"
      "v_add_f32 v78, v78, v173\n"
      "v_fmac_f32 v79, v173, v173\n"
      "v_add_f32 v78, v78, v174\n"
      "v_fmac_f32 v79, v174, v174\n"
      "v_add_f32 v78, v78, v175\n"
      "v_fmac_f32 v79, v175, v175\n"
      "ds_write_b64 v10, v[78:79] offset:4352\n"
      "s_add_u32 s30, s48, 442368\n"
      "buffer_store_dwordx4 v[80:83], v6, s[8:11], s30 offen sc1\n"
      "buffer_store_dwordx2 v[84:85], v7, s[8:11], s30 offen sc1\n"
      "v_lshlrev_b32 v164, 16, v80\n"
      "v_and_b32 v165, 0xffff0000, v80\n"
      "v_lshlrev_b32 v166, 16, v81\n"
      "v_and_b32 v167, 0xffff0000, v81\n"
      "v_lshlrev_b32 v168, 16, v82\n"
      "v_and_b32 v169, 0xffff0000, v82\n"
      "v_lshlrev_b32 v170, 16, v83\n"
      "v_and_b32 v171, 0xffff0000, v83\n"
      "v_lshlrev_b32 v172, 16, v84\n"
      "v_and_b32 v173, 0xffff0000, v84\n"
      "v_lshlrev_b32 v174, 16, v85\n"
      "v_and_b32 v175, 0xffff0000, v85\n"
      "v_add_f32 v86, v164, v165\n"
      "v_mul_f32 v87, v164, v164\n"
      "v_fmac_f32 v87, v165, v165\n"
      "v_add_f32 v86, v86, v166\n"
      "v_fmac_f32 v87, v166, v166\n"
      "v_add_f32 v86, v86, v167\n"
      "v_fmac_f32 v87, v167, v167\n"
      "v_add_f32 v86, v86, v168\n"
      "v_fmac_f32 v87, v168, v168\n"
      "v_add_f32 v86, v86, v169\n"
      "v_fmac_f32 v87, v169, v169\n"
      "v_add_f32 v86, v86, v170\n"
      "v_fmac_f32 v87, v170, v170\n"
      "v_add_f32 v86, v86, v171\n"
      "v_fmac_f32 v87, v171, v171\n"
      "v_add_f32 v86, v86, v172\n"
      "v_fmac_f32 v87, v172, v172\n"
      "v_add_f32 v86, v86, v173\n"
      "v_fmac_f32 v87, v173, v173\n"
      "v_add_f32 v86, v86, v174\n"
      "v_fmac_f32 v87, v174, v174\n"
      "v_add_f32 v86, v86, v175\n"
      "v_fmac_f32 v87, v175, v175\n"
      "ds_write_b64 v10, v[86:87] offset:6528\n"
      "s_add_u32 s30, s48, 589824\n"
      "buffer_store_dwordx4 v[88:91], v6, s[8:11], s30 offen sc1\n"
      "buffer_store_dwordx2 v[92:93], v7, s[8:11], s30 offen sc1\n"
      "v_lshlrev_b32 v164, 16, v88\n"
      "v_and_b32 v165, 0xffff0000, v88\n"
      "v_lshlrev_b32 v166, 16, v89\n"
      "v_and_b32 v167, 0xffff0000, v89\n"
      "v_lshlrev_b32 v168, 16, v90\n"
      "v_and_b32 v169, 0xffff0000, v90\n"
      "v_lshlrev_b32 v170, 16, v91\n"
      "v_and_b32 v171, 0xffff0000, v91\n"
      "v_lshlrev_b32 v172, 16, v92\n"
      "v_and_b32 v173, 0xffff0000, v92\n"
      "v_lshlrev_b32 v174, 16, v93\n"
      "v_and_b32 v175, 0xffff0000, v93\n"
      "v_add_f32 v94, v164, v165\n"
      "v_mul_f32 v95, v164, v164\n"
      "v_fmac_f32 v95, v165, v165\n"
      "v_add_f32 v94, v94, v166\n"
      "v_fmac_f32 v95, v166, v166\n"
      "v_add_f32 v94, v94, v167\n"
      "v_fmac_f32 v95, v167, v167\n"
      "v_add_f32 v94, v94, v168\n"
      "v_fmac_f32 v95, v168, v168\n"
      "v_add_f32 v94, v94, v169\n"
      "v_fmac_f32 v95, v169, v169\n"
      "v_add_f32 v94, v94, v170\n"
      "v_fmac_f32 v95, v170, v170\n"
      "v_add_f32 v94, v94, v171\n"
      "v_fmac_f32 v95, v171, v171\n"
      "v_add_f32 v94, v94, v172\n"
      "v_fmac_f32 v95, v172, v172\n"
      "v_add_f32 v94, v94, v173\n"
      "v_fmac_f32 v95, v173, v173\n"
      "v_add_f32 v94, v94, v174\n"
      "v_fmac_f32 v95, v174, v174\n"
      "v_add_f32 v94, v94, v175\n"
      "v_fmac_f32 v95, v175, v175\n"
      "ds_write_b64 v10, v[94:95] offset:8704\n"
      "buffer_store_dwordx4 v[96:99], v6, s[8:11], s49 offen sc1\n"
      "buffer_store_dwordx2 v[100:101], v7, s[8:11], s49 offen sc1\n"
      "v_lshlrev_b32 v164, 16, v96\n"
      "v_and_b32 v165, 0xffff0000, v96\n"
      "v_lshlrev_b32 v166, 16, v97\n"
      "v_and_b32 v167, 0xffff0000, v97\n"
      "v_lshlrev_b32 v168, 16, v98\n"
      "v_and_b32 v169, 0xffff0000, v98\n"
      "v_lshlrev_b32 v170, 16, v99\n"
      "v_and_b32 v171, 0xffff0000, v99\n"
      "v_lshlrev_b32 v172, 16, v100\n"
      "v_and_b32 v173, 0xffff0000, v100\n"
      "v_lshlrev_b32 v174, 16, v101\n"
      "v_and_b32 v175, 0xffff0000, v101\n"
      "v_add_f32 v102, v164, v165\n"
      "v_mul_f32 v103, v164, v164\n"
      "v_fmac_f32 v103, v165, v165\n"
      "v_add_f32 v102, v102, v166\n"
      "v_fmac_f32 v103, v166, v166\n"
      "v_add_f32 v102, v102, v167\n"
      "v_fmac_f32 v103, v167, v167\n"
      "v_add_f32 v102, v102, v168\n"
      "v_fmac_f32 v103, v168, v168\n"
      "v_add_f32 v102, v102, v169\n"
      "v_fmac_f32 v103, v169, v169\n"
      "v_add_f32 v102, v102, v170\n"
      "v_fmac_f32 v103, v170, v170\n"
      "v_add_f32 v102, v102, v171\n"
      "v_fmac_f32 v103, v171, v171\n"
      "v_add_f32 v102, v102, v172\n"
      "v_fmac_f32 v103, v172, v172\n"
      "v_add_f32 v102, v102, v173\n"
      "v_fmac_f32 v103, v173, v173\n"
      "v_add_f32 v102, v102, v174\n"
      "v_fmac_f32 v103, v174, v174\n"
      "v_add_f32 v102, v102, v175\n"
      "v_fmac_f32 v103, v175, v175\n"
      "ds_write_b64 v10, v[102:103] offset:10880\n"
      "s_waitcnt lgkmcnt(0)\n"
      "s_barrier\n"
      "ds_read_b64 v[154:155], v11 offset:2176\n"
      "ds_read_b64 v[156:157], v11 offset:4352\n"
      "ds_read_b64 v[158:159], v11 offset:6528\n"
      "ds_read_b64 v[160:161], v11 offset:8704\n"
      "ds_read_b64 v[162:163], v11 offset:10880\n"
      "s_waitcnt lgkmcnt(0)\n"
      "v_add_f32_dpp v154, v154, v154 row_ror:8 row_mask:0xf bank_mask:0xf\n"
      "v_add_f32_dpp v155, v155, v155 row_ror:8 row_mask:0xf bank_mask:0xf\n"
      "v_add_f32_dpp v156, v156, v156 row_ror:8 row_mask:0xf bank_mask:0xf\n"
      "v_add_f32_dpp v157, v157, v157 row_ror:8 row_mask:0xf bank_mask:0xf\n"
      "v_add_f32_dpp v158, v158, v158 row_ror:8 row_mask:0xf bank_mask:0xf\n"
      "v_add_f32_dpp v159, v159, v159 row_ror:8 row_mask:0xf bank_mask:0xf\n"
      "v_add_f32_dpp v160, v160, v160 row_ror:8 row_mask:0xf bank_mask:0xf\n"
      "v_add_f32_dpp v161, v161, v161 row_ror:8 row_mask:0xf bank_mask:0xf\n"
      "v_add_f32_dpp v162, v162, v162 row_ror:8 row_mask:0xf bank_mask:0xf\n"
      "v_add_f32_dpp v163, v163, v163 row_ror:8 row_mask:0xf bank_mask:0xf\n"
      "v_add_f32_dpp v154, v154, v154 row_ror:4 row_mask:0xf bank_mask:0xf\n"
      "v_add_f32_dpp v155, v155, v155 row_ror:4 row_mask:0xf bank_mask:0xf\n"
      "v_add_f32_dpp v156, v156, v156 row_ror:4 row_mask:0xf bank_mask:0xf\n"
      "v_add_f32_dpp v157, v157, v157 row_ror:4 row_mask:0xf bank_mask:0xf\n"
      "v_add_f32_dpp v158, v158, v158 row_ror:4 row_mask:0xf bank_mask:0xf\n"
      "v_add_f32_dpp v159, v159, v159 row_ror:4 row_mask:0xf bank_mask:0xf\n"
      "v_add_f32_dpp v160, v160, v160 row_ror:4 row_mask:0xf bank_mask:0xf\n"
      "v_add_f32_dpp v161, v161, v161 row_ror:4 row_mask:0xf bank_mask:0xf\n"
      "v_add_f32_dpp v162, v162, v162 row_ror:4 row_mask:0xf bank_mask:0xf\n"
      "v_add_f32_dpp v163, v163, v163 row_ror:4 row_mask:0xf bank_mask:0xf\n"
      "v_add_f32_dpp v154, v154, v154 row_ror:2 row_mask:0xf bank_mask:0xf\n"
      "v_add_f32_dpp v155, v155, v155 row_ror:2 row_mask:0xf bank_mask:0xf\n"
      "v_add_f32_dpp v156, v156, v156 row_ror:2 row_mask:0xf bank_mask:0xf\n"
      "v_add_f32_dpp v157, v157, v157 row_ror:2 row_mask:0xf bank_mask:0xf\n"
      "v_add_f32_dpp v158, v158, v158 row_ror:2 row_mask:0xf bank_mask:0xf\n"
      "v_add_f32_dpp v159, v159, v159 row_ror:2 row_mask:0xf bank_mask:0xf\n"
      "v_add_f32_dpp v160, v160, v160 row_ror:2 row_mask:0xf bank_mask:0xf\n"
      "v_add_f32_dpp v161, v161, v161 row_ror:2 row_mask:0xf bank_mask:0xf\n"
      "v_add_f32_dpp v162, v162, v162 row_ror:2 row_mask:0xf bank_mask:0xf\n"
      "v_add_f32_dpp v163, v163, v163 row_ror:2 row_mask:0xf bank_mask:0xf\n"
      "v_add_f32_dpp v154, v154, v154 row_ror:1 row_mask:0xf bank_mask:0xf\n"
      "v_add_f32_dpp v155, v155, v155 row_ror:1 row_mask:0xf bank_mask:0xf\n"
      "v_add_f32_dpp v156, v156, v156 row_ror:1 row_mask:0xf bank_mask:0xf\n"
      "v_add_f32_dpp v157, v157, v157 row_ror:1 row_mask:0xf bank_mask:0xf\n"
      "v_add_f32_dpp v158, v158, v158 row_ror:1 row_mask:0xf bank_mask:0xf\n"
      "v_add_f32_dpp v159, v159, v159 row_ror:1 row_mask:0xf bank_mask:0xf\n"
      "v_add_f32_dpp v160, v160, v160 row_ror:1 row_mask:0xf bank_mask:0xf\n"
      "v_add_f32_dpp v161, v161, v161 row_ror:1 row_mask:0xf bank_mask:0xf\n"
      "v_add_f32_dpp v162, v162, v162 row_ror:1 row_mask:0xf bank_mask:0xf\n"
      "v_add_f32_dpp v163, v163, v163 row_ror:1 row_mask:0xf bank_mask:0xf\n"
      "v_mul_f32 v154, 0x3ae38e39, v154\n"
      "v_add_f32 v155, 0x3afba882, v155\n"
      "v_mul_f32 v155, 0x3ae38e39, v155\n"
      "v_mul_f32 v156, 0x3ae38e39, v156\n"
      "v_add_f32 v157, 0x3afba882, v157\n"
      "v_mul_f32 v157, 0x3ae38e39, v157\n"
      "v_mul_f32 v158, 0x3ae38e39, v158\n"
      "v_add_f32 v159, 0x3afba882, v159\n"
      "v_mul_f32 v159, 0x3ae38e39, v159\n"
      "v_mul_f32 v160, 0x3ae38e39, v160\n"
      "v_add_f32 v161, 0x3afba882, v161\n"
      "v_mul_f32 v161, 0x3ae38e39, v161\n"
      "v_mul_f32 v162, 0x3ae38e39, v162\n"
      "v_add_f32 v163, 0x3afba882, v163\n"
      "v_mul_f32 v163, 0x3ae38e39, v163\n"
      "s_add_u32 s30, s48, 147456\n"
      "buffer_store_dwordx2 v[154:155], v12, s[8:11], s30 offen sc1\n"
      "s_add_u32 s30, s48, 294912\n"
      "buffer_store_dwordx2 v[156:157], v12, s[8:11], s30 offen sc1\n"
      "s_add_u32 s30, s48, 442368\n"
      "buffer_store_dwordx2 v[158:159], v12, s[8:11], s30 offen sc1\n"
      "s_add_u32 s30, s48, 589824\n"
      "buffer_store_dwordx2 v[160:161], v12, s[8:11], s30 offen sc1\n"
      "buffer_store_dwordx2 v[162:163], v12, s[8:11], s49 offen sc1\n"
      "s_waitcnt vmcnt(0)\n"
      "s_add_u32 s30, s48, 147456\n"
      "buffer_store_dword v18, v5, s[8:11], s30 offen sc1\n"
      "s_add_u32 s30, s48, 294912\n"
      "buffer_store_dword v18, v5, s[8:11], s30 offen sc1\n"
      "s_add_u32 s30, s48, 442368\n"
      "buffer_store_dword v18, v5, s[8:11], s30 offen sc1\n"
      "s_add_u32 s30, s48, 589824\n"
      "buffer_store_dword v18, v5, s[8:11], s30 offen sc1\n"
      "buffer_store_dword v18, v5, s[8:11], s49 offen sc1\n"
      "buffer_store_dword v18, v5, s[8:11], s50 offen sc1\n"
      "s_cmp_eq_u32 s45, 0\n"
      "s_cselect_b32 s30, 0x40000000, s37\n"
      "buffer_store_dword v18, v3, s[8:11], s30 offen sc1\n"
      "s_add_u32 m0, s42, 0\n"
      "s_nop 0\n"
      "buffer_load_dwordx4 v178, s[12:15], s44 offen offset:0 lds\n"
      "buffer_load_dwordx4 v179, s[12:15], s44 offen offset:1024 lds\n"
      "buffer_load_dwordx4 v180, s[12:15], s44 offen offset:2048 lds\n"
      "s_add_u32 s44, s44, 36864\n"
      "s_add_u32 m0, s42, 12288\n"
      "s_nop 0\n"
      "buffer_load_dwordx4 v178, s[12:15], s44 offen offset:0 lds\n"
      "buffer_load_dwordx4 v179, s[12:15], s44 offen offset:1024 lds\n"
      "buffer_load_dwordx4 v180, s[12:15], s44 offen offset:2048 lds\n"
      "s_add_u32 s44, s44, 36864\n"
      "s_add_u32 m0, s42, 24576\n"
      "s_nop 0\n"
      "buffer_load_dwordx4 v178, s[12:15], s44 offen offset:0 lds\n"
      "buffer_load_dwordx4 v179, s[12:15], s44 offen offset:1024 lds\n"
      "buffer_load_dwordx4 v180, s[12:15], s44 offen offset:2048 lds\n"
      "s_add_u32 s44, s44, 36864\n"
      "s_add_u32 m0, s42, 36864\n"
      "s_nop 0\n"
      "buffer_load_dwordx4 v178, s[12:15], s44 offen offset:0 lds\n"
      "buffer_load_dwordx4 v179, s[12:15], s44 offen offset:1024 lds\n"
      "buffer_load_dwordx4 v180, s[12:15], s44 offen offset:2048 lds\n"
      "s_add_u32 s44, s44, 36864\n"
      "s_add_u32 m0, s42, 49152\n"
      "s_nop 0\n"
      "buffer_load_dwordx4 v178, s[12:15], s44 offen offset:0 lds\n"
      "buffer_load_dwordx4 v179, s[12:15], s44 offen offset:1024 lds\n"
      "buffer_load_dwordx4 v180, s[12:15], s44 offen offset:2048 lds\n"
      "s_add_u32 s44, s44, 36864\n"
      "s_add_u32 m0, s42, 61440\n"
      "s_nop 0\n"
      "buffer_load_dwordx4 v178, s[12:15], s44 offen offset:0 lds\n"
      "buffer_load_dwordx4 v179, s[12:15], s44 offen offset:1024 lds\n"
      "buffer_load_dwordx4 v180, s[12:15], s44 offen offset:2048 lds\n"
      "s_add_u32 s44, s44, 36864\n"
      "s_add_u32 m0, s42, 73728\n"
      "s_nop 0\n"
      "buffer_load_dwordx4 v178, s[12:15], s44 offen offset:0 lds\n"
      "buffer_load_dwordx4 v179, s[12:15], s44 offen offset:1024 lds\n"
      "buffer_load_dwordx4 v180, s[12:15], s44 offen offset:2048 lds\n"
      "s_add_u32 s44, s44, 36864\n"
      "s_add_u32 m0, s42, 86016\n"
      "s_nop 0\n"
      "buffer_load_dwordx4 v178, s[12:15], s44 offen offset:0 lds\n"
      "buffer_load_dwordx4 v179, s[12:15], s44 offen offset:1024 lds\n"
      "buffer_load_dwordx4 v180, s[12:15], s44 offen offset:2048 lds\n"
      "s_add_u32 s44, s44, 36864\n"
      "s_add_u32 m0, s42, 98304\n"
      "s_nop 0\n"
      "buffer_load_dwordx4 v178, s[12:15], s44 offen offset:0 lds\n"
      "buffer_load_dwordx4 v179, s[12:15], s44 offen offset:1024 lds\n"
      "buffer_load_dwordx4 v180, s[12:15], s44 offen offset:2048 lds\n"
      "s_add_u32 s44, s44, 36864\n"
      "v_add_u32 v20, 61440, v176\n"
      "v_add_u32 v21, 61440, v177\n"
      "s_waitcnt vmcnt(24)\n"
      "s_barrier\n"
      "ds_read_b64_tr_b16 a[0:1], v176 offset:0\n"
      "ds_read_b64_tr_b16 a[2:3], v176 offset:1536\n"
      "ds_read_b64_tr_b16 a[96:97], v176 offset:8\n"
      "ds_read_b64_tr_b16 a[98:99], v176 offset:1544\n"
      "ds_read_b64_tr_b16 a[192:193], v177 offset:0\n"
      "ds_read_b64_tr_b16 a[194:195], v177 offset:1536\n"
      "s_add_u32 m0, s42, 110592\n"
      "s_nop 0\n"
      "buffer_load_dwordx4 v178, s[12:15], s44 offen offset:0 lds\n"
      "buffer_load_dwordx4 v179, s[12:15], s44 offen offset:1024 lds\n"
      "buffer_load_dwordx4 v180, s[12:15], s44 offen offset:2048 lds\n"
      "s_add_u32 s44, s44, 36864\n"
      "s_waitcnt vmcnt(24) lgkmcnt(0)\n"
      "s_barrier\n"
      "ds_read_b64_tr_b16 a[4:5], v176 offset:12288\n"
      "ds_read_b64_tr_b16 a[6:7], v176 offset:13824\n"
      "ds_read_b64_tr_b16 a[100:101], v176 offset:12296\n"
      "ds_read_b64_tr_b16 a[102:103], v176 offset:13832\n"
      "ds_read_b64_tr_b16 a[196:197], v177 offset:12288\n"
      "ds_read_b64_tr_b16 a[198:199], v177 offset:13824\n"
      "s_add_u32 m0, s42, 0\n"
      "s_nop 0\n"
      "buffer_load_dwordx4 v178, s[12:15], s44 offen offset:0 lds\n"
      "buffer_load_dwordx4 v179, s[12:15], s44 offen offset:1024 lds\n"
      "buffer_load_dwordx4 v180, s[12:15], s44 offen offset:2048 lds\n"
      "s_add_u32 s44, s44, 36864\n"
      "s_waitcnt vmcnt(24) lgkmcnt(0)\n"
      "s_barrier\n"
      "ds_read_b64_tr_b16 a[8:9], v176 offset:24576\n"
      "ds_read_b64_tr_b16 a[10:11], v176 offset:26112\n"
      "ds_read_b64_tr_b16 a[104:105], v176 offset:24584\n"
      "ds_read_b64_tr_b16 a[106:107], v176 offset:26120\n"
      "ds_read_b64_tr_b16 a[200:201], v177 offset:24576\n"
      "ds_read_b64_tr_b16 a[202:203], v177 offset:26112\n"
      "s_add_u32 m0, s42, 12288\n"
      "s_nop 0\n"
      "buffer_load_dwordx4 v178, s[12:15], s44 offen offset:0 lds\n"
      "buffer_load_dwordx4 v179, s[12:15], s44 offen offset:1024 lds\n"
      "buffer_load_dwordx4 v180, s[12:15], s44 offen offset:2048 lds\n"
      "s_add_u32 s44, s44, 36864\n"
      "s_waitcnt vmcnt(24) lgkmcnt(0)\n"
      "s_barrier\n"
      "ds_read_b64_tr_b16 a[12:13], v176 offset:36864\n"
      "ds_read_b64_tr_b16 a[14:15], v176 offset:38400\n"
      "ds_read_b64_tr_b16 a[108:109], v176 offset:36872\n"
      "ds_read_b64_tr_b16 a[110:111], v176 offset:38408\n"
      "ds_read_b64_tr_b16 a[204:205], v177 offset:36864\n"
      "ds_read_b64_tr_b16 a[206:207], v177 offset:38400\n"
      "s_add_u32 m0, s42, 24576\n"
      "s_nop 0\n"
      "buffer_load_dwordx4 v178, s[12:15], s44 offen offset:0 lds\n"
      "buffer_load_dwordx4 v179, s[12:15], s44 offen offset:1024 lds\n"
      "buffer_load_dwordx4 v180, s[12:15], s44 offen offset:2048 lds\n"
      "s_add_u32 s44, s44, 36864\n"
      "s_waitcnt vmcnt(24) lgkmcnt(0)\n"
      "s_barrier\n"
      "ds_read_b64_tr_b16 a[16:17], v176 offset:49152\n"
      "ds_read_b64_tr_b16 a[18:19], v176 offset:50688\n"
      "ds_read_b64_tr_b16 a[112:113], v176 offset:49160\n"
      "ds_read_b64_tr_b16 a[114:115], v176 offset:50696\n"
      "ds_read_b64_tr_b16 a[208:209], v177 offset:49152\n"
      "ds_read_b64_tr_b16 a[210:211], v177 offset:50688\n"
      "s_add_u32 m0, s42, 36864\n"
      "s_nop 0\n"
      "buffer_load_dwordx4 v178, s[12:15], s44 offen offset:0 lds\n"
      "buffer_load_dwordx4 v179, s[12:15], s44 offen offset:1024 lds\n"
      "buffer_load_dwordx4 v180, s[12:15], s44 offen offset:2048 lds\n"
      "s_add_u32 s44, s44, 36864\n"
      "s_waitcnt vmcnt(24) lgkmcnt(0)\n"
      "s_barrier\n"
      "ds_read_b64_tr_b16 a[20:21], v20 offset:0\n"
      "ds_read_b64_tr_b16 a[22:23], v20 offset:1536\n"
      "ds_read_b64_tr_b16 a[116:117], v20 offset:8\n"
      "ds_read_b64_tr_b16 a[118:119], v20 offset:1544\n"
      "ds_read_b64_tr_b16 a[212:213], v21 offset:0\n"
      "ds_read_b64_tr_b16 a[214:215], v21 offset:1536\n"
      "s_add_u32 m0, s42, 49152\n"
      "s_nop 0\n"
      "buffer_load_dwordx4 v178, s[12:15], s44 offen offset:0 lds\n"
      "buffer_load_dwordx4 v179, s[12:15], s44 offen offset:1024 lds\n"
      "buffer_load_dwordx4 v180, s[12:15], s44 offen offset:2048 lds\n"
      "s_add_u32 s44, s44, 36864\n"
      "s_waitcnt vmcnt(24) lgkmcnt(0)\n"
      "s_barrier\n"
      "ds_read_b64_tr_b16 a[24:25], v20 offset:12288\n"
      "ds_read_b64_tr_b16 a[26:27], v20 offset:13824\n"
      "ds_read_b64_tr_b16 a[120:121], v20 offset:12296\n"
      "ds_read_b64_tr_b16 a[122:123], v20 offset:13832\n"
      "ds_read_b64_tr_b16 a[216:217], v21 offset:12288\n"
      "ds_read_b64_tr_b16 a[218:219], v21 offset:13824\n"
      "s_add_u32 m0, s42, 61440\n"
      "s_nop 0\n"
      "buffer_load_dwordx4 v178, s[12:15], s44 offen offset:0 lds\n"
      "buffer_load_dwordx4 v179, s[12:15], s44 offen offset:1024 lds\n"
      "buffer_load_dwordx4 v180, s[12:15], s44 offen offset:2048 lds\n"
      "s_add_u32 s44, s44, 36864\n"
      "s_waitcnt vmcnt(24) lgkmcnt(0)\n"
      "s_barrier\n"
      "ds_read_b64_tr_b16 a[28:29], v20 offset:24576\n"
      "ds_read_b64_tr_b16 a[30:31], v20 offset:26112\n"
      "ds_read_b64_tr_b16 a[124:125], v20 offset:24584\n"
      "ds_read_b64_tr_b16 a[126:127], v20 offset:26120\n"
      "ds_read_b64_tr_b16 a[220:221], v21 offset:24576\n"
      "ds_read_b64_tr_b16 a[222:223], v21 offset:26112\n"
      "s_add_u32 m0, s42, 73728\n"
      "s_nop 0\n"
      "buffer_load_dwordx4 v178, s[12:15], s44 offen offset:0 lds\n"
      "buffer_load_dwordx4 v179, s[12:15], s44 offen offset:1024 lds\n"
      "buffer_load_dwordx4 v180, s[12:15], s44 offen offset:2048 lds\n"
      "s_add_u32 s44, s44, 36864\n"
      "s_waitcnt vmcnt(24) lgkmcnt(0)\n"
      "s_barrier\n"
      "ds_read_b64_tr_b16 a[32:33], v20 offset:36864\n"
      "ds_read_b64_tr_b16 a[34:35], v20 offset:38400\n"
      "ds_read_b64_tr_b16 a[128:129], v20 offset:36872\n"
      "ds_read_b64_tr_b16 a[130:131], v20 offset:38408\n"
      "ds_read_b64_tr_b16 a[224:225], v21 offset:36864\n"
      "ds_read_b64_tr_b16 a[226:227], v21 offset:38400\n"
      "s_add_u32 m0, s42, 86016\n"
      "s_nop 0\n"
      "buffer_load_dwordx4 v178, s[12:15], s44 offen offset:0 lds\n"
      "buffer_load_dwordx4 v179, s[12:15], s44 offen offset:1024 lds\n"
      "buffer_load_dwordx4 v180, s[12:15], s44 offen offset:2048 lds\n"
      "s_add_u32 s44, s44, 36864\n"
      "s_waitcnt vmcnt(24) lgkmcnt(0)\n"
      "s_barrier\n"
      "ds_read_b64_tr_b16 a[36:37], v20 offset:49152\n"
      "ds_read_b64_tr_b16 a[38:39], v20 offset:50688\n"
      "ds_read_b64_tr_b16 a[132:133], v20 offset:49160\n"
      "ds_read_b64_tr_b16 a[134:135], v20 offset:50696\n"
      "ds_read_b64_tr_b16 a[228:229], v21 offset:49152\n"
      "ds_read_b64_tr_b16 a[230:231], v21 offset:50688\n"
      "s_add_u32 m0, s42, 98304\n"
      "s_nop 0\n"
      "buffer_load_dwordx4 v178, s[12:15], s44 offen offset:0 lds\n"
      "buffer_load_dwordx4 v179, s[12:15], s44 offen offset:1024 lds\n"
      "buffer_load_dwordx4 v180, s[12:15], s44 offen offset:2048 lds\n"
      "s_add_u32 s44, s44, 36864\n"
      "s_waitcnt vmcnt(24) lgkmcnt(0)\n"
      "s_barrier\n"
      "ds_read_b64_tr_b16 a[40:41], v176 offset:0\n"
      "ds_read_b64_tr_b16 a[42:43], v176 offset:1536\n"
      "ds_read_b64_tr_b16 a[136:137], v176 offset:8\n"
      "ds_read_b64_tr_b16 a[138:139], v176 offset:1544\n"
      "ds_read_b64_tr_b16 a[232:233], v177 offset:0\n"
      "ds_read_b64_tr_b16 a[234:235], v177 offset:1536\n"
      "s_add_u32 m0, s42, 110592\n"
      "s_nop 0\n"
      "buffer_load_dwordx4 v178, s[12:15], s44 offen offset:0 lds\n"
      "buffer_load_dwordx4 v179, s[12:15], s44 offen offset:1024 lds\n"
      "buffer_load_dwordx4 v180, s[12:15], s44 offen offset:2048 lds\n"
      "s_add_u32 s44, s44, 36864\n"
      "s_waitcnt vmcnt(24) lgkmcnt(0)\n"
      "s_barrier\n"
      "ds_read_b64_tr_b16 a[44:45], v176 offset:12288\n"
      "ds_read_b64_tr_b16 a[46:47], v176 offset:13824\n"
      "ds_read_b64_tr_b16 a[140:141], v176 offset:12296\n"
      "ds_read_b64_tr_b16 a[142:143], v176 offset:13832\n"
      "ds_read_b64_tr_b16 a[236:237], v177 offset:12288\n"
      "ds_read_b64_tr_b16 a[238:239], v177 offset:13824\n"
      "s_add_u32 m0, s42, 0\n"
      "s_nop 0\n"
      "buffer_load_dwordx4 v178, s[12:15], s44 offen offset:0 lds\n"
      "buffer_load_dwordx4 v179, s[12:15], s44 offen offset:1024 lds\n"
      "buffer_load_dwordx4 v180, s[12:15], s44 offen offset:2048 lds\n"
      "s_add_u32 s44, s44, 36864\n"
      "s_waitcnt vmcnt(24) lgkmcnt(0)\n"
      "s_barrier\n"
      "ds_read_b64_tr_b16 a[48:49], v176 offset:24576\n"
      "ds_read_b64_tr_b16 a[50:51], v176 offset:26112\n"
      "ds_read_b64_tr_b16 a[144:145], v176 offset:24584\n"
      "ds_read_b64_tr_b16 a[146:147], v176 offset:26120\n"
      "ds_read_b64_tr_b16 a[240:241], v177 offset:24576\n"
      "ds_read_b64_tr_b16 a[242:243], v177 offset:26112\n"
      "s_add_u32 m0, s42, 12288\n"
      "s_nop 0\n"
      "buffer_load_dwordx4 v178, s[12:15], s44 offen offset:0 lds\n"
      "buffer_load_dwordx4 v179, s[12:15], s44 offen offset:1024 lds\n"
      "buffer_load_dwordx4 v180, s[12:15], s44 offen offset:2048 lds\n"
      "s_add_u32 s44, s44, 36864\n"
      "s_waitcnt vmcnt(24) lgkmcnt(0)\n"
      "s_barrier\n"
      "ds_read_b64_tr_b16 a[52:53], v176 offset:36864\n"
      "ds_read_b64_tr_b16 a[54:55], v176 offset:38400\n"
      "ds_read_b64_tr_b16 a[148:149], v176 offset:36872\n"
      "ds_read_b64_tr_b16 a[150:151], v176 offset:38408\n"
      "ds_read_b64_tr_b16 a[244:245], v177 offset:36864\n"
      "ds_read_b64_tr_b16 a[246:247], v177 offset:38400\n"
      "s_add_u32 m0, s42, 24576\n"
      "s_nop 0\n"
      "buffer_load_dwordx4 v178, s[12:15], s44 offen offset:0 lds\n"
      "buffer_load_dwordx4 v179, s[12:15], s44 offen offset:1024 lds\n"
      "buffer_load_dwordx4 v180, s[12:15], s44 offen offset:2048 lds\n"
      "s_add_u32 s44, s44, 36864\n"
      "s_waitcnt vmcnt(24) lgkmcnt(0)\n"
      "s_barrier\n"
      "ds_read_b64_tr_b16 a[56:57], v176 offset:49152\n"
      "ds_read_b64_tr_b16 a[58:59], v176 offset:50688\n"
      "ds_read_b64_tr_b16 a[152:153], v176 offset:49160\n"
      "ds_read_b64_tr_b16 a[154:155], v176 offset:50696\n"
      "ds_read_b64_tr_b16 a[248:249], v177 offset:49152\n"
      "ds_read_b64_tr_b16 a[250:251], v177 offset:50688\n"
      "s_add_u32 m0, s42, 36864\n"
      "s_nop 0\n"
      "buffer_load_dwordx4 v178, s[12:15], s44 offen offset:0 lds\n"
      "buffer_load_dwordx4 v179, s[12:15], s44 offen offset:1024 lds\n"
      "buffer_load_dwordx4 v180, s[12:15], s44 offen offset:2048 lds\n"
      "s_add_u32 s44, s44, 36864\n"
      "s_waitcnt vmcnt(24) lgkmcnt(0)\n"
      "s_barrier\n"
      "ds_read_b64_tr_b16 a[60:61], v20 offset:0\n"
      "ds_read_b64_tr_b16 a[62:63], v20 offset:1536\n"
      "ds_read_b64_tr_b16 a[156:157], v20 offset:8\n"
      "ds_read_b64_tr_b16 a[158:159], v20 offset:1544\n"
      "ds_read_b64_tr_b16 a[252:253], v21 offset:0\n"
      "ds_read_b64_tr_b16 a[254:255], v21 offset:1536\n"
      "s_waitcnt vmcnt(21) lgkmcnt(0)\n"
      "s_barrier\n"
      "ds_read_b64_tr_b16 a[64:65], v20 offset:12288\n"
      "ds_read_b64_tr_b16 a[66:67], v20 offset:13824\n"
      "ds_read_b64_tr_b16 a[160:161], v20 offset:12296\n"
      "ds_read_b64_tr_b16 a[162:163], v20 offset:13832\n"
      "ds_read_b64_tr_b16 v[24:25], v21 offset:12288\n"
      "ds_read_b64_tr_b16 v[26:27], v21 offset:13824\n"
      "s_waitcnt vmcnt(18) lgkmcnt(0)\n"
      "s_barrier\n"
      "ds_read_b64_tr_b16 a[68:69], v20 offset:24576\n"
      "ds_read_b64_tr_b16 a[70:71], v20 offset:26112\n"
      "ds_read_b64_tr_b16 a[164:165], v20 offset:24584\n"
      "ds_read_b64_tr_b16 a[166:167], v20 offset:26120\n"
      "ds_read_b64_tr_b16 v[28:29], v21 offset:24576\n"
      "ds_read_b64_tr_b16 v[30:31], v21 offset:26112\n"
      "s_waitcnt vmcnt(15) lgkmcnt(0)\n"
      "s_barrier\n"
      "ds_read_b64_tr_b16 a[72:73], v20 offset:36864\n"
      "ds_read_b64_tr_b16 a[74:75], v20 offset:38400\n"
      "ds_read_b64_tr_b16 a[168:169], v20 offset:36872\n"
      "ds_read_b64_tr_b16 a[170:171], v20 offset:38408\n"
      "ds_read_b64_tr_b16 v[32:33], v21 offset:36864\n"
      "ds_read_b64_tr_b16 v[34:35], v21 offset:38400\n"
      "s_waitcnt vmcnt(12) lgkmcnt(0)\n"
      "s_barrier\n"
      "ds_read_b64_tr_b16 a[76:77], v20 offset:49152\n"
      "ds_read_b64_tr_b16 a[78:79], v20 offset:50688\n"
      "ds_read_b64_tr_b16 a[172:173], v20 offset:49160\n"
      "ds_read_b64_tr_b16 a[174:175], v20 offset:50696\n"
      "ds_read_b64_tr_b16 v[36:37], v21 offset:49152\n"
      "ds_read_b64_tr_b16 v[38:39], v21 offset:50688\n"
      "s_waitcnt vmcnt(9) lgkmcnt(0)\n"
      "s_barrier\n"
      "ds_read_b64_tr_b16 a[80:81], v176 offset:0\n"
      "ds_read_b64_tr_b16 a[82:83], v176 offset:1536\n"
      "ds_read_b64_tr_b16 a[176:177], v176 offset:8\n"
      "ds_read_b64_tr_b16 a[178:179], v176 offset:1544\n"
      "ds_read_b64_tr_b16 v[40:41], v177 offset:0\n"
      "ds_read_b64_tr_b16 v[42:43], v177 offset:1536\n"
      "s_waitcnt vmcnt(6) lgkmcnt(0)\n"
      "s_barrier\n"
      "ds_read_b64_tr_b16 a[84:85], v176 offset:12288\n"
      "ds_read_b64_tr_b16 a[86:87], v176 offset:13824\n"
      "ds_read_b64_tr_b16 a[180:181], v176 offset:12296\n"
      "ds_read_b64_tr_b16 a[182:183], v176 offset:13832\n"
      "ds_read_b64_tr_b16 v[44:45], v177 offset:12288\n"
      "ds_read_b64_tr_b16 v[46:47], v177 offset:13824\n"
      "s_waitcnt vmcnt(3) lgkmcnt(0)\n"
      "s_barrier\n"
      "ds_read_b64_tr_b16 a[88:89], v176 offset:24576\n"
      "ds_read_b64_tr_b16 a[90:91], v176 offset:26112\n"
      "ds_read_b64_tr_b16 a[184:185], v176 offset:24584\n"
      "ds_read_b64_tr_b16 a[186:187], v176 offset:26120\n"
      "ds_read_b64_tr_b16 v[48:49], v177 offset:24576\n"
      "ds_read_b64_tr_b16 v[50:51], v177 offset:26112\n"
      "s_waitcnt vmcnt(0) lgkmcnt(0)\n"
      "s_barrier\n"
      "ds_read_b64_tr_b16 a[92:93], v176 offset:36864\n"
      "ds_read_b64_tr_b16 a[94:95], v176 offset:38400\n"
      "ds_read_b64_tr_b16 a[188:189], v176 offset:36872\n"
      "ds_read_b64_tr_b16 a[190:191], v176 offset:38408\n"
      "ds_read_b64_tr_b16 v[52:53], v177 offset:36864\n"
      "ds_read_b64_tr_b16 v[54:55], v177 offset:38400\n"
      "s_waitcnt lgkmcnt(0)\n"
      "v_add_u32 v22, 49152, v0\n"
      "v_add_u32 v23, 98304, v0\n"
      "v_not_b32 v56, v18\n"
      "v_not_b32 v57, v18\n"
      "v_not_b32 v58, v18\n"
      "v_not_b32 v59, v18\n"
      "s_add_u32 s30, s37, 0\n"
      "buffer_store_dwordx4 v[56:59], v183, s[8:11], s30 offen sc1\n"
      "s_add_u32 s30, s37, 0\n"
      "buffer_store_dwordx4 v[56:59], v181, s[8:11], s30 offen sc1\n"
      "s_add_u32 s30, s37, 599040\n"
      "buffer_store_dwordx4 v[56:59], v181, s[8:11], s30 offen sc1\n"
      "s_add_u32 s30, s37, 1198080\n"
      "buffer_store_dwordx4 v[56:59], v181, s[8:11], s30 offen sc1\n"
      "s_add_u32 s30, s37, 1797120\n"
      "buffer_store_dwordx4 v[56:59], v181, s[8:11], s30 offen sc1\n"
      "s_add_u32 s30, s37, 2396160\n"
      "buffer_store_dwordx4 v[56:59], v181, s[8:11], s30 offen sc1\n"
      "s_add_u32 s30, s37, 2995200\n"
      "buffer_store_dwordx4 v[56:59], v181, s[8:11], s30 offen sc1\n"
      "s_add_u32 s30, s37, 3594240\n"
      "buffer_store_dwordx4 v[56:59], v181, s[8:11], s30 offen sc1\n"
      "s_add_u32 s30, s37, 4193280\n"
      "buffer_store_dwordx4 v[56:59], v181, s[8:11], s30 offen sc1\n"
      "s_add_u32 s30, s37, 4792320\n"
      "buffer_store_dwordx4 v[56:59], v181, s[8:11], s30 offen sc1\n"
      "s_add_u32 s30, s37, 5391360\n"
      "buffer_store_dwordx4 v[56:59], v181, s[8:11], s30 offen sc1\n"
      "s_add_u32 s30, s37, 5990400\n"
      "buffer_store_dwordx4 v[56:59], v181, s[8:11], s30 offen sc1\n"
      "s_add_u32 s30, s48, 0\n"
      "buffer_store_dwordx4 v[56:59], v182, s[8:11], s30 offen sc1\n"
      "s_barrier\n"
      "v_mov_b32 v14, 0\n"
      "ds_write_b32 v16, v14\n"
      "s_waitcnt lgkmcnt(0)\n"
      "s_barrier\n"
      "s_add_u32 s36, s36, 4294801408\n"
      "s_add_u32 s53, s37, 0\n"
      "s_add_u32 s52, s37, 4294681088\n"
      "s_add_u32 s37, s37, 4294894080\n"
      "s_mov_b32 s56, 0x40000000\n"
      "s_mov_b32 s57, 0x40000000\n"
      "s_mov_b32 s59, 63\n"
      "s_cmp_eq_u32 s45, 1\n"
      "s_cbranch_scc1 .Ld_in1\n"
      "s_cmp_eq_u32 s45, 2\n"
      "s_cbranch_scc1 .Ld_in2\n"
      "s_cmp_eq_u32 s45, 3\n"
      "s_cbranch_scc1 .Ld_in3\n"
      "s_cmp_eq_u32 s45, 4\n"
      "s_cbranch_scc1 .Ld_in4\n"
      "s_cmp_eq_u32 s45, 5\n"
      "s_cbranch_scc1 .Ld_in5\n"
      "s_add_u32 s36, s36, 165888\n"
      "buffer_load_dword v20, v2, s[8:11], s36 offen sc1\n"
      "s_mov_b32 s51, s36\n"
      "s_add_u32 s36, s36, 165888\n"
      "s_add_u32 m0, s43, 0\n"
      "s_waitcnt vmcnt(0)\n"
      "v_cmp_ne_u32 vcc, s21, v20\n"
      "s_cbranch_vccnz .Lsp_d0_1\n"
      ".Lbk_d0_1:\n"
      "buffer_load_dwordx4 v0, s[8:11], s51 offen offset:0 sc1 lds\n"
      "buffer_load_dwordx4 v0, s[8:11], s51 offen offset:2048 sc1 lds\n"
      "buffer_load_dwordx4 v0, s[8:11], s51 offen offset:3072 sc1 lds\n"
      "buffer_load_dwordx4 v0, s[8:11], s51 offen offset:1024 sc1 lds\n"
      "s_add_u32 m0, s43, 4096\n"
      "s_nop 0\n"
      "buffer_load_dwordx4 v1, s[8:11], s51 offen offset:0 sc1 lds\n"
      "buffer_load_dword v20, v2, s[8:11], s36 offen sc1\n"
      "buffer_load_dwordx4 v1, s[8:11], s51 offen offset:1024 sc1 lds\n"
      "s_mov_b32 s51, s36\n"
      "buffer_load_dwordx4 v[216:219], v6, s[8:11], s38 offen sc1\n"
      "s_add_u32 m0, s43, 24576\n"
      "v_add_u32 v14, 1, v14\n"
      "buffer_load_dwordx2 v[220:221], v7, s[8:11], s38 offen sc1\n"
      "s_add_u32 s36, s36, 165888\n"
      "s_waitcnt vmcnt(3)\n"
      "v_cmp_ne_u32 vcc, s21, v20\n"
      "s_add_u32 s38, s38, 147456\n"
      "s_cbranch_vccnz .Lsp_d1_2\n"
      ".Lbk_d1_2:\n"
      "buffer_load_dword v20, v2, s[8:11], s36 offen sc1\n"
      "s_waitcnt vmcnt(3)\n"
      "ds_write_b32 v16, v14\n"
      "buffer_load_dwordx4 v0, s[8:11], s51 offen offset:0 sc1 lds\n"
      "buffer_load_dwordx4 v0, s[8:11], s51 offen offset:1024 sc1 lds\n"
      "buffer_load_dwordx4 v0, s[8:11], s51 offen offset:2048 sc1 lds\n"
      "ds_read_b32 v15, v17\n"
      "buffer_load_dwordx4 v0, s[8:11], s51 offen offset:3072 sc1 lds\n"
      "s_add_u32 m0, s43, 28672\n"
      "s_nop 0\n"
      "buffer_load_dwordx4 v1, s[8:11], s51 offen offset:1024 sc1 lds\n"
      "s_waitcnt lgkmcnt(0)\n"
      "v_cmp_gt_i32 vcc, v14, v15\n"
      "s_cbranch_vccnz .Lsp_dy0_3\n"
      ".Lbk_dy0_3:\n"
      "buffer_load_dwordx4 v1, s[8:11], s51 offen offset:0 sc1 lds\n"
      "ds_read_b128 v[60:63], v0 offset:1024\n"
      "ds_read_b128 v[92:95], v0 offset:9216\n"
      "ds_read_b128 v[56:59], v0 offset:0\n"
      "ds_read_b128 v[64:67], v0 offset:2048\n"
      "ds_read_b128 v[68:71], v0 offset:3072\n"
      "ds_read_b128 v[72:75], v0 offset:4096\n"
      "ds_read_b128 v[88:91], v0 offset:8192\n"
      "ds_read_b128 v[84:87], v0 offset:7168\n"
      "ds_read_b128 v[96:99], v0 offset:10240\n"
      "ds_read_b128 v[80:83], v0 offset:6144\n"
      "ds_read_b128 v[76:79], v0 offset:5120\n"
      "ds_read_b128 v[100:103], v0 offset:11264\n"
      "ds_read_b128 v[140:143], v0 offset:21504\n"
      "s_waitcnt vmcnt(0) lgkmcnt(0)\n"
      "s_nop 4\n"
      ".p2align 6\n"
      ".Ld_t0:\n"
      "v_mfma_f32_16x16x32_bf16 v[152:155], a[0:3], v[56:59], 0\n"
      "ds_read_b128 v[112:115], v0 offset:14336\n"
      "ds_read_b128 v[124:127], v0 offset:17408\n"
      "v_mfma_f32_16x16x32_bf16 v[152:155], a[4:7], v[60:63], v[152:155]\n"
      "s_add_u32 s52, s52, 106496\n"
      "s_mov_b32 s51, s36\n"
      "ds_read_b128 v[116:119], v0 offset:15360\n"
      "v_mfma_f32_16x16x32_bf16 v[152:155], a[8:11], v[64:67], v[152:155]\n"
      "ds_read_b128 v[108:111], v0 offset:13312\n"
      "v_cvt_pk_bf16_f32 v229, v162, v163\n"
      "v_mfma_f32_16x16x32_bf16 v[152:155], a[12:15], v[68:71], v[152:155]\n"
      "s_waitcnt lgkmcnt(7)\n"
      "ds_read_b128 v[104:107], v0 offset:12288\n"
      "v_add_f32_dpp v239, v239, v239 row_ror:8 row_mask:0xf bank_mask:0xf\n"
      "v_mfma_f32_16x16x32_bf16 v[152:155], a[16:19], v[72:75], v[152:155]\n"
      "s_waitcnt lgkmcnt(7)\n"
      "buffer_store_dwordx4 v[176:179], v8, s[16:19], s57 offen\n"
      "v_mfma_f32_16x16x32_bf16 v[152:155], a[20:23], v[76:79], v[152:155]\n"
      "ds_read_b128 v[120:123], v0 offset:16384\n"
      "v_cvt_pk_bf16_f32 v228, v160, v161\n"
      "v_mfma_f32_16x16x32_bf16 v[152:155], a[24:27], v[80:83], v[152:155]\n"
      "v_add_f32_dpp v238, v238, v238 row_ror:8 row_mask:0xf bank_mask:0xf\n"
      "v_mfma_f32_16x16x32_bf16 v[152:155], a[28:31], v[84:87], v[152:155]\n"
      "ds_read_b128 v[132:135], v0 offset:19456\n"
      "v_add_f32_dpp v239, v239, v239 row_ror:4 row_mask:0xf bank_mask:0xf\n"
      "v_mfma_f32_16x16x32_bf16 v[152:155], a[32:35], v[88:91], v[152:155]\n"
      "ds_read_b128 v[128:131], v0 offset:18432\n"
      "v_add_f32_dpp v238, v238, v238 row_ror:4 row_mask:0xf bank_mask:0xf\n"
      "v_mfma_f32_16x16x32_bf16 v[152:155], a[36:39], v[92:95], v[152:155]\n"
      "buffer_store_dwordx2 v[180:181], v9, s[16:19], s57 offen\n"
      "v_mfma_f32_16x16x32_bf16 v[152:155], a[40:43], v[96:99], v[152:155]\n"
      "s_waitcnt lgkmcnt(3)\n"
      "ds_read_b128 v[136:139], v0 offset:20480\n"
      "v_mfma_f32_16x16x32_bf16 v[152:155], a[44:47], v[100:103], v[152:155]\n"
      "ds_read_b128 v[148:151], v0 offset:23552\n"
      "v_add_f32_dpp v239, v239, v239 row_ror:2 row_mask:0xf bank_mask:0xf\n"
      "v_mfma_f32_16x16x32_bf16 v[152:155], a[48:51], v[104:107], v[152:155]\n"
      "v_add_f32_dpp v239, v239, v239 row_ror:1 row_mask:0xf bank_mask:0xf\n"
      "v_add_f32_dpp v238, v238, v238 row_ror:2 row_mask:0xf bank_mask:0xf\n"
      "v_mfma_f32_16x16x32_bf16 v[152:155], a[52:55], v[108:111], v[152:155]\n"
      "buffer_store_dwordx2 v[228:229], v13, s[8:11], s37 offen sc1\n"
      "v_mfma_f32_16x16x32_bf16 v[152:155], a[56:59], v[112:115], v[152:155]\n"
      "s_waitcnt vmcnt(11) lgkmcnt(3)\n"
      "ds_read_b128 v[144:147], v0 offset:22528\n"
      "v_cmp_ne_u32 vcc, s21, v20\n"
      "v_mfma_f32_16x16x32_bf16 v[152:155], a[60:63], v[116:119], v[152:155]\n"
      "buffer_store_dword v18, v4, s[8:11], s52 offen sc1\n"
      "v_mfma_f32_16x16x32_bf16 v[152:155], a[64:67], v[120:123], v[152:155]\n"
      "v_mfma_f32_16x16x32_bf16 v[152:155], a[68:71], v[124:127], v[152:155]\n"
      "s_waitcnt lgkmcnt(2)\n"
      "v_add_f32 v239, 0x3afba882, v239\n"
      "s_add_u32 m0, s43, 49152\n"
      "v_mfma_f32_16x16x32_bf16 v[152:155], a[72:75], v[128:131], v[152:155]\n"
      "buffer_store_dwordx2 v[180:181], v7, s[8:11], s56 offen sc1\n"
      "v_mfma_f32_16x16x32_bf16 v[152:155], a[76:79], v[132:135], v[152:155]\n"
      "s_cbranch_vccnz .Lsp_d2_4\n"
      ".Lbk_d2_4:\n"
      "v_mfma_f32_16x16x32_bf16 v[152:155], a[80:83], v[136:139], v[152:155]\n"
      "buffer_load_dwordx4 v0, s[8:11], s51 offen offset:2048 sc1 lds\n"
      "v_mfma_f32_16x16x32_bf16 v[152:155], a[84:87], v[140:143], v[152:155]\n"
      "s_waitcnt lgkmcnt(0)\n"
      "v_add_f32_dpp v238, v238, v238 row_ror:1 row_mask:0xf bank_mask:0xf\n"
      "s_add_u32 s36, s36, 165888\n"
      "v_mfma_f32_16x16x32_bf16 v[152:155], a[88:91], v[144:147], v[152:155]\n"
      "v_mul_f32 v238, 0x3ae38e39, v238\n"
      "v_mfma_f32_16x16x32_bf16 v[152:155], a[92:95], v[148:151], v[152:155]\n"
      "v_mul_f32 v239, 0x3ae38e39, v239\n"
      "v_add_u32 v14, 1, v14\n"
      "v_mfma_f32_16x16x32_bf16 v[156:159], a[96:99], v[56:59], 0\n"
      "buffer_store_dwordx2 v[238:239], v12, s[8:11], s56 offen sc1\n"
      "v_mfma_f32_16x16x32_bf16 v[156:159], a[100:103], v[60:63], v[156:159]\n"
      "v_and_b32 v167, 0xffff0000, v217\n"
      "v_mfma_f32_16x16x32_bf16 v[156:159], a[104:107], v[64:67], v[156:159]\n"
      "v_mfma_f32_16x16x32_bf16 v[156:159], a[108:111], v[68:71], v[156:159]\n"
      "v_lshlrev_b32 v164, 16, v216\n"
      "v_mfma_f32_16x16x32_bf16 v[156:159], a[112:115], v[72:75], v[156:159]\n"
      "v_mfma_f32_16x16x32_bf16 v[156:159], a[116:119], v[76:79], v[156:159]\n"
      "buffer_load_dword v20, v2, s[8:11], s36 offen sc1\n"
      "v_mfma_f32_16x16x32_bf16 v[156:159], a[120:123], v[80:83], v[156:159]\n"
      "v_mfma_f32_16x16x32_bf16 v[156:159], a[124:127], v[84:87], v[156:159]\n"
      "buffer_load_dwordx4 v0, s[8:11], s51 offen offset:0 sc1 lds\n"
      "v_mfma_f32_16x16x32_bf16 v[156:159], a[128:131], v[88:91], v[156:159]\n"
      "v_mfma_f32_16x16x32_bf16 v[156:159], a[132:135], v[92:95], v[156:159]\n"
      "s_waitcnt vmcnt(11)\n"
      "ds_write_b32 v16, v14\n"
      "v_mfma_f32_16x16x32_bf16 v[156:159], a[136:139], v[96:99], v[156:159]\n"
      "v_and_b32 v165, 0xffff0000, v216\n"
      "v_mfma_f32_16x16x32_bf16 v[156:159], a[140:143], v[100:103], v[156:159]\n"
      "buffer_load_dwordx4 v0, s[8:11], s51 offen offset:3072 sc1 lds\n"
      "v_mfma_f32_16x16x32_bf16 v[156:159], a[144:147], v[104:107], v[156:159]\n"
      "v_mfma_f32_16x16x32_bf16 v[156:159], a[148:151], v[108:111], v[156:159]\n"
      "v_mfma_f32_16x16x32_bf16 v[156:159], a[152:155], v[112:115], v[156:159]\n"
      "s_add_u32 s37, s37, 173056\n"
      "v_lshlrev_b32 v166, 16, v217\n"
      "v_mfma_f32_16x16x32_bf16 v[156:159], a[156:159], v[116:119], v[156:159]\n"
      "buffer_load_dwordx4 v0, s[8:11], s51 offen offset:1024 sc1 lds\n"
      "v_mfma_f32_16x16x32_bf16 v[156:159], a[160:163], v[120:123], v[156:159]\n"
      "v_mfma_f32_16x16x32_bf16 v[156:159], a[164:167], v[124:127], v[156:159]\n"
      "v_mfma_f32_16x16x32_bf16 v[156:159], a[168:171], v[128:131], v[156:159]\n"
      "s_add_u32 m0, s43, 53248\n"
      "ds_read_b32 v15, v17\n"
      "v_mfma_f32_16x16x32_bf16 v[156:159], a[172:175], v[132:135], v[156:159]\n"
      "v_add_f32 v166, v166, v154\n"
      "v_and_b32 v169, 0xffff0000, v218\n"
      "v_mfma_f32_16x16x32_bf16 v[156:159], a[176:179], v[136:139], v[156:159]\n"
      "buffer_load_dwordx4 v1, s[8:11], s51 offen offset:0 sc1 lds\n"
      "v_mfma_f32_16x16x32_bf16 v[156:159], a[180:183], v[140:143], v[156:159]\n"
      "v_mfma_f32_16x16x32_bf16 v[156:159], a[184:187], v[144:147], v[156:159]\n"
      "v_lshlrev_b32 v170, 16, v219\n"
      "v_mfma_f32_16x16x32_bf16 v[156:159], a[188:191], v[148:151], v[156:159]\n"
      "s_waitcnt lgkmcnt(0)\n"
      "v_cmp_gt_i32 vcc, v14, v15\n"
      "v_mfma_f32_16x16x32_bf16 v[160:163], a[192:195], v[56:59], 0\n"
      "buffer_load_dwordx4 v1, s[8:11], s51 offen offset:1024 sc1 lds\n"
      "v_mfma_f32_16x16x32_bf16 v[160:163], a[196:199], v[60:63], v[160:163]\n"
      "v_mfma_f32_16x16x32_bf16 v[160:163], a[200:203], v[64:67], v[160:163]\n"
      "s_cbranch_vccnz .Lsp_dy1_5\n"
      ".Lbk_dy1_5:\n"
      "s_mov_b32 s51, s36\n"
      "v_mfma_f32_16x16x32_bf16 v[160:163], a[204:207], v[68:71], v[160:163]\n"
      "ds_read_b128 v[64:67], v0 offset:26624\n"
      "v_add_f32 v164, v164, v152\n"
      "v_mfma_f32_16x16x32_bf16 v[160:163], a[208:211], v[72:75], v[160:163]\n"
      "v_mfma_f32_16x16x32_bf16 v[160:163], a[212:215], v[76:79], v[160:163]\n"
      "v_mfma_f32_16x16x32_bf16 v[160:163], a[216:219], v[80:83], v[160:163]\n"
      "ds_read_b128 v[60:63], v0 offset:25600\n"
      "v_lshlrev_b32 v168, 16, v218\n"
      "v_mfma_f32_16x16x32_bf16 v[160:163], a[220:223], v[84:87], v[160:163]\n"
      "v_mfma_f32_16x16x32_bf16 v[160:163], a[224:227], v[88:91], v[160:163]\n"
      "v_mfma_f32_16x16x32_bf16 v[160:163], a[228:231], v[92:95], v[160:163]\n"
      "ds_read_b128 v[84:87], v0 offset:31744\n"
      "v_mfma_f32_16x16x32_bf16 v[160:163], a[232:235], v[96:99], v[160:163]\n"
      "ds_read_b128 v[76:79], v0 offset:29696\n"
      "v_mfma_f32_16x16x32_bf16 v[160:163], a[236:239], v[100:103], v[160:163]\n"
      "ds_read_b128 v[80:83], v0 offset:30720\n"
      "v_mfma_f32_16x16x32_bf16 v[160:163], a[240:243], v[104:107], v[160:163]\n"
      "ds_read_b128 v[56:59], v0 offset:24576\n"
      "v_add_f32 v167, v167, v155\n"
      "v_mfma_f32_16x16x32_bf16 v[160:163], a[244:247], v[108:111], v[160:163]\n"
      "v_mfma_f32_16x16x32_bf16 v[160:163], a[248:251], v[112:115], v[160:163]\n"
      "ds_read_b128 v[72:75], v0 offset:28672\n"
      "v_add_f32 v170, v170, v158\n"
      "v_mfma_f32_16x16x32_bf16 v[160:163], a[252:255], v[116:119], v[160:163]\n"
      "ds_read_b128 v[88:91], v0 offset:32768\n"
      "v_mfma_f32_16x16x32_bf16 v[160:163], v[24:27], v[120:123], v[160:163]\n"
      "ds_read_b128 v[92:95], v0 offset:33792\n"
      "v_add_f32 v169, v169, v157\n"
      "v_mfma_f32_16x16x32_bf16 v[160:163], v[28:31], v[124:127], v[160:163]\n"
      "ds_read_b128 v[68:71], v0 offset:27648\n"
      "v_add_f32 v165, v165, v153\n"
      "v_mfma_f32_16x16x32_bf16 v[160:163], v[32:35], v[128:131], v[160:163]\n"
      "ds_read_b128 v[96:99], v0 offset:34816\n"
      "v_mfma_f32_16x16x32_bf16 v[160:163], v[36:39], v[132:135], v[160:163]\n"
      "ds_read_b128 v[104:107], v0 offset:36864\n"
      "v_mfma_f32_16x16x32_bf16 v[160:163], v[40:43], v[136:139], v[160:163]\n"
      "ds_read_b128 v[100:103], v0 offset:35840\n"
      "v_add_f32 v168, v168, v156\n"
      "v_mfma_f32_16x16x32_bf16 v[160:163], v[44:47], v[140:143], v[160:163]\n"
      "ds_read_b128 v[124:127], v0 offset:41984\n"
      "v_mfma_f32_16x16x32_bf16 v[160:163], v[48:51], v[144:147], v[160:163]\n"
      "s_waitcnt lgkmcnt(6)\n"
      "ds_read_b128 v[128:131], v0 offset:43008\n"
      "v_mfma_f32_16x16x32_bf16 v[160:163], v[52:55], v[148:151], v[160:163]\n"
      "s_sub_u32 s59, s59, 1\n"
      "s_cbranch_scc1 .Ld_out0\n"
      ".Ld_t1:\n"
      "v_mfma_f32_16x16x32_bf16 v[152:155], a[0:3], v[56:59], 0\n"
      "ds_read_b128 v[108:111], v0 offset:37888\n"
      "v_mfma_f32_16x16x32_bf16 v[152:155], a[4:7], v[60:63], v[152:155]\n"
      "ds_read_b128 v[112:115], v0 offset:38912\n"
      "v_lshlrev_b32 v172, 16, v220\n"
      "v_mfma_f32_16x16x32_bf16 v[152:155], a[8:11], v[64:67], v[152:155]\n"
      "s_waitcnt lgkmcnt(5)\n"
      "s_add_u32 m0, s43, 73728\n"
      "v_mfma_f32_16x16x32_bf16 v[152:155], a[12:15], v[68:71], v[152:155]\n"
      "s_add_u32 s52, s52, 106496\n"
      "ds_read_b128 v[116:119], v0 offset:39936\n"
      "v_mfma_f32_16x16x32_bf16 v[152:155], a[16:19], v[72:75], v[152:155]\n"
      "v_and_b32 v171, 0xffff0000, v219\n"
      "s_add_u32 s36, s36, 165888\n"
      "v_mfma_f32_16x16x32_bf16 v[152:155], a[20:23], v[76:79], v[152:155]\n"
      "v_mfma_f32_16x16x32_bf16 v[152:155], a[24:27], v[80:83], v[152:155]\n"
      "ds_read_b128 v[120:123], v0 offset:40960\n"
      "v_mfma_f32_16x16x32_bf16 v[152:155], a[28:31], v[84:87], v[152:155]\n"
      "ds_read_b128 v[136:139], v0 offset:45056\n"
      "v_mfma_f32_16x16x32_bf16 v[152:155], a[32:35], v[88:91], v[152:155]\n"
      "ds_read_b128 v[148:151], v0 offset:48128\n"
      "v_mfma_f32_16x16x32_bf16 v[152:155], a[36:39], v[92:95], v[152:155]\n"
      "ds_read_b128 v[140:143], v0 offset:46080\n"
      "v_mfma_f32_16x16x32_bf16 v[152:155], a[40:43], v[96:99], v[152:155]\n"
      "s_waitcnt lgkmcnt(4)\n"
      "v_mfma_f32_16x16x32_bf16 v[152:155], a[44:47], v[100:103], v[152:155]\n"
      "ds_read_b128 v[132:135], v0 offset:44032\n"
      "v_lshlrev_b32 v174, 16, v221\n"
      "v_mfma_f32_16x16x32_bf16 v[152:155], a[48:51], v[104:107], v[152:155]\n"
      "s_waitcnt vmcnt(10)\n"
      "buffer_store_dword v18, v4, s[8:11], s52 offen sc1\n"
      "v_mfma_f32_16x16x32_bf16 v[152:155], a[52:55], v[108:111], v[152:155]\n"
      "v_mfma_f32_16x16x32_bf16 v[152:155], a[56:59], v[112:115], v[152:155]\n"
      "ds_read_b128 v[144:147], v0 offset:47104\n"
      "v_mfma_f32_16x16x32_bf16 v[152:155], a[60:63], v[116:119], v[152:155]\n"
      "s_waitcnt lgkmcnt(3)\n"
      "v_add_f32 v171, v171, v159\n"
      "v_mfma_f32_16x16x32_bf16 v[152:155], a[64:67], v[120:123], v[152:155]\n"
      "s_waitcnt vmcnt(7) lgkmcnt(2)\n"
      "buffer_store_dword v18, v5, s[8:11], s56 offen sc1\n"
      "v_mfma_f32_16x16x32_bf16 v[152:155], a[68:71], v[124:127], v[152:155]\n"
      "s_waitcnt vmcnt(7)\n"
      "v_cmp_ne_u32 vcc, s21, v20\n"
      "v_mfma_f32_16x16x32_bf16 v[152:155], a[72:75], v[128:131], v[152:155]\n"
      "s_waitcnt lgkmcnt(1)\n"
      "v_mfma_f32_16x16x32_bf16 v[152:155], a[76:79], v[132:135], v[152:155]\n"
      "v_mfma_f32_16x16x32_bf16 v[152:155], a[80:83], v[136:139], v[152:155]\n"
      "s_cbranch_vccnz .Lsp_d3_6\n"
      ".Lbk_d3_6:\n"
      "v_mfma_f32_16x16x32_bf16 v[152:155], a[84:87], v[140:143], v[152:155]\n"
      "s_waitcnt lgkmcnt(0)\n"
      "buffer_load_dwordx4 v0, s[8:11], s51 offen offset:0 sc1 lds\n"
      "v_mfma_f32_16x16x32_bf16 v[152:155], a[88:91], v[144:147], v[152:155]\n"
      "v_mfma_f32_16x16x32_bf16 v[152:155], a[92:95], v[148:151], v[152:155]\n"
      "v_mfma_f32_16x16x32_bf16 v[156:159], a[96:99], v[56:59], 0\n"
      "v_add_f32 v172, v172, v160\n"
      "v_mfma_f32_16x16x32_bf16 v[156:159], a[100:103], v[60:63], v[156:159]\n"
      "buffer_load_dwordx4 v0, s[8:11], s51 offen offset:1024 sc1 lds\n"
      "v_mfma_f32_16x16x32_bf16 v[156:159], a[104:107], v[64:67], v[156:159]\n"
      "v_add_u32 v14, 1, v14\n"
      "v_mfma_f32_16x16x32_bf16 v[156:159], a[108:111], v[68:71], v[156:159]\n"
      "v_mfma_f32_16x16x32_bf16 v[156:159], a[112:115], v[72:75], v[156:159]\n"
      "v_add_f32 v174, v174, v162\n"
      "v_mfma_f32_16x16x32_bf16 v[156:159], a[116:119], v[76:79], v[156:159]\n"
      "v_mfma_f32_16x16x32_bf16 v[156:159], a[120:123], v[80:83], v[156:159]\n"
      "buffer_load_dword v20, v2, s[8:11], s36 offen sc1\n"
      "v_mfma_f32_16x16x32_bf16 v[156:159], a[124:127], v[84:87], v[156:159]\n"
      "v_and_b32 v173, 0xffff0000, v220\n"
      "v_mfma_f32_16x16x32_bf16 v[156:159], a[128:131], v[88:91], v[156:159]\n"
      "buffer_load_dwordx4 v0, s[8:11], s51 offen offset:2048 sc1 lds\n"
      "v_mfma_f32_16x16x32_bf16 v[156:159], a[132:135], v[92:95], v[156:159]\n"
      "s_waitcnt vmcnt(6)\n"
      "ds_write_b32 v16, v14\n"
      "v_mfma_f32_16x16x32_bf16 v[156:159], a[136:139], v[96:99], v[156:159]\n"
      "v_mfma_f32_16x16x32_bf16 v[156:159], a[140:143], v[100:103], v[156:159]\n"
      "v_mfma_f32_16x16x32_bf16 v[156:159], a[144:147], v[104:107], v[156:159]\n"
      "buffer_load_dwordx4 v0, s[8:11], s51 offen offset:3072 sc1 lds\n"
      "v_mfma_f32_16x16x32_bf16 v[156:159], a[148:151], v[108:111], v[156:159]\n"
      "v_and_b32 v175, 0xffff0000, v221\n"
      "v_mfma_f32_16x16x32_bf16 v[156:159], a[152:155], v[112:115], v[156:159]\n"
      "v_add_f32 v173, v173, v161\n"
      "v_add_f32 v175, v175, v163\n"
      "v_mfma_f32_16x16x32_bf16 v[156:159], a[156:159], v[116:119], v[156:159]\n"
      "s_add_u32 m0, s43, 77824\n"
      "v_cvt_pk_bf16_f32 v224, v152, v153\n"
      "v_mfma_f32_16x16x32_bf16 v[156:159], a[160:163], v[120:123], v[156:159]\n"
      "v_mfma_f32_16x16x32_bf16 v[156:159], a[164:167], v[124:127], v[156:159]\n"
      "v_mfma_f32_16x16x32_bf16 v[156:159], a[168:171], v[128:131], v[156:159]\n"
      "ds_read_b32 v15, v17\n"
      "v_mfma_f32_16x16x32_bf16 v[156:159], a[172:175], v[132:135], v[156:159]\n"
      "buffer_load_dwordx4 v1, s[8:11], s51 offen offset:0 sc1 lds\n"
      "v_mfma_f32_16x16x32_bf16 v[156:159], a[176:179], v[136:139], v[156:159]\n"
      "v_mfma_f32_16x16x32_bf16 v[156:159], a[180:183], v[140:143], v[156:159]\n"
      "v_mfma_f32_16x16x32_bf16 v[156:159], a[184:187], v[144:147], v[156:159]\n"
      "v_mfma_f32_16x16x32_bf16 v[156:159], a[188:191], v[148:151], v[156:159]\n"
      "s_waitcnt lgkmcnt(0)\n"
      "v_cmp_gt_i32 vcc, v14, v15\n"
      "v_mfma_f32_16x16x32_bf16 v[160:163], a[192:195], v[56:59], 0\n"
      "buffer_load_dwordx4 v1, s[8:11], s51 offen offset:1024 sc1 lds\n"
      "v_mfma_f32_16x16x32_bf16 v[160:163], a[196:199], v[60:63], v[160:163]\n"
      "v_mfma_f32_16x16x32_bf16 v[160:163], a[200:203], v[64:67], v[160:163]\n"
      "s_cbranch_vccnz .Lsp_dy2_7\n"
      ".Lbk_dy2_7:\n"
      "ds_read_b128 v[60:63], v22 offset:1024\n"
      "v_mfma_f32_16x16x32_bf16 v[160:163], a[204:207], v[68:71], v[160:163]\n"
      "ds_read_b128 v[56:59], v22 offset:0\n"
      "v_mfma_f32_16x16x32_bf16 v[160:163], a[208:211], v[72:75], v[160:163]\n"
      "ds_read_b128 v[68:71], v22 offset:3072\n"
      "v_cvt_pk_bf16_f32 v227, v158, v159\n"
      "v_mfma_f32_16x16x32_bf16 v[160:163], a[212:215], v[76:79], v[160:163]\n"
      "v_mfma_f32_16x16x32_bf16 v[160:163], a[216:219], v[80:83], v[160:163]\n"
      "ds_read_b128 v[72:75], v22 offset:4096\n"
      "v_cvt_pk_bf16_f32 v226, v156, v157\n"
      "v_mfma_f32_16x16x32_bf16 v[160:163], a[220:223], v[84:87], v[160:163]\n"
      "ds_read_b128 v[64:67], v22 offset:2048\n"
      "v_mfma_f32_16x16x32_bf16 v[160:163], a[224:227], v[88:91], v[160:163]\n"
      "v_mfma_f32_16x16x32_bf16 v[160:163], a[228:231], v[92:95], v[160:163]\n"
      "ds_read_b128 v[84:87], v22 offset:7168\n"
      "v_mfma_f32_16x16x32_bf16 v[160:163], a[232:235], v[96:99], v[160:163]\n"
      "ds_read_b128 v[80:83], v22 offset:6144\n"
      "v_mfma_f32_16x16x32_bf16 v[160:163], a[236:239], v[100:103], v[160:163]\n"
      "ds_read_b128 v[88:91], v22 offset:8192\n"
      "v_mfma_f32_16x16x32_bf16 v[160:163], a[240:243], v[104:107], v[160:163]\n"
      "ds_read_b128 v[92:95], v22 offset:9216\n"
      "v_cvt_pk_bf16_f32 v225, v154, v155\n"
      "v_mfma_f32_16x16x32_bf16 v[160:163], a[244:247], v[108:111], v[160:163]\n"
      "ds_read_b128 v[104:107], v22 offset:12288\n"
      "v_mfma_f32_16x16x32_bf16 v[160:163], a[248:251], v[112:115], v[160:163]\n"
      "ds_read_b128 v[108:111], v22 offset:13312\n"
      "v_mfma_f32_16x16x32_bf16 v[160:163], a[252:255], v[116:119], v[160:163]\n"
      "v_mfma_f32_16x16x32_bf16 v[160:163], v[24:27], v[120:123], v[160:163]\n"
      "buffer_store_dwordx4 v[224:227], v0, s[8:11], s37 offen sc1\n"
      "v_mfma_f32_16x16x32_bf16 v[160:163], v[28:31], v[124:127], v[160:163]\n"
      "ds_read_b128 v[76:79], v22 offset:5120\n"
      "s_mov_b32 s51, s36\n"
      "v_mfma_f32_16x16x32_bf16 v[160:163], v[32:35], v[128:131], v[160:163]\n"
      "ds_read_b128 v[100:103], v22 offset:11264\n"
      "v_mfma_f32_16x16x32_bf16 v[160:163], v[36:39], v[132:135], v[160:163]\n"
      "ds_read_b128 v[128:131], v22 offset:18432\n"
      "v_mfma_f32_16x16x32_bf16 v[160:163], v[40:43], v[136:139], v[160:163]\n"
      "ds_read_b128 v[96:99], v22 offset:10240\n"
      "v_mfma_f32_16x16x32_bf16 v[160:163], v[44:47], v[140:143], v[160:163]\n"
      "s_add_u32 s52, s52, 173056\n"
      "v_mfma_f32_16x16x32_bf16 v[160:163], v[48:51], v[144:147], v[160:163]\n"
      "s_waitcnt lgkmcnt(4)\n"
      "ds_read_b128 v[116:119], v22 offset:15360\n"
      "s_add_u32 m0, s43, 98304\n"
      "v_mfma_f32_16x16x32_bf16 v[160:163], v[52:55], v[148:151], v[160:163]\n"
      "s_sub_u32 s59, s59, 1\n"
      "s_cbranch_scc1 .Ld_out1\n"
      ".Ld_t2:\n"
      "v_mfma_f32_16x16x32_bf16 v[152:155], a[0:3], v[56:59], 0\n"
      "ds_read_b128 v[124:127], v22 offset:17408\n"
      "v_mfma_f32_16x16x32_bf16 v[152:155], a[4:7], v[60:63], v[152:155]\n"
      "ds_read_b128 v[144:147], v22 offset:22528\n"
      "v_mfma_f32_16x16x32_bf16 v[152:155], a[8:11], v[64:67], v[152:155]\n"
      "ds_read_b128 v[136:139], v22 offset:20480\n"
      "v_mfma_f32_16x16x32_bf16 v[152:155], a[12:15], v[68:71], v[152:155]\n"
      "v_cvt_pk_bf16_f32 v228, v160, v161\n"
      "v_cvt_pk_bf16_f32 v229, v162, v163\n"
      "v_mfma_f32_16x16x32_bf16 v[152:155], a[16:19], v[72:75], v[152:155]\n"
      "s_waitcnt lgkmcnt(4)\n"
      "ds_read_b128 v[140:143], v22 offset:21504\n"
      "v_mfma_f32_16x16x32_bf16 v[152:155], a[20:23], v[76:79], v[152:155]\n"
      "ds_read_b128 v[112:115], v22 offset:14336\n"
      "v_mfma_f32_16x16x32_bf16 v[152:155], a[24:27], v[80:83], v[152:155]\n"
      "buffer_store_dwordx2 v[228:229], v13, s[8:11], s37 offen sc1\n"
      "v_mfma_f32_16x16x32_bf16 v[152:155], a[28:31], v[84:87], v[152:155]\n"
      "ds_read_b128 v[120:123], v22 offset:16384\n"
      "v_mfma_f32_16x16x32_bf16 v[152:155], a[32:35], v[88:91], v[152:155]\n"
      "v_mfma_f32_16x16x32_bf16 v[152:155], a[36:39], v[92:95], v[152:155]\n"
      "s_add_u32 s37, s37, 106496\n"
      "v_mfma_f32_16x16x32_bf16 v[152:155], a[40:43], v[96:99], v[152:155]\n"
      "ds_read_b128 v[132:135], v22 offset:19456\n"
      "v_mfma_f32_16x16x32_bf16 v[152:155], a[44:47], v[100:103], v[152:155]\n"
      "v_add_u32 v14, 1, v14\n"
      "v_mfma_f32_16x16x32_bf16 v[152:155], a[48:51], v[104:107], v[152:155]\n"
      "s_add_u32 s36, s36, 165888\n"
      "ds_read_b128 v[148:151], v22 offset:23552\n"
      "v_mfma_f32_16x16x32_bf16 v[152:155], a[52:55], v[108:111], v[152:155]\n"
      "s_waitcnt lgkmcnt(3)\n"
      "v_mfma_f32_16x16x32_bf16 v[152:155], a[56:59], v[112:115], v[152:155]\n"
      "s_waitcnt vmcnt(6) lgkmcnt(2)\n"
      "v_cmp_ne_u32 vcc, s21, v20\n"
      "v_mfma_f32_16x16x32_bf16 v[152:155], a[60:63], v[116:119], v[152:155]\n"
      "v_mfma_f32_16x16x32_bf16 v[152:155], a[64:67], v[120:123], v[152:155]\n"
      "v_mfma_f32_16x16x32_bf16 v[152:155], a[68:71], v[124:127], v[152:155]\n"
      "v_mfma_f32_16x16x32_bf16 v[152:155], a[72:75], v[128:131], v[152:155]\n"
      "s_waitcnt lgkmcnt(1)\n"
      "s_cbranch_vccnz .Lsp_d4_8\n"
      ".Lbk_d4_8:\n"
      "v_mfma_f32_16x16x32_bf16 v[152:155], a[76:79], v[132:135], v[152:155]\n"
      "buffer_load_dword v20, v2, s[8:11], s36 offen sc1\n"
      "v_mfma_f32_16x16x32_bf16 v[152:155], a[80:83], v[136:139], v[152:155]\n"
      "buffer_load_dwordx4 v0, s[8:11], s51 offen offset:0 sc1 lds\n"
      "v_mfma_f32_16x16x32_bf16 v[152:155], a[84:87], v[140:143], v[152:155]\n"
      "v_mfma_f32_16x16x32_bf16 v[152:155], a[88:91], v[144:147], v[152:155]\n"
      "s_waitcnt lgkmcnt(0)\n"
      "v_mfma_f32_16x16x32_bf16 v[152:155], a[92:95], v[148:151], v[152:155]\n"
      "v_mfma_f32_16x16x32_bf16 v[156:159], a[96:99], v[56:59], 0\n"
      "v_mfma_f32_16x16x32_bf16 v[156:159], a[100:103], v[60:63], v[156:159]\n"
      "buffer_load_dwordx4 v0, s[8:11], s51 offen offset:1024 sc1 lds\n"
      "v_mfma_f32_16x16x32_bf16 v[156:159], a[104:107], v[64:67], v[156:159]\n"
      "v_mfma_f32_16x16x32_bf16 v[156:159], a[108:111], v[68:71], v[156:159]\n"
      "v_mfma_f32_16x16x32_bf16 v[156:159], a[112:115], v[72:75], v[156:159]\n"
      "v_mfma_f32_16x16x32_bf16 v[156:159], a[116:119], v[76:79], v[156:159]\n"
      "v_mfma_f32_16x16x32_bf16 v[156:159], a[120:123], v[80:83], v[156:159]\n"
      "buffer_load_dwordx4 v0, s[8:11], s51 offen offset:3072 sc1 lds\n"
      "v_mfma_f32_16x16x32_bf16 v[156:159], a[124:127], v[84:87], v[156:159]\n"
      "v_mfma_f32_16x16x32_bf16 v[156:159], a[128:131], v[88:91], v[156:159]\n"
      "v_mfma_f32_16x16x32_bf16 v[156:159], a[132:135], v[92:95], v[156:159]\n"
      "s_waitcnt vmcnt(6)\n"
      "ds_write_b32 v16, v14\n"
      "v_mfma_f32_16x16x32_bf16 v[156:159], a[136:139], v[96:99], v[156:159]\n"
      "buffer_load_dwordx4 v0, s[8:11], s51 offen offset:2048 sc1 lds\n"
      "v_mfma_f32_16x16x32_bf16 v[156:159], a[140:143], v[100:103], v[156:159]\n"
      "s_add_u32 m0, s43, 102400\n"
      "v_mfma_f32_16x16x32_bf16 v[156:159], a[144:147], v[104:107], v[156:159]\n"
      "v_mfma_f32_16x16x32_bf16 v[156:159], a[148:151], v[108:111], v[156:159]\n"
      "v_mfma_f32_16x16x32_bf16 v[156:159], a[152:155], v[112:115], v[156:159]\n"
      "v_mfma_f32_16x16x32_bf16 v[156:159], a[156:159], v[116:119], v[156:159]\n"
      "buffer_load_dwordx4 v1, s[8:11], s51 offen offset:1024 sc1 lds\n"
      "v_mfma_f32_16x16x32_bf16 v[156:159], a[160:163], v[120:123], v[156:159]\n"
      "v_mfma_f32_16x16x32_bf16 v[156:159], a[164:167], v[124:127], v[156:159]\n"
      "v_mfma_f32_16x16x32_bf16 v[156:159], a[168:171], v[128:131], v[156:159]\n"
      "ds_read_b32 v15, v17\n"
      "v_mfma_f32_16x16x32_bf16 v[156:159], a[172:175], v[132:135], v[156:159]\n"
      "buffer_load_dwordx4 v1, s[8:11], s51 offen offset:0 sc1 lds\n"
      "v_mfma_f32_16x16x32_bf16 v[156:159], a[176:179], v[136:139], v[156:159]\n"
      "v_mfma_f32_16x16x32_bf16 v[156:159], a[180:183], v[140:143], v[156:159]\n"
      "v_mfma_f32_16x16x32_bf16 v[156:159], a[184:187], v[144:147], v[156:159]\n"
      "v_mfma_f32_16x16x32_bf16 v[156:159], a[188:191], v[148:151], v[156:159]\n"
      "s_waitcnt lgkmcnt(0)\n"
      "v_cmp_gt_i32 vcc, v14, v15\n"
      "v_mfma_f32_16x16x32_bf16 v[160:163], a[192:195], v[56:59], 0\n"
      "buffer_load_dword v21, v3, s[8:11], s53 offen sc1\n"
      "v_mfma_f32_16x16x32_bf16 v[160:163], a[196:199], v[60:63], v[160:163]\n"
      "v_cvt_pk_bf16_f32 v224, v152, v153\n"
      "v_mfma_f32_16x16x32_bf16 v[160:163], a[200:203], v[64:67], v[160:163]\n"
      "s_cbranch_vccnz .Lsp_dy3_9\n"
      ".Lbk_dy3_9:\n"
      "v_mfma_f32_16x16x32_bf16 v[160:163], a[204:207], v[68:71], v[160:163]\n"
      "ds_read_b128 v[68:71], v22 offset:27648\n"
      "v_mfma_f32_16x16x32_bf16 v[160:163], a[208:211], v[72:75], v[160:163]\n"
      "ds_read_b128 v[56:59], v22 offset:24576\n"
      "v_mfma_f32_16x16x32_bf16 v[160:163], a[212:215], v[76:79], v[160:163]\n"
      "v_cvt_pk_bf16_f32 v226, v156, v157\n"
      "v_mfma_f32_16x16x32_bf16 v[160:163], a[216:219], v[80:83], v[160:163]\n"
      "ds_read_b128 v[80:83], v22 offset:30720\n"
      "v_mfma_f32_16x16x32_bf16 v[160:163], a[220:223], v[84:87], v[160:163]\n"
      "v_mfma_f32_16x16x32_bf16 v[160:163], a[224:227], v[88:91], v[160:163]\n"
      "ds_read_b128 v[84:87], v22 offset:31744\n"
      "v_mfma_f32_16x16x32_bf16 v[160:163], a[228:231], v[92:95], v[160:163]\n"
      "ds_read_b128 v[92:95], v22 offset:33792\n"
      "v_mfma_f32_16x16x32_bf16 v[160:163], a[232:235], v[96:99], v[160:163]\n"
      "ds_read_b128 v[88:91], v22 offset:32768\n"
      "v_mfma_f32_16x16x32_bf16 v[160:163], a[236:239], v[100:103], v[160:163]\n"
      "ds_read_b128 v[60:63], v22 offset:25600\n"
      "v_mfma_f32_16x16x32_bf16 v[160:163], a[240:243], v[104:107], v[160:163]\n"
      "ds_read_b128 v[72:75], v22 offset:28672\n"
      "v_mfma_f32_16x16x32_bf16 v[160:163], a[244:247], v[108:111], v[160:163]\n"
      "ds_read_b128 v[64:67], v22 offset:26624\n"
      "v_mfma_f32_16x16x32_bf16 v[160:163], a[248:251], v[112:115], v[160:163]\n"
      "v_cvt_pk_bf16_f32 v225, v154, v155\n"
      "s_mov_b32 s51, s36\n"
      "v_mfma_f32_16x16x32_bf16 v[160:163], a[252:255], v[116:119], v[160:163]\n"
      "ds_read_b128 v[96:99], v22 offset:34816\n"
      "v_mfma_f32_16x16x32_bf16 v[160:163], v[24:27], v[120:123], v[160:163]\n"
      "ds_read_b128 v[100:103], v22 offset:35840\n"
      "v_mfma_f32_16x16x32_bf16 v[160:163], v[28:31], v[124:127], v[160:163]\n"
      "v_mfma_f32_16x16x32_bf16 v[160:163], v[32:35], v[128:131], v[160:163]\n"
      "ds_read_b128 v[116:119], v22 offset:39936\n"
      "v_mfma_f32_16x16x32_bf16 v[160:163], v[36:39], v[132:135], v[160:163]\n"
      "ds_read_b128 v[76:79], v22 offset:29696\n"
      "v_mfma_f32_16x16x32_bf16 v[160:163], v[40:43], v[136:139], v[160:163]\n"
      "ds_read_b128 v[104:107], v22 offset:36864\n"
      "v_cvt_pk_bf16_f32 v227, v158, v159\n"
      "v_mfma_f32_16x16x32_bf16 v[160:163], v[44:47], v[140:143], v[160:163]\n"
      "ds_read_b128 v[128:131], v22 offset:43008\n"
      "v_mfma_f32_16x16x32_bf16 v[160:163], v[48:51], v[144:147], v[160:163]\n"
      "s_waitcnt vmcnt(8) lgkmcnt(5)\n"
      "buffer_store_dword v18, v4, s[8:11], s52 offen sc1\n"
      "v_mfma_f32_16x16x32_bf16 v[160:163], v[52:55], v[148:151], v[160:163]\n"
      "s_sub_u32 s59, s59, 1\n"
      "s_cbranch_scc1 .Ld_out2\n"
      ".Ld_t3:\n"
      "v_mfma_f32_16x16x32_bf16 v[152:155], a[0:3], v[56:59], 0\n"
      "s_add_u32 m0, s43, 122880\n"
      "ds_read_b128 v[108:111], v22 offset:37888\n"
      "v_mfma_f32_16x16x32_bf16 v[152:155], a[4:7], v[60:63], v[152:155]\n"
      "buffer_store_dwordx4 v[224:227], v0, s[8:11], s37 offen sc1\n"
      "v_mfma_f32_16x16x32_bf16 v[152:155], a[8:11], v[64:67], v[152:155]\n"
      "ds_read_b128 v[124:127], v22 offset:41984\n"
      "v_mfma_f32_16x16x32_bf16 v[152:155], a[12:15], v[68:71], v[152:155]\n"
      "s_waitcnt vmcnt(9) lgkmcnt(3)\n"
      "ds_read_b128 v[120:123], v22 offset:40960\n"
      "v_cmp_ne_u32 vcc, s21, v20\n"
      "v_mfma_f32_16x16x32_bf16 v[152:155], a[16:19], v[72:75], v[152:155]\n"
      "ds_read_b128 v[112:115], v22 offset:38912\n"
      "v_mfma_f32_16x16x32_bf16 v[152:155], a[20:23], v[76:79], v[152:155]\n"
      "ds_read_b128 v[148:151], v22 offset:48128\n"
      "v_mfma_f32_16x16x32_bf16 v[152:155], a[24:27], v[80:83], v[152:155]\n"
      "v_cvt_pk_bf16_f32 v228, v160, v161\n"
      "v_mfma_f32_16x16x32_bf16 v[152:155], a[28:31], v[84:87], v[152:155]\n"
      "s_cbranch_vccnz .Lsp_d5_10\n"
      ".Lbk_d5_10:\n"
      "ds_read_b128 v[132:135], v22 offset:44032\n"
      "v_mfma_f32_16x16x32_bf16 v[152:155], a[32:35], v[88:91], v[152:155]\n"
      "buffer_load_dwordx4 v0, s[8:11], s51 offen offset:0 sc1 lds\n"
      "v_mfma_f32_16x16x32_bf16 v[152:155], a[36:39], v[92:95], v[152:155]\n"
      "v_mfma_f32_16x16x32_bf16 v[152:155], a[40:43], v[96:99], v[152:155]\n"
      "ds_read_b128 v[136:139], v22 offset:45056\n"
      "s_add_u32 s36, s36, 165888\n"
      "v_mfma_f32_16x16x32_bf16 v[152:155], a[44:47], v[100:103], v[152:155]\n"
      "ds_read_b128 v[144:147], v22 offset:47104\n"
      "v_mfma_f32_16x16x32_bf16 v[152:155], a[48:51], v[104:107], v[152:155]\n"
      "s_waitcnt lgkmcnt(3)\n"
      "ds_read_b128 v[140:143], v22 offset:46080\n"
      "v_cvt_pk_bf16_f32 v229, v162, v163\n"
      "v_mfma_f32_16x16x32_bf16 v[152:155], a[52:55], v[108:111], v[152:155]\n"
      "buffer_store_dwordx2 v[228:229], v13, s[8:11], s37 offen sc1\n"
      "v_mfma_f32_16x16x32_bf16 v[152:155], a[56:59], v[112:115], v[152:155]\n"
      "v_mfma_f32_16x16x32_bf16 v[152:155], a[60:63], v[116:119], v[152:155]\n"
      "buffer_load_dword v20, v2, s[8:11], s36 offen sc1\n"
      "v_mfma_f32_16x16x32_bf16 v[152:155], a[64:67], v[120:123], v[152:155]\n"
      "v_mfma_f32_16x16x32_bf16 v[152:155], a[68:71], v[124:127], v[152:155]\n"
      "buffer_load_dwordx4 v0, s[8:11], s51 offen offset:1024 sc1 lds\n"
      "v_mfma_f32_16x16x32_bf16 v[152:155], a[72:75], v[128:131], v[152:155]\n"
      "s_waitcnt lgkmcnt(1)\n"
      "v_add_u32 v14, 1, v14\n"
      "v_mfma_f32_16x16x32_bf16 v[152:155], a[76:79], v[132:135], v[152:155]\n"
      "v_mfma_f32_16x16x32_bf16 v[152:155], a[80:83], v[136:139], v[152:155]\n"
      "s_waitcnt lgkmcnt(0)\n"
      "v_mfma_f32_16x16x32_bf16 v[152:155], a[84:87], v[140:143], v[152:155]\n"
      "buffer_load_dwordx4 v0, s[8:11], s51 offen offset:2048 sc1 lds\n"
      "v_mfma_f32_16x16x32_bf16 v[152:155], a[88:91], v[144:147], v[152:155]\n"
      "s_add_u32 s54, s53, 13312\n"
      "v_mfma_f32_16x16x32_bf16 v[152:155], a[92:95], v[148:151], v[152:155]\n"
      "v_mfma_f32_16x16x32_bf16 v[156:159], a[96:99], v[56:59], 0\n"
      "v_mfma_f32_16x16x32_bf16 v[156:159], a[100:103], v[60:63], v[156:159]\n"
      "s_cmp_lt_u32 s41, 32\n"
      "s_cselect_b32 s56, s39, 0x40000000\n"
      "v_mfma_f32_16x16x32_bf16 v[156:159], a[104:107], v[64:67], v[156:159]\n"
      "buffer_load_dwordx4 v0, s[8:11], s51 offen offset:3072 sc1 lds\n"
      "v_mfma_f32_16x16x32_bf16 v[156:159], a[108:111], v[68:71], v[156:159]\n"
      "s_add_u32 m0, s43, 126976\n"
      "v_mfma_f32_16x16x32_bf16 v[156:159], a[112:115], v[72:75], v[156:159]\n"
      "v_mfma_f32_16x16x32_bf16 v[156:159], a[116:119], v[76:79], v[156:159]\n"
      "v_mfma_f32_16x16x32_bf16 v[156:159], a[120:123], v[80:83], v[156:159]\n"
      "buffer_load_dwordx4 v1, s[8:11], s51 offen offset:0 sc1 lds\n"
      "v_mfma_f32_16x16x32_bf16 v[156:159], a[124:127], v[84:87], v[156:159]\n"
      "s_waitcnt vmcnt(10)\n"
      "ds_write_b32 v16, v14\n"
      "v_mfma_f32_16x16x32_bf16 v[156:159], a[128:131], v[88:91], v[156:159]\n"
      "s_waitcnt vmcnt(9)\n"
      "v_cmp_ne_u32 vcc, s21, v21\n"
      "v_mfma_f32_16x16x32_bf16 v[156:159], a[132:135], v[92:95], v[156:159]\n"
      "v_mfma_f32_16x16x32_bf16 v[156:159], a[136:139], v[96:99], v[156:159]\n"
      "v_mfma_f32_16x16x32_bf16 v[156:159], a[140:143], v[100:103], v[156:159]\n"
      "buffer_load_dwordx4 v1, s[8:11], s51 offen offset:1024 sc1 lds\n"
      "v_mfma_f32_16x16x32_bf16 v[156:159], a[144:147], v[104:107], v[156:159]\n"
      "s_add_u32 s58, s41, 4294967264\n"
      "v_cvt_pk_bf16_f32 v224, v152, v153\n"
      "v_mfma_f32_16x16x32_bf16 v[156:159], a[148:151], v[108:111], v[156:159]\n"
      "s_cbranch_vccnz .Lsp_df_11\n"
      ".Lbk_df_11:\n"
      "v_mfma_f32_16x16x32_bf16 v[156:159], a[152:155], v[112:115], v[156:159]\n"
      "v_mfma_f32_16x16x32_bf16 v[156:159], a[156:159], v[116:119], v[156:159]\n"
      "buffer_load_dwordx4 v[176:179], v0, s[8:11], s53 offen sc1\n"
      "v_mfma_f32_16x16x32_bf16 v[156:159], a[160:163], v[120:123], v[156:159]\n"
      "ds_read_b32 v15, v17\n"
      "v_mfma_f32_16x16x32_bf16 v[156:159], a[164:167], v[124:127], v[156:159]\n"
      "s_cmp_lt_u32 s58, 32\n"
      "s_cselect_b32 s57, s40, 0x40000000\n"
      "v_mfma_f32_16x16x32_bf16 v[156:159], a[168:171], v[128:131], v[156:159]\n"
      "s_add_u32 s55, s53, 6656\n"
      "v_mfma_f32_16x16x32_bf16 v[156:159], a[172:175], v[132:135], v[156:159]\n"
      "buffer_load_dwordx2 v[180:181], v13, s[8:11], s53 offen sc1\n"
      "v_mfma_f32_16x16x32_bf16 v[156:159], a[176:179], v[136:139], v[156:159]\n"
      "v_cvt_pk_bf16_f32 v225, v154, v155\n"
      "v_mfma_f32_16x16x32_bf16 v[156:159], a[180:183], v[140:143], v[156:159]\n"
      "s_waitcnt lgkmcnt(0)\n"
      "s_add_u32 s37, s37, 106496\n"
      "v_cmp_gt_i32 vcc, v14, v15\n"
      "v_mfma_f32_16x16x32_bf16 v[156:159], a[184:187], v[144:147], v[156:159]\n"
      "buffer_load_dwordx4 v[184:187], v0, s[8:11], s55 offen sc1\n"
      "v_mfma_f32_16x16x32_bf16 v[156:159], a[188:191], v[148:151], v[156:159]\n"
      "v_mfma_f32_16x16x32_bf16 v[160:163], a[192:195], v[56:59], 0\n"
      "v_mfma_f32_16x16x32_bf16 v[160:163], a[196:199], v[60:63], v[160:163]\n"
      "s_cbranch_vccnz .Lsp_dy4_12\n"
      ".Lbk_dy4_12:\n"
      "v_mfma_f32_16x16x32_bf16 v[160:163], a[200:203], v[64:67], v[160:163]\n"
      "buffer_load_dwordx2 v[188:189], v13, s[8:11], s55 offen sc1\n"
      "v_mfma_f32_16x16x32_bf16 v[160:163], a[204:207], v[68:71], v[160:163]\n"
      "ds_read_b128 v[64:67], v23 offset:2048\n"
      "s_add_u32 s39, s39, 147456\n"
      "v_mfma_f32_16x16x32_bf16 v[160:163], a[208:211], v[72:75], v[160:163]\n"
      "ds_read_b128 v[56:59], v23 offset:0\n"
      "v_mfma_f32_16x16x32_bf16 v[160:163], a[212:215], v[76:79], v[160:163]\n"
      "ds_read_b128 v[60:63], v23 offset:1024\n"
      "s_add_u32 s52, s52, 106496\n"
      "v_mfma_f32_16x16x32_bf16 v[160:163], a[216:219], v[80:83], v[160:163]\n"
      "buffer_load_dwordx4 v[192:195], v0, s[8:11], s54 offen sc1\n"
      "v_mfma_f32_16x16x32_bf16 v[160:163], a[220:223], v[84:87], v[160:163]\n"
      "s_mov_b32 s51, s36\n"
      "ds_read_b128 v[80:83], v23 offset:6144\n"
      "s_add_u32 s55, s53, 19968\n"
      "v_mfma_f32_16x16x32_bf16 v[160:163], a[224:227], v[88:91], v[160:163]\n"
      "v_mfma_f32_16x16x32_bf16 v[160:163], a[228:231], v[92:95], v[160:163]\n"
      "ds_read_b128 v[68:71], v23 offset:3072\n"
      "v_mfma_f32_16x16x32_bf16 v[160:163], a[232:235], v[96:99], v[160:163]\n"
      "buffer_load_dwordx2 v[196:197], v13, s[8:11], s54 offen sc1\n"
      "v_mfma_f32_16x16x32_bf16 v[160:163], a[236:239], v[100:103], v[160:163]\n"
      "ds_read_b128 v[92:95], v23 offset:9216\n"
      "v_cvt_pk_bf16_f32 v227, v158, v159\n"
      "v_mfma_f32_16x16x32_bf16 v[160:163], a[240:243], v[104:107], v[160:163]\n"
      "ds_read_b128 v[88:91], v23 offset:8192\n"
      "v_mfma_f32_16x16x32_bf16 v[160:163], a[244:247], v[108:111], v[160:163]\n"
      "ds_read_b128 v[76:79], v23 offset:5120\n"
      "v_mfma_f32_16x16x32_bf16 v[160:163], a[248:251], v[112:115], v[160:163]\n"
      "buffer_load_dwordx2 v[204:205], v13, s[8:11], s55 offen sc1\n"
      "v_mfma_f32_16x16x32_bf16 v[160:163], a[252:255], v[116:119], v[160:163]\n"
      "ds_read_b128 v[108:111], v23 offset:13312\n"
      "v_mfma_f32_16x16x32_bf16 v[160:163], v[24:27], v[120:123], v[160:163]\n"
      "buffer_load_dwordx4 v[200:203], v0, s[8:11], s55 offen sc1\n"
      "v_mfma_f32_16x16x32_bf16 v[160:163], v[28:31], v[124:127], v[160:163]\n"
      "s_add_u32 s54, s53, 26624\n"
      "ds_read_b128 v[72:75], v23 offset:4096\n"
      "ds_read_b128 v[96:99], v23 offset:10240\n"
      "v_mfma_f32_16x16x32_bf16 v[160:163], v[32:35], v[128:131], v[160:163]\n"
      "s_add_u32 s41, s41, 6\n"
      "ds_read_b128 v[84:87], v23 offset:7168\n"
      "s_add_u32 s36, s36, 165888\n"
      "v_mfma_f32_16x16x32_bf16 v[160:163], v[36:39], v[132:135], v[160:163]\n"
      "ds_read_b128 v[104:107], v23 offset:12288\n"
      "v_mfma_f32_16x16x32_bf16 v[160:163], v[40:43], v[136:139], v[160:163]\n"
      "buffer_load_dwordx2 v[212:213], v13, s[8:11], s54 offen sc1\n"
      "v_mfma_f32_16x16x32_bf16 v[160:163], v[44:47], v[140:143], v[160:163]\n"
      "s_add_u32 m0, s43, 0\n"
      "ds_read_b128 v[120:123], v23 offset:16384\n"
      "v_cvt_pk_bf16_f32 v226, v156, v157\n"
      "v_mfma_f32_16x16x32_bf16 v[160:163], v[48:51], v[144:147], v[160:163]\n"
      "s_waitcnt lgkmcnt(5)\n"
      "s_add_u32 s40, s40, 4294856704\n"
      "ds_read_b128 v[124:127], v23 offset:17408\n"
      "v_mfma_f32_16x16x32_bf16 v[160:163], v[52:55], v[148:151], v[160:163]\n"
      "s_sub_u32 s59, s59, 1\n"
      "s_cbranch_scc1 .Ld_out3\n"
      ".Ld_t4:\n"
      "v_mfma_f32_16x16x32_bf16 v[152:155], a[0:3], v[56:59], 0\n"
      "buffer_load_dwordx4 v[208:211], v0, s[8:11], s54 offen sc1\n"
      "v_mfma_f32_16x16x32_bf16 v[152:155], a[4:7], v[60:63], v[152:155]\n"
      "ds_read_b128 v[148:151], v23 offset:23552\n"
      "v_mfma_f32_16x16x32_bf16 v[152:155], a[8:11], v[64:67], v[152:155]\n"
      "ds_read_b128 v[100:103], v23 offset:11264\n"
      "s_add_u32 s53, s53, 599040\n"
      "v_mfma_f32_16x16x32_bf16 v[152:155], a[12:15], v[68:71], v[152:155]\n"
      "s_waitcnt lgkmcnt(4)\n"
      "v_cvt_pk_bf16_f32 v229, v162, v163\n"
      "v_cvt_pk_bf16_f32 v228, v160, v161\n"
      "v_mfma_f32_16x16x32_bf16 v[152:155], a[16:19], v[72:75], v[152:155]\n"
      "buffer_store_dwordx4 v[224:227], v0, s[8:11], s37 offen sc1\n"
      "v_mfma_f32_16x16x32_bf16 v[152:155], a[20:23], v[76:79], v[152:155]\n"
      "ds_read_b128 v[116:119], v23 offset:15360\n"
      "v_mfma_f32_16x16x32_bf16 v[152:155], a[24:27], v[80:83], v[152:155]\n"
      "s_waitcnt vmcnt(16) lgkmcnt(3)\n"
      "ds_read_b128 v[112:115], v23 offset:14336\n"
      "v_cmp_ne_u32 vcc, s21, v20\n"
      "v_mfma_f32_16x16x32_bf16 v[152:155], a[28:31], v[84:87], v[152:155]\n"
      "ds_read_b128 v[128:131], v23 offset:18432\n"
      "v_mfma_f32_16x16x32_bf16 v[152:155], a[32:35], v[88:91], v[152:155]\n"
      "buffer_store_dword v18, v4, s[8:11], s52 offen sc1\n"
      "v_mfma_f32_16x16x32_bf16 v[152:155], a[36:39], v[92:95], v[152:155]\n"
      "ds_read_b128 v[132:135], v23 offset:19456\n"
      "v_add_u32 v14, 1, v14\n"
      "v_mfma_f32_16x16x32_bf16 v[152:155], a[40:43], v[96:99], v[152:155]\n"
      "s_waitcnt lgkmcnt(4)\n"
      "buffer_store_dwordx2 v[228:229], v13, s[8:11], s37 offen sc1\n"
      "v_mfma_f32_16x16x32_bf16 v[152:155], a[44:47], v[100:103], v[152:155]\n"
      "ds_read_b128 v[140:143], v23 offset:21504\n"
      "v_mfma_f32_16x16x32_bf16 v[152:155], a[48:51], v[104:107], v[152:155]\n"
      "s_cbranch_vccnz .Lsp_d0_13\n"
      ".Lbk_d0_13:\n"
      "ds_read_b128 v[136:139], v23 offset:20480\n"
      "v_mfma_f32_16x16x32_bf16 v[152:155], a[52:55], v[108:111], v[152:155]\n"
      "s_waitcnt lgkmcnt(4)\n"
      "buffer_load_dwordx4 v0, s[8:11], s51 offen offset:0 sc1 lds\n"
      "v_mfma_f32_16x16x32_bf16 v[152:155], a[56:59], v[112:115], v[152:155]\n"
      "ds_read_b128 v[144:147], v23 offset:22528\n"
      "v_mfma_f32_16x16x32_bf16 v[152:155], a[60:63], v[116:119], v[152:155]\n"
      "v_mfma_f32_16x16x32_bf16 v[152:155], a[64:67], v[120:123], v[152:155]\n"
      "v_mfma_f32_16x16x32_bf16 v[152:155], a[68:71], v[124:127], v[152:155]\n"
      "s_waitcnt lgkmcnt(3)\n"
      "buffer_load_dwordx4 v0, s[8:11], s51 offen offset:2048 sc1 lds\n"
      "v_mfma_f32_16x16x32_bf16 v[152:155], a[72:75], v[128:131], v[152:155]\n"
      "v_mfma_f32_16x16x32_bf16 v[152:155], a[76:79], v[132:135], v[152:155]\n"
      "s_waitcnt lgkmcnt(1)\n"
      "v_mfma_f32_16x16x32_bf16 v[152:155], a[80:83], v[136:139], v[152:155]\n"
      "v_mfma_f32_16x16x32_bf16 v[152:155], a[84:87], v[140:143], v[152:155]\n"
      "s_waitcnt lgkmcnt(0)\n"
      "v_mfma_f32_16x16x32_bf16 v[152:155], a[88:91], v[144:147], v[152:155]\n"
      "buffer_load_dwordx4 v0, s[8:11], s51 offen offset:3072 sc1 lds\n"
      "v_mfma_f32_16x16x32_bf16 v[152:155], a[92:95], v[148:151], v[152:155]\n"
      "s_waitcnt vmcnt(15)\n"
      "v_lshlrev_b32 v232, 16, v176\n"
      "v_lshlrev_b32 v233, 16, v177\n"
      "v_mfma_f32_16x16x32_bf16 v[156:159], a[96:99], v[56:59], 0\n"
      "v_and_b32 v176, 0xffff0000, v176\n"
      "v_lshlrev_b32 v234, 16, v178\n"
      "v_mfma_f32_16x16x32_bf16 v[156:159], a[100:103], v[60:63], v[156:159]\n"
      "v_add_f32 v166, v166, v233\n"
      "v_mfma_f32_16x16x32_bf16 v[156:159], a[104:107], v[64:67], v[156:159]\n"
      "buffer_load_dwordx4 v0, s[8:11], s51 offen offset:1024 sc1 lds\n"
      "v_mfma_f32_16x16x32_bf16 v[156:159], a[108:111], v[68:71], v[156:159]\n"
      "s_waitcnt vmcnt(15)\n"
      "v_add_f32 v164, v164, v232\n"
      "v_lshlrev_b32 v233, 16, v181\n"
      "v_mfma_f32_16x16x32_bf16 v[156:159], a[112:115], v[72:75], v[156:159]\n"
      "ds_write_b32 v16, v14\n"
      "v_mfma_f32_16x16x32_bf16 v[156:159], a[116:119], v[76:79], v[156:159]\n"
      "s_add_u32 m0, s43, 4096\n"
      "v_add_f32 v168, v168, v234\n"
      "v_lshlrev_b32 v232, 16, v180\n"
      "v_mfma_f32_16x16x32_bf16 v[156:159], a[120:123], v[80:83], v[156:159]\n"
      "s_add_u32 s52, s52, 106496\n"
      "v_add_f32 v174, v174, v233\n"
      "v_mfma_f32_16x16x32_bf16 v[156:159], a[124:127], v[84:87], v[156:159]\n"
      "s_waitcnt vmcnt(14)\n"
      "v_lshlrev_b32 v233, 16, v185\n"
      "v_and_b32 v185, 0xffff0000, v185\n"
      "v_mfma_f32_16x16x32_bf16 v[156:159], a[128:131], v[88:91], v[156:159]\n"
      "buffer_load_dwordx4 v1, s[8:11], s51 offen offset:0 sc1 lds\n"
      "v_mfma_f32_16x16x32_bf16 v[156:159], a[132:135], v[92:95], v[156:159]\n"
      "v_add_f32 v172, v172, v232\n"
      "v_lshlrev_b32 v232, 16, v184\n"
      "v_mfma_f32_16x16x32_bf16 v[156:159], a[136:139], v[96:99], v[156:159]\n"
      "s_waitcnt vmcnt(14)\n"
      "v_add_f32 v164, v164, v232\n"
      "v_lshlrev_b32 v232, 16, v188\n"
      "v_mfma_f32_16x16x32_bf16 v[156:159], a[140:143], v[100:103], v[156:159]\n"
      "v_lshlrev_b32 v234, 16, v186\n"
      "v_add_f32 v165, v165, v176\n"
      "v_mfma_f32_16x16x32_bf16 v[156:159], a[144:147], v[104:107], v[156:159]\n"
      "buffer_load_dword v20, v2, s[8:11], s36 offen sc1\n"
      "v_mfma_f32_16x16x32_bf16 v[156:159], a[148:151], v[108:111], v[156:159]\n"
      "v_add_f32 v166, v166, v233\n"
      "v_and_b32 v177, 0xffff0000, v177\n"
      "v_mfma_f32_16x16x32_bf16 v[156:159], a[152:155], v[112:115], v[156:159]\n"
      "v_lshlrev_b32 v233, 16, v189\n"
      "v_lshlrev_b32 v235, 16, v179\n"
      "v_mfma_f32_16x16x32_bf16 v[156:159], a[156:159], v[116:119], v[156:159]\n"
      "v_add_f32 v172, v172, v232\n"
      "v_add_f32 v174, v174, v233\n"
      "v_mfma_f32_16x16x32_bf16 v[156:159], a[160:163], v[120:123], v[156:159]\n"
      "ds_read_b32 v15, v17\n"
      "v_and_b32 v184, 0xffff0000, v184\n"
      "v_mfma_f32_16x16x32_bf16 v[156:159], a[164:167], v[124:127], v[156:159]\n"
      "s_waitcnt vmcnt(14) lgkmcnt(1)\n"
      "v_add_f32 v167, v167, v177\n"
      "v_lshlrev_b32 v233, 16, v193\n"
      "v_mfma_f32_16x16x32_bf16 v[156:159], a[168:171], v[128:131], v[156:159]\n"
      "v_add_f32 v166, v166, v233\n"
      "v_lshlrev_b32 v232, 16, v192\n"
      "v_mfma_f32_16x16x32_bf16 v[156:159], a[172:175], v[132:135], v[156:159]\n"
      "buffer_load_dwordx4 v1, s[8:11], s51 offen offset:1024 sc1 lds\n"
      "v_mfma_f32_16x16x32_bf16 v[156:159], a[176:179], v[136:139], v[156:159]\n"
      "v_add_f32 v165, v165, v184\n"
      "v_and_b32 v189, 0xffff0000, v189\n"
      "v_mfma_f32_16x16x32_bf16 v[156:159], a[180:183], v[140:143], v[156:159]\n"
      "s_mov_b32 s51, s36\n"
      "v_and_b32 v186, 0xffff0000, v186\n"
      "v_add_f32 v164, v164, v232\n"
      "v_mfma_f32_16x16x32_bf16 v[156:159], a[184:187], v[144:147], v[156:159]\n"
      "s_waitcnt vmcnt(14) lgkmcnt(0)\n"
      "v_lshlrev_b32 v232, 16, v196\n"
      "v_cmp_gt_i32 vcc, v14, v15\n"
      "v_mfma_f32_16x16x32_bf16 v[156:159], a[188:191], v[148:151], v[156:159]\n"
      "v_lshlrev_b32 v233, 16, v197\n"
      "v_add_f32 v174, v174, v233\n"
      "v_mfma_f32_16x16x32_bf16 v[160:163], a[192:195], v[56:59], 0\n"
      "s_waitcnt vmcnt(12)\n"
      "v_lshlrev_b32 v233, 16, v201\n"
      "v_and_b32 v178, 0xffff0000, v178\n"
      "v_add_f32 v170, v170, v235\n"
      "v_mfma_f32_16x16x32_bf16 v[160:163], a[196:199], v[60:63], v[160:163]\n"
      "v_add_f32 v172, v172, v232\n"
      "v_add_f32 v166, v166, v233\n"
      "v_mfma_f32_16x16x32_bf16 v[160:163], a[200:203], v[64:67], v[160:163]\n"
      "s_cbranch_vccnz .Lsp_dy5_14\n"
      ".Lbk_dy5_14:\n"
      "ds_read_b128 v[60:63], v23 offset:25600\n"
      "v_mfma_f32_16x16x32_bf16 v[160:163], a[204:207], v[68:71], v[160:163]\n"
      "ds_read_b128 v[64:67], v23 offset:26624\n"
      "v_cvt_pk_bf16_f32 v225, v154, v155\n"
      "v_mfma_f32_16x16x32_bf16 v[160:163], a[208:211], v[72:75], v[160:163]\n"
      "v_lshlrev_b32 v232, 16, v200\n"
      "v_add_f32 v164, v164, v232\n"
      "v_mfma_f32_16x16x32_bf16 v[160:163], a[212:215], v[76:79], v[160:163]\n"
      "v_lshlrev_b32 v233, 16, v205\n"
      "v_cvt_pk_bf16_f32 v226, v156, v157\n"
      "v_mfma_f32_16x16x32_bf16 v[160:163], a[216:219], v[80:83], v[160:163]\n"
      "v_and_b32 v192, 0xffff0000, v192\n"
      "v_and_b32 v193, 0xffff0000, v193\n"
      "v_mfma_f32_16x16x32_bf16 v[160:163], a[220:223], v[84:87], v[160:163]\n"
      "ds_read_b128 v[84:87], v23 offset:31744\n"
      "v_and_b32 v201, 0xffff0000, v201\n"
      "v_mfma_f32_16x16x32_bf16 v[160:163], a[224:227], v[88:91], v[160:163]\n"
      "ds_read_b128 v[76:79], v23 offset:29696\n"
      "v_cvt_pk_bf16_f32 v224, v152, v153\n"
      "v_mfma_f32_16x16x32_bf16 v[160:163], a[228:231], v[92:95], v[160:163]\n"
      "ds_read_b128 v[92:95], v23 offset:33792\n"
      "v_add_f32 v174, v174, v233\n"
      "v_mfma_f32_16x16x32_bf16 v[160:163], a[232:235], v[96:99], v[160:163]\n"
      "ds_read_b128 v[80:83], v23 offset:30720\n"
      "v_add_f32 v169, v169, v178\n"
      "v_lshlrev_b32 v232, 16, v204\n"
      "v_mfma_f32_16x16x32_bf16 v[160:163], a[236:239], v[100:103], v[160:163]\n"
      "v_add_f32 v172, v172, v232\n"
      "v_add_f32 v165, v165, v192\n"
      "v_mfma_f32_16x16x32_bf16 v[160:163], a[240:243], v[104:107], v[160:163]\n"
      "s_waitcnt vmcnt(10) lgkmcnt(4)\n"
      "v_lshlrev_b32 v232, 16, v208\n"
      "v_lshlrev_b32 v233, 16, v209\n"
      "v_mfma_f32_16x16x32_bf16 v[160:163], a[244:247], v[108:111], v[160:163]\n"
      "ds_read_b128 v[56:59], v23 offset:24576\n"
      "v_cvt_pk_bf16_f32 v227, v158, v159\n"
      "v_mfma_f32_16x16x32_bf16 v[160:163], a[248:251], v[112:115], v[160:163]\n"
      "ds_read_b128 v[96:99], v23 offset:34816\n"
      "v_and_b32 v197, 0xffff0000, v197\n"
      "v_mfma_f32_16x16x32_bf16 v[160:163], a[252:255], v[116:119], v[160:163]\n"
      "ds_read_b128 v[108:111], v23 offset:37888\n"
      "v_add_f32 v164, v164, v232\n"
      "v_mfma_f32_16x16x32_bf16 v[160:163], v[24:27], v[120:123], v[160:163]\n"
      "ds_read_b128 v[120:123], v23 offset:40960\n"
      "v_mul_f32 v237, v164, v164\n"
      "v_mfma_f32_16x16x32_bf16 v[160:163], v[28:31], v[124:127], v[160:163]\n"
      "v_lshlrev_b32 v235, 16, v187\n"
      "v_and_b32 v209, 0xffff0000, v209\n"
      "v_mfma_f32_16x16x32_bf16 v[160:163], v[32:35], v[128:131], v[160:163]\n"
      "ds_read_b128 v[72:75], v23 offset:28672\n"
      "v_add_f32 v170, v170, v235\n"
      "v_mfma_f32_16x16x32_bf16 v[160:163], v[36:39], v[132:135], v[160:163]\n"
      "ds_read_b128 v[68:71], v23 offset:27648\n"
      "v_and_b32 v200, 0xffff0000, v200\n"
      "v_mfma_f32_16x16x32_bf16 v[160:163], v[40:43], v[136:139], v[160:163]\n"
      "ds_read_b128 v[88:91], v23 offset:32768\n"
      "v_add_f32 v168, v168, v234\n"
      "v_mfma_f32_16x16x32_bf16 v[160:163], v[44:47], v[140:143], v[160:163]\n"
      "ds_read_b128 v[136:139], v23 offset:45056\n"
      "v_add_f32 v166, v166, v233\n"
      "v_mfma_f32_16x16x32_bf16 v[160:163], v[48:51], v[144:147], v[160:163]\n"
      "s_waitcnt lgkmcnt(5)\n"
      "ds_read_b128 v[116:119], v23 offset:39936\n"
      "v_lshlrev_b32 v232, 16, v212\n"
      "v_mfma_f32_16x16x32_bf16 v[160:163], v[52:55], v[148:151], v[160:163]\n"
      "s_sub_u32 s59, s59, 1\n"
      "s_cbranch_scc1 .Ld_out4\n"
      ".Ld_t5:\n"
      "v_mfma_f32_16x16x32_bf16 v[152:155], a[0:3], v[56:59], 0\n"
      "ds_read_b128 v[104:107], v23 offset:36864\n"
      "v_lshlrev_b32 v234, 16, v194\n"
      "v_add_f32 v165, v165, v200\n"
      "v_mfma_f32_16x16x32_bf16 v[152:155], a[4:7], v[60:63], v[152:155]\n"
      "ds_read_b128 v[100:103], v23 offset:35840\n"
      "v_and_b32 v208, 0xffff0000, v208\n"
      "v_add_f32 v167, v167, v185\n"
      "v_mfma_f32_16x16x32_bf16 v[152:155], a[8:11], v[64:67], v[152:155]\n"
      "s_waitcnt lgkmcnt(5)\n"
      "buffer_load_dwordx4 v[216:219], v6, s[8:11], s38 offen sc1\n"
      "v_mfma_f32_16x16x32_bf16 v[152:155], a[12:15], v[68:71], v[152:155]\n"
      "ds_read_b128 v[112:115], v23 offset:38912\n"
      "v_add_f32 v168, v168, v234\n"
      "v_mfma_f32_16x16x32_bf16 v[152:155], a[16:19], v[72:75], v[152:155]\n"
      "s_add_u32 s37, s37, 106496\n"
      "ds_read_b128 v[148:151], v23 offset:48128\n"
      "v_add_f32 v172, v172, v232\n"
      "v_mfma_f32_16x16x32_bf16 v[152:155], a[20:23], v[76:79], v[152:155]\n"
      "v_lshlrev_b32 v235, 16, v195\n"
      "v_cvt_pk_bf16_f32 v228, v160, v161\n"
      "v_mfma_f32_16x16x32_bf16 v[152:155], a[24:27], v[80:83], v[152:155]\n"
      "s_waitcnt vmcnt(8) lgkmcnt(4)\n"
      "buffer_store_dword v18, v4, s[8:11], s52 offen sc1\n"
      "v_mfma_f32_16x16x32_bf16 v[152:155], a[28:31], v[84:87], v[152:155]\n"
      "ds_read_b128 v[124:127], v23 offset:41984\n"
      "v_and_b32 v195, 0xffff0000, v195\n"
      "v_mfma_f32_16x16x32_bf16 v[152:155], a[32:35], v[88:91], v[152:155]\n"
      "s_add_u32 m0, s43, 24576\n"
      "v_add_f32 v165, v165, v208\n"
      "v_add_u32 v14, 1, v14\n"
      "v_mfma_f32_16x16x32_bf16 v[152:155], a[36:39], v[92:95], v[152:155]\n"
      "ds_read_b128 v[128:131], v23 offset:43008\n"
      "v_and_b32 v212, 0xffff0000, v212\n"
      "v_mfma_f32_16x16x32_bf16 v[152:155], a[40:43], v[96:99], v[152:155]\n"
      "s_waitcnt lgkmcnt(3)\n"
      "ds_read_b128 v[144:147], v23 offset:47104\n"
      "buffer_load_dwordx2 v[220:221], v7, s[8:11], s38 offen sc1\n"
      "v_mfma_f32_16x16x32_bf16 v[152:155], a[44:47], v[100:103], v[152:155]\n"
      "s_add_u32 s36, s36, 165888\n"
      "ds_read_b128 v[132:135], v23 offset:44032\n"
      "v_and_b32 v187, 0xffff0000, v187\n"
      "v_mfma_f32_16x16x32_bf16 v[152:155], a[48:51], v[104:107], v[152:155]\n"
      "buffer_store_dwordx4 v[224:227], v0, s[8:11], s37 offen sc1\n"
      "v_mfma_f32_16x16x32_bf16 v[152:155], a[52:55], v[108:111], v[152:155]\n"
      "ds_read_b128 v[140:143], v23 offset:46080\n"
      "v_and_b32 v179, 0xffff0000, v179\n"
      "v_mfma_f32_16x16x32_bf16 v[152:155], a[56:59], v[112:115], v[152:155]\n"
      "v_add_f32 v167, v167, v193\n"
      "v_add_f32 v236, v164, v165\n"
      "v_mfma_f32_16x16x32_bf16 v[152:155], a[60:63], v[116:119], v[152:155]\n"
      "v_cvt_pk_bf16_f32 v229, v162, v163\n"
      "v_add_f32 v170, v170, v235\n"
      "v_mfma_f32_16x16x32_bf16 v[152:155], a[64:67], v[120:123], v[152:155]\n"
      "s_waitcnt lgkmcnt(3)\n"
      "v_lshlrev_b32 v234, 16, v202\n"
      "v_fmac_f32 v237, v165, v165\n"
      "v_mfma_f32_16x16x32_bf16 v[152:155], a[68:71], v[124:127], v[152:155]\n"
      "v_add_f32 v167, v167, v201\n"
      "v_lshlrev_b32 v235, 16, v203\n"
      "v_mfma_f32_16x16x32_bf16 v[152:155], a[72:75], v[128:131], v[152:155]\n"
      "s_waitcnt lgkmcnt(1)\n"
      "buffer_store_dwordx2 v[228:229], v13, s[8:11], s37 offen sc1\n"
      "v_mfma_f32_16x16x32_bf16 v[152:155], a[76:79], v[132:135], v[152:155]\n"
      "v_add_f32 v167, v167, v209\n"
      "v_add_f32 v236, v236, v166\n"
      "v_mfma_f32_16x16x32_bf16 v[152:155], a[80:83], v[136:139], v[152:155]\n"
      "s_waitcnt lgkmcnt(0)\n"
      "v_add_f32 v168, v168, v234\n"
      "v_lshlrev_b32 v234, 16, v210\n"
      "v_mfma_f32_16x16x32_bf16 v[152:155], a[84:87], v[140:143], v[152:155]\n"
      "v_fmac_f32 v237, v166, v166\n"
      "v_add_f32 v236, v236, v167\n"
      "v_mfma_f32_16x16x32_bf16 v[152:155], a[88:91], v[144:147], v[152:155]\n"
      "s_waitcnt vmcnt(6)\n"
      "v_fmac_f32 v237, v167, v167\n"
      "v_cmp_ne_u32 vcc, s21, v20\n"
      "v_mfma_f32_16x16x32_bf16 v[152:155], a[92:95], v[148:151], v[152:155]\n"
      "v_and_b32 v203, 0xffff0000, v203\n"
      "v_cvt_pk_bf16_f32 v176, v164, v165\n"
      "v_mfma_f32_16x16x32_bf16 v[156:159], a[96:99], v[56:59], 0\n"
      "s_add_u32 s38, s38, 147456\n"
      "v_and_b32 v181, 0xffff0000, v181\n"
      "v_and_b32 v188, 0xffff0000, v188\n"
      "v_add_f32 v168, v168, v234\n"
      "v_mfma_f32_16x16x32_bf16 v[156:159], a[100:103], v[60:63], v[156:159]\n"
      "s_cbranch_vccnz .Lsp_d1_15\n"
      ".Lbk_d1_15:\n"
      "v_and_b32 v210, 0xffff0000, v210\n"
      "v_mfma_f32_16x16x32_bf16 v[156:159], a[104:107], v[64:67], v[156:159]\n"
      "buffer_load_dword v20, v2, s[8:11], s36 offen sc1\n"
      "v_mfma_f32_16x16x32_bf16 v[156:159], a[108:111], v[68:71], v[156:159]\n"
      "s_waitcnt vmcnt(6)\n"
      "ds_write_b32 v16, v14\n"
      "v_mfma_f32_16x16x32_bf16 v[156:159], a[112:115], v[72:75], v[156:159]\n"
      "buffer_load_dwordx4 v0, s[8:11], s51 offen offset:0 sc1 lds\n"
      "v_mfma_f32_16x16x32_bf16 v[156:159], a[116:119], v[76:79], v[156:159]\n"
      "v_add_f32 v169, v169, v186\n"
      "v_and_b32 v194, 0xffff0000, v194\n"
      "v_mfma_f32_16x16x32_bf16 v[156:159], a[120:123], v[80:83], v[156:159]\n"
      "v_add_f32 v169, v169, v194\n"
      "v_fmac_f32 v237, v168, v168\n"
      "v_mfma_f32_16x16x32_bf16 v[156:159], a[124:127], v[84:87], v[156:159]\n"
      "v_add_f32 v171, v171, v179\n"
      "v_and_b32 v202, 0xffff0000, v202\n"
      "v_mfma_f32_16x16x32_bf16 v[156:159], a[128:131], v[88:91], v[156:159]\n"
      "buffer_load_dwordx4 v0, s[8:11], s51 offen offset:1024 sc1 lds\n"
      "v_and_b32 v180, 0xffff0000, v180\n"
      "v_mfma_f32_16x16x32_bf16 v[156:159], a[132:135], v[92:95], v[156:159]\n"
      "v_add_f32 v175, v175, v181\n"
      "v_add_f32 v170, v170, v235\n"
      "v_mfma_f32_16x16x32_bf16 v[156:159], a[136:139], v[96:99], v[156:159]\n"
      "v_lshlrev_b32 v235, 16, v211\n"
      "v_add_f32 v170, v170, v235\n"
      "v_mfma_f32_16x16x32_bf16 v[156:159], a[140:143], v[100:103], v[156:159]\n"
      "v_and_b32 v204, 0xffff0000, v204\n"
      "v_and_b32 v211, 0xffff0000, v211\n"
      "v_cvt_pk_bf16_f32 v225, v154, v155\n"
      "v_mfma_f32_16x16x32_bf16 v[156:159], a[144:147], v[104:107], v[156:159]\n"
      "buffer_load_dwordx4 v0, s[8:11], s51 offen offset:2048 sc1 lds\n"
      "v_add_f32 v171, v171, v187\n"
      "v_mfma_f32_16x16x32_bf16 v[156:159], a[148:151], v[108:111], v[156:159]\n"
      "ds_read_b32 v15, v17\n"
      "v_add_f32 v171, v171, v195\n"
      "v_mfma_f32_16x16x32_bf16 v[156:159], a[152:155], v[112:115], v[156:159]\n"
      "v_add_f32 v173, v173, v180\n"
      "v_add_f32 v236, v236, v168\n"
      "v_mfma_f32_16x16x32_bf16 v[156:159], a[156:159], v[116:119], v[156:159]\n"
      "v_add_f32 v175, v175, v189\n"
      "v_add_f32 v169, v169, v202\n"
      "v_mfma_f32_16x16x32_bf16 v[156:159], a[160:163], v[120:123], v[156:159]\n"
      "v_add_f32 v169, v169, v210\n"
      "v_fmac_f32 v237, v169, v169\n"
      "v_mfma_f32_16x16x32_bf16 v[156:159], a[164:167], v[124:127], v[156:159]\n"
      "buffer_load_dwordx4 v0, s[8:11], s51 offen offset:3072 sc1 lds\n"
      "v_mfma_f32_16x16x32_bf16 v[156:159], a[168:171], v[128:131], v[156:159]\n"
      "v_and_b32 v196, 0xffff0000, v196\n"
      "v_add_f32 v171, v171, v203\n"
      "v_mfma_f32_16x16x32_bf16 v[156:159], a[172:175], v[132:135], v[156:159]\n"
      "s_add_u32 m0, s43, 28672\n"
      "v_add_f32 v173, v173, v188\n"
      "v_add_f32 v173, v173, v196\n"
      "v_mfma_f32_16x16x32_bf16 v[156:159], a[176:179], v[136:139], v[156:159]\n"
      "v_add_f32 v171, v171, v211\n"
      "v_fmac_f32 v237, v170, v170\n"
      "v_mfma_f32_16x16x32_bf16 v[156:159], a[180:183], v[140:143], v[156:159]\n"
      "s_waitcnt lgkmcnt(0)\n"
      "buffer_load_dwordx4 v1, s[8:11], s51 offen offset:1024 sc1 lds\n"
      "v_cmp_gt_i32 vcc, v14, v15\n"
      "v_lshlrev_b32 v233, 16, v213\n"
      "v_mfma_f32_16x16x32_bf16 v[156:159], a[184:187], v[144:147], v[156:159]\n"
      "v_add_f32 v173, v173, v204\n"
      "v_cvt_pk_bf16_f32 v179, v170, v171\n"
      "v_add_f32 v236, v236, v169\n"
      "v_mfma_f32_16x16x32_bf16 v[156:159], a[188:191], v[148:151], v[156:159]\n"
      "v_add_f32 v236, v236, v170\n"
      "v_add_f32 v236, v236, v171\n"
      "v_fmac_f32 v237, v171, v171\n"
      "v_mfma_f32_16x16x32_bf16 v[160:163], a[192:195], v[56:59], 0\n"
      "s_cbranch_vccnz .Lsp_dy0_16\n"
      ".Lbk_dy0_16:\n"
      "v_cvt_pk_bf16_f32 v177, v166, v167\n"
      "v_add_f32 v236, v236, v172\n"
      "v_mfma_f32_16x16x32_bf16 v[160:163], a[196:199], v[60:63], v[160:163]\n"
      "buffer_load_dwordx4 v1, s[8:11], s51 offen offset:0 sc1 lds\n"
      "v_mfma_f32_16x16x32_bf16 v[160:163], a[200:203], v[64:67], v[160:163]\n"
      "v_add_f32 v173, v173, v212\n"
      "v_cvt_pk_bf16_f32 v178, v168, v169\n"
      "v_mfma_f32_16x16x32_bf16 v[160:163], a[204:207], v[68:71], v[160:163]\n"
      "s_add_u32 s37, s37, 106496\n"
      "v_fmac_f32 v237, v172, v172\n"
      "v_and_b32 v213, 0xffff0000, v213\n"
      "v_mfma_f32_16x16x32_bf16 v[160:163], a[208:211], v[72:75], v[160:163]\n"
      "v_and_b32 v205, 0xffff0000, v205\n"
      "v_add_f32 v174, v174, v233\n"
      "v_mfma_f32_16x16x32_bf16 v[160:163], a[212:215], v[76:79], v[160:163]\n"
      "v_add_f32 v236, v236, v173\n"
      "v_fmac_f32 v237, v173, v173\n"
      "v_mfma_f32_16x16x32_bf16 v[160:163], a[216:219], v[80:83], v[160:163]\n"
      "ds_read_b128 v[60:63], v0 offset:1024\n"
      "v_cvt_pk_bf16_f32 v224, v152, v153\n"
      "v_mfma_f32_16x16x32_bf16 v[160:163], a[220:223], v[84:87], v[160:163]\n"
      "v_add_f32 v175, v175, v197\n"
      "v_add_f32 v236, v236, v174\n"
      "v_mfma_f32_16x16x32_bf16 v[160:163], a[224:227], v[88:91], v[160:163]\n"
      "v_add_f32 v175, v175, v205\n"
      "v_cvt_pk_bf16_f32 v227, v158, v159\n"
      "v_mfma_f32_16x16x32_bf16 v[160:163], a[228:231], v[92:95], v[160:163]\n"
      "ds_read_b128 v[92:95], v0 offset:9216\n"
      "v_add_f32 v175, v175, v213\n"
      "v_mfma_f32_16x16x32_bf16 v[160:163], a[232:235], v[96:99], v[160:163]\n"
      "v_cvt_pk_bf16_f32 v226, v156, v157\n"
      "v_fmac_f32 v237, v174, v174\n"
      "v_mfma_f32_16x16x32_bf16 v[160:163], a[236:239], v[100:103], v[160:163]\n"
      "v_add_f32 v236, v236, v175\n"
      "v_fmac_f32 v237, v175, v175\n"
      "v_mfma_f32_16x16x32_bf16 v[160:163], a[240:243], v[104:107], v[160:163]\n"
      "ds_write_b64 v10, v[236:237]\n"
      "v_mfma_f32_16x16x32_bf16 v[160:163], a[244:247], v[108:111], v[160:163]\n"
      "ds_read_b128 v[56:59], v0 offset:0\n"
      "v_mfma_f32_16x16x32_bf16 v[160:163], a[248:251], v[112:115], v[160:163]\n"
      "buffer_store_dwordx4 v[224:227], v0, s[8:11], s37 offen sc1\n"
      "v_mfma_f32_16x16x32_bf16 v[160:163], a[252:255], v[116:119], v[160:163]\n"
      "ds_read_b128 v[64:67], v0 offset:2048\n"
      "ds_read_b128 v[68:71], v0 offset:3072\n"
      "v_mfma_f32_16x16x32_bf16 v[160:163], v[24:27], v[120:123], v[160:163]\n"
      "ds_read_b128 v[72:75], v0 offset:4096\n"
      "ds_read_b128 v[88:91], v0 offset:8192\n"
      "v_mfma_f32_16x16x32_bf16 v[160:163], v[28:31], v[124:127], v[160:163]\n"
      "s_waitcnt lgkmcnt(5)\n"
      "s_barrier\n"
      "v_mfma_f32_16x16x32_bf16 v[160:163], v[32:35], v[128:131], v[160:163]\n"
      "ds_read_b128 v[84:87], v0 offset:7168\n"
      "ds_read_b64 v[238:239], v11\n"
      "v_mfma_f32_16x16x32_bf16 v[160:163], v[36:39], v[132:135], v[160:163]\n"
      "buffer_store_dwordx4 v[176:179], v6, s[8:11], s56 offen sc1\n"
      "ds_read_b128 v[96:99], v0 offset:10240\n"
      "v_mfma_f32_16x16x32_bf16 v[160:163], v[40:43], v[136:139], v[160:163]\n"
      "ds_read_b128 v[80:83], v0 offset:6144\n"
      "v_cvt_pk_bf16_f32 v181, v174, v175\n"
      "v_mfma_f32_16x16x32_bf16 v[160:163], v[44:47], v[140:143], v[160:163]\n"
      "ds_read_b128 v[76:79], v0 offset:5120\n"
      "v_cvt_pk_bf16_f32 v180, v172, v173\n"
      "v_mfma_f32_16x16x32_bf16 v[160:163], v[48:51], v[144:147], v[160:163]\n"
      "s_waitcnt lgkmcnt(7)\n"
      "ds_read_b128 v[100:103], v0 offset:11264\n"
      "ds_read_b128 v[140:143], v0 offset:21504\n"
      "v_mfma_f32_16x16x32_bf16 v[160:163], v[52:55], v[148:151], v[160:163]\n"
      "s_sub_u32 s59, s59, 1\n"
      "s_cbranch_scc0 .Ld_t0\n"
      ".Ld_out5:\n"
      "s_add_u32 s52, s52, 106496\n"
      "s_nop 7\n"
      "v_cvt_pk_bf16_f32 v229, v162, v163\n"
      "s_waitcnt lgkmcnt(5)\n"
      "v_add_f32_dpp v239, v239, v239 row_ror:8 row_mask:0xf bank_mask:0xf\n"
      "buffer_store_dwordx4 v[176:179], v8, s[16:19], s57 offen\n"
      "v_cvt_pk_bf16_f32 v228, v160, v161\n"
      "v_add_f32_dpp v238, v238, v238 row_ror:8 row_mask:0xf bank_mask:0xf\n"
      "v_add_f32_dpp v239, v239, v239 row_ror:4 row_mask:0xf bank_mask:0xf\n"
      "s_nop 0\n"
      "v_add_f32_dpp v238, v238, v238 row_ror:4 row_mask:0xf bank_mask:0xf\n"
      "buffer_store_dwordx2 v[180:181], v9, s[16:19], s57 offen\n"
      "v_add_f32_dpp v239, v239, v239 row_ror:2 row_mask:0xf bank_mask:0xf\n"
      "s_nop 1\n"
      "v_add_f32_dpp v239, v239, v239 row_ror:1 row_mask:0xf bank_mask:0xf\n"
      "v_add_f32_dpp v238, v238, v238 row_ror:2 row_mask:0xf bank_mask:0xf\n"
      "buffer_store_dwordx2 v[228:229], v13, s[8:11], s37 offen sc1\n"
      "s_waitcnt vmcnt(12)\n"
      "buffer_store_dword v18, v4, s[8:11], s52 offen sc1\n"
      "v_add_f32 v239, 0x3afba882, v239\n"
      "buffer_store_dwordx2 v[180:181], v7, s[8:11], s56 offen sc1\n"
      "v_add_f32_dpp v238, v238, v238 row_ror:1 row_mask:0xf bank_mask:0xf\n"
      "v_mul_f32 v238, 0x3ae38e39, v238\n"
      "v_mul_f32 v239, 0x3ae38e39, v239\n"
      "buffer_store_dwordx2 v[238:239], v12, s[8:11], s56 offen sc1\n"
      "s_add_u32 s37, s37, 173056\n"
      "s_add_u32 s52, s52, 106496\n"
      "s_waitcnt vmcnt(3)\n"
      "buffer_store_dword v18, v4, s[8:11], s52 offen sc1\n"
      "s_waitcnt vmcnt(1)\n"
      "buffer_store_dword v18, v5, s[8:11], s56 offen sc1\n"
      "s_add_u32 s52, s52, 173056\n"
      "s_branch .Ld_end\n"
      ".Ld_out0:\n"
      "v_lshlrev_b32 v172, 16, v220\n"
      "s_add_u32 s52, s52, 106496\n"
      "v_and_b32 v171, 0xffff0000, v219\n"
      "v_lshlrev_b32 v174, 16, v221\n"
      "s_waitcnt vmcnt(10)\n"
      "buffer_store_dword v18, v4, s[8:11], s52 offen sc1\n"
      "v_add_f32 v171, v171, v159\n"
      "s_waitcnt vmcnt(7)\n"
      "buffer_store_dword v18, v5, s[8:11], s56 offen sc1\n"
      "v_add_f32 v172, v172, v160\n"
      "v_add_f32 v174, v174, v162\n"
      "v_and_b32 v173, 0xffff0000, v220\n"
      "v_and_b32 v175, 0xffff0000, v221\n"
      "v_add_f32 v173, v173, v161\n"
      "v_add_f32 v175, v175, v163\n"
      "s_add_u32 s52, s52, 173056\n"
      "buffer_load_dword v21, v3, s[8:11], s53 offen sc1\n"
      "s_add_u32 s54, s53, 13312\n"
      "s_cmp_lt_u32 s41, 32\n"
      "s_cselect_b32 s56, s39, 0x40000000\n"
      "s_waitcnt vmcnt(0)\n"
      "v_cmp_ne_u32 vcc, s21, v21\n"
      "s_add_u32 s58, s41, 4294967264\n"
      "s_cbranch_vccnz .Lsp_df_17\n"
      ".Lbk_df_17:\n"
      "buffer_load_dwordx4 v[176:179], v0, s[8:11], s53 offen sc1\n"
      "s_cmp_lt_u32 s58, 32\n"
      "s_cselect_b32 s57, s40, 0x40000000\n"
      "s_add_u32 s55, s53, 6656\n"
      "buffer_load_dwordx2 v[180:181], v13, s[8:11], s53 offen sc1\n"
      "buffer_load_dwordx4 v[184:187], v0, s[8:11], s55 offen sc1\n"
      "buffer_load_dwordx2 v[188:189], v13, s[8:11], s55 offen sc1\n"
      "s_add_u32 s39, s39, 147456\n"
      "buffer_load_dwordx4 v[192:195], v0, s[8:11], s54 offen sc1\n"
      "s_add_u32 s55, s53, 19968\n"
      "buffer_load_dwordx2 v[196:197], v13, s[8:11], s54 offen sc1\n"
      "buffer_load_dwordx2 v[204:205], v13, s[8:11], s55 offen sc1\n"
      "buffer_load_dwordx4 v[200:203], v0, s[8:11], s55 offen sc1\n"
      "s_add_u32 s54, s53, 26624\n"
      "s_add_u32 s41, s41, 6\n"
      "buffer_load_dwordx2 v[212:213], v13, s[8:11], s54 offen sc1\n"
      "s_add_u32 s40, s40, 4294856704\n"
      "buffer_load_dwordx4 v[208:211], v0, s[8:11], s54 offen sc1\n"
      "s_add_u32 s53, s53, 599040\n"
      "s_waitcnt vmcnt(9)\n"
      "v_lshlrev_b32 v232, 16, v176\n"
      "v_lshlrev_b32 v233, 16, v177\n"
      "v_and_b32 v176, 0xffff0000, v176\n"
      "v_lshlrev_b32 v234, 16, v178\n"
      "v_add_f32 v166, v166, v233\n"
      "v_add_f32 v164, v164, v232\n"
      "s_waitcnt vmcnt(8)\n"
      "v_lshlrev_b32 v233, 16, v181\n"
      "v_add_f32 v168, v168, v234\n"
      "v_lshlrev_b32 v232, 16, v180\n"
      "v_add_f32 v174, v174, v233\n"
      "s_waitcnt vmcnt(7)\n"
      "v_lshlrev_b32 v233, 16, v185\n"
      "v_and_b32 v185, 0xffff0000, v185\n"
      "v_add_f32 v172, v172, v232\n"
      "v_lshlrev_b32 v232, 16, v184\n"
      "v_add_f32 v164, v164, v232\n"
      "s_waitcnt vmcnt(6)\n"
      "v_lshlrev_b32 v232, 16, v188\n"
      "v_lshlrev_b32 v234, 16, v186\n"
      "v_add_f32 v165, v165, v176\n"
      "v_add_f32 v166, v166, v233\n"
      "v_and_b32 v177, 0xffff0000, v177\n"
      "v_lshlrev_b32 v233, 16, v189\n"
      "v_lshlrev_b32 v235, 16, v179\n"
      "v_add_f32 v172, v172, v232\n"
      "v_add_f32 v174, v174, v233\n"
      "v_and_b32 v184, 0xffff0000, v184\n"
      "v_add_f32 v167, v167, v177\n"
      "s_waitcnt vmcnt(5)\n"
      "v_lshlrev_b32 v233, 16, v193\n"
      "v_add_f32 v166, v166, v233\n"
      "v_lshlrev_b32 v232, 16, v192\n"
      "v_add_f32 v165, v165, v184\n"
      "v_and_b32 v189, 0xffff0000, v189\n"
      "v_and_b32 v186, 0xffff0000, v186\n"
      "v_add_f32 v164, v164, v232\n"
      "s_waitcnt vmcnt(4)\n"
      "v_lshlrev_b32 v232, 16, v196\n"
      "v_lshlrev_b32 v233, 16, v197\n"
      "v_add_f32 v174, v174, v233\n"
      "s_waitcnt vmcnt(2)\n"
      "v_lshlrev_b32 v233, 16, v201\n"
      "v_and_b32 v178, 0xffff0000, v178\n"
      "v_add_f32 v170, v170, v235\n"
      "v_add_f32 v172, v172, v232\n"
      "v_add_f32 v166, v166, v233\n"
      "v_lshlrev_b32 v232, 16, v200\n"
      "v_add_f32 v164, v164, v232\n"
      "v_lshlrev_b32 v233, 16, v205\n"
      "v_and_b32 v192, 0xffff0000, v192\n"
      "v_and_b32 v193, 0xffff0000, v193\n"
      "v_and_b32 v201, 0xffff0000, v201\n"
      "v_add_f32 v174, v174, v233\n"
      "v_add_f32 v169, v169, v178\n"
      "v_lshlrev_b32 v232, 16, v204\n"
      "v_add_f32 v172, v172, v232\n"
      "v_add_f32 v165, v165, v192\n"
      "s_waitcnt vmcnt(0)\n"
      "v_lshlrev_b32 v232, 16, v208\n"
      "v_lshlrev_b32 v233, 16, v209\n"
      "v_and_b32 v197, 0xffff0000, v197\n"
      "v_add_f32 v164, v164, v232\n"
      "v_mul_f32 v237, v164, v164\n"
      "v_lshlrev_b32 v235, 16, v187\n"
      "v_and_b32 v209, 0xffff0000, v209\n"
      "v_add_f32 v170, v170, v235\n"
      "v_and_b32 v200, 0xffff0000, v200\n"
      "v_add_f32 v168, v168, v234\n"
      "v_add_f32 v166, v166, v233\n"
      "v_lshlrev_b32 v232, 16, v212\n"
      "v_lshlrev_b32 v234, 16, v194\n"
      "v_add_f32 v165, v165, v200\n"
      "v_and_b32 v208, 0xffff0000, v208\n"
      "v_add_f32 v167, v167, v185\n"
      "v_add_f32 v168, v168, v234\n"
      "v_add_f32 v172, v172, v232\n"
      "v_lshlrev_b32 v235, 16, v195\n"
      "v_and_b32 v195, 0xffff0000, v195\n"
      "v_add_f32 v165, v165, v208\n"
      "v_and_b32 v212, 0xffff0000, v212\n"
      "v_and_b32 v187, 0xffff0000, v187\n"
      "v_and_b32 v179, 0xffff0000, v179\n"
      "v_add_f32 v167, v167, v193\n"
      "v_add_f32 v236, v164, v165\n"
      "v_add_f32 v170, v170, v235\n"
      "v_lshlrev_b32 v234, 16, v202\n"
      "v_fmac_f32 v237, v165, v165\n"
      "v_add_f32 v167, v167, v201\n"
      "v_lshlrev_b32 v235, 16, v203\n"
      "v_add_f32 v167, v167, v209\n"
      "v_add_f32 v236, v236, v166\n"
      "v_add_f32 v168, v168, v234\n"
      "v_lshlrev_b32 v234, 16, v210\n"
      "v_fmac_f32 v237, v166, v166\n"
      "v_add_f32 v236, v236, v167\n"
      "v_fmac_f32 v237, v167, v167\n"
      "v_and_b32 v203, 0xffff0000, v203\n"
      "v_cvt_pk_bf16_f32 v176, v164, v165\n"
      "v_and_b32 v181, 0xffff0000, v181\n"
      "v_and_b32 v188, 0xffff0000, v188\n"
      "v_add_f32 v168, v168, v234\n"
      "v_and_b32 v210, 0xffff0000, v210\n"
      "v_add_f32 v169, v169, v186\n"
      "v_and_b32 v194, 0xffff0000, v194\n"
      "v_add_f32 v169, v169, v194\n"
      "v_fmac_f32 v237, v168, v168\n"
      "v_add_f32 v171, v171, v179\n"
      "v_and_b32 v202, 0xffff0000, v202\n"
      "v_and_b32 v180, 0xffff0000, v180\n"
      "v_add_f32 v175, v175, v181\n"
      "v_add_f32 v170, v170, v235\n"
      "v_lshlrev_b32 v235, 16, v211\n"
      "v_add_f32 v170, v170, v235\n"
      "v_and_b32 v204, 0xffff0000, v204\n"
      "v_and_b32 v211, 0xffff0000, v211\n"
      "v_add_f32 v171, v171, v187\n"
      "v_add_f32 v171, v171, v195\n"
      "v_add_f32 v173, v173, v180\n"
      "v_add_f32 v236, v236, v168\n"
      "v_add_f32 v175, v175, v189\n"
      "v_add_f32 v169, v169, v202\n"
      "v_add_f32 v169, v169, v210\n"
      "v_fmac_f32 v237, v169, v169\n"
      "v_and_b32 v196, 0xffff0000, v196\n"
      "v_add_f32 v171, v171, v203\n"
      "v_add_f32 v173, v173, v188\n"
      "v_add_f32 v173, v173, v196\n"
      "v_add_f32 v171, v171, v211\n"
      "v_fmac_f32 v237, v170, v170\n"
      "v_lshlrev_b32 v233, 16, v213\n"
      "v_add_f32 v173, v173, v204\n"
      "v_cvt_pk_bf16_f32 v179, v170, v171\n"
      "v_add_f32 v236, v236, v169\n"
      "v_add_f32 v236, v236, v170\n"
      "v_add_f32 v236, v236, v171\n"
      "v_fmac_f32 v237, v171, v171\n"
      "v_cvt_pk_bf16_f32 v177, v166, v167\n"
      "v_add_f32 v236, v236, v172\n"
      "v_add_f32 v173, v173, v212\n"
      "v_cvt_pk_bf16_f32 v178, v168, v169\n"
      "v_fmac_f32 v237, v172, v172\n"
      "v_and_b32 v213, 0xffff0000, v213\n"
      "v_and_b32 v205, 0xffff0000, v205\n"
      "v_add_f32 v174, v174, v233\n"
      "v_add_f32 v236, v236, v173\n"
      "v_fmac_f32 v237, v173, v173\n"
      "v_add_f32 v175, v175, v197\n"
      "v_add_f32 v236, v236, v174\n"
      "v_add_f32 v175, v175, v205\n"
      "v_add_f32 v175, v175, v213\n"
      "v_fmac_f32 v237, v174, v174\n"
      "v_add_f32 v236, v236, v175\n"
      "v_fmac_f32 v237, v175, v175\n"
      "ds_write_b64 v10, v[236:237]\n"
      "s_waitcnt lgkmcnt(0)\n"
      "s_barrier\n"
      "ds_read_b64 v[238:239], v11\n"
      "buffer_store_dwordx4 v[176:179], v6, s[8:11], s56 offen sc1\n"
      "v_cvt_pk_bf16_f32 v181, v174, v175\n"
      "v_cvt_pk_bf16_f32 v180, v172, v173\n"
      "s_waitcnt lgkmcnt(0)\n"
      "v_add_f32_dpp v239, v239, v239 row_ror:8 row_mask:0xf bank_mask:0xf\n"
      "buffer_store_dwordx4 v[176:179], v8, s[16:19], s57 offen\n"
      "v_add_f32_dpp v238, v238, v238 row_ror:8 row_mask:0xf bank_mask:0xf\n"
      "v_add_f32_dpp v239, v239, v239 row_ror:4 row_mask:0xf bank_mask:0xf\n"
      "s_nop 0\n"
      "v_add_f32_dpp v238, v238, v238 row_ror:4 row_mask:0xf bank_mask:0xf\n"
      "buffer_store_dwordx2 v[180:181], v9, s[16:19], s57 offen\n"
      "v_add_f32_dpp v239, v239, v239 row_ror:2 row_mask:0xf bank_mask:0xf\n"
      "s_nop 1\n"
      "v_add_f32_dpp v239, v239, v239 row_ror:1 row_mask:0xf bank_mask:0xf\n"
      "v_add_f32_dpp v238, v238, v238 row_ror:2 row_mask:0xf bank_mask:0xf\n"
      "v_add_f32 v239, 0x3afba882, v239\n"
      "buffer_store_dwordx2 v[180:181], v7, s[8:11], s56 offen sc1\n"
      "v_add_f32_dpp v238, v238, v238 row_ror:1 row_mask:0xf bank_mask:0xf\n"
      "v_mul_f32 v238, 0x3ae38e39, v238\n"
      "v_mul_f32 v239, 0x3ae38e39, v239\n"
      "buffer_store_dwordx2 v[238:239], v12, s[8:11], s56 offen sc1\n"
      "s_waitcnt vmcnt(0)\n"
      "buffer_store_dword v18, v5, s[8:11], s56 offen sc1\n"
      "s_branch .Ld_end\n"
      ".Ld_out1:\n"
      "s_nop 8\n"
      "v_cvt_pk_bf16_f32 v228, v160, v161\n"
      "v_cvt_pk_bf16_f32 v229, v162, v163\n"
      "buffer_store_dwordx2 v[228:229], v13, s[8:11], s37 offen sc1\n"
      "s_add_u32 s37, s37, 106496\n"
      "buffer_load_dword v21, v3, s[8:11], s53 offen sc1\n"
      "s_waitcnt vmcnt(1)\n"
      "buffer_store_dword v18, v4, s[8:11], s52 offen sc1\n"
      "s_add_u32 s54, s53, 13312\n"
      "s_cmp_lt_u32 s41, 32\n"
      "s_cselect_b32 s56, s39, 0x40000000\n"
      "s_waitcnt vmcnt(1)\n"
      "v_cmp_ne_u32 vcc, s21, v21\n"
      "s_add_u32 s58, s41, 4294967264\n"
      "s_cbranch_vccnz .Lsp_df_18\n"
      ".Lbk_df_18:\n"
      "buffer_load_dwordx4 v[176:179], v0, s[8:11], s53 offen sc1\n"
      "s_cmp_lt_u32 s58, 32\n"
      "s_cselect_b32 s57, s40, 0x40000000\n"
      "s_add_u32 s55, s53, 6656\n"
      "buffer_load_dwordx2 v[180:181], v13, s[8:11], s53 offen sc1\n"
      "buffer_load_dwordx4 v[184:187], v0, s[8:11], s55 offen sc1\n"
      "buffer_load_dwordx2 v[188:189], v13, s[8:11], s55 offen sc1\n"
      "s_add_u32 s39, s39, 147456\n"
      "s_add_u32 s52, s52, 106496\n"
      "buffer_load_dwordx4 v[192:195], v0, s[8:11], s54 offen sc1\n"
      "s_add_u32 s55, s53, 19968\n"
      "buffer_load_dwordx2 v[196:197], v13, s[8:11], s54 offen sc1\n"
      "buffer_load_dwordx2 v[204:205], v13, s[8:11], s55 offen sc1\n"
      "buffer_load_dwordx4 v[200:203], v0, s[8:11], s55 offen sc1\n"
      "s_add_u32 s54, s53, 26624\n"
      "s_add_u32 s41, s41, 6\n"
      "buffer_load_dwordx2 v[212:213], v13, s[8:11], s54 offen sc1\n"
      "s_add_u32 s40, s40, 4294856704\n"
      "buffer_load_dwordx4 v[208:211], v0, s[8:11], s54 offen sc1\n"
      "s_add_u32 s53, s53, 599040\n"
      "s_waitcnt vmcnt(9)\n"
      "v_lshlrev_b32 v232, 16, v176\n"
      "v_lshlrev_b32 v233, 16, v177\n"
      "v_and_b32 v176, 0xffff0000, v176\n"
      "v_lshlrev_b32 v234, 16, v178\n"
      "v_add_f32 v166, v166, v233\n"
      "v_add_f32 v164, v164, v232\n"
      "s_waitcnt vmcnt(8)\n"
      "v_lshlrev_b32 v233, 16, v181\n"
      "v_add_f32 v168, v168, v234\n"
      "v_lshlrev_b32 v232, 16, v180\n"
      "v_add_f32 v174, v174, v233\n"
      "s_waitcnt vmcnt(7)\n"
      "v_lshlrev_b32 v233, 16, v185\n"
      "v_and_b32 v185, 0xffff0000, v185\n"
      "v_add_f32 v172, v172, v232\n"
      "v_lshlrev_b32 v232, 16, v184\n"
      "v_add_f32 v164, v164, v232\n"
      "s_waitcnt vmcnt(6)\n"
      "v_lshlrev_b32 v232, 16, v188\n"
      "v_lshlrev_b32 v234, 16, v186\n"
      "v_add_f32 v165, v165, v176\n"
      "v_add_f32 v166, v166, v233\n"
      "v_and_b32 v177, 0xffff0000, v177\n"
      "v_lshlrev_b32 v233, 16, v189\n"
      "v_lshlrev_b32 v235, 16, v179\n"
      "v_add_f32 v172, v172, v232\n"
      "v_add_f32 v174, v174, v233\n"
      "v_and_b32 v184, 0xffff0000, v184\n"
      "v_add_f32 v167, v167, v177\n"
      "s_waitcnt vmcnt(5)\n"
      "v_lshlrev_b32 v233, 16, v193\n"
      "v_add_f32 v166, v166, v233\n"
      "v_lshlrev_b32 v232, 16, v192\n"
      "v_add_f32 v165, v165, v184\n"
      "v_and_b32 v189, 0xffff0000, v189\n"
      "v_and_b32 v186, 0xffff0000, v186\n"
      "v_add_f32 v164, v164, v232\n"
      "s_waitcnt vmcnt(4)\n"
      "v_lshlrev_b32 v232, 16, v196\n"
      "v_lshlrev_b32 v233, 16, v197\n"
      "v_add_f32 v174, v174, v233\n"
      "s_waitcnt vmcnt(2)\n"
      "v_lshlrev_b32 v233, 16, v201\n"
      "v_and_b32 v178, 0xffff0000, v178\n"
      "v_add_f32 v170, v170, v235\n"
      "v_add_f32 v172, v172, v232\n"
      "v_add_f32 v166, v166, v233\n"
      "v_lshlrev_b32 v232, 16, v200\n"
      "v_add_f32 v164, v164, v232\n"
      "v_lshlrev_b32 v233, 16, v205\n"
      "v_and_b32 v192, 0xffff0000, v192\n"
      "v_and_b32 v193, 0xffff0000, v193\n"
      "v_and_b32 v201, 0xffff0000, v201\n"
      "v_add_f32 v174, v174, v233\n"
      "v_add_f32 v169, v169, v178\n"
      "v_lshlrev_b32 v232, 16, v204\n"
      "v_add_f32 v172, v172, v232\n"
      "v_add_f32 v165, v165, v192\n"
      "s_waitcnt vmcnt(0)\n"
      "v_lshlrev_b32 v232, 16, v208\n"
      "v_lshlrev_b32 v233, 16, v209\n"
      "v_and_b32 v197, 0xffff0000, v197\n"
      "v_add_f32 v164, v164, v232\n"
      "v_mul_f32 v237, v164, v164\n"
      "v_lshlrev_b32 v235, 16, v187\n"
      "v_and_b32 v209, 0xffff0000, v209\n"
      "v_add_f32 v170, v170, v235\n"
      "v_and_b32 v200, 0xffff0000, v200\n"
      "v_add_f32 v168, v168, v234\n"
      "v_add_f32 v166, v166, v233\n"
      "v_lshlrev_b32 v232, 16, v212\n"
      "v_lshlrev_b32 v234, 16, v194\n"
      "v_add_f32 v165, v165, v200\n"
      "v_and_b32 v208, 0xffff0000, v208\n"
      "v_add_f32 v167, v167, v185\n"
      "v_add_f32 v168, v168, v234\n"
      "v_add_f32 v172, v172, v232\n"
      "v_lshlrev_b32 v235, 16, v195\n"
      "v_and_b32 v195, 0xffff0000, v195\n"
      "v_add_f32 v165, v165, v208\n"
      "v_and_b32 v212, 0xffff0000, v212\n"
      "v_and_b32 v187, 0xffff0000, v187\n"
      "v_and_b32 v179, 0xffff0000, v179\n"
      "v_add_f32 v167, v167, v193\n"
      "v_add_f32 v236, v164, v165\n"
      "v_add_f32 v170, v170, v235\n"
      "v_lshlrev_b32 v234, 16, v202\n"
      "v_fmac_f32 v237, v165, v165\n"
      "v_add_f32 v167, v167, v201\n"
      "v_lshlrev_b32 v235, 16, v203\n"
      "v_add_f32 v167, v167, v209\n"
      "v_add_f32 v236, v236, v166\n"
      "v_add_f32 v168, v168, v234\n"
      "v_lshlrev_b32 v234, 16, v210\n"
      "v_fmac_f32 v237, v166, v166\n"
      "v_add_f32 v236, v236, v167\n"
      "v_fmac_f32 v237, v167, v167\n"
      "v_and_b32 v203, 0xffff0000, v203\n"
      "v_cvt_pk_bf16_f32 v176, v164, v165\n"
      "v_and_b32 v181, 0xffff0000, v181\n"
      "v_and_b32 v188, 0xffff0000, v188\n"
      "v_add_f32 v168, v168, v234\n"
      "v_and_b32 v210, 0xffff0000, v210\n"
      "v_add_f32 v169, v169, v186\n"
      "v_and_b32 v194, 0xffff0000, v194\n"
      "v_add_f32 v169, v169, v194\n"
      "v_fmac_f32 v237, v168, v168\n"
      "v_add_f32 v171, v171, v179\n"
      "v_and_b32 v202, 0xffff0000, v202\n"
      "v_and_b32 v180, 0xffff0000, v180\n"
      "v_add_f32 v175, v175, v181\n"
      "v_add_f32 v170, v170, v235\n"
      "v_lshlrev_b32 v235, 16, v211\n"
      "v_add_f32 v170, v170, v235\n"
      "v_and_b32 v204, 0xffff0000, v204\n"
      "v_and_b32 v211, 0xffff0000, v211\n"
      "v_add_f32 v171, v171, v187\n"
      "v_add_f32 v171, v171, v195\n"
      "v_add_f32 v173, v173, v180\n"
      "v_add_f32 v236, v236, v168\n"
      "v_add_f32 v175, v175, v189\n"
      "v_add_f32 v169, v169, v202\n"
      "v_add_f32 v169, v169, v210\n"
      "v_fmac_f32 v237, v169, v169\n"
      "v_and_b32 v196, 0xffff0000, v196\n"
      "v_add_f32 v171, v171, v203\n"
      "v_add_f32 v173, v173, v188\n"
      "v_add_f32 v173, v173, v196\n"
      "v_add_f32 v171, v171, v211\n"
      "v_fmac_f32 v237, v170, v170\n"
      "v_lshlrev_b32 v233, 16, v213\n"
      "v_add_f32 v173, v173, v204\n"
      "v_cvt_pk_bf16_f32 v179, v170, v171\n"
      "v_add_f32 v236, v236, v169\n"
      "v_add_f32 v236, v236, v170\n"
      "v_add_f32 v236, v236, v171\n"
      "v_fmac_f32 v237, v171, v171\n"
      "v_cvt_pk_bf16_f32 v177, v166, v167\n"
      "v_add_f32 v236, v236, v172\n"
      "v_add_f32 v173, v173, v212\n"
      "v_cvt_pk_bf16_f32 v178, v168, v169\n"
      "v_fmac_f32 v237, v172, v172\n"
      "v_and_b32 v213, 0xffff0000, v213\n"
      "v_and_b32 v205, 0xffff0000, v205\n"
      "v_add_f32 v174, v174, v233\n"
      "v_add_f32 v236, v236, v173\n"
      "v_fmac_f32 v237, v173, v173\n"
      "v_add_f32 v175, v175, v197\n"
      "v_add_f32 v236, v236, v174\n"
      "v_add_f32 v175, v175, v205\n"
      "v_add_f32 v175, v175, v213\n"
      "v_fmac_f32 v237, v174, v174\n"
      "v_add_f32 v236, v236, v175\n"
      "v_fmac_f32 v237, v175, v175\n"
      "ds_write_b64 v10, v[236:237]\n"
      "s_waitcnt lgkmcnt(0)\n"
      "s_barrier\n"
      "ds_read_b64 v[238:239], v11\n"
      "buffer_store_dwordx4 v[176:179], v6, s[8:11], s56 offen sc1\n"
      "v_cvt_pk_bf16_f32 v181, v174, v175\n"
      "v_cvt_pk_bf16_f32 v180, v172, v173\n"
      "s_waitcnt lgkmcnt(0)\n"
      "v_add_f32_dpp v239, v239, v239 row_ror:8 row_mask:0xf bank_mask:0xf\n"
      "buffer_store_dwordx4 v[176:179], v8, s[16:19], s57 offen\n"
      "v_add_f32_dpp v238, v238, v238 row_ror:8 row_mask:0xf bank_mask:0xf\n"
      "v_add_f32_dpp v239, v239, v239 row_ror:4 row_mask:0xf bank_mask:0xf\n"
      "s_nop 0\n"
      "v_add_f32_dpp v238, v238, v238 row_ror:4 row_mask:0xf bank_mask:0xf\n"
      "buffer_store_dwordx2 v[180:181], v9, s[16:19], s57 offen\n"
      "v_add_f32_dpp v239, v239, v239 row_ror:2 row_mask:0xf bank_mask:0xf\n"
      "s_nop 1\n"
      "v_add_f32_dpp v239, v239, v239 row_ror:1 row_mask:0xf bank_mask:0xf\n"
      "v_add_f32_dpp v238, v238, v238 row_ror:2 row_mask:0xf bank_mask:0xf\n"
      "v_add_f32 v239, 0x3afba882, v239\n"
      "buffer_store_dwordx2 v[180:181], v7, s[8:11], s56 offen sc1\n"
      "v_add_f32_dpp v238, v238, v238 row_ror:1 row_mask:0xf bank_mask:0xf\n"
      "v_mul_f32 v238, 0x3ae38e39, v238\n"
      "v_mul_f32 v239, 0x3ae38e39, v239\n"
      "buffer_store_dwordx2 v[238:239], v12, s[8:11], s56 offen sc1\n"
      "s_waitcnt vmcnt(0)\n"
      "buffer_store_dword v18, v5, s[8:11], s56 offen sc1\n"
      "s_branch .Ld_end\n"
      ".Ld_out2:\n"
      "buffer_store_dwordx4 v[224:227], v0, s[8:11], s37 offen sc1\n"
      "s_nop 7\n"
      "v_cvt_pk_bf16_f32 v228, v160, v161\n"
      "v_cvt_pk_bf16_f32 v229, v162, v163\n"
      "buffer_store_dwordx2 v[228:229], v13, s[8:11], s37 offen sc1\n"
      "s_add_u32 s54, s53, 13312\n"
      "s_cmp_lt_u32 s41, 32\n"
      "s_cselect_b32 s56, s39, 0x40000000\n"
      "s_waitcnt vmcnt(3)\n"
      "v_cmp_ne_u32 vcc, s21, v21\n"
      "s_add_u32 s58, s41, 4294967264\n"
      "s_cbranch_vccnz .Lsp_df_19\n"
      ".Lbk_df_19:\n"
      "buffer_load_dwordx4 v[176:179], v0, s[8:11], s53 offen sc1\n"
      "s_cmp_lt_u32 s58, 32\n"
      "s_cselect_b32 s57, s40, 0x40000000\n"
      "s_add_u32 s55, s53, 6656\n"
      "buffer_load_dwordx2 v[180:181], v13, s[8:11], s53 offen sc1\n"
      "s_add_u32 s37, s37, 106496\n"
      "buffer_load_dwordx4 v[184:187], v0, s[8:11], s55 offen sc1\n"
      "buffer_load_dwordx2 v[188:189], v13, s[8:11], s55 offen sc1\n"
      "s_add_u32 s39, s39, 147456\n"
      "s_add_u32 s52, s52, 106496\n"
      "buffer_load_dwordx4 v[192:195], v0, s[8:11], s54 offen sc1\n"
      "s_add_u32 s55, s53, 19968\n"
      "buffer_load_dwordx2 v[196:197], v13, s[8:11], s54 offen sc1\n"
      "buffer_load_dwordx2 v[204:205], v13, s[8:11], s55 offen sc1\n"
      "buffer_load_dwordx4 v[200:203], v0, s[8:11], s55 offen sc1\n"
      "s_add_u32 s54, s53, 26624\n"
      "s_add_u32 s41, s41, 6\n"
      "buffer_load_dwordx2 v[212:213], v13, s[8:11], s54 offen sc1\n"
      "s_add_u32 s40, s40, 4294856704\n"
      "buffer_load_dwordx4 v[208:211], v0, s[8:11], s54 offen sc1\n"
      "s_add_u32 s53, s53, 599040\n"
      "s_waitcnt vmcnt(10)\n"
      "buffer_store_dword v18, v4, s[8:11], s52 offen sc1\n"
      "s_waitcnt vmcnt(10)\n"
      "v_lshlrev_b32 v232, 16, v176\n"
      "v_lshlrev_b32 v233, 16, v177\n"
      "v_and_b32 v176, 0xffff0000, v176\n"
      "v_lshlrev_b32 v234, 16, v178\n"
      "v_add_f32 v166, v166, v233\n"
      "v_add_f32 v164, v164, v232\n"
      "s_waitcnt vmcnt(9)\n"
      "v_lshlrev_b32 v233, 16, v181\n"
      "v_add_f32 v168, v168, v234\n"
      "v_lshlrev_b32 v232, 16, v180\n"
      "s_add_u32 s52, s52, 106496\n"
      "v_add_f32 v174, v174, v233\n"
      "s_waitcnt vmcnt(8)\n"
      "v_lshlrev_b32 v233, 16, v185\n"
      "v_and_b32 v185, 0xffff0000, v185\n"
      "v_add_f32 v172, v172, v232\n"
      "v_lshlrev_b32 v232, 16, v184\n"
      "v_add_f32 v164, v164, v232\n"
      "s_waitcnt vmcnt(7)\n"
      "v_lshlrev_b32 v232, 16, v188\n"
      "v_lshlrev_b32 v234, 16, v186\n"
      "v_add_f32 v165, v165, v176\n"
      "v_add_f32 v166, v166, v233\n"
      "v_and_b32 v177, 0xffff0000, v177\n"
      "v_lshlrev_b32 v233, 16, v189\n"
      "v_lshlrev_b32 v235, 16, v179\n"
      "v_add_f32 v172, v172, v232\n"
      "v_add_f32 v174, v174, v233\n"
      "v_and_b32 v184, 0xffff0000, v184\n"
      "v_add_f32 v167, v167, v177\n"
      "s_waitcnt vmcnt(6)\n"
      "v_lshlrev_b32 v233, 16, v193\n"
      "v_add_f32 v166, v166, v233\n"
      "v_lshlrev_b32 v232, 16, v192\n"
      "v_add_f32 v165, v165, v184\n"
      "v_and_b32 v189, 0xffff0000, v189\n"
      "v_and_b32 v186, 0xffff0000, v186\n"
      "v_add_f32 v164, v164, v232\n"
      "s_waitcnt vmcnt(5)\n"
      "v_lshlrev_b32 v232, 16, v196\n"
      "v_lshlrev_b32 v233, 16, v197\n"
      "v_add_f32 v174, v174, v233\n"
      "s_waitcnt vmcnt(3)\n"
      "v_lshlrev_b32 v233, 16, v201\n"
      "v_and_b32 v178, 0xffff0000, v178\n"
      "v_add_f32 v170, v170, v235\n"
      "v_add_f32 v172, v172, v232\n"
      "v_add_f32 v166, v166, v233\n"
      "v_lshlrev_b32 v232, 16, v200\n"
      "v_add_f32 v164, v164, v232\n"
      "v_lshlrev_b32 v233, 16, v205\n"
      "v_and_b32 v192, 0xffff0000, v192\n"
      "v_and_b32 v193, 0xffff0000, v193\n"
      "v_and_b32 v201, 0xffff0000, v201\n"
      "v_add_f32 v174, v174, v233\n"
      "v_add_f32 v169, v169, v178\n"
      "v_lshlrev_b32 v232, 16, v204\n"
      "v_add_f32 v172, v172, v232\n"
      "v_add_f32 v165, v165, v192\n"
      "s_waitcnt vmcnt(1)\n"
      "v_lshlrev_b32 v232, 16, v208\n"
      "v_lshlrev_b32 v233, 16, v209\n"
      "v_and_b32 v197, 0xffff0000, v197\n"
      "v_add_f32 v164, v164, v232\n"
      "v_mul_f32 v237, v164, v164\n"
      "v_lshlrev_b32 v235, 16, v187\n"
      "v_and_b32 v209, 0xffff0000, v209\n"
      "v_add_f32 v170, v170, v235\n"
      "v_and_b32 v200, 0xffff0000, v200\n"
      "v_add_f32 v168, v168, v234\n"
      "v_add_f32 v166, v166, v233\n"
      "v_lshlrev_b32 v232, 16, v212\n"
      "v_lshlrev_b32 v234, 16, v194\n"
      "v_add_f32 v165, v165, v200\n"
      "v_and_b32 v208, 0xffff0000, v208\n"
      "v_add_f32 v167, v167, v185\n"
      "v_add_f32 v168, v168, v234\n"
      "v_add_f32 v172, v172, v232\n"
      "v_lshlrev_b32 v235, 16, v195\n"
      "v_and_b32 v195, 0xffff0000, v195\n"
      "v_add_f32 v165, v165, v208\n"
      "v_and_b32 v212, 0xffff0000, v212\n"
      "v_and_b32 v187, 0xffff0000, v187\n"
      "v_and_b32 v179, 0xffff0000, v179\n"
      "v_add_f32 v167, v167, v193\n"
      "v_add_f32 v236, v164, v165\n"
      "v_add_f32 v170, v170, v235\n"
      "v_lshlrev_b32 v234, 16, v202\n"
      "v_fmac_f32 v237, v165, v165\n"
      "v_add_f32 v167, v167, v201\n"
      "v_lshlrev_b32 v235, 16, v203\n"
      "v_add_f32 v167, v167, v209\n"
      "v_add_f32 v236, v236, v166\n"
      "v_add_f32 v168, v168, v234\n"
      "v_lshlrev_b32 v234, 16, v210\n"
      "v_fmac_f32 v237, v166, v166\n"
      "v_add_f32 v236, v236, v167\n"
      "v_fmac_f32 v237, v167, v167\n"
      "v_and_b32 v203, 0xffff0000, v203\n"
      "v_cvt_pk_bf16_f32 v176, v164, v165\n"
      "v_and_b32 v181, 0xffff0000, v181\n"
      "v_and_b32 v188, 0xffff0000, v188\n"
      "v_add_f32 v168, v168, v234\n"
      "v_and_b32 v210, 0xffff0000, v210\n"
      "v_add_f32 v169, v169, v186\n"
      "v_and_b32 v194, 0xffff0000, v194\n"
      "v_add_f32 v169, v169, v194\n"
      "v_fmac_f32 v237, v168, v168\n"
      "v_add_f32 v171, v171, v179\n"
      "v_and_b32 v202, 0xffff0000, v202\n"
      "v_and_b32 v180, 0xffff0000, v180\n"
      "v_add_f32 v175, v175, v181\n"
      "v_add_f32 v170, v170, v235\n"
      "v_lshlrev_b32 v235, 16, v211\n"
      "v_add_f32 v170, v170, v235\n"
      "v_and_b32 v204, 0xffff0000, v204\n"
      "v_and_b32 v211, 0xffff0000, v211\n"
      "v_add_f32 v171, v171, v187\n"
      "v_add_f32 v171, v171, v195\n"
      "v_add_f32 v173, v173, v180\n"
      "v_add_f32 v236, v236, v168\n"
      "v_add_f32 v175, v175, v189\n"
      "v_add_f32 v169, v169, v202\n"
      "v_add_f32 v169, v169, v210\n"
      "v_fmac_f32 v237, v169, v169\n"
      "v_and_b32 v196, 0xffff0000, v196\n"
      "v_add_f32 v171, v171, v203\n"
      "v_add_f32 v173, v173, v188\n"
      "v_add_f32 v173, v173, v196\n"
      "v_add_f32 v171, v171, v211\n"
      "v_fmac_f32 v237, v170, v170\n"
      "v_lshlrev_b32 v233, 16, v213\n"
      "v_add_f32 v173, v173, v204\n"
      "v_cvt_pk_bf16_f32 v179, v170, v171\n"
      "v_add_f32 v236, v236, v169\n"
      "v_add_f32 v236, v236, v170\n"
      "v_add_f32 v236, v236, v171\n"
      "v_fmac_f32 v237, v171, v171\n"
      "v_cvt_pk_bf16_f32 v177, v166, v167\n"
      "v_add_f32 v236, v236, v172\n"
      "v_add_f32 v173, v173, v212\n"
      "v_cvt_pk_bf16_f32 v178, v168, v169\n"
      "v_fmac_f32 v237, v172, v172\n"
      "v_and_b32 v213, 0xffff0000, v213\n"
      "v_and_b32 v205, 0xffff0000, v205\n"
      "v_add_f32 v174, v174, v233\n"
      "v_add_f32 v236, v236, v173\n"
      "v_fmac_f32 v237, v173, v173\n"
      "v_add_f32 v175, v175, v197\n"
      "v_add_f32 v236, v236, v174\n"
      "v_add_f32 v175, v175, v205\n"
      "v_add_f32 v175, v175, v213\n"
      "v_fmac_f32 v237, v174, v174\n"
      "v_add_f32 v236, v236, v175\n"
      "v_fmac_f32 v237, v175, v175\n"
      "ds_write_b64 v10, v[236:237]\n"
      "s_waitcnt lgkmcnt(0)\n"
      "s_barrier\n"
      "ds_read_b64 v[238:239], v11\n"
      "buffer_store_dwordx4 v[176:179], v6, s[8:11], s56 offen sc1\n"
      "v_cvt_pk_bf16_f32 v181, v174, v175\n"
      "v_cvt_pk_bf16_f32 v180, v172, v173\n"
      "s_waitcnt lgkmcnt(0)\n"
      "v_add_f32_dpp v239, v239, v239 row_ror:8 row_mask:0xf bank_mask:0xf\n"
      "buffer_store_dwordx4 v[176:179], v8, s[16:19], s57 offen\n"
      "v_add_f32_dpp v238, v238, v238 row_ror:8 row_mask:0xf bank_mask:0xf\n"
      "v_add_f32_dpp v239, v239, v239 row_ror:4 row_mask:0xf bank_mask:0xf\n"
      "s_nop 0\n"
      "v_add_f32_dpp v238, v238, v238 row_ror:4 row_mask:0xf bank_mask:0xf\n"
      "buffer_store_dwordx2 v[180:181], v9, s[16:19], s57 offen\n"
      "v_add_f32_dpp v239, v239, v239 row_ror:2 row_mask:0xf bank_mask:0xf\n"
      "s_nop 1\n"
      "v_add_f32_dpp v239, v239, v239 row_ror:1 row_mask:0xf bank_mask:0xf\n"
      "v_add_f32_dpp v238, v238, v238 row_ror:2 row_mask:0xf bank_mask:0xf\n"
      "v_add_f32 v239, 0x3afba882, v239\n"
      "buffer_store_dwordx2 v[180:181], v7, s[8:11], s56 offen sc1\n"
      "v_add_f32_dpp v238, v238, v238 row_ror:1 row_mask:0xf bank_mask:0xf\n"
      "v_mul_f32 v238, 0x3ae38e39, v238\n"
      "v_mul_f32 v239, 0x3ae38e39, v239\n"
      "buffer_store_dwordx2 v[238:239], v12, s[8:11], s56 offen sc1\n"
      "s_waitcnt vmcnt(0)\n"
      "buffer_store_dword v18, v5, s[8:11], s56 offen sc1\n"
      "s_branch .Ld_end\n"
      ".Ld_out3:\n"
      "buffer_load_dwordx4 v[208:211], v0, s[8:11], s54 offen sc1\n"
      "s_add_u32 s53, s53, 599040\n"
      "s_nop 6\n"
      "v_cvt_pk_bf16_f32 v229, v162, v163\n"
      "v_cvt_pk_bf16_f32 v228, v160, v161\n"
      "buffer_store_dwordx4 v[224:227], v0, s[8:11], s37 offen sc1\n"
      "s_waitcnt vmcnt(17)\n"
      "buffer_store_dword v18, v4, s[8:11], s52 offen sc1\n"
      "buffer_store_dwordx2 v[228:229], v13, s[8:11], s37 offen sc1\n"
      "s_waitcnt vmcnt(12)\n"
      "v_lshlrev_b32 v232, 16, v176\n"
      "v_lshlrev_b32 v233, 16, v177\n"
      "v_and_b32 v176, 0xffff0000, v176\n"
      "v_lshlrev_b32 v234, 16, v178\n"
      "v_add_f32 v166, v166, v233\n"
      "v_add_f32 v164, v164, v232\n"
      "s_waitcnt vmcnt(11)\n"
      "v_lshlrev_b32 v233, 16, v181\n"
      "v_add_f32 v168, v168, v234\n"
      "v_lshlrev_b32 v232, 16, v180\n"
      "s_add_u32 s52, s52, 106496\n"
      "v_add_f32 v174, v174, v233\n"
      "s_waitcnt vmcnt(10)\n"
      "v_lshlrev_b32 v233, 16, v185\n"
      "v_and_b32 v185, 0xffff0000, v185\n"
      "v_add_f32 v172, v172, v232\n"
      "v_lshlrev_b32 v232, 16, v184\n"
      "v_add_f32 v164, v164, v232\n"
      "s_waitcnt vmcnt(9)\n"
      "v_lshlrev_b32 v232, 16, v188\n"
      "v_lshlrev_b32 v234, 16, v186\n"
      "v_add_f32 v165, v165, v176\n"
      "v_add_f32 v166, v166, v233\n"
      "v_and_b32 v177, 0xffff0000, v177\n"
      "v_lshlrev_b32 v233, 16, v189\n"
      "v_lshlrev_b32 v235, 16, v179\n"
      "v_add_f32 v172, v172, v232\n"
      "v_add_f32 v174, v174, v233\n"
      "v_and_b32 v184, 0xffff0000, v184\n"
      "v_add_f32 v167, v167, v177\n"
      "s_waitcnt vmcnt(8)\n"
      "v_lshlrev_b32 v233, 16, v193\n"
      "v_add_f32 v166, v166, v233\n"
      "v_lshlrev_b32 v232, 16, v192\n"
      "v_add_f32 v165, v165, v184\n"
      "v_and_b32 v189, 0xffff0000, v189\n"
      "v_and_b32 v186, 0xffff0000, v186\n"
      "v_add_f32 v164, v164, v232\n"
      "s_waitcnt vmcnt(7)\n"
      "v_lshlrev_b32 v232, 16, v196\n"
      "v_lshlrev_b32 v233, 16, v197\n"
      "v_add_f32 v174, v174, v233\n"
      "s_waitcnt vmcnt(5)\n"
      "v_lshlrev_b32 v233, 16, v201\n"
      "v_and_b32 v178, 0xffff0000, v178\n"
      "v_add_f32 v170, v170, v235\n"
      "v_add_f32 v172, v172, v232\n"
      "v_add_f32 v166, v166, v233\n"
      "v_lshlrev_b32 v232, 16, v200\n"
      "v_add_f32 v164, v164, v232\n"
      "v_lshlrev_b32 v233, 16, v205\n"
      "v_and_b32 v192, 0xffff0000, v192\n"
      "v_and_b32 v193, 0xffff0000, v193\n"
      "v_and_b32 v201, 0xffff0000, v201\n"
      "v_add_f32 v174, v174, v233\n"
      "v_add_f32 v169, v169, v178\n"
      "v_lshlrev_b32 v232, 16, v204\n"
      "v_add_f32 v172, v172, v232\n"
      "v_add_f32 v165, v165, v192\n"
      "s_waitcnt vmcnt(3)\n"
      "v_lshlrev_b32 v232, 16, v208\n"
      "v_lshlrev_b32 v233, 16, v209\n"
      "v_and_b32 v197, 0xffff0000, v197\n"
      "v_add_f32 v164, v164, v232\n"
      "v_mul_f32 v237, v164, v164\n"
      "v_lshlrev_b32 v235, 16, v187\n"
      "v_and_b32 v209, 0xffff0000, v209\n"
      "v_add_f32 v170, v170, v235\n"
      "v_and_b32 v200, 0xffff0000, v200\n"
      "v_add_f32 v168, v168, v234\n"
      "v_add_f32 v166, v166, v233\n"
      "v_lshlrev_b32 v232, 16, v212\n"
      "v_lshlrev_b32 v234, 16, v194\n"
      "v_add_f32 v165, v165, v200\n"
      "v_and_b32 v208, 0xffff0000, v208\n"
      "v_add_f32 v167, v167, v185\n"
      "v_add_f32 v168, v168, v234\n"
      "s_add_u32 s37, s37, 106496\n"
      "v_add_f32 v172, v172, v232\n"
      "v_lshlrev_b32 v235, 16, v195\n"
      "s_waitcnt vmcnt(0)\n"
      "buffer_store_dword v18, v4, s[8:11], s52 offen sc1\n"
      "v_and_b32 v195, 0xffff0000, v195\n"
      "v_add_f32 v165, v165, v208\n"
      "v_and_b32 v212, 0xffff0000, v212\n"
      "v_and_b32 v187, 0xffff0000, v187\n"
      "v_and_b32 v179, 0xffff0000, v179\n"
      "v_add_f32 v167, v167, v193\n"
      "v_add_f32 v236, v164, v165\n"
      "v_add_f32 v170, v170, v235\n"
      "v_lshlrev_b32 v234, 16, v202\n"
      "v_fmac_f32 v237, v165, v165\n"
      "v_add_f32 v167, v167, v201\n"
      "v_lshlrev_b32 v235, 16, v203\n"
      "v_add_f32 v167, v167, v209\n"
      "v_add_f32 v236, v236, v166\n"
      "v_add_f32 v168, v168, v234\n"
      "v_lshlrev_b32 v234, 16, v210\n"
      "v_fmac_f32 v237, v166, v166\n"
      "v_add_f32 v236, v236, v167\n"
      "v_fmac_f32 v237, v167, v167\n"
      "v_and_b32 v203, 0xffff0000, v203\n"
      "v_cvt_pk_bf16_f32 v176, v164, v165\n"
      "v_and_b32 v181, 0xffff0000, v181\n"
      "v_and_b32 v188, 0xffff0000, v188\n"
      "v_add_f32 v168, v168, v234\n"
      "v_and_b32 v210, 0xffff0000, v210\n"
      "v_add_f32 v169, v169, v186\n"
      "v_and_b32 v194, 0xffff0000, v194\n"
      "v_add_f32 v169, v169, v194\n"
      "v_fmac_f32 v237, v168, v168\n"
      "v_add_f32 v171, v171, v179\n"
      "v_and_b32 v202, 0xffff0000, v202\n"
      "v_and_b32 v180, 0xffff0000, v180\n"
      "v_add_f32 v175, v175, v181\n"
      "v_add_f32 v170, v170, v235\n"
      "v_lshlrev_b32 v235, 16, v211\n"
      "v_add_f32 v170, v170, v235\n"
      "v_and_b32 v204, 0xffff0000, v204\n"
      "v_and_b32 v211, 0xffff0000, v211\n"
      "v_add_f32 v171, v171, v187\n"
      "v_add_f32 v171, v171, v195\n"
      "v_add_f32 v173, v173, v180\n"
      "v_add_f32 v236, v236, v168\n"
      "v_add_f32 v175, v175, v189\n"
      "v_add_f32 v169, v169, v202\n"
      "v_add_f32 v169, v169, v210\n"
      "v_fmac_f32 v237, v169, v169\n"
      "v_and_b32 v196, 0xffff0000, v196\n"
      "v_add_f32 v171, v171, v203\n"
      "v_add_f32 v173, v173, v188\n"
      "v_add_f32 v173, v173, v196\n"
      "v_add_f32 v171, v171, v211\n"
      "v_fmac_f32 v237, v170, v170\n"
      "v_lshlrev_b32 v233, 16, v213\n"
      "v_add_f32 v173, v173, v204\n"
      "v_cvt_pk_bf16_f32 v179, v170, v171\n"
      "v_add_f32 v236, v236, v169\n"
      "v_add_f32 v236, v236, v170\n"
      "v_add_f32 v236, v236, v171\n"
      "v_fmac_f32 v237, v171, v171\n"
      "v_cvt_pk_bf16_f32 v177, v166, v167\n"
      "v_add_f32 v236, v236, v172\n"
      "v_add_f32 v173, v173, v212\n"
      "v_cvt_pk_bf16_f32 v178, v168, v169\n"
      "v_fmac_f32 v237, v172, v172\n"
      "v_and_b32 v213, 0xffff0000, v213\n"
      "v_and_b32 v205, 0xffff0000, v205\n"
      "v_add_f32 v174, v174, v233\n"
      "v_add_f32 v236, v236, v173\n"
      "v_fmac_f32 v237, v173, v173\n"
      "v_add_f32 v175, v175, v197\n"
      "v_add_f32 v236, v236, v174\n"
      "v_add_f32 v175, v175, v205\n"
      "v_add_f32 v175, v175, v213\n"
      "v_fmac_f32 v237, v174, v174\n"
      "v_add_f32 v236, v236, v175\n"
      "v_fmac_f32 v237, v175, v175\n"
      "ds_write_b64 v10, v[236:237]\n"
      "s_waitcnt lgkmcnt(0)\n"
      "s_barrier\n"
      "ds_read_b64 v[238:239], v11\n"
      "buffer_store_dwordx4 v[176:179], v6, s[8:11], s56 offen sc1\n"
      "v_cvt_pk_bf16_f32 v181, v174, v175\n"
      "v_cvt_pk_bf16_f32 v180, v172, v173\n"
      "s_add_u32 s52, s52, 106496\n"
      "s_waitcnt lgkmcnt(0)\n"
      "v_add_f32_dpp v239, v239, v239 row_ror:8 row_mask:0xf bank_mask:0xf\n"
      "buffer_store_dwordx4 v[176:179], v8, s[16:19], s57 offen\n"
      "v_add_f32_dpp v238, v238, v238 row_ror:8 row_mask:0xf bank_mask:0xf\n"
      "v_add_f32_dpp v239, v239, v239 row_ror:4 row_mask:0xf bank_mask:0xf\n"
      "s_nop 0\n"
      "v_add_f32_dpp v238, v238, v238 row_ror:4 row_mask:0xf bank_mask:0xf\n"
      "buffer_store_dwordx2 v[180:181], v9, s[16:19], s57 offen\n"
      "v_add_f32_dpp v239, v239, v239 row_ror:2 row_mask:0xf bank_mask:0xf\n"
      "s_nop 1\n"
      "v_add_f32_dpp v239, v239, v239 row_ror:1 row_mask:0xf bank_mask:0xf\n"
      "v_add_f32_dpp v238, v238, v238 row_ror:2 row_mask:0xf bank_mask:0xf\n"
      "v_add_f32 v239, 0x3afba882, v239\n"
      "buffer_store_dwordx2 v[180:181], v7, s[8:11], s56 offen sc1\n"
      "v_add_f32_dpp v238, v238, v238 row_ror:1 row_mask:0xf bank_mask:0xf\n"
      "v_mul_f32 v238, 0x3ae38e39, v238\n"
      "v_mul_f32 v239, 0x3ae38e39, v239\n"
      "buffer_store_dwordx2 v[238:239], v12, s[8:11], s56 offen sc1\n"
      "s_waitcnt vmcnt(0)\n"
      "buffer_store_dword v18, v5, s[8:11], s56 offen sc1\n"
      "s_branch .Ld_end\n"
      ".Ld_out4:\n"
      "v_lshlrev_b32 v234, 16, v194\n"
      "v_add_f32 v165, v165, v200\n"
      "v_and_b32 v208, 0xffff0000, v208\n"
      "v_add_f32 v167, v167, v185\n"
      "v_add_f32 v168, v168, v234\n"
      "s_add_u32 s37, s37, 106496\n"
      "v_add_f32 v172, v172, v232\n"
      "v_lshlrev_b32 v235, 16, v195\n"
      "s_nop 0\n"
      "v_cvt_pk_bf16_f32 v228, v160, v161\n"
      "s_waitcnt vmcnt(7)\n"
      "buffer_store_dword v18, v4, s[8:11], s52 offen sc1\n"
      "v_and_b32 v195, 0xffff0000, v195\n"
      "v_add_f32 v165, v165, v208\n"
      "v_and_b32 v212, 0xffff0000, v212\n"
      "v_and_b32 v187, 0xffff0000, v187\n"
      "buffer_store_dwordx4 v[224:227], v0, s[8:11], s37 offen sc1\n"
      "v_and_b32 v179, 0xffff0000, v179\n"
      "v_add_f32 v167, v167, v193\n"
      "v_add_f32 v236, v164, v165\n"
      "v_cvt_pk_bf16_f32 v229, v162, v163\n"
      "v_add_f32 v170, v170, v235\n"
      "v_lshlrev_b32 v234, 16, v202\n"
      "v_fmac_f32 v237, v165, v165\n"
      "v_add_f32 v167, v167, v201\n"
      "v_lshlrev_b32 v235, 16, v203\n"
      "buffer_store_dwordx2 v[228:229], v13, s[8:11], s37 offen sc1\n"
      "v_add_f32 v167, v167, v209\n"
      "v_add_f32 v236, v236, v166\n"
      "v_add_f32 v168, v168, v234\n"
      "v_lshlrev_b32 v234, 16, v210\n"
      "v_fmac_f32 v237, v166, v166\n"
      "v_add_f32 v236, v236, v167\n"
      "v_fmac_f32 v237, v167, v167\n"
      "v_and_b32 v203, 0xffff0000, v203\n"
      "v_cvt_pk_bf16_f32 v176, v164, v165\n"
      "v_and_b32 v181, 0xffff0000, v181\n"
      "v_and_b32 v188, 0xffff0000, v188\n"
      "v_add_f32 v168, v168, v234\n"
      "v_and_b32 v210, 0xffff0000, v210\n"
      "v_add_f32 v169, v169, v186\n"
      "v_and_b32 v194, 0xffff0000, v194\n"
      "v_add_f32 v169, v169, v194\n"
      "v_fmac_f32 v237, v168, v168\n"
      "v_add_f32 v171, v171, v179\n"
      "v_and_b32 v202, 0xffff0000, v202\n"
      "v_and_b32 v180, 0xffff0000, v180\n"
      "v_add_f32 v175, v175, v181\n"
      "v_add_f32 v170, v170, v235\n"
      "v_lshlrev_b32 v235, 16, v211\n"
      "v_add_f32 v170, v170, v235\n"
      "v_and_b32 v204, 0xffff0000, v204\n"
      "v_and_b32 v211, 0xffff0000, v211\n"
      "v_add_f32 v171, v171, v187\n"
      "v_add_f32 v171, v171, v195\n"
      "v_add_f32 v173, v173, v180\n"
      "v_add_f32 v236, v236, v168\n"
      "v_add_f32 v175, v175, v189\n"
      "v_add_f32 v169, v169, v202\n"
      "v_add_f32 v169, v169, v210\n"
      "v_fmac_f32 v237, v169, v169\n"
      "v_and_b32 v196, 0xffff0000, v196\n"
      "v_add_f32 v171, v171, v203\n"
      "v_add_f32 v173, v173, v188\n"
      "v_add_f32 v173, v173, v196\n"
      "v_add_f32 v171, v171, v211\n"
      "v_fmac_f32 v237, v170, v170\n"
      "v_lshlrev_b32 v233, 16, v213\n"
      "v_add_f32 v173, v173, v204\n"
      "v_cvt_pk_bf16_f32 v179, v170, v171\n"
      "v_add_f32 v236, v236, v169\n"
      "v_add_f32 v236, v236, v170\n"
      "v_add_f32 v236, v236, v171\n"
      "v_fmac_f32 v237, v171, v171\n"
      "v_cvt_pk_bf16_f32 v177, v166, v167\n"
      "v_add_f32 v236, v236, v172\n"
      "v_add_f32 v173, v173, v212\n"
      "v_cvt_pk_bf16_f32 v178, v168, v169\n"
      "s_add_u32 s37, s37, 106496\n"
      "v_fmac_f32 v237, v172, v172\n"
      "v_and_b32 v213, 0xffff0000, v213\n"
      "v_and_b32 v205, 0xffff0000, v205\n"
      "v_add_f32 v174, v174, v233\n"
      "v_add_f32 v236, v236, v173\n"
      "v_fmac_f32 v237, v173, v173\n"
      "v_add_f32 v175, v175, v197\n"
      "v_add_f32 v236, v236, v174\n"
      "v_add_f32 v175, v175, v205\n"
      "v_add_f32 v175, v175, v213\n"
      "v_fmac_f32 v237, v174, v174\n"
      "v_add_f32 v236, v236, v175\n"
      "v_fmac_f32 v237, v175, v175\n"
      "ds_write_b64 v10, v[236:237]\n"
      "s_waitcnt lgkmcnt(0)\n"
      "s_barrier\n"
      "ds_read_b64 v[238:239], v11\n"
      "buffer_store_dwordx4 v[176:179], v6, s[8:11], s56 offen sc1\n"
      "v_cvt_pk_bf16_f32 v181, v174, v175\n"
      "v_cvt_pk_bf16_f32 v180, v172, v173\n"
      "s_add_u32 s52, s52, 106496\n"
      "s_waitcnt lgkmcnt(0)\n"
      "v_add_f32_dpp v239, v239, v239 row_ror:8 row_mask:0xf bank_mask:0xf\n"
      "buffer_store_dwordx4 v[176:179], v8, s[16:19], s57 offen\n"
      "v_add_f32_dpp v238, v238, v238 row_ror:8 row_mask:0xf bank_mask:0xf\n"
      "v_add_f32_dpp v239, v239, v239 row_ror:4 row_mask:0xf bank_mask:0xf\n"
      "s_nop 0\n"
      "v_add_f32_dpp v238, v238, v238 row_ror:4 row_mask:0xf bank_mask:0xf\n"
      "buffer_store_dwordx2 v[180:181], v9, s[16:19], s57 offen\n"
      "v_add_f32_dpp v239, v239, v239 row_ror:2 row_mask:0xf bank_mask:0xf\n"
      "s_nop 1\n"
      "v_add_f32_dpp v239, v239, v239 row_ror:1 row_mask:0xf bank_mask:0xf\n"
      "v_add_f32_dpp v238, v238, v238 row_ror:2 row_mask:0xf bank_mask:0xf\n"
      "s_waitcnt vmcnt(3)\n"
      "buffer_store_dword v18, v4, s[8:11], s52 offen sc1\n"
      "v_add_f32 v239, 0x3afba882, v239\n"
      "buffer_store_dwordx2 v[180:181], v7, s[8:11], s56 offen sc1\n"
      "v_add_f32_dpp v238, v238, v238 row_ror:1 row_mask:0xf bank_mask:0xf\n"
      "v_mul_f32 v238, 0x3ae38e39, v238\n"
      "v_mul_f32 v239, 0x3ae38e39, v239\n"
      "buffer_store_dwordx2 v[238:239], v12, s[8:11], s56 offen sc1\n"
      "s_add_u32 s52, s52, 106496\n"
      "s_waitcnt vmcnt(0)\n"
      "buffer_store_dword v18, v5, s[8:11], s56 offen sc1\n"
      "s_branch .Ld_end\n"
      ".Ld_in1:\n"
      "s_add_u32 s36, s36, 165888\n"
      "s_mov_b32 s51, s36\n"
      "s_add_u32 s36, s36, 165888\n"
      "buffer_load_dword v20, v2, s[8:11], s36 offen sc1\n"
      "s_mov_b32 s51, s36\n"
      "s_add_u32 m0, s43, 24576\n"
      "s_add_u32 s36, s36, 165888\n"
      "s_waitcnt vmcnt(0)\n"
      "v_cmp_ne_u32 vcc, s21, v20\n"
      "s_add_u32 s38, s38, 147456\n"
      "s_cbranch_vccnz .Lsp_d1_20\n"
      ".Lbk_d1_20:\n"
      "buffer_load_dword v20, v2, s[8:11], s36 offen sc1\n"
      "buffer_load_dwordx4 v0, s[8:11], s51 offen offset:0 sc1 lds\n"
      "buffer_load_dwordx4 v0, s[8:11], s51 offen offset:1024 sc1 lds\n"
      "buffer_load_dwordx4 v0, s[8:11], s51 offen offset:2048 sc1 lds\n"
      "buffer_load_dwordx4 v0, s[8:11], s51 offen offset:3072 sc1 lds\n"
      "s_add_u32 m0, s43, 28672\n"
      "s_nop 0\n"
      "buffer_load_dwordx4 v1, s[8:11], s51 offen offset:1024 sc1 lds\n"
      "buffer_load_dwordx4 v1, s[8:11], s51 offen offset:0 sc1 lds\n"
      "s_add_u32 s52, s52, 106496\n"
      "s_mov_b32 s51, s36\n"
      "s_waitcnt vmcnt(6)\n"
      "v_cmp_ne_u32 vcc, s21, v20\n"
      "s_add_u32 m0, s43, 49152\n"
      "s_cbranch_vccnz .Lsp_d2_21\n"
      ".Lbk_d2_21:\n"
      "buffer_load_dwordx4 v0, s[8:11], s51 offen offset:2048 sc1 lds\n"
      "s_add_u32 s36, s36, 165888\n"
      "v_add_u32 v14, 1, v14\n"
      "buffer_load_dword v20, v2, s[8:11], s36 offen sc1\n"
      "buffer_load_dwordx4 v0, s[8:11], s51 offen offset:0 sc1 lds\n"
      "s_waitcnt vmcnt(3)\n"
      "ds_write_b32 v16, v14\n"
      "buffer_load_dwordx4 v0, s[8:11], s51 offen offset:3072 sc1 lds\n"
      "s_add_u32 s37, s37, 173056\n"
      "buffer_load_dwordx4 v0, s[8:11], s51 offen offset:1024 sc1 lds\n"
      "s_add_u32 m0, s43, 53248\n"
      "ds_read_b32 v15, v17\n"
      "buffer_load_dwordx4 v1, s[8:11], s51 offen offset:0 sc1 lds\n"
      "s_waitcnt lgkmcnt(0)\n"
      "v_cmp_gt_i32 vcc, v14, v15\n"
      "buffer_load_dwordx4 v1, s[8:11], s51 offen offset:1024 sc1 lds\n"
      "s_cbranch_vccnz .Lsp_dy1_22\n"
      ".Lbk_dy1_22:\n"
      "s_mov_b32 s51, s36\n"
      "ds_read_b128 v[64:67], v0 offset:26624\n"
      "ds_read_b128 v[60:63], v0 offset:25600\n"
      "ds_read_b128 v[84:87], v0 offset:31744\n"
      "ds_read_b128 v[76:79], v0 offset:29696\n"
      "ds_read_b128 v[80:83], v0 offset:30720\n"
      "ds_read_b128 v[56:59], v0 offset:24576\n"
      "ds_read_b128 v[72:75], v0 offset:28672\n"
      "ds_read_b128 v[88:91], v0 offset:32768\n"
      "ds_read_b128 v[92:95], v0 offset:33792\n"
      "ds_read_b128 v[68:71], v0 offset:27648\n"
      "ds_read_b128 v[96:99], v0 offset:34816\n"
      "ds_read_b128 v[104:107], v0 offset:36864\n"
      "ds_read_b128 v[100:103], v0 offset:35840\n"
      "ds_read_b128 v[124:127], v0 offset:41984\n"
      "ds_read_b128 v[128:131], v0 offset:43008\n"
      "s_waitcnt vmcnt(0) lgkmcnt(0)\n"
      "s_nop 4\n"
      "s_branch .Ld_t1\n"
      ".Ld_in2:\n"
      "s_add_u32 s36, s36, 165888\n"
      "s_mov_b32 s51, s36\n"
      "s_add_u32 s36, s36, 165888\n"
      "s_mov_b32 s51, s36\n"
      "s_add_u32 s36, s36, 165888\n"
      "s_add_u32 s38, s38, 147456\n"
      "buffer_load_dword v20, v2, s[8:11], s36 offen sc1\n"
      "s_add_u32 s52, s52, 106496\n"
      "s_mov_b32 s51, s36\n"
      "s_waitcnt vmcnt(0)\n"
      "v_cmp_ne_u32 vcc, s21, v20\n"
      "s_add_u32 m0, s43, 49152\n"
      "s_cbranch_vccnz .Lsp_d2_23\n"
      ".Lbk_d2_23:\n"
      "buffer_load_dwordx4 v0, s[8:11], s51 offen offset:2048 sc1 lds\n"
      "s_add_u32 s36, s36, 165888\n"
      "buffer_load_dword v20, v2, s[8:11], s36 offen sc1\n"
      "buffer_load_dwordx4 v0, s[8:11], s51 offen offset:0 sc1 lds\n"
      "buffer_load_dwordx4 v0, s[8:11], s51 offen offset:3072 sc1 lds\n"
      "s_add_u32 s37, s37, 173056\n"
      "buffer_load_dwordx4 v0, s[8:11], s51 offen offset:1024 sc1 lds\n"
      "s_add_u32 m0, s43, 53248\n"
      "s_nop 0\n"
      "buffer_load_dwordx4 v1, s[8:11], s51 offen offset:0 sc1 lds\n"
      "buffer_load_dwordx4 v1, s[8:11], s51 offen offset:1024 sc1 lds\n"
      "s_mov_b32 s51, s36\n"
      "s_add_u32 m0, s43, 73728\n"
      "s_add_u32 s52, s52, 106496\n"
      "s_add_u32 s36, s36, 165888\n"
      "s_waitcnt vmcnt(5)\n"
      "v_cmp_ne_u32 vcc, s21, v20\n"
      "s_cbranch_vccnz .Lsp_d3_24\n"
      ".Lbk_d3_24:\n"
      "buffer_load_dwordx4 v0, s[8:11], s51 offen offset:0 sc1 lds\n"
      "buffer_load_dwordx4 v0, s[8:11], s51 offen offset:1024 sc1 lds\n"
      "v_add_u32 v14, 1, v14\n"
      "buffer_load_dword v20, v2, s[8:11], s36 offen sc1\n"
      "buffer_load_dwordx4 v0, s[8:11], s51 offen offset:2048 sc1 lds\n"
      "s_waitcnt vmcnt(4)\n"
      "ds_write_b32 v16, v14\n"
      "buffer_load_dwordx4 v0, s[8:11], s51 offen offset:3072 sc1 lds\n"
      "s_add_u32 m0, s43, 77824\n"
      "ds_read_b32 v15, v17\n"
      "buffer_load_dwordx4 v1, s[8:11], s51 offen offset:0 sc1 lds\n"
      "s_waitcnt lgkmcnt(0)\n"
      "v_cmp_gt_i32 vcc, v14, v15\n"
      "buffer_load_dwordx4 v1, s[8:11], s51 offen offset:1024 sc1 lds\n"
      "s_cbranch_vccnz .Lsp_dy2_25\n"
      ".Lbk_dy2_25:\n"
      "ds_read_b128 v[60:63], v22 offset:1024\n"
      "ds_read_b128 v[56:59], v22 offset:0\n"
      "ds_read_b128 v[68:71], v22 offset:3072\n"
      "ds_read_b128 v[72:75], v22 offset:4096\n"
      "ds_read_b128 v[64:67], v22 offset:2048\n"
      "ds_read_b128 v[84:87], v22 offset:7168\n"
      "ds_read_b128 v[80:83], v22 offset:6144\n"
      "ds_read_b128 v[88:91], v22 offset:8192\n"
      "ds_read_b128 v[92:95], v22 offset:9216\n"
      "ds_read_b128 v[104:107], v22 offset:12288\n"
      "ds_read_b128 v[108:111], v22 offset:13312\n"
      "ds_read_b128 v[76:79], v22 offset:5120\n"
      "s_mov_b32 s51, s36\n"
      "ds_read_b128 v[100:103], v22 offset:11264\n"
      "ds_read_b128 v[128:131], v22 offset:18432\n"
      "ds_read_b128 v[96:99], v22 offset:10240\n"
      "s_add_u32 s52, s52, 173056\n"
      "ds_read_b128 v[116:119], v22 offset:15360\n"
      "s_add_u32 m0, s43, 98304\n"
      "s_waitcnt vmcnt(0) lgkmcnt(0)\n"
      "s_nop 4\n"
      "s_branch .Ld_t2\n"
      ".Ld_in3:\n"
      "s_add_u32 s36, s36, 165888\n"
      "s_mov_b32 s51, s36\n"
      "s_add_u32 s36, s36, 165888\n"
      "s_mov_b32 s51, s36\n"
      "s_add_u32 s36, s36, 165888\n"
      "s_add_u32 s38, s38, 147456\n"
      "s_add_u32 s52, s52, 106496\n"
      "s_mov_b32 s51, s36\n"
      "s_add_u32 s36, s36, 165888\n"
      "buffer_load_dword v20, v2, s[8:11], s36 offen sc1\n"
      "s_add_u32 s37, s37, 173056\n"
      "s_mov_b32 s51, s36\n"
      "s_add_u32 m0, s43, 73728\n"
      "s_add_u32 s52, s52, 106496\n"
      "s_add_u32 s36, s36, 165888\n"
      "s_waitcnt vmcnt(0)\n"
      "v_cmp_ne_u32 vcc, s21, v20\n"
      "s_cbranch_vccnz .Lsp_d3_26\n"
      ".Lbk_d3_26:\n"
      "buffer_load_dwordx4 v0, s[8:11], s51 offen offset:0 sc1 lds\n"
      "buffer_load_dwordx4 v0, s[8:11], s51 offen offset:1024 sc1 lds\n"
      "buffer_load_dword v20, v2, s[8:11], s36 offen sc1\n"
      "buffer_load_dwordx4 v0, s[8:11], s51 offen offset:2048 sc1 lds\n"
      "buffer_load_dwordx4 v0, s[8:11], s51 offen offset:3072 sc1 lds\n"
      "s_add_u32 m0, s43, 77824\n"
      "s_nop 0\n"
      "buffer_load_dwordx4 v1, s[8:11], s51 offen offset:0 sc1 lds\n"
      "buffer_load_dwordx4 v1, s[8:11], s51 offen offset:1024 sc1 lds\n"
      "s_mov_b32 s51, s36\n"
      "s_add_u32 s52, s52, 173056\n"
      "s_add_u32 m0, s43, 98304\n"
      "s_add_u32 s37, s37, 106496\n"
      "v_add_u32 v14, 1, v14\n"
      "s_add_u32 s36, s36, 165888\n"
      "s_waitcnt vmcnt(4)\n"
      "v_cmp_ne_u32 vcc, s21, v20\n"
      "s_cbranch_vccnz .Lsp_d4_27\n"
      ".Lbk_d4_27:\n"
      "buffer_load_dword v20, v2, s[8:11], s36 offen sc1\n"
      "buffer_load_dwordx4 v0, s[8:11], s51 offen offset:0 sc1 lds\n"
      "buffer_load_dwordx4 v0, s[8:11], s51 offen offset:1024 sc1 lds\n"
      "buffer_load_dwordx4 v0, s[8:11], s51 offen offset:3072 sc1 lds\n"
      "s_waitcnt vmcnt(4)\n"
      "ds_write_b32 v16, v14\n"
      "buffer_load_dwordx4 v0, s[8:11], s51 offen offset:2048 sc1 lds\n"
      "s_add_u32 m0, s43, 102400\n"
      "s_nop 0\n"
      "buffer_load_dwordx4 v1, s[8:11], s51 offen offset:1024 sc1 lds\n"
      "ds_read_b32 v15, v17\n"
      "buffer_load_dwordx4 v1, s[8:11], s51 offen offset:0 sc1 lds\n"
      "s_waitcnt lgkmcnt(0)\n"
      "v_cmp_gt_i32 vcc, v14, v15\n"
      "s_cbranch_vccnz .Lsp_dy3_28\n"
      ".Lbk_dy3_28:\n"
      "ds_read_b128 v[68:71], v22 offset:27648\n"
      "ds_read_b128 v[56:59], v22 offset:24576\n"
      "ds_read_b128 v[80:83], v22 offset:30720\n"
      "ds_read_b128 v[84:87], v22 offset:31744\n"
      "ds_read_b128 v[92:95], v22 offset:33792\n"
      "ds_read_b128 v[88:91], v22 offset:32768\n"
      "ds_read_b128 v[60:63], v22 offset:25600\n"
      "ds_read_b128 v[72:75], v22 offset:28672\n"
      "ds_read_b128 v[64:67], v22 offset:26624\n"
      "s_mov_b32 s51, s36\n"
      "ds_read_b128 v[96:99], v22 offset:34816\n"
      "ds_read_b128 v[100:103], v22 offset:35840\n"
      "ds_read_b128 v[116:119], v22 offset:39936\n"
      "ds_read_b128 v[76:79], v22 offset:29696\n"
      "ds_read_b128 v[104:107], v22 offset:36864\n"
      "ds_read_b128 v[128:131], v22 offset:43008\n"
      "s_waitcnt vmcnt(0) lgkmcnt(0)\n"
      "s_nop 4\n"
      "s_branch .Ld_t3\n"
      ".Ld_in4:\n"
      "s_add_u32 s36, s36, 165888\n"
      "s_mov_b32 s51, s36\n"
      "s_add_u32 s36, s36, 165888\n"
      "s_mov_b32 s51, s36\n"
      "s_add_u32 s36, s36, 165888\n"
      "s_add_u32 s38, s38, 147456\n"
      "s_add_u32 s52, s52, 106496\n"
      "s_mov_b32 s51, s36\n"
      "s_add_u32 s36, s36, 165888\n"
      "s_add_u32 s37, s37, 173056\n"
      "s_mov_b32 s51, s36\n"
      "s_add_u32 s52, s52, 106496\n"
      "s_add_u32 s36, s36, 165888\n"
      "buffer_load_dword v20, v2, s[8:11], s36 offen sc1\n"
      "s_mov_b32 s51, s36\n"
      "s_add_u32 s52, s52, 173056\n"
      "s_add_u32 m0, s43, 98304\n"
      "s_add_u32 s37, s37, 106496\n"
      "s_add_u32 s36, s36, 165888\n"
      "s_waitcnt vmcnt(0)\n"
      "v_cmp_ne_u32 vcc, s21, v20\n"
      "s_cbranch_vccnz .Lsp_d4_29\n"
      ".Lbk_d4_29:\n"
      "buffer_load_dword v20, v2, s[8:11], s36 offen sc1\n"
      "buffer_load_dwordx4 v0, s[8:11], s51 offen offset:0 sc1 lds\n"
      "buffer_load_dwordx4 v0, s[8:11], s51 offen offset:1024 sc1 lds\n"
      "buffer_load_dwordx4 v0, s[8:11], s51 offen offset:3072 sc1 lds\n"
      "buffer_load_dwordx4 v0, s[8:11], s51 offen offset:2048 sc1 lds\n"
      "s_add_u32 m0, s43, 102400\n"
      "s_nop 0\n"
      "buffer_load_dwordx4 v1, s[8:11], s51 offen offset:1024 sc1 lds\n"
      "buffer_load_dwordx4 v1, s[8:11], s51 offen offset:0 sc1 lds\n"
      "s_mov_b32 s51, s36\n"
      "s_add_u32 m0, s43, 122880\n"
      "s_waitcnt vmcnt(6)\n"
      "v_cmp_ne_u32 vcc, s21, v20\n"
      "s_cbranch_vccnz .Lsp_d5_30\n"
      ".Lbk_d5_30:\n"
      "buffer_load_dwordx4 v0, s[8:11], s51 offen offset:0 sc1 lds\n"
      "s_add_u32 s36, s36, 165888\n"
      "buffer_load_dword v20, v2, s[8:11], s36 offen sc1\n"
      "buffer_load_dwordx4 v0, s[8:11], s51 offen offset:1024 sc1 lds\n"
      "v_add_u32 v14, 1, v14\n"
      "buffer_load_dwordx4 v0, s[8:11], s51 offen offset:2048 sc1 lds\n"
      "s_cmp_lt_u32 s41, 32\n"
      "s_cselect_b32 s56, s39, 0x40000000\n"
      "buffer_load_dwordx4 v0, s[8:11], s51 offen offset:3072 sc1 lds\n"
      "s_add_u32 m0, s43, 126976\n"
      "s_nop 0\n"
      "buffer_load_dwordx4 v1, s[8:11], s51 offen offset:0 sc1 lds\n"
      "s_waitcnt vmcnt(6)\n"
      "ds_write_b32 v16, v14\n"
      "buffer_load_dwordx4 v1, s[8:11], s51 offen offset:1024 sc1 lds\n"
      "s_add_u32 s58, s41, 4294967264\n"
      "ds_read_b32 v15, v17\n"
      "s_cmp_lt_u32 s58, 32\n"
      "s_cselect_b32 s57, s40, 0x40000000\n"
      "s_add_u32 s37, s37, 106496\n"
      "s_waitcnt lgkmcnt(0)\n"
      "v_cmp_gt_i32 vcc, v14, v15\n"
      "s_cbranch_vccnz .Lsp_dy4_31\n"
      ".Lbk_dy4_31:\n"
      "ds_read_b128 v[64:67], v23 offset:2048\n"
      "s_add_u32 s39, s39, 147456\n"
      "ds_read_b128 v[56:59], v23 offset:0\n"
      "ds_read_b128 v[60:63], v23 offset:1024\n"
      "s_add_u32 s52, s52, 106496\n"
      "s_mov_b32 s51, s36\n"
      "ds_read_b128 v[80:83], v23 offset:6144\n"
      "ds_read_b128 v[68:71], v23 offset:3072\n"
      "ds_read_b128 v[92:95], v23 offset:9216\n"
      "ds_read_b128 v[88:91], v23 offset:8192\n"
      "ds_read_b128 v[76:79], v23 offset:5120\n"
      "ds_read_b128 v[108:111], v23 offset:13312\n"
      "ds_read_b128 v[72:75], v23 offset:4096\n"
      "ds_read_b128 v[96:99], v23 offset:10240\n"
      "s_add_u32 s41, s41, 6\n"
      "ds_read_b128 v[84:87], v23 offset:7168\n"
      "s_add_u32 s36, s36, 165888\n"
      "ds_read_b128 v[104:107], v23 offset:12288\n"
      "s_add_u32 m0, s43, 0\n"
      "ds_read_b128 v[120:123], v23 offset:16384\n"
      "s_add_u32 s40, s40, 4294856704\n"
      "ds_read_b128 v[124:127], v23 offset:17408\n"
      "s_waitcnt vmcnt(0) lgkmcnt(0)\n"
      "s_nop 4\n"
      "s_branch .Ld_t4\n"
      ".Ld_in5:\n"
      "s_add_u32 s36, s36, 165888\n"
      "s_mov_b32 s51, s36\n"
      "s_add_u32 s36, s36, 165888\n"
      "s_mov_b32 s51, s36\n"
      "s_add_u32 s36, s36, 165888\n"
      "s_add_u32 s38, s38, 147456\n"
      "s_add_u32 s52, s52, 106496\n"
      "s_mov_b32 s51, s36\n"
      "s_add_u32 s36, s36, 165888\n"
      "s_add_u32 s37, s37, 173056\n"
      "s_mov_b32 s51, s36\n"
      "s_add_u32 s52, s52, 106496\n"
      "s_add_u32 s36, s36, 165888\n"
      "s_mov_b32 s51, s36\n"
      "s_add_u32 s52, s52, 173056\n"
      "s_add_u32 s37, s37, 106496\n"
      "s_add_u32 s36, s36, 165888\n"
      "buffer_load_dword v20, v2, s[8:11], s36 offen sc1\n"
      "s_mov_b32 s51, s36\n"
      "s_add_u32 m0, s43, 122880\n"
      "s_waitcnt vmcnt(0)\n"
      "v_cmp_ne_u32 vcc, s21, v20\n"
      "s_cbranch_vccnz .Lsp_d5_32\n"
      ".Lbk_d5_32:\n"
      "buffer_load_dwordx4 v0, s[8:11], s51 offen offset:0 sc1 lds\n"
      "s_add_u32 s36, s36, 165888\n"
      "buffer_load_dword v20, v2, s[8:11], s36 offen sc1\n"
      "buffer_load_dwordx4 v0, s[8:11], s51 offen offset:1024 sc1 lds\n"
      "buffer_load_dwordx4 v0, s[8:11], s51 offen offset:2048 sc1 lds\n"
      "s_cmp_lt_u32 s41, 32\n"
      "s_cselect_b32 s56, s39, 0x40000000\n"
      "buffer_load_dwordx4 v0, s[8:11], s51 offen offset:3072 sc1 lds\n"
      "s_add_u32 m0, s43, 126976\n"
      "s_nop 0\n"
      "buffer_load_dwordx4 v1, s[8:11], s51 offen offset:0 sc1 lds\n"
      "buffer_load_dwordx4 v1, s[8:11], s51 offen offset:1024 sc1 lds\n"
      "s_add_u32 s58, s41, 4294967264\n"
      "s_cmp_lt_u32 s58, 32\n"
      "s_cselect_b32 s57, s40, 0x40000000\n"
      "s_add_u32 s37, s37, 106496\n"
      "s_add_u32 s39, s39, 147456\n"
      "s_add_u32 s52, s52, 106496\n"
      "s_mov_b32 s51, s36\n"
      "s_add_u32 s41, s41, 6\n"
      "s_add_u32 s36, s36, 165888\n"
      "s_add_u32 m0, s43, 0\n"
      "s_add_u32 s40, s40, 4294856704\n"
      "s_add_u32 s53, s53, 599040\n"
      "s_waitcnt vmcnt(5)\n"
      "v_cmp_ne_u32 vcc, s21, v20\n"
      "v_add_u32 v14, 1, v14\n"
      "s_cbranch_vccnz .Lsp_d0_33\n"
      ".Lbk_d0_33:\n"
      "buffer_load_dwordx4 v0, s[8:11], s51 offen offset:0 sc1 lds\n"
      "buffer_load_dwordx4 v0, s[8:11], s51 offen offset:2048 sc1 lds\n"
      "buffer_load_dwordx4 v0, s[8:11], s51 offen offset:3072 sc1 lds\n"
      "buffer_load_dwordx4 v0, s[8:11], s51 offen offset:1024 sc1 lds\n"
      "s_waitcnt vmcnt(4)\n"
      "ds_write_b32 v16, v14\n"
      "s_add_u32 m0, s43, 4096\n"
      "s_add_u32 s52, s52, 106496\n"
      "buffer_load_dwordx4 v1, s[8:11], s51 offen offset:0 sc1 lds\n"
      "buffer_load_dword v20, v2, s[8:11], s36 offen sc1\n"
      "ds_read_b32 v15, v17\n"
      "buffer_load_dwordx4 v1, s[8:11], s51 offen offset:1024 sc1 lds\n"
      "s_mov_b32 s51, s36\n"
      "s_waitcnt lgkmcnt(0)\n"
      "v_cmp_gt_i32 vcc, v14, v15\n"
      "s_cbranch_vccnz .Lsp_dy5_34\n"
      ".Lbk_dy5_34:\n"
      "ds_read_b128 v[60:63], v23 offset:25600\n"
      "ds_read_b128 v[64:67], v23 offset:26624\n"
      "ds_read_b128 v[84:87], v23 offset:31744\n"
      "ds_read_b128 v[76:79], v23 offset:29696\n"
      "ds_read_b128 v[92:95], v23 offset:33792\n"
      "ds_read_b128 v[80:83], v23 offset:30720\n"
      "ds_read_b128 v[56:59], v23 offset:24576\n"
      "ds_read_b128 v[96:99], v23 offset:34816\n"
      "ds_read_b128 v[108:111], v23 offset:37888\n"
      "ds_read_b128 v[120:123], v23 offset:40960\n"
      "ds_read_b128 v[72:75], v23 offset:28672\n"
      "ds_read_b128 v[68:71], v23 offset:27648\n"
      "ds_read_b128 v[88:91], v23 offset:32768\n"
      "ds_read_b128 v[136:139], v23 offset:45056\n"
      "ds_read_b128 v[116:119], v23 offset:39936\n"
      "s_waitcnt vmcnt(0) lgkmcnt(0)\n"
      "s_nop 4\n"
      "s_branch .Ld_t5\n"
      ".Ld_end:\n"
      "s_endpgm\n"
      ".Lsp_d0_1:\n"
      "s_mov_b32 s22, s23\n"
      ".Lsl_d0_1:\n"
      "buffer_load_dword v20, v2, s[8:11], s51 offen sc1\n"
      "s_waitcnt vmcnt(0)\n"
      "v_cmp_ne_u32 vcc, s21, v20\n"
      "s_cbranch_vccz .Lbk_d0_1\n"
      "s_sleep 4\n"
      "s_sub_u32 s22, s22, 1\n"
      "s_cbranch_scc0 .Lsl_d0_1\n"
      "s_mov_b32 s23, 0\n"
      "s_branch .Lbk_d0_1\n"
      ".Lsp_d1_2:\n"
      "s_mov_b32 s22, s23\n"
      ".Lsl_d1_2:\n"
      "buffer_load_dword v20, v2, s[8:11], s51 offen sc1\n"
      "s_waitcnt vmcnt(0)\n"
      "v_cmp_ne_u32 vcc, s21, v20\n"
      "s_cbranch_vccz .Lbk_d1_2\n"
      "s_sleep 4\n"
      "s_sub_u32 s22, s22, 1\n"
      "s_cbranch_scc0 .Lsl_d1_2\n"
      "s_mov_b32 s23, 0\n"
      "s_branch .Lbk_d1_2\n"
      ".Lsp_dy0_3:\n"
      "s_lshl_b32 s22, s23, 4\n"
      ".Lsl_dy0_3:\n"
      "ds_read_b32 v15, v17\n"
      "s_waitcnt lgkmcnt(0)\n"
      "v_cmp_gt_i32 vcc, v14, v15\n"
      "s_cbranch_vccz .Lbk_dy0_3\n"
      "s_sub_u32 s22, s22, 1\n"
      "s_cbranch_scc0 .Lsl_dy0_3\n"
      "s_mov_b32 s23, 0\n"
      "s_branch .Lbk_dy0_3\n"
      ".Lsp_d2_4:\n"
      "s_mov_b32 s22, s23\n"
      "buffer_load_dword v20, v2, s[8:11], s51 offen sc1\n"
      "s_waitcnt vmcnt(0)\n"
      "v_cmp_ne_u32 vcc, s21, v20\n"
      "s_add_u32 s30, s52, 106496\n"
      "buffer_store_dword v18, v4, s[8:11], s30 offen sc1\n"
      "s_cbranch_vccz .Lbk_d2_4\n"
      ".Lsl_d2_4:\n"
      "buffer_load_dword v20, v2, s[8:11], s51 offen sc1\n"
      "s_waitcnt vmcnt(0)\n"
      "v_cmp_ne_u32 vcc, s21, v20\n"
      "s_cbranch_vccz .Lbk_d2_4\n"
      "s_sleep 4\n"
      "s_sub_u32 s22, s22, 1\n"
      "s_cbranch_scc0 .Lsl_d2_4\n"
      "s_mov_b32 s23, 0\n"
      "s_branch .Lbk_d2_4\n"
      ".Lsp_dy1_5:\n"
      "s_lshl_b32 s22, s23, 4\n"
      ".Lsl_dy1_5:\n"
      "ds_read_b32 v15, v17\n"
      "s_waitcnt lgkmcnt(0)\n"
      "v_cmp_gt_i32 vcc, v14, v15\n"
      "s_cbranch_vccz .Lbk_dy1_5\n"
      "s_sub_u32 s22, s22, 1\n"
      "s_cbranch_scc0 .Lsl_dy1_5\n"
      "s_mov_b32 s23, 0\n"
      "s_branch .Lbk_dy1_5\n"
      ".Lsp_d3_6:\n"
      "s_mov_b32 s22, s23\n"
      ".Lsl_d3_6:\n"
      "buffer_load_dword v20, v2, s[8:11], s51 offen sc1\n"
      "s_waitcnt vmcnt(0)\n"
      "v_cmp_ne_u32 vcc, s21, v20\n"
      "s_cbranch_vccz .Lbk_d3_6\n"
      "s_sleep 4\n"
      "s_sub_u32 s22, s22, 1\n"
      "s_cbranch_scc0 .Lsl_d3_6\n"
      "s_mov_b32 s23, 0\n"
      "s_branch .Lbk_d3_6\n"
      ".Lsp_dy2_7:\n"
      "s_lshl_b32 s22, s23, 4\n"
      ".Lsl_dy2_7:\n"
      "ds_read_b32 v15, v17\n"
      "s_waitcnt lgkmcnt(0)\n"
      "v_cmp_gt_i32 vcc, v14, v15\n"
      "s_cbranch_vccz .Lbk_dy2_7\n"
      "s_sub_u32 s22, s22, 1\n"
      "s_cbranch_scc0 .Lsl_dy2_7\n"
      "s_mov_b32 s23, 0\n"
      "s_branch .Lbk_dy2_7\n"
      ".Lsp_d4_8:\n"
      "s_mov_b32 s22, s23\n"
      "buffer_load_dword v20, v2, s[8:11], s51 offen sc1\n"
      "s_waitcnt vmcnt(0)\n"
      "v_cmp_ne_u32 vcc, s21, v20\n"
      "s_add_u32 s30, s52, 0\n"
      "buffer_store_dword v18, v4, s[8:11], s30 offen sc1\n"
      "s_cbranch_vccz .Lbk_d4_8\n"
      ".Lsl_d4_8:\n"
      "buffer_load_dword v20, v2, s[8:11], s51 offen sc1\n"
      "s_waitcnt vmcnt(0)\n"
      "v_cmp_ne_u32 vcc, s21, v20\n"
      "s_cbranch_vccz .Lbk_d4_8\n"
      "s_sleep 4\n"
      "s_sub_u32 s22, s22, 1\n"
      "s_cbranch_scc0 .Lsl_d4_8\n"
      "s_mov_b32 s23, 0\n"
      "s_branch .Lbk_d4_8\n"
      ".Lsp_dy3_9:\n"
      "s_lshl_b32 s22, s23, 4\n"
      ".Lsl_dy3_9:\n"
      "ds_read_b32 v15, v17\n"
      "s_waitcnt lgkmcnt(0)\n"
      "v_cmp_gt_i32 vcc, v14, v15\n"
      "s_cbranch_vccz .Lbk_dy3_9\n"
      "s_sub_u32 s22, s22, 1\n"
      "s_cbranch_scc0 .Lsl_dy3_9\n"
      "s_mov_b32 s23, 0\n"
      "s_branch .Lbk_dy3_9\n"
      ".Lsp_d5_10:\n"
      "s_mov_b32 s22, s23\n"
      ".Lsl_d5_10:\n"
      "buffer_load_dword v20, v2, s[8:11], s51 offen sc1\n"
      "s_waitcnt vmcnt(0)\n"
      "v_cmp_ne_u32 vcc, s21, v20\n"
      "s_cbranch_vccz .Lbk_d5_10\n"
      "s_sleep 4\n"
      "s_sub_u32 s22, s22, 1\n"
      "s_cbranch_scc0 .Lsl_d5_10\n"
      "s_mov_b32 s23, 0\n"
      "s_branch .Lbk_d5_10\n"
      ".Lsp_df_11:\n"
      "s_mov_b32 s22, s23\n"
      "buffer_load_dword v21, v3, s[8:11], s53 offen sc1\n"
      "s_waitcnt vmcnt(0)\n"
      "v_cmp_ne_u32 vcc, s21, v21\n"
      "s_add_u32 s30, s52, 106496\n"
      "buffer_store_dword v18, v4, s[8:11], s30 offen sc1\n"
      "s_cbranch_vccz .Lbk_df_11\n"
      ".Lsl_df_11:\n"
      "buffer_load_dword v21, v3, s[8:11], s53 offen sc1\n"
      "s_waitcnt vmcnt(0)\n"
      "v_cmp_ne_u32 vcc, s21, v21\n"
      "s_cbranch_vccz .Lbk_df_11\n"
      "s_sleep 4\n"
      "s_sub_u32 s22, s22, 1\n"
      "s_cbranch_scc0 .Lsl_df_11\n"
      "s_mov_b32 s23, 0\n"
      "s_branch .Lbk_df_11\n"
      ".Lsp_dy4_12:\n"
      "s_lshl_b32 s22, s23, 4\n"
      ".Lsl_dy4_12:\n"
      "ds_read_b32 v15, v17\n"
      "s_waitcnt lgkmcnt(0)\n"
      "v_cmp_gt_i32 vcc, v14, v15\n"
      "s_cbranch_vccz .Lbk_dy4_12\n"
      "s_sub_u32 s22, s22, 1\n"
      "s_cbranch_scc0 .Lsl_dy4_12\n"
      "s_mov_b32 s23, 0\n"
      "s_branch .Lbk_dy4_12\n"
      ".Lsp_d0_13:\n"
      "s_mov_b32 s22, s23\n"
      "buffer_load_dword v20, v2, s[8:11], s51 offen sc1\n"
      "s_waitcnt vmcnt(0)\n"
      "v_cmp_ne_u32 vcc, s21, v20\n"
      "s_add_u32 s30, s52, 106496\n"
      "buffer_store_dword v18, v4, s[8:11], s30 offen sc1\n"
      "s_cbranch_vccz .Lbk_d0_13\n"
      ".Lsl_d0_13:\n"
      "buffer_load_dword v20, v2, s[8:11], s51 offen sc1\n"
      "s_waitcnt vmcnt(0)\n"
      "v_cmp_ne_u32 vcc, s21, v20\n"
      "s_cbranch_vccz .Lbk_d0_13\n"
      "s_sleep 4\n"
      "s_sub_u32 s22, s22, 1\n"
      "s_cbranch_scc0 .Lsl_d0_13\n"
      "s_mov_b32 s23, 0\n"
      "s_branch .Lbk_d0_13\n"
      ".Lsp_dy5_14:\n"
      "s_lshl_b32 s22, s23, 4\n"
      ".Lsl_dy5_14:\n"
      "ds_read_b32 v15, v17\n"
      "s_waitcnt lgkmcnt(0)\n"
      "v_cmp_gt_i32 vcc, v14, v15\n"
      "s_cbranch_vccz .Lbk_dy5_14\n"
      "s_sub_u32 s22, s22, 1\n"
      "s_cbranch_scc0 .Lsl_dy5_14\n"
      "s_mov_b32 s23, 0\n"
      "s_branch .Lbk_dy5_14\n"
      ".Lsp_d1_15:\n"
      "s_mov_b32 s22, s23\n"
      "buffer_load_dword v20, v2, s[8:11], s51 offen sc1\n"
      "s_waitcnt vmcnt(0)\n"
      "v_cmp_ne_u32 vcc, s21, v20\n"
      "s_add_u32 s30, s52, 106496\n"
      "buffer_store_dword v18, v4, s[8:11], s30 offen sc1\n"
      "s_cbranch_vccz .Lbk_d1_15\n"
      ".Lsl_d1_15:\n"
      "buffer_load_dword v20, v2, s[8:11], s51 offen sc1\n"
      "s_waitcnt vmcnt(0)\n"
      "v_cmp_ne_u32 vcc, s21, v20\n"
      "s_cbranch_vccz .Lbk_d1_15\n"
      "s_sleep 4\n"
      "s_sub_u32 s22, s22, 1\n"
      "s_cbranch_scc0 .Lsl_d1_15\n"
      "s_mov_b32 s23, 0\n"
      "s_branch .Lbk_d1_15\n"
      ".Lsp_dy0_16:\n"
      "s_lshl_b32 s22, s23, 4\n"
      ".Lsl_dy0_16:\n"
      "ds_read_b32 v15, v17\n"
      "s_waitcnt lgkmcnt(0)\n"
      "v_cmp_gt_i32 vcc, v14, v15\n"
      "s_cbranch_vccz .Lbk_dy0_16\n"
      "s_sub_u32 s22, s22, 1\n"
      "s_cbranch_scc0 .Lsl_dy0_16\n"
      "s_mov_b32 s23, 0\n"
      "s_branch .Lbk_dy0_16\n"
      ".Lsp_df_17:\n"
      "s_mov_b32 s22, s23\n"
      "buffer_load_dword v21, v3, s[8:11], s53 offen sc1\n"
      "s_waitcnt vmcnt(0)\n"
      "v_cmp_ne_u32 vcc, s21, v21\n"
      "s_add_u32 s30, s52, 106496\n"
      "buffer_store_dword v18, v4, s[8:11], s30 offen sc1\n"
      "s_cbranch_vccz .Lbk_df_17\n"
      ".Lsl_df_17:\n"
      "buffer_load_dword v21, v3, s[8:11], s53 offen sc1\n"
      "s_waitcnt vmcnt(0)\n"
      "v_cmp_ne_u32 vcc, s21, v21\n"
      "s_cbranch_vccz .Lbk_df_17\n"
      "s_sleep 4\n"
      "s_sub_u32 s22, s22, 1\n"
      "s_cbranch_scc0 .Lsl_df_17\n"
      "s_mov_b32 s23, 0\n"
      "s_branch .Lbk_df_17\n"
      ".Lsp_df_18:\n"
      "s_mov_b32 s22, s23\n"
      "buffer_load_dword v21, v3, s[8:11], s53 offen sc1\n"
      "s_waitcnt vmcnt(0)\n"
      "v_cmp_ne_u32 vcc, s21, v21\n"
      "s_add_u32 s30, s52, 106496\n"
      "buffer_store_dword v18, v4, s[8:11], s30 offen sc1\n"
      "s_cbranch_vccz .Lbk_df_18\n"
      ".Lsl_df_18:\n"
      "buffer_load_dword v21, v3, s[8:11], s53 offen sc1\n"
      "s_waitcnt vmcnt(0)\n"
      "v_cmp_ne_u32 vcc, s21, v21\n"
      "s_cbranch_vccz .Lbk_df_18\n"
      "s_sleep 4\n"
      "s_sub_u32 s22, s22, 1\n"
      "s_cbranch_scc0 .Lsl_df_18\n"
      "s_mov_b32 s23, 0\n"
      "s_branch .Lbk_df_18\n"
      ".Lsp_df_19:\n"
      "s_mov_b32 s22, s23\n"
      "buffer_load_dword v21, v3, s[8:11], s53 offen sc1\n"
      "s_waitcnt vmcnt(0)\n"
      "v_cmp_ne_u32 vcc, s21, v21\n"
      "s_add_u32 s30, s52, 106496\n"
      "buffer_store_dword v18, v4, s[8:11], s30 offen sc1\n"
      "s_cbranch_vccz .Lbk_df_19\n"
      ".Lsl_df_19:\n"
      "buffer_load_dword v21, v3, s[8:11], s53 offen sc1\n"
      "s_waitcnt vmcnt(0)\n"
      "v_cmp_ne_u32 vcc, s21, v21\n"
      "s_cbranch_vccz .Lbk_df_19\n"
      "s_sleep 4\n"
      "s_sub_u32 s22, s22, 1\n"
      "s_cbranch_scc0 .Lsl_df_19\n"
      "s_mov_b32 s23, 0\n"
      "s_branch .Lbk_df_19\n"
      ".Lsp_d1_20:\n"
      "s_mov_b32 s22, s23\n"
      ".Lsl_d1_20:\n"
      "buffer_load_dword v20, v2, s[8:11], s51 offen sc1\n"
      "s_waitcnt vmcnt(0)\n"
      "v_cmp_ne_u32 vcc, s21, v20\n"
      "s_cbranch_vccz .Lbk_d1_20\n"
      "s_sleep 4\n"
      "s_sub_u32 s22, s22, 1\n"
      "s_cbranch_scc0 .Lsl_d1_20\n"
      "s_mov_b32 s23, 0\n"
      "s_branch .Lbk_d1_20\n"
      ".Lsp_d2_21:\n"
      "s_mov_b32 s22, s23\n"
      ".Lsl_d2_21:\n"
      "buffer_load_dword v20, v2, s[8:11], s51 offen sc1\n"
      "s_waitcnt vmcnt(0)\n"
      "v_cmp_ne_u32 vcc, s21, v20\n"
      "s_cbranch_vccz .Lbk_d2_21\n"
      "s_sleep 4\n"
      "s_sub_u32 s22, s22, 1\n"
      "s_cbranch_scc0 .Lsl_d2_21\n"
      "s_mov_b32 s23, 0\n"
      "s_branch .Lbk_d2_21\n"
      ".Lsp_dy1_22:\n"
      "s_lshl_b32 s22, s23, 4\n"
      ".Lsl_dy1_22:\n"
      "ds_read_b32 v15, v17\n"
      "s_waitcnt lgkmcnt(0)\n"
      "v_cmp_gt_i32 vcc, v14, v15\n"
      "s_cbranch_vccz .Lbk_dy1_22\n"
      "s_sub_u32 s22, s22, 1\n"
      "s_cbranch_scc0 .Lsl_dy1_22\n"
      "s_mov_b32 s23, 0\n"
      "s_branch .Lbk_dy1_22\n"
      ".Lsp_d2_23:\n"
      "s_mov_b32 s22, s23\n"
      ".Lsl_d2_23:\n"
      "buffer_load_dword v20, v2, s[8:11], s51 offen sc1\n"
      "s_waitcnt vmcnt(0)\n"
      "v_cmp_ne_u32 vcc, s21, v20\n"
      "s_cbranch_vccz .Lbk_d2_23\n"
      "s_sleep 4\n"
      "s_sub_u32 s22, s22, 1\n"
      "s_cbranch_scc0 .Lsl_d2_23\n"
      "s_mov_b32 s23, 0\n"
      "s_branch .Lbk_d2_23\n"
      ".Lsp_d3_24:\n"
      "s_mov_b32 s22, s23\n"
      ".Lsl_d3_24:\n"
      "buffer_load_dword v20, v2, s[8:11], s51 offen sc1\n"
      "s_waitcnt vmcnt(0)\n"
      "v_cmp_ne_u32 vcc, s21, v20\n"
      "s_cbranch_vccz .Lbk_d3_24\n"
      "s_sleep 4\n"
      "s_sub_u32 s22, s22, 1\n"
      "s_cbranch_scc0 .Lsl_d3_24\n"
      "s_mov_b32 s23, 0\n"
      "s_branch .Lbk_d3_24\n"
      ".Lsp_dy2_25:\n"
      "s_lshl_b32 s22, s23, 4\n"
      ".Lsl_dy2_25:\n"
      "ds_read_b32 v15, v17\n"
      "s_waitcnt lgkmcnt(0)\n"
      "v_cmp_gt_i32 vcc, v14, v15\n"
      "s_cbranch_vccz .Lbk_dy2_25\n"
      "s_sub_u32 s22, s22, 1\n"
      "s_cbranch_scc0 .Lsl_dy2_25\n"
      "s_mov_b32 s23, 0\n"
      "s_branch .Lbk_dy2_25\n"
      ".Lsp_d3_26:\n"
      "s_mov_b32 s22, s23\n"
      ".Lsl_d3_26:\n"
      "buffer_load_dword v20, v2, s[8:11], s51 offen sc1\n"
      "s_waitcnt vmcnt(0)\n"
      "v_cmp_ne_u32 vcc, s21, v20\n"
      "s_cbranch_vccz .Lbk_d3_26\n"
      "s_sleep 4\n"
      "s_sub_u32 s22, s22, 1\n"
      "s_cbranch_scc0 .Lsl_d3_26\n"
      "s_mov_b32 s23, 0\n"
      "s_branch .Lbk_d3_26\n"
      ".Lsp_d4_27:\n"
      "s_mov_b32 s22, s23\n"
      ".Lsl_d4_27:\n"
      "buffer_load_dword v20, v2, s[8:11], s51 offen sc1\n"
      "s_waitcnt vmcnt(0)\n"
      "v_cmp_ne_u32 vcc, s21, v20\n"
      "s_cbranch_vccz .Lbk_d4_27\n"
      "s_sleep 4\n"
      "s_sub_u32 s22, s22, 1\n"
      "s_cbranch_scc0 .Lsl_d4_27\n"
      "s_mov_b32 s23, 0\n"
      "s_branch .Lbk_d4_27\n"
      ".Lsp_dy3_28:\n"
      "s_lshl_b32 s22, s23, 4\n"
      ".Lsl_dy3_28:\n"
      "ds_read_b32 v15, v17\n"
      "s_waitcnt lgkmcnt(0)\n"
      "v_cmp_gt_i32 vcc, v14, v15\n"
      "s_cbranch_vccz .Lbk_dy3_28\n"
      "s_sub_u32 s22, s22, 1\n"
      "s_cbranch_scc0 .Lsl_dy3_28\n"
      "s_mov_b32 s23, 0\n"
      "s_branch .Lbk_dy3_28\n"
      ".Lsp_d4_29:\n"
      "s_mov_b32 s22, s23\n"
      ".Lsl_d4_29:\n"
      "buffer_load_dword v20, v2, s[8:11], s51 offen sc1\n"
      "s_waitcnt vmcnt(0)\n"
      "v_cmp_ne_u32 vcc, s21, v20\n"
      "s_cbranch_vccz .Lbk_d4_29\n"
      "s_sleep 4\n"
      "s_sub_u32 s22, s22, 1\n"
      "s_cbranch_scc0 .Lsl_d4_29\n"
      "s_mov_b32 s23, 0\n"
      "s_branch .Lbk_d4_29\n"
      ".Lsp_d5_30:\n"
      "s_mov_b32 s22, s23\n"
      ".Lsl_d5_30:\n"
      "buffer_load_dword v20, v2, s[8:11], s51 offen sc1\n"
      "s_waitcnt vmcnt(0)\n"
      "v_cmp_ne_u32 vcc, s21, v20\n"
      "s_cbranch_vccz .Lbk_d5_30\n"
      "s_sleep 4\n"
      "s_sub_u32 s22, s22, 1\n"
      "s_cbranch_scc0 .Lsl_d5_30\n"
      "s_mov_b32 s23, 0\n"
      "s_branch .Lbk_d5_30\n"
      ".Lsp_dy4_31:\n"
      "s_lshl_b32 s22, s23, 4\n"
      ".Lsl_dy4_31:\n"
      "ds_read_b32 v15, v17\n"
      "s_waitcnt lgkmcnt(0)\n"
      "v_cmp_gt_i32 vcc, v14, v15\n"
      "s_cbranch_vccz .Lbk_dy4_31\n"
      "s_sub_u32 s22, s22, 1\n"
      "s_cbranch_scc0 .Lsl_dy4_31\n"
      "s_mov_b32 s23, 0\n"
      "s_branch .Lbk_dy4_31\n"
      ".Lsp_d5_32:\n"
      "s_mov_b32 s22, s23\n"
      ".Lsl_d5_32:\n"
      "buffer_load_dword v20, v2, s[8:11], s51 offen sc1\n"
      "s_waitcnt vmcnt(0)\n"
      "v_cmp_ne_u32 vcc, s21, v20\n"
      "s_cbranch_vccz .Lbk_d5_32\n"
      "s_sleep 4\n"
      "s_sub_u32 s22, s22, 1\n"
      "s_cbranch_scc0 .Lsl_d5_32\n"
      "s_mov_b32 s23, 0\n"
      "s_branch .Lbk_d5_32\n"
      ".Lsp_d0_33:\n"
      "s_mov_b32 s22, s23\n"
      ".Lsl_d0_33:\n"
      "buffer_load_dword v20, v2, s[8:11], s51 offen sc1\n"
      "s_waitcnt vmcnt(0)\n"
      "v_cmp_ne_u32 vcc, s21, v20\n"
      "s_cbranch_vccz .Lbk_d0_33\n"
      "s_sleep 4\n"
      "s_sub_u32 s22, s22, 1\n"
      "s_cbranch_scc0 .Lsl_d0_33\n"
      "s_mov_b32 s23, 0\n"
      "s_branch .Lbk_d0_33\n"
      ".Lsp_dy5_34:\n"
      "s_lshl_b32 s22, s23, 4\n"
      ".Lsl_dy5_34:\n"
      "ds_read_b32 v15, v17\n"
      "s_waitcnt lgkmcnt(0)\n"
      "v_cmp_gt_i32 vcc, v14, v15\n"
      "s_cbranch_vccz .Lbk_dy5_34\n"
      "s_sub_u32 s22, s22, 1\n"
      "s_cbranch_scc0 .Lsl_dy5_34\n"
      "s_mov_b32 s23, 0\n"
      "s_branch .Lbk_dy5_34\n"
      ".Ld_last:\n"
      :
      : "{s[8:11]}"(rsWS), "{s[12:15]}"(rsA), "{s[16:19]}"(rsB), "{s[92:95]}"(rsC), "{v0}"(v_l16), "{v1}"(v_l16b), "{v2}"(v_fa), "{v3}"(v_ga), "{v4}"(v_ra), "{v5}"(v_rf), "{v6}"(v_xp), "{v7}"(v_xs), "{v8}"(v_op), "{v9}"(v_os), "{v10}"(v_sw), "{v11}"(v_sr), "{v12}"(v_ss), "{v13}"(v_l8), "{v15}"(v_al), "{v16}"(v_yw), "{v17}"(v_yr), "{v176}"(v_at), "{v177}"(v_as), "{v178}"(v_vo0), "{v179}"(v_vo1), "{v180}"(v_vo2), "{v181}"(v_pw), "{v182}"(v_pv), "{v183}"(v_pu), "{s36}"(s_sh), "{s37}"(s_ps), "{s38}"(s_x1), "{s39}"(s_xr), "{s40}"(s_or), "{s41}"(s_p0), "{s42}"(s_m3), "{s43}"(s_m6), "{s44}"(s_so), "{s45}"(s_ph), "{s46}"(s_i0), "{s47}"(s_i5), "{s48}"(s_j0), "{s49}"(s_j5), "{s50}"(s_jx)
      : "v255", "a255", "s99", "vcc", "scc", "memory");

  } else {
    const unsigned tr = xcd * 3 + (slot - 14) / 6, j = 6 * unit_ks(tr) + (slot - 14) % 6, gr = unit_gr(tr), f = 4 * j + w;
    const u32x4 rsA = srd(w >> 1 ? Wu : Wg, 576 * 4608 * 2), rsB = srd(Wqkv, 576 * 1728 * 2), rsC = srd(Wo, 576 * 576 * 2);
    const unsigned wv = (slot - 14) * 4 + w, hf = (xcd >> 2) * 512;
    const unsigned v_tq[2] = {i16 < 15 ? (8 * wv + g) * 3456 + i16 / 5 * 1152 + hf + i16 % 5 * 128 : OOB, i16 < 15 ? v_tq[0] + 4 * 3456 : OOB};
    const unsigned v_tw = (lane & 7) < 5 ? (8 * wv + l8) * 1152 + hf + (lane & 7) * 128 : OOB, v_td = (wv * 8 + (lane & 7)) * 128;
    const unsigned v_tc = i16 * 64, v_l5 = w < 3 ? v_l16 + 16384 - w * 3072 : OOB;
    const unsigned v_fa = 19456 - w * 4096 + (lane >> 3) * 128 + (lane & 7) * 4, v_ra = line + 147456 - f * 1024 + f * 128;
    const unsigned v_st = 18432 + i16 * 32 + q * 8, v_pw = 147456 - f * 1024 + f * 128 + l8 * HREC + l7;
    const unsigned v_at = 65536 + (8 * g + q) * 256 + ((4 * w + p) ^ (4 * q)) * 16;
    const unsigned v_vo0 = (16 * (w & 1) + g) * 9216 + 256 * j + (i16 ^ (4 * g)) * 16;
    const unsigned v_vo1 = v_vo0 + 4 * 9216 - 1024, v_vo2 = v_vo1 + 4 * 9216 - 1024, v_vo3 = v_vo2 + 4 * 9216 - 1024;
    const unsigned s_sx = WS_X1 + gr * NREC * REC + w * 4096, s_sh = WS_H + gr * NREC * HREC + f * 1024;
    const unsigned s_m4 = w * 4096, s_m5 = 16384 + w * 1024;
    asm volatile(
      "s_buffer_load_dword s21, s[8:11], 0\n"
      "s_mov_b32 s23, 65536\n"
      "s_mov_b32 s31, 0x40000000\n"
      "s_mov_b32 s42, 0\n"
      "buffer_load_dword v60, v56, s[16:19], s42 offen\n"
      "buffer_load_dword v61, v57, s[16:19], s42 offen\n"
      "buffer_load_dword v62, v58, s[92:95], s42 offen\n"
      "s_getpc_b64 s[96:97]\n"
      ".Lg_pc:\n"
      "s_sub_u32 s96, s96, .Lg_pc-k_all\n"
      "s_subb_u32 s97, s97, 0\n"
      "v_min_u32 v59, .Lq_last-k_all, v59\n"
      "global_load_dword v63, v59, s[96:97]\n"
      "s_waitcnt vmcnt(0)\n"
      "s_add_u32 m0, s38, 65536\n"
      "s_nop 0\n"
      "buffer_load_dwordx4 v6, s[12:15], s42 offen offset:0 lds\n"
      "buffer_load_dwordx4 v7, s[12:15], s42 offen offset:1024 lds\n"
      "buffer_load_dwordx4 v8, s[12:15], s42 offen offset:2048 lds\n"
      "buffer_load_dwordx4 v9, s[12:15], s42 offen offset:3072 lds\n"
      "s_add_u32 s42, s42, 294912\n"
      "s_add_u32 m0, s38, 81920\n"
      "s_nop 0\n"
      "buffer_load_dwordx4 v6, s[12:15], s42 offen offset:0 lds\n"
      "buffer_load_dwordx4 v7, s[12:15], s42 offen offset:1024 lds\n"
      "buffer_load_dwordx4 v8, s[12:15], s42 offen offset:2048 lds\n"
      "buffer_load_dwordx4 v9, s[12:15], s42 offen offset:3072 lds\n"
      "s_add_u32 s42, s42, 294912\n"
      "s_add_u32 m0, s38, 98304\n"
      "s_nop 0\n"
      "buffer_load_dwordx4 v6, s[12:15], s42 offen offset:0 lds\n"
      "buffer_load_dwordx4 v7, s[12:15], s42 offen offset:1024 lds\n"
      "buffer_load_dwordx4 v8, s[12:15], s42 offen offset:2048 lds\n"
      "buffer_load_dwordx4 v9, s[12:15], s42 offen offset:3072 lds\n"
      "s_add_u32 s42, s42, 294912\n"
      "s_add_u32 m0, s38, 114688\n"
      "s_nop 0\n"
      "buffer_load_dwordx4 v6, s[12:15], s42 offen offset:0 lds\n"
      "buffer_load_dwordx4 v7, s[12:15], s42 offen offset:1024 lds\n"
      "buffer_load_dwordx4 v8, s[12:15], s42 offen offset:2048 lds\n"
      "buffer_load_dwordx4 v9, s[12:15], s42 offen offset:3072 lds\n"
      "s_add_u32 s42, s42, 294912\n"
      "s_add_u32 m0, s38, 131072\n"
      "s_nop 0\n"
      "buffer_load_dwordx4 v6, s[12:15], s42 offen offset:0 lds\n"
      "buffer_load_dwordx4 v7, s[12:15], s42 offen offset:1024 lds\n"
      "buffer_load_dwordx4 v8, s[12:15], s42 offen offset:2048 lds\n"
      "buffer_load_dwordx4 v9, s[12:15], s42 offen offset:3072 lds\n"
      "s_add_u32 s42, s42, 294912\n"
      "v_mov_b32 v176, 0x3f803f80\n"
      "v_mov_b32 v177, 0x3f803f80\n"
      "v_mov_b32 v178, 0x3f803f80\n"
      "v_mov_b32 v179, 0x3f803f80\n"
      "v_add_u32 v21, 49152, v5\n"
      "s_waitcnt vmcnt(16)\n"
      "s_barrier\n"
      "ds_read_b64_tr_b16 a[0:1], v5 offset:0\n"
      "ds_read_b64_tr_b16 a[2:3], v5 offset:1024\n"
      "ds_read_b64_tr_b16 a[72:73], v5 offset:8192\n"
      "ds_read_b64_tr_b16 a[74:75], v5 offset:9216\n"
      "ds_read_b64_tr_b16 a[144:145], v5 offset:8\n"
      "ds_read_b64_tr_b16 a[146:147], v5 offset:1032\n"
      "ds_read_b64_tr_b16 a[216:217], v5 offset:8200\n"
      "ds_read_b64_tr_b16 a[218:219], v5 offset:9224\n"
      "s_add_u32 m0, s38, 147456\n"
      "s_nop 0\n"
      "buffer_load_dwordx4 v6, s[12:15], s42 offen offset:0 lds\n"
      "buffer_load_dwordx4 v7, s[12:15], s42 offen offset:1024 lds\n"
      "buffer_load_dwordx4 v8, s[12:15], s42 offen offset:2048 lds\n"
      "buffer_load_dwordx4 v9, s[12:15], s42 offen offset:3072 lds\n"
      "s_add_u32 s42, s42, 294912\n"
      "s_waitcnt vmcnt(16) lgkmcnt(0)\n"
      "s_barrier\n"
      "ds_read_b64_tr_b16 a[4:5], v5 offset:16384\n"
      "ds_read_b64_tr_b16 a[6:7], v5 offset:17408\n"
      "ds_read_b64_tr_b16 a[76:77], v5 offset:24576\n"
      "ds_read_b64_tr_b16 a[78:79], v5 offset:25600\n"
      "ds_read_b64_tr_b16 a[148:149], v5 offset:16392\n"
      "ds_read_b64_tr_b16 a[150:151], v5 offset:17416\n"
      "ds_read_b64_tr_b16 a[220:221], v5 offset:24584\n"
      "ds_read_b64_tr_b16 a[222:223], v5 offset:25608\n"
      "v_mfma_f32_16x16x32_bf16 v[144:147], a[0:3], v[176:179], 0\n"
      "v_mfma_f32_16x16x32_bf16 v[148:151], a[72:75], v[176:179], 0\n"
      "v_mfma_f32_16x16x32_bf16 v[152:155], a[144:147], v[176:179], 0\n"
      "v_mfma_f32_16x16x32_bf16 v[156:159], a[216:219], v[176:179], 0\n"
      "s_add_u32 m0, s38, 65536\n"
      "s_nop 0\n"
      "buffer_load_dwordx4 v6, s[12:15], s42 offen offset:0 lds\n"
      "buffer_load_dwordx4 v7, s[12:15], s42 offen offset:1024 lds\n"
      "buffer_load_dwordx4 v8, s[12:15], s42 offen offset:2048 lds\n"
      "buffer_load_dwordx4 v9, s[12:15], s42 offen offset:3072 lds\n"
      "s_add_u32 s42, s42, 294912\n"
      "s_waitcnt vmcnt(16) lgkmcnt(0)\n"
      "s_barrier\n"
      "ds_read_b64_tr_b16 a[8:9], v5 offset:32768\n"
      "ds_read_b64_tr_b16 a[10:11], v5 offset:33792\n"
      "ds_read_b64_tr_b16 a[80:81], v5 offset:40960\n"
      "ds_read_b64_tr_b16 a[82:83], v5 offset:41984\n"
      "ds_read_b64_tr_b16 a[152:153], v5 offset:32776\n"
      "ds_read_b64_tr_b16 a[154:155], v5 offset:33800\n"
      "ds_read_b64_tr_b16 a[224:225], v5 offset:40968\n"
      "ds_read_b64_tr_b16 a[226:227], v5 offset:41992\n"
      "v_mfma_f32_16x16x32_bf16 v[144:147], a[4:7], v[176:179], v[144:147]\n"
      "v_mfma_f32_16x16x32_bf16 v[148:151], a[76:79], v[176:179], v[148:151]\n"
      "v_mfma_f32_16x16x32_bf16 v[152:155], a[148:151], v[176:179], v[152:155]\n"
      "v_mfma_f32_16x16x32_bf16 v[156:159], a[220:223], v[176:179], v[156:159]\n"
      "s_add_u32 m0, s38, 81920\n"
      "s_nop 0\n"
      "buffer_load_dwordx4 v6, s[12:15], s42 offen offset:0 lds\n"
      "buffer_load_dwordx4 v7, s[12:15], s42 offen offset:1024 lds\n"
      "buffer_load_dwordx4 v8, s[12:15], s42 offen offset:2048 lds\n"
      "buffer_load_dwordx4 v9, s[12:15], s42 offen offset:3072 lds\n"
      "s_add_u32 s42, s42, 294912\n"
      "s_waitcnt vmcnt(16) lgkmcnt(0)\n"
      "s_barrier\n"
      "ds_read_b64_tr_b16 a[12:13], v21 offset:0\n"
      "ds_read_b64_tr_b16 a[14:15], v21 offset:1024\n"
      "ds_read_b64_tr_b16 a[84:85], v21 offset:8192\n"
      "ds_read_b64_tr_b16 a[86:87], v21 offset:9216\n"
      "ds_read_b64_tr_b16 a[156:157], v21 offset:8\n"
      "ds_read_b64_tr_b16 a[158:159], v21 offset:1032\n"
      "ds_read_b64_tr_b16 a[228:229], v21 offset:8200\n"
      "ds_read_b64_tr_b16 a[230:231], v21 offset:9224\n"
      "v_mfma_f32_16x16x32_bf16 v[144:147], a[8:11], v[176:179], v[144:147]\n"
      "v_mfma_f32_16x16x32_bf16 v[148:151], a[80:83], v[176:179], v[148:151]\n"
      "v_mfma_f32_16x16x32_bf16 v[152:155], a[152:155], v[176:179], v[152:155]\n"
      "v_mfma_f32_16x16x32_bf16 v[156:159], a[224:227], v[176:179], v[156:159]\n"
      "s_add_u32 m0, s38, 98304\n"
      "s_nop 0\n"
      "buffer_load_dwordx4 v6, s[12:15], s42 offen offset:0 lds\n"
      "buffer_load_dwordx4 v7, s[12:15], s42 offen offset:1024 lds\n"
      "buffer_load_dwordx4 v8, s[12:15], s42 offen offset:2048 lds\n"
      "buffer_load_dwordx4 v9, s[12:15], s42 offen offset:3072 lds\n"
      "s_add_u32 s42, s42, 294912\n"
      "s_waitcnt vmcnt(16) lgkmcnt(0)\n"
      "s_barrier\n"
      "ds_read_b64_tr_b16 a[16:17], v21 offset:16384\n"
      "ds_read_b64_tr_b16 a[18:19], v21 offset:17408\n"
      "ds_read_b64_tr_b16 a[88:89], v21 offset:24576\n"
      "ds_read_b64_tr_b16 a[90:91], v21 offset:25600\n"
      "ds_read_b64_tr_b16 a[160:161], v21 offset:16392\n"
      "ds_read_b64_tr_b16 a[162:163], v21 offset:17416\n"
      "ds_read_b64_tr_b16 a[232:233], v21 offset:24584\n"
      "ds_read_b64_tr_b16 a[234:235], v21 offset:25608\n"
      "v_mfma_f32_16x16x32_bf16 v[144:147], a[12:15], v[176:179], v[144:147]\n"
      "v_mfma_f32_16x16x32_bf16 v[148:151], a[84:87], v[176:179], v[148:151]\n"
      "v_mfma_f32_16x16x32_bf16 v[152:155], a[156:159], v[176:179], v[152:155]\n"
      "v_mfma_f32_16x16x32_bf16 v[156:159], a[228:231], v[176:179], v[156:159]\n"
      "s_add_u32 m0, s38, 114688\n"
      "s_nop 0\n"
      "buffer_load_dwordx4 v6, s[12:15], s42 offen offset:0 lds\n"
      "buffer_load_dwordx4 v7, s[12:15], s42 offen offset:1024 lds\n"
      "buffer_load_dwordx4 v8, s[12:15], s42 offen offset:2048 lds\n"
      "buffer_load_dwordx4 v9, s[12:15], s42 offen offset:3072 lds\n"
      "s_add_u32 s42, s42, 294912\n"
      "s_waitcnt vmcnt(16) lgkmcnt(0)\n"
      "s_barrier\n"
      "ds_read_b64_tr_b16 a[20:21], v21 offset:32768\n"
      "ds_read_b64_tr_b16 a[22:23], v21 offset:33792\n"
      "ds_read_b64_tr_b16 a[92:93], v21 offset:40960\n"
      "ds_read_b64_tr_b16 a[94:95], v21 offset:41984\n"
      "ds_read_b64_tr_b16 a[164:165], v21 offset:32776\n"
      "ds_read_b64_tr_b16 a[166:167], v21 offset:33800\n"
      "ds_read_b64_tr_b16 a[236:237], v21 offset:40968\n"
      "ds_read_b64_tr_b16 a[238:239], v21 offset:41992\n"
      "v_mfma_f32_16x16x32_bf16 v[144:147], a[16:19], v[176:179], v[144:147]\n"
      "v_mfma_f32_16x16x32_bf16 v[148:151], a[88:91], v[176:179], v[148:151]\n"
      "v_mfma_f32_16x16x32_bf16 v[152:155], a[160:163], v[176:179], v[152:155]\n"
      "v_mfma_f32_16x16x32_bf16 v[156:159], a[232:235], v[176:179], v[156:159]\n"
      "s_add_u32 m0, s38, 131072\n"
      "s_nop 0\n"
      "buffer_load_dwordx4 v6, s[12:15], s42 offen offset:0 lds\n"
      "buffer_load_dwordx4 v7, s[12:15], s42 offen offset:1024 lds\n"
      "buffer_load_dwordx4 v8, s[12:15], s42 offen offset:2048 lds\n"
      "buffer_load_dwordx4 v9, s[12:15], s42 offen offset:3072 lds\n"
      "s_add_u32 s42, s42, 294912\n"
      "s_waitcnt vmcnt(16) lgkmcnt(0)\n"
      "s_barrier\n"
      "ds_read_b64_tr_b16 a[24:25], v5 offset:0\n"
      "ds_read_b64_tr_b16 a[26:27], v5 offset:1024\n"
      "ds_read_b64_tr_b16 a[96:97], v5 offset:8192\n"
      "ds_read_b64_tr_b16 a[98:99], v5 offset:9216\n"
      "ds_read_b64_tr_b16 a[168:169], v5 offset:8\n"
      "ds_read_b64_tr_b16 a[170:171], v5 offset:1032\n"
      "ds_read_b64_tr_b16 a[240:241], v5 offset:8200\n"
      "ds_read_b64_tr_b16 a[242:243], v5 offset:9224\n"
      "v_mfma_f32_16x16x32_bf16 v[144:147], a[20:23], v[176:179], v[144:147]\n"
      "v_mfma_f32_16x16x32_bf16 v[148:151], a[92:95], v[176:179], v[148:151]\n"
      "v_mfma_f32_16x16x32_bf16 v[152:155], a[164:167], v[176:179], v[152:155]\n"
      "v_mfma_f32_16x16x32_bf16 v[156:159], a[236:239], v[176:179], v[156:159]\n"
      "s_add_u32 m0, s38, 147456\n"
      "s_nop 0\n"
      "buffer_load_dwordx4 v6, s[12:15], s42 offen offset:0 lds\n"
      "buffer_load_dwordx4 v7, s[12:15], s42 offen offset:1024 lds\n"
      "buffer_load_dwordx4 v8, s[12:15], s42 offen offset:2048 lds\n"
      "buffer_load_dwordx4 v9, s[12:15], s42 offen offset:3072 lds\n"
      "s_add_u32 s42, s42, 294912\n"
      "s_waitcnt vmcnt(16) lgkmcnt(0)\n"
      "s_barrier\n"
      "ds_read_b64_tr_b16 a[28:29], v5 offset:16384\n"
      "ds_read_b64_tr_b16 a[30:31], v5 offset:17408\n"
      "ds_read_b64_tr_b16 a[100:101], v5 offset:24576\n"
      "ds_read_b64_tr_b16 a[102:103], v5 offset:25600\n"
      "ds_read_b64_tr_b16 a[172:173], v5 offset:16392\n"
      "ds_read_b64_tr_b16 a[174:175], v5 offset:17416\n"
      "ds_read_b64_tr_b16 a[244:245], v5 offset:24584\n"
      "ds_read_b64_tr_b16 a[246:247], v5 offset:25608\n"
      "v_mfma_f32_16x16x32_bf16 v[144:147], a[24:27], v[176:179], v[144:147]\n"
      "v_mfma_f32_16x16x32_bf16 v[148:151], a[96:99], v[176:179], v[148:151]\n"
      "v_mfma_f32_16x16x32_bf16 v[152:155], a[168:171], v[176:179], v[152:155]\n"
      "v_mfma_f32_16x16x32_bf16 v[156:159], a[240:243], v[176:179], v[156:159]\n"
      "s_add_u32 m0, s38, 65536\n"
      "s_nop 0\n"
      "buffer_load_dwordx4 v6, s[12:15], s42 offen offset:0 lds\n"
      "buffer_load_dwordx4 v7, s[12:15], s42 offen offset:1024 lds\n"
      "buffer_load_dwordx4 v8, s[12:15], s42 offen offset:2048 lds\n"
      "buffer_load_dwordx4 v9, s[12:15], s42 offen offset:3072 lds\n"
      "s_add_u32 s42, s42, 294912\n"
      "s_waitcnt vmcnt(16) lgkmcnt(0)\n"
      "s_barrier\n"
      "ds_read_b64_tr_b16 a[32:33], v5 offset:32768\n"
      "ds_read_b64_tr_b16 a[34:35], v5 offset:33792\n"
      "ds_read_b64_tr_b16 a[104:105], v5 offset:40960\n"
      "ds_read_b64_tr_b16 a[106:107], v5 offset:41984\n"
      "ds_read_b64_tr_b16 a[176:177], v5 offset:32776\n"
      "ds_read_b64_tr_b16 a[178:179], v5 offset:33800\n"
      "ds_read_b64_tr_b16 a[248:249], v5 offset:40968\n"
      "ds_read_b64_tr_b16 a[250:251], v5 offset:41992\n"
      "v_mfma_f32_16x16x32_bf16 v[144:147], a[28:31], v[176:179], v[144:147]\n"
      "v_mfma_f32_16x16x32_bf16 v[148:151], a[100:103], v[176:179], v[148:151]\n"
      "v_mfma_f32_16x16x32_bf16 v[152:155], a[172:175], v[176:179], v[152:155]\n"
      "v_mfma_f32_16x16x32_bf16 v[156:159], a[244:247], v[176:179], v[156:159]\n"
      "s_add_u32 m0, s38, 81920\n"
      "s_nop 0\n"
      "buffer_load_dwordx4 v6, s[12:15], s42 offen offset:0 lds\n"
      "buffer_load_dwordx4 v7, s[12:15], s42 offen offset:1024 lds\n"
      "buffer_load_dwordx4 v8, s[12:15], s42 offen offset:2048 lds\n"
      "buffer_load_dwordx4 v9, s[12:15], s42 offen offset:3072 lds\n"
      "s_add_u32 s42, s42, 294912\n"
      "s_waitcnt vmcnt(16) lgkmcnt(0)\n"
      "s_barrier\n"
      "ds_read_b64_tr_b16 a[36:37], v21 offset:0\n"
      "ds_read_b64_tr_b16 a[38:39], v21 offset:1024\n"
      "ds_read_b64_tr_b16 a[108:109], v21 offset:8192\n"
      "ds_read_b64_tr_b16 a[110:111], v21 offset:9216\n"
      "ds_read_b64_tr_b16 a[180:181], v21 offset:8\n"
      "ds_read_b64_tr_b16 a[182:183], v21 offset:1032\n"
      "ds_read_b64_tr_b16 a[252:253], v21 offset:8200\n"
      "ds_read_b64_tr_b16 a[254:255], v21 offset:9224\n"
      "v_mfma_f32_16x16x32_bf16 v[144:147], a[32:35], v[176:179], v[144:147]\n"
      "v_mfma_f32_16x16x32_bf16 v[148:151], a[104:107], v[176:179], v[148:151]\n"
      "v_mfma_f32_16x16x32_bf16 v[152:155], a[176:179], v[176:179], v[152:155]\n"
      "v_mfma_f32_16x16x32_bf16 v[156:159], a[248:251], v[176:179], v[156:159]\n"
      "s_add_u32 m0, s38, 98304\n"
      "s_nop 0\n"
      "buffer_load_dwordx4 v6, s[12:15], s42 offen offset:0 lds\n"
      "buffer_load_dwordx4 v7, s[12:15], s42 offen offset:1024 lds\n"
      "buffer_load_dwordx4 v8, s[12:15], s42 offen offset:2048 lds\n"
      "buffer_load_dwordx4 v9, s[12:15], s42 offen offset:3072 lds\n"
      "s_add_u32 s42, s42, 294912\n"
      "s_waitcnt vmcnt(16) lgkmcnt(0)\n"
      "s_barrier\n"
      "ds_read_b64_tr_b16 a[40:41], v21 offset:16384\n"
      "ds_read_b64_tr_b16 a[42:43], v21 offset:17408\n"
      "ds_read_b64_tr_b16 a[112:113], v21 offset:24576\n"
      "ds_read_b64_tr_b16 a[114:115], v21 offset:25600\n"
      "ds_read_b64_tr_b16 a[184:185], v21 offset:16392\n"
      "ds_read_b64_tr_b16 a[186:187], v21 offset:17416\n"
      "ds_read_b64_tr_b16 v[24:25], v21 offset:24584\n"
      "ds_read_b64_tr_b16 v[26:27], v21 offset:25608\n"
      "v_mfma_f32_16x16x32_bf16 v[144:147], a[36:39], v[176:179], v[144:147]\n"
      "v_mfma_f32_16x16x32_bf16 v[148:151], a[108:111], v[176:179], v[148:151]\n"
      "v_mfma_f32_16x16x32_bf16 v[152:155], a[180:183], v[176:179], v[152:155]\n"
      "v_mfma_f32_16x16x32_bf16 v[156:159], a[252:255], v[176:179], v[156:159]\n"
      "s_add_u32 m0, s38, 114688\n"
      "s_nop 0\n"
      "buffer_load_dwordx4 v6, s[12:15], s42 offen offset:0 lds\n"
      "buffer_load_dwordx4 v7, s[12:15], s42 offen offset:1024 lds\n"
      "buffer_load_dwordx4 v8, s[12:15], s42 offen offset:2048 lds\n"
      "buffer_load_dwordx4 v9, s[12:15], s42 offen offset:3072 lds\n"
      "s_add_u32 s42, s42, 294912\n"
      "s_waitcnt vmcnt(16) lgkmcnt(0)\n"
      "s_barrier\n"
      "ds_read_b64_tr_b16 a[44:45], v21 offset:32768\n"
      "ds_read_b64_tr_b16 a[46:47], v21 offset:33792\n"
      "ds_read_b64_tr_b16 a[116:117], v21 offset:40960\n"
      "ds_read_b64_tr_b16 a[118:119], v21 offset:41984\n"
      "ds_read_b64_tr_b16 a[188:189], v21 offset:32776\n"
      "ds_read_b64_tr_b16 a[190:191], v21 offset:33800\n"
      "ds_read_b64_tr_b16 v[28:29], v21 offset:40968\n"
      "ds_read_b64_tr_b16 v[30:31], v21 offset:41992\n"
      "v_mfma_f32_16x16x32_bf16 v[144:147], a[40:43], v[176:179], v[144:147]\n"
      "v_mfma_f32_16x16x32_bf16 v[148:151], a[112:115], v[176:179], v[148:151]\n"
      "v_mfma_f32_16x16x32_bf16 v[152:155], a[184:187], v[176:179], v[152:155]\n"
      "v_mfma_f32_16x16x32_bf16 v[156:159], v[24:27], v[176:179], v[156:159]\n"
      "s_add_u32 m0, s38, 131072\n"
      "s_nop 0\n"
      "buffer_load_dwordx4 v6, s[12:15], s42 offen offset:0 lds\n"
      "buffer_load_dwordx4 v7, s[12:15], s42 offen offset:1024 lds\n"
      "buffer_load_dwordx4 v8, s[12:15], s42 offen offset:2048 lds\n"
      "buffer_load_dwordx4 v9, s[12:15], s42 offen offset:3072 lds\n"
      "s_add_u32 s42, s42, 294912\n"
      "s_waitcnt vmcnt(16) lgkmcnt(0)\n"
      "s_barrier\n"
      "ds_read_b64_tr_b16 a[48:49], v5 offset:0\n"
      "ds_read_b64_tr_b16 a[50:51], v5 offset:1024\n"
      "ds_read_b64_tr_b16 a[120:121], v5 offset:8192\n"
      "ds_read_b64_tr_b16 a[122:123], v5 offset:9216\n"
      "ds_read_b64_tr_b16 a[192:193], v5 offset:8\n"
      "ds_read_b64_tr_b16 a[194:195], v5 offset:1032\n"
      "ds_read_b64_tr_b16 v[32:33], v5 offset:8200\n"
      "ds_read_b64_tr_b16 v[34:35], v5 offset:9224\n"
      "v_mfma_f32_16x16x32_bf16 v[144:147], a[44:47], v[176:179], v[144:147]\n"
      "v_mfma_f32_16x16x32_bf16 v[148:151], a[116:119], v[176:179], v[148:151]\n"
      "v_mfma_f32_16x16x32_bf16 v[152:155], a[188:191], v[176:179], v[152:155]\n"
      "v_mfma_f32_16x16x32_bf16 v[156:159], v[28:31], v[176:179], v[156:159]\n"
      "s_add_u32 m0, s38, 147456\n"
      "s_nop 0\n"
      "buffer_load_dwordx4 v6, s[12:15], s42 offen offset:0 lds\n"
      "buffer_load_dwordx4 v7, s[12:15], s42 offen offset:1024 lds\n"
      "buffer_load_dwordx4 v8, s[12:15], s42 offen offset:2048 lds\n"
      "buffer_load_dwordx4 v9, s[12:15], s42 offen offset:3072 lds\n"
      "s_add_u32 s42, s42, 294912\n"
      "s_waitcnt vmcnt(16) lgkmcnt(0)\n"
      "s_barrier\n"
      "ds_read_b64_tr_b16 a[52:53], v5 offset:16384\n"
      "ds_read_b64_tr_b16 a[54:55], v5 offset:17408\n"
      "ds_read_b64_tr_b16 a[124:125], v5 offset:24576\n"
      "ds_read_b64_tr_b16 a[126:127], v5 offset:25600\n"
      "ds_read_b64_tr_b16 a[196:197], v5 offset:16392\n"
      "ds_read_b64_tr_b16 a[198:199], v5 offset:17416\n"
      "ds_read_b64_tr_b16 v[36:37], v5 offset:24584\n"
      "ds_read_b64_tr_b16 v[38:39], v5 offset:25608\n"
      "v_mfma_f32_16x16x32_bf16 v[144:147], a[48:51], v[176:179], v[144:147]\n"
      "v_mfma_f32_16x16x32_bf16 v[148:151], a[120:123], v[176:179], v[148:151]\n"
      "v_mfma_f32_16x16x32_bf16 v[152:155], a[192:195], v[176:179], v[152:155]\n"
      "v_mfma_f32_16x16x32_bf16 v[156:159], v[32:35], v[176:179], v[156:159]\n"
      "s_waitcnt vmcnt(12) lgkmcnt(0)\n"
      "s_barrier\n"
      "ds_read_b64_tr_b16 a[56:57], v5 offset:32768\n"
      "ds_read_b64_tr_b16 a[58:59], v5 offset:33792\n"
      "ds_read_b64_tr_b16 a[128:129], v5 offset:40960\n"
      "ds_read_b64_tr_b16 a[130:131], v5 offset:41984\n"
      "ds_read_b64_tr_b16 a[200:201], v5 offset:32776\n"
      "ds_read_b64_tr_b16 a[202:203], v5 offset:33800\n"
      "ds_read_b64_tr_b16 v[40:41], v5 offset:40968\n"
      "ds_read_b64_tr_b16 v[42:43], v5 offset:41992\n"
      "v_mfma_f32_16x16x32_bf16 v[144:147], a[52:55], v[176:179], v[144:147]\n"
      "v_mfma_f32_16x16x32_bf16 v[148:151], a[124:127], v[176:179], v[148:151]\n"
      "v_mfma_f32_16x16x32_bf16 v[152:155], a[196:199], v[176:179], v[152:155]\n"
      "v_mfma_f32_16x16x32_bf16 v[156:159], v[36:39], v[176:179], v[156:159]\n"
      "s_waitcnt vmcnt(8) lgkmcnt(0)\n"
      "s_barrier\n"
      "ds_read_b64_tr_b16 a[60:61], v21 offset:0\n"
      "ds_read_b64_tr_b16 a[62:63], v21 offset:1024\n"
      "ds_read_b64_tr_b16 a[132:133], v21 offset:8192\n"
      "ds_read_b64_tr_b16 a[134:135], v21 offset:9216\n"
      "ds_read_b64_tr_b16 a[204:205], v21 offset:8\n"
      "ds_read_b64_tr_b16 a[206:207], v21 offset:1032\n"
      "ds_read_b64_tr_b16 v[44:45], v21 offset:8200\n"
      "ds_read_b64_tr_b16 v[46:47], v21 offset:9224\n"
      "v_mfma_f32_16x16x32_bf16 v[144:147], a[56:59], v[176:179], v[144:147]\n"
      "v_mfma_f32_16x16x32_bf16 v[148:151], a[128:131], v[176:179], v[148:151]\n"
      "v_mfma_f32_16x16x32_bf16 v[152:155], a[200:203], v[176:179], v[152:155]\n"
      "v_mfma_f32_16x16x32_bf16 v[156:159], v[40:43], v[176:179], v[156:159]\n"
      "s_waitcnt vmcnt(4) lgkmcnt(0)\n"
      "s_barrier\n"
      "ds_read_b64_tr_b16 a[64:65], v21 offset:16384\n"
      "ds_read_b64_tr_b16 a[66:67], v21 offset:17408\n"
      "ds_read_b64_tr_b16 a[136:137], v21 offset:24576\n"
      "ds_read_b64_tr_b16 a[138:139], v21 offset:25600\n"
      "ds_read_b64_tr_b16 a[208:209], v21 offset:16392\n"
      "ds_read_b64_tr_b16 a[210:211], v21 offset:17416\n"
      "ds_read_b64_tr_b16 v[48:49], v21 offset:24584\n"
      "ds_read_b64_tr_b16 v[50:51], v21 offset:25608\n"
      "v_mfma_f32_16x16x32_bf16 v[144:147], a[60:63], v[176:179], v[144:147]\n"
      "v_mfma_f32_16x16x32_bf16 v[148:151], a[132:135], v[176:179], v[148:151]\n"
      "v_mfma_f32_16x16x32_bf16 v[152:155], a[204:207], v[176:179], v[152:155]\n"
      "v_mfma_f32_16x16x32_bf16 v[156:159], v[44:47], v[176:179], v[156:159]\n"
      "s_waitcnt vmcnt(0) lgkmcnt(0)\n"
      "s_barrier\n"
      "ds_read_b64_tr_b16 a[68:69], v21 offset:32768\n"
      "ds_read_b64_tr_b16 a[70:71], v21 offset:33792\n"
      "ds_read_b64_tr_b16 a[140:141], v21 offset:40960\n"
      "ds_read_b64_tr_b16 a[142:143], v21 offset:41984\n"
      "ds_read_b64_tr_b16 a[212:213], v21 offset:32776\n"
      "ds_read_b64_tr_b16 a[214:215], v21 offset:33800\n"
      "ds_read_b64_tr_b16 v[52:53], v21 offset:40968\n"
      "ds_read_b64_tr_b16 v[54:55], v21 offset:41992\n"
      "v_mfma_f32_16x16x32_bf16 v[144:147], a[64:67], v[176:179], v[144:147]\n"
      "v_mfma_f32_16x16x32_bf16 v[148:151], a[136:139], v[176:179], v[148:151]\n"
      "v_mfma_f32_16x16x32_bf16 v[152:155], a[208:211], v[176:179], v[152:155]\n"
      "v_mfma_f32_16x16x32_bf16 v[156:159], v[48:51], v[176:179], v[156:159]\n"
      "s_waitcnt lgkmcnt(0)\n"
      "v_mfma_f32_16x16x32_bf16 v[144:147], a[68:71], v[176:179], v[144:147]\n"
      "v_mfma_f32_16x16x32_bf16 v[148:151], a[140:143], v[176:179], v[148:151]\n"
      "v_mfma_f32_16x16x32_bf16 v[152:155], a[212:215], v[176:179], v[152:155]\n"
      "v_mfma_f32_16x16x32_bf16 v[156:159], v[52:55], v[176:179], v[156:159]\n"
      "s_mul_i32 s21, s21, 1664525\n"
      "s_add_u32 s21, s21, 1013904223\n"
      "v_mov_b32 v16, s21\n"
      "buffer_store_dword v16, v13, s[8:11], 0 offen sc1\n"
      "v_not_b32 v56, v16\n"
      "v_not_b32 v57, v16\n"
      "v_not_b32 v58, v16\n"
      "v_not_b32 v59, v16\n"
      "s_add_u32 s30, s37, 0\n"
      "buffer_store_dwordx4 v[56:59], v11, s[8:11], s30 offen sc1\n"
      "s_add_u32 s30, s37, 1327104\n"
      "buffer_store_dwordx4 v[56:59], v11, s[8:11], s30 offen sc1\n"
      "s_add_u32 s30, s37, 2654208\n"
      "buffer_store_dwordx4 v[56:59], v11, s[8:11], s30 offen sc1\n"
      "s_add_u32 s30, s37, 3981312\n"
      "buffer_store_dwordx4 v[56:59], v11, s[8:11], s30 offen sc1\n"
      "s_add_u32 s30, s37, 5308416\n"
      "buffer_store_dwordx4 v[56:59], v11, s[8:11], s30 offen sc1\n"
      "s_add_u32 s30, s37, 6635520\n"
      "buffer_store_dwordx4 v[56:59], v11, s[8:11], s30 offen sc1\n"
      "s_add_u32 s30, s37, 7962624\n"
      "buffer_store_dwordx4 v[56:59], v11, s[8:11], s30 offen sc1\n"
      "s_add_u32 s30, s37, 9289728\n"
      "buffer_store_dwordx4 v[56:59], v11, s[8:11], s30 offen sc1\n"
      "s_add_u32 s30, s37, 10616832\n"
      "buffer_store_dword v16, v3, s[8:11], s30 offen sc1\n"
      "s_add_u32 s30, s37, 10782720\n"
      "buffer_store_dword v16, v3, s[8:11], s30 offen sc1\n"
      "s_add_u32 s30, s37, 10948608\n"
      "buffer_store_dword v16, v3, s[8:11], s30 offen sc1\n"
      "s_barrier\n"
      "v_mov_b32 v12, 0\n"
      "ds_write_b32 v14, v12\n"
      "s_waitcnt lgkmcnt(0)\n"
      "s_barrier\n"
      "s_add_u32 s36, s36, 4294942720\n"
      "s_mov_b32 s41, 40960\n"
      "s_add_u32 s43, s37, 4294469632\n"
      "s_add_u32 s37, s37, 4294801408\n"
      "s_add_u32 s36, s36, 24576\n"
      "buffer_load_dword v20, v2, s[8:11], s36 offen sc1\n"
      "s_mov_b32 s40, s36\n"
      "s_add_u32 s36, s36, 24576\n"
      "s_waitcnt vmcnt(0)\n"
      "v_cmp_ne_u32 vcc, s21, v20\n"
      "s_add_u32 s41, s41, 20480\n"
      "s_cbranch_vccnz .Lsp_g_1\n"
      ".Lbk_g_1:\n"
      "s_cmp_eq_u32 s41, 61440\n"
      "s_cselect_b32 s41, 0, s41\n"
      "s_add_u32 m0, s41, s38\n"
      "buffer_load_dword v20, v2, s[8:11], s36 offen sc1\n"
      "buffer_load_dwordx4 v0, s[8:11], s40 offen offset:0 sc1 lds\n"
      "buffer_load_dwordx4 v0, s[8:11], s40 offen offset:1024 sc1 lds\n"
      "buffer_load_dwordx4 v0, s[8:11], s40 offen offset:2048 sc1 lds\n"
      "buffer_load_dwordx4 v0, s[8:11], s40 offen offset:3072 sc1 lds\n"
      "s_add_u32 m0, s41, s39\n"
      "s_nop 0\n"
      "buffer_load_dwordx4 v1, s[8:11], s40 offen offset:0 sc1 lds\n"
      "v_add_u32 v19, s41, v4\n"
      "v_add_u32 v18, s41, v0\n"
      "s_mov_b32 s40, s36\n"
      "s_add_u32 s36, s36, 24576\n"
      "s_waitcnt vmcnt(5)\n"
      "v_cmp_ne_u32 vcc, s21, v20\n"
      "s_add_u32 s41, s41, 20480\n"
      "s_cbranch_vccnz .Lsp_g_2\n"
      ".Lbk_g_2:\n"
      "s_cmp_eq_u32 s41, 61440\n"
      "s_cselect_b32 s41, 0, s41\n"
      "s_add_u32 m0, s41, s38\n"
      "buffer_load_dword v20, v2, s[8:11], s36 offen sc1\n"
      "buffer_load_dword v17, v14, s[8:11], s31 offen\n"
      "buffer_load_dwordx4 v0, s[8:11], s40 offen offset:0 sc1 lds\n"
      "v_add_u32 v12, 1, v12\n"
      "buffer_load_dwordx4 v0, s[8:11], s40 offen offset:1024 sc1 lds\n"
      "s_waitcnt vmcnt(4)\n"
      "ds_write_b32 v14, v12\n"
      "buffer_load_dwordx4 v0, s[8:11], s40 offen offset:2048 sc1 lds\n"
      "buffer_load_dwordx4 v0, s[8:11], s40 offen offset:3072 sc1 lds\n"
      "ds_read_b32 v13, v15\n"
      "s_add_u32 m0, s41, s39\n"
      "s_nop 0\n"
      "buffer_load_dwordx4 v1, s[8:11], s40 offen offset:0 sc1 lds\n"
      "s_waitcnt lgkmcnt(0)\n"
      "v_cmp_gt_i32 vcc, v12, v13\n"
      "buffer_load_dword v17, v14, s[8:11], s31 offen\n"
      "s_cbranch_vccnz .Lsp_gy_3\n"
      ".Lbk_gy_3:\n"
      "ds_read2_b64 v[172:175], v19 offset1:64\n"
      "ds_read_b128 v[64:67], v18 offset:2048\n"
      "v_add_u32 v19, s41, v4\n"
      "ds_read_b128 v[68:71], v18 offset:3072\n"
      "ds_read_b128 v[72:75], v18 offset:4096\n"
      "ds_read_b128 v[56:59], v18 offset:0\n"
      "ds_read_b128 v[60:63], v18 offset:1024\n"
      "ds_read_b128 v[76:79], v18 offset:5120\n"
      "ds_read_b128 v[92:95], v18 offset:9216\n"
      "ds_read_b128 v[88:91], v18 offset:8192\n"
      "ds_read_b128 v[96:99], v18 offset:10240\n"
      "ds_read_b128 v[80:83], v18 offset:6144\n"
      "ds_read_b128 v[84:87], v18 offset:7168\n"
      "ds_read_b128 v[100:103], v18 offset:11264\n"
      "s_mov_b32 s20, 31\n"
      "s_waitcnt lgkmcnt(0)\n"
      "s_nop 4\n"
      ".p2align 6\n"
      ".Lg_loop:\n"
      "v_mfma_f32_16x16x32_bf16 v[128:131], a[0:3], v[56:59], 0\n"
      "ds_read_b128 v[116:119], v18 offset:15360\n"
      "v_mul_f32 v133, v133, v161\n"
      "v_mfma_f32_16x16x32_bf16 v[128:131], a[4:7], v[60:63], v[128:131]\n"
      "ds_read_b128 v[104:107], v18 offset:12288\n"
      "v_mul_f32 v135, v135, v163\n"
      "v_mfma_f32_16x16x32_bf16 v[128:131], a[8:11], v[64:67], v[128:131]\n"
      "ds_read_b128 v[108:111], v18 offset:13312\n"
      "ds_read_b128 v[120:123], v18 offset:16384\n"
      "v_mfma_f32_16x16x32_bf16 v[128:131], a[12:15], v[68:71], v[128:131]\n"
      "ds_read_b128 v[112:115], v18 offset:14336\n"
      "v_mul_f32 v167, v139, v167\n"
      "v_mfma_f32_16x16x32_bf16 v[128:131], a[16:19], v[72:75], v[128:131]\n"
      "ds_read_b128 v[124:127], v18 offset:17408\n"
      "v_add_u32 v18, s41, v0\n"
      "v_mfma_f32_16x16x32_bf16 v[128:131], a[20:23], v[76:79], v[128:131]\n"
      "s_waitcnt lgkmcnt(6)\n"
      "v_fmac_f32 v140, v180, v156\n"
      "v_rcp_f32 v164, v164\n"
      "v_add_f32 v172, v172, v174\n"
      "v_mfma_f32_16x16x32_bf16 v[128:131], a[24:27], v[80:83], v[128:131]\n"
      "s_mov_b32 s40, s36\n"
      "v_mul_f32 v134, v134, v162\n"
      "v_add_f32 v173, v173, v175\n"
      "v_mfma_f32_16x16x32_bf16 v[128:131], a[28:31], v[84:87], v[128:131]\n"
      "v_fmac_f32 v132, v180, v148\n"
      "v_fma_f32 v173, -v172, v172, v173\n"
      "v_mfma_f32_16x16x32_bf16 v[128:131], a[32:35], v[88:91], v[128:131]\n"
      "s_add_u32 s36, s36, 24576\n"
      "v_fmac_f32 v143, v180, v159\n"
      "v_fma_f32 v166, v166, v181, v181\n"
      "v_mfma_f32_16x16x32_bf16 v[128:131], a[36:39], v[92:95], v[128:131]\n"
      "v_rcp_f32 v165, v165\n"
      "v_mfma_f32_16x16x32_bf16 v[128:131], a[40:43], v[96:99], v[128:131]\n"
      "v_mul_f32 v165, v137, v165\n"
      "v_max_f32 v181, 0x3727c5ac, v173\n"
      "v_mfma_f32_16x16x32_bf16 v[128:131], a[44:47], v[100:103], v[128:131]\n"
      "s_waitcnt vmcnt(7) lgkmcnt(0)\n"
      "v_mul_f32 v164, v136, v164\n"
      "v_cmp_ne_u32 vcc, s21, v20\n"
      "v_mfma_f32_16x16x32_bf16 v[128:131], a[48:51], v[104:107], v[128:131]\n"
      "s_add_u32 s41, s41, 20480\n"
      "v_mul_f32 v132, v132, v160\n"
      "v_fmac_f32 v142, v180, v158\n"
      "v_mfma_f32_16x16x32_bf16 v[128:131], a[52:55], v[108:111], v[128:131]\n"
      "v_rsq_f32 v174, v181\n"
      "v_mfma_f32_16x16x32_bf16 v[128:131], a[56:59], v[112:115], v[128:131]\n"
      "v_mul_f32 v140, v140, v164\n"
      "v_mul_f32 v182, 0xc0135761, v174\n"
      "v_mfma_f32_16x16x32_bf16 v[128:131], a[60:63], v[116:119], v[128:131]\n"
      "v_mul_f32 v175, 0x3d372713, v182\n"
      "v_rcp_f32 v166, v166\n"
      "v_mfma_f32_16x16x32_bf16 v[128:131], a[64:67], v[120:123], v[128:131]\n"
      "s_cbranch_vccnz .Lsp_g_4\n"
      ".Lbk_g_4:\n"
      "v_mul_f32 v166, v138, v166\n"
      "v_mfma_f32_16x16x32_bf16 v[128:131], a[68:71], v[124:127], v[128:131]\n"
      "s_cmp_eq_u32 s41, 61440\n"
      "s_cselect_b32 s41, 0, s41\n"
      "v_mul_f32 v143, v143, v167\n"
      "v_mfma_f32_16x16x32_bf16 v[136:139], a[144:147], v[56:59], 0\n"
      "s_add_u32 s43, s43, 165888\n"
      "v_cvt_pk_bf16_f32 v169, v134, v135\n"
      "v_fmac_f32 v141, v180, v157\n"
      "v_mfma_f32_16x16x32_bf16 v[136:139], a[148:151], v[60:63], v[136:139]\n"
      "s_add_u32 m0, s41, s38\n"
      "v_mul_f32 v141, v141, v165\n"
      "v_mul_f32 v142, v142, v166\n"
      "v_cvt_pk_bf16_f32 v170, v140, v141\n"
      "v_mfma_f32_16x16x32_bf16 v[136:139], a[152:155], v[64:67], v[136:139]\n"
      "buffer_load_dword v20, v2, s[8:11], s36 offen sc1\n"
      "v_mfma_f32_16x16x32_bf16 v[136:139], a[156:159], v[68:71], v[136:139]\n"
      "v_cvt_pk_bf16_f32 v168, v132, v133\n"
      "v_cvt_pk_bf16_f32 v171, v142, v143\n"
      "v_mfma_f32_16x16x32_bf16 v[136:139], a[160:163], v[72:75], v[136:139]\n"
      "buffer_store_dwordx4 v[168:171], v0, s[8:11], s37 offen sc1\n"
      "v_mfma_f32_16x16x32_bf16 v[136:139], a[164:167], v[76:79], v[136:139]\n"
      "v_sub_f32 v180, 0, v172\n"
      "v_fmac_f32 v129, v180, v145\n"
      "v_mfma_f32_16x16x32_bf16 v[136:139], a[168:171], v[80:83], v[136:139]\n"
      "v_mul_f32 v161, v129, v129\n"
      "v_fmac_f32 v131, v180, v147\n"
      "v_mfma_f32_16x16x32_bf16 v[136:139], a[172:175], v[84:87], v[136:139]\n"
      "v_mul_f32 v174, v174, v174\n"
      "v_fmac_f32 v128, v180, v144\n"
      "v_mfma_f32_16x16x32_bf16 v[136:139], a[176:179], v[88:91], v[136:139]\n"
      "buffer_load_dwordx4 v0, s[8:11], s40 offen offset:0 sc1 lds\n"
      "v_mul_f32 v183, v175, v174\n"
      "v_mfma_f32_16x16x32_bf16 v[136:139], a[180:183], v[92:95], v[136:139]\n"
      "v_mul_f32 v163, v131, v131\n"
      "v_add_u32 v12, 1, v12\n"
      "v_mfma_f32_16x16x32_bf16 v[136:139], a[184:187], v[96:99], v[136:139]\n"
      "v_mul_f32 v160, v128, v128\n"
      "v_fma_f32 v161, v161, v183, v182\n"
      "v_mfma_f32_16x16x32_bf16 v[136:139], a[188:191], v[100:103], v[136:139]\n"
      "v_fma_f32 v160, v160, v183, v182\n"
      "v_mul_f32 v161, v129, v161\n"
      "v_mfma_f32_16x16x32_bf16 v[136:139], a[192:195], v[104:107], v[136:139]\n"
      "v_exp_f32 v161, v161\n"
      "v_mfma_f32_16x16x32_bf16 v[136:139], a[196:199], v[108:111], v[136:139]\n"
      "buffer_load_dwordx4 v0, s[8:11], s40 offen offset:1024 sc1 lds\n"
      "v_mfma_f32_16x16x32_bf16 v[136:139], a[200:203], v[112:115], v[136:139]\n"
      "v_fma_f32 v161, v161, v181, v181\n"
      "v_fmac_f32 v130, v180, v146\n"
      "v_mfma_f32_16x16x32_bf16 v[136:139], a[204:207], v[116:119], v[136:139]\n"
      "s_waitcnt vmcnt(5)\n"
      "ds_write_b32 v14, v12\n"
      "v_mul_f32 v160, v128, v160\n"
      "v_mfma_f32_16x16x32_bf16 v[136:139], a[208:211], v[120:123], v[136:139]\n"
      "v_fma_f32 v163, v163, v183, v182\n"
      "v_mul_f32 v163, v131, v163\n"
      "v_mfma_f32_16x16x32_bf16 v[136:139], a[212:215], v[124:127], v[136:139]\n"
      "v_rcp_f32 v161, v161\n"
      "v_mfma_f32_16x16x32_bf16 v[132:135], a[72:75], v[56:59], 0\n"
      "buffer_load_dwordx4 v0, s[8:11], s40 offen offset:2048 sc1 lds\n"
      "v_mfma_f32_16x16x32_bf16 v[132:135], a[76:79], v[60:63], v[132:135]\n"
      "v_mul_f32 v162, v130, v130\n"
      "v_fma_f32 v162, v162, v183, v182\n"
      "v_mfma_f32_16x16x32_bf16 v[132:135], a[80:83], v[64:67], v[132:135]\n"
      "v_exp_f32 v163, v163\n"
      "v_mfma_f32_16x16x32_bf16 v[132:135], a[84:87], v[68:71], v[132:135]\n"
      "v_fma_f32 v163, v163, v181, v181\n"
      "v_mul_f32 v161, v129, v161\n"
      "v_mfma_f32_16x16x32_bf16 v[132:135], a[88:91], v[72:75], v[132:135]\n"
      "v_fmac_f32 v137, v180, v153\n"
      "v_mul_f32 v162, v130, v162\n"
      "v_mfma_f32_16x16x32_bf16 v[132:135], a[92:95], v[76:79], v[132:135]\n"
      "s_add_u32 s37, s37, 165888\n"
      "v_exp_f32 v160, v160\n"
      "v_mfma_f32_16x16x32_bf16 v[132:135], a[96:99], v[80:83], v[132:135]\n"
      "buffer_load_dwordx4 v0, s[8:11], s40 offen offset:3072 sc1 lds\n"
      "v_mfma_f32_16x16x32_bf16 v[132:135], a[100:103], v[84:87], v[132:135]\n"
      "ds_read_b32 v13, v15\n"
      "v_fmac_f32 v139, v180, v155\n"
      "v_mul_f32 v167, v139, v139\n"
      "v_mfma_f32_16x16x32_bf16 v[132:135], a[104:107], v[88:91], v[132:135]\n"
      "v_fma_f32 v167, v167, v183, v182\n"
      "v_exp_f32 v162, v162\n"
      "v_mul_f32 v167, v139, v167\n"
      "v_mfma_f32_16x16x32_bf16 v[132:135], a[108:111], v[92:95], v[132:135]\n"
      "v_fma_f32 v160, v160, v181, v181\n"
      "v_mul_f32 v165, v137, v137\n"
      "v_mfma_f32_16x16x32_bf16 v[132:135], a[112:115], v[96:99], v[132:135]\n"
      "v_fmac_f32 v136, v180, v152\n"
      "v_fma_f32 v165, v165, v183, v182\n"
      "v_mfma_f32_16x16x32_bf16 v[132:135], a[116:119], v[100:103], v[132:135]\n"
      "s_add_u32 m0, s41, s39\n"
      "v_exp_f32 v167, v167\n"
      "v_mfma_f32_16x16x32_bf16 v[132:135], a[120:123], v[104:107], v[132:135]\n"
      "buffer_load_dwordx4 v1, s[8:11], s40 offen offset:0 sc1 lds\n"
      "v_mfma_f32_16x16x32_bf16 v[132:135], a[124:127], v[108:111], v[132:135]\n"
      "v_rcp_f32 v163, v163\n"
      "v_mfma_f32_16x16x32_bf16 v[132:135], a[128:131], v[112:115], v[132:135]\n"
      "v_fmac_f32 v138, v180, v154\n"
      "v_mul_f32 v166, v138, v138\n"
      "v_mfma_f32_16x16x32_bf16 v[132:135], a[132:135], v[116:119], v[132:135]\n"
      "s_waitcnt lgkmcnt(0)\n"
      "v_rcp_f32 v160, v160\n"
      "v_cmp_gt_i32 vcc, v12, v13\n"
      "v_mfma_f32_16x16x32_bf16 v[132:135], a[136:139], v[120:123], v[132:135]\n"
      "buffer_store_dword v16, v3, s[8:11], s43 offen sc1\n"
      "v_mfma_f32_16x16x32_bf16 v[132:135], a[140:143], v[124:127], v[132:135]\n"
      "v_fma_f32 v162, v162, v181, v181\n"
      "v_mul_f32 v164, v136, v136\n"
      "v_mfma_f32_16x16x32_bf16 v[140:143], a[216:219], v[56:59], 0\n"
      "s_cbranch_vccnz .Lsp_gy_5\n"
      ".Lbk_gy_5:\n"
      "ds_read2_b64 v[172:175], v19 offset1:64\n"
      "v_mfma_f32_16x16x32_bf16 v[140:143], a[220:223], v[60:63], v[140:143]\n"
      "v_mul_f32 v165, v137, v165\n"
      "v_fma_f32 v164, v164, v183, v182\n"
      "v_mfma_f32_16x16x32_bf16 v[140:143], a[224:227], v[64:67], v[140:143]\n"
      "ds_read_b128 v[64:67], v18 offset:2048\n"
      "v_add_u32 v19, s41, v4\n"
      "v_mfma_f32_16x16x32_bf16 v[140:143], a[228:231], v[68:71], v[140:143]\n"
      "ds_read_b128 v[68:71], v18 offset:3072\n"
      "v_rcp_f32 v162, v162\n"
      "v_mfma_f32_16x16x32_bf16 v[140:143], a[232:235], v[72:75], v[140:143]\n"
      "ds_read_b128 v[72:75], v18 offset:4096\n"
      "v_fma_f32 v166, v166, v183, v182\n"
      "v_mfma_f32_16x16x32_bf16 v[140:143], a[236:239], v[76:79], v[140:143]\n"
      "ds_read_b128 v[56:59], v18 offset:0\n"
      "v_mul_f32 v164, v136, v164\n"
      "v_mfma_f32_16x16x32_bf16 v[140:143], a[240:243], v[80:83], v[140:143]\n"
      "ds_read_b128 v[60:63], v18 offset:1024\n"
      "v_exp_f32 v164, v164\n"
      "v_mfma_f32_16x16x32_bf16 v[140:143], a[244:247], v[84:87], v[140:143]\n"
      "v_exp_f32 v165, v165\n"
      "v_mfma_f32_16x16x32_bf16 v[140:143], a[248:251], v[88:91], v[140:143]\n"
      "ds_read_b128 v[76:79], v18 offset:5120\n"
      "v_fma_f32 v165, v165, v181, v181\n"
      "v_fma_f32 v167, v167, v181, v181\n"
      "v_mfma_f32_16x16x32_bf16 v[140:143], a[252:255], v[92:95], v[140:143]\n"
      "v_mul_f32 v162, v130, v162\n"
      "v_fmac_f32 v135, v180, v151\n"
      "v_fma_f32 v164, v164, v181, v181\n"
      "v_mfma_f32_16x16x32_bf16 v[140:143], v[24:27], v[96:99], v[140:143]\n"
      "ds_read_b128 v[92:95], v18 offset:9216\n"
      "v_mul_f32 v166, v138, v166\n"
      "v_mfma_f32_16x16x32_bf16 v[140:143], v[28:31], v[100:103], v[140:143]\n"
      "ds_read_b128 v[88:91], v18 offset:8192\n"
      "v_rcp_f32 v167, v167\n"
      "v_mfma_f32_16x16x32_bf16 v[140:143], v[32:35], v[104:107], v[140:143]\n"
      "v_exp_f32 v166, v166\n"
      "v_mfma_f32_16x16x32_bf16 v[140:143], v[36:39], v[108:111], v[140:143]\n"
      "ds_read_b128 v[96:99], v18 offset:10240\n"
      "v_mul_f32 v160, v128, v160\n"
      "v_mfma_f32_16x16x32_bf16 v[140:143], v[40:43], v[112:115], v[140:143]\n"
      "ds_read_b128 v[80:83], v18 offset:6144\n"
      "v_fmac_f32 v134, v180, v150\n"
      "v_mfma_f32_16x16x32_bf16 v[140:143], v[44:47], v[116:119], v[140:143]\n"
      "ds_read_b128 v[84:87], v18 offset:7168\n"
      "v_mul_f32 v163, v131, v163\n"
      "v_mfma_f32_16x16x32_bf16 v[140:143], v[48:51], v[120:123], v[140:143]\n"
      "s_waitcnt lgkmcnt(5)\n"
      "ds_read_b128 v[100:103], v18 offset:11264\n"
      "v_fmac_f32 v133, v180, v149\n"
      "v_mfma_f32_16x16x32_bf16 v[140:143], v[52:55], v[124:127], v[140:143]\n"
      "v_mfma_f32_16x16x32_bf16 v[128:131], a[0:3], v[56:59], 0\n"
      "ds_read_b128 v[116:119], v18 offset:15360\n"
      "v_mul_f32 v133, v133, v161\n"
      "v_mfma_f32_16x16x32_bf16 v[128:131], a[4:7], v[60:63], v[128:131]\n"
      "ds_read_b128 v[104:107], v18 offset:12288\n"
      "v_mul_f32 v135, v135, v163\n"
      "v_mfma_f32_16x16x32_bf16 v[128:131], a[8:11], v[64:67], v[128:131]\n"
      "ds_read_b128 v[108:111], v18 offset:13312\n"
      "ds_read_b128 v[120:123], v18 offset:16384\n"
      "v_mfma_f32_16x16x32_bf16 v[128:131], a[12:15], v[68:71], v[128:131]\n"
      "ds_read_b128 v[112:115], v18 offset:14336\n"
      "v_mul_f32 v167, v139, v167\n"
      "v_mfma_f32_16x16x32_bf16 v[128:131], a[16:19], v[72:75], v[128:131]\n"
      "ds_read_b128 v[124:127], v18 offset:17408\n"
      "v_add_u32 v18, s41, v0\n"
      "v_mfma_f32_16x16x32_bf16 v[128:131], a[20:23], v[76:79], v[128:131]\n"
      "s_waitcnt lgkmcnt(6)\n"
      "v_fmac_f32 v140, v180, v156\n"
      "v_rcp_f32 v164, v164\n"
      "v_add_f32 v172, v172, v174\n"
      "v_mfma_f32_16x16x32_bf16 v[128:131], a[24:27], v[80:83], v[128:131]\n"
      "s_mov_b32 s40, s36\n"
      "v_mul_f32 v134, v134, v162\n"
      "v_add_f32 v173, v173, v175\n"
      "v_mfma_f32_16x16x32_bf16 v[128:131], a[28:31], v[84:87], v[128:131]\n"
      "v_fmac_f32 v132, v180, v148\n"
      "v_fma_f32 v173, -v172, v172, v173\n"
      "v_mfma_f32_16x16x32_bf16 v[128:131], a[32:35], v[88:91], v[128:131]\n"
      "s_add_u32 s36, s36, 24576\n"
      "v_fmac_f32 v143, v180, v159\n"
      "v_fma_f32 v166, v166, v181, v181\n"
      "v_mfma_f32_16x16x32_bf16 v[128:131], a[36:39], v[92:95], v[128:131]\n"
      "v_rcp_f32 v165, v165\n"
      "v_mfma_f32_16x16x32_bf16 v[128:131], a[40:43], v[96:99], v[128:131]\n"
      "v_mul_f32 v165, v137, v165\n"
      "v_max_f32 v181, 0x3727c5ac, v173\n"
      "v_mfma_f32_16x16x32_bf16 v[128:131], a[44:47], v[100:103], v[128:131]\n"
      "s_waitcnt vmcnt(7) lgkmcnt(0)\n"
      "v_mul_f32 v164, v136, v164\n"
      "v_cmp_ne_u32 vcc, s21, v20\n"
      "v_mfma_f32_16x16x32_bf16 v[128:131], a[48:51], v[104:107], v[128:131]\n"
      "s_add_u32 s41, s41, 20480\n"
      "v_mul_f32 v132, v132, v160\n"
      "v_fmac_f32 v142, v180, v158\n"
      "v_mfma_f32_16x16x32_bf16 v[128:131], a[52:55], v[108:111], v[128:131]\n"
      "v_rsq_f32 v174, v181\n"
      "v_mfma_f32_16x16x32_bf16 v[128:131], a[56:59], v[112:115], v[128:131]\n"
      "v_mul_f32 v140, v140, v164\n"
      "v_mul_f32 v182, 0xc0135761, v174\n"
      "v_mfma_f32_16x16x32_bf16 v[128:131], a[60:63], v[116:119], v[128:131]\n"
      "v_mul_f32 v175, 0x3d372713, v182\n"
      "v_rcp_f32 v166, v166\n"
      "v_mfma_f32_16x16x32_bf16 v[128:131], a[64:67], v[120:123], v[128:131]\n"
      "s_cbranch_vccnz .Lsp_g_6\n"
      ".Lbk_g_6:\n"
      "v_mul_f32 v166, v138, v166\n"
      "v_mfma_f32_16x16x32_bf16 v[128:131], a[68:71], v[124:127], v[128:131]\n"
      "s_cmp_eq_u32 s41, 61440\n"
      "s_cselect_b32 s41, 0, s41\n"
      "v_mul_f32 v143, v143, v167\n"
      "v_mfma_f32_16x16x32_bf16 v[136:139], a[144:147], v[56:59], 0\n"
      "s_add_u32 s43, s43, 165888\n"
      "v_cvt_pk_bf16_f32 v169, v134, v135\n"
      "v_fmac_f32 v141, v180, v157\n"
      "v_mfma_f32_16x16x32_bf16 v[136:139], a[148:151], v[60:63], v[136:139]\n"
      "s_add_u32 m0, s41, s38\n"
      "v_mul_f32 v141, v141, v165\n"
      "v_mul_f32 v142, v142, v166\n"
      "v_cvt_pk_bf16_f32 v170, v140, v141\n"
      "v_mfma_f32_16x16x32_bf16 v[136:139], a[152:155], v[64:67], v[136:139]\n"
      "buffer_load_dword v20, v2, s[8:11], s36 offen sc1\n"
      "v_mfma_f32_16x16x32_bf16 v[136:139], a[156:159], v[68:71], v[136:139]\n"
      "v_cvt_pk_bf16_f32 v168, v132, v133\n"
      "v_cvt_pk_bf16_f32 v171, v142, v143\n"
      "v_mfma_f32_16x16x32_bf16 v[136:139], a[160:163], v[72:75], v[136:139]\n"
      "buffer_store_dwordx4 v[168:171], v0, s[8:11], s37 offen sc1\n"
      "v_mfma_f32_16x16x32_bf16 v[136:139], a[164:167], v[76:79], v[136:139]\n"
      "v_sub_f32 v180, 0, v172\n"
      "v_fmac_f32 v129, v180, v145\n"
      "v_mfma_f32_16x16x32_bf16 v[136:139], a[168:171], v[80:83], v[136:139]\n"
      "v_mul_f32 v161, v129, v129\n"
      "v_fmac_f32 v131, v180, v147\n"
      "v_mfma_f32_16x16x32_bf16 v[136:139], a[172:175], v[84:87], v[136:139]\n"
      "v_mul_f32 v174, v174, v174\n"
      "v_fmac_f32 v128, v180, v144\n"
      "v_mfma_f32_16x16x32_bf16 v[136:139], a[176:179], v[88:91], v[136:139]\n"
      "buffer_load_dwordx4 v0, s[8:11], s40 offen offset:0 sc1 lds\n"
      "v_mul_f32 v183, v175, v174\n"
      "v_mfma_f32_16x16x32_bf16 v[136:139], a[180:183], v[92:95], v[136:139]\n"
      "v_mul_f32 v163, v131, v131\n"
      "v_add_u32 v12, 1, v12\n"
      "v_mfma_f32_16x16x32_bf16 v[136:139], a[184:187], v[96:99], v[136:139]\n"
      "v_mul_f32 v160, v128, v128\n"
      "v_fma_f32 v161, v161, v183, v182\n"
      "v_mfma_f32_16x16x32_bf16 v[136:139], a[188:191], v[100:103], v[136:139]\n"
      "v_fma_f32 v160, v160, v183, v182\n"
      "v_mul_f32 v161, v129, v161\n"
      "v_mfma_f32_16x16x32_bf16 v[136:139], a[192:195], v[104:107], v[136:139]\n"
      "v_exp_f32 v161, v161\n"
      "v_mfma_f32_16x16x32_bf16 v[136:139], a[196:199], v[108:111], v[136:139]\n"
      "buffer_load_dwordx4 v0, s[8:11], s40 offen offset:1024 sc1 lds\n"
      "v_mfma_f32_16x16x32_bf16 v[136:139], a[200:203], v[112:115], v[136:139]\n"
      "v_fma_f32 v161, v161, v181, v181\n"
      "v_fmac_f32 v130, v180, v146\n"
      "v_mfma_f32_16x16x32_bf16 v[136:139], a[204:207], v[116:119], v[136:139]\n"
      "s_waitcnt vmcnt(5)\n"
      "ds_write_b32 v14, v12\n"
      "v_mul_f32 v160, v128, v160\n"
      "v_mfma_f32_16x16x32_bf16 v[136:139], a[208:211], v[120:123], v[136:139]\n"
      "v_fma_f32 v163, v163, v183, v182\n"
      "v_mul_f32 v163, v131, v163\n"
      "v_mfma_f32_16x16x32_bf16 v[136:139], a[212:215], v[124:127], v[136:139]\n"
      "v_rcp_f32 v161, v161\n"
      "v_mfma_f32_16x16x32_bf16 v[132:135], a[72:75], v[56:59], 0\n"
      "buffer_load_dwordx4 v0, s[8:11], s40 offen offset:2048 sc1 lds\n"
      "v_mfma_f32_16x16x32_bf16 v[132:135], a[76:79], v[60:63], v[132:135]\n"
      "v_mul_f32 v162, v130, v130\n"
      "v_fma_f32 v162, v162, v183, v182\n"
      "v_mfma_f32_16x16x32_bf16 v[132:135], a[80:83], v[64:67], v[132:135]\n"
      "v_exp_f32 v163, v163\n"
      "v_mfma_f32_16x16x32_bf16 v[132:135], a[84:87], v[68:71], v[132:135]\n"
      "v_fma_f32 v163, v163, v181, v181\n"
      "v_mul_f32 v161, v129, v161\n"
      "v_mfma_f32_16x16x32_bf16 v[132:135], a[88:91], v[72:75], v[132:135]\n"
      "v_fmac_f32 v137, v180, v153\n"
      "v_mul_f32 v162, v130, v162\n"
      "v_mfma_f32_16x16x32_bf16 v[132:135], a[92:95], v[76:79], v[132:135]\n"
      "s_add_u32 s37, s37, 165888\n"
      "v_exp_f32 v160, v160\n"
      "v_mfma_f32_16x16x32_bf16 v[132:135], a[96:99], v[80:83], v[132:135]\n"
      "buffer_load_dwordx4 v0, s[8:11], s40 offen offset:3072 sc1 lds\n"
      "v_mfma_f32_16x16x32_bf16 v[132:135], a[100:103], v[84:87], v[132:135]\n"
      "ds_read_b32 v13, v15\n"
      "v_fmac_f32 v139, v180, v155\n"
      "v_mul_f32 v167, v139, v139\n"
      "v_mfma_f32_16x16x32_bf16 v[132:135], a[104:107], v[88:91], v[132:135]\n"
      "v_fma_f32 v167, v167, v183, v182\n"
      "v_exp_f32 v162, v162\n"
      "v_mul_f32 v167, v139, v167\n"
      "v_mfma_f32_16x16x32_bf16 v[132:135], a[108:111], v[92:95], v[132:135]\n"
      "v_fma_f32 v160, v160, v181, v181\n"
      "v_mul_f32 v165, v137, v137\n"
      "v_mfma_f32_16x16x32_bf16 v[132:135], a[112:115], v[96:99], v[132:135]\n"
      "v_fmac_f32 v136, v180, v152\n"
      "v_fma_f32 v165, v165, v183, v182\n"
      "v_mfma_f32_16x16x32_bf16 v[132:135], a[116:119], v[100:103], v[132:135]\n"
      "s_add_u32 m0, s41, s39\n"
      "v_exp_f32 v167, v167\n"
      "v_mfma_f32_16x16x32_bf16 v[132:135], a[120:123], v[104:107], v[132:135]\n"
      "buffer_load_dwordx4 v1, s[8:11], s40 offen offset:0 sc1 lds\n"
      "v_mfma_f32_16x16x32_bf16 v[132:135], a[124:127], v[108:111], v[132:135]\n"
      "v_rcp_f32 v163, v163\n"
      "v_mfma_f32_16x16x32_bf16 v[132:135], a[128:131], v[112:115], v[132:135]\n"
      "v_fmac_f32 v138, v180, v154\n"
      "v_mul_f32 v166, v138, v138\n"
      "v_mfma_f32_16x16x32_bf16 v[132:135], a[132:135], v[116:119], v[132:135]\n"
      "s_waitcnt lgkmcnt(0)\n"
      "v_rcp_f32 v160, v160\n"
      "v_cmp_gt_i32 vcc, v12, v13\n"
      "v_mfma_f32_16x16x32_bf16 v[132:135], a[136:139], v[120:123], v[132:135]\n"
      "buffer_store_dword v16, v3, s[8:11], s43 offen sc1\n"
      "v_mfma_f32_16x16x32_bf16 v[132:135], a[140:143], v[124:127], v[132:135]\n"
      "v_fma_f32 v162, v162, v181, v181\n"
      "v_mul_f32 v164, v136, v136\n"
      "v_mfma_f32_16x16x32_bf16 v[140:143], a[216:219], v[56:59], 0\n"
      "s_cbranch_vccnz .Lsp_gy_7\n"
      ".Lbk_gy_7:\n"
      "ds_read2_b64 v[172:175], v19 offset1:64\n"
      "v_mfma_f32_16x16x32_bf16 v[140:143], a[220:223], v[60:63], v[140:143]\n"
      "v_mul_f32 v165, v137, v165\n"
      "v_fma_f32 v164, v164, v183, v182\n"
      "v_mfma_f32_16x16x32_bf16 v[140:143], a[224:227], v[64:67], v[140:143]\n"
      "ds_read_b128 v[64:67], v18 offset:2048\n"
      "v_add_u32 v19, s41, v4\n"
      "v_mfma_f32_16x16x32_bf16 v[140:143], a[228:231], v[68:71], v[140:143]\n"
      "ds_read_b128 v[68:71], v18 offset:3072\n"
      "v_rcp_f32 v162, v162\n"
      "v_mfma_f32_16x16x32_bf16 v[140:143], a[232:235], v[72:75], v[140:143]\n"
      "ds_read_b128 v[72:75], v18 offset:4096\n"
      "v_fma_f32 v166, v166, v183, v182\n"
      "v_mfma_f32_16x16x32_bf16 v[140:143], a[236:239], v[76:79], v[140:143]\n"
      "ds_read_b128 v[56:59], v18 offset:0\n"
      "v_mul_f32 v164, v136, v164\n"
      "v_mfma_f32_16x16x32_bf16 v[140:143], a[240:243], v[80:83], v[140:143]\n"
      "ds_read_b128 v[60:63], v18 offset:1024\n"
      "v_exp_f32 v164, v164\n"
      "v_mfma_f32_16x16x32_bf16 v[140:143], a[244:247], v[84:87], v[140:143]\n"
      "v_exp_f32 v165, v165\n"
      "v_mfma_f32_16x16x32_bf16 v[140:143], a[248:251], v[88:91], v[140:143]\n"
      "ds_read_b128 v[76:79], v18 offset:5120\n"
      "v_fma_f32 v165, v165, v181, v181\n"
      "v_fma_f32 v167, v167, v181, v181\n"
      "v_mfma_f32_16x16x32_bf16 v[140:143], a[252:255], v[92:95], v[140:143]\n"
      "v_mul_f32 v162, v130, v162\n"
      "v_fmac_f32 v135, v180, v151\n"
      "v_fma_f32 v164, v164, v181, v181\n"
      "v_mfma_f32_16x16x32_bf16 v[140:143], v[24:27], v[96:99], v[140:143]\n"
      "ds_read_b128 v[92:95], v18 offset:9216\n"
      "v_mul_f32 v166, v138, v166\n"
      "v_mfma_f32_16x16x32_bf16 v[140:143], v[28:31], v[100:103], v[140:143]\n"
      "ds_read_b128 v[88:91], v18 offset:8192\n"
      "v_rcp_f32 v167, v167\n"
      "v_mfma_f32_16x16x32_bf16 v[140:143], v[32:35], v[104:107], v[140:143]\n"
      "v_exp_f32 v166, v166\n"
      "v_mfma_f32_16x16x32_bf16 v[140:143], v[36:39], v[108:111], v[140:143]\n"
      "ds_read_b128 v[96:99], v18 offset:10240\n"
      "v_mul_f32 v160, v128, v160\n"
      "v_mfma_f32_16x16x32_bf16 v[140:143], v[40:43], v[112:115], v[140:143]\n"
      "ds_read_b128 v[80:83], v18 offset:6144\n"
      "v_fmac_f32 v134, v180, v150\n"
      "v_mfma_f32_16x16x32_bf16 v[140:143], v[44:47], v[116:119], v[140:143]\n"
      "ds_read_b128 v[84:87], v18 offset:7168\n"
      "v_mul_f32 v163, v131, v163\n"
      "v_mfma_f32_16x16x32_bf16 v[140:143], v[48:51], v[120:123], v[140:143]\n"
      "s_waitcnt lgkmcnt(5)\n"
      "ds_read_b128 v[100:103], v18 offset:11264\n"
      "v_fmac_f32 v133, v180, v149\n"
      "v_mfma_f32_16x16x32_bf16 v[140:143], v[52:55], v[124:127], v[140:143]\n"
      "s_sub_u32 s20, s20, 1\n"
      "s_cbranch_scc0 .Lg_loop\n"
      "v_mul_f32 v133, v133, v161\n"
      "v_mul_f32 v135, v135, v163\n"
      "v_mul_f32 v167, v139, v167\n"
      "s_nop 5\n"
      "v_fmac_f32 v140, v180, v156\n"
      "v_rcp_f32 v164, v164\n"
      "v_mul_f32 v134, v134, v162\n"
      "v_fmac_f32 v132, v180, v148\n"
      "v_fmac_f32 v143, v180, v159\n"
      "v_fma_f32 v166, v166, v181, v181\n"
      "v_rcp_f32 v165, v165\n"
      "s_nop 0\n"
      "v_mul_f32 v165, v137, v165\n"
      "v_mul_f32 v164, v136, v164\n"
      "v_mul_f32 v132, v132, v160\n"
      "v_fmac_f32 v142, v180, v158\n"
      "v_mul_f32 v140, v140, v164\n"
      "v_rcp_f32 v166, v166\n"
      "s_nop 0\n"
      "v_mul_f32 v166, v138, v166\n"
      "v_mul_f32 v143, v143, v167\n"
      "s_add_u32 s43, s43, 165888\n"
      "v_cvt_pk_bf16_f32 v169, v134, v135\n"
      "v_fmac_f32 v141, v180, v157\n"
      "v_mul_f32 v141, v141, v165\n"
      "v_mul_f32 v142, v142, v166\n"
      "v_cvt_pk_bf16_f32 v170, v140, v141\n"
      "v_cvt_pk_bf16_f32 v168, v132, v133\n"
      "v_cvt_pk_bf16_f32 v171, v142, v143\n"
      "buffer_store_dwordx4 v[168:171], v0, s[8:11], s37 offen sc1\n"
      "s_add_u32 s37, s37, 165888\n"
      "s_waitcnt vmcnt(7)\n"
      "buffer_store_dword v16, v3, s[8:11], s43 offen sc1\n"
      "s_add_u32 s43, s43, 165888\n"
      "s_waitcnt vmcnt(1)\n"
      "buffer_store_dword v16, v3, s[8:11], s43 offen sc1\n"
      "s_add_u32 s43, s43, 165888\n"
      "s_endpgm\n"
      ".Lsp_g_1:\n"
      "s_mov_b32 s22, s23\n"
      ".Lsl_g_1:\n"
      "buffer_load_dword v20, v2, s[8:11], s40 offen sc1\n"
      "s_waitcnt vmcnt(0)\n"
      "v_cmp_ne_u32 vcc, s21, v20\n"
      "s_cbranch_vccz .Lbk_g_1\n"
      "s_sleep 4\n"
      "s_sub_u32 s22, s22, 1\n"
      "s_cbranch_scc0 .Lsl_g_1\n"
      "s_mov_b32 s23, 0\n"
      "s_branch .Lbk_g_1\n"
      ".Lsp_g_2:\n"
      "s_mov_b32 s22, s23\n"
      ".Lsl_g_2:\n"
      "buffer_load_dword v20, v2, s[8:11], s40 offen sc1\n"
      "s_waitcnt vmcnt(0)\n"
      "v_cmp_ne_u32 vcc, s21, v20\n"
      "s_cbranch_vccz .Lbk_g_2\n"
      "s_sleep 4\n"
      "s_sub_u32 s22, s22, 1\n"
      "s_cbranch_scc0 .Lsl_g_2\n"
      "s_mov_b32 s23, 0\n"
      "s_branch .Lbk_g_2\n"
      ".Lsp_gy_3:\n"
      "s_lshl_b32 s22, s23, 4\n"
      ".Lsl_gy_3:\n"
      "ds_read_b32 v13, v15\n"
      "s_waitcnt lgkmcnt(0)\n"
      "v_cmp_gt_i32 vcc, v12, v13\n"
      "s_cbranch_vccz .Lbk_gy_3\n"
      "s_sub_u32 s22, s22, 1\n"
      "s_cbranch_scc0 .Lsl_gy_3\n"
      "s_mov_b32 s23, 0\n"
      "s_branch .Lbk_gy_3\n"
      ".Lsp_g_4:\n"
      "s_mov_b32 s22, s23\n"
      "buffer_load_dword v20, v2, s[8:11], s40 offen sc1\n"
      "s_waitcnt vmcnt(0)\n"
      "v_cmp_ne_u32 vcc, s21, v20\n"
      "s_add_u32 s30, s43, 165888\n"
      "buffer_store_dword v16, v3, s[8:11], s30 offen sc1\n"
      "s_cbranch_vccz .Lbk_g_4\n"
      ".Lsl_g_4:\n"
      "buffer_load_dword v20, v2, s[8:11], s40 offen sc1\n"
      "s_waitcnt vmcnt(0)\n"
      "v_cmp_ne_u32 vcc, s21, v20\n"
      "s_cbranch_vccz .Lbk_g_4\n"
      "s_sleep 4\n"
      "s_sub_u32 s22, s22, 1\n"
      "s_cbranch_scc0 .Lsl_g_4\n"
      "s_mov_b32 s23, 0\n"
      "s_branch .Lbk_g_4\n"
      ".Lsp_gy_5:\n"
      "s_lshl_b32 s22, s23, 4\n"
      ".Lsl_gy_5:\n"
      "ds_read_b32 v13, v15\n"
      "s_waitcnt lgkmcnt(0)\n"
      "v_cmp_gt_i32 vcc, v12, v13\n"
      "s_cbranch_vccz .Lbk_gy_5\n"
      "s_sub_u32 s22, s22, 1\n"
      "s_cbranch_scc0 .Lsl_gy_5\n"
      "s_mov_b32 s23, 0\n"
      "s_branch .Lbk_gy_5\n"
      ".Lsp_g_6:\n"
      "s_mov_b32 s22, s23\n"
      "buffer_load_dword v20, v2, s[8:11], s40 offen sc1\n"
      "s_waitcnt vmcnt(0)\n"
      "v_cmp_ne_u32 vcc, s21, v20\n"
      "s_add_u32 s30, s43, 165888\n"
      "buffer_store_dword v16, v3, s[8:11], s30 offen sc1\n"
      "s_cbranch_vccz .Lbk_g_6\n"
      ".Lsl_g_6:\n"
      "buffer_load_dword v20, v2, s[8:11], s40 offen sc1\n"
      "s_waitcnt vmcnt(0)\n"
      "v_cmp_ne_u32 vcc, s21, v20\n"
      "s_cbranch_vccz .Lbk_g_6\n"
      "s_sleep 4\n"
      "s_sub_u32 s22, s22, 1\n"
      "s_cbranch_scc0 .Lsl_g_6\n"
      "s_mov_b32 s23, 0\n"
      "s_branch .Lbk_g_6\n"
      ".Lsp_gy_7:\n"
      "s_lshl_b32 s22, s23, 4\n"
      ".Lsl_gy_7:\n"
      "ds_read_b32 v13, v15\n"
      "s_waitcnt lgkmcnt(0)\n"
      "v_cmp_gt_i32 vcc, v12, v13\n"
      "s_cbranch_vccz .Lbk_gy_7\n"
      "s_sub_u32 s22, s22, 1\n"
      "s_cbranch_scc0 .Lsl_gy_7\n"
      "s_mov_b32 s23, 0\n"
      "s_branch .Lbk_gy_7\n"
      ".Lg_last:\n"
      :
      : "{s[8:11]}"(rsWS), "{s[12:15]}"(rsA), "{s[16:19]}"(rsB), "{s[92:95]}"(rsC), "{v0}"(v_l16), "{v1}"(v_l5), "{v2}"(v_fa), "{v3}"(v_ra), "{v4}"(v_st), "{v5}"(v_at), "{v6}"(v_vo0), "{v7}"(v_vo1), "{v8}"(v_vo2), "{v9}"(v_vo3), "{v10}"(v_tc), "{v11}"(v_pw), "{v13}"(v_al), "{v14}"(v_yw), "{v15}"(v_yr), "{v56}"(v_tq[0]), "{v57}"(v_tq[1]), "{v58}"(v_tw), "{v59}"(v_td), "{s36}"(s_sx), "{s37}"(s_sh), "{s38}"(s_m4), "{s39}"(s_m5)
      : "v255", "a255", "s99", "vcc", "scc", "memory");

  }
}

extern "C" void kernel_launch(void* const* d_in, const int* in_sizes, int n_in, void* d_out, int out_size,
                              void* d_ws, size_t ws_size, hipStream_t stream) {
  char* ws = (char*)d_ws + ((256 - ((size_t)d_ws & 255)) & 255);
  (void)hipFuncSetAttribute((const void*)k_all, hipFuncAttributeMaxDynamicSharedMemorySize, LDS_BYTES);
  k_all<<<256, 256, LDS_BYTES, stream>>>((const u16*)d_in[0], (const u16*)d_in[1], (const u16*)d_in[2], (const u16*)d_in[4],
                                         (const u16*)d_in[5], (const u16*)d_in[6], ws, (u16*)d_out);
}
```

```cpp
#include <hip/hip_runtime.h>
#include <stdint.h>

typedef unsigned short u16;
typedef __attribute__((ext_vector_type(4))) unsigned u32x4;
constexpr unsigned WS_DBG = 65536u;
constexpr unsigned WS_XIN = 1310720u;
constexpr unsigned WS_OB = 9175040u;
constexpr unsigned WS_X1 = 17039360u;
constexpr unsigned WS_H = 26034176u;
constexpr unsigned WS_PS = 78589952u;
constexpr unsigned WS_END = 109740032u;
constexpr unsigned PSK = 12u;
constexpr unsigned PCN = 18u;
constexpr unsigned ROT = 0u;
constexpr unsigned QMAP = 3u;
constexpr unsigned UMAP = 0u;
constexpr unsigned DSH = 4u;
constexpr unsigned PW = 1664u;
constexpr unsigned REC = 24576u;
constexpr unsigned HREC = 165888u;
constexpr unsigned PREC = 99840u;
constexpr unsigned NREC = 80u;
constexpr unsigned OOB = 0x40000000u, NEG = 0xff61b1e6u;
constexpr int LDS_BYTES = 160 * 1024;

__device__ __forceinline__ u32x4 srd(const void* p, unsigned bytes) {
  const unsigned long long a = (unsigned long long)p;
  return u32x4{(unsigned)a, (unsigned)(a >> 32) & 0xffffu, bytes, 0x00020000u};
}

__device__ __forceinline__ unsigned unit_ks(unsigned t) { return UMAP == 0 ? t >> 2 : UMAP == 1 ? t % 12 >> 1 : t % 6; }
__device__ __forceinline__ unsigned unit_gr(unsigned t) { return UMAP == 0 ? t & 3 : UMAP == 1 ? t / 12 * 2 + (t & 1) : t / 6; }

extern "C" __global__ __launch_bounds__(256, 1) __attribute__((amdgpu_waves_per_eu(1, 1))) void k_all(
    const u16* X, const u16* Wqkv, const u16* Wo, const u16* Wg, const u16* Wu, const u16* Wd, char* ws, u16* out) {
  const unsigned b = blockIdx.x, xcd = b & 7, slot = b >> 3;
  const unsigned w = __builtin_amdgcn_readfirstlane(threadIdx.x >> 6), lane = threadIdx.x & 63;
  const unsigned i16 = lane & 15, g = lane >> 4, q = i16 >> 2, p = lane & 3;
  const u32x4 rsWS = srd(ws, WS_END);
  const unsigned v_l16 = lane * 16, v_dbg = WS_DBG + (b * 4 + w) * 1024 + lane * 4;
  const unsigned yb = slot >= 14 || slot == 4 ? 81920 : slot < 4 ? 61440 : 149760, v_yw = yb + w * 4, v_yr = yb + p * 4;
  const unsigned half = (i16 + 16 * (g >> 1)) * 16 + (g & 1) * 8;
  const unsigned line = lane < 32 ? lane * 4 : OOB, v_al = w ? OOB : line + 128 * (1 + b);
  const unsigned up = lane >> 5, l5 = (lane & 31) * 4, v_hi = 0u - up;
  const unsigned l8 = lane >> 3, l7 = (lane & 7) * 16;
  (void)v_dbg;
  if (slot < 4) {
    const unsigned gr = QMAP == 2 ? 3 - (xcd & 3) : slot, hg = QMAP == 2 ? (xcd >> 2) * 4 + slot : xcd, h = 4 * hg + w, par = i16 & 1;
    const u32x4 rsA = srd(Wqkv, 576 * 1728 * 2);
    const unsigned v_l5 = w < 3 || lane < 32 ? v_l16 + 16384 - w * 3072 : OOB;
    const unsigned v_fa = 20480 - w * 4096 + (lane < 48 ? lane >> 2 : 11) * 128 + p * 4, v_ra = up * 8 * REC + l5 + 20480 + h * 128;
    const unsigned v_st = 18432 + i16 * 32 + q * 8, v_tw = 61568 + w * 128 + i16 * 8, v_tr = 61568 + w * 128 + g * 32;
    const unsigned v_om = h * 512 + half, v_oe = g ? OOB : 16384 + h * 128 + i16 * 8;
    const unsigned v_pw = 20480 + h * 128 + l8 * REC + l7;
    unsigned v_vo[4], v_bc[4], v_bp[4], v_mk[4], v_sel[4] = {0, 0, 0, 0}, v_ci[4];
#pragma unroll
    for (unsigned i = 0; i < 4; ++i) {
      const unsigned kk = 4 * g + i, ka = 4 * g + 2 * (i & 1) + par;
      v_bc[i] = (i & 1) == par && kk <= i16 ? 0u : NEG;
      v_bp[i] = (i & 1) == par && kk >= i16 + 2 ? 0u : NEG;
      v_mk[i] = (i < 2 ? ka > i16 : ka <= i16) ? 0xffffu << (16 * par) : 0u;
      v_ci[i] = p == 2 ? 0x3f800000u : 0u;
    }
    v_sel[3] = lane == 16 ? 0x3f80u : lane == 17 ? 0x3f800000u : 0u;
#pragma unroll
    for (unsigned i = 0; i < 4; ++i) {
      const unsigned c = 64 * i + lane, cc = c % 30;
      v_vo[i] = c < 240 && cc < 27 ? (8 * w + c / 30) * 3456 + cc / 9 * 1152 + 144 * hg + cc % 9 * 16 - 1024 * i : OOB;
    }
    const unsigned rb = 65536 + g * 4224 + q * 480;
    const unsigned v_at = rb + 36 * w + 4 * (w & 1) + 8 * p, v_ax = rb + (p < 2 ? p : 2) * 144 + 72 * (w >> 1) + 32;
    const unsigned v_pm = (16 * g + 4 * (i16 >> 1 & 3) + 2 * (w & 1) + par) * 4, v_ms = i16 < 6 ? ~0u : 0u;
    const unsigned s_sx = WS_XIN + gr * NREC * REC + w * 4096, s_ev = s_sx + 32 * REC, s_del = 0u - (gr ? NREC + 32 : 32) * REC;
    const unsigned s_kil = gr ? 0x7f7fffffu : 0xff7fffffu, s_so = WS_OB + gr * NREC * REC;
    const unsigned s_m4 = w * 4096, s_m5 = 16384 + w * 1024, s_ms = w * 4224, s_up = b + w;
    asm volatile(
      "s_buffer_load_dword s21, s[8:11], 0\n"
      "s_mov_b32 s23, 65536\n"
      "s_mov_b32 s31, 0x40000000\n"
      "s_mov_b32 s51, 0\n"
      "s_add_u32 m0, s43, 65536\n"
      "s_nop 0\n"
      "buffer_load_dwordx4 v48, s[12:15], s51 offen offset:0 lds\n"
      "buffer_load_dwordx4 v49, s[12:15], s51 offen offset:1024 lds\n"
      "buffer_load_dwordx4 v50, s[12:15], s51 offen offset:2048 lds\n"
      "buffer_load_dwordx4 v51, s[12:15], s51 offen offset:3072 lds\n"
      "s_add_u32 s51, s51, 110592\n"
      "s_add_u32 m0, s43, 82432\n"
      "s_nop 0\n"
      "buffer_load_dwordx4 v48, s[12:15], s51 offen offset:0 lds\n"
      "buffer_load_dwordx4 v49, s[12:15], s51 offen offset:1024 lds\n"
      "buffer_load_dwordx4 v50, s[12:15], s51 offen offset:2048 lds\n"
      "buffer_load_dwordx4 v51, s[12:15], s51 offen offset:3072 lds\n"
      "s_add_u32 s51, s51, 110592\n"
      "s_add_u32 m0, s43, 99328\n"
      "s_nop 0\n"
      "buffer_load_dwordx4 v48, s[12:15], s51 offen offset:0 lds\n"
      "buffer_load_dwordx4 v49, s[12:15], s51 offen offset:1024 lds\n"
      "buffer_load_dwordx4 v50, s[12:15], s51 offen offset:2048 lds\n"
      "buffer_load_dwordx4 v51, s[12:15], s51 offen offset:3072 lds\n"
      "s_add_u32 s51, s51, 110592\n"
      "s_add_u32 m0, s43, 116224\n"
      "s_nop 0\n"
      "buffer_load_dwordx4 v48, s[12:15], s51 offen offset:0 lds\n"
      "buffer_load_dwordx4 v49, s[12:15], s51 offen offset:1024 lds\n"
      "buffer_load_dwordx4 v50, s[12:15], s51 offen offset:2048 lds\n"
      "buffer_load_dwordx4 v51, s[12:15], s51 offen offset:3072 lds\n"
      "s_add_u32 s51, s51, 110592\n"
      "s_add_u32 s6, s36, 1572864\n"
      "s_add_u32 s36, s36, 4294942720\n"
      "s_add_u32 s48, s37, 811008\n"
      "s_mov_b32 s53, 0x7f7fffff\n"
      "v_mov_b32 v179, 0\n"
      "s_mov_b32 s54, 0xffff\n"
      "s_mov_b32 s55, 0\n"
      "v_mov_b32 v9, 0\n"
      "ds_write_b32 v11, v9\n"
      "v_mov_b32 v56, 0x3f803f80\n"
      "v_mov_b32 v57, 0x3f803f80\n"
      "v_mov_b32 v58, 0x3f803f80\n"
      "v_mov_b32 v59, 0x3f803f80\n"
      "s_waitcnt vmcnt(12) lgkmcnt(0)\n"
      "s_barrier\n"
      "ds_read_b64_tr_b16 v[60:61], v53 offset:0\n"
      "ds_read_b64_tr_b16 v[62:63], v53 offset:1920\n"
      "ds_read_b64_tr_b16 a[0:1], v52 offset:144\n"
      "ds_read_b64_tr_b16 a[2:3], v52 offset:2064\n"
      "ds_read_b64_tr_b16 a[72:73], v52 offset:288\n"
      "ds_read_b64_tr_b16 a[74:75], v52 offset:2208\n"
      "ds_read_b64_tr_b16 a[144:145], v52 offset:0\n"
      "ds_read_b64_tr_b16 a[146:147], v52 offset:1920\n"
      "v_add_u32 v52, 16896, v52\n"
      "v_add_u32 v53, 16896, v53\n"
      "s_add_u32 m0, s43, 133120\n"
      "s_nop 0\n"
      "buffer_load_dwordx4 v48, s[12:15], s51 offen offset:0 lds\n"
      "buffer_load_dwordx4 v49, s[12:15], s51 offen offset:1024 lds\n"
      "buffer_load_dwordx4 v50, s[12:15], s51 offen offset:2048 lds\n"
      "buffer_load_dwordx4 v51, s[12:15], s51 offen offset:3072 lds\n"
      "s_add_u32 s51, s51, 110592\n"
      "s_waitcnt vmcnt(12) lgkmcnt(0)\n"
      "s_barrier\n"
      "ds_read_b64_tr_b16 v[68:69], v53 offset:0\n"
      "ds_read_b64_tr_b16 v[70:71], v53 offset:1920\n"
      "ds_read_b64_tr_b16 a[4:5], v52 offset:144\n"
      "ds_read_b64_tr_b16 a[6:7], v52 offset:2064\n"
      "ds_read_b64_tr_b16 a[76:77], v52 offset:288\n"
      "ds_read_b64_tr_b16 a[78:79], v52 offset:2208\n"
      "ds_read_b64_tr_b16 a[148:149], v52 offset:0\n"
      "ds_read_b64_tr_b16 a[150:151], v52 offset:1920\n"
      "v_add_u32 v52, 16896, v52\n"
      "v_add_u32 v53, 16896, v53\n"
      "ds_bpermute_b32 v64, v54, v60\n"
      "ds_bpermute_b32 v65, v54, v61\n"
      "ds_bpermute_b32 v66, v54, v62\n"
      "ds_bpermute_b32 v67, v54, v63\n"
      "v_mfma_f32_16x16x32_bf16 v[136:139], a[0:3], v[56:59], 0\n"
      "v_mfma_f32_16x16x32_bf16 v[144:147], v[56:59], a[72:75], 0\n"
      "v_mfma_f32_16x16x32_bf16 v[148:151], a[144:147], v[56:59], 0\n"
      "s_add_u32 m0, s43, 65536\n"
      "s_nop 0\n"
      "buffer_load_dwordx4 v48, s[12:15], s51 offen offset:0 lds\n"
      "buffer_load_dwordx4 v49, s[12:15], s51 offen offset:1024 lds\n"
      "buffer_load_dwordx4 v50, s[12:15], s51 offen offset:2048 lds\n"
      "buffer_load_dwordx4 v51, s[12:15], s51 offen offset:3072 lds\n"
      "s_add_u32 s51, s51, 110592\n"
      "s_waitcnt vmcnt(12) lgkmcnt(0)\n"
      "s_barrier\n"
      "ds_read_b64_tr_b16 v[60:61], v53 offset:0\n"
      "ds_read_b64_tr_b16 v[62:63], v53 offset:1920\n"
      "ds_read_b64_tr_b16 a[8:9], v52 offset:144\n"
      "ds_read_b64_tr_b16 a[10:11], v52 offset:2064\n"
      "ds_read_b64_tr_b16 a[80:81], v52 offset:288\n"
      "ds_read_b64_tr_b16 a[82:83], v52 offset:2208\n"
      "ds_read_b64_tr_b16 a[152:153], v52 offset:0\n"
      "ds_read_b64_tr_b16 a[154:155], v52 offset:1920\n"
      "v_add_u32 v52, 16896, v52\n"
      "v_add_u32 v53, 16896, v53\n"
      "ds_bpermute_b32 v72, v54, v68\n"
      "ds_bpermute_b32 v73, v54, v69\n"
      "ds_bpermute_b32 v74, v54, v70\n"
      "ds_bpermute_b32 v75, v54, v71\n"
      "v_mfma_f32_16x16x32_bf16 v[136:139], a[4:7], v[56:59], v[136:139]\n"
      "v_mfma_f32_16x16x32_bf16 v[144:147], v[56:59], a[76:79], v[144:147]\n"
      "v_mfma_f32_16x16x32_bf16 v[148:151], a[148:151], v[56:59], v[148:151]\n"
      "s_add_u32 m0, s43, 82432\n"
      "s_nop 0\n"
      "buffer_load_dwordx4 v48, s[12:15], s51 offen offset:0 lds\n"
      "buffer_load_dwordx4 v49, s[12:15], s51 offen offset:1024 lds\n"
      "buffer_load_dwordx4 v50, s[12:15], s51 offen offset:2048 lds\n"
      "buffer_load_dwordx4 v51, s[12:15], s51 offen offset:3072 lds\n"
      "s_add_u32 s51, s51, 110592\n"
      "v_and_b32 v64, v55, v64\n"
      "v_and_b32 v65, v55, v65\n"
      "v_and_b32 v66, v55, v66\n"
      "v_and_b32 v67, v55, v67\n"
      "v_accvgpr_write_b32 a216, v64\n"
      "v_accvgpr_write_b32 a217, v65\n"
      "v_accvgpr_write_b32 a218, v66\n"
      "v_accvgpr_write_b32 a219, v67\n"
      "v_mfma_f32_16x16x32_bf16 v[140:143], v[64:67], v[56:59], 0\n"
      "s_waitcnt vmcnt(12) lgkmcnt(0)\n"
      "s_barrier\n"
      "ds_read_b64_tr_b16 v[68:69], v53 offset:0\n"
      "ds_read_b64_tr_b16 v[70:71], v53 offset:1920\n"
      "ds_read_b64_tr_b16 a[12:13], v52 offset:144\n"
      "ds_read_b64_tr_b16 a[14:15], v52 offset:2064\n"
      "ds_read_b64_tr_b16 a[84:85], v52 offset:288\n"
      "ds_read_b64_tr_b16 a[86:87], v52 offset:2208\n"
      "ds_read_b64_tr_b16 a[156:157], v52 offset:0\n"
      "ds_read_b64_tr_b16 a[158:159], v52 offset:1920\n"
      "v_add_u32 v52, 16896, v52\n"
      "v_add_u32 v53, 16896, v53\n"
      "ds_bpermute_b32 v64, v54, v60\n"
      "ds_bpermute_b32 v65, v54, v61\n"
      "ds_bpermute_b32 v66, v54, v62\n"
      "ds_bpermute_b32 v67, v54, v63\n"
      "v_mfma_f32_16x16x32_bf16 v[136:139], a[8:11], v[56:59], v[136:139]\n"
      "v_mfma_f32_16x16x32_bf16 v[144:147], v[56:59], a[80:83], v[144:147]\n"
      "v_mfma_f32_16x16x32_bf16 v[148:151], a[152:155], v[56:59], v[148:151]\n"
      "s_add_u32 m0, s43, 99328\n"
      "s_nop 0\n"
      "buffer_load_dwordx4 v48, s[12:15], s51 offen offset:0 lds\n"
      "buffer_load_dwordx4 v49, s[12:15], s51 offen offset:1024 lds\n"
      "buffer_load_dwordx4 v50, s[12:15], s51 offen offset:2048 lds\n"
      "buffer_load_dwordx4 v51, s[12:15], s51 offen offset:3072 lds\n"
      "s_add_u32 s51, s51, 110592\n"
      "v_and_b32 v72, v55, v72\n"
      "v_and_b32 v73, v55, v73\n"
      "v_and_b32 v74, v55, v74\n"
      "v_and_b32 v75, v55, v75\n"
      "v_accvgpr_write_b32 a220, v72\n"
      "v_accvgpr_write_b32 a221, v73\n"
      "v_accvgpr_write_b32 a222, v74\n"
      "v_accvgpr_write_b32 a223, v75\n"
      "v_mfma_f32_16x16x32_bf16 v[140:143], v[72:75], v[56:59], v[140:143]\n"
      "s_waitcnt vmcnt(12) lgkmcnt(0)\n"
      "s_barrier\n"
      "ds_read_b64_tr_b16 v[60:61], v53 offset:0\n"
      "ds_read_b64_tr_b16 v[62:63], v53 offset:1920\n"
      "ds_read_b64_tr_b16 a[16:17], v52 offset:144\n"
      "ds_read_b64_tr_b16 a[18:19], v52 offset:2064\n"
      "ds_read_b64_tr_b16 a[88:89], v52 offset:288\n"
      "ds_read_b64_tr_b16 a[90:91], v52 offset:2208\n"
      "ds_read_b64_tr_b16 a[160:161], v52 offset:0\n"
      "ds_read_b64_tr_b16 a[162:163], v52 offset:1920\n"
      "v_add_u32 v52, 4294899712, v52\n"
      "v_add_u32 v53, 4294899712, v53\n"
      "ds_bpermute_b32 v72, v54, v68\n"
      "ds_bpermute_b32 v73, v54, v69\n"
      "ds_bpermute_b32 v74, v54, v70\n"
      "ds_bpermute_b32 v75, v54, v71\n"
      "v_mfma_f32_16x16x32_bf16 v[136:139], a[12:15], v[56:59], v[136:139]\n"
      "v_mfma_f32_16x16x32_bf16 v[144:147], v[56:59], a[84:87], v[144:147]\n"
      "v_mfma_f32_16x16x32_bf16 v[148:151], a[156:159], v[56:59], v[148:151]\n"
      "s_add_u32 m0, s43, 116224\n"
      "s_nop 0\n"
      "buffer_load_dwordx4 v48, s[12:15], s51 offen offset:0 lds\n"
      "buffer_load_dwordx4 v49, s[12:15], s51 offen offset:1024 lds\n"
      "buffer_load_dwordx4 v50, s[12:15], s51 offen offset:2048 lds\n"
      "buffer_load_dwordx4 v51, s[12:15], s51 offen offset:3072 lds\n"
      "s_add_u32 s51, s51, 110592\n"
      "v_and_b32 v64, v55, v64\n"
      "v_and_b32 v65, v55, v65\n"
      "v_and_b32 v66, v55, v66\n"
      "v_and_b32 v67, v55, v67\n"
      "v_accvgpr_write_b32 a224, v64\n"
      "v_accvgpr_write_b32 a225, v65\n"
      "v_accvgpr_write_b32 a226, v66\n"
      "v_accvgpr_write_b32 a227, v67\n"
      "v_mfma_f32_16x16x32_bf16 v[140:143], v[64:67], v[56:59], v[140:143]\n"
      "s_waitcnt vmcnt(12) lgkmcnt(0)\n"
      "s_barrier\n"
      "ds_read_b64_tr_b16 v[68:69], v53 offset:0\n"
      "ds_read_b64_tr_b16 v[70:71], v53 offset:1920\n"
      "ds_read_b64_tr_b16 a[20:21], v52 offset:144\n"
      "ds_read_b64_tr_b16 a[22:23], v52 offset:2064\n"
      "ds_read_b64_tr_b16 a[92:93], v52 offset:288\n"
      "ds_read_b64_tr_b16 a[94:95], v52 offset:2208\n"
      "ds_read_b64_tr_b16 a[164:165], v52 offset:0\n"
      "ds_read_b64_tr_b16 a[166:167], v52 offset:1920\n"
      "v_add_u32 v52, 16896, v52\n"
      "v_add_u32 v53, 16896, v53\n"
      "ds_bpermute_b32 v64, v54, v60\n"
      "ds_bpermute_b32 v65, v54, v61\n"
      "ds_bpermute_b32 v66, v54, v62\n"
      "ds_bpermute_b32 v67, v54, v63\n"
      "v_mfma_f32_16x16x32_bf16 v[136:139], a[16:19], v[56:59], v[136:139]\n"
      "v_mfma_f32_16x16x32_bf16 v[144:147], v[56:59], a[88:91], v[144:147]\n"
      "v_mfma_f32_16x16x32_bf16 v[148:151], a[160:163], v[56:59], v[148:151]\n"
      "s_add_u32 m0, s43, 133120\n"
      "s_nop 0\n"
      "buffer_load_dwordx4 v48, s[12:15], s51 offen offset:0 lds\n"
      "buffer_load_dwordx4 v49, s[12:15], s51 offen offset:1024 lds\n"
      "buffer_load_dwordx4 v50, s[12:15], s51 offen offset:2048 lds\n"
      "buffer_load_dwordx4 v51, s[12:15], s51 offen offset:3072 lds\n"
      "s_add_u32 s51, s51, 110592\n"
      "v_and_b32 v72, v55, v72\n"
      "v_and_b32 v73, v55, v73\n"
      "v_and_b32 v74, v55, v74\n"
      "v_and_b32 v75, v55, v75\n"
      "v_accvgpr_write_b32 a228, v72\n"
      "v_accvgpr_write_b32 a229, v73\n"
      "v_accvgpr_write_b32 a230, v74\n"
      "v_accvgpr_write_b32 a231, v75\n"
      "v_mfma_f32_16x16x32_bf16 v[140:143], v[72:75], v[56:59], v[140:143]\n"
      "s_waitcnt vmcnt(12) lgkmcnt(0)\n"
      "s_barrier\n"
      "ds_read_b64_tr_b16 v[60:61], v53 offset:0\n"
      "ds_read_b64_tr_b16 v[62:63], v53 offset:1920\n"
      "ds_read_b64_tr_b16 a[24:25], v52 offset:144\n"
      "ds_read_b64_tr_b16 a[26:27], v52 offset:2064\n"
      "ds_read_b64_tr_b16 a[96:97], v52 offset:288\n"
      "ds_read_b64_tr_b16 a[98:99], v52 offset:2208\n"
      "ds_read_b64_tr_b16 a[168:169], v52 offset:0\n"
      "ds_read_b64_tr_b16 a[170:171], v52 offset:1920\n"
      "v_add_u32 v52, 16896, v52\n"
      "v_add_u32 v53, 16896, v53\n"
      "ds_bpermute_b32 v72, v54, v68\n"
      "ds_bpermute_b32 v73, v54, v69\n"
      "ds_bpermute_b32 v74, v54, v70\n"
      "ds_bpermute_b32 v75, v54, v71\n"
      "v_mfma_f32_16x16x32_bf16 v[136:139], a[20:23], v[56:59], v[136:139]\n"
      "v_mfma_f32_16x16x32_bf16 v[144:147], v[56:59], a[92:95], v[144:147]\n"
      "v_mfma_f32_16x16x32_bf16 v[148:151], a[164:167], v[56:59], v[148:151]\n"
      "s_add_u32 m0, s43, 65536\n"
      "s_nop 0\n"
      "buffer_load_dwordx4 v48, s[12:15], s51 offen offset:0 lds\n"
      "buffer_load_dwordx4 v49, s[12:15], s51 offen offset:1024 lds\n"
      "buffer_load_dwordx4 v50, s[12:15], s51 offen offset:2048 lds\n"
      "buffer_load_dwordx4 v51, s[12:15], s51 offen offset:3072 lds\n"
      "s_add_u32 s51, s51, 110592\n"
      "v_and_b32 v64, v55, v64\n"
      "v_and_b32 v65, v55, v65\n"
      "v_and_b32 v66, v55, v66\n"
      "v_and_b32 v67, v55, v67\n"
      "v_accvgpr_write_b32 a232, v64\n"
      "v_accvgpr_write_b32 a233, v65\n"
      "v_accvgpr_write_b32 a234, v66\n"
      "v_accvgpr_write_b32 a235, v67\n"
      "v_mfma_f32_16x16x32_bf16 v[140:143], v[64:67], v[56:59], v[140:143]\n"
      "s_waitcnt vmcnt(12) lgkmcnt(0)\n"
      "s_barrier\n"
      "ds_read_b64_tr_b16 v[68:69], v53 offset:0\n"
      "ds_read_b64_tr_b16 v[70:71], v53 offset:1920\n"
      "ds_read_b64_tr_b16 a[28:29], v52 offset:144\n"
      "ds_read_b64_tr_b16 a[30:31], v52 offset:2064\n"
      "ds_read_b64_tr_b16 a[100:101], v52 offset:288\n"
      "ds_read_b64_tr_b16 a[102:103], v52 offset:2208\n"
      "ds_read_b64_tr_b16 a[172:173], v52 offset:0\n"
      "ds_read_b64_tr_b16 a[174:175], v52 offset:1920\n"
      "v_add_u32 v52, 16896, v52\n"
      "v_add_u32 v53, 16896, v53\n"
      "ds_bpermute_b32 v64, v54, v60\n"
      "ds_bpermute_b32 v65, v54, v61\n"
      "ds_bpermute_b32 v66, v54, v62\n"
      "ds_bpermute_b32 v67, v54, v63\n"
      "v_mfma_f32_16x16x32_bf16 v[136:139], a[24:27], v[56:59], v[136:139]\n"
      "v_mfma_f32_16x16x32_bf16 v[144:147], v[56:59], a[96:99], v[144:147]\n"
      "v_mfma_f32_16x16x32_bf16 v[148:151], a[168:171], v[56:59], v[148:151]\n"
      "s_add_u32 m0, s43, 82432\n"
      "s_nop 0\n"
      "buffer_load_dwordx4 v48, s[12:15], s51 offen offset:0 lds\n"
      "buffer_load_dwordx4 v49, s[12:15], s51 offen offset:1024 lds\n"
      "buffer_load_dwordx4 v50, s[12:15], s51 offen offset:2048 lds\n"
      "buffer_load_dwordx4 v51, s[12:15], s51 offen offset:3072 lds\n"
      "s_add_u32 s51, s51, 110592\n"
      "v_and_b32 v72, v55, v72\n"
      "v_and_b32 v73, v55, v73\n"
      "v_and_b32 v74, v55, v74\n"
      "v_and_b32 v75, v55, v75\n"
      "v_accvgpr_write_b32 a236, v72\n"
      "v_accvgpr_write_b32 a237, v73\n"
      "v_accvgpr_write_b32 a238, v74\n"
      "v_accvgpr_write_b32 a239, v75\n"
      "v_mfma_f32_16x16x32_bf16 v[140:143], v[72:75], v[56:59], v[140:143]\n"
      "s_waitcnt vmcnt(12) lgkmcnt(0)\n"
      "s_barrier\n"
      "ds_read_b64_tr_b16 v[60:61], v53 offset:0\n"
      "ds_read_b64_tr_b16 v[62:63], v53 offset:1920\n"
      "ds_read_b64_tr_b16 a[32:33], v52 offset:144\n"
      "ds_read_b64_tr_b16 a[34:35], v52 offset:2064\n"
      "ds_read_b64_tr_b16 a[104:105], v52 offset:288\n"
      "ds_read_b64_tr_b16 a[106:107], v52 offset:2208\n"
      "ds_read_b64_tr_b16 a[176:177], v52 offset:0\n"
      "ds_read_b64_tr_b16 a[178:179], v52 offset:1920\n"
      "v_add_u32 v52, 16896, v52\n"
      "v_add_u32 v53, 16896, v53\n"
      "ds_bpermute_b32 v72, v54, v68\n"
      "ds_bpermute_b32 v73, v54, v69\n"
      "ds_bpermute_b32 v74, v54, v70\n"
      "ds_bpermute_b32 v75, v54, v71\n"
      "v_mfma_f32_16x16x32_bf16 v[136:139], a[28:31], v[56:59], v[136:139]\n"
      "v_mfma_f32_16x16x32_bf16 v[144:147], v[56:59], a[100:103], v[144:147]\n"
      "v_mfma_f32_16x16x32_bf16 v[148:151], a[172:175], v[56:59], v[148:151]\n"
      "s_add_u32 m0, s43, 99328\n"
      "s_nop 0\n"
      "buffer_load_dwordx4 v48, s[12:15], s51 offen offset:0 lds\n"
      "buffer_load_dwordx4 v49, s[12:15], s51 offen offset:1024 lds\n"
      "buffer_load_dwordx4 v50, s[12:15], s51 offen offset:2048 lds\n"
      "buffer_load_dwordx4 v51, s[12:15], s51 offen offset:3072 lds\n"
      "s_add_u32 s51, s51, 110592\n"
      "v_and_b32 v64, v55, v64\n"
      "v_and_b32 v65, v55, v65\n"
      "v_and_b32 v66, v55, v66\n"
      "v_and_b32 v67, v55, v67\n"
      "v_accvgpr_write_b32 a240, v64\n"
      "v_accvgpr_write_b32 a241, v65\n"
      "v_accvgpr_write_b32 a242, v66\n"
      "v_accvgpr_write_b32 a243, v67\n"
      "v_mfma_f32_16x16x32_bf16 v[140:143], v[64:67], v[56:59], v[140:143]\n"
      "s_waitcnt vmcnt(12) lgkmcnt(0)\n"
      "s_barrier\n"
      "ds_read_b64_tr_b16 v[68:69], v53 offset:0\n"
      "ds_read_b64_tr_b16 v[70:71], v53 offset:1920\n"
      "ds_read_b64_tr_b16 a[36:37], v52 offset:144\n"
      "ds_read_b64_tr_b16 a[38:39], v52 offset:2064\n"
      "ds_read_b64_tr_b16 a[108:109], v52 offset:288\n"
      "ds_read_b64_tr_b16 a[110:111], v52 offset:2208\n"
      "ds_read_b64_tr_b16 a[180:181], v52 offset:0\n"
      "ds_read_b64_tr_b16 a[182:183], v52 offset:1920\n"
      "v_add_u32 v52, 4294899712, v52\n"
      "v_add_u32 v53, 4294899712, v53\n"
      "ds_bpermute_b32 v64, v54, v60\n"
      "ds_bpermute_b32 v65, v54, v61\n"
      "ds_bpermute_b32 v66, v54, v62\n"
      "ds_bpermute_b32 v67, v54, v63\n"
      "v_mfma_f32_16x16x32_bf16 v[136:139], a[32:35], v[56:59], v[136:139]\n"
      "v_mfma_f32_16x16x32_bf16 v[144:147], v[56:59], a[104:107], v[144:147]\n"
      "v_mfma_f32_16x16x32_bf16 v[148:151], a[176:179], v[56:59], v[148:151]\n"
      "s_add_u32 m0, s43, 116224\n"
      "s_nop 0\n"
      "buffer_load_dwordx4 v48, s[12:15], s51 offen offset:0 lds\n"
      "buffer_load_dwordx4 v49, s[12:15], s51 offen offset:1024 lds\n"
      "buffer_load_dwordx4 v50, s[12:15], s51 offen offset:2048 lds\n"
      "buffer_load_dwordx4 v51, s[12:15], s51 offen offset:3072 lds\n"
      "s_add_u32 s51, s51, 110592\n"
      "v_and_b32 v72, v55, v72\n"
      "v_and_b32 v73, v55, v73\n"
      "v_and_b32 v74, v55, v74\n"
      "v_and_b32 v75, v55, v75\n"
      "v_accvgpr_write_b32 a244, v72\n"
      "v_accvgpr_write_b32 a245, v73\n"
      "v_accvgpr_write_b32 a246, v74\n"
      "v_accvgpr_write_b32 a247, v75\n"
      "v_mfma_f32_16x16x32_bf16 v[140:143], v[72:75], v[56:59], v[140:143]\n"
      "s_waitcnt vmcnt(12) lgkmcnt(0)\n"
      "s_barrier\n"
      "ds_read_b64_tr_b16 v[60:61], v53 offset:0\n"
      "ds_read_b64_tr_b16 v[62:63], v53 offset:1920\n"
      "ds_read_b64_tr_b16 a[40:41], v52 offset:144\n"
      "ds_read_b64_tr_b16 a[42:43], v52 offset:2064\n"
      "ds_read_b64_tr_b16 a[112:113], v52 offset:288\n"
      "ds_read_b64_tr_b16 a[114:115], v52 offset:2208\n"
      "ds_read_b64_tr_b16 a[184:185], v52 offset:0\n"
      "ds_read_b64_tr_b16 a[186:187], v52 offset:1920\n"
      "v_add_u32 v52, 16896, v52\n"
      "v_add_u32 v53, 16896, v53\n"
      "ds_bpermute_b32 v72, v54, v68\n"
      "ds_bpermute_b32 v73, v54, v69\n"
      "ds_bpermute_b32 v74, v54, v70\n"
      "ds_bpermute_b32 v75, v54, v71\n"
      "v_mfma_f32_16x16x32_bf16 v[136:139], a[36:39], v[56:59], v[136:139]\n"
      "v_mfma_f32_16x16x32_bf16 v[144:147], v[56:59], a[108:111], v[144:147]\n"
      "v_mfma_f32_16x16x32_bf16 v[148:151], a[180:183], v[56:59], v[148:151]\n"
      "s_add_u32 m0, s43, 133120\n"
      "s_nop 0\n"
      "buffer_load_dwordx4 v48, s[12:15], s51 offen offset:0 lds\n"
      "buffer_load_dwordx4 v49, s[12:15], s51 offen offset:1024 lds\n"
      "buffer_load_dwordx4 v50, s[12:15], s51 offen offset:2048 lds\n"
      "buffer_load_dwordx4 v51, s[12:15], s51 offen offset:3072 lds\n"
      "s_add_u32 s51, s51, 110592\n"
      "v_and_b32 v64, v55, v64\n"
      "v_and_b32 v65, v55, v65\n"
      "v_and_b32 v66, v55, v66\n"
      "v_and_b32 v67, v55, v67\n"
      "v_accvgpr_write_b32 a248, v64\n"
      "v_accvgpr_write_b32 a249, v65\n"
      "v_accvgpr_write_b32 a250, v66\n"
      "v_accvgpr_write_b32 a251, v67\n"
      "v_mfma_f32_16x16x32_bf16 v[140:143], v[64:67], v[56:59], v[140:143]\n"
      "s_add_u32 s30, s36, 24576\n"
      "buffer_load_dword v78, v2, s[8:11], s30 offen sc1\n"
      "s_add_u32 s30, s36, 49152\n"
      "buffer_load_dword v79, v2, s[8:11], s30 offen sc1\n"
      "s_waitcnt vmcnt(14) lgkmcnt(0)\n"
      "s_barrier\n"
      "ds_read_b64_tr_b16 v[68:69], v53 offset:0\n"
      "ds_read_b64_tr_b16 v[70:71], v53 offset:1920\n"
      "ds_read_b64_tr_b16 a[44:45], v52 offset:144\n"
      "ds_read_b64_tr_b16 a[46:47], v52 offset:2064\n"
      "ds_read_b64_tr_b16 a[116:117], v52 offset:288\n"
      "ds_read_b64_tr_b16 a[118:119], v52 offset:2208\n"
      "ds_read_b64_tr_b16 a[188:189], v52 offset:0\n"
      "ds_read_b64_tr_b16 a[190:191], v52 offset:1920\n"
      "v_add_u32 v52, 16896, v52\n"
      "v_add_u32 v53, 16896, v53\n"
      "ds_bpermute_b32 v64, v54, v60\n"
      "ds_bpermute_b32 v65, v54, v61\n"
      "ds_bpermute_b32 v66, v54, v62\n"
      "ds_bpermute_b32 v67, v54, v63\n"
      "v_mfma_f32_16x16x32_bf16 v[136:139], a[40:43], v[56:59], v[136:139]\n"
      "v_mfma_f32_16x16x32_bf16 v[144:147], v[56:59], a[112:115], v[144:147]\n"
      "v_mfma_f32_16x16x32_bf16 v[148:151], a[184:187], v[56:59], v[148:151]\n"
      "s_add_u32 m0, s43, 65536\n"
      "s_nop 0\n"
      "buffer_load_dwordx4 v48, s[12:15], s51 offen offset:0 lds\n"
      "buffer_load_dwordx4 v49, s[12:15], s51 offen offset:1024 lds\n"
      "buffer_load_dwordx4 v50, s[12:15], s51 offen offset:2048 lds\n"
      "buffer_load_dwordx4 v51, s[12:15], s51 offen offset:3072 lds\n"
      "s_add_u32 s51, s51, 110592\n"
      "v_and_b32 v72, v55, v72\n"
      "v_and_b32 v73, v55, v73\n"
      "v_and_b32 v74, v55, v74\n"
      "v_and_b32 v75, v55, v75\n"
      "v_accvgpr_write_b32 a252, v72\n"
      "v_accvgpr_write_b32 a253, v73\n"
      "v_accvgpr_write_b32 a254, v74\n"
      "v_accvgpr_write_b32 a255, v75\n"
      "v_mfma_f32_16x16x32_bf16 v[140:143], v[72:75], v[56:59], v[140:143]\n"
      "s_waitcnt vmcnt(14) lgkmcnt(0)\n"
      "s_barrier\n"
      "ds_read_b64_tr_b16 v[60:61], v53 offset:0\n"
      "ds_read_b64_tr_b16 v[62:63], v53 offset:1920\n"
      "ds_read_b64_tr_b16 a[48:49], v52 offset:144\n"
      "ds_read_b64_tr_b16 a[50:51], v52 offset:2064\n"
      "ds_read_b64_tr_b16 a[120:121], v52 offset:288\n"
      "ds_read_b64_tr_b16 a[122:123], v52 offset:2208\n"
      "ds_read_b64_tr_b16 a[192:193], v52 offset:0\n"
      "ds_read_b64_tr_b16 a[194:195], v52 offset:1920\n"
      "v_add_u32 v52, 16896, v52\n"
      "v_add_u32 v53, 16896, v53\n"
      "ds_bpermute_b32 v72, v54, v68\n"
      "ds_bpermute_b32 v73, v54, v69\n"
      "ds_bpermute_b32 v74, v54, v70\n"
      "ds_bpermute_b32 v75, v54, v71\n"
      "v_mfma_f32_16x16x32_bf16 v[136:139], a[44:47], v[56:59], v[136:139]\n"
      "v_mfma_f32_16x16x32_bf16 v[144:147], v[56:59], a[116:119], v[144:147]\n"
      "v_mfma_f32_16x16x32_bf16 v[148:151], a[188:191], v[56:59], v[148:151]\n"
      "s_add_u32 m0, s43, 82432\n"
      "s_nop 0\n"
      "buffer_load_dwordx4 v48, s[12:15], s51 offen offset:0 lds\n"
      "buffer_load_dwordx4 v49, s[12:15], s51 offen offset:1024 lds\n"
      "buffer_load_dwordx4 v50, s[12:15], s51 offen offset:2048 lds\n"
      "buffer_load_dwordx4 v51, s[12:15], s51 offen offset:3072 lds\n"
      "s_add_u32 s51, s51, 110592\n"
      "v_and_b32 v64, v55, v64\n"
      "v_and_b32 v65, v55, v65\n"
      "v_and_b32 v66, v55, v66\n"
      "v_and_b32 v67, v55, v67\n"
      "v_mov_b32 v16, v64\n"
      "v_mov_b32 v17, v65\n"
      "v_mov_b32 v18, v66\n"
      "v_mov_b32 v19, v67\n"
      "v_mfma_f32_16x16x32_bf16 v[140:143], v[64:67], v[56:59], v[140:143]\n"
      "s_waitcnt vmcnt(14) lgkmcnt(0)\n"
      "s_barrier\n"
      "ds_read_b64_tr_b16 v[68:69], v53 offset:0\n"
      "ds_read_b64_tr_b16 v[70:71], v53 offset:1920\n"
      "ds_read_b64_tr_b16 a[52:53], v52 offset:144\n"
      "ds_read_b64_tr_b16 a[54:55], v52 offset:2064\n"
      "ds_read_b64_tr_b16 a[124:125], v52 offset:288\n"
      "ds_read_b64_tr_b16 a[126:127], v52 offset:2208\n"
      "ds_read_b64_tr_b16 a[196:197], v52 offset:0\n"
      "ds_read_b64_tr_b16 a[198:199], v52 offset:1920\n"
      "v_add_u32 v52, 16896, v52\n"
      "v_add_u32 v53, 16896, v53\n"
      "ds_bpermute_b32 v64, v54, v60\n"
      "ds_bpermute_b32 v65, v54, v61\n"
      "ds_bpermute_b32 v66, v54, v62\n"
      "ds_bpermute_b32 v67, v54, v63\n"
      "v_mfma_f32_16x16x32_bf16 v[136:139], a[48:51], v[56:59], v[136:139]\n"
      "v_mfma_f32_16x16x32_bf16 v[144:147], v[56:59], a[120:123], v[144:147]\n"
      "v_mfma_f32_16x16x32_bf16 v[148:151], a[192:195], v[56:59], v[148:151]\n"
      "s_add_u32 m0, s43, 99328\n"
      "s_nop 0\n"
      "buffer_load_dwordx4 v48, s[12:15], s51 offen offset:0 lds\n"
      "buffer_load_dwordx4 v49, s[12:15], s51 offen offset:1024 lds\n"
      "buffer_load_dwordx4 v50, s[12:15], s51 offen offset:2048 lds\n"
      "buffer_load_dwordx4 v51, s[12:15], s51 offen offset:3072 lds\n"
      "s_add_u32 s51, s51, 110592\n"
      "v_and_b32 v72, v55, v72\n"
      "v_and_b32 v73, v55, v73\n"
      "v_and_b32 v74, v55, v74\n"
      "v_and_b32 v75, v55, v75\n"
      "v_mov_b32 v20, v72\n"
      "v_mov_b32 v21, v73\n"
      "v_mov_b32 v22, v74\n"
      "v_mov_b32 v23, v75\n"
      "v_mfma_f32_16x16x32_bf16 v[140:143], v[72:75], v[56:59], v[140:143]\n"
      "s_mul_i32 s21, s21, 1664525\n"
      "s_add_u32 s21, s21, 1013904223\n"
      "v_mov_b32 v13, s21\n"
      "buffer_store_dword v13, v10, s[8:11], 0 offen sc1\n"
      "v_not_b32 v80, v13\n"
      "v_not_b32 v81, v13\n"
      "v_not_b32 v82, v13\n"
      "v_not_b32 v83, v13\n"
      "s_add_u32 s30, s40, 0\n"
      "buffer_store_dwordx4 v[80:83], v76, s[8:11], s30 offen sc1\n"
      "v_xor_b32 v13, v13, v14\n"
      "s_add_u32 s52, s40, 4294893568\n"
      "s_add_u32 s40, s40, 4294918144\n"
      "s_add_u32 s36, s36, 24576\n"
      "s_mov_b32 s45, s36\n"
      "s_min_u32 s45, s45, s6\n"
      "s_add_u32 m0, s41, 0\n"
      "s_waitcnt vmcnt(15)\n"
      "v_cmp_ne_u32 vcc, s21, v78\n"
      "s_mov_b32 s46, s45\n"
      "s_cbranch_vccnz .Lsp_q0_1\n"
      ".Lbk_q0_1:\n"
      "buffer_load_dwordx4 v0, s[8:11], s46 offen offset:1024 sc1 lds\n"
      "s_add_u32 s36, s36, 24576\n"
      "s_mov_b32 s45, s36\n"
      "buffer_load_dwordx4 v0, s[8:11], s46 offen offset:0 sc1 lds\n"
      "buffer_load_dwordx4 v0, s[8:11], s46 offen offset:2048 sc1 lds\n"
      "s_min_u32 s45, s45, s6\n"
      "buffer_load_dwordx4 v0, s[8:11], s46 offen offset:3072 sc1 lds\n"
      "s_add_u32 m0, s42, 0\n"
      "s_nop 0\n"
      "buffer_load_dwordx4 v1, s[8:11], s46 offen offset:0 sc1 lds\n"
      "s_add_u32 s36, s36, 24576\n"
      "s_cmp_eq_u32 s36, s37\n"
      "s_cselect_b32 s47, s38, 0\n"
      "s_cselect_b32 s37, s48, s37\n"
      "s_cselect_b32 s49, s39, s53\n"
      "s_add_u32 m0, s41, 20480\n"
      "s_mov_b32 s46, s45\n"
      "s_waitcnt vmcnt(19)\n"
      "v_cmp_ne_u32 vcc, s21, v79\n"
      "s_add_u32 s45, s36, s47\n"
      "s_min_u32 s45, s45, s6\n"
      "s_cbranch_vccnz .Lsp_q1_2\n"
      ".Lbk_q1_2:\n"
      "buffer_load_dword v15, v2, s[8:11], s45 offen sc1\n"
      "buffer_load_dwordx4 v0, s[8:11], s46 offen offset:0 sc1 lds\n"
      "buffer_load_dwordx4 v0, s[8:11], s46 offen offset:1024 sc1 lds\n"
      "buffer_load_dwordx4 v0, s[8:11], s46 offen offset:2048 sc1 lds\n"
      "buffer_load_dwordx4 v0, s[8:11], s46 offen offset:3072 sc1 lds\n"
      "s_add_u32 m0, s42, 20480\n"
      "s_nop 0\n"
      "buffer_load_dwordx4 v1, s[8:11], s46 offen offset:0 sc1 lds\n"
      "s_waitcnt lgkmcnt(0)\n"
      "s_barrier\n"
      "ds_read_b64_tr_b16 v[60:61], v53 offset:0\n"
      "ds_read_b64_tr_b16 v[62:63], v53 offset:1920\n"
      "ds_read_b64_tr_b16 a[56:57], v52 offset:144\n"
      "ds_read_b64_tr_b16 a[58:59], v52 offset:2064\n"
      "ds_read_b64_tr_b16 a[128:129], v52 offset:288\n"
      "ds_read_b64_tr_b16 a[130:131], v52 offset:2208\n"
      "ds_read_b64_tr_b16 a[200:201], v52 offset:0\n"
      "ds_read_b64_tr_b16 a[202:203], v52 offset:1920\n"
      "v_add_u32 v52, 4294899712, v52\n"
      "v_add_u32 v53, 4294899712, v53\n"
      "ds_bpermute_b32 v72, v54, v68\n"
      "ds_bpermute_b32 v73, v54, v69\n"
      "ds_bpermute_b32 v74, v54, v70\n"
      "ds_bpermute_b32 v75, v54, v71\n"
      "v_mfma_f32_16x16x32_bf16 v[136:139], a[52:55], v[56:59], v[136:139]\n"
      "v_mfma_f32_16x16x32_bf16 v[144:147], v[56:59], a[124:127], v[144:147]\n"
      "v_mfma_f32_16x16x32_bf16 v[148:151], a[196:199], v[56:59], v[148:151]\n"
      "v_and_b32 v64, v55, v64\n"
      "v_and_b32 v65, v55, v65\n"
      "v_and_b32 v66, v55, v66\n"
      "v_and_b32 v67, v55, v67\n"
      "v_mov_b32 v24, v64\n"
      "v_mov_b32 v25, v65\n"
      "v_mov_b32 v26, v66\n"
      "v_mov_b32 v27, v67\n"
      "v_mfma_f32_16x16x32_bf16 v[140:143], v[64:67], v[56:59], v[140:143]\n"
      "s_waitcnt vmcnt(21) lgkmcnt(0)\n"
      "s_barrier\n"
      "ds_read_b64_tr_b16 v[68:69], v53 offset:0\n"
      "ds_read_b64_tr_b16 v[70:71], v53 offset:1920\n"
      "ds_read_b64_tr_b16 a[60:61], v52 offset:144\n"
      "ds_read_b64_tr_b16 a[62:63], v52 offset:2064\n"
      "ds_read_b64_tr_b16 a[132:133], v52 offset:288\n"
      "ds_read_b64_tr_b16 a[134:135], v52 offset:2208\n"
      "ds_read_b64_tr_b16 a[204:205], v52 offset:0\n"
      "ds_read_b64_tr_b16 a[206:207], v52 offset:1920\n"
      "v_add_u32 v52, 16896, v52\n"
      "v_add_u32 v53, 16896, v53\n"
      "ds_bpermute_b32 v64, v54, v60\n"
      "ds_bpermute_b32 v65, v54, v61\n"
      "ds_bpermute_b32 v66, v54, v62\n"
      "ds_bpermute_b32 v67, v54, v63\n"
      "v_mfma_f32_16x16x32_bf16 v[136:139], a[56:59], v[56:59], v[136:139]\n"
      "v_mfma_f32_16x16x32_bf16 v[144:147], v[56:59], a[128:131], v[144:147]\n"
      "v_mfma_f32_16x16x32_bf16 v[148:151], a[200:203], v[56:59], v[148:151]\n"
      "v_and_b32 v72, v55, v72\n"
      "v_and_b32 v73, v55, v73\n"
      "v_and_b32 v74, v55, v74\n"
      "v_and_b32 v75, v55, v75\n"
      "v_mov_b32 v28, v72\n"
      "v_mov_b32 v29, v73\n"
      "v_mov_b32 v30, v74\n"
      "v_mov_b32 v31, v75\n"
      "v_mfma_f32_16x16x32_bf16 v[140:143], v[72:75], v[56:59], v[140:143]\n"
      "s_waitcnt vmcnt(17) lgkmcnt(0)\n"
      "s_barrier\n"
      "ds_read_b64_tr_b16 v[60:61], v53 offset:0\n"
      "ds_read_b64_tr_b16 v[62:63], v53 offset:1920\n"
      "ds_read_b64_tr_b16 a[64:65], v52 offset:144\n"
      "ds_read_b64_tr_b16 a[66:67], v52 offset:2064\n"
      "ds_read_b64_tr_b16 a[136:137], v52 offset:288\n"
      "ds_read_b64_tr_b16 a[138:139], v52 offset:2208\n"
      "ds_read_b64_tr_b16 a[208:209], v52 offset:0\n"
      "ds_read_b64_tr_b16 a[210:211], v52 offset:1920\n"
      "v_add_u32 v52, 16896, v52\n"
      "v_add_u32 v53, 16896, v53\n"
      "ds_bpermute_b32 v72, v54, v68\n"
      "ds_bpermute_b32 v73, v54, v69\n"
      "ds_bpermute_b32 v74, v54, v70\n"
      "ds_bpermute_b32 v75, v54, v71\n"
      "v_mfma_f32_16x16x32_bf16 v[136:139], a[60:63], v[56:59], v[136:139]\n"
      "v_mfma_f32_16x16x32_bf16 v[144:147], v[56:59], a[132:135], v[144:147]\n"
      "v_mfma_f32_16x16x32_bf16 v[148:151], a[204:207], v[56:59], v[148:151]\n"
      "v_and_b32 v64, v55, v64\n"
      "v_and_b32 v65, v55, v65\n"
      "v_and_b32 v66, v55, v66\n"
      "v_and_b32 v67, v55, v67\n"
      "v_mov_b32 v32, v64\n"
      "v_mov_b32 v33, v65\n"
      "v_mov_b32 v34, v66\n"
      "v_mov_b32 v35, v67\n"
      "v_mfma_f32_16x16x32_bf16 v[140:143], v[64:67], v[56:59], v[140:143]\n"
      "s_waitcnt vmcnt(13) lgkmcnt(0)\n"
      "s_barrier\n"
      "ds_read_b64_tr_b16 v[68:69], v53 offset:0\n"
      "ds_read_b64_tr_b16 v[70:71], v53 offset:1920\n"
      "ds_read_b64_tr_b16 a[68:69], v52 offset:144\n"
      "ds_read_b64_tr_b16 a[70:71], v52 offset:2064\n"
      "ds_read_b64_tr_b16 a[140:141], v52 offset:288\n"
      "ds_read_b64_tr_b16 a[142:143], v52 offset:2208\n"
      "ds_read_b64_tr_b16 a[212:213], v52 offset:0\n"
      "ds_read_b64_tr_b16 a[214:215], v52 offset:1920\n"
      "v_add_u32 v52, 16896, v52\n"
      "v_add_u32 v53, 16896, v53\n"
      "ds_bpermute_b32 v64, v54, v60\n"
      "ds_bpermute_b32 v65, v54, v61\n"
      "ds_bpermute_b32 v66, v54, v62\n"
      "ds_bpermute_b32 v67, v54, v63\n"
      "v_mfma_f32_16x16x32_bf16 v[136:139], a[64:67], v[56:59], v[136:139]\n"
      "v_mfma_f32_16x16x32_bf16 v[144:147], v[56:59], a[136:139], v[144:147]\n"
      "v_mfma_f32_16x16x32_bf16 v[148:151], a[208:211], v[56:59], v[148:151]\n"
      "v_and_b32 v72, v55, v72\n"
      "v_and_b32 v73, v55, v73\n"
      "v_and_b32 v74, v55, v74\n"
      "v_and_b32 v75, v55, v75\n"
      "v_mov_b32 v36, v72\n"
      "v_mov_b32 v37, v73\n"
      "v_mov_b32 v38, v74\n"
      "v_mov_b32 v39, v75\n"
      "v_mfma_f32_16x16x32_bf16 v[140:143], v[72:75], v[56:59], v[140:143]\n"
      "s_waitcnt lgkmcnt(0)\n"
      "ds_bpermute_b32 v72, v54, v68\n"
      "ds_bpermute_b32 v73, v54, v69\n"
      "ds_bpermute_b32 v74, v54, v70\n"
      "ds_bpermute_b32 v75, v54, v71\n"
      "v_mfma_f32_16x16x32_bf16 v[136:139], a[68:71], v[56:59], v[136:139]\n"
      "v_mfma_f32_16x16x32_bf16 v[144:147], v[56:59], a[140:143], v[144:147]\n"
      "v_mfma_f32_16x16x32_bf16 v[148:151], a[212:215], v[56:59], v[148:151]\n"
      "v_and_b32 v64, v55, v64\n"
      "v_and_b32 v65, v55, v65\n"
      "v_and_b32 v66, v55, v66\n"
      "v_and_b32 v67, v55, v67\n"
      "v_mov_b32 v40, v64\n"
      "v_mov_b32 v41, v65\n"
      "v_mov_b32 v42, v66\n"
      "v_mov_b32 v43, v67\n"
      "v_mfma_f32_16x16x32_bf16 v[140:143], v[64:67], v[56:59], v[140:143]\n"
      "s_waitcnt lgkmcnt(0)\n"
      "v_and_b32 v72, v55, v72\n"
      "v_and_b32 v73, v55, v73\n"
      "v_and_b32 v74, v55, v74\n"
      "v_and_b32 v75, v55, v75\n"
      "v_mov_b32 v44, v72\n"
      "v_mov_b32 v45, v73\n"
      "v_mov_b32 v46, v74\n"
      "v_mov_b32 v47, v75\n"
      "v_mfma_f32_16x16x32_bf16 v[140:143], v[72:75], v[56:59], v[140:143]\n"
      "v_add_u32 v9, 1, v9\n"
      "s_waitcnt vmcnt(6)\n"
      "ds_write_b32 v11, v9\n"
      "ds_read_b32 v10, v12\n"
      "s_waitcnt lgkmcnt(0)\n"
      "v_cmp_gt_i32 vcc, v9, v10\n"
      "s_cbranch_vccnz .Lsp_qy0_3\n"
      ".Lbk_qy0_3:\n"
      "ds_read_b128 v[56:59], v0 offset:2048\n"
      "ds_read_b128 v[48:51], v0 offset:0\n"
      "ds_read2st64_b64 v[212:215], v4 offset0:0 offset1:1\n"
      "ds_read_b128 v[64:67], v0 offset:4096\n"
      "ds_read_b128 v[60:63], v0 offset:3072\n"
      "ds_read_b128 v[52:55], v0 offset:1024\n"
      "ds_read_b128 v[76:79], v0 offset:7168\n"
      "ds_read_b128 v[92:95], v0 offset:11264\n"
      "s_waitcnt lgkmcnt(5)\n"
      "v_add_f32 v212, v212, v214\n"
      "ds_read_b128 v[88:91], v0 offset:10240\n"
      "ds_read_b128 v[72:75], v0 offset:6144\n"
      "ds_read_b128 v[68:71], v0 offset:5120\n"
      "s_mov_b32 s20, 21\n"
      "s_waitcnt lgkmcnt(0)\n"
      "s_nop 4\n"
      ".p2align 6\n"
      ".Lq_loop:\n"
      "v_mfma_f32_16x16x32_bf16 v[120:123], a[0:3], v[48:51], 0\n"
      "s_waitcnt lgkmcnt(5)\n"
      "ds_read_b128 v[80:83], v0 offset:8192\n"
      "ds_read_b128 v[112:115], v0 offset:16384\n"
      "v_mfma_f32_16x16x32_bf16 v[120:123], a[4:7], v[52:55], v[120:123]\n"
      "ds_read_b128 v[84:87], v0 offset:9216\n"
      "v_mul_f32 v206, v206, v210\n"
      "v_mfma_f32_16x16x32_bf16 v[120:123], a[8:11], v[56:59], v[120:123]\n"
      "ds_read_b128 v[104:107], v0 offset:14336\n"
      "v_fma_f32 v132, -v218, v148, v132\n"
      "v_mfma_f32_16x16x32_bf16 v[120:123], a[12:15], v[60:63], v[120:123]\n"
      "ds_read_b64 v[216:217], v4 offset:1024\n"
      "v_fma_f32 v135, -v218, v151, v135\n"
      "v_mfma_f32_16x16x32_bf16 v[120:123], a[16:19], v[64:67], v[120:123]\n"
      "s_waitcnt lgkmcnt(5)\n"
      "ds_read_b128 v[96:99], v0 offset:12288\n"
      "v_fma_f32 v134, -v218, v150, v134\n"
      "v_mfma_f32_16x16x32_bf16 v[120:123], a[20:23], v[68:71], v[120:123]\n"
      "ds_read_b128 v[108:111], v0 offset:15360\n"
      "v_cvt_pk_bf16_f32 v177, v134, v135\n"
      "v_mfma_f32_16x16x32_bf16 v[120:123], a[24:27], v[72:75], v[120:123]\n"
      "ds_read_b128 v[100:103], v0 offset:13312\n"
      "v_fma_f32 v133, -v218, v149, v133\n"
      "v_mfma_f32_16x16x32_bf16 v[120:123], a[28:31], v[76:79], v[120:123]\n"
      "s_waitcnt lgkmcnt(6)\n"
      "v_cvt_pk_bf16_f32 v176, v132, v133\n"
      "v_mul_f32 v232, 0x3eae1a93, v219\n"
      "v_mfma_f32_16x16x32_bf16 v[120:123], a[32:35], v[80:83], v[120:123]\n"
      "ds_read_b128 v[116:119], v0 offset:17408\n"
      "v_mfma_f32_16x16x32_bf16 v[188:191], v[172:175], v[176:179], v[152:155]\n"
      "s_waitcnt lgkmcnt(5)\n"
      "s_add_u32 s36, s36, 24576\n"
      "v_mfma_f32_16x16x32_bf16 v[120:123], a[36:39], v[84:87], v[120:123]\n"
      "s_waitcnt vmcnt(6) lgkmcnt(4)\n"
      "buffer_store_dword v13, v3, s[8:11], s52 offen sc1\n"
      "v_mfma_f32_16x16x32_bf16 v[120:123], a[40:43], v[88:91], v[120:123]\n"
      "s_add_u32 m0, s41, 40960\n"
      "v_mul_f32 v205, v205, v210\n"
      "v_add_f32 v213, v213, v215\n"
      "v_mfma_f32_16x16x32_bf16 v[120:123], a[44:47], v[92:95], v[120:123]\n"
      "s_waitcnt lgkmcnt(2)\n"
      "v_mul_f32 v209, v209, v210\n"
      "v_add_f32 v213, v213, v217\n"
      "v_mfma_f32_16x16x32_bf16 v[120:123], a[48:51], v[96:99], v[120:123]\n"
      "s_waitcnt lgkmcnt(1)\n"
      "v_mul_f32 v204, v204, v210\n"
      "v_mul_f32 v208, v208, v210\n"
      "v_mfma_f32_16x16x32_bf16 v[120:123], a[52:55], v[100:103], v[120:123]\n"
      "v_cvt_pk_bf16_f32 v230, v208, v209\n"
      "v_add_f32 v218, v212, v216\n"
      "v_mfma_f32_16x16x32_bf16 v[120:123], a[56:59], v[104:107], v[120:123]\n"
      "buffer_store_dwordx2 v[230:231], v8, s[8:11], s40 offen sc1\n"
      "v_mfma_f32_16x16x32_bf16 v[120:123], a[60:63], v[108:111], v[120:123]\n"
      "s_mov_b32 s46, s45\n"
      "v_cvt_pk_bf16_f32 v228, v204, v205\n"
      "v_fma_f32 v213, -v218, v218, v213\n"
      "v_mfma_f32_16x16x32_bf16 v[120:123], a[64:67], v[112:115], v[120:123]\n"
      "s_waitcnt lgkmcnt(0)\n"
      "v_mul_f32 v207, v207, v210\n"
      "v_mov_b32 v187, v185\n"
      "v_mfma_f32_16x16x32_bf16 v[120:123], a[68:71], v[116:119], v[120:123]\n"
      "v_cvt_pk_bf16_f32 v229, v206, v207\n"
      "v_mov_b32 v182, v180\n"
      "v_mfma_f32_16x16x32_bf16 v[124:127], a[216:219], v[48:51], 0\n"
      "buffer_store_dwordx2 v[228:229], v7, s[8:11], s40 offen sc1\n"
      "v_mfma_f32_16x16x32_bf16 v[124:127], a[220:223], v[52:55], v[124:127]\n"
      "s_waitcnt vmcnt(8)\n"
      "v_cmp_ne_u32 vcc, s21, v15\n"
      "s_add_u32 s52, s52, 24576\n"
      "v_mfma_f32_16x16x32_bf16 v[124:127], a[224:227], v[56:59], v[124:127]\n"
      "v_mfma_f32_16x16x32_bf16 v[124:127], a[228:231], v[60:63], v[124:127]\n"
      "v_max_f32 v213, 0x3727c5ac, v213\n"
      "v_mfma_f32_16x16x32_bf16 v[124:127], a[232:235], v[64:67], v[124:127]\n"
      "v_rsq_f32 v219, v213\n"
      "v_mfma_f32_16x16x32_bf16 v[124:127], a[236:239], v[68:71], v[124:127]\n"
      "v_mov_b32 v183, v181\n"
      "v_fma_f32 v120, -v218, v136, v120\n"
      "v_mfma_f32_16x16x32_bf16 v[124:127], a[240:243], v[72:75], v[124:127]\n"
      "v_cndmask_b32 v233, v219, 1.0, s[54:55]\n"
      "v_mul_f32 v120, v219, v120\n"
      "v_mfma_f32_16x16x32_bf16 v[124:127], a[244:247], v[76:79], v[124:127]\n"
      "s_cbranch_vccnz .Lsp_q2_4\n"
      ".Lbk_q2_4:\n"
      "v_mfma_f32_16x16x32_bf16 v[124:127], a[248:251], v[80:83], v[124:127]\n"
      "buffer_load_dwordx4 v0, s[8:11], s46 offen offset:0 sc1 lds\n"
      "v_mfma_f32_16x16x32_bf16 v[124:127], a[252:255], v[84:87], v[124:127]\n"
      "v_mfma_f32_16x16x32_bf16 v[124:127], v[16:19], v[88:91], v[124:127]\n"
      "v_fma_f32 v121, -v218, v137, v121\n"
      "s_mov_b32 s45, s36\n"
      "v_mfma_f32_16x16x32_bf16 v[124:127], v[20:23], v[92:95], v[124:127]\n"
      "v_fma_f32 v123, -v218, v139, v123\n"
      "s_min_u32 s45, s45, s6\n"
      "v_mfma_f32_16x16x32_bf16 v[124:127], v[24:27], v[96:99], v[124:127]\n"
      "buffer_load_dwordx4 v0, s[8:11], s46 offen offset:1024 sc1 lds\n"
      "v_mfma_f32_16x16x32_bf16 v[124:127], v[28:31], v[100:103], v[124:127]\n"
      "v_mul_f32 v121, v219, v121\n"
      "v_mul_f32 v123, v219, v123\n"
      "v_mfma_f32_16x16x32_bf16 v[124:127], v[32:35], v[104:107], v[124:127]\n"
      "v_fma_f32 v122, -v218, v138, v122\n"
      "v_cvt_pk_bf16_f32 v172, v120, v121\n"
      "v_mfma_f32_16x16x32_bf16 v[124:127], v[36:39], v[108:111], v[124:127]\n"
      "v_mul_f32 v122, v219, v122\n"
      "v_add_u32 v9, 1, v9\n"
      "v_mfma_f32_16x16x32_bf16 v[124:127], v[40:43], v[112:115], v[124:127]\n"
      "v_cvt_pk_bf16_f32 v173, v122, v123\n"
      "v_mfma_f32_16x16x32_bf16 v[124:127], v[44:47], v[116:119], v[124:127]\n"
      "buffer_load_dword v15, v2, s[8:11], s45 offen sc1\n"
      "v_mfma_f32_16x16x32_bf16 v[128:131], v[48:51], a[72:75], 0\n"
      "s_waitcnt vmcnt(6)\n"
      "ds_write_b32 v11, v9\n"
      "v_mfma_f32_16x16x32_bf16 v[128:131], v[52:55], a[76:79], v[128:131]\n"
      "buffer_load_dwordx4 v0, s[8:11], s46 offen offset:2048 sc1 lds\n"
      "v_mfma_f32_16x16x32_bf16 v[128:131], v[56:59], a[80:83], v[128:131]\n"
      "v_fma_f32 v126, -v218, v142, v126\n"
      "v_fma_f32 v127, -v218, v143, v127\n"
      "v_mfma_f32_16x16x32_bf16 v[128:131], v[60:63], a[84:87], v[128:131]\n"
      "v_fma_f32 v124, -v218, v140, v124\n"
      "v_fma_f32 v125, -v218, v141, v125\n"
      "v_mfma_f32_16x16x32_bf16 v[128:131], v[64:67], a[88:91], v[128:131]\n"
      "v_mul_f32 v127, v219, v127\n"
      "v_mul_f32 v126, v219, v126\n"
      "v_mfma_f32_16x16x32_bf16 v[128:131], v[68:71], a[92:95], v[128:131]\n"
      "v_mul_f32 v125, v233, v125\n"
      "v_mul_f32 v124, v233, v124\n"
      "v_mfma_f32_16x16x32_bf16 v[128:131], v[72:75], a[96:99], v[128:131]\n"
      "v_cvt_pk_bf16_f32 v174, v126, v127\n"
      "v_cvt_pk_bf16_f32 v175, v124, v125\n"
      "v_mfma_f32_16x16x32_bf16 v[128:131], v[76:79], a[100:103], v[128:131]\n"
      "ds_write_b64 v5, v[218:219] offset:0\n"
      "v_mfma_f32_16x16x32_bf16 v[192:195], v[172:175], v[176:179], v[156:159]\n"
      "buffer_load_dwordx4 v0, s[8:11], s46 offen offset:3072 sc1 lds\n"
      "v_mfma_f32_16x16x32_bf16 v[196:199], v[172:175], v[164:167], v[168:171]\n"
      "v_mov_b32 v178, v175\n"
      "s_add_u32 m0, s42, 40960\n"
      "v_mfma_f32_16x16x32_bf16 v[128:131], v[80:83], a[104:107], v[128:131]\n"
      "ds_read_b32 v10, v12\n"
      "v_mfma_f32_16x16x32_bf16 v[128:131], v[84:87], a[108:111], v[128:131]\n"
      "v_max_f32 v235, v194, v195\n"
      "v_max_f32 v234, v192, v193\n"
      "v_mfma_f32_16x16x32_bf16 v[128:131], v[88:91], a[112:115], v[128:131]\n"
      "v_max3_f32 v234, v188, v189, v234\n"
      "v_max3_f32 v235, v190, v191, v235\n"
      "v_mfma_f32_16x16x32_bf16 v[128:131], v[92:95], a[116:119], v[128:131]\n"
      "v_max_f32 v236, v234, v235\n"
      "v_mov_b32 v237, v236\n"
      "v_mfma_f32_16x16x32_bf16 v[128:131], v[96:99], a[120:123], v[128:131]\n"
      "v_permlane16_swap_b32 v236, v237\n"
      "v_mfma_f32_16x16x32_bf16 v[128:131], v[100:103], a[124:127], v[128:131]\n"
      "ds_read_b128 v[220:223], v6 offset:0\n"
      "v_max_f32_e64 v236, v236, v237\n"
      "v_mfma_f32_16x16x32_bf16 v[128:131], v[104:107], a[128:131], v[128:131]\n"
      "s_waitcnt lgkmcnt(1)\n"
      "v_mov_b32_e64 v237, v236\n"
      "v_cmp_gt_i32 vcc, v9, v10\n"
      "v_mfma_f32_16x16x32_bf16 v[128:131], v[108:111], a[132:135], v[128:131]\n"
      "v_permlane32_swap_b32 v236, v237\n"
      "v_mfma_f32_16x16x32_bf16 v[128:131], v[112:115], a[136:139], v[128:131]\n"
      "ds_read_b128 v[224:227], v6 offset:16\n"
      "v_max_f32_e64 v236, v236, v237\n"
      "v_mfma_f32_16x16x32_bf16 v[128:131], v[116:119], a[140:143], v[128:131]\n"
      "s_cbranch_vccnz .Lsp_qy1_5\n"
      ".Lbk_qy1_5:\n"
      "v_mul_f32 v236, v236, v232\n"
      "v_mfma_f32_16x16x32_bf16 v[132:135], a[144:147], v[48:51], 0\n"
      "buffer_load_dwordx4 v1, s[8:11], s46 offen offset:0 sc1 lds\n"
      "v_mfma_f32_16x16x32_bf16 v[132:135], a[148:151], v[52:55], v[132:135]\n"
      "ds_read_b128 v[48:51], v0 offset:20480\n"
      "v_fma_f32 v235, v235, v232, -v236\n"
      "v_mfma_f32_16x16x32_bf16 v[132:135], a[152:155], v[56:59], v[132:135]\n"
      "s_waitcnt lgkmcnt(2)\n"
      "ds_read_b128 v[56:59], v0 offset:22528\n"
      "v_fma_f32 v128, -v220, v145, v128\n"
      "v_mfma_f32_16x16x32_bf16 v[132:135], a[156:159], v[60:63], v[132:135]\n"
      "ds_read_b128 v[52:55], v0 offset:21504\n"
      "v_cvt_pk_bf16_f32 v184, v196, v197\n"
      "v_mfma_f32_16x16x32_bf16 v[132:135], a[160:163], v[64:67], v[132:135]\n"
      "v_exp_f32 v235, v235\n"
      "v_mfma_f32_16x16x32_bf16 v[132:135], a[164:167], v[68:71], v[132:135]\n"
      "s_waitcnt lgkmcnt(3)\n"
      "v_fma_f32 v234, v234, v232, -v236\n"
      "v_fma_f32 v130, -v224, v147, v130\n"
      "v_mfma_f32_16x16x32_bf16 v[132:135], a[168:171], v[72:75], v[132:135]\n"
      "v_exp_f32 v234, v234\n"
      "v_mfma_f32_16x16x32_bf16 v[132:135], a[172:175], v[76:79], v[132:135]\n"
      "v_cvt_pk_bf16_f32 v235, v235, v235\n"
      "v_fma_f32 v131, -v226, v144, v131\n"
      "v_mfma_f32_16x16x32_bf16 v[132:135], a[176:179], v[80:83], v[132:135]\n"
      "v_cvt_pk_bf16_f32 v234, v234, v234\n"
      "v_mul_f32 v131, v227, v131\n"
      "v_mfma_f32_16x16x32_bf16 v[132:135], a[180:183], v[84:87], v[132:135]\n"
      "v_and_b32 v200, v160, v234\n"
      "v_mul_f32 v128, v221, v128\n"
      "v_mfma_f32_16x16x32_bf16 v[132:135], a[184:187], v[88:91], v[132:135]\n"
      "v_cvt_pk_bf16_f32 v185, v198, v199\n"
      "v_mul_f32 v130, v225, v130\n"
      "v_mfma_f32_16x16x32_bf16 v[132:135], a[188:191], v[92:95], v[132:135]\n"
      "v_and_b32 v203, v163, v235\n"
      "v_and_b32 v201, v161, v235\n"
      "v_mfma_f32_16x16x32_bf16 v[132:135], a[192:195], v[96:99], v[132:135]\n"
      "v_fma_f32 v129, -v222, v146, v129\n"
      "v_mul_f32 v129, v223, v129\n"
      "v_mfma_f32_16x16x32_bf16 v[132:135], a[196:199], v[100:103], v[132:135]\n"
      "ds_read_b128 v[64:67], v0 offset:24576\n"
      "v_and_b32 v202, v162, v234\n"
      "v_mfma_f32_16x16x32_bf16 v[132:135], a[200:203], v[104:107], v[132:135]\n"
      "v_cvt_pk_bf16_f32 v180, v128, v129\n"
      "v_cvt_pk_bf16_f32 v181, v130, v131\n"
      "v_mfma_f32_16x16x32_bf16 v[208:211], v[184:187], v[200:203], 0\n"
      "ds_read_b128 v[60:63], v0 offset:23552\n"
      "ds_read_b128 v[68:71], v0 offset:25600\n"
      "v_mfma_f32_16x16x32_bf16 v[204:207], v[180:183], v[200:203], 0\n"
      "ds_read_b128 v[92:95], v0 offset:31744\n"
      "v_mfma_f32_16x16x32_bf16 v[132:135], a[204:207], v[108:111], v[132:135]\n"
      "ds_read_b128 v[80:83], v0 offset:28672\n"
      "ds_read_b128 v[72:75], v0 offset:26624\n"
      "v_mfma_f32_16x16x32_bf16 v[132:135], a[208:211], v[112:115], v[132:135]\n"
      "v_rcp_f32 v210, v210\n"
      "v_mfma_f32_16x16x32_bf16 v[132:135], a[212:215], v[116:119], v[132:135]\n"
      "s_waitcnt lgkmcnt(6)\n"
      "ds_read_b128 v[76:79], v0 offset:27648\n"
      "ds_read2st64_b64 v[212:215], v4 offset0:40 offset1:41\n"
      "v_mfma_f32_16x16x32_bf16 v[120:123], a[0:3], v[48:51], 0\n"
      "ds_read_b128 v[108:111], v0 offset:35840\n"
      "v_mul_f32 v208, v208, v210\n"
      "v_mfma_f32_16x16x32_bf16 v[120:123], a[4:7], v[52:55], v[120:123]\n"
      "ds_read_b128 v[88:91], v0 offset:30720\n"
      "v_mfma_f32_16x16x32_bf16 v[120:123], a[8:11], v[56:59], v[120:123]\n"
      "s_waitcnt lgkmcnt(7)\n"
      "ds_read_b128 v[84:87], v0 offset:29696\n"
      "v_mul_f32 v209, v209, v210\n"
      "v_mfma_f32_16x16x32_bf16 v[120:123], a[12:15], v[60:63], v[120:123]\n"
      "ds_read_b128 v[112:115], v0 offset:36864\n"
      "v_fma_f32 v133, -v218, v149, v133\n"
      "v_mfma_f32_16x16x32_bf16 v[120:123], a[16:19], v[64:67], v[120:123]\n"
      "ds_read_b128 v[96:99], v0 offset:32768\n"
      "v_fma_f32 v135, -v218, v151, v135\n"
      "v_mfma_f32_16x16x32_bf16 v[120:123], a[20:23], v[68:71], v[120:123]\n"
      "s_waitcnt lgkmcnt(7)\n"
      "ds_read_b128 v[104:107], v0 offset:34816\n"
      "v_fma_f32 v132, -v218, v148, v132\n"
      "v_mfma_f32_16x16x32_bf16 v[120:123], a[24:27], v[72:75], v[120:123]\n"
      "s_waitcnt lgkmcnt(6)\n"
      "ds_read_b128 v[100:103], v0 offset:33792\n"
      "v_fma_f32 v134, -v218, v150, v134\n"
      "v_mfma_f32_16x16x32_bf16 v[120:123], a[28:31], v[76:79], v[120:123]\n"
      "v_cvt_pk_bf16_f32 v177, v134, v135\n"
      "v_cvt_pk_bf16_f32 v176, v132, v133\n"
      "v_mfma_f32_16x16x32_bf16 v[120:123], a[32:35], v[80:83], v[120:123]\n"
      "s_add_u32 s40, s40, 24576\n"
      "ds_read_b128 v[116:119], v0 offset:37888\n"
      "v_add_f32 v213, v213, v215\n"
      "v_mfma_f32_16x16x32_bf16 v[188:191], v[172:175], v[176:179], v[152:155]\n"
      "s_waitcnt lgkmcnt(5)\n"
      "v_mul_f32 v206, v206, v210\n"
      "v_mov_b32 v187, v185\n"
      "v_mfma_f32_16x16x32_bf16 v[120:123], a[36:39], v[84:87], v[120:123]\n"
      "ds_read_b64 v[216:217], v4 offset:21504\n"
      "v_mfma_f32_16x16x32_bf16 v[120:123], a[40:43], v[88:91], v[120:123]\n"
      "s_waitcnt vmcnt(6) lgkmcnt(4)\n"
      "buffer_store_dword v13, v3, s[8:11], s52 offen sc1\n"
      "v_mfma_f32_16x16x32_bf16 v[120:123], a[44:47], v[92:95], v[120:123]\n"
      "v_add_f32 v212, v212, v214\n"
      "v_mul_f32 v204, v204, v210\n"
      "v_mfma_f32_16x16x32_bf16 v[120:123], a[48:51], v[96:99], v[120:123]\n"
      "s_waitcnt lgkmcnt(2)\n"
      "v_cvt_pk_bf16_f32 v230, v208, v209\n"
      "v_mov_b32 v186, v184\n"
      "v_mfma_f32_16x16x32_bf16 v[120:123], a[52:55], v[100:103], v[120:123]\n"
      "v_mul_f32 v207, v207, v210\n"
      "s_mov_b32 s50, s49\n"
      "v_mfma_f32_16x16x32_bf16 v[120:123], a[56:59], v[104:107], v[120:123]\n"
      "buffer_store_dwordx2 v[230:231], v8, s[8:11], s40 offen sc1\n"
      "v_mfma_f32_16x16x32_bf16 v[120:123], a[60:63], v[108:111], v[120:123]\n"
      "s_add_u32 m0, s41, 0\n"
      "v_cvt_pk_bf16_f32 v229, v206, v207\n"
      "v_mul_f32 v232, 0x3eae1a93, v219\n"
      "v_mfma_f32_16x16x32_bf16 v[120:123], a[64:67], v[112:115], v[120:123]\n"
      "s_waitcnt lgkmcnt(0)\n"
      "v_add_f32 v218, v212, v216\n"
      "v_mov_b32 v183, v181\n"
      "v_mfma_f32_16x16x32_bf16 v[120:123], a[68:71], v[116:119], v[120:123]\n"
      "v_mul_f32 v205, v205, v210\n"
      "v_add_f32 v213, v213, v217\n"
      "v_mfma_f32_16x16x32_bf16 v[124:127], a[216:219], v[48:51], 0\n"
      "s_waitcnt vmcnt(5)\n"
      "v_cvt_pk_bf16_f32 v228, v204, v205\n"
      "v_cmp_ne_u32 vcc, s21, v15\n"
      "v_mfma_f32_16x16x32_bf16 v[124:127], a[220:223], v[52:55], v[124:127]\n"
      "v_fma_f32 v213, -v218, v218, v213\n"
      "s_mov_b32 s46, s45\n"
      "v_mfma_f32_16x16x32_bf16 v[124:127], a[224:227], v[56:59], v[124:127]\n"
      "buffer_store_dwordx2 v[228:229], v7, s[8:11], s40 offen sc1\n"
      "v_mfma_f32_16x16x32_bf16 v[124:127], a[228:231], v[60:63], v[124:127]\n"
      "v_fma_f32 v123, -v218, v139, v123\n"
      "v_mfma_f32_16x16x32_bf16 v[124:127], a[232:235], v[64:67], v[124:127]\n"
      "v_max_f32 v213, 0x3727c5ac, v213\n"
      "v_fma_f32 v121, -v218, v137, v121\n"
      "v_mfma_f32_16x16x32_bf16 v[124:127], a[236:239], v[68:71], v[124:127]\n"
      "v_rsq_f32 v219, v213\n"
      "v_mfma_f32_16x16x32_bf16 v[124:127], a[240:243], v[72:75], v[124:127]\n"
      "s_cbranch_vccnz .Lsp_q0_6\n"
      ".Lbk_q0_6:\n"
      "v_mul_f32 v123, v219, v123\n"
      "v_mfma_f32_16x16x32_bf16 v[124:127], a[244:247], v[76:79], v[124:127]\n"
      "buffer_load_dwordx4 v0, s[8:11], s46 offen offset:1024 sc1 lds\n"
      "v_mfma_f32_16x16x32_bf16 v[124:127], a[248:251], v[80:83], v[124:127]\n"
      "v_mfma_f32_16x16x32_bf16 v[124:127], a[252:255], v[84:87], v[124:127]\n"
      "s_add_u32 s36, s36, 24576\n"
      "v_fma_f32 v122, -v218, v138, v122\n"
      "v_mfma_f32_16x16x32_bf16 v[124:127], v[16:19], v[88:91], v[124:127]\n"
      "ds_write_b64 v5, v[218:219] offset:512\n"
      "v_mfma_f32_16x16x32_bf16 v[124:127], v[20:23], v[92:95], v[124:127]\n"
      "v_fma_f32 v120, -v218, v136, v120\n"
      "v_mfma_f32_16x16x32_bf16 v[124:127], v[24:27], v[96:99], v[124:127]\n"
      "v_mul_f32 v122, v219, v122\n"
      "v_mfma_f32_16x16x32_bf16 v[124:127], v[28:31], v[100:103], v[124:127]\n"
      "s_mov_b32 s45, s36\n"
      "v_mul_f32 v120, v219, v120\n"
      "v_mul_f32 v121, v219, v121\n"
      "v_mfma_f32_16x16x32_bf16 v[124:127], v[32:35], v[104:107], v[124:127]\n"
      "buffer_load_dwordx4 v0, s[8:11], s46 offen offset:0 sc1 lds\n"
      "v_mfma_f32_16x16x32_bf16 v[124:127], v[36:39], v[108:111], v[124:127]\n"
      "v_add_u32 v9, 1, v9\n"
      "v_mfma_f32_16x16x32_bf16 v[124:127], v[40:43], v[112:115], v[124:127]\n"
      "v_cvt_pk_bf16_f32 v172, v120, v121\n"
      "v_mfma_f32_16x16x32_bf16 v[124:127], v[44:47], v[116:119], v[124:127]\n"
      "s_waitcnt vmcnt(5) lgkmcnt(0)\n"
      "ds_write_b32 v11, v9\n"
      "v_mfma_f32_16x16x32_bf16 v[128:131], v[48:51], a[72:75], 0\n"
      "buffer_load_dwordx4 v0, s[8:11], s46 offen offset:2048 sc1 lds\n"
      "v_mfma_f32_16x16x32_bf16 v[128:131], v[52:55], a[76:79], v[128:131]\n"
      "v_cndmask_b32 v233, v219, 1.0, s[54:55]\n"
      "v_cvt_pk_bf16_f32 v173, v122, v123\n"
      "v_mfma_f32_16x16x32_bf16 v[128:131], v[56:59], a[80:83], v[128:131]\n"
      "v_fma_f32 v124, -v218, v140, v124\n"
      "v_fma_f32 v125, -v218, v141, v125\n"
      "v_mfma_f32_16x16x32_bf16 v[128:131], v[60:63], a[84:87], v[128:131]\n"
      "s_min_u32 s45, s45, s6\n"
      "v_fma_f32 v127, -v218, v143, v127\n"
      "v_mul_f32 v125, v233, v125\n"
      "v_mfma_f32_16x16x32_bf16 v[128:131], v[64:67], a[88:91], v[128:131]\n"
      "v_mul_f32 v127, v219, v127\n"
      "v_fma_f32 v126, -v218, v142, v126\n"
      "v_mfma_f32_16x16x32_bf16 v[128:131], v[68:71], a[92:95], v[128:131]\n"
      "v_mul_f32 v126, v219, v126\n"
      "v_cvt_pk_bf16_f32 v174, v126, v127\n"
      "v_mfma_f32_16x16x32_bf16 v[128:131], v[72:75], a[96:99], v[128:131]\n"
      "v_mul_f32 v124, v233, v124\n"
      "v_cvt_pk_bf16_f32 v175, v124, v125\n"
      "v_mfma_f32_16x16x32_bf16 v[128:131], v[76:79], a[100:103], v[128:131]\n"
      "buffer_load_dword v15, v2, s[8:11], s45 offen sc1\n"
      "v_mfma_f32_16x16x32_bf16 v[192:195], v[172:175], v[176:179], v[156:159]\n"
      "ds_read_b128 v[220:223], v6 offset:512\n"
      "v_mov_b32 v182, v180\n"
      "v_mfma_f32_16x16x32_bf16 v[196:199], v[172:175], v[164:167], v[168:171]\n"
      "ds_read_b128 v[224:227], v6 offset:528\n"
      "ds_read_b32 v10, v12\n"
      "v_mfma_f32_16x16x32_bf16 v[128:131], v[80:83], a[104:107], v[128:131]\n"
      "buffer_load_dwordx4 v0, s[8:11], s46 offen offset:3072 sc1 lds\n"
      "v_mfma_f32_16x16x32_bf16 v[128:131], v[84:87], a[108:111], v[128:131]\n"
      "v_max_f32 v234, v192, v193\n"
      "v_max3_f32 v234, v188, v189, v234\n"
      "v_mfma_f32_16x16x32_bf16 v[128:131], v[88:91], a[112:115], v[128:131]\n"
      "s_add_u32 m0, s42, 0\n"
      "v_max_f32 v235, v194, v195\n"
      "v_max3_f32 v235, v190, v191, v235\n"
      "v_mfma_f32_16x16x32_bf16 v[128:131], v[92:95], a[116:119], v[128:131]\n"
      "v_max_f32 v236, v234, v235\n"
      "v_mov_b32 v237, v236\n"
      "v_mfma_f32_16x16x32_bf16 v[128:131], v[96:99], a[120:123], v[128:131]\n"
      "buffer_load_dwordx4 v1, s[8:11], s46 offen offset:0 sc1 lds\n"
      "v_mfma_f32_16x16x32_bf16 v[128:131], v[100:103], a[124:127], v[128:131]\n"
      "v_permlane16_swap_b32 v236, v237\n"
      "v_mfma_f32_16x16x32_bf16 v[128:131], v[104:107], a[128:131], v[128:131]\n"
      "s_waitcnt lgkmcnt(0)\n"
      "v_cmp_gt_i32 vcc, v9, v10\n"
      "v_max_f32 v236, v236, v237\n"
      "v_mfma_f32_16x16x32_bf16 v[128:131], v[108:111], a[132:135], v[128:131]\n"
      "v_cvt_pk_bf16_f32 v185, v198, v199\n"
      "v_mov_b32_e64 v237, v236\n"
      "v_mfma_f32_16x16x32_bf16 v[128:131], v[112:115], a[136:139], v[128:131]\n"
      "v_permlane32_swap_b32 v236, v237\n"
      "v_mfma_f32_16x16x32_bf16 v[128:131], v[116:119], a[140:143], v[128:131]\n"
      "s_cbranch_vccnz .Lsp_qy2_7\n"
      ".Lbk_qy2_7:\n"
      "v_max_f32 v236, v236, v237\n"
      "v_mfma_f32_16x16x32_bf16 v[132:135], a[144:147], v[48:51], 0\n"
      "v_mul_f32 v236, v236, v232\n"
      "v_fma_f32 v235, v235, v232, -v236\n"
      "v_mfma_f32_16x16x32_bf16 v[132:135], a[148:151], v[52:55], v[132:135]\n"
      "ds_read_b128 v[48:51], v0 offset:40960\n"
      "v_fma_f32 v234, v234, v232, -v236\n"
      "v_mfma_f32_16x16x32_bf16 v[132:135], a[152:155], v[56:59], v[132:135]\n"
      "v_fma_f32 v128, -v220, v145, v128\n"
      "v_fma_f32 v130, -v224, v147, v130\n"
      "v_mfma_f32_16x16x32_bf16 v[132:135], a[156:159], v[60:63], v[132:135]\n"
      "ds_read2st64_b64 v[212:215], v4 offset0:80 offset1:81\n"
      "v_mul_f32 v128, v221, v128\n"
      "v_mfma_f32_16x16x32_bf16 v[132:135], a[160:163], v[64:67], v[132:135]\n"
      "v_exp_f32 v235, v235\n"
      "v_mfma_f32_16x16x32_bf16 v[132:135], a[164:167], v[68:71], v[132:135]\n"
      "v_exp_f32 v234, v234\n"
      "v_mfma_f32_16x16x32_bf16 v[132:135], a[168:171], v[72:75], v[132:135]\n"
      "v_cvt_pk_bf16_f32 v184, v196, v197\n"
      "v_cvt_pk_bf16_f32 v235, v235, v235\n"
      "v_mfma_f32_16x16x32_bf16 v[132:135], a[172:175], v[76:79], v[132:135]\n"
      "ds_read_b128 v[64:67], v0 offset:45056\n"
      "v_and_b32 v201, v161, v235\n"
      "v_mfma_f32_16x16x32_bf16 v[132:135], a[176:179], v[80:83], v[132:135]\n"
      "v_fma_f32 v129, -v222, v146, v129\n"
      "v_mul_f32 v130, v225, v130\n"
      "v_cvt_pk_bf16_f32 v234, v234, v234\n"
      "v_mfma_f32_16x16x32_bf16 v[132:135], a[180:183], v[84:87], v[132:135]\n"
      "v_fma_f32 v131, -v226, v144, v131\n"
      "v_mul_f32 v129, v223, v129\n"
      "v_mfma_f32_16x16x32_bf16 v[132:135], a[184:187], v[88:91], v[132:135]\n"
      "ds_read_b128 v[68:71], v0 offset:46080\n"
      "v_and_b32 v200, v160, v234\n"
      "v_mfma_f32_16x16x32_bf16 v[132:135], a[188:191], v[92:95], v[132:135]\n"
      "s_add_u32 s52, s52, 24576\n"
      "ds_read_b128 v[56:59], v0 offset:43008\n"
      "v_and_b32 v202, v162, v234\n"
      "v_mfma_f32_16x16x32_bf16 v[132:135], a[192:195], v[96:99], v[132:135]\n"
      "v_and_b32 v203, v163, v235\n"
      "v_mul_f32 v131, v227, v131\n"
      "v_mfma_f32_16x16x32_bf16 v[132:135], a[196:199], v[100:103], v[132:135]\n"
      "ds_read_b128 v[52:55], v0 offset:41984\n"
      "v_cvt_pk_bf16_f32 v180, v128, v129\n"
      "v_mfma_f32_16x16x32_bf16 v[132:135], a[200:203], v[104:107], v[132:135]\n"
      "ds_read_b128 v[72:75], v0 offset:47104\n"
      "v_cvt_pk_bf16_f32 v181, v130, v131\n"
      "v_mfma_f32_16x16x32_bf16 v[208:211], v[184:187], v[200:203], 0\n"
      "ds_read_b128 v[60:63], v0 offset:44032\n"
      "v_mov_b32 v178, v175\n"
      "v_mfma_f32_16x16x32_bf16 v[204:207], v[180:183], v[200:203], 0\n"
      "s_waitcnt lgkmcnt(5)\n"
      "ds_read_b128 v[80:83], v0 offset:49152\n"
      "v_add_f32 v213, v213, v215\n"
      "v_mfma_f32_16x16x32_bf16 v[132:135], a[204:207], v[108:111], v[132:135]\n"
      "ds_read_b128 v[84:87], v0 offset:50176\n"
      "v_mfma_f32_16x16x32_bf16 v[132:135], a[208:211], v[112:115], v[132:135]\n"
      "s_add_u32 s36, s36, 24576\n"
      "v_rcp_f32 v210, v210\n"
      "v_mfma_f32_16x16x32_bf16 v[132:135], a[212:215], v[116:119], v[132:135]\n"
      "ds_read_b128 v[76:79], v0 offset:48128\n"
      "v_mul_f32 v208, v208, v210\n"
      "v_mfma_f32_16x16x32_bf16 v[120:123], a[0:3], v[48:51], 0\n"
      "s_waitcnt lgkmcnt(5)\n"
      "ds_read_b128 v[96:99], v0 offset:53248\n"
      "v_mul_f32 v209, v209, v210\n"
      "v_mfma_f32_16x16x32_bf16 v[120:123], a[4:7], v[52:55], v[120:123]\n"
      "ds_read_b128 v[92:95], v0 offset:52224\n"
      "v_mul_f32 v204, v204, v210\n"
      "v_mfma_f32_16x16x32_bf16 v[120:123], a[8:11], v[56:59], v[120:123]\n"
      "s_waitcnt lgkmcnt(5)\n"
      "ds_read_b128 v[88:91], v0 offset:51200\n"
      "v_fma_f32 v133, -v218, v149, v133\n"
      "v_mfma_f32_16x16x32_bf16 v[120:123], a[12:15], v[60:63], v[120:123]\n"
      "s_cmp_eq_u32 s36, s37\n"
      "ds_read_b128 v[100:103], v0 offset:54272\n"
      "v_fma_f32 v132, -v218, v148, v132\n"
      "v_mfma_f32_16x16x32_bf16 v[120:123], a[16:19], v[64:67], v[120:123]\n"
      "s_cselect_b32 s47, s38, 0\n"
      "ds_read_b128 v[104:107], v0 offset:55296\n"
      "v_fma_f32 v135, -v218, v151, v135\n"
      "v_mfma_f32_16x16x32_bf16 v[120:123], a[20:23], v[68:71], v[120:123]\n"
      "s_cselect_b32 s37, s48, s37\n"
      "ds_read_b128 v[112:115], v0 offset:57344\n"
      "v_cvt_pk_bf16_f32 v176, v132, v133\n"
      "v_mfma_f32_16x16x32_bf16 v[120:123], a[24:27], v[72:75], v[120:123]\n"
      "s_waitcnt lgkmcnt(6)\n"
      "s_cselect_b32 s49, s39, s53\n"
      "v_fma_f32 v134, -v218, v150, v134\n"
      "v_mfma_f32_16x16x32_bf16 v[120:123], a[28:31], v[76:79], v[120:123]\n"
      "ds_read_b64 v[216:217], v4 offset:41984\n"
      "v_cvt_pk_bf16_f32 v177, v134, v135\n"
      "v_mfma_f32_16x16x32_bf16 v[120:123], a[32:35], v[80:83], v[120:123]\n"
      "ds_read_b128 v[108:111], v0 offset:56320\n"
      "s_add_u32 s40, s40, 24576\n"
      "v_mfma_f32_16x16x32_bf16 v[188:191], v[172:175], v[176:179], v[152:155]\n"
      "ds_read_b128 v[116:119], v0 offset:58368\n"
      "v_cvt_pk_bf16_f32 v230, v208, v209\n"
      "v_mfma_f32_16x16x32_bf16 v[120:123], a[36:39], v[84:87], v[120:123]\n"
      "s_waitcnt lgkmcnt(5)\n"
      "v_mfma_f32_16x16x32_bf16 v[120:123], a[40:43], v[88:91], v[120:123]\n"
      "v_mul_f32 v205, v205, v210\n"
      "v_add_f32 v212, v212, v214\n"
      "v_mfma_f32_16x16x32_bf16 v[120:123], a[44:47], v[92:95], v[120:123]\n"
      "s_waitcnt vmcnt(6) lgkmcnt(3)\n"
      "buffer_store_dword v13, v3, s[8:11], s52 offen sc1\n"
      "v_mfma_f32_16x16x32_bf16 v[120:123], a[48:51], v[96:99], v[120:123]\n"
      "v_mul_f32 v207, v207, v210\n"
      "v_cvt_pk_bf16_f32 v228, v204, v205\n"
      "v_mfma_f32_16x16x32_bf16 v[120:123], a[52:55], v[100:103], v[120:123]\n"
      "s_waitcnt lgkmcnt(2)\n"
      "v_add_f32 v218, v212, v216\n"
      "v_mfma_f32_16x16x32_bf16 v[120:123], a[56:59], v[104:107], v[120:123]\n"
      "s_waitcnt lgkmcnt(1)\n"
      "v_mov_b32 v186, v184\n"
      "v_mul_f32 v232, 0x3eae1a93, v219\n"
      "v_mfma_f32_16x16x32_bf16 v[120:123], a[60:63], v[108:111], v[120:123]\n"
      "buffer_store_dwordx2 v[230:231], v8, s[8:11], s40 offen sc1\n"
      "v_mfma_f32_16x16x32_bf16 v[120:123], a[64:67], v[112:115], v[120:123]\n"
      "s_waitcnt lgkmcnt(0)\n"
      "v_add_f32 v213, v213, v217\n"
      "v_fma_f32 v213, -v218, v218, v213\n"
      "v_mfma_f32_16x16x32_bf16 v[120:123], a[68:71], v[116:119], v[120:123]\n"
      "v_mul_f32 v206, v206, v210\n"
      "v_max_f32 v213, 0x3727c5ac, v213\n"
      "v_mfma_f32_16x16x32_bf16 v[124:127], a[216:219], v[48:51], 0\n"
      "v_cvt_pk_bf16_f32 v229, v206, v207\n"
      "v_mfma_f32_16x16x32_bf16 v[124:127], a[220:223], v[52:55], v[124:127]\n"
      "v_rsq_f32 v219, v213\n"
      "v_mfma_f32_16x16x32_bf16 v[124:127], a[224:227], v[56:59], v[124:127]\n"
      "buffer_store_dwordx2 v[228:229], v7, s[8:11], s40 offen sc1\n"
      "v_mfma_f32_16x16x32_bf16 v[124:127], a[228:231], v[60:63], v[124:127]\n"
      "v_fma_f32 v123, -v218, v139, v123\n"
      "s_add_u32 m0, s41, 20480\n"
      "v_mfma_f32_16x16x32_bf16 v[124:127], a[232:235], v[64:67], v[124:127]\n"
      "v_mov_b32 v187, v185\n"
      "v_mul_f32 v123, v219, v123\n"
      "v_mfma_f32_16x16x32_bf16 v[124:127], a[236:239], v[68:71], v[124:127]\n"
      "v_fma_f32 v121, -v218, v137, v121\n"
      "v_fma_f32 v120, -v218, v136, v120\n"
      "v_mfma_f32_16x16x32_bf16 v[124:127], a[240:243], v[72:75], v[124:127]\n"
      "s_mov_b32 s46, s45\n"
      "v_mul_f32 v120, v219, v120\n"
      "v_add_u32 v9, 1, v9\n"
      "v_mfma_f32_16x16x32_bf16 v[124:127], a[244:247], v[76:79], v[124:127]\n"
      "v_fma_f32 v122, -v218, v138, v122\n"
      "v_mul_f32 v122, v219, v122\n"
      "v_mfma_f32_16x16x32_bf16 v[124:127], a[248:251], v[80:83], v[124:127]\n"
      "s_waitcnt vmcnt(5)\n"
      "v_cmp_ne_u32 vcc, s21, v15\n"
      "v_mul_f32 v121, v219, v121\n"
      "v_mfma_f32_16x16x32_bf16 v[124:127], a[252:255], v[84:87], v[124:127]\n"
      "s_add_u32 s45, s36, s47\n"
      "v_cvt_pk_bf16_f32 v172, v120, v121\n"
      "v_mfma_f32_16x16x32_bf16 v[124:127], v[16:19], v[88:91], v[124:127]\n"
      "v_mfma_f32_16x16x32_bf16 v[124:127], v[20:23], v[92:95], v[124:127]\n"
      "s_add_u32 s40, s40, 24576\n"
      "ds_write_b64 v5, v[218:219] offset:1024\n"
      "v_mfma_f32_16x16x32_bf16 v[124:127], v[24:27], v[96:99], v[124:127]\n"
      "s_min_u32 s45, s45, s6\n"
      "v_mov_b32 v182, v180\n"
      "v_mfma_f32_16x16x32_bf16 v[124:127], v[28:31], v[100:103], v[124:127]\n"
      "v_cvt_pk_bf16_f32 v173, v122, v123\n"
      "v_mfma_f32_16x16x32_bf16 v[124:127], v[32:35], v[104:107], v[124:127]\n"
      "s_waitcnt vmcnt(3)\n"
      "ds_write_b32 v11, v9\n"
      "v_mfma_f32_16x16x32_bf16 v[124:127], v[36:39], v[108:111], v[124:127]\n"
      "s_cbranch_vccnz .Lsp_q1_8\n"
      ".Lbk_q1_8:\n"
      "v_cndmask_b32 v233, v219, 1.0, s[54:55]\n"
      "v_mfma_f32_16x16x32_bf16 v[124:127], v[40:43], v[112:115], v[124:127]\n"
      "v_mov_b32 v183, v181\n"
      "v_mfma_f32_16x16x32_bf16 v[124:127], v[44:47], v[116:119], v[124:127]\n"
      "buffer_load_dword v15, v2, s[8:11], s45 offen sc1\n"
      "v_mfma_f32_16x16x32_bf16 v[128:131], v[48:51], a[72:75], 0\n"
      "ds_read_b128 v[220:223], v6 offset:1024\n"
      "v_mfma_f32_16x16x32_bf16 v[128:131], v[52:55], a[76:79], v[128:131]\n"
      "buffer_load_dwordx4 v0, s[8:11], s46 offen offset:0 sc1 lds\n"
      "v_mfma_f32_16x16x32_bf16 v[128:131], v[56:59], a[80:83], v[128:131]\n"
      "v_fma_f32 v126, -v218, v142, v126\n"
      "v_fma_f32 v124, -v218, v140, v124\n"
      "v_mfma_f32_16x16x32_bf16 v[128:131], v[60:63], a[84:87], v[128:131]\n"
      "v_fma_f32 v125, -v218, v141, v125\n"
      "v_mul_f32 v125, v233, v125\n"
      "v_mfma_f32_16x16x32_bf16 v[128:131], v[64:67], a[88:91], v[128:131]\n"
      "v_fma_f32 v127, -v218, v143, v127\n"
      "v_mul_f32 v124, v233, v124\n"
      "v_mfma_f32_16x16x32_bf16 v[128:131], v[68:71], a[92:95], v[128:131]\n"
      "v_mul_f32 v126, v219, v126\n"
      "v_mul_f32 v127, v219, v127\n"
      "v_mfma_f32_16x16x32_bf16 v[128:131], v[72:75], a[96:99], v[128:131]\n"
      "v_cvt_pk_bf16_f32 v174, v126, v127\n"
      "v_cvt_pk_bf16_f32 v175, v124, v125\n"
      "v_mfma_f32_16x16x32_bf16 v[128:131], v[76:79], a[100:103], v[128:131]\n"
      "buffer_load_dwordx4 v0, s[8:11], s46 offen offset:1024 sc1 lds\n"
      "v_mfma_f32_16x16x32_bf16 v[192:195], v[172:175], v[176:179], v[156:159]\n"
      "ds_read_b32 v10, v12\n"
      "v_mov_b32 v178, v175\n"
      "v_mfma_f32_16x16x32_bf16 v[196:199], v[172:175], v[164:167], v[168:171]\n"
      "s_add_u32 s52, s52, 24576\n"
      "ds_read_b128 v[224:227], v6 offset:1040\n"
      "v_mfma_f32_16x16x32_bf16 v[128:131], v[80:83], a[104:107], v[128:131]\n"
      "buffer_load_dwordx4 v0, s[8:11], s46 offen offset:2048 sc1 lds\n"
      "v_mfma_f32_16x16x32_bf16 v[128:131], v[84:87], a[108:111], v[128:131]\n"
      "v_med3_f32 v235, v194, v195, s50\n"
      "v_med3_f32 v234, v192, v193, s50\n"
      "v_mfma_f32_16x16x32_bf16 v[128:131], v[88:91], a[112:115], v[128:131]\n"
      "v_max3_f32 v234, v188, v189, v234\n"
      "v_max3_f32 v235, v190, v191, v235\n"
      "v_mfma_f32_16x16x32_bf16 v[128:131], v[92:95], a[116:119], v[128:131]\n"
      "s_waitcnt lgkmcnt(1)\n"
      "v_max_f32 v236, v234, v235\n"
      "v_mov_b32 v237, v236\n"
      "v_cmp_gt_i32 vcc, v9, v10\n"
      "v_mfma_f32_16x16x32_bf16 v[128:131], v[96:99], a[120:123], v[128:131]\n"
      "v_permlane16_swap_b32 v236, v237\n"
      "v_mfma_f32_16x16x32_bf16 v[128:131], v[100:103], a[124:127], v[128:131]\n"
      "v_max_f32 v236, v236, v237\n"
      "v_mov_b32 v237, v236\n"
      "v_mfma_f32_16x16x32_bf16 v[128:131], v[104:107], a[128:131], v[128:131]\n"
      "buffer_load_dwordx4 v0, s[8:11], s46 offen offset:3072 sc1 lds\n"
      "v_mfma_f32_16x16x32_bf16 v[128:131], v[108:111], a[132:135], v[128:131]\n"
      "s_add_u32 m0, s42, 20480\n"
      "v_permlane32_swap_b32 v236, v237\n"
      "v_mfma_f32_16x16x32_bf16 v[128:131], v[112:115], a[136:139], v[128:131]\n"
      "v_max_f32_e64 v236, v236, v237\n"
      "v_mul_f32_e64 v236, v236, v232\n"
      "v_mfma_f32_16x16x32_bf16 v[128:131], v[116:119], a[140:143], v[128:131]\n"
      "v_fma_f32 v235, v235, v232, -v236\n"
      "v_exp_f32_e64 v235, v235\n"
      "v_mfma_f32_16x16x32_bf16 v[132:135], a[144:147], v[48:51], 0\n"
      "s_cbranch_vccnz .Lsp_qy0_9\n"
      ".Lbk_qy0_9:\n"
      "v_cvt_pk_bf16_f32 v235, v235, v235\n"
      "v_mfma_f32_16x16x32_bf16 v[132:135], a[148:151], v[52:55], v[132:135]\n"
      "buffer_load_dwordx4 v1, s[8:11], s46 offen offset:0 sc1 lds\n"
      "v_mfma_f32_16x16x32_bf16 v[132:135], a[152:155], v[56:59], v[132:135]\n"
      "v_fma_f32 v234, v234, v232, -v236\n"
      "v_exp_f32 v234, v234\n"
      "v_mfma_f32_16x16x32_bf16 v[132:135], a[156:159], v[60:63], v[132:135]\n"
      "ds_read_b128 v[56:59], v0 offset:2048\n"
      "v_cvt_pk_bf16_f32 v234, v234, v234\n"
      "v_mfma_f32_16x16x32_bf16 v[132:135], a[160:163], v[64:67], v[132:135]\n"
      "s_waitcnt lgkmcnt(1)\n"
      "ds_read_b128 v[48:51], v0 offset:0\n"
      "v_fma_f32 v131, -v226, v144, v131\n"
      "v_mfma_f32_16x16x32_bf16 v[132:135], a[164:167], v[68:71], v[132:135]\n"
      "ds_read2st64_b64 v[212:215], v4 offset0:0 offset1:1\n"
      "v_mul_f32 v131, v227, v131\n"
      "v_mfma_f32_16x16x32_bf16 v[132:135], a[168:171], v[72:75], v[132:135]\n"
      "ds_read_b128 v[64:67], v0 offset:4096\n"
      "v_fma_f32 v128, -v220, v145, v128\n"
      "v_mfma_f32_16x16x32_bf16 v[132:135], a[172:175], v[76:79], v[132:135]\n"
      "v_fma_f32 v129, -v222, v146, v129\n"
      "v_mul_f32 v129, v223, v129\n"
      "v_mfma_f32_16x16x32_bf16 v[132:135], a[176:179], v[80:83], v[132:135]\n"
      "ds_read_b128 v[60:63], v0 offset:3072\n"
      "v_cvt_pk_bf16_f32 v184, v196, v197\n"
      "v_mfma_f32_16x16x32_bf16 v[132:135], a[180:183], v[84:87], v[132:135]\n"
      "v_mul_f32 v128, v221, v128\n"
      "v_cvt_pk_bf16_f32 v185, v198, v199\n"
      "v_mfma_f32_16x16x32_bf16 v[132:135], a[184:187], v[88:91], v[132:135]\n"
      "v_and_b32 v202, v162, v234\n"
      "v_and_b32 v203, v163, v235\n"
      "v_mfma_f32_16x16x32_bf16 v[132:135], a[188:191], v[92:95], v[132:135]\n"
      "v_and_b32 v200, v160, v234\n"
      "v_fma_f32 v130, -v224, v147, v130\n"
      "v_mfma_f32_16x16x32_bf16 v[132:135], a[192:195], v[96:99], v[132:135]\n"
      "v_mul_f32 v130, v225, v130\n"
      "v_and_b32 v201, v161, v235\n"
      "v_mfma_f32_16x16x32_bf16 v[132:135], a[196:199], v[100:103], v[132:135]\n"
      "ds_read_b128 v[52:55], v0 offset:1024\n"
      "v_cvt_pk_bf16_f32 v181, v130, v131\n"
      "v_mfma_f32_16x16x32_bf16 v[132:135], a[200:203], v[104:107], v[132:135]\n"
      "ds_read_b128 v[76:79], v0 offset:7168\n"
      "v_cvt_pk_bf16_f32 v180, v128, v129\n"
      "v_mfma_f32_16x16x32_bf16 v[208:211], v[184:187], v[200:203], 0\n"
      "s_waitcnt lgkmcnt(2)\n"
      "ds_read_b128 v[92:95], v0 offset:11264\n"
      "v_add_f32 v212, v212, v214\n"
      "v_mfma_f32_16x16x32_bf16 v[204:207], v[180:183], v[200:203], 0\n"
      "ds_read_b128 v[88:91], v0 offset:10240\n"
      "v_mov_b32 v186, v184\n"
      "v_mfma_f32_16x16x32_bf16 v[132:135], a[204:207], v[108:111], v[132:135]\n"
      "ds_read_b128 v[72:75], v0 offset:6144\n"
      "ds_read_b128 v[68:71], v0 offset:5120\n"
      "v_mfma_f32_16x16x32_bf16 v[132:135], a[208:211], v[112:115], v[132:135]\n"
      "v_rcp_f32 v210, v210\n"
      "v_mfma_f32_16x16x32_bf16 v[132:135], a[212:215], v[116:119], v[132:135]\n"
      "s_sub_u32 s20, s20, 1\n"
      "s_cbranch_scc0 .Lq_loop\n"
      ".Lq_tail:\n"
      "v_mul_f32 v206, v206, v210\n"
      "s_nop 7\n"
      "v_fma_f32 v132, -v218, v148, v132\n"
      "v_fma_f32 v135, -v218, v151, v135\n"
      "v_fma_f32 v134, -v218, v150, v134\n"
      "v_cvt_pk_bf16_f32 v177, v134, v135\n"
      "v_fma_f32 v133, -v218, v149, v133\n"
      "v_cvt_pk_bf16_f32 v176, v132, v133\n"
      "v_mul_f32 v232, 0x3eae1a93, v219\n"
      "s_nop 0\n"
      "v_mfma_f32_16x16x32_bf16 v[188:191], v[172:175], v[176:179], v[152:155]\n"
      "s_waitcnt vmcnt(6)\n"
      "buffer_store_dword v13, v3, s[8:11], s52 offen sc1\n"
      "v_mul_f32 v205, v205, v210\n"
      "v_mul_f32 v209, v209, v210\n"
      "v_mul_f32 v204, v204, v210\n"
      "v_mul_f32 v208, v208, v210\n"
      "v_cvt_pk_bf16_f32 v230, v208, v209\n"
      "buffer_store_dwordx2 v[230:231], v8, s[8:11], s40 offen sc1\n"
      "v_cvt_pk_bf16_f32 v228, v204, v205\n"
      "v_mul_f32 v207, v207, v210\n"
      "v_mov_b32 v187, v185\n"
      "v_cvt_pk_bf16_f32 v229, v206, v207\n"
      "v_mov_b32 v182, v180\n"
      "buffer_store_dwordx2 v[228:229], v7, s[8:11], s40 offen sc1\n"
      "s_add_u32 s52, s52, 24576\n"
      "v_mov_b32 v183, v181\n"
      "s_add_u32 s40, s40, 24576\n"
      "s_waitcnt vmcnt(0)\n"
      "buffer_store_dword v13, v3, s[8:11], s52 offen sc1\n"
      "s_add_u32 s52, s52, 24576\n"
      "s_cmp_lg_u32 s44, 0\n"
      "s_cbranch_scc1 .Lq_done\n"
      "v_lshlrev_b32 v48, 3, v0\n"
      "s_mov_b32 s22, s23\n"
      ".Lq_up:\n"
      "s_mov_b32 s30, 128\n"
      "buffer_load_dword v49, v48, s[8:11], s30 offen sc1\n"
      "s_mov_b32 s30, 8320\n"
      "buffer_load_dword v50, v48, s[8:11], s30 offen sc1\n"
      "s_mov_b32 s30, 16512\n"
      "buffer_load_dword v51, v48, s[8:11], s30 offen sc1\n"
      "s_mov_b32 s30, 24704\n"
      "buffer_load_dword v52, v48, s[8:11], s30 offen sc1\n"
      "s_waitcnt vmcnt(0)\n"
      "v_xor_b32 v49, s21, v49\n"
      "v_xor_b32 v50, s21, v50\n"
      "v_xor_b32 v51, s21, v51\n"
      "v_xor_b32 v52, s21, v52\n"
      "v_or3_b32 v49, v49, v50, v51\n"
      "v_or_b32 v49, v49, v52\n"
      "v_cmp_ne_u32 vcc, 0, v49\n"
      "s_cbranch_vccz .Lq_ok\n"
      "s_sleep 4\n"
      "s_sub_u32 s22, s22, 1\n"
      "s_cbranch_scc0 .Lq_up\n"
      ".Lq_ok:\n"
      "v_lshrrev_b32 v48, 2, v0\n"
      "v_and_b32 v48, 0x7c, v48\n"
      "v_mov_b32 v49, s21\n"
      "buffer_store_dword v49, v48, s[8:11], 0 offen sc1\n"
      ".Lq_done:\n"
      "s_endpgm\n"
      ".Lsp_q0_1:\n"
      "s_mov_b32 s22, s23\n"
      ".Lsl_q0_1:\n"
      "buffer_load_dword v15, v2, s[8:11], s46 offen sc1\n"
      "s_waitcnt vmcnt(0)\n"
      "v_cmp_ne_u32 vcc, s21, v15\n"
      "s_cbranch_vccz .Lbk_q0_1\n"
      "s_sleep 4\n"
      "s_sub_u32 s22, s22, 1\n"
      "s_cbranch_scc0 .Lsl_q0_1\n"
      "s_mov_b32 s23, 0\n"
      "s_branch .Lbk_q0_1\n"
      ".Lsp_q1_2:\n"
      "s_mov_b32 s22, s23\n"
      ".Lsl_q1_2:\n"
      "buffer_load_dword v15, v2, s[8:11], s46 offen sc1\n"
      "s_waitcnt vmcnt(0)\n"
      "v_cmp_ne_u32 vcc, s21, v15\n"
      "s_cbranch_vccz .Lbk_q1_2\n"
      "s_sleep 4\n"
      "s_sub_u32 s22, s22, 1\n"
      "s_cbranch_scc0 .Lsl_q1_2\n"
      "s_mov_b32 s23, 0\n"
      "s_branch .Lbk_q1_2\n"
      ".Lsp_qy0_3:\n"
      "s_lshl_b32 s22, s23, 4\n"
      ".Lsl_qy0_3:\n"
      "ds_read_b32 v10, v12\n"
      "s_waitcnt lgkmcnt(0)\n"
      "v_cmp_gt_i32 vcc, v9, v10\n"
      "s_cbranch_vccz .Lbk_qy0_3\n"
      "s_sub_u32 s22, s22, 1\n"
      "s_cbranch_scc0 .Lsl_qy0_3\n"
      "s_mov_b32 s23, 0\n"
      "s_branch .Lbk_qy0_3\n"
      ".Lsp_q2_4:\n"
      "s_mov_b32 s22, s23\n"
      "buffer_load_dword v15, v2, s[8:11], s46 offen sc1\n"
      "s_waitcnt vmcnt(0)\n"
      "v_cmp_ne_u32 vcc, s21, v15\n"
      "s_add_u32 s30, s52, 0\n"
      "buffer_store_dword v13, v3, s[8:11], s30 offen sc1\n"
      "s_cbranch_vccz .Lbk_q2_4\n"
      ".Lsl_q2_4:\n"
      "buffer_load_dword v15, v2, s[8:11], s46 offen sc1\n"
      "s_waitcnt vmcnt(0)\n"
      "v_cmp_ne_u32 vcc, s21, v15\n"
      "s_cbranch_vccz .Lbk_q2_4\n"
      "s_sleep 4\n"
      "s_sub_u32 s22, s22, 1\n"
      "s_cbranch_scc0 .Lsl_q2_4\n"
      "s_mov_b32 s23, 0\n"
      "s_branch .Lbk_q2_4\n"
      ".Lsp_qy1_5:\n"
      "s_lshl_b32 s22, s23, 4\n"
      ".Lsl_qy1_5:\n"
      "ds_read_b32 v10, v12\n"
      "s_waitcnt lgkmcnt(0)\n"
      "v_cmp_gt_i32 vcc, v9, v10\n"
      "s_cbranch_vccz .Lbk_qy1_5\n"
      "s_sub_u32 s22, s22, 1\n"
      "s_cbranch_scc0 .Lsl_qy1_5\n"
      "s_mov_b32 s23, 0\n"
      "s_branch .Lbk_qy1_5\n"
      ".Lsp_q0_6:\n"
      "s_mov_b32 s22, s23\n"
      "buffer_load_dword v15, v2, s[8:11], s46 offen sc1\n"
      "s_waitcnt vmcnt(0)\n"
      "v_cmp_ne_u32 vcc, s21, v15\n"
      "s_add_u32 s30, s52, 24576\n"
      "buffer_store_dword v13, v3, s[8:11], s30 offen sc1\n"
      "s_cbranch_vccz .Lbk_q0_6\n"
      ".Lsl_q0_6:\n"
      "buffer_load_dword v15, v2, s[8:11], s46 offen sc1\n"
      "s_waitcnt vmcnt(0)\n"
      "v_cmp_ne_u32 vcc, s21, v15\n"
      "s_cbranch_vccz .Lbk_q0_6\n"
      "s_sleep 4\n"
      "s_sub_u32 s22, s22, 1\n"
      "s_cbranch_scc0 .Lsl_q0_6\n"
      "s_mov_b32 s23, 0\n"
      "s_branch .Lbk_q0_6\n"
      ".Lsp_qy2_7:\n"
      "s_lshl_b32 s22, s23, 4\n"
      ".Lsl_qy2_7:\n"
      "ds_read_b32 v10, v12\n"
      "s_waitcnt lgkmcnt(0)\n"
      "v_cmp_gt_i32 vcc, v9, v10\n"
      "s_cbranch_vccz .Lbk_qy2_7\n"
      "s_sub_u32 s22, s22, 1\n"
      "s_cbranch_scc0 .Lsl_qy2_7\n"
      "s_mov_b32 s23, 0\n"
      "s_branch .Lbk_qy2_7\n"
      ".Lsp_q1_8:\n"
      "s_mov_b32 s22, s23\n"
      "buffer_load_dword v15, v2, s[8:11], s46 offen sc1\n"
      "s_waitcnt vmcnt(0)\n"
      "v_cmp_ne_u32 vcc, s21, v15\n"
      "s_add_u32 s30, s52, 24576\n"
      "buffer_store_dword v13, v3, s[8:11], s30 offen sc1\n"
      "s_cbranch_vccz .Lbk_q1_8\n"
      ".Lsl_q1_8:\n"
      "buffer_load_dword v15, v2, s[8:11], s46 offen sc1\n"
      "s_waitcnt vmcnt(0)\n"
      "v_cmp_ne_u32 vcc, s21, v15\n"
      "s_cbranch_vccz .Lbk_q1_8\n"
      "s_sleep 4\n"
      "s_sub_u32 s22, s22, 1\n"
      "s_cbranch_scc0 .Lsl_q1_8\n"
      "s_mov_b32 s23, 0\n"
      "s_branch .Lbk_q1_8\n"
      ".Lsp_qy0_9:\n"
      "s_lshl_b32 s22, s23, 4\n"
      ".Lsl_qy0_9:\n"
      "ds_read_b32 v10, v12\n"
      "s_waitcnt lgkmcnt(0)\n"
      "v_cmp_gt_i32 vcc, v9, v10\n"
      "s_cbranch_vccz .Lbk_qy0_9\n"
      "s_sub_u32 s22, s22, 1\n"
      "s_cbranch_scc0 .Lsl_qy0_9\n"
      "s_mov_b32 s23, 0\n"
      "s_branch .Lbk_qy0_9\n"
      ".Lq_last:\n"
      :
      : "{s[8:11]}"(rsWS), "{s[12:15]}"(rsA), "{v0}"(v_l16), "{v1}"(v_l5), "{v2}"(v_fa), "{v3}"(v_ra), "{v4}"(v_st), "{v5}"(v_tw), "{v6}"(v_tr), "{v7}"(v_om), "{v8}"(v_oe), "{v10}"(v_al), "{v11}"(v_yw), "{v12}"(v_yr), "{v14}"(v_hi), "{v152}"(v_bc[0]), "{v153}"(v_bc[1]), "{v154}"(v_bc[2]), "{v155}"(v_bc[3]), "{v156}"(v_bp[0]), "{v157}"(v_bp[1]), "{v158}"(v_bp[2]), "{v159}"(v_bp[3]), "{v160}"(v_mk[0]), "{v161}"(v_mk[1]), "{v162}"(v_mk[2]), "{v163}"(v_mk[3]), "{v164}"(v_sel[0]), "{v165}"(v_sel[1]), "{v166}"(v_sel[2]), "{v167}"(v_sel[3]), "{v168}"(v_ci[0]), "{v169}"(v_ci[1]), "{v170}"(v_ci[2]), "{v171}"(v_ci[3]), "{v48}"(v_vo[0]), "{v49}"(v_vo[1]), "{v50}"(v_vo[2]), "{v51}"(v_vo[3]), "{v52}"(v_at), "{v53}"(v_ax), "{v54}"(v_pm), "{v55}"(v_ms), "{v76}"(v_pw), "{s36}"(s_sx), "{s37}"(s_ev), "{s38}"(s_del), "{s39}"(s_kil), "{s40}"(s_so), "{s41}"(s_m4), "{s42}"(s_m5), "{s43}"(s_ms), "{s44}"(s_up)
      : "v255", "a255", "s99", "vcc", "scc", "memory");

  } else if (slot == 4) {
    const unsigned gr = 3 - (xcd & 3), c = xcd >> 2, o = w & 1;
    const u32x4 rsA = srd(Wo, 576 * 576 * 2);
    const unsigned v_l5 = v_l16 + 16384 - w * 3072, e0 = 16384 + o * 2048 + g * 512 + i16 * 8, e1 = e0 + 2048 - o * 4096;
    const unsigned v_ex[8] = {e0, e1, e0 + 20480, e1 + 20480, e0 + 40960, e1 + 40960, e0 + 61440, e1 + 61440}, v_l2 = v_l16 + 40960;
    const unsigned v_fa = 20480 - w * 4096 + (8 * w + (lane >> 3)) * 128 + (lane & 7) * 4, v_ra = up * 8 * REC + l5 + 19456 + (4 * c + w) * 128;
    const unsigned v_xp = (9 * c + 2 * w) * 1024 + v_l16, v_xs = (9 * c + 8) * 1024 + (w >> 1) * 512 + half;
    const unsigned v_rp = v_xp + WS_XIN, v_rs = v_xs + WS_XIN;
    const unsigned v_ew = 82944 + (w >> 1) * 1024 + v_l16;
    const unsigned v_sw = 86016 + i16 * 144 + (4 * w + g) * 8, v_sr = 86016 + (8 * (w >> 1) + l8) * 144 + l7;
    const unsigned v_ss = 18432 + c * 512 + (w >> 1) * 256 + l8 * 32 + p * 8;
    const unsigned v_pw = 19456 + (4 * c + w) * 128 + l8 * REC + l7;
    unsigned v_vo[9], v_vp[9];
#pragma unroll
    for (unsigned j = 0; j < 9; ++j) {
      const unsigned ch = 64 * j + lane, r = ch / 36, x = 576 * c + ch % 36 * 16 - 1024 * (j & 3);
      v_vo[j] = (20 * o + r) * 1152 + x;
      v_vp[j] = (36 * (4 * o + (r >> 2)) + 16 + (r & 3)) * 1152 + x;
    }
    const unsigned ro = (8 * g + q) * 576;
    const unsigned v_t0 = ro + o * 18432 + 128 * w + 16 * p, v_t1 = v_t0 + 18432 - 2 * o * 18432, v_ts = ro + o * 18432 + 512 + 32 * (w >> 1) + 8 * p;
    const unsigned s_sx = WS_OB + gr * NREC * REC + w * 4096, s_ev = s_sx + 31 * REC, s_sy = WS_X1 + gr * NREC * REC;
    const unsigned s_m4 = w * 4096, s_m5 = 16384 + w * 1024, s_m9 = w * 9216, s_so = (w >> 1) * 8 * 41472;
    const unsigned s_s8 = s_so, s_wg = o, s_rb = WS_OB + w * 4096;
    asm volatile(
      "s_buffer_load_dword s21, s[8:11], 0\n"
      "s_mov_b32 s23, 65536\n"
      "s_mov_b32 s31, 0x40000000\n"
      "s_add_u32 m0, s41, 0\n"
      "s_nop 0\n"
      "buffer_load_dwordx4 v100, s[12:15], s42 offen offset:0 lds\n"
      "buffer_load_dwordx4 v101, s[12:15], s42 offen offset:1024 lds\n"
      "buffer_load_dwordx4 v102, s[12:15], s42 offen offset:2048 lds\n"
      "buffer_load_dwordx4 v103, s[12:15], s42 offen offset:3072 lds\n"
      "s_add_u32 m0, s41, 4096\n"
      "s_nop 0\n"
      "buffer_load_dwordx4 v104, s[12:15], s42 offen offset:0 lds\n"
      "buffer_load_dwordx4 v105, s[12:15], s42 offen offset:1024 lds\n"
      "buffer_load_dwordx4 v106, s[12:15], s42 offen offset:2048 lds\n"
      "buffer_load_dwordx4 v107, s[12:15], s42 offen offset:3072 lds\n"
      "s_add_u32 m0, s41, 8192\n"
      "s_nop 0\n"
      "buffer_load_dwordx4 v108, s[12:15], s42 offen offset:0 lds\n"
      "s_add_u32 s42, s42, 41472\n"
      "s_add_u32 m0, s41, 36864\n"
      "s_nop 0\n"
      "buffer_load_dwordx4 v100, s[12:15], s42 offen offset:0 lds\n"
      "buffer_load_dwordx4 v101, s[12:15], s42 offen offset:1024 lds\n"
      "buffer_load_dwordx4 v102, s[12:15], s42 offen offset:2048 lds\n"
      "buffer_load_dwordx4 v103, s[12:15], s42 offen offset:3072 lds\n"
      "s_add_u32 m0, s41, 40960\n"
      "s_nop 0\n"
      "buffer_load_dwordx4 v104, s[12:15], s42 offen offset:0 lds\n"
      "buffer_load_dwordx4 v105, s[12:15], s42 offen offset:1024 lds\n"
      "buffer_load_dwordx4 v106, s[12:15], s42 offen offset:2048 lds\n"
      "buffer_load_dwordx4 v107, s[12:15], s42 offen offset:3072 lds\n"
      "s_add_u32 m0, s41, 45056\n"
      "s_nop 0\n"
      "buffer_load_dwordx4 v108, s[12:15], s42 offen offset:0 lds\n"
      "s_add_u32 s42, s42, 41472\n"
      "s_add_u32 m0, s41, 73728\n"
      "s_nop 0\n"
      "buffer_load_dwordx4 v100, s[12:15], s42 offen offset:0 lds\n"
      "buffer_load_dwordx4 v101, s[12:15], s42 offen offset:1024 lds\n"
      "buffer_load_dwordx4 v102, s[12:15], s42 offen offset:2048 lds\n"
      "buffer_load_dwordx4 v103, s[12:15], s42 offen offset:3072 lds\n"
      "s_add_u32 m0, s41, 77824\n"
      "s_nop 0\n"
      "buffer_load_dwordx4 v104, s[12:15], s42 offen offset:0 lds\n"
      "buffer_load_dwordx4 v105, s[12:15], s42 offen offset:1024 lds\n"
      "buffer_load_dwordx4 v106, s[12:15], s42 offen offset:2048 lds\n"
      "buffer_load_dwordx4 v107, s[12:15], s42 offen offset:3072 lds\n"
      "s_add_u32 m0, s41, 81920\n"
      "s_nop 0\n"
      "buffer_load_dwordx4 v108, s[12:15], s42 offen offset:0 lds\n"
      "s_add_u32 s42, s42, 41472\n"
      "s_waitcnt vmcnt(18)\n"
      "s_barrier\n"
      "ds_read_b64_tr_b16 v[64:65], v120 offset:0\n"
      "ds_read_b64_tr_b16 v[66:67], v120 offset:2304\n"
      "ds_read_b64_tr_b16 a[0:1], v118 offset:0\n"
      "ds_read_b64_tr_b16 a[2:3], v118 offset:2304\n"
      "ds_read_b64_tr_b16 a[72:73], v118 offset:8\n"
      "ds_read_b64_tr_b16 a[74:75], v118 offset:2312\n"
      "ds_read_b64_tr_b16 a[144:145], v118 offset:64\n"
      "ds_read_b64_tr_b16 a[146:147], v118 offset:2368\n"
      "ds_read_b64_tr_b16 a[216:217], v118 offset:72\n"
      "ds_read_b64_tr_b16 a[218:219], v118 offset:2376\n"
      "ds_read_b64_tr_b16 a[36:37], v119 offset:0\n"
      "ds_read_b64_tr_b16 a[38:39], v119 offset:2304\n"
      "ds_read_b64_tr_b16 a[108:109], v119 offset:8\n"
      "ds_read_b64_tr_b16 a[110:111], v119 offset:2312\n"
      "ds_read_b64_tr_b16 a[180:181], v119 offset:64\n"
      "ds_read_b64_tr_b16 a[182:183], v119 offset:2368\n"
      "ds_read_b64_tr_b16 a[252:253], v119 offset:72\n"
      "ds_read_b64_tr_b16 a[254:255], v119 offset:2376\n"
      "v_add_u32 v118, 36864, v118\n"
      "v_add_u32 v119, 36864, v119\n"
      "v_add_u32 v120, 36864, v120\n"
      "s_add_u32 m0, s41, 110592\n"
      "s_nop 0\n"
      "buffer_load_dwordx4 v100, s[12:15], s42 offen offset:0 lds\n"
      "buffer_load_dwordx4 v101, s[12:15], s42 offen offset:1024 lds\n"
      "buffer_load_dwordx4 v102, s[12:15], s42 offen offset:2048 lds\n"
      "buffer_load_dwordx4 v103, s[12:15], s42 offen offset:3072 lds\n"
      "s_add_u32 m0, s41, 114688\n"
      "s_nop 0\n"
      "buffer_load_dwordx4 v104, s[12:15], s42 offen offset:0 lds\n"
      "buffer_load_dwordx4 v105, s[12:15], s42 offen offset:1024 lds\n"
      "buffer_load_dwordx4 v106, s[12:15], s42 offen offset:2048 lds\n"
      "buffer_load_dwordx4 v107, s[12:15], s42 offen offset:3072 lds\n"
      "s_add_u32 m0, s41, 118784\n"
      "s_nop 0\n"
      "buffer_load_dwordx4 v108, s[12:15], s42 offen offset:0 lds\n"
      "s_add_u32 s42, s42, 41472\n"
      "s_waitcnt vmcnt(18) lgkmcnt(0)\n"
      "s_barrier\n"
      "ds_read_b64_tr_b16 v[68:69], v120 offset:0\n"
      "ds_read_b64_tr_b16 v[70:71], v120 offset:2304\n"
      "ds_read_b64_tr_b16 a[4:5], v118 offset:0\n"
      "ds_read_b64_tr_b16 a[6:7], v118 offset:2304\n"
      "ds_read_b64_tr_b16 a[76:77], v118 offset:8\n"
      "ds_read_b64_tr_b16 a[78:79], v118 offset:2312\n"
      "ds_read_b64_tr_b16 a[148:149], v118 offset:64\n"
      "ds_read_b64_tr_b16 a[150:151], v118 offset:2368\n"
      "ds_read_b64_tr_b16 a[220:221], v118 offset:72\n"
      "ds_read_b64_tr_b16 a[222:223], v118 offset:2376\n"
      "ds_read_b64_tr_b16 a[40:41], v119 offset:0\n"
      "ds_read_b64_tr_b16 a[42:43], v119 offset:2304\n"
      "ds_read_b64_tr_b16 a[112:113], v119 offset:8\n"
      "ds_read_b64_tr_b16 a[114:115], v119 offset:2312\n"
      "ds_read_b64_tr_b16 a[184:185], v119 offset:64\n"
      "ds_read_b64_tr_b16 a[186:187], v119 offset:2368\n"
      "ds_read_b64_tr_b16 v[32:33], v119 offset:72\n"
      "ds_read_b64_tr_b16 v[34:35], v119 offset:2376\n"
      "v_add_u32 v118, 36864, v118\n"
      "v_add_u32 v119, 36864, v119\n"
      "v_add_u32 v120, 36864, v120\n"
      "s_add_u32 m0, s41, 0\n"
      "s_nop 0\n"
      "buffer_load_dwordx4 v100, s[12:15], s42 offen offset:0 lds\n"
      "buffer_load_dwordx4 v101, s[12:15], s42 offen offset:1024 lds\n"
      "buffer_load_dwordx4 v102, s[12:15], s42 offen offset:2048 lds\n"
      "buffer_load_dwordx4 v103, s[12:15], s42 offen offset:3072 lds\n"
      "s_add_u32 m0, s41, 4096\n"
      "s_nop 0\n"
      "buffer_load_dwordx4 v104, s[12:15], s42 offen offset:0 lds\n"
      "buffer_load_dwordx4 v105, s[12:15], s42 offen offset:1024 lds\n"
      "buffer_load_dwordx4 v106, s[12:15], s42 offen offset:2048 lds\n"
      "buffer_load_dwordx4 v107, s[12:15], s42 offen offset:3072 lds\n"
      "s_add_u32 m0, s41, 8192\n"
      "s_nop 0\n"
      "buffer_load_dwordx4 v108, s[12:15], s42 offen offset:0 lds\n"
      "s_add_u32 s42, s42, 41472\n"
      "s_waitcnt vmcnt(18) lgkmcnt(0)\n"
      "s_barrier\n"
      "ds_read_b64_tr_b16 v[72:73], v120 offset:0\n"
      "ds_read_b64_tr_b16 v[74:75], v120 offset:2304\n"
      "ds_read_b64_tr_b16 a[8:9], v118 offset:0\n"
      "ds_read_b64_tr_b16 a[10:11], v118 offset:2304\n"
      "ds_read_b64_tr_b16 a[80:81], v118 offset:8\n"
      "ds_read_b64_tr_b16 a[82:83], v118 offset:2312\n"
      "ds_read_b64_tr_b16 a[152:153], v118 offset:64\n"
      "ds_read_b64_tr_b16 a[154:155], v118 offset:2368\n"
      "ds_read_b64_tr_b16 a[224:225], v118 offset:72\n"
      "ds_read_b64_tr_b16 a[226:227], v118 offset:2376\n"
      "ds_read_b64_tr_b16 a[44:45], v119 offset:0\n"
      "ds_read_b64_tr_b16 a[46:47], v119 offset:2304\n"
      "ds_read_b64_tr_b16 a[116:117], v119 offset:8\n"
      "ds_read_b64_tr_b16 a[118:119], v119 offset:2312\n"
      "ds_read_b64_tr_b16 a[188:189], v119 offset:64\n"
      "ds_read_b64_tr_b16 a[190:191], v119 offset:2368\n"
      "ds_read_b64_tr_b16 v[36:37], v119 offset:72\n"
      "ds_read_b64_tr_b16 v[38:39], v119 offset:2376\n"
      "v_add_u32 v118, 36864, v118\n"
      "v_add_u32 v119, 36864, v119\n"
      "v_add_u32 v120, 36864, v120\n"
      "s_add_u32 m0, s41, 36864\n"
      "s_nop 0\n"
      "buffer_load_dwordx4 v100, s[12:15], s42 offen offset:0 lds\n"
      "buffer_load_dwordx4 v101, s[12:15], s42 offen offset:1024 lds\n"
      "buffer_load_dwordx4 v102, s[12:15], s42 offen offset:2048 lds\n"
      "buffer_load_dwordx4 v103, s[12:15], s42 offen offset:3072 lds\n"
      "s_add_u32 m0, s41, 40960\n"
      "s_nop 0\n"
      "buffer_load_dwordx4 v104, s[12:15], s42 offen offset:0 lds\n"
      "buffer_load_dwordx4 v105, s[12:15], s42 offen offset:1024 lds\n"
      "buffer_load_dwordx4 v106, s[12:15], s42 offen offset:2048 lds\n"
      "buffer_load_dwordx4 v107, s[12:15], s42 offen offset:3072 lds\n"
      "s_add_u32 m0, s41, 45056\n"
      "s_nop 0\n"
      "buffer_load_dwordx4 v108, s[12:15], s42 offen offset:0 lds\n"
      "s_add_u32 s42, s42, 41472\n"
      "s_waitcnt vmcnt(18) lgkmcnt(0)\n"
      "s_barrier\n"
      "ds_read_b64_tr_b16 v[76:77], v120 offset:0\n"
      "ds_read_b64_tr_b16 v[78:79], v120 offset:2304\n"
      "ds_read_b64_tr_b16 a[12:13], v118 offset:0\n"
      "ds_read_b64_tr_b16 a[14:15], v118 offset:2304\n"
      "ds_read_b64_tr_b16 a[84:85], v118 offset:8\n"
      "ds_read_b64_tr_b16 a[86:87], v118 offset:2312\n"
      "ds_read_b64_tr_b16 a[156:157], v118 offset:64\n"
      "ds_read_b64_tr_b16 a[158:159], v118 offset:2368\n"
      "ds_read_b64_tr_b16 a[228:229], v118 offset:72\n"
      "ds_read_b64_tr_b16 a[230:231], v118 offset:2376\n"
      "ds_read_b64_tr_b16 a[48:49], v119 offset:0\n"
      "ds_read_b64_tr_b16 a[50:51], v119 offset:2304\n"
      "ds_read_b64_tr_b16 a[120:121], v119 offset:8\n"
      "ds_read_b64_tr_b16 a[122:123], v119 offset:2312\n"
      "ds_read_b64_tr_b16 a[192:193], v119 offset:64\n"
      "ds_read_b64_tr_b16 a[194:195], v119 offset:2368\n"
      "ds_read_b64_tr_b16 v[40:41], v119 offset:72\n"
      "ds_read_b64_tr_b16 v[42:43], v119 offset:2376\n"
      "v_add_u32 v118, 4294856704, v118\n"
      "v_add_u32 v119, 4294856704, v119\n"
      "v_add_u32 v120, 4294856704, v120\n"
      "s_add_u32 m0, s41, 73728\n"
      "s_nop 0\n"
      "buffer_load_dwordx4 v100, s[12:15], s42 offen offset:0 lds\n"
      "buffer_load_dwordx4 v101, s[12:15], s42 offen offset:1024 lds\n"
      "buffer_load_dwordx4 v102, s[12:15], s42 offen offset:2048 lds\n"
      "buffer_load_dwordx4 v103, s[12:15], s42 offen offset:3072 lds\n"
      "s_add_u32 m0, s41, 77824\n"
      "s_nop 0\n"
      "buffer_load_dwordx4 v104, s[12:15], s42 offen offset:0 lds\n"
      "buffer_load_dwordx4 v105, s[12:15], s42 offen offset:1024 lds\n"
      "buffer_load_dwordx4 v106, s[12:15], s42 offen offset:2048 lds\n"
      "buffer_load_dwordx4 v107, s[12:15], s42 offen offset:3072 lds\n"
      "s_add_u32 m0, s41, 81920\n"
      "s_nop 0\n"
      "buffer_load_dwordx4 v108, s[12:15], s42 offen offset:0 lds\n"
      "s_add_u32 s42, s42, 41472\n"
      "s_waitcnt vmcnt(18) lgkmcnt(0)\n"
      "s_barrier\n"
      "ds_read_b64_tr_b16 v[80:81], v120 offset:0\n"
      "ds_read_b64_tr_b16 v[82:83], v120 offset:2304\n"
      "ds_read_b64_tr_b16 a[16:17], v118 offset:0\n"
      "ds_read_b64_tr_b16 a[18:19], v118 offset:2304\n"
      "ds_read_b64_tr_b16 a[88:89], v118 offset:8\n"
      "ds_read_b64_tr_b16 a[90:91], v118 offset:2312\n"
      "ds_read_b64_tr_b16 a[160:161], v118 offset:64\n"
      "ds_read_b64_tr_b16 a[162:163], v118 offset:2368\n"
      "ds_read_b64_tr_b16 a[232:233], v118 offset:72\n"
      "ds_read_b64_tr_b16 a[234:235], v118 offset:2376\n"
      "ds_read_b64_tr_b16 a[52:53], v119 offset:0\n"
      "ds_read_b64_tr_b16 a[54:55], v119 offset:2304\n"
      "ds_read_b64_tr_b16 a[124:125], v119 offset:8\n"
      "ds_read_b64_tr_b16 a[126:127], v119 offset:2312\n"
      "ds_read_b64_tr_b16 a[196:197], v119 offset:64\n"
      "ds_read_b64_tr_b16 a[198:199], v119 offset:2368\n"
      "ds_read_b64_tr_b16 v[44:45], v119 offset:72\n"
      "ds_read_b64_tr_b16 v[46:47], v119 offset:2376\n"
      "v_add_u32 v118, 36864, v118\n"
      "v_add_u32 v119, 36864, v119\n"
      "v_add_u32 v120, 36864, v120\n"
      "s_add_u32 m0, s41, 110592\n"
      "s_nop 0\n"
      "buffer_load_dwordx4 v100, s[12:15], s42 offen offset:0 lds\n"
      "buffer_load_dwordx4 v101, s[12:15], s42 offen offset:1024 lds\n"
      "buffer_load_dwordx4 v102, s[12:15], s42 offen offset:2048 lds\n"
      "buffer_load_dwordx4 v103, s[12:15], s42 offen offset:3072 lds\n"
      "s_add_u32 m0, s41, 114688\n"
      "s_nop 0\n"
      "buffer_load_dwordx4 v104, s[12:15], s42 offen offset:0 lds\n"
      "buffer_load_dwordx4 v105, s[12:15], s42 offen offset:1024 lds\n"
      "buffer_load_dwordx4 v106, s[12:15], s42 offen offset:2048 lds\n"
      "buffer_load_dwordx4 v107, s[12:15], s42 offen offset:3072 lds\n"
      "s_add_u32 m0, s41, 118784\n"
      "s_nop 0\n"
      "buffer_load_dwordx4 v108, s[12:15], s42 offen offset:0 lds\n"
      "s_add_u32 s42, s42, 41472\n"
      "s_waitcnt vmcnt(18) lgkmcnt(0)\n"
      "s_barrier\n"
      "ds_read_b64_tr_b16 v[84:85], v120 offset:0\n"
      "ds_read_b64_tr_b16 v[86:87], v120 offset:2304\n"
      "ds_read_b64_tr_b16 a[20:21], v118 offset:0\n"
      "ds_read_b64_tr_b16 a[22:23], v118 offset:2304\n"
      "ds_read_b64_tr_b16 a[92:93], v118 offset:8\n"
      "ds_read_b64_tr_b16 a[94:95], v118 offset:2312\n"
      "ds_read_b64_tr_b16 a[164:165], v118 offset:64\n"
      "ds_read_b64_tr_b16 a[166:167], v118 offset:2368\n"
      "ds_read_b64_tr_b16 a[236:237], v118 offset:72\n"
      "ds_read_b64_tr_b16 a[238:239], v118 offset:2376\n"
      "ds_read_b64_tr_b16 a[56:57], v119 offset:0\n"
      "ds_read_b64_tr_b16 a[58:59], v119 offset:2304\n"
      "ds_read_b64_tr_b16 a[128:129], v119 offset:8\n"
      "ds_read_b64_tr_b16 a[130:131], v119 offset:2312\n"
      "ds_read_b64_tr_b16 a[200:201], v119 offset:64\n"
      "ds_read_b64_tr_b16 a[202:203], v119 offset:2368\n"
      "ds_read_b64_tr_b16 v[48:49], v119 offset:72\n"
      "ds_read_b64_tr_b16 v[50:51], v119 offset:2376\n"
      "v_add_u32 v118, 36864, v118\n"
      "v_add_u32 v119, 36864, v119\n"
      "v_add_u32 v120, 36864, v120\n"
      "s_add_u32 m0, s41, 0\n"
      "s_nop 0\n"
      "buffer_load_dwordx4 v109, s[12:15], s43 offen offset:0 lds\n"
      "buffer_load_dwordx4 v110, s[12:15], s43 offen offset:1024 lds\n"
      "buffer_load_dwordx4 v111, s[12:15], s43 offen offset:2048 lds\n"
      "buffer_load_dwordx4 v112, s[12:15], s43 offen offset:3072 lds\n"
      "s_add_u32 m0, s41, 4096\n"
      "s_nop 0\n"
      "buffer_load_dwordx4 v113, s[12:15], s43 offen offset:0 lds\n"
      "buffer_load_dwordx4 v114, s[12:15], s43 offen offset:1024 lds\n"
      "buffer_load_dwordx4 v115, s[12:15], s43 offen offset:2048 lds\n"
      "buffer_load_dwordx4 v116, s[12:15], s43 offen offset:3072 lds\n"
      "s_add_u32 m0, s41, 8192\n"
      "s_nop 0\n"
      "buffer_load_dwordx4 v117, s[12:15], s43 offen offset:0 lds\n"
      "s_add_u32 s42, s42, 41472\n"
      "s_waitcnt vmcnt(18) lgkmcnt(0)\n"
      "s_barrier\n"
      "ds_read_b64_tr_b16 v[88:89], v120 offset:0\n"
      "ds_read_b64_tr_b16 v[90:91], v120 offset:2304\n"
      "ds_read_b64_tr_b16 a[24:25], v118 offset:0\n"
      "ds_read_b64_tr_b16 a[26:27], v118 offset:2304\n"
      "ds_read_b64_tr_b16 a[96:97], v118 offset:8\n"
      "ds_read_b64_tr_b16 a[98:99], v118 offset:2312\n"
      "ds_read_b64_tr_b16 a[168:169], v118 offset:64\n"
      "ds_read_b64_tr_b16 a[170:171], v118 offset:2368\n"
      "ds_read_b64_tr_b16 a[240:241], v118 offset:72\n"
      "ds_read_b64_tr_b16 a[242:243], v118 offset:2376\n"
      "ds_read_b64_tr_b16 a[60:61], v119 offset:0\n"
      "ds_read_b64_tr_b16 a[62:63], v119 offset:2304\n"
      "ds_read_b64_tr_b16 a[132:133], v119 offset:8\n"
      "ds_read_b64_tr_b16 a[134:135], v119 offset:2312\n"
      "ds_read_b64_tr_b16 a[204:205], v119 offset:64\n"
      "ds_read_b64_tr_b16 a[206:207], v119 offset:2368\n"
      "ds_read_b64_tr_b16 v[52:53], v119 offset:72\n"
      "ds_read_b64_tr_b16 v[54:55], v119 offset:2376\n"
      "v_add_u32 v118, 36864, v118\n"
      "v_add_u32 v119, 36864, v119\n"
      "v_add_u32 v120, 36864, v120\n"
      "s_waitcnt vmcnt(9) lgkmcnt(0)\n"
      "s_barrier\n"
      "ds_read_b64_tr_b16 v[92:93], v120 offset:0\n"
      "ds_read_b64_tr_b16 v[94:95], v120 offset:2304\n"
      "ds_read_b64_tr_b16 a[28:29], v118 offset:0\n"
      "ds_read_b64_tr_b16 a[30:31], v118 offset:2304\n"
      "ds_read_b64_tr_b16 a[100:101], v118 offset:8\n"
      "ds_read_b64_tr_b16 a[102:103], v118 offset:2312\n"
      "ds_read_b64_tr_b16 a[172:173], v118 offset:64\n"
      "ds_read_b64_tr_b16 a[174:175], v118 offset:2368\n"
      "ds_read_b64_tr_b16 a[244:245], v118 offset:72\n"
      "ds_read_b64_tr_b16 a[246:247], v118 offset:2376\n"
      "ds_read_b64_tr_b16 a[64:65], v119 offset:0\n"
      "ds_read_b64_tr_b16 a[66:67], v119 offset:2304\n"
      "ds_read_b64_tr_b16 a[136:137], v119 offset:8\n"
      "ds_read_b64_tr_b16 a[138:139], v119 offset:2312\n"
      "ds_read_b64_tr_b16 a[208:209], v119 offset:64\n"
      "ds_read_b64_tr_b16 a[210:211], v119 offset:2368\n"
      "ds_read_b64_tr_b16 v[56:57], v119 offset:72\n"
      "ds_read_b64_tr_b16 v[58:59], v119 offset:2376\n"
      "v_add_u32 v118, 4294856704, v118\n"
      "v_add_u32 v119, 4294856704, v119\n"
      "v_add_u32 v120, 4294856704, v120\n"
      "s_waitcnt vmcnt(0) lgkmcnt(0)\n"
      "s_barrier\n"
      "ds_read_b64_tr_b16 v[96:97], v120 offset:0\n"
      "ds_read_b64_tr_b16 v[98:99], v120 offset:2304\n"
      "ds_read_b64_tr_b16 a[32:33], v118 offset:0\n"
      "ds_read_b64_tr_b16 a[34:35], v118 offset:2304\n"
      "ds_read_b64_tr_b16 a[104:105], v118 offset:8\n"
      "ds_read_b64_tr_b16 a[106:107], v118 offset:2312\n"
      "ds_read_b64_tr_b16 a[176:177], v118 offset:64\n"
      "ds_read_b64_tr_b16 a[178:179], v118 offset:2368\n"
      "ds_read_b64_tr_b16 a[248:249], v118 offset:72\n"
      "ds_read_b64_tr_b16 a[250:251], v118 offset:2376\n"
      "ds_read_b64_tr_b16 a[68:69], v119 offset:0\n"
      "ds_read_b64_tr_b16 a[70:71], v119 offset:2304\n"
      "ds_read_b64_tr_b16 a[140:141], v119 offset:8\n"
      "ds_read_b64_tr_b16 a[142:143], v119 offset:2312\n"
      "ds_read_b64_tr_b16 a[212:213], v119 offset:64\n"
      "ds_read_b64_tr_b16 a[214:215], v119 offset:2368\n"
      "ds_read_b64_tr_b16 v[60:61], v119 offset:72\n"
      "ds_read_b64_tr_b16 v[62:63], v119 offset:2376\n"
      "v_add_u32 v118, 36864, v118\n"
      "v_add_u32 v119, 36864, v119\n"
      "v_add_u32 v120, 36864, v120\n"
      "s_waitcnt lgkmcnt(0)\n"
      "s_mul_i32 s21, s21, 1664525\n"
      "s_add_u32 s21, s21, 1013904223\n"
      "v_mov_b32 v25, s21\n"
      "buffer_store_dword v25, v22, s[8:11], 0 offen sc1\n"
      "v_not_b32 v124, v25\n"
      "v_not_b32 v125, v25\n"
      "v_not_b32 v126, v25\n"
      "v_not_b32 v127, v25\n"
      "s_add_u32 s30, s38, 0\n"
      "buffer_store_dwordx4 v[124:127], v121, s[8:11], s30 offen sc1\n"
      "v_xor_b32 v25, v25, v26\n"
      "v_mov_b32 v27, 0x3ae38e39\n"
      "s_barrier\n"
      "v_mov_b32 v21, 0\n"
      "ds_write_b32 v23, v21\n"
      "s_waitcnt lgkmcnt(0)\n"
      "s_barrier\n"
      "s_add_u32 s6, s36, 1572864\n"
      "s_add_u32 s36, s36, 4294942720\n"
      "s_mov_b32 s51, 24576\n"
      "s_waitcnt vmcnt(0) lgkmcnt(0)\n"
      "s_cmp_lg_u32 s44, 0\n"
      "s_cbranch_scc1 .Lv_start\n"
      "s_add_u32 s50, s38, 4294893568\n"
      "s_add_u32 s49, s38, 4294893568\n"
      "s_add_u32 s38, s38, 4294942720\n"
      "s_cmp_eq_u32 s36, s37\n"
      "s_cselect_b32 s48, 49152, s51\n"
      "s_add_u32 s36, s36, s48\n"
      "s_min_u32 s36, s36, s6\n"
      "buffer_load_dword v28, v11, s[8:11], s36 offen sc1\n"
      "s_mov_b32 s46, s36\n"
      "s_add_u32 s36, s36, 24576\n"
      "s_waitcnt vmcnt(0)\n"
      "v_cmp_ne_u32 vcc, s21, v28\n"
      "s_min_u32 s36, s36, s6\n"
      "s_cbranch_vccnz .Lsp_w0_1\n"
      ".Lbk_w0_1:\n"
      "buffer_load_dword v28, v11, s[8:11], s36 offen sc1\n"
      "s_add_u32 m0, s39, 0\n"
      "s_nop 0\n"
      "buffer_load_dwordx4 v0, s[8:11], s46 offen offset:0 sc1 lds\n"
      "buffer_load_dwordx4 v0, s[8:11], s46 offen offset:1024 sc1 lds\n"
      "buffer_load_dwordx4 v0, s[8:11], s46 offen offset:3072 sc1 lds\n"
      "buffer_load_dwordx4 v0, s[8:11], s46 offen offset:2048 sc1 lds\n"
      "s_add_u32 m0, s40, 0\n"
      "s_nop 0\n"
      "buffer_load_dwordx4 v1, s[8:11], s46 offen offset:0 sc1 lds\n"
      "s_sub_u32 s47, s46, s45\n"
      "s_mov_b32 s46, s36\n"
      "buffer_load_dwordx2 v[200:201], v14, s[8:11], s47 offen sc1\n"
      "buffer_load_dwordx4 v[192:195], v13, s[8:11], s47 offen sc1\n"
      "s_cmp_eq_u32 s36, s37\n"
      "s_cselect_b32 s48, 49152, s51\n"
      "s_add_u32 s36, s36, s48\n"
      "s_waitcnt vmcnt(7)\n"
      "v_cmp_ne_u32 vcc, s21, v28\n"
      "buffer_load_dwordx4 v[196:199], v13, s[8:11], s47 offen offset:1024 sc1\n"
      "s_add_u32 m0, s39, 20480\n"
      "s_cbranch_vccnz .Lsp_w1_2\n"
      ".Lbk_w1_2:\n"
      "s_min_u32 s36, s36, s6\n"
      "buffer_load_dword v28, v11, s[8:11], s36 offen sc1\n"
      "buffer_load_dwordx4 v0, s[8:11], s46 offen offset:0 sc1 lds\n"
      "v_add_u32 v21, 1, v21\n"
      "buffer_load_dwordx4 v0, s[8:11], s46 offen offset:1024 sc1 lds\n"
      "s_waitcnt vmcnt(6)\n"
      "ds_write_b32 v23, v21\n"
      "buffer_load_dwordx4 v0, s[8:11], s46 offen offset:2048 sc1 lds\n"
      "buffer_load_dwordx4 v0, s[8:11], s46 offen offset:3072 sc1 lds\n"
      "s_add_u32 m0, s40, 20480\n"
      "s_nop 0\n"
      "buffer_load_dwordx4 v1, s[8:11], s46 offen offset:0 sc1 lds\n"
      "ds_read_b32 v22, v24\n"
      "s_waitcnt vmcnt(7)\n"
      "v_and_b32 v175, 0xffff0000, v193\n"
      "s_waitcnt lgkmcnt(0)\n"
      "v_cmp_gt_i32 vcc, v21, v22\n"
      "v_lshlrev_b32 v178, 16, v195\n"
      "s_cbranch_vccnz .Lsp_wy0_3\n"
      ".Lbk_wy0_3:\n"
      "ds_read_b128 v[104:107], v0 offset:1024\n"
      "ds_read_b128 v[100:103], v0 offset:0\n"
      "ds_read_b128 v[108:111], v0 offset:2048\n"
      "v_and_b32 v179, 0xffff0000, v195\n"
      "v_lshlrev_b32 v190, 16, v201\n"
      "ds_read_b128 v[116:119], v0 offset:4096\n"
      "v_and_b32 v191, 0xffff0000, v201\n"
      "s_sub_u32 s47, s46, s45\n"
      "ds_read_b128 v[120:123], v0 offset:5120\n"
      "v_and_b32 v173, 0xffff0000, v192\n"
      "v_and_b32 v177, 0xffff0000, v194\n"
      "v_lshlrev_b32 v174, 16, v193\n"
      "ds_read_b128 v[112:115], v0 offset:3072\n"
      "ds_read2_b32 v[134:135], v2 offset0:64 offset1:96\n"
      "v_lshlrev_b32 v176, 16, v194\n"
      "ds_read_b128 v[124:127], v0 offset:6144\n"
      "ds_read_b128 v[144:147], v0 offset:10240\n"
      "v_and_b32 v189, 0xffff0000, v200\n"
      "ds_read_b128 v[152:155], v0 offset:12288\n"
      "v_lshlrev_b32 v172, 16, v192\n"
      "v_lshlrev_b32 v188, 16, v200\n"
      "buffer_load_dwordx2 v[200:201], v14, s[8:11], s47 offen sc1\n"
      "ds_read_b128 v[128:131], v0 offset:7168\n"
      "s_mov_b32 s20, 15\n"
      "s_waitcnt lgkmcnt(0)\n"
      "s_nop 4\n"
      ".p2align 6\n"
      ".Lw_loop:\n"
      "v_mfma_f32_16x16x32_bf16 v[188:191], v[64:67], v[100:103], v[188:191]\n"
      "ds_read2_b32 v[132:133], v2 offset0:0 offset1:32\n"
      "v_add_f32 v218, v218, v183\n"
      "v_mfma_f32_16x16x32_bf16 v[188:191], v[68:71], v[104:107], v[188:191]\n"
      "ds_read_b128 v[160:163], v0 offset:14336\n"
      "v_add_u32 v21, 1, v21\n"
      "v_mfma_f32_16x16x32_bf16 v[188:191], v[72:75], v[108:111], v[188:191]\n"
      "buffer_load_dwordx4 v[192:195], v13, s[8:11], s47 offen sc1\n"
      "v_mfma_f32_16x16x32_bf16 v[188:191], v[76:79], v[112:115], v[188:191]\n"
      "v_fmac_f32 v219, v183, v183\n"
      "v_mfma_f32_16x16x32_bf16 v[188:191], v[80:83], v[116:119], v[188:191]\n"
      "ds_read_b128 v[164:167], v0 offset:15360\n"
      "v_cvt_pk_bf16_f32 v212, v180, v181\n"
      "v_mfma_f32_16x16x32_bf16 v[188:191], v[84:87], v[120:123], v[188:191]\n"
      "s_waitcnt lgkmcnt(3)\n"
      "v_add_f32 v218, v218, v184\n"
      "v_mfma_f32_16x16x32_bf16 v[188:191], v[88:91], v[124:127], v[188:191]\n"
      "buffer_store_dwordx4 v[208:211], v15, s[8:11], s38 offen sc1\n"
      "v_mfma_f32_16x16x32_bf16 v[188:191], v[92:95], v[128:131], v[188:191]\n"
      "s_waitcnt lgkmcnt(2)\n"
      "ds_read_b128 v[140:143], v0 offset:9216\n"
      "v_add_f32 v218, v218, v185\n"
      "v_mfma_f32_16x16x32_bf16 v[188:191], v[96:99], v[132:135], v[188:191]\n"
      "ds_read2_b32 v[170:171], v3 offset0:64 offset1:96\n"
      "v_add_f32 v218, v218, v186\n"
      "v_mfma_f32_16x16x32_bf16 v[172:175], a[0:3], v[100:103], v[172:175]\n"
      "s_add_u32 s50, s50, 24576\n"
      "ds_read_b128 v[136:139], v0 offset:8192\n"
      "v_fmac_f32 v219, v184, v184\n"
      "v_mfma_f32_16x16x32_bf16 v[172:175], a[4:7], v[104:107], v[172:175]\n"
      "ds_read_b128 v[156:159], v0 offset:13312\n"
      "v_fmac_f32 v219, v185, v185\n"
      "v_mfma_f32_16x16x32_bf16 v[172:175], a[8:11], v[108:111], v[172:175]\n"
      "s_mov_b32 s46, s36\n"
      "v_fmac_f32 v219, v186, v186\n"
      "v_add_f32 v218, v218, v187\n"
      "v_mfma_f32_16x16x32_bf16 v[172:175], a[12:15], v[112:115], v[172:175]\n"
      "ds_read_b128 v[148:151], v0 offset:11264\n"
      "v_mfma_f32_16x16x32_bf16 v[172:175], a[16:19], v[116:119], v[172:175]\n"
      "ds_read2_b32 v[168:169], v3 offset0:0 offset1:32\n"
      "v_cvt_pk_bf16_f32 v215, v186, v187\n"
      "v_mfma_f32_16x16x32_bf16 v[172:175], a[20:23], v[120:123], v[172:175]\n"
      "buffer_store_dwordx2 v[216:217], v16, s[8:11], s38 offen sc1\n"
      "v_mfma_f32_16x16x32_bf16 v[172:175], a[24:27], v[124:127], v[172:175]\n"
      "s_waitcnt vmcnt(10) lgkmcnt(4)\n"
      "v_cvt_pk_bf16_f32 v214, v184, v185\n"
      "v_and_b32 v185, 0xffff0000, v198\n"
      "v_mfma_f32_16x16x32_bf16 v[172:175], a[28:31], v[128:131], v[172:175]\n"
      "v_and_b32 v183, 0xffff0000, v197\n"
      "v_fmac_f32 v219, v187, v187\n"
      "v_mfma_f32_16x16x32_bf16 v[172:175], a[32:35], v[132:135], v[172:175]\n"
      "s_waitcnt lgkmcnt(2)\n"
      "ds_write_b64 v18, v[218:219]\n"
      "v_mfma_f32_16x16x32_bf16 v[172:175], a[36:39], v[136:139], v[172:175]\n"
      "buffer_store_dword v25, v12, s[8:11], s50 offen sc1\n"
      "v_mfma_f32_16x16x32_bf16 v[172:175], a[40:43], v[140:143], v[172:175]\n"
      "v_mfma_f32_16x16x32_bf16 v[172:175], a[44:47], v[144:147], v[172:175]\n"
      "s_waitcnt lgkmcnt(1)\n"
      "ds_write_b32 v23, v21\n"
      "v_mfma_f32_16x16x32_bf16 v[172:175], a[48:51], v[148:151], v[172:175]\n"
      "s_add_u32 s36, s36, 24576\n"
      "v_lshlrev_b32 v180, 16, v196\n"
      "v_lshlrev_b32 v186, 16, v199\n"
      "v_mfma_f32_16x16x32_bf16 v[172:175], a[52:55], v[152:155], v[172:175]\n"
      "v_lshlrev_b32_e64 v182, 16, v197\n"
      "v_mfma_f32_16x16x32_bf16 v[172:175], a[56:59], v[156:159], v[172:175]\n"
      "buffer_store_dwordx4 v[212:215], v15, s[8:11], s38 offen offset:1024 sc1\n"
      "v_mfma_f32_16x16x32_bf16 v[172:175], a[60:63], v[160:163], v[172:175]\n"
      "v_lshlrev_b32_e64 v184, 16, v198\n"
      "v_mfma_f32_16x16x32_bf16 v[172:175], a[64:67], v[164:167], v[172:175]\n"
      "v_and_b32 v181, 0xffff0000, v196\n"
      "v_mfma_f32_16x16x32_bf16 v[172:175], a[68:71], v[168:171], v[172:175]\n"
      "v_and_b32 v187, 0xffff0000, v199\n"
      "v_mfma_f32_16x16x32_bf16 v[176:179], a[72:75], v[100:103], v[176:179]\n"
      "s_waitcnt vmcnt(11) lgkmcnt(0)\n"
      "v_cmp_ne_u32 vcc, s21, v28\n"
      "v_mfma_f32_16x16x32_bf16 v[176:179], a[76:79], v[104:107], v[176:179]\n"
      "v_mfma_f32_16x16x32_bf16 v[176:179], a[80:83], v[108:111], v[176:179]\n"
      "buffer_load_dwordx4 v[196:199], v13, s[8:11], s47 offen offset:1024 sc1\n"
      "v_mfma_f32_16x16x32_bf16 v[176:179], a[84:87], v[112:115], v[176:179]\n"
      "ds_read_b32 v22, v24\n"
      "v_mfma_f32_16x16x32_bf16 v[176:179], a[88:91], v[116:119], v[176:179]\n"
      "s_add_u32 s50, s50, 24576\n"
      "v_mul_f32_e64 v219, v172, v172\n"
      "s_min_u32 s36, s36, s6\n"
      "v_mfma_f32_16x16x32_bf16 v[176:179], a[92:95], v[120:123], v[176:179]\n"
      "v_mfma_f32_16x16x32_bf16 v[176:179], a[96:99], v[124:127], v[176:179]\n"
      "s_cbranch_vccnz .Lsp_w0_4\n"
      ".Lbk_w0_4:\n"
      "v_fmac_f32 v219, v173, v173\n"
      "v_mfma_f32_16x16x32_bf16 v[176:179], a[100:103], v[128:131], v[176:179]\n"
      "buffer_load_dword v28, v11, s[8:11], s36 offen sc1\n"
      "v_mfma_f32_16x16x32_bf16 v[176:179], a[104:107], v[132:135], v[176:179]\n"
      "s_waitcnt lgkmcnt(0)\n"
      "v_cmp_gt_i32 vcc, v21, v22\n"
      "s_add_u32 m0, s39, 40960\n"
      "v_mfma_f32_16x16x32_bf16 v[176:179], a[108:111], v[136:139], v[176:179]\n"
      "buffer_load_dwordx4 v0, s[8:11], s46 offen offset:0 sc1 lds\n"
      "v_mfma_f32_16x16x32_bf16 v[176:179], a[112:115], v[140:143], v[176:179]\n"
      "v_add_f32 v218, v172, v173\n"
      "v_add_f32 v218, v218, v174\n"
      "v_mfma_f32_16x16x32_bf16 v[176:179], a[116:119], v[144:147], v[176:179]\n"
      "v_add_f32 v218, v218, v175\n"
      "v_mfma_f32_16x16x32_bf16 v[176:179], a[120:123], v[148:151], v[176:179]\n"
      "s_cbranch_vccnz .Lsp_wz0_5\n"
      ".Lbk_wz0_5:\n"
      "ds_read_b128 v[204:207], v17\n"
      "v_mfma_f32_16x16x32_bf16 v[176:179], a[124:127], v[152:155], v[176:179]\n"
      "buffer_load_dwordx4 v0, s[8:11], s46 offen offset:1024 sc1 lds\n"
      "v_mfma_f32_16x16x32_bf16 v[176:179], a[128:131], v[156:159], v[176:179]\n"
      "v_mfma_f32_16x16x32_bf16 v[176:179], a[132:135], v[160:163], v[176:179]\n"
      "v_fmac_f32 v219, v174, v174\n"
      "v_add_u32 v21, 1, v21\n"
      "v_mfma_f32_16x16x32_bf16 v[176:179], a[136:139], v[164:167], v[176:179]\n"
      "v_cvt_pk_bf16_f32 v209, v174, v175\n"
      "v_fmac_f32 v219, v175, v175\n"
      "v_mfma_f32_16x16x32_bf16 v[176:179], a[140:143], v[168:171], v[176:179]\n"
      "s_waitcnt vmcnt(10)\n"
      "ds_write_b32 v23, v21\n"
      "v_mfma_f32_16x16x32_bf16 v[180:183], a[144:147], v[100:103], v[180:183]\n"
      "buffer_load_dwordx4 v0, s[8:11], s46 offen offset:3072 sc1 lds\n"
      "v_mfma_f32_16x16x32_bf16 v[180:183], a[148:151], v[104:107], v[180:183]\n"
      "s_waitcnt lgkmcnt(1)\n"
      "v_add_f32 v188, v188, v204\n"
      "v_mfma_f32_16x16x32_bf16 v[180:183], a[152:155], v[108:111], v[180:183]\n"
      "v_fmac_f32 v219, v176, v176\n"
      "v_fmac_f32 v219, v177, v177\n"
      "v_mfma_f32_16x16x32_bf16 v[180:183], a[156:159], v[112:115], v[180:183]\n"
      "v_add_f32 v189, v189, v205\n"
      "v_add_f32 v218, v218, v176\n"
      "v_mfma_f32_16x16x32_bf16 v[180:183], a[160:163], v[116:119], v[180:183]\n"
      "buffer_load_dwordx4 v0, s[8:11], s46 offen offset:2048 sc1 lds\n"
      "v_mfma_f32_16x16x32_bf16 v[180:183], a[164:167], v[120:123], v[180:183]\n"
      "v_cvt_pk_bf16_f32 v211, v178, v179\n"
      "v_add_f32 v191, v191, v207\n"
      "v_mfma_f32_16x16x32_bf16 v[180:183], a[168:171], v[124:127], v[180:183]\n"
      "v_add_f32 v218, v218, v177\n"
      "v_add_f32 v218, v218, v178\n"
      "v_mfma_f32_16x16x32_bf16 v[180:183], a[172:175], v[128:131], v[180:183]\n"
      "s_add_u32 m0, s40, 40960\n"
      "v_cvt_pk_bf16_f32 v210, v176, v177\n"
      "v_cvt_pk_bf16_f32 v208, v172, v173\n"
      "v_mfma_f32_16x16x32_bf16 v[180:183], a[176:179], v[132:135], v[180:183]\n"
      "buffer_load_dwordx4 v1, s[8:11], s46 offen offset:0 sc1 lds\n"
      "v_mfma_f32_16x16x32_bf16 v[180:183], a[180:183], v[136:139], v[180:183]\n"
      "ds_read_b32 v22, v24\n"
      "v_fmac_f32 v219, v178, v178\n"
      "v_mfma_f32_16x16x32_bf16 v[180:183], a[184:187], v[140:143], v[180:183]\n"
      "s_add_u32 s38, s38, 24576\n"
      "v_fmac_f32 v219, v179, v179\n"
      "v_cvt_pk_bf16_f32 v216, v188, v189\n"
      "v_mfma_f32_16x16x32_bf16 v[180:183], a[188:191], v[144:147], v[180:183]\n"
      "v_add_f32 v218, v218, v179\n"
      "v_fmac_f32 v219, v188, v188\n"
      "v_mfma_f32_16x16x32_bf16 v[180:183], a[192:195], v[148:151], v[180:183]\n"
      "v_add_f32 v190, v190, v206\n"
      "v_add_f32 v218, v218, v188\n"
      "v_mfma_f32_16x16x32_bf16 v[180:183], a[196:199], v[152:155], v[180:183]\n"
      "v_fmac_f32 v219, v189, v189\n"
      "v_mfma_f32_16x16x32_bf16 v[180:183], a[200:203], v[156:159], v[180:183]\n"
      "s_waitcnt lgkmcnt(0)\n"
      "v_cmp_gt_i32 vcc, v21, v22\n"
      "v_mfma_f32_16x16x32_bf16 v[180:183], a[204:207], v[160:163], v[180:183]\n"
      "v_add_f32 v218, v218, v189\n"
      "s_sub_u32 s47, s46, s45\n"
      "v_mfma_f32_16x16x32_bf16 v[180:183], a[208:211], v[164:167], v[180:183]\n"
      "v_add_f32 v218, v218, v190\n"
      "v_cvt_pk_bf16_f32 v217, v190, v191\n"
      "v_mfma_f32_16x16x32_bf16 v[180:183], a[212:215], v[168:171], v[180:183]\n"
      "v_fmac_f32 v219, v190, v190\n"
      "v_add_f32 v218, v218, v191\n"
      "v_mfma_f32_16x16x32_bf16 v[184:187], a[216:219], v[100:103], v[184:187]\n"
      "s_waitcnt vmcnt(12)\n"
      "v_fmac_f32 v219, v191, v191\n"
      "v_lshlrev_b32 v188, 16, v200\n"
      "v_mfma_f32_16x16x32_bf16 v[184:187], a[220:223], v[104:107], v[184:187]\n"
      "s_cbranch_vccnz .Lsp_wy1_6\n"
      ".Lbk_wy1_6:\n"
      "ds_read_b128 v[100:103], v0 offset:20480\n"
      "v_mfma_f32_16x16x32_bf16 v[184:187], a[224:227], v[108:111], v[184:187]\n"
      "v_lshlrev_b32 v190, 16, v201\n"
      "s_mov_b32 s46, s36\n"
      "v_mfma_f32_16x16x32_bf16 v[184:187], a[228:231], v[112:115], v[184:187]\n"
      "s_waitcnt vmcnt(11)\n"
      "ds_read_b128 v[112:115], v0 offset:23552\n"
      "v_and_b32 v173, 0xffff0000, v192\n"
      "v_mfma_f32_16x16x32_bf16 v[184:187], a[232:235], v[116:119], v[184:187]\n"
      "ds_read_b128 v[108:111], v0 offset:22528\n"
      "v_and_b32 v191, 0xffff0000, v201\n"
      "v_mfma_f32_16x16x32_bf16 v[184:187], a[236:239], v[120:123], v[184:187]\n"
      "v_and_b32 v175, 0xffff0000, v193\n"
      "v_and_b32 v179, 0xffff0000, v195\n"
      "v_mfma_f32_16x16x32_bf16 v[184:187], a[240:243], v[124:127], v[184:187]\n"
      "v_lshlrev_b32 v172, 16, v192\n"
      "v_mfma_f32_16x16x32_bf16 v[184:187], a[244:247], v[128:131], v[184:187]\n"
      "v_lshlrev_b32 v176, 16, v194\n"
      "v_add_f32 v218, v218, v180\n"
      "v_mfma_f32_16x16x32_bf16 v[184:187], a[248:251], v[132:135], v[184:187]\n"
      "ds_read2_b32 v[134:135], v4 offset0:64 offset1:96\n"
      "v_and_b32 v177, 0xffff0000, v194\n"
      "v_mfma_f32_16x16x32_bf16 v[184:187], a[252:255], v[136:139], v[184:187]\n"
      "v_and_b32 v189, 0xffff0000, v200\n"
      "v_mfma_f32_16x16x32_bf16 v[184:187], v[32:35], v[140:143], v[184:187]\n"
      "buffer_load_dwordx2 v[200:201], v14, s[8:11], s47 offen sc1\n"
      "v_mfma_f32_16x16x32_bf16 v[184:187], v[36:39], v[144:147], v[184:187]\n"
      "ds_read_b128 v[104:107], v0 offset:21504\n"
      "v_lshlrev_b32 v178, 16, v195\n"
      "v_mfma_f32_16x16x32_bf16 v[184:187], v[40:43], v[148:151], v[184:187]\n"
      "ds_read_b128 v[116:119], v0 offset:24576\n"
      "v_lshlrev_b32 v174, 16, v193\n"
      "v_mfma_f32_16x16x32_bf16 v[184:187], v[44:47], v[152:155], v[184:187]\n"
      "ds_read_b128 v[120:123], v0 offset:25600\n"
      "v_mfma_f32_16x16x32_bf16 v[184:187], v[48:51], v[156:159], v[184:187]\n"
      "ds_read2_b32 v[132:133], v4 offset0:0 offset1:32\n"
      "v_fmac_f32 v219, v180, v180\n"
      "v_mfma_f32_16x16x32_bf16 v[184:187], v[52:55], v[160:163], v[184:187]\n"
      "buffer_load_dwordx4 v[192:195], v13, s[8:11], s47 offen sc1\n"
      "v_mfma_f32_16x16x32_bf16 v[184:187], v[56:59], v[164:167], v[184:187]\n"
      "ds_read_b128 v[124:127], v0 offset:26624\n"
      "v_add_f32 v218, v218, v181\n"
      "v_mfma_f32_16x16x32_bf16 v[184:187], v[60:63], v[168:171], v[184:187]\n"
      "s_waitcnt lgkmcnt(5)\n"
      "ds_read_b128 v[128:131], v0 offset:27648\n"
      "v_add_f32 v218, v218, v182\n"
      "v_mfma_f32_16x16x32_bf16 v[188:191], v[64:67], v[100:103], v[188:191]\n"
      "s_waitcnt lgkmcnt(5)\n"
      "v_cvt_pk_bf16_f32 v212, v180, v181\n"
      "v_add_f32 v218, v218, v183\n"
      "v_mfma_f32_16x16x32_bf16 v[188:191], v[68:71], v[104:107], v[188:191]\n"
      "s_cmp_eq_u32 s36, s37\n"
      "s_cselect_b32 s48, 49152, s51\n"
      "ds_read2_b32 v[170:171], v5 offset0:64 offset1:96\n"
      "v_mfma_f32_16x16x32_bf16 v[188:191], v[72:75], v[108:111], v[188:191]\n"
      "ds_read_b128 v[148:151], v0 offset:31744\n"
      "v_fmac_f32 v219, v181, v181\n"
      "v_mfma_f32_16x16x32_bf16 v[188:191], v[76:79], v[112:115], v[188:191]\n"
      "s_waitcnt lgkmcnt(4)\n"
      "buffer_store_dwordx4 v[208:211], v15, s[8:11], s38 offen sc1\n"
      "v_mfma_f32_16x16x32_bf16 v[188:191], v[80:83], v[116:119], v[188:191]\n"
      "v_fmac_f32 v219, v182, v182\n"
      "v_add_f32 v218, v218, v184\n"
      "v_mfma_f32_16x16x32_bf16 v[188:191], v[84:87], v[120:123], v[188:191]\n"
      "s_waitcnt lgkmcnt(3)\n"
      "ds_read_b128 v[164:167], v0 offset:35840\n"
      "v_mfma_f32_16x16x32_bf16 v[188:191], v[88:91], v[124:127], v[188:191]\n"
      "s_waitcnt lgkmcnt(3)\n"
      "v_fmac_f32 v219, v183, v183\n"
      "v_cvt_pk_bf16_f32 v215, v186, v187\n"
      "v_mfma_f32_16x16x32_bf16 v[188:191], v[92:95], v[128:131], v[188:191]\n"
      "ds_read_b128 v[160:163], v0 offset:34816\n"
      "v_fmac_f32 v219, v184, v184\n"
      "v_mfma_f32_16x16x32_bf16 v[188:191], v[96:99], v[132:135], v[188:191]\n"
      "s_add_u32 s36, s36, s48\n"
      "ds_read_b128 v[152:155], v0 offset:32768\n"
      "v_mfma_f32_16x16x32_bf16 v[172:175], a[0:3], v[100:103], v[172:175]\n"
      "ds_read_b128 v[140:143], v0 offset:29696\n"
      "v_fmac_f32 v219, v185, v185\n"
      "v_mfma_f32_16x16x32_bf16 v[172:175], a[4:7], v[104:107], v[172:175]\n"
      "ds_read_b128 v[136:139], v0 offset:28672\n"
      "v_cvt_pk_bf16_f32 v213, v182, v183\n"
      "v_mfma_f32_16x16x32_bf16 v[172:175], a[8:11], v[108:111], v[172:175]\n"
      "ds_read2_b32 v[168:169], v5 offset0:0 offset1:32\n"
      "v_fmac_f32 v219, v186, v186\n"
      "v_mfma_f32_16x16x32_bf16 v[172:175], a[12:15], v[112:115], v[172:175]\n"
      "ds_read_b128 v[144:147], v0 offset:30720\n"
      "v_add_f32 v218, v218, v185\n"
      "v_mfma_f32_16x16x32_bf16 v[172:175], a[16:19], v[116:119], v[172:175]\n"
      "v_add_f32 v218, v218, v186\n"
      "v_add_f32 v218, v218, v187\n"
      "v_mfma_f32_16x16x32_bf16 v[172:175], a[20:23], v[120:123], v[172:175]\n"
      "ds_read_b128 v[156:159], v0 offset:33792\n"
      "v_cvt_pk_bf16_f32 v214, v184, v185\n"
      "v_mfma_f32_16x16x32_bf16 v[172:175], a[24:27], v[124:127], v[172:175]\n"
      "buffer_store_dwordx2 v[216:217], v16, s[8:11], s38 offen sc1\n"
      "v_mfma_f32_16x16x32_bf16 v[172:175], a[28:31], v[128:131], v[172:175]\n"
      "s_waitcnt vmcnt(10) lgkmcnt(4)\n"
      "v_fmac_f32 v219, v187, v187\n"
      "v_and_b32 v185, 0xffff0000, v198\n"
      "v_mfma_f32_16x16x32_bf16 v[172:175], a[32:35], v[132:135], v[172:175]\n"
      "s_waitcnt lgkmcnt(3)\n"
      "ds_write_b64 v18, v[218:219]\n"
      "v_mfma_f32_16x16x32_bf16 v[172:175], a[36:39], v[136:139], v[172:175]\n"
      "v_add_u32 v21, 1, v21\n"
      "v_mfma_f32_16x16x32_bf16 v[172:175], a[40:43], v[140:143], v[172:175]\n"
      "s_waitcnt lgkmcnt(2)\n"
      "buffer_store_dwordx4 v[212:215], v15, s[8:11], s38 offen offset:1024 sc1\n"
      "v_mfma_f32_16x16x32_bf16 v[172:175], a[44:47], v[144:147], v[172:175]\n"
      "ds_write_b32 v23, v21\n"
      "v_mfma_f32_16x16x32_bf16 v[172:175], a[48:51], v[148:151], v[172:175]\n"
      "v_mfma_f32_16x16x32_bf16 v[172:175], a[52:55], v[152:155], v[172:175]\n"
      "s_waitcnt lgkmcnt(2)\n"
      "v_lshlrev_b32 v186, 16, v199\n"
      "v_and_b32 v183, 0xffff0000, v197\n"
      "v_mfma_f32_16x16x32_bf16 v[172:175], a[56:59], v[156:159], v[172:175]\n"
      "v_lshlrev_b32 v182, 16, v197\n"
      "v_lshlrev_b32 v184, 16, v198\n"
      "v_mfma_f32_16x16x32_bf16 v[172:175], a[60:63], v[160:163], v[172:175]\n"
      "v_lshlrev_b32 v180, 16, v196\n"
      "v_mfma_f32_16x16x32_bf16 v[172:175], a[64:67], v[164:167], v[172:175]\n"
      "buffer_store_dword v25, v12, s[8:11], s50 offen sc1\n"
      "v_mfma_f32_16x16x32_bf16 v[172:175], a[68:71], v[168:171], v[172:175]\n"
      "v_and_b32 v181, 0xffff0000, v196\n"
      "v_mfma_f32_16x16x32_bf16 v[176:179], a[72:75], v[100:103], v[176:179]\n"
      "s_waitcnt vmcnt(11) lgkmcnt(0)\n"
      "v_and_b32 v187, 0xffff0000, v199\n"
      "v_cmp_ne_u32 vcc, s21, v28\n"
      "v_mfma_f32_16x16x32_bf16 v[176:179], a[76:79], v[104:107], v[176:179]\n"
      "buffer_load_dwordx4 v[196:199], v13, s[8:11], s47 offen offset:1024 sc1\n"
      "v_mfma_f32_16x16x32_bf16 v[176:179], a[80:83], v[108:111], v[176:179]\n"
      "ds_read_b32 v22, v24\n"
      "v_mul_f32 v219, v172, v172\n"
      "v_mfma_f32_16x16x32_bf16 v[176:179], a[84:87], v[112:115], v[176:179]\n"
      "s_add_u32 m0, s39, 61440\n"
      "v_mfma_f32_16x16x32_bf16 v[176:179], a[88:91], v[116:119], v[176:179]\n"
      "v_mfma_f32_16x16x32_bf16 v[176:179], a[92:95], v[120:123], v[176:179]\n"
      "v_add_f32 v218, v172, v173\n"
      "v_mfma_f32_16x16x32_bf16 v[176:179], a[96:99], v[124:127], v[176:179]\n"
      "s_cbranch_vccnz .Lsp_w1_7\n"
      ".Lbk_w1_7:\n"
      "s_min_u32 s36, s36, s6\n"
      "v_mfma_f32_16x16x32_bf16 v[176:179], a[100:103], v[128:131], v[176:179]\n"
      "buffer_load_dword v28, v11, s[8:11], s36 offen sc1\n"
      "v_mfma_f32_16x16x32_bf16 v[176:179], a[104:107], v[132:135], v[176:179]\n"
      "s_waitcnt lgkmcnt(0)\n"
      "v_cmp_gt_i32 vcc, v21, v22\n"
      "v_mfma_f32_16x16x32_bf16 v[176:179], a[108:111], v[136:139], v[176:179]\n"
      "buffer_load_dwordx4 v0, s[8:11], s46 offen offset:0 sc1 lds\n"
      "v_mfma_f32_16x16x32_bf16 v[176:179], a[112:115], v[140:143], v[176:179]\n"
      "v_cvt_pk_bf16_f32 v208, v172, v173\n"
      "v_fmac_f32 v219, v173, v173\n"
      "v_mfma_f32_16x16x32_bf16 v[176:179], a[116:119], v[144:147], v[176:179]\n"
      "s_cbranch_vccnz .Lsp_wz1_8\n"
      ".Lbk_wz1_8:\n"
      "v_mfma_f32_16x16x32_bf16 v[176:179], a[120:123], v[148:151], v[176:179]\n"
      "v_add_u32 v21, 1, v21\n"
      "v_mfma_f32_16x16x32_bf16 v[176:179], a[124:127], v[152:155], v[176:179]\n"
      "buffer_load_dwordx4 v0, s[8:11], s46 offen offset:1024 sc1 lds\n"
      "v_mfma_f32_16x16x32_bf16 v[176:179], a[128:131], v[156:159], v[176:179]\n"
      "ds_read_b128 v[204:207], v17\n"
      "v_fmac_f32 v219, v174, v174\n"
      "v_mfma_f32_16x16x32_bf16 v[176:179], a[132:135], v[160:163], v[176:179]\n"
      "v_mfma_f32_16x16x32_bf16 v[176:179], a[136:139], v[164:167], v[176:179]\n"
      "s_add_u32 s38, s38, 24576\n"
      "v_cvt_pk_bf16_f32 v209, v174, v175\n"
      "v_add_f32 v218, v218, v174\n"
      "v_mfma_f32_16x16x32_bf16 v[176:179], a[140:143], v[168:171], v[176:179]\n"
      "s_waitcnt vmcnt(10)\n"
      "ds_write_b32 v23, v21\n"
      "v_mfma_f32_16x16x32_bf16 v[180:183], a[144:147], v[100:103], v[180:183]\n"
      "buffer_load_dwordx4 v0, s[8:11], s46 offen offset:2048 sc1 lds\n"
      "v_mfma_f32_16x16x32_bf16 v[180:183], a[148:151], v[104:107], v[180:183]\n"
      "v_add_f32 v218, v218, v175\n"
      "v_fmac_f32 v219, v175, v175\n"
      "v_mfma_f32_16x16x32_bf16 v[180:183], a[152:155], v[108:111], v[180:183]\n"
      "v_add_f32 v218, v218, v176\n"
      "v_fmac_f32 v219, v176, v176\n"
      "v_mfma_f32_16x16x32_bf16 v[180:183], a[156:159], v[112:115], v[180:183]\n"
      "v_fmac_f32 v219, v177, v177\n"
      "v_mfma_f32_16x16x32_bf16 v[180:183], a[160:163], v[116:119], v[180:183]\n"
      "buffer_load_dwordx4 v0, s[8:11], s46 offen offset:3072 sc1 lds\n"
      "v_mfma_f32_16x16x32_bf16 v[180:183], a[164:167], v[120:123], v[180:183]\n"
      "v_add_f32 v218, v218, v177\n"
      "v_fmac_f32 v219, v178, v178\n"
      "v_mfma_f32_16x16x32_bf16 v[180:183], a[168:171], v[124:127], v[180:183]\n"
      "v_add_f32 v218, v218, v178\n"
      "v_fmac_f32 v219, v179, v179\n"
      "v_mfma_f32_16x16x32_bf16 v[180:183], a[172:175], v[128:131], v[180:183]\n"
      "s_waitcnt lgkmcnt(0)\n"
      "v_add_f32 v191, v191, v207\n"
      "s_add_u32 m0, s40, 61440\n"
      "v_mfma_f32_16x16x32_bf16 v[180:183], a[176:179], v[132:135], v[180:183]\n"
      "buffer_load_dwordx4 v1, s[8:11], s46 offen offset:0 sc1 lds\n"
      "v_mfma_f32_16x16x32_bf16 v[180:183], a[180:183], v[136:139], v[180:183]\n"
      "v_add_f32 v189, v189, v205\n"
      "v_cvt_pk_bf16_f32 v211, v178, v179\n"
      "v_mfma_f32_16x16x32_bf16 v[180:183], a[184:187], v[140:143], v[180:183]\n"
      "ds_read_b32 v22, v24\n"
      "v_cvt_pk_bf16_f32 v210, v176, v177\n"
      "v_mfma_f32_16x16x32_bf16 v[180:183], a[188:191], v[144:147], v[180:183]\n"
      "v_add_f32 v218, v218, v179\n"
      "v_add_f32 v190, v190, v206\n"
      "v_mfma_f32_16x16x32_bf16 v[180:183], a[192:195], v[148:151], v[180:183]\n"
      "s_waitcnt vmcnt(11)\n"
      "v_add_f32 v188, v188, v204\n"
      "v_and_b32 v175, 0xffff0000, v193\n"
      "v_mfma_f32_16x16x32_bf16 v[180:183], a[196:199], v[152:155], v[180:183]\n"
      "v_fmac_f32 v219, v188, v188\n"
      "v_fmac_f32 v219, v189, v189\n"
      "v_mfma_f32_16x16x32_bf16 v[180:183], a[200:203], v[156:159], v[180:183]\n"
      "v_add_f32 v218, v218, v188\n"
      "v_fmac_f32 v219, v190, v190\n"
      "v_mfma_f32_16x16x32_bf16 v[180:183], a[204:207], v[160:163], v[180:183]\n"
      "s_waitcnt lgkmcnt(0)\n"
      "v_fmac_f32 v219, v191, v191\n"
      "v_cmp_gt_i32 vcc, v21, v22\n"
      "v_mfma_f32_16x16x32_bf16 v[180:183], a[208:211], v[164:167], v[180:183]\n"
      "v_add_f32 v218, v218, v189\n"
      "v_cvt_pk_bf16_f32 v217, v190, v191\n"
      "v_mfma_f32_16x16x32_bf16 v[180:183], a[212:215], v[168:171], v[180:183]\n"
      "v_lshlrev_b32 v178, 16, v195\n"
      "v_mfma_f32_16x16x32_bf16 v[184:187], a[216:219], v[100:103], v[184:187]\n"
      "v_add_f32 v218, v218, v190\n"
      "v_add_f32 v218, v218, v191\n"
      "v_mfma_f32_16x16x32_bf16 v[184:187], a[220:223], v[104:107], v[184:187]\n"
      "s_cbranch_vccnz .Lsp_wy0_9\n"
      ".Lbk_wy0_9:\n"
      "ds_read_b128 v[104:107], v10 offset:1024\n"
      "v_mfma_f32_16x16x32_bf16 v[184:187], a[224:227], v[108:111], v[184:187]\n"
      "ds_read_b128 v[100:103], v10 offset:0\n"
      "v_fmac_f32 v219, v180, v180\n"
      "v_mfma_f32_16x16x32_bf16 v[184:187], a[228:231], v[112:115], v[184:187]\n"
      "v_cvt_pk_bf16_f32 v213, v182, v183\n"
      "v_cvt_pk_bf16_f32 v216, v188, v189\n"
      "v_mfma_f32_16x16x32_bf16 v[184:187], a[232:235], v[116:119], v[184:187]\n"
      "ds_read_b128 v[108:111], v10 offset:2048\n"
      "v_and_b32 v179, 0xffff0000, v195\n"
      "v_mfma_f32_16x16x32_bf16 v[184:187], a[236:239], v[120:123], v[184:187]\n"
      "v_fmac_f32 v219, v181, v181\n"
      "v_lshlrev_b32 v190, 16, v201\n"
      "v_mfma_f32_16x16x32_bf16 v[184:187], a[240:243], v[124:127], v[184:187]\n"
      "ds_read_b128 v[116:119], v10 offset:4096\n"
      "v_and_b32 v191, 0xffff0000, v201\n"
      "v_mfma_f32_16x16x32_bf16 v[184:187], a[244:247], v[128:131], v[184:187]\n"
      "s_sub_u32 s47, s46, s45\n"
      "ds_read_b128 v[120:123], v10 offset:5120\n"
      "v_and_b32 v173, 0xffff0000, v192\n"
      "v_mfma_f32_16x16x32_bf16 v[184:187], a[248:251], v[132:135], v[184:187]\n"
      "v_and_b32 v177, 0xffff0000, v194\n"
      "v_lshlrev_b32 v174, 16, v193\n"
      "v_mfma_f32_16x16x32_bf16 v[184:187], a[252:255], v[136:139], v[184:187]\n"
      "ds_read_b128 v[112:115], v10 offset:3072\n"
      "v_add_f32 v218, v218, v180\n"
      "v_mfma_f32_16x16x32_bf16 v[184:187], v[32:35], v[140:143], v[184:187]\n"
      "ds_read2_b32 v[134:135], v6 offset0:64 offset1:96\n"
      "v_lshlrev_b32 v176, 16, v194\n"
      "v_mfma_f32_16x16x32_bf16 v[184:187], v[36:39], v[144:147], v[184:187]\n"
      "ds_read_b128 v[124:127], v10 offset:6144\n"
      "v_add_f32 v218, v218, v181\n"
      "v_mfma_f32_16x16x32_bf16 v[184:187], v[40:43], v[148:151], v[184:187]\n"
      "ds_read_b128 v[144:147], v10 offset:10240\n"
      "v_and_b32 v189, 0xffff0000, v200\n"
      "v_mfma_f32_16x16x32_bf16 v[184:187], v[44:47], v[152:155], v[184:187]\n"
      "ds_read_b128 v[152:155], v10 offset:12288\n"
      "v_lshlrev_b32 v172, 16, v192\n"
      "v_mfma_f32_16x16x32_bf16 v[184:187], v[48:51], v[156:159], v[184:187]\n"
      "v_fmac_f32 v219, v182, v182\n"
      "v_lshlrev_b32 v188, 16, v200\n"
      "v_mfma_f32_16x16x32_bf16 v[184:187], v[52:55], v[160:163], v[184:187]\n"
      "buffer_load_dwordx2 v[200:201], v14, s[8:11], s47 offen sc1\n"
      "v_mfma_f32_16x16x32_bf16 v[184:187], v[56:59], v[164:167], v[184:187]\n"
      "s_waitcnt lgkmcnt(4)\n"
      "ds_read_b128 v[128:131], v10 offset:7168\n"
      "v_add_f32 v218, v218, v182\n"
      "v_mfma_f32_16x16x32_bf16 v[184:187], v[60:63], v[168:171], v[184:187]\n"
      "v_mfma_f32_16x16x32_bf16 v[188:191], v[64:67], v[100:103], v[188:191]\n"
      "ds_read2_b32 v[132:133], v6 offset0:0 offset1:32\n"
      "v_add_f32 v218, v218, v183\n"
      "v_mfma_f32_16x16x32_bf16 v[188:191], v[68:71], v[104:107], v[188:191]\n"
      "ds_read_b128 v[160:163], v10 offset:14336\n"
      "v_add_u32 v21, 1, v21\n"
      "v_mfma_f32_16x16x32_bf16 v[188:191], v[72:75], v[108:111], v[188:191]\n"
      "buffer_load_dwordx4 v[192:195], v13, s[8:11], s47 offen sc1\n"
      "v_mfma_f32_16x16x32_bf16 v[188:191], v[76:79], v[112:115], v[188:191]\n"
      "v_fmac_f32 v219, v183, v183\n"
      "v_mfma_f32_16x16x32_bf16 v[188:191], v[80:83], v[116:119], v[188:191]\n"
      "ds_read_b128 v[164:167], v10 offset:15360\n"
      "v_cvt_pk_bf16_f32 v212, v180, v181\n"
      "v_mfma_f32_16x16x32_bf16 v[188:191], v[84:87], v[120:123], v[188:191]\n"
      "s_waitcnt lgkmcnt(3)\n"
      "v_add_f32 v218, v218, v184\n"
      "v_mfma_f32_16x16x32_bf16 v[188:191], v[88:91], v[124:127], v[188:191]\n"
      "buffer_store_dwordx4 v[208:211], v15, s[8:11], s38 offen sc1\n"
      "v_mfma_f32_16x16x32_bf16 v[188:191], v[92:95], v[128:131], v[188:191]\n"
      "s_waitcnt lgkmcnt(2)\n"
      "ds_read_b128 v[140:143], v10 offset:9216\n"
      "v_add_f32 v218, v218, v185\n"
      "v_mfma_f32_16x16x32_bf16 v[188:191], v[96:99], v[132:135], v[188:191]\n"
      "ds_read2_b32 v[170:171], v7 offset0:64 offset1:96\n"
      "v_add_f32 v218, v218, v186\n"
      "v_mfma_f32_16x16x32_bf16 v[172:175], a[0:3], v[100:103], v[172:175]\n"
      "s_add_u32 s50, s50, 24576\n"
      "ds_read_b128 v[136:139], v10 offset:8192\n"
      "v_fmac_f32 v219, v184, v184\n"
      "v_mfma_f32_16x16x32_bf16 v[172:175], a[4:7], v[104:107], v[172:175]\n"
      "ds_read_b128 v[156:159], v10 offset:13312\n"
      "v_fmac_f32 v219, v185, v185\n"
      "v_mfma_f32_16x16x32_bf16 v[172:175], a[8:11], v[108:111], v[172:175]\n"
      "s_mov_b32 s46, s36\n"
      "v_fmac_f32 v219, v186, v186\n"
      "v_add_f32 v218, v218, v187\n"
      "v_mfma_f32_16x16x32_bf16 v[172:175], a[12:15], v[112:115], v[172:175]\n"
      "ds_read_b128 v[148:151], v10 offset:11264\n"
      "v_mfma_f32_16x16x32_bf16 v[172:175], a[16:19], v[116:119], v[172:175]\n"
      "ds_read2_b32 v[168:169], v7 offset0:0 offset1:32\n"
      "v_cvt_pk_bf16_f32 v215, v186, v187\n"
      "v_mfma_f32_16x16x32_bf16 v[172:175], a[20:23], v[120:123], v[172:175]\n"
      "buffer_store_dwordx2 v[216:217], v16, s[8:11], s38 offen sc1\n"
      "v_mfma_f32_16x16x32_bf16 v[172:175], a[24:27], v[124:127], v[172:175]\n"
      "s_waitcnt vmcnt(10) lgkmcnt(4)\n"
      "v_cvt_pk_bf16_f32 v214, v184, v185\n"
      "v_and_b32 v185, 0xffff0000, v198\n"
      "v_mfma_f32_16x16x32_bf16 v[172:175], a[28:31], v[128:131], v[172:175]\n"
      "v_and_b32 v183, 0xffff0000, v197\n"
      "v_fmac_f32 v219, v187, v187\n"
      "v_mfma_f32_16x16x32_bf16 v[172:175], a[32:35], v[132:135], v[172:175]\n"
      "s_waitcnt lgkmcnt(2)\n"
      "ds_write_b64 v18, v[218:219]\n"
      "v_mfma_f32_16x16x32_bf16 v[172:175], a[36:39], v[136:139], v[172:175]\n"
      "buffer_store_dword v25, v12, s[8:11], s50 offen sc1\n"
      "v_mfma_f32_16x16x32_bf16 v[172:175], a[40:43], v[140:143], v[172:175]\n"
      "v_mfma_f32_16x16x32_bf16 v[172:175], a[44:47], v[144:147], v[172:175]\n"
      "s_waitcnt lgkmcnt(1)\n"
      "ds_write_b32 v23, v21\n"
      "v_mfma_f32_16x16x32_bf16 v[172:175], a[48:51], v[148:151], v[172:175]\n"
      "s_add_u32 s36, s36, 24576\n"
      "v_lshlrev_b32 v180, 16, v196\n"
      "v_lshlrev_b32 v186, 16, v199\n"
      "v_mfma_f32_16x16x32_bf16 v[172:175], a[52:55], v[152:155], v[172:175]\n"
      "v_lshlrev_b32 v182, 16, v197\n"
      "v_mfma_f32_16x16x32_bf16 v[172:175], a[56:59], v[156:159], v[172:175]\n"
      "buffer_store_dwordx4 v[212:215], v15, s[8:11], s38 offen offset:1024 sc1\n"
      "v_mfma_f32_16x16x32_bf16 v[172:175], a[60:63], v[160:163], v[172:175]\n"
      "v_lshlrev_b32 v184, 16, v198\n"
      "v_mfma_f32_16x16x32_bf16 v[172:175], a[64:67], v[164:167], v[172:175]\n"
      "v_and_b32 v181, 0xffff0000, v196\n"
      "v_mfma_f32_16x16x32_bf16 v[172:175], a[68:71], v[168:171], v[172:175]\n"
      "v_and_b32 v187, 0xffff0000, v199\n"
      "v_mfma_f32_16x16x32_bf16 v[176:179], a[72:75], v[100:103], v[176:179]\n"
      "s_waitcnt vmcnt(11) lgkmcnt(0)\n"
      "v_cmp_ne_u32 vcc, s21, v28\n"
      "v_mfma_f32_16x16x32_bf16 v[176:179], a[76:79], v[104:107], v[176:179]\n"
      "v_mfma_f32_16x16x32_bf16 v[176:179], a[80:83], v[108:111], v[176:179]\n"
      "buffer_load_dwordx4 v[196:199], v13, s[8:11], s47 offen offset:1024 sc1\n"
      "v_mfma_f32_16x16x32_bf16 v[176:179], a[84:87], v[112:115], v[176:179]\n"
      "ds_read_b32 v22, v24\n"
      "v_mfma_f32_16x16x32_bf16 v[176:179], a[88:91], v[116:119], v[176:179]\n"
      "s_add_u32 s50, s50, 24576\n"
      "v_mul_f32 v219, v172, v172\n"
      "s_min_u32 s36, s36, s6\n"
      "v_mfma_f32_16x16x32_bf16 v[176:179], a[92:95], v[120:123], v[176:179]\n"
      "v_mfma_f32_16x16x32_bf16 v[176:179], a[96:99], v[124:127], v[176:179]\n"
      "s_cbranch_vccnz .Lsp_w0_10\n"
      ".Lbk_w0_10:\n"
      "v_fmac_f32 v219, v173, v173\n"
      "v_mfma_f32_16x16x32_bf16 v[176:179], a[100:103], v[128:131], v[176:179]\n"
      "buffer_load_dword v28, v11, s[8:11], s36 offen sc1\n"
      "v_mfma_f32_16x16x32_bf16 v[176:179], a[104:107], v[132:135], v[176:179]\n"
      "s_waitcnt lgkmcnt(0)\n"
      "v_cmp_gt_i32 vcc, v21, v22\n"
      "s_add_u32 m0, s39, 0\n"
      "v_mfma_f32_16x16x32_bf16 v[176:179], a[108:111], v[136:139], v[176:179]\n"
      "buffer_load_dwordx4 v0, s[8:11], s46 offen offset:0 sc1 lds\n"
      "v_mfma_f32_16x16x32_bf16 v[176:179], a[112:115], v[140:143], v[176:179]\n"
      "v_add_f32 v218, v172, v173\n"
      "v_add_f32 v218, v218, v174\n"
      "v_mfma_f32_16x16x32_bf16 v[176:179], a[116:119], v[144:147], v[176:179]\n"
      "v_add_f32 v218, v218, v175\n"
      "v_mfma_f32_16x16x32_bf16 v[176:179], a[120:123], v[148:151], v[176:179]\n"
      "s_cbranch_vccnz .Lsp_wz0_11\n"
      ".Lbk_wz0_11:\n"
      "ds_read_b128 v[204:207], v17\n"
      "v_mfma_f32_16x16x32_bf16 v[176:179], a[124:127], v[152:155], v[176:179]\n"
      "buffer_load_dwordx4 v0, s[8:11], s46 offen offset:1024 sc1 lds\n"
      "v_mfma_f32_16x16x32_bf16 v[176:179], a[128:131], v[156:159], v[176:179]\n"
      "v_mfma_f32_16x16x32_bf16 v[176:179], a[132:135], v[160:163], v[176:179]\n"
      "v_fmac_f32 v219, v174, v174\n"
      "v_add_u32 v21, 1, v21\n"
      "v_mfma_f32_16x16x32_bf16 v[176:179], a[136:139], v[164:167], v[176:179]\n"
      "v_cvt_pk_bf16_f32 v209, v174, v175\n"
      "v_fmac_f32 v219, v175, v175\n"
      "v_mfma_f32_16x16x32_bf16 v[176:179], a[140:143], v[168:171], v[176:179]\n"
      "s_waitcnt vmcnt(10)\n"
      "ds_write_b32 v23, v21\n"
      "v_mfma_f32_16x16x32_bf16 v[180:183], a[144:147], v[100:103], v[180:183]\n"
      "buffer_load_dwordx4 v0, s[8:11], s46 offen offset:3072 sc1 lds\n"
      "v_mfma_f32_16x16x32_bf16 v[180:183], a[148:151], v[104:107], v[180:183]\n"
      "s_waitcnt lgkmcnt(1)\n"
      "v_add_f32 v188, v188, v204\n"
      "v_mfma_f32_16x16x32_bf16 v[180:183], a[152:155], v[108:111], v[180:183]\n"
      "v_fmac_f32 v219, v176, v176\n"
      "v_fmac_f32 v219, v177, v177\n"
      "v_mfma_f32_16x16x32_bf16 v[180:183], a[156:159], v[112:115], v[180:183]\n"
      "v_add_f32 v189, v189, v205\n"
      "v_add_f32 v218, v218, v176\n"
      "v_mfma_f32_16x16x32_bf16 v[180:183], a[160:163], v[116:119], v[180:183]\n"
      "buffer_load_dwordx4 v0, s[8:11], s46 offen offset:2048 sc1 lds\n"
      "v_mfma_f32_16x16x32_bf16 v[180:183], a[164:167], v[120:123], v[180:183]\n"
      "v_cvt_pk_bf16_f32 v211, v178, v179\n"
      "v_add_f32 v191, v191, v207\n"
      "v_mfma_f32_16x16x32_bf16 v[180:183], a[168:171], v[124:127], v[180:183]\n"
      "v_add_f32 v218, v218, v177\n"
      "v_add_f32 v218, v218, v178\n"
      "v_mfma_f32_16x16x32_bf16 v[180:183], a[172:175], v[128:131], v[180:183]\n"
      "s_add_u32 m0, s40, 0\n"
      "v_cvt_pk_bf16_f32 v210, v176, v177\n"
      "v_cvt_pk_bf16_f32 v208, v172, v173\n"
      "v_mfma_f32_16x16x32_bf16 v[180:183], a[176:179], v[132:135], v[180:183]\n"
      "buffer_load_dwordx4 v1, s[8:11], s46 offen offset:0 sc1 lds\n"
      "v_mfma_f32_16x16x32_bf16 v[180:183], a[180:183], v[136:139], v[180:183]\n"
      "ds_read_b32 v22, v24\n"
      "v_fmac_f32 v219, v178, v178\n"
      "v_mfma_f32_16x16x32_bf16 v[180:183], a[184:187], v[140:143], v[180:183]\n"
      "s_add_u32 s38, s38, 24576\n"
      "v_fmac_f32 v219, v179, v179\n"
      "v_cvt_pk_bf16_f32 v216, v188, v189\n"
      "v_mfma_f32_16x16x32_bf16 v[180:183], a[188:191], v[144:147], v[180:183]\n"
      "v_add_f32 v218, v218, v179\n"
      "v_fmac_f32 v219, v188, v188\n"
      "v_mfma_f32_16x16x32_bf16 v[180:183], a[192:195], v[148:151], v[180:183]\n"
      "v_add_f32 v190, v190, v206\n"
      "v_add_f32 v218, v218, v188\n"
      "v_mfma_f32_16x16x32_bf16 v[180:183], a[196:199], v[152:155], v[180:183]\n"
      "v_fmac_f32 v219, v189, v189\n"
      "v_mfma_f32_16x16x32_bf16 v[180:183], a[200:203], v[156:159], v[180:183]\n"
      "s_waitcnt lgkmcnt(0)\n"
      "v_cmp_gt_i32 vcc, v21, v22\n"
      "v_mfma_f32_16x16x32_bf16 v[180:183], a[204:207], v[160:163], v[180:183]\n"
      "v_add_f32 v218, v218, v189\n"
      "s_sub_u32 s47, s46, s45\n"
      "v_mfma_f32_16x16x32_bf16 v[180:183], a[208:211], v[164:167], v[180:183]\n"
      "v_add_f32 v218, v218, v190\n"
      "v_cvt_pk_bf16_f32 v217, v190, v191\n"
      "v_mfma_f32_16x16x32_bf16 v[180:183], a[212:215], v[168:171], v[180:183]\n"
      "v_fmac_f32 v219, v190, v190\n"
      "v_add_f32 v218, v218, v191\n"
      "v_mfma_f32_16x16x32_bf16 v[184:187], a[216:219], v[100:103], v[184:187]\n"
      "s_waitcnt vmcnt(12)\n"
      "v_fmac_f32 v219, v191, v191\n"
      "v_lshlrev_b32 v188, 16, v200\n"
      "v_mfma_f32_16x16x32_bf16 v[184:187], a[220:223], v[104:107], v[184:187]\n"
      "s_cbranch_vccnz .Lsp_wy1_12\n"
      ".Lbk_wy1_12:\n"
      "ds_read_b128 v[100:103], v10 offset:20480\n"
      "v_mfma_f32_16x16x32_bf16 v[184:187], a[224:227], v[108:111], v[184:187]\n"
      "v_lshlrev_b32 v190, 16, v201\n"
      "s_mov_b32 s46, s36\n"
      "v_mfma_f32_16x16x32_bf16 v[184:187], a[228:231], v[112:115], v[184:187]\n"
      "s_waitcnt vmcnt(11)\n"
      "ds_read_b128 v[112:115], v10 offset:23552\n"
      "v_and_b32 v173, 0xffff0000, v192\n"
      "v_mfma_f32_16x16x32_bf16 v[184:187], a[232:235], v[116:119], v[184:187]\n"
      "ds_read_b128 v[108:111], v10 offset:22528\n"
      "v_and_b32 v191, 0xffff0000, v201\n"
      "v_mfma_f32_16x16x32_bf16 v[184:187], a[236:239], v[120:123], v[184:187]\n"
      "v_and_b32 v175, 0xffff0000, v193\n"
      "v_and_b32 v179, 0xffff0000, v195\n"
      "v_mfma_f32_16x16x32_bf16 v[184:187], a[240:243], v[124:127], v[184:187]\n"
      "v_lshlrev_b32 v172, 16, v192\n"
      "v_mfma_f32_16x16x32_bf16 v[184:187], a[244:247], v[128:131], v[184:187]\n"
      "v_lshlrev_b32 v176, 16, v194\n"
      "v_add_f32 v218, v218, v180\n"
      "v_mfma_f32_16x16x32_bf16 v[184:187], a[248:251], v[132:135], v[184:187]\n"
      "ds_read2_b32 v[134:135], v8 offset0:64 offset1:96\n"
      "v_and_b32 v177, 0xffff0000, v194\n"
      "v_mfma_f32_16x16x32_bf16 v[184:187], a[252:255], v[136:139], v[184:187]\n"
      "v_and_b32 v189, 0xffff0000, v200\n"
      "v_mfma_f32_16x16x32_bf16 v[184:187], v[32:35], v[140:143], v[184:187]\n"
      "buffer_load_dwordx2 v[200:201], v14, s[8:11], s47 offen sc1\n"
      "v_mfma_f32_16x16x32_bf16 v[184:187], v[36:39], v[144:147], v[184:187]\n"
      "ds_read_b128 v[104:107], v10 offset:21504\n"
      "v_lshlrev_b32 v178, 16, v195\n"
      "v_mfma_f32_16x16x32_bf16 v[184:187], v[40:43], v[148:151], v[184:187]\n"
      "ds_read_b128 v[116:119], v10 offset:24576\n"
      "v_lshlrev_b32 v174, 16, v193\n"
      "v_mfma_f32_16x16x32_bf16 v[184:187], v[44:47], v[152:155], v[184:187]\n"
      "ds_read_b128 v[120:123], v10 offset:25600\n"
      "v_mfma_f32_16x16x32_bf16 v[184:187], v[48:51], v[156:159], v[184:187]\n"
      "ds_read2_b32 v[132:133], v8 offset0:0 offset1:32\n"
      "v_fmac_f32 v219, v180, v180\n"
      "v_mfma_f32_16x16x32_bf16 v[184:187], v[52:55], v[160:163], v[184:187]\n"
      "buffer_load_dwordx4 v[192:195], v13, s[8:11], s47 offen sc1\n"
      "v_mfma_f32_16x16x32_bf16 v[184:187], v[56:59], v[164:167], v[184:187]\n"
      "ds_read_b128 v[124:127], v10 offset:26624\n"
      "v_add_f32 v218, v218, v181\n"
      "v_mfma_f32_16x16x32_bf16 v[184:187], v[60:63], v[168:171], v[184:187]\n"
      "s_waitcnt lgkmcnt(5)\n"
      "ds_read_b128 v[128:131], v10 offset:27648\n"
      "v_add_f32 v218, v218, v182\n"
      "v_mfma_f32_16x16x32_bf16 v[188:191], v[64:67], v[100:103], v[188:191]\n"
      "s_waitcnt lgkmcnt(5)\n"
      "v_cvt_pk_bf16_f32 v212, v180, v181\n"
      "v_add_f32 v218, v218, v183\n"
      "v_mfma_f32_16x16x32_bf16 v[188:191], v[68:71], v[104:107], v[188:191]\n"
      "s_cmp_eq_u32 s36, s37\n"
      "s_cselect_b32 s48, 49152, s51\n"
      "ds_read2_b32 v[170:171], v9 offset0:64 offset1:96\n"
      "v_mfma_f32_16x16x32_bf16 v[188:191], v[72:75], v[108:111], v[188:191]\n"
      "ds_read_b128 v[148:151], v10 offset:31744\n"
      "v_fmac_f32 v219, v181, v181\n"
      "v_mfma_f32_16x16x32_bf16 v[188:191], v[76:79], v[112:115], v[188:191]\n"
      "s_waitcnt lgkmcnt(4)\n"
      "buffer_store_dwordx4 v[208:211], v15, s[8:11], s38 offen sc1\n"
      "v_mfma_f32_16x16x32_bf16 v[188:191], v[80:83], v[116:119], v[188:191]\n"
      "v_fmac_f32 v219, v182, v182\n"
      "v_add_f32 v218, v218, v184\n"
      "v_mfma_f32_16x16x32_bf16 v[188:191], v[84:87], v[120:123], v[188:191]\n"
      "s_waitcnt lgkmcnt(3)\n"
      "ds_read_b128 v[164:167], v10 offset:35840\n"
      "v_mfma_f32_16x16x32_bf16 v[188:191], v[88:91], v[124:127], v[188:191]\n"
      "s_waitcnt lgkmcnt(3)\n"
      "v_fmac_f32 v219, v183, v183\n"
      "v_cvt_pk_bf16_f32 v215, v186, v187\n"
      "v_mfma_f32_16x16x32_bf16 v[188:191], v[92:95], v[128:131], v[188:191]\n"
      "ds_read_b128 v[160:163], v10 offset:34816\n"
      "v_fmac_f32 v219, v184, v184\n"
      "v_mfma_f32_16x16x32_bf16 v[188:191], v[96:99], v[132:135], v[188:191]\n"
      "s_add_u32 s36, s36, s48\n"
      "ds_read_b128 v[152:155], v10 offset:32768\n"
      "v_mfma_f32_16x16x32_bf16 v[172:175], a[0:3], v[100:103], v[172:175]\n"
      "ds_read_b128 v[140:143], v10 offset:29696\n"
      "v_fmac_f32 v219, v185, v185\n"
      "v_mfma_f32_16x16x32_bf16 v[172:175], a[4:7], v[104:107], v[172:175]\n"
      "ds_read_b128 v[136:139], v10 offset:28672\n"
      "v_cvt_pk_bf16_f32 v213, v182, v183\n"
      "v_mfma_f32_16x16x32_bf16 v[172:175], a[8:11], v[108:111], v[172:175]\n"
      "ds_read2_b32 v[168:169], v9 offset0:0 offset1:32\n"
      "v_fmac_f32 v219, v186, v186\n"
      "v_mfma_f32_16x16x32_bf16 v[172:175], a[12:15], v[112:115], v[172:175]\n"
      "ds_read_b128 v[144:147], v10 offset:30720\n"
      "v_add_f32 v218, v218, v185\n"
      "v_mfma_f32_16x16x32_bf16 v[172:175], a[16:19], v[116:119], v[172:175]\n"
      "v_add_f32 v218, v218, v186\n"
      "v_add_f32 v218, v218, v187\n"
      "v_mfma_f32_16x16x32_bf16 v[172:175], a[20:23], v[120:123], v[172:175]\n"
      "ds_read_b128 v[156:159], v10 offset:33792\n"
      "v_cvt_pk_bf16_f32 v214, v184, v185\n"
      "v_mfma_f32_16x16x32_bf16 v[172:175], a[24:27], v[124:127], v[172:175]\n"
      "buffer_store_dwordx2 v[216:217], v16, s[8:11], s38 offen sc1\n"
      "v_mfma_f32_16x16x32_bf16 v[172:175], a[28:31], v[128:131], v[172:175]\n"
      "s_waitcnt vmcnt(10) lgkmcnt(4)\n"
      "v_fmac_f32 v219, v187, v187\n"
      "v_and_b32 v185, 0xffff0000, v198\n"
      "v_mfma_f32_16x16x32_bf16 v[172:175], a[32:35], v[132:135], v[172:175]\n"
      "s_waitcnt lgkmcnt(3)\n"
      "ds_write_b64 v18, v[218:219]\n"
      "v_mfma_f32_16x16x32_bf16 v[172:175], a[36:39], v[136:139], v[172:175]\n"
      "v_add_u32 v21, 1, v21\n"
      "v_mfma_f32_16x16x32_bf16 v[172:175], a[40:43], v[140:143], v[172:175]\n"
      "s_waitcnt lgkmcnt(2)\n"
      "buffer_store_dwordx4 v[212:215], v15, s[8:11], s38 offen offset:1024 sc1\n"
      "v_mfma_f32_16x16x32_bf16 v[172:175], a[44:47], v[144:147], v[172:175]\n"
      "ds_write_b32 v23, v21\n"
      "v_mfma_f32_16x16x32_bf16 v[172:175], a[48:51], v[148:151], v[172:175]\n"
      "v_mfma_f32_16x16x32_bf16 v[172:175], a[52:55], v[152:155], v[172:175]\n"
      "s_waitcnt lgkmcnt(2)\n"
      "v_lshlrev_b32 v186, 16, v199\n"
      "v_and_b32 v183, 0xffff0000, v197\n"
      "v_mfma_f32_16x16x32_bf16 v[172:175], a[56:59], v[156:159], v[172:175]\n"
      "v_lshlrev_b32 v182, 16, v197\n"
      "v_lshlrev_b32 v184, 16, v198\n"
      "v_mfma_f32_16x16x32_bf16 v[172:175], a[60:63], v[160:163], v[172:175]\n"
      "v_lshlrev_b32 v180, 16, v196\n"
      "v_mfma_f32_16x16x32_bf16 v[172:175], a[64:67], v[164:167], v[172:175]\n"
      "buffer_store_dword v25, v12, s[8:11], s50 offen sc1\n"
      "v_mfma_f32_16x16x32_bf16 v[172:175], a[68:71], v[168:171], v[172:175]\n"
      "v_and_b32 v181, 0xffff0000, v196\n"
      "v_mfma_f32_16x16x32_bf16 v[176:179], a[72:75], v[100:103], v[176:179]\n"
      "s_waitcnt vmcnt(11) lgkmcnt(0)\n"
      "v_and_b32 v187, 0xffff0000, v199\n"
      "v_cmp_ne_u32 vcc, s21, v28\n"
      "v_mfma_f32_16x16x32_bf16 v[176:179], a[76:79], v[104:107], v[176:179]\n"
      "buffer_load_dwordx4 v[196:199], v13, s[8:11], s47 offen offset:1024 sc1\n"
      "v_mfma_f32_16x16x32_bf16 v[176:179], a[80:83], v[108:111], v[176:179]\n"
      "ds_read_b32 v22, v24\n"
      "v_mul_f32 v219, v172, v172\n"
      "v_mfma_f32_16x16x32_bf16 v[176:179], a[84:87], v[112:115], v[176:179]\n"
      "s_add_u32 m0, s39, 20480\n"
      "v_mfma_f32_16x16x32_bf16 v[176:179], a[88:91], v[116:119], v[176:179]\n"
      "v_mfma_f32_16x16x32_bf16 v[176:179], a[92:95], v[120:123], v[176:179]\n"
      "v_add_f32 v218, v172, v173\n"
      "v_mfma_f32_16x16x32_bf16 v[176:179], a[96:99], v[124:127], v[176:179]\n"
      "s_cbranch_vccnz .Lsp_w1_13\n"
      ".Lbk_w1_13:\n"
      "s_min_u32 s36, s36, s6\n"
      "v_mfma_f32_16x16x32_bf16 v[176:179], a[100:103], v[128:131], v[176:179]\n"
      "buffer_load_dword v28, v11, s[8:11], s36 offen sc1\n"
      "v_mfma_f32_16x16x32_bf16 v[176:179], a[104:107], v[132:135], v[176:179]\n"
      "s_waitcnt lgkmcnt(0)\n"
      "v_cmp_gt_i32 vcc, v21, v22\n"
      "v_mfma_f32_16x16x32_bf16 v[176:179], a[108:111], v[136:139], v[176:179]\n"
      "buffer_load_dwordx4 v0, s[8:11], s46 offen offset:0 sc1 lds\n"
      "v_mfma_f32_16x16x32_bf16 v[176:179], a[112:115], v[140:143], v[176:179]\n"
      "v_cvt_pk_bf16_f32 v208, v172, v173\n"
      "v_fmac_f32 v219, v173, v173\n"
      "v_mfma_f32_16x16x32_bf16 v[176:179], a[116:119], v[144:147], v[176:179]\n"
      "s_cbranch_vccnz .Lsp_wz1_14\n"
      ".Lbk_wz1_14:\n"
      "v_mfma_f32_16x16x32_bf16 v[176:179], a[120:123], v[148:151], v[176:179]\n"
      "v_add_u32 v21, 1, v21\n"
      "v_mfma_f32_16x16x32_bf16 v[176:179], a[124:127], v[152:155], v[176:179]\n"
      "buffer_load_dwordx4 v0, s[8:11], s46 offen offset:1024 sc1 lds\n"
      "v_mfma_f32_16x16x32_bf16 v[176:179], a[128:131], v[156:159], v[176:179]\n"
      "ds_read_b128 v[204:207], v17\n"
      "v_fmac_f32 v219, v174, v174\n"
      "v_mfma_f32_16x16x32_bf16 v[176:179], a[132:135], v[160:163], v[176:179]\n"
      "v_mfma_f32_16x16x32_bf16 v[176:179], a[136:139], v[164:167], v[176:179]\n"
      "s_add_u32 s38, s38, 24576\n"
      "v_cvt_pk_bf16_f32 v209, v174, v175\n"
      "v_add_f32 v218, v218, v174\n"
      "v_mfma_f32_16x16x32_bf16 v[176:179], a[140:143], v[168:171], v[176:179]\n"
      "s_waitcnt vmcnt(10)\n"
      "ds_write_b32 v23, v21\n"
      "v_mfma_f32_16x16x32_bf16 v[180:183], a[144:147], v[100:103], v[180:183]\n"
      "buffer_load_dwordx4 v0, s[8:11], s46 offen offset:2048 sc1 lds\n"
      "v_mfma_f32_16x16x32_bf16 v[180:183], a[148:151], v[104:107], v[180:183]\n"
      "v_add_f32 v218, v218, v175\n"
      "v_fmac_f32 v219, v175, v175\n"
      "v_mfma_f32_16x16x32_bf16 v[180:183], a[152:155], v[108:111], v[180:183]\n"
      "v_add_f32 v218, v218, v176\n"
      "v_fmac_f32 v219, v176, v176\n"
      "v_mfma_f32_16x16x32_bf16 v[180:183], a[156:159], v[112:115], v[180:183]\n"
      "v_fmac_f32 v219, v177, v177\n"
      "v_mfma_f32_16x16x32_bf16 v[180:183], a[160:163], v[116:119], v[180:183]\n"
      "buffer_load_dwordx4 v0, s[8:11], s46 offen offset:3072 sc1 lds\n"
      "v_mfma_f32_16x16x32_bf16 v[180:183], a[164:167], v[120:123], v[180:183]\n"
      "v_add_f32 v218, v218, v177\n"
      "v_fmac_f32 v219, v178, v178\n"
      "v_mfma_f32_16x16x32_bf16 v[180:183], a[168:171], v[124:127], v[180:183]\n"
      "v_add_f32 v218, v218, v178\n"
      "v_fmac_f32 v219, v179, v179\n"
      "v_mfma_f32_16x16x32_bf16 v[180:183], a[172:175], v[128:131], v[180:183]\n"
      "s_waitcnt lgkmcnt(0)\n"
      "v_add_f32 v191, v191, v207\n"
      "s_add_u32 m0, s40, 20480\n"
      "v_mfma_f32_16x16x32_bf16 v[180:183], a[176:179], v[132:135], v[180:183]\n"
      "buffer_load_dwordx4 v1, s[8:11], s46 offen offset:0 sc1 lds\n"
      "v_mfma_f32_16x16x32_bf16 v[180:183], a[180:183], v[136:139], v[180:183]\n"
      "v_add_f32 v189, v189, v205\n"
      "v_cvt_pk_bf16_f32 v211, v178, v179\n"
      "v_mfma_f32_16x16x32_bf16 v[180:183], a[184:187], v[140:143], v[180:183]\n"
      "ds_read_b32 v22, v24\n"
      "v_cvt_pk_bf16_f32 v210, v176, v177\n"
      "v_mfma_f32_16x16x32_bf16 v[180:183], a[188:191], v[144:147], v[180:183]\n"
      "v_add_f32 v218, v218, v179\n"
      "v_add_f32 v190, v190, v206\n"
      "v_mfma_f32_16x16x32_bf16 v[180:183], a[192:195], v[148:151], v[180:183]\n"
      "s_waitcnt vmcnt(11)\n"
      "v_add_f32 v188, v188, v204\n"
      "v_and_b32 v175, 0xffff0000, v193\n"
      "v_mfma_f32_16x16x32_bf16 v[180:183], a[196:199], v[152:155], v[180:183]\n"
      "v_fmac_f32 v219, v188, v188\n"
      "v_fmac_f32 v219, v189, v189\n"
      "v_mfma_f32_16x16x32_bf16 v[180:183], a[200:203], v[156:159], v[180:183]\n"
      "v_add_f32 v218, v218, v188\n"
      "v_fmac_f32 v219, v190, v190\n"
      "v_mfma_f32_16x16x32_bf16 v[180:183], a[204:207], v[160:163], v[180:183]\n"
      "s_waitcnt lgkmcnt(0)\n"
      "v_fmac_f32 v219, v191, v191\n"
      "v_cmp_gt_i32 vcc, v21, v22\n"
      "v_mfma_f32_16x16x32_bf16 v[180:183], a[208:211], v[164:167], v[180:183]\n"
      "v_add_f32 v218, v218, v189\n"
      "v_cvt_pk_bf16_f32 v217, v190, v191\n"
      "v_mfma_f32_16x16x32_bf16 v[180:183], a[212:215], v[168:171], v[180:183]\n"
      "v_lshlrev_b32 v178, 16, v195\n"
      "v_mfma_f32_16x16x32_bf16 v[184:187], a[216:219], v[100:103], v[184:187]\n"
      "v_add_f32 v218, v218, v190\n"
      "v_add_f32 v218, v218, v191\n"
      "v_mfma_f32_16x16x32_bf16 v[184:187], a[220:223], v[104:107], v[184:187]\n"
      "s_cbranch_vccnz .Lsp_wy0_15\n"
      ".Lbk_wy0_15:\n"
      "ds_read_b128 v[104:107], v0 offset:1024\n"
      "v_mfma_f32_16x16x32_bf16 v[184:187], a[224:227], v[108:111], v[184:187]\n"
      "ds_read_b128 v[100:103], v0 offset:0\n"
      "v_fmac_f32 v219, v180, v180\n"
      "v_mfma_f32_16x16x32_bf16 v[184:187], a[228:231], v[112:115], v[184:187]\n"
      "v_cvt_pk_bf16_f32 v213, v182, v183\n"
      "v_cvt_pk_bf16_f32 v216, v188, v189\n"
      "v_mfma_f32_16x16x32_bf16 v[184:187], a[232:235], v[116:119], v[184:187]\n"
      "ds_read_b128 v[108:111], v0 offset:2048\n"
      "v_and_b32 v179, 0xffff0000, v195\n"
      "v_mfma_f32_16x16x32_bf16 v[184:187], a[236:239], v[120:123], v[184:187]\n"
      "v_fmac_f32 v219, v181, v181\n"
      "v_lshlrev_b32 v190, 16, v201\n"
      "v_mfma_f32_16x16x32_bf16 v[184:187], a[240:243], v[124:127], v[184:187]\n"
      "ds_read_b128 v[116:119], v0 offset:4096\n"
      "v_and_b32 v191, 0xffff0000, v201\n"
      "v_mfma_f32_16x16x32_bf16 v[184:187], a[244:247], v[128:131], v[184:187]\n"
      "s_sub_u32 s47, s46, s45\n"
      "ds_read_b128 v[120:123], v0 offset:5120\n"
      "v_and_b32 v173, 0xffff0000, v192\n"
      "v_mfma_f32_16x16x32_bf16 v[184:187], a[248:251], v[132:135], v[184:187]\n"
      "v_and_b32 v177, 0xffff0000, v194\n"
      "v_lshlrev_b32 v174, 16, v193\n"
      "v_mfma_f32_16x16x32_bf16 v[184:187], a[252:255], v[136:139], v[184:187]\n"
      "ds_read_b128 v[112:115], v0 offset:3072\n"
      "v_add_f32 v218, v218, v180\n"
      "v_mfma_f32_16x16x32_bf16 v[184:187], v[32:35], v[140:143], v[184:187]\n"
      "ds_read2_b32 v[134:135], v2 offset0:64 offset1:96\n"
      "v_lshlrev_b32 v176, 16, v194\n"
      "v_mfma_f32_16x16x32_bf16 v[184:187], v[36:39], v[144:147], v[184:187]\n"
      "ds_read_b128 v[124:127], v0 offset:6144\n"
      "v_add_f32 v218, v218, v181\n"
      "v_mfma_f32_16x16x32_bf16 v[184:187], v[40:43], v[148:151], v[184:187]\n"
      "ds_read_b128 v[144:147], v0 offset:10240\n"
      "v_and_b32 v189, 0xffff0000, v200\n"
      "v_mfma_f32_16x16x32_bf16 v[184:187], v[44:47], v[152:155], v[184:187]\n"
      "ds_read_b128 v[152:155], v0 offset:12288\n"
      "v_lshlrev_b32 v172, 16, v192\n"
      "v_mfma_f32_16x16x32_bf16 v[184:187], v[48:51], v[156:159], v[184:187]\n"
      "v_fmac_f32 v219, v182, v182\n"
      "v_lshlrev_b32 v188, 16, v200\n"
      "v_mfma_f32_16x16x32_bf16 v[184:187], v[52:55], v[160:163], v[184:187]\n"
      "buffer_load_dwordx2 v[200:201], v14, s[8:11], s47 offen sc1\n"
      "v_mfma_f32_16x16x32_bf16 v[184:187], v[56:59], v[164:167], v[184:187]\n"
      "s_waitcnt lgkmcnt(4)\n"
      "ds_read_b128 v[128:131], v0 offset:7168\n"
      "v_add_f32 v218, v218, v182\n"
      "v_mfma_f32_16x16x32_bf16 v[184:187], v[60:63], v[168:171], v[184:187]\n"
      "s_sub_u32 s20, s20, 1\n"
      "s_cbranch_scc0 .Lw_loop\n"
      ".Lw_tail:\n"
      "v_add_f32 v218, v218, v183\n"
      "v_fmac_f32 v219, v183, v183\n"
      "v_cvt_pk_bf16_f32 v212, v180, v181\n"
      "s_nop 5\n"
      "v_add_f32 v218, v218, v184\n"
      "buffer_store_dwordx4 v[208:211], v15, s[8:11], s38 offen sc1\n"
      "v_add_f32 v218, v218, v185\n"
      "v_add_f32 v218, v218, v186\n"
      "s_add_u32 s50, s50, 24576\n"
      "v_fmac_f32 v219, v184, v184\n"
      "v_fmac_f32 v219, v185, v185\n"
      "v_fmac_f32 v219, v186, v186\n"
      "v_add_f32 v218, v218, v187\n"
      "v_cvt_pk_bf16_f32 v215, v186, v187\n"
      "buffer_store_dwordx2 v[216:217], v16, s[8:11], s38 offen sc1\n"
      "v_cvt_pk_bf16_f32 v214, v184, v185\n"
      "v_fmac_f32 v219, v187, v187\n"
      "ds_write_b64 v18, v[218:219]\n"
      "s_waitcnt lgkmcnt(0)\n"
      "s_barrier\n"
      "s_waitcnt vmcnt(11)\n"
      "buffer_store_dword v25, v12, s[8:11], s50 offen sc1\n"
      "buffer_store_dwordx4 v[212:215], v15, s[8:11], s38 offen offset:1024 sc1\n"
      "s_add_u32 s50, s50, 24576\n"
      "s_add_u32 s38, s38, 24576\n"
      "s_waitcnt vmcnt(0)\n"
      "buffer_store_dword v25, v12, s[8:11], s50 offen sc1\n"
      "s_add_u32 s50, s50, 24576\n"
      "s_endpgm\n"
      ".Lv_start:\n"
      "s_add_u32 s50, s38, 4294893568\n"
      "s_add_u32 s49, s38, 4294942720\n"
      "s_add_u32 s38, s38, 4294942720\n"
      "s_cmp_eq_u32 s36, s37\n"
      "s_cselect_b32 s48, 49152, s51\n"
      "s_add_u32 s36, s36, s48\n"
      "s_min_u32 s36, s36, s6\n"
      "buffer_load_dword v28, v11, s[8:11], s36 offen sc1\n"
      "s_mov_b32 s46, s36\n"
      "s_add_u32 s36, s36, 24576\n"
      "s_add_u32 m0, s39, 0\n"
      "s_min_u32 s36, s36, s6\n"
      "s_waitcnt vmcnt(0)\n"
      "v_cmp_ne_u32 vcc, s21, v28\n"
      "s_cbranch_vccnz .Lsp_v0_16\n"
      ".Lbk_v0_16:\n"
      "buffer_load_dword v28, v11, s[8:11], s36 offen sc1\n"
      "buffer_load_dwordx4 v0, s[8:11], s46 offen offset:1024 sc1 lds\n"
      "buffer_load_dwordx4 v0, s[8:11], s46 offen offset:0 sc1 lds\n"
      "buffer_load_dwordx4 v0, s[8:11], s46 offen offset:3072 sc1 lds\n"
      "buffer_load_dwordx4 v0, s[8:11], s46 offen offset:2048 sc1 lds\n"
      "s_add_u32 m0, s40, 0\n"
      "s_nop 0\n"
      "buffer_load_dwordx4 v1, s[8:11], s46 offen offset:0 sc1 lds\n"
      "s_sub_u32 s47, s46, s45\n"
      "s_mov_b32 s46, s36\n"
      "buffer_load_dwordx4 v[192:195], v13, s[8:11], s47 offen sc1\n"
      "s_add_u32 m0, s39, 20480\n"
      "buffer_load_dword v26, v23, s[8:11], s31 offen\n"
      "s_cmp_eq_u32 s36, s37\n"
      "s_cselect_b32 s48, 49152, s51\n"
      "buffer_load_dwordx4 v[196:199], v13, s[8:11], s47 offen offset:1024 sc1\n"
      "s_waitcnt vmcnt(8)\n"
      "v_cmp_ne_u32 vcc, s21, v28\n"
      "s_add_u32 s36, s36, s48\n"
      "s_min_u32 s36, s36, s6\n"
      "s_cbranch_vccnz .Lsp_v1_17\n"
      ".Lbk_v1_17:\n"
      "buffer_load_dword v28, v11, s[8:11], s36 offen sc1\n"
      "buffer_load_dwordx4 v0, s[8:11], s46 offen offset:0 sc1 lds\n"
      "s_sub_u32 s47, s46, s45\n"
      "buffer_load_dwordx4 v0, s[8:11], s46 offen offset:1024 sc1 lds\n"
      "v_add_u32 v21, 1, v21\n"
      "s_waitcnt vmcnt(6)\n"
      "ds_write_b32 v23, v21\n"
      "buffer_load_dwordx4 v0, s[8:11], s46 offen offset:3072 sc1 lds\n"
      "buffer_load_dwordx4 v0, s[8:11], s46 offen offset:2048 sc1 lds\n"
      "s_add_u32 m0, s40, 20480\n"
      "s_nop 0\n"
      "buffer_load_dwordx4 v1, s[8:11], s46 offen offset:0 sc1 lds\n"
      "buffer_load_dword v26, v23, s[8:11], s31 offen\n"
      "ds_read_b32 v22, v24\n"
      "buffer_load_dword v26, v23, s[8:11], s31 offen\n"
      "s_waitcnt vmcnt(10)\n"
      "v_and_b32 v177, 0xffff0000, v194\n"
      "s_waitcnt lgkmcnt(0)\n"
      "v_cmp_gt_i32 vcc, v21, v22\n"
      "v_and_b32 v173, 0xffff0000, v192\n"
      "s_cbranch_vccnz .Lsp_vy0_18\n"
      ".Lbk_vy0_18:\n"
      "ds_read_b128 v[104:107], v0 offset:9216\n"
      "ds_read_b128 v[100:103], v0 offset:8192\n"
      "v_lshlrev_b32 v172, 16, v192\n"
      "s_mov_b32 s46, s36\n"
      "ds_read_b128 v[116:119], v0 offset:12288\n"
      "v_and_b32 v179, 0xffff0000, v195\n"
      "v_and_b32 v175, 0xffff0000, v193\n"
      "ds_read_b128 v[112:115], v0 offset:11264\n"
      "v_lshlrev_b32 v174, 16, v193\n"
      "ds_read_b128 v[120:123], v0 offset:13312\n"
      "ds_read_b128 v[124:127], v0 offset:14336\n"
      "ds_read_b128 v[128:131], v0 offset:15360\n"
      "buffer_load_dword v26, v23, s[8:11], s31 offen\n"
      "ds_read_b128 v[108:111], v0 offset:10240\n"
      "ds_read2_b32 v[132:133], v2 offset0:0 offset1:32\n"
      "ds_read_b128 v[144:147], v0 offset:2048\n"
      "v_lshlrev_b32 v178, 16, v195\n"
      "ds_read2_b32 v[134:135], v2 offset0:64 offset1:96\n"
      "ds_read_b128 v[152:155], v0 offset:4096\n"
      "s_mov_b32 s20, 15\n"
      "s_waitcnt lgkmcnt(0)\n"
      "s_nop 4\n"
      ".p2align 6\n"
      ".Lv_loop:\n"
      "v_mfma_f32_16x16x32_bf16 v[188:191], v[64:67], v[100:103], 0\n"
      "ds_read_b128 v[148:151], v0 offset:3072\n"
      "v_mfma_f32_16x16x32_bf16 v[188:191], v[68:71], v[104:107], v[188:191]\n"
      "s_waitcnt lgkmcnt(5)\n"
      "ds_read_b128 v[136:139], v0 offset:0\n"
      "v_lshlrev_b32 v176, 16, v194\n"
      "v_mfma_f32_16x16x32_bf16 v[188:191], v[72:75], v[108:111], v[188:191]\n"
      "buffer_load_dwordx4 v[192:195], v13, s[8:11], s47 offen sc1\n"
      "v_mfma_f32_16x16x32_bf16 v[188:191], v[76:79], v[112:115], v[188:191]\n"
      "ds_read_b128 v[156:159], v0 offset:5120\n"
      "v_add_f32 v218, v218, v184\n"
      "v_mfma_f32_16x16x32_bf16 v[188:191], v[80:83], v[116:119], v[188:191]\n"
      "ds_read_b128 v[140:143], v0 offset:1024\n"
      "v_fmac_f32 v219, v184, v184\n"
      "v_mfma_f32_16x16x32_bf16 v[188:191], v[84:87], v[120:123], v[188:191]\n"
      "s_waitcnt vmcnt(10) lgkmcnt(4)\n"
      "v_and_b32 v183, 0xffff0000, v197\n"
      "v_mfma_f32_16x16x32_bf16 v[188:191], v[88:91], v[124:127], v[188:191]\n"
      "v_cvt_pk_bf16_f32 v214, v184, v185\n"
      "v_fmac_f32 v219, v185, v185\n"
      "v_mfma_f32_16x16x32_bf16 v[188:191], v[92:95], v[128:131], v[188:191]\n"
      "ds_read_b128 v[160:163], v0 offset:6144\n"
      "v_add_f32 v218, v218, v185\n"
      "v_mfma_f32_16x16x32_bf16 v[188:191], v[96:99], v[132:135], v[188:191]\n"
      "v_cvt_pk_bf16_f32 v215, v186, v187\n"
      "v_and_b32 v185, 0xffff0000, v198\n"
      "v_mfma_f32_16x16x32_bf16 v[172:175], a[0:3], v[100:103], v[172:175]\n"
      "ds_read2_b32 v[170:171], v3 offset0:64 offset1:96\n"
      "v_add_f32 v218, v218, v186\n"
      "v_mfma_f32_16x16x32_bf16 v[172:175], a[4:7], v[104:107], v[172:175]\n"
      "ds_read2_b32 v[168:169], v3 offset0:0 offset1:32\n"
      "s_add_u32 s36, s36, 24576\n"
      "v_mfma_f32_16x16x32_bf16 v[172:175], a[8:11], v[108:111], v[172:175]\n"
      "ds_read_b128 v[164:167], v0 offset:7168\n"
      "v_add_f32 v218, v218, v187\n"
      "v_mfma_f32_16x16x32_bf16 v[172:175], a[12:15], v[112:115], v[172:175]\n"
      "v_fmac_f32 v219, v186, v186\n"
      "v_fmac_f32 v219, v187, v187\n"
      "v_mfma_f32_16x16x32_bf16 v[172:175], a[16:19], v[116:119], v[172:175]\n"
      "s_add_u32 m0, s39, 40960\n"
      "ds_write_b64 v18, v[218:219]\n"
      "v_mfma_f32_16x16x32_bf16 v[172:175], a[20:23], v[120:123], v[172:175]\n"
      "v_and_b32 v187, 0xffff0000, v199\n"
      "s_min_u32 s36, s36, s6\n"
      "v_mfma_f32_16x16x32_bf16 v[172:175], a[24:27], v[124:127], v[172:175]\n"
      "v_lshlrev_b32 v186, 16, v199\n"
      "v_add_u32 v21, 1, v21\n"
      "v_mfma_f32_16x16x32_bf16 v[172:175], a[28:31], v[128:131], v[172:175]\n"
      "ds_write_b128 v17, v[188:191]\n"
      "v_mfma_f32_16x16x32_bf16 v[172:175], a[32:35], v[132:135], v[172:175]\n"
      "s_waitcnt lgkmcnt(3)\n"
      "buffer_store_dwordx4 v[212:215], v15, s[8:11], s38 offen offset:1024 sc1\n"
      "v_mfma_f32_16x16x32_bf16 v[172:175], a[36:39], v[136:139], v[172:175]\n"
      "v_and_b32 v181, 0xffff0000, v196\n"
      "v_mfma_f32_16x16x32_bf16 v[172:175], a[40:43], v[140:143], v[172:175]\n"
      "s_add_u32 s50, s50, 24576\n"
      "v_lshlrev_b32 v180, 16, v196\n"
      "v_lshlrev_b32_e64 v182, 16, v197\n"
      "v_mfma_f32_16x16x32_bf16 v[172:175], a[44:47], v[144:147], v[172:175]\n"
      "ds_write_b32 v23, v21\n"
      "v_mfma_f32_16x16x32_bf16 v[172:175], a[48:51], v[148:151], v[172:175]\n"
      "v_mfma_f32_16x16x32_bf16 v[172:175], a[52:55], v[152:155], v[172:175]\n"
      "s_waitcnt vmcnt(10) lgkmcnt(2)\n"
      "v_cmp_ne_u32 vcc, s21, v28\n"
      "v_mfma_f32_16x16x32_bf16 v[172:175], a[56:59], v[156:159], v[172:175]\n"
      "v_mfma_f32_16x16x32_bf16 v[172:175], a[60:63], v[160:163], v[172:175]\n"
      "v_lshlrev_b32_e64 v184, 16, v198\n"
      "v_mfma_f32_16x16x32_bf16 v[172:175], a[64:67], v[164:167], v[172:175]\n"
      "v_mfma_f32_16x16x32_bf16 v[172:175], a[68:71], v[168:171], v[172:175]\n"
      "buffer_load_dwordx4 v[196:199], v13, s[8:11], s47 offen offset:1024 sc1\n"
      "v_mfma_f32_16x16x32_bf16 v[176:179], a[72:75], v[100:103], v[176:179]\n"
      "v_mfma_f32_16x16x32_bf16 v[176:179], a[76:79], v[104:107], v[176:179]\n"
      "s_add_u32 s38, s38, 24576\n"
      "s_cbranch_vccnz .Lsp_v0_19\n"
      ".Lbk_v0_19:\n"
      "v_mfma_f32_16x16x32_bf16 v[176:179], a[80:83], v[108:111], v[176:179]\n"
      "v_mul_f32 v219, v172, v172\n"
      "v_mfma_f32_16x16x32_bf16 v[176:179], a[84:87], v[112:115], v[176:179]\n"
      "buffer_load_dword v28, v11, s[8:11], s36 offen sc1\n"
      "v_mfma_f32_16x16x32_bf16 v[176:179], a[88:91], v[116:119], v[176:179]\n"
      "ds_read_b32 v22, v24\n"
      "v_mfma_f32_16x16x32_bf16 v[176:179], a[92:95], v[120:123], v[176:179]\n"
      "v_cvt_pk_bf16_f32 v209, v174, v175\n"
      "v_mfma_f32_16x16x32_bf16 v[176:179], a[96:99], v[124:127], v[176:179]\n"
      "buffer_load_dwordx4 v0, s[8:11], s46 offen offset:1024 sc1 lds\n"
      "v_mfma_f32_16x16x32_bf16 v[176:179], a[100:103], v[128:131], v[176:179]\n"
      "v_cvt_pk_bf16_f32 v208, v172, v173\n"
      "v_fmac_f32 v219, v173, v173\n"
      "v_mfma_f32_16x16x32_bf16 v[176:179], a[104:107], v[132:135], v[176:179]\n"
      "v_fmac_f32 v219, v174, v174\n"
      "v_mfma_f32_16x16x32_bf16 v[176:179], a[108:111], v[136:139], v[176:179]\n"
      "s_waitcnt lgkmcnt(0)\n"
      "v_cmp_gt_i32 vcc, v21, v22\n"
      "v_mfma_f32_16x16x32_bf16 v[176:179], a[112:115], v[140:143], v[176:179]\n"
      "buffer_load_dwordx4 v0, s[8:11], s46 offen offset:0 sc1 lds\n"
      "v_mfma_f32_16x16x32_bf16 v[176:179], a[116:119], v[144:147], v[176:179]\n"
      "v_mfma_f32_16x16x32_bf16 v[176:179], a[120:123], v[148:151], v[176:179]\n"
      "s_cbranch_vccnz .Lsp_vz0_20\n"
      ".Lbk_vz0_20:\n"
      "v_mfma_f32_16x16x32_bf16 v[176:179], a[124:127], v[152:155], v[176:179]\n"
      "ds_read_b128 v[204:207], v19\n"
      "v_mfma_f32_16x16x32_bf16 v[176:179], a[128:131], v[156:159], v[176:179]\n"
      "buffer_load_dwordx4 v0, s[8:11], s46 offen offset:3072 sc1 lds\n"
      "v_mfma_f32_16x16x32_bf16 v[176:179], a[132:135], v[160:163], v[176:179]\n"
      "v_add_f32 v218, v172, v173\n"
      "v_mfma_f32_16x16x32_bf16 v[176:179], a[136:139], v[164:167], v[176:179]\n"
      "v_fmac_f32 v219, v175, v175\n"
      "v_add_u32 v21, 1, v21\n"
      "v_mfma_f32_16x16x32_bf16 v[176:179], a[140:143], v[168:171], v[176:179]\n"
      "s_waitcnt vmcnt(10)\n"
      "ds_write_b32 v23, v21\n"
      "v_mfma_f32_16x16x32_bf16 v[180:183], a[144:147], v[100:103], v[180:183]\n"
      "buffer_load_dwordx4 v0, s[8:11], s46 offen offset:2048 sc1 lds\n"
      "v_mfma_f32_16x16x32_bf16 v[180:183], a[148:151], v[104:107], v[180:183]\n"
      "v_add_f32 v218, v218, v174\n"
      "s_add_u32 m0, s40, 40960\n"
      "v_mfma_f32_16x16x32_bf16 v[180:183], a[152:155], v[108:111], v[180:183]\n"
      "s_waitcnt lgkmcnt(1)\n"
      "v_add_f32 v204, v204, v206\n"
      "v_add_f32 v205, v205, v207\n"
      "v_mfma_f32_16x16x32_bf16 v[180:183], a[156:159], v[112:115], v[180:183]\n"
      "v_add_f32_dpp v204, v204, v204 quad_perm:[1,0,3,2] row_mask:0xf bank_mask:0xf\n"
      "v_add_f32_dpp v205, v205, v205 quad_perm:[1,0,3,2] row_mask:0xf bank_mask:0xf\n"
      "v_mfma_f32_16x16x32_bf16 v[180:183], a[160:163], v[116:119], v[180:183]\n"
      "buffer_load_dwordx4 v1, s[8:11], s46 offen offset:0 sc1 lds\n"
      "v_mfma_f32_16x16x32_bf16 v[180:183], a[164:167], v[120:123], v[180:183]\n"
      "v_add_f32_dpp v204, v204, v204 quad_perm:[2,3,0,1] row_mask:0xf bank_mask:0xf\n"
      "v_add_f32_dpp v205, v205, v205 quad_perm:[2,3,0,1] row_mask:0xf bank_mask:0xf\n"
      "v_mfma_f32_16x16x32_bf16 v[180:183], a[168:171], v[124:127], v[180:183]\n"
      "v_add_f32 v218, v218, v175\n"
      "v_cvt_pk_bf16_f32 v210, v176, v177\n"
      "v_mfma_f32_16x16x32_bf16 v[180:183], a[172:175], v[128:131], v[180:183]\n"
      "v_add_f32_dpp v204, v204, v204 row_half_mirror row_mask:0xf bank_mask:0xf\n"
      "v_mul_f32 v204, v27, v204\n"
      "v_mfma_f32_16x16x32_bf16 v[180:183], a[176:179], v[132:135], v[180:183]\n"
      "v_add_f32_dpp v205, v205, v205 row_half_mirror row_mask:0xf bank_mask:0xf\n"
      "v_mfma_f32_16x16x32_bf16 v[180:183], a[180:183], v[136:139], v[180:183]\n"
      "ds_read_b32 v22, v24\n"
      "v_fmaak_f32 v205, v27, v205, 0x36a7c5ac\n"
      "v_mfma_f32_16x16x32_bf16 v[180:183], a[184:187], v[140:143], v[180:183]\n"
      "s_waitcnt vmcnt(10) lgkmcnt(1)\n"
      "buffer_store_dword v25, v12, s[8:11], s50 offen sc1\n"
      "v_mfma_f32_16x16x32_bf16 v[180:183], a[188:191], v[144:147], v[180:183]\n"
      "v_add_f32 v218, v218, v176\n"
      "v_cvt_pk_bf16_f32 v211, v178, v179\n"
      "v_mfma_f32_16x16x32_bf16 v[180:183], a[192:195], v[148:151], v[180:183]\n"
      "buffer_store_dwordx2 v[204:205], v20, s[8:11], s49 offen sc1\n"
      "v_mfma_f32_16x16x32_bf16 v[180:183], a[196:199], v[152:155], v[180:183]\n"
      "v_fmac_f32 v219, v176, v176\n"
      "v_add_f32 v218, v218, v177\n"
      "v_mfma_f32_16x16x32_bf16 v[180:183], a[200:203], v[156:159], v[180:183]\n"
      "s_waitcnt lgkmcnt(0)\n"
      "v_add_f32 v218, v218, v178\n"
      "v_cmp_gt_i32 vcc, v21, v22\n"
      "v_mfma_f32_16x16x32_bf16 v[180:183], a[204:207], v[160:163], v[180:183]\n"
      "v_fmac_f32 v219, v177, v177\n"
      "v_mfma_f32_16x16x32_bf16 v[180:183], a[208:211], v[164:167], v[180:183]\n"
      "v_fmac_f32_e64 v219, v178, v178\n"
      "v_add_f32_e64 v218, v218, v179\n"
      "v_mfma_f32_16x16x32_bf16 v[180:183], a[212:215], v[168:171], v[180:183]\n"
      "s_waitcnt vmcnt(10)\n"
      "v_fmac_f32_e64 v219, v179, v179\n"
      "v_lshlrev_b32_e64 v178, 16, v195\n"
      "v_mfma_f32_16x16x32_bf16 v[184:187], a[216:219], v[100:103], v[184:187]\n"
      "s_cbranch_vccnz .Lsp_vy1_21\n"
      ".Lbk_vy1_21:\n"
      "ds_read_b128 v[100:103], v0 offset:28672\n"
      "v_mfma_f32_16x16x32_bf16 v[184:187], a[220:223], v[104:107], v[184:187]\n"
      "v_lshlrev_b32 v172, 16, v192\n"
      "v_and_b32 v177, 0xffff0000, v194\n"
      "v_mfma_f32_16x16x32_bf16 v[184:187], a[224:227], v[108:111], v[184:187]\n"
      "v_and_b32 v179, 0xffff0000, v195\n"
      "s_add_u32 s49, s49, 24576\n"
      "v_mfma_f32_16x16x32_bf16 v[184:187], a[228:231], v[112:115], v[184:187]\n"
      "v_lshlrev_b32 v176, 16, v194\n"
      "s_sub_u32 s47, s46, s45\n"
      "v_mfma_f32_16x16x32_bf16 v[184:187], a[232:235], v[116:119], v[184:187]\n"
      "ds_read_b128 v[108:111], v0 offset:30720\n"
      "v_fmac_f32 v219, v180, v180\n"
      "v_mfma_f32_16x16x32_bf16 v[184:187], a[236:239], v[120:123], v[184:187]\n"
      "ds_read_b128 v[116:119], v0 offset:32768\n"
      "v_and_b32 v175, 0xffff0000, v193\n"
      "v_mfma_f32_16x16x32_bf16 v[184:187], a[240:243], v[124:127], v[184:187]\n"
      "buffer_store_dwordx4 v[208:211], v15, s[8:11], s38 offen sc1\n"
      "v_mfma_f32_16x16x32_bf16 v[184:187], a[244:247], v[128:131], v[184:187]\n"
      "ds_read_b128 v[120:123], v0 offset:33792\n"
      "v_mfma_f32_16x16x32_bf16 v[184:187], a[248:251], v[132:135], v[184:187]\n"
      "ds_read_b128 v[112:115], v0 offset:31744\n"
      "v_mfma_f32_16x16x32_bf16 v[184:187], a[252:255], v[136:139], v[184:187]\n"
      "ds_read_b128 v[104:107], v0 offset:29696\n"
      "v_cvt_pk_bf16_f32 v213, v182, v183\n"
      "v_mfma_f32_16x16x32_bf16 v[184:187], v[32:35], v[140:143], v[184:187]\n"
      "s_mov_b32 s46, s36\n"
      "ds_read_b128 v[128:131], v0 offset:35840\n"
      "v_add_f32 v218, v218, v180\n"
      "v_mfma_f32_16x16x32_bf16 v[184:187], v[36:39], v[144:147], v[184:187]\n"
      "v_mfma_f32_16x16x32_bf16 v[184:187], v[40:43], v[148:151], v[184:187]\n"
      "ds_read_b128 v[136:139], v0 offset:20480\n"
      "v_add_f32 v218, v218, v181\n"
      "v_mfma_f32_16x16x32_bf16 v[184:187], v[44:47], v[152:155], v[184:187]\n"
      "ds_read_b128 v[124:127], v0 offset:34816\n"
      "v_mfma_f32_16x16x32_bf16 v[184:187], v[48:51], v[156:159], v[184:187]\n"
      "ds_read_b128 v[144:147], v0 offset:22528\n"
      "v_lshlrev_b32 v174, 16, v193\n"
      "v_mfma_f32_16x16x32_bf16 v[184:187], v[52:55], v[160:163], v[184:187]\n"
      "ds_read2_b32 v[132:133], v4 offset0:0 offset1:32\n"
      "v_add_f32 v218, v218, v182\n"
      "v_mfma_f32_16x16x32_bf16 v[184:187], v[56:59], v[164:167], v[184:187]\n"
      "ds_read2_b32 v[134:135], v4 offset0:64 offset1:96\n"
      "v_and_b32 v173, 0xffff0000, v192\n"
      "v_mfma_f32_16x16x32_bf16 v[184:187], v[60:63], v[168:171], v[184:187]\n"
      "s_waitcnt lgkmcnt(5)\n"
      "buffer_load_dwordx4 v[192:195], v13, s[8:11], s47 offen sc1\n"
      "v_mfma_f32_16x16x32_bf16 v[188:191], v[64:67], v[100:103], 0\n"
      "ds_read_b128 v[160:163], v0 offset:26624\n"
      "v_mfma_f32_16x16x32_bf16 v[188:191], v[68:71], v[104:107], v[188:191]\n"
      "ds_read_b128 v[140:143], v0 offset:21504\n"
      "v_cvt_pk_bf16_f32 v212, v180, v181\n"
      "v_mfma_f32_16x16x32_bf16 v[188:191], v[72:75], v[108:111], v[188:191]\n"
      "v_add_u32 v21, 1, v21\n"
      "v_mfma_f32_16x16x32_bf16 v[188:191], v[76:79], v[112:115], v[188:191]\n"
      "ds_read2_b32 v[170:171], v5 offset0:64 offset1:96\n"
      "v_mfma_f32_16x16x32_bf16 v[188:191], v[80:83], v[116:119], v[188:191]\n"
      "ds_read_b128 v[152:155], v0 offset:24576\n"
      "v_mfma_f32_16x16x32_bf16 v[188:191], v[84:87], v[120:123], v[188:191]\n"
      "s_waitcnt lgkmcnt(4)\n"
      "ds_read_b128 v[164:167], v0 offset:27648\n"
      "v_fmac_f32 v219, v181, v181\n"
      "v_mfma_f32_16x16x32_bf16 v[188:191], v[88:91], v[124:127], v[188:191]\n"
      "ds_read_b128 v[148:151], v0 offset:23552\n"
      "v_fmac_f32 v219, v182, v182\n"
      "v_mfma_f32_16x16x32_bf16 v[188:191], v[92:95], v[128:131], v[188:191]\n"
      "ds_read_b128 v[156:159], v0 offset:25600\n"
      "v_cvt_pk_bf16_f32 v214, v184, v185\n"
      "v_mfma_f32_16x16x32_bf16 v[188:191], v[96:99], v[132:135], v[188:191]\n"
      "s_add_u32 m0, s39, 61440\n"
      "v_add_f32 v218, v218, v183\n"
      "v_mfma_f32_16x16x32_bf16 v[172:175], a[0:3], v[100:103], v[172:175]\n"
      "v_add_f32 v218, v218, v184\n"
      "v_fmac_f32 v219, v183, v183\n"
      "v_mfma_f32_16x16x32_bf16 v[172:175], a[4:7], v[104:107], v[172:175]\n"
      "ds_read2_b32 v[168:169], v5 offset0:0 offset1:32\n"
      "v_mfma_f32_16x16x32_bf16 v[172:175], a[8:11], v[108:111], v[172:175]\n"
      "v_add_f32 v218, v218, v185\n"
      "v_fmac_f32 v219, v184, v184\n"
      "v_mfma_f32_16x16x32_bf16 v[172:175], a[12:15], v[112:115], v[172:175]\n"
      "ds_write_b128 v17, v[188:191]\n"
      "v_mfma_f32_16x16x32_bf16 v[172:175], a[16:19], v[116:119], v[172:175]\n"
      "s_waitcnt vmcnt(10) lgkmcnt(3)\n"
      "v_fmac_f32 v219, v185, v185\n"
      "v_and_b32 v181, 0xffff0000, v196\n"
      "v_mfma_f32_16x16x32_bf16 v[172:175], a[20:23], v[120:123], v[172:175]\n"
      "v_add_f32 v218, v218, v186\n"
      "v_mfma_f32_16x16x32_bf16 v[172:175], a[24:27], v[124:127], v[172:175]\n"
      "v_fmac_f32 v219, v186, v186\n"
      "v_add_f32 v218, v218, v187\n"
      "v_mfma_f32_16x16x32_bf16 v[172:175], a[28:31], v[128:131], v[172:175]\n"
      "v_cvt_pk_bf16_f32 v215, v186, v187\n"
      "v_fmac_f32 v219, v187, v187\n"
      "v_mfma_f32_16x16x32_bf16 v[172:175], a[32:35], v[132:135], v[172:175]\n"
      "ds_write_b64 v18, v[218:219]\n"
      "v_mfma_f32_16x16x32_bf16 v[172:175], a[36:39], v[136:139], v[172:175]\n"
      "v_lshlrev_b32 v186, 16, v199\n"
      "v_mfma_f32_16x16x32_bf16 v[172:175], a[40:43], v[140:143], v[172:175]\n"
      "buffer_store_dwordx4 v[212:215], v15, s[8:11], s38 offen offset:1024 sc1\n"
      "v_mfma_f32_16x16x32_bf16 v[172:175], a[44:47], v[144:147], v[172:175]\n"
      "ds_write_b32 v23, v21\n"
      "v_mfma_f32_16x16x32_bf16 v[172:175], a[48:51], v[148:151], v[172:175]\n"
      "v_mfma_f32_16x16x32_bf16 v[172:175], a[52:55], v[152:155], v[172:175]\n"
      "s_waitcnt lgkmcnt(2)\n"
      "v_and_b32 v183, 0xffff0000, v197\n"
      "v_lshlrev_b32 v184, 16, v198\n"
      "v_mfma_f32_16x16x32_bf16 v[172:175], a[56:59], v[156:159], v[172:175]\n"
      "s_add_u32 s50, s50, 24576\n"
      "v_lshlrev_b32 v180, 16, v196\n"
      "v_mfma_f32_16x16x32_bf16 v[172:175], a[60:63], v[160:163], v[172:175]\n"
      "v_lshlrev_b32 v182, 16, v197\n"
      "v_and_b32 v185, 0xffff0000, v198\n"
      "v_mfma_f32_16x16x32_bf16 v[172:175], a[64:67], v[164:167], v[172:175]\n"
      "s_cmp_eq_u32 s36, s37\n"
      "s_cselect_b32 s48, 49152, s51\n"
      "v_and_b32 v187, 0xffff0000, v199\n"
      "v_mfma_f32_16x16x32_bf16 v[172:175], a[68:71], v[168:171], v[172:175]\n"
      "buffer_load_dwordx4 v[196:199], v13, s[8:11], s47 offen offset:1024 sc1\n"
      "v_mfma_f32_16x16x32_bf16 v[176:179], a[72:75], v[100:103], v[176:179]\n"
      "s_waitcnt vmcnt(11) lgkmcnt(0)\n"
      "v_cmp_ne_u32 vcc, s21, v28\n"
      "s_add_u32 s36, s36, s48\n"
      "v_mfma_f32_16x16x32_bf16 v[176:179], a[76:79], v[104:107], v[176:179]\n"
      "s_min_u32 s36, s36, s6\n"
      "v_mfma_f32_16x16x32_bf16 v[176:179], a[80:83], v[108:111], v[176:179]\n"
      "ds_read_b32 v22, v24\n"
      "v_mfma_f32_16x16x32_bf16 v[176:179], a[84:87], v[112:115], v[176:179]\n"
      "s_cbranch_vccnz .Lsp_v1_22\n"
      ".Lbk_v1_22:\n"
      "v_mfma_f32_16x16x32_bf16 v[176:179], a[88:91], v[116:119], v[176:179]\n"
      "buffer_load_dword v28, v11, s[8:11], s36 offen sc1\n"
      "v_mfma_f32_16x16x32_bf16 v[176:179], a[92:95], v[120:123], v[176:179]\n"
      "v_add_f32 v218, v172, v173\n"
      "v_mul_f32 v219, v172, v172\n"
      "v_mfma_f32_16x16x32_bf16 v[176:179], a[96:99], v[124:127], v[176:179]\n"
      "buffer_load_dwordx4 v0, s[8:11], s46 offen offset:0 sc1 lds\n"
      "v_mfma_f32_16x16x32_bf16 v[176:179], a[100:103], v[128:131], v[176:179]\n"
      "v_mfma_f32_16x16x32_bf16 v[176:179], a[104:107], v[132:135], v[176:179]\n"
      "s_waitcnt lgkmcnt(0)\n"
      "v_cmp_gt_i32 vcc, v21, v22\n"
      "v_fmac_f32 v219, v173, v173\n"
      "v_mfma_f32_16x16x32_bf16 v[176:179], a[108:111], v[136:139], v[176:179]\n"
      "v_cvt_pk_bf16_f32 v209, v174, v175\n"
      "s_sub_u32 s47, s46, s45\n"
      "v_mfma_f32_16x16x32_bf16 v[176:179], a[112:115], v[140:143], v[176:179]\n"
      "buffer_load_dwordx4 v0, s[8:11], s46 offen offset:1024 sc1 lds\n"
      "v_mfma_f32_16x16x32_bf16 v[176:179], a[116:119], v[144:147], v[176:179]\n"
      "s_cbranch_vccnz .Lsp_vz1_23\n"
      ".Lbk_vz1_23:\n"
      "ds_read_b128 v[204:207], v19\n"
      "v_mfma_f32_16x16x32_bf16 v[176:179], a[120:123], v[148:151], v[176:179]\n"
      "v_add_u32 v21, 1, v21\n"
      "v_mfma_f32_16x16x32_bf16 v[176:179], a[124:127], v[152:155], v[176:179]\n"
      "v_mfma_f32_16x16x32_bf16 v[176:179], a[128:131], v[156:159], v[176:179]\n"
      "s_waitcnt vmcnt(9)\n"
      "ds_write_b32 v23, v21\n"
      "v_mfma_f32_16x16x32_bf16 v[176:179], a[132:135], v[160:163], v[176:179]\n"
      "v_fmac_f32 v219, v174, v174\n"
      "v_mfma_f32_16x16x32_bf16 v[176:179], a[136:139], v[164:167], v[176:179]\n"
      "buffer_load_dwordx4 v0, s[8:11], s46 offen offset:3072 sc1 lds\n"
      "v_mfma_f32_16x16x32_bf16 v[176:179], a[140:143], v[168:171], v[176:179]\n"
      "v_cvt_pk_bf16_f32 v208, v172, v173\n"
      "v_add_f32 v218, v218, v174\n"
      "v_mfma_f32_16x16x32_bf16 v[180:183], a[144:147], v[100:103], v[180:183]\n"
      "s_waitcnt lgkmcnt(1)\n"
      "v_add_f32 v205, v205, v207\n"
      "v_mfma_f32_16x16x32_bf16 v[180:183], a[148:151], v[104:107], v[180:183]\n"
      "v_add_f32_dpp v205, v205, v205 quad_perm:[1,0,3,2] row_mask:0xf bank_mask:0xf\n"
      "v_mfma_f32_16x16x32_bf16 v[180:183], a[152:155], v[108:111], v[180:183]\n"
      "buffer_load_dwordx4 v0, s[8:11], s46 offen offset:2048 sc1 lds\n"
      "v_mfma_f32_16x16x32_bf16 v[180:183], a[156:159], v[112:115], v[180:183]\n"
      "v_add_f32 v204, v204, v206\n"
      "v_mfma_f32_16x16x32_bf16 v[180:183], a[160:163], v[116:119], v[180:183]\n"
      "v_add_f32_dpp v204, v204, v204 quad_perm:[1,0,3,2] row_mask:0xf bank_mask:0xf\n"
      "v_add_f32_dpp v205, v205, v205 quad_perm:[2,3,0,1] row_mask:0xf bank_mask:0xf\n"
      "v_mfma_f32_16x16x32_bf16 v[180:183], a[164:167], v[120:123], v[180:183]\n"
      "s_add_u32 m0, s40, 61440\n"
      "v_add_f32_dpp v204, v204, v204 quad_perm:[2,3,0,1] row_mask:0xf bank_mask:0xf\n"
      "v_cvt_pk_bf16_f32 v210, v176, v177\n"
      "v_mfma_f32_16x16x32_bf16 v[180:183], a[168:171], v[124:127], v[180:183]\n"
      "buffer_load_dwordx4 v1, s[8:11], s46 offen offset:0 sc1 lds\n"
      "v_mfma_f32_16x16x32_bf16 v[180:183], a[172:175], v[128:131], v[180:183]\n"
      "s_add_u32 s38, s38, 24576\n"
      "v_add_f32_dpp v204, v204, v204 row_half_mirror row_mask:0xf bank_mask:0xf\n"
      "v_add_f32 v218, v218, v175\n"
      "v_mfma_f32_16x16x32_bf16 v[180:183], a[176:179], v[132:135], v[180:183]\n"
      "v_fmac_f32 v219, v175, v175\n"
      "v_add_f32 v218, v218, v176\n"
      "v_mfma_f32_16x16x32_bf16 v[180:183], a[180:183], v[136:139], v[180:183]\n"
      "v_fmac_f32 v219, v176, v176\n"
      "v_fmac_f32 v219, v177, v177\n"
      "v_mfma_f32_16x16x32_bf16 v[180:183], a[184:187], v[140:143], v[180:183]\n"
      "s_waitcnt vmcnt(10) lgkmcnt(0)\n"
      "buffer_store_dword v25, v12, s[8:11], s50 offen sc1\n"
      "v_mfma_f32_16x16x32_bf16 v[180:183], a[188:191], v[144:147], v[180:183]\n"
      "ds_read_b32 v22, v24\n"
      "v_add_f32_dpp v205, v205, v205 row_half_mirror row_mask:0xf bank_mask:0xf\n"
      "v_mfma_f32_16x16x32_bf16 v[180:183], a[192:195], v[148:151], v[180:183]\n"
      "v_mul_f32 v204, v27, v204\n"
      "v_add_f32 v218, v218, v177\n"
      "v_mfma_f32_16x16x32_bf16 v[180:183], a[196:199], v[152:155], v[180:183]\n"
      "v_fmaak_f32 v205, v27, v205, 0x36a7c5ac\n"
      "v_fmac_f32 v219, v178, v178\n"
      "v_mfma_f32_16x16x32_bf16 v[180:183], a[200:203], v[156:159], v[180:183]\n"
      "buffer_store_dwordx2 v[204:205], v20, s[8:11], s49 offen sc1\n"
      "v_mfma_f32_16x16x32_bf16 v[180:183], a[204:207], v[160:163], v[180:183]\n"
      "s_waitcnt vmcnt(10)\n"
      "v_and_b32 v177, 0xffff0000, v194\n"
      "v_add_f32 v218, v218, v178\n"
      "v_mfma_f32_16x16x32_bf16 v[180:183], a[208:211], v[164:167], v[180:183]\n"
      "s_waitcnt lgkmcnt(0)\n"
      "v_cmp_gt_i32 vcc, v21, v22\n"
      "v_and_b32 v173, 0xffff0000, v192\n"
      "v_mfma_f32_16x16x32_bf16 v[180:183], a[212:215], v[168:171], v[180:183]\n"
      "v_mfma_f32_16x16x32_bf16 v[184:187], a[216:219], v[100:103], v[184:187]\n"
      "v_fmac_f32 v219, v179, v179\n"
      "v_add_f32 v218, v218, v179\n"
      "v_mfma_f32_16x16x32_bf16 v[184:187], a[220:223], v[104:107], v[184:187]\n"
      "s_cbranch_vccnz .Lsp_vy0_24\n"
      ".Lbk_vy0_24:\n"
      "ds_read_b128 v[104:107], v10 offset:9216\n"
      "v_mfma_f32_16x16x32_bf16 v[184:187], a[224:227], v[108:111], v[184:187]\n"
      "v_cvt_pk_bf16_f32 v211, v178, v179\n"
      "v_mfma_f32_16x16x32_bf16 v[184:187], a[228:231], v[112:115], v[184:187]\n"
      "ds_read_b128 v[100:103], v10 offset:8192\n"
      "v_lshlrev_b32 v172, 16, v192\n"
      "v_mfma_f32_16x16x32_bf16 v[184:187], a[232:235], v[116:119], v[184:187]\n"
      "s_mov_b32 s46, s36\n"
      "ds_read_b128 v[116:119], v10 offset:12288\n"
      "v_and_b32 v179, 0xffff0000, v195\n"
      "v_mfma_f32_16x16x32_bf16 v[184:187], a[236:239], v[120:123], v[184:187]\n"
      "v_fmac_f32 v219, v180, v180\n"
      "v_and_b32 v175, 0xffff0000, v193\n"
      "v_mfma_f32_16x16x32_bf16 v[184:187], a[240:243], v[124:127], v[184:187]\n"
      "ds_read_b128 v[112:115], v10 offset:11264\n"
      "v_lshlrev_b32 v174, 16, v193\n"
      "v_mfma_f32_16x16x32_bf16 v[184:187], a[244:247], v[128:131], v[184:187]\n"
      "ds_read_b128 v[120:123], v10 offset:13312\n"
      "v_fmac_f32 v219, v181, v181\n"
      "v_mfma_f32_16x16x32_bf16 v[184:187], a[248:251], v[132:135], v[184:187]\n"
      "ds_read_b128 v[124:127], v10 offset:14336\n"
      "v_add_f32 v218, v218, v180\n"
      "v_mfma_f32_16x16x32_bf16 v[184:187], a[252:255], v[136:139], v[184:187]\n"
      "s_add_u32 s49, s49, 24576\n"
      "ds_read_b128 v[128:131], v10 offset:15360\n"
      "v_add_f32 v218, v218, v181\n"
      "v_mfma_f32_16x16x32_bf16 v[184:187], v[32:35], v[140:143], v[184:187]\n"
      "v_add_f32 v218, v218, v182\n"
      "v_add_f32 v218, v218, v183\n"
      "v_mfma_f32_16x16x32_bf16 v[184:187], v[36:39], v[144:147], v[184:187]\n"
      "buffer_store_dwordx4 v[208:211], v15, s[8:11], s38 offen sc1\n"
      "v_mfma_f32_16x16x32_bf16 v[184:187], v[40:43], v[148:151], v[184:187]\n"
      "ds_read_b128 v[108:111], v10 offset:10240\n"
      "v_fmac_f32 v219, v182, v182\n"
      "v_mfma_f32_16x16x32_bf16 v[184:187], v[44:47], v[152:155], v[184:187]\n"
      "ds_read2_b32 v[132:133], v6 offset0:0 offset1:32\n"
      "v_cvt_pk_bf16_f32 v213, v182, v183\n"
      "v_mfma_f32_16x16x32_bf16 v[184:187], v[48:51], v[156:159], v[184:187]\n"
      "ds_read_b128 v[144:147], v10 offset:2048\n"
      "v_lshlrev_b32 v178, 16, v195\n"
      "v_mfma_f32_16x16x32_bf16 v[184:187], v[52:55], v[160:163], v[184:187]\n"
      "ds_read2_b32 v[134:135], v6 offset0:64 offset1:96\n"
      "v_cvt_pk_bf16_f32 v212, v180, v181\n"
      "v_mfma_f32_16x16x32_bf16 v[184:187], v[56:59], v[164:167], v[184:187]\n"
      "s_waitcnt lgkmcnt(4)\n"
      "ds_read_b128 v[152:155], v10 offset:4096\n"
      "v_fmac_f32 v219, v183, v183\n"
      "v_mfma_f32_16x16x32_bf16 v[184:187], v[60:63], v[168:171], v[184:187]\n"
      "v_mfma_f32_16x16x32_bf16 v[188:191], v[64:67], v[100:103], 0\n"
      "ds_read_b128 v[148:151], v10 offset:3072\n"
      "v_mfma_f32_16x16x32_bf16 v[188:191], v[68:71], v[104:107], v[188:191]\n"
      "s_waitcnt lgkmcnt(5)\n"
      "ds_read_b128 v[136:139], v10 offset:0\n"
      "v_lshlrev_b32 v176, 16, v194\n"
      "v_mfma_f32_16x16x32_bf16 v[188:191], v[72:75], v[108:111], v[188:191]\n"
      "buffer_load_dwordx4 v[192:195], v13, s[8:11], s47 offen sc1\n"
      "v_mfma_f32_16x16x32_bf16 v[188:191], v[76:79], v[112:115], v[188:191]\n"
      "ds_read_b128 v[156:159], v10 offset:5120\n"
      "v_add_f32 v218, v218, v184\n"
      "v_mfma_f32_16x16x32_bf16 v[188:191], v[80:83], v[116:119], v[188:191]\n"
      "ds_read_b128 v[140:143], v10 offset:1024\n"
      "v_fmac_f32 v219, v184, v184\n"
      "v_mfma_f32_16x16x32_bf16 v[188:191], v[84:87], v[120:123], v[188:191]\n"
      "s_waitcnt vmcnt(10) lgkmcnt(4)\n"
      "v_and_b32 v183, 0xffff0000, v197\n"
      "v_mfma_f32_16x16x32_bf16 v[188:191], v[88:91], v[124:127], v[188:191]\n"
      "v_cvt_pk_bf16_f32 v214, v184, v185\n"
      "v_fmac_f32 v219, v185, v185\n"
      "v_mfma_f32_16x16x32_bf16 v[188:191], v[92:95], v[128:131], v[188:191]\n"
      "ds_read_b128 v[160:163], v10 offset:6144\n"
      "v_add_f32 v218, v218, v185\n"
      "v_mfma_f32_16x16x32_bf16 v[188:191], v[96:99], v[132:135], v[188:191]\n"
      "v_cvt_pk_bf16_f32 v215, v186, v187\n"
      "v_and_b32 v185, 0xffff0000, v198\n"
      "v_mfma_f32_16x16x32_bf16 v[172:175], a[0:3], v[100:103], v[172:175]\n"
      "ds_read2_b32 v[170:171], v7 offset0:64 offset1:96\n"
      "v_add_f32 v218, v218, v186\n"
      "v_mfma_f32_16x16x32_bf16 v[172:175], a[4:7], v[104:107], v[172:175]\n"
      "ds_read2_b32 v[168:169], v7 offset0:0 offset1:32\n"
      "s_add_u32 s36, s36, 24576\n"
      "v_mfma_f32_16x16x32_bf16 v[172:175], a[8:11], v[108:111], v[172:175]\n"
      "ds_read_b128 v[164:167], v10 offset:7168\n"
      "v_add_f32 v218, v218, v187\n"
      "v_mfma_f32_16x16x32_bf16 v[172:175], a[12:15], v[112:115], v[172:175]\n"
      "v_fmac_f32 v219, v186, v186\n"
      "v_fmac_f32 v219, v187, v187\n"
      "v_mfma_f32_16x16x32_bf16 v[172:175], a[16:19], v[116:119], v[172:175]\n"
      "s_add_u32 m0, s39, 0\n"
      "ds_write_b64 v18, v[218:219]\n"
      "v_mfma_f32_16x16x32_bf16 v[172:175], a[20:23], v[120:123], v[172:175]\n"
      "v_and_b32 v187, 0xffff0000, v199\n"
      "s_min_u32 s36, s36, s6\n"
      "v_mfma_f32_16x16x32_bf16 v[172:175], a[24:27], v[124:127], v[172:175]\n"
      "v_lshlrev_b32 v186, 16, v199\n"
      "v_add_u32 v21, 1, v21\n"
      "v_mfma_f32_16x16x32_bf16 v[172:175], a[28:31], v[128:131], v[172:175]\n"
      "ds_write_b128 v17, v[188:191]\n"
      "v_mfma_f32_16x16x32_bf16 v[172:175], a[32:35], v[132:135], v[172:175]\n"
      "s_waitcnt lgkmcnt(3)\n"
      "buffer_store_dwordx4 v[212:215], v15, s[8:11], s38 offen offset:1024 sc1\n"
      "v_mfma_f32_16x16x32_bf16 v[172:175], a[36:39], v[136:139], v[172:175]\n"
      "v_and_b32 v181, 0xffff0000, v196\n"
      "v_mfma_f32_16x16x32_bf16 v[172:175], a[40:43], v[140:143], v[172:175]\n"
      "s_add_u32 s50, s50, 24576\n"
      "v_lshlrev_b32 v180, 16, v196\n"
      "v_lshlrev_b32 v182, 16, v197\n"
      "v_mfma_f32_16x16x32_bf16 v[172:175], a[44:47], v[144:147], v[172:175]\n"
      "ds_write_b32 v23, v21\n"
      "v_mfma_f32_16x16x32_bf16 v[172:175], a[48:51], v[148:151], v[172:175]\n"
      "v_mfma_f32_16x16x32_bf16 v[172:175], a[52:55], v[152:155], v[172:175]\n"
      "s_waitcnt vmcnt(10) lgkmcnt(2)\n"
      "v_cmp_ne_u32 vcc, s21, v28\n"
      "v_mfma_f32_16x16x32_bf16 v[172:175], a[56:59], v[156:159], v[172:175]\n"
      "v_mfma_f32_16x16x32_bf16 v[172:175], a[60:63], v[160:163], v[172:175]\n"
      "v_lshlrev_b32 v184, 16, v198\n"
      "v_mfma_f32_16x16x32_bf16 v[172:175], a[64:67], v[164:167], v[172:175]\n"
      "v_mfma_f32_16x16x32_bf16 v[172:175], a[68:71], v[168:171], v[172:175]\n"
      "buffer_load_dwordx4 v[196:199], v13, s[8:11], s47 offen offset:1024 sc1\n"
      "v_mfma_f32_16x16x32_bf16 v[176:179], a[72:75], v[100:103], v[176:179]\n"
      "v_mfma_f32_16x16x32_bf16 v[176:179], a[76:79], v[104:107], v[176:179]\n"
      "s_add_u32 s38, s38, 24576\n"
      "s_cbranch_vccnz .Lsp_v0_25\n"
      ".Lbk_v0_25:\n"
      "v_mfma_f32_16x16x32_bf16 v[176:179], a[80:83], v[108:111], v[176:179]\n"
      "v_mul_f32 v219, v172, v172\n"
      "v_mfma_f32_16x16x32_bf16 v[176:179], a[84:87], v[112:115], v[176:179]\n"
      "buffer_load_dword v28, v11, s[8:11], s36 offen sc1\n"
      "v_mfma_f32_16x16x32_bf16 v[176:179], a[88:91], v[116:119], v[176:179]\n"
      "ds_read_b32 v22, v24\n"
      "v_mfma_f32_16x16x32_bf16 v[176:179], a[92:95], v[120:123], v[176:179]\n"
      "v_cvt_pk_bf16_f32 v209, v174, v175\n"
      "v_mfma_f32_16x16x32_bf16 v[176:179], a[96:99], v[124:127], v[176:179]\n"
      "buffer_load_dwordx4 v0, s[8:11], s46 offen offset:1024 sc1 lds\n"
      "v_mfma_f32_16x16x32_bf16 v[176:179], a[100:103], v[128:131], v[176:179]\n"
      "v_cvt_pk_bf16_f32 v208, v172, v173\n"
      "v_fmac_f32 v219, v173, v173\n"
      "v_mfma_f32_16x16x32_bf16 v[176:179], a[104:107], v[132:135], v[176:179]\n"
      "v_fmac_f32 v219, v174, v174\n"
      "v_mfma_f32_16x16x32_bf16 v[176:179], a[108:111], v[136:139], v[176:179]\n"
      "s_waitcnt lgkmcnt(0)\n"
      "v_cmp_gt_i32 vcc, v21, v22\n"
      "v_mfma_f32_16x16x32_bf16 v[176:179], a[112:115], v[140:143], v[176:179]\n"
      "buffer_load_dwordx4 v0, s[8:11], s46 offen offset:0 sc1 lds\n"
      "v_mfma_f32_16x16x32_bf16 v[176:179], a[116:119], v[144:147], v[176:179]\n"
      "v_mfma_f32_16x16x32_bf16 v[176:179], a[120:123], v[148:151], v[176:179]\n"
      "s_cbranch_vccnz .Lsp_vz0_26\n"
      ".Lbk_vz0_26:\n"
      "v_mfma_f32_16x16x32_bf16 v[176:179], a[124:127], v[152:155], v[176:179]\n"
      "ds_read_b128 v[204:207], v19\n"
      "v_mfma_f32_16x16x32_bf16 v[176:179], a[128:131], v[156:159], v[176:179]\n"
      "buffer_load_dwordx4 v0, s[8:11], s46 offen offset:3072 sc1 lds\n"
      "v_mfma_f32_16x16x32_bf16 v[176:179], a[132:135], v[160:163], v[176:179]\n"
      "v_add_f32 v218, v172, v173\n"
      "v_mfma_f32_16x16x32_bf16 v[176:179], a[136:139], v[164:167], v[176:179]\n"
      "v_fmac_f32 v219, v175, v175\n"
      "v_add_u32 v21, 1, v21\n"
      "v_mfma_f32_16x16x32_bf16 v[176:179], a[140:143], v[168:171], v[176:179]\n"
      "s_waitcnt vmcnt(10)\n"
      "ds_write_b32 v23, v21\n"
      "v_mfma_f32_16x16x32_bf16 v[180:183], a[144:147], v[100:103], v[180:183]\n"
      "buffer_load_dwordx4 v0, s[8:11], s46 offen offset:2048 sc1 lds\n"
      "v_mfma_f32_16x16x32_bf16 v[180:183], a[148:151], v[104:107], v[180:183]\n"
      "v_add_f32 v218, v218, v174\n"
      "s_add_u32 m0, s40, 0\n"
      "v_mfma_f32_16x16x32_bf16 v[180:183], a[152:155], v[108:111], v[180:183]\n"
      "s_waitcnt lgkmcnt(1)\n"
      "v_add_f32 v204, v204, v206\n"
      "v_add_f32 v205, v205, v207\n"
      "v_mfma_f32_16x16x32_bf16 v[180:183], a[156:159], v[112:115], v[180:183]\n"
      "v_add_f32_dpp v204, v204, v204 quad_perm:[1,0,3,2] row_mask:0xf bank_mask:0xf\n"
      "v_add_f32_dpp v205, v205, v205 quad_perm:[1,0,3,2] row_mask:0xf bank_mask:0xf\n"
      "v_mfma_f32_16x16x32_bf16 v[180:183], a[160:163], v[116:119], v[180:183]\n"
      "buffer_load_dwordx4 v1, s[8:11], s46 offen offset:0 sc1 lds\n"
      "v_mfma_f32_16x16x32_bf16 v[180:183], a[164:167], v[120:123], v[180:183]\n"
      "v_add_f32_dpp v204, v204, v204 quad_perm:[2,3,0,1] row_mask:0xf bank_mask:0xf\n"
      "v_add_f32_dpp v205, v205, v205 quad_perm:[2,3,0,1] row_mask:0xf bank_mask:0xf\n"
      "v_mfma_f32_16x16x32_bf16 v[180:183], a[168:171], v[124:127], v[180:183]\n"
      "v_add_f32 v218, v218, v175\n"
      "v_cvt_pk_bf16_f32 v210, v176, v177\n"
      "v_mfma_f32_16x16x32_bf16 v[180:183], a[172:175], v[128:131], v[180:183]\n"
      "v_add_f32_dpp v204, v204, v204 row_half_mirror row_mask:0xf bank_mask:0xf\n"
      "v_mul_f32 v204, v27, v204\n"
      "v_mfma_f32_16x16x32_bf16 v[180:183], a[176:179], v[132:135], v[180:183]\n"
      "v_add_f32_dpp v205, v205, v205 row_half_mirror row_mask:0xf bank_mask:0xf\n"
      "v_mfma_f32_16x16x32_bf16 v[180:183], a[180:183], v[136:139], v[180:183]\n"
      "ds_read_b32 v22, v24\n"
      "v_fmaak_f32 v205, v27, v205, 0x36a7c5ac\n"
      "v_mfma_f32_16x16x32_bf16 v[180:183], a[184:187], v[140:143], v[180:183]\n"
      "s_waitcnt vmcnt(10) lgkmcnt(1)\n"
      "buffer_store_dword v25, v12, s[8:11], s50 offen sc1\n"
      "v_mfma_f32_16x16x32_bf16 v[180:183], a[188:191], v[144:147], v[180:183]\n"
      "v_add_f32 v218, v218, v176\n"
      "v_cvt_pk_bf16_f32 v211, v178, v179\n"
      "v_mfma_f32_16x16x32_bf16 v[180:183], a[192:195], v[148:151], v[180:183]\n"
      "buffer_store_dwordx2 v[204:205], v20, s[8:11], s49 offen sc1\n"
      "v_mfma_f32_16x16x32_bf16 v[180:183], a[196:199], v[152:155], v[180:183]\n"
      "v_fmac_f32 v219, v176, v176\n"
      "v_add_f32 v218, v218, v177\n"
      "v_mfma_f32_16x16x32_bf16 v[180:183], a[200:203], v[156:159], v[180:183]\n"
      "s_waitcnt lgkmcnt(0)\n"
      "v_add_f32 v218, v218, v178\n"
      "v_cmp_gt_i32 vcc, v21, v22\n"
      "v_mfma_f32_16x16x32_bf16 v[180:183], a[204:207], v[160:163], v[180:183]\n"
      "v_fmac_f32 v219, v177, v177\n"
      "v_mfma_f32_16x16x32_bf16 v[180:183], a[208:211], v[164:167], v[180:183]\n"
      "v_fmac_f32_e64 v219, v178, v178\n"
      "v_add_f32_e64 v218, v218, v179\n"
      "v_mfma_f32_16x16x32_bf16 v[180:183], a[212:215], v[168:171], v[180:183]\n"
      "s_waitcnt vmcnt(10)\n"
      "v_fmac_f32_e64 v219, v179, v179\n"
      "v_lshlrev_b32_e64 v178, 16, v195\n"
      "v_mfma_f32_16x16x32_bf16 v[184:187], a[216:219], v[100:103], v[184:187]\n"
      "s_cbranch_vccnz .Lsp_vy1_27\n"
      ".Lbk_vy1_27:\n"
      "ds_read_b128 v[100:103], v10 offset:28672\n"
      "v_mfma_f32_16x16x32_bf16 v[184:187], a[220:223], v[104:107], v[184:187]\n"
      "v_lshlrev_b32 v172, 16, v192\n"
      "v_and_b32 v177, 0xffff0000, v194\n"
      "v_mfma_f32_16x16x32_bf16 v[184:187], a[224:227], v[108:111], v[184:187]\n"
      "v_and_b32 v179, 0xffff0000, v195\n"
      "s_add_u32 s49, s49, 24576\n"
      "v_mfma_f32_16x16x32_bf16 v[184:187], a[228:231], v[112:115], v[184:187]\n"
      "v_lshlrev_b32 v176, 16, v194\n"
      "s_sub_u32 s47, s46, s45\n"
      "v_mfma_f32_16x16x32_bf16 v[184:187], a[232:235], v[116:119], v[184:187]\n"
      "ds_read_b128 v[108:111], v10 offset:30720\n"
      "v_fmac_f32 v219, v180, v180\n"
      "v_mfma_f32_16x16x32_bf16 v[184:187], a[236:239], v[120:123], v[184:187]\n"
      "ds_read_b128 v[116:119], v10 offset:32768\n"
      "v_and_b32 v175, 0xffff0000, v193\n"
      "v_mfma_f32_16x16x32_bf16 v[184:187], a[240:243], v[124:127], v[184:187]\n"
      "buffer_store_dwordx4 v[208:211], v15, s[8:11], s38 offen sc1\n"
      "v_mfma_f32_16x16x32_bf16 v[184:187], a[244:247], v[128:131], v[184:187]\n"
      "ds_read_b128 v[120:123], v10 offset:33792\n"
      "v_mfma_f32_16x16x32_bf16 v[184:187], a[248:251], v[132:135], v[184:187]\n"
      "ds_read_b128 v[112:115], v10 offset:31744\n"
      "v_mfma_f32_16x16x32_bf16 v[184:187], a[252:255], v[136:139], v[184:187]\n"
      "ds_read_b128 v[104:107], v10 offset:29696\n"
      "v_cvt_pk_bf16_f32 v213, v182, v183\n"
      "v_mfma_f32_16x16x32_bf16 v[184:187], v[32:35], v[140:143], v[184:187]\n"
      "s_mov_b32 s46, s36\n"
      "ds_read_b128 v[128:131], v10 offset:35840\n"
      "v_add_f32 v218, v218, v180\n"
      "v_mfma_f32_16x16x32_bf16 v[184:187], v[36:39], v[144:147], v[184:187]\n"
      "v_mfma_f32_16x16x32_bf16 v[184:187], v[40:43], v[148:151], v[184:187]\n"
      "ds_read_b128 v[136:139], v10 offset:20480\n"
      "v_add_f32 v218, v218, v181\n"
      "v_mfma_f32_16x16x32_bf16 v[184:187], v[44:47], v[152:155], v[184:187]\n"
      "ds_read_b128 v[124:127], v10 offset:34816\n"
      "v_mfma_f32_16x16x32_bf16 v[184:187], v[48:51], v[156:159], v[184:187]\n"
      "ds_read_b128 v[144:147], v10 offset:22528\n"
      "v_lshlrev_b32 v174, 16, v193\n"
      "v_mfma_f32_16x16x32_bf16 v[184:187], v[52:55], v[160:163], v[184:187]\n"
      "ds_read2_b32 v[132:133], v8 offset0:0 offset1:32\n"
      "v_add_f32 v218, v218, v182\n"
      "v_mfma_f32_16x16x32_bf16 v[184:187], v[56:59], v[164:167], v[184:187]\n"
      "ds_read2_b32 v[134:135], v8 offset0:64 offset1:96\n"
      "v_and_b32 v173, 0xffff0000, v192\n"
      "v_mfma_f32_16x16x32_bf16 v[184:187], v[60:63], v[168:171], v[184:187]\n"
      "s_waitcnt lgkmcnt(5)\n"
      "buffer_load_dwordx4 v[192:195], v13, s[8:11], s47 offen sc1\n"
      "v_mfma_f32_16x16x32_bf16 v[188:191], v[64:67], v[100:103], 0\n"
      "ds_read_b128 v[160:163], v10 offset:26624\n"
      "v_mfma_f32_16x16x32_bf16 v[188:191], v[68:71], v[104:107], v[188:191]\n"
      "ds_read_b128 v[140:143], v10 offset:21504\n"
      "v_cvt_pk_bf16_f32 v212, v180, v181\n"
      "v_mfma_f32_16x16x32_bf16 v[188:191], v[72:75], v[108:111], v[188:191]\n"
      "v_add_u32 v21, 1, v21\n"
      "v_mfma_f32_16x16x32_bf16 v[188:191], v[76:79], v[112:115], v[188:191]\n"
      "ds_read2_b32 v[170:171], v9 offset0:64 offset1:96\n"
      "v_mfma_f32_16x16x32_bf16 v[188:191], v[80:83], v[116:119], v[188:191]\n"
      "ds_read_b128 v[152:155], v10 offset:24576\n"
      "v_mfma_f32_16x16x32_bf16 v[188:191], v[84:87], v[120:123], v[188:191]\n"
      "s_waitcnt lgkmcnt(4)\n"
      "ds_read_b128 v[164:167], v10 offset:27648\n"
      "v_fmac_f32 v219, v181, v181\n"
      "v_mfma_f32_16x16x32_bf16 v[188:191], v[88:91], v[124:127], v[188:191]\n"
      "ds_read_b128 v[148:151], v10 offset:23552\n"
      "v_fmac_f32 v219, v182, v182\n"
      "v_mfma_f32_16x16x32_bf16 v[188:191], v[92:95], v[128:131], v[188:191]\n"
      "ds_read_b128 v[156:159], v10 offset:25600\n"
      "v_cvt_pk_bf16_f32 v214, v184, v185\n"
      "v_mfma_f32_16x16x32_bf16 v[188:191], v[96:99], v[132:135], v[188:191]\n"
      "s_add_u32 m0, s39, 20480\n"
      "v_add_f32 v218, v218, v183\n"
      "v_mfma_f32_16x16x32_bf16 v[172:175], a[0:3], v[100:103], v[172:175]\n"
      "v_add_f32 v218, v218, v184\n"
      "v_fmac_f32 v219, v183, v183\n"
      "v_mfma_f32_16x16x32_bf16 v[172:175], a[4:7], v[104:107], v[172:175]\n"
      "ds_read2_b32 v[168:169], v9 offset0:0 offset1:32\n"
      "v_mfma_f32_16x16x32_bf16 v[172:175], a[8:11], v[108:111], v[172:175]\n"
      "v_add_f32 v218, v218, v185\n"
      "v_fmac_f32 v219, v184, v184\n"
      "v_mfma_f32_16x16x32_bf16 v[172:175], a[12:15], v[112:115], v[172:175]\n"
      "ds_write_b128 v17, v[188:191]\n"
      "v_mfma_f32_16x16x32_bf16 v[172:175], a[16:19], v[116:119], v[172:175]\n"
      "s_waitcnt vmcnt(10) lgkmcnt(3)\n"
      "v_fmac_f32 v219, v185, v185\n"
      "v_and_b32 v181, 0xffff0000, v196\n"
      "v_mfma_f32_16x16x32_bf16 v[172:175], a[20:23], v[120:123], v[172:175]\n"
      "v_add_f32 v218, v218, v186\n"
      "v_mfma_f32_16x16x32_bf16 v[172:175], a[24:27], v[124:127], v[172:175]\n"
      "v_fmac_f32 v219, v186, v186\n"
      "v_add_f32 v218, v218, v187\n"
      "v_mfma_f32_16x16x32_bf16 v[172:175], a[28:31], v[128:131], v[172:175]\n"
      "v_cvt_pk_bf16_f32 v215, v186, v187\n"
      "v_fmac_f32 v219, v187, v187\n"
      "v_mfma_f32_16x16x32_bf16 v[172:175], a[32:35], v[132:135], v[172:175]\n"
      "ds_write_b64 v18, v[218:219]\n"
      "v_mfma_f32_16x16x32_bf16 v[172:175], a[36:39], v[136:139], v[172:175]\n"
      "v_lshlrev_b32 v186, 16, v199\n"
      "v_mfma_f32_16x16x32_bf16 v[172:175], a[40:43], v[140:143], v[172:175]\n"
      "buffer_store_dwordx4 v[212:215], v15, s[8:11], s38 offen offset:1024 sc1\n"
      "v_mfma_f32_16x16x32_bf16 v[172:175], a[44:47], v[144:147], v[172:175]\n"
      "ds_write_b32 v23, v21\n"
      "v_mfma_f32_16x16x32_bf16 v[172:175], a[48:51], v[148:151], v[172:175]\n"
      "v_mfma_f32_16x16x32_bf16 v[172:175], a[52:55], v[152:155], v[172:175]\n"
      "s_waitcnt lgkmcnt(2)\n"
      "v_and_b32 v183, 0xffff0000, v197\n"
      "v_lshlrev_b32 v184, 16, v198\n"
      "v_mfma_f32_16x16x32_bf16 v[172:175], a[56:59], v[156:159], v[172:175]\n"
      "s_add_u32 s50, s50, 24576\n"
      "v_lshlrev_b32 v180, 16, v196\n"
      "v_mfma_f32_16x16x32_bf16 v[172:175], a[60:63], v[160:163], v[172:175]\n"
      "v_lshlrev_b32 v182, 16, v197\n"
      "v_and_b32 v185, 0xffff0000, v198\n"
      "v_mfma_f32_16x16x32_bf16 v[172:175], a[64:67], v[164:167], v[172:175]\n"
      "s_cmp_eq_u32 s36, s37\n"
      "s_cselect_b32 s48, 49152, s51\n"
      "v_and_b32 v187, 0xffff0000, v199\n"
      "v_mfma_f32_16x16x32_bf16 v[172:175], a[68:71], v[168:171], v[172:175]\n"
      "buffer_load_dwordx4 v[196:199], v13, s[8:11], s47 offen offset:1024 sc1\n"
      "v_mfma_f32_16x16x32_bf16 v[176:179], a[72:75], v[100:103], v[176:179]\n"
      "s_waitcnt vmcnt(11) lgkmcnt(0)\n"
      "v_cmp_ne_u32 vcc, s21, v28\n"
      "s_add_u32 s36, s36, s48\n"
      "v_mfma_f32_16x16x32_bf16 v[176:179], a[76:79], v[104:107], v[176:179]\n"
      "s_min_u32 s36, s36, s6\n"
      "v_mfma_f32_16x16x32_bf16 v[176:179], a[80:83], v[108:111], v[176:179]\n"
      "ds_read_b32 v22, v24\n"
      "v_mfma_f32_16x16x32_bf16 v[176:179], a[84:87], v[112:115], v[176:179]\n"
      "s_cbranch_vccnz .Lsp_v1_28\n"
      ".Lbk_v1_28:\n"
      "v_mfma_f32_16x16x32_bf16 v[176:179], a[88:91], v[116:119], v[176:179]\n"
      "buffer_load_dword v28, v11, s[8:11], s36 offen sc1\n"
      "v_mfma_f32_16x16x32_bf16 v[176:179], a[92:95], v[120:123], v[176:179]\n"
      "v_add_f32 v218, v172, v173\n"
      "v_mul_f32 v219, v172, v172\n"
      "v_mfma_f32_16x16x32_bf16 v[176:179], a[96:99], v[124:127], v[176:179]\n"
      "buffer_load_dwordx4 v0, s[8:11], s46 offen offset:0 sc1 lds\n"
      "v_mfma_f32_16x16x32_bf16 v[176:179], a[100:103], v[128:131], v[176:179]\n"
      "v_mfma_f32_16x16x32_bf16 v[176:179], a[104:107], v[132:135], v[176:179]\n"
      "s_waitcnt lgkmcnt(0)\n"
      "v_cmp_gt_i32 vcc, v21, v22\n"
      "v_fmac_f32 v219, v173, v173\n"
      "v_mfma_f32_16x16x32_bf16 v[176:179], a[108:111], v[136:139], v[176:179]\n"
      "v_cvt_pk_bf16_f32 v209, v174, v175\n"
      "s_sub_u32 s47, s46, s45\n"
      "v_mfma_f32_16x16x32_bf16 v[176:179], a[112:115], v[140:143], v[176:179]\n"
      "buffer_load_dwordx4 v0, s[8:11], s46 offen offset:1024 sc1 lds\n"
      "v_mfma_f32_16x16x32_bf16 v[176:179], a[116:119], v[144:147], v[176:179]\n"
      "s_cbranch_vccnz .Lsp_vz1_29\n"
      ".Lbk_vz1_29:\n"
      "ds_read_b128 v[204:207], v19\n"
      "v_mfma_f32_16x16x32_bf16 v[176:179], a[120:123], v[148:151], v[176:179]\n"
      "v_add_u32 v21, 1, v21\n"
      "v_mfma_f32_16x16x32_bf16 v[176:179], a[124:127], v[152:155], v[176:179]\n"
      "v_mfma_f32_16x16x32_bf16 v[176:179], a[128:131], v[156:159], v[176:179]\n"
      "s_waitcnt vmcnt(9)\n"
      "ds_write_b32 v23, v21\n"
      "v_mfma_f32_16x16x32_bf16 v[176:179], a[132:135], v[160:163], v[176:179]\n"
      "v_fmac_f32 v219, v174, v174\n"
      "v_mfma_f32_16x16x32_bf16 v[176:179], a[136:139], v[164:167], v[176:179]\n"
      "buffer_load_dwordx4 v0, s[8:11], s46 offen offset:3072 sc1 lds\n"
      "v_mfma_f32_16x16x32_bf16 v[176:179], a[140:143], v[168:171], v[176:179]\n"
      "v_cvt_pk_bf16_f32 v208, v172, v173\n"
      "v_add_f32 v218, v218, v174\n"
      "v_mfma_f32_16x16x32_bf16 v[180:183], a[144:147], v[100:103], v[180:183]\n"
      "s_waitcnt lgkmcnt(1)\n"
      "v_add_f32 v205, v205, v207\n"
      "v_mfma_f32_16x16x32_bf16 v[180:183], a[148:151], v[104:107], v[180:183]\n"
      "v_add_f32_dpp v205, v205, v205 quad_perm:[1,0,3,2] row_mask:0xf bank_mask:0xf\n"
      "v_mfma_f32_16x16x32_bf16 v[180:183], a[152:155], v[108:111], v[180:183]\n"
      "buffer_load_dwordx4 v0, s[8:11], s46 offen offset:2048 sc1 lds\n"
      "v_mfma_f32_16x16x32_bf16 v[180:183], a[156:159], v[112:115], v[180:183]\n"
      "v_add_f32 v204, v204, v206\n"
      "v_mfma_f32_16x16x32_bf16 v[180:183], a[160:163], v[116:119], v[180:183]\n"
      "v_add_f32_dpp v204, v204, v204 quad_perm:[1,0,3,2] row_mask:0xf bank_mask:0xf\n"
      "v_add_f32_dpp v205, v205, v205 quad_perm:[2,3,0,1] row_mask:0xf bank_mask:0xf\n"
      "v_mfma_f32_16x16x32_bf16 v[180:183], a[164:167], v[120:123], v[180:183]\n"
      "s_add_u32 m0, s40, 20480\n"
      "v_add_f32_dpp v204, v204, v204 quad_perm:[2,3,0,1] row_mask:0xf bank_mask:0xf\n"
      "v_cvt_pk_bf16_f32 v210, v176, v177\n"
      "v_mfma_f32_16x16x32_bf16 v[180:183], a[168:171], v[124:127], v[180:183]\n"
      "buffer_load_dwordx4 v1, s[8:11], s46 offen offset:0 sc1 lds\n"
      "v_mfma_f32_16x16x32_bf16 v[180:183], a[172:175], v[128:131], v[180:183]\n"
      "s_add_u32 s38, s38, 24576\n"
      "v_add_f32_dpp v204, v204, v204 row_half_mirror row_mask:0xf bank_mask:0xf\n"
      "v_add_f32 v218, v218, v175\n"
      "v_mfma_f32_16x16x32_bf16 v[180:183], a[176:179], v[132:135], v[180:183]\n"
      "v_fmac_f32 v219, v175, v175\n"
      "v_add_f32 v218, v218, v176\n"
      "v_mfma_f32_16x16x32_bf16 v[180:183], a[180:183], v[136:139], v[180:183]\n"
      "v_fmac_f32 v219, v176, v176\n"
      "v_fmac_f32 v219, v177, v177\n"
      "v_mfma_f32_16x16x32_bf16 v[180:183], a[184:187], v[140:143], v[180:183]\n"
      "s_waitcnt vmcnt(10) lgkmcnt(0)\n"
      "buffer_store_dword v25, v12, s[8:11], s50 offen sc1\n"
      "v_mfma_f32_16x16x32_bf16 v[180:183], a[188:191], v[144:147], v[180:183]\n"
      "ds_read_b32 v22, v24\n"
      "v_add_f32_dpp v205, v205, v205 row_half_mirror row_mask:0xf bank_mask:0xf\n"
      "v_mfma_f32_16x16x32_bf16 v[180:183], a[192:195], v[148:151], v[180:183]\n"
      "v_mul_f32 v204, v27, v204\n"
      "v_add_f32 v218, v218, v177\n"
      "v_mfma_f32_16x16x32_bf16 v[180:183], a[196:199], v[152:155], v[180:183]\n"
      "v_fmaak_f32 v205, v27, v205, 0x36a7c5ac\n"
      "v_fmac_f32 v219, v178, v178\n"
      "v_mfma_f32_16x16x32_bf16 v[180:183], a[200:203], v[156:159], v[180:183]\n"
      "buffer_store_dwordx2 v[204:205], v20, s[8:11], s49 offen sc1\n"
      "v_mfma_f32_16x16x32_bf16 v[180:183], a[204:207], v[160:163], v[180:183]\n"
      "s_waitcnt vmcnt(10)\n"
      "v_and_b32 v177, 0xffff0000, v194\n"
      "v_add_f32 v218, v218, v178\n"
      "v_mfma_f32_16x16x32_bf16 v[180:183], a[208:211], v[164:167], v[180:183]\n"
      "s_waitcnt lgkmcnt(0)\n"
      "v_cmp_gt_i32 vcc, v21, v22\n"
      "v_and_b32 v173, 0xffff0000, v192\n"
      "v_mfma_f32_16x16x32_bf16 v[180:183], a[212:215], v[168:171], v[180:183]\n"
      "v_mfma_f32_16x16x32_bf16 v[184:187], a[216:219], v[100:103], v[184:187]\n"
      "v_fmac_f32 v219, v179, v179\n"
      "v_add_f32 v218, v218, v179\n"
      "v_mfma_f32_16x16x32_bf16 v[184:187], a[220:223], v[104:107], v[184:187]\n"
      "s_cbranch_vccnz .Lsp_vy0_30\n"
      ".Lbk_vy0_30:\n"
      "ds_read_b128 v[104:107], v0 offset:9216\n"
      "v_mfma_f32_16x16x32_bf16 v[184:187], a[224:227], v[108:111], v[184:187]\n"
      "v_cvt_pk_bf16_f32 v211, v178, v179\n"
      "v_mfma_f32_16x16x32_bf16 v[184:187], a[228:231], v[112:115], v[184:187]\n"
      "ds_read_b128 v[100:103], v0 offset:8192\n"
      "v_lshlrev_b32 v172, 16, v192\n"
      "v_mfma_f32_16x16x32_bf16 v[184:187], a[232:235], v[116:119], v[184:187]\n"
      "s_mov_b32 s46, s36\n"
      "ds_read_b128 v[116:119], v0 offset:12288\n"
      "v_and_b32 v179, 0xffff0000, v195\n"
      "v_mfma_f32_16x16x32_bf16 v[184:187], a[236:239], v[120:123], v[184:187]\n"
      "v_fmac_f32 v219, v180, v180\n"
      "v_and_b32 v175, 0xffff0000, v193\n"
      "v_mfma_f32_16x16x32_bf16 v[184:187], a[240:243], v[124:127], v[184:187]\n"
      "ds_read_b128 v[112:115], v0 offset:11264\n"
      "v_lshlrev_b32 v174, 16, v193\n"
      "v_mfma_f32_16x16x32_bf16 v[184:187], a[244:247], v[128:131], v[184:187]\n"
      "ds_read_b128 v[120:123], v0 offset:13312\n"
      "v_fmac_f32 v219, v181, v181\n"
      "v_mfma_f32_16x16x32_bf16 v[184:187], a[248:251], v[132:135], v[184:187]\n"
      "ds_read_b128 v[124:127], v0 offset:14336\n"
      "v_add_f32 v218, v218, v180\n"
      "v_mfma_f32_16x16x32_bf16 v[184:187], a[252:255], v[136:139], v[184:187]\n"
      "s_add_u32 s49, s49, 24576\n"
      "ds_read_b128 v[128:131], v0 offset:15360\n"
      "v_add_f32 v218, v218, v181\n"
      "v_mfma_f32_16x16x32_bf16 v[184:187], v[32:35], v[140:143], v[184:187]\n"
      "v_add_f32 v218, v218, v182\n"
      "v_add_f32 v218, v218, v183\n"
      "v_mfma_f32_16x16x32_bf16 v[184:187], v[36:39], v[144:147], v[184:187]\n"
      "buffer_store_dwordx4 v[208:211], v15, s[8:11], s38 offen sc1\n"
      "v_mfma_f32_16x16x32_bf16 v[184:187], v[40:43], v[148:151], v[184:187]\n"
      "ds_read_b128 v[108:111], v0 offset:10240\n"
      "v_fmac_f32 v219, v182, v182\n"
      "v_mfma_f32_16x16x32_bf16 v[184:187], v[44:47], v[152:155], v[184:187]\n"
      "ds_read2_b32 v[132:133], v2 offset0:0 offset1:32\n"
      "v_cvt_pk_bf16_f32 v213, v182, v183\n"
      "v_mfma_f32_16x16x32_bf16 v[184:187], v[48:51], v[156:159], v[184:187]\n"
      "ds_read_b128 v[144:147], v0 offset:2048\n"
      "v_lshlrev_b32 v178, 16, v195\n"
      "v_mfma_f32_16x16x32_bf16 v[184:187], v[52:55], v[160:163], v[184:187]\n"
      "ds_read2_b32 v[134:135], v2 offset0:64 offset1:96\n"
      "v_cvt_pk_bf16_f32 v212, v180, v181\n"
      "v_mfma_f32_16x16x32_bf16 v[184:187], v[56:59], v[164:167], v[184:187]\n"
      "s_waitcnt lgkmcnt(4)\n"
      "ds_read_b128 v[152:155], v0 offset:4096\n"
      "v_fmac_f32 v219, v183, v183\n"
      "v_mfma_f32_16x16x32_bf16 v[184:187], v[60:63], v[168:171], v[184:187]\n"
      "s_sub_u32 s20, s20, 1\n"
      "s_cbranch_scc0 .Lv_loop\n"
      ".Lv_tail:\n"
      "s_nop 8\n"
      "v_add_f32 v218, v218, v184\n"
      "v_fmac_f32 v219, v184, v184\n"
      "v_cvt_pk_bf16_f32 v214, v184, v185\n"
      "v_fmac_f32 v219, v185, v185\n"
      "v_add_f32 v218, v218, v185\n"
      "v_cvt_pk_bf16_f32 v215, v186, v187\n"
      "v_add_f32 v218, v218, v186\n"
      "v_add_f32 v218, v218, v187\n"
      "v_fmac_f32 v219, v186, v186\n"
      "v_fmac_f32 v219, v187, v187\n"
      "ds_write_b64 v18, v[218:219]\n"
      "s_waitcnt lgkmcnt(0)\n"
      "s_barrier\n"
      "buffer_store_dwordx4 v[212:215], v15, s[8:11], s38 offen offset:1024 sc1\n"
      "s_add_u32 s50, s50, 24576\n"
      "s_add_u32 s38, s38, 24576\n"
      "ds_read_b128 v[204:207], v19\n"
      "s_waitcnt lgkmcnt(0)\n"
      "v_add_f32 v204, v204, v206\n"
      "v_add_f32 v205, v205, v207\n"
      "s_nop 0\n"
      "v_add_f32_dpp v204, v204, v204 quad_perm:[1,0,3,2] row_mask:0xf bank_mask:0xf\n"
      "v_add_f32_dpp v205, v205, v205 quad_perm:[1,0,3,2] row_mask:0xf bank_mask:0xf\n"
      "s_nop 0\n"
      "v_add_f32_dpp v204, v204, v204 quad_perm:[2,3,0,1] row_mask:0xf bank_mask:0xf\n"
      "v_add_f32_dpp v205, v205, v205 quad_perm:[2,3,0,1] row_mask:0xf bank_mask:0xf\n"
      "s_nop 0\n"
      "v_add_f32_dpp v204, v204, v204 row_half_mirror row_mask:0xf bank_mask:0xf\n"
      "v_mul_f32 v204, v27, v204\n"
      "v_add_f32_dpp v205, v205, v205 row_half_mirror row_mask:0xf bank_mask:0xf\n"
      "v_fmaak_f32 v205, v27, v205, 0x36a7c5ac\n"
      "s_waitcnt vmcnt(2)\n"
      "buffer_store_dword v25, v12, s[8:11], s50 offen sc1\n"
      "buffer_store_dwordx2 v[204:205], v20, s[8:11], s49 offen sc1\n"
      "s_add_u32 s49, s49, 24576\n"
      "s_add_u32 s50, s50, 24576\n"
      "s_waitcnt vmcnt(0)\n"
      "buffer_store_dword v25, v12, s[8:11], s50 offen sc1\n"
      "s_add_u32 s50, s50, 24576\n"
      "s_endpgm\n"
      ".Lsp_w0_1:\n"
      "s_mov_b32 s22, s23\n"
      ".Lsl_w0_1:\n"
      "buffer_load_dword v28, v11, s[8:11], s46 offen sc1\n"
      "s_waitcnt vmcnt(0)\n"
      "v_cmp_ne_u32 vcc, s21, v28\n"
      "s_cbranch_vccz .Lbk_w0_1\n"
      "s_sleep 4\n"
      "s_sub_u32 s22, s22, 1\n"
      "s_cbranch_scc0 .Lsl_w0_1\n"
      "s_mov_b32 s23, 0\n"
      "s_branch .Lbk_w0_1\n"
      ".Lsp_w1_2:\n"
      "s_mov_b32 s22, s23\n"
      ".Lsl_w1_2:\n"
      "buffer_load_dword v28, v11, s[8:11], s46 offen sc1\n"
      "s_waitcnt vmcnt(0)\n"
      "v_cmp_ne_u32 vcc, s21, v28\n"
      "s_cbranch_vccz .Lbk_w1_2\n"
      "s_sleep 4\n"
      "s_sub_u32 s22, s22, 1\n"
      "s_cbranch_scc0 .Lsl_w1_2\n"
      "s_mov_b32 s23, 0\n"
      "s_branch .Lbk_w1_2\n"
      ".Lsp_wy0_3:\n"
      "s_lshl_b32 s22, s23, 4\n"
      ".Lsl_wy0_3:\n"
      "ds_read_b32 v22, v24\n"
      "s_waitcnt lgkmcnt(0)\n"
      "v_cmp_gt_i32 vcc, v21, v22\n"
      "s_cbranch_vccz .Lbk_wy0_3\n"
      "s_sub_u32 s22, s22, 1\n"
      "s_cbranch_scc0 .Lsl_wy0_3\n"
      "s_mov_b32 s23, 0\n"
      "s_branch .Lbk_wy0_3\n"
      ".Lsp_w0_4:\n"
      "s_mov_b32 s22, s23\n"
      "buffer_load_dword v28, v11, s[8:11], s46 offen sc1\n"
      "s_waitcnt vmcnt(0)\n"
      "v_cmp_ne_u32 vcc, s21, v28\n"
      "s_add_u32 s30, s50, 0\n"
      "buffer_store_dword v25, v12, s[8:11], s30 offen sc1\n"
      "s_cbranch_vccz .Lbk_w0_4\n"
      ".Lsl_w0_4:\n"
      "buffer_load_dword v28, v11, s[8:11], s46 offen sc1\n"
      "s_waitcnt vmcnt(0)\n"
      "v_cmp_ne_u32 vcc, s21, v28\n"
      "s_cbranch_vccz .Lbk_w0_4\n"
      "s_sleep 4\n"
      "s_sub_u32 s22, s22, 1\n"
      "s_cbranch_scc0 .Lsl_w0_4\n"
      "s_mov_b32 s23, 0\n"
      "s_branch .Lbk_w0_4\n"
      ".Lsp_wz0_5:\n"
      "s_lshl_b32 s22, s23, 4\n"
      ".Lsl_wz0_5:\n"
      "ds_read_b32 v22, v24\n"
      "s_waitcnt lgkmcnt(0)\n"
      "v_cmp_gt_i32 vcc, v21, v22\n"
      "s_cbranch_vccz .Lbk_wz0_5\n"
      "s_sub_u32 s22, s22, 1\n"
      "s_cbranch_scc0 .Lsl_wz0_5\n"
      "s_mov_b32 s23, 0\n"
      "s_branch .Lbk_wz0_5\n"
      ".Lsp_wy1_6:\n"
      "s_lshl_b32 s22, s23, 4\n"
      ".Lsl_wy1_6:\n"
      "ds_read_b32 v22, v24\n"
      "s_waitcnt lgkmcnt(0)\n"
      "v_cmp_gt_i32 vcc, v21, v22\n"
      "s_cbranch_vccz .Lbk_wy1_6\n"
      "s_sub_u32 s22, s22, 1\n"
      "s_cbranch_scc0 .Lsl_wy1_6\n"
      "s_mov_b32 s23, 0\n"
      "s_branch .Lbk_wy1_6\n"
      ".Lsp_w1_7:\n"
      "s_mov_b32 s22, s23\n"
      "buffer_load_dword v28, v11, s[8:11], s46 offen sc1\n"
      "s_waitcnt vmcnt(0)\n"
      "v_cmp_ne_u32 vcc, s21, v28\n"
      "s_add_u32 s30, s50, 24576\n"
      "buffer_store_dword v25, v12, s[8:11], s30 offen sc1\n"
      "s_cbranch_vccz .Lbk_w1_7\n"
      ".Lsl_w1_7:\n"
      "buffer_load_dword v28, v11, s[8:11], s46 offen sc1\n"
      "s_waitcnt vmcnt(0)\n"
      "v_cmp_ne_u32 vcc, s21, v28\n"
      "s_cbranch_vccz .Lbk_w1_7\n"
      "s_sleep 4\n"
      "s_sub_u32 s22, s22, 1\n"
      "s_cbranch_scc0 .Lsl_w1_7\n"
      "s_mov_b32 s23, 0\n"
      "s_branch .Lbk_w1_7\n"
      ".Lsp_wz1_8:\n"
      "s_lshl_b32 s22, s23, 4\n"
      ".Lsl_wz1_8:\n"
      "ds_read_b32 v22, v24\n"
      "s_waitcnt lgkmcnt(0)\n"
      "v_cmp_gt_i32 vcc, v21, v22\n"
      "s_cbranch_vccz .Lbk_wz1_8\n"
      "s_sub_u32 s22, s22, 1\n"
      "s_cbranch_scc0 .Lsl_wz1_8\n"
      "s_mov_b32 s23, 0\n"
      "s_branch .Lbk_wz1_8\n"
      ".Lsp_wy0_9:\n"
      "s_lshl_b32 s22, s23, 4\n"
      ".Lsl_wy0_9:\n"
      "ds_read_b32 v22, v24\n"
      "s_waitcnt lgkmcnt(0)\n"
      "v_cmp_gt_i32 vcc, v21, v22\n"
      "s_cbranch_vccz .Lbk_wy0_9\n"
      "s_sub_u32 s22, s22, 1\n"
      "s_cbranch_scc0 .Lsl_wy0_9\n"
      "s_mov_b32 s23, 0\n"
      "s_branch .Lbk_wy0_9\n"
      ".Lsp_w0_10:\n"
      "s_mov_b32 s22, s23\n"
      "buffer_load_dword v28, v11, s[8:11], s46 offen sc1\n"
      "s_waitcnt vmcnt(0)\n"
      "v_cmp_ne_u32 vcc, s21, v28\n"
      "s_add_u32 s30, s50, 0\n"
      "buffer_store_dword v25, v12, s[8:11], s30 offen sc1\n"
      "s_cbranch_vccz .Lbk_w0_10\n"
      ".Lsl_w0_10:\n"
      "buffer_load_dword v28, v11, s[8:11], s46 offen sc1\n"
      "s_waitcnt vmcnt(0)\n"
      "v_cmp_ne_u32 vcc, s21, v28\n"
      "s_cbranch_vccz .Lbk_w0_10\n"
      "s_sleep 4\n"
      "s_sub_u32 s22, s22, 1\n"
      "s_cbranch_scc0 .Lsl_w0_10\n"
      "s_mov_b32 s23, 0\n"
      "s_branch .Lbk_w0_10\n"
      ".Lsp_wz0_11:\n"
      "s_lshl_b32 s22, s23, 4\n"
      ".Lsl_wz0_11:\n"
      "ds_read_b32 v22, v24\n"
      "s_waitcnt lgkmcnt(0)\n"
      "v_cmp_gt_i32 vcc, v21, v22\n"
      "s_cbranch_vccz .Lbk_wz0_11\n"
      "s_sub_u32 s22, s22, 1\n"
      "s_cbranch_scc0 .Lsl_wz0_11\n"
      "s_mov_b32 s23, 0\n"
      "s_branch .Lbk_wz0_11\n"
      ".Lsp_wy1_12:\n"
      "s_lshl_b32 s22, s23, 4\n"
      ".Lsl_wy1_12:\n"
      "ds_read_b32 v22, v24\n"
      "s_waitcnt lgkmcnt(0)\n"
      "v_cmp_gt_i32 vcc, v21, v22\n"
      "s_cbranch_vccz .Lbk_wy1_12\n"
      "s_sub_u32 s22, s22, 1\n"
      "s_cbranch_scc0 .Lsl_wy1_12\n"
      "s_mov_b32 s23, 0\n"
      "s_branch .Lbk_wy1_12\n"
      ".Lsp_w1_13:\n"
      "s_mov_b32 s22, s23\n"
      "buffer_load_dword v28, v11, s[8:11], s46 offen sc1\n"
      "s_waitcnt vmcnt(0)\n"
      "v_cmp_ne_u32 vcc, s21, v28\n"
      "s_add_u32 s30, s50, 24576\n"
      "buffer_store_dword v25, v12, s[8:11], s30 offen sc1\n"
      "s_cbranch_vccz .Lbk_w1_13\n"
      ".Lsl_w1_13:\n"
      "buffer_load_dword v28, v11, s[8:11], s46 offen sc1\n"
      "s_waitcnt vmcnt(0)\n"
      "v_cmp_ne_u32 vcc, s21, v28\n"
      "s_cbranch_vccz .Lbk_w1_13\n"
      "s_sleep 4\n"
      "s_sub_u32 s22, s22, 1\n"
      "s_cbranch_scc0 .Lsl_w1_13\n"
      "s_mov_b32 s23, 0\n"
      "s_branch .Lbk_w1_13\n"
      ".Lsp_wz1_14:\n"
      "s_lshl_b32 s22, s23, 4\n"
      ".Lsl_wz1_14:\n"
      "ds_read_b32 v22, v24\n"
      "s_waitcnt lgkmcnt(0)\n"
      "v_cmp_gt_i32 vcc, v21, v22\n"
      "s_cbranch_vccz .Lbk_wz1_14\n"
      "s_sub_u32 s22, s22, 1\n"
      "s_cbranch_scc0 .Lsl_wz1_14\n"
      "s_mov_b32 s23, 0\n"
      "s_branch .Lbk_wz1_14\n"
      ".Lsp_wy0_15:\n"
      "s_lshl_b32 s22, s23, 4\n"
      ".Lsl_wy0_15:\n"
      "ds_read_b32 v22, v24\n"
      "s_waitcnt lgkmcnt(0)\n"
      "v_cmp_gt_i32 vcc, v21, v22\n"
      "s_cbranch_vccz .Lbk_wy0_15\n"
      "s_sub_u32 s22, s22, 1\n"
      "s_cbranch_scc0 .Lsl_wy0_15\n"
      "s_mov_b32 s23, 0\n"
      "s_branch .Lbk_wy0_15\n"
      ".Lsp_v0_16:\n"
      "s_mov_b32 s22, s23\n"
      ".Lsl_v0_16:\n"
      "buffer_load_dword v28, v11, s[8:11], s46 offen sc1\n"
      "s_waitcnt vmcnt(0)\n"
      "v_cmp_ne_u32 vcc, s21, v28\n"
      "s_cbranch_vccz .Lbk_v0_16\n"
      "s_sleep 4\n"
      "s_sub_u32 s22, s22, 1\n"
      "s_cbranch_scc0 .Lsl_v0_16\n"
      "s_mov_b32 s23, 0\n"
      "s_branch .Lbk_v0_16\n"
      ".Lsp_v1_17:\n"
      "s_mov_b32 s22, s23\n"
      ".Lsl_v1_17:\n"
      "buffer_load_dword v28, v11, s[8:11], s46 offen sc1\n"
      "s_waitcnt vmcnt(0)\n"
      "v_cmp_ne_u32 vcc, s21, v28\n"
      "s_cbranch_vccz .Lbk_v1_17\n"
      "s_sleep 4\n"
      "s_sub_u32 s22, s22, 1\n"
      "s_cbranch_scc0 .Lsl_v1_17\n"
      "s_mov_b32 s23, 0\n"
      "s_branch .Lbk_v1_17\n"
      ".Lsp_vy0_18:\n"
      "s_lshl_b32 s22, s23, 4\n"
      ".Lsl_vy0_18:\n"
      "ds_read_b32 v22, v24\n"
      "s_waitcnt lgkmcnt(0)\n"
      "v_cmp_gt_i32 vcc, v21, v22\n"
      "s_cbranch_vccz .Lbk_vy0_18\n"
      "s_sub_u32 s22, s22, 1\n"
      "s_cbranch_scc0 .Lsl_vy0_18\n"
      "s_mov_b32 s23, 0\n"
      "s_branch .Lbk_vy0_18\n"
      ".Lsp_v0_19:\n"
      "s_mov_b32 s22, s23\n"
      "buffer_load_dword v28, v11, s[8:11], s46 offen sc1\n"
      "s_waitcnt vmcnt(0)\n"
      "v_cmp_ne_u32 vcc, s21, v28\n"
      "s_add_u32 s30, s50, 0\n"
      "buffer_store_dword v25, v12, s[8:11], s30 offen sc1\n"
      "s_cbranch_vccz .Lbk_v0_19\n"
      ".Lsl_v0_19:\n"
      "buffer_load_dword v28, v11, s[8:11], s46 offen sc1\n"
      "s_waitcnt vmcnt(0)\n"
      "v_cmp_ne_u32 vcc, s21, v28\n"
      "s_cbranch_vccz .Lbk_v0_19\n"
      "s_sleep 4\n"
      "s_sub_u32 s22, s22, 1\n"
      "s_cbranch_scc0 .Lsl_v0_19\n"
      "s_mov_b32 s23, 0\n"
      "s_branch .Lbk_v0_19\n"
      ".Lsp_vz0_20:\n"
      "s_lshl_b32 s22, s23, 4\n"
      ".Lsl_vz0_20:\n"
      "ds_read_b32 v22, v24\n"
      "s_waitcnt lgkmcnt(0)\n"
      "v_cmp_gt_i32 vcc, v21, v22\n"
      "s_cbranch_vccz .Lbk_vz0_20\n"
      "s_sub_u32 s22, s22, 1\n"
      "s_cbranch_scc0 .Lsl_vz0_20\n"
      "s_mov_b32 s23, 0\n"
      "s_branch .Lbk_vz0_20\n"
      ".Lsp_vy1_21:\n"
      "s_lshl_b32 s22, s23, 4\n"
      ".Lsl_vy1_21:\n"
      "ds_read_b32 v22, v24\n"
      "s_waitcnt lgkmcnt(0)\n"
      "v_cmp_gt_i32 vcc, v21, v22\n"
      "s_cbranch_vccz .Lbk_vy1_21\n"
      "s_sub_u32 s22, s22, 1\n"
      "s_cbranch_scc0 .Lsl_vy1_21\n"
      "s_mov_b32 s23, 0\n"
      "s_branch .Lbk_vy1_21\n"
      ".Lsp_v1_22:\n"
      "s_mov_b32 s22, s23\n"
      "buffer_load_dword v28, v11, s[8:11], s46 offen sc1\n"
      "s_waitcnt vmcnt(0)\n"
      "v_cmp_ne_u32 vcc, s21, v28\n"
      "s_add_u32 s30, s50, 0\n"
      "buffer_store_dword v25, v12, s[8:11], s30 offen sc1\n"
      "s_cbranch_vccz .Lbk_v1_22\n"
      ".Lsl_v1_22:\n"
      "buffer_load_dword v28, v11, s[8:11], s46 offen sc1\n"
      "s_waitcnt vmcnt(0)\n"
      "v_cmp_ne_u32 vcc, s21, v28\n"
      "s_cbranch_vccz .Lbk_v1_22\n"
      "s_sleep 4\n"
      "s_sub_u32 s22, s22, 1\n"
      "s_cbranch_scc0 .Lsl_v1_22\n"
      "s_mov_b32 s23, 0\n"
      "s_branch .Lbk_v1_22\n"
      ".Lsp_vz1_23:\n"
      "s_lshl_b32 s22, s23, 4\n"
      ".Lsl_vz1_23:\n"
      "ds_read_b32 v22, v24\n"
      "s_waitcnt lgkmcnt(0)\n"
      "v_cmp_gt_i32 vcc, v21, v22\n"
      "s_cbranch_vccz .Lbk_vz1_23\n"
      "s_sub_u32 s22, s22, 1\n"
      "s_cbranch_scc0 .Lsl_vz1_23\n"
      "s_mov_b32 s23, 0\n"
      "s_branch .Lbk_vz1_23\n"
      ".Lsp_vy0_24:\n"
      "s_lshl_b32 s22, s23, 4\n"
      ".Lsl_vy0_24:\n"
      "ds_read_b32 v22, v24\n"
      "s_waitcnt lgkmcnt(0)\n"
      "v_cmp_gt_i32 vcc, v21, v22\n"
      "s_cbranch_vccz .Lbk_vy0_24\n"
      "s_sub_u32 s22, s22, 1\n"
      "s_cbranch_scc0 .Lsl_vy0_24\n"
      "s_mov_b32 s23, 0\n"
      "s_branch .Lbk_vy0_24\n"
      ".Lsp_v0_25:\n"
      "s_mov_b32 s22, s23\n"
      "buffer_load_dword v28, v11, s[8:11], s46 offen sc1\n"
      "s_waitcnt vmcnt(0)\n"
      "v_cmp_ne_u32 vcc, s21, v28\n"
      "s_add_u32 s30, s50, 0\n"
      "buffer_store_dword v25, v12, s[8:11], s30 offen sc1\n"
      "s_cbranch_vccz .Lbk_v0_25\n"
      ".Lsl_v0_25:\n"
      "buffer_load_dword v28, v11, s[8:11], s46 offen sc1\n"
      "s_waitcnt vmcnt(0)\n"
      "v_cmp_ne_u32 vcc, s21, v28\n"
      "s_cbranch_vccz .Lbk_v0_25\n"
      "s_sleep 4\n"
      "s_sub_u32 s22, s22, 1\n"
      "s_cbranch_scc0 .Lsl_v0_25\n"
      "s_mov_b32 s23, 0\n"
      "s_branch .Lbk_v0_25\n"
      ".Lsp_vz0_26:\n"
      "s_lshl_b32 s22, s23, 4\n"
      ".Lsl_vz0_26:\n"
      "ds_read_b32 v22, v24\n"
      "s_waitcnt lgkmcnt(0)\n"
      "v_cmp_gt_i32 vcc, v21, v22\n"
      "s_cbranch_vccz .Lbk_vz0_26\n"
      "s_sub_u32 s22, s22, 1\n"
      "s_cbranch_scc0 .Lsl_vz0_26\n"
      "s_mov_b32 s23, 0\n"
      "s_branch .Lbk_vz0_26\n"
      ".Lsp_vy1_27:\n"
      "s_lshl_b32 s22, s23, 4\n"
      ".Lsl_vy1_27:\n"
      "ds_read_b32 v22, v24\n"
      "s_waitcnt lgkmcnt(0)\n"
      "v_cmp_gt_i32 vcc, v21, v22\n"
      "s_cbranch_vccz .Lbk_vy1_27\n"
      "s_sub_u32 s22, s22, 1\n"
      "s_cbranch_scc0 .Lsl_vy1_27\n"
      "s_mov_b32 s23, 0\n"
      "s_branch .Lbk_vy1_27\n"
      ".Lsp_v1_28:\n"
      "s_mov_b32 s22, s23\n"
      "buffer_load_dword v28, v11, s[8:11], s46 offen sc1\n"
      "s_waitcnt vmcnt(0)\n"
      "v_cmp_ne_u32 vcc, s21, v28\n"
      "s_add_u32 s30, s50, 0\n"
      "buffer_store_dword v25, v12, s[8:11], s30 offen sc1\n"
      "s_cbranch_vccz .Lbk_v1_28\n"
      ".Lsl_v1_28:\n"
      "buffer_load_dword v28, v11, s[8:11], s46 offen sc1\n"
      "s_waitcnt vmcnt(0)\n"
      "v_cmp_ne_u32 vcc, s21, v28\n"
      "s_cbranch_vccz .Lbk_v1_28\n"
      "s_sleep 4\n"
      "s_sub_u32 s22, s22, 1\n"
      "s_cbranch_scc0 .Lsl_v1_28\n"
      "s_mov_b32 s23, 0\n"
      "s_branch .Lbk_v1_28\n"
      ".Lsp_vz1_29:\n"
      "s_lshl_b32 s22, s23, 4\n"
      ".Lsl_vz1_29:\n"
      "ds_read_b32 v22, v24\n"
      "s_waitcnt lgkmcnt(0)\n"
      "v_cmp_gt_i32 vcc, v21, v22\n"
      "s_cbranch_vccz .Lbk_vz1_29\n"
      "s_sub_u32 s22, s22, 1\n"
      "s_cbranch_scc0 .Lsl_vz1_29\n"
      "s_mov_b32 s23, 0\n"
      "s_branch .Lbk_vz1_29\n"
      ".Lsp_vy0_30:\n"
      "s_lshl_b32 s22, s23, 4\n"
      ".Lsl_vy0_30:\n"
      "ds_read_b32 v22, v24\n"
      "s_waitcnt lgkmcnt(0)\n"
      "v_cmp_gt_i32 vcc, v21, v22\n"
      "s_cbranch_vccz .Lbk_vy0_30\n"
      "s_sub_u32 s22, s22, 1\n"
      "s_cbranch_scc0 .Lsl_vy0_30\n"
      "s_mov_b32 s23, 0\n"
      "s_branch .Lbk_vy0_30\n"
      ".Lw_last:\n"
      :
      : "{s[8:11]}"(rsWS), "{s[12:15]}"(rsA), "{v0}"(v_l16), "{v1}"(v_l5), "{v2}"(v_ex[0]), "{v3}"(v_ex[1]), "{v4}"(v_ex[2]), "{v5}"(v_ex[3]), "{v6}"(v_ex[4]), "{v7}"(v_ex[5]), "{v8}"(v_ex[6]), "{v9}"(v_ex[7]), "{v10}"(v_l2), "{v11}"(v_fa), "{v12}"(v_ra), "{v13}"(v_rp), "{v14}"(v_rs), "{v15}"(v_xp), "{v16}"(v_xs), "{v17}"(v_ew), "{v18}"(v_sw), "{v19}"(v_sr), "{v20}"(v_ss), "{v22}"(v_al), "{v23}"(v_yw), "{v24}"(v_yr), "{v26}"(v_hi), "{v100}"(v_vo[0]), "{v101}"(v_vo[1]), "{v102}"(v_vo[2]), "{v103}"(v_vo[3]), "{v104}"(v_vo[4]), "{v105}"(v_vo[5]), "{v106}"(v_vo[6]), "{v107}"(v_vo[7]), "{v108}"(v_vo[8]), "{v109}"(v_vp[0]), "{v110}"(v_vp[1]), "{v111}"(v_vp[2]), "{v112}"(v_vp[3]), "{v113}"(v_vp[4]), "{v114}"(v_vp[5]), "{v115}"(v_vp[6]), "{v116}"(v_vp[7]), "{v117}"(v_vp[8]), "{v118}"(v_t0), "{v119}"(v_t1), "{v120}"(v_ts), "{v121}"(v_pw), "{s36}"(s_sx), "{s37}"(s_ev), "{s38}"(s_sy), "{s39}"(s_m4), "{s40}"(s_m5), "{s41}"(s_m9), "{s42}"(s_so), "{s43}"(s_s8), "{s44}"(s_wg), "{s45}"(s_rb)
      : "v255", "a255", "s99", "vcc", "scc", "memory");

  } else if (slot < 14) {
    const unsigned tr = ((xcd + DSH) & 7) * 3 + (slot - 5) / 3, n3 = (slot - 5) % 3, ks = unit_ks(tr), gr = unit_gr(tr);
    const u32x4 rsA = srd(Wd, 4608 * 576 * 2), rsB = srd(out, 2048 * 576 * 2), rsC = srd(X, 2048 * 576 * 2);
    const unsigned v_l16b = v_l16 + 4096, v_l8 = 1024 + lane * 8, f0 = 24 * ks + 6 * w;
    const unsigned v_fa = 147456 - f0 * 1024 + (f0 + (l8 < 5 ? l8 : 5)) * 128 + l7 / 4, v_ga = (l8 < 4 ? l8 : 4) * 4 * PW + 1536 + l7 / 4;
    const unsigned v_ra = up * 12 * PREC + l5 + 1536, v_rf = up * 33 * REC + l5 + 20480 + (4 * n3 + w) * 128;
    const unsigned v_pw = l8 < 5 ? (l8 + 7) * PREC + l8 * 4 * PW + 1536 + l7 : OOB, v_pu = ks && l8 + ks < 5 ? OOB : v_pw - 6 * PREC;
    const unsigned pf = 3 * (w >> 1) + 2 * (w & 1), sf = 3 * (w >> 1) + 1;
    const unsigned v_at = (8 * g + q) * 384 + 64 * pf + 16 * p, v_as = (8 * g + q) * 384 + 64 * sf + 32 * (w & 1) + 8 * p;
    auto vo = [&](unsigned i) { const unsigned c = 64 * i + lane; return (8 * w + c / 24) * 1152 + 384 * n3 + c % 24 * 16 - 1024 * i; };
    const unsigned v_vo0 = vo(0), v_vo1 = vo(1), v_vo2 = vo(2);
    const unsigned v_xp = (6 * n3 + pf) * 1024 + v_l16, v_xs = (6 * n3 + sf) * 1024 + (w & 1) * 512 + half;
    const unsigned v_op = i16 * 1152 + 384 * n3 + 64 * pf + 16 * g, v_os = i16 * 1152 + 384 * n3 + 64 * sf + 32 * (w & 1) + 8 * g;
    const unsigned v_sw = 147456 + i16 * 136 + (4 * w + g) * 8, v_sr = 147456 + (4 * w + g) * 136 + i16 * 8;
    const unsigned v_ss = 18432 + n3 * 512 + w * 128 + g * 32 + p * 8;
    const unsigned to0 = p < 3 ? (4 * (48 * w + (lane >> 2)) + gr) * 1152 + 384 * n3 + p * 128 : OOB;
    const unsigned v_to[3] = {to0, to0 + 64 * 1152, to0 + 128 * 1152};
    const unsigned s_ph = ks ? 6 - ks : 0, s_p0 = 0u - s_ph, r0 = gr * NREC + s_p0;
    const unsigned s_sh = WS_H + r0 * HREC + f0 * 1024, s_ps = WS_PS + r0 * PREC + (n3 * 20 + w) * PW;
    const unsigned s_x1 = WS_X1 + r0 * REC, s_xr = WS_XIN + (r0 + 33) * REC, s_or = (32 * gr + 63 - s_p0) * 18432;
    const unsigned s_k0 = ROT * 6 * gr;
    const unsigned s_m3 = w * 3072, s_m6 = w * 6144, s_so = ks * 768 * 1152;
    const unsigned s_i0 = (32 * gr + 31 - ks) * 18432, s_i5 = ks < 2 ? s_i0 - 30 * 18432 : OOB;
    const unsigned s_j0 = WS_XIN + (gr * NREC + ks) * REC, s_j5 = ks < 2 ? s_j0 + 30 * REC : OOB, s_he = s_sh + (63 + s_ph) * HREC;
    asm volatile(
      "s_buffer_load_dword s21, s[8:11], 0\n"
      "s_mov_b32 s23, 65536\n"
      "s_mov_b32 s31, 0x40000000\n"
      "buffer_load_dwordx4 v[56:59], v8, s[92:95], s46 offen\n"
      "buffer_load_dwordx2 v[60:61], v9, s[92:95], s46 offen\n"
      "s_add_u32 s30, s46, 4294856704\n"
      "buffer_load_dwordx4 v[64:67], v8, s[92:95], s30 offen\n"
      "buffer_load_dwordx2 v[68:69], v9, s[92:95], s30 offen\n"
      "s_add_u32 s30, s46, 4294746112\n"
      "buffer_load_dwordx4 v[72:75], v8, s[92:95], s30 offen\n"
      "buffer_load_dwordx2 v[76:77], v9, s[92:95], s30 offen\n"
      "s_add_u32 s30, s46, 4294635520\n"
      "buffer_load_dwordx4 v[80:83], v8, s[92:95], s30 offen\n"
      "buffer_load_dwordx2 v[84:85], v9, s[92:95], s30 offen\n"
      "s_add_u32 s30, s46, 4294524928\n"
      "buffer_load_dwordx4 v[88:91], v8, s[92:95], s30 offen\n"
      "buffer_load_dwordx2 v[92:93], v9, s[92:95], s30 offen\n"
      "buffer_load_dwordx4 v[96:99], v8, s[92:95], s47 offen\n"
      "buffer_load_dwordx2 v[100:101], v9, s[92:95], s47 offen\n"
      "s_waitcnt lgkmcnt(0)\n"
      "s_mul_i32 s21, s21, 1664525\n"
      "s_add_u32 s21, s21, 1013904223\n"
      "v_mov_b32 v18, s21\n"
      "buffer_store_dword v18, v15, s[8:11], 0 offen sc1\n"
      "s_cmp_eq_u32 s45, 0\n"
      "s_cselect_b32 s30, 0x40000000, s37\n"
      "buffer_store_dword v18, v3, s[8:11], s30 offen sc1\n"
      "v_not_b32 v224, v18\n"
      "v_not_b32 v225, v18\n"
      "v_not_b32 v226, v18\n"
      "v_not_b32 v227, v18\n"
      "v_xor_b32 v18, v18, v19\n"
      "s_waitcnt vmcnt(12)\n"
      "buffer_store_dwordx4 v[56:59], v6, s[8:11], s48 offen sc1\n"
      "buffer_store_dwordx2 v[60:61], v7, s[8:11], s48 offen sc1\n"
      "v_lshlrev_b32 v164, 16, v56\n"
      "v_and_b32 v165, 0xffff0000, v56\n"
      "v_lshlrev_b32 v166, 16, v57\n"
      "v_and_b32 v167, 0xffff0000, v57\n"
      "v_lshlrev_b32 v168, 16, v58\n"
      "v_and_b32 v169, 0xffff0000, v58\n"
      "v_lshlrev_b32 v170, 16, v59\n"
      "v_and_b32 v171, 0xffff0000, v59\n"
      "v_lshlrev_b32 v172, 16, v60\n"
      "v_and_b32 v173, 0xffff0000, v60\n"
      "v_lshlrev_b32 v174, 16, v61\n"
      "v_and_b32 v175, 0xffff0000, v61\n"
      "v_add_f32 v62, v164, v165\n"
      "v_mul_f32 v63, v164, v164\n"
      "v_fmac_f32 v63, v165, v165\n"
      "v_add_f32 v62, v62, v166\n"
      "v_fmac_f32 v63, v166, v166\n"
      "v_add_f32 v62, v62, v167\n"
      "v_fmac_f32 v63, v167, v167\n"
      "v_add_f32 v62, v62, v168\n"
      "v_fmac_f32 v63, v168, v168\n"
      "v_add_f32 v62, v62, v169\n"
      "v_fmac_f32 v63, v169, v169\n"
      "v_add_f32 v62, v62, v170\n"
      "v_fmac_f32 v63, v170, v170\n"
      "v_add_f32 v62, v62, v171\n"
      "v_fmac_f32 v63, v171, v171\n"
      "v_add_f32 v62, v62, v172\n"
      "v_fmac_f32 v63, v172, v172\n"
      "v_add_f32 v62, v62, v173\n"
      "v_fmac_f32 v63, v173, v173\n"
      "v_add_f32 v62, v62, v174\n"
      "v_fmac_f32 v63, v174, v174\n"
      "v_add_f32 v62, v62, v175\n"
      "v_fmac_f32 v63, v175, v175\n"
      "ds_write_b64 v10, v[62:63] offset:0\n"
      "s_waitcnt lgkmcnt(0)\n"
      "s_barrier\n"
      "ds_read_b64 v[152:153], v11 offset:0\n"
      "s_waitcnt lgkmcnt(0)\n"
      "v_add_f32_dpp v152, v152, v152 row_ror:8 row_mask:0xf bank_mask:0xf\n"
      "v_add_f32_dpp v153, v153, v153 row_ror:8 row_mask:0xf bank_mask:0xf\n"
      "s_nop 0\n"
      "v_add_f32_dpp v152, v152, v152 row_ror:4 row_mask:0xf bank_mask:0xf\n"
      "v_add_f32_dpp v153, v153, v153 row_ror:4 row_mask:0xf bank_mask:0xf\n"
      "s_nop 0\n"
      "v_add_f32_dpp v152, v152, v152 row_ror:2 row_mask:0xf bank_mask:0xf\n"
      "v_add_f32_dpp v153, v153, v153 row_ror:2 row_mask:0xf bank_mask:0xf\n"
      "s_nop 0\n"
      "v_add_f32_dpp v152, v152, v152 row_ror:1 row_mask:0xf bank_mask:0xf\n"
      "v_add_f32_dpp v153, v153, v153 row_ror:1 row_mask:0xf bank_mask:0xf\n"
      "v_mul_f32 v152, 0x3ae38e39, v152\n"
      "v_add_f32 v153, 0x3afba882, v153\n"
      "v_mul_f32 v153, 0x3ae38e39, v153\n"
      "buffer_store_dwordx2 v[152:153], v12, s[8:11], s48 offen sc1\n"
      "s_waitcnt vmcnt(13)\n"
      "s_add_u32 s30, s48, 147456\n"
      "buffer_store_dwordx4 v[64:67], v6, s[8:11], s30 offen sc1\n"
      "buffer_store_dwordx2 v[68:69], v7, s[8:11], s30 offen sc1\n"
      "v_lshlrev_b32 v164, 16, v64\n"
      "v_and_b32 v165, 0xffff0000, v64\n"
      "v_lshlrev_b32 v166, 16, v65\n"
      "v_and_b32 v167, 0xffff0000, v65\n"
      "v_lshlrev_b32 v168, 16, v66\n"
      "v_and_b32 v169, 0xffff0000, v66\n"
      "v_lshlrev_b32 v170, 16, v67\n"
      "v_and_b32 v171, 0xffff0000, v67\n"
      "v_lshlrev_b32 v172, 16, v68\n"
      "v_and_b32 v173, 0xffff0000, v68\n"
      "v_lshlrev_b32 v174, 16, v69\n"
      "v_and_b32 v175, 0xffff0000, v69\n"
      "v_add_f32 v70, v164, v165\n"
      "v_mul_f32 v71, v164, v164\n"
      "v_fmac_f32 v71, v165, v165\n"
      "v_add_f32 v70, v70, v166\n"
      "v_fmac_f32 v71, v166, v166\n"
      "v_add_f32 v70, v70, v167\n"
      "v_fmac_f32 v71, v167, v167\n"
      "v_add_f32 v70, v70, v168\n"
      "v_fmac_f32 v71, v168, v168\n"
      "v_add_f32 v70, v70, v169\n"
      "v_fmac_f32 v71, v169, v169\n"
      "v_add_f32 v70, v70, v170\n"
      "v_fmac_f32 v71, v170, v170\n"
      "v_add_f32 v70, v70, v171\n"
      "v_fmac_f32 v71, v171, v171\n"
      "v_add_f32 v70, v70, v172\n"
      "v_fmac_f32 v71, v172, v172\n"
      "v_add_f32 v70, v70, v173\n"
      "v_fmac_f32 v71, v173, v173\n"
      "v_add_f32 v70, v70, v174\n"
      "v_fmac_f32 v71, v174, v174\n"
      "v_add_f32 v70, v70, v175\n"
      "v_fmac_f32 v71, v175, v175\n"
      "ds_write_b64 v10, v[70:71] offset:2176\n"
      "s_waitcnt vmcnt(2)\n"
      "buffer_store_dword v18, v5, s[8:11], s48 offen sc1\n"
      "s_add_u32 s30, s48, 294912\n"
      "buffer_store_dwordx4 v[72:75], v6, s[8:11], s30 offen sc1\n"
      "buffer_store_dwordx2 v[76:77], v7, s[8:11], s30 offen sc1\n"
      "v_lshlrev_b32 v164, 16, v72\n"
      "v_and_b32 v165, 0xffff0000, v72\n"
      "v_lshlrev_b32 v166, 16, v73\n"
      "v_and_b32 v167, 0xffff0000, v73\n"
      "v_lshlrev_b32 v168, 16, v74\n"
      "v_and_b32 v169, 0xffff0000, v74\n"
      "v_lshlrev_b32 v170, 16, v75\n"
      "v_and_b32 v171, 0xffff0000, v75\n"
      "v_lshlrev_b32 v172, 16, v76\n"
      "v_and_b32 v173, 0xffff0000, v76\n"
      "v_lshlrev_b32 v174, 16, v77\n"
      "v_and_b32 v175, 0xffff0000, v77\n"
      "v_add_f32 v78, v164, v165\n"
      "v_mul_f32 v79, v164, v164\n"
      "v_fmac_f32 v79, v165, v165\n"
      "v_add_f32 v78, v78, v166\n"
      "v_fmac_f32 v79, v166, v166\n"
      "v_add_f32 v78, v78, v167\n"
      "v_fmac_f32 v79, v167, v167\n"
      "v_add_f32 v78, v78, v168\n"
      "v_fmac_f32 v79, v168, v168\n"
      "v_add_f32 v78, v78, v169\n"
      "v_fmac_f32 v79, v169, v169\n"
      "v_add_f32 v78, v78, v170\n"
      "v_fmac_f32 v79, v170, v170\n"
      "v_add_f32 v78, v78, v171\n"
      "v_fmac_f32 v79, v171, v171\n"
      "v_add_f32 v78, v78, v172\n"
      "v_fmac_f32 v79, v172, v172\n"
      "v_add_f32 v78, v78, v173\n"
      "v_fmac_f32 v79, v173, v173\n"
      "v_add_f32 v78, v78, v174\n"
      "v_fmac_f32 v79, v174, v174\n"
      "v_add_f32 v78, v78, v175\n"
      "v_fmac_f32 v79, v175, v175\n"
      "ds_write_b64 v10, v[78:79] offset:4352\n"
      "s_add_u32 s30, s48, 442368\n"
      "buffer_store_dwordx4 v[80:83], v6, s[8:11], s30 offen sc1\n"
      "buffer_store_dwordx2 v[84:85], v7, s[8:11], s30 offen sc1\n"
      "v_lshlrev_b32 v164, 16, v80\n"
      "v_and_b32 v165, 0xffff0000, v80\n"
      "v_lshlrev_b32 v166, 16, v81\n"
      "v_and_b32 v167, 0xffff0000, v81\n"
      "v_lshlrev_b32 v168, 16, v82\n"
      "v_and_b32 v169, 0xffff0000, v82\n"
      "v_lshlrev_b32 v170, 16, v83\n"
      "v_and_b32 v171, 0xffff0000, v83\n"
      "v_lshlrev_b32 v172, 16, v84\n"
      "v_and_b32 v173, 0xffff0000, v84\n"
      "v_lshlrev_b32 v174, 16, v85\n"
      "v_and_b32 v175, 0xffff0000, v85\n"
      "v_add_f32 v86, v164, v165\n"
      "v_mul_f32 v87, v164, v164\n"
      "v_fmac_f32 v87, v165, v165\n"
      "v_add_f32 v86, v86, v166\n"
      "v_fmac_f32 v87, v166, v166\n"
      "v_add_f32 v86, v86, v167\n"
      "v_fmac_f32 v87, v167, v167\n"
      "v_add_f32 v86, v86, v168\n"
      "v_fmac_f32 v87, v168, v168\n"
      "v_add_f32 v86, v86, v169\n"
      "v_fmac_f32 v87, v169, v169\n"
      "v_add_f32 v86, v86, v170\n"
      "v_fmac_f32 v87, v170, v170\n"
      "v_add_f32 v86, v86, v171\n"
      "v_fmac_f32 v87, v171, v171\n"
      "v_add_f32 v86, v86, v172\n"
      "v_fmac_f32 v87, v172, v172\n"
      "v_add_f32 v86, v86, v173\n"
      "v_fmac_f32 v87, v173, v173\n"
      "v_add_f32 v86, v86, v174\n"
      "v_fmac_f32 v87, v174, v174\n"
      "v_add_f32 v86, v86, v175\n"
      "v_fmac_f32 v87, v175, v175\n"
      "ds_write_b64 v10, v[86:87] offset:6528\n"
      "s_add_u32 s30, s48, 589824\n"
      "buffer_store_dwordx4 v[88:91], v6, s[8:11], s30 offen sc1\n"
      "buffer_store_dwordx2 v[92:93], v7, s[8:11], s30 offen sc1\n"
      "v_lshlrev_b32 v164, 16, v88\n"
      "v_and_b32 v165, 0xffff0000, v88\n"
      "v_lshlrev_b32 v166, 16, v89\n"
      "v_and_b32 v167, 0xffff0000, v89\n"
      "v_lshlrev_b32 v168, 16, v90\n"
      "v_and_b32 v169, 0xffff0000, v90\n"
      "v_lshlrev_b32 v170, 16, v91\n"
      "v_and_b32 v171, 0xffff0000, v91\n"
      "v_lshlrev_b32 v172, 16, v92\n"
      "v_and_b32 v173, 0xffff0000, v92\n"
      "v_lshlrev_b32 v174, 16, v93\n"
      "v_and_b32 v175, 0xffff0000, v93\n"
      "v_add_f32 v94, v164, v165\n"
      "v_mul_f32 v95, v164, v164\n"
      "v_fmac_f32 v95, v165, v165\n"
      "v_add_f32 v94, v94, v166\n"
      "v_fmac_f32 v95, v166, v166\n"
      "v_add_f32 v94, v94, v167\n"
      "v_fmac_f32 v95, v167, v167\n"
      "v_add_f32 v94, v94, v168\n"
      "v_fmac_f32 v95, v168, v168\n"
      "v_add_f32 v94, v94, v169\n"
      "v_fmac_f32 v95, v169, v169\n"
      "v_add_f32 v94, v94, v170\n"
      "v_fmac_f32 v95, v170, v170\n"
      "v_add_f32 v94, v94, v171\n"
      "v_fmac_f32 v95, v171, v171\n"
      "v_add_f32 v94, v94, v172\n"
      "v_fmac_f32 v95, v172, v172\n"
      "v_add_f32 v94, v94, v173\n"
      "v_fmac_f32 v95, v173, v173\n"
      "v_add_f32 v94, v94, v174\n"
      "v_fmac_f32 v95, v174, v174\n"
      "v_add_f32 v94, v94, v175\n"
      "v_fmac_f32 v95, v175, v175\n"
      "ds_write_b64 v10, v[94:95] offset:8704\n"
      "buffer_store_dwordx4 v[96:99], v6, s[8:11], s49 offen sc1\n"
      "buffer_store_dwordx2 v[100:101], v7, s[8:11], s49 offen sc1\n"
      "v_lshlrev_b32 v164, 16, v96\n"
      "v_and_b32 v165, 0xffff0000, v96\n"
      "v_lshlrev_b32 v166, 16, v97\n"
      "v_and_b32 v167, 0xffff0000, v97\n"
      "v_lshlrev_b32 v168, 16, v98\n"
      "v_and_b32 v169, 0xffff0000, v98\n"
      "v_lshlrev_b32 v170, 16, v99\n"
      "v_and_b32 v171, 0xffff0000, v99\n"
      "v_lshlrev_b32 v172, 16, v100\n"
      "v_and_b32 v173, 0xffff0000, v100\n"
      "v_lshlrev_b32 v174, 16, v101\n"
      "v_and_b32 v175, 0xffff0000, v101\n"
      "v_add_f32 v102, v164, v165\n"
      "v_mul_f32 v103, v164, v164\n"
      "v_fmac_f32 v103, v165, v165\n"
      "v_add_f32 v102, v102, v166\n"
      "v_fmac_f32 v103, v166, v166\n"
      "v_add_f32 v102, v102, v167\n"
      "v_fmac_f32 v103, v167, v167\n"
      "v_add_f32 v102, v102, v168\n"
      "v_fmac_f32 v103, v168, v168\n"
      "v_add_f32 v102, v102, v169\n"
      "v_fmac_f32 v103, v169, v169\n"
      "v_add_f32 v102, v102, v170\n"
      "v_fmac_f32 v103, v170, v170\n"
      "v_add_f32 v102, v102, v171\n"
      "v_fmac_f32 v103, v171, v171\n"
      "v_add_f32 v102, v102, v172\n"
      "v_fmac_f32 v103, v172, v172\n"
      "v_add_f32 v102, v102, v173\n"
      "v_fmac_f32 v103, v173, v173\n"
      "v_add_f32 v102, v102, v174\n"
      "v_fmac_f32 v103, v174, v174\n"
      "v_add_f32 v102, v102, v175\n"
      "v_fmac_f32 v103, v175, v175\n"
      "ds_write_b64 v10, v[102:103] offset:10880\n"
      "s_waitcnt lgkmcnt(0)\n"
      "s_barrier\n"
      "ds_read_b64 v[154:155], v11 offset:2176\n"
      "ds_read_b64 v[156:157], v11 offset:4352\n"
      "ds_read_b64 v[158:159], v11 offset:6528\n"
      "ds_read_b64 v[160:161], v11 offset:8704\n"
      "ds_read_b64 v[162:163], v11 offset:10880\n"
      "s_waitcnt lgkmcnt(0)\n"
      "v_add_f32_dpp v154, v154, v154 row_ror:8 row_mask:0xf bank_mask:0xf\n"
      "v_add_f32_dpp v155, v155, v155 row_ror:8 row_mask:0xf bank_mask:0xf\n"
      "v_add_f32_dpp v156, v156, v156 row_ror:8 row_mask:0xf bank_mask:0xf\n"
      "v_add_f32_dpp v157, v157, v157 row_ror:8 row_mask:0xf bank_mask:0xf\n"
      "v_add_f32_dpp v158, v158, v158 row_ror:8 row_mask:0xf bank_mask:0xf\n"
      "v_add_f32_dpp v159, v159, v159 row_ror:8 row_mask:0xf bank_mask:0xf\n"
      "v_add_f32_dpp v160, v160, v160 row_ror:8 row_mask:0xf bank_mask:0xf\n"
      "v_add_f32_dpp v161, v161, v161 row_ror:8 row_mask:0xf bank_mask:0xf\n"
      "v_add_f32_dpp v162, v162, v162 row_ror:8 row_mask:0xf bank_mask:0xf\n"
      "v_add_f32_dpp v163, v163, v163 row_ror:8 row_mask:0xf bank_mask:0xf\n"
      "v_add_f32_dpp v154, v154, v154 row_ror:4 row_mask:0xf bank_mask:0xf\n"
      "v_add_f32_dpp v155, v155, v155 row_ror:4 row_mask:0xf bank_mask:0xf\n"
      "v_add_f32_dpp v156, v156, v156 row_ror:4 row_mask:0xf bank_mask:0xf\n"
      "v_add_f32_dpp v157, v157, v157 row_ror:4 row_mask:0xf bank_mask:0xf\n"
      "v_add_f32_dpp v158, v158, v158 row_ror:4 row_mask:0xf bank_mask:0xf\n"
      "v_add_f32_dpp v159, v159, v159 row_ror:4 row_mask:0xf bank_mask:0xf\n"
      "v_add_f32_dpp v160, v160, v160 row_ror:4 row_mask:0xf bank_mask:0xf\n"
      "v_add_f32_dpp v161, v161, v161 row_ror:4 row_mask:0xf bank_mask:0xf\n"
      "v_add_f32_dpp v162, v162, v162 row_ror:4 row_mask:0xf bank_mask:0xf\n"
      "v_add_f32_dpp v163, v163, v163 row_ror:4 row_mask:0xf bank_mask:0xf\n"
      "v_add_f32_dpp v154, v154, v154 row_ror:2 row_mask:0xf bank_mask:0xf\n"
      "v_add_f32_dpp v155, v155, v155 row_ror:2 row_mask:0xf bank_mask:0xf\n"
      "v_add_f32_dpp v156, v156, v156 row_ror:2 row_mask:0xf bank_mask:0xf\n"
      "v_add_f32_dpp v157, v157, v157 row_ror:2 row_mask:0xf bank_mask:0xf\n"
      "v_add_f32_dpp v158, v158, v158 row_ror:2 row_mask:0xf bank_mask:0xf\n"
      "v_add_f32_dpp v159, v159, v159 row_ror:2 row_mask:0xf bank_mask:0xf\n"
      "v_add_f32_dpp v160, v160, v160 row_ror:2 row_mask:0xf bank_mask:0xf\n"
      "v_add_f32_dpp v161, v161, v161 row_ror:2 row_mask:0xf bank_mask:0xf\n"
      "v_add_f32_dpp v162, v162, v162 row_ror:2 row_mask:0xf bank_mask:0xf\n"
      "v_add_f32_dpp v163, v163, v163 row_ror:2 row_mask:0xf bank_mask:0xf\n"
      "v_add_f32_dpp v154, v154, v154 row_ror:1 row_mask:0xf bank_mask:0xf\n"
      "v_add_f32_dpp v155, v155, v155 row_ror:1 row_mask:0xf bank_mask:0xf\n"
      "v_add_f32_dpp v156, v156, v156 row_ror:1 row_mask:0xf bank_mask:0xf\n"
      "v_add_f32_dpp v157, v157, v157 row_ror:1 row_mask:0xf bank_mask:0xf\n"
      "v_add_f32_dpp v158, v158, v158 row_ror:1 row_mask:0xf bank_mask:0xf\n"
      "v_add_f32_dpp v159, v159, v159 row_ror:1 row_mask:0xf bank_mask:0xf\n"
      "v_add_f32_dpp v160, v160, v160 row_ror:1 row_mask:0xf bank_mask:0xf\n"
      "v_add_f32_dpp v161, v161, v161 row_ror:1 row_mask:0xf bank_mask:0xf\n"
      "v_add_f32_dpp v162, v162, v162 row_ror:1 row_mask:0xf bank_mask:0xf\n"
      "v_add_f32_dpp v163, v163, v163 row_ror:1 row_mask:0xf bank_mask:0xf\n"
      "v_mul_f32 v154, 0x3ae38e39, v154\n"
      "v_add_f32 v155, 0x3afba882, v155\n"
      "v_mul_f32 v155, 0x3ae38e39, v155\n"
      "v_mul_f32 v156, 0x3ae38e39, v156\n"
      "v_add_f32 v157, 0x3afba882, v157\n"
      "v_mul_f32 v157, 0x3ae38e39, v157\n"
      "v_mul_f32 v158, 0x3ae38e39, v158\n"
      "v_add_f32 v159, 0x3afba882, v159\n"
      "v_mul_f32 v159, 0x3ae38e39, v159\n"
      "v_mul_f32 v160, 0x3ae38e39, v160\n"
      "v_add_f32 v161, 0x3afba882, v161\n"
      "v_mul_f32 v161, 0x3ae38e39, v161\n"
      "v_mul_f32 v162, 0x3ae38e39, v162\n"
      "v_add_f32 v163, 0x3afba882, v163\n"
      "v_mul_f32 v163, 0x3ae38e39, v163\n"
      "s_add_u32 s30, s48, 147456\n"
      "buffer_store_dwordx2 v[154:155], v12, s[8:11], s30 offen sc1\n"
      "s_add_u32 s30, s48, 294912\n"
      "buffer_store_dwordx2 v[156:157], v12, s[8:11], s30 offen sc1\n"
      "s_add_u32 s30, s48, 442368\n"
      "buffer_store_dwordx2 v[158:159], v12, s[8:11], s30 offen sc1\n"
      "s_add_u32 s30, s48, 589824\n"
      "buffer_store_dwordx2 v[160:161], v12, s[8:11], s30 offen sc1\n"
      "buffer_store_dwordx2 v[162:163], v12, s[8:11], s49 offen sc1\n"
      "s_waitcnt vmcnt(0)\n"
      "s_add_u32 s30, s48, 147456\n"
      "buffer_store_dword v18, v5, s[8:11], s30 offen sc1\n"
      "s_add_u32 s30, s48, 294912\n"
      "buffer_store_dword v18, v5, s[8:11], s30 offen sc1\n"
      "s_add_u32 s30, s48, 442368\n"
      "buffer_store_dword v18, v5, s[8:11], s30 offen sc1\n"
      "s_add_u32 s30, s48, 589824\n"
      "buffer_store_dword v18, v5, s[8:11], s30 offen sc1\n"
      "buffer_store_dword v18, v5, s[8:11], s49 offen sc1\n"
      "v_and_b32 v20, 0x40000000, v19\n"
      "v_or_b32 v5, v20, v5\n"
      "s_add_u32 s30, s37, 0\n"
      "buffer_store_dwordx4 v[224:227], v183, s[8:11], s30 offen sc1\n"
      "s_add_u32 s30, s37, 0\n"
      "buffer_store_dwordx4 v[224:227], v181, s[8:11], s30 offen sc1\n"
      "s_sleep 32\n"
      "s_sleep 32\n"
      "s_waitcnt vmcnt(0) lgkmcnt(0)\n"
      "s_mov_b32 s61, s60\n"
      "s_mov_b32 s62, 6\n"
      ".Ld_r0:\n"
      "s_mul_i32 s63, s61, 36864\n"
      "s_add_u32 s63, s63, s44\n"
      "s_mul_i32 s30, s61, 8192\n"
      "s_lshr_b32 s30, s30, 16\n"
      "s_mul_i32 s30, s30, 8\n"
      "s_sub_u32 s30, s61, s30\n"
      "s_mul_i32 s30, s30, 12288\n"
      "s_add_u32 s30, s30, 0\n"
      "s_add_u32 m0, s42, s30\n"
      "s_add_u32 s61, s61, 1\n"
      "buffer_load_dwordx4 v178, s[12:15], s63 offen offset:0 lds\n"
      "buffer_load_dwordx4 v179, s[12:15], s63 offen offset:1024 lds\n"
      "buffer_load_dwordx4 v180, s[12:15], s63 offen offset:2048 lds\n"
      "s_cmp_eq_u32 s61, 24\n"
      "s_cselect_b32 s61, 0, s61\n"
      "s_sub_u32 s62, s62, 1\n"
      "s_cbranch_scc0 .Ld_r0\n"
      "v_add_u32 v20, 49152, v176\n"
      "v_add_u32 v21, 49152, v177\n"
      "v_add_u32 v22, 98304, v176\n"
      "v_add_u32 v23, 98304, v177\n"
      "s_mov_b32 s62, 23\n"
      "s_cmp_eq_u32 s60, 6\n"
      "s_cbranch_scc1 .Ld_rs6\n"
      "s_cmp_eq_u32 s60, 12\n"
      "s_cbranch_scc1 .Ld_rs12\n"
      "s_cmp_eq_u32 s60, 18\n"
      "s_cbranch_scc1 .Ld_rs18\n"
      ".Ld_rs0:\n"
      "s_add_u32 s63, s44, 258048\n"
      "s_cmp_ge_u32 s62, 7\n"
      "s_cselect_b32 s63, s63, s31\n"
      "s_add_u32 m0, s42, 86016\n"
      "s_waitcnt vmcnt(18) lgkmcnt(0)\n"
      "s_barrier\n"
      "ds_read_b64_tr_b16 a[0:1], v176 offset:0\n"
      "ds_read_b64_tr_b16 a[2:3], v176 offset:1536\n"
      "ds_read_b64_tr_b16 a[96:97], v176 offset:8\n"
      "ds_read_b64_tr_b16 a[98:99], v176 offset:1544\n"
      "ds_read_b64_tr_b16 a[192:193], v177 offset:0\n"
      "ds_read_b64_tr_b16 a[194:195], v177 offset:1536\n"
      "buffer_load_dwordx4 v178, s[12:15], s63 offen offset:0 lds\n"
      "buffer_load_dwordx4 v179, s[12:15], s63 offen offset:1024 lds\n"
      "buffer_load_dwordx4 v180, s[12:15], s63 offen offset:2048 lds\n"
      "s_sub_u32 s62, s62, 1\n"
      ".Ld_rs1:\n"
      "s_add_u32 s63, s44, 294912\n"
      "s_cmp_ge_u32 s62, 7\n"
      "s_cselect_b32 s63, s63, s31\n"
      "s_add_u32 m0, s42, 0\n"
      "s_waitcnt vmcnt(18) lgkmcnt(0)\n"
      "s_barrier\n"
      "ds_read_b64_tr_b16 a[4:5], v176 offset:12288\n"
      "ds_read_b64_tr_b16 a[6:7], v176 offset:13824\n"
      "ds_read_b64_tr_b16 a[100:101], v176 offset:12296\n"
      "ds_read_b64_tr_b16 a[102:103], v176 offset:13832\n"
      "ds_read_b64_tr_b16 a[196:197], v177 offset:12288\n"
      "ds_read_b64_tr_b16 a[198:199], v177 offset:13824\n"
      "buffer_load_dwordx4 v178, s[12:15], s63 offen offset:0 lds\n"
      "buffer_load_dwordx4 v179, s[12:15], s63 offen offset:1024 lds\n"
      "buffer_load_dwordx4 v180, s[12:15], s63 offen offset:2048 lds\n"
      "s_sub_u32 s62, s62, 1\n"
      ".Ld_rs2:\n"
      "s_add_u32 s63, s44, 331776\n"
      "s_cmp_ge_u32 s62, 7\n"
      "s_cselect_b32 s63, s63, s31\n"
      "s_add_u32 m0, s42, 12288\n"
      "s_waitcnt vmcnt(18) lgkmcnt(0)\n"
      "s_barrier\n"
      "ds_read_b64_tr_b16 a[8:9], v176 offset:24576\n"
      "ds_read_b64_tr_b16 a[10:11], v176 offset:26112\n"
      "ds_read_b64_tr_b16 a[104:105], v176 offset:24584\n"
      "ds_read_b64_tr_b16 a[106:107], v176 offset:26120\n"
      "ds_read_b64_tr_b16 a[200:201], v177 offset:24576\n"
      "ds_read_b64_tr_b16 a[202:203], v177 offset:26112\n"
      "buffer_load_dwordx4 v178, s[12:15], s63 offen offset:0 lds\n"
      "buffer_load_dwordx4 v179, s[12:15], s63 offen offset:1024 lds\n"
      "buffer_load_dwordx4 v180, s[12:15], s63 offen offset:2048 lds\n"
      "s_sub_u32 s62, s62, 1\n"
      ".Ld_rs3:\n"
      "s_add_u32 s63, s44, 368640\n"
      "s_cmp_ge_u32 s62, 7\n"
      "s_cselect_b32 s63, s63, s31\n"
      "s_add_u32 m0, s42, 24576\n"
      "s_waitcnt vmcnt(18) lgkmcnt(0)\n"
      "s_barrier\n"
      "ds_read_b64_tr_b16 a[12:13], v176 offset:36864\n"
      "ds_read_b64_tr_b16 a[14:15], v176 offset:38400\n"
      "ds_read_b64_tr_b16 a[108:109], v176 offset:36872\n"
      "ds_read_b64_tr_b16 a[110:111], v176 offset:38408\n"
      "ds_read_b64_tr_b16 a[204:205], v177 offset:36864\n"
      "ds_read_b64_tr_b16 a[206:207], v177 offset:38400\n"
      "buffer_load_dwordx4 v178, s[12:15], s63 offen offset:0 lds\n"
      "buffer_load_dwordx4 v179, s[12:15], s63 offen offset:1024 lds\n"
      "buffer_load_dwordx4 v180, s[12:15], s63 offen offset:2048 lds\n"
      "s_sub_u32 s62, s62, 1\n"
      ".Ld_rs4:\n"
      "s_add_u32 s63, s44, 405504\n"
      "s_cmp_ge_u32 s62, 7\n"
      "s_cselect_b32 s63, s63, s31\n"
      "s_add_u32 m0, s42, 36864\n"
      "s_waitcnt vmcnt(18) lgkmcnt(0)\n"
      "s_barrier\n"
      "ds_read_b64_tr_b16 a[16:17], v20 offset:0\n"
      "ds_read_b64_tr_b16 a[18:19], v20 offset:1536\n"
      "ds_read_b64_tr_b16 a[112:113], v20 offset:8\n"
      "ds_read_b64_tr_b16 a[114:115], v20 offset:1544\n"
      "ds_read_b64_tr_b16 a[208:209], v21 offset:0\n"
      "ds_read_b64_tr_b16 a[210:211], v21 offset:1536\n"
      "buffer_load_dwordx4 v178, s[12:15], s63 offen offset:0 lds\n"
      "buffer_load_dwordx4 v179, s[12:15], s63 offen offset:1024 lds\n"
      "buffer_load_dwordx4 v180, s[12:15], s63 offen offset:2048 lds\n"
      "s_sub_u32 s62, s62, 1\n"
      ".Ld_rs5:\n"
      "s_add_u32 s63, s44, 442368\n"
      "s_cmp_ge_u32 s62, 7\n"
      "s_cselect_b32 s63, s63, s31\n"
      "s_add_u32 m0, s42, 49152\n"
      "s_waitcnt vmcnt(18) lgkmcnt(0)\n"
      "s_barrier\n"
      "ds_read_b64_tr_b16 a[20:21], v20 offset:12288\n"
      "ds_read_b64_tr_b16 a[22:23], v20 offset:13824\n"
      "ds_read_b64_tr_b16 a[116:117], v20 offset:12296\n"
      "ds_read_b64_tr_b16 a[118:119], v20 offset:13832\n"
      "ds_read_b64_tr_b16 a[212:213], v21 offset:12288\n"
      "ds_read_b64_tr_b16 a[214:215], v21 offset:13824\n"
      "buffer_load_dwordx4 v178, s[12:15], s63 offen offset:0 lds\n"
      "buffer_load_dwordx4 v179, s[12:15], s63 offen offset:1024 lds\n"
      "buffer_load_dwordx4 v180, s[12:15], s63 offen offset:2048 lds\n"
      "s_sub_u32 s62, s62, 1\n"
      "s_cbranch_scc1 .Ld_re\n"
      ".Ld_rs6:\n"
      "s_add_u32 s63, s44, 479232\n"
      "s_cmp_ge_u32 s62, 7\n"
      "s_cselect_b32 s63, s63, s31\n"
      "s_add_u32 m0, s42, 61440\n"
      "s_waitcnt vmcnt(18) lgkmcnt(0)\n"
      "s_barrier\n"
      "ds_read_b64_tr_b16 a[24:25], v20 offset:24576\n"
      "ds_read_b64_tr_b16 a[26:27], v20 offset:26112\n"
      "ds_read_b64_tr_b16 a[120:121], v20 offset:24584\n"
      "ds_read_b64_tr_b16 a[122:123], v20 offset:26120\n"
      "ds_read_b64_tr_b16 a[216:217], v21 offset:24576\n"
      "ds_read_b64_tr_b16 a[218:219], v21 offset:26112\n"
      "buffer_load_dwordx4 v178, s[12:15], s63 offen offset:0 lds\n"
      "buffer_load_dwordx4 v179, s[12:15], s63 offen offset:1024 lds\n"
      "buffer_load_dwordx4 v180, s[12:15], s63 offen offset:2048 lds\n"
      "s_sub_u32 s62, s62, 1\n"
      ".Ld_rs7:\n"
      "s_add_u32 s63, s44, 516096\n"
      "s_cmp_ge_u32 s62, 7\n"
      "s_cselect_b32 s63, s63, s31\n"
      "s_add_u32 m0, s42, 73728\n"
      "s_waitcnt vmcnt(18) lgkmcnt(0)\n"
      "s_barrier\n"
      "ds_read_b64_tr_b16 a[28:29], v20 offset:36864\n"
      "ds_read_b64_tr_b16 a[30:31], v20 offset:38400\n"
      "ds_read_b64_tr_b16 a[124:125], v20 offset:36872\n"
      "ds_read_b64_tr_b16 a[126:127], v20 offset:38408\n"
      "ds_read_b64_tr_b16 a[220:221], v21 offset:36864\n"
      "ds_read_b64_tr_b16 a[222:223], v21 offset:38400\n"
      "buffer_load_dwordx4 v178, s[12:15], s63 offen offset:0 lds\n"
      "buffer_load_dwordx4 v179, s[12:15], s63 offen offset:1024 lds\n"
      "buffer_load_dwordx4 v180, s[12:15], s63 offen offset:2048 lds\n"
      "s_sub_u32 s62, s62, 1\n"
      ".Ld_rs8:\n"
      "s_add_u32 s63, s44, 552960\n"
      "s_cmp_ge_u32 s62, 7\n"
      "s_cselect_b32 s63, s63, s31\n"
      "s_add_u32 m0, s42, 86016\n"
      "s_waitcnt vmcnt(18) lgkmcnt(0)\n"
      "s_barrier\n"
      "ds_read_b64_tr_b16 a[32:33], v176 offset:0\n"
      "ds_read_b64_tr_b16 a[34:35], v176 offset:1536\n"
      "ds_read_b64_tr_b16 a[128:129], v176 offset:8\n"
      "ds_read_b64_tr_b16 a[130:131], v176 offset:1544\n"
      "ds_read_b64_tr_b16 a[224:225], v177 offset:0\n"
      "ds_read_b64_tr_b16 a[226:227], v177 offset:1536\n"
      "buffer_load_dwordx4 v178, s[12:15], s63 offen offset:0 lds\n"
      "buffer_load_dwordx4 v179, s[12:15], s63 offen offset:1024 lds\n"
      "buffer_load_dwordx4 v180, s[12:15], s63 offen offset:2048 lds\n"
      "s_sub_u32 s62, s62, 1\n"
      ".Ld_rs9:\n"
      "s_add_u32 s63, s44, 589824\n"
      "s_cmp_ge_u32 s62, 7\n"
      "s_cselect_b32 s63, s63, s31\n"
      "s_add_u32 m0, s42, 0\n"
      "s_waitcnt vmcnt(18) lgkmcnt(0)\n"
      "s_barrier\n"
      "ds_read_b64_tr_b16 a[36:37], v176 offset:12288\n"
      "ds_read_b64_tr_b16 a[38:39], v176 offset:13824\n"
      "ds_read_b64_tr_b16 a[132:133], v176 offset:12296\n"
      "ds_read_b64_tr_b16 a[134:135], v176 offset:13832\n"
      "ds_read_b64_tr_b16 a[228:229], v177 offset:12288\n"
      "ds_read_b64_tr_b16 a[230:231], v177 offset:13824\n"
      "buffer_load_dwordx4 v178, s[12:15], s63 offen offset:0 lds\n"
      "buffer_load_dwordx4 v179, s[12:15], s63 offen offset:1024 lds\n"
      "buffer_load_dwordx4 v180, s[12:15], s63 offen offset:2048 lds\n"
      "s_sub_u32 s62, s62, 1\n"
      ".Ld_rs10:\n"
      "s_add_u32 s63, s44, 626688\n"
      "s_cmp_ge_u32 s62, 7\n"
      "s_cselect_b32 s63, s63, s31\n"
      "s_add_u32 m0, s42, 12288\n"
      "s_waitcnt vmcnt(18) lgkmcnt(0)\n"
      "s_barrier\n"
      "ds_read_b64_tr_b16 a[40:41], v176 offset:24576\n"
      "ds_read_b64_tr_b16 a[42:43], v176 offset:26112\n"
      "ds_read_b64_tr_b16 a[136:137], v176 offset:24584\n"
      "ds_read_b64_tr_b16 a[138:139], v176 offset:26120\n"
      "ds_read_b64_tr_b16 a[232:233], v177 offset:24576\n"
      "ds_read_b64_tr_b16 a[234:235], v177 offset:26112\n"
      "buffer_load_dwordx4 v178, s[12:15], s63 offen offset:0 lds\n"
      "buffer_load_dwordx4 v179, s[12:15], s63 offen offset:1024 lds\n"
      "buffer_load_dwordx4 v180, s[12:15], s63 offen offset:2048 lds\n"
      "s_sub_u32 s62, s62, 1\n"
      ".Ld_rs11:\n"
      "s_add_u32 s63, s44, 663552\n"
      "s_cmp_ge_u32 s62, 7\n"
      "s_cselect_b32 s63, s63, s31\n"
      "s_add_u32 m0, s42, 24576\n"
      "s_waitcnt vmcnt(18) lgkmcnt(0)\n"
      "s_barrier\n"
      "ds_read_b64_tr_b16 a[44:45], v176 offset:36864\n"
      "ds_read_b64_tr_b16 a[46:47], v176 offset:38400\n"
      "ds_read_b64_tr_b16 a[140:141], v176 offset:36872\n"
      "ds_read_b64_tr_b16 a[142:143], v176 offset:38408\n"
      "ds_read_b64_tr_b16 a[236:237], v177 offset:36864\n"
      "ds_read_b64_tr_b16 a[238:239], v177 offset:38400\n"
      "buffer_load_dwordx4 v178, s[12:15], s63 offen offset:0 lds\n"
      "buffer_load_dwordx4 v179, s[12:15], s63 offen offset:1024 lds\n"
      "buffer_load_dwordx4 v180, s[12:15], s63 offen offset:2048 lds\n"
      "s_sub_u32 s62, s62, 1\n"
      "s_cbranch_scc1 .Ld_re\n"
      ".Ld_rs12:\n"
      "s_add_u32 s63, s44, 700416\n"
      "s_cmp_ge_u32 s62, 7\n"
      "s_cselect_b32 s63, s63, s31\n"
      "s_add_u32 m0, s42, 36864\n"
      "s_waitcnt vmcnt(18) lgkmcnt(0)\n"
      "s_barrier\n"
      "ds_read_b64_tr_b16 a[48:49], v20 offset:0\n"
      "ds_read_b64_tr_b16 a[50:51], v20 offset:1536\n"
      "ds_read_b64_tr_b16 a[144:145], v20 offset:8\n"
      "ds_read_b64_tr_b16 a[146:147], v20 offset:1544\n"
      "ds_read_b64_tr_b16 a[240:241], v21 offset:0\n"
      "ds_read_b64_tr_b16 a[242:243], v21 offset:1536\n"
      "buffer_load_dwordx4 v178, s[12:15], s63 offen offset:0 lds\n"
      "buffer_load_dwordx4 v179, s[12:15], s63 offen offset:1024 lds\n"
      "buffer_load_dwordx4 v180, s[12:15], s63 offen offset:2048 lds\n"
      "s_sub_u32 s62, s62, 1\n"
      ".Ld_rs13:\n"
      "s_add_u32 s63, s44, 737280\n"
      "s_cmp_ge_u32 s62, 7\n"
      "s_cselect_b32 s63, s63, s31\n"
      "s_add_u32 m0, s42, 49152\n"
      "s_waitcnt vmcnt(18) lgkmcnt(0)\n"
      "s_barrier\n"
      "ds_read_b64_tr_b16 a[52:53], v20 offset:12288\n"
      "ds_read_b64_tr_b16 a[54:55], v20 offset:13824\n"
      "ds_read_b64_tr_b16 a[148:149], v20 offset:12296\n"
      "ds_read_b64_tr_b16 a[150:151], v20 offset:13832\n"
      "ds_read_b64_tr_b16 a[244:245], v21 offset:12288\n"
      "ds_read_b64_tr_b16 a[246:247], v21 offset:13824\n"
      "buffer_load_dwordx4 v178, s[12:15], s63 offen offset:0 lds\n"
      "buffer_load_dwordx4 v179, s[12:15], s63 offen offset:1024 lds\n"
      "buffer_load_dwordx4 v180, s[12:15], s63 offen offset:2048 lds\n"
      "s_sub_u32 s62, s62, 1\n"
      ".Ld_rs14:\n"
      "s_add_u32 s63, s44, 774144\n"
      "s_cmp_ge_u32 s62, 7\n"
      "s_cselect_b32 s63, s63, s31\n"
      "s_add_u32 m0, s42, 61440\n"
      "s_waitcnt vmcnt(18) lgkmcnt(0)\n"
      "s_barrier\n"
      "ds_read_b64_tr_b16 a[56:57], v20 offset:24576\n"
      "ds_read_b64_tr_b16 a[58:59], v20 offset:26112\n"
      "ds_read_b64_tr_b16 a[152:153], v20 offset:24584\n"
      "ds_read_b64_tr_b16 a[154:155], v20 offset:26120\n"
      "ds_read_b64_tr_b16 a[248:249], v21 offset:24576\n"
      "ds_read_b64_tr_b16 a[250:251], v21 offset:26112\n"
      "buffer_load_dwordx4 v178, s[12:15], s63 offen offset:0 lds\n"
      "buffer_load_dwordx4 v179, s[12:15], s63 offen offset:1024 lds\n"
      "buffer_load_dwordx4 v180, s[12:15], s63 offen offset:2048 lds\n"
      "s_sub_u32 s62, s62, 1\n"
      ".Ld_rs15:\n"
      "s_add_u32 s63, s44, 811008\n"
      "s_cmp_ge_u32 s62, 7\n"
      "s_cselect_b32 s63, s63, s31\n"
      "s_add_u32 m0, s42, 73728\n"
      "s_waitcnt vmcnt(18) lgkmcnt(0)\n"
      "s_barrier\n"
      "ds_read_b64_tr_b16 a[60:61], v20 offset:36864\n"
      "ds_read_b64_tr_b16 a[62:63], v20 offset:38400\n"
      "ds_read_b64_tr_b16 a[156:157], v20 offset:36872\n"
      "ds_read_b64_tr_b16 a[158:159], v20 offset:38408\n"
      "ds_read_b64_tr_b16 a[252:253], v21 offset:36864\n"
      "ds_read_b64_tr_b16 a[254:255], v21 offset:38400\n"
      "buffer_load_dwordx4 v178, s[12:15], s63 offen offset:0 lds\n"
      "buffer_load_dwordx4 v179, s[12:15], s63 offen offset:1024 lds\n"
      "buffer_load_dwordx4 v180, s[12:15], s63 offen offset:2048 lds\n"
      "s_sub_u32 s62, s62, 1\n"
      ".Ld_rs16:\n"
      "s_add_u32 s63, s44, 847872\n"
      "s_cmp_ge_u32 s62, 7\n"
      "s_cselect_b32 s63, s63, s31\n"
      "s_add_u32 m0, s42, 86016\n"
      "s_waitcnt vmcnt(18) lgkmcnt(0)\n"
      "s_barrier\n"
      "ds_read_b64_tr_b16 a[64:65], v176 offset:0\n"
      "ds_read_b64_tr_b16 a[66:67], v176 offset:1536\n"
      "ds_read_b64_tr_b16 a[160:161], v176 offset:8\n"
      "ds_read_b64_tr_b16 a[162:163], v176 offset:1544\n"
      "ds_read_b64_tr_b16 v[24:25], v177 offset:0\n"
      "ds_read_b64_tr_b16 v[26:27], v177 offset:1536\n"
      "buffer_load_dwordx4 v178, s[12:15], s63 offen offset:0 lds\n"
      "buffer_load_dwordx4 v179, s[12:15], s63 offen offset:1024 lds\n"
      "buffer_load_dwordx4 v180, s[12:15], s63 offen offset:2048 lds\n"
      "s_sub_u32 s62, s62, 1\n"
      ".Ld_rs17:\n"
      "s_add_u32 s63, s44, 0\n"
      "s_cmp_ge_u32 s62, 7\n"
      "s_cselect_b32 s63, s63, s31\n"
      "s_add_u32 m0, s42, 0\n"
      "s_waitcnt vmcnt(18) lgkmcnt(0)\n"
      "s_barrier\n"
      "ds_read_b64_tr_b16 a[68:69], v176 offset:12288\n"
      "ds_read_b64_tr_b16 a[70:71], v176 offset:13824\n"
      "ds_read_b64_tr_b16 a[164:165], v176 offset:12296\n"
      "ds_read_b64_tr_b16 a[166:167], v176 offset:13832\n"
      "ds_read_b64_tr_b16 v[28:29], v177 offset:12288\n"
      "ds_read_b64_tr_b16 v[30:31], v177 offset:13824\n"
      "buffer_load_dwordx4 v178, s[12:15], s63 offen offset:0 lds\n"
      "buffer_load_dwordx4 v179, s[12:15], s63 offen offset:1024 lds\n"
      "buffer_load_dwordx4 v180, s[12:15], s63 offen offset:2048 lds\n"
      "s_sub_u32 s62, s62, 1\n"
      "s_cbranch_scc1 .Ld_re\n"
      ".Ld_rs18:\n"
      "s_add_u32 s63, s44, 36864\n"
      "s_cmp_ge_u32 s62, 7\n"
      "s_cselect_b32 s63, s63, s31\n"
      "s_add_u32 m0, s42, 12288\n"
      "s_waitcnt vmcnt(18) lgkmcnt(0)\n"
      "s_barrier\n"
      "ds_read_b64_tr_b16 a[72:73], v176 offset:24576\n"
      "ds_read_b64_tr_b16 a[74:75], v176 offset:26112\n"
      "ds_read_b64_tr_b16 a[168:169], v176 offset:24584\n"
      "ds_read_b64_tr_b16 a[170:171], v176 offset:26120\n"
      "ds_read_b64_tr_b16 v[32:33], v177 offset:24576\n"
      "ds_read_b64_tr_b16 v[34:35], v177 offset:26112\n"
      "buffer_load_dwordx4 v178, s[12:15], s63 offen offset:0 lds\n"
      "buffer_load_dwordx4 v179, s[12:15], s63 offen offset:1024 lds\n"
      "buffer_load_dwordx4 v180, s[12:15], s63 offen offset:2048 lds\n"
      "s_sub_u32 s62, s62, 1\n"
      ".Ld_rs19:\n"
      "s_add_u32 s63, s44, 73728\n"
      "s_cmp_ge_u32 s62, 7\n"
      "s_cselect_b32 s63, s63, s31\n"
      "s_add_u32 m0, s42, 24576\n"
      "s_waitcnt vmcnt(18) lgkmcnt(0)\n"
      "s_barrier\n"
      "ds_read_b64_tr_b16 a[76:77], v176 offset:36864\n"
      "ds_read_b64_tr_b16 a[78:79], v176 offset:38400\n"
      "ds_read_b64_tr_b16 a[172:173], v176 offset:36872\n"
      "ds_read_b64_tr_b16 a[174:175], v176 offset:38408\n"
      "ds_read_b64_tr_b16 v[36:37], v177 offset:36864\n"
      "ds_read_b64_tr_b16 v[38:39], v177 offset:38400\n"
      "buffer_load_dwordx4 v178, s[12:15], s63 offen offset:0 lds\n"
      "buffer_load_dwordx4 v179, s[12:15], s63 offen offset:1024 lds\n"
      "buffer_load_dwordx4 v180, s[12:15], s63 offen offset:2048 lds\n"
      "s_sub_u32 s62, s62, 1\n"
      ".Ld_rs20:\n"
      "s_add_u32 s63, s44, 110592\n"
      "s_cmp_ge_u32 s62, 7\n"
      "s_cselect_b32 s63, s63, s31\n"
      "s_add_u32 m0, s42, 36864\n"
      "s_waitcnt vmcnt(18) lgkmcnt(0)\n"
      "s_barrier\n"
      "ds_read_b64_tr_b16 a[80:81], v20 offset:0\n"
      "ds_read_b64_tr_b16 a[82:83], v20 offset:1536\n"
      "ds_read_b64_tr_b16 a[176:177], v20 offset:8\n"
      "ds_read_b64_tr_b16 a[178:179], v20 offset:1544\n"
      "ds_read_b64_tr_b16 v[40:41], v21 offset:0\n"
      "ds_read_b64_tr_b16 v[42:43], v21 offset:1536\n"
      "buffer_load_dwordx4 v178, s[12:15], s63 offen offset:0 lds\n"
      "buffer_load_dwordx4 v179, s[12:15], s63 offen offset:1024 lds\n"
      "buffer_load_dwordx4 v180, s[12:15], s63 offen offset:2048 lds\n"
      "s_sub_u32 s62, s62, 1\n"
      ".Ld_rs21:\n"
      "s_add_u32 s63, s44, 147456\n"
      "s_cmp_ge_u32 s62, 7\n"
      "s_cselect_b32 s63, s63, s31\n"
      "s_add_u32 m0, s42, 49152\n"
      "s_waitcnt vmcnt(18) lgkmcnt(0)\n"
      "s_barrier\n"
      "ds_read_b64_tr_b16 a[84:85], v20 offset:12288\n"
      "ds_read_b64_tr_b16 a[86:87], v20 offset:13824\n"
      "ds_read_b64_tr_b16 a[180:181], v20 offset:12296\n"
      "ds_read_b64_tr_b16 a[182:183], v20 offset:13832\n"
      "ds_read_b64_tr_b16 v[44:45], v21 offset:12288\n"
      "ds_read_b64_tr_b16 v[46:47], v21 offset:13824\n"
      "buffer_load_dwordx4 v178, s[12:15], s63 offen offset:0 lds\n"
      "buffer_load_dwordx4 v179, s[12:15], s63 offen offset:1024 lds\n"
      "buffer_load_dwordx4 v180, s[12:15], s63 offen offset:2048 lds\n"
      "s_sub_u32 s62, s62, 1\n"
      ".Ld_rs22:\n"
      "s_add_u32 s63, s44, 184320\n"
      "s_cmp_ge_u32 s62, 7\n"
      "s_cselect_b32 s63, s63, s31\n"
      "s_add_u32 m0, s42, 61440\n"
      "s_waitcnt vmcnt(18) lgkmcnt(0)\n"
      "s_barrier\n"
      "ds_read_b64_tr_b16 a[88:89], v20 offset:24576\n"
      "ds_read_b64_tr_b16 a[90:91], v20 offset:26112\n"
      "ds_read_b64_tr_b16 a[184:185], v20 offset:24584\n"
      "ds_read_b64_tr_b16 a[186:187], v20 offset:26120\n"
      "ds_read_b64_tr_b16 v[48:49], v21 offset:24576\n"
      "ds_read_b64_tr_b16 v[50:51], v21 offset:26112\n"
      "buffer_load_dwordx4 v178, s[12:15], s63 offen offset:0 lds\n"
      "buffer_load_dwordx4 v179, s[12:15], s63 offen offset:1024 lds\n"
      "buffer_load_dwordx4 v180, s[12:15], s63 offen offset:2048 lds\n"
      "s_sub_u32 s62, s62, 1\n"
      ".Ld_rs23:\n"
      "s_add_u32 s63, s44, 221184\n"
      "s_cmp_ge_u32 s62, 7\n"
      "s_cselect_b32 s63, s63, s31\n"
      "s_add_u32 m0, s42, 73728\n"
      "s_waitcnt vmcnt(18) lgkmcnt(0)\n"
      "s_barrier\n"
      "ds_read_b64_tr_b16 a[92:93], v20 offset:36864\n"
      "ds_read_b64_tr_b16 a[94:95], v20 offset:38400\n"
      "ds_read_b64_tr_b16 a[188:189], v20 offset:36872\n"
      "ds_read_b64_tr_b16 a[190:191], v20 offset:38408\n"
      "ds_read_b64_tr_b16 v[52:53], v21 offset:36864\n"
      "ds_read_b64_tr_b16 v[54:55], v21 offset:38400\n"
      "buffer_load_dwordx4 v178, s[12:15], s63 offen offset:0 lds\n"
      "buffer_load_dwordx4 v179, s[12:15], s63 offen offset:1024 lds\n"
      "buffer_load_dwordx4 v180, s[12:15], s63 offen offset:2048 lds\n"
      "s_sub_u32 s62, s62, 1\n"
      "s_cbranch_scc0 .Ld_rs0\n"
      ".Ld_re:\n"
      "s_waitcnt vmcnt(0) lgkmcnt(0)\n"
      "v_add_u32 v22, 49152, v0\n"
      "v_add_u32 v23, 98304, v0\n"
      "s_barrier\n"
      "v_mov_b32 v14, 0\n"
      "ds_write_b32 v16, v14\n"
      "s_waitcnt lgkmcnt(0)\n"
      "s_barrier\n"
      "s_mov_b32 s6, s50\n"
      "s_add_u32 s36, s36, 4294801408\n"
      "s_add_u32 s53, s37, 0\n"
      "s_add_u32 s52, s37, 4294787584\n"
      "s_add_u32 s37, s37, 4294894080\n"
      "s_mov_b32 s56, 0x40000000\n"
      "s_mov_b32 s57, 0x40000000\n"
      "s_mov_b32 s59, 63\n"
      "s_cmp_eq_u32 s45, 1\n"
      "s_cbranch_scc1 .Ld_in1\n"
      "s_cmp_eq_u32 s45, 2\n"
      "s_cbranch_scc1 .Ld_in2\n"
      "s_cmp_eq_u32 s45, 3\n"
      "s_cbranch_scc1 .Ld_in3\n"
      "s_cmp_eq_u32 s45, 4\n"
      "s_cbranch_scc1 .Ld_in4\n"
      "s_cmp_eq_u32 s45, 5\n"
      "s_cbranch_scc1 .Ld_in5\n"
      "s_add_u32 s36, s36, 165888\n"
      "s_min_u32 s36, s36, s6\n"
      "buffer_load_dword v20, v2, s[8:11], s36 offen sc1\n"
      "s_mov_b32 s51, s36\n"
      "s_add_u32 m0, s43, 0\n"
      "s_add_u32 s36, s36, 165888\n"
      "s_waitcnt vmcnt(0)\n"
      "v_cmp_ne_u32 vcc, s21, v20\n"
      "s_cbranch_vccnz .Lsp_d0_1\n"
      ".Lbk_d0_1:\n"
      "buffer_load_dwordx4 v0, s[8:11], s51 offen offset:1024 sc1 lds\n"
      "buffer_load_dwordx4 v0, s[8:11], s51 offen offset:0 sc1 lds\n"
      "buffer_load_dwordx4 v0, s[8:11], s51 offen offset:3072 sc1 lds\n"
      "buffer_load_dwordx4 v0, s[8:11], s51 offen offset:2048 sc1 lds\n"
      "s_min_u32 s36, s36, s6\n"
      "s_add_u32 m0, s43, 4096\n"
      "s_nop 0\n"
      "buffer_load_dwordx4 v1, s[8:11], s51 offen offset:0 sc1 lds\n"
      "buffer_load_dwordx4 v1, s[8:11], s51 offen offset:1024 sc1 lds\n"
      "buffer_load_dword v20, v2, s[8:11], s36 offen sc1\n"
      "buffer_load_dwordx2 v[220:221], v7, s[8:11], s38 offen sc1\n"
      "v_add_u32 v14, 1, v14\n"
      "s_mov_b32 s51, s36\n"
      "buffer_load_dwordx4 v[216:219], v6, s[8:11], s38 offen sc1\n"
      "s_add_u32 m0, s43, 24576\n"
      "s_waitcnt vmcnt(3)\n"
      "ds_write_b32 v16, v14\n"
      "s_waitcnt vmcnt(2)\n"
      "v_cmp_ne_u32 vcc, s21, v20\n"
      "s_add_u32 s36, s36, 165888\n"
      "s_cbranch_vccnz .Lsp_d1_2\n"
      ".Lbk_d1_2:\n"
      "buffer_load_dwordx4 v0, s[8:11], s51 offen offset:0 sc1 lds\n"
      "ds_read_b32 v15, v17\n"
      "buffer_load_dwordx4 v0, s[8:11], s51 offen offset:1024 sc1 lds\n"
      "s_waitcnt lgkmcnt(0)\n"
      "v_cmp_gt_i32 vcc, v14, v15\n"
      "buffer_load_dwordx4 v0, s[8:11], s51 offen offset:3072 sc1 lds\n"
      "s_cbranch_vccnz .Lsp_dy0_3\n"
      ".Lbk_dy0_3:\n"
      "buffer_load_dwordx4 v0, s[8:11], s51 offen offset:2048 sc1 lds\n"
      "s_add_u32 m0, s43, 28672\n"
      "ds_read_b128 v[64:67], v0 offset:2048\n"
      "ds_read_b128 v[56:59], v0 offset:0\n"
      "buffer_load_dwordx4 v1, s[8:11], s51 offen offset:1024 sc1 lds\n"
      "ds_read_b128 v[72:75], v0 offset:4096\n"
      "buffer_load_dwordx4 v1, s[8:11], s51 offen offset:0 sc1 lds\n"
      "ds_read_b128 v[68:71], v0 offset:3072\n"
      "ds_read_b128 v[60:63], v0 offset:1024\n"
      "s_min_u32 s36, s36, s6\n"
      "ds_read_b128 v[76:79], v0 offset:5120\n"
      "ds_read_b128 v[92:95], v0 offset:9216\n"
      "s_add_u32 s38, s38, 147456\n"
      "ds_read_b128 v[112:115], v0 offset:14336\n"
      "buffer_load_dword v20, v2, s[8:11], s36 offen sc1\n"
      "ds_read_b128 v[84:87], v0 offset:7168\n"
      "ds_read_b128 v[80:83], v0 offset:6144\n"
      "ds_read_b128 v[100:103], v0 offset:11264\n"
      "ds_read_b128 v[116:119], v0 offset:15360\n"
      "ds_read_b128 v[88:91], v0 offset:8192\n"
      "ds_read_b128 v[124:127], v0 offset:17408\n"
      "s_waitcnt vmcnt(0) lgkmcnt(0)\n"
      "s_nop 4\n"
      ".p2align 6\n"
      ".Ld_t0:\n"
      "v_mfma_f32_16x16x32_bf16 v[152:155], a[0:3], v[56:59], 0\n"
      "s_waitcnt lgkmcnt(5)\n"
      "ds_read_b128 v[96:99], v0 offset:10240\n"
      "v_and_b32 v212, 0xffff0000, v212\n"
      "v_mfma_f32_16x16x32_bf16 v[152:155], a[4:7], v[60:63], v[152:155]\n"
      "ds_read_b128 v[108:111], v0 offset:13312\n"
      "v_add_f32 v175, v175, v205\n"
      "v_mfma_f32_16x16x32_bf16 v[152:155], a[8:11], v[64:67], v[152:155]\n"
      "ds_read_b128 v[104:107], v0 offset:12288\n"
      "v_cvt_pk_bf16_f32 v229, v162, v163\n"
      "v_mfma_f32_16x16x32_bf16 v[152:155], a[12:15], v[68:71], v[152:155]\n"
      "ds_read_b128 v[120:123], v0 offset:16384\n"
      "s_mov_b32 s51, s36\n"
      "v_mfma_f32_16x16x32_bf16 v[152:155], a[16:19], v[72:75], v[152:155]\n"
      "v_add_f32 v236, v236, v171\n"
      "v_add_f32 v236, v236, v172\n"
      "v_mfma_f32_16x16x32_bf16 v[152:155], a[20:23], v[76:79], v[152:155]\n"
      "s_waitcnt lgkmcnt(4)\n"
      "v_add_f32 v175, v175, v213\n"
      "v_cvt_pk_bf16_f32 v228, v160, v161\n"
      "v_mfma_f32_16x16x32_bf16 v[152:155], a[24:27], v[80:83], v[152:155]\n"
      "ds_read_b128 v[128:131], v0 offset:18432\n"
      "v_add_f32 v173, v173, v212\n"
      "v_mfma_f32_16x16x32_bf16 v[152:155], a[28:31], v[84:87], v[152:155]\n"
      "v_cvt_pk_bf16_f32 v176, v164, v165\n"
      "v_fmac_f32 v237, v171, v171\n"
      "v_mfma_f32_16x16x32_bf16 v[152:155], a[32:35], v[88:91], v[152:155]\n"
      "v_fmac_f32 v237, v172, v172\n"
      "v_add_f32 v236, v236, v173\n"
      "v_mfma_f32_16x16x32_bf16 v[152:155], a[36:39], v[92:95], v[152:155]\n"
      "s_waitcnt lgkmcnt(2)\n"
      "buffer_store_dwordx2 v[228:229], v13, s[8:11], s37 offen sc1\n"
      "v_mfma_f32_16x16x32_bf16 v[152:155], a[40:43], v[96:99], v[152:155]\n"
      "ds_read_b128 v[136:139], v0 offset:20480\n"
      "v_fmac_f32 v237, v173, v173\n"
      "v_mfma_f32_16x16x32_bf16 v[152:155], a[44:47], v[100:103], v[152:155]\n"
      "ds_read_b128 v[132:135], v0 offset:19456\n"
      "v_add_f32 v236, v236, v174\n"
      "v_mfma_f32_16x16x32_bf16 v[152:155], a[48:51], v[104:107], v[152:155]\n"
      "ds_read_b128 v[140:143], v0 offset:21504\n"
      "v_cvt_pk_bf16_f32 v179, v170, v171\n"
      "v_mfma_f32_16x16x32_bf16 v[152:155], a[52:55], v[108:111], v[152:155]\n"
      "buffer_store_dwordx4 v[176:179], v6, s[8:11], s56 offen sc1\n"
      "v_mfma_f32_16x16x32_bf16 v[152:155], a[56:59], v[112:115], v[152:155]\n"
      "ds_read_b128 v[144:147], v0 offset:22528\n"
      "v_fmac_f32 v237, v174, v174\n"
      "v_mfma_f32_16x16x32_bf16 v[152:155], a[60:63], v[116:119], v[152:155]\n"
      "s_waitcnt lgkmcnt(4)\n"
      "ds_read_b128 v[148:151], v0 offset:23552\n"
      "v_add_f32 v236, v236, v175\n"
      "v_mfma_f32_16x16x32_bf16 v[152:155], a[64:67], v[120:123], v[152:155]\n"
      "v_fmac_f32 v237, v175, v175\n"
      "v_mfma_f32_16x16x32_bf16 v[152:155], a[68:71], v[124:127], v[152:155]\n"
      "ds_write_b64 v10, v[236:237]\n"
      "v_mfma_f32_16x16x32_bf16 v[152:155], a[72:75], v[128:131], v[152:155]\n"
      "s_waitcnt lgkmcnt(4)\n"
      "v_mfma_f32_16x16x32_bf16 v[152:155], a[76:79], v[132:135], v[152:155]\n"
      "buffer_store_dwordx4 v[176:179], v8, s[16:19], s57 offen\n"
      "v_mfma_f32_16x16x32_bf16 v[152:155], a[80:83], v[136:139], v[152:155]\n"
      "s_waitcnt lgkmcnt(0)\n"
      "s_barrier\n"
      "ds_read_b64 v[238:239], v11\n"
      "v_mfma_f32_16x16x32_bf16 v[152:155], a[84:87], v[140:143], v[152:155]\n"
      "v_cvt_pk_bf16_f32 v180, v172, v173\n"
      "v_mfma_f32_16x16x32_bf16 v[152:155], a[88:91], v[144:147], v[152:155]\n"
      "s_add_u32 s36, s36, 165888\n"
      "v_add_u32 v14, 1, v14\n"
      "v_mfma_f32_16x16x32_bf16 v[152:155], a[92:95], v[148:151], v[152:155]\n"
      "s_min_u32 s36, s36, s6\n"
      "v_cvt_pk_bf16_f32 v181, v174, v175\n"
      "v_mfma_f32_16x16x32_bf16 v[156:159], a[96:99], v[56:59], 0\n"
      "s_waitcnt vmcnt(12)\n"
      "v_lshlrev_b32 v164, 16, v216\n"
      "v_mfma_f32_16x16x32_bf16 v[156:159], a[100:103], v[60:63], v[156:159]\n"
      "v_and_b32 v167, 0xffff0000, v217\n"
      "v_mfma_f32_16x16x32_bf16 v[156:159], a[104:107], v[64:67], v[156:159]\n"
      "buffer_store_dwordx2 v[180:181], v7, s[8:11], s56 offen sc1\n"
      "v_mfma_f32_16x16x32_bf16 v[156:159], a[108:111], v[68:71], v[156:159]\n"
      "s_waitcnt lgkmcnt(0)\n"
      "v_add_f32_dpp v238, v238, v238 row_ror:8 row_mask:0xf bank_mask:0xf\n"
      "v_add_f32_dpp v239, v239, v239 row_ror:8 row_mask:0xf bank_mask:0xf\n"
      "v_mfma_f32_16x16x32_bf16 v[156:159], a[112:115], v[72:75], v[156:159]\n"
      "s_add_u32 m0, s43, 49152\n"
      "v_add_f32_dpp v238, v238, v238 row_ror:4 row_mask:0xf bank_mask:0xf\n"
      "v_add_f32_dpp v239, v239, v239 row_ror:4 row_mask:0xf bank_mask:0xf\n"
      "v_mfma_f32_16x16x32_bf16 v[156:159], a[116:119], v[76:79], v[156:159]\n"
      "v_add_f32_dpp v238, v238, v238 row_ror:2 row_mask:0xf bank_mask:0xf\n"
      "v_add_f32_dpp v239, v239, v239 row_ror:2 row_mask:0xf bank_mask:0xf\n"
      "v_mfma_f32_16x16x32_bf16 v[156:159], a[120:123], v[80:83], v[156:159]\n"
      "buffer_store_dwordx2 v[180:181], v9, s[16:19], s57 offen\n"
      "v_mfma_f32_16x16x32_bf16 v[156:159], a[124:127], v[84:87], v[156:159]\n"
      "v_add_f32_dpp v238, v238, v238 row_ror:1 row_mask:0xf bank_mask:0xf\n"
      "v_add_f32_dpp v239, v239, v239 row_ror:1 row_mask:0xf bank_mask:0xf\n"
      "v_mfma_f32_16x16x32_bf16 v[156:159], a[128:131], v[88:91], v[156:159]\n"
      "s_waitcnt vmcnt(8)\n"
      "ds_write_b32 v16, v14\n"
      "v_add_f32 v239, 0x3afba882, v239\n"
      "v_mfma_f32_16x16x32_bf16 v[156:159], a[132:135], v[92:95], v[156:159]\n"
      "v_mul_f32 v239, 0x3ae38e39, v239\n"
      "v_mul_f32 v238, 0x3ae38e39, v238\n"
      "v_mfma_f32_16x16x32_bf16 v[156:159], a[136:139], v[96:99], v[156:159]\n"
      "buffer_store_dwordx2 v[238:239], v12, s[8:11], s56 offen sc1\n"
      "v_mfma_f32_16x16x32_bf16 v[156:159], a[140:143], v[100:103], v[156:159]\n"
      "v_mfma_f32_16x16x32_bf16 v[156:159], a[144:147], v[104:107], v[156:159]\n"
      "s_waitcnt vmcnt(7)\n"
      "v_cmp_ne_u32 vcc, s21, v20\n"
      "v_mfma_f32_16x16x32_bf16 v[156:159], a[148:151], v[108:111], v[156:159]\n"
      "v_and_b32 v165, 0xffff0000, v216\n"
      "v_mfma_f32_16x16x32_bf16 v[156:159], a[152:155], v[112:115], v[156:159]\n"
      "v_lshlrev_b32 v166, 16, v217\n"
      "v_add_f32 v167, v167, v155\n"
      "v_mfma_f32_16x16x32_bf16 v[156:159], a[156:159], v[116:119], v[156:159]\n"
      "s_cbranch_vccnz .Lsp_d2_4\n"
      ".Lbk_d2_4:\n"
      "buffer_load_dwordx4 v0, s[8:11], s51 offen offset:0 sc1 lds\n"
      "v_mfma_f32_16x16x32_bf16 v[156:159], a[160:163], v[120:123], v[156:159]\n"
      "v_add_f32 v166, v166, v154\n"
      "v_mfma_f32_16x16x32_bf16 v[156:159], a[164:167], v[124:127], v[156:159]\n"
      "ds_read_b32 v15, v17\n"
      "v_mfma_f32_16x16x32_bf16 v[156:159], a[168:171], v[128:131], v[156:159]\n"
      "v_mfma_f32_16x16x32_bf16 v[156:159], a[172:175], v[132:135], v[156:159]\n"
      "buffer_load_dwordx4 v0, s[8:11], s51 offen offset:3072 sc1 lds\n"
      "v_mfma_f32_16x16x32_bf16 v[156:159], a[176:179], v[136:139], v[156:159]\n"
      "s_add_u32 s52, s52, 106496\n"
      "v_mfma_f32_16x16x32_bf16 v[156:159], a[180:183], v[140:143], v[156:159]\n"
      "v_mfma_f32_16x16x32_bf16 v[156:159], a[184:187], v[144:147], v[156:159]\n"
      "s_waitcnt lgkmcnt(0)\n"
      "v_cmp_gt_i32 vcc, v14, v15\n"
      "v_mfma_f32_16x16x32_bf16 v[156:159], a[188:191], v[148:151], v[156:159]\n"
      "buffer_load_dwordx4 v0, s[8:11], s51 offen offset:2048 sc1 lds\n"
      "v_mfma_f32_16x16x32_bf16 v[160:163], a[192:195], v[56:59], 0\n"
      "v_mfma_f32_16x16x32_bf16 v[160:163], a[196:199], v[60:63], v[160:163]\n"
      "v_mfma_f32_16x16x32_bf16 v[160:163], a[200:203], v[64:67], v[160:163]\n"
      "s_cbranch_vccnz .Lsp_dy1_5\n"
      ".Lbk_dy1_5:\n"
      "v_mfma_f32_16x16x32_bf16 v[160:163], a[204:207], v[68:71], v[160:163]\n"
      "buffer_load_dword v20, v2, s[8:11], s36 offen sc1\n"
      "v_mfma_f32_16x16x32_bf16 v[160:163], a[208:211], v[72:75], v[160:163]\n"
      "ds_read_b128 v[56:59], v0 offset:24576\n"
      "v_mfma_f32_16x16x32_bf16 v[160:163], a[212:215], v[76:79], v[160:163]\n"
      "buffer_load_dwordx4 v0, s[8:11], s51 offen offset:1024 sc1 lds\n"
      "v_mfma_f32_16x16x32_bf16 v[160:163], a[216:219], v[80:83], v[160:163]\n"
      "v_mfma_f32_16x16x32_bf16 v[160:163], a[220:223], v[84:87], v[160:163]\n"
      "ds_read_b128 v[68:71], v0 offset:27648\n"
      "v_mfma_f32_16x16x32_bf16 v[160:163], a[224:227], v[88:91], v[160:163]\n"
      "ds_read_b128 v[84:87], v0 offset:31744\n"
      "v_mfma_f32_16x16x32_bf16 v[160:163], a[228:231], v[92:95], v[160:163]\n"
      "s_add_u32 m0, s43, 53248\n"
      "ds_read_b128 v[60:63], v0 offset:25600\n"
      "v_and_b32 v169, 0xffff0000, v218\n"
      "v_mfma_f32_16x16x32_bf16 v[160:163], a[232:235], v[96:99], v[160:163]\n"
      "buffer_load_dwordx4 v1, s[8:11], s51 offen offset:0 sc1 lds\n"
      "v_mfma_f32_16x16x32_bf16 v[160:163], a[236:239], v[100:103], v[160:163]\n"
      "ds_read_b128 v[100:103], v0 offset:35840\n"
      "v_mfma_f32_16x16x32_bf16 v[160:163], a[240:243], v[104:107], v[160:163]\n"
      "buffer_load_dwordx4 v1, s[8:11], s51 offen offset:1024 sc1 lds\n"
      "v_mfma_f32_16x16x32_bf16 v[160:163], a[244:247], v[108:111], v[160:163]\n"
      "ds_read_b128 v[104:107], v0 offset:36864\n"
      "v_add_f32 v169, v169, v157\n"
      "v_mfma_f32_16x16x32_bf16 v[160:163], a[248:251], v[112:115], v[160:163]\n"
      "s_mov_b32 s51, s36\n"
      "ds_read_b128 v[72:75], v0 offset:28672\n"
      "v_mfma_f32_16x16x32_bf16 v[160:163], a[252:255], v[116:119], v[160:163]\n"
      "ds_read_b128 v[108:111], v0 offset:37888\n"
      "v_mfma_f32_16x16x32_bf16 v[160:163], v[24:27], v[120:123], v[160:163]\n"
      "s_waitcnt vmcnt(12) lgkmcnt(4)\n"
      "buffer_store_dword v18, v4, s[8:11], s52 offen sc1\n"
      "v_mfma_f32_16x16x32_bf16 v[160:163], v[28:31], v[124:127], v[160:163]\n"
      "ds_read_b128 v[64:67], v0 offset:26624\n"
      "v_lshlrev_b32 v170, 16, v219\n"
      "v_mfma_f32_16x16x32_bf16 v[160:163], v[32:35], v[128:131], v[160:163]\n"
      "ds_read_b128 v[92:95], v0 offset:33792\n"
      "v_add_f32 v164, v164, v152\n"
      "v_mfma_f32_16x16x32_bf16 v[160:163], v[36:39], v[132:135], v[160:163]\n"
      "ds_read_b128 v[80:83], v0 offset:30720\n"
      "v_add_f32 v165, v165, v153\n"
      "v_mfma_f32_16x16x32_bf16 v[160:163], v[40:43], v[136:139], v[160:163]\n"
      "ds_read_b128 v[96:99], v0 offset:34816\n"
      "v_lshlrev_b32 v168, 16, v218\n"
      "v_mfma_f32_16x16x32_bf16 v[160:163], v[44:47], v[140:143], v[160:163]\n"
      "ds_read_b128 v[76:79], v0 offset:29696\n"
      "v_mfma_f32_16x16x32_bf16 v[160:163], v[48:51], v[144:147], v[160:163]\n"
      "ds_read_b128 v[116:119], v0 offset:39936\n"
      "v_mfma_f32_16x16x32_bf16 v[160:163], v[52:55], v[148:151], v[160:163]\n"
      "s_sub_u32 s59, s59, 1\n"
      "s_cbranch_scc1 .Ld_out0\n"
      ".Ld_t1:\n"
      "v_mfma_f32_16x16x32_bf16 v[152:155], a[0:3], v[56:59], 0\n"
      "ds_read_b128 v[88:91], v0 offset:32768\n"
      "v_mfma_f32_16x16x32_bf16 v[152:155], a[4:7], v[60:63], v[152:155]\n"
      "s_waitcnt lgkmcnt(5)\n"
      "ds_read_b128 v[112:115], v0 offset:38912\n"
      "v_and_b32 v171, 0xffff0000, v219\n"
      "v_mfma_f32_16x16x32_bf16 v[152:155], a[8:11], v[64:67], v[152:155]\n"
      "ds_read_b128 v[144:147], v0 offset:47104\n"
      "v_lshlrev_b32 v174, 16, v221\n"
      "v_mfma_f32_16x16x32_bf16 v[152:155], a[12:15], v[68:71], v[152:155]\n"
      "ds_read_b128 v[132:135], v0 offset:44032\n"
      "v_mfma_f32_16x16x32_bf16 v[152:155], a[16:19], v[72:75], v[152:155]\n"
      "s_waitcnt lgkmcnt(5)\n"
      "ds_read_b128 v[120:123], v0 offset:40960\n"
      "v_mfma_f32_16x16x32_bf16 v[152:155], a[20:23], v[76:79], v[152:155]\n"
      "v_mfma_f32_16x16x32_bf16 v[152:155], a[24:27], v[80:83], v[152:155]\n"
      "ds_read_b128 v[124:127], v0 offset:41984\n"
      "v_mfma_f32_16x16x32_bf16 v[152:155], a[28:31], v[84:87], v[152:155]\n"
      "s_waitcnt lgkmcnt(5)\n"
      "ds_read_b128 v[140:143], v0 offset:46080\n"
      "v_mfma_f32_16x16x32_bf16 v[152:155], a[32:35], v[88:91], v[152:155]\n"
      "ds_read_b128 v[128:131], v0 offset:43008\n"
      "v_add_f32 v168, v168, v156\n"
      "v_mfma_f32_16x16x32_bf16 v[152:155], a[36:39], v[92:95], v[152:155]\n"
      "v_add_f32 v174, v174, v162\n"
      "v_mfma_f32_16x16x32_bf16 v[152:155], a[40:43], v[96:99], v[152:155]\n"
      "s_waitcnt vmcnt(8) lgkmcnt(4)\n"
      "buffer_store_dword v18, v5, s[8:11], s56 offen sc1\n"
      "v_mfma_f32_16x16x32_bf16 v[152:155], a[44:47], v[100:103], v[152:155]\n"
      "ds_read_b128 v[136:139], v0 offset:45056\n"
      "v_mfma_f32_16x16x32_bf16 v[152:155], a[48:51], v[104:107], v[152:155]\n"
      "v_mfma_f32_16x16x32_bf16 v[152:155], a[52:55], v[108:111], v[152:155]\n"
      "v_mfma_f32_16x16x32_bf16 v[152:155], a[56:59], v[112:115], v[152:155]\n"
      "ds_read_b128 v[148:151], v0 offset:48128\n"
      "v_lshlrev_b32 v172, 16, v220\n"
      "v_mfma_f32_16x16x32_bf16 v[152:155], a[60:63], v[116:119], v[152:155]\n"
      "s_waitcnt lgkmcnt(2)\n"
      "v_mfma_f32_16x16x32_bf16 v[152:155], a[64:67], v[120:123], v[152:155]\n"
      "v_mfma_f32_16x16x32_bf16 v[152:155], a[68:71], v[124:127], v[152:155]\n"
      "v_mfma_f32_16x16x32_bf16 v[152:155], a[72:75], v[128:131], v[152:155]\n"
      "v_add_f32 v171, v171, v159\n"
      "v_mfma_f32_16x16x32_bf16 v[152:155], a[76:79], v[132:135], v[152:155]\n"
      "s_waitcnt lgkmcnt(1)\n"
      "s_add_u32 s37, s37, 173056\n"
      "v_and_b32 v175, 0xffff0000, v221\n"
      "v_mfma_f32_16x16x32_bf16 v[152:155], a[80:83], v[136:139], v[152:155]\n"
      "v_and_b32 v173, 0xffff0000, v220\n"
      "v_mfma_f32_16x16x32_bf16 v[152:155], a[84:87], v[140:143], v[152:155]\n"
      "v_mfma_f32_16x16x32_bf16 v[152:155], a[88:91], v[144:147], v[152:155]\n"
      "s_waitcnt lgkmcnt(0)\n"
      "v_add_f32 v173, v173, v161\n"
      "v_add_f32 v170, v170, v158\n"
      "v_mfma_f32_16x16x32_bf16 v[152:155], a[92:95], v[148:151], v[152:155]\n"
      "v_mfma_f32_16x16x32_bf16 v[156:159], a[96:99], v[56:59], 0\n"
      "v_mfma_f32_16x16x32_bf16 v[156:159], a[100:103], v[60:63], v[156:159]\n"
      "v_mfma_f32_16x16x32_bf16 v[156:159], a[104:107], v[64:67], v[156:159]\n"
      "s_add_u32 s36, s36, 165888\n"
      "s_min_u32 s36, s36, s6\n"
      "v_mfma_f32_16x16x32_bf16 v[156:159], a[108:111], v[68:71], v[156:159]\n"
      "v_mfma_f32_16x16x32_bf16 v[156:159], a[112:115], v[72:75], v[156:159]\n"
      "v_mfma_f32_16x16x32_bf16 v[156:159], a[116:119], v[76:79], v[156:159]\n"
      "v_add_u32 v14, 1, v14\n"
      "v_add_f32 v175, v175, v163\n"
      "v_mfma_f32_16x16x32_bf16 v[156:159], a[120:123], v[80:83], v[156:159]\n"
      "s_waitcnt vmcnt(5)\n"
      "v_cmp_ne_u32 vcc, s21, v20\n"
      "v_add_f32 v172, v172, v160\n"
      "v_mfma_f32_16x16x32_bf16 v[156:159], a[124:127], v[84:87], v[156:159]\n"
      "v_mfma_f32_16x16x32_bf16 v[156:159], a[128:131], v[88:91], v[156:159]\n"
      "v_mfma_f32_16x16x32_bf16 v[156:159], a[132:135], v[92:95], v[156:159]\n"
      "s_waitcnt vmcnt(2)\n"
      "ds_write_b32 v16, v14\n"
      "v_mfma_f32_16x16x32_bf16 v[156:159], a[136:139], v[96:99], v[156:159]\n"
      "s_cbranch_vccnz .Lsp_d3_6\n"
      ".Lbk_d3_6:\n"
      "s_add_u32 m0, s43, 73728\n"
      "v_mfma_f32_16x16x32_bf16 v[156:159], a[140:143], v[100:103], v[156:159]\n"
      "buffer_load_dwordx4 v0, s[8:11], s51 offen offset:2048 sc1 lds\n"
      "v_mfma_f32_16x16x32_bf16 v[156:159], a[144:147], v[104:107], v[156:159]\n"
      "v_mfma_f32_16x16x32_bf16 v[156:159], a[148:151], v[108:111], v[156:159]\n"
      "v_mfma_f32_16x16x32_bf16 v[156:159], a[152:155], v[112:115], v[156:159]\n"
      "v_mfma_f32_16x16x32_bf16 v[156:159], a[156:159], v[116:119], v[156:159]\n"
      "v_cvt_pk_bf16_f32 v224, v152, v153\n"
      "v_mfma_f32_16x16x32_bf16 v[156:159], a[160:163], v[120:123], v[156:159]\n"
      "v_mfma_f32_16x16x32_bf16 v[156:159], a[164:167], v[124:127], v[156:159]\n"
      "buffer_load_dwordx4 v0, s[8:11], s51 offen offset:0 sc1 lds\n"
      "v_mfma_f32_16x16x32_bf16 v[156:159], a[168:171], v[128:131], v[156:159]\n"
      "ds_read_b32 v15, v17\n"
      "v_mfma_f32_16x16x32_bf16 v[156:159], a[172:175], v[132:135], v[156:159]\n"
      "s_add_u32 s52, s52, 173056\n"
      "v_mfma_f32_16x16x32_bf16 v[156:159], a[176:179], v[136:139], v[156:159]\n"
      "v_mfma_f32_16x16x32_bf16 v[156:159], a[180:183], v[140:143], v[156:159]\n"
      "buffer_load_dwordx4 v0, s[8:11], s51 offen offset:1024 sc1 lds\n"
      "v_mfma_f32_16x16x32_bf16 v[156:159], a[184:187], v[144:147], v[156:159]\n"
      "v_mfma_f32_16x16x32_bf16 v[156:159], a[188:191], v[148:151], v[156:159]\n"
      "s_waitcnt lgkmcnt(0)\n"
      "v_cmp_gt_i32 vcc, v14, v15\n"
      "v_mfma_f32_16x16x32_bf16 v[160:163], a[192:195], v[56:59], 0\n"
      "v_mfma_f32_16x16x32_bf16 v[160:163], a[196:199], v[60:63], v[160:163]\n"
      "buffer_load_dword v20, v2, s[8:11], s36 offen sc1\n"
      "v_mfma_f32_16x16x32_bf16 v[160:163], a[200:203], v[64:67], v[160:163]\n"
      "s_cbranch_vccnz .Lsp_dy2_7\n"
      ".Lbk_dy2_7:\n"
      "v_mfma_f32_16x16x32_bf16 v[160:163], a[204:207], v[68:71], v[160:163]\n"
      "buffer_load_dwordx4 v0, s[8:11], s51 offen offset:3072 sc1 lds\n"
      "v_mfma_f32_16x16x32_bf16 v[160:163], a[208:211], v[72:75], v[160:163]\n"
      "ds_read_b128 v[68:71], v22 offset:3072\n"
      "s_add_u32 m0, s43, 77824\n"
      "v_mfma_f32_16x16x32_bf16 v[160:163], a[212:215], v[76:79], v[160:163]\n"
      "ds_read_b128 v[60:63], v22 offset:1024\n"
      "v_mfma_f32_16x16x32_bf16 v[160:163], a[216:219], v[80:83], v[160:163]\n"
      "ds_read_b128 v[56:59], v22 offset:0\n"
      "v_cvt_pk_bf16_f32 v225, v154, v155\n"
      "v_mfma_f32_16x16x32_bf16 v[160:163], a[220:223], v[84:87], v[160:163]\n"
      "ds_read_b128 v[72:75], v22 offset:4096\n"
      "v_mfma_f32_16x16x32_bf16 v[160:163], a[224:227], v[88:91], v[160:163]\n"
      "buffer_load_dwordx4 v1, s[8:11], s51 offen offset:0 sc1 lds\n"
      "v_mfma_f32_16x16x32_bf16 v[160:163], a[228:231], v[92:95], v[160:163]\n"
      "ds_read_b128 v[64:67], v22 offset:2048\n"
      "v_mfma_f32_16x16x32_bf16 v[160:163], a[232:235], v[96:99], v[160:163]\n"
      "ds_read_b128 v[80:83], v22 offset:6144\n"
      "v_mfma_f32_16x16x32_bf16 v[160:163], a[236:239], v[100:103], v[160:163]\n"
      "ds_read_b128 v[96:99], v22 offset:10240\n"
      "v_mfma_f32_16x16x32_bf16 v[160:163], a[240:243], v[104:107], v[160:163]\n"
      "v_mfma_f32_16x16x32_bf16 v[160:163], a[244:247], v[108:111], v[160:163]\n"
      "buffer_load_dwordx4 v1, s[8:11], s51 offen offset:1024 sc1 lds\n"
      "v_mfma_f32_16x16x32_bf16 v[160:163], a[248:251], v[112:115], v[160:163]\n"
      "ds_read_b128 v[112:115], v22 offset:14336\n"
      "v_mfma_f32_16x16x32_bf16 v[160:163], a[252:255], v[116:119], v[160:163]\n"
      "ds_read_b128 v[92:95], v22 offset:9216\n"
      "v_mfma_f32_16x16x32_bf16 v[160:163], v[24:27], v[120:123], v[160:163]\n"
      "ds_read_b128 v[84:87], v22 offset:7168\n"
      "v_mfma_f32_16x16x32_bf16 v[160:163], v[28:31], v[124:127], v[160:163]\n"
      "ds_read_b128 v[88:91], v22 offset:8192\n"
      "v_cvt_pk_bf16_f32 v227, v158, v159\n"
      "v_mfma_f32_16x16x32_bf16 v[160:163], v[32:35], v[128:131], v[160:163]\n"
      "v_mfma_f32_16x16x32_bf16 v[160:163], v[36:39], v[132:135], v[160:163]\n"
      "ds_read_b128 v[108:111], v22 offset:13312\n"
      "v_mfma_f32_16x16x32_bf16 v[160:163], v[40:43], v[136:139], v[160:163]\n"
      "ds_read_b128 v[76:79], v22 offset:5120\n"
      "v_mfma_f32_16x16x32_bf16 v[160:163], v[44:47], v[140:143], v[160:163]\n"
      "ds_read_b128 v[100:103], v22 offset:11264\n"
      "v_mfma_f32_16x16x32_bf16 v[160:163], v[48:51], v[144:147], v[160:163]\n"
      "s_waitcnt lgkmcnt(5)\n"
      "ds_read_b128 v[120:123], v22 offset:16384\n"
      "v_cvt_pk_bf16_f32 v226, v156, v157\n"
      "v_mfma_f32_16x16x32_bf16 v[160:163], v[52:55], v[148:151], v[160:163]\n"
      "s_sub_u32 s59, s59, 1\n"
      "s_cbranch_scc1 .Ld_out1\n"
      ".Ld_t2:\n"
      "v_mfma_f32_16x16x32_bf16 v[152:155], a[0:3], v[56:59], 0\n"
      "ds_read_b128 v[128:131], v22 offset:18432\n"
      "v_mfma_f32_16x16x32_bf16 v[152:155], a[4:7], v[60:63], v[152:155]\n"
      "buffer_store_dwordx4 v[224:227], v0, s[8:11], s37 offen sc1\n"
      "v_mfma_f32_16x16x32_bf16 v[152:155], a[8:11], v[64:67], v[152:155]\n"
      "ds_read_b128 v[104:107], v22 offset:12288\n"
      "v_mfma_f32_16x16x32_bf16 v[152:155], a[12:15], v[68:71], v[152:155]\n"
      "ds_read_b128 v[124:127], v22 offset:17408\n"
      "v_mfma_f32_16x16x32_bf16 v[152:155], a[16:19], v[72:75], v[152:155]\n"
      "s_waitcnt lgkmcnt(4)\n"
      "v_cvt_pk_bf16_f32 v229, v162, v163\n"
      "v_mfma_f32_16x16x32_bf16 v[152:155], a[20:23], v[76:79], v[152:155]\n"
      "ds_read_b128 v[116:119], v22 offset:15360\n"
      "v_cvt_pk_bf16_f32 v228, v160, v161\n"
      "v_mfma_f32_16x16x32_bf16 v[152:155], a[24:27], v[80:83], v[152:155]\n"
      "ds_read_b128 v[132:135], v22 offset:19456\n"
      "v_mfma_f32_16x16x32_bf16 v[152:155], a[28:31], v[84:87], v[152:155]\n"
      "ds_read_b128 v[140:143], v22 offset:21504\n"
      "v_mfma_f32_16x16x32_bf16 v[152:155], a[32:35], v[88:91], v[152:155]\n"
      "buffer_store_dwordx2 v[228:229], v13, s[8:11], s37 offen sc1\n"
      "v_mfma_f32_16x16x32_bf16 v[152:155], a[36:39], v[92:95], v[152:155]\n"
      "v_mfma_f32_16x16x32_bf16 v[152:155], a[40:43], v[96:99], v[152:155]\n"
      "v_mfma_f32_16x16x32_bf16 v[152:155], a[44:47], v[100:103], v[152:155]\n"
      "s_waitcnt lgkmcnt(3)\n"
      "ds_read_b128 v[136:139], v22 offset:20480\n"
      "s_add_u32 m0, s43, 98304\n"
      "v_mfma_f32_16x16x32_bf16 v[152:155], a[48:51], v[104:107], v[152:155]\n"
      "ds_read_b128 v[144:147], v22 offset:22528\n"
      "s_mov_b32 s51, s36\n"
      "v_mfma_f32_16x16x32_bf16 v[152:155], a[52:55], v[108:111], v[152:155]\n"
      "v_mfma_f32_16x16x32_bf16 v[152:155], a[56:59], v[112:115], v[152:155]\n"
      "s_waitcnt lgkmcnt(2)\n"
      "v_mfma_f32_16x16x32_bf16 v[152:155], a[60:63], v[116:119], v[152:155]\n"
      "ds_read_b128 v[148:151], v22 offset:23552\n"
      "v_mfma_f32_16x16x32_bf16 v[152:155], a[64:67], v[120:123], v[152:155]\n"
      "v_mfma_f32_16x16x32_bf16 v[152:155], a[68:71], v[124:127], v[152:155]\n"
      "v_mfma_f32_16x16x32_bf16 v[152:155], a[72:75], v[128:131], v[152:155]\n"
      "v_mfma_f32_16x16x32_bf16 v[152:155], a[76:79], v[132:135], v[152:155]\n"
      "s_waitcnt lgkmcnt(1)\n"
      "s_add_u32 s36, s36, 165888\n"
      "s_add_u32 s37, s37, 106496\n"
      "v_mfma_f32_16x16x32_bf16 v[152:155], a[80:83], v[136:139], v[152:155]\n"
      "v_mfma_f32_16x16x32_bf16 v[152:155], a[84:87], v[140:143], v[152:155]\n"
      "v_mfma_f32_16x16x32_bf16 v[152:155], a[88:91], v[144:147], v[152:155]\n"
      "s_waitcnt vmcnt(5) lgkmcnt(0)\n"
      "v_cmp_ne_u32 vcc, s21, v20\n"
      "v_mfma_f32_16x16x32_bf16 v[152:155], a[92:95], v[148:151], v[152:155]\n"
      "v_mfma_f32_16x16x32_bf16 v[156:159], a[96:99], v[56:59], 0\n"
      "v_mfma_f32_16x16x32_bf16 v[156:159], a[100:103], v[60:63], v[156:159]\n"
      "s_min_u32 s36, s36, s6\n"
      "v_mfma_f32_16x16x32_bf16 v[156:159], a[104:107], v[64:67], v[156:159]\n"
      "v_mfma_f32_16x16x32_bf16 v[156:159], a[108:111], v[68:71], v[156:159]\n"
      "s_cbranch_vccnz .Lsp_d4_8\n"
      ".Lbk_d4_8:\n"
      "v_mfma_f32_16x16x32_bf16 v[156:159], a[112:115], v[72:75], v[156:159]\n"
      "buffer_load_dwordx4 v0, s[8:11], s51 offen offset:1024 sc1 lds\n"
      "v_mfma_f32_16x16x32_bf16 v[156:159], a[116:119], v[76:79], v[156:159]\n"
      "v_add_u32_e64 v14, 1, v14\n"
      "v_mfma_f32_16x16x32_bf16 v[156:159], a[120:123], v[80:83], v[156:159]\n"
      "v_mfma_f32_16x16x32_bf16 v[156:159], a[124:127], v[84:87], v[156:159]\n"
      "v_mfma_f32_16x16x32_bf16 v[156:159], a[128:131], v[88:91], v[156:159]\n"
      "buffer_load_dwordx4 v0, s[8:11], s51 offen offset:2048 sc1 lds\n"
      "v_mfma_f32_16x16x32_bf16 v[156:159], a[132:135], v[92:95], v[156:159]\n"
      "s_waitcnt vmcnt(4)\n"
      "ds_write_b32 v16, v14\n"
      "v_mfma_f32_16x16x32_bf16 v[156:159], a[136:139], v[96:99], v[156:159]\n"
      "v_cvt_pk_bf16_f32 v224, v152, v153\n"
      "v_mfma_f32_16x16x32_bf16 v[156:159], a[140:143], v[100:103], v[156:159]\n"
      "v_mfma_f32_16x16x32_bf16 v[156:159], a[144:147], v[104:107], v[156:159]\n"
      "buffer_load_dwordx4 v0, s[8:11], s51 offen offset:3072 sc1 lds\n"
      "v_mfma_f32_16x16x32_bf16 v[156:159], a[148:151], v[108:111], v[156:159]\n"
      "v_mfma_f32_16x16x32_bf16 v[156:159], a[152:155], v[112:115], v[156:159]\n"
      "v_mfma_f32_16x16x32_bf16 v[156:159], a[156:159], v[116:119], v[156:159]\n"
      "v_mfma_f32_16x16x32_bf16 v[156:159], a[160:163], v[120:123], v[156:159]\n"
      "buffer_load_dwordx4 v0, s[8:11], s51 offen offset:0 sc1 lds\n"
      "v_mfma_f32_16x16x32_bf16 v[156:159], a[164:167], v[124:127], v[156:159]\n"
      "v_cvt_pk_bf16_f32 v225, v154, v155\n"
      "v_mfma_f32_16x16x32_bf16 v[156:159], a[168:171], v[128:131], v[156:159]\n"
      "ds_read_b32 v15, v17\n"
      "v_mfma_f32_16x16x32_bf16 v[156:159], a[172:175], v[132:135], v[156:159]\n"
      "v_mfma_f32_16x16x32_bf16 v[156:159], a[176:179], v[136:139], v[156:159]\n"
      "v_mfma_f32_16x16x32_bf16 v[156:159], a[180:183], v[140:143], v[156:159]\n"
      "s_add_u32 m0, s43, 102400\n"
      "v_mfma_f32_16x16x32_bf16 v[156:159], a[184:187], v[144:147], v[156:159]\n"
      "buffer_load_dword v20, v2, s[8:11], s36 offen sc1\n"
      "v_mfma_f32_16x16x32_bf16 v[156:159], a[188:191], v[148:151], v[156:159]\n"
      "s_waitcnt lgkmcnt(0)\n"
      "v_cmp_gt_i32 vcc, v14, v15\n"
      "v_mfma_f32_16x16x32_bf16 v[160:163], a[192:195], v[56:59], 0\n"
      "v_mfma_f32_16x16x32_bf16 v[160:163], a[196:199], v[60:63], v[160:163]\n"
      "buffer_load_dwordx4 v1, s[8:11], s51 offen offset:1024 sc1 lds\n"
      "v_mfma_f32_16x16x32_bf16 v[160:163], a[200:203], v[64:67], v[160:163]\n"
      "s_cbranch_vccnz .Lsp_dy3_9\n"
      ".Lbk_dy3_9:\n"
      "v_mfma_f32_16x16x32_bf16 v[160:163], a[204:207], v[68:71], v[160:163]\n"
      "v_cvt_pk_bf16_f32 v226, v156, v157\n"
      "v_mfma_f32_16x16x32_bf16 v[160:163], a[208:211], v[72:75], v[160:163]\n"
      "ds_read_b128 v[64:67], v22 offset:26624\n"
      "v_mfma_f32_16x16x32_bf16 v[160:163], a[212:215], v[76:79], v[160:163]\n"
      "buffer_load_dwordx4 v1, s[8:11], s51 offen offset:0 sc1 lds\n"
      "v_mfma_f32_16x16x32_bf16 v[160:163], a[216:219], v[80:83], v[160:163]\n"
      "ds_read_b128 v[56:59], v22 offset:24576\n"
      "v_mfma_f32_16x16x32_bf16 v[160:163], a[220:223], v[84:87], v[160:163]\n"
      "v_mfma_f32_16x16x32_bf16 v[160:163], a[224:227], v[88:91], v[160:163]\n"
      "ds_read_b128 v[60:63], v22 offset:25600\n"
      "v_mfma_f32_16x16x32_bf16 v[160:163], a[228:231], v[92:95], v[160:163]\n"
      "s_waitcnt vmcnt(7)\n"
      "buffer_store_dword v18, v4, s[8:11], s52 offen sc1\n"
      "v_mfma_f32_16x16x32_bf16 v[160:163], a[232:235], v[96:99], v[160:163]\n"
      "ds_read_b128 v[80:83], v22 offset:30720\n"
      "v_mfma_f32_16x16x32_bf16 v[160:163], a[236:239], v[100:103], v[160:163]\n"
      "s_mov_b32 s51, s36\n"
      "ds_read_b128 v[72:75], v22 offset:28672\n"
      "v_mfma_f32_16x16x32_bf16 v[160:163], a[240:243], v[104:107], v[160:163]\n"
      "ds_read_b128 v[92:95], v22 offset:33792\n"
      "v_mfma_f32_16x16x32_bf16 v[160:163], a[244:247], v[108:111], v[160:163]\n"
      "ds_read_b128 v[88:91], v22 offset:32768\n"
      "v_mfma_f32_16x16x32_bf16 v[160:163], a[248:251], v[112:115], v[160:163]\n"
      "ds_read_b128 v[84:87], v22 offset:31744\n"
      "v_mfma_f32_16x16x32_bf16 v[160:163], a[252:255], v[116:119], v[160:163]\n"
      "ds_read_b128 v[108:111], v22 offset:37888\n"
      "v_mfma_f32_16x16x32_bf16 v[160:163], v[24:27], v[120:123], v[160:163]\n"
      "ds_read_b128 v[96:99], v22 offset:34816\n"
      "v_mfma_f32_16x16x32_bf16 v[160:163], v[28:31], v[124:127], v[160:163]\n"
      "ds_read_b128 v[76:79], v22 offset:29696\n"
      "v_cvt_pk_bf16_f32 v227, v158, v159\n"
      "v_mfma_f32_16x16x32_bf16 v[160:163], v[32:35], v[128:131], v[160:163]\n"
      "ds_read_b128 v[68:71], v22 offset:27648\n"
      "v_mfma_f32_16x16x32_bf16 v[160:163], v[36:39], v[132:135], v[160:163]\n"
      "ds_read_b128 v[124:127], v22 offset:41984\n"
      "v_mfma_f32_16x16x32_bf16 v[160:163], v[40:43], v[136:139], v[160:163]\n"
      "buffer_load_dword v21, v3, s[8:11], s53 offen sc1\n"
      "v_mfma_f32_16x16x32_bf16 v[160:163], v[44:47], v[140:143], v[160:163]\n"
      "v_mfma_f32_16x16x32_bf16 v[160:163], v[48:51], v[144:147], v[160:163]\n"
      "s_waitcnt lgkmcnt(4)\n"
      "ds_read_b128 v[120:123], v22 offset:40960\n"
      "v_mfma_f32_16x16x32_bf16 v[160:163], v[52:55], v[148:151], v[160:163]\n"
      "s_sub_u32 s59, s59, 1\n"
      "s_cbranch_scc1 .Ld_out2\n"
      ".Ld_t3:\n"
      "v_mfma_f32_16x16x32_bf16 v[152:155], a[0:3], v[56:59], 0\n"
      "ds_read_b128 v[144:147], v22 offset:47104\n"
      "v_mfma_f32_16x16x32_bf16 v[152:155], a[4:7], v[60:63], v[152:155]\n"
      "ds_read_b128 v[140:143], v22 offset:46080\n"
      "v_mfma_f32_16x16x32_bf16 v[152:155], a[8:11], v[64:67], v[152:155]\n"
      "s_waitcnt lgkmcnt(3)\n"
      "buffer_store_dwordx4 v[224:227], v0, s[8:11], s37 offen sc1\n"
      "v_mfma_f32_16x16x32_bf16 v[152:155], a[12:15], v[68:71], v[152:155]\n"
      "ds_read_b128 v[100:103], v22 offset:35840\n"
      "v_mfma_f32_16x16x32_bf16 v[152:155], a[16:19], v[72:75], v[152:155]\n"
      "ds_read_b128 v[104:107], v22 offset:36864\n"
      "v_cvt_pk_bf16_f32 v228, v160, v161\n"
      "v_mfma_f32_16x16x32_bf16 v[152:155], a[20:23], v[76:79], v[152:155]\n"
      "ds_read_b128 v[112:115], v22 offset:38912\n"
      "v_mfma_f32_16x16x32_bf16 v[152:155], a[24:27], v[80:83], v[152:155]\n"
      "ds_read_b128 v[116:119], v22 offset:39936\n"
      "v_mfma_f32_16x16x32_bf16 v[152:155], a[28:31], v[84:87], v[152:155]\n"
      "ds_read_b128 v[128:131], v22 offset:43008\n"
      "v_mfma_f32_16x16x32_bf16 v[152:155], a[32:35], v[88:91], v[152:155]\n"
      "v_mfma_f32_16x16x32_bf16 v[152:155], a[36:39], v[92:95], v[152:155]\n"
      "s_add_u32 s36, s36, 165888\n"
      "ds_read_b128 v[136:139], v22 offset:45056\n"
      "v_cvt_pk_bf16_f32 v229, v162, v163\n"
      "v_mfma_f32_16x16x32_bf16 v[152:155], a[40:43], v[96:99], v[152:155]\n"
      "s_waitcnt lgkmcnt(5)\n"
      "ds_read_b128 v[132:135], v22 offset:44032\n"
      "v_mfma_f32_16x16x32_bf16 v[152:155], a[44:47], v[100:103], v[152:155]\n"
      "s_waitcnt lgkmcnt(5)\n"
      "buffer_store_dwordx2 v[228:229], v13, s[8:11], s37 offen sc1\n"
      "v_mfma_f32_16x16x32_bf16 v[152:155], a[48:51], v[104:107], v[152:155]\n"
      "s_add_u32 m0, s43, 122880\n"
      "ds_read_b128 v[148:151], v22 offset:48128\n"
      "v_mfma_f32_16x16x32_bf16 v[152:155], a[52:55], v[108:111], v[152:155]\n"
      "s_waitcnt lgkmcnt(4)\n"
      "s_min_u32 s36, s36, s6\n"
      "v_mfma_f32_16x16x32_bf16 v[152:155], a[56:59], v[112:115], v[152:155]\n"
      "v_mfma_f32_16x16x32_bf16 v[152:155], a[60:63], v[116:119], v[152:155]\n"
      "v_mfma_f32_16x16x32_bf16 v[152:155], a[64:67], v[120:123], v[152:155]\n"
      "v_mfma_f32_16x16x32_bf16 v[152:155], a[68:71], v[124:127], v[152:155]\n"
      "s_waitcnt lgkmcnt(1)\n"
      "s_add_u32 s52, s52, 106496\n"
      "v_mfma_f32_16x16x32_bf16 v[152:155], a[72:75], v[128:131], v[152:155]\n"
      "s_waitcnt vmcnt(6)\n"
      "v_cmp_ne_u32 vcc, s21, v20\n"
      "v_mfma_f32_16x16x32_bf16 v[152:155], a[76:79], v[132:135], v[152:155]\n"
      "v_mfma_f32_16x16x32_bf16 v[152:155], a[80:83], v[136:139], v[152:155]\n"
      "v_mfma_f32_16x16x32_bf16 v[152:155], a[84:87], v[140:143], v[152:155]\n"
      "s_cbranch_vccnz .Lsp_d5_10\n"
      ".Lbk_d5_10:\n"
      "v_mfma_f32_16x16x32_bf16 v[152:155], a[88:91], v[144:147], v[152:155]\n"
      "s_waitcnt lgkmcnt(0)\n"
      "buffer_load_dwordx4 v0, s[8:11], s51 offen offset:2048 sc1 lds\n"
      "v_mfma_f32_16x16x32_bf16 v[152:155], a[92:95], v[148:151], v[152:155]\n"
      "v_mfma_f32_16x16x32_bf16 v[156:159], a[96:99], v[56:59], 0\n"
      "v_mfma_f32_16x16x32_bf16 v[156:159], a[100:103], v[60:63], v[156:159]\n"
      "v_mfma_f32_16x16x32_bf16 v[156:159], a[104:107], v[64:67], v[156:159]\n"
      "buffer_load_dwordx4 v0, s[8:11], s51 offen offset:3072 sc1 lds\n"
      "v_mfma_f32_16x16x32_bf16 v[156:159], a[108:111], v[68:71], v[156:159]\n"
      "v_add_u32 v14, 1, v14\n"
      "v_mfma_f32_16x16x32_bf16 v[156:159], a[112:115], v[72:75], v[156:159]\n"
      "s_waitcnt vmcnt(6)\n"
      "ds_write_b32 v16, v14\n"
      "v_mfma_f32_16x16x32_bf16 v[156:159], a[116:119], v[76:79], v[156:159]\n"
      "v_mfma_f32_16x16x32_bf16 v[156:159], a[120:123], v[80:83], v[156:159]\n"
      "buffer_load_dwordx4 v0, s[8:11], s51 offen offset:0 sc1 lds\n"
      "v_mfma_f32_16x16x32_bf16 v[156:159], a[124:127], v[84:87], v[156:159]\n"
      "v_mfma_f32_16x16x32_bf16 v[156:159], a[128:131], v[88:91], v[156:159]\n"
      "v_mfma_f32_16x16x32_bf16 v[156:159], a[132:135], v[92:95], v[156:159]\n"
      "v_mfma_f32_16x16x32_bf16 v[156:159], a[136:139], v[96:99], v[156:159]\n"
      "buffer_load_dwordx4 v0, s[8:11], s51 offen offset:1024 sc1 lds\n"
      "v_mfma_f32_16x16x32_bf16 v[156:159], a[140:143], v[100:103], v[156:159]\n"
      "s_add_u32 s55, s53, 6656\n"
      "v_cvt_pk_bf16_f32 v224, v152, v153\n"
      "v_mfma_f32_16x16x32_bf16 v[156:159], a[144:147], v[104:107], v[156:159]\n"
      "s_add_u32 m0, s43, 126976\n"
      "v_mfma_f32_16x16x32_bf16 v[156:159], a[148:151], v[108:111], v[156:159]\n"
      "ds_read_b32 v15, v17\n"
      "v_mfma_f32_16x16x32_bf16 v[156:159], a[152:155], v[112:115], v[156:159]\n"
      "buffer_load_dwordx4 v1, s[8:11], s51 offen offset:0 sc1 lds\n"
      "v_mfma_f32_16x16x32_bf16 v[156:159], a[156:159], v[116:119], v[156:159]\n"
      "v_mfma_f32_16x16x32_bf16 v[156:159], a[160:163], v[120:123], v[156:159]\n"
      "v_mfma_f32_16x16x32_bf16 v[156:159], a[164:167], v[124:127], v[156:159]\n"
      "v_mfma_f32_16x16x32_bf16 v[156:159], a[168:171], v[128:131], v[156:159]\n"
      "s_waitcnt lgkmcnt(0)\n"
      "v_cmp_gt_i32 vcc, v14, v15\n"
      "v_mfma_f32_16x16x32_bf16 v[156:159], a[172:175], v[132:135], v[156:159]\n"
      "buffer_load_dwordx4 v1, s[8:11], s51 offen offset:1024 sc1 lds\n"
      "v_mfma_f32_16x16x32_bf16 v[156:159], a[176:179], v[136:139], v[156:159]\n"
      "v_mfma_f32_16x16x32_bf16 v[156:159], a[180:183], v[140:143], v[156:159]\n"
      "v_cvt_pk_bf16_f32 v225, v154, v155\n"
      "v_mfma_f32_16x16x32_bf16 v[156:159], a[184:187], v[144:147], v[156:159]\n"
      "s_waitcnt vmcnt(8)\n"
      "s_cbranch_vccnz .Lsp_dy4_11\n"
      ".Lbk_dy4_11:\n"
      "v_cmp_ne_u32 vcc, s21, v21\n"
      "v_mfma_f32_16x16x32_bf16 v[156:159], a[188:191], v[148:151], v[156:159]\n"
      "v_mfma_f32_16x16x32_bf16 v[160:163], a[192:195], v[56:59], 0\n"
      "buffer_load_dword v20, v2, s[8:11], s36 offen sc1\n"
      "v_mfma_f32_16x16x32_bf16 v[160:163], a[196:199], v[60:63], v[160:163]\n"
      "s_cbranch_vccnz .Lsp_df_12\n"
      ".Lbk_df_12:\n"
      "v_mfma_f32_16x16x32_bf16 v[160:163], a[200:203], v[64:67], v[160:163]\n"
      "buffer_load_dwordx4 v[176:179], v0, s[8:11], s53 offen sc1\n"
      "v_mfma_f32_16x16x32_bf16 v[160:163], a[204:207], v[68:71], v[160:163]\n"
      "s_cmp_lt_u32 s41, 32\n"
      "s_cselect_b32 s56, s39, 0x40000000\n"
      "ds_read_b128 v[56:59], v23 offset:0\n"
      "v_mfma_f32_16x16x32_bf16 v[160:163], a[208:211], v[72:75], v[160:163]\n"
      "ds_read_b128 v[72:75], v23 offset:4096\n"
      "v_mfma_f32_16x16x32_bf16 v[160:163], a[212:215], v[76:79], v[160:163]\n"
      "ds_read_b128 v[68:71], v23 offset:3072\n"
      "v_mfma_f32_16x16x32_bf16 v[160:163], a[216:219], v[80:83], v[160:163]\n"
      "buffer_load_dwordx2 v[180:181], v13, s[8:11], s53 offen sc1\n"
      "v_mfma_f32_16x16x32_bf16 v[160:163], a[220:223], v[84:87], v[160:163]\n"
      "ds_read_b128 v[76:79], v23 offset:5120\n"
      "v_mfma_f32_16x16x32_bf16 v[160:163], a[224:227], v[88:91], v[160:163]\n"
      "ds_read_b128 v[80:83], v23 offset:6144\n"
      "v_mfma_f32_16x16x32_bf16 v[160:163], a[228:231], v[92:95], v[160:163]\n"
      "ds_read_b128 v[64:67], v23 offset:2048\n"
      "v_mfma_f32_16x16x32_bf16 v[160:163], a[232:235], v[96:99], v[160:163]\n"
      "ds_read_b128 v[88:91], v23 offset:8192\n"
      "v_mfma_f32_16x16x32_bf16 v[160:163], a[236:239], v[100:103], v[160:163]\n"
      "s_waitcnt vmcnt(9) lgkmcnt(5)\n"
      "buffer_store_dword v18, v4, s[8:11], s52 offen sc1\n"
      "v_mfma_f32_16x16x32_bf16 v[160:163], a[240:243], v[104:107], v[160:163]\n"
      "s_add_u32 s58, s41, 4294967264\n"
      "ds_read_b128 v[60:63], v23 offset:1024\n"
      "v_mfma_f32_16x16x32_bf16 v[160:163], a[244:247], v[108:111], v[160:163]\n"
      "buffer_load_dwordx2 v[188:189], v13, s[8:11], s55 offen sc1\n"
      "v_mfma_f32_16x16x32_bf16 v[160:163], a[248:251], v[112:115], v[160:163]\n"
      "s_add_u32 s37, s37, 106496\n"
      "ds_read_b128 v[100:103], v23 offset:11264\n"
      "v_mfma_f32_16x16x32_bf16 v[160:163], a[252:255], v[116:119], v[160:163]\n"
      "ds_read_b128 v[96:99], v23 offset:10240\n"
      "v_mfma_f32_16x16x32_bf16 v[160:163], v[24:27], v[120:123], v[160:163]\n"
      "ds_read_b128 v[116:119], v23 offset:15360\n"
      "v_mfma_f32_16x16x32_bf16 v[160:163], v[28:31], v[124:127], v[160:163]\n"
      "buffer_load_dwordx4 v[184:187], v0, s[8:11], s55 offen sc1\n"
      "v_mfma_f32_16x16x32_bf16 v[160:163], v[32:35], v[128:131], v[160:163]\n"
      "ds_read_b128 v[112:115], v23 offset:14336\n"
      "s_add_u32 s54, s53, 13312\n"
      "v_mfma_f32_16x16x32_bf16 v[160:163], v[36:39], v[132:135], v[160:163]\n"
      "ds_read_b128 v[104:107], v23 offset:12288\n"
      "v_cvt_pk_bf16_f32 v226, v156, v157\n"
      "v_mfma_f32_16x16x32_bf16 v[160:163], v[40:43], v[136:139], v[160:163]\n"
      "ds_read_b128 v[84:87], v23 offset:7168\n"
      "v_mfma_f32_16x16x32_bf16 v[160:163], v[44:47], v[140:143], v[160:163]\n"
      "ds_read_b128 v[120:123], v23 offset:16384\n"
      "v_mfma_f32_16x16x32_bf16 v[160:163], v[48:51], v[144:147], v[160:163]\n"
      "buffer_load_dwordx4 v[192:195], v0, s[8:11], s54 offen sc1\n"
      "v_mfma_f32_16x16x32_bf16 v[160:163], v[52:55], v[148:151], v[160:163]\n"
      "s_sub_u32 s59, s59, 1\n"
      "s_cbranch_scc1 .Ld_out3\n"
      ".Ld_t4:\n"
      "v_mfma_f32_16x16x32_bf16 v[152:155], a[0:3], v[56:59], 0\n"
      "s_waitcnt lgkmcnt(4)\n"
      "ds_read_b128 v[140:143], v23 offset:21504\n"
      "v_mfma_f32_16x16x32_bf16 v[152:155], a[4:7], v[60:63], v[152:155]\n"
      "s_add_u32 s55, s53, 19968\n"
      "ds_read_b128 v[92:95], v23 offset:9216\n"
      "v_mfma_f32_16x16x32_bf16 v[152:155], a[8:11], v[64:67], v[152:155]\n"
      "buffer_load_dwordx2 v[204:205], v13, s[8:11], s55 offen sc1\n"
      "v_mfma_f32_16x16x32_bf16 v[152:155], a[12:15], v[68:71], v[152:155]\n"
      "ds_read_b128 v[124:127], v23 offset:17408\n"
      "v_cvt_pk_bf16_f32 v227, v158, v159\n"
      "v_mfma_f32_16x16x32_bf16 v[152:155], a[16:19], v[72:75], v[152:155]\n"
      "ds_read_b128 v[108:111], v23 offset:13312\n"
      "s_mov_b32 s51, s36\n"
      "v_mfma_f32_16x16x32_bf16 v[152:155], a[20:23], v[76:79], v[152:155]\n"
      "ds_read_b128 v[128:131], v23 offset:18432\n"
      "v_cvt_pk_bf16_f32 v229, v162, v163\n"
      "v_mfma_f32_16x16x32_bf16 v[152:155], a[24:27], v[80:83], v[152:155]\n"
      "s_waitcnt lgkmcnt(5)\n"
      "buffer_store_dwordx4 v[224:227], v0, s[8:11], s37 offen sc1\n"
      "v_mfma_f32_16x16x32_bf16 v[152:155], a[28:31], v[84:87], v[152:155]\n"
      "v_cvt_pk_bf16_f32 v228, v160, v161\n"
      "v_mfma_f32_16x16x32_bf16 v[152:155], a[32:35], v[88:91], v[152:155]\n"
      "s_waitcnt lgkmcnt(3)\n"
      "v_mfma_f32_16x16x32_bf16 v[152:155], a[36:39], v[92:95], v[152:155]\n"
      "ds_read_b128 v[136:139], v23 offset:20480\n"
      "v_mfma_f32_16x16x32_bf16 v[152:155], a[40:43], v[96:99], v[152:155]\n"
      "ds_read_b128 v[132:135], v23 offset:19456\n"
      "s_add_u32 m0, s43, 0\n"
      "v_mfma_f32_16x16x32_bf16 v[152:155], a[44:47], v[100:103], v[152:155]\n"
      "v_mfma_f32_16x16x32_bf16 v[152:155], a[48:51], v[104:107], v[152:155]\n"
      "s_waitcnt lgkmcnt(2)\n"
      "buffer_store_dwordx2 v[228:229], v13, s[8:11], s37 offen sc1\n"
      "v_mfma_f32_16x16x32_bf16 v[152:155], a[52:55], v[108:111], v[152:155]\n"
      "s_add_u32 s36, s36, 165888\n"
      "ds_read_b128 v[144:147], v23 offset:22528\n"
      "v_mfma_f32_16x16x32_bf16 v[152:155], a[56:59], v[112:115], v[152:155]\n"
      "v_mfma_f32_16x16x32_bf16 v[152:155], a[60:63], v[116:119], v[152:155]\n"
      "ds_read_b128 v[148:151], v23 offset:23552\n"
      "v_add_u32 v14, 1, v14\n"
      "v_mfma_f32_16x16x32_bf16 v[152:155], a[64:67], v[120:123], v[152:155]\n"
      "s_cmp_lt_u32 s58, 32\n"
      "s_cselect_b32 s57, s40, 0x40000000\n"
      "v_mfma_f32_16x16x32_bf16 v[152:155], a[68:71], v[124:127], v[152:155]\n"
      "buffer_load_dwordx2 v[196:197], v13, s[8:11], s54 offen sc1\n"
      "v_mfma_f32_16x16x32_bf16 v[152:155], a[72:75], v[128:131], v[152:155]\n"
      "s_waitcnt lgkmcnt(2)\n"
      "s_add_u32 s54, s53, 26624\n"
      "v_mfma_f32_16x16x32_bf16 v[152:155], a[76:79], v[132:135], v[152:155]\n"
      "s_add_u32 s53, s53, 599040\n"
      "s_add_u32 s52, s52, 106496\n"
      "v_mfma_f32_16x16x32_bf16 v[152:155], a[80:83], v[136:139], v[152:155]\n"
      "v_mfma_f32_16x16x32_bf16 v[152:155], a[84:87], v[140:143], v[152:155]\n"
      "s_waitcnt lgkmcnt(1)\n"
      "buffer_load_dwordx4 v[200:203], v0, s[8:11], s55 offen sc1\n"
      "v_mfma_f32_16x16x32_bf16 v[152:155], a[88:91], v[144:147], v[152:155]\n"
      "s_waitcnt vmcnt(10) lgkmcnt(0)\n"
      "v_lshlrev_b32 v234, 16, v178\n"
      "v_cmp_ne_u32 vcc, s21, v20\n"
      "v_mfma_f32_16x16x32_bf16 v[152:155], a[92:95], v[148:151], v[152:155]\n"
      "v_lshlrev_b32 v232, 16, v176\n"
      "v_mfma_f32_16x16x32_bf16 v[156:159], a[96:99], v[56:59], 0\n"
      "v_and_b32 v176, 0xffff0000, v176\n"
      "v_mfma_f32_16x16x32_bf16 v[156:159], a[100:103], v[60:63], v[156:159]\n"
      "buffer_load_dwordx4 v[208:211], v0, s[8:11], s54 offen sc1\n"
      "v_mfma_f32_16x16x32_bf16 v[156:159], a[104:107], v[64:67], v[156:159]\n"
      "ds_write_b32 v16, v14\n"
      "v_mfma_f32_16x16x32_bf16 v[156:159], a[108:111], v[68:71], v[156:159]\n"
      "s_waitcnt vmcnt(10)\n"
      "v_add_f32 v164, v164, v232\n"
      "v_lshlrev_b32 v232, 16, v180\n"
      "v_mfma_f32_16x16x32_bf16 v[156:159], a[112:115], v[72:75], v[156:159]\n"
      "s_cbranch_vccnz .Lsp_d0_13\n"
      ".Lbk_d0_13:\n"
      "v_lshlrev_b32 v233, 16, v177\n"
      "v_mfma_f32_16x16x32_bf16 v[156:159], a[116:119], v[76:79], v[156:159]\n"
      "v_cvt_pk_bf16_f32 v225, v154, v155\n"
      "v_add_f32 v172, v172, v232\n"
      "v_mfma_f32_16x16x32_bf16 v[156:159], a[120:123], v[80:83], v[156:159]\n"
      "buffer_load_dwordx4 v0, s[8:11], s51 offen offset:1024 sc1 lds\n"
      "v_mfma_f32_16x16x32_bf16 v[156:159], a[124:127], v[84:87], v[156:159]\n"
      "v_add_f32 v166, v166, v233\n"
      "v_and_b32 v178, 0xffff0000, v178\n"
      "v_mfma_f32_16x16x32_bf16 v[156:159], a[128:131], v[88:91], v[156:159]\n"
      "v_add_f32 v168, v168, v234\n"
      "v_mfma_f32_16x16x32_bf16 v[156:159], a[132:135], v[92:95], v[156:159]\n"
      "v_lshlrev_b32 v233, 16, v181\n"
      "v_add_f32 v174, v174, v233\n"
      "v_mfma_f32_16x16x32_bf16 v[156:159], a[136:139], v[96:99], v[156:159]\n"
      "buffer_load_dwordx2 v[212:213], v13, s[8:11], s54 offen sc1\n"
      "v_mfma_f32_16x16x32_bf16 v[156:159], a[140:143], v[100:103], v[156:159]\n"
      "v_add_f32 v169, v169, v178\n"
      "v_mfma_f32_16x16x32_bf16 v[156:159], a[144:147], v[104:107], v[156:159]\n"
      "ds_read_b32 v15, v17\n"
      "v_lshlrev_b32 v235, 16, v179\n"
      "v_mfma_f32_16x16x32_bf16 v[156:159], a[148:151], v[108:111], v[156:159]\n"
      "s_waitcnt vmcnt(9) lgkmcnt(1)\n"
      "v_lshlrev_b32 v232, 16, v184\n"
      "v_lshlrev_b32 v233, 16, v185\n"
      "v_mfma_f32_16x16x32_bf16 v[156:159], a[152:155], v[112:115], v[156:159]\n"
      "v_add_f32 v164, v164, v232\n"
      "v_add_f32 v170, v170, v235\n"
      "v_mfma_f32_16x16x32_bf16 v[156:159], a[156:159], v[116:119], v[156:159]\n"
      "buffer_load_dwordx4 v0, s[8:11], s51 offen offset:0 sc1 lds\n"
      "v_mfma_f32_16x16x32_bf16 v[156:159], a[160:163], v[120:123], v[156:159]\n"
      "v_lshlrev_b32 v234, 16, v186\n"
      "v_lshlrev_b32 v232, 16, v188\n"
      "v_mfma_f32_16x16x32_bf16 v[156:159], a[164:167], v[124:127], v[156:159]\n"
      "v_add_f32 v172, v172, v232\n"
      "v_add_f32 v168, v168, v234\n"
      "v_mfma_f32_16x16x32_bf16 v[156:159], a[168:171], v[128:131], v[156:159]\n"
      "s_waitcnt lgkmcnt(0)\n"
      "v_add_f32 v166, v166, v233\n"
      "v_cmp_gt_i32 vcc, v14, v15\n"
      "v_mfma_f32_16x16x32_bf16 v[156:159], a[172:175], v[132:135], v[156:159]\n"
      "buffer_load_dwordx4 v0, s[8:11], s51 offen offset:3072 sc1 lds\n"
      "v_mfma_f32_16x16x32_bf16 v[156:159], a[176:179], v[136:139], v[156:159]\n"
      "s_waitcnt vmcnt(10)\n"
      "v_lshlrev_b32 v232, 16, v192\n"
      "v_mfma_f32_16x16x32_bf16 v[156:159], a[180:183], v[140:143], v[156:159]\n"
      "v_lshlrev_b32_e64 v233, 16, v189\n"
      "v_lshlrev_b32_e64 v234, 16, v194\n"
      "v_mfma_f32_16x16x32_bf16 v[156:159], a[184:187], v[144:147], v[156:159]\n"
      "s_add_u32 s39, s39, 147456\n"
      "v_and_b32 v186, 0xffff0000, v186\n"
      "v_cvt_pk_bf16_f32 v224, v152, v153\n"
      "v_mfma_f32_16x16x32_bf16 v[156:159], a[188:191], v[148:151], v[156:159]\n"
      "buffer_load_dwordx4 v0, s[8:11], s51 offen offset:2048 sc1 lds\n"
      "v_mfma_f32_16x16x32_bf16 v[160:163], a[192:195], v[56:59], 0\n"
      "s_min_u32 s36, s36, s6\n"
      "v_add_f32_e64 v174, v174, v233\n"
      "v_and_b32 v177, 0xffff0000, v177\n"
      "v_mfma_f32_16x16x32_bf16 v[160:163], a[196:199], v[60:63], v[160:163]\n"
      "s_cbranch_vccnz .Lsp_dy5_14\n"
      ".Lbk_dy5_14:\n"
      "v_and_b32 v192, 0xffff0000, v192\n"
      "v_mfma_f32_16x16x32_bf16 v[160:163], a[200:203], v[64:67], v[160:163]\n"
      "ds_read_b128 v[60:63], v23 offset:25600\n"
      "v_cvt_pk_bf16_f32 v226, v156, v157\n"
      "v_mfma_f32_16x16x32_bf16 v[160:163], a[204:207], v[68:71], v[160:163]\n"
      "s_add_u32 m0, s43, 4096\n"
      "ds_read_b128 v[64:67], v23 offset:26624\n"
      "v_add_f32 v168, v168, v234\n"
      "v_mfma_f32_16x16x32_bf16 v[160:163], a[208:211], v[72:75], v[160:163]\n"
      "buffer_load_dwordx4 v1, s[8:11], s51 offen offset:0 sc1 lds\n"
      "v_mfma_f32_16x16x32_bf16 v[160:163], a[212:215], v[76:79], v[160:163]\n"
      "ds_read_b128 v[76:79], v23 offset:29696\n"
      "v_lshlrev_b32 v233, 16, v193\n"
      "v_mfma_f32_16x16x32_bf16 v[160:163], a[216:219], v[80:83], v[160:163]\n"
      "s_add_u32 s37, s37, 106496\n"
      "v_and_b32 v181, 0xffff0000, v181\n"
      "v_mfma_f32_16x16x32_bf16 v[160:163], a[220:223], v[84:87], v[160:163]\n"
      "ds_read_b128 v[72:75], v23 offset:28672\n"
      "v_lshlrev_b32 v235, 16, v187\n"
      "v_mfma_f32_16x16x32_bf16 v[160:163], a[224:227], v[88:91], v[160:163]\n"
      "buffer_load_dwordx4 v1, s[8:11], s51 offen offset:1024 sc1 lds\n"
      "v_mfma_f32_16x16x32_bf16 v[160:163], a[228:231], v[92:95], v[160:163]\n"
      "ds_read_b128 v[92:95], v23 offset:33792\n"
      "v_add_f32 v166, v166, v233\n"
      "v_mfma_f32_16x16x32_bf16 v[160:163], a[232:235], v[96:99], v[160:163]\n"
      "ds_read_b128 v[80:83], v23 offset:30720\n"
      "v_add_f32 v170, v170, v235\n"
      "v_mfma_f32_16x16x32_bf16 v[160:163], a[236:239], v[100:103], v[160:163]\n"
      "ds_read_b128 v[84:87], v23 offset:31744\n"
      "v_and_b32 v184, 0xffff0000, v184\n"
      "v_mfma_f32_16x16x32_bf16 v[160:163], a[240:243], v[104:107], v[160:163]\n"
      "buffer_load_dword v20, v2, s[8:11], s36 offen sc1\n"
      "v_mfma_f32_16x16x32_bf16 v[160:163], a[244:247], v[108:111], v[160:163]\n"
      "s_waitcnt vmcnt(10) lgkmcnt(4)\n"
      "ds_read_b128 v[88:91], v23 offset:32768\n"
      "v_lshlrev_b32 v233, 16, v197\n"
      "v_mfma_f32_16x16x32_bf16 v[160:163], a[248:251], v[112:115], v[160:163]\n"
      "ds_read_b128 v[56:59], v23 offset:24576\n"
      "v_add_f32 v174, v174, v233\n"
      "v_mfma_f32_16x16x32_bf16 v[160:163], a[252:255], v[116:119], v[160:163]\n"
      "buffer_store_dword v18, v4, s[8:11], s52 offen sc1\n"
      "v_mfma_f32_16x16x32_bf16 v[160:163], v[24:27], v[120:123], v[160:163]\n"
      "ds_read_b128 v[68:71], v23 offset:27648\n"
      "v_add_f32 v165, v165, v176\n"
      "v_mfma_f32_16x16x32_bf16 v[160:163], v[28:31], v[124:127], v[160:163]\n"
      "ds_read_b128 v[108:111], v23 offset:37888\n"
      "v_add_f32 v164, v164, v232\n"
      "v_mfma_f32_16x16x32_bf16 v[160:163], v[32:35], v[128:131], v[160:163]\n"
      "ds_read_b128 v[112:115], v23 offset:38912\n"
      "v_and_b32 v179, 0xffff0000, v179\n"
      "v_mfma_f32_16x16x32_bf16 v[160:163], v[36:39], v[132:135], v[160:163]\n"
      "s_waitcnt vmcnt(10) lgkmcnt(5)\n"
      "ds_read_b128 v[116:119], v23 offset:39936\n"
      "v_lshlrev_b32 v234, 16, v202\n"
      "v_mfma_f32_16x16x32_bf16 v[160:163], v[40:43], v[136:139], v[160:163]\n"
      "ds_read_b128 v[128:131], v23 offset:43008\n"
      "v_add_f32 v167, v167, v177\n"
      "v_mfma_f32_16x16x32_bf16 v[160:163], v[44:47], v[140:143], v[160:163]\n"
      "ds_read_b128 v[124:127], v23 offset:41984\n"
      "v_cvt_pk_bf16_f32 v227, v158, v159\n"
      "v_mfma_f32_16x16x32_bf16 v[160:163], v[48:51], v[144:147], v[160:163]\n"
      "s_waitcnt lgkmcnt(6)\n"
      "ds_read_b128 v[96:99], v23 offset:34816\n"
      "v_lshlrev_b32 v235, 16, v195\n"
      "v_mfma_f32_16x16x32_bf16 v[160:163], v[52:55], v[148:151], v[160:163]\n"
      "s_sub_u32 s59, s59, 1\n"
      "s_cbranch_scc1 .Ld_out4\n"
      ".Ld_t5:\n"
      "v_mfma_f32_16x16x32_bf16 v[152:155], a[0:3], v[56:59], 0\n"
      "ds_read_b128 v[136:139], v23 offset:45056\n"
      "v_lshlrev_b32 v233, 16, v201\n"
      "v_mfma_f32_16x16x32_bf16 v[152:155], a[4:7], v[60:63], v[152:155]\n"
      "ds_read_b128 v[100:103], v23 offset:35840\n"
      "v_add_f32 v170, v170, v235\n"
      "v_mfma_f32_16x16x32_bf16 v[152:155], a[8:11], v[64:67], v[152:155]\n"
      "s_waitcnt lgkmcnt(5)\n"
      "buffer_load_dwordx2 v[220:221], v7, s[8:11], s38 offen sc1\n"
      "v_mfma_f32_16x16x32_bf16 v[152:155], a[12:15], v[68:71], v[152:155]\n"
      "ds_read_b128 v[104:107], v23 offset:36864\n"
      "v_add_f32 v166, v166, v233\n"
      "v_mfma_f32_16x16x32_bf16 v[152:155], a[16:19], v[72:75], v[152:155]\n"
      "ds_read_b128 v[120:123], v23 offset:40960\n"
      "v_and_b32 v180, 0xffff0000, v180\n"
      "v_mfma_f32_16x16x32_bf16 v[152:155], a[20:23], v[76:79], v[152:155]\n"
      "s_add_u32 s40, s40, 4294856704\n"
      "v_lshlrev_b32 v235, 16, v203\n"
      "v_add_f32 v169, v169, v186\n"
      "v_mfma_f32_16x16x32_bf16 v[152:155], a[24:27], v[80:83], v[152:155]\n"
      "buffer_store_dwordx4 v[224:227], v0, s[8:11], s37 offen sc1\n"
      "v_mfma_f32_16x16x32_bf16 v[152:155], a[28:31], v[84:87], v[152:155]\n"
      "ds_read_b128 v[144:147], v23 offset:47104\n"
      "v_cvt_pk_bf16_f32 v229, v162, v163\n"
      "v_mfma_f32_16x16x32_bf16 v[152:155], a[32:35], v[88:91], v[152:155]\n"
      "ds_read_b128 v[132:135], v23 offset:44032\n"
      "v_cvt_pk_bf16_f32 v228, v160, v161\n"
      "v_mfma_f32_16x16x32_bf16 v[152:155], a[36:39], v[92:95], v[152:155]\n"
      "s_waitcnt lgkmcnt(4)\n"
      "v_add_f32 v168, v168, v234\n"
      "v_lshlrev_b32 v232, 16, v196\n"
      "v_mfma_f32_16x16x32_bf16 v[152:155], a[40:43], v[96:99], v[152:155]\n"
      "v_add_f32 v165, v165, v184\n"
      "v_add_f32 v165, v165, v192\n"
      "v_and_b32 v197, 0xffff0000, v197\n"
      "v_mfma_f32_16x16x32_bf16 v[152:155], a[44:47], v[100:103], v[152:155]\n"
      "s_waitcnt lgkmcnt(2)\n"
      "ds_read_b128 v[140:143], v23 offset:46080\n"
      "v_add_f32 v173, v173, v180\n"
      "v_mfma_f32_16x16x32_bf16 v[152:155], a[48:51], v[104:107], v[152:155]\n"
      "buffer_store_dwordx2 v[228:229], v13, s[8:11], s37 offen sc1\n"
      "v_mfma_f32_16x16x32_bf16 v[152:155], a[52:55], v[108:111], v[152:155]\n"
      "s_waitcnt vmcnt(12)\n"
      "v_and_b32 v202, 0xffff0000, v202\n"
      "v_lshlrev_b32 v234, 16, v210\n"
      "v_mfma_f32_16x16x32_bf16 v[152:155], a[56:59], v[112:115], v[152:155]\n"
      "ds_read_b128 v[148:151], v23 offset:48128\n"
      "v_add_f32 v172, v172, v232\n"
      "v_mfma_f32_16x16x32_bf16 v[152:155], a[60:63], v[116:119], v[152:155]\n"
      "s_add_u32 s52, s52, 106496\n"
      "v_lshlrev_b32 v233, 16, v205\n"
      "v_add_u32 v14, 1, v14\n"
      "v_mfma_f32_16x16x32_bf16 v[152:155], a[64:67], v[120:123], v[152:155]\n"
      "v_and_b32 v185, 0xffff0000, v185\n"
      "v_lshlrev_b32 v232, 16, v200\n"
      "v_mfma_f32_16x16x32_bf16 v[152:155], a[68:71], v[124:127], v[152:155]\n"
      "v_add_f32 v170, v170, v235\n"
      "v_add_f32 v174, v174, v233\n"
      "v_mfma_f32_16x16x32_bf16 v[152:155], a[72:75], v[128:131], v[152:155]\n"
      "s_waitcnt lgkmcnt(1)\n"
      "v_add_f32 v164, v164, v232\n"
      "v_and_b32 v189, 0xffff0000, v189\n"
      "v_mfma_f32_16x16x32_bf16 v[152:155], a[76:79], v[132:135], v[152:155]\n"
      "v_and_b32 v195, 0xffff0000, v195\n"
      "v_lshlrev_b32 v235, 16, v211\n"
      "v_mfma_f32_16x16x32_bf16 v[152:155], a[80:83], v[136:139], v[152:155]\n"
      "s_add_u32 s37, s37, 106496\n"
      "v_and_b32 v194, 0xffff0000, v194\n"
      "v_lshlrev_b32 v233, 16, v209\n"
      "v_mfma_f32_16x16x32_bf16 v[152:155], a[84:87], v[140:143], v[152:155]\n"
      "v_lshlrev_b32 v232, 16, v204\n"
      "v_add_f32 v171, v171, v179\n"
      "v_mfma_f32_16x16x32_bf16 v[152:155], a[88:91], v[144:147], v[152:155]\n"
      "s_waitcnt lgkmcnt(0)\n"
      "v_and_b32 v201, 0xffff0000, v201\n"
      "v_add_f32 v172, v172, v232\n"
      "v_mfma_f32_16x16x32_bf16 v[152:155], a[92:95], v[148:151], v[152:155]\n"
      "s_mov_b32 s51, s36\n"
      "v_and_b32 v193, 0xffff0000, v193\n"
      "v_and_b32 v203, 0xffff0000, v203\n"
      "v_mfma_f32_16x16x32_bf16 v[156:159], a[96:99], v[56:59], 0\n"
      "v_and_b32 v188, 0xffff0000, v188\n"
      "v_lshlrev_b32 v232, 16, v208\n"
      "v_mfma_f32_16x16x32_bf16 v[156:159], a[100:103], v[60:63], v[156:159]\n"
      "s_waitcnt vmcnt(10)\n"
      "v_add_f32 v166, v166, v233\n"
      "v_lshlrev_b32 v233, 16, v213\n"
      "v_mfma_f32_16x16x32_bf16 v[156:159], a[104:107], v[64:67], v[156:159]\n"
      "buffer_load_dwordx4 v[216:219], v6, s[8:11], s38 offen sc1\n"
      "v_mfma_f32_16x16x32_bf16 v[156:159], a[108:111], v[68:71], v[156:159]\n"
      "v_and_b32 v213, 0xffff0000, v213\n"
      "v_and_b32 v200, 0xffff0000, v200\n"
      "v_mfma_f32_16x16x32_bf16 v[156:159], a[112:115], v[72:75], v[156:159]\n"
      "s_add_u32 m0, s43, 24576\n"
      "v_add_f32 v169, v169, v194\n"
      "v_and_b32 v196, 0xffff0000, v196\n"
      "v_mfma_f32_16x16x32_bf16 v[156:159], a[116:119], v[76:79], v[156:159]\n"
      "s_waitcnt vmcnt(6)\n"
      "ds_write_b32 v16, v14\n"
      "v_mfma_f32_16x16x32_bf16 v[156:159], a[120:123], v[80:83], v[156:159]\n"
      "v_add_f32 v174, v174, v233\n"
      "v_and_b32 v208, 0xffff0000, v208\n"
      "v_mfma_f32_16x16x32_bf16 v[156:159], a[124:127], v[84:87], v[156:159]\n"
      "v_add_f32 v165, v165, v200\n"
      "v_add_f32 v167, v167, v185\n"
      "v_mfma_f32_16x16x32_bf16 v[156:159], a[128:131], v[88:91], v[156:159]\n"
      "s_waitcnt vmcnt(5)\n"
      "v_add_f32 v167, v167, v193\n"
      "v_cmp_ne_u32 vcc, s21, v20\n"
      "v_mfma_f32_16x16x32_bf16 v[156:159], a[132:135], v[92:95], v[156:159]\n"
      "s_add_u32 s36, s36, 165888\n"
      "v_add_f32 v175, v175, v181\n"
      "v_and_b32 v187, 0xffff0000, v187\n"
      "v_mfma_f32_16x16x32_bf16 v[156:159], a[136:139], v[96:99], v[156:159]\n"
      "v_add_f32 v171, v171, v187\n"
      "v_add_f32 v170, v170, v235\n"
      "v_mfma_f32_16x16x32_bf16 v[156:159], a[140:143], v[100:103], v[156:159]\n"
      "v_add_f32 v164, v164, v232\n"
      "v_add_f32_e64 v171, v171, v195\n"
      "v_mfma_f32_16x16x32_bf16 v[156:159], a[144:147], v[104:107], v[156:159]\n"
      "s_cbranch_vccnz .Lsp_d1_15\n"
      ".Lbk_d1_15:\n"
      "v_add_f32 v173, v173, v188\n"
      "v_mfma_f32_16x16x32_bf16 v[156:159], a[148:151], v[108:111], v[156:159]\n"
      "buffer_load_dwordx4 v0, s[8:11], s51 offen offset:0 sc1 lds\n"
      "v_mfma_f32_16x16x32_bf16 v[156:159], a[152:155], v[112:115], v[156:159]\n"
      "v_add_f32 v169, v169, v202\n"
      "v_cvt_pk_bf16_f32 v224, v152, v153\n"
      "v_mfma_f32_16x16x32_bf16 v[156:159], a[156:159], v[116:119], v[156:159]\n"
      "ds_read_b32 v15, v17\n"
      "v_lshlrev_b32 v232, 16, v212\n"
      "v_mfma_f32_16x16x32_bf16 v[156:159], a[160:163], v[120:123], v[156:159]\n"
      "v_add_f32 v165, v165, v208\n"
      "v_add_f32 v175, v175, v189\n"
      "v_add_f32 v236, v164, v165\n"
      "v_mfma_f32_16x16x32_bf16 v[156:159], a[164:167], v[124:127], v[156:159]\n"
      "buffer_load_dwordx4 v0, s[8:11], s51 offen offset:1024 sc1 lds\n"
      "v_mfma_f32_16x16x32_bf16 v[156:159], a[168:171], v[128:131], v[156:159]\n"
      "v_add_f32 v167, v167, v201\n"
      "v_and_b32 v211, 0xffff0000, v211\n"
      "v_mfma_f32_16x16x32_bf16 v[156:159], a[172:175], v[132:135], v[156:159]\n"
      "v_add_f32 v172, v172, v232\n"
      "v_add_f32 v171, v171, v203\n"
      "v_and_b32 v210, 0xffff0000, v210\n"
      "v_mfma_f32_16x16x32_bf16 v[156:159], a[176:179], v[136:139], v[156:159]\n"
      "s_waitcnt lgkmcnt(0)\n"
      "v_add_f32 v168, v168, v234\n"
      "v_cmp_gt_i32 vcc, v14, v15\n"
      "v_mfma_f32_16x16x32_bf16 v[156:159], a[180:183], v[140:143], v[156:159]\n"
      "buffer_load_dwordx4 v0, s[8:11], s51 offen offset:3072 sc1 lds\n"
      "v_mfma_f32_16x16x32_bf16 v[156:159], a[184:187], v[144:147], v[156:159]\n"
      "v_add_f32 v173, v173, v196\n"
      "v_add_f32 v169, v169, v210\n"
      "v_mfma_f32_16x16x32_bf16 v[156:159], a[188:191], v[148:151], v[156:159]\n"
      "v_and_b32 v205, 0xffff0000, v205\n"
      "v_and_b32 v204, 0xffff0000, v204\n"
      "v_mfma_f32_16x16x32_bf16 v[160:163], a[192:195], v[56:59], 0\n"
      "s_cbranch_vccnz .Lsp_dy0_16\n"
      ".Lbk_dy0_16:\n"
      "v_add_f32 v173, v173, v204\n"
      "v_mfma_f32_16x16x32_bf16 v[160:163], a[196:199], v[60:63], v[160:163]\n"
      "buffer_load_dwordx4 v0, s[8:11], s51 offen offset:2048 sc1 lds\n"
      "v_mfma_f32_16x16x32_bf16 v[160:163], a[200:203], v[64:67], v[160:163]\n"
      "s_add_u32 m0, s43, 28672\n"
      "ds_read_b128 v[64:67], v0 offset:2048\n"
      "v_cvt_pk_bf16_f32 v178, v168, v169\n"
      "v_mfma_f32_16x16x32_bf16 v[160:163], a[204:207], v[68:71], v[160:163]\n"
      "v_and_b32 v209, 0xffff0000, v209\n"
      "v_add_f32 v171, v171, v211\n"
      "v_mfma_f32_16x16x32_bf16 v[160:163], a[208:211], v[72:75], v[160:163]\n"
      "ds_read_b128 v[56:59], v0 offset:0\n"
      "v_add_f32 v236, v236, v166\n"
      "v_mfma_f32_16x16x32_bf16 v[160:163], a[212:215], v[76:79], v[160:163]\n"
      "v_add_f32 v167, v167, v209\n"
      "v_mul_f32 v237, v164, v164\n"
      "v_mfma_f32_16x16x32_bf16 v[160:163], a[216:219], v[80:83], v[160:163]\n"
      "buffer_load_dwordx4 v1, s[8:11], s51 offen offset:1024 sc1 lds\n"
      "v_mfma_f32_16x16x32_bf16 v[160:163], a[220:223], v[84:87], v[160:163]\n"
      "ds_read_b128 v[72:75], v0 offset:4096\n"
      "v_fmac_f32 v237, v165, v165\n"
      "v_mfma_f32_16x16x32_bf16 v[160:163], a[224:227], v[88:91], v[160:163]\n"
      "v_add_f32 v236, v236, v167\n"
      "v_cvt_pk_bf16_f32 v227, v158, v159\n"
      "v_mfma_f32_16x16x32_bf16 v[160:163], a[228:231], v[92:95], v[160:163]\n"
      "v_add_f32 v175, v175, v197\n"
      "v_add_f32 v236, v236, v168\n"
      "v_mfma_f32_16x16x32_bf16 v[160:163], a[232:235], v[96:99], v[160:163]\n"
      "buffer_load_dwordx4 v1, s[8:11], s51 offen offset:0 sc1 lds\n"
      "v_mfma_f32_16x16x32_bf16 v[160:163], a[236:239], v[100:103], v[160:163]\n"
      "ds_read_b128 v[68:71], v0 offset:3072\n"
      "v_cvt_pk_bf16_f32 v226, v156, v157\n"
      "v_mfma_f32_16x16x32_bf16 v[160:163], a[240:243], v[104:107], v[160:163]\n"
      "ds_read_b128 v[60:63], v0 offset:1024\n"
      "v_cvt_pk_bf16_f32 v225, v154, v155\n"
      "v_mfma_f32_16x16x32_bf16 v[160:163], a[244:247], v[108:111], v[160:163]\n"
      "s_min_u32 s36, s36, s6\n"
      "ds_read_b128 v[76:79], v0 offset:5120\n"
      "v_fmac_f32 v237, v166, v166\n"
      "v_mfma_f32_16x16x32_bf16 v[160:163], a[248:251], v[112:115], v[160:163]\n"
      "s_waitcnt vmcnt(7) lgkmcnt(3)\n"
      "buffer_store_dword v18, v4, s[8:11], s52 offen sc1\n"
      "v_mfma_f32_16x16x32_bf16 v[160:163], a[252:255], v[116:119], v[160:163]\n"
      "ds_read_b128 v[92:95], v0 offset:9216\n"
      "v_fmac_f32 v237, v167, v167\n"
      "v_mfma_f32_16x16x32_bf16 v[160:163], v[24:27], v[120:123], v[160:163]\n"
      "s_add_u32 s38, s38, 147456\n"
      "ds_read_b128 v[112:115], v0 offset:14336\n"
      "buffer_load_dword v20, v2, s[8:11], s36 offen sc1\n"
      "v_add_f32 v236, v236, v169\n"
      "v_mfma_f32_16x16x32_bf16 v[160:163], v[28:31], v[124:127], v[160:163]\n"
      "ds_read_b128 v[84:87], v0 offset:7168\n"
      "v_add_f32 v236, v236, v170\n"
      "v_mfma_f32_16x16x32_bf16 v[160:163], v[32:35], v[128:131], v[160:163]\n"
      "ds_read_b128 v[80:83], v0 offset:6144\n"
      "v_fmac_f32 v237, v168, v168\n"
      "v_mfma_f32_16x16x32_bf16 v[160:163], v[36:39], v[132:135], v[160:163]\n"
      "ds_read_b128 v[100:103], v0 offset:11264\n"
      "v_fmac_f32 v237, v169, v169\n"
      "v_mfma_f32_16x16x32_bf16 v[160:163], v[40:43], v[136:139], v[160:163]\n"
      "ds_read_b128 v[116:119], v0 offset:15360\n"
      "v_fmac_f32 v237, v170, v170\n"
      "v_mfma_f32_16x16x32_bf16 v[160:163], v[44:47], v[140:143], v[160:163]\n"
      "s_add_u32 s41, s41, 6\n"
      "ds_read_b128 v[88:91], v0 offset:8192\n"
      "v_cvt_pk_bf16_f32 v177, v166, v167\n"
      "v_mfma_f32_16x16x32_bf16 v[160:163], v[48:51], v[144:147], v[160:163]\n"
      "buffer_store_dwordx4 v[224:227], v0, s[8:11], s37 offen sc1\n"
      "ds_read_b128 v[124:127], v0 offset:17408\n"
      "v_mfma_f32_16x16x32_bf16 v[160:163], v[52:55], v[148:151], v[160:163]\n"
      "s_sub_u32 s59, s59, 1\n"
      "s_cbranch_scc0 .Ld_t0\n"
      ".Ld_out5:\n"
      "v_and_b32 v212, 0xffff0000, v212\n"
      "v_add_f32 v175, v175, v205\n"
      "s_nop 6\n"
      "v_cvt_pk_bf16_f32 v229, v162, v163\n"
      "v_add_f32 v175, v175, v213\n"
      "v_cvt_pk_bf16_f32 v228, v160, v161\n"
      "v_add_f32 v173, v173, v212\n"
      "v_cvt_pk_bf16_f32 v176, v164, v165\n"
      "buffer_store_dwordx2 v[228:229], v13, s[8:11], s37 offen sc1\n"
      "v_cvt_pk_bf16_f32 v179, v170, v171\n"
      "buffer_store_dwordx4 v[176:179], v8, s[16:19], s57 offen\n"
      "v_cvt_pk_bf16_f32 v180, v172, v173\n"
      "v_cvt_pk_bf16_f32 v181, v174, v175\n"
      "buffer_store_dwordx2 v[180:181], v9, s[16:19], s57 offen\n"
      "s_add_u32 s52, s52, 106496\n"
      "s_waitcnt vmcnt(2)\n"
      "buffer_store_dword v18, v4, s[8:11], s52 offen sc1\n"
      "s_add_u32 s37, s37, 173056\n"
      "s_add_u32 s52, s52, 173056\n"
      "s_branch .Ld_end\n"
      ".Ld_out0:\n"
      "v_and_b32 v171, 0xffff0000, v219\n"
      "v_lshlrev_b32 v174, 16, v221\n"
      "v_add_f32 v168, v168, v156\n"
      "s_nop 5\n"
      "v_add_f32 v174, v174, v162\n"
      "v_lshlrev_b32 v172, 16, v220\n"
      "v_add_f32 v171, v171, v159\n"
      "s_add_u32 s37, s37, 173056\n"
      "v_and_b32 v175, 0xffff0000, v221\n"
      "v_and_b32 v173, 0xffff0000, v220\n"
      "v_add_f32 v173, v173, v161\n"
      "v_add_f32 v170, v170, v158\n"
      "v_add_f32 v175, v175, v163\n"
      "v_add_f32 v172, v172, v160\n"
      "s_add_u32 s52, s52, 173056\n"
      "buffer_load_dword v21, v3, s[8:11], s53 offen sc1\n"
      "s_add_u32 s55, s53, 6656\n"
      "s_waitcnt vmcnt(0)\n"
      "v_cmp_ne_u32 vcc, s21, v21\n"
      "s_cbranch_vccnz .Lsp_df_17\n"
      ".Lbk_df_17:\n"
      "buffer_load_dwordx4 v[176:179], v0, s[8:11], s53 offen sc1\n"
      "s_cmp_lt_u32 s41, 32\n"
      "s_cselect_b32 s56, s39, 0x40000000\n"
      "buffer_load_dwordx2 v[180:181], v13, s[8:11], s53 offen sc1\n"
      "s_add_u32 s58, s41, 4294967264\n"
      "buffer_load_dwordx2 v[188:189], v13, s[8:11], s55 offen sc1\n"
      "buffer_load_dwordx4 v[184:187], v0, s[8:11], s55 offen sc1\n"
      "s_add_u32 s54, s53, 13312\n"
      "buffer_load_dwordx4 v[192:195], v0, s[8:11], s54 offen sc1\n"
      "s_add_u32 s55, s53, 19968\n"
      "buffer_load_dwordx2 v[204:205], v13, s[8:11], s55 offen sc1\n"
      "s_cmp_lt_u32 s58, 32\n"
      "s_cselect_b32 s57, s40, 0x40000000\n"
      "buffer_load_dwordx2 v[196:197], v13, s[8:11], s54 offen sc1\n"
      "s_add_u32 s54, s53, 26624\n"
      "s_add_u32 s53, s53, 599040\n"
      "buffer_load_dwordx4 v[200:203], v0, s[8:11], s55 offen sc1\n"
      "s_waitcnt vmcnt(7)\n"
      "v_lshlrev_b32 v234, 16, v178\n"
      "v_lshlrev_b32 v232, 16, v176\n"
      "v_and_b32 v176, 0xffff0000, v176\n"
      "buffer_load_dwordx4 v[208:211], v0, s[8:11], s54 offen sc1\n"
      "v_add_f32 v164, v164, v232\n"
      "s_waitcnt vmcnt(7)\n"
      "v_lshlrev_b32 v232, 16, v180\n"
      "v_lshlrev_b32 v233, 16, v177\n"
      "v_add_f32 v172, v172, v232\n"
      "v_add_f32 v166, v166, v233\n"
      "v_and_b32 v178, 0xffff0000, v178\n"
      "v_add_f32 v168, v168, v234\n"
      "v_lshlrev_b32 v233, 16, v181\n"
      "v_add_f32 v174, v174, v233\n"
      "buffer_load_dwordx2 v[212:213], v13, s[8:11], s54 offen sc1\n"
      "v_add_f32 v169, v169, v178\n"
      "v_lshlrev_b32 v235, 16, v179\n"
      "s_waitcnt vmcnt(6)\n"
      "v_lshlrev_b32 v232, 16, v184\n"
      "v_lshlrev_b32 v233, 16, v185\n"
      "v_add_f32 v164, v164, v232\n"
      "v_add_f32 v170, v170, v235\n"
      "v_lshlrev_b32 v234, 16, v186\n"
      "v_lshlrev_b32 v232, 16, v188\n"
      "v_add_f32 v172, v172, v232\n"
      "v_add_f32 v168, v168, v234\n"
      "v_add_f32 v166, v166, v233\n"
      "s_waitcnt vmcnt(5)\n"
      "v_lshlrev_b32 v232, 16, v192\n"
      "v_lshlrev_b32 v233, 16, v189\n"
      "v_lshlrev_b32 v234, 16, v194\n"
      "s_add_u32 s39, s39, 147456\n"
      "v_and_b32 v186, 0xffff0000, v186\n"
      "v_add_f32 v174, v174, v233\n"
      "v_and_b32 v177, 0xffff0000, v177\n"
      "v_and_b32 v192, 0xffff0000, v192\n"
      "v_add_f32 v168, v168, v234\n"
      "v_lshlrev_b32 v233, 16, v193\n"
      "v_and_b32 v181, 0xffff0000, v181\n"
      "v_lshlrev_b32 v235, 16, v187\n"
      "v_add_f32 v166, v166, v233\n"
      "v_add_f32 v170, v170, v235\n"
      "v_and_b32 v184, 0xffff0000, v184\n"
      "s_waitcnt vmcnt(3)\n"
      "v_lshlrev_b32 v233, 16, v197\n"
      "v_add_f32 v174, v174, v233\n"
      "v_add_f32 v165, v165, v176\n"
      "v_add_f32 v164, v164, v232\n"
      "v_and_b32 v179, 0xffff0000, v179\n"
      "s_waitcnt vmcnt(2)\n"
      "v_lshlrev_b32 v234, 16, v202\n"
      "v_add_f32 v167, v167, v177\n"
      "v_lshlrev_b32 v235, 16, v195\n"
      "v_lshlrev_b32 v233, 16, v201\n"
      "v_add_f32 v170, v170, v235\n"
      "v_add_f32 v166, v166, v233\n"
      "v_and_b32 v180, 0xffff0000, v180\n"
      "s_add_u32 s40, s40, 4294856704\n"
      "v_lshlrev_b32 v235, 16, v203\n"
      "v_add_f32 v169, v169, v186\n"
      "v_add_f32 v168, v168, v234\n"
      "v_lshlrev_b32 v232, 16, v196\n"
      "v_add_f32 v165, v165, v184\n"
      "v_add_f32 v165, v165, v192\n"
      "v_and_b32 v197, 0xffff0000, v197\n"
      "v_add_f32 v173, v173, v180\n"
      "v_and_b32 v202, 0xffff0000, v202\n"
      "s_waitcnt vmcnt(1)\n"
      "v_lshlrev_b32 v234, 16, v210\n"
      "v_add_f32 v172, v172, v232\n"
      "v_lshlrev_b32 v233, 16, v205\n"
      "v_and_b32 v185, 0xffff0000, v185\n"
      "v_lshlrev_b32 v232, 16, v200\n"
      "v_add_f32 v170, v170, v235\n"
      "v_add_f32 v174, v174, v233\n"
      "v_add_f32 v164, v164, v232\n"
      "v_and_b32 v189, 0xffff0000, v189\n"
      "v_and_b32 v195, 0xffff0000, v195\n"
      "v_lshlrev_b32 v235, 16, v211\n"
      "v_and_b32 v194, 0xffff0000, v194\n"
      "v_lshlrev_b32 v233, 16, v209\n"
      "v_lshlrev_b32 v232, 16, v204\n"
      "v_add_f32 v171, v171, v179\n"
      "v_and_b32 v201, 0xffff0000, v201\n"
      "v_add_f32 v172, v172, v232\n"
      "v_and_b32 v193, 0xffff0000, v193\n"
      "v_and_b32 v203, 0xffff0000, v203\n"
      "v_and_b32 v188, 0xffff0000, v188\n"
      "v_lshlrev_b32 v232, 16, v208\n"
      "v_add_f32 v166, v166, v233\n"
      "s_waitcnt vmcnt(0)\n"
      "v_lshlrev_b32 v233, 16, v213\n"
      "v_and_b32 v213, 0xffff0000, v213\n"
      "v_and_b32 v200, 0xffff0000, v200\n"
      "v_add_f32 v169, v169, v194\n"
      "v_and_b32 v196, 0xffff0000, v196\n"
      "v_add_f32 v174, v174, v233\n"
      "v_and_b32 v208, 0xffff0000, v208\n"
      "v_add_f32 v165, v165, v200\n"
      "v_add_f32 v167, v167, v185\n"
      "v_add_f32 v167, v167, v193\n"
      "v_add_f32 v175, v175, v181\n"
      "v_and_b32 v187, 0xffff0000, v187\n"
      "v_add_f32 v171, v171, v187\n"
      "v_add_f32 v170, v170, v235\n"
      "v_add_f32 v164, v164, v232\n"
      "v_add_f32 v171, v171, v195\n"
      "v_add_f32 v173, v173, v188\n"
      "v_add_f32 v169, v169, v202\n"
      "v_lshlrev_b32 v232, 16, v212\n"
      "v_add_f32 v165, v165, v208\n"
      "v_add_f32 v175, v175, v189\n"
      "v_add_f32 v167, v167, v201\n"
      "v_and_b32 v211, 0xffff0000, v211\n"
      "v_add_f32 v172, v172, v232\n"
      "v_add_f32 v171, v171, v203\n"
      "v_and_b32 v210, 0xffff0000, v210\n"
      "v_add_f32 v168, v168, v234\n"
      "v_add_f32 v173, v173, v196\n"
      "v_add_f32 v169, v169, v210\n"
      "v_and_b32 v205, 0xffff0000, v205\n"
      "v_and_b32 v204, 0xffff0000, v204\n"
      "v_add_f32 v173, v173, v204\n"
      "v_cvt_pk_bf16_f32 v178, v168, v169\n"
      "v_and_b32 v209, 0xffff0000, v209\n"
      "v_add_f32 v171, v171, v211\n"
      "v_add_f32 v167, v167, v209\n"
      "v_add_f32 v175, v175, v197\n"
      "s_add_u32 s41, s41, 6\n"
      "v_cvt_pk_bf16_f32 v177, v166, v167\n"
      "v_and_b32 v212, 0xffff0000, v212\n"
      "v_add_f32 v175, v175, v205\n"
      "v_add_f32 v175, v175, v213\n"
      "v_add_f32 v173, v173, v212\n"
      "v_cvt_pk_bf16_f32 v176, v164, v165\n"
      "v_cvt_pk_bf16_f32 v179, v170, v171\n"
      "buffer_store_dwordx4 v[176:179], v8, s[16:19], s57 offen\n"
      "v_cvt_pk_bf16_f32 v180, v172, v173\n"
      "v_cvt_pk_bf16_f32 v181, v174, v175\n"
      "buffer_store_dwordx2 v[180:181], v9, s[16:19], s57 offen\n"
      "s_branch .Ld_end\n"
      ".Ld_out1:\n"
      "buffer_store_dwordx4 v[224:227], v0, s[8:11], s37 offen sc1\n"
      "s_nop 7\n"
      "v_cvt_pk_bf16_f32 v229, v162, v163\n"
      "v_cvt_pk_bf16_f32 v228, v160, v161\n"
      "buffer_store_dwordx2 v[228:229], v13, s[8:11], s37 offen sc1\n"
      "s_add_u32 s37, s37, 106496\n"
      "s_waitcnt vmcnt(0)\n"
      "buffer_store_dword v18, v4, s[8:11], s52 offen sc1\n"
      "buffer_load_dword v21, v3, s[8:11], s53 offen sc1\n"
      "s_add_u32 s52, s52, 106496\n"
      "s_add_u32 s55, s53, 6656\n"
      "s_waitcnt vmcnt(0)\n"
      "v_cmp_ne_u32 vcc, s21, v21\n"
      "s_cbranch_vccnz .Lsp_df_18\n"
      ".Lbk_df_18:\n"
      "buffer_load_dwordx4 v[176:179], v0, s[8:11], s53 offen sc1\n"
      "s_cmp_lt_u32 s41, 32\n"
      "s_cselect_b32 s56, s39, 0x40000000\n"
      "buffer_load_dwordx2 v[180:181], v13, s[8:11], s53 offen sc1\n"
      "s_add_u32 s58, s41, 4294967264\n"
      "buffer_load_dwordx2 v[188:189], v13, s[8:11], s55 offen sc1\n"
      "buffer_load_dwordx4 v[184:187], v0, s[8:11], s55 offen sc1\n"
      "s_add_u32 s54, s53, 13312\n"
      "buffer_load_dwordx4 v[192:195], v0, s[8:11], s54 offen sc1\n"
      "s_add_u32 s55, s53, 19968\n"
      "buffer_load_dwordx2 v[204:205], v13, s[8:11], s55 offen sc1\n"
      "s_cmp_lt_u32 s58, 32\n"
      "s_cselect_b32 s57, s40, 0x40000000\n"
      "buffer_load_dwordx2 v[196:197], v13, s[8:11], s54 offen sc1\n"
      "s_add_u32 s54, s53, 26624\n"
      "s_add_u32 s53, s53, 599040\n"
      "buffer_load_dwordx4 v[200:203], v0, s[8:11], s55 offen sc1\n"
      "s_waitcnt vmcnt(7)\n"
      "v_lshlrev_b32 v234, 16, v178\n"
      "v_lshlrev_b32 v232, 16, v176\n"
      "v_and_b32 v176, 0xffff0000, v176\n"
      "buffer_load_dwordx4 v[208:211], v0, s[8:11], s54 offen sc1\n"
      "v_add_f32 v164, v164, v232\n"
      "s_waitcnt vmcnt(7)\n"
      "v_lshlrev_b32 v232, 16, v180\n"
      "v_lshlrev_b32 v233, 16, v177\n"
      "v_add_f32 v172, v172, v232\n"
      "v_add_f32 v166, v166, v233\n"
      "v_and_b32 v178, 0xffff0000, v178\n"
      "v_add_f32 v168, v168, v234\n"
      "v_lshlrev_b32 v233, 16, v181\n"
      "v_add_f32 v174, v174, v233\n"
      "buffer_load_dwordx2 v[212:213], v13, s[8:11], s54 offen sc1\n"
      "v_add_f32 v169, v169, v178\n"
      "v_lshlrev_b32 v235, 16, v179\n"
      "s_waitcnt vmcnt(6)\n"
      "v_lshlrev_b32 v232, 16, v184\n"
      "v_lshlrev_b32 v233, 16, v185\n"
      "v_add_f32 v164, v164, v232\n"
      "v_add_f32 v170, v170, v235\n"
      "v_lshlrev_b32 v234, 16, v186\n"
      "v_lshlrev_b32 v232, 16, v188\n"
      "v_add_f32 v172, v172, v232\n"
      "v_add_f32 v168, v168, v234\n"
      "v_add_f32 v166, v166, v233\n"
      "s_waitcnt vmcnt(5)\n"
      "v_lshlrev_b32 v232, 16, v192\n"
      "v_lshlrev_b32 v233, 16, v189\n"
      "v_lshlrev_b32 v234, 16, v194\n"
      "s_add_u32 s39, s39, 147456\n"
      "v_and_b32 v186, 0xffff0000, v186\n"
      "v_add_f32 v174, v174, v233\n"
      "v_and_b32 v177, 0xffff0000, v177\n"
      "v_and_b32 v192, 0xffff0000, v192\n"
      "v_add_f32 v168, v168, v234\n"
      "v_lshlrev_b32 v233, 16, v193\n"
      "v_and_b32 v181, 0xffff0000, v181\n"
      "v_lshlrev_b32 v235, 16, v187\n"
      "v_add_f32 v166, v166, v233\n"
      "v_add_f32 v170, v170, v235\n"
      "v_and_b32 v184, 0xffff0000, v184\n"
      "s_waitcnt vmcnt(3)\n"
      "v_lshlrev_b32 v233, 16, v197\n"
      "v_add_f32 v174, v174, v233\n"
      "v_add_f32 v165, v165, v176\n"
      "v_add_f32 v164, v164, v232\n"
      "v_and_b32 v179, 0xffff0000, v179\n"
      "s_waitcnt vmcnt(2)\n"
      "v_lshlrev_b32 v234, 16, v202\n"
      "v_add_f32 v167, v167, v177\n"
      "v_lshlrev_b32 v235, 16, v195\n"
      "v_lshlrev_b32 v233, 16, v201\n"
      "v_add_f32 v170, v170, v235\n"
      "v_add_f32 v166, v166, v233\n"
      "v_and_b32 v180, 0xffff0000, v180\n"
      "s_add_u32 s40, s40, 4294856704\n"
      "v_lshlrev_b32 v235, 16, v203\n"
      "v_add_f32 v169, v169, v186\n"
      "v_add_f32 v168, v168, v234\n"
      "v_lshlrev_b32 v232, 16, v196\n"
      "v_add_f32 v165, v165, v184\n"
      "v_add_f32 v165, v165, v192\n"
      "v_and_b32 v197, 0xffff0000, v197\n"
      "v_add_f32 v173, v173, v180\n"
      "v_and_b32 v202, 0xffff0000, v202\n"
      "s_waitcnt vmcnt(1)\n"
      "v_lshlrev_b32 v234, 16, v210\n"
      "v_add_f32 v172, v172, v232\n"
      "v_lshlrev_b32 v233, 16, v205\n"
      "v_and_b32 v185, 0xffff0000, v185\n"
      "v_lshlrev_b32 v232, 16, v200\n"
      "v_add_f32 v170, v170, v235\n"
      "v_add_f32 v174, v174, v233\n"
      "v_add_f32 v164, v164, v232\n"
      "v_and_b32 v189, 0xffff0000, v189\n"
      "v_and_b32 v195, 0xffff0000, v195\n"
      "v_lshlrev_b32 v235, 16, v211\n"
      "v_and_b32 v194, 0xffff0000, v194\n"
      "v_lshlrev_b32 v233, 16, v209\n"
      "v_lshlrev_b32 v232, 16, v204\n"
      "v_add_f32 v171, v171, v179\n"
      "v_and_b32 v201, 0xffff0000, v201\n"
      "v_add_f32 v172, v172, v232\n"
      "v_and_b32 v193, 0xffff0000, v193\n"
      "v_and_b32 v203, 0xffff0000, v203\n"
      "v_and_b32 v188, 0xffff0000, v188\n"
      "v_lshlrev_b32 v232, 16, v208\n"
      "v_add_f32 v166, v166, v233\n"
      "s_waitcnt vmcnt(0)\n"
      "v_lshlrev_b32 v233, 16, v213\n"
      "v_and_b32 v213, 0xffff0000, v213\n"
      "v_and_b32 v200, 0xffff0000, v200\n"
      "v_add_f32 v169, v169, v194\n"
      "v_and_b32 v196, 0xffff0000, v196\n"
      "v_add_f32 v174, v174, v233\n"
      "v_and_b32 v208, 0xffff0000, v208\n"
      "v_add_f32 v165, v165, v200\n"
      "v_add_f32 v167, v167, v185\n"
      "v_add_f32 v167, v167, v193\n"
      "v_add_f32 v175, v175, v181\n"
      "v_and_b32 v187, 0xffff0000, v187\n"
      "v_add_f32 v171, v171, v187\n"
      "v_add_f32 v170, v170, v235\n"
      "v_add_f32 v164, v164, v232\n"
      "v_add_f32 v171, v171, v195\n"
      "v_add_f32 v173, v173, v188\n"
      "v_add_f32 v169, v169, v202\n"
      "v_lshlrev_b32 v232, 16, v212\n"
      "v_add_f32 v165, v165, v208\n"
      "v_add_f32 v175, v175, v189\n"
      "v_add_f32 v167, v167, v201\n"
      "v_and_b32 v211, 0xffff0000, v211\n"
      "v_add_f32 v172, v172, v232\n"
      "v_add_f32 v171, v171, v203\n"
      "v_and_b32 v210, 0xffff0000, v210\n"
      "v_add_f32 v168, v168, v234\n"
      "v_add_f32 v173, v173, v196\n"
      "v_add_f32 v169, v169, v210\n"
      "v_and_b32 v205, 0xffff0000, v205\n"
      "v_and_b32 v204, 0xffff0000, v204\n"
      "v_add_f32 v173, v173, v204\n"
      "v_cvt_pk_bf16_f32 v178, v168, v169\n"
      "v_and_b32 v209, 0xffff0000, v209\n"
      "v_add_f32 v171, v171, v211\n"
      "v_add_f32 v167, v167, v209\n"
      "v_add_f32 v175, v175, v197\n"
      "s_add_u32 s41, s41, 6\n"
      "v_cvt_pk_bf16_f32 v177, v166, v167\n"
      "v_and_b32 v212, 0xffff0000, v212\n"
      "v_add_f32 v175, v175, v205\n"
      "v_add_f32 v175, v175, v213\n"
      "v_add_f32 v173, v173, v212\n"
      "v_cvt_pk_bf16_f32 v176, v164, v165\n"
      "v_cvt_pk_bf16_f32 v179, v170, v171\n"
      "buffer_store_dwordx4 v[176:179], v8, s[16:19], s57 offen\n"
      "v_cvt_pk_bf16_f32 v180, v172, v173\n"
      "v_cvt_pk_bf16_f32 v181, v174, v175\n"
      "buffer_store_dwordx2 v[180:181], v9, s[16:19], s57 offen\n"
      "s_branch .Ld_end\n"
      ".Ld_out2:\n"
      "buffer_store_dwordx4 v[224:227], v0, s[8:11], s37 offen sc1\n"
      "s_nop 7\n"
      "v_cvt_pk_bf16_f32 v228, v160, v161\n"
      "v_cvt_pk_bf16_f32 v229, v162, v163\n"
      "buffer_store_dwordx2 v[228:229], v13, s[8:11], s37 offen sc1\n"
      "s_add_u32 s52, s52, 106496\n"
      "s_add_u32 s55, s53, 6656\n"
      "s_waitcnt vmcnt(2)\n"
      "v_cmp_ne_u32 vcc, s21, v21\n"
      "s_cbranch_vccnz .Lsp_df_19\n"
      ".Lbk_df_19:\n"
      "buffer_load_dwordx4 v[176:179], v0, s[8:11], s53 offen sc1\n"
      "s_cmp_lt_u32 s41, 32\n"
      "s_cselect_b32 s56, s39, 0x40000000\n"
      "buffer_load_dwordx2 v[180:181], v13, s[8:11], s53 offen sc1\n"
      "s_waitcnt vmcnt(2)\n"
      "buffer_store_dword v18, v4, s[8:11], s52 offen sc1\n"
      "s_add_u32 s58, s41, 4294967264\n"
      "buffer_load_dwordx2 v[188:189], v13, s[8:11], s55 offen sc1\n"
      "s_add_u32 s37, s37, 106496\n"
      "buffer_load_dwordx4 v[184:187], v0, s[8:11], s55 offen sc1\n"
      "s_add_u32 s54, s53, 13312\n"
      "buffer_load_dwordx4 v[192:195], v0, s[8:11], s54 offen sc1\n"
      "s_add_u32 s55, s53, 19968\n"
      "buffer_load_dwordx2 v[204:205], v13, s[8:11], s55 offen sc1\n"
      "s_cmp_lt_u32 s58, 32\n"
      "s_cselect_b32 s57, s40, 0x40000000\n"
      "buffer_load_dwordx2 v[196:197], v13, s[8:11], s54 offen sc1\n"
      "s_add_u32 s54, s53, 26624\n"
      "s_add_u32 s53, s53, 599040\n"
      "s_add_u32 s52, s52, 106496\n"
      "buffer_load_dwordx4 v[200:203], v0, s[8:11], s55 offen sc1\n"
      "s_waitcnt vmcnt(8)\n"
      "v_lshlrev_b32 v234, 16, v178\n"
      "v_lshlrev_b32 v232, 16, v176\n"
      "v_and_b32 v176, 0xffff0000, v176\n"
      "buffer_load_dwordx4 v[208:211], v0, s[8:11], s54 offen sc1\n"
      "v_add_f32 v164, v164, v232\n"
      "s_waitcnt vmcnt(8)\n"
      "v_lshlrev_b32 v232, 16, v180\n"
      "v_lshlrev_b32 v233, 16, v177\n"
      "v_add_f32 v172, v172, v232\n"
      "v_add_f32 v166, v166, v233\n"
      "v_and_b32 v178, 0xffff0000, v178\n"
      "v_add_f32 v168, v168, v234\n"
      "v_lshlrev_b32 v233, 16, v181\n"
      "v_add_f32 v174, v174, v233\n"
      "buffer_load_dwordx2 v[212:213], v13, s[8:11], s54 offen sc1\n"
      "v_add_f32 v169, v169, v178\n"
      "v_lshlrev_b32 v235, 16, v179\n"
      "s_waitcnt vmcnt(6)\n"
      "v_lshlrev_b32 v232, 16, v184\n"
      "v_lshlrev_b32 v233, 16, v185\n"
      "v_add_f32 v164, v164, v232\n"
      "v_add_f32 v170, v170, v235\n"
      "v_lshlrev_b32 v234, 16, v186\n"
      "v_lshlrev_b32 v232, 16, v188\n"
      "v_add_f32 v172, v172, v232\n"
      "v_add_f32 v168, v168, v234\n"
      "v_add_f32 v166, v166, v233\n"
      "s_waitcnt vmcnt(5)\n"
      "v_lshlrev_b32 v232, 16, v192\n"
      "v_lshlrev_b32 v233, 16, v189\n"
      "v_lshlrev_b32 v234, 16, v194\n"
      "s_add_u32 s39, s39, 147456\n"
      "v_and_b32 v186, 0xffff0000, v186\n"
      "v_add_f32 v174, v174, v233\n"
      "v_and_b32 v177, 0xffff0000, v177\n"
      "v_and_b32 v192, 0xffff0000, v192\n"
      "v_add_f32 v168, v168, v234\n"
      "v_lshlrev_b32 v233, 16, v193\n"
      "v_and_b32 v181, 0xffff0000, v181\n"
      "v_lshlrev_b32 v235, 16, v187\n"
      "v_add_f32 v166, v166, v233\n"
      "v_add_f32 v170, v170, v235\n"
      "v_and_b32 v184, 0xffff0000, v184\n"
      "s_waitcnt vmcnt(3)\n"
      "v_lshlrev_b32 v233, 16, v197\n"
      "v_add_f32 v174, v174, v233\n"
      "v_add_f32 v165, v165, v176\n"
      "v_add_f32 v164, v164, v232\n"
      "v_and_b32 v179, 0xffff0000, v179\n"
      "s_waitcnt vmcnt(2)\n"
      "v_lshlrev_b32 v234, 16, v202\n"
      "v_add_f32 v167, v167, v177\n"
      "v_lshlrev_b32 v235, 16, v195\n"
      "v_lshlrev_b32 v233, 16, v201\n"
      "v_add_f32 v170, v170, v235\n"
      "v_add_f32 v166, v166, v233\n"
      "v_and_b32 v180, 0xffff0000, v180\n"
      "s_add_u32 s40, s40, 4294856704\n"
      "v_lshlrev_b32 v235, 16, v203\n"
      "v_add_f32 v169, v169, v186\n"
      "v_add_f32 v168, v168, v234\n"
      "v_lshlrev_b32 v232, 16, v196\n"
      "v_add_f32 v165, v165, v184\n"
      "v_add_f32 v165, v165, v192\n"
      "v_and_b32 v197, 0xffff0000, v197\n"
      "v_add_f32 v173, v173, v180\n"
      "v_and_b32 v202, 0xffff0000, v202\n"
      "s_waitcnt vmcnt(1)\n"
      "v_lshlrev_b32 v234, 16, v210\n"
      "v_add_f32 v172, v172, v232\n"
      "v_lshlrev_b32 v233, 16, v205\n"
      "v_and_b32 v185, 0xffff0000, v185\n"
      "v_lshlrev_b32 v232, 16, v200\n"
      "v_add_f32 v170, v170, v235\n"
      "v_add_f32 v174, v174, v233\n"
      "v_add_f32 v164, v164, v232\n"
      "v_and_b32 v189, 0xffff0000, v189\n"
      "v_and_b32 v195, 0xffff0000, v195\n"
      "v_lshlrev_b32 v235, 16, v211\n"
      "v_and_b32 v194, 0xffff0000, v194\n"
      "v_lshlrev_b32 v233, 16, v209\n"
      "v_lshlrev_b32 v232, 16, v204\n"
      "v_add_f32 v171, v171, v179\n"
      "v_and_b32 v201, 0xffff0000, v201\n"
      "v_add_f32 v172, v172, v232\n"
      "v_and_b32 v193, 0xffff0000, v193\n"
      "v_and_b32 v203, 0xffff0000, v203\n"
      "v_and_b32 v188, 0xffff0000, v188\n"
      "v_lshlrev_b32 v232, 16, v208\n"
      "v_add_f32 v166, v166, v233\n"
      "s_waitcnt vmcnt(0)\n"
      "v_lshlrev_b32 v233, 16, v213\n"
      "v_and_b32 v213, 0xffff0000, v213\n"
      "v_and_b32 v200, 0xffff0000, v200\n"
      "v_add_f32 v169, v169, v194\n"
      "v_and_b32 v196, 0xffff0000, v196\n"
      "v_add_f32 v174, v174, v233\n"
      "v_and_b32 v208, 0xffff0000, v208\n"
      "v_add_f32 v165, v165, v200\n"
      "v_add_f32 v167, v167, v185\n"
      "v_add_f32 v167, v167, v193\n"
      "v_add_f32 v175, v175, v181\n"
      "v_and_b32 v187, 0xffff0000, v187\n"
      "v_add_f32 v171, v171, v187\n"
      "v_add_f32 v170, v170, v235\n"
      "v_add_f32 v164, v164, v232\n"
      "v_add_f32 v171, v171, v195\n"
      "v_add_f32 v173, v173, v188\n"
      "v_add_f32 v169, v169, v202\n"
      "v_lshlrev_b32 v232, 16, v212\n"
      "v_add_f32 v165, v165, v208\n"
      "v_add_f32 v175, v175, v189\n"
      "v_add_f32 v167, v167, v201\n"
      "v_and_b32 v211, 0xffff0000, v211\n"
      "v_add_f32 v172, v172, v232\n"
      "v_add_f32 v171, v171, v203\n"
      "v_and_b32 v210, 0xffff0000, v210\n"
      "v_add_f32 v168, v168, v234\n"
      "v_add_f32 v173, v173, v196\n"
      "v_add_f32 v169, v169, v210\n"
      "v_and_b32 v205, 0xffff0000, v205\n"
      "v_and_b32 v204, 0xffff0000, v204\n"
      "v_add_f32 v173, v173, v204\n"
      "v_cvt_pk_bf16_f32 v178, v168, v169\n"
      "v_and_b32 v209, 0xffff0000, v209\n"
      "v_add_f32 v171, v171, v211\n"
      "v_add_f32 v167, v167, v209\n"
      "v_add_f32 v175, v175, v197\n"
      "s_add_u32 s41, s41, 6\n"
      "v_cvt_pk_bf16_f32 v177, v166, v167\n"
      "v_and_b32 v212, 0xffff0000, v212\n"
      "v_add_f32 v175, v175, v205\n"
      "v_add_f32 v175, v175, v213\n"
      "v_add_f32 v173, v173, v212\n"
      "v_cvt_pk_bf16_f32 v176, v164, v165\n"
      "v_cvt_pk_bf16_f32 v179, v170, v171\n"
      "buffer_store_dwordx4 v[176:179], v8, s[16:19], s57 offen\n"
      "v_cvt_pk_bf16_f32 v180, v172, v173\n"
      "v_cvt_pk_bf16_f32 v181, v174, v175\n"
      "buffer_store_dwordx2 v[180:181], v9, s[16:19], s57 offen\n"
      "s_branch .Ld_end\n"
      ".Ld_out3:\n"
      "s_add_u32 s55, s53, 19968\n"
      "buffer_load_dwordx2 v[204:205], v13, s[8:11], s55 offen sc1\n"
      "v_cvt_pk_bf16_f32 v227, v158, v159\n"
      "s_nop 5\n"
      "v_cvt_pk_bf16_f32 v229, v162, v163\n"
      "buffer_store_dwordx4 v[224:227], v0, s[8:11], s37 offen sc1\n"
      "v_cvt_pk_bf16_f32 v228, v160, v161\n"
      "buffer_store_dwordx2 v[228:229], v13, s[8:11], s37 offen sc1\n"
      "s_cmp_lt_u32 s58, 32\n"
      "s_cselect_b32 s57, s40, 0x40000000\n"
      "buffer_load_dwordx2 v[196:197], v13, s[8:11], s54 offen sc1\n"
      "s_add_u32 s54, s53, 26624\n"
      "s_add_u32 s53, s53, 599040\n"
      "s_add_u32 s52, s52, 106496\n"
      "buffer_load_dwordx4 v[200:203], v0, s[8:11], s55 offen sc1\n"
      "s_waitcnt vmcnt(10)\n"
      "v_lshlrev_b32 v234, 16, v178\n"
      "v_lshlrev_b32 v232, 16, v176\n"
      "v_and_b32 v176, 0xffff0000, v176\n"
      "buffer_load_dwordx4 v[208:211], v0, s[8:11], s54 offen sc1\n"
      "v_add_f32 v164, v164, v232\n"
      "s_waitcnt vmcnt(10)\n"
      "v_lshlrev_b32 v232, 16, v180\n"
      "v_lshlrev_b32 v233, 16, v177\n"
      "v_add_f32 v172, v172, v232\n"
      "v_add_f32 v166, v166, v233\n"
      "v_and_b32 v178, 0xffff0000, v178\n"
      "v_add_f32 v168, v168, v234\n"
      "v_lshlrev_b32 v233, 16, v181\n"
      "v_add_f32 v174, v174, v233\n"
      "buffer_load_dwordx2 v[212:213], v13, s[8:11], s54 offen sc1\n"
      "v_add_f32 v169, v169, v178\n"
      "v_lshlrev_b32 v235, 16, v179\n"
      "s_waitcnt vmcnt(8)\n"
      "v_lshlrev_b32 v232, 16, v184\n"
      "v_lshlrev_b32 v233, 16, v185\n"
      "v_add_f32 v164, v164, v232\n"
      "v_add_f32 v170, v170, v235\n"
      "v_lshlrev_b32 v234, 16, v186\n"
      "v_lshlrev_b32 v232, 16, v188\n"
      "v_add_f32 v172, v172, v232\n"
      "v_add_f32 v168, v168, v234\n"
      "v_add_f32 v166, v166, v233\n"
      "s_waitcnt vmcnt(7)\n"
      "v_lshlrev_b32 v232, 16, v192\n"
      "v_lshlrev_b32 v233, 16, v189\n"
      "v_lshlrev_b32 v234, 16, v194\n"
      "s_add_u32 s39, s39, 147456\n"
      "v_and_b32 v186, 0xffff0000, v186\n"
      "v_add_f32 v174, v174, v233\n"
      "v_and_b32 v177, 0xffff0000, v177\n"
      "v_and_b32 v192, 0xffff0000, v192\n"
      "v_add_f32 v168, v168, v234\n"
      "v_lshlrev_b32 v233, 16, v193\n"
      "s_add_u32 s37, s37, 106496\n"
      "v_and_b32 v181, 0xffff0000, v181\n"
      "v_lshlrev_b32 v235, 16, v187\n"
      "v_add_f32 v166, v166, v233\n"
      "v_add_f32 v170, v170, v235\n"
      "v_and_b32 v184, 0xffff0000, v184\n"
      "s_waitcnt vmcnt(3)\n"
      "v_lshlrev_b32 v233, 16, v197\n"
      "v_add_f32 v174, v174, v233\n"
      "buffer_store_dword v18, v4, s[8:11], s52 offen sc1\n"
      "v_add_f32 v165, v165, v176\n"
      "v_add_f32 v164, v164, v232\n"
      "v_and_b32 v179, 0xffff0000, v179\n"
      "s_waitcnt vmcnt(3)\n"
      "v_lshlrev_b32 v234, 16, v202\n"
      "v_add_f32 v167, v167, v177\n"
      "v_lshlrev_b32 v235, 16, v195\n"
      "v_lshlrev_b32 v233, 16, v201\n"
      "v_add_f32 v170, v170, v235\n"
      "v_add_f32 v166, v166, v233\n"
      "v_and_b32 v180, 0xffff0000, v180\n"
      "s_add_u32 s40, s40, 4294856704\n"
      "v_lshlrev_b32 v235, 16, v203\n"
      "v_add_f32 v169, v169, v186\n"
      "v_add_f32 v168, v168, v234\n"
      "v_lshlrev_b32 v232, 16, v196\n"
      "v_add_f32 v165, v165, v184\n"
      "v_add_f32 v165, v165, v192\n"
      "v_and_b32 v197, 0xffff0000, v197\n"
      "v_add_f32 v173, v173, v180\n"
      "v_and_b32 v202, 0xffff0000, v202\n"
      "s_waitcnt vmcnt(2)\n"
      "v_lshlrev_b32 v234, 16, v210\n"
      "v_add_f32 v172, v172, v232\n"
      "s_add_u32 s52, s52, 106496\n"
      "v_lshlrev_b32 v233, 16, v205\n"
      "v_and_b32 v185, 0xffff0000, v185\n"
      "v_lshlrev_b32 v232, 16, v200\n"
      "v_add_f32 v170, v170, v235\n"
      "v_add_f32 v174, v174, v233\n"
      "v_add_f32 v164, v164, v232\n"
      "v_and_b32 v189, 0xffff0000, v189\n"
      "v_and_b32 v195, 0xffff0000, v195\n"
      "v_lshlrev_b32 v235, 16, v211\n"
      "v_and_b32 v194, 0xffff0000, v194\n"
      "v_lshlrev_b32 v233, 16, v209\n"
      "v_lshlrev_b32 v232, 16, v204\n"
      "v_add_f32 v171, v171, v179\n"
      "v_and_b32 v201, 0xffff0000, v201\n"
      "v_add_f32 v172, v172, v232\n"
      "v_and_b32 v193, 0xffff0000, v193\n"
      "v_and_b32 v203, 0xffff0000, v203\n"
      "v_and_b32 v188, 0xffff0000, v188\n"
      "v_lshlrev_b32 v232, 16, v208\n"
      "v_add_f32 v166, v166, v233\n"
      "s_waitcnt vmcnt(1)\n"
      "v_lshlrev_b32 v233, 16, v213\n"
      "v_and_b32 v213, 0xffff0000, v213\n"
      "v_and_b32 v200, 0xffff0000, v200\n"
      "v_add_f32 v169, v169, v194\n"
      "v_and_b32 v196, 0xffff0000, v196\n"
      "v_add_f32 v174, v174, v233\n"
      "v_and_b32 v208, 0xffff0000, v208\n"
      "v_add_f32 v165, v165, v200\n"
      "v_add_f32 v167, v167, v185\n"
      "v_add_f32 v167, v167, v193\n"
      "v_add_f32 v175, v175, v181\n"
      "v_and_b32 v187, 0xffff0000, v187\n"
      "v_add_f32 v171, v171, v187\n"
      "v_add_f32 v170, v170, v235\n"
      "v_add_f32 v164, v164, v232\n"
      "v_add_f32 v171, v171, v195\n"
      "v_add_f32 v173, v173, v188\n"
      "v_add_f32 v169, v169, v202\n"
      "v_lshlrev_b32 v232, 16, v212\n"
      "v_add_f32 v165, v165, v208\n"
      "v_add_f32 v175, v175, v189\n"
      "v_add_f32 v167, v167, v201\n"
      "v_and_b32 v211, 0xffff0000, v211\n"
      "v_add_f32 v172, v172, v232\n"
      "v_add_f32 v171, v171, v203\n"
      "v_and_b32 v210, 0xffff0000, v210\n"
      "v_add_f32 v168, v168, v234\n"
      "v_add_f32 v173, v173, v196\n"
      "v_add_f32 v169, v169, v210\n"
      "v_and_b32 v205, 0xffff0000, v205\n"
      "v_and_b32 v204, 0xffff0000, v204\n"
      "v_add_f32 v173, v173, v204\n"
      "v_cvt_pk_bf16_f32 v178, v168, v169\n"
      "v_and_b32 v209, 0xffff0000, v209\n"
      "v_add_f32 v171, v171, v211\n"
      "v_add_f32 v167, v167, v209\n"
      "v_add_f32 v175, v175, v197\n"
      "s_add_u32 s41, s41, 6\n"
      "v_cvt_pk_bf16_f32 v177, v166, v167\n"
      "v_and_b32 v212, 0xffff0000, v212\n"
      "v_add_f32 v175, v175, v205\n"
      "v_add_f32 v175, v175, v213\n"
      "v_add_f32 v173, v173, v212\n"
      "v_cvt_pk_bf16_f32 v176, v164, v165\n"
      "v_cvt_pk_bf16_f32 v179, v170, v171\n"
      "buffer_store_dwordx4 v[176:179], v8, s[16:19], s57 offen\n"
      "v_cvt_pk_bf16_f32 v180, v172, v173\n"
      "v_cvt_pk_bf16_f32 v181, v174, v175\n"
      "buffer_store_dwordx2 v[180:181], v9, s[16:19], s57 offen\n"
      "s_branch .Ld_end\n"
      ".Ld_out4:\n"
      "v_lshlrev_b32 v233, 16, v201\n"
      "v_add_f32 v170, v170, v235\n"
      "v_add_f32 v166, v166, v233\n"
      "v_and_b32 v180, 0xffff0000, v180\n"
      "s_add_u32 s40, s40, 4294856704\n"
      "v_lshlrev_b32 v235, 16, v203\n"
      "v_add_f32 v169, v169, v186\n"
      "buffer_store_dwordx4 v[224:227], v0, s[8:11], s37 offen sc1\n"
      "s_nop 0\n"
      "v_cvt_pk_bf16_f32 v229, v162, v163\n"
      "v_cvt_pk_bf16_f32 v228, v160, v161\n"
      "v_add_f32 v168, v168, v234\n"
      "v_lshlrev_b32 v232, 16, v196\n"
      "v_add_f32 v165, v165, v184\n"
      "v_add_f32 v165, v165, v192\n"
      "v_and_b32 v197, 0xffff0000, v197\n"
      "v_add_f32 v173, v173, v180\n"
      "buffer_store_dwordx2 v[228:229], v13, s[8:11], s37 offen sc1\n"
      "v_and_b32 v202, 0xffff0000, v202\n"
      "s_waitcnt vmcnt(11)\n"
      "v_lshlrev_b32 v234, 16, v210\n"
      "v_add_f32 v172, v172, v232\n"
      "s_add_u32 s52, s52, 106496\n"
      "v_lshlrev_b32 v233, 16, v205\n"
      "v_and_b32 v185, 0xffff0000, v185\n"
      "v_lshlrev_b32 v232, 16, v200\n"
      "v_add_f32 v170, v170, v235\n"
      "v_add_f32 v174, v174, v233\n"
      "v_add_f32 v164, v164, v232\n"
      "v_and_b32 v189, 0xffff0000, v189\n"
      "v_and_b32 v195, 0xffff0000, v195\n"
      "v_lshlrev_b32 v235, 16, v211\n"
      "s_add_u32 s37, s37, 106496\n"
      "v_and_b32 v194, 0xffff0000, v194\n"
      "v_lshlrev_b32 v233, 16, v209\n"
      "v_lshlrev_b32 v232, 16, v204\n"
      "v_add_f32 v171, v171, v179\n"
      "v_and_b32 v201, 0xffff0000, v201\n"
      "v_add_f32 v172, v172, v232\n"
      "v_and_b32 v193, 0xffff0000, v193\n"
      "v_and_b32 v203, 0xffff0000, v203\n"
      "v_and_b32 v188, 0xffff0000, v188\n"
      "v_lshlrev_b32 v232, 16, v208\n"
      "v_add_f32 v166, v166, v233\n"
      "s_waitcnt vmcnt(9)\n"
      "v_lshlrev_b32 v233, 16, v213\n"
      "v_and_b32 v213, 0xffff0000, v213\n"
      "v_and_b32 v200, 0xffff0000, v200\n"
      "v_add_f32 v169, v169, v194\n"
      "v_and_b32 v196, 0xffff0000, v196\n"
      "v_add_f32 v174, v174, v233\n"
      "v_and_b32 v208, 0xffff0000, v208\n"
      "v_add_f32 v165, v165, v200\n"
      "v_add_f32 v167, v167, v185\n"
      "v_add_f32 v167, v167, v193\n"
      "v_add_f32 v175, v175, v181\n"
      "v_and_b32 v187, 0xffff0000, v187\n"
      "v_add_f32 v171, v171, v187\n"
      "v_add_f32 v170, v170, v235\n"
      "v_add_f32 v164, v164, v232\n"
      "v_add_f32 v171, v171, v195\n"
      "v_add_f32 v173, v173, v188\n"
      "v_add_f32 v169, v169, v202\n"
      "v_lshlrev_b32 v232, 16, v212\n"
      "v_add_f32 v165, v165, v208\n"
      "v_add_f32 v175, v175, v189\n"
      "v_add_f32 v167, v167, v201\n"
      "v_and_b32 v211, 0xffff0000, v211\n"
      "v_add_f32 v172, v172, v232\n"
      "v_add_f32 v171, v171, v203\n"
      "v_and_b32 v210, 0xffff0000, v210\n"
      "v_add_f32 v168, v168, v234\n"
      "v_add_f32 v173, v173, v196\n"
      "v_add_f32 v169, v169, v210\n"
      "v_and_b32 v205, 0xffff0000, v205\n"
      "v_and_b32 v204, 0xffff0000, v204\n"
      "v_add_f32 v173, v173, v204\n"
      "v_cvt_pk_bf16_f32 v178, v168, v169\n"
      "v_and_b32 v209, 0xffff0000, v209\n"
      "v_add_f32 v171, v171, v211\n"
      "v_add_f32 v167, v167, v209\n"
      "v_add_f32 v175, v175, v197\n"
      "s_waitcnt vmcnt(0)\n"
      "buffer_store_dword v18, v4, s[8:11], s52 offen sc1\n"
      "s_add_u32 s41, s41, 6\n"
      "v_cvt_pk_bf16_f32 v177, v166, v167\n"
      "v_and_b32 v212, 0xffff0000, v212\n"
      "v_add_f32 v175, v175, v205\n"
      "v_add_f32 v175, v175, v213\n"
      "v_add_f32 v173, v173, v212\n"
      "v_cvt_pk_bf16_f32 v176, v164, v165\n"
      "v_cvt_pk_bf16_f32 v179, v170, v171\n"
      "buffer_store_dwordx4 v[176:179], v8, s[16:19], s57 offen\n"
      "v_cvt_pk_bf16_f32 v180, v172, v173\n"
      "v_cvt_pk_bf16_f32 v181, v174, v175\n"
      "buffer_store_dwordx2 v[180:181], v9, s[16:19], s57 offen\n"
      "s_add_u32 s52, s52, 106496\n"
      "s_branch .Ld_end\n"
      ".Ld_in1:\n"
      "s_add_u32 s36, s36, 165888\n"
      "s_min_u32 s36, s36, s6\n"
      "s_mov_b32 s51, s36\n"
      "s_add_u32 s36, s36, 165888\n"
      "s_min_u32 s36, s36, s6\n"
      "buffer_load_dword v20, v2, s[8:11], s36 offen sc1\n"
      "s_mov_b32 s51, s36\n"
      "s_add_u32 m0, s43, 24576\n"
      "s_waitcnt vmcnt(0)\n"
      "v_cmp_ne_u32 vcc, s21, v20\n"
      "s_add_u32 s36, s36, 165888\n"
      "s_cbranch_vccnz .Lsp_d1_20\n"
      ".Lbk_d1_20:\n"
      "buffer_load_dwordx4 v0, s[8:11], s51 offen offset:0 sc1 lds\n"
      "buffer_load_dwordx4 v0, s[8:11], s51 offen offset:1024 sc1 lds\n"
      "buffer_load_dwordx4 v0, s[8:11], s51 offen offset:3072 sc1 lds\n"
      "buffer_load_dwordx4 v0, s[8:11], s51 offen offset:2048 sc1 lds\n"
      "s_add_u32 m0, s43, 28672\n"
      "s_nop 0\n"
      "buffer_load_dwordx4 v1, s[8:11], s51 offen offset:1024 sc1 lds\n"
      "buffer_load_dwordx4 v1, s[8:11], s51 offen offset:0 sc1 lds\n"
      "s_min_u32 s36, s36, s6\n"
      "s_add_u32 s38, s38, 147456\n"
      "buffer_load_dword v20, v2, s[8:11], s36 offen sc1\n"
      "s_mov_b32 s51, s36\n"
      "s_add_u32 s36, s36, 165888\n"
      "v_add_u32 v14, 1, v14\n"
      "s_min_u32 s36, s36, s6\n"
      "s_add_u32 m0, s43, 49152\n"
      "s_waitcnt vmcnt(1)\n"
      "ds_write_b32 v16, v14\n"
      "s_waitcnt vmcnt(0)\n"
      "v_cmp_ne_u32 vcc, s21, v20\n"
      "s_cbranch_vccnz .Lsp_d2_21\n"
      ".Lbk_d2_21:\n"
      "buffer_load_dwordx4 v0, s[8:11], s51 offen offset:0 sc1 lds\n"
      "ds_read_b32 v15, v17\n"
      "buffer_load_dwordx4 v0, s[8:11], s51 offen offset:3072 sc1 lds\n"
      "s_add_u32 s52, s52, 106496\n"
      "s_waitcnt lgkmcnt(0)\n"
      "v_cmp_gt_i32 vcc, v14, v15\n"
      "buffer_load_dwordx4 v0, s[8:11], s51 offen offset:2048 sc1 lds\n"
      "s_cbranch_vccnz .Lsp_dy1_22\n"
      ".Lbk_dy1_22:\n"
      "buffer_load_dword v20, v2, s[8:11], s36 offen sc1\n"
      "ds_read_b128 v[56:59], v0 offset:24576\n"
      "buffer_load_dwordx4 v0, s[8:11], s51 offen offset:1024 sc1 lds\n"
      "ds_read_b128 v[68:71], v0 offset:27648\n"
      "ds_read_b128 v[84:87], v0 offset:31744\n"
      "s_add_u32 m0, s43, 53248\n"
      "ds_read_b128 v[60:63], v0 offset:25600\n"
      "buffer_load_dwordx4 v1, s[8:11], s51 offen offset:0 sc1 lds\n"
      "ds_read_b128 v[100:103], v0 offset:35840\n"
      "buffer_load_dwordx4 v1, s[8:11], s51 offen offset:1024 sc1 lds\n"
      "ds_read_b128 v[104:107], v0 offset:36864\n"
      "s_mov_b32 s51, s36\n"
      "ds_read_b128 v[72:75], v0 offset:28672\n"
      "ds_read_b128 v[108:111], v0 offset:37888\n"
      "ds_read_b128 v[64:67], v0 offset:26624\n"
      "ds_read_b128 v[92:95], v0 offset:33792\n"
      "ds_read_b128 v[80:83], v0 offset:30720\n"
      "ds_read_b128 v[96:99], v0 offset:34816\n"
      "ds_read_b128 v[76:79], v0 offset:29696\n"
      "ds_read_b128 v[116:119], v0 offset:39936\n"
      "s_waitcnt vmcnt(0) lgkmcnt(0)\n"
      "s_nop 4\n"
      "s_branch .Ld_t1\n"
      ".Ld_in2:\n"
      "s_add_u32 s36, s36, 165888\n"
      "s_min_u32 s36, s36, s6\n"
      "s_mov_b32 s51, s36\n"
      "s_add_u32 s36, s36, 165888\n"
      "s_min_u32 s36, s36, s6\n"
      "s_mov_b32 s51, s36\n"
      "s_add_u32 s36, s36, 165888\n"
      "s_min_u32 s36, s36, s6\n"
      "s_add_u32 s38, s38, 147456\n"
      "buffer_load_dword v20, v2, s[8:11], s36 offen sc1\n"
      "s_mov_b32 s51, s36\n"
      "s_add_u32 s36, s36, 165888\n"
      "s_min_u32 s36, s36, s6\n"
      "s_add_u32 m0, s43, 49152\n"
      "s_waitcnt vmcnt(0)\n"
      "v_cmp_ne_u32 vcc, s21, v20\n"
      "s_cbranch_vccnz .Lsp_d2_23\n"
      ".Lbk_d2_23:\n"
      "buffer_load_dwordx4 v0, s[8:11], s51 offen offset:0 sc1 lds\n"
      "buffer_load_dwordx4 v0, s[8:11], s51 offen offset:3072 sc1 lds\n"
      "s_add_u32 s52, s52, 106496\n"
      "buffer_load_dwordx4 v0, s[8:11], s51 offen offset:2048 sc1 lds\n"
      "buffer_load_dword v20, v2, s[8:11], s36 offen sc1\n"
      "buffer_load_dwordx4 v0, s[8:11], s51 offen offset:1024 sc1 lds\n"
      "s_add_u32 m0, s43, 53248\n"
      "s_nop 0\n"
      "buffer_load_dwordx4 v1, s[8:11], s51 offen offset:0 sc1 lds\n"
      "buffer_load_dwordx4 v1, s[8:11], s51 offen offset:1024 sc1 lds\n"
      "s_mov_b32 s51, s36\n"
      "s_add_u32 s37, s37, 173056\n"
      "s_add_u32 s36, s36, 165888\n"
      "s_min_u32 s36, s36, s6\n"
      "v_add_u32 v14, 1, v14\n"
      "s_waitcnt vmcnt(3)\n"
      "v_cmp_ne_u32 vcc, s21, v20\n"
      "s_waitcnt vmcnt(0)\n"
      "ds_write_b32 v16, v14\n"
      "s_cbranch_vccnz .Lsp_d3_24\n"
      ".Lbk_d3_24:\n"
      "s_add_u32 m0, s43, 73728\n"
      "s_nop 0\n"
      "buffer_load_dwordx4 v0, s[8:11], s51 offen offset:2048 sc1 lds\n"
      "buffer_load_dwordx4 v0, s[8:11], s51 offen offset:0 sc1 lds\n"
      "ds_read_b32 v15, v17\n"
      "s_add_u32 s52, s52, 173056\n"
      "buffer_load_dwordx4 v0, s[8:11], s51 offen offset:1024 sc1 lds\n"
      "s_waitcnt lgkmcnt(0)\n"
      "v_cmp_gt_i32 vcc, v14, v15\n"
      "buffer_load_dword v20, v2, s[8:11], s36 offen sc1\n"
      "s_cbranch_vccnz .Lsp_dy2_25\n"
      ".Lbk_dy2_25:\n"
      "buffer_load_dwordx4 v0, s[8:11], s51 offen offset:3072 sc1 lds\n"
      "ds_read_b128 v[68:71], v22 offset:3072\n"
      "s_add_u32 m0, s43, 77824\n"
      "ds_read_b128 v[60:63], v22 offset:1024\n"
      "ds_read_b128 v[56:59], v22 offset:0\n"
      "ds_read_b128 v[72:75], v22 offset:4096\n"
      "buffer_load_dwordx4 v1, s[8:11], s51 offen offset:0 sc1 lds\n"
      "ds_read_b128 v[64:67], v22 offset:2048\n"
      "ds_read_b128 v[80:83], v22 offset:6144\n"
      "ds_read_b128 v[96:99], v22 offset:10240\n"
      "buffer_load_dwordx4 v1, s[8:11], s51 offen offset:1024 sc1 lds\n"
      "ds_read_b128 v[112:115], v22 offset:14336\n"
      "ds_read_b128 v[92:95], v22 offset:9216\n"
      "ds_read_b128 v[84:87], v22 offset:7168\n"
      "ds_read_b128 v[88:91], v22 offset:8192\n"
      "ds_read_b128 v[108:111], v22 offset:13312\n"
      "ds_read_b128 v[76:79], v22 offset:5120\n"
      "ds_read_b128 v[100:103], v22 offset:11264\n"
      "ds_read_b128 v[120:123], v22 offset:16384\n"
      "s_waitcnt vmcnt(0) lgkmcnt(0)\n"
      "s_nop 4\n"
      "s_branch .Ld_t2\n"
      ".Ld_in3:\n"
      "s_add_u32 s36, s36, 165888\n"
      "s_min_u32 s36, s36, s6\n"
      "s_mov_b32 s51, s36\n"
      "s_add_u32 s36, s36, 165888\n"
      "s_min_u32 s36, s36, s6\n"
      "s_mov_b32 s51, s36\n"
      "s_add_u32 s36, s36, 165888\n"
      "s_min_u32 s36, s36, s6\n"
      "s_add_u32 s38, s38, 147456\n"
      "s_mov_b32 s51, s36\n"
      "s_add_u32 s36, s36, 165888\n"
      "s_min_u32 s36, s36, s6\n"
      "s_add_u32 s52, s52, 106496\n"
      "buffer_load_dword v20, v2, s[8:11], s36 offen sc1\n"
      "s_mov_b32 s51, s36\n"
      "s_add_u32 s37, s37, 173056\n"
      "s_add_u32 s36, s36, 165888\n"
      "s_min_u32 s36, s36, s6\n"
      "s_waitcnt vmcnt(0)\n"
      "v_cmp_ne_u32 vcc, s21, v20\n"
      "s_cbranch_vccnz .Lsp_d3_26\n"
      ".Lbk_d3_26:\n"
      "s_add_u32 m0, s43, 73728\n"
      "s_nop 0\n"
      "buffer_load_dwordx4 v0, s[8:11], s51 offen offset:2048 sc1 lds\n"
      "buffer_load_dwordx4 v0, s[8:11], s51 offen offset:0 sc1 lds\n"
      "s_add_u32 s52, s52, 173056\n"
      "buffer_load_dwordx4 v0, s[8:11], s51 offen offset:1024 sc1 lds\n"
      "buffer_load_dword v20, v2, s[8:11], s36 offen sc1\n"
      "buffer_load_dwordx4 v0, s[8:11], s51 offen offset:3072 sc1 lds\n"
      "s_add_u32 m0, s43, 77824\n"
      "s_nop 0\n"
      "buffer_load_dwordx4 v1, s[8:11], s51 offen offset:0 sc1 lds\n"
      "buffer_load_dwordx4 v1, s[8:11], s51 offen offset:1024 sc1 lds\n"
      "s_add_u32 m0, s43, 98304\n"
      "s_mov_b32 s51, s36\n"
      "s_add_u32 s36, s36, 165888\n"
      "s_add_u32 s37, s37, 106496\n"
      "s_waitcnt vmcnt(3)\n"
      "v_cmp_ne_u32 vcc, s21, v20\n"
      "s_min_u32 s36, s36, s6\n"
      "s_cbranch_vccnz .Lsp_d4_27\n"
      ".Lbk_d4_27:\n"
      "buffer_load_dwordx4 v0, s[8:11], s51 offen offset:1024 sc1 lds\n"
      "v_add_u32 v14, 1, v14\n"
      "buffer_load_dwordx4 v0, s[8:11], s51 offen offset:2048 sc1 lds\n"
      "s_waitcnt vmcnt(2)\n"
      "ds_write_b32 v16, v14\n"
      "buffer_load_dwordx4 v0, s[8:11], s51 offen offset:3072 sc1 lds\n"
      "buffer_load_dwordx4 v0, s[8:11], s51 offen offset:0 sc1 lds\n"
      "ds_read_b32 v15, v17\n"
      "s_add_u32 m0, s43, 102400\n"
      "buffer_load_dword v20, v2, s[8:11], s36 offen sc1\n"
      "s_waitcnt lgkmcnt(0)\n"
      "v_cmp_gt_i32 vcc, v14, v15\n"
      "buffer_load_dwordx4 v1, s[8:11], s51 offen offset:1024 sc1 lds\n"
      "s_cbranch_vccnz .Lsp_dy3_28\n"
      ".Lbk_dy3_28:\n"
      "ds_read_b128 v[64:67], v22 offset:26624\n"
      "buffer_load_dwordx4 v1, s[8:11], s51 offen offset:0 sc1 lds\n"
      "ds_read_b128 v[56:59], v22 offset:24576\n"
      "ds_read_b128 v[60:63], v22 offset:25600\n"
      "ds_read_b128 v[80:83], v22 offset:30720\n"
      "s_mov_b32 s51, s36\n"
      "ds_read_b128 v[72:75], v22 offset:28672\n"
      "ds_read_b128 v[92:95], v22 offset:33792\n"
      "ds_read_b128 v[88:91], v22 offset:32768\n"
      "ds_read_b128 v[84:87], v22 offset:31744\n"
      "ds_read_b128 v[108:111], v22 offset:37888\n"
      "ds_read_b128 v[96:99], v22 offset:34816\n"
      "ds_read_b128 v[76:79], v22 offset:29696\n"
      "ds_read_b128 v[68:71], v22 offset:27648\n"
      "ds_read_b128 v[124:127], v22 offset:41984\n"
      "ds_read_b128 v[120:123], v22 offset:40960\n"
      "s_waitcnt vmcnt(0) lgkmcnt(0)\n"
      "s_nop 4\n"
      "s_branch .Ld_t3\n"
      ".Ld_in4:\n"
      "s_add_u32 s36, s36, 165888\n"
      "s_min_u32 s36, s36, s6\n"
      "s_mov_b32 s51, s36\n"
      "s_add_u32 s36, s36, 165888\n"
      "s_min_u32 s36, s36, s6\n"
      "s_mov_b32 s51, s36\n"
      "s_add_u32 s36, s36, 165888\n"
      "s_min_u32 s36, s36, s6\n"
      "s_add_u32 s38, s38, 147456\n"
      "s_mov_b32 s51, s36\n"
      "s_add_u32 s36, s36, 165888\n"
      "s_min_u32 s36, s36, s6\n"
      "s_add_u32 s52, s52, 106496\n"
      "s_mov_b32 s51, s36\n"
      "s_add_u32 s37, s37, 173056\n"
      "s_add_u32 s36, s36, 165888\n"
      "s_min_u32 s36, s36, s6\n"
      "s_add_u32 s52, s52, 173056\n"
      "buffer_load_dword v20, v2, s[8:11], s36 offen sc1\n"
      "s_add_u32 m0, s43, 98304\n"
      "s_mov_b32 s51, s36\n"
      "s_add_u32 s36, s36, 165888\n"
      "s_add_u32 s37, s37, 106496\n"
      "s_waitcnt vmcnt(0)\n"
      "v_cmp_ne_u32 vcc, s21, v20\n"
      "s_min_u32 s36, s36, s6\n"
      "s_cbranch_vccnz .Lsp_d4_29\n"
      ".Lbk_d4_29:\n"
      "buffer_load_dwordx4 v0, s[8:11], s51 offen offset:1024 sc1 lds\n"
      "buffer_load_dwordx4 v0, s[8:11], s51 offen offset:2048 sc1 lds\n"
      "buffer_load_dwordx4 v0, s[8:11], s51 offen offset:3072 sc1 lds\n"
      "buffer_load_dwordx4 v0, s[8:11], s51 offen offset:0 sc1 lds\n"
      "s_add_u32 m0, s43, 102400\n"
      "buffer_load_dword v20, v2, s[8:11], s36 offen sc1\n"
      "buffer_load_dwordx4 v1, s[8:11], s51 offen offset:1024 sc1 lds\n"
      "buffer_load_dwordx4 v1, s[8:11], s51 offen offset:0 sc1 lds\n"
      "s_mov_b32 s51, s36\n"
      "s_add_u32 s36, s36, 165888\n"
      "s_add_u32 m0, s43, 122880\n"
      "s_min_u32 s36, s36, s6\n"
      "s_add_u32 s52, s52, 106496\n"
      "s_waitcnt vmcnt(2)\n"
      "v_cmp_ne_u32 vcc, s21, v20\n"
      "s_cbranch_vccnz .Lsp_d5_30\n"
      ".Lbk_d5_30:\n"
      "buffer_load_dwordx4 v0, s[8:11], s51 offen offset:2048 sc1 lds\n"
      "buffer_load_dwordx4 v0, s[8:11], s51 offen offset:3072 sc1 lds\n"
      "v_add_u32 v14, 1, v14\n"
      "s_waitcnt vmcnt(2)\n"
      "ds_write_b32 v16, v14\n"
      "buffer_load_dwordx4 v0, s[8:11], s51 offen offset:0 sc1 lds\n"
      "buffer_load_dwordx4 v0, s[8:11], s51 offen offset:1024 sc1 lds\n"
      "s_add_u32 m0, s43, 126976\n"
      "ds_read_b32 v15, v17\n"
      "buffer_load_dwordx4 v1, s[8:11], s51 offen offset:0 sc1 lds\n"
      "s_waitcnt lgkmcnt(0)\n"
      "v_cmp_gt_i32 vcc, v14, v15\n"
      "buffer_load_dwordx4 v1, s[8:11], s51 offen offset:1024 sc1 lds\n"
      "s_cbranch_vccnz .Lsp_dy4_31\n"
      ".Lbk_dy4_31:\n"
      "buffer_load_dword v20, v2, s[8:11], s36 offen sc1\n"
      "s_cmp_lt_u32 s41, 32\n"
      "s_cselect_b32 s56, s39, 0x40000000\n"
      "ds_read_b128 v[56:59], v23 offset:0\n"
      "ds_read_b128 v[72:75], v23 offset:4096\n"
      "ds_read_b128 v[68:71], v23 offset:3072\n"
      "ds_read_b128 v[76:79], v23 offset:5120\n"
      "ds_read_b128 v[80:83], v23 offset:6144\n"
      "ds_read_b128 v[64:67], v23 offset:2048\n"
      "ds_read_b128 v[88:91], v23 offset:8192\n"
      "s_add_u32 s58, s41, 4294967264\n"
      "ds_read_b128 v[60:63], v23 offset:1024\n"
      "s_add_u32 s37, s37, 106496\n"
      "ds_read_b128 v[100:103], v23 offset:11264\n"
      "ds_read_b128 v[96:99], v23 offset:10240\n"
      "ds_read_b128 v[116:119], v23 offset:15360\n"
      "ds_read_b128 v[112:115], v23 offset:14336\n"
      "ds_read_b128 v[104:107], v23 offset:12288\n"
      "ds_read_b128 v[84:87], v23 offset:7168\n"
      "ds_read_b128 v[120:123], v23 offset:16384\n"
      "s_waitcnt vmcnt(0) lgkmcnt(0)\n"
      "s_nop 4\n"
      "s_branch .Ld_t4\n"
      ".Ld_in5:\n"
      "s_add_u32 s36, s36, 165888\n"
      "s_min_u32 s36, s36, s6\n"
      "s_mov_b32 s51, s36\n"
      "s_add_u32 s36, s36, 165888\n"
      "s_min_u32 s36, s36, s6\n"
      "s_mov_b32 s51, s36\n"
      "s_add_u32 s36, s36, 165888\n"
      "s_min_u32 s36, s36, s6\n"
      "s_add_u32 s38, s38, 147456\n"
      "s_mov_b32 s51, s36\n"
      "s_add_u32 s36, s36, 165888\n"
      "s_min_u32 s36, s36, s6\n"
      "s_add_u32 s52, s52, 106496\n"
      "s_mov_b32 s51, s36\n"
      "s_add_u32 s37, s37, 173056\n"
      "s_add_u32 s36, s36, 165888\n"
      "s_min_u32 s36, s36, s6\n"
      "s_add_u32 s52, s52, 173056\n"
      "s_mov_b32 s51, s36\n"
      "s_add_u32 s36, s36, 165888\n"
      "s_add_u32 s37, s37, 106496\n"
      "s_min_u32 s36, s36, s6\n"
      "buffer_load_dword v20, v2, s[8:11], s36 offen sc1\n"
      "s_mov_b32 s51, s36\n"
      "s_add_u32 s36, s36, 165888\n"
      "s_add_u32 m0, s43, 122880\n"
      "s_min_u32 s36, s36, s6\n"
      "s_add_u32 s52, s52, 106496\n"
      "s_waitcnt vmcnt(0)\n"
      "v_cmp_ne_u32 vcc, s21, v20\n"
      "s_cbranch_vccnz .Lsp_d5_32\n"
      ".Lbk_d5_32:\n"
      "buffer_load_dwordx4 v0, s[8:11], s51 offen offset:2048 sc1 lds\n"
      "buffer_load_dwordx4 v0, s[8:11], s51 offen offset:3072 sc1 lds\n"
      "buffer_load_dwordx4 v0, s[8:11], s51 offen offset:0 sc1 lds\n"
      "buffer_load_dwordx4 v0, s[8:11], s51 offen offset:1024 sc1 lds\n"
      "s_add_u32 m0, s43, 126976\n"
      "s_nop 0\n"
      "buffer_load_dwordx4 v1, s[8:11], s51 offen offset:0 sc1 lds\n"
      "buffer_load_dwordx4 v1, s[8:11], s51 offen offset:1024 sc1 lds\n"
      "buffer_load_dword v20, v2, s[8:11], s36 offen sc1\n"
      "s_cmp_lt_u32 s41, 32\n"
      "s_cselect_b32 s56, s39, 0x40000000\n"
      "s_add_u32 s58, s41, 4294967264\n"
      "s_add_u32 s37, s37, 106496\n"
      "s_mov_b32 s51, s36\n"
      "s_add_u32 m0, s43, 0\n"
      "s_add_u32 s36, s36, 165888\n"
      "v_add_u32 v14, 1, v14\n"
      "s_cmp_lt_u32 s58, 32\n"
      "s_cselect_b32 s57, s40, 0x40000000\n"
      "s_add_u32 s53, s53, 599040\n"
      "s_add_u32 s52, s52, 106496\n"
      "s_waitcnt vmcnt(0)\n"
      "v_cmp_ne_u32 vcc, s21, v20\n"
      "ds_write_b32 v16, v14\n"
      "s_cbranch_vccnz .Lsp_d0_33\n"
      ".Lbk_d0_33:\n"
      "buffer_load_dwordx4 v0, s[8:11], s51 offen offset:1024 sc1 lds\n"
      "ds_read_b32 v15, v17\n"
      "buffer_load_dwordx4 v0, s[8:11], s51 offen offset:0 sc1 lds\n"
      "s_waitcnt lgkmcnt(0)\n"
      "v_cmp_gt_i32 vcc, v14, v15\n"
      "buffer_load_dwordx4 v0, s[8:11], s51 offen offset:3072 sc1 lds\n"
      "s_add_u32 s39, s39, 147456\n"
      "buffer_load_dwordx4 v0, s[8:11], s51 offen offset:2048 sc1 lds\n"
      "s_min_u32 s36, s36, s6\n"
      "s_cbranch_vccnz .Lsp_dy5_34\n"
      ".Lbk_dy5_34:\n"
      "ds_read_b128 v[60:63], v23 offset:25600\n"
      "s_add_u32 m0, s43, 4096\n"
      "ds_read_b128 v[64:67], v23 offset:26624\n"
      "buffer_load_dwordx4 v1, s[8:11], s51 offen offset:0 sc1 lds\n"
      "ds_read_b128 v[76:79], v23 offset:29696\n"
      "s_add_u32 s37, s37, 106496\n"
      "ds_read_b128 v[72:75], v23 offset:28672\n"
      "buffer_load_dwordx4 v1, s[8:11], s51 offen offset:1024 sc1 lds\n"
      "ds_read_b128 v[92:95], v23 offset:33792\n"
      "ds_read_b128 v[80:83], v23 offset:30720\n"
      "ds_read_b128 v[84:87], v23 offset:31744\n"
      "buffer_load_dword v20, v2, s[8:11], s36 offen sc1\n"
      "ds_read_b128 v[88:91], v23 offset:32768\n"
      "ds_read_b128 v[56:59], v23 offset:24576\n"
      "ds_read_b128 v[68:71], v23 offset:27648\n"
      "ds_read_b128 v[108:111], v23 offset:37888\n"
      "ds_read_b128 v[112:115], v23 offset:38912\n"
      "ds_read_b128 v[116:119], v23 offset:39936\n"
      "ds_read_b128 v[128:131], v23 offset:43008\n"
      "ds_read_b128 v[124:127], v23 offset:41984\n"
      "ds_read_b128 v[96:99], v23 offset:34816\n"
      "s_waitcnt vmcnt(0) lgkmcnt(0)\n"
      "s_nop 4\n"
      "s_branch .Ld_t5\n"
      ".Ld_end:\n"
      "s_endpgm\n"
      ".Lsp_d0_1:\n"
      "s_mov_b32 s22, s23\n"
      ".Lsl_d0_1:\n"
      "buffer_load_dword v20, v2, s[8:11], s51 offen sc1\n"
      "s_waitcnt vmcnt(0)\n"
      "v_cmp_ne_u32 vcc, s21, v20\n"
      "s_cbranch_vccz .Lbk_d0_1\n"
      "s_sleep 4\n"
      "s_sub_u32 s22, s22, 1\n"
      "s_cbranch_scc0 .Lsl_d0_1\n"
      "s_mov_b32 s23, 0\n"
      "s_branch .Lbk_d0_1\n"
      ".Lsp_d1_2:\n"
      "s_mov_b32 s22, s23\n"
      ".Lsl_d1_2:\n"
      "buffer_load_dword v20, v2, s[8:11], s51 offen sc1\n"
      "s_waitcnt vmcnt(0)\n"
      "v_cmp_ne_u32 vcc, s21, v20\n"
      "s_cbranch_vccz .Lbk_d1_2\n"
      "s_sleep 4\n"
      "s_sub_u32 s22, s22, 1\n"
      "s_cbranch_scc0 .Lsl_d1_2\n"
      "s_mov_b32 s23, 0\n"
      "s_branch .Lbk_d1_2\n"
      ".Lsp_dy0_3:\n"
      "s_lshl_b32 s22, s23, 4\n"
      ".Lsl_dy0_3:\n"
      "ds_read_b32 v15, v17\n"
      "s_waitcnt lgkmcnt(0)\n"
      "v_cmp_gt_i32 vcc, v14, v15\n"
      "s_cbranch_vccz .Lbk_dy0_3\n"
      "s_sub_u32 s22, s22, 1\n"
      "s_cbranch_scc0 .Lsl_dy0_3\n"
      "s_mov_b32 s23, 0\n"
      "s_branch .Lbk_dy0_3\n"
      ".Lsp_d2_4:\n"
      "s_mov_b32 s22, s23\n"
      "buffer_load_dword v20, v2, s[8:11], s51 offen sc1\n"
      "s_waitcnt vmcnt(0)\n"
      "v_cmp_ne_u32 vcc, s21, v20\n"
      "s_add_u32 s30, s52, 106496\n"
      "buffer_store_dword v18, v4, s[8:11], s30 offen sc1\n"
      "buffer_store_dword v18, v5, s[8:11], s56 offen sc1\n"
      "s_cbranch_vccz .Lbk_d2_4\n"
      ".Lsl_d2_4:\n"
      "buffer_load_dword v20, v2, s[8:11], s51 offen sc1\n"
      "s_waitcnt vmcnt(0)\n"
      "v_cmp_ne_u32 vcc, s21, v20\n"
      "s_cbranch_vccz .Lbk_d2_4\n"
      "s_sleep 4\n"
      "s_sub_u32 s22, s22, 1\n"
      "s_cbranch_scc0 .Lsl_d2_4\n"
      "s_mov_b32 s23, 0\n"
      "s_branch .Lbk_d2_4\n"
      ".Lsp_dy1_5:\n"
      "s_lshl_b32 s22, s23, 4\n"
      ".Lsl_dy1_5:\n"
      "ds_read_b32 v15, v17\n"
      "s_waitcnt lgkmcnt(0)\n"
      "v_cmp_gt_i32 vcc, v14, v15\n"
      "s_cbranch_vccz .Lbk_dy1_5\n"
      "s_sub_u32 s22, s22, 1\n"
      "s_cbranch_scc0 .Lsl_dy1_5\n"
      "s_mov_b32 s23, 0\n"
      "s_branch .Lbk_dy1_5\n"
      ".Lsp_d3_6:\n"
      "s_mov_b32 s22, s23\n"
      ".Lsl_d3_6:\n"
      "buffer_load_dword v20, v2, s[8:11], s51 offen sc1\n"
      "s_waitcnt vmcnt(0)\n"
      "v_cmp_ne_u32 vcc, s21, v20\n"
      "s_cbranch_vccz .Lbk_d3_6\n"
      "s_sleep 4\n"
      "s_sub_u32 s22, s22, 1\n"
      "s_cbranch_scc0 .Lsl_d3_6\n"
      "s_mov_b32 s23, 0\n"
      "s_branch .Lbk_d3_6\n"
      ".Lsp_dy2_7:\n"
      "s_lshl_b32 s22, s23, 4\n"
      ".Lsl_dy2_7:\n"
      "ds_read_b32 v15, v17\n"
      "s_waitcnt lgkmcnt(0)\n"
      "v_cmp_gt_i32 vcc, v14, v15\n"
      "s_cbranch_vccz .Lbk_dy2_7\n"
      "s_sub_u32 s22, s22, 1\n"
      "s_cbranch_scc0 .Lsl_dy2_7\n"
      "s_mov_b32 s23, 0\n"
      "s_branch .Lbk_dy2_7\n"
      ".Lsp_d4_8:\n"
      "s_mov_b32 s22, s23\n"
      "buffer_load_dword v20, v2, s[8:11], s51 offen sc1\n"
      "s_waitcnt vmcnt(0)\n"
      "v_cmp_ne_u32 vcc, s21, v20\n"
      "s_add_u32 s30, s52, 0\n"
      "buffer_store_dword v18, v4, s[8:11], s30 offen sc1\n"
      "s_cbranch_vccz .Lbk_d4_8\n"
      ".Lsl_d4_8:\n"
      "buffer_load_dword v20, v2, s[8:11], s51 offen sc1\n"
      "s_waitcnt vmcnt(0)\n"
      "v_cmp_ne_u32 vcc, s21, v20\n"
      "s_cbranch_vccz .Lbk_d4_8\n"
      "s_sleep 4\n"
      "s_sub_u32 s22, s22, 1\n"
      "s_cbranch_scc0 .Lsl_d4_8\n"
      "s_mov_b32 s23, 0\n"
      "s_branch .Lbk_d4_8\n"
      ".Lsp_dy3_9:\n"
      "s_lshl_b32 s22, s23, 4\n"
      ".Lsl_dy3_9:\n"
      "ds_read_b32 v15, v17\n"
      "s_waitcnt lgkmcnt(0)\n"
      "v_cmp_gt_i32 vcc, v14, v15\n"
      "s_cbranch_vccz .Lbk_dy3_9\n"
      "s_sub_u32 s22, s22, 1\n"
      "s_cbranch_scc0 .Lsl_dy3_9\n"
      "s_mov_b32 s23, 0\n"
      "s_branch .Lbk_dy3_9\n"
      ".Lsp_d5_10:\n"
      "s_mov_b32 s22, s23\n"
      "buffer_load_dword v20, v2, s[8:11], s51 offen sc1\n"
      "s_waitcnt vmcnt(0)\n"
      "v_cmp_ne_u32 vcc, s21, v20\n"
      "s_add_u32 s30, s52, 0\n"
      "buffer_store_dword v18, v4, s[8:11], s30 offen sc1\n"
      "s_cbranch_vccz .Lbk_d5_10\n"
      ".Lsl_d5_10:\n"
      "buffer_load_dword v20, v2, s[8:11], s51 offen sc1\n"
      "s_waitcnt vmcnt(0)\n"
      "v_cmp_ne_u32 vcc, s21, v20\n"
      "s_cbranch_vccz .Lbk_d5_10\n"
      "s_sleep 4\n"
      "s_sub_u32 s22, s22, 1\n"
      "s_cbranch_scc0 .Lsl_d5_10\n"
      "s_mov_b32 s23, 0\n"
      "s_branch .Lbk_d5_10\n"
      ".Lsp_dy4_11:\n"
      "s_lshl_b32 s22, s23, 4\n"
      ".Lsl_dy4_11:\n"
      "ds_read_b32 v15, v17\n"
      "s_waitcnt lgkmcnt(0)\n"
      "v_cmp_gt_i32 vcc, v14, v15\n"
      "s_cbranch_vccz .Lbk_dy4_11\n"
      "s_sub_u32 s22, s22, 1\n"
      "s_cbranch_scc0 .Lsl_dy4_11\n"
      "s_mov_b32 s23, 0\n"
      "s_branch .Lbk_dy4_11\n"
      ".Lsp_df_12:\n"
      "s_mov_b32 s22, s23\n"
      "buffer_load_dword v21, v3, s[8:11], s53 offen sc1\n"
      "s_waitcnt vmcnt(0)\n"
      "v_cmp_ne_u32 vcc, s21, v21\n"
      "s_add_u32 s30, s52, 0\n"
      "buffer_store_dword v18, v4, s[8:11], s30 offen sc1\n"
      "s_cbranch_vccz .Lbk_df_12\n"
      ".Lsl_df_12:\n"
      "buffer_load_dword v21, v3, s[8:11], s53 offen sc1\n"
      "s_waitcnt vmcnt(0)\n"
      "v_cmp_ne_u32 vcc, s21, v21\n"
      "s_cbranch_vccz .Lbk_df_12\n"
      "s_sleep 4\n"
      "s_sub_u32 s22, s22, 1\n"
      "s_cbranch_scc0 .Lsl_df_12\n"
      "s_mov_b32 s23, 0\n"
      "s_branch .Lbk_df_12\n"
      ".Lsp_d0_13:\n"
      "s_mov_b32 s22, s23\n"
      "buffer_load_dword v20, v2, s[8:11], s51 offen sc1\n"
      "s_waitcnt vmcnt(0)\n"
      "v_cmp_ne_u32 vcc, s21, v20\n"
      "s_add_u32 s30, s52, 0\n"
      "buffer_store_dword v18, v4, s[8:11], s30 offen sc1\n"
      "s_cbranch_vccz .Lbk_d0_13\n"
      ".Lsl_d0_13:\n"
      "buffer_load_dword v20, v2, s[8:11], s51 offen sc1\n"
      "s_waitcnt vmcnt(0)\n"
      "v_cmp_ne_u32 vcc, s21, v20\n"
      "s_cbranch_vccz .Lbk_d0_13\n"
      "s_sleep 4\n"
      "s_sub_u32 s22, s22, 1\n"
      "s_cbranch_scc0 .Lsl_d0_13\n"
      "s_mov_b32 s23, 0\n"
      "s_branch .Lbk_d0_13\n"
      ".Lsp_dy5_14:\n"
      "s_lshl_b32 s22, s23, 4\n"
      ".Lsl_dy5_14:\n"
      "ds_read_b32 v15, v17\n"
      "s_waitcnt lgkmcnt(0)\n"
      "v_cmp_gt_i32 vcc, v14, v15\n"
      "s_cbranch_vccz .Lbk_dy5_14\n"
      "s_sub_u32 s22, s22, 1\n"
      "s_cbranch_scc0 .Lsl_dy5_14\n"
      "s_mov_b32 s23, 0\n"
      "s_branch .Lbk_dy5_14\n"
      ".Lsp_d1_15:\n"
      "s_mov_b32 s22, s23\n"
      "buffer_load_dword v20, v2, s[8:11], s51 offen sc1\n"
      "s_waitcnt vmcnt(0)\n"
      "v_cmp_ne_u32 vcc, s21, v20\n"
      "s_add_u32 s30, s52, 0\n"
      "buffer_store_dword v18, v4, s[8:11], s30 offen sc1\n"
      "s_cbranch_vccz .Lbk_d1_15\n"
      ".Lsl_d1_15:\n"
      "buffer_load_dword v20, v2, s[8:11], s51 offen sc1\n"
      "s_waitcnt vmcnt(0)\n"
      "v_cmp_ne_u32 vcc, s21, v20\n"
      "s_cbranch_vccz .Lbk_d1_15\n"
      "s_sleep 4\n"
      "s_sub_u32 s22, s22, 1\n"
      "s_cbranch_scc0 .Lsl_d1_15\n"
      "s_mov_b32 s23, 0\n"
      "s_branch .Lbk_d1_15\n"
      ".Lsp_dy0_16:\n"
      "s_lshl_b32 s22, s23, 4\n"
      ".Lsl_dy0_16:\n"
      "ds_read_b32 v15, v17\n"
      "s_waitcnt lgkmcnt(0)\n"
      "v_cmp_gt_i32 vcc, v14, v15\n"
      "s_cbranch_vccz .Lbk_dy0_16\n"
      "s_sub_u32 s22, s22, 1\n"
      "s_cbranch_scc0 .Lsl_dy0_16\n"
      "s_mov_b32 s23, 0\n"
      "s_branch .Lbk_dy0_16\n"
      ".Lsp_df_17:\n"
      "s_mov_b32 s22, s23\n"
      "buffer_load_dword v21, v3, s[8:11], s53 offen sc1\n"
      "s_waitcnt vmcnt(0)\n"
      "v_cmp_ne_u32 vcc, s21, v21\n"
      "s_add_u32 s30, s52, 0\n"
      "buffer_store_dword v18, v4, s[8:11], s30 offen sc1\n"
      "s_cbranch_vccz .Lbk_df_17\n"
      ".Lsl_df_17:\n"
      "buffer_load_dword v21, v3, s[8:11], s53 offen sc1\n"
      "s_waitcnt vmcnt(0)\n"
      "v_cmp_ne_u32 vcc, s21, v21\n"
      "s_cbranch_vccz .Lbk_df_17\n"
      "s_sleep 4\n"
      "s_sub_u32 s22, s22, 1\n"
      "s_cbranch_scc0 .Lsl_df_17\n"
      "s_mov_b32 s23, 0\n"
      "s_branch .Lbk_df_17\n"
      ".Lsp_df_18:\n"
      "s_mov_b32 s22, s23\n"
      "buffer_load_dword v21, v3, s[8:11], s53 offen sc1\n"
      "s_waitcnt vmcnt(0)\n"
      "v_cmp_ne_u32 vcc, s21, v21\n"
      "s_add_u32 s30, s52, 0\n"
      "buffer_store_dword v18, v4, s[8:11], s30 offen sc1\n"
      "s_cbranch_vccz .Lbk_df_18\n"
      ".Lsl_df_18:\n"
      "buffer_load_dword v21, v3, s[8:11], s53 offen sc1\n"
      "s_waitcnt vmcnt(0)\n"
      "v_cmp_ne_u32 vcc, s21, v21\n"
      "s_cbranch_vccz .Lbk_df_18\n"
      "s_sleep 4\n"
      "s_sub_u32 s22, s22, 1\n"
      "s_cbranch_scc0 .Lsl_df_18\n"
      "s_mov_b32 s23, 0\n"
      "s_branch .Lbk_df_18\n"
      ".Lsp_df_19:\n"
      "s_mov_b32 s22, s23\n"
      "buffer_load_dword v21, v3, s[8:11], s53 offen sc1\n"
      "s_waitcnt vmcnt(0)\n"
      "v_cmp_ne_u32 vcc, s21, v21\n"
      "s_add_u32 s30, s52, 0\n"
      "buffer_store_dword v18, v4, s[8:11], s30 offen sc1\n"
      "s_cbranch_vccz .Lbk_df_19\n"
      ".Lsl_df_19:\n"
      "buffer_load_dword v21, v3, s[8:11], s53 offen sc1\n"
      "s_waitcnt vmcnt(0)\n"
      "v_cmp_ne_u32 vcc, s21, v21\n"
      "s_cbranch_vccz .Lbk_df_19\n"
      "s_sleep 4\n"
      "s_sub_u32 s22, s22, 1\n"
      "s_cbranch_scc0 .Lsl_df_19\n"
      "s_mov_b32 s23, 0\n"
      "s_branch .Lbk_df_19\n"
      ".Lsp_d1_20:\n"
      "s_mov_b32 s22, s23\n"
      ".Lsl_d1_20:\n"
      "buffer_load_dword v20, v2, s[8:11], s51 offen sc1\n"
      "s_waitcnt vmcnt(0)\n"
      "v_cmp_ne_u32 vcc, s21, v20\n"
      "s_cbranch_vccz .Lbk_d1_20\n"
      "s_sleep 4\n"
      "s_sub_u32 s22, s22, 1\n"
      "s_cbranch_scc0 .Lsl_d1_20\n"
      "s_mov_b32 s23, 0\n"
      "s_branch .Lbk_d1_20\n"
      ".Lsp_d2_21:\n"
      "s_mov_b32 s22, s23\n"
      ".Lsl_d2_21:\n"
      "buffer_load_dword v20, v2, s[8:11], s51 offen sc1\n"
      "s_waitcnt vmcnt(0)\n"
      "v_cmp_ne_u32 vcc, s21, v20\n"
      "s_cbranch_vccz .Lbk_d2_21\n"
      "s_sleep 4\n"
      "s_sub_u32 s22, s22, 1\n"
      "s_cbranch_scc0 .Lsl_d2_21\n"
      "s_mov_b32 s23, 0\n"
      "s_branch .Lbk_d2_21\n"
      ".Lsp_dy1_22:\n"
      "s_lshl_b32 s22, s23, 4\n"
      ".Lsl_dy1_22:\n"
      "ds_read_b32 v15, v17\n"
      "s_waitcnt lgkmcnt(0)\n"
      "v_cmp_gt_i32 vcc, v14, v15\n"
      "s_cbranch_vccz .Lbk_dy1_22\n"
      "s_sub_u32 s22, s22, 1\n"
      "s_cbranch_scc0 .Lsl_dy1_22\n"
      "s_mov_b32 s23, 0\n"
      "s_branch .Lbk_dy1_22\n"
      ".Lsp_d2_23:\n"
      "s_mov_b32 s22, s23\n"
      ".Lsl_d2_23:\n"
      "buffer_load_dword v20, v2, s[8:11], s51 offen sc1\n"
      "s_waitcnt vmcnt(0)\n"
      "v_cmp_ne_u32 vcc, s21, v20\n"
      "s_cbranch_vccz .Lbk_d2_23\n"
      "s_sleep 4\n"
      "s_sub_u32 s22, s22, 1\n"
      "s_cbranch_scc0 .Lsl_d2_23\n"
      "s_mov_b32 s23, 0\n"
      "s_branch .Lbk_d2_23\n"
      ".Lsp_d3_24:\n"
      "s_mov_b32 s22, s23\n"
      ".Lsl_d3_24:\n"
      "buffer_load_dword v20, v2, s[8:11], s51 offen sc1\n"
      "s_waitcnt vmcnt(0)\n"
      "v_cmp_ne_u32 vcc, s21, v20\n"
      "s_cbranch_vccz .Lbk_d3_24\n"
      "s_sleep 4\n"
      "s_sub_u32 s22, s22, 1\n"
      "s_cbranch_scc0 .Lsl_d3_24\n"
      "s_mov_b32 s23, 0\n"
      "s_branch .Lbk_d3_24\n"
      ".Lsp_dy2_25:\n"
      "s_lshl_b32 s22, s23, 4\n"
      ".Lsl_dy2_25:\n"
      "ds_read_b32 v15, v17\n"
      "s_waitcnt lgkmcnt(0)\n"
      "v_cmp_gt_i32 vcc, v14, v15\n"
      "s_cbranch_vccz .Lbk_dy2_25\n"
      "s_sub_u32 s22, s22, 1\n"
      "s_cbranch_scc0 .Lsl_dy2_25\n"
      "s_mov_b32 s23, 0\n"
      "s_branch .Lbk_dy2_25\n"
      ".Lsp_d3_26:\n"
      "s_mov_b32 s22, s23\n"
      ".Lsl_d3_26:\n"
      "buffer_load_dword v20, v2, s[8:11], s51 offen sc1\n"
      "s_waitcnt vmcnt(0)\n"
      "v_cmp_ne_u32 vcc, s21, v20\n"
      "s_cbranch_vccz .Lbk_d3_26\n"
      "s_sleep 4\n"
      "s_sub_u32 s22, s22, 1\n"
      "s_cbranch_scc0 .Lsl_d3_26\n"
      "s_mov_b32 s23, 0\n"
      "s_branch .Lbk_d3_26\n"
      ".Lsp_d4_27:\n"
      "s_mov_b32 s22, s23\n"
      ".Lsl_d4_27:\n"
      "buffer_load_dword v20, v2, s[8:11], s51 offen sc1\n"
      "s_waitcnt vmcnt(0)\n"
      "v_cmp_ne_u32 vcc, s21, v20\n"
      "s_cbranch_vccz .Lbk_d4_27\n"
      "s_sleep 4\n"
      "s_sub_u32 s22, s22, 1\n"
      "s_cbranch_scc0 .Lsl_d4_27\n"
      "s_mov_b32 s23, 0\n"
      "s_branch .Lbk_d4_27\n"
      ".Lsp_dy3_28:\n"
      "s_lshl_b32 s22, s23, 4\n"
      ".Lsl_dy3_28:\n"
      "ds_read_b32 v15, v17\n"
      "s_waitcnt lgkmcnt(0)\n"
      "v_cmp_gt_i32 vcc, v14, v15\n"
      "s_cbranch_vccz .Lbk_dy3_28\n"
      "s_sub_u32 s22, s22, 1\n"
      "s_cbranch_scc0 .Lsl_dy3_28\n"
      "s_mov_b32 s23, 0\n"
      "s_branch .Lbk_dy3_28\n"
      ".Lsp_d4_29:\n"
      "s_mov_b32 s22, s23\n"
      ".Lsl_d4_29:\n"
      "buffer_load_dword v20, v2, s[8:11], s51 offen sc1\n"
      "s_waitcnt vmcnt(0)\n"
      "v_cmp_ne_u32 vcc, s21, v20\n"
      "s_cbranch_vccz .Lbk_d4_29\n"
      "s_sleep 4\n"
      "s_sub_u32 s22, s22, 1\n"
      "s_cbranch_scc0 .Lsl_d4_29\n"
      "s_mov_b32 s23, 0\n"
      "s_branch .Lbk_d4_29\n"
      ".Lsp_d5_30:\n"
      "s_mov_b32 s22, s23\n"
      ".Lsl_d5_30:\n"
      "buffer_load_dword v20, v2, s[8:11], s51 offen sc1\n"
      "s_waitcnt vmcnt(0)\n"
      "v_cmp_ne_u32 vcc, s21, v20\n"
      "s_cbranch_vccz .Lbk_d5_30\n"
      "s_sleep 4\n"
      "s_sub_u32 s22, s22, 1\n"
      "s_cbranch_scc0 .Lsl_d5_30\n"
      "s_mov_b32 s23, 0\n"
      "s_branch .Lbk_d5_30\n"
      ".Lsp_dy4_31:\n"
      "s_lshl_b32 s22, s23, 4\n"
      ".Lsl_dy4_31:\n"
      "ds_read_b32 v15, v17\n"
      "s_waitcnt lgkmcnt(0)\n"
      "v_cmp_gt_i32 vcc, v14, v15\n"
      "s_cbranch_vccz .Lbk_dy4_31\n"
      "s_sub_u32 s22, s22, 1\n"
      "s_cbranch_scc0 .Lsl_dy4_31\n"
      "s_mov_b32 s23, 0\n"
      "s_branch .Lbk_dy4_31\n"
      ".Lsp_d5_32:\n"
      "s_mov_b32 s22, s23\n"
      ".Lsl_d5_32:\n"
      "buffer_load_dword v20, v2, s[8:11], s51 offen sc1\n"
      "s_waitcnt vmcnt(0)\n"
      "v_cmp_ne_u32 vcc, s21, v20\n"
      "s_cbranch_vccz .Lbk_d5_32\n"
      "s_sleep 4\n"
      "s_sub_u32 s22, s22, 1\n"
      "s_cbranch_scc0 .Lsl_d5_32\n"
      "s_mov_b32 s23, 0\n"
      "s_branch .Lbk_d5_32\n"
      ".Lsp_d0_33:\n"
      "s_mov_b32 s22, s23\n"
      ".Lsl_d0_33:\n"
      "buffer_load_dword v20, v2, s[8:11], s51 offen sc1\n"
      "s_waitcnt vmcnt(0)\n"
      "v_cmp_ne_u32 vcc, s21, v20\n"
      "s_cbranch_vccz .Lbk_d0_33\n"
      "s_sleep 4\n"
      "s_sub_u32 s22, s22, 1\n"
      "s_cbranch_scc0 .Lsl_d0_33\n"
      "s_mov_b32 s23, 0\n"
      "s_branch .Lbk_d0_33\n"
      ".Lsp_dy5_34:\n"
      "s_lshl_b32 s22, s23, 4\n"
      ".Lsl_dy5_34:\n"
      "ds_read_b32 v15, v17\n"
      "s_waitcnt lgkmcnt(0)\n"
      "v_cmp_gt_i32 vcc, v14, v15\n"
      "s_cbranch_vccz .Lbk_dy5_34\n"
      "s_sub_u32 s22, s22, 1\n"
      "s_cbranch_scc0 .Lsl_dy5_34\n"
      "s_mov_b32 s23, 0\n"
      "s_branch .Lbk_dy5_34\n"
      ".Ld_last:\n"
      :
      : "{s[8:11]}"(rsWS), "{s[12:15]}"(rsA), "{s[16:19]}"(rsB), "{s[92:95]}"(rsC), "{v0}"(v_l16), "{v1}"(v_l16b), "{v2}"(v_fa), "{v3}"(v_ga), "{v4}"(v_ra), "{v5}"(v_rf), "{v6}"(v_xp), "{v7}"(v_xs), "{v8}"(v_op), "{v9}"(v_os), "{v10}"(v_sw), "{v11}"(v_sr), "{v12}"(v_ss), "{v13}"(v_l8), "{v15}"(v_al), "{v16}"(v_yw), "{v17}"(v_yr), "{v19}"(v_hi), "{v176}"(v_at), "{v177}"(v_as), "{v178}"(v_vo0), "{v179}"(v_vo1), "{v180}"(v_vo2), "{v181}"(v_pw), "{v183}"(v_pu), "{s36}"(s_sh), "{s37}"(s_ps), "{s38}"(s_x1), "{s39}"(s_xr), "{s40}"(s_or), "{s41}"(s_p0), "{s42}"(s_m3), "{s43}"(s_m6), "{s44}"(s_so), "{s45}"(s_ph), "{s46}"(s_i0), "{s47}"(s_i5), "{s48}"(s_j0), "{s49}"(s_j5), "{s50}"(s_he), "{s60}"(s_k0)
      : "v255", "a255", "s99", "vcc", "scc", "memory");

  } else {
    const unsigned tr = xcd * 3 + (slot - 14) / 6, j = 6 * unit_ks(tr) + (slot - 14) % 6, gr = unit_gr(tr), f = 4 * j + w;
    const u32x4 rsA = srd(w >> 1 ? Wu : Wg, 576 * 4608 * 2), rsB = srd(Wqkv, 576 * 1728 * 2), rsC = srd(Wo, 576 * 576 * 2);
    const unsigned wv = (slot - 14) * 4 + w, hf = (xcd >> 2) * 512;
    const unsigned tq2 = i16 < 15 ? (8 * wv + g) * 3456 + i16 / 5 * 1152 + hf + i16 % 5 * 128 : OOB;
    const unsigned tq3 = (lane & 7) < 6 ? (8 * wv + l8) * 3456 + (lane >> 1 & 3) * 1152 + xcd * 128 + (lane & 1) * 128 : OOB;
    const unsigned v_tq[2] = {QMAP == 2 ? tq2 : tq3, QMAP == 2 ? tq2 + 4 * 3456 : OOB};
    const unsigned s_wv = wv < PSK ? 0 : (wv - PSK) * PCN / 16;
    const unsigned v_tw = (lane & 7) < 5 ? (8 * wv + l8) * 1152 + hf + (lane & 7) * 128 : OOB, v_td = (wv * 8 + (lane & 7)) * 128;
    const unsigned v_l5 = w < 3 ? v_l16 + 16384 - w * 3072 : OOB;
    const unsigned v_fa = 19456 - w * 4096 + (lane >> 3) * 128 + (lane & 7) * 4, v_ra = up * 8 * HREC + l5 + 147456 - f * 1024 + f * 128;
    const unsigned v_st = 18432 + i16 * 32 + q * 8, v_pw = 147456 - f * 1024 + f * 128 + l8 * HREC + l7;
    const unsigned v_at = 65536 + (8 * g + q) * 256 + ((4 * w + p) ^ (4 * q)) * 16;
    const unsigned v_vo0 = (16 * (w & 1) + g) * 9216 + 256 * j + (i16 ^ (4 * g)) * 16;
    const unsigned v_vo1 = v_vo0 + 4 * 9216 - 1024, v_vo2 = v_vo1 + 4 * 9216 - 1024, v_vo3 = v_vo2 + 4 * 9216 - 1024;
    const unsigned to0 = (4 * (72 * (w & 1) + (lane >> 1)) + gr) * 9216 + 256 * j + (lane & 1) * 128;
    const unsigned v_to[3] = {to0, to0 + 128 * 9216, lane < 16 ? to0 + 256 * 9216 : OOB};
    const unsigned s_sx = WS_X1 + gr * NREC * REC + w * 4096, s_sh = WS_H + gr * NREC * HREC + f * 1024;
    const unsigned s_m4 = w * 4096, s_m5 = 16384 + w * 1024, s_k0 = ROT * ((9 * gr + 1) >> 1);
    (void)s_k0;
    asm volatile(
      "s_buffer_load_dword s21, s[8:11], 0\n"
      "s_mov_b32 s23, 65536\n"
      "s_mov_b32 s31, 0x40000000\n"
      "s_mov_b32 s41, 0\n"
      "s_getpc_b64 s[96:97]\n"
      ".Lg_pc:\n"
      "s_sub_u32 s96, s96, .Lg_pc-k_all\n"
      "s_subb_u32 s97, s97, 0\n"
      "v_min_u32 v55, .Lq_last-k_all, v55\n"
      "global_load_dword v59, v55, s[96:97]\n"
      ".Lg_pace:\n"
      "s_sleep 1\n"
      "s_sub_u32 s43, s43, 1\n"
      "s_cbranch_scc0 .Lg_pace\n"
      "buffer_load_dword v56, v52, s[16:19], s41 offen\n"
      "buffer_load_dword v57, v53, s[16:19], s41 offen\n"
      "buffer_load_dword v58, v54, s[92:95], s41 offen\n"
      "s_waitcnt vmcnt(0)\n"
      "s_add_u32 m0, s38, 65536\n"
      "s_nop 0\n"
      "buffer_load_dwordx4 v6, s[12:15], s41 offen offset:0 lds\n"
      "buffer_load_dwordx4 v7, s[12:15], s41 offen offset:1024 lds\n"
      "buffer_load_dwordx4 v8, s[12:15], s41 offen offset:2048 lds\n"
      "buffer_load_dwordx4 v9, s[12:15], s41 offen offset:3072 lds\n"
      "s_add_u32 s41, s41, 294912\n"
      "s_add_u32 m0, s38, 81920\n"
      "s_nop 0\n"
      "buffer_load_dwordx4 v6, s[12:15], s41 offen offset:0 lds\n"
      "buffer_load_dwordx4 v7, s[12:15], s41 offen offset:1024 lds\n"
      "buffer_load_dwordx4 v8, s[12:15], s41 offen offset:2048 lds\n"
      "buffer_load_dwordx4 v9, s[12:15], s41 offen offset:3072 lds\n"
      "s_add_u32 s41, s41, 294912\n"
      "s_add_u32 m0, s38, 98304\n"
      "s_nop 0\n"
      "buffer_load_dwordx4 v6, s[12:15], s41 offen offset:0 lds\n"
      "buffer_load_dwordx4 v7, s[12:15], s41 offen offset:1024 lds\n"
      "buffer_load_dwordx4 v8, s[12:15], s41 offen offset:2048 lds\n"
      "buffer_load_dwordx4 v9, s[12:15], s41 offen offset:3072 lds\n"
      "s_add_u32 s41, s41, 294912\n"
      "s_add_u32 m0, s38, 114688\n"
      "s_nop 0\n"
      "buffer_load_dwordx4 v6, s[12:15], s41 offen offset:0 lds\n"
      "buffer_load_dwordx4 v7, s[12:15], s41 offen offset:1024 lds\n"
      "buffer_load_dwordx4 v8, s[12:15], s41 offen offset:2048 lds\n"
      "buffer_load_dwordx4 v9, s[12:15], s41 offen offset:3072 lds\n"
      "s_add_u32 s41, s41, 294912\n"
      "s_add_u32 m0, s38, 131072\n"
      "s_nop 0\n"
      "buffer_load_dwordx4 v6, s[12:15], s41 offen offset:0 lds\n"
      "buffer_load_dwordx4 v7, s[12:15], s41 offen offset:1024 lds\n"
      "buffer_load_dwordx4 v8, s[12:15], s41 offen offset:2048 lds\n"
      "buffer_load_dwordx4 v9, s[12:15], s41 offen offset:3072 lds\n"
      "s_add_u32 s41, s41, 294912\n"
      "v_mov_b32 v172, 0x3f803f80\n"
      "v_mov_b32 v173, 0x3f803f80\n"
      "v_mov_b32 v174, 0x3f803f80\n"
      "v_mov_b32 v175, 0x3f803f80\n"
      "v_add_u32 v19, 49152, v5\n"
      "s_waitcnt vmcnt(16)\n"
      "s_barrier\n"
      "ds_read_b64_tr_b16 a[0:1], v5 offset:0\n"
      "ds_read_b64_tr_b16 a[2:3], v5 offset:1024\n"
      "ds_read_b64_tr_b16 a[72:73], v5 offset:8192\n"
      "ds_read_b64_tr_b16 a[74:75], v5 offset:9216\n"
      "ds_read_b64_tr_b16 a[144:145], v5 offset:8\n"
      "ds_read_b64_tr_b16 a[146:147], v5 offset:1032\n"
      "ds_read_b64_tr_b16 a[216:217], v5 offset:8200\n"
      "ds_read_b64_tr_b16 a[218:219], v5 offset:9224\n"
      "s_add_u32 m0, s38, 147456\n"
      "s_nop 0\n"
      "buffer_load_dwordx4 v6, s[12:15], s41 offen offset:0 lds\n"
      "buffer_load_dwordx4 v7, s[12:15], s41 offen offset:1024 lds\n"
      "buffer_load_dwordx4 v8, s[12:15], s41 offen offset:2048 lds\n"
      "buffer_load_dwordx4 v9, s[12:15], s41 offen offset:3072 lds\n"
      "s_add_u32 s41, s41, 294912\n"
      "s_waitcnt vmcnt(16) lgkmcnt(0)\n"
      "s_barrier\n"
      "ds_read_b64_tr_b16 a[4:5], v5 offset:16384\n"
      "ds_read_b64_tr_b16 a[6:7], v5 offset:17408\n"
      "ds_read_b64_tr_b16 a[76:77], v5 offset:24576\n"
      "ds_read_b64_tr_b16 a[78:79], v5 offset:25600\n"
      "ds_read_b64_tr_b16 a[148:149], v5 offset:16392\n"
      "ds_read_b64_tr_b16 a[150:151], v5 offset:17416\n"
      "ds_read_b64_tr_b16 a[220:221], v5 offset:24584\n"
      "ds_read_b64_tr_b16 a[222:223], v5 offset:25608\n"
      "v_mfma_f32_16x16x32_bf16 v[140:143], a[0:3], v[172:175], 0\n"
      "v_mfma_f32_16x16x32_bf16 v[144:147], a[72:75], v[172:175], 0\n"
      "v_mfma_f32_16x16x32_bf16 v[148:151], a[144:147], v[172:175], 0\n"
      "v_mfma_f32_16x16x32_bf16 v[152:155], a[216:219], v[172:175], 0\n"
      "s_add_u32 m0, s38, 65536\n"
      "s_nop 0\n"
      "buffer_load_dwordx4 v6, s[12:15], s41 offen offset:0 lds\n"
      "buffer_load_dwordx4 v7, s[12:15], s41 offen offset:1024 lds\n"
      "buffer_load_dwordx4 v8, s[12:15], s41 offen offset:2048 lds\n"
      "buffer_load_dwordx4 v9, s[12:15], s41 offen offset:3072 lds\n"
      "s_add_u32 s41, s41, 294912\n"
      "s_waitcnt vmcnt(16) lgkmcnt(0)\n"
      "s_barrier\n"
      "ds_read_b64_tr_b16 a[8:9], v5 offset:32768\n"
      "ds_read_b64_tr_b16 a[10:11], v5 offset:33792\n"
      "ds_read_b64_tr_b16 a[80:81], v5 offset:40960\n"
      "ds_read_b64_tr_b16 a[82:83], v5 offset:41984\n"
      "ds_read_b64_tr_b16 a[152:153], v5 offset:32776\n"
      "ds_read_b64_tr_b16 a[154:155], v5 offset:33800\n"
      "ds_read_b64_tr_b16 a[224:225], v5 offset:40968\n"
      "ds_read_b64_tr_b16 a[226:227], v5 offset:41992\n"
      "v_mfma_f32_16x16x32_bf16 v[140:143], a[4:7], v[172:175], v[140:143]\n"
      "v_mfma_f32_16x16x32_bf16 v[144:147], a[76:79], v[172:175], v[144:147]\n"
      "v_mfma_f32_16x16x32_bf16 v[148:151], a[148:151], v[172:175], v[148:151]\n"
      "v_mfma_f32_16x16x32_bf16 v[152:155], a[220:223], v[172:175], v[152:155]\n"
      "s_add_u32 m0, s38, 81920\n"
      "s_nop 0\n"
      "buffer_load_dwordx4 v6, s[12:15], s41 offen offset:0 lds\n"
      "buffer_load_dwordx4 v7, s[12:15], s41 offen offset:1024 lds\n"
      "buffer_load_dwordx4 v8, s[12:15], s41 offen offset:2048 lds\n"
      "buffer_load_dwordx4 v9, s[12:15], s41 offen offset:3072 lds\n"
      "s_add_u32 s41, s41, 294912\n"
      "s_waitcnt vmcnt(16) lgkmcnt(0)\n"
      "s_barrier\n"
      "ds_read_b64_tr_b16 a[12:13], v19 offset:0\n"
      "ds_read_b64_tr_b16 a[14:15], v19 offset:1024\n"
      "ds_read_b64_tr_b16 a[84:85], v19 offset:8192\n"
      "ds_read_b64_tr_b16 a[86:87], v19 offset:9216\n"
      "ds_read_b64_tr_b16 a[156:157], v19 offset:8\n"
      "ds_read_b64_tr_b16 a[158:159], v19 offset:1032\n"
      "ds_read_b64_tr_b16 a[228:229], v19 offset:8200\n"
      "ds_read_b64_tr_b16 a[230:231], v19 offset:9224\n"
      "v_mfma_f32_16x16x32_bf16 v[140:143], a[8:11], v[172:175], v[140:143]\n"
      "v_mfma_f32_16x16x32_bf16 v[144:147], a[80:83], v[172:175], v[144:147]\n"
      "v_mfma_f32_16x16x32_bf16 v[148:151], a[152:155], v[172:175], v[148:151]\n"
      "v_mfma_f32_16x16x32_bf16 v[152:155], a[224:227], v[172:175], v[152:155]\n"
      "s_add_u32 m0, s38, 98304\n"
      "s_nop 0\n"
      "buffer_load_dwordx4 v6, s[12:15], s41 offen offset:0 lds\n"
      "buffer_load_dwordx4 v7, s[12:15], s41 offen offset:1024 lds\n"
      "buffer_load_dwordx4 v8, s[12:15], s41 offen offset:2048 lds\n"
      "buffer_load_dwordx4 v9, s[12:15], s41 offen offset:3072 lds\n"
      "s_add_u32 s41, s41, 294912\n"
      "s_waitcnt vmcnt(16) lgkmcnt(0)\n"
      "s_barrier\n"
      "ds_read_b64_tr_b16 a[16:17], v19 offset:16384\n"
      "ds_read_b64_tr_b16 a[18:19], v19 offset:17408\n"
      "ds_read_b64_tr_b16 a[88:89], v19 offset:24576\n"
      "ds_read_b64_tr_b16 a[90:91], v19 offset:25600\n"
      "ds_read_b64_tr_b16 a[160:161], v19 offset:16392\n"
      "ds_read_b64_tr_b16 a[162:163], v19 offset:17416\n"
      "ds_read_b64_tr_b16 a[232:233], v19 offset:24584\n"
      "ds_read_b64_tr_b16 a[234:235], v19 offset:25608\n"
      "v_mfma_f32_16x16x32_bf16 v[140:143], a[12:15], v[172:175], v[140:143]\n"
      "v_mfma_f32_16x16x32_bf16 v[144:147], a[84:87], v[172:175], v[144:147]\n"
      "v_mfma_f32_16x16x32_bf16 v[148:151], a[156:159], v[172:175], v[148:151]\n"
      "v_mfma_f32_16x16x32_bf16 v[152:155], a[228:231], v[172:175], v[152:155]\n"
      "s_add_u32 m0, s38, 114688\n"
      "s_nop 0\n"
      "buffer_load_dwordx4 v6, s[12:15], s41 offen offset:0 lds\n"
      "buffer_load_dwordx4 v7, s[12:15], s41 offen offset:1024 lds\n"
      "buffer_load_dwordx4 v8, s[12:15], s41 offen offset:2048 lds\n"
      "buffer_load_dwordx4 v9, s[12:15], s41 offen offset:3072 lds\n"
      "s_add_u32 s41, s41, 294912\n"
      "s_waitcnt vmcnt(16) lgkmcnt(0)\n"
      "s_barrier\n"
      "ds_read_b64_tr_b16 a[20:21], v19 offset:32768\n"
      "ds_read_b64_tr_b16 a[22:23], v19 offset:33792\n"
      "ds_read_b64_tr_b16 a[92:93], v19 offset:40960\n"
      "ds_read_b64_tr_b16 a[94:95], v19 offset:41984\n"
      "ds_read_b64_tr_b16 a[164:165], v19 offset:32776\n"
      "ds_read_b64_tr_b16 a[166:167], v19 offset:33800\n"
      "ds_read_b64_tr_b16 a[236:237], v19 offset:40968\n"
      "ds_read_b64_tr_b16 a[238:239], v19 offset:41992\n"
      "v_mfma_f32_16x16x32_bf16 v[140:143], a[16:19], v[172:175], v[140:143]\n"
      "v_mfma_f32_16x16x32_bf16 v[144:147], a[88:91], v[172:175], v[144:147]\n"
      "v_mfma_f32_16x16x32_bf16 v[148:151], a[160:163], v[172:175], v[148:151]\n"
      "v_mfma_f32_16x16x32_bf16 v[152:155], a[232:235], v[172:175], v[152:155]\n"
      "s_add_u32 m0, s38, 131072\n"
      "s_nop 0\n"
      "buffer_load_dwordx4 v6, s[12:15], s41 offen offset:0 lds\n"
      "buffer_load_dwordx4 v7, s[12:15], s41 offen offset:1024 lds\n"
      "buffer_load_dwordx4 v8, s[12:15], s41 offen offset:2048 lds\n"
      "buffer_load_dwordx4 v9, s[12:15], s41 offen offset:3072 lds\n"
      "s_add_u32 s41, s41, 294912\n"
      "s_waitcnt vmcnt(16) lgkmcnt(0)\n"
      "s_barrier\n"
      "ds_read_b64_tr_b16 a[24:25], v5 offset:0\n"
      "ds_read_b64_tr_b16 a[26:27], v5 offset:1024\n"
      "ds_read_b64_tr_b16 a[96:97], v5 offset:8192\n"
      "ds_read_b64_tr_b16 a[98:99], v5 offset:9216\n"
      "ds_read_b64_tr_b16 a[168:169], v5 offset:8\n"
      "ds_read_b64_tr_b16 a[170:171], v5 offset:1032\n"
      "ds_read_b64_tr_b16 a[240:241], v5 offset:8200\n"
      "ds_read_b64_tr_b16 a[242:243], v5 offset:9224\n"
      "v_mfma_f32_16x16x32_bf16 v[140:143], a[20:23], v[172:175], v[140:143]\n"
      "v_mfma_f32_16x16x32_bf16 v[144:147], a[92:95], v[172:175], v[144:147]\n"
      "v_mfma_f32_16x16x32_bf16 v[148:151], a[164:167], v[172:175], v[148:151]\n"
      "v_mfma_f32_16x16x32_bf16 v[152:155], a[236:239], v[172:175], v[152:155]\n"
      "s_add_u32 m0, s38, 147456\n"
      "s_nop 0\n"
      "buffer_load_dwordx4 v6, s[12:15], s41 offen offset:0 lds\n"
      "buffer_load_dwordx4 v7, s[12:15], s41 offen offset:1024 lds\n"
      "buffer_load_dwordx4 v8, s[12:15], s41 offen offset:2048 lds\n"
      "buffer_load_dwordx4 v9, s[12:15], s41 offen offset:3072 lds\n"
      "s_add_u32 s41, s41, 294912\n"
      "s_waitcnt vmcnt(16) lgkmcnt(0)\n"
      "s_barrier\n"
      "ds_read_b64_tr_b16 a[28:29], v5 offset:16384\n"
      "ds_read_b64_tr_b16 a[30:31], v5 offset:17408\n"
      "ds_read_b64_tr_b16 a[100:101], v5 offset:24576\n"
      "ds_read_b64_tr_b16 a[102:103], v5 offset:25600\n"
      "ds_read_b64_tr_b16 a[172:173], v5 offset:16392\n"
      "ds_read_b64_tr_b16 a[174:175], v5 offset:17416\n"
      "ds_read_b64_tr_b16 a[244:245], v5 offset:24584\n"
      "ds_read_b64_tr_b16 a[246:247], v5 offset:25608\n"
      "v_mfma_f32_16x16x32_bf16 v[140:143], a[24:27], v[172:175], v[140:143]\n"
      "v_mfma_f32_16x16x32_bf16 v[144:147], a[96:99], v[172:175], v[144:147]\n"
      "v_mfma_f32_16x16x32_bf16 v[148:151], a[168:171], v[172:175], v[148:151]\n"
      "v_mfma_f32_16x16x32_bf16 v[152:155], a[240:243], v[172:175], v[152:155]\n"
      "s_add_u32 m0, s38, 65536\n"
      "s_nop 0\n"
      "buffer_load_dwordx4 v6, s[12:15], s41 offen offset:0 lds\n"
      "buffer_load_dwordx4 v7, s[12:15], s41 offen offset:1024 lds\n"
      "buffer_load_dwordx4 v8, s[12:15], s41 offen offset:2048 lds\n"
      "buffer_load_dwordx4 v9, s[12:15], s41 offen offset:3072 lds\n"
      "s_add_u32 s41, s41, 294912\n"
      "s_waitcnt vmcnt(16) lgkmcnt(0)\n"
      "s_barrier\n"
      "ds_read_b64_tr_b16 a[32:33], v5 offset:32768\n"
      "ds_read_b64_tr_b16 a[34:35], v5 offset:33792\n"
      "ds_read_b64_tr_b16 a[104:105], v5 offset:40960\n"
      "ds_read_b64_tr_b16 a[106:107], v5 offset:41984\n"
      "ds_read_b64_tr_b16 a[176:177], v5 offset:32776\n"
      "ds_read_b64_tr_b16 a[178:179], v5 offset:33800\n"
      "ds_read_b64_tr_b16 a[248:249], v5 offset:40968\n"
      "ds_read_b64_tr_b16 a[250:251], v5 offset:41992\n"
      "v_mfma_f32_16x16x32_bf16 v[140:143], a[28:31], v[172:175], v[140:143]\n"
      "v_mfma_f32_16x16x32_bf16 v[144:147], a[100:103], v[172:175], v[144:147]\n"
      "v_mfma_f32_16x16x32_bf16 v[148:151], a[172:175], v[172:175], v[148:151]\n"
      "v_mfma_f32_16x16x32_bf16 v[152:155], a[244:247], v[172:175], v[152:155]\n"
      "s_add_u32 m0, s38, 81920\n"
      "s_nop 0\n"
      "buffer_load_dwordx4 v6, s[12:15], s41 offen offset:0 lds\n"
      "buffer_load_dwordx4 v7, s[12:15], s41 offen offset:1024 lds\n"
      "buffer_load_dwordx4 v8, s[12:15], s41 offen offset:2048 lds\n"
      "buffer_load_dwordx4 v9, s[12:15], s41 offen offset:3072 lds\n"
      "s_add_u32 s41, s41, 294912\n"
      "s_waitcnt vmcnt(16) lgkmcnt(0)\n"
      "s_barrier\n"
      "ds_read_b64_tr_b16 a[36:37], v19 offset:0\n"
      "ds_read_b64_tr_b16 a[38:39], v19 offset:1024\n"
      "ds_read_b64_tr_b16 a[108:109], v19 offset:8192\n"
      "ds_read_b64_tr_b16 a[110:111], v19 offset:9216\n"
      "ds_read_b64_tr_b16 a[180:181], v19 offset:8\n"
      "ds_read_b64_tr_b16 a[182:183], v19 offset:1032\n"
      "ds_read_b64_tr_b16 a[252:253], v19 offset:8200\n"
      "ds_read_b64_tr_b16 a[254:255], v19 offset:9224\n"
      "v_mfma_f32_16x16x32_bf16 v[140:143], a[32:35], v[172:175], v[140:143]\n"
      "v_mfma_f32_16x16x32_bf16 v[144:147], a[104:107], v[172:175], v[144:147]\n"
      "v_mfma_f32_16x16x32_bf16 v[148:151], a[176:179], v[172:175], v[148:151]\n"
      "v_mfma_f32_16x16x32_bf16 v[152:155], a[248:251], v[172:175], v[152:155]\n"
      "s_add_u32 m0, s38, 98304\n"
      "s_nop 0\n"
      "buffer_load_dwordx4 v6, s[12:15], s41 offen offset:0 lds\n"
      "buffer_load_dwordx4 v7, s[12:15], s41 offen offset:1024 lds\n"
      "buffer_load_dwordx4 v8, s[12:15], s41 offen offset:2048 lds\n"
      "buffer_load_dwordx4 v9, s[12:15], s41 offen offset:3072 lds\n"
      "s_add_u32 s41, s41, 294912\n"
      "s_waitcnt vmcnt(16) lgkmcnt(0)\n"
      "s_barrier\n"
      "ds_read_b64_tr_b16 a[40:41], v19 offset:16384\n"
      "ds_read_b64_tr_b16 a[42:43], v19 offset:17408\n"
      "ds_read_b64_tr_b16 a[112:113], v19 offset:24576\n"
      "ds_read_b64_tr_b16 a[114:115], v19 offset:25600\n"
      "ds_read_b64_tr_b16 a[184:185], v19 offset:16392\n"
      "ds_read_b64_tr_b16 a[186:187], v19 offset:17416\n"
      "ds_read_b64_tr_b16 v[20:21], v19 offset:24584\n"
      "ds_read_b64_tr_b16 v[22:23], v19 offset:25608\n"
      "v_mfma_f32_16x16x32_bf16 v[140:143], a[36:39], v[172:175], v[140:143]\n"
      "v_mfma_f32_16x16x32_bf16 v[144:147], a[108:111], v[172:175], v[144:147]\n"
      "v_mfma_f32_16x16x32_bf16 v[148:151], a[180:183], v[172:175], v[148:151]\n"
      "v_mfma_f32_16x16x32_bf16 v[152:155], a[252:255], v[172:175], v[152:155]\n"
      "s_add_u32 m0, s38, 114688\n"
      "s_nop 0\n"
      "buffer_load_dwordx4 v6, s[12:15], s41 offen offset:0 lds\n"
      "buffer_load_dwordx4 v7, s[12:15], s41 offen offset:1024 lds\n"
      "buffer_load_dwordx4 v8, s[12:15], s41 offen offset:2048 lds\n"
      "buffer_load_dwordx4 v9, s[12:15], s41 offen offset:3072 lds\n"
      "s_add_u32 s41, s41, 294912\n"
      "s_waitcnt vmcnt(16) lgkmcnt(0)\n"
      "s_barrier\n"
      "ds_read_b64_tr_b16 a[44:45], v19 offset:32768\n"
      "ds_read_b64_tr_b16 a[46:47], v19 offset:33792\n"
      "ds_read_b64_tr_b16 a[116:117], v19 offset:40960\n"
      "ds_read_b64_tr_b16 a[118:119], v19 offset:41984\n"
      "ds_read_b64_tr_b16 a[188:189], v19 offset:32776\n"
      "ds_read_b64_tr_b16 a[190:191], v19 offset:33800\n"
      "ds_read_b64_tr_b16 v[24:25], v19 offset:40968\n"
      "ds_read_b64_tr_b16 v[26:27], v19 offset:41992\n"
      "v_mfma_f32_16x16x32_bf16 v[140:143], a[40:43], v[172:175], v[140:143]\n"
      "v_mfma_f32_16x16x32_bf16 v[144:147], a[112:115], v[172:175], v[144:147]\n"
      "v_mfma_f32_16x16x32_bf16 v[148:151], a[184:187], v[172:175], v[148:151]\n"
      "v_mfma_f32_16x16x32_bf16 v[152:155], v[20:23], v[172:175], v[152:155]\n"
      "s_add_u32 m0, s38, 131072\n"
      "s_nop 0\n"
      "buffer_load_dwordx4 v6, s[12:15], s41 offen offset:0 lds\n"
      "buffer_load_dwordx4 v7, s[12:15], s41 offen offset:1024 lds\n"
      "buffer_load_dwordx4 v8, s[12:15], s41 offen offset:2048 lds\n"
      "buffer_load_dwordx4 v9, s[12:15], s41 offen offset:3072 lds\n"
      "s_add_u32 s41, s41, 294912\n"
      "s_waitcnt vmcnt(16) lgkmcnt(0)\n"
      "s_barrier\n"
      "ds_read_b64_tr_b16 a[48:49], v5 offset:0\n"
      "ds_read_b64_tr_b16 a[50:51], v5 offset:1024\n"
      "ds_read_b64_tr_b16 a[120:121], v5 offset:8192\n"
      "ds_read_b64_tr_b16 a[122:123], v5 offset:9216\n"
      "ds_read_b64_tr_b16 a[192:193], v5 offset:8\n"
      "ds_read_b64_tr_b16 a[194:195], v5 offset:1032\n"
      "ds_read_b64_tr_b16 v[28:29], v5 offset:8200\n"
      "ds_read_b64_tr_b16 v[30:31], v5 offset:9224\n"
      "v_mfma_f32_16x16x32_bf16 v[140:143], a[44:47], v[172:175], v[140:143]\n"
      "v_mfma_f32_16x16x32_bf16 v[144:147], a[116:119], v[172:175], v[144:147]\n"
      "v_mfma_f32_16x16x32_bf16 v[148:151], a[188:191], v[172:175], v[148:151]\n"
      "v_mfma_f32_16x16x32_bf16 v[152:155], v[24:27], v[172:175], v[152:155]\n"
      "s_add_u32 m0, s38, 147456\n"
      "s_nop 0\n"
      "buffer_load_dwordx4 v6, s[12:15], s41 offen offset:0 lds\n"
      "buffer_load_dwordx4 v7, s[12:15], s41 offen offset:1024 lds\n"
      "buffer_load_dwordx4 v8, s[12:15], s41 offen offset:2048 lds\n"
      "buffer_load_dwordx4 v9, s[12:15], s41 offen offset:3072 lds\n"
      "s_add_u32 s41, s41, 294912\n"
      "s_waitcnt vmcnt(16) lgkmcnt(0)\n"
      "s_barrier\n"
      "ds_read_b64_tr_b16 a[52:53], v5 offset:16384\n"
      "ds_read_b64_tr_b16 a[54:55], v5 offset:17408\n"
      "ds_read_b64_tr_b16 a[124:125], v5 offset:24576\n"
      "ds_read_b64_tr_b16 a[126:127], v5 offset:25600\n"
      "ds_read_b64_tr_b16 a[196:197], v5 offset:16392\n"
      "ds_read_b64_tr_b16 a[198:199], v5 offset:17416\n"
      "ds_read_b64_tr_b16 v[32:33], v5 offset:24584\n"
      "ds_read_b64_tr_b16 v[34:35], v5 offset:25608\n"
      "v_mfma_f32_16x16x32_bf16 v[140:143], a[48:51], v[172:175], v[140:143]\n"
      "v_mfma_f32_16x16x32_bf16 v[144:147], a[120:123], v[172:175], v[144:147]\n"
      "v_mfma_f32_16x16x32_bf16 v[148:151], a[192:195], v[172:175], v[148:151]\n"
      "v_mfma_f32_16x16x32_bf16 v[152:155], v[28:31], v[172:175], v[152:155]\n"
      "s_waitcnt vmcnt(12) lgkmcnt(0)\n"
      "s_barrier\n"
      "ds_read_b64_tr_b16 a[56:57], v5 offset:32768\n"
      "ds_read_b64_tr_b16 a[58:59], v5 offset:33792\n"
      "ds_read_b64_tr_b16 a[128:129], v5 offset:40960\n"
      "ds_read_b64_tr_b16 a[130:131], v5 offset:41984\n"
      "ds_read_b64_tr_b16 a[200:201], v5 offset:32776\n"
      "ds_read_b64_tr_b16 a[202:203], v5 offset:33800\n"
      "ds_read_b64_tr_b16 v[36:37], v5 offset:40968\n"
      "ds_read_b64_tr_b16 v[38:39], v5 offset:41992\n"
      "v_mfma_f32_16x16x32_bf16 v[140:143], a[52:55], v[172:175], v[140:143]\n"
      "v_mfma_f32_16x16x32_bf16 v[144:147], a[124:127], v[172:175], v[144:147]\n"
      "v_mfma_f32_16x16x32_bf16 v[148:151], a[196:199], v[172:175], v[148:151]\n"
      "v_mfma_f32_16x16x32_bf16 v[152:155], v[32:35], v[172:175], v[152:155]\n"
      "s_waitcnt vmcnt(8) lgkmcnt(0)\n"
      "s_barrier\n"
      "ds_read_b64_tr_b16 a[60:61], v19 offset:0\n"
      "ds_read_b64_tr_b16 a[62:63], v19 offset:1024\n"
      "ds_read_b64_tr_b16 a[132:133], v19 offset:8192\n"
      "ds_read_b64_tr_b16 a[134:135], v19 offset:9216\n"
      "ds_read_b64_tr_b16 a[204:205], v19 offset:8\n"
      "ds_read_b64_tr_b16 a[206:207], v19 offset:1032\n"
      "ds_read_b64_tr_b16 v[40:41], v19 offset:8200\n"
      "ds_read_b64_tr_b16 v[42:43], v19 offset:9224\n"
      "v_mfma_f32_16x16x32_bf16 v[140:143], a[56:59], v[172:175], v[140:143]\n"
      "v_mfma_f32_16x16x32_bf16 v[144:147], a[128:131], v[172:175], v[144:147]\n"
      "v_mfma_f32_16x16x32_bf16 v[148:151], a[200:203], v[172:175], v[148:151]\n"
      "v_mfma_f32_16x16x32_bf16 v[152:155], v[36:39], v[172:175], v[152:155]\n"
      "s_waitcnt vmcnt(4) lgkmcnt(0)\n"
      "s_barrier\n"
      "ds_read_b64_tr_b16 a[64:65], v19 offset:16384\n"
      "ds_read_b64_tr_b16 a[66:67], v19 offset:17408\n"
      "ds_read_b64_tr_b16 a[136:137], v19 offset:24576\n"
      "ds_read_b64_tr_b16 a[138:139], v19 offset:25600\n"
      "ds_read_b64_tr_b16 a[208:209], v19 offset:16392\n"
      "ds_read_b64_tr_b16 a[210:211], v19 offset:17416\n"
      "ds_read_b64_tr_b16 v[44:45], v19 offset:24584\n"
      "ds_read_b64_tr_b16 v[46:47], v19 offset:25608\n"
      "v_mfma_f32_16x16x32_bf16 v[140:143], a[60:63], v[172:175], v[140:143]\n"
      "v_mfma_f32_16x16x32_bf16 v[144:147], a[132:135], v[172:175], v[144:147]\n"
      "v_mfma_f32_16x16x32_bf16 v[148:151], a[204:207], v[172:175], v[148:151]\n"
      "v_mfma_f32_16x16x32_bf16 v[152:155], v[40:43], v[172:175], v[152:155]\n"
      "s_waitcnt vmcnt(0) lgkmcnt(0)\n"
      "s_barrier\n"
      "ds_read_b64_tr_b16 a[68:69], v19 offset:32768\n"
      "ds_read_b64_tr_b16 a[70:71], v19 offset:33792\n"
      "ds_read_b64_tr_b16 a[140:141], v19 offset:40960\n"
      "ds_read_b64_tr_b16 a[142:143], v19 offset:41984\n"
      "ds_read_b64_tr_b16 a[212:213], v19 offset:32776\n"
      "ds_read_b64_tr_b16 a[214:215], v19 offset:33800\n"
      "ds_read_b64_tr_b16 v[48:49], v19 offset:40968\n"
      "ds_read_b64_tr_b16 v[50:51], v19 offset:41992\n"
      "v_mfma_f32_16x16x32_bf16 v[140:143], a[64:67], v[172:175], v[140:143]\n"
      "v_mfma_f32_16x16x32_bf16 v[144:147], a[136:139], v[172:175], v[144:147]\n"
      "v_mfma_f32_16x16x32_bf16 v[148:151], a[208:211], v[172:175], v[148:151]\n"
      "v_mfma_f32_16x16x32_bf16 v[152:155], v[44:47], v[172:175], v[152:155]\n"
      "s_waitcnt lgkmcnt(0)\n"
      "v_mfma_f32_16x16x32_bf16 v[140:143], a[68:71], v[172:175], v[140:143]\n"
      "v_mfma_f32_16x16x32_bf16 v[144:147], a[140:143], v[172:175], v[144:147]\n"
      "v_mfma_f32_16x16x32_bf16 v[148:151], a[212:215], v[172:175], v[148:151]\n"
      "v_mfma_f32_16x16x32_bf16 v[152:155], v[48:51], v[172:175], v[152:155]\n"
      "s_mul_i32 s21, s21, 1664525\n"
      "s_add_u32 s21, s21, 1013904223\n"
      "v_mov_b32 v15, s21\n"
      "buffer_store_dword v15, v12, s[8:11], 0 offen sc1\n"
      "v_not_b32 v52, v15\n"
      "v_not_b32 v53, v15\n"
      "v_not_b32 v54, v15\n"
      "v_not_b32 v55, v15\n"
      "s_add_u32 s30, s37, 0\n"
      "buffer_store_dwordx4 v[52:55], v10, s[8:11], s30 offen sc1\n"
      "v_xor_b32 v15, v15, v16\n"
      "v_add_u32 v17, 40960, v0\n"
      "s_barrier\n"
      "v_mov_b32 v11, 0\n"
      "ds_write_b32 v13, v11\n"
      "s_waitcnt lgkmcnt(0)\n"
      "s_barrier\n"
      "s_add_u32 s6, s36, 1548288\n"
      "s_add_u32 s36, s36, 4294942720\n"
      "s_add_u32 s42, s37, 4294635520\n"
      "s_add_u32 s37, s37, 4294801408\n"
      "s_add_u32 s36, s36, 24576\n"
      "s_min_u32 s36, s36, s6\n"
      "buffer_load_dword v18, v2, s[8:11], s36 offen sc1\n"
      "s_mov_b32 s40, s36\n"
      "s_add_u32 m0, s38, 0\n"
      "s_waitcnt vmcnt(0)\n"
      "v_cmp_ne_u32 vcc, s21, v18\n"
      "s_cbranch_vccnz .Lsp_g0_1\n"
      ".Lbk_g0_1:\n"
      "buffer_load_dwordx4 v0, s[8:11], s40 offen offset:0 sc1 lds\n"
      "s_add_u32 s36, s36, 24576\n"
      "buffer_load_dwordx4 v0, s[8:11], s40 offen offset:2048 sc1 lds\n"
      "s_min_u32 s36, s36, s6\n"
      "buffer_load_dwordx4 v0, s[8:11], s40 offen offset:1024 sc1 lds\n"
      "buffer_load_dwordx4 v0, s[8:11], s40 offen offset:3072 sc1 lds\n"
      "s_add_u32 m0, s39, 0\n"
      "s_nop 0\n"
      "buffer_load_dwordx4 v1, s[8:11], s40 offen offset:0 sc1 lds\n"
      "buffer_load_dword v18, v2, s[8:11], s36 offen sc1\n"
      "s_mov_b32 s40, s36\n"
      "v_add_u32 v11, 1, v11\n"
      "s_add_u32 m0, s38, 20480\n"
      "s_waitcnt vmcnt(1)\n"
      "ds_write_b32 v13, v11\n"
      "s_waitcnt vmcnt(0)\n"
      "v_cmp_ne_u32 vcc, s21, v18\n"
      "ds_read_b32 v12, v14\n"
      "s_cbranch_vccnz .Lsp_g0_2\n"
      ".Lbk_g0_2:\n"
      "buffer_load_dwordx4 v0, s[8:11], s40 offen offset:0 sc1 lds\n"
      "s_add_u32 s36, s36, 24576\n"
      "s_waitcnt lgkmcnt(0)\n"
      "v_cmp_gt_i32 vcc, v11, v12\n"
      "buffer_load_dwordx4 v0, s[8:11], s40 offen offset:2048 sc1 lds\n"
      "s_min_u32 s36, s36, s6\n"
      "s_cbranch_vccnz .Lsp_gy0_3\n"
      ".Lbk_gy0_3:\n"
      "ds_read_b128 v[52:55], v0 offset:0\n"
      "ds_read_b128 v[56:59], v0 offset:1024\n"
      "buffer_load_dwordx4 v0, s[8:11], s40 offen offset:1024 sc1 lds\n"
      "ds_read_b128 v[60:63], v0 offset:2048\n"
      "ds_read_b128 v[72:75], v0 offset:5120\n"
      "buffer_load_dwordx4 v0, s[8:11], s40 offen offset:3072 sc1 lds\n"
      "ds_read2st64_b64 v[168:171], v4 offset0:0 offset1:1\n"
      "ds_read_b128 v[64:67], v0 offset:3072\n"
      "s_add_u32 m0, s39, 20480\n"
      "ds_read_b128 v[76:79], v0 offset:6144\n"
      "buffer_load_dwordx4 v1, s[8:11], s40 offen offset:0 sc1 lds\n"
      "ds_read_b128 v[68:71], v0 offset:4096\n"
      "ds_read_b128 v[80:83], v0 offset:7168\n"
      "ds_read_b128 v[92:95], v0 offset:10240\n"
      "buffer_load_dword v16, v13, s[8:11], s31 offen\n"
      "ds_read_b128 v[88:91], v0 offset:9216\n"
      "ds_read_b128 v[108:111], v0 offset:14336\n"
      "ds_read_b128 v[96:99], v0 offset:11264\n"
      "buffer_load_dword v18, v2, s[8:11], s36 offen sc1\n"
      "ds_read_b128 v[100:103], v0 offset:12288\n"
      "s_mov_b32 s20, 15\n"
      "s_waitcnt lgkmcnt(0)\n"
      "s_nop 4\n"
      ".p2align 6\n"
      ".Lg_loop:\n"
      "v_mfma_f32_16x16x32_bf16 v[124:127], a[0:3], v[52:55], 0\n"
      "ds_read_b128 v[84:87], v0 offset:8192\n"
      "v_mul_f32 v162, v134, v162\n"
      "v_mfma_f32_16x16x32_bf16 v[124:127], a[4:7], v[56:59], v[124:127]\n"
      "ds_read_b128 v[116:119], v0 offset:16384\n"
      "v_fmac_f32 v128, v176, v144\n"
      "v_mfma_f32_16x16x32_bf16 v[124:127], a[8:11], v[60:63], v[124:127]\n"
      "v_rcp_f32 v161, v161\n"
      "v_mfma_f32_16x16x32_bf16 v[124:127], a[12:15], v[64:67], v[124:127]\n"
      "s_waitcnt lgkmcnt(5)\n"
      "ds_read_b128 v[120:123], v0 offset:17408\n"
      "v_fma_f32 v160, v160, v177, v177\n"
      "v_mfma_f32_16x16x32_bf16 v[124:127], a[16:19], v[68:71], v[124:127]\n"
      "v_fmac_f32 v129, v176, v145\n"
      "v_fmac_f32 v139, v176, v155\n"
      "v_mfma_f32_16x16x32_bf16 v[124:127], a[20:23], v[72:75], v[124:127]\n"
      "ds_read_b128 v[104:107], v0 offset:13312\n"
      "v_fmac_f32 v131, v176, v147\n"
      "v_mfma_f32_16x16x32_bf16 v[124:127], a[24:27], v[76:79], v[124:127]\n"
      "ds_read_b128 v[112:115], v0 offset:15360\n"
      "v_add_f32 v169, v169, v171\n"
      "v_mfma_f32_16x16x32_bf16 v[124:127], a[28:31], v[80:83], v[124:127]\n"
      "s_waitcnt lgkmcnt(4)\n"
      "v_mul_f32 v131, v131, v159\n"
      "v_mul_f32 v161, v133, v161\n"
      "v_mfma_f32_16x16x32_bf16 v[124:127], a[32:35], v[84:87], v[124:127]\n"
      "v_mul_f32 v129, v129, v157\n"
      "v_cvt_pk_bf16_f32 v165, v130, v131\n"
      "v_mfma_f32_16x16x32_bf16 v[124:127], a[36:39], v[88:91], v[124:127]\n"
      "v_fma_f32 v163, v163, v177, v177\n"
      "v_rcp_f32 v163, v163\n"
      "v_mfma_f32_16x16x32_bf16 v[124:127], a[40:43], v[92:95], v[124:127]\n"
      "v_fmac_f32 v136, v176, v152\n"
      "v_fmac_f32 v138, v176, v154\n"
      "v_mfma_f32_16x16x32_bf16 v[124:127], a[44:47], v[96:99], v[124:127]\n"
      "v_fmac_f32 v137, v176, v153\n"
      "v_mul_f32 v128, v128, v156\n"
      "v_mfma_f32_16x16x32_bf16 v[124:127], a[48:51], v[100:103], v[124:127]\n"
      "s_waitcnt lgkmcnt(1)\n"
      "v_cvt_pk_bf16_f32 v164, v128, v129\n"
      "v_mul_f32 v137, v137, v161\n"
      "v_mfma_f32_16x16x32_bf16 v[124:127], a[52:55], v[104:107], v[124:127]\n"
      "v_mul_f32 v163, v135, v163\n"
      "v_sub_f32 v176, -v168, v170\n"
      "v_mfma_f32_16x16x32_bf16 v[124:127], a[56:59], v[108:111], v[124:127]\n"
      "s_waitcnt lgkmcnt(0)\n"
      "v_rcp_f32 v160, v160\n"
      "v_mfma_f32_16x16x32_bf16 v[124:127], a[60:63], v[112:115], v[124:127]\n"
      "v_mul_f32 v160, v132, v160\n"
      "v_mul_f32 v138, v138, v162\n"
      "v_mfma_f32_16x16x32_bf16 v[124:127], a[64:67], v[116:119], v[124:127]\n"
      "v_mul_f32 v136, v136, v160\n"
      "v_mul_f32 v139, v139, v163\n"
      "v_mfma_f32_16x16x32_bf16 v[124:127], a[68:71], v[120:123], v[124:127]\n"
      "v_cvt_pk_bf16_f32 v166, v136, v137\n"
      "v_cvt_pk_bf16_f32 v167, v138, v139\n"
      "v_mfma_f32_16x16x32_bf16 v[132:135], a[144:147], v[52:55], 0\n"
      "buffer_store_dwordx4 v[164:167], v0, s[8:11], s37 offen sc1\n"
      "v_mfma_f32_16x16x32_bf16 v[132:135], a[148:151], v[56:59], v[132:135]\n"
      "v_fma_f32 v169, -v176, v176, v169\n"
      "v_max_f32 v177, 0x3727c5ac, v169\n"
      "v_mfma_f32_16x16x32_bf16 v[132:135], a[152:155], v[60:63], v[132:135]\n"
      "v_rsq_f32 v170, v177\n"
      "v_mfma_f32_16x16x32_bf16 v[132:135], a[156:159], v[64:67], v[132:135]\n"
      "v_fmac_f32 v124, v176, v140\n"
      "v_mul_f32 v178, 0xc0135761, v170\n"
      "v_mfma_f32_16x16x32_bf16 v[132:135], a[160:163], v[68:71], v[132:135]\n"
      "v_fmac_f32 v127, v176, v143\n"
      "v_fmac_f32 v126, v176, v142\n"
      "v_mfma_f32_16x16x32_bf16 v[132:135], a[164:167], v[72:75], v[132:135]\n"
      "v_mul_f32 v170, v170, v170\n"
      "v_fmac_f32 v125, v176, v141\n"
      "v_mfma_f32_16x16x32_bf16 v[132:135], a[168:171], v[76:79], v[132:135]\n"
      "v_mul_f32 v171, 0x3d372713, v178\n"
      "v_mul_f32 v157, v125, v125\n"
      "v_mfma_f32_16x16x32_bf16 v[132:135], a[172:175], v[80:83], v[132:135]\n"
      "v_mul_f32 v179, v171, v170\n"
      "v_mul_f32 v158, v126, v126\n"
      "v_mfma_f32_16x16x32_bf16 v[132:135], a[176:179], v[84:87], v[132:135]\n"
      "v_mul_f32 v156, v124, v124\n"
      "v_fma_f32 v157, v157, v179, v178\n"
      "v_mfma_f32_16x16x32_bf16 v[132:135], a[180:183], v[88:91], v[132:135]\n"
      "s_mov_b32 s40, s36\n"
      "v_fma_f32 v156, v156, v179, v178\n"
      "v_mul_f32 v157, v125, v157\n"
      "v_mfma_f32_16x16x32_bf16 v[132:135], a[184:187], v[92:95], v[132:135]\n"
      "v_fma_f32 v158, v158, v179, v178\n"
      "v_mul_f32 v158, v126, v158\n"
      "v_mfma_f32_16x16x32_bf16 v[132:135], a[188:191], v[96:99], v[132:135]\n"
      "v_mul_f32 v159, v127, v127\n"
      "v_add_u32 v11, 1, v11\n"
      "v_mfma_f32_16x16x32_bf16 v[132:135], a[192:195], v[100:103], v[132:135]\n"
      "v_mul_f32 v156, v124, v156\n"
      "v_fma_f32 v159, v159, v179, v178\n"
      "v_mfma_f32_16x16x32_bf16 v[132:135], a[196:199], v[104:107], v[132:135]\n"
      "v_exp_f32 v156, v156\n"
      "v_mfma_f32_16x16x32_bf16 v[132:135], a[200:203], v[108:111], v[132:135]\n"
      "v_mul_f32 v159, v127, v159\n"
      "v_fma_f32 v156, v156, v177, v177\n"
      "v_mfma_f32_16x16x32_bf16 v[132:135], a[204:207], v[112:115], v[132:135]\n"
      "v_exp_f32 v159, v159\n"
      "v_mfma_f32_16x16x32_bf16 v[132:135], a[208:211], v[116:119], v[132:135]\n"
      "v_exp_f32 v158, v158\n"
      "v_mfma_f32_16x16x32_bf16 v[132:135], a[212:215], v[120:123], v[132:135]\n"
      "s_add_u32 m0, s38, 40960\n"
      "v_rcp_f32 v156, v156\n"
      "v_mfma_f32_16x16x32_bf16 v[128:131], a[72:75], v[52:55], 0\n"
      "s_waitcnt vmcnt(3)\n"
      "ds_write_b32 v13, v11\n"
      "v_mfma_f32_16x16x32_bf16 v[128:131], a[76:79], v[56:59], v[128:131]\n"
      "v_exp_f32 v157, v157\n"
      "v_mfma_f32_16x16x32_bf16 v[128:131], a[80:83], v[60:63], v[128:131]\n"
      "v_fmac_f32 v133, v176, v149\n"
      "v_fmac_f32 v134, v176, v150\n"
      "v_mfma_f32_16x16x32_bf16 v[128:131], a[84:87], v[64:67], v[128:131]\n"
      "v_mul_f32 v162, v134, v134\n"
      "v_fma_f32 v162, v162, v179, v178\n"
      "v_mfma_f32_16x16x32_bf16 v[128:131], a[88:91], v[68:71], v[128:131]\n"
      "v_fmac_f32 v132, v176, v148\n"
      "v_mul_f32 v161, v133, v133\n"
      "v_mfma_f32_16x16x32_bf16 v[128:131], a[92:95], v[72:75], v[128:131]\n"
      "s_add_u32 s37, s37, 165888\n"
      "v_mul_f32 v160, v132, v132\n"
      "v_fmac_f32 v135, v176, v151\n"
      "v_mfma_f32_16x16x32_bf16 v[128:131], a[96:99], v[76:79], v[128:131]\n"
      "s_waitcnt vmcnt(1)\n"
      "v_fma_f32 v157, v157, v177, v177\n"
      "v_cmp_ne_u32 vcc, s21, v18\n"
      "v_mfma_f32_16x16x32_bf16 v[128:131], a[100:103], v[80:83], v[128:131]\n"
      "v_fma_f32 v159, v159, v177, v177\n"
      "v_fma_f32 v160, v160, v179, v178\n"
      "v_mfma_f32_16x16x32_bf16 v[128:131], a[104:107], v[84:87], v[128:131]\n"
      "v_rcp_f32_e64 v159, v159\n"
      "v_mfma_f32_16x16x32_bf16 v[128:131], a[108:111], v[88:91], v[128:131]\n"
      "ds_read_b32 v12, v14\n"
      "v_fma_f32 v158, v158, v177, v177\n"
      "v_mfma_f32_16x16x32_bf16 v[128:131], a[112:115], v[92:95], v[128:131]\n"
      "s_cbranch_vccnz .Lsp_g0_4\n"
      ".Lbk_g0_4:\n"
      "v_fma_f32 v161, v161, v179, v178\n"
      "v_mfma_f32_16x16x32_bf16 v[128:131], a[116:119], v[96:99], v[128:131]\n"
      "buffer_load_dwordx4 v0, s[8:11], s40 offen offset:0 sc1 lds\n"
      "v_mfma_f32_16x16x32_bf16 v[128:131], a[120:123], v[100:103], v[128:131]\n"
      "v_mul_f32 v163, v135, v135\n"
      "v_mul_f32 v161, v133, v161\n"
      "v_mfma_f32_16x16x32_bf16 v[128:131], a[124:127], v[104:107], v[128:131]\n"
      "s_add_u32 s36, s36, 24576\n"
      "v_mul_f32 v159, v127, v159\n"
      "v_mul_f32 v162, v134, v162\n"
      "v_mfma_f32_16x16x32_bf16 v[128:131], a[128:131], v[108:111], v[128:131]\n"
      "s_waitcnt lgkmcnt(0)\n"
      "v_mul_f32 v160, v132, v160\n"
      "v_cmp_gt_i32 vcc, v11, v12\n"
      "v_mfma_f32_16x16x32_bf16 v[128:131], a[132:135], v[112:115], v[128:131]\n"
      "buffer_load_dwordx4 v0, s[8:11], s40 offen offset:2048 sc1 lds\n"
      "v_fma_f32 v163, v163, v179, v178\n"
      "v_mfma_f32_16x16x32_bf16 v[128:131], a[136:139], v[116:119], v[128:131]\n"
      "s_min_u32 s36, s36, s6\n"
      "v_rcp_f32 v158, v158\n"
      "v_mfma_f32_16x16x32_bf16 v[128:131], a[140:143], v[120:123], v[128:131]\n"
      "s_cbranch_vccnz .Lsp_gy0_5\n"
      ".Lbk_gy0_5:\n"
      "v_mul_f32 v163, v135, v163\n"
      "v_mfma_f32_16x16x32_bf16 v[136:139], a[216:219], v[52:55], 0\n"
      "ds_read_b128 v[52:55], v0 offset:20480\n"
      "v_exp_f32 v162, v162\n"
      "v_mfma_f32_16x16x32_bf16 v[136:139], a[220:223], v[56:59], v[136:139]\n"
      "s_add_u32 s42, s42, 165888\n"
      "ds_read_b128 v[56:59], v0 offset:21504\n"
      "buffer_load_dwordx4 v0, s[8:11], s40 offen offset:1024 sc1 lds\n"
      "v_mfma_f32_16x16x32_bf16 v[136:139], a[224:227], v[60:63], v[136:139]\n"
      "v_exp_f32 v163, v163\n"
      "v_mfma_f32_16x16x32_bf16 v[136:139], a[228:231], v[64:67], v[136:139]\n"
      "v_exp_f32 v161, v161\n"
      "v_mfma_f32_16x16x32_bf16 v[136:139], a[232:235], v[68:71], v[136:139]\n"
      "ds_read_b128 v[60:63], v0 offset:22528\n"
      "v_fmac_f32 v130, v176, v146\n"
      "v_mfma_f32_16x16x32_bf16 v[136:139], a[236:239], v[72:75], v[136:139]\n"
      "ds_read_b128 v[72:75], v0 offset:25600\n"
      "buffer_load_dwordx4 v0, s[8:11], s40 offen offset:3072 sc1 lds\n"
      "v_mfma_f32_16x16x32_bf16 v[136:139], a[240:243], v[76:79], v[136:139]\n"
      "ds_read2st64_b64 v[168:171], v4 offset0:40 offset1:41\n"
      "v_mul_f32 v158, v126, v158\n"
      "v_mfma_f32_16x16x32_bf16 v[136:139], a[244:247], v[80:83], v[136:139]\n"
      "ds_read_b128 v[64:67], v0 offset:23552\n"
      "v_mul_f32 v130, v130, v158\n"
      "v_mfma_f32_16x16x32_bf16 v[136:139], a[248:251], v[84:87], v[136:139]\n"
      "v_rcp_f32 v157, v157\n"
      "v_mfma_f32_16x16x32_bf16 v[136:139], a[252:255], v[88:91], v[136:139]\n"
      "s_add_u32 m0, s39, 40960\n"
      "ds_read_b128 v[76:79], v0 offset:26624\n"
      "v_fma_f32 v162, v162, v177, v177\n"
      "v_mfma_f32_16x16x32_bf16 v[136:139], v[20:23], v[92:95], v[136:139]\n"
      "buffer_load_dwordx4 v1, s[8:11], s40 offen offset:0 sc1 lds\n"
      "ds_read_b128 v[68:71], v0 offset:24576\n"
      "v_mfma_f32_16x16x32_bf16 v[136:139], v[24:27], v[96:99], v[136:139]\n"
      "v_exp_f32 v160, v160\n"
      "v_mfma_f32_16x16x32_bf16 v[136:139], v[28:31], v[100:103], v[136:139]\n"
      "ds_read_b128 v[80:83], v0 offset:27648\n"
      "v_mul_f32 v157, v125, v157\n"
      "v_mfma_f32_16x16x32_bf16 v[136:139], v[32:35], v[104:107], v[136:139]\n"
      "ds_read_b128 v[92:95], v0 offset:30720\n"
      "v_mul_f32 v156, v124, v156\n"
      "v_mfma_f32_16x16x32_bf16 v[136:139], v[36:39], v[108:111], v[136:139]\n"
      "s_waitcnt vmcnt(5) lgkmcnt(4)\n"
      "buffer_store_dword v15, v3, s[8:11], s42 offen sc1\n"
      "ds_read_b128 v[88:91], v0 offset:29696\n"
      "v_mfma_f32_16x16x32_bf16 v[136:139], v[40:43], v[112:115], v[136:139]\n"
      "ds_read_b128 v[108:111], v0 offset:34816\n"
      "v_fma_f32 v161, v161, v177, v177\n"
      "v_mfma_f32_16x16x32_bf16 v[136:139], v[44:47], v[116:119], v[136:139]\n"
      "ds_read_b128 v[96:99], v0 offset:31744\n"
      "buffer_load_dword v18, v2, s[8:11], s36 offen sc1\n"
      "v_mfma_f32_16x16x32_bf16 v[136:139], v[48:51], v[120:123], v[136:139]\n"
      "ds_read_b128 v[100:103], v0 offset:32768\n"
      "v_rcp_f32 v162, v162\n"
      "v_mfma_f32_16x16x32_bf16 v[124:127], a[0:3], v[52:55], 0\n"
      "ds_read_b128 v[84:87], v0 offset:28672\n"
      "v_mul_f32 v162, v134, v162\n"
      "v_mfma_f32_16x16x32_bf16 v[124:127], a[4:7], v[56:59], v[124:127]\n"
      "ds_read_b128 v[116:119], v0 offset:36864\n"
      "v_fmac_f32 v128, v176, v144\n"
      "v_mfma_f32_16x16x32_bf16 v[124:127], a[8:11], v[60:63], v[124:127]\n"
      "v_rcp_f32 v161, v161\n"
      "v_mfma_f32_16x16x32_bf16 v[124:127], a[12:15], v[64:67], v[124:127]\n"
      "s_waitcnt lgkmcnt(5)\n"
      "ds_read_b128 v[120:123], v0 offset:37888\n"
      "v_fma_f32 v160, v160, v177, v177\n"
      "v_mfma_f32_16x16x32_bf16 v[124:127], a[16:19], v[68:71], v[124:127]\n"
      "v_fmac_f32 v129, v176, v145\n"
      "v_fmac_f32 v139, v176, v155\n"
      "v_mfma_f32_16x16x32_bf16 v[124:127], a[20:23], v[72:75], v[124:127]\n"
      "ds_read_b128 v[104:107], v0 offset:33792\n"
      "v_fmac_f32 v131, v176, v147\n"
      "v_mfma_f32_16x16x32_bf16 v[124:127], a[24:27], v[76:79], v[124:127]\n"
      "ds_read_b128 v[112:115], v0 offset:35840\n"
      "v_add_f32 v169, v169, v171\n"
      "v_mfma_f32_16x16x32_bf16 v[124:127], a[28:31], v[80:83], v[124:127]\n"
      "s_waitcnt lgkmcnt(4)\n"
      "v_mul_f32 v131, v131, v159\n"
      "v_mul_f32 v161, v133, v161\n"
      "v_mfma_f32_16x16x32_bf16 v[124:127], a[32:35], v[84:87], v[124:127]\n"
      "v_mul_f32 v129, v129, v157\n"
      "v_cvt_pk_bf16_f32 v165, v130, v131\n"
      "v_mfma_f32_16x16x32_bf16 v[124:127], a[36:39], v[88:91], v[124:127]\n"
      "v_fma_f32 v163, v163, v177, v177\n"
      "v_rcp_f32 v163, v163\n"
      "v_mfma_f32_16x16x32_bf16 v[124:127], a[40:43], v[92:95], v[124:127]\n"
      "v_fmac_f32 v136, v176, v152\n"
      "v_fmac_f32 v138, v176, v154\n"
      "v_mfma_f32_16x16x32_bf16 v[124:127], a[44:47], v[96:99], v[124:127]\n"
      "v_fmac_f32 v137, v176, v153\n"
      "v_mul_f32 v128, v128, v156\n"
      "v_mfma_f32_16x16x32_bf16 v[124:127], a[48:51], v[100:103], v[124:127]\n"
      "s_waitcnt lgkmcnt(1)\n"
      "v_cvt_pk_bf16_f32 v164, v128, v129\n"
      "v_mul_f32 v137, v137, v161\n"
      "v_mfma_f32_16x16x32_bf16 v[124:127], a[52:55], v[104:107], v[124:127]\n"
      "v_mul_f32 v163, v135, v163\n"
      "v_sub_f32 v176, -v168, v170\n"
      "v_mfma_f32_16x16x32_bf16 v[124:127], a[56:59], v[108:111], v[124:127]\n"
      "s_waitcnt lgkmcnt(0)\n"
      "v_rcp_f32 v160, v160\n"
      "v_mfma_f32_16x16x32_bf16 v[124:127], a[60:63], v[112:115], v[124:127]\n"
      "v_mul_f32 v160, v132, v160\n"
      "v_mul_f32 v138, v138, v162\n"
      "v_mfma_f32_16x16x32_bf16 v[124:127], a[64:67], v[116:119], v[124:127]\n"
      "v_mul_f32 v136, v136, v160\n"
      "v_mul_f32 v139, v139, v163\n"
      "v_mfma_f32_16x16x32_bf16 v[124:127], a[68:71], v[120:123], v[124:127]\n"
      "v_cvt_pk_bf16_f32 v166, v136, v137\n"
      "v_cvt_pk_bf16_f32 v167, v138, v139\n"
      "v_mfma_f32_16x16x32_bf16 v[132:135], a[144:147], v[52:55], 0\n"
      "buffer_store_dwordx4 v[164:167], v0, s[8:11], s37 offen sc1\n"
      "v_mfma_f32_16x16x32_bf16 v[132:135], a[148:151], v[56:59], v[132:135]\n"
      "v_fma_f32 v169, -v176, v176, v169\n"
      "v_max_f32 v177, 0x3727c5ac, v169\n"
      "v_mfma_f32_16x16x32_bf16 v[132:135], a[152:155], v[60:63], v[132:135]\n"
      "v_rsq_f32 v170, v177\n"
      "v_mfma_f32_16x16x32_bf16 v[132:135], a[156:159], v[64:67], v[132:135]\n"
      "v_fmac_f32 v124, v176, v140\n"
      "v_mul_f32 v178, 0xc0135761, v170\n"
      "v_mfma_f32_16x16x32_bf16 v[132:135], a[160:163], v[68:71], v[132:135]\n"
      "v_fmac_f32 v127, v176, v143\n"
      "v_fmac_f32 v126, v176, v142\n"
      "v_mfma_f32_16x16x32_bf16 v[132:135], a[164:167], v[72:75], v[132:135]\n"
      "v_mul_f32 v170, v170, v170\n"
      "v_fmac_f32 v125, v176, v141\n"
      "v_mfma_f32_16x16x32_bf16 v[132:135], a[168:171], v[76:79], v[132:135]\n"
      "v_mul_f32 v171, 0x3d372713, v178\n"
      "v_mul_f32 v157, v125, v125\n"
      "v_mfma_f32_16x16x32_bf16 v[132:135], a[172:175], v[80:83], v[132:135]\n"
      "v_mul_f32 v179, v171, v170\n"
      "v_mul_f32 v158, v126, v126\n"
      "v_mfma_f32_16x16x32_bf16 v[132:135], a[176:179], v[84:87], v[132:135]\n"
      "v_mul_f32 v156, v124, v124\n"
      "v_fma_f32 v157, v157, v179, v178\n"
      "v_mfma_f32_16x16x32_bf16 v[132:135], a[180:183], v[88:91], v[132:135]\n"
      "s_mov_b32 s40, s36\n"
      "v_fma_f32 v156, v156, v179, v178\n"
      "v_mul_f32 v157, v125, v157\n"
      "v_mfma_f32_16x16x32_bf16 v[132:135], a[184:187], v[92:95], v[132:135]\n"
      "v_fma_f32 v158, v158, v179, v178\n"
      "v_mul_f32 v158, v126, v158\n"
      "v_mfma_f32_16x16x32_bf16 v[132:135], a[188:191], v[96:99], v[132:135]\n"
      "v_mul_f32 v159, v127, v127\n"
      "v_add_u32 v11, 1, v11\n"
      "v_mfma_f32_16x16x32_bf16 v[132:135], a[192:195], v[100:103], v[132:135]\n"
      "v_mul_f32 v156, v124, v156\n"
      "v_fma_f32 v159, v159, v179, v178\n"
      "v_mfma_f32_16x16x32_bf16 v[132:135], a[196:199], v[104:107], v[132:135]\n"
      "v_exp_f32 v156, v156\n"
      "v_mfma_f32_16x16x32_bf16 v[132:135], a[200:203], v[108:111], v[132:135]\n"
      "v_mul_f32 v159, v127, v159\n"
      "v_fma_f32 v156, v156, v177, v177\n"
      "v_mfma_f32_16x16x32_bf16 v[132:135], a[204:207], v[112:115], v[132:135]\n"
      "v_exp_f32 v159, v159\n"
      "v_mfma_f32_16x16x32_bf16 v[132:135], a[208:211], v[116:119], v[132:135]\n"
      "v_exp_f32 v158, v158\n"
      "v_mfma_f32_16x16x32_bf16 v[132:135], a[212:215], v[120:123], v[132:135]\n"
      "s_add_u32 m0, s38, 61440\n"
      "v_rcp_f32 v156, v156\n"
      "v_mfma_f32_16x16x32_bf16 v[128:131], a[72:75], v[52:55], 0\n"
      "s_waitcnt vmcnt(3)\n"
      "ds_write_b32 v13, v11\n"
      "v_mfma_f32_16x16x32_bf16 v[128:131], a[76:79], v[56:59], v[128:131]\n"
      "v_exp_f32 v157, v157\n"
      "v_mfma_f32_16x16x32_bf16 v[128:131], a[80:83], v[60:63], v[128:131]\n"
      "v_fmac_f32 v133, v176, v149\n"
      "v_fmac_f32 v134, v176, v150\n"
      "v_mfma_f32_16x16x32_bf16 v[128:131], a[84:87], v[64:67], v[128:131]\n"
      "v_mul_f32 v162, v134, v134\n"
      "v_fma_f32 v162, v162, v179, v178\n"
      "v_mfma_f32_16x16x32_bf16 v[128:131], a[88:91], v[68:71], v[128:131]\n"
      "v_fmac_f32 v132, v176, v148\n"
      "v_mul_f32 v161, v133, v133\n"
      "v_mfma_f32_16x16x32_bf16 v[128:131], a[92:95], v[72:75], v[128:131]\n"
      "s_add_u32 s37, s37, 165888\n"
      "v_mul_f32 v160, v132, v132\n"
      "v_fmac_f32 v135, v176, v151\n"
      "v_mfma_f32_16x16x32_bf16 v[128:131], a[96:99], v[76:79], v[128:131]\n"
      "s_waitcnt vmcnt(1)\n"
      "v_fma_f32 v157, v157, v177, v177\n"
      "v_cmp_ne_u32 vcc, s21, v18\n"
      "v_mfma_f32_16x16x32_bf16 v[128:131], a[100:103], v[80:83], v[128:131]\n"
      "v_fma_f32 v159, v159, v177, v177\n"
      "v_fma_f32 v160, v160, v179, v178\n"
      "v_mfma_f32_16x16x32_bf16 v[128:131], a[104:107], v[84:87], v[128:131]\n"
      "v_rcp_f32 v159, v159\n"
      "v_mfma_f32_16x16x32_bf16 v[128:131], a[108:111], v[88:91], v[128:131]\n"
      "ds_read_b32 v12, v14\n"
      "v_fma_f32 v158, v158, v177, v177\n"
      "v_mfma_f32_16x16x32_bf16 v[128:131], a[112:115], v[92:95], v[128:131]\n"
      "s_cbranch_vccnz .Lsp_g0_6\n"
      ".Lbk_g0_6:\n"
      "v_fma_f32 v161, v161, v179, v178\n"
      "v_mfma_f32_16x16x32_bf16 v[128:131], a[116:119], v[96:99], v[128:131]\n"
      "buffer_load_dwordx4 v0, s[8:11], s40 offen offset:0 sc1 lds\n"
      "v_mfma_f32_16x16x32_bf16 v[128:131], a[120:123], v[100:103], v[128:131]\n"
      "v_mul_f32 v163, v135, v135\n"
      "v_mul_f32 v161, v133, v161\n"
      "v_mfma_f32_16x16x32_bf16 v[128:131], a[124:127], v[104:107], v[128:131]\n"
      "s_add_u32 s36, s36, 24576\n"
      "v_mul_f32 v159, v127, v159\n"
      "v_mul_f32 v162, v134, v162\n"
      "v_mfma_f32_16x16x32_bf16 v[128:131], a[128:131], v[108:111], v[128:131]\n"
      "s_waitcnt lgkmcnt(0)\n"
      "v_mul_f32 v160, v132, v160\n"
      "v_cmp_gt_i32 vcc, v11, v12\n"
      "v_mfma_f32_16x16x32_bf16 v[128:131], a[132:135], v[112:115], v[128:131]\n"
      "buffer_load_dwordx4 v0, s[8:11], s40 offen offset:2048 sc1 lds\n"
      "v_fma_f32 v163, v163, v179, v178\n"
      "v_mfma_f32_16x16x32_bf16 v[128:131], a[136:139], v[116:119], v[128:131]\n"
      "s_min_u32 s36, s36, s6\n"
      "v_rcp_f32 v158, v158\n"
      "v_mfma_f32_16x16x32_bf16 v[128:131], a[140:143], v[120:123], v[128:131]\n"
      "s_cbranch_vccnz .Lsp_gy0_7\n"
      ".Lbk_gy0_7:\n"
      "v_mul_f32 v163, v135, v163\n"
      "v_mfma_f32_16x16x32_bf16 v[136:139], a[216:219], v[52:55], 0\n"
      "ds_read_b128 v[52:55], v17 offset:0\n"
      "v_exp_f32 v162, v162\n"
      "v_mfma_f32_16x16x32_bf16 v[136:139], a[220:223], v[56:59], v[136:139]\n"
      "s_add_u32 s42, s42, 165888\n"
      "ds_read_b128 v[56:59], v17 offset:1024\n"
      "buffer_load_dwordx4 v0, s[8:11], s40 offen offset:1024 sc1 lds\n"
      "v_mfma_f32_16x16x32_bf16 v[136:139], a[224:227], v[60:63], v[136:139]\n"
      "v_exp_f32 v163, v163\n"
      "v_mfma_f32_16x16x32_bf16 v[136:139], a[228:231], v[64:67], v[136:139]\n"
      "v_exp_f32 v161, v161\n"
      "v_mfma_f32_16x16x32_bf16 v[136:139], a[232:235], v[68:71], v[136:139]\n"
      "ds_read_b128 v[60:63], v17 offset:2048\n"
      "v_fmac_f32 v130, v176, v146\n"
      "v_mfma_f32_16x16x32_bf16 v[136:139], a[236:239], v[72:75], v[136:139]\n"
      "ds_read_b128 v[72:75], v17 offset:5120\n"
      "buffer_load_dwordx4 v0, s[8:11], s40 offen offset:3072 sc1 lds\n"
      "v_mfma_f32_16x16x32_bf16 v[136:139], a[240:243], v[76:79], v[136:139]\n"
      "ds_read2st64_b64 v[168:171], v4 offset0:80 offset1:81\n"
      "v_mul_f32 v158, v126, v158\n"
      "v_mfma_f32_16x16x32_bf16 v[136:139], a[244:247], v[80:83], v[136:139]\n"
      "ds_read_b128 v[64:67], v17 offset:3072\n"
      "v_mul_f32 v130, v130, v158\n"
      "v_mfma_f32_16x16x32_bf16 v[136:139], a[248:251], v[84:87], v[136:139]\n"
      "v_rcp_f32 v157, v157\n"
      "v_mfma_f32_16x16x32_bf16 v[136:139], a[252:255], v[88:91], v[136:139]\n"
      "s_add_u32 m0, s39, 61440\n"
      "ds_read_b128 v[76:79], v17 offset:6144\n"
      "v_fma_f32 v162, v162, v177, v177\n"
      "v_mfma_f32_16x16x32_bf16 v[136:139], v[20:23], v[92:95], v[136:139]\n"
      "buffer_load_dwordx4 v1, s[8:11], s40 offen offset:0 sc1 lds\n"
      "ds_read_b128 v[68:71], v17 offset:4096\n"
      "v_mfma_f32_16x16x32_bf16 v[136:139], v[24:27], v[96:99], v[136:139]\n"
      "v_exp_f32 v160, v160\n"
      "v_mfma_f32_16x16x32_bf16 v[136:139], v[28:31], v[100:103], v[136:139]\n"
      "ds_read_b128 v[80:83], v17 offset:7168\n"
      "v_mul_f32 v157, v125, v157\n"
      "v_mfma_f32_16x16x32_bf16 v[136:139], v[32:35], v[104:107], v[136:139]\n"
      "ds_read_b128 v[92:95], v17 offset:10240\n"
      "v_mul_f32 v156, v124, v156\n"
      "v_mfma_f32_16x16x32_bf16 v[136:139], v[36:39], v[108:111], v[136:139]\n"
      "s_waitcnt vmcnt(5) lgkmcnt(4)\n"
      "buffer_store_dword v15, v3, s[8:11], s42 offen sc1\n"
      "ds_read_b128 v[88:91], v17 offset:9216\n"
      "v_mfma_f32_16x16x32_bf16 v[136:139], v[40:43], v[112:115], v[136:139]\n"
      "ds_read_b128 v[108:111], v17 offset:14336\n"
      "v_fma_f32 v161, v161, v177, v177\n"
      "v_mfma_f32_16x16x32_bf16 v[136:139], v[44:47], v[116:119], v[136:139]\n"
      "ds_read_b128 v[96:99], v17 offset:11264\n"
      "buffer_load_dword v18, v2, s[8:11], s36 offen sc1\n"
      "v_mfma_f32_16x16x32_bf16 v[136:139], v[48:51], v[120:123], v[136:139]\n"
      "ds_read_b128 v[100:103], v17 offset:12288\n"
      "v_rcp_f32 v162, v162\n"
      "v_mfma_f32_16x16x32_bf16 v[124:127], a[0:3], v[52:55], 0\n"
      "ds_read_b128 v[84:87], v17 offset:8192\n"
      "v_mul_f32 v162, v134, v162\n"
      "v_mfma_f32_16x16x32_bf16 v[124:127], a[4:7], v[56:59], v[124:127]\n"
      "ds_read_b128 v[116:119], v17 offset:16384\n"
      "v_fmac_f32 v128, v176, v144\n"
      "v_mfma_f32_16x16x32_bf16 v[124:127], a[8:11], v[60:63], v[124:127]\n"
      "v_rcp_f32 v161, v161\n"
      "v_mfma_f32_16x16x32_bf16 v[124:127], a[12:15], v[64:67], v[124:127]\n"
      "s_waitcnt lgkmcnt(5)\n"
      "ds_read_b128 v[120:123], v17 offset:17408\n"
      "v_fma_f32 v160, v160, v177, v177\n"
      "v_mfma_f32_16x16x32_bf16 v[124:127], a[16:19], v[68:71], v[124:127]\n"
      "v_fmac_f32 v129, v176, v145\n"
      "v_fmac_f32 v139, v176, v155\n"
      "v_mfma_f32_16x16x32_bf16 v[124:127], a[20:23], v[72:75], v[124:127]\n"
      "ds_read_b128 v[104:107], v17 offset:13312\n"
      "v_fmac_f32 v131, v176, v147\n"
      "v_mfma_f32_16x16x32_bf16 v[124:127], a[24:27], v[76:79], v[124:127]\n"
      "ds_read_b128 v[112:115], v17 offset:15360\n"
      "v_add_f32 v169, v169, v171\n"
      "v_mfma_f32_16x16x32_bf16 v[124:127], a[28:31], v[80:83], v[124:127]\n"
      "s_waitcnt lgkmcnt(4)\n"
      "v_mul_f32 v131, v131, v159\n"
      "v_mul_f32 v161, v133, v161\n"
      "v_mfma_f32_16x16x32_bf16 v[124:127], a[32:35], v[84:87], v[124:127]\n"
      "v_mul_f32 v129, v129, v157\n"
      "v_cvt_pk_bf16_f32 v165, v130, v131\n"
      "v_mfma_f32_16x16x32_bf16 v[124:127], a[36:39], v[88:91], v[124:127]\n"
      "v_fma_f32 v163, v163, v177, v177\n"
      "v_rcp_f32 v163, v163\n"
      "v_mfma_f32_16x16x32_bf16 v[124:127], a[40:43], v[92:95], v[124:127]\n"
      "v_fmac_f32 v136, v176, v152\n"
      "v_fmac_f32 v138, v176, v154\n"
      "v_mfma_f32_16x16x32_bf16 v[124:127], a[44:47], v[96:99], v[124:127]\n"
      "v_fmac_f32 v137, v176, v153\n"
      "v_mul_f32 v128, v128, v156\n"
      "v_mfma_f32_16x16x32_bf16 v[124:127], a[48:51], v[100:103], v[124:127]\n"
      "s_waitcnt lgkmcnt(1)\n"
      "v_cvt_pk_bf16_f32 v164, v128, v129\n"
      "v_mul_f32 v137, v137, v161\n"
      "v_mfma_f32_16x16x32_bf16 v[124:127], a[52:55], v[104:107], v[124:127]\n"
      "v_mul_f32 v163, v135, v163\n"
      "v_sub_f32 v176, -v168, v170\n"
      "v_mfma_f32_16x16x32_bf16 v[124:127], a[56:59], v[108:111], v[124:127]\n"
      "s_waitcnt lgkmcnt(0)\n"
      "v_rcp_f32 v160, v160\n"
      "v_mfma_f32_16x16x32_bf16 v[124:127], a[60:63], v[112:115], v[124:127]\n"
      "v_mul_f32 v160, v132, v160\n"
      "v_mul_f32 v138, v138, v162\n"
      "v_mfma_f32_16x16x32_bf16 v[124:127], a[64:67], v[116:119], v[124:127]\n"
      "v_mul_f32 v136, v136, v160\n"
      "v_mul_f32 v139, v139, v163\n"
      "v_mfma_f32_16x16x32_bf16 v[124:127], a[68:71], v[120:123], v[124:127]\n"
      "v_cvt_pk_bf16_f32 v166, v136, v137\n"
      "v_cvt_pk_bf16_f32 v167, v138, v139\n"
      "v_mfma_f32_16x16x32_bf16 v[132:135], a[144:147], v[52:55], 0\n"
      "buffer_store_dwordx4 v[164:167], v0, s[8:11], s37 offen sc1\n"
      "v_mfma_f32_16x16x32_bf16 v[132:135], a[148:151], v[56:59], v[132:135]\n"
      "v_fma_f32 v169, -v176, v176, v169\n"
      "v_max_f32 v177, 0x3727c5ac, v169\n"
      "v_mfma_f32_16x16x32_bf16 v[132:135], a[152:155], v[60:63], v[132:135]\n"
      "v_rsq_f32 v170, v177\n"
      "v_mfma_f32_16x16x32_bf16 v[132:135], a[156:159], v[64:67], v[132:135]\n"
      "v_fmac_f32 v124, v176, v140\n"
      "v_mul_f32 v178, 0xc0135761, v170\n"
      "v_mfma_f32_16x16x32_bf16 v[132:135], a[160:163], v[68:71], v[132:135]\n"
      "v_fmac_f32 v127, v176, v143\n"
      "v_fmac_f32 v126, v176, v142\n"
      "v_mfma_f32_16x16x32_bf16 v[132:135], a[164:167], v[72:75], v[132:135]\n"
      "v_mul_f32 v170, v170, v170\n"
      "v_fmac_f32 v125, v176, v141\n"
      "v_mfma_f32_16x16x32_bf16 v[132:135], a[168:171], v[76:79], v[132:135]\n"
      "v_mul_f32 v171, 0x3d372713, v178\n"
      "v_mul_f32 v157, v125, v125\n"
      "v_mfma_f32_16x16x32_bf16 v[132:135], a[172:175], v[80:83], v[132:135]\n"
      "v_mul_f32 v179, v171, v170\n"
      "v_mul_f32 v158, v126, v126\n"
      "v_mfma_f32_16x16x32_bf16 v[132:135], a[176:179], v[84:87], v[132:135]\n"
      "v_mul_f32 v156, v124, v124\n"
      "v_fma_f32 v157, v157, v179, v178\n"
      "v_mfma_f32_16x16x32_bf16 v[132:135], a[180:183], v[88:91], v[132:135]\n"
      "s_mov_b32 s40, s36\n"
      "v_fma_f32 v156, v156, v179, v178\n"
      "v_mul_f32 v157, v125, v157\n"
      "v_mfma_f32_16x16x32_bf16 v[132:135], a[184:187], v[92:95], v[132:135]\n"
      "v_fma_f32 v158, v158, v179, v178\n"
      "v_mul_f32 v158, v126, v158\n"
      "v_mfma_f32_16x16x32_bf16 v[132:135], a[188:191], v[96:99], v[132:135]\n"
      "v_mul_f32 v159, v127, v127\n"
      "v_add_u32 v11, 1, v11\n"
      "v_mfma_f32_16x16x32_bf16 v[132:135], a[192:195], v[100:103], v[132:135]\n"
      "v_mul_f32 v156, v124, v156\n"
      "v_fma_f32 v159, v159, v179, v178\n"
      "v_mfma_f32_16x16x32_bf16 v[132:135], a[196:199], v[104:107], v[132:135]\n"
      "v_exp_f32 v156, v156\n"
      "v_mfma_f32_16x16x32_bf16 v[132:135], a[200:203], v[108:111], v[132:135]\n"
      "v_mul_f32 v159, v127, v159\n"
      "v_fma_f32 v156, v156, v177, v177\n"
      "v_mfma_f32_16x16x32_bf16 v[132:135], a[204:207], v[112:115], v[132:135]\n"
      "v_exp_f32 v159, v159\n"
      "v_mfma_f32_16x16x32_bf16 v[132:135], a[208:211], v[116:119], v[132:135]\n"
      "v_exp_f32 v158, v158\n"
      "v_mfma_f32_16x16x32_bf16 v[132:135], a[212:215], v[120:123], v[132:135]\n"
      "s_add_u32 m0, s38, 0\n"
      "v_rcp_f32 v156, v156\n"
      "v_mfma_f32_16x16x32_bf16 v[128:131], a[72:75], v[52:55], 0\n"
      "s_waitcnt vmcnt(3)\n"
      "ds_write_b32 v13, v11\n"
      "v_mfma_f32_16x16x32_bf16 v[128:131], a[76:79], v[56:59], v[128:131]\n"
      "v_exp_f32 v157, v157\n"
      "v_mfma_f32_16x16x32_bf16 v[128:131], a[80:83], v[60:63], v[128:131]\n"
      "v_fmac_f32 v133, v176, v149\n"
      "v_fmac_f32 v134, v176, v150\n"
      "v_mfma_f32_16x16x32_bf16 v[128:131], a[84:87], v[64:67], v[128:131]\n"
      "v_mul_f32 v162, v134, v134\n"
      "v_fma_f32 v162, v162, v179, v178\n"
      "v_mfma_f32_16x16x32_bf16 v[128:131], a[88:91], v[68:71], v[128:131]\n"
      "v_fmac_f32 v132, v176, v148\n"
      "v_mul_f32 v161, v133, v133\n"
      "v_mfma_f32_16x16x32_bf16 v[128:131], a[92:95], v[72:75], v[128:131]\n"
      "s_add_u32 s37, s37, 165888\n"
      "v_mul_f32 v160, v132, v132\n"
      "v_fmac_f32 v135, v176, v151\n"
      "v_mfma_f32_16x16x32_bf16 v[128:131], a[96:99], v[76:79], v[128:131]\n"
      "s_waitcnt vmcnt(1)\n"
      "v_fma_f32 v157, v157, v177, v177\n"
      "v_cmp_ne_u32 vcc, s21, v18\n"
      "v_mfma_f32_16x16x32_bf16 v[128:131], a[100:103], v[80:83], v[128:131]\n"
      "v_fma_f32 v159, v159, v177, v177\n"
      "v_fma_f32 v160, v160, v179, v178\n"
      "v_mfma_f32_16x16x32_bf16 v[128:131], a[104:107], v[84:87], v[128:131]\n"
      "v_rcp_f32 v159, v159\n"
      "v_mfma_f32_16x16x32_bf16 v[128:131], a[108:111], v[88:91], v[128:131]\n"
      "ds_read_b32 v12, v14\n"
      "v_fma_f32 v158, v158, v177, v177\n"
      "v_mfma_f32_16x16x32_bf16 v[128:131], a[112:115], v[92:95], v[128:131]\n"
      "s_cbranch_vccnz .Lsp_g0_8\n"
      ".Lbk_g0_8:\n"
      "v_fma_f32 v161, v161, v179, v178\n"
      "v_mfma_f32_16x16x32_bf16 v[128:131], a[116:119], v[96:99], v[128:131]\n"
      "buffer_load_dwordx4 v0, s[8:11], s40 offen offset:0 sc1 lds\n"
      "v_mfma_f32_16x16x32_bf16 v[128:131], a[120:123], v[100:103], v[128:131]\n"
      "v_mul_f32 v163, v135, v135\n"
      "v_mul_f32 v161, v133, v161\n"
      "v_mfma_f32_16x16x32_bf16 v[128:131], a[124:127], v[104:107], v[128:131]\n"
      "s_add_u32 s36, s36, 24576\n"
      "v_mul_f32 v159, v127, v159\n"
      "v_mul_f32 v162, v134, v162\n"
      "v_mfma_f32_16x16x32_bf16 v[128:131], a[128:131], v[108:111], v[128:131]\n"
      "s_waitcnt lgkmcnt(0)\n"
      "v_mul_f32 v160, v132, v160\n"
      "v_cmp_gt_i32 vcc, v11, v12\n"
      "v_mfma_f32_16x16x32_bf16 v[128:131], a[132:135], v[112:115], v[128:131]\n"
      "buffer_load_dwordx4 v0, s[8:11], s40 offen offset:2048 sc1 lds\n"
      "v_fma_f32 v163, v163, v179, v178\n"
      "v_mfma_f32_16x16x32_bf16 v[128:131], a[136:139], v[116:119], v[128:131]\n"
      "s_min_u32 s36, s36, s6\n"
      "v_rcp_f32 v158, v158\n"
      "v_mfma_f32_16x16x32_bf16 v[128:131], a[140:143], v[120:123], v[128:131]\n"
      "s_cbranch_vccnz .Lsp_gy0_9\n"
      ".Lbk_gy0_9:\n"
      "v_mul_f32 v163, v135, v163\n"
      "v_mfma_f32_16x16x32_bf16 v[136:139], a[216:219], v[52:55], 0\n"
      "ds_read_b128 v[52:55], v17 offset:20480\n"
      "v_exp_f32 v162, v162\n"
      "v_mfma_f32_16x16x32_bf16 v[136:139], a[220:223], v[56:59], v[136:139]\n"
      "s_add_u32 s42, s42, 165888\n"
      "ds_read_b128 v[56:59], v17 offset:21504\n"
      "buffer_load_dwordx4 v0, s[8:11], s40 offen offset:1024 sc1 lds\n"
      "v_mfma_f32_16x16x32_bf16 v[136:139], a[224:227], v[60:63], v[136:139]\n"
      "v_exp_f32 v163, v163\n"
      "v_mfma_f32_16x16x32_bf16 v[136:139], a[228:231], v[64:67], v[136:139]\n"
      "v_exp_f32 v161, v161\n"
      "v_mfma_f32_16x16x32_bf16 v[136:139], a[232:235], v[68:71], v[136:139]\n"
      "ds_read_b128 v[60:63], v17 offset:22528\n"
      "v_fmac_f32 v130, v176, v146\n"
      "v_mfma_f32_16x16x32_bf16 v[136:139], a[236:239], v[72:75], v[136:139]\n"
      "ds_read_b128 v[72:75], v17 offset:25600\n"
      "buffer_load_dwordx4 v0, s[8:11], s40 offen offset:3072 sc1 lds\n"
      "v_mfma_f32_16x16x32_bf16 v[136:139], a[240:243], v[76:79], v[136:139]\n"
      "ds_read2st64_b64 v[168:171], v4 offset0:120 offset1:121\n"
      "v_mul_f32 v158, v126, v158\n"
      "v_mfma_f32_16x16x32_bf16 v[136:139], a[244:247], v[80:83], v[136:139]\n"
      "ds_read_b128 v[64:67], v17 offset:23552\n"
      "v_mul_f32 v130, v130, v158\n"
      "v_mfma_f32_16x16x32_bf16 v[136:139], a[248:251], v[84:87], v[136:139]\n"
      "v_rcp_f32 v157, v157\n"
      "v_mfma_f32_16x16x32_bf16 v[136:139], a[252:255], v[88:91], v[136:139]\n"
      "s_add_u32 m0, s39, 0\n"
      "ds_read_b128 v[76:79], v17 offset:26624\n"
      "v_fma_f32 v162, v162, v177, v177\n"
      "v_mfma_f32_16x16x32_bf16 v[136:139], v[20:23], v[92:95], v[136:139]\n"
      "buffer_load_dwordx4 v1, s[8:11], s40 offen offset:0 sc1 lds\n"
      "ds_read_b128 v[68:71], v17 offset:24576\n"
      "v_mfma_f32_16x16x32_bf16 v[136:139], v[24:27], v[96:99], v[136:139]\n"
      "v_exp_f32 v160, v160\n"
      "v_mfma_f32_16x16x32_bf16 v[136:139], v[28:31], v[100:103], v[136:139]\n"
      "ds_read_b128 v[80:83], v17 offset:27648\n"
      "v_mul_f32 v157, v125, v157\n"
      "v_mfma_f32_16x16x32_bf16 v[136:139], v[32:35], v[104:107], v[136:139]\n"
      "ds_read_b128 v[92:95], v17 offset:30720\n"
      "v_mul_f32 v156, v124, v156\n"
      "v_mfma_f32_16x16x32_bf16 v[136:139], v[36:39], v[108:111], v[136:139]\n"
      "s_waitcnt vmcnt(5) lgkmcnt(4)\n"
      "buffer_store_dword v15, v3, s[8:11], s42 offen sc1\n"
      "ds_read_b128 v[88:91], v17 offset:29696\n"
      "v_mfma_f32_16x16x32_bf16 v[136:139], v[40:43], v[112:115], v[136:139]\n"
      "ds_read_b128 v[108:111], v17 offset:34816\n"
      "v_fma_f32 v161, v161, v177, v177\n"
      "v_mfma_f32_16x16x32_bf16 v[136:139], v[44:47], v[116:119], v[136:139]\n"
      "ds_read_b128 v[96:99], v17 offset:31744\n"
      "buffer_load_dword v18, v2, s[8:11], s36 offen sc1\n"
      "v_mfma_f32_16x16x32_bf16 v[136:139], v[48:51], v[120:123], v[136:139]\n"
      "ds_read_b128 v[100:103], v17 offset:32768\n"
      "v_rcp_f32 v162, v162\n"
      "v_mfma_f32_16x16x32_bf16 v[124:127], a[0:3], v[52:55], 0\n"
      "ds_read_b128 v[84:87], v17 offset:28672\n"
      "v_mul_f32 v162, v134, v162\n"
      "v_mfma_f32_16x16x32_bf16 v[124:127], a[4:7], v[56:59], v[124:127]\n"
      "ds_read_b128 v[116:119], v17 offset:36864\n"
      "v_fmac_f32 v128, v176, v144\n"
      "v_mfma_f32_16x16x32_bf16 v[124:127], a[8:11], v[60:63], v[124:127]\n"
      "v_rcp_f32 v161, v161\n"
      "v_mfma_f32_16x16x32_bf16 v[124:127], a[12:15], v[64:67], v[124:127]\n"
      "s_waitcnt lgkmcnt(5)\n"
      "ds_read_b128 v[120:123], v17 offset:37888\n"
      "v_fma_f32 v160, v160, v177, v177\n"
      "v_mfma_f32_16x16x32_bf16 v[124:127], a[16:19], v[68:71], v[124:127]\n"
      "v_fmac_f32 v129, v176, v145\n"
      "v_fmac_f32 v139, v176, v155\n"
      "v_mfma_f32_16x16x32_bf16 v[124:127], a[20:23], v[72:75], v[124:127]\n"
      "ds_read_b128 v[104:107], v17 offset:33792\n"
      "v_fmac_f32 v131, v176, v147\n"
      "v_mfma_f32_16x16x32_bf16 v[124:127], a[24:27], v[76:79], v[124:127]\n"
      "ds_read_b128 v[112:115], v17 offset:35840\n"
      "v_add_f32 v169, v169, v171\n"
      "v_mfma_f32_16x16x32_bf16 v[124:127], a[28:31], v[80:83], v[124:127]\n"
      "s_waitcnt lgkmcnt(4)\n"
      "v_mul_f32 v131, v131, v159\n"
      "v_mul_f32 v161, v133, v161\n"
      "v_mfma_f32_16x16x32_bf16 v[124:127], a[32:35], v[84:87], v[124:127]\n"
      "v_mul_f32 v129, v129, v157\n"
      "v_cvt_pk_bf16_f32 v165, v130, v131\n"
      "v_mfma_f32_16x16x32_bf16 v[124:127], a[36:39], v[88:91], v[124:127]\n"
      "v_fma_f32 v163, v163, v177, v177\n"
      "v_rcp_f32 v163, v163\n"
      "v_mfma_f32_16x16x32_bf16 v[124:127], a[40:43], v[92:95], v[124:127]\n"
      "v_fmac_f32 v136, v176, v152\n"
      "v_fmac_f32 v138, v176, v154\n"
      "v_mfma_f32_16x16x32_bf16 v[124:127], a[44:47], v[96:99], v[124:127]\n"
      "v_fmac_f32 v137, v176, v153\n"
      "v_mul_f32 v128, v128, v156\n"
      "v_mfma_f32_16x16x32_bf16 v[124:127], a[48:51], v[100:103], v[124:127]\n"
      "s_waitcnt lgkmcnt(1)\n"
      "v_cvt_pk_bf16_f32 v164, v128, v129\n"
      "v_mul_f32 v137, v137, v161\n"
      "v_mfma_f32_16x16x32_bf16 v[124:127], a[52:55], v[104:107], v[124:127]\n"
      "v_mul_f32 v163, v135, v163\n"
      "v_sub_f32 v176, -v168, v170\n"
      "v_mfma_f32_16x16x32_bf16 v[124:127], a[56:59], v[108:111], v[124:127]\n"
      "s_waitcnt lgkmcnt(0)\n"
      "v_rcp_f32 v160, v160\n"
      "v_mfma_f32_16x16x32_bf16 v[124:127], a[60:63], v[112:115], v[124:127]\n"
      "v_mul_f32 v160, v132, v160\n"
      "v_mul_f32 v138, v138, v162\n"
      "v_mfma_f32_16x16x32_bf16 v[124:127], a[64:67], v[116:119], v[124:127]\n"
      "v_mul_f32 v136, v136, v160\n"
      "v_mul_f32 v139, v139, v163\n"
      "v_mfma_f32_16x16x32_bf16 v[124:127], a[68:71], v[120:123], v[124:127]\n"
      "v_cvt_pk_bf16_f32 v166, v136, v137\n"
      "v_cvt_pk_bf16_f32 v167, v138, v139\n"
      "v_mfma_f32_16x16x32_bf16 v[132:135], a[144:147], v[52:55], 0\n"
      "buffer_store_dwordx4 v[164:167], v0, s[8:11], s37 offen sc1\n"
      "v_mfma_f32_16x16x32_bf16 v[132:135], a[148:151], v[56:59], v[132:135]\n"
      "v_fma_f32 v169, -v176, v176, v169\n"
      "v_max_f32 v177, 0x3727c5ac, v169\n"
      "v_mfma_f32_16x16x32_bf16 v[132:135], a[152:155], v[60:63], v[132:135]\n"
      "v_rsq_f32 v170, v177\n"
      "v_mfma_f32_16x16x32_bf16 v[132:135], a[156:159], v[64:67], v[132:135]\n"
      "v_fmac_f32 v124, v176, v140\n"
      "v_mul_f32 v178, 0xc0135761, v170\n"
      "v_mfma_f32_16x16x32_bf16 v[132:135], a[160:163], v[68:71], v[132:135]\n"
      "v_fmac_f32 v127, v176, v143\n"
      "v_fmac_f32 v126, v176, v142\n"
      "v_mfma_f32_16x16x32_bf16 v[132:135], a[164:167], v[72:75], v[132:135]\n"
      "v_mul_f32 v170, v170, v170\n"
      "v_fmac_f32 v125, v176, v141\n"
      "v_mfma_f32_16x16x32_bf16 v[132:135], a[168:171], v[76:79], v[132:135]\n"
      "v_mul_f32 v171, 0x3d372713, v178\n"
      "v_mul_f32 v157, v125, v125\n"
      "v_mfma_f32_16x16x32_bf16 v[132:135], a[172:175], v[80:83], v[132:135]\n"
      "v_mul_f32 v179, v171, v170\n"
      "v_mul_f32 v158, v126, v126\n"
      "v_mfma_f32_16x16x32_bf16 v[132:135], a[176:179], v[84:87], v[132:135]\n"
      "v_mul_f32 v156, v124, v124\n"
      "v_fma_f32 v157, v157, v179, v178\n"
      "v_mfma_f32_16x16x32_bf16 v[132:135], a[180:183], v[88:91], v[132:135]\n"
      "s_mov_b32 s40, s36\n"
      "v_fma_f32 v156, v156, v179, v178\n"
      "v_mul_f32 v157, v125, v157\n"
      "v_mfma_f32_16x16x32_bf16 v[132:135], a[184:187], v[92:95], v[132:135]\n"
      "v_fma_f32 v158, v158, v179, v178\n"
      "v_mul_f32 v158, v126, v158\n"
      "v_mfma_f32_16x16x32_bf16 v[132:135], a[188:191], v[96:99], v[132:135]\n"
      "v_mul_f32 v159, v127, v127\n"
      "v_add_u32 v11, 1, v11\n"
      "v_mfma_f32_16x16x32_bf16 v[132:135], a[192:195], v[100:103], v[132:135]\n"
      "v_mul_f32 v156, v124, v156\n"
      "v_fma_f32 v159, v159, v179, v178\n"
      "v_mfma_f32_16x16x32_bf16 v[132:135], a[196:199], v[104:107], v[132:135]\n"
      "v_exp_f32 v156, v156\n"
      "v_mfma_f32_16x16x32_bf16 v[132:135], a[200:203], v[108:111], v[132:135]\n"
      "v_mul_f32 v159, v127, v159\n"
      "v_fma_f32 v156, v156, v177, v177\n"
      "v_mfma_f32_16x16x32_bf16 v[132:135], a[204:207], v[112:115], v[132:135]\n"
      "v_exp_f32 v159, v159\n"
      "v_mfma_f32_16x16x32_bf16 v[132:135], a[208:211], v[116:119], v[132:135]\n"
      "v_exp_f32 v158, v158\n"
      "v_mfma_f32_16x16x32_bf16 v[132:135], a[212:215], v[120:123], v[132:135]\n"
      "s_add_u32 m0, s38, 20480\n"
      "v_rcp_f32 v156, v156\n"
      "v_mfma_f32_16x16x32_bf16 v[128:131], a[72:75], v[52:55], 0\n"
      "s_waitcnt vmcnt(3)\n"
      "ds_write_b32 v13, v11\n"
      "v_mfma_f32_16x16x32_bf16 v[128:131], a[76:79], v[56:59], v[128:131]\n"
      "v_exp_f32 v157, v157\n"
      "v_mfma_f32_16x16x32_bf16 v[128:131], a[80:83], v[60:63], v[128:131]\n"
      "v_fmac_f32 v133, v176, v149\n"
      "v_fmac_f32 v134, v176, v150\n"
      "v_mfma_f32_16x16x32_bf16 v[128:131], a[84:87], v[64:67], v[128:131]\n"
      "v_mul_f32 v162, v134, v134\n"
      "v_fma_f32 v162, v162, v179, v178\n"
      "v_mfma_f32_16x16x32_bf16 v[128:131], a[88:91], v[68:71], v[128:131]\n"
      "v_fmac_f32 v132, v176, v148\n"
      "v_mul_f32 v161, v133, v133\n"
      "v_mfma_f32_16x16x32_bf16 v[128:131], a[92:95], v[72:75], v[128:131]\n"
      "s_add_u32 s37, s37, 165888\n"
      "v_mul_f32 v160, v132, v132\n"
      "v_fmac_f32 v135, v176, v151\n"
      "v_mfma_f32_16x16x32_bf16 v[128:131], a[96:99], v[76:79], v[128:131]\n"
      "s_waitcnt vmcnt(1)\n"
      "v_fma_f32 v157, v157, v177, v177\n"
      "v_cmp_ne_u32 vcc, s21, v18\n"
      "v_mfma_f32_16x16x32_bf16 v[128:131], a[100:103], v[80:83], v[128:131]\n"
      "v_fma_f32 v159, v159, v177, v177\n"
      "v_fma_f32 v160, v160, v179, v178\n"
      "v_mfma_f32_16x16x32_bf16 v[128:131], a[104:107], v[84:87], v[128:131]\n"
      "v_rcp_f32 v159, v159\n"
      "v_mfma_f32_16x16x32_bf16 v[128:131], a[108:111], v[88:91], v[128:131]\n"
      "ds_read_b32 v12, v14\n"
      "v_fma_f32 v158, v158, v177, v177\n"
      "v_mfma_f32_16x16x32_bf16 v[128:131], a[112:115], v[92:95], v[128:131]\n"
      "s_cbranch_vccnz .Lsp_g0_10\n"
      ".Lbk_g0_10:\n"
      "v_fma_f32 v161, v161, v179, v178\n"
      "v_mfma_f32_16x16x32_bf16 v[128:131], a[116:119], v[96:99], v[128:131]\n"
      "buffer_load_dwordx4 v0, s[8:11], s40 offen offset:0 sc1 lds\n"
      "v_mfma_f32_16x16x32_bf16 v[128:131], a[120:123], v[100:103], v[128:131]\n"
      "v_mul_f32 v163, v135, v135\n"
      "v_mul_f32 v161, v133, v161\n"
      "v_mfma_f32_16x16x32_bf16 v[128:131], a[124:127], v[104:107], v[128:131]\n"
      "s_add_u32 s36, s36, 24576\n"
      "v_mul_f32 v159, v127, v159\n"
      "v_mul_f32 v162, v134, v162\n"
      "v_mfma_f32_16x16x32_bf16 v[128:131], a[128:131], v[108:111], v[128:131]\n"
      "s_waitcnt lgkmcnt(0)\n"
      "v_mul_f32 v160, v132, v160\n"
      "v_cmp_gt_i32 vcc, v11, v12\n"
      "v_mfma_f32_16x16x32_bf16 v[128:131], a[132:135], v[112:115], v[128:131]\n"
      "buffer_load_dwordx4 v0, s[8:11], s40 offen offset:2048 sc1 lds\n"
      "v_fma_f32 v163, v163, v179, v178\n"
      "v_mfma_f32_16x16x32_bf16 v[128:131], a[136:139], v[116:119], v[128:131]\n"
      "s_min_u32 s36, s36, s6\n"
      "v_rcp_f32 v158, v158\n"
      "v_mfma_f32_16x16x32_bf16 v[128:131], a[140:143], v[120:123], v[128:131]\n"
      "s_cbranch_vccnz .Lsp_gy0_11\n"
      ".Lbk_gy0_11:\n"
      "v_mul_f32 v163, v135, v163\n"
      "v_mfma_f32_16x16x32_bf16 v[136:139], a[216:219], v[52:55], 0\n"
      "ds_read_b128 v[52:55], v0 offset:0\n"
      "v_exp_f32 v162, v162\n"
      "v_mfma_f32_16x16x32_bf16 v[136:139], a[220:223], v[56:59], v[136:139]\n"
      "s_add_u32 s42, s42, 165888\n"
      "ds_read_b128 v[56:59], v0 offset:1024\n"
      "buffer_load_dwordx4 v0, s[8:11], s40 offen offset:1024 sc1 lds\n"
      "v_mfma_f32_16x16x32_bf16 v[136:139], a[224:227], v[60:63], v[136:139]\n"
      "v_exp_f32 v163, v163\n"
      "v_mfma_f32_16x16x32_bf16 v[136:139], a[228:231], v[64:67], v[136:139]\n"
      "v_exp_f32 v161, v161\n"
      "v_mfma_f32_16x16x32_bf16 v[136:139], a[232:235], v[68:71], v[136:139]\n"
      "ds_read_b128 v[60:63], v0 offset:2048\n"
      "v_fmac_f32 v130, v176, v146\n"
      "v_mfma_f32_16x16x32_bf16 v[136:139], a[236:239], v[72:75], v[136:139]\n"
      "ds_read_b128 v[72:75], v0 offset:5120\n"
      "buffer_load_dwordx4 v0, s[8:11], s40 offen offset:3072 sc1 lds\n"
      "v_mfma_f32_16x16x32_bf16 v[136:139], a[240:243], v[76:79], v[136:139]\n"
      "ds_read2st64_b64 v[168:171], v4 offset0:0 offset1:1\n"
      "v_mul_f32 v158, v126, v158\n"
      "v_mfma_f32_16x16x32_bf16 v[136:139], a[244:247], v[80:83], v[136:139]\n"
      "ds_read_b128 v[64:67], v0 offset:3072\n"
      "v_mul_f32 v130, v130, v158\n"
      "v_mfma_f32_16x16x32_bf16 v[136:139], a[248:251], v[84:87], v[136:139]\n"
      "v_rcp_f32 v157, v157\n"
      "v_mfma_f32_16x16x32_bf16 v[136:139], a[252:255], v[88:91], v[136:139]\n"
      "s_add_u32 m0, s39, 20480\n"
      "ds_read_b128 v[76:79], v0 offset:6144\n"
      "v_fma_f32 v162, v162, v177, v177\n"
      "v_mfma_f32_16x16x32_bf16 v[136:139], v[20:23], v[92:95], v[136:139]\n"
      "buffer_load_dwordx4 v1, s[8:11], s40 offen offset:0 sc1 lds\n"
      "ds_read_b128 v[68:71], v0 offset:4096\n"
      "v_mfma_f32_16x16x32_bf16 v[136:139], v[24:27], v[96:99], v[136:139]\n"
      "v_exp_f32 v160, v160\n"
      "v_mfma_f32_16x16x32_bf16 v[136:139], v[28:31], v[100:103], v[136:139]\n"
      "ds_read_b128 v[80:83], v0 offset:7168\n"
      "v_mul_f32 v157, v125, v157\n"
      "v_mfma_f32_16x16x32_bf16 v[136:139], v[32:35], v[104:107], v[136:139]\n"
      "ds_read_b128 v[92:95], v0 offset:10240\n"
      "v_mul_f32 v156, v124, v156\n"
      "v_mfma_f32_16x16x32_bf16 v[136:139], v[36:39], v[108:111], v[136:139]\n"
      "s_waitcnt vmcnt(5) lgkmcnt(4)\n"
      "buffer_store_dword v15, v3, s[8:11], s42 offen sc1\n"
      "ds_read_b128 v[88:91], v0 offset:9216\n"
      "v_mfma_f32_16x16x32_bf16 v[136:139], v[40:43], v[112:115], v[136:139]\n"
      "ds_read_b128 v[108:111], v0 offset:14336\n"
      "v_fma_f32 v161, v161, v177, v177\n"
      "v_mfma_f32_16x16x32_bf16 v[136:139], v[44:47], v[116:119], v[136:139]\n"
      "ds_read_b128 v[96:99], v0 offset:11264\n"
      "buffer_load_dword v18, v2, s[8:11], s36 offen sc1\n"
      "v_mfma_f32_16x16x32_bf16 v[136:139], v[48:51], v[120:123], v[136:139]\n"
      "ds_read_b128 v[100:103], v0 offset:12288\n"
      "v_rcp_f32 v162, v162\n"
      "s_sub_u32 s20, s20, 1\n"
      "s_cbranch_scc0 .Lg_loop\n"
      ".Lg_tail:\n"
      "s_nop 0\n"
      "v_mul_f32 v162, v134, v162\n"
      "v_fmac_f32 v128, v176, v144\n"
      "v_rcp_f32 v161, v161\n"
      "v_fma_f32 v160, v160, v177, v177\n"
      "v_fmac_f32 v129, v176, v145\n"
      "s_nop 0\n"
      "v_fmac_f32 v139, v176, v155\n"
      "v_fmac_f32 v131, v176, v147\n"
      "v_mul_f32 v131, v131, v159\n"
      "v_mul_f32 v161, v133, v161\n"
      "v_mul_f32 v129, v129, v157\n"
      "v_cvt_pk_bf16_f32 v165, v130, v131\n"
      "v_fma_f32 v163, v163, v177, v177\n"
      "v_rcp_f32 v163, v163\n"
      "v_fmac_f32 v136, v176, v152\n"
      "v_fmac_f32 v138, v176, v154\n"
      "v_fmac_f32 v137, v176, v153\n"
      "v_mul_f32 v128, v128, v156\n"
      "v_cvt_pk_bf16_f32 v164, v128, v129\n"
      "v_mul_f32 v137, v137, v161\n"
      "v_mul_f32 v163, v135, v163\n"
      "v_rcp_f32 v160, v160\n"
      "s_nop 0\n"
      "v_mul_f32 v160, v132, v160\n"
      "v_mul_f32 v138, v138, v162\n"
      "v_mul_f32 v136, v136, v160\n"
      "v_mul_f32 v139, v139, v163\n"
      "v_cvt_pk_bf16_f32 v166, v136, v137\n"
      "v_cvt_pk_bf16_f32 v167, v138, v139\n"
      "buffer_store_dwordx4 v[164:167], v0, s[8:11], s37 offen sc1\n"
      "s_add_u32 s37, s37, 165888\n"
      "s_add_u32 s42, s42, 165888\n"
      "s_waitcnt vmcnt(0)\n"
      "buffer_store_dword v15, v3, s[8:11], s42 offen sc1\n"
      "s_add_u32 s42, s42, 165888\n"
      "s_endpgm\n"
      ".Lsp_g0_1:\n"
      "s_mov_b32 s22, s23\n"
      ".Lsl_g0_1:\n"
      "buffer_load_dword v18, v2, s[8:11], s40 offen sc1\n"
      "s_waitcnt vmcnt(0)\n"
      "v_cmp_ne_u32 vcc, s21, v18\n"
      "s_cbranch_vccz .Lbk_g0_1\n"
      "s_sleep 4\n"
      "s_sub_u32 s22, s22, 1\n"
      "s_cbranch_scc0 .Lsl_g0_1\n"
      "s_mov_b32 s23, 0\n"
      "s_branch .Lbk_g0_1\n"
      ".Lsp_g0_2:\n"
      "s_mov_b32 s22, s23\n"
      ".Lsl_g0_2:\n"
      "buffer_load_dword v18, v2, s[8:11], s40 offen sc1\n"
      "s_waitcnt vmcnt(0)\n"
      "v_cmp_ne_u32 vcc, s21, v18\n"
      "s_cbranch_vccz .Lbk_g0_2\n"
      "s_sleep 4\n"
      "s_sub_u32 s22, s22, 1\n"
      "s_cbranch_scc0 .Lsl_g0_2\n"
      "s_mov_b32 s23, 0\n"
      "s_branch .Lbk_g0_2\n"
      ".Lsp_gy0_3:\n"
      "s_lshl_b32 s22, s23, 4\n"
      ".Lsl_gy0_3:\n"
      "ds_read_b32 v12, v14\n"
      "s_waitcnt lgkmcnt(0)\n"
      "v_cmp_gt_i32 vcc, v11, v12\n"
      "s_cbranch_vccz .Lbk_gy0_3\n"
      "s_sub_u32 s22, s22, 1\n"
      "s_cbranch_scc0 .Lsl_gy0_3\n"
      "s_mov_b32 s23, 0\n"
      "s_branch .Lbk_gy0_3\n"
      ".Lsp_g0_4:\n"
      "s_mov_b32 s22, s23\n"
      "buffer_load_dword v18, v2, s[8:11], s40 offen sc1\n"
      "s_waitcnt vmcnt(0)\n"
      "v_cmp_ne_u32 vcc, s21, v18\n"
      "s_add_u32 s30, s42, 165888\n"
      "buffer_store_dword v15, v3, s[8:11], s30 offen sc1\n"
      "s_cbranch_vccz .Lbk_g0_4\n"
      ".Lsl_g0_4:\n"
      "buffer_load_dword v18, v2, s[8:11], s40 offen sc1\n"
      "s_waitcnt vmcnt(0)\n"
      "v_cmp_ne_u32 vcc, s21, v18\n"
      "s_cbranch_vccz .Lbk_g0_4\n"
      "s_sleep 4\n"
      "s_sub_u32 s22, s22, 1\n"
      "s_cbranch_scc0 .Lsl_g0_4\n"
      "s_mov_b32 s23, 0\n"
      "s_branch .Lbk_g0_4\n"
      ".Lsp_gy0_5:\n"
      "s_lshl_b32 s22, s23, 4\n"
      ".Lsl_gy0_5:\n"
      "ds_read_b32 v12, v14\n"
      "s_waitcnt lgkmcnt(0)\n"
      "v_cmp_gt_i32 vcc, v11, v12\n"
      "s_cbranch_vccz .Lbk_gy0_5\n"
      "s_sub_u32 s22, s22, 1\n"
      "s_cbranch_scc0 .Lsl_gy0_5\n"
      "s_mov_b32 s23, 0\n"
      "s_branch .Lbk_gy0_5\n"
      ".Lsp_g0_6:\n"
      "s_mov_b32 s22, s23\n"
      "buffer_load_dword v18, v2, s[8:11], s40 offen sc1\n"
      "s_waitcnt vmcnt(0)\n"
      "v_cmp_ne_u32 vcc, s21, v18\n"
      "s_add_u32 s30, s42, 165888\n"
      "buffer_store_dword v15, v3, s[8:11], s30 offen sc1\n"
      "s_cbranch_vccz .Lbk_g0_6\n"
      ".Lsl_g0_6:\n"
      "buffer_load_dword v18, v2, s[8:11], s40 offen sc1\n"
      "s_waitcnt vmcnt(0)\n"
      "v_cmp_ne_u32 vcc, s21, v18\n"
      "s_cbranch_vccz .Lbk_g0_6\n"
      "s_sleep 4\n"
      "s_sub_u32 s22, s22, 1\n"
      "s_cbranch_scc0 .Lsl_g0_6\n"
      "s_mov_b32 s23, 0\n"
      "s_branch .Lbk_g0_6\n"
      ".Lsp_gy0_7:\n"
      "s_lshl_b32 s22, s23, 4\n"
      ".Lsl_gy0_7:\n"
      "ds_read_b32 v12, v14\n"
      "s_waitcnt lgkmcnt(0)\n"
      "v_cmp_gt_i32 vcc, v11, v12\n"
      "s_cbranch_vccz .Lbk_gy0_7\n"
      "s_sub_u32 s22, s22, 1\n"
      "s_cbranch_scc0 .Lsl_gy0_7\n"
      "s_mov_b32 s23, 0\n"
      "s_branch .Lbk_gy0_7\n"
      ".Lsp_g0_8:\n"
      "s_mov_b32 s22, s23\n"
      "buffer_load_dword v18, v2, s[8:11], s40 offen sc1\n"
      "s_waitcnt vmcnt(0)\n"
      "v_cmp_ne_u32 vcc, s21, v18\n"
      "s_add_u32 s30, s42, 165888\n"
      "buffer_store_dword v15, v3, s[8:11], s30 offen sc1\n"
      "s_cbranch_vccz .Lbk_g0_8\n"
      ".Lsl_g0_8:\n"
      "buffer_load_dword v18, v2, s[8:11], s40 offen sc1\n"
      "s_waitcnt vmcnt(0)\n"
      "v_cmp_ne_u32 vcc, s21, v18\n"
      "s_cbranch_vccz .Lbk_g0_8\n"
      "s_sleep 4\n"
      "s_sub_u32 s22, s22, 1\n"
      "s_cbranch_scc0 .Lsl_g0_8\n"
      "s_mov_b32 s23, 0\n"
      "s_branch .Lbk_g0_8\n"
      ".Lsp_gy0_9:\n"
      "s_lshl_b32 s22, s23, 4\n"
      ".Lsl_gy0_9:\n"
      "ds_read_b32 v12, v14\n"
      "s_waitcnt lgkmcnt(0)\n"
      "v_cmp_gt_i32 vcc, v11, v12\n"
      "s_cbranch_vccz .Lbk_gy0_9\n"
      "s_sub_u32 s22, s22, 1\n"
      "s_cbranch_scc0 .Lsl_gy0_9\n"
      "s_mov_b32 s23, 0\n"
      "s_branch .Lbk_gy0_9\n"
      ".Lsp_g0_10:\n"
      "s_mov_b32 s22, s23\n"
      "buffer_load_dword v18, v2, s[8:11], s40 offen sc1\n"
      "s_waitcnt vmcnt(0)\n"
      "v_cmp_ne_u32 vcc, s21, v18\n"
      "s_add_u32 s30, s42, 165888\n"
      "buffer_store_dword v15, v3, s[8:11], s30 offen sc1\n"
      "s_cbranch_vccz .Lbk_g0_10\n"
      ".Lsl_g0_10:\n"
      "buffer_load_dword v18, v2, s[8:11], s40 offen sc1\n"
      "s_waitcnt vmcnt(0)\n"
      "v_cmp_ne_u32 vcc, s21, v18\n"
      "s_cbranch_vccz .Lbk_g0_10\n"
      "s_sleep 4\n"
      "s_sub_u32 s22, s22, 1\n"
      "s_cbranch_scc0 .Lsl_g0_10\n"
      "s_mov_b32 s23, 0\n"
      "s_branch .Lbk_g0_10\n"
      ".Lsp_gy0_11:\n"
      "s_lshl_b32 s22, s23, 4\n"
      ".Lsl_gy0_11:\n"
      "ds_read_b32 v12, v14\n"
      "s_waitcnt lgkmcnt(0)\n"
      "v_cmp_gt_i32 vcc, v11, v12\n"
      "s_cbranch_vccz .Lbk_gy0_11\n"
      "s_sub_u32 s22, s22, 1\n"
      "s_cbranch_scc0 .Lsl_gy0_11\n"
      "s_mov_b32 s23, 0\n"
      "s_branch .Lbk_gy0_11\n"
      ".Lg_last:\n"
      :
      : "{s[8:11]}"(rsWS), "{s[12:15]}"(rsA), "{s[16:19]}"(rsB), "{s[92:95]}"(rsC), "{v0}"(v_l16), "{v1}"(v_l5), "{v2}"(v_fa), "{v3}"(v_ra), "{v4}"(v_st), "{v5}"(v_at), "{v6}"(v_vo0), "{v7}"(v_vo1), "{v8}"(v_vo2), "{v9}"(v_vo3), "{v10}"(v_pw), "{v12}"(v_al), "{v13}"(v_yw), "{v14}"(v_yr), "{v16}"(v_hi), "{v55}"(v_td), "{v52}"(v_tq[0]), "{v53}"(v_tq[1]), "{v54}"(v_tw), "{s36}"(s_sx), "{s37}"(s_sh), "{s38}"(s_m4), "{s39}"(s_m5), "{s43}"(s_wv)
      : "v255", "a255", "s99", "vcc", "scc", "memory");

  }
}

extern "C" void kernel_launch(void* const* d_in, const int* in_sizes, int n_in, void* d_out, int out_size,
                              void* d_ws, size_t ws_size, hipStream_t stream) {
  char* ws = (char*)d_ws + ((256 - ((size_t)d_ws & 255)) & 255);
  (void)hipFuncSetAttribute((const void*)k_all, hipFuncAttributeMaxDynamicSharedMemorySize, LDS_BYTES);
  k_all<<<256, 256, LDS_BYTES, stream>>>((const u16*)d_in[0], (const u16*)d_in[1], (const u16*)d_in[2], (const u16*)d_in[4],
                                         (const u16*)d_in[5], (const u16*)d_in[6], ws, (u16*)d_out);
}
```

```cpp
#include <hip/hip_runtime.h>
#include <stdint.h>

typedef unsigned short u16;
typedef __attribute__((ext_vector_type(4))) unsigned u32x4;
constexpr unsigned WS_DBG = 65536u;
constexpr unsigned WS_XIN = 1310720u;
constexpr unsigned WS_OB = 9175040u;
constexpr unsigned WS_X1 = 17039360u;
constexpr unsigned WS_H = 26034176u;
constexpr unsigned WS_PS = 78589952u;
constexpr unsigned WS_END = 109740032u;
constexpr unsigned ENT = 1u;
constexpr unsigned PSK = 12u;
constexpr unsigned PCN = 18u;
constexpr unsigned ROT = 0u;
constexpr unsigned QMAP = 3u;
constexpr unsigned UMAP = 0u;
constexpr unsigned DSH = 4u;
constexpr unsigned PW = 1664u;
constexpr unsigned REC = 24576u;
constexpr unsigned HREC = 165888u;
constexpr unsigned PREC = 99840u;
constexpr unsigned NREC = 80u;
constexpr unsigned OOB = 0x40000000u, NEG = 0xff61b1e6u;
constexpr int LDS_BYTES = 160 * 1024;

__device__ __forceinline__ u32x4 srd(const void* p, unsigned bytes) {
  const unsigned long long a = (unsigned long long)p;
  return u32x4{(unsigned)a, (unsigned)(a >> 32) & 0xffffu, bytes, 0x00020000u};
}

__device__ __forceinline__ unsigned unit_ks(unsigned t) { return UMAP == 0 ? t >> 2 : UMAP == 1 ? t % 12 >> 1 : t % 6; }
__device__ __forceinline__ unsigned unit_gr(unsigned t) { return UMAP == 0 ? t & 3 : UMAP == 1 ? t / 12 * 2 + (t & 1) : t / 6; }

extern "C" __global__ __launch_bounds__(256, 1) __attribute__((amdgpu_waves_per_eu(1, 1))) void k_all(
    const u16* X, const u16* Wqkv, const u16* Wo, const u16* Wg, const u16* Wu, const u16* Wd, char* ws, u16* out) {
  const unsigned b = blockIdx.x, xcd = b & 7, slot = b >> 3;
  const unsigned w = __builtin_amdgcn_readfirstlane(threadIdx.x >> 6), lane = threadIdx.x & 63;
  if (ENT) {
    const unsigned wi = slot * 4 + w, n = wi * 27 + lane, r = n / 6, m = (xcd * 128 + wi) * 4 + lane - 27, t = m / 144;
    unsigned oc = (wi * 8 + (lane & 7)) * 128;
    asm volatile("s_getpc_b64 vcc\n.Lent_pc:\n\ts_sub_u32 vcc_lo, vcc_lo, .Lent_pc-k_all\n\ts_subb_u32 vcc_hi, vcc_hi, 0\n\ts_mov_b32 m0, %1\n\t"
                 "v_min_u32 %0, .Lq_last-k_all, %0\n\tglobal_load_lds_dword %0, vcc" : "+v"(oc) : "s"(162816 + w * 256) : "vcc", "scc", "memory");
    unsigned oq = r * 3456 + (n - 6 * r >> 1) * 1152 + (xcd + (n & 1)) * 128, ox = (32 * (t & 3) + 31 - (t >> 2)) * 18432 + (m - 144 * t) * 128;
    asm volatile("" : "+v"(oq), "+v"(ox));
    oq = lane < 27 ? oq : lane < 31 ? ox : 0;
    asm volatile("" : "+v"(oq));
    __builtin_amdgcn_sched_barrier(0);
    const char* pa = (const char*)(lane < 27 ? Wqkv : X) + oq;
    asm volatile("global_load_lds_dword %0, off" ::"v"(pa) : "memory");
    __builtin_amdgcn_sched_barrier(0);
  }
  const unsigned i16 = lane & 15, g = lane >> 4, q = i16 >> 2, p = lane & 3;
  const u32x4 rsWS = srd(ws, WS_END);
  const unsigned v_l16 = lane * 16, v_dbg = WS_DBG + (b * 4 + w) * 1024 + lane * 4;
  const unsigned yb = slot >= 14 || slot == 4 ? 81920 : slot < 4 ? 61440 : 149760, v_yw = yb + w * 4, v_yr = yb + p * 4;
  const unsigned half = (i16 + 16 * (g >> 1)) * 16 + (g & 1) * 8;
  const unsigned line = lane < 32 ? lane * 4 : OOB, v_al = w ? OOB : line + 128 * (1 + b);
  const unsigned up = lane >> 5, l5 = (lane & 31) * 4, v_hi = 0u - up;
  const unsigned l8 = lane >> 3, l7 = (lane & 7) * 16;
  (void)v_dbg;
  if (slot < 4) {
    const unsigned gr = QMAP == 2 ? 3 - (xcd & 3) : slot, hg = QMAP == 2 ? (xcd >> 2) * 4 + slot : xcd, h = 4 * hg + w, par = i16 & 1;
    const u32x4 rsA = srd(Wqkv, 576 * 1728 * 2);
    const unsigned v_l5 = w < 3 || lane < 32 ? v_l16 + 16384 - w * 3072 : OOB;
    const unsigned v_fa = 20480 - w * 4096 + (lane < 48 ? lane >> 2 : 11) * 128 + p * 4, v_ra = up * 8 * REC + l5 + 20480 + h * 128;
    const unsigned v_st = 18432 + i16 * 32 + q * 8, v_tw = 61568 + w * 128 + i16 * 8, v_tr = 61568 + w * 128 + g * 32;
    const unsigned v_om = h * 512 + half, v_oe = g ? OOB : 16384 + h * 128 + i16 * 8;
    const unsigned v_pw = 20480 + h * 128 + l8 * REC + l7;
    unsigned v_vo[4], v_bc[4], v_bp[4], v_mk[4], v_sel[4] = {0, 0, 0, 0}, v_ci[4];
#pragma unroll
    for (unsigned i = 0; i < 4; ++i) {
      const unsigned kk = 4 * g + i, ka = 4 * g + 2 * (i & 1) + par;
      v_bc[i] = (i & 1) == par && kk <= i16 ? 0u : NEG;
      v_bp[i] = (i & 1) == par && kk >= i16 + 2 ? 0u : NEG;
      v_mk[i] = (i < 2 ? ka > i16 : ka <= i16) ? 0xffffu << (16 * par) : 0u;
      v_ci[i] = p == 2 ? 0x3f800000u : 0u;
    }
    v_sel[3] = lane == 16 ? 0x3f80u : lane == 17 ? 0x3f800000u : 0u;
#pragma unroll
    for (unsigned i = 0; i < 4; ++i) {
      const unsigned c = 64 * i + lane, cc = c % 30;
      v_vo[i] = c < 240 && cc < 27 ? (8 * w + c / 30) * 3456 + cc / 9 * 1152 + 144 * hg + cc % 9 * 16 - 1024 * i : OOB;
    }
    const unsigned rb = 65536 + g * 4224 + q * 480;
    const unsigned v_at = rb + 36 * w + 4 * (w & 1) + 8 * p, v_ax = rb + (p < 2 ? p : 2) * 144 + 72 * (w >> 1) + 32;
    const unsigned v_pm = (16 * g + 4 * (i16 >> 1 & 3) + 2 * (w & 1) + par) * 4, v_ms = i16 < 6 ? ~0u : 0u;
    const unsigned s_sx = WS_XIN + gr * NREC * REC + w * 4096, s_ev = s_sx + 32 * REC, s_del = 0u - (gr ? NREC + 32 : 32) * REC;
    const unsigned s_kil = gr ? 0x7f7fffffu : 0xff7fffffu, s_so = WS_OB + gr * NREC * REC;
    const unsigned s_m4 = w * 4096, s_m5 = 16384 + w * 1024, s_ms = w * 4224, s_up = b + w;
    asm volatile(
      "s_buffer_load_dword s21, s[8:11], 0\n"
      "s_mov_b32 s23, 65536\n"
      "s_mov_b32 s31, 0x40000000\n"
      "s_mov_b32 s51, 0\n"
      "s_add_u32 m0, s43, 65536\n"
      "s_nop 0\n"
      "buffer_load_dwordx4 v48, s[12:15], s51 offen offset:0 lds\n"
      "buffer_load_dwordx4 v49, s[12:15], s51 offen offset:1024 lds\n"
      "buffer_load_dwordx4 v50, s[12:15], s51 offen offset:2048 lds\n"
      "buffer_load_dwordx4 v51, s[12:15], s51 offen offset:3072 lds\n"
      "s_add_u32 s51, s51, 110592\n"
      "s_add_u32 m0, s43, 82432\n"
      "s_nop 0\n"
      "buffer_load_dwordx4 v48, s[12:15], s51 offen offset:0 lds\n"
      "buffer_load_dwordx4 v49, s[12:15], s51 offen offset:1024 lds\n"
      "buffer_load_dwordx4 v50, s[12:15], s51 offen offset:2048 lds\n"
      "buffer_load_dwordx4 v51, s[12:15], s51 offen offset:3072 lds\n"
      "s_add_u32 s51, s51, 110592\n"
      "s_add_u32 m0, s43, 99328\n"
      "s_nop 0\n"
      "buffer_load_dwordx4 v48, s[12:15], s51 offen offset:0 lds\n"
      "buffer_load_dwordx4 v49, s[12:15], s51 offen offset:1024 lds\n"
      "buffer_load_dwordx4 v50, s[12:15], s51 offen offset:2048 lds\n"
      "buffer_load_dwordx4 v51, s[12:15], s51 offen offset:3072 lds\n"
      "s_add_u32 s51, s51, 110592\n"
      "s_add_u32 s6, s36, 1572864\n"
      "s_add_u32 s36, s36, 4294942720\n"
      "s_add_u32 s48, s37, 811008\n"
      "s_mov_b32 s53, 0x7f7fffff\n"
      "v_mov_b32 v179, 0\n"
      "s_mov_b32 s54, 0xffff\n"
      "s_mov_b32 s55, 0\n"
      "v_mov_b32 v9, 0\n"
      "ds_write_b32 v11, v9\n"
      "v_mov_b32 v56, 0x3f803f80\n"
      "v_mov_b32 v57, 0x3f803f80\n"
      "v_mov_b32 v58, 0x3f803f80\n"
      "v_mov_b32 v59, 0x3f803f80\n"
      "s_mov_b64 exec, 0\n"
      "s_lshr_b32 s22, s41, 12\n"
      ".Lq_pl:\n"
      "s_cmp_eq_u32 s22, 0\n"
      "s_cbranch_scc1 .Lq_p0\n"
      "s_cmp_eq_u32 s22, 1\n"
      "s_cbranch_scc1 .Lq_p1\n"
      "s_branch .Lq_pe\n"
      ".Lq_pr:\n"
      "s_add_u32 s22, s22, 4\n"
      "s_branch .Lq_pl\n"
      ".Lq_pe:\n"
      "s_mov_b64 exec, -1\n"
      "s_waitcnt vmcnt(8) lgkmcnt(0)\n"
      "s_barrier\n"
      "ds_read_b64_tr_b16 v[60:61], v53 offset:0\n"
      "ds_read_b64_tr_b16 v[62:63], v53 offset:1920\n"
      "ds_read_b64_tr_b16 a[0:1], v52 offset:144\n"
      "ds_read_b64_tr_b16 a[2:3], v52 offset:2064\n"
      "ds_read_b64_tr_b16 a[72:73], v52 offset:288\n"
      "ds_read_b64_tr_b16 a[74:75], v52 offset:2208\n"
      "ds_read_b64_tr_b16 a[144:145], v52 offset:0\n"
      "ds_read_b64_tr_b16 a[146:147], v52 offset:1920\n"
      "v_add_u32 v52, 16896, v52\n"
      "v_add_u32 v53, 16896, v53\n"
      "s_add_u32 m0, s43, 116224\n"
      "s_nop 0\n"
      "buffer_load_dwordx4 v48, s[12:15], s51 offen offset:0 lds\n"
      "buffer_load_dwordx4 v49, s[12:15], s51 offen offset:1024 lds\n"
      "buffer_load_dwordx4 v50, s[12:15], s51 offen offset:2048 lds\n"
      "buffer_load_dwordx4 v51, s[12:15], s51 offen offset:3072 lds\n"
      "s_add_u32 s51, s51, 110592\n"
      "s_waitcnt vmcnt(8) lgkmcnt(0)\n"
      "s_barrier\n"
      "ds_read_b64_tr_b16 v[68:69], v53 offset:0\n"
      "ds_read_b64_tr_b16 v[70:71], v53 offset:1920\n"
      "ds_read_b64_tr_b16 a[4:5], v52 offset:144\n"
      "ds_read_b64_tr_b16 a[6:7], v52 offset:2064\n"
      "ds_read_b64_tr_b16 a[76:77], v52 offset:288\n"
      "ds_read_b64_tr_b16 a[78:79], v52 offset:2208\n"
      "ds_read_b64_tr_b16 a[148:149], v52 offset:0\n"
      "ds_read_b64_tr_b16 a[150:151], v52 offset:1920\n"
      "v_add_u32 v52, 16896, v52\n"
      "v_add_u32 v53, 16896, v53\n"
      "ds_bpermute_b32 v64, v54, v60\n"
      "ds_bpermute_b32 v65, v54, v61\n"
      "ds_bpermute_b32 v66, v54, v62\n"
      "ds_bpermute_b32 v67, v54, v63\n"
      "v_mfma_f32_16x16x32_bf16 v[136:139], a[0:3], v[56:59], 0\n"
      "v_mfma_f32_16x16x32_bf16 v[144:147], v[56:59], a[72:75], 0\n"
      "v_mfma_f32_16x16x32_bf16 v[148:151], a[144:147], v[56:59], 0\n"
      "s_add_u32 m0, s43, 65536\n"
      "s_nop 0\n"
      "buffer_load_dwordx4 v48, s[12:15], s51 offen offset:0 lds\n"
      "buffer_load_dwordx4 v49, s[12:15], s51 offen offset:1024 lds\n"
      "buffer_load_dwordx4 v50, s[12:15], s51 offen offset:2048 lds\n"
      "buffer_load_dwordx4 v51, s[12:15], s51 offen offset:3072 lds\n"
      "s_add_u32 s51, s51, 110592\n"
      "s_waitcnt vmcnt(8) lgkmcnt(0)\n"
      "s_barrier\n"
      "ds_read_b64_tr_b16 v[60:61], v53 offset:0\n"
      "ds_read_b64_tr_b16 v[62:63], v53 offset:1920\n"
      "ds_read_b64_tr_b16 a[8:9], v52 offset:144\n"
      "ds_read_b64_tr_b16 a[10:11], v52 offset:2064\n"
      "ds_read_b64_tr_b16 a[80:81], v52 offset:288\n"
      "ds_read_b64_tr_b16 a[82:83], v52 offset:2208\n"
      "ds_read_b64_tr_b16 a[152:153], v52 offset:0\n"
      "ds_read_b64_tr_b16 a[154:155], v52 offset:1920\n"
      "v_add_u32 v52, 16896, v52\n"
      "v_add_u32 v53, 16896, v53\n"
      "ds_bpermute_b32 v72, v54, v68\n"
      "ds_bpermute_b32 v73, v54, v69\n"
      "ds_bpermute_b32 v74, v54, v70\n"
      "ds_bpermute_b32 v75, v54, v71\n"
      "v_mfma_f32_16x16x32_bf16 v[136:139], a[4:7], v[56:59], v[136:139]\n"
      "v_mfma_f32_16x16x32_bf16 v[144:147], v[56:59], a[76:79], v[144:147]\n"
      "v_mfma_f32_16x16x32_bf16 v[148:151], a[148:151], v[56:59], v[148:151]\n"
      "s_add_u32 m0, s43, 82432\n"
      "s_nop 0\n"
      "buffer_load_dwordx4 v48, s[12:15], s51 offen offset:0 lds\n"
      "buffer_load_dwordx4 v49, s[12:15], s51 offen offset:1024 lds\n"
      "buffer_load_dwordx4 v50, s[12:15], s51 offen offset:2048 lds\n"
      "buffer_load_dwordx4 v51, s[12:15], s51 offen offset:3072 lds\n"
      "s_add_u32 s51, s51, 110592\n"
      "v_and_b32 v64, v55, v64\n"
      "v_and_b32 v65, v55, v65\n"
      "v_and_b32 v66, v55, v66\n"
      "v_and_b32 v67, v55, v67\n"
      "v_accvgpr_write_b32 a216, v64\n"
      "v_accvgpr_write_b32 a217, v65\n"
      "v_accvgpr_write_b32 a218, v66\n"
      "v_accvgpr_write_b32 a219, v67\n"
      "v_mfma_f32_16x16x32_bf16 v[140:143], v[64:67], v[56:59], 0\n"
      "s_waitcnt vmcnt(8) lgkmcnt(0)\n"
      "s_barrier\n"
      "ds_read_b64_tr_b16 v[68:69], v53 offset:0\n"
      "ds_read_b64_tr_b16 v[70:71], v53 offset:1920\n"
      "ds_read_b64_tr_b16 a[12:13], v52 offset:144\n"
      "ds_read_b64_tr_b16 a[14:15], v52 offset:2064\n"
      "ds_read_b64_tr_b16 a[84:85], v52 offset:288\n"
      "ds_read_b64_tr_b16 a[86:87], v52 offset:2208\n"
      "ds_read_b64_tr_b16 a[156:157], v52 offset:0\n"
      "ds_read_b64_tr_b16 a[158:159], v52 offset:1920\n"
      "v_add_u32 v52, 4294916608, v52\n"
      "v_add_u32 v53, 4294916608, v53\n"
      "ds_bpermute_b32 v64, v54, v60\n"
      "ds_bpermute_b32 v65, v54, v61\n"
      "ds_bpermute_b32 v66, v54, v62\n"
      "ds_bpermute_b32 v67, v54, v63\n"
      "v_mfma_f32_16x16x32_bf16 v[136:139], a[8:11], v[56:59], v[136:139]\n"
      "v_mfma_f32_16x16x32_bf16 v[144:147], v[56:59], a[80:83], v[144:147]\n"
      "v_mfma_f32_16x16x32_bf16 v[148:151], a[152:155], v[56:59], v[148:151]\n"
      "s_add_u32 m0, s43, 99328\n"
      "s_nop 0\n"
      "buffer_load_dwordx4 v48, s[12:15], s51 offen offset:0 lds\n"
      "buffer_load_dwordx4 v49, s[12:15], s51 offen offset:1024 lds\n"
      "buffer_load_dwordx4 v50, s[12:15], s51 offen offset:2048 lds\n"
      "buffer_load_dwordx4 v51, s[12:15], s51 offen offset:3072 lds\n"
      "s_add_u32 s51, s51, 110592\n"
      "v_and_b32 v72, v55, v72\n"
      "v_and_b32 v73, v55, v73\n"
      "v_and_b32 v74, v55, v74\n"
      "v_and_b32 v75, v55, v75\n"
      "v_accvgpr_write_b32 a220, v72\n"
      "v_accvgpr_write_b32 a221, v73\n"
      "v_accvgpr_write_b32 a222, v74\n"
      "v_accvgpr_write_b32 a223, v75\n"
      "v_mfma_f32_16x16x32_bf16 v[140:143], v[72:75], v[56:59], v[140:143]\n"
      "s_waitcnt vmcnt(8) lgkmcnt(0)\n"
      "s_barrier\n"
      "ds_read_b64_tr_b16 v[60:61], v53 offset:0\n"
      "ds_read_b64_tr_b16 v[62:63], v53 offset:1920\n"
      "ds_read_b64_tr_b16 a[16:17], v52 offset:144\n"
      "ds_read_b64_tr_b16 a[18:19], v52 offset:2064\n"
      "ds_read_b64_tr_b16 a[88:89], v52 offset:288\n"
      "ds_read_b64_tr_b16 a[90:91], v52 offset:2208\n"
      "ds_read_b64_tr_b16 a[160:161], v52 offset:0\n"
      "ds_read_b64_tr_b16 a[162:163], v52 offset:1920\n"
      "v_add_u32 v52, 16896, v52\n"
      "v_add_u32 v53, 16896, v53\n"
      "ds_bpermute_b32 v72, v54, v68\n"
      "ds_bpermute_b32 v73, v54, v69\n"
      "ds_bpermute_b32 v74, v54, v70\n"
      "ds_bpermute_b32 v75, v54, v71\n"
      "v_mfma_f32_16x16x32_bf16 v[136:139], a[12:15], v[56:59], v[136:139]\n"
      "v_mfma_f32_16x16x32_bf16 v[144:147], v[56:59], a[84:87], v[144:147]\n"
      "v_mfma_f32_16x16x32_bf16 v[148:151], a[156:159], v[56:59], v[148:151]\n"
      "s_add_u32 m0, s43, 116224\n"
      "s_nop 0\n"
      "buffer_load_dwordx4 v48, s[12:15], s51 offen offset:0 lds\n"
      "buffer_load_dwordx4 v49, s[12:15], s51 offen offset:1024 lds\n"
      "buffer_load_dwordx4 v50, s[12:15], s51 offen offset:2048 lds\n"
      "buffer_load_dwordx4 v51, s[12:15], s51 offen offset:3072 lds\n"
      "s_add_u32 s51, s51, 110592\n"
      "v_and_b32 v64, v55, v64\n"
      "v_and_b32 v65, v55, v65\n"
      "v_and_b32 v66, v55, v66\n"
      "v_and_b32 v67, v55, v67\n"
      "v_accvgpr_write_b32 a224, v64\n"
      "v_accvgpr_write_b32 a225, v65\n"
      "v_accvgpr_write_b32 a226, v66\n"
      "v_accvgpr_write_b32 a227, v67\n"
      "v_mfma_f32_16x16x32_bf16 v[140:143], v[64:67], v[56:59], v[140:143]\n"
      "s_waitcnt vmcnt(8) lgkmcnt(0)\n"
      "s_barrier\n"
      "ds_read_b64_tr_b16 v[68:69], v53 offset:0\n"
      "ds_read_b64_tr_b16 v[70:71], v53 offset:1920\n"
      "ds_read_b64_tr_b16 a[20:21], v52 offset:144\n"
      "ds_read_b64_tr_b16 a[22:23], v52 offset:2064\n"
      "ds_read_b64_tr_b16 a[92:93], v52 offset:288\n"
      "ds_read_b64_tr_b16 a[94:95], v52 offset:2208\n"
      "ds_read_b64_tr_b16 a[164:165], v52 offset:0\n"
      "ds_read_b64_tr_b16 a[166:167], v52 offset:1920\n"
      "v_add_u32 v52, 16896, v52\n"
      "v_add_u32 v53, 16896, v53\n"
      "ds_bpermute_b32 v64, v54, v60\n"
      "ds_bpermute_b32 v65, v54, v61\n"
      "ds_bpermute_b32 v66, v54, v62\n"
      "ds_bpermute_b32 v67, v54, v63\n"
      "v_mfma_f32_16x16x32_bf16 v[136:139], a[16:19], v[56:59], v[136:139]\n"
      "v_mfma_f32_16x16x32_bf16 v[144:147], v[56:59], a[88:91], v[144:147]\n"
      "v_mfma_f32_16x16x32_bf16 v[148:151], a[160:163], v[56:59], v[148:151]\n"
      "s_add_u32 m0, s43, 65536\n"
      "s_nop 0\n"
      "buffer_load_dwordx4 v48, s[12:15], s51 offen offset:0 lds\n"
      "buffer_load_dwordx4 v49, s[12:15], s51 offen offset:1024 lds\n"
      "buffer_load_dwordx4 v50, s[12:15], s51 offen offset:2048 lds\n"
      "buffer_load_dwordx4 v51, s[12:15], s51 offen offset:3072 lds\n"
      "s_add_u32 s51, s51, 110592\n"
      "v_and_b32 v72, v55, v72\n"
      "v_and_b32 v73, v55, v73\n"
      "v_and_b32 v74, v55, v74\n"
      "v_and_b32 v75, v55, v75\n"
      "v_accvgpr_write_b32 a228, v72\n"
      "v_accvgpr_write_b32 a229, v73\n"
      "v_accvgpr_write_b32 a230, v74\n"
      "v_accvgpr_write_b32 a231, v75\n"
      "v_mfma_f32_16x16x32_bf16 v[140:143], v[72:75], v[56:59], v[140:143]\n"
      "s_waitcnt vmcnt(8) lgkmcnt(0)\n"
      "s_barrier\n"
      "ds_read_b64_tr_b16 v[60:61], v53 offset:0\n"
      "ds_read_b64_tr_b16 v[62:63], v53 offset:1920\n"
      "ds_read_b64_tr_b16 a[24:25], v52 offset:144\n"
      "ds_read_b64_tr_b16 a[26:27], v52 offset:2064\n"
      "ds_read_b64_tr_b16 a[96:97], v52 offset:288\n"
      "ds_read_b64_tr_b16 a[98:99], v52 offset:2208\n"
      "ds_read_b64_tr_b16 a[168:169], v52 offset:0\n"
      "ds_read_b64_tr_b16 a[170:171], v52 offset:1920\n"
      "v_add_u32 v52, 16896, v52\n"
      "v_add_u32 v53, 16896, v53\n"
      "ds_bpermute_b32 v72, v54, v68\n"
      "ds_bpermute_b32 v73, v54, v69\n"
      "ds_bpermute_b32 v74, v54, v70\n"
      "ds_bpermute_b32 v75, v54, v71\n"
      "v_mfma_f32_16x16x32_bf16 v[136:139], a[20:23], v[56:59], v[136:139]\n"
      "v_mfma_f32_16x16x32_bf16 v[144:147], v[56:59], a[92:95], v[144:147]\n"
      "v_mfma_f32_16x16x32_bf16 v[148:151], a[164:167], v[56:59], v[148:151]\n"
      "s_add_u32 m0, s43, 82432\n"
      "s_nop 0\n"
      "buffer_load_dwordx4 v48, s[12:15], s51 offen offset:0 lds\n"
      "buffer_load_dwordx4 v49, s[12:15], s51 offen offset:1024 lds\n"
      "buffer_load_dwordx4 v50, s[12:15], s51 offen offset:2048 lds\n"
      "buffer_load_dwordx4 v51, s[12:15], s51 offen offset:3072 lds\n"
      "s_add_u32 s51, s51, 110592\n"
      "v_and_b32 v64, v55, v64\n"
      "v_and_b32 v65, v55, v65\n"
      "v_and_b32 v66, v55, v66\n"
      "v_and_b32 v67, v55, v67\n"
      "v_accvgpr_write_b32 a232, v64\n"
      "v_accvgpr_write_b32 a233, v65\n"
      "v_accvgpr_write_b32 a234, v66\n"
      "v_accvgpr_write_b32 a235, v67\n"
      "v_mfma_f32_16x16x32_bf16 v[140:143], v[64:67], v[56:59], v[140:143]\n"
      "s_waitcnt vmcnt(8) lgkmcnt(0)\n"
      "s_barrier\n"
      "ds_read_b64_tr_b16 v[68:69], v53 offset:0\n"
      "ds_read_b64_tr_b16 v[70:71], v53 offset:1920\n"
      "ds_read_b64_tr_b16 a[28:29], v52 offset:144\n"
      "ds_read_b64_tr_b16 a[30:31], v52 offset:2064\n"
      "ds_read_b64_tr_b16 a[100:101], v52 offset:288\n"
      "ds_read_b64_tr_b16 a[102:103], v52 offset:2208\n"
      "ds_read_b64_tr_b16 a[172:173], v52 offset:0\n"
      "ds_read_b64_tr_b16 a[174:175], v52 offset:1920\n"
      "v_add_u32 v52, 4294916608, v52\n"
      "v_add_u32 v53, 4294916608, v53\n"
      "ds_bpermute_b32 v64, v54, v60\n"
      "ds_bpermute_b32 v65, v54, v61\n"
      "ds_bpermute_b32 v66, v54, v62\n"
      "ds_bpermute_b32 v67, v54, v63\n"
      "v_mfma_f32_16x16x32_bf16 v[136:139], a[24:27], v[56:59], v[136:139]\n"
      "v_mfma_f32_16x16x32_bf16 v[144:147], v[56:59], a[96:99], v[144:147]\n"
      "v_mfma_f32_16x16x32_bf16 v[148:151], a[168:171], v[56:59], v[148:151]\n"
      "s_add_u32 m0, s43, 99328\n"
      "s_nop 0\n"
      "buffer_load_dwordx4 v48, s[12:15], s51 offen offset:0 lds\n"
      "buffer_load_dwordx4 v49, s[12:15], s51 offen offset:1024 lds\n"
      "buffer_load_dwordx4 v50, s[12:15], s51 offen offset:2048 lds\n"
      "buffer_load_dwordx4 v51, s[12:15], s51 offen offset:3072 lds\n"
      "s_add_u32 s51, s51, 110592\n"
      "v_and_b32 v72, v55, v72\n"
      "v_and_b32 v73, v55, v73\n"
      "v_and_b32 v74, v55, v74\n"
      "v_and_b32 v75, v55, v75\n"
      "v_accvgpr_write_b32 a236, v72\n"
      "v_accvgpr_write_b32 a237, v73\n"
      "v_accvgpr_write_b32 a238, v74\n"
      "v_accvgpr_write_b32 a239, v75\n"
      "v_mfma_f32_16x16x32_bf16 v[140:143], v[72:75], v[56:59], v[140:143]\n"
      "s_waitcnt vmcnt(8) lgkmcnt(0)\n"
      "s_barrier\n"
      "ds_read_b64_tr_b16 v[60:61], v53 offset:0\n"
      "ds_read_b64_tr_b16 v[62:63], v53 offset:1920\n"
      "ds_read_b64_tr_b16 a[32:33], v52 offset:144\n"
      "ds_read_b64_tr_b16 a[34:35], v52 offset:2064\n"
      "ds_read_b64_tr_b16 a[104:105], v52 offset:288\n"
      "ds_read_b64_tr_b16 a[106:107], v52 offset:2208\n"
      "ds_read_b64_tr_b16 a[176:177], v52 offset:0\n"
      "ds_read_b64_tr_b16 a[178:179], v52 offset:1920\n"
      "v_add_u32 v52, 16896, v52\n"
      "v_add_u32 v53, 16896, v53\n"
      "ds_bpermute_b32 v72, v54, v68\n"
      "ds_bpermute_b32 v73, v54, v69\n"
      "ds_bpermute_b32 v74, v54, v70\n"
      "ds_bpermute_b32 v75, v54, v71\n"
      "v_mfma_f32_16x16x32_bf16 v[136:139], a[28:31], v[56:59], v[136:139]\n"
      "v_mfma_f32_16x16x32_bf16 v[144:147], v[56:59], a[100:103], v[144:147]\n"
      "v_mfma_f32_16x16x32_bf16 v[148:151], a[172:175], v[56:59], v[148:151]\n"
      "s_add_u32 m0, s43, 116224\n"
      "s_nop 0\n"
      "buffer_load_dwordx4 v48, s[12:15], s51 offen offset:0 lds\n"
      "buffer_load_dwordx4 v49, s[12:15], s51 offen offset:1024 lds\n"
      "buffer_load_dwordx4 v50, s[12:15], s51 offen offset:2048 lds\n"
      "buffer_load_dwordx4 v51, s[12:15], s51 offen offset:3072 lds\n"
      "s_add_u32 s51, s51, 110592\n"
      "v_and_b32 v64, v55, v64\n"
      "v_and_b32 v65, v55, v65\n"
      "v_and_b32 v66, v55, v66\n"
      "v_and_b32 v67, v55, v67\n"
      "v_accvgpr_write_b32 a240, v64\n"
      "v_accvgpr_write_b32 a241, v65\n"
      "v_accvgpr_write_b32 a242, v66\n"
      "v_accvgpr_write_b32 a243, v67\n"
      "v_mfma_f32_16x16x32_bf16 v[140:143], v[64:67], v[56:59], v[140:143]\n"
      "s_waitcnt vmcnt(8) lgkmcnt(0)\n"
      "s_barrier\n"
      "ds_read_b64_tr_b16 v[68:69], v53 offset:0\n"
      "ds_read_b64_tr_b16 v[70:71], v53 offset:1920\n"
      "ds_read_b64_tr_b16 a[36:37], v52 offset:144\n"
      "ds_read_b64_tr_b16 a[38:39], v52 offset:2064\n"
      "ds_read_b64_tr_b16 a[108:109], v52 offset:288\n"
      "ds_read_b64_tr_b16 a[110:111], v52 offset:2208\n"
      "ds_read_b64_tr_b16 a[180:181], v52 offset:0\n"
      "ds_read_b64_tr_b16 a[182:183], v52 offset:1920\n"
      "v_add_u32 v52, 16896, v52\n"
      "v_add_u32 v53, 16896, v53\n"
      "ds_bpermute_b32 v64, v54, v60\n"
      "ds_bpermute_b32 v65, v54, v61\n"
      "ds_bpermute_b32 v66, v54, v62\n"
      "ds_bpermute_b32 v67, v54, v63\n"
      "v_mfma_f32_16x16x32_bf16 v[136:139], a[32:35], v[56:59], v[136:139]\n"
      "v_mfma_f32_16x16x32_bf16 v[144:147], v[56:59], a[104:107], v[144:147]\n"
      "v_mfma_f32_16x16x32_bf16 v[148:151], a[176:179], v[56:59], v[148:151]\n"
      "s_add_u32 m0, s43, 65536\n"
      "s_nop 0\n"
      "buffer_load_dwordx4 v48, s[12:15], s51 offen offset:0 lds\n"
      "buffer_load_dwordx4 v49, s[12:15], s51 offen offset:1024 lds\n"
      "buffer_load_dwordx4 v50, s[12:15], s51 offen offset:2048 lds\n"
      "buffer_load_dwordx4 v51, s[12:15], s51 offen offset:3072 lds\n"
      "s_add_u32 s51, s51, 110592\n"
      "v_and_b32 v72, v55, v72\n"
      "v_and_b32 v73, v55, v73\n"
      "v_and_b32 v74, v55, v74\n"
      "v_and_b32 v75, v55, v75\n"
      "v_accvgpr_write_b32 a244, v72\n"
      "v_accvgpr_write_b32 a245, v73\n"
      "v_accvgpr_write_b32 a246, v74\n"
      "v_accvgpr_write_b32 a247, v75\n"
      "v_mfma_f32_16x16x32_bf16 v[140:143], v[72:75], v[56:59], v[140:143]\n"
      "s_waitcnt vmcnt(8) lgkmcnt(0)\n"
      "s_barrier\n"
      "ds_read_b64_tr_b16 v[60:61], v53 offset:0\n"
      "ds_read_b64_tr_b16 v[62:63], v53 offset:1920\n"
      "ds_read_b64_tr_b16 a[40:41], v52 offset:144\n"
      "ds_read_b64_tr_b16 a[42:43], v52 offset:2064\n"
      "ds_read_b64_tr_b16 a[112:113], v52 offset:288\n"
      "ds_read_b64_tr_b16 a[114:115], v52 offset:2208\n"
      "ds_read_b64_tr_b16 a[184:185], v52 offset:0\n"
      "ds_read_b64_tr_b16 a[186:187], v52 offset:1920\n"
      "v_add_u32 v52, 16896, v52\n"
      "v_add_u32 v53, 16896, v53\n"
      "ds_bpermute_b32 v72, v54, v68\n"
      "ds_bpermute_b32 v73, v54, v69\n"
      "ds_bpermute_b32 v74, v54, v70\n"
      "ds_bpermute_b32 v75, v54, v71\n"
      "v_mfma_f32_16x16x32_bf16 v[136:139], a[36:39], v[56:59], v[136:139]\n"
      "v_mfma_f32_16x16x32_bf16 v[144:147], v[56:59], a[108:111], v[144:147]\n"
      "v_mfma_f32_16x16x32_bf16 v[148:151], a[180:183], v[56:59], v[148:151]\n"
      "s_add_u32 m0, s43, 82432\n"
      "s_nop 0\n"
      "buffer_load_dwordx4 v48, s[12:15], s51 offen offset:0 lds\n"
      "buffer_load_dwordx4 v49, s[12:15], s51 offen offset:1024 lds\n"
      "buffer_load_dwordx4 v50, s[12:15], s51 offen offset:2048 lds\n"
      "buffer_load_dwordx4 v51, s[12:15], s51 offen offset:3072 lds\n"
      "s_add_u32 s51, s51, 110592\n"
      "v_and_b32 v64, v55, v64\n"
      "v_and_b32 v65, v55, v65\n"
      "v_and_b32 v66, v55, v66\n"
      "v_and_b32 v67, v55, v67\n"
      "v_accvgpr_write_b32 a248, v64\n"
      "v_accvgpr_write_b32 a249, v65\n"
      "v_accvgpr_write_b32 a250, v66\n"
      "v_accvgpr_write_b32 a251, v67\n"
      "v_mfma_f32_16x16x32_bf16 v[140:143], v[64:67], v[56:59], v[140:143]\n"
      "s_waitcnt vmcnt(8) lgkmcnt(0)\n"
      "s_barrier\n"
      "ds_read_b64_tr_b16 v[68:69], v53 offset:0\n"
      "ds_read_b64_tr_b16 v[70:71], v53 offset:1920\n"
      "ds_read_b64_tr_b16 a[44:45], v52 offset:144\n"
      "ds_read_b64_tr_b16 a[46:47], v52 offset:2064\n"
      "ds_read_b64_tr_b16 a[116:117], v52 offset:288\n"
      "ds_read_b64_tr_b16 a[118:119], v52 offset:2208\n"
      "ds_read_b64_tr_b16 a[188:189], v52 offset:0\n"
      "ds_read_b64_tr_b16 a[190:191], v52 offset:1920\n"
      "v_add_u32 v52, 4294916608, v52\n"
      "v_add_u32 v53, 4294916608, v53\n"
      "ds_bpermute_b32 v64, v54, v60\n"
      "ds_bpermute_b32 v65, v54, v61\n"
      "ds_bpermute_b32 v66, v54, v62\n"
      "ds_bpermute_b32 v67, v54, v63\n"
      "v_mfma_f32_16x16x32_bf16 v[136:139], a[40:43], v[56:59], v[136:139]\n"
      "v_mfma_f32_16x16x32_bf16 v[144:147], v[56:59], a[112:115], v[144:147]\n"
      "v_mfma_f32_16x16x32_bf16 v[148:151], a[184:187], v[56:59], v[148:151]\n"
      "s_add_u32 m0, s43, 99328\n"
      "s_nop 0\n"
      "buffer_load_dwordx4 v48, s[12:15], s51 offen offset:0 lds\n"
      "buffer_load_dwordx4 v49, s[12:15], s51 offen offset:1024 lds\n"
      "buffer_load_dwordx4 v50, s[12:15], s51 offen offset:2048 lds\n"
      "buffer_load_dwordx4 v51, s[12:15], s51 offen offset:3072 lds\n"
      "s_add_u32 s51, s51, 110592\n"
      "v_and_b32 v72, v55, v72\n"
      "v_and_b32 v73, v55, v73\n"
      "v_and_b32 v74, v55, v74\n"
      "v_and_b32 v75, v55, v75\n"
      "v_accvgpr_write_b32 a252, v72\n"
      "v_accvgpr_write_b32 a253, v73\n"
      "v_accvgpr_write_b32 a254, v74\n"
      "v_accvgpr_write_b32 a255, v75\n"
      "v_mfma_f32_16x16x32_bf16 v[140:143], v[72:75], v[56:59], v[140:143]\n"
      "s_waitcnt vmcnt(8) lgkmcnt(0)\n"
      "s_barrier\n"
      "ds_read_b64_tr_b16 v[60:61], v53 offset:0\n"
      "ds_read_b64_tr_b16 v[62:63], v53 offset:1920\n"
      "ds_read_b64_tr_b16 a[48:49], v52 offset:144\n"
      "ds_read_b64_tr_b16 a[50:51], v52 offset:2064\n"
      "ds_read_b64_tr_b16 a[120:121], v52 offset:288\n"
      "ds_read_b64_tr_b16 a[122:123], v52 offset:2208\n"
      "ds_read_b64_tr_b16 a[192:193], v52 offset:0\n"
      "ds_read_b64_tr_b16 a[194:195], v52 offset:1920\n"
      "v_add_u32 v52, 16896, v52\n"
      "v_add_u32 v53, 16896, v53\n"
      "ds_bpermute_b32 v72, v54, v68\n"
      "ds_bpermute_b32 v73, v54, v69\n"
      "ds_bpermute_b32 v74, v54, v70\n"
      "ds_bpermute_b32 v75, v54, v71\n"
      "v_mfma_f32_16x16x32_bf16 v[136:139], a[44:47], v[56:59], v[136:139]\n"
      "v_mfma_f32_16x16x32_bf16 v[144:147], v[56:59], a[116:119], v[144:147]\n"
      "v_mfma_f32_16x16x32_bf16 v[148:151], a[188:191], v[56:59], v[148:151]\n"
      "s_add_u32 m0, s43, 116224\n"
      "s_nop 0\n"
      "buffer_load_dwordx4 v48, s[12:15], s51 offen offset:0 lds\n"
      "buffer_load_dwordx4 v49, s[12:15], s51 offen offset:1024 lds\n"
      "buffer_load_dwordx4 v50, s[12:15], s51 offen offset:2048 lds\n"
      "buffer_load_dwordx4 v51, s[12:15], s51 offen offset:3072 lds\n"
      "s_add_u32 s51, s51, 110592\n"
      "v_and_b32 v64, v55, v64\n"
      "v_and_b32 v65, v55, v65\n"
      "v_and_b32 v66, v55, v66\n"
      "v_and_b32 v67, v55, v67\n"
      "v_mov_b32 v16, v64\n"
      "v_mov_b32 v17, v65\n"
      "v_mov_b32 v18, v66\n"
      "v_mov_b32 v19, v67\n"
      "v_mfma_f32_16x16x32_bf16 v[140:143], v[64:67], v[56:59], v[140:143]\n"
      "s_add_u32 s30, s36, 24576\n"
      "buffer_load_dword v78, v2, s[8:11], s30 offen sc1\n"
      "s_add_u32 s30, s36, 49152\n"
      "buffer_load_dword v79, v2, s[8:11], s30 offen sc1\n"
      "s_waitcnt vmcnt(10) lgkmcnt(0)\n"
      "s_barrier\n"
      "ds_read_b64_tr_b16 v[68:69], v53 offset:0\n"
      "ds_read_b64_tr_b16 v[70:71], v53 offset:1920\n"
      "ds_read_b64_tr_b16 a[52:53], v52 offset:144\n"
      "ds_read_b64_tr_b16 a[54:55], v52 offset:2064\n"
      "ds_read_b64_tr_b16 a[124:125], v52 offset:288\n"
      "ds_read_b64_tr_b16 a[126:127], v52 offset:2208\n"
      "ds_read_b64_tr_b16 a[196:197], v52 offset:0\n"
      "ds_read_b64_tr_b16 a[198:199], v52 offset:1920\n"
      "v_add_u32 v52, 16896, v52\n"
      "v_add_u32 v53, 16896, v53\n"
      "ds_bpermute_b32 v64, v54, v60\n"
      "ds_bpermute_b32 v65, v54, v61\n"
      "ds_bpermute_b32 v66, v54, v62\n"
      "ds_bpermute_b32 v67, v54, v63\n"
      "v_mfma_f32_16x16x32_bf16 v[136:139], a[48:51], v[56:59], v[136:139]\n"
      "v_mfma_f32_16x16x32_bf16 v[144:147], v[56:59], a[120:123], v[144:147]\n"
      "v_mfma_f32_16x16x32_bf16 v[148:151], a[192:195], v[56:59], v[148:151]\n"
      "s_add_u32 m0, s43, 65536\n"
      "s_nop 0\n"
      "buffer_load_dwordx4 v48, s[12:15], s51 offen offset:0 lds\n"
      "buffer_load_dwordx4 v49, s[12:15], s51 offen offset:1024 lds\n"
      "buffer_load_dwordx4 v50, s[12:15], s51 offen offset:2048 lds\n"
      "buffer_load_dwordx4 v51, s[12:15], s51 offen offset:3072 lds\n"
      "s_add_u32 s51, s51, 110592\n"
      "v_and_b32 v72, v55, v72\n"
      "v_and_b32 v73, v55, v73\n"
      "v_and_b32 v74, v55, v74\n"
      "v_and_b32 v75, v55, v75\n"
      "v_mov_b32 v20, v72\n"
      "v_mov_b32 v21, v73\n"
      "v_mov_b32 v22, v74\n"
      "v_mov_b32 v23, v75\n"
      "v_mfma_f32_16x16x32_bf16 v[140:143], v[72:75], v[56:59], v[140:143]\n"
      "s_waitcnt vmcnt(10) lgkmcnt(0)\n"
      "s_barrier\n"
      "ds_read_b64_tr_b16 v[60:61], v53 offset:0\n"
      "ds_read_b64_tr_b16 v[62:63], v53 offset:1920\n"
      "ds_read_b64_tr_b16 a[56:57], v52 offset:144\n"
      "ds_read_b64_tr_b16 a[58:59], v52 offset:2064\n"
      "ds_read_b64_tr_b16 a[128:129], v52 offset:288\n"
      "ds_read_b64_tr_b16 a[130:131], v52 offset:2208\n"
      "ds_read_b64_tr_b16 a[200:201], v52 offset:0\n"
      "ds_read_b64_tr_b16 a[202:203], v52 offset:1920\n"
      "v_add_u32 v52, 16896, v52\n"
      "v_add_u32 v53, 16896, v53\n"
      "ds_bpermute_b32 v72, v54, v68\n"
      "ds_bpermute_b32 v73, v54, v69\n"
      "ds_bpermute_b32 v74, v54, v70\n"
      "ds_bpermute_b32 v75, v54, v71\n"
      "v_mfma_f32_16x16x32_bf16 v[136:139], a[52:55], v[56:59], v[136:139]\n"
      "v_mfma_f32_16x16x32_bf16 v[144:147], v[56:59], a[124:127], v[144:147]\n"
      "v_mfma_f32_16x16x32_bf16 v[148:151], a[196:199], v[56:59], v[148:151]\n"
      "s_add_u32 m0, s43, 82432\n"
      "s_nop 0\n"
      "buffer_load_dwordx4 v48, s[12:15], s51 offen offset:0 lds\n"
      "buffer_load_dwordx4 v49, s[12:15], s51 offen offset:1024 lds\n"
      "buffer_load_dwordx4 v50, s[12:15], s51 offen offset:2048 lds\n"
      "buffer_load_dwordx4 v51, s[12:15], s51 offen offset:3072 lds\n"
      "s_add_u32 s51, s51, 110592\n"
      "v_and_b32 v64, v55, v64\n"
      "v_and_b32 v65, v55, v65\n"
      "v_and_b32 v66, v55, v66\n"
      "v_and_b32 v67, v55, v67\n"
      "v_mov_b32 v24, v64\n"
      "v_mov_b32 v25, v65\n"
      "v_mov_b32 v26, v66\n"
      "v_mov_b32 v27, v67\n"
      "v_mfma_f32_16x16x32_bf16 v[140:143], v[64:67], v[56:59], v[140:143]\n"
      "s_mul_i32 s21, s21, 1664525\n"
      "s_add_u32 s21, s21, 1013904223\n"
      "v_mov_b32 v13, s21\n"
      "buffer_store_dword v13, v10, s[8:11], 0 offen sc1\n"
      "v_not_b32 v80, v13\n"
      "v_not_b32 v81, v13\n"
      "v_not_b32 v82, v13\n"
      "v_not_b32 v83, v13\n"
      "s_add_u32 s30, s40, 0\n"
      "buffer_store_dwordx4 v[80:83], v76, s[8:11], s30 offen sc1\n"
      "v_xor_b32 v13, v13, v14\n"
      "s_add_u32 s52, s40, 4294893568\n"
      "s_add_u32 s40, s40, 4294918144\n"
      "s_add_u32 s36, s36, 24576\n"
      "s_mov_b32 s45, s36\n"
      "s_min_u32 s45, s45, s6\n"
      "s_waitcnt vmcnt(11)\n"
      "v_cmp_ne_u32 vcc, s21, v78\n"
      "s_add_u32 m0, s41, 0\n"
      "s_add_u32 s36, s36, 24576\n"
      "s_mov_b32 s46, s45\n"
      "s_cbranch_vccnz .Lsp_q0_1\n"
      ".Lbk_q0_1:\n"
      "buffer_load_dwordx4 v0, s[8:11], s46 offen offset:1024 sc1 lds\n"
      "buffer_load_dwordx4 v0, s[8:11], s46 offen offset:0 sc1 lds\n"
      "buffer_load_dwordx4 v0, s[8:11], s46 offen offset:2048 sc1 lds\n"
      "s_mov_b32 s45, s36\n"
      "s_min_u32 s45, s45, s6\n"
      "buffer_load_dwordx4 v0, s[8:11], s46 offen offset:3072 sc1 lds\n"
      "s_add_u32 m0, s42, 0\n"
      "s_nop 0\n"
      "buffer_load_dwordx4 v1, s[8:11], s46 offen offset:0 sc1 lds\n"
      "s_waitcnt lgkmcnt(0)\n"
      "s_barrier\n"
      "ds_read_b64_tr_b16 v[68:69], v53 offset:0\n"
      "ds_read_b64_tr_b16 v[70:71], v53 offset:1920\n"
      "ds_read_b64_tr_b16 a[60:61], v52 offset:144\n"
      "ds_read_b64_tr_b16 a[62:63], v52 offset:2064\n"
      "ds_read_b64_tr_b16 a[132:133], v52 offset:288\n"
      "ds_read_b64_tr_b16 a[134:135], v52 offset:2208\n"
      "ds_read_b64_tr_b16 a[204:205], v52 offset:0\n"
      "ds_read_b64_tr_b16 a[206:207], v52 offset:1920\n"
      "v_add_u32 v52, 4294916608, v52\n"
      "v_add_u32 v53, 4294916608, v53\n"
      "ds_bpermute_b32 v64, v54, v60\n"
      "ds_bpermute_b32 v65, v54, v61\n"
      "ds_bpermute_b32 v66, v54, v62\n"
      "ds_bpermute_b32 v67, v54, v63\n"
      "v_mfma_f32_16x16x32_bf16 v[136:139], a[56:59], v[56:59], v[136:139]\n"
      "v_mfma_f32_16x16x32_bf16 v[144:147], v[56:59], a[128:131], v[144:147]\n"
      "v_mfma_f32_16x16x32_bf16 v[148:151], a[200:203], v[56:59], v[148:151]\n"
      "v_and_b32 v72, v55, v72\n"
      "v_and_b32 v73, v55, v73\n"
      "v_and_b32 v74, v55, v74\n"
      "v_and_b32 v75, v55, v75\n"
      "v_mov_b32 v28, v72\n"
      "v_mov_b32 v29, v73\n"
      "v_mov_b32 v30, v74\n"
      "v_mov_b32 v31, v75\n"
      "v_mfma_f32_16x16x32_bf16 v[140:143], v[72:75], v[56:59], v[140:143]\n"
      "s_waitcnt vmcnt(11) lgkmcnt(0)\n"
      "s_barrier\n"
      "ds_read_b64_tr_b16 v[60:61], v53 offset:0\n"
      "ds_read_b64_tr_b16 v[62:63], v53 offset:1920\n"
      "ds_read_b64_tr_b16 a[64:65], v52 offset:144\n"
      "ds_read_b64_tr_b16 a[66:67], v52 offset:2064\n"
      "ds_read_b64_tr_b16 a[136:137], v52 offset:288\n"
      "ds_read_b64_tr_b16 a[138:139], v52 offset:2208\n"
      "ds_read_b64_tr_b16 a[208:209], v52 offset:0\n"
      "ds_read_b64_tr_b16 a[210:211], v52 offset:1920\n"
      "v_add_u32 v52, 16896, v52\n"
      "v_add_u32 v53, 16896, v53\n"
      "ds_bpermute_b32 v72, v54, v68\n"
      "ds_bpermute_b32 v73, v54, v69\n"
      "ds_bpermute_b32 v74, v54, v70\n"
      "ds_bpermute_b32 v75, v54, v71\n"
      "v_mfma_f32_16x16x32_bf16 v[136:139], a[60:63], v[56:59], v[136:139]\n"
      "v_mfma_f32_16x16x32_bf16 v[144:147], v[56:59], a[132:135], v[144:147]\n"
      "v_mfma_f32_16x16x32_bf16 v[148:151], a[204:207], v[56:59], v[148:151]\n"
      "v_and_b32 v64, v55, v64\n"
      "v_and_b32 v65, v55, v65\n"
      "v_and_b32 v66, v55, v66\n"
      "v_and_b32 v67, v55, v67\n"
      "v_mov_b32 v32, v64\n"
      "v_mov_b32 v33, v65\n"
      "v_mov_b32 v34, v66\n"
      "v_mov_b32 v35, v67\n"
      "v_mfma_f32_16x16x32_bf16 v[140:143], v[64:67], v[56:59], v[140:143]\n"
      "s_waitcnt vmcnt(7) lgkmcnt(0)\n"
      "s_barrier\n"
      "ds_read_b64_tr_b16 v[68:69], v53 offset:0\n"
      "ds_read_b64_tr_b16 v[70:71], v53 offset:1920\n"
      "ds_read_b64_tr_b16 a[68:69], v52 offset:144\n"
      "ds_read_b64_tr_b16 a[70:71], v52 offset:2064\n"
      "ds_read_b64_tr_b16 a[140:141], v52 offset:288\n"
      "ds_read_b64_tr_b16 a[142:143], v52 offset:2208\n"
      "ds_read_b64_tr_b16 a[212:213], v52 offset:0\n"
      "ds_read_b64_tr_b16 a[214:215], v52 offset:1920\n"
      "v_add_u32 v52, 16896, v52\n"
      "v_add_u32 v53, 16896, v53\n"
      "ds_bpermute_b32 v64, v54, v60\n"
      "ds_bpermute_b32 v65, v54, v61\n"
      "ds_bpermute_b32 v66, v54, v62\n"
      "ds_bpermute_b32 v67, v54, v63\n"
      "v_mfma_f32_16x16x32_bf16 v[136:139], a[64:67], v[56:59], v[136:139]\n"
      "v_mfma_f32_16x16x32_bf16 v[144:147], v[56:59], a[136:139], v[144:147]\n"
      "v_mfma_f32_16x16x32_bf16 v[148:151], a[208:211], v[56:59], v[148:151]\n"
      "v_and_b32 v72, v55, v72\n"
      "v_and_b32 v73, v55, v73\n"
      "v_and_b32 v74, v55, v74\n"
      "v_and_b32 v75, v55, v75\n"
      "v_mov_b32 v36, v72\n"
      "v_mov_b32 v37, v73\n"
      "v_mov_b32 v38, v74\n"
      "v_mov_b32 v39, v75\n"
      "v_mfma_f32_16x16x32_bf16 v[140:143], v[72:75], v[56:59], v[140:143]\n"
      "s_waitcnt lgkmcnt(0)\n"
      "ds_bpermute_b32 v72, v54, v68\n"
      "ds_bpermute_b32 v73, v54, v69\n"
      "ds_bpermute_b32 v74, v54, v70\n"
      "ds_bpermute_b32 v75, v54, v71\n"
      "v_mfma_f32_16x16x32_bf16 v[136:139], a[68:71], v[56:59], v[136:139]\n"
      "v_mfma_f32_16x16x32_bf16 v[144:147], v[56:59], a[140:143], v[144:147]\n"
      "v_mfma_f32_16x16x32_bf16 v[148:151], a[212:215], v[56:59], v[148:151]\n"
      "v_and_b32 v64, v55, v64\n"
      "v_and_b32 v65, v55, v65\n"
      "v_and_b32 v66, v55, v66\n"
      "v_and_b32 v67, v55, v67\n"
      "v_mov_b32 v40, v64\n"
      "v_mov_b32 v41, v65\n"
      "v_mov_b32 v42, v66\n"
      "v_mov_b32 v43, v67\n"
      "v_mfma_f32_16x16x32_bf16 v[140:143], v[64:67], v[56:59], v[140:143]\n"
      "s_waitcnt lgkmcnt(0)\n"
      "v_and_b32 v72, v55, v72\n"
      "v_and_b32 v73, v55, v73\n"
      "v_and_b32 v74, v55, v74\n"
      "v_and_b32 v75, v55, v75\n"
      "v_mov_b32 v44, v72\n"
      "v_mov_b32 v45, v73\n"
      "v_mov_b32 v46, v74\n"
      "v_mov_b32 v47, v75\n"
      "v_mfma_f32_16x16x32_bf16 v[140:143], v[72:75], v[56:59], v[140:143]\n"
      "s_add_u32 s36, s36, 24576\n"
      "s_cmp_eq_u32 s36, s37\n"
      "s_cselect_b32 s47, s38, 0\n"
      "s_cselect_b32 s37, s48, s37\n"
      "s_cselect_b32 s49, s39, s53\n"
      "s_add_u32 m0, s41, 20480\n"
      "s_mov_b32 s46, s45\n"
      "s_add_u32 s45, s36, s47\n"
      "v_add_u32 v9, 1, v9\n"
      "v_cmp_ne_u32 vcc, s21, v79\n"
      "s_min_u32 s45, s45, s6\n"
      "s_cbranch_vccnz .Lsp_q1_2\n"
      ".Lbk_q1_2:\n"
      "buffer_load_dwordx4 v0, s[8:11], s46 offen offset:1024 sc1 lds\n"
      "s_waitcnt vmcnt(1)\n"
      "ds_write_b32 v11, v9\n"
      "buffer_load_dwordx4 v0, s[8:11], s46 offen offset:0 sc1 lds\n"
      "buffer_load_dwordx4 v0, s[8:11], s46 offen offset:3072 sc1 lds\n"
      "buffer_load_dword v15, v2, s[8:11], s45 offen sc1\n"
      "ds_read_b32 v10, v12\n"
      "buffer_load_dwordx4 v0, s[8:11], s46 offen offset:2048 sc1 lds\n"
      "s_waitcnt lgkmcnt(0)\n"
      "v_cmp_gt_i32 vcc, v9, v10\n"
      "s_add_u32 m0, s42, 20480\n"
      "s_nop 0\n"
      "buffer_load_dwordx4 v1, s[8:11], s46 offen offset:0 sc1 lds\n"
      "s_cbranch_vccnz .Lsp_qy0_3\n"
      ".Lbk_qy0_3:\n"
      "ds_read_b128 v[52:55], v0 offset:1024\n"
      "ds_read_b128 v[48:51], v0 offset:0\n"
      "ds_read_b128 v[56:59], v0 offset:2048\n"
      "ds_read_b128 v[68:71], v0 offset:5120\n"
      "ds_read_b128 v[64:67], v0 offset:4096\n"
      "ds_read_b128 v[60:63], v0 offset:3072\n"
      "buffer_load_dword v14, v11, s[8:11], s31 offen\n"
      "ds_read2st64_b64 v[212:215], v4 offset0:0 offset1:1\n"
      "ds_read_b128 v[72:75], v0 offset:6144\n"
      "ds_read_b128 v[76:79], v0 offset:7168\n"
      "ds_read_b128 v[88:91], v0 offset:10240\n"
      "ds_read_b128 v[108:111], v0 offset:15360\n"
      "s_mov_b32 s20, 21\n"
      "s_waitcnt lgkmcnt(0)\n"
      "s_nop 4\n"
      "s_branch .Lq_loop\n"
      ".p2align 12\n"
      ".Lq_p0:\n"
      "s_branch .Lq_pr\n"
      ".p2align 5\n"
      ".Lq_loop:\n"
      "v_mfma_f32_16x16x32_bf16 v[120:123], a[0:3], v[48:51], 0\n"
      "ds_read_b128 v[80:83], v0 offset:8192\n"
      "ds_read_b128 v[84:87], v0 offset:9216\n"
      "v_mfma_f32_16x16x32_bf16 v[120:123], a[4:7], v[52:55], v[120:123]\n"
      "ds_read_b128 v[96:99], v0 offset:12288\n"
      "v_mul_f32 v232, 0x3eae1a93, v219\n"
      "v_mfma_f32_16x16x32_bf16 v[120:123], a[8:11], v[56:59], v[120:123]\n"
      "s_waitcnt lgkmcnt(7)\n"
      "ds_read_b128 v[104:107], v0 offset:14336\n"
      "v_fma_f32 v135, -v218, v151, v135\n"
      "v_mfma_f32_16x16x32_bf16 v[120:123], a[12:15], v[60:63], v[120:123]\n"
      "v_fma_f32 v133, -v218, v149, v133\n"
      "v_fma_f32 v134, -v218, v150, v134\n"
      "v_mfma_f32_16x16x32_bf16 v[120:123], a[16:19], v[64:67], v[120:123]\n"
      "ds_read_b128 v[92:95], v0 offset:11264\n"
      "v_fma_f32 v132, -v218, v148, v132\n"
      "v_mfma_f32_16x16x32_bf16 v[120:123], a[20:23], v[68:71], v[120:123]\n"
      "s_waitcnt lgkmcnt(5)\n"
      "ds_read_b64 v[216:217], v4 offset:1024\n"
      "v_cvt_pk_bf16_f32 v176, v132, v133\n"
      "v_mfma_f32_16x16x32_bf16 v[120:123], a[24:27], v[72:75], v[120:123]\n"
      "ds_read_b128 v[100:103], v0 offset:13312\n"
      "v_cvt_pk_bf16_f32 v177, v134, v135\n"
      "v_mfma_f32_16x16x32_bf16 v[120:123], a[28:31], v[76:79], v[120:123]\n"
      "s_waitcnt lgkmcnt(5)\n"
      "v_rcp_f32 v210, v210\n"
      "v_mfma_f32_16x16x32_bf16 v[120:123], a[32:35], v[80:83], v[120:123]\n"
      "v_mul_f32 v204, v204, v210\n"
      "v_add_f32 v212, v212, v214\n"
      "v_mfma_f32_16x16x32_bf16 v[188:191], v[172:175], v[176:179], v[152:155]\n"
      "ds_read_b128 v[112:115], v0 offset:16384\n"
      "v_mfma_f32_16x16x32_bf16 v[120:123], a[36:39], v[84:87], v[120:123]\n"
      "ds_read_b128 v[116:119], v0 offset:17408\n"
      "v_mul_f32 v208, v208, v210\n"
      "v_mfma_f32_16x16x32_bf16 v[120:123], a[40:43], v[88:91], v[120:123]\n"
      "s_waitcnt lgkmcnt(4)\n"
      "s_add_u32 s36, s36, 24576\n"
      "v_mul_f32 v207, v207, v210\n"
      "v_mfma_f32_16x16x32_bf16 v[120:123], a[44:47], v[92:95], v[120:123]\n"
      "v_mul_f32 v209, v209, v210\n"
      "v_cvt_pk_bf16_f32 v230, v208, v209\n"
      "v_mfma_f32_16x16x32_bf16 v[120:123], a[48:51], v[96:99], v[120:123]\n"
      "s_waitcnt lgkmcnt(2)\n"
      "buffer_store_dwordx2 v[230:231], v8, s[8:11], s40 offen sc1\n"
      "v_mfma_f32_16x16x32_bf16 v[120:123], a[52:55], v[100:103], v[120:123]\n"
      "v_mul_f32 v206, v206, v210\n"
      "v_add_f32 v218, v212, v216\n"
      "v_mfma_f32_16x16x32_bf16 v[120:123], a[56:59], v[104:107], v[120:123]\n"
      "s_mov_b32 s46, s45\n"
      "v_add_f32 v213, v213, v215\n"
      "v_mfma_f32_16x16x32_bf16 v[120:123], a[60:63], v[108:111], v[120:123]\n"
      "s_waitcnt lgkmcnt(1)\n"
      "v_cvt_pk_bf16_f32 v229, v206, v207\n"
      "v_mfma_f32_16x16x32_bf16 v[120:123], a[64:67], v[112:115], v[120:123]\n"
      "s_waitcnt lgkmcnt(0)\n"
      "v_mul_f32 v205, v205, v210\n"
      "v_cvt_pk_bf16_f32 v228, v204, v205\n"
      "v_mfma_f32_16x16x32_bf16 v[120:123], a[68:71], v[116:119], v[120:123]\n"
      "buffer_store_dwordx2 v[228:229], v7, s[8:11], s40 offen sc1\n"
      "v_mfma_f32_16x16x32_bf16 v[124:127], a[216:219], v[48:51], 0\n"
      "v_add_f32 v213, v213, v217\n"
      "v_fma_f32 v213, -v218, v218, v213\n"
      "v_mfma_f32_16x16x32_bf16 v[124:127], a[220:223], v[52:55], v[124:127]\n"
      "v_max_f32 v213, 0x3727c5ac, v213\n"
      "s_mov_b32 s45, s36\n"
      "v_mfma_f32_16x16x32_bf16 v[124:127], a[224:227], v[56:59], v[124:127]\n"
      "v_mov_b32 v187, v185\n"
      "v_mfma_f32_16x16x32_bf16 v[124:127], a[228:231], v[60:63], v[124:127]\n"
      "v_rsq_f32 v219, v213\n"
      "v_mfma_f32_16x16x32_bf16 v[124:127], a[232:235], v[64:67], v[124:127]\n"
      "s_add_u32 m0, s41, 40960\n"
      "ds_write_b64 v5, v[218:219] offset:0\n"
      "v_mfma_f32_16x16x32_bf16 v[124:127], a[236:239], v[68:71], v[124:127]\n"
      "s_waitcnt vmcnt(5)\n"
      "v_mov_b32 v186, v184\n"
      "v_cmp_ne_u32 vcc, s21, v15\n"
      "v_mfma_f32_16x16x32_bf16 v[124:127], a[240:243], v[72:75], v[124:127]\n"
      "s_min_u32 s45, s45, s6\n"
      "v_mfma_f32_16x16x32_bf16 v[124:127], a[244:247], v[76:79], v[124:127]\n"
      "v_cndmask_b32 v233, v219, 1.0, s[54:55]\n"
      "v_add_u32 v9, 1, v9\n"
      "v_mfma_f32_16x16x32_bf16 v[124:127], a[248:251], v[80:83], v[124:127]\n"
      "v_fma_f32 v123, -v218, v139, v123\n"
      "v_fma_f32 v122, -v218, v138, v122\n"
      "v_mfma_f32_16x16x32_bf16 v[124:127], a[252:255], v[84:87], v[124:127]\n"
      "v_fma_f32 v120, -v218, v136, v120\n"
      "v_fma_f32 v121, -v218, v137, v121\n"
      "v_mfma_f32_16x16x32_bf16 v[124:127], v[16:19], v[88:91], v[124:127]\n"
      "ds_read_b128 v[224:227], v6 offset:16\n"
      "v_mul_f32 v121, v219, v121\n"
      "v_mfma_f32_16x16x32_bf16 v[124:127], v[20:23], v[92:95], v[124:127]\n"
      "v_mul_f32 v120, v219, v120\n"
      "v_mfma_f32_16x16x32_bf16 v[124:127], v[24:27], v[96:99], v[124:127]\n"
      "s_cbranch_vccnz .Lsp_q2_4\n"
      ".Lbk_q2_4:\n"
      "v_mfma_f32_16x16x32_bf16 v[124:127], v[28:31], v[100:103], v[124:127]\n"
      "buffer_load_dword v15, v2, s[8:11], s45 offen sc1\n"
      "v_mfma_f32_16x16x32_bf16 v[124:127], v[32:35], v[104:107], v[124:127]\n"
      "v_mul_f32 v123, v219, v123\n"
      "v_mul_f32 v122, v219, v122\n"
      "v_mfma_f32_16x16x32_bf16 v[124:127], v[36:39], v[108:111], v[124:127]\n"
      "s_waitcnt vmcnt(4) lgkmcnt(1)\n"
      "ds_write_b32 v11, v9\n"
      "v_mfma_f32_16x16x32_bf16 v[124:127], v[40:43], v[112:115], v[124:127]\n"
      "v_mov_b32 v182, v180\n"
      "v_mfma_f32_16x16x32_bf16 v[124:127], v[44:47], v[116:119], v[124:127]\n"
      "v_cvt_pk_bf16_f32 v173, v122, v123\n"
      "v_mfma_f32_16x16x32_bf16 v[128:131], v[48:51], a[72:75], 0\n"
      "buffer_load_dwordx4 v0, s[8:11], s46 offen offset:1024 sc1 lds\n"
      "v_mfma_f32_16x16x32_bf16 v[128:131], v[52:55], a[76:79], v[128:131]\n"
      "ds_read_b128 v[220:223], v6 offset:0\n"
      "v_cvt_pk_bf16_f32 v172, v120, v121\n"
      "v_mfma_f32_16x16x32_bf16 v[128:131], v[56:59], a[80:83], v[128:131]\n"
      "v_fma_f32 v126, -v218, v142, v126\n"
      "v_fma_f32 v124, -v218, v140, v124\n"
      "v_mfma_f32_16x16x32_bf16 v[128:131], v[60:63], a[84:87], v[128:131]\n"
      "s_add_u32 s52, s52, 24576\n"
      "v_fma_f32 v125, -v218, v141, v125\n"
      "v_mul_f32 v124, v233, v124\n"
      "v_mfma_f32_16x16x32_bf16 v[128:131], v[64:67], a[88:91], v[128:131]\n"
      "v_mul_f32 v126, v219, v126\n"
      "v_mul_f32 v125, v233, v125\n"
      "v_mfma_f32_16x16x32_bf16 v[128:131], v[68:71], a[92:95], v[128:131]\n"
      "v_fma_f32 v127, -v218, v143, v127\n"
      "v_cvt_pk_bf16_f32 v175, v124, v125\n"
      "v_mfma_f32_16x16x32_bf16 v[128:131], v[72:75], a[96:99], v[128:131]\n"
      "v_mul_f32 v127, v219, v127\n"
      "v_cvt_pk_bf16_f32 v174, v126, v127\n"
      "v_mfma_f32_16x16x32_bf16 v[128:131], v[76:79], a[100:103], v[128:131]\n"
      "buffer_load_dwordx4 v0, s[8:11], s46 offen offset:2048 sc1 lds\n"
      "v_mfma_f32_16x16x32_bf16 v[192:195], v[172:175], v[176:179], v[156:159]\n"
      "ds_read_b32 v10, v12\n"
      "v_mfma_f32_16x16x32_bf16 v[196:199], v[172:175], v[164:167], v[168:171]\n"
      "v_mov_b32 v183, v181\n"
      "v_mov_b32 v178, v175\n"
      "v_mfma_f32_16x16x32_bf16 v[128:131], v[80:83], a[104:107], v[128:131]\n"
      "buffer_load_dwordx4 v0, s[8:11], s46 offen offset:0 sc1 lds\n"
      "v_mfma_f32_16x16x32_bf16 v[128:131], v[84:87], a[108:111], v[128:131]\n"
      "v_max_f32 v235, v194, v195\n"
      "v_max_f32 v234, v192, v193\n"
      "v_mfma_f32_16x16x32_bf16 v[128:131], v[88:91], a[112:115], v[128:131]\n"
      "v_max3_f32 v234, v188, v189, v234\n"
      "v_max3_f32 v235, v190, v191, v235\n"
      "v_mfma_f32_16x16x32_bf16 v[128:131], v[92:95], a[116:119], v[128:131]\n"
      "v_max_f32 v236, v234, v235\n"
      "v_mov_b32 v237, v236\n"
      "v_mfma_f32_16x16x32_bf16 v[128:131], v[96:99], a[120:123], v[128:131]\n"
      "v_permlane16_swap_b32 v236, v237\n"
      "v_mfma_f32_16x16x32_bf16 v[128:131], v[100:103], a[124:127], v[128:131]\n"
      "v_max_f32 v236, v236, v237\n"
      "v_mov_b32 v237, v236\n"
      "v_mfma_f32_16x16x32_bf16 v[128:131], v[104:107], a[128:131], v[128:131]\n"
      "s_waitcnt lgkmcnt(0)\n"
      "v_permlane32_swap_b32 v236, v237\n"
      "v_cmp_gt_i32 vcc, v9, v10\n"
      "v_mfma_f32_16x16x32_bf16 v[128:131], v[108:111], a[132:135], v[128:131]\n"
      "buffer_load_dwordx4 v0, s[8:11], s46 offen offset:3072 sc1 lds\n"
      "v_mfma_f32_16x16x32_bf16 v[128:131], v[112:115], a[136:139], v[128:131]\n"
      "s_add_u32 m0, s42, 40960\n"
      "v_max_f32 v236, v236, v237\n"
      "v_mul_f32 v236, v236, v232\n"
      "v_mfma_f32_16x16x32_bf16 v[128:131], v[116:119], a[140:143], v[128:131]\n"
      "s_add_u32 s40, s40, 24576\n"
      "v_fma_f32 v234, v234, v232, -v236\n"
      "v_fma_f32 v235, v235, v232, -v236\n"
      "v_mfma_f32_16x16x32_bf16 v[132:135], a[144:147], v[48:51], 0\n"
      "v_exp_f32 v234, v234\n"
      "v_mfma_f32_16x16x32_bf16 v[132:135], a[148:151], v[52:55], v[132:135]\n"
      "s_cbranch_vccnz .Lsp_qy1_5\n"
      ".Lbk_qy1_5:\n"
      "ds_read_b128 v[48:51], v0 offset:20480\n"
      "v_mfma_f32_16x16x32_bf16 v[132:135], a[152:155], v[56:59], v[132:135]\n"
      "v_exp_f32 v235, v235\n"
      "v_mfma_f32_16x16x32_bf16 v[132:135], a[156:159], v[60:63], v[132:135]\n"
      "ds_read_b128 v[56:59], v0 offset:22528\n"
      "v_fma_f32 v130, -v224, v147, v130\n"
      "v_mfma_f32_16x16x32_bf16 v[132:135], a[160:163], v[64:67], v[132:135]\n"
      "ds_read_b64 v[216:217], v4 offset:21504\n"
      "v_fma_f32 v129, -v222, v146, v129\n"
      "v_mfma_f32_16x16x32_bf16 v[132:135], a[164:167], v[68:71], v[132:135]\n"
      "buffer_load_dwordx4 v1, s[8:11], s46 offen offset:0 sc1 lds\n"
      "v_mfma_f32_16x16x32_bf16 v[132:135], a[168:171], v[72:75], v[132:135]\n"
      "v_cvt_pk_bf16_f32 v234, v234, v234\n"
      "v_fma_f32 v131, -v226, v144, v131\n"
      "v_mfma_f32_16x16x32_bf16 v[132:135], a[172:175], v[76:79], v[132:135]\n"
      "ds_read_b128 v[60:63], v0 offset:23552\n"
      "v_and_b32 v200, v160, v234\n"
      "v_mfma_f32_16x16x32_bf16 v[132:135], a[176:179], v[80:83], v[132:135]\n"
      "v_cvt_pk_bf16_f32 v235, v235, v235\n"
      "v_mul_f32 v129, v223, v129\n"
      "v_mfma_f32_16x16x32_bf16 v[132:135], a[180:183], v[84:87], v[132:135]\n"
      "ds_read_b128 v[52:55], v0 offset:21504\n"
      "v_mul_f32 v131, v227, v131\n"
      "v_mfma_f32_16x16x32_bf16 v[132:135], a[184:187], v[88:91], v[132:135]\n"
      "ds_read_b128 v[88:91], v0 offset:30720\n"
      "v_mul_f32 v130, v225, v130\n"
      "v_cvt_pk_bf16_f32 v185, v198, v199\n"
      "v_mfma_f32_16x16x32_bf16 v[132:135], a[188:191], v[92:95], v[132:135]\n"
      "v_cvt_pk_bf16_f32 v184, v196, v197\n"
      "v_and_b32 v201, v161, v235\n"
      "v_fma_f32 v128, -v220, v145, v128\n"
      "v_mfma_f32_16x16x32_bf16 v[132:135], a[192:195], v[96:99], v[132:135]\n"
      "ds_read_b128 v[68:71], v0 offset:25600\n"
      "v_and_b32 v203, v163, v235\n"
      "v_mfma_f32_16x16x32_bf16 v[132:135], a[196:199], v[100:103], v[132:135]\n"
      "v_mul_f32 v128, v221, v128\n"
      "v_and_b32 v202, v162, v234\n"
      "v_mfma_f32_16x16x32_bf16 v[132:135], a[200:203], v[104:107], v[132:135]\n"
      "v_cvt_pk_bf16_f32 v180, v128, v129\n"
      "v_cvt_pk_bf16_f32 v181, v130, v131\n"
      "v_mfma_f32_16x16x32_bf16 v[208:211], v[184:187], v[200:203], 0\n"
      "ds_read_b128 v[64:67], v0 offset:24576\n"
      "ds_read_b128 v[72:75], v0 offset:26624\n"
      "v_mfma_f32_16x16x32_bf16 v[204:207], v[180:183], v[200:203], 0\n"
      "s_waitcnt vmcnt(6) lgkmcnt(4)\n"
      "buffer_store_dword v13, v3, s[8:11], s52 offen sc1\n"
      "v_mfma_f32_16x16x32_bf16 v[132:135], a[204:207], v[108:111], v[132:135]\n"
      "ds_read2st64_b64 v[212:215], v4 offset0:40 offset1:41\n"
      "v_mfma_f32_16x16x32_bf16 v[132:135], a[208:211], v[112:115], v[132:135]\n"
      "v_rcp_f32 v210, v210\n"
      "v_mfma_f32_16x16x32_bf16 v[132:135], a[212:215], v[116:119], v[132:135]\n"
      "ds_read_b128 v[76:79], v0 offset:27648\n"
      "v_mul_f32 v209, v209, v210\n"
      "v_mfma_f32_16x16x32_bf16 v[120:123], a[0:3], v[48:51], 0\n"
      "ds_read_b128 v[80:83], v0 offset:28672\n"
      "ds_read_b128 v[100:103], v0 offset:33792\n"
      "v_mfma_f32_16x16x32_bf16 v[120:123], a[4:7], v[52:55], v[120:123]\n"
      "ds_read_b128 v[92:95], v0 offset:31744\n"
      "v_mul_f32 v208, v208, v210\n"
      "v_mfma_f32_16x16x32_bf16 v[120:123], a[8:11], v[56:59], v[120:123]\n"
      "ds_read_b128 v[84:87], v0 offset:29696\n"
      "v_fma_f32 v134, -v218, v150, v134\n"
      "v_mfma_f32_16x16x32_bf16 v[120:123], a[12:15], v[60:63], v[120:123]\n"
      "s_waitcnt lgkmcnt(6)\n"
      "ds_read_b128 v[104:107], v0 offset:34816\n"
      "v_fma_f32 v132, -v218, v148, v132\n"
      "v_mfma_f32_16x16x32_bf16 v[120:123], a[16:19], v[64:67], v[120:123]\n"
      "s_mov_b32 s50, s49\n"
      "ds_read_b128 v[96:99], v0 offset:32768\n"
      "v_mfma_f32_16x16x32_bf16 v[120:123], a[20:23], v[68:71], v[120:123]\n"
      "ds_read_b128 v[108:111], v0 offset:35840\n"
      "v_fma_f32 v133, -v218, v149, v133\n"
      "v_mfma_f32_16x16x32_bf16 v[120:123], a[24:27], v[72:75], v[120:123]\n"
      "s_waitcnt lgkmcnt(7)\n"
      "v_fma_f32 v135, -v218, v151, v135\n"
      "v_cvt_pk_bf16_f32 v177, v134, v135\n"
      "v_mfma_f32_16x16x32_bf16 v[120:123], a[28:31], v[76:79], v[120:123]\n"
      "s_waitcnt lgkmcnt(5)\n"
      "ds_read_b128 v[112:115], v0 offset:36864\n"
      "v_cvt_pk_bf16_f32 v176, v132, v133\n"
      "v_mfma_f32_16x16x32_bf16 v[120:123], a[32:35], v[80:83], v[120:123]\n"
      "v_mov_b32 v187, v185\n"
      "v_mul_f32 v232, 0x3eae1a93, v219\n"
      "v_mfma_f32_16x16x32_bf16 v[188:191], v[172:175], v[176:179], v[152:155]\n"
      "s_waitcnt lgkmcnt(4)\n"
      "ds_read_b128 v[116:119], v0 offset:37888\n"
      "v_mul_f32 v207, v207, v210\n"
      "v_mfma_f32_16x16x32_bf16 v[120:123], a[36:39], v[84:87], v[120:123]\n"
      "v_mul_f32 v204, v204, v210\n"
      "v_mfma_f32_16x16x32_bf16 v[120:123], a[40:43], v[88:91], v[120:123]\n"
      "v_cvt_pk_bf16_f32 v230, v208, v209\n"
      "v_add_f32 v213, v213, v215\n"
      "v_mfma_f32_16x16x32_bf16 v[120:123], a[44:47], v[92:95], v[120:123]\n"
      "s_waitcnt lgkmcnt(2)\n"
      "v_mul_f32 v205, v205, v210\n"
      "v_mfma_f32_16x16x32_bf16 v[120:123], a[48:51], v[96:99], v[120:123]\n"
      "buffer_store_dwordx2 v[230:231], v8, s[8:11], s40 offen sc1\n"
      "v_mfma_f32_16x16x32_bf16 v[120:123], a[52:55], v[100:103], v[120:123]\n"
      "v_add_f32 v212, v212, v214\n"
      "v_mfma_f32_16x16x32_bf16 v[120:123], a[56:59], v[104:107], v[120:123]\n"
      "v_cvt_pk_bf16_f32 v228, v204, v205\n"
      "v_mul_f32 v206, v206, v210\n"
      "v_mfma_f32_16x16x32_bf16 v[120:123], a[60:63], v[108:111], v[120:123]\n"
      "s_waitcnt vmcnt(7) lgkmcnt(0)\n"
      "v_cvt_pk_bf16_f32 v229, v206, v207\n"
      "v_cmp_ne_u32 vcc, s21, v15\n"
      "v_mfma_f32_16x16x32_bf16 v[120:123], a[64:67], v[112:115], v[120:123]\n"
      "s_add_u32 m0, s41, 0\n"
      "v_mov_b32 v182, v180\n"
      "v_mfma_f32_16x16x32_bf16 v[120:123], a[68:71], v[116:119], v[120:123]\n"
      "buffer_store_dwordx2 v[228:229], v7, s[8:11], s40 offen sc1\n"
      "v_mfma_f32_16x16x32_bf16 v[124:127], a[216:219], v[48:51], 0\n"
      "v_add_f32 v218, v212, v216\n"
      "v_add_f32_e64 v213, v213, v217\n"
      "v_mfma_f32_16x16x32_bf16 v[124:127], a[220:223], v[52:55], v[124:127]\n"
      "s_add_u32 s36, s36, 24576\n"
      "v_fma_f32 v213, -v218, v218, v213\n"
      "v_mfma_f32_16x16x32_bf16 v[124:127], a[224:227], v[56:59], v[124:127]\n"
      "s_mov_b32 s46, s45\n"
      "s_cbranch_vccnz .Lsp_q0_6\n"
      ".Lbk_q0_6:\n"
      "v_mfma_f32_16x16x32_bf16 v[124:127], a[228:231], v[60:63], v[124:127]\n"
      "buffer_load_dwordx4 v0, s[8:11], s46 offen offset:1024 sc1 lds\n"
      "v_mfma_f32_16x16x32_bf16 v[124:127], a[232:235], v[64:67], v[124:127]\n"
      "v_max_f32 v213, 0x3727c5ac, v213\n"
      "v_fma_f32 v120, -v218, v136, v120\n"
      "v_mfma_f32_16x16x32_bf16 v[124:127], a[236:239], v[68:71], v[124:127]\n"
      "v_mov_b32 v186, v184\n"
      "v_mfma_f32_16x16x32_bf16 v[124:127], a[240:243], v[72:75], v[124:127]\n"
      "v_add_u32 v9, 1, v9\n"
      "v_mfma_f32_16x16x32_bf16 v[124:127], a[244:247], v[76:79], v[124:127]\n"
      "s_add_u32 s40, s40, 24576\n"
      "v_fma_f32 v123, -v218, v139, v123\n"
      "v_mfma_f32_16x16x32_bf16 v[124:127], a[248:251], v[80:83], v[124:127]\n"
      "v_rsq_f32 v219, v213\n"
      "v_mfma_f32_16x16x32_bf16 v[124:127], a[252:255], v[84:87], v[124:127]\n"
      "v_mfma_f32_16x16x32_bf16 v[124:127], v[16:19], v[88:91], v[124:127]\n"
      "v_fma_f32 v121, -v218, v137, v121\n"
      "v_fma_f32 v122, -v218, v138, v122\n"
      "v_mfma_f32_16x16x32_bf16 v[124:127], v[20:23], v[92:95], v[124:127]\n"
      "buffer_load_dwordx4 v0, s[8:11], s46 offen offset:0 sc1 lds\n"
      "v_mfma_f32_16x16x32_bf16 v[124:127], v[24:27], v[96:99], v[124:127]\n"
      "v_mul_f32 v120, v219, v120\n"
      "v_mul_f32 v122, v219, v122\n"
      "v_mfma_f32_16x16x32_bf16 v[124:127], v[28:31], v[100:103], v[124:127]\n"
      "ds_write_b64 v5, v[218:219] offset:512\n"
      "v_mfma_f32_16x16x32_bf16 v[124:127], v[32:35], v[104:107], v[124:127]\n"
      "v_mul_f32 v121, v219, v121\n"
      "v_mul_f32 v123, v219, v123\n"
      "v_mfma_f32_16x16x32_bf16 v[124:127], v[36:39], v[108:111], v[124:127]\n"
      "buffer_load_dwordx4 v0, s[8:11], s46 offen offset:2048 sc1 lds\n"
      "v_mfma_f32_16x16x32_bf16 v[124:127], v[40:43], v[112:115], v[124:127]\n"
      "s_mov_b32 s45, s36\n"
      "ds_read_b128 v[224:227], v6 offset:528\n"
      "v_mfma_f32_16x16x32_bf16 v[124:127], v[44:47], v[116:119], v[124:127]\n"
      "s_min_u32 s45, s45, s6\n"
      "v_cndmask_b32 v233, v219, 1.0, s[54:55]\n"
      "v_mfma_f32_16x16x32_bf16 v[128:131], v[48:51], a[72:75], 0\n"
      "s_add_u32 s52, s52, 24576\n"
      "v_cvt_pk_bf16_f32 v172, v120, v121\n"
      "v_cvt_pk_bf16_f32 v173, v122, v123\n"
      "v_mfma_f32_16x16x32_bf16 v[128:131], v[52:55], a[76:79], v[128:131]\n"
      "buffer_load_dwordx4 v0, s[8:11], s46 offen offset:3072 sc1 lds\n"
      "v_mfma_f32_16x16x32_bf16 v[128:131], v[56:59], a[80:83], v[128:131]\n"
      "v_fma_f32 v126, -v218, v142, v126\n"
      "v_fma_f32 v124, -v218, v140, v124\n"
      "v_mul_f32 v126, v219, v126\n"
      "v_mfma_f32_16x16x32_bf16 v[128:131], v[60:63], a[84:87], v[128:131]\n"
      "v_fma_f32 v125, -v218, v141, v125\n"
      "v_mul_f32 v125, v233, v125\n"
      "v_mfma_f32_16x16x32_bf16 v[128:131], v[64:67], a[88:91], v[128:131]\n"
      "s_waitcnt vmcnt(7) lgkmcnt(1)\n"
      "ds_write_b32 v11, v9\n"
      "v_mfma_f32_16x16x32_bf16 v[128:131], v[68:71], a[92:95], v[128:131]\n"
      "v_fma_f32 v127, -v218, v143, v127\n"
      "v_mul_f32 v124, v233, v124\n"
      "v_mul_f32 v127, v219, v127\n"
      "v_mfma_f32_16x16x32_bf16 v[128:131], v[72:75], a[96:99], v[128:131]\n"
      "v_cvt_pk_bf16_f32 v174, v126, v127\n"
      "v_cvt_pk_bf16_f32 v175, v124, v125\n"
      "v_mfma_f32_16x16x32_bf16 v[128:131], v[76:79], a[100:103], v[128:131]\n"
      "buffer_load_dword v15, v2, s[8:11], s45 offen sc1\n"
      "v_mfma_f32_16x16x32_bf16 v[192:195], v[172:175], v[176:179], v[156:159]\n"
      "v_mov_b32 v183, v181\n"
      "v_mfma_f32_16x16x32_bf16 v[196:199], v[172:175], v[164:167], v[168:171]\n"
      "ds_read_b128 v[220:223], v6 offset:512\n"
      "s_add_u32 m0, s42, 0\n"
      "v_mfma_f32_16x16x32_bf16 v[128:131], v[80:83], a[104:107], v[128:131]\n"
      "buffer_load_dwordx4 v1, s[8:11], s46 offen offset:0 sc1 lds\n"
      "v_mfma_f32_16x16x32_bf16 v[128:131], v[84:87], a[108:111], v[128:131]\n"
      "v_max_f32 v235, v194, v195\n"
      "v_max3_f32 v235, v190, v191, v235\n"
      "v_mfma_f32_16x16x32_bf16 v[128:131], v[88:91], a[112:115], v[128:131]\n"
      "v_max_f32 v234, v192, v193\n"
      "v_max3_f32 v234, v188, v189, v234\n"
      "v_mfma_f32_16x16x32_bf16 v[128:131], v[92:95], a[116:119], v[128:131]\n"
      "ds_read_b32 v10, v12\n"
      "v_max_f32 v236, v234, v235\n"
      "v_mfma_f32_16x16x32_bf16 v[128:131], v[96:99], a[120:123], v[128:131]\n"
      "v_mov_b32 v237, v236\n"
      "v_cvt_pk_bf16_f32 v185, v198, v199\n"
      "v_mfma_f32_16x16x32_bf16 v[128:131], v[100:103], a[124:127], v[128:131]\n"
      "v_permlane16_swap_b32 v236, v237\n"
      "v_mfma_f32_16x16x32_bf16 v[128:131], v[104:107], a[128:131], v[128:131]\n"
      "v_max_f32 v236, v236, v237\n"
      "v_mov_b32 v237, v236\n"
      "v_mfma_f32_16x16x32_bf16 v[128:131], v[108:111], a[132:135], v[128:131]\n"
      "v_permlane32_swap_b32 v236, v237\n"
      "v_mfma_f32_16x16x32_bf16 v[128:131], v[112:115], a[136:139], v[128:131]\n"
      "s_waitcnt lgkmcnt(0)\n"
      "v_cmp_gt_i32 vcc, v9, v10\n"
      "v_max_f32 v236, v236, v237\n"
      "v_mfma_f32_16x16x32_bf16 v[128:131], v[116:119], a[140:143], v[128:131]\n"
      "v_mul_f32_e64 v236, v236, v232\n"
      "v_fma_f32 v235, v235, v232, -v236\n"
      "v_mfma_f32_16x16x32_bf16 v[132:135], a[144:147], v[48:51], 0\n"
      "v_exp_f32_e64 v235, v235\n"
      "v_mfma_f32_16x16x32_bf16 v[132:135], a[148:151], v[52:55], v[132:135]\n"
      "s_cbranch_vccnz .Lsp_qy2_7\n"
      ".Lbk_qy2_7:\n"
      "v_cvt_pk_bf16_f32 v235, v235, v235\n"
      "v_mfma_f32_16x16x32_bf16 v[132:135], a[152:155], v[56:59], v[132:135]\n"
      "v_fma_f32 v130, -v224, v147, v130\n"
      "v_mul_f32 v130, v225, v130\n"
      "v_mfma_f32_16x16x32_bf16 v[132:135], a[156:159], v[60:63], v[132:135]\n"
      "v_fma_f32 v129, -v222, v146, v129\n"
      "v_cvt_pk_bf16_f32 v184, v196, v197\n"
      "v_mfma_f32_16x16x32_bf16 v[132:135], a[160:163], v[64:67], v[132:135]\n"
      "v_fma_f32 v234, v234, v232, -v236\n"
      "v_fma_f32 v131, -v226, v144, v131\n"
      "v_mfma_f32_16x16x32_bf16 v[132:135], a[164:167], v[68:71], v[132:135]\n"
      "ds_read_b128 v[52:55], v0 offset:41984\n"
      "ds_read_b128 v[64:67], v0 offset:45056\n"
      "v_mfma_f32_16x16x32_bf16 v[132:135], a[168:171], v[72:75], v[132:135]\n"
      "ds_read_b128 v[56:59], v0 offset:43008\n"
      "v_mul_f32 v129, v223, v129\n"
      "v_mfma_f32_16x16x32_bf16 v[132:135], a[172:175], v[76:79], v[132:135]\n"
      "v_exp_f32 v234, v234\n"
      "v_mfma_f32_16x16x32_bf16 v[132:135], a[176:179], v[80:83], v[132:135]\n"
      "v_fma_f32 v128, -v220, v145, v128\n"
      "v_and_b32 v203, v163, v235\n"
      "v_mfma_f32_16x16x32_bf16 v[132:135], a[180:183], v[84:87], v[132:135]\n"
      "v_cvt_pk_bf16_f32 v234, v234, v234\n"
      "v_and_b32 v201, v161, v235\n"
      "v_mfma_f32_16x16x32_bf16 v[132:135], a[184:187], v[88:91], v[132:135]\n"
      "ds_read_b128 v[48:51], v0 offset:40960\n"
      "v_and_b32 v202, v162, v234\n"
      "v_mfma_f32_16x16x32_bf16 v[132:135], a[188:191], v[92:95], v[132:135]\n"
      "ds_read_b128 v[60:63], v0 offset:44032\n"
      "v_mul_f32 v128, v221, v128\n"
      "v_mfma_f32_16x16x32_bf16 v[132:135], a[192:195], v[96:99], v[132:135]\n"
      "ds_read_b128 v[76:79], v0 offset:48128\n"
      "v_cvt_pk_bf16_f32 v180, v128, v129\n"
      "v_mfma_f32_16x16x32_bf16 v[132:135], a[196:199], v[100:103], v[132:135]\n"
      "ds_read_b128 v[100:103], v0 offset:54272\n"
      "v_and_b32 v200, v160, v234\n"
      "v_mfma_f32_16x16x32_bf16 v[132:135], a[200:203], v[104:107], v[132:135]\n"
      "v_mul_f32 v131, v227, v131\n"
      "v_cvt_pk_bf16_f32 v181, v130, v131\n"
      "v_mfma_f32_16x16x32_bf16 v[208:211], v[184:187], v[200:203], 0\n"
      "s_waitcnt vmcnt(6) lgkmcnt(4)\n"
      "buffer_store_dword v13, v3, s[8:11], s52 offen sc1\n"
      "v_mfma_f32_16x16x32_bf16 v[204:207], v[180:183], v[200:203], 0\n"
      "ds_read_b128 v[84:87], v0 offset:50176\n"
      "ds_read_b128 v[72:75], v0 offset:47104\n"
      "v_mfma_f32_16x16x32_bf16 v[132:135], a[204:207], v[108:111], v[132:135]\n"
      "ds_read_b128 v[68:71], v0 offset:46080\n"
      "v_mov_b32 v178, v175\n"
      "v_mfma_f32_16x16x32_bf16 v[132:135], a[208:211], v[112:115], v[132:135]\n"
      "v_rcp_f32 v210, v210\n"
      "v_mfma_f32_16x16x32_bf16 v[132:135], a[212:215], v[116:119], v[132:135]\n"
      "s_waitcnt lgkmcnt(4)\n"
      "ds_read_b128 v[92:95], v0 offset:52224\n"
      "v_mul_f32 v205, v205, v210\n"
      "v_mfma_f32_16x16x32_bf16 v[120:123], a[0:3], v[48:51], 0\n"
      "s_add_u32 s36, s36, 24576\n"
      "ds_read_b128 v[80:83], v0 offset:49152\n"
      "v_mov_b32 v182, v180\n"
      "v_mfma_f32_16x16x32_bf16 v[120:123], a[4:7], v[52:55], v[120:123]\n"
      "ds_read_b128 v[96:99], v0 offset:53248\n"
      "v_mfma_f32_16x16x32_bf16 v[120:123], a[8:11], v[56:59], v[120:123]\n"
      "ds_read_b128 v[88:91], v0 offset:51200\n"
      "v_fma_f32 v132, -v218, v148, v132\n"
      "v_mfma_f32_16x16x32_bf16 v[120:123], a[12:15], v[60:63], v[120:123]\n"
      "s_cmp_eq_u32 s36, s37\n"
      "v_fma_f32 v133, -v218, v149, v133\n"
      "v_cvt_pk_bf16_f32 v176, v132, v133\n"
      "v_mfma_f32_16x16x32_bf16 v[120:123], a[16:19], v[64:67], v[120:123]\n"
      "s_waitcnt lgkmcnt(4)\n"
      "ds_read2st64_b64 v[212:215], v4 offset0:80 offset1:81\n"
      "s_cselect_b32 s47, s38, 0\n"
      "v_mfma_f32_16x16x32_bf16 v[120:123], a[20:23], v[68:71], v[120:123]\n"
      "s_cselect_b32 s37, s48, s37\n"
      "ds_read_b128 v[112:115], v0 offset:57344\n"
      "v_fma_f32 v135, -v218, v151, v135\n"
      "v_mfma_f32_16x16x32_bf16 v[120:123], a[24:27], v[72:75], v[120:123]\n"
      "s_cselect_b32 s49, s39, s53\n"
      "v_fma_f32 v134, -v218, v150, v134\n"
      "v_mov_b32 v183, v181\n"
      "v_mfma_f32_16x16x32_bf16 v[120:123], a[28:31], v[76:79], v[120:123]\n"
      "s_waitcnt lgkmcnt(4)\n"
      "ds_read_b128 v[104:107], v0 offset:55296\n"
      "v_cvt_pk_bf16_f32 v177, v134, v135\n"
      "v_mfma_f32_16x16x32_bf16 v[120:123], a[32:35], v[80:83], v[120:123]\n"
      "ds_read_b128 v[108:111], v0 offset:56320\n"
      "v_mul_f32 v206, v206, v210\n"
      "v_mfma_f32_16x16x32_bf16 v[188:191], v[172:175], v[176:179], v[152:155]\n"
      "ds_read_b64 v[216:217], v4 offset:41984\n"
      "v_mul_f32 v207, v207, v210\n"
      "v_mfma_f32_16x16x32_bf16 v[120:123], a[36:39], v[84:87], v[120:123]\n"
      "s_waitcnt lgkmcnt(5)\n"
      "s_add_u32 m0, s41, 20480\n"
      "ds_read_b128 v[116:119], v0 offset:58368\n"
      "v_mfma_f32_16x16x32_bf16 v[120:123], a[40:43], v[88:91], v[120:123]\n"
      "s_mov_b32 s46, s45\n"
      "s_add_u32 s45, s36, s47\n"
      "v_mul_f32 v209, v209, v210\n"
      "v_mfma_f32_16x16x32_bf16 v[120:123], a[44:47], v[92:95], v[120:123]\n"
      "v_cvt_pk_bf16_f32 v229, v206, v207\n"
      "v_mfma_f32_16x16x32_bf16 v[120:123], a[48:51], v[96:99], v[120:123]\n"
      "s_waitcnt lgkmcnt(4)\n"
      "v_add_f32 v213, v213, v215\n"
      "v_mfma_f32_16x16x32_bf16 v[120:123], a[52:55], v[100:103], v[120:123]\n"
      "s_waitcnt lgkmcnt(3)\n"
      "v_mul_f32 v232, 0x3eae1a93, v219\n"
      "v_mul_f32 v204, v204, v210\n"
      "v_mfma_f32_16x16x32_bf16 v[120:123], a[56:59], v[104:107], v[120:123]\n"
      "s_waitcnt lgkmcnt(2)\n"
      "v_mul_f32 v208, v208, v210\n"
      "v_cvt_pk_bf16_f32 v230, v208, v209\n"
      "v_mfma_f32_16x16x32_bf16 v[120:123], a[60:63], v[108:111], v[120:123]\n"
      "v_mfma_f32_16x16x32_bf16 v[120:123], a[64:67], v[112:115], v[120:123]\n"
      "s_waitcnt lgkmcnt(0)\n"
      "buffer_store_dwordx2 v[230:231], v8, s[8:11], s40 offen sc1\n"
      "v_mfma_f32_16x16x32_bf16 v[120:123], a[68:71], v[116:119], v[120:123]\n"
      "v_cvt_pk_bf16_f32 v228, v204, v205\n"
      "v_mfma_f32_16x16x32_bf16 v[124:127], a[216:219], v[48:51], 0\n"
      "v_add_f32 v212, v212, v214\n"
      "v_add_f32 v213, v213, v217\n"
      "v_mfma_f32_16x16x32_bf16 v[124:127], a[220:223], v[52:55], v[124:127]\n"
      "v_add_f32 v218, v212, v216\n"
      "v_fma_f32 v213, -v218, v218, v213\n"
      "v_mfma_f32_16x16x32_bf16 v[124:127], a[224:227], v[56:59], v[124:127]\n"
      "buffer_store_dwordx2 v[228:229], v7, s[8:11], s40 offen sc1\n"
      "v_mfma_f32_16x16x32_bf16 v[124:127], a[228:231], v[60:63], v[124:127]\n"
      "s_add_u32 s52, s52, 24576\n"
      "v_fma_f32 v123, -v218, v139, v123\n"
      "v_add_u32 v9, 1, v9\n"
      "v_mfma_f32_16x16x32_bf16 v[124:127], a[232:235], v[64:67], v[124:127]\n"
      "v_fma_f32 v122, -v218, v138, v122\n"
      "v_mfma_f32_16x16x32_bf16 v[124:127], a[236:239], v[68:71], v[124:127]\n"
      "s_waitcnt vmcnt(4)\n"
      "v_max_f32 v213, 0x3727c5ac, v213\n"
      "v_cmp_ne_u32 vcc, s21, v15\n"
      "v_mfma_f32_16x16x32_bf16 v[124:127], a[240:243], v[72:75], v[124:127]\n"
      "v_rsq_f32 v219, v213\n"
      "v_mfma_f32_16x16x32_bf16 v[124:127], a[244:247], v[76:79], v[124:127]\n"
      "v_mov_b32 v186, v184\n"
      "v_fma_f32 v120, -v218, v136, v120\n"
      "v_mfma_f32_16x16x32_bf16 v[124:127], a[248:251], v[80:83], v[124:127]\n"
      "s_min_u32 s45, s45, s6\n"
      "ds_write_b64 v5, v[218:219] offset:1024\n"
      "v_mfma_f32_16x16x32_bf16 v[124:127], a[252:255], v[84:87], v[124:127]\n"
      "s_cbranch_vccnz .Lsp_q1_8\n"
      ".Lbk_q1_8:\n"
      "v_mfma_f32_16x16x32_bf16 v[124:127], v[16:19], v[88:91], v[124:127]\n"
      "buffer_load_dwordx4 v0, s[8:11], s46 offen offset:1024 sc1 lds\n"
      "v_mfma_f32_16x16x32_bf16 v[124:127], v[20:23], v[92:95], v[124:127]\n"
      "v_cndmask_b32 v233, v219, 1.0, s[54:55]\n"
      "v_mul_f32 v123, v219, v123\n"
      "v_mfma_f32_16x16x32_bf16 v[124:127], v[24:27], v[96:99], v[124:127]\n"
      "ds_read_b128 v[220:223], v6 offset:1024\n"
      "v_mul_f32 v120, v219, v120\n"
      "v_mfma_f32_16x16x32_bf16 v[124:127], v[28:31], v[100:103], v[124:127]\n"
      "v_mul_f32 v122, v219, v122\n"
      "v_fma_f32 v121, -v218, v137, v121\n"
      "v_mfma_f32_16x16x32_bf16 v[124:127], v[32:35], v[104:107], v[124:127]\n"
      "s_waitcnt vmcnt(4)\n"
      "ds_write_b32 v11, v9\n"
      "v_mfma_f32_16x16x32_bf16 v[124:127], v[36:39], v[108:111], v[124:127]\n"
      "buffer_load_dwordx4 v0, s[8:11], s46 offen offset:0 sc1 lds\n"
      "v_mfma_f32_16x16x32_bf16 v[124:127], v[40:43], v[112:115], v[124:127]\n"
      "v_mfma_f32_16x16x32_bf16 v[124:127], v[44:47], v[116:119], v[124:127]\n"
      "v_cvt_pk_bf16_f32 v173, v122, v123\n"
      "v_mfma_f32_16x16x32_bf16 v[128:131], v[48:51], a[72:75], 0\n"
      "v_mul_f32 v121, v219, v121\n"
      "v_cvt_pk_bf16_f32 v172, v120, v121\n"
      "v_mfma_f32_16x16x32_bf16 v[128:131], v[52:55], a[76:79], v[128:131]\n"
      "buffer_load_dwordx4 v0, s[8:11], s46 offen offset:3072 sc1 lds\n"
      "v_mfma_f32_16x16x32_bf16 v[128:131], v[56:59], a[80:83], v[128:131]\n"
      "v_fma_f32 v125, -v218, v141, v125\n"
      "v_fma_f32 v124, -v218, v140, v124\n"
      "v_mfma_f32_16x16x32_bf16 v[128:131], v[60:63], a[84:87], v[128:131]\n"
      "v_fma_f32 v127, -v218, v143, v127\n"
      "v_mul_f32 v125, v233, v125\n"
      "v_mfma_f32_16x16x32_bf16 v[128:131], v[64:67], a[88:91], v[128:131]\n"
      "v_mul_f32 v124, v233, v124\n"
      "v_mul_f32 v127, v219, v127\n"
      "v_mfma_f32_16x16x32_bf16 v[128:131], v[68:71], a[92:95], v[128:131]\n"
      "v_fma_f32 v126, -v218, v142, v126\n"
      "v_mul_f32 v126, v219, v126\n"
      "v_mfma_f32_16x16x32_bf16 v[128:131], v[72:75], a[96:99], v[128:131]\n"
      "v_cvt_pk_bf16_f32 v175, v124, v125\n"
      "v_cvt_pk_bf16_f32 v174, v126, v127\n"
      "v_mfma_f32_16x16x32_bf16 v[128:131], v[76:79], a[100:103], v[128:131]\n"
      "buffer_load_dword v15, v2, s[8:11], s45 offen sc1\n"
      "v_mfma_f32_16x16x32_bf16 v[192:195], v[172:175], v[176:179], v[156:159]\n"
      "ds_read_b128 v[224:227], v6 offset:1040\n"
      "v_mov_b32 v187, v185\n"
      "v_mfma_f32_16x16x32_bf16 v[196:199], v[172:175], v[164:167], v[168:171]\n"
      "ds_read_b32 v10, v12\n"
      "v_mfma_f32_16x16x32_bf16 v[128:131], v[80:83], a[104:107], v[128:131]\n"
      "buffer_load_dwordx4 v0, s[8:11], s46 offen offset:2048 sc1 lds\n"
      "v_mfma_f32_16x16x32_bf16 v[128:131], v[84:87], a[108:111], v[128:131]\n"
      "v_med3_f32 v234, v192, v193, s50\n"
      "v_med3_f32 v235, v194, v195, s50\n"
      "v_mfma_f32_16x16x32_bf16 v[128:131], v[88:91], a[112:115], v[128:131]\n"
      "v_cvt_pk_bf16_f32 v185, v198, v199\n"
      "v_max3_f32 v234, v188, v189, v234\n"
      "v_mfma_f32_16x16x32_bf16 v[128:131], v[92:95], a[116:119], v[128:131]\n"
      "v_max3_f32 v235, v190, v191, v235\n"
      "v_max_f32 v236, v234, v235\n"
      "v_mfma_f32_16x16x32_bf16 v[128:131], v[96:99], a[120:123], v[128:131]\n"
      "s_waitcnt lgkmcnt(0)\n"
      "v_mov_b32 v237, v236\n"
      "v_cmp_gt_i32 vcc, v9, v10\n"
      "v_mfma_f32_16x16x32_bf16 v[128:131], v[100:103], a[124:127], v[128:131]\n"
      "s_add_u32 m0, s42, 20480\n"
      "v_permlane16_swap_b32 v236, v237\n"
      "v_mfma_f32_16x16x32_bf16 v[128:131], v[104:107], a[128:131], v[128:131]\n"
      "v_max_f32 v236, v236, v237\n"
      "v_mov_b32 v237, v236\n"
      "v_mfma_f32_16x16x32_bf16 v[128:131], v[108:111], a[132:135], v[128:131]\n"
      "buffer_load_dwordx4 v1, s[8:11], s46 offen offset:0 sc1 lds\n"
      "v_mfma_f32_16x16x32_bf16 v[128:131], v[112:115], a[136:139], v[128:131]\n"
      ".Lq_p1:\n"
      "s_cbranch_execz .Lq_pr\n"
      "v_permlane32_swap_b32 v236, v237\n"
      "v_mfma_f32_16x16x32_bf16 v[128:131], v[116:119], a[140:143], v[128:131]\n"
      "v_max_f32 v236, v236, v237\n"
      "v_mul_f32_e64 v236, v236, v232\n"
      "v_mfma_f32_16x16x32_bf16 v[132:135], a[144:147], v[48:51], 0\n"
      "s_cbranch_vccnz .Lsp_qy0_9\n"
      ".Lbk_qy0_9:\n"
      "v_fma_f32 v234, v234, v232, -v236\n"
      "v_mfma_f32_16x16x32_bf16 v[132:135], a[148:151], v[52:55], v[132:135]\n"
      "ds_read_b128 v[52:55], v0 offset:1024\n"
      "ds_read_b128 v[48:51], v0 offset:0\n"
      "v_mfma_f32_16x16x32_bf16 v[132:135], a[152:155], v[56:59], v[132:135]\n"
      "v_fma_f32 v131, -v226, v144, v131\n"
      "v_fma_f32 v235, v235, v232, -v236\n"
      "v_mfma_f32_16x16x32_bf16 v[132:135], a[156:159], v[60:63], v[132:135]\n"
      "ds_read_b128 v[56:59], v0 offset:2048\n"
      "v_fma_f32 v128, -v220, v145, v128\n"
      "v_mfma_f32_16x16x32_bf16 v[132:135], a[160:163], v[64:67], v[132:135]\n"
      "v_exp_f32 v234, v234\n"
      "v_mfma_f32_16x16x32_bf16 v[132:135], a[164:167], v[68:71], v[132:135]\n"
      "v_exp_f32 v235, v235\n"
      "v_mfma_f32_16x16x32_bf16 v[132:135], a[168:171], v[72:75], v[132:135]\n"
      "v_cvt_pk_bf16_f32 v234, v234, v234\n"
      "v_fma_f32 v129, -v222, v146, v129\n"
      "v_mfma_f32_16x16x32_bf16 v[132:135], a[172:175], v[76:79], v[132:135]\n"
      "v_mul_f32 v129, v223, v129\n"
      "v_fma_f32 v130, -v224, v147, v130\n"
      "v_mfma_f32_16x16x32_bf16 v[132:135], a[176:179], v[80:83], v[132:135]\n"
      "v_cvt_pk_bf16_f32 v235, v235, v235\n"
      "v_and_b32 v200, v160, v234\n"
      "v_mfma_f32_16x16x32_bf16 v[132:135], a[180:183], v[84:87], v[132:135]\n"
      "v_cvt_pk_bf16_f32 v184, v196, v197\n"
      "v_and_b32 v201, v161, v235\n"
      "v_mfma_f32_16x16x32_bf16 v[132:135], a[184:187], v[88:91], v[132:135]\n"
      "ds_read_b128 v[68:71], v0 offset:5120\n"
      "v_mul_f32 v130, v225, v130\n"
      "v_mfma_f32_16x16x32_bf16 v[132:135], a[188:191], v[92:95], v[132:135]\n"
      "v_and_b32 v203, v163, v235\n"
      "v_mul_f32_e64 v131, v227, v131\n"
      "v_mfma_f32_16x16x32_bf16 v[132:135], a[192:195], v[96:99], v[132:135]\n"
      "ds_read_b128 v[64:67], v0 offset:4096\n"
      "v_mul_f32_e64 v128, v221, v128\n"
      "v_mfma_f32_16x16x32_bf16 v[132:135], a[196:199], v[100:103], v[132:135]\n"
      "v_and_b32_e64 v202, v162, v234\n"
      "v_cvt_pk_bf16_f32 v181, v130, v131\n"
      "v_mfma_f32_16x16x32_bf16 v[132:135], a[200:203], v[104:107], v[132:135]\n"
      "s_add_u32 s40, s40, 24576\n"
      "ds_read_b128 v[60:63], v0 offset:3072\n"
      "v_cvt_pk_bf16_f32 v180, v128, v129\n"
      "v_mfma_f32_16x16x32_bf16 v[208:211], v[184:187], v[200:203], 0\n"
      "s_waitcnt vmcnt(6) lgkmcnt(3)\n"
      "buffer_store_dword v13, v3, s[8:11], s52 offen sc1\n"
      "ds_read2st64_b64 v[212:215], v4 offset0:0 offset1:1\n"
      "v_mfma_f32_16x16x32_bf16 v[204:207], v[180:183], v[200:203], 0\n"
      "ds_read_b128 v[72:75], v0 offset:6144\n"
      "v_mfma_f32_16x16x32_bf16 v[132:135], a[204:207], v[108:111], v[132:135]\n"
      "ds_read_b128 v[76:79], v0 offset:7168\n"
      "v_mov_b32_e64 v178, v175\n"
      "v_mfma_f32_16x16x32_bf16 v[132:135], a[208:211], v[112:115], v[132:135]\n"
      "ds_read_b128 v[88:91], v0 offset:10240\n"
      "ds_read_b128 v[108:111], v0 offset:15360\n"
      "v_mfma_f32_16x16x32_bf16 v[132:135], a[212:215], v[116:119], v[132:135]\n"
      "s_sub_u32 s20, s20, 1\n"
      "s_cbranch_scc0 .Lq_loop\n"
      "v_mul_f32 v232, 0x3eae1a93, v219\n"
      "s_nop 7\n"
      "v_fma_f32 v135, -v218, v151, v135\n"
      "v_fma_f32 v133, -v218, v149, v133\n"
      "v_fma_f32 v134, -v218, v150, v134\n"
      "v_fma_f32 v132, -v218, v148, v132\n"
      "v_cvt_pk_bf16_f32 v176, v132, v133\n"
      "v_cvt_pk_bf16_f32 v177, v134, v135\n"
      "v_rcp_f32 v210, v210\n"
      "s_nop 0\n"
      "v_mul_f32 v204, v204, v210\n"
      "v_mfma_f32_16x16x32_bf16 v[188:191], v[172:175], v[176:179], v[152:155]\n"
      "v_mul_f32 v208, v208, v210\n"
      "v_mul_f32 v207, v207, v210\n"
      "v_mul_f32 v209, v209, v210\n"
      "v_cvt_pk_bf16_f32 v230, v208, v209\n"
      "buffer_store_dwordx2 v[230:231], v8, s[8:11], s40 offen sc1\n"
      "v_mul_f32 v206, v206, v210\n"
      "v_cvt_pk_bf16_f32 v229, v206, v207\n"
      "v_mul_f32 v205, v205, v210\n"
      "v_cvt_pk_bf16_f32 v228, v204, v205\n"
      "buffer_store_dwordx2 v[228:229], v7, s[8:11], s40 offen sc1\n"
      "v_mov_b32 v187, v185\n"
      "v_mov_b32 v186, v184\n"
      "v_mov_b32 v182, v180\n"
      "s_add_u32 s52, s52, 24576\n"
      "v_mov_b32 v183, v181\n"
      "s_add_u32 s40, s40, 24576\n"
      "s_waitcnt vmcnt(0)\n"
      "buffer_store_dword v13, v3, s[8:11], s52 offen sc1\n"
      "s_add_u32 s52, s52, 24576\n"
      "s_cmp_lg_u32 s44, 0\n"
      "s_cbranch_scc1 .Lq_done\n"
      "v_lshlrev_b32 v48, 3, v0\n"
      "s_mov_b32 s22, s23\n"
      ".Lq_up:\n"
      "s_mov_b32 s30, 128\n"
      "buffer_load_dword v49, v48, s[8:11], s30 offen sc1\n"
      "s_mov_b32 s30, 8320\n"
      "buffer_load_dword v50, v48, s[8:11], s30 offen sc1\n"
      "s_mov_b32 s30, 16512\n"
      "buffer_load_dword v51, v48, s[8:11], s30 offen sc1\n"
      "s_mov_b32 s30, 24704\n"
      "buffer_load_dword v52, v48, s[8:11], s30 offen sc1\n"
      "s_waitcnt vmcnt(0)\n"
      "v_xor_b32 v49, s21, v49\n"
      "v_xor_b32 v50, s21, v50\n"
      "v_xor_b32 v51, s21, v51\n"
      "v_xor_b32 v52, s21, v52\n"
      "v_or3_b32 v49, v49, v50, v51\n"
      "v_or_b32 v49, v49, v52\n"
      "v_cmp_ne_u32 vcc, 0, v49\n"
      "s_cbranch_vccz .Lq_ok\n"
      "s_sleep 4\n"
      "s_sub_u32 s22, s22, 1\n"
      "s_cbranch_scc0 .Lq_up\n"
      ".Lq_ok:\n"
      "v_lshrrev_b32 v48, 2, v0\n"
      "v_and_b32 v48, 0x7c, v48\n"
      "v_mov_b32 v49, s21\n"
      "buffer_store_dword v49, v48, s[8:11], 0 offen sc1\n"
      ".Lq_done:\n"
      "s_endpgm\n"
      ".Lsp_q0_1:\n"
      "s_mov_b32 s22, s23\n"
      ".Lsl_q0_1:\n"
      "buffer_load_dword v15, v2, s[8:11], s46 offen sc1\n"
      "s_waitcnt vmcnt(0)\n"
      "v_cmp_ne_u32 vcc, s21, v15\n"
      "s_cbranch_vccz .Lbk_q0_1\n"
      "s_sleep 4\n"
      "s_sub_u32 s22, s22, 1\n"
      "s_cbranch_scc0 .Lsl_q0_1\n"
      "s_mov_b32 s23, 0\n"
      "s_branch .Lbk_q0_1\n"
      ".Lsp_q1_2:\n"
      "s_mov_b32 s22, s23\n"
      ".Lsl_q1_2:\n"
      "buffer_load_dword v15, v2, s[8:11], s46 offen sc1\n"
      "s_waitcnt vmcnt(0)\n"
      "v_cmp_ne_u32 vcc, s21, v15\n"
      "s_cbranch_vccz .Lbk_q1_2\n"
      "s_sleep 4\n"
      "s_sub_u32 s22, s22, 1\n"
      "s_cbranch_scc0 .Lsl_q1_2\n"
      "s_mov_b32 s23, 0\n"
      "s_branch .Lbk_q1_2\n"
      ".Lsp_qy0_3:\n"
      "s_lshl_b32 s22, s23, 4\n"
      ".Lsl_qy0_3:\n"
      "ds_read_b32 v10, v12\n"
      "s_waitcnt lgkmcnt(0)\n"
      "v_cmp_gt_i32 vcc, v9, v10\n"
      "s_cbranch_vccz .Lbk_qy0_3\n"
      "s_sub_u32 s22, s22, 1\n"
      "s_cbranch_scc0 .Lsl_qy0_3\n"
      "s_mov_b32 s23, 0\n"
      "s_branch .Lbk_qy0_3\n"
      ".Lsp_q2_4:\n"
      "s_mov_b32 s22, s23\n"
      "buffer_load_dword v15, v2, s[8:11], s46 offen sc1\n"
      "s_waitcnt vmcnt(0)\n"
      "v_cmp_ne_u32 vcc, s21, v15\n"
      "s_add_u32 s30, s52, 24576\n"
      "buffer_store_dword v13, v3, s[8:11], s30 offen sc1\n"
      "s_cbranch_vccz .Lbk_q2_4\n"
      ".Lsl_q2_4:\n"
      "buffer_load_dword v15, v2, s[8:11], s46 offen sc1\n"
      "s_waitcnt vmcnt(0)\n"
      "v_cmp_ne_u32 vcc, s21, v15\n"
      "s_cbranch_vccz .Lbk_q2_4\n"
      "s_sleep 4\n"
      "s_sub_u32 s22, s22, 1\n"
      "s_cbranch_scc0 .Lsl_q2_4\n"
      "s_mov_b32 s23, 0\n"
      "s_branch .Lbk_q2_4\n"
      ".Lsp_qy1_5:\n"
      "s_lshl_b32 s22, s23, 4\n"
      ".Lsl_qy1_5:\n"
      "ds_read_b32 v10, v12\n"
      "s_waitcnt lgkmcnt(0)\n"
      "v_cmp_gt_i32 vcc, v9, v10\n"
      "s_cbranch_vccz .Lbk_qy1_5\n"
      "s_sub_u32 s22, s22, 1\n"
      "s_cbranch_scc0 .Lsl_qy1_5\n"
      "s_mov_b32 s23, 0\n"
      "s_branch .Lbk_qy1_5\n"
      ".Lsp_q0_6:\n"
      "s_mov_b32 s22, s23\n"
      "buffer_load_dword v15, v2, s[8:11], s46 offen sc1\n"
      "s_waitcnt vmcnt(0)\n"
      "v_cmp_ne_u32 vcc, s21, v15\n"
      "s_add_u32 s30, s52, 24576\n"
      "buffer_store_dword v13, v3, s[8:11], s30 offen sc1\n"
      "s_cbranch_vccz .Lbk_q0_6\n"
      ".Lsl_q0_6:\n"
      "buffer_load_dword v15, v2, s[8:11], s46 offen sc1\n"
      "s_waitcnt vmcnt(0)\n"
      "v_cmp_ne_u32 vcc, s21, v15\n"
      "s_cbranch_vccz .Lbk_q0_6\n"
      "s_sleep 4\n"
      "s_sub_u32 s22, s22, 1\n"
      "s_cbranch_scc0 .Lsl_q0_6\n"
      "s_mov_b32 s23, 0\n"
      "s_branch .Lbk_q0_6\n"
      ".Lsp_qy2_7:\n"
      "s_lshl_b32 s22, s23, 4\n"
      ".Lsl_qy2_7:\n"
      "ds_read_b32 v10, v12\n"
      "s_waitcnt lgkmcnt(0)\n"
      "v_cmp_gt_i32 vcc, v9, v10\n"
      "s_cbranch_vccz .Lbk_qy2_7\n"
      "s_sub_u32 s22, s22, 1\n"
      "s_cbranch_scc0 .Lsl_qy2_7\n"
      "s_mov_b32 s23, 0\n"
      "s_branch .Lbk_qy2_7\n"
      ".Lsp_q1_8:\n"
      "s_mov_b32 s22, s23\n"
      "buffer_load_dword v15, v2, s[8:11], s46 offen sc1\n"
      "s_waitcnt vmcnt(0)\n"
      "v_cmp_ne_u32 vcc, s21, v15\n"
      "s_add_u32 s30, s52, 0\n"
      "buffer_store_dword v13, v3, s[8:11], s30 offen sc1\n"
      "s_cbranch_vccz .Lbk_q1_8\n"
      ".Lsl_q1_8:\n"
      "buffer_load_dword v15, v2, s[8:11], s46 offen sc1\n"
      "s_waitcnt vmcnt(0)\n"
      "v_cmp_ne_u32 vcc, s21, v15\n"
      "s_cbranch_vccz .Lbk_q1_8\n"
      "s_sleep 4\n"
      "s_sub_u32 s22, s22, 1\n"
      "s_cbranch_scc0 .Lsl_q1_8\n"
      "s_mov_b32 s23, 0\n"
      "s_branch .Lbk_q1_8\n"
      ".Lsp_qy0_9:\n"
      "s_lshl_b32 s22, s23, 4\n"
      ".Lsl_qy0_9:\n"
      "ds_read_b32 v10, v12\n"
      "s_waitcnt lgkmcnt(0)\n"
      "v_cmp_gt_i32 vcc, v9, v10\n"
      "s_cbranch_vccz .Lbk_qy0_9\n"
      "s_sub_u32 s22, s22, 1\n"
      "s_cbranch_scc0 .Lsl_qy0_9\n"
      "s_mov_b32 s23, 0\n"
      "s_branch .Lbk_qy0_9\n"
      ".Lq_last:\n"
      :
      : "{s[8:11]}"(rsWS), "{s[12:15]}"(rsA), "{v0}"(v_l16), "{v1}"(v_l5), "{v2}"(v_fa), "{v3}"(v_ra), "{v4}"(v_st), "{v5}"(v_tw), "{v6}"(v_tr), "{v7}"(v_om), "{v8}"(v_oe), "{v10}"(v_al), "{v11}"(v_yw), "{v12}"(v_yr), "{v14}"(v_hi), "{v152}"(v_bc[0]), "{v153}"(v_bc[1]), "{v154}"(v_bc[2]), "{v155}"(v_bc[3]), "{v156}"(v_bp[0]), "{v157}"(v_bp[1]), "{v158}"(v_bp[2]), "{v159}"(v_bp[3]), "{v160}"(v_mk[0]), "{v161}"(v_mk[1]), "{v162}"(v_mk[2]), "{v163}"(v_mk[3]), "{v164}"(v_sel[0]), "{v165}"(v_sel[1]), "{v166}"(v_sel[2]), "{v167}"(v_sel[3]), "{v168}"(v_ci[0]), "{v169}"(v_ci[1]), "{v170}"(v_ci[2]), "{v171}"(v_ci[3]), "{v48}"(v_vo[0]), "{v49}"(v_vo[1]), "{v50}"(v_vo[2]), "{v51}"(v_vo[3]), "{v52}"(v_at), "{v53}"(v_ax), "{v54}"(v_pm), "{v55}"(v_ms), "{v76}"(v_pw), "{s36}"(s_sx), "{s37}"(s_ev), "{s38}"(s_del), "{s39}"(s_kil), "{s40}"(s_so), "{s41}"(s_m4), "{s42}"(s_m5), "{s43}"(s_ms), "{s44}"(s_up)
      : "v255", "a255", "s99", "vcc", "scc", "memory");

  } else if (slot == 4) {
    const unsigned gr = 3 - (xcd & 3), c = xcd >> 2, o = w & 1;
    const u32x4 rsA = srd(Wo, 576 * 576 * 2);
    const unsigned v_l5 = v_l16 + 16384 - w * 3072, e0 = 16384 + o * 2048 + g * 512 + i16 * 8, e1 = e0 + 2048 - o * 4096;
    const unsigned v_ex[8] = {e0, e1, e0 + 20480, e1 + 20480, e0 + 40960, e1 + 40960, e0 + 61440, e1 + 61440}, v_l2 = v_l16 + 40960;
    const unsigned v_fa = 20480 - w * 4096 + (8 * w + (lane >> 3)) * 128 + (lane & 7) * 4, v_ra = up * 8 * REC + l5 + 19456 + (4 * c + w) * 128;
    const unsigned v_xp = (9 * c + 2 * w) * 1024 + v_l16, v_xs = (9 * c + 8) * 1024 + (w >> 1) * 512 + half;
    const unsigned v_rp = v_xp + WS_XIN, v_rs = v_xs + WS_XIN;
    const unsigned v_ew = 82944 + (w >> 1) * 1024 + v_l16;
    const unsigned v_sw = 86016 + i16 * 144 + (4 * w + g) * 8, v_sr = 86016 + (8 * (w >> 1) + l8) * 144 + l7;
    const unsigned v_ss = 18432 + c * 512 + (w >> 1) * 256 + l8 * 32 + p * 8;
    const unsigned v_pw = 19456 + (4 * c + w) * 128 + l8 * REC + l7;
    unsigned v_vo[9], v_vp[9];
#pragma unroll
    for (unsigned j = 0; j < 9; ++j) {
      const unsigned ch = 64 * j + lane, r = ch / 36, x = 576 * c + ch % 36 * 16 - 1024 * (j & 3);
      v_vo[j] = (20 * o + r) * 1152 + x;
      v_vp[j] = (36 * (4 * o + (r >> 2)) + 16 + (r & 3)) * 1152 + x;
    }
    const unsigned ro = (8 * g + q) * 576;
    const unsigned v_t0 = ro + o * 18432 + 128 * w + 16 * p, v_t1 = v_t0 + 18432 - 2 * o * 18432, v_ts = ro + o * 18432 + 512 + 32 * (w >> 1) + 8 * p;
    const unsigned s_sx = WS_OB + gr * NREC * REC + w * 4096, s_ev = s_sx + 31 * REC, s_sy = WS_X1 + gr * NREC * REC;
    const unsigned s_m4 = w * 4096, s_m5 = 16384 + w * 1024, s_m9 = w * 9216, s_so = (w >> 1) * 8 * 41472;
    const unsigned s_s8 = s_so, s_wg = o, s_rb = WS_OB + w * 4096;
    asm volatile(
      "s_buffer_load_dword s21, s[8:11], 0\n"
      "s_mov_b32 s23, 65536\n"
      "s_mov_b32 s31, 0x40000000\n"
      "s_add_u32 m0, s41, 0\n"
      "s_nop 0\n"
      "buffer_load_dwordx4 v100, s[12:15], s42 offen offset:0 lds\n"
      "buffer_load_dwordx4 v101, s[12:15], s42 offen offset:1024 lds\n"
      "buffer_load_dwordx4 v102, s[12:15], s42 offen offset:2048 lds\n"
      "buffer_load_dwordx4 v103, s[12:15], s42 offen offset:3072 lds\n"
      "s_add_u32 m0, s41, 4096\n"
      "s_nop 0\n"
      "buffer_load_dwordx4 v104, s[12:15], s42 offen offset:0 lds\n"
      "buffer_load_dwordx4 v105, s[12:15], s42 offen offset:1024 lds\n"
      "buffer_load_dwordx4 v106, s[12:15], s42 offen offset:2048 lds\n"
      "buffer_load_dwordx4 v107, s[12:15], s42 offen offset:3072 lds\n"
      "s_add_u32 m0, s41, 8192\n"
      "s_nop 0\n"
      "buffer_load_dwordx4 v108, s[12:15], s42 offen offset:0 lds\n"
      "s_add_u32 s42, s42, 41472\n"
      "s_add_u32 m0, s41, 36864\n"
      "s_nop 0\n"
      "buffer_load_dwordx4 v100, s[12:15], s42 offen offset:0 lds\n"
      "buffer_load_dwordx4 v101, s[12:15], s42 offen offset:1024 lds\n"
      "buffer_load_dwordx4 v102, s[12:15], s42 offen offset:2048 lds\n"
      "buffer_load_dwordx4 v103, s[12:15], s42 offen offset:3072 lds\n"
      "s_add_u32 m0, s41, 40960\n"
      "s_nop 0\n"
      "buffer_load_dwordx4 v104, s[12:15], s42 offen offset:0 lds\n"
      "buffer_load_dwordx4 v105, s[12:15], s42 offen offset:1024 lds\n"
      "buffer_load_dwordx4 v106, s[12:15], s42 offen offset:2048 lds\n"
      "buffer_load_dwordx4 v107, s[12:15], s42 offen offset:3072 lds\n"
      "s_add_u32 m0, s41, 45056\n"
      "s_nop 0\n"
      "buffer_load_dwordx4 v108, s[12:15], s42 offen offset:0 lds\n"
      "s_add_u32 s42, s42, 41472\n"
      "s_add_u32 m0, s41, 73728\n"
      "s_nop 0\n"
      "buffer_load_dwordx4 v100, s[12:15], s42 offen offset:0 lds\n"
      "buffer_load_dwordx4 v101, s[12:15], s42 offen offset:1024 lds\n"
      "buffer_load_dwordx4 v102, s[12:15], s42 offen offset:2048 lds\n"
      "buffer_load_dwordx4 v103, s[12:15], s42 offen offset:3072 lds\n"
      "s_add_u32 m0, s41, 77824\n"
      "s_nop 0\n"
      "buffer_load_dwordx4 v104, s[12:15], s42 offen offset:0 lds\n"
      "buffer_load_dwordx4 v105, s[12:15], s42 offen offset:1024 lds\n"
      "buffer_load_dwordx4 v106, s[12:15], s42 offen offset:2048 lds\n"
      "buffer_load_dwordx4 v107, s[12:15], s42 offen offset:3072 lds\n"
      "s_add_u32 m0, s41, 81920\n"
      "s_nop 0\n"
      "buffer_load_dwordx4 v108, s[12:15], s42 offen offset:0 lds\n"
      "s_add_u32 s42, s42, 41472\n"
      "s_waitcnt vmcnt(18)\n"
      "s_barrier\n"
      "ds_read_b64_tr_b16 v[64:65], v120 offset:0\n"
      "ds_read_b64_tr_b16 v[66:67], v120 offset:2304\n"
      "ds_read_b64_tr_b16 a[0:1], v118 offset:0\n"
      "ds_read_b64_tr_b16 a[2:3], v118 offset:2304\n"
      "ds_read_b64_tr_b16 a[72:73], v118 offset:8\n"
      "ds_read_b64_tr_b16 a[74:75], v118 offset:2312\n"
      "ds_read_b64_tr_b16 a[144:145], v118 offset:64\n"
      "ds_read_b64_tr_b16 a[146:147], v118 offset:2368\n"
      "ds_read_b64_tr_b16 a[216:217], v118 offset:72\n"
      "ds_read_b64_tr_b16 a[218:219], v118 offset:2376\n"
      "ds_read_b64_tr_b16 a[36:37], v119 offset:0\n"
      "ds_read_b64_tr_b16 a[38:39], v119 offset:2304\n"
      "ds_read_b64_tr_b16 a[108:109], v119 offset:8\n"
      "ds_read_b64_tr_b16 a[110:111], v119 offset:2312\n"
      "ds_read_b64_tr_b16 a[180:181], v119 offset:64\n"
      "ds_read_b64_tr_b16 a[182:183], v119 offset:2368\n"
      "ds_read_b64_tr_b16 a[252:253], v119 offset:72\n"
      "ds_read_b64_tr_b16 a[254:255], v119 offset:2376\n"
      "v_add_u32 v118, 36864, v118\n"
      "v_add_u32 v119, 36864, v119\n"
      "v_add_u32 v120, 36864, v120\n"
      "s_add_u32 m0, s41, 110592\n"
      "s_nop 0\n"
      "buffer_load_dwordx4 v100, s[12:15], s42 offen offset:0 lds\n"
      "buffer_load_dwordx4 v101, s[12:15], s42 offen offset:1024 lds\n"
      "buffer_load_dwordx4 v102, s[12:15], s42 offen offset:2048 lds\n"
      "buffer_load_dwordx4 v103, s[12:15], s42 offen offset:3072 lds\n"
      "s_add_u32 m0, s41, 114688\n"
      "s_nop 0\n"
      "buffer_load_dwordx4 v104, s[12:15], s42 offen offset:0 lds\n"
      "buffer_load_dwordx4 v105, s[12:15], s42 offen offset:1024 lds\n"
      "buffer_load_dwordx4 v106, s[12:15], s42 offen offset:2048 lds\n"
      "buffer_load_dwordx4 v107, s[12:15], s42 offen offset:3072 lds\n"
      "s_add_u32 m0, s41, 118784\n"
      "s_nop 0\n"
      "buffer_load_dwordx4 v108, s[12:15], s42 offen offset:0 lds\n"
      "s_add_u32 s42, s42, 41472\n"
      "s_waitcnt vmcnt(18) lgkmcnt(0)\n"
      "s_barrier\n"
      "ds_read_b64_tr_b16 v[68:69], v120 offset:0\n"
      "ds_read_b64_tr_b16 v[70:71], v120 offset:2304\n"
      "ds_read_b64_tr_b16 a[4:5], v118 offset:0\n"
      "ds_read_b64_tr_b16 a[6:7], v118 offset:2304\n"
      "ds_read_b64_tr_b16 a[76:77], v118 offset:8\n"
      "ds_read_b64_tr_b16 a[78:79], v118 offset:2312\n"
      "ds_read_b64_tr_b16 a[148:149], v118 offset:64\n"
      "ds_read_b64_tr_b16 a[150:151], v118 offset:2368\n"
      "ds_read_b64_tr_b16 a[220:221], v118 offset:72\n"
      "ds_read_b64_tr_b16 a[222:223], v118 offset:2376\n"
      "ds_read_b64_tr_b16 a[40:41], v119 offset:0\n"
      "ds_read_b64_tr_b16 a[42:43], v119 offset:2304\n"
      "ds_read_b64_tr_b16 a[112:113], v119 offset:8\n"
      "ds_read_b64_tr_b16 a[114:115], v119 offset:2312\n"
      "ds_read_b64_tr_b16 a[184:185], v119 offset:64\n"
      "ds_read_b64_tr_b16 a[186:187], v119 offset:2368\n"
      "ds_read_b64_tr_b16 v[32:33], v119 offset:72\n"
      "ds_read_b64_tr_b16 v[34:35], v119 offset:2376\n"
      "v_add_u32 v118, 36864, v118\n"
      "v_add_u32 v119, 36864, v119\n"
      "v_add_u32 v120, 36864, v120\n"
      "s_add_u32 m0, s41, 0\n"
      "s_nop 0\n"
      "buffer_load_dwordx4 v100, s[12:15], s42 offen offset:0 lds\n"
      "buffer_load_dwordx4 v101, s[12:15], s42 offen offset:1024 lds\n"
      "buffer_load_dwordx4 v102, s[12:15], s42 offen offset:2048 lds\n"
      "buffer_load_dwordx4 v103, s[12:15], s42 offen offset:3072 lds\n"
      "s_add_u32 m0, s41, 4096\n"
      "s_nop 0\n"
      "buffer_load_dwordx4 v104, s[12:15], s42 offen offset:0 lds\n"
      "buffer_load_dwordx4 v105, s[12:15], s42 offen offset:1024 lds\n"
      "buffer_load_dwordx4 v106, s[12:15], s42 offen offset:2048 lds\n"
      "buffer_load_dwordx4 v107, s[12:15], s42 offen offset:3072 lds\n"
      "s_add_u32 m0, s41, 8192\n"
      "s_nop 0\n"
      "buffer_load_dwordx4 v108, s[12:15], s42 offen offset:0 lds\n"
      "s_add_u32 s42, s42, 41472\n"
      "s_waitcnt vmcnt(18) lgkmcnt(0)\n"
      "s_barrier\n"
      "ds_read_b64_tr_b16 v[72:73], v120 offset:0\n"
      "ds_read_b64_tr_b16 v[74:75], v120 offset:2304\n"
      "ds_read_b64_tr_b16 a[8:9], v118 offset:0\n"
      "ds_read_b64_tr_b16 a[10:11], v118 offset:2304\n"
      "ds_read_b64_tr_b16 a[80:81], v118 offset:8\n"
      "ds_read_b64_tr_b16 a[82:83], v118 offset:2312\n"
      "ds_read_b64_tr_b16 a[152:153], v118 offset:64\n"
      "ds_read_b64_tr_b16 a[154:155], v118 offset:2368\n"
      "ds_read_b64_tr_b16 a[224:225], v118 offset:72\n"
      "ds_read_b64_tr_b16 a[226:227], v118 offset:2376\n"
      "ds_read_b64_tr_b16 a[44:45], v119 offset:0\n"
      "ds_read_b64_tr_b16 a[46:47], v119 offset:2304\n"
      "ds_read_b64_tr_b16 a[116:117], v119 offset:8\n"
      "ds_read_b64_tr_b16 a[118:119], v119 offset:2312\n"
      "ds_read_b64_tr_b16 a[188:189], v119 offset:64\n"
      "ds_read_b64_tr_b16 a[190:191], v119 offset:2368\n"
      "ds_read_b64_tr_b16 v[36:37], v119 offset:72\n"
      "ds_read_b64_tr_b16 v[38:39], v119 offset:2376\n"
      "v_add_u32 v118, 36864, v118\n"
      "v_add_u32 v119, 36864, v119\n"
      "v_add_u32 v120, 36864, v120\n"
      "s_add_u32 m0, s41, 36864\n"
      "s_nop 0\n"
      "buffer_load_dwordx4 v100, s[12:15], s42 offen offset:0 lds\n"
      "buffer_load_dwordx4 v101, s[12:15], s42 offen offset:1024 lds\n"
      "buffer_load_dwordx4 v102, s[12:15], s42 offen offset:2048 lds\n"
      "buffer_load_dwordx4 v103, s[12:15], s42 offen offset:3072 lds\n"
      "s_add_u32 m0, s41, 40960\n"
      "s_nop 0\n"
      "buffer_load_dwordx4 v104, s[12:15], s42 offen offset:0 lds\n"
      "buffer_load_dwordx4 v105, s[12:15], s42 offen offset:1024 lds\n"
      "buffer_load_dwordx4 v106, s[12:15], s42 offen offset:2048 lds\n"
      "buffer_load_dwordx4 v107, s[12:15], s42 offen offset:3072 lds\n"
      "s_add_u32 m0, s41, 45056\n"
      "s_nop 0\n"
      "buffer_load_dwordx4 v108, s[12:15], s42 offen offset:0 lds\n"
      "s_add_u32 s42, s42, 41472\n"
      "s_waitcnt vmcnt(18) lgkmcnt(0)\n"
      "s_barrier\n"
      "ds_read_b64_tr_b16 v[76:77], v120 offset:0\n"
      "ds_read_b64_tr_b16 v[78:79], v120 offset:2304\n"
      "ds_read_b64_tr_b16 a[12:13], v118 offset:0\n"
      "ds_read_b64_tr_b16 a[14:15], v118 offset:2304\n"
      "ds_read_b64_tr_b16 a[84:85], v118 offset:8\n"
      "ds_read_b64_tr_b16 a[86:87], v118 offset:2312\n"
      "ds_read_b64_tr_b16 a[156:157], v118 offset:64\n"
      "ds_read_b64_tr_b16 a[158:159], v118 offset:2368\n"
      "ds_read_b64_tr_b16 a[228:229], v118 offset:72\n"
      "ds_read_b64_tr_b16 a[230:231], v118 offset:2376\n"
      "ds_read_b64_tr_b16 a[48:49], v119 offset:0\n"
      "ds_read_b64_tr_b16 a[50:51], v119 offset:2304\n"
      "ds_read_b64_tr_b16 a[120:121], v119 offset:8\n"
      "ds_read_b64_tr_b16 a[122:123], v119 offset:2312\n"
      "ds_read_b64_tr_b16 a[192:193], v119 offset:64\n"
      "ds_read_b64_tr_b16 a[194:195], v119 offset:2368\n"
      "ds_read_b64_tr_b16 v[40:41], v119 offset:72\n"
      "ds_read_b64_tr_b16 v[42:43], v119 offset:2376\n"
      "v_add_u32 v118, 4294856704, v118\n"
      "v_add_u32 v119, 4294856704, v119\n"
      "v_add_u32 v120, 4294856704, v120\n"
      "s_add_u32 m0, s41, 73728\n"
      "s_nop 0\n"
      "buffer_load_dwordx4 v100, s[12:15], s42 offen offset:0 lds\n"
      "buffer_load_dwordx4 v101, s[12:15], s42 offen offset:1024 lds\n"
      "buffer_load_dwordx4 v102, s[12:15], s42 offen offset:2048 lds\n"
      "buffer_load_dwordx4 v103, s[12:15], s42 offen offset:3072 lds\n"
      "s_add_u32 m0, s41, 77824\n"
      "s_nop 0\n"
      "buffer_load_dwordx4 v104, s[12:15], s42 offen offset:0 lds\n"
      "buffer_load_dwordx4 v105, s[12:15], s42 offen offset:1024 lds\n"
      "buffer_load_dwordx4 v106, s[12:15], s42 offen offset:2048 lds\n"
      "buffer_load_dwordx4 v107, s[12:15], s42 offen offset:3072 lds\n"
      "s_add_u32 m0, s41, 81920\n"
      "s_nop 0\n"
      "buffer_load_dwordx4 v108, s[12:15], s42 offen offset:0 lds\n"
      "s_add_u32 s42, s42, 41472\n"
      "s_waitcnt vmcnt(18) lgkmcnt(0)\n"
      "s_barrier\n"
      "ds_read_b64_tr_b16 v[80:81], v120 offset:0\n"
      "ds_read_b64_tr_b16 v[82:83], v120 offset:2304\n"
      "ds_read_b64_tr_b16 a[16:17], v118 offset:0\n"
      "ds_read_b64_tr_b16 a[18:19], v118 offset:2304\n"
      "ds_read_b64_tr_b16 a[88:89], v118 offset:8\n"
      "ds_read_b64_tr_b16 a[90:91], v118 offset:2312\n"
      "ds_read_b64_tr_b16 a[160:161], v118 offset:64\n"
      "ds_read_b64_tr_b16 a[162:163], v118 offset:2368\n"
      "ds_read_b64_tr_b16 a[232:233], v118 offset:72\n"
      "ds_read_b64_tr_b16 a[234:235], v118 offset:2376\n"
      "ds_read_b64_tr_b16 a[52:53], v119 offset:0\n"
      "ds_read_b64_tr_b16 a[54:55], v119 offset:2304\n"
      "ds_read_b64_tr_b16 a[124:125], v119 offset:8\n"
      "ds_read_b64_tr_b16 a[126:127], v119 offset:2312\n"
      "ds_read_b64_tr_b16 a[196:197], v119 offset:64\n"
      "ds_read_b64_tr_b16 a[198:199], v119 offset:2368\n"
      "ds_read_b64_tr_b16 v[44:45], v119 offset:72\n"
      "ds_read_b64_tr_b16 v[46:47], v119 offset:2376\n"
      "v_add_u32 v118, 36864, v118\n"
      "v_add_u32 v119, 36864, v119\n"
      "v_add_u32 v120, 36864, v120\n"
      "s_add_u32 m0, s41, 110592\n"
      "s_nop 0\n"
      "buffer_load_dwordx4 v100, s[12:15], s42 offen offset:0 lds\n"
      "buffer_load_dwordx4 v101, s[12:15], s42 offen offset:1024 lds\n"
      "buffer_load_dwordx4 v102, s[12:15], s42 offen offset:2048 lds\n"
      "buffer_load_dwordx4 v103, s[12:15], s42 offen offset:3072 lds\n"
      "s_add_u32 m0, s41, 114688\n"
      "s_nop 0\n"
      "buffer_load_dwordx4 v104, s[12:15], s42 offen offset:0 lds\n"
      "buffer_load_dwordx4 v105, s[12:15], s42 offen offset:1024 lds\n"
      "buffer_load_dwordx4 v106, s[12:15], s42 offen offset:2048 lds\n"
      "buffer_load_dwordx4 v107, s[12:15], s42 offen offset:3072 lds\n"
      "s_add_u32 m0, s41, 118784\n"
      "s_nop 0\n"
      "buffer_load_dwordx4 v108, s[12:15], s42 offen offset:0 lds\n"
      "s_add_u32 s42, s42, 41472\n"
      "s_waitcnt vmcnt(18) lgkmcnt(0)\n"
      "s_barrier\n"
      "ds_read_b64_tr_b16 v[84:85], v120 offset:0\n"
      "ds_read_b64_tr_b16 v[86:87], v120 offset:2304\n"
      "ds_read_b64_tr_b16 a[20:21], v118 offset:0\n"
      "ds_read_b64_tr_b16 a[22:23], v118 offset:2304\n"
      "ds_read_b64_tr_b16 a[92:93], v118 offset:8\n"
      "ds_read_b64_tr_b16 a[94:95], v118 offset:2312\n"
      "ds_read_b64_tr_b16 a[164:165], v118 offset:64\n"
      "ds_read_b64_tr_b16 a[166:167], v118 offset:2368\n"
      "ds_read_b64_tr_b16 a[236:237], v118 offset:72\n"
      "ds_read_b64_tr_b16 a[238:239], v118 offset:2376\n"
      "ds_read_b64_tr_b16 a[56:57], v119 offset:0\n"
      "ds_read_b64_tr_b16 a[58:59], v119 offset:2304\n"
      "ds_read_b64_tr_b16 a[128:129], v119 offset:8\n"
      "ds_read_b64_tr_b16 a[130:131], v119 offset:2312\n"
      "ds_read_b64_tr_b16 a[200:201], v119 offset:64\n"
      "ds_read_b64_tr_b16 a[202:203], v119 offset:2368\n"
      "ds_read_b64_tr_b16 v[48:49], v119 offset:72\n"
      "ds_read_b64_tr_b16 v[50:51], v119 offset:2376\n"
      "v_add_u32 v118, 36864, v118\n"
      "v_add_u32 v119, 36864, v119\n"
      "v_add_u32 v120, 36864, v120\n"
      "s_add_u32 m0, s41, 0\n"
      "s_nop 0\n"
      "buffer_load_dwordx4 v109, s[12:15], s43 offen offset:0 lds\n"
      "buffer_load_dwordx4 v110, s[12:15], s43 offen offset:1024 lds\n"
      "buffer_load_dwordx4 v111, s[12:15], s43 offen offset:2048 lds\n"
      "buffer_load_dwordx4 v112, s[12:15], s43 offen offset:3072 lds\n"
      "s_add_u32 m0, s41, 4096\n"
      "s_nop 0\n"
      "buffer_load_dwordx4 v113, s[12:15], s43 offen offset:0 lds\n"
      "buffer_load_dwordx4 v114, s[12:15], s43 offen offset:1024 lds\n"
      "buffer_load_dwordx4 v115, s[12:15], s43 offen offset:2048 lds\n"
      "buffer_load_dwordx4 v116, s[12:15], s43 offen offset:3072 lds\n"
      "s_add_u32 m0, s41, 8192\n"
      "s_nop 0\n"
      "buffer_load_dwordx4 v117, s[12:15], s43 offen offset:0 lds\n"
      "s_add_u32 s42, s42, 41472\n"
      "s_waitcnt vmcnt(18) lgkmcnt(0)\n"
      "s_barrier\n"
      "ds_read_b64_tr_b16 v[88:89], v120 offset:0\n"
      "ds_read_b64_tr_b16 v[90:91], v120 offset:2304\n"
      "ds_read_b64_tr_b16 a[24:25], v118 offset:0\n"
      "ds_read_b64_tr_b16 a[26:27], v118 offset:2304\n"
      "ds_read_b64_tr_b16 a[96:97], v118 offset:8\n"
      "ds_read_b64_tr_b16 a[98:99], v118 offset:2312\n"
      "ds_read_b64_tr_b16 a[168:169], v118 offset:64\n"
      "ds_read_b64_tr_b16 a[170:171], v118 offset:2368\n"
      "ds_read_b64_tr_b16 a[240:241], v118 offset:72\n"
      "ds_read_b64_tr_b16 a[242:243], v118 offset:2376\n"
      "ds_read_b64_tr_b16 a[60:61], v119 offset:0\n"
      "ds_read_b64_tr_b16 a[62:63], v119 offset:2304\n"
      "ds_read_b64_tr_b16 a[132:133], v119 offset:8\n"
      "ds_read_b64_tr_b16 a[134:135], v119 offset:2312\n"
      "ds_read_b64_tr_b16 a[204:205], v119 offset:64\n"
      "ds_read_b64_tr_b16 a[206:207], v119 offset:2368\n"
      "ds_read_b64_tr_b16 v[52:53], v119 offset:72\n"
      "ds_read_b64_tr_b16 v[54:55], v119 offset:2376\n"
      "v_add_u32 v118, 36864, v118\n"
      "v_add_u32 v119, 36864, v119\n"
      "v_add_u32 v120, 36864, v120\n"
      "s_waitcnt vmcnt(9) lgkmcnt(0)\n"
      "s_barrier\n"
      "ds_read_b64_tr_b16 v[92:93], v120 offset:0\n"
      "ds_read_b64_tr_b16 v[94:95], v120 offset:2304\n"
      "ds_read_b64_tr_b16 a[28:29], v118 offset:0\n"
      "ds_read_b64_tr_b16 a[30:31], v118 offset:2304\n"
      "ds_read_b64_tr_b16 a[100:101], v118 offset:8\n"
      "ds_read_b64_tr_b16 a[102:103], v118 offset:2312\n"
      "ds_read_b64_tr_b16 a[172:173], v118 offset:64\n"
      "ds_read_b64_tr_b16 a[174:175], v118 offset:2368\n"
      "ds_read_b64_tr_b16 a[244:245], v118 offset:72\n"
      "ds_read_b64_tr_b16 a[246:247], v118 offset:2376\n"
      "ds_read_b64_tr_b16 a[64:65], v119 offset:0\n"
      "ds_read_b64_tr_b16 a[66:67], v119 offset:2304\n"
      "ds_read_b64_tr_b16 a[136:137], v119 offset:8\n"
      "ds_read_b64_tr_b16 a[138:139], v119 offset:2312\n"
      "ds_read_b64_tr_b16 a[208:209], v119 offset:64\n"
      "ds_read_b64_tr_b16 a[210:211], v119 offset:2368\n"
      "ds_read_b64_tr_b16 v[56:57], v119 offset:72\n"
      "ds_read_b64_tr_b16 v[58:59], v119 offset:2376\n"
      "v_add_u32 v118, 4294856704, v118\n"
      "v_add_u32 v119, 4294856704, v119\n"
      "v_add_u32 v120, 4294856704, v120\n"
      "s_waitcnt vmcnt(0) lgkmcnt(0)\n"
      "s_barrier\n"
      "ds_read_b64_tr_b16 v[96:97], v120 offset:0\n"
      "ds_read_b64_tr_b16 v[98:99], v120 offset:2304\n"
      "ds_read_b64_tr_b16 a[32:33], v118 offset:0\n"
      "ds_read_b64_tr_b16 a[34:35], v118 offset:2304\n"
      "ds_read_b64_tr_b16 a[104:105], v118 offset:8\n"
      "ds_read_b64_tr_b16 a[106:107], v118 offset:2312\n"
      "ds_read_b64_tr_b16 a[176:177], v118 offset:64\n"
      "ds_read_b64_tr_b16 a[178:179], v118 offset:2368\n"
      "ds_read_b64_tr_b16 a[248:249], v118 offset:72\n"
      "ds_read_b64_tr_b16 a[250:251], v118 offset:2376\n"
      "ds_read_b64_tr_b16 a[68:69], v119 offset:0\n"
      "ds_read_b64_tr_b16 a[70:71], v119 offset:2304\n"
      "ds_read_b64_tr_b16 a[140:141], v119 offset:8\n"
      "ds_read_b64_tr_b16 a[142:143], v119 offset:2312\n"
      "ds_read_b64_tr_b16 a[212:213], v119 offset:64\n"
      "ds_read_b64_tr_b16 a[214:215], v119 offset:2368\n"
      "ds_read_b64_tr_b16 v[60:61], v119 offset:72\n"
      "ds_read_b64_tr_b16 v[62:63], v119 offset:2376\n"
      "v_add_u32 v118, 36864, v118\n"
      "v_add_u32 v119, 36864, v119\n"
      "v_add_u32 v120, 36864, v120\n"
      "s_waitcnt lgkmcnt(0)\n"
      "s_mul_i32 s21, s21, 1664525\n"
      "s_add_u32 s21, s21, 1013904223\n"
      "v_mov_b32 v25, s21\n"
      "buffer_store_dword v25, v22, s[8:11], 0 offen sc1\n"
      "v_not_b32 v124, v25\n"
      "v_not_b32 v125, v25\n"
      "v_not_b32 v126, v25\n"
      "v_not_b32 v127, v25\n"
      "s_add_u32 s30, s38, 0\n"
      "buffer_store_dwordx4 v[124:127], v121, s[8:11], s30 offen sc1\n"
      "v_xor_b32 v25, v25, v26\n"
      "v_mov_b32 v27, 0x3ae38e39\n"
      "s_barrier\n"
      "v_mov_b32 v21, 0\n"
      "ds_write_b32 v23, v21\n"
      "s_waitcnt lgkmcnt(0)\n"
      "s_barrier\n"
      "s_add_u32 s6, s36, 1572864\n"
      "s_add_u32 s36, s36, 4294942720\n"
      "s_mov_b32 s51, 24576\n"
      "s_waitcnt vmcnt(0) lgkmcnt(0)\n"
      "s_cmp_lg_u32 s44, 0\n"
      "s_cbranch_scc1 .Lv_start\n"
      "s_mov_b64 exec, 0\n"
      "s_lshr_b32 s22, s39, 13\n"
      ".Lw_pl:\n"
      "s_cmp_eq_u32 s22, 0\n"
      "s_cbranch_scc1 .Lw_p0\n"
      "s_cmp_eq_u32 s22, 1\n"
      "s_cbranch_scc1 .Lw_p1\n"
      "s_branch .Lw_pe\n"
      ".Lw_pr:\n"
      "s_add_u32 s22, s22, 2\n"
      "s_branch .Lw_pl\n"
      ".Lw_pe:\n"
      "s_mov_b64 exec, -1\n"
      "s_add_u32 s50, s38, 4294918144\n"
      "s_add_u32 s49, s38, 4294918144\n"
      "s_add_u32 s38, s38, 4294942720\n"
      "s_cmp_eq_u32 s36, s37\n"
      "s_cselect_b32 s48, 49152, s51\n"
      "s_add_u32 s36, s36, s48\n"
      "s_min_u32 s36, s36, s6\n"
      "buffer_load_dword v28, v11, s[8:11], s36 offen sc1\n"
      "s_mov_b32 s46, s36\n"
      "s_add_u32 s36, s36, 24576\n"
      "s_add_u32 m0, s39, 0\n"
      "s_waitcnt vmcnt(0)\n"
      "v_cmp_ne_u32 vcc, s21, v28\n"
      "s_min_u32 s36, s36, s6\n"
      "s_cbranch_vccnz .Lsp_w0_1\n"
      ".Lbk_w0_1:\n"
      "buffer_load_dword v28, v11, s[8:11], s36 offen sc1\n"
      "buffer_load_dwordx4 v0, s[8:11], s46 offen offset:0 sc1 lds\n"
      "buffer_load_dwordx4 v0, s[8:11], s46 offen offset:1024 sc1 lds\n"
      "buffer_load_dwordx4 v0, s[8:11], s46 offen offset:3072 sc1 lds\n"
      "buffer_load_dwordx4 v0, s[8:11], s46 offen offset:2048 sc1 lds\n"
      "s_add_u32 m0, s40, 0\n"
      "s_nop 0\n"
      "buffer_load_dwordx4 v1, s[8:11], s46 offen offset:0 sc1 lds\n"
      "s_sub_u32 s47, s46, s45\n"
      "buffer_load_dwordx2 v[200:201], v14, s[8:11], s47 offen sc1\n"
      "buffer_load_dwordx4 v[192:195], v13, s[8:11], s47 offen sc1\n"
      "s_mov_b32 s46, s36\n"
      "buffer_load_dword v26, v23, s[8:11], s31 offen\n"
      "buffer_load_dword v26, v23, s[8:11], s31 offen\n"
      "buffer_load_dword v26, v23, s[8:11], s31 offen\n"
      "s_add_u32 m0, s39, 20480\n"
      "s_waitcnt vmcnt(10)\n"
      "v_cmp_ne_u32 vcc, s21, v28\n"
      "s_cmp_eq_u32 s36, s37\n"
      "s_cselect_b32 s48, 49152, s51\n"
      "s_cbranch_vccnz .Lsp_w1_2\n"
      ".Lbk_w1_2:\n"
      "buffer_load_dwordx4 v0, s[8:11], s46 offen offset:0 sc1 lds\n"
      "s_add_u32 s36, s36, s48\n"
      "s_min_u32 s36, s36, s6\n"
      "buffer_load_dword v28, v11, s[8:11], s36 offen sc1\n"
      "buffer_load_dwordx4 v0, s[8:11], s46 offen offset:1024 sc1 lds\n"
      "buffer_load_dwordx4 v0, s[8:11], s46 offen offset:3072 sc1 lds\n"
      "v_add_u32 v21, 1, v21\n"
      "s_waitcnt vmcnt(9)\n"
      "ds_write_b32 v23, v21\n"
      "buffer_load_dwordx4 v0, s[8:11], s46 offen offset:2048 sc1 lds\n"
      "s_add_u32 m0, s40, 20480\n"
      "s_nop 0\n"
      "buffer_load_dwordx4 v1, s[8:11], s46 offen offset:0 sc1 lds\n"
      "ds_read_b32 v22, v24\n"
      "buffer_load_dwordx4 v[196:199], v13, s[8:11], s47 offen offset:1024 sc1\n"
      "s_sub_u32 s47, s46, s45\n"
      "s_waitcnt lgkmcnt(0)\n"
      "v_cmp_gt_i32 vcc, v21, v22\n"
      "buffer_load_dword v26, v23, s[8:11], s31 offen\n"
      "s_cbranch_vccnz .Lsp_wy0_3\n"
      ".Lbk_wy0_3:\n"
      "ds_read_b128 v[104:107], v0 offset:1024\n"
      "s_waitcnt vmcnt(12)\n"
      "v_lshlrev_b32 v190, 16, v201\n"
      "s_waitcnt vmcnt(11)\n"
      "v_lshlrev_b32 v176, 16, v194\n"
      "ds_read_b128 v[100:103], v0 offset:0\n"
      "v_and_b32 v191, 0xffff0000, v201\n"
      "ds_read_b128 v[120:123], v0 offset:5120\n"
      "v_lshlrev_b32 v174, 16, v193\n"
      "v_lshlrev_b32 v178, 16, v195\n"
      "ds_read_b128 v[112:115], v0 offset:3072\n"
      "ds_read_b128 v[108:111], v0 offset:2048\n"
      "ds_read_b128 v[124:127], v0 offset:6144\n"
      "ds_read_b128 v[116:119], v0 offset:4096\n"
      "v_and_b32 v189, 0xffff0000, v200\n"
      "ds_read_b128 v[140:143], v0 offset:9216\n"
      "s_mov_b32 s46, s36\n"
      "ds_read2_b32 v[132:133], v2 offset0:0 offset1:32\n"
      "v_and_b32 v179, 0xffff0000, v195\n"
      "ds_read_b128 v[128:131], v0 offset:7168\n"
      "ds_read_b128 v[136:139], v0 offset:8192\n"
      "v_lshlrev_b32 v188, 16, v200\n"
      "v_and_b32 v175, 0xffff0000, v193\n"
      "s_mov_b32 s20, 15\n"
      "s_waitcnt lgkmcnt(0)\n"
      "s_nop 4\n"
      "s_branch .Lw_loop\n"
      ".p2align 12\n"
      ".Lw_p0:\n"
      "s_branch .Lw_pr\n"
      ".p2align 5\n"
      ".Lw_loop:\n"
      "v_mfma_f32_16x16x32_bf16 v[188:191], v[64:67], v[100:103], v[188:191]\n"
      "ds_read2_b32 v[134:135], v2 offset0:64 offset1:96\n"
      "v_and_b32 v173, 0xffff0000, v192\n"
      "v_mfma_f32_16x16x32_bf16 v[188:191], v[68:71], v[104:107], v[188:191]\n"
      "s_add_u32 s36, s36, 24576\n"
      "ds_read_b128 v[156:159], v0 offset:13312\n"
      "v_add_f32 v218, v218, v183\n"
      "v_mfma_f32_16x16x32_bf16 v[188:191], v[72:75], v[108:111], v[188:191]\n"
      "v_fmac_f32 v219, v182, v182\n"
      "v_fmac_f32 v219, v183, v183\n"
      "v_mfma_f32_16x16x32_bf16 v[188:191], v[76:79], v[112:115], v[188:191]\n"
      "s_waitcnt lgkmcnt(3)\n"
      "v_add_f32 v218, v218, v184\n"
      "v_fmac_f32 v219, v184, v184\n"
      "v_mfma_f32_16x16x32_bf16 v[188:191], v[80:83], v[116:119], v[188:191]\n"
      "buffer_load_dwordx2 v[200:201], v14, s[8:11], s47 offen sc1\n"
      "v_mfma_f32_16x16x32_bf16 v[188:191], v[84:87], v[120:123], v[188:191]\n"
      "ds_read_b128 v[148:151], v0 offset:11264\n"
      "v_and_b32 v177, 0xffff0000, v194\n"
      "v_mfma_f32_16x16x32_bf16 v[188:191], v[88:91], v[124:127], v[188:191]\n"
      "ds_read_b128 v[160:163], v0 offset:14336\n"
      "v_cvt_pk_bf16_f32 v215, v186, v187\n"
      "v_mfma_f32_16x16x32_bf16 v[188:191], v[92:95], v[128:131], v[188:191]\n"
      "s_waitcnt lgkmcnt(3)\n"
      "v_fmac_f32 v219, v185, v185\n"
      "v_lshlrev_b32 v172, 16, v192\n"
      "v_mfma_f32_16x16x32_bf16 v[188:191], v[96:99], v[132:135], v[188:191]\n"
      "buffer_load_dwordx4 v[192:195], v13, s[8:11], s47 offen sc1\n"
      "v_mfma_f32_16x16x32_bf16 v[172:175], a[0:3], v[100:103], v[172:175]\n"
      "v_fmac_f32 v219, v186, v186\n"
      "v_fmac_f32 v219, v187, v187\n"
      "v_mfma_f32_16x16x32_bf16 v[172:175], a[4:7], v[104:107], v[172:175]\n"
      "ds_read2_b32 v[170:171], v3 offset0:64 offset1:96\n"
      "v_add_u32 v21, 1, v21\n"
      "v_mfma_f32_16x16x32_bf16 v[172:175], a[8:11], v[108:111], v[172:175]\n"
      "ds_read_b128 v[144:147], v0 offset:10240\n"
      "v_add_f32 v218, v218, v185\n"
      "v_mfma_f32_16x16x32_bf16 v[172:175], a[12:15], v[112:115], v[172:175]\n"
      "ds_read2_b32 v[168:169], v3 offset0:0 offset1:32\n"
      "v_mfma_f32_16x16x32_bf16 v[172:175], a[16:19], v[116:119], v[172:175]\n"
      "s_add_u32 m0, s39, 40960\n"
      "ds_read_b128 v[164:167], v0 offset:15360\n"
      "v_add_f32 v218, v218, v186\n"
      "v_mfma_f32_16x16x32_bf16 v[172:175], a[20:23], v[120:123], v[172:175]\n"
      "ds_read_b128 v[152:155], v0 offset:12288\n"
      "v_mfma_f32_16x16x32_bf16 v[172:175], a[24:27], v[124:127], v[172:175]\n"
      "v_mfma_f32_16x16x32_bf16 v[172:175], a[28:31], v[128:131], v[172:175]\n"
      "v_cvt_pk_bf16_f32 v213, v182, v183\n"
      "v_add_f32 v218, v218, v187\n"
      "v_mfma_f32_16x16x32_bf16 v[172:175], a[32:35], v[132:135], v[172:175]\n"
      "ds_write_b64 v18, v[218:219]\n"
      "v_mfma_f32_16x16x32_bf16 v[172:175], a[36:39], v[136:139], v[172:175]\n"
      "v_mfma_f32_16x16x32_bf16 v[172:175], a[40:43], v[140:143], v[172:175]\n"
      "s_waitcnt lgkmcnt(3)\n"
      "buffer_store_dwordx2 v[216:217], v16, s[8:11], s38 offen sc1\n"
      "v_mfma_f32_16x16x32_bf16 v[172:175], a[44:47], v[144:147], v[172:175]\n"
      "ds_write_b32 v23, v21\n"
      "v_mfma_f32_16x16x32_bf16 v[172:175], a[48:51], v[148:151], v[172:175]\n"
      "s_waitcnt lgkmcnt(2)\n"
      "v_cvt_pk_bf16_f32 v214, v184, v185\n"
      "v_mfma_f32_16x16x32_bf16 v[172:175], a[52:55], v[152:155], v[172:175]\n"
      "v_cvt_pk_bf16_f32 v212, v180, v181\n"
      "v_mfma_f32_16x16x32_bf16 v[172:175], a[56:59], v[156:159], v[172:175]\n"
      "buffer_store_dwordx4 v[212:215], v15, s[8:11], s38 offen offset:1024 sc1\n"
      "v_mfma_f32_16x16x32_bf16 v[172:175], a[60:63], v[160:163], v[172:175]\n"
      "v_mfma_f32_16x16x32_bf16 v[172:175], a[64:67], v[164:167], v[172:175]\n"
      "s_waitcnt vmcnt(10) lgkmcnt(1)\n"
      "v_cmp_ne_u32 vcc, s21, v28\n"
      "v_mfma_f32_16x16x32_bf16 v[172:175], a[68:71], v[168:171], v[172:175]\n"
      "v_mfma_f32_16x16x32_bf16 v[176:179], a[72:75], v[100:103], v[176:179]\n"
      "v_mfma_f32_16x16x32_bf16 v[176:179], a[76:79], v[104:107], v[176:179]\n"
      "s_min_u32 s36, s36, s6\n"
      "s_cbranch_vccnz .Lsp_w0_4\n"
      ".Lbk_w0_4:\n"
      "v_mfma_f32_16x16x32_bf16 v[176:179], a[80:83], v[108:111], v[176:179]\n"
      "buffer_load_dword v28, v11, s[8:11], s36 offen sc1\n"
      "v_mfma_f32_16x16x32_bf16 v[176:179], a[84:87], v[112:115], v[176:179]\n"
      "ds_read_b32 v22, v24\n"
      "v_mfma_f32_16x16x32_bf16 v[176:179], a[88:91], v[116:119], v[176:179]\n"
      "v_cvt_pk_bf16_f32 v208, v172, v173\n"
      "v_mfma_f32_16x16x32_bf16 v[176:179], a[92:95], v[120:123], v[176:179]\n"
      "buffer_load_dwordx4 v0, s[8:11], s46 offen offset:0 sc1 lds\n"
      "v_mfma_f32_16x16x32_bf16 v[176:179], a[96:99], v[124:127], v[176:179]\n"
      "v_add_f32 v218, v172, v173\n"
      "v_mfma_f32_16x16x32_bf16 v[176:179], a[100:103], v[128:131], v[176:179]\n"
      "s_waitcnt vmcnt(7) lgkmcnt(1)\n"
      "v_and_b32 v181, 0xffff0000, v196\n"
      "v_mul_f32 v219, v172, v172\n"
      "v_mfma_f32_16x16x32_bf16 v[176:179], a[104:107], v[132:135], v[176:179]\n"
      "s_waitcnt lgkmcnt(0)\n"
      "v_cmp_gt_i32 vcc, v21, v22\n"
      "v_fmac_f32_e64 v219, v173, v173\n"
      "v_mfma_f32_16x16x32_bf16 v[176:179], a[108:111], v[136:139], v[176:179]\n"
      "buffer_store_dword v25, v12, s[8:11], s50 offen sc1\n"
      "v_mfma_f32_16x16x32_bf16 v[176:179], a[112:115], v[140:143], v[176:179]\n"
      "v_lshlrev_b32_e64 v180, 16, v196\n"
      "v_and_b32 v183, 0xffff0000, v197\n"
      "v_mfma_f32_16x16x32_bf16 v[176:179], a[116:119], v[144:147], v[176:179]\n"
      "s_cbranch_vccnz .Lsp_wz0_5\n"
      ".Lbk_wz0_5:\n"
      "v_add_f32 v218, v218, v174\n"
      "v_mfma_f32_16x16x32_bf16 v[176:179], a[120:123], v[148:151], v[176:179]\n"
      "ds_read_b128 v[204:207], v17\n"
      "v_add_u32 v21, 1, v21\n"
      "v_mfma_f32_16x16x32_bf16 v[176:179], a[124:127], v[152:155], v[176:179]\n"
      "buffer_load_dwordx4 v0, s[8:11], s46 offen offset:1024 sc1 lds\n"
      "v_mfma_f32_16x16x32_bf16 v[176:179], a[128:131], v[156:159], v[176:179]\n"
      "v_lshlrev_b32 v182, 16, v197\n"
      "v_mfma_f32_16x16x32_bf16 v[176:179], a[132:135], v[160:163], v[176:179]\n"
      "ds_write_b32 v23, v21\n"
      "v_mfma_f32_16x16x32_bf16 v[176:179], a[136:139], v[164:167], v[176:179]\n"
      "s_add_u32 s50, s50, 24576\n"
      "v_fmac_f32 v219, v174, v174\n"
      "v_add_f32 v218, v218, v175\n"
      "v_mfma_f32_16x16x32_bf16 v[176:179], a[140:143], v[168:171], v[176:179]\n"
      "buffer_load_dwordx4 v0, s[8:11], s46 offen offset:3072 sc1 lds\n"
      "v_mfma_f32_16x16x32_bf16 v[180:183], a[144:147], v[100:103], v[180:183]\n"
      "v_and_b32 v185, 0xffff0000, v198\n"
      "v_mfma_f32_16x16x32_bf16 v[180:183], a[148:151], v[104:107], v[180:183]\n"
      "s_waitcnt lgkmcnt(1)\n"
      "v_cvt_pk_bf16_f32 v209, v174, v175\n"
      "v_add_f32 v190, v190, v206\n"
      "v_mfma_f32_16x16x32_bf16 v[180:183], a[152:155], v[108:111], v[180:183]\n"
      "v_add_f32 v189, v189, v205\n"
      "v_lshlrev_b32 v184, 16, v198\n"
      "v_mfma_f32_16x16x32_bf16 v[180:183], a[156:159], v[112:115], v[180:183]\n"
      "buffer_load_dwordx4 v0, s[8:11], s46 offen offset:2048 sc1 lds\n"
      "v_mfma_f32_16x16x32_bf16 v[180:183], a[160:163], v[116:119], v[180:183]\n"
      "v_cvt_pk_bf16_f32 v211, v178, v179\n"
      "v_add_f32 v191, v191, v207\n"
      "v_mfma_f32_16x16x32_bf16 v[180:183], a[164:167], v[120:123], v[180:183]\n"
      "v_and_b32 v187, 0xffff0000, v199\n"
      "s_add_u32 m0, s40, 40960\n"
      "v_mfma_f32_16x16x32_bf16 v[180:183], a[168:171], v[124:127], v[180:183]\n"
      "v_cvt_pk_bf16_f32 v217, v190, v191\n"
      "v_add_f32 v188, v188, v204\n"
      "v_mfma_f32_16x16x32_bf16 v[180:183], a[172:175], v[128:131], v[180:183]\n"
      "buffer_load_dwordx4 v1, s[8:11], s46 offen offset:0 sc1 lds\n"
      "v_mfma_f32_16x16x32_bf16 v[180:183], a[176:179], v[132:135], v[180:183]\n"
      "ds_read_b32 v22, v24\n"
      "v_lshlrev_b32 v186, 16, v199\n"
      "v_mfma_f32_16x16x32_bf16 v[180:183], a[180:183], v[136:139], v[180:183]\n"
      "v_fmac_f32 v219, v175, v175\n"
      "v_add_f32 v218, v218, v176\n"
      "v_mfma_f32_16x16x32_bf16 v[180:183], a[184:187], v[140:143], v[180:183]\n"
      "v_fmac_f32 v219, v176, v176\n"
      "v_fmac_f32 v219, v177, v177\n"
      "v_mfma_f32_16x16x32_bf16 v[180:183], a[188:191], v[144:147], v[180:183]\n"
      "v_fmac_f32 v219, v178, v178\n"
      "v_fmac_f32 v219, v179, v179\n"
      "v_mfma_f32_16x16x32_bf16 v[180:183], a[192:195], v[148:151], v[180:183]\n"
      "v_fmac_f32 v219, v188, v188\n"
      "v_fmac_f32 v219, v189, v189\n"
      "v_mfma_f32_16x16x32_bf16 v[180:183], a[196:199], v[152:155], v[180:183]\n"
      "buffer_load_dwordx4 v[196:199], v13, s[8:11], s47 offen offset:1024 sc1\n"
      "v_mfma_f32_16x16x32_bf16 v[180:183], a[200:203], v[156:159], v[180:183]\n"
      "v_add_f32 v218, v218, v177\n"
      "v_mfma_f32_16x16x32_bf16 v[180:183], a[204:207], v[160:163], v[180:183]\n"
      "v_add_f32 v218, v218, v178\n"
      "v_add_f32 v218, v218, v179\n"
      "v_mfma_f32_16x16x32_bf16 v[180:183], a[208:211], v[164:167], v[180:183]\n"
      "s_waitcnt lgkmcnt(0)\n"
      "v_add_f32 v218, v218, v188\n"
      "v_cmp_gt_i32 vcc, v21, v22\n"
      "v_mfma_f32_16x16x32_bf16 v[180:183], a[212:215], v[168:171], v[180:183]\n"
      "v_add_f32 v218, v218, v189\n"
      "v_fmac_f32 v219, v190, v190\n"
      "v_mfma_f32_16x16x32_bf16 v[184:187], a[216:219], v[100:103], v[184:187]\n"
      "v_cvt_pk_bf16_f32 v216, v188, v189\n"
      "v_add_f32 v218, v218, v190\n"
      "v_mfma_f32_16x16x32_bf16 v[184:187], a[220:223], v[104:107], v[184:187]\n"
      "s_cbranch_vccnz .Lsp_wy1_6\n"
      ".Lbk_wy1_6:\n"
      "v_add_f32 v218, v218, v191\n"
      "v_mfma_f32_16x16x32_bf16 v[184:187], a[224:227], v[108:111], v[184:187]\n"
      "s_waitcnt vmcnt(11)\n"
      "ds_read_b128 v[108:111], v0 offset:22528\n"
      "v_lshlrev_b32 v190, 16, v201\n"
      "v_mfma_f32_16x16x32_bf16 v[184:187], a[228:231], v[112:115], v[184:187]\n"
      "s_add_u32 s38, s38, 24576\n"
      "v_lshlrev_b32 v188, 16, v200\n"
      "v_mfma_f32_16x16x32_bf16 v[184:187], a[232:235], v[116:119], v[184:187]\n"
      "ds_read_b128 v[100:103], v0 offset:20480\n"
      "v_mfma_f32_16x16x32_bf16 v[184:187], a[236:239], v[120:123], v[184:187]\n"
      "ds_read_b128 v[116:119], v0 offset:24576\n"
      "v_cvt_pk_bf16_f32 v210, v176, v177\n"
      "v_mfma_f32_16x16x32_bf16 v[184:187], a[240:243], v[124:127], v[184:187]\n"
      "ds_read_b128 v[104:107], v0 offset:21504\n"
      "v_fmac_f32 v219, v191, v191\n"
      "v_mfma_f32_16x16x32_bf16 v[184:187], a[244:247], v[128:131], v[184:187]\n"
      "v_and_b32 v189, 0xffff0000, v200\n"
      "s_sub_u32 s47, s46, s45\n"
      "v_mfma_f32_16x16x32_bf16 v[184:187], a[248:251], v[132:135], v[184:187]\n"
      "v_mfma_f32_16x16x32_bf16 v[184:187], a[252:255], v[136:139], v[184:187]\n"
      "s_waitcnt vmcnt(10) lgkmcnt(3)\n"
      "ds_read_b128 v[112:115], v0 offset:23552\n"
      "v_lshlrev_b32 v178, 16, v195\n"
      "v_mfma_f32_16x16x32_bf16 v[184:187], v[32:35], v[140:143], v[184:187]\n"
      "ds_read_b128 v[140:143], v0 offset:29696\n"
      "v_and_b32 v173, 0xffff0000, v192\n"
      "v_mfma_f32_16x16x32_bf16 v[184:187], v[36:39], v[144:147], v[184:187]\n"
      "buffer_store_dwordx4 v[208:211], v15, s[8:11], s38 offen sc1\n"
      "v_mfma_f32_16x16x32_bf16 v[184:187], v[40:43], v[148:151], v[184:187]\n"
      "ds_read_b128 v[120:123], v0 offset:25600\n"
      "v_fmac_f32 v219, v180, v180\n"
      "v_mfma_f32_16x16x32_bf16 v[184:187], v[44:47], v[152:155], v[184:187]\n"
      "ds_read2_b32 v[134:135], v4 offset0:64 offset1:96\n"
      "v_and_b32 v191, 0xffff0000, v201\n"
      "v_mfma_f32_16x16x32_bf16 v[184:187], v[48:51], v[156:159], v[184:187]\n"
      "ds_read2_b32 v[132:133], v4 offset0:0 offset1:32\n"
      "v_lshlrev_b32 v174, 16, v193\n"
      "v_mfma_f32_16x16x32_bf16 v[184:187], v[52:55], v[160:163], v[184:187]\n"
      "ds_read_b128 v[156:159], v0 offset:33792\n"
      "v_fmac_f32 v219, v181, v181\n"
      "v_mfma_f32_16x16x32_bf16 v[184:187], v[56:59], v[164:167], v[184:187]\n"
      "ds_read_b128 v[124:127], v0 offset:26624\n"
      "v_add_f32 v218, v218, v180\n"
      "v_mfma_f32_16x16x32_bf16 v[184:187], v[60:63], v[168:171], v[184:187]\n"
      "s_waitcnt lgkmcnt(5)\n"
      "ds_read_b128 v[128:131], v0 offset:27648\n"
      "v_lshlrev_b32 v172, 16, v192\n"
      "v_mfma_f32_16x16x32_bf16 v[188:191], v[64:67], v[100:103], v[188:191]\n"
      "v_lshlrev_b32 v176, 16, v194\n"
      "v_and_b32 v177, 0xffff0000, v194\n"
      "v_mfma_f32_16x16x32_bf16 v[188:191], v[68:71], v[104:107], v[188:191]\n"
      "buffer_load_dwordx2 v[200:201], v14, s[8:11], s47 offen sc1\n"
      "v_mfma_f32_16x16x32_bf16 v[188:191], v[72:75], v[108:111], v[188:191]\n"
      "v_and_b32 v175, 0xffff0000, v193\n"
      "v_and_b32 v179, 0xffff0000, v195\n"
      "v_mfma_f32_16x16x32_bf16 v[188:191], v[76:79], v[112:115], v[188:191]\n"
      "ds_read_b128 v[144:147], v0 offset:30720\n"
      "v_add_f32 v218, v218, v181\n"
      "v_mfma_f32_16x16x32_bf16 v[188:191], v[80:83], v[116:119], v[188:191]\n"
      "s_waitcnt lgkmcnt(3)\n"
      "ds_read_b128 v[136:139], v0 offset:28672\n"
      "v_fmac_f32 v219, v182, v182\n"
      "v_mfma_f32_16x16x32_bf16 v[188:191], v[84:87], v[120:123], v[188:191]\n"
      "s_waitcnt lgkmcnt(3)\n"
      "v_fmac_f32 v219, v183, v183\n"
      "v_fmac_f32 v219, v184, v184\n"
      "v_mfma_f32_16x16x32_bf16 v[188:191], v[88:91], v[124:127], v[188:191]\n"
      "s_waitcnt lgkmcnt(2)\n"
      "buffer_load_dwordx4 v[192:195], v13, s[8:11], s47 offen sc1\n"
      "v_mfma_f32_16x16x32_bf16 v[188:191], v[92:95], v[128:131], v[188:191]\n"
      "ds_read2_b32 v[170:171], v5 offset0:64 offset1:96\n"
      "v_add_f32 v218, v218, v182\n"
      "v_mfma_f32_16x16x32_bf16 v[188:191], v[96:99], v[132:135], v[188:191]\n"
      "v_add_f32 v218, v218, v183\n"
      "v_mfma_f32_16x16x32_bf16 v[172:175], a[0:3], v[100:103], v[172:175]\n"
      "ds_read_b128 v[160:163], v0 offset:34816\n"
      "v_add_u32 v21, 1, v21\n"
      "v_mfma_f32_16x16x32_bf16 v[172:175], a[4:7], v[104:107], v[172:175]\n"
      "ds_read_b128 v[148:151], v0 offset:31744\n"
      "v_add_f32 v218, v218, v184\n"
      "v_mfma_f32_16x16x32_bf16 v[172:175], a[8:11], v[108:111], v[172:175]\n"
      "ds_read2_b32 v[168:169], v5 offset0:0 offset1:32\n"
      "s_mov_b32 s46, s36\n"
      "v_mfma_f32_16x16x32_bf16 v[172:175], a[12:15], v[112:115], v[172:175]\n"
      "ds_read_b128 v[152:155], v0 offset:32768\n"
      "v_add_f32 v218, v218, v185\n"
      "v_mfma_f32_16x16x32_bf16 v[172:175], a[16:19], v[116:119], v[172:175]\n"
      "buffer_store_dwordx2 v[216:217], v16, s[8:11], s38 offen sc1\n"
      "v_mfma_f32_16x16x32_bf16 v[172:175], a[20:23], v[120:123], v[172:175]\n"
      "ds_read_b128 v[164:167], v0 offset:35840\n"
      "v_add_f32 v218, v218, v186\n"
      "v_mfma_f32_16x16x32_bf16 v[172:175], a[24:27], v[124:127], v[172:175]\n"
      "v_fmac_f32 v219, v185, v185\n"
      "v_fmac_f32 v219, v186, v186\n"
      "v_mfma_f32_16x16x32_bf16 v[172:175], a[28:31], v[128:131], v[172:175]\n"
      "v_add_f32 v218, v218, v187\n"
      "v_fmac_f32 v219, v187, v187\n"
      "v_mfma_f32_16x16x32_bf16 v[172:175], a[32:35], v[132:135], v[172:175]\n"
      "s_waitcnt lgkmcnt(3)\n"
      "ds_write_b64 v18, v[218:219]\n"
      "v_mfma_f32_16x16x32_bf16 v[172:175], a[36:39], v[136:139], v[172:175]\n"
      "s_waitcnt vmcnt(12) lgkmcnt(3)\n"
      "buffer_store_dword v25, v12, s[8:11], s50 offen sc1\n"
      "v_mfma_f32_16x16x32_bf16 v[172:175], a[40:43], v[140:143], v[172:175]\n"
      "v_cvt_pk_bf16_f32 v213, v182, v183\n"
      "v_cvt_pk_bf16_f32 v215, v186, v187\n"
      "v_mfma_f32_16x16x32_bf16 v[172:175], a[44:47], v[144:147], v[172:175]\n"
      "ds_write_b32 v23, v21\n"
      "v_mfma_f32_16x16x32_bf16 v[172:175], a[48:51], v[148:151], v[172:175]\n"
      "s_waitcnt lgkmcnt(2)\n"
      "v_cvt_pk_bf16_f32 v212, v180, v181\n"
      "v_cvt_pk_bf16_f32 v214, v184, v185\n"
      "v_mfma_f32_16x16x32_bf16 v[172:175], a[52:55], v[152:155], v[172:175]\n"
      "buffer_store_dwordx4 v[212:215], v15, s[8:11], s38 offen offset:1024 sc1\n"
      "v_mfma_f32_16x16x32_bf16 v[172:175], a[56:59], v[156:159], v[172:175]\n"
      "s_add_u32 m0, s39, 61440\n"
      "s_add_u32 s38, s38, 24576\n"
      "v_mfma_f32_16x16x32_bf16 v[172:175], a[60:63], v[160:163], v[172:175]\n"
      "s_waitcnt vmcnt(13) lgkmcnt(1)\n"
      "v_cmp_ne_u32 vcc, s21, v28\n"
      "v_mfma_f32_16x16x32_bf16 v[172:175], a[64:67], v[164:167], v[172:175]\n"
      "v_mfma_f32_16x16x32_bf16 v[172:175], a[68:71], v[168:171], v[172:175]\n"
      "v_mfma_f32_16x16x32_bf16 v[176:179], a[72:75], v[100:103], v[176:179]\n"
      "s_cmp_eq_u32 s36, s37\n"
      "s_cselect_b32 s48, 49152, s51\n"
      "v_mfma_f32_16x16x32_bf16 v[176:179], a[76:79], v[104:107], v[176:179]\n"
      "s_cbranch_vccnz .Lsp_w1_7\n"
      ".Lbk_w1_7:\n"
      "v_mfma_f32_16x16x32_bf16 v[176:179], a[80:83], v[108:111], v[176:179]\n"
      "buffer_load_dwordx4 v0, s[8:11], s46 offen offset:0 sc1 lds\n"
      "v_mfma_f32_16x16x32_bf16 v[176:179], a[84:87], v[112:115], v[176:179]\n"
      "ds_read_b32 v22, v24\n"
      "v_cvt_pk_bf16_f32 v208, v172, v173\n"
      "v_mfma_f32_16x16x32_bf16 v[176:179], a[88:91], v[116:119], v[176:179]\n"
      "v_add_f32 v218, v172, v173\n"
      "v_mul_f32 v219, v172, v172\n"
      "v_mfma_f32_16x16x32_bf16 v[176:179], a[92:95], v[120:123], v[176:179]\n"
      "s_add_u32 s36, s36, s48\n"
      "v_add_f32 v218, v218, v174\n"
      "v_mfma_f32_16x16x32_bf16 v[176:179], a[96:99], v[124:127], v[176:179]\n"
      "s_min_u32 s36, s36, s6\n"
      "v_fmac_f32 v219, v173, v173\n"
      "v_mfma_f32_16x16x32_bf16 v[176:179], a[100:103], v[128:131], v[176:179]\n"
      "buffer_load_dword v28, v11, s[8:11], s36 offen sc1\n"
      "v_mfma_f32_16x16x32_bf16 v[176:179], a[104:107], v[132:135], v[176:179]\n"
      "s_waitcnt lgkmcnt(0)\n"
      "v_cmp_gt_i32 vcc, v21, v22\n"
      "v_fmac_f32 v219, v174, v174\n"
      "v_mfma_f32_16x16x32_bf16 v[176:179], a[108:111], v[136:139], v[176:179]\n"
      "buffer_load_dwordx4 v0, s[8:11], s46 offen offset:1024 sc1 lds\n"
      "v_mfma_f32_16x16x32_bf16 v[176:179], a[112:115], v[140:143], v[176:179]\n"
      "s_waitcnt vmcnt(9)\n"
      "v_lshlrev_b32 v182, 16, v197\n"
      "v_and_b32 v185, 0xffff0000, v198\n"
      "v_mfma_f32_16x16x32_bf16 v[176:179], a[116:119], v[144:147], v[176:179]\n"
      "v_lshlrev_b32 v180, 16, v196\n"
      "v_and_b32 v183, 0xffff0000, v197\n"
      "v_mfma_f32_16x16x32_bf16 v[176:179], a[120:123], v[148:151], v[176:179]\n"
      "s_cbranch_vccnz .Lsp_wz1_8\n"
      ".Lbk_wz1_8:\n"
      "ds_read_b128 v[204:207], v17\n"
      "v_mfma_f32_16x16x32_bf16 v[176:179], a[124:127], v[152:155], v[176:179]\n"
      "buffer_load_dwordx4 v0, s[8:11], s46 offen offset:3072 sc1 lds\n"
      "v_mfma_f32_16x16x32_bf16 v[176:179], a[128:131], v[156:159], v[176:179]\n"
      "v_add_f32 v218, v218, v175\n"
      "v_mfma_f32_16x16x32_bf16 v[176:179], a[132:135], v[160:163], v[176:179]\n"
      "v_add_u32 v21, 1, v21\n"
      "v_lshlrev_b32 v184, 16, v198\n"
      "v_mfma_f32_16x16x32_bf16 v[176:179], a[136:139], v[164:167], v[176:179]\n"
      "v_and_b32 v181, 0xffff0000, v196\n"
      "v_cvt_pk_bf16_f32 v209, v174, v175\n"
      "v_mfma_f32_16x16x32_bf16 v[176:179], a[140:143], v[168:171], v[176:179]\n"
      "ds_write_b32 v23, v21\n"
      "v_mfma_f32_16x16x32_bf16 v[180:183], a[144:147], v[100:103], v[180:183]\n"
      "buffer_load_dwordx4 v0, s[8:11], s46 offen offset:2048 sc1 lds\n"
      "v_mfma_f32_16x16x32_bf16 v[180:183], a[148:151], v[104:107], v[180:183]\n"
      "s_waitcnt lgkmcnt(1)\n"
      "v_add_f32 v188, v188, v204\n"
      "v_add_f32 v190, v190, v206\n"
      "v_mfma_f32_16x16x32_bf16 v[180:183], a[152:155], v[108:111], v[180:183]\n"
      "v_add_f32 v191, v191, v207\n"
      "v_lshlrev_b32 v186, 16, v199\n"
      "v_mfma_f32_16x16x32_bf16 v[180:183], a[156:159], v[112:115], v[180:183]\n"
      "v_add_f32 v218, v218, v176\n"
      "v_fmac_f32 v219, v175, v175\n"
      "v_mfma_f32_16x16x32_bf16 v[180:183], a[160:163], v[116:119], v[180:183]\n"
      "s_add_u32 m0, s40, 61440\n"
      "v_cvt_pk_bf16_f32 v217, v190, v191\n"
      "v_add_f32 v218, v218, v177\n"
      "v_mfma_f32_16x16x32_bf16 v[180:183], a[164:167], v[120:123], v[180:183]\n"
      "s_add_u32 s50, s50, 24576\n"
      "v_cvt_pk_bf16_f32 v211, v178, v179\n"
      "v_and_b32 v187, 0xffff0000, v199\n"
      "v_mfma_f32_16x16x32_bf16 v[180:183], a[168:171], v[124:127], v[180:183]\n"
      "buffer_load_dwordx4 v1, s[8:11], s46 offen offset:0 sc1 lds\n"
      "v_mfma_f32_16x16x32_bf16 v[180:183], a[172:175], v[128:131], v[180:183]\n"
      "v_add_f32 v218, v218, v178\n"
      "v_add_f32 v189, v189, v205\n"
      "v_mfma_f32_16x16x32_bf16 v[180:183], a[176:179], v[132:135], v[180:183]\n"
      "v_fmac_f32 v219, v176, v176\n"
      "v_mfma_f32_16x16x32_bf16 v[180:183], a[180:183], v[136:139], v[180:183]\n"
      "ds_read_b32 v22, v24\n"
      "v_mfma_f32_16x16x32_bf16 v[180:183], a[184:187], v[140:143], v[180:183]\n"
      "buffer_load_dwordx4 v[196:199], v13, s[8:11], s47 offen offset:1024 sc1\n"
      "v_mfma_f32_16x16x32_bf16 v[180:183], a[188:191], v[144:147], v[180:183]\n"
      "v_fmac_f32 v219, v177, v177\n"
      "v_add_f32 v218, v218, v179\n"
      "v_mfma_f32_16x16x32_bf16 v[180:183], a[192:195], v[148:151], v[180:183]\n"
      "v_fmac_f32 v219, v178, v178\n"
      "v_fmac_f32 v219, v179, v179\n"
      "v_mfma_f32_16x16x32_bf16 v[180:183], a[196:199], v[152:155], v[180:183]\n"
      "v_fmac_f32 v219, v188, v188\n"
      "v_fmac_f32 v219, v189, v189\n"
      "v_mfma_f32_16x16x32_bf16 v[180:183], a[200:203], v[156:159], v[180:183]\n"
      "s_sub_u32 s47, s46, s45\n"
      "v_fmac_f32 v219, v190, v190\n"
      "v_add_f32 v218, v218, v188\n"
      "v_mfma_f32_16x16x32_bf16 v[180:183], a[204:207], v[160:163], v[180:183]\n"
      "v_add_f32 v218, v218, v189\n"
      "v_add_f32 v218, v218, v190\n"
      "v_mfma_f32_16x16x32_bf16 v[180:183], a[208:211], v[164:167], v[180:183]\n"
      "s_waitcnt lgkmcnt(0)\n"
      "v_cvt_pk_bf16_f32 v210, v176, v177\n"
      "v_cmp_gt_i32 vcc, v21, v22\n"
      "v_mfma_f32_16x16x32_bf16 v[180:183], a[212:215], v[168:171], v[180:183]\n"
      "v_add_f32 v218, v218, v191\n"
      "v_fmac_f32 v219, v191, v191\n"
      "v_mfma_f32_16x16x32_bf16 v[184:187], a[216:219], v[100:103], v[184:187]\n"
      "buffer_store_dwordx4 v[208:211], v15, s[8:11], s38 offen sc1\n"
      "v_mfma_f32_16x16x32_bf16 v[184:187], a[220:223], v[104:107], v[184:187]\n"
      "s_cbranch_vccnz .Lsp_wy0_9\n"
      ".Lbk_wy0_9:\n"
      "v_mfma_f32_16x16x32_bf16 v[184:187], a[224:227], v[108:111], v[184:187]\n"
      "s_waitcnt vmcnt(12)\n"
      "ds_read_b128 v[104:107], v10 offset:1024\n"
      "v_lshlrev_b32 v190, 16, v201\n"
      "v_mfma_f32_16x16x32_bf16 v[184:187], a[228:231], v[112:115], v[184:187]\n"
      "s_waitcnt vmcnt(11)\n"
      "v_lshlrev_b32 v176, 16, v194\n"
      "v_mfma_f32_16x16x32_bf16 v[184:187], a[232:235], v[116:119], v[184:187]\n"
      "ds_read_b128 v[100:103], v10 offset:0\n"
      "v_and_b32 v191, 0xffff0000, v201\n"
      "v_mfma_f32_16x16x32_bf16 v[184:187], a[236:239], v[120:123], v[184:187]\n"
      "ds_read_b128 v[120:123], v10 offset:5120\n"
      "v_lshlrev_b32 v174, 16, v193\n"
      "v_mfma_f32_16x16x32_bf16 v[184:187], a[240:243], v[124:127], v[184:187]\n"
      "v_add_f32 v218, v218, v180\n"
      "v_lshlrev_b32 v178, 16, v195\n"
      "v_mfma_f32_16x16x32_bf16 v[184:187], a[244:247], v[128:131], v[184:187]\n"
      "ds_read_b128 v[112:115], v10 offset:3072\n"
      "v_mfma_f32_16x16x32_bf16 v[184:187], a[248:251], v[132:135], v[184:187]\n"
      "ds_read_b128 v[108:111], v10 offset:2048\n"
      "v_cvt_pk_bf16_f32 v216, v188, v189\n"
      "v_mfma_f32_16x16x32_bf16 v[184:187], a[252:255], v[136:139], v[184:187]\n"
      "ds_read_b128 v[124:127], v10 offset:6144\n"
      "v_add_f32 v218, v218, v181\n"
      "v_mfma_f32_16x16x32_bf16 v[184:187], v[32:35], v[140:143], v[184:187]\n"
      "ds_read_b128 v[116:119], v10 offset:4096\n"
      "v_and_b32 v189, 0xffff0000, v200\n"
      "v_mfma_f32_16x16x32_bf16 v[184:187], v[36:39], v[144:147], v[184:187]\n"
      "ds_read_b128 v[140:143], v10 offset:9216\n"
      "s_mov_b32 s46, s36\n"
      "v_mfma_f32_16x16x32_bf16 v[184:187], v[40:43], v[148:151], v[184:187]\n"
      "ds_read2_b32 v[132:133], v6 offset0:0 offset1:32\n"
      "v_and_b32 v179, 0xffff0000, v195\n"
      "v_mfma_f32_16x16x32_bf16 v[184:187], v[44:47], v[152:155], v[184:187]\n"
      "v_add_f32 v218, v218, v182\n"
      "v_mfma_f32_16x16x32_bf16 v[184:187], v[48:51], v[156:159], v[184:187]\n"
      "ds_read_b128 v[128:131], v10 offset:7168\n"
      "v_fmac_f32 v219, v180, v180\n"
      "v_mfma_f32_16x16x32_bf16 v[184:187], v[52:55], v[160:163], v[184:187]\n"
      "ds_read_b128 v[136:139], v10 offset:8192\n"
      "v_lshlrev_b32 v188, 16, v200\n"
      "v_mfma_f32_16x16x32_bf16 v[184:187], v[56:59], v[164:167], v[184:187]\n"
      "s_waitcnt lgkmcnt(5)\n"
      "v_fmac_f32 v219, v181, v181\n"
      "v_and_b32 v175, 0xffff0000, v193\n"
      "v_mfma_f32_16x16x32_bf16 v[184:187], v[60:63], v[168:171], v[184:187]\n"
      "v_mfma_f32_16x16x32_bf16 v[188:191], v[64:67], v[100:103], v[188:191]\n"
      "ds_read2_b32 v[134:135], v6 offset0:64 offset1:96\n"
      "v_and_b32 v173, 0xffff0000, v192\n"
      "v_mfma_f32_16x16x32_bf16 v[188:191], v[68:71], v[104:107], v[188:191]\n"
      "s_add_u32 s36, s36, 24576\n"
      "ds_read_b128 v[156:159], v10 offset:13312\n"
      "v_add_f32 v218, v218, v183\n"
      "v_mfma_f32_16x16x32_bf16 v[188:191], v[72:75], v[108:111], v[188:191]\n"
      "v_fmac_f32 v219, v182, v182\n"
      "v_fmac_f32 v219, v183, v183\n"
      "v_mfma_f32_16x16x32_bf16 v[188:191], v[76:79], v[112:115], v[188:191]\n"
      "s_waitcnt lgkmcnt(3)\n"
      "v_add_f32 v218, v218, v184\n"
      "v_fmac_f32 v219, v184, v184\n"
      "v_mfma_f32_16x16x32_bf16 v[188:191], v[80:83], v[116:119], v[188:191]\n"
      "buffer_load_dwordx2 v[200:201], v14, s[8:11], s47 offen sc1\n"
      "v_mfma_f32_16x16x32_bf16 v[188:191], v[84:87], v[120:123], v[188:191]\n"
      "ds_read_b128 v[148:151], v10 offset:11264\n"
      "v_and_b32 v177, 0xffff0000, v194\n"
      "v_mfma_f32_16x16x32_bf16 v[188:191], v[88:91], v[124:127], v[188:191]\n"
      "ds_read_b128 v[160:163], v10 offset:14336\n"
      "v_cvt_pk_bf16_f32 v215, v186, v187\n"
      "v_mfma_f32_16x16x32_bf16 v[188:191], v[92:95], v[128:131], v[188:191]\n"
      "s_waitcnt lgkmcnt(3)\n"
      "v_fmac_f32 v219, v185, v185\n"
      "v_lshlrev_b32 v172, 16, v192\n"
      "v_mfma_f32_16x16x32_bf16 v[188:191], v[96:99], v[132:135], v[188:191]\n"
      "buffer_load_dwordx4 v[192:195], v13, s[8:11], s47 offen sc1\n"
      "v_mfma_f32_16x16x32_bf16 v[172:175], a[0:3], v[100:103], v[172:175]\n"
      "v_fmac_f32 v219, v186, v186\n"
      "v_fmac_f32 v219, v187, v187\n"
      "v_mfma_f32_16x16x32_bf16 v[172:175], a[4:7], v[104:107], v[172:175]\n"
      "ds_read2_b32 v[170:171], v7 offset0:64 offset1:96\n"
      "v_add_u32 v21, 1, v21\n"
      "v_mfma_f32_16x16x32_bf16 v[172:175], a[8:11], v[108:111], v[172:175]\n"
      "ds_read_b128 v[144:147], v10 offset:10240\n"
      "v_add_f32 v218, v218, v185\n"
      "v_mfma_f32_16x16x32_bf16 v[172:175], a[12:15], v[112:115], v[172:175]\n"
      "ds_read2_b32 v[168:169], v7 offset0:0 offset1:32\n"
      "v_mfma_f32_16x16x32_bf16 v[172:175], a[16:19], v[116:119], v[172:175]\n"
      "s_add_u32 m0, s39, 0\n"
      "ds_read_b128 v[164:167], v10 offset:15360\n"
      "v_add_f32 v218, v218, v186\n"
      "v_mfma_f32_16x16x32_bf16 v[172:175], a[20:23], v[120:123], v[172:175]\n"
      "ds_read_b128 v[152:155], v10 offset:12288\n"
      "v_mfma_f32_16x16x32_bf16 v[172:175], a[24:27], v[124:127], v[172:175]\n"
      "v_mfma_f32_16x16x32_bf16 v[172:175], a[28:31], v[128:131], v[172:175]\n"
      "v_cvt_pk_bf16_f32 v213, v182, v183\n"
      "v_add_f32 v218, v218, v187\n"
      "v_mfma_f32_16x16x32_bf16 v[172:175], a[32:35], v[132:135], v[172:175]\n"
      "ds_write_b64 v18, v[218:219]\n"
      "v_mfma_f32_16x16x32_bf16 v[172:175], a[36:39], v[136:139], v[172:175]\n"
      "v_mfma_f32_16x16x32_bf16 v[172:175], a[40:43], v[140:143], v[172:175]\n"
      "s_waitcnt lgkmcnt(3)\n"
      "buffer_store_dwordx2 v[216:217], v16, s[8:11], s38 offen sc1\n"
      "v_mfma_f32_16x16x32_bf16 v[172:175], a[44:47], v[144:147], v[172:175]\n"
      "ds_write_b32 v23, v21\n"
      "v_mfma_f32_16x16x32_bf16 v[172:175], a[48:51], v[148:151], v[172:175]\n"
      "s_waitcnt lgkmcnt(2)\n"
      "v_cvt_pk_bf16_f32 v214, v184, v185\n"
      "v_mfma_f32_16x16x32_bf16 v[172:175], a[52:55], v[152:155], v[172:175]\n"
      "v_cvt_pk_bf16_f32 v212, v180, v181\n"
      "v_mfma_f32_16x16x32_bf16 v[172:175], a[56:59], v[156:159], v[172:175]\n"
      "buffer_store_dwordx4 v[212:215], v15, s[8:11], s38 offen offset:1024 sc1\n"
      "v_mfma_f32_16x16x32_bf16 v[172:175], a[60:63], v[160:163], v[172:175]\n"
      "v_mfma_f32_16x16x32_bf16 v[172:175], a[64:67], v[164:167], v[172:175]\n"
      "s_waitcnt vmcnt(10) lgkmcnt(1)\n"
      "v_cmp_ne_u32 vcc, s21, v28\n"
      "v_mfma_f32_16x16x32_bf16 v[172:175], a[68:71], v[168:171], v[172:175]\n"
      "v_mfma_f32_16x16x32_bf16 v[176:179], a[72:75], v[100:103], v[176:179]\n"
      "v_mfma_f32_16x16x32_bf16 v[176:179], a[76:79], v[104:107], v[176:179]\n"
      "s_min_u32 s36, s36, s6\n"
      "s_cbranch_vccnz .Lsp_w0_10\n"
      ".Lbk_w0_10:\n"
      "v_mfma_f32_16x16x32_bf16 v[176:179], a[80:83], v[108:111], v[176:179]\n"
      "buffer_load_dword v28, v11, s[8:11], s36 offen sc1\n"
      "v_mfma_f32_16x16x32_bf16 v[176:179], a[84:87], v[112:115], v[176:179]\n"
      "ds_read_b32 v22, v24\n"
      "v_mfma_f32_16x16x32_bf16 v[176:179], a[88:91], v[116:119], v[176:179]\n"
      "v_cvt_pk_bf16_f32 v208, v172, v173\n"
      "v_mfma_f32_16x16x32_bf16 v[176:179], a[92:95], v[120:123], v[176:179]\n"
      "buffer_load_dwordx4 v0, s[8:11], s46 offen offset:0 sc1 lds\n"
      "v_mfma_f32_16x16x32_bf16 v[176:179], a[96:99], v[124:127], v[176:179]\n"
      "v_add_f32 v218, v172, v173\n"
      "v_mfma_f32_16x16x32_bf16 v[176:179], a[100:103], v[128:131], v[176:179]\n"
      "s_waitcnt vmcnt(7) lgkmcnt(1)\n"
      "v_and_b32 v181, 0xffff0000, v196\n"
      "v_mul_f32 v219, v172, v172\n"
      "v_mfma_f32_16x16x32_bf16 v[176:179], a[104:107], v[132:135], v[176:179]\n"
      "s_waitcnt lgkmcnt(0)\n"
      "v_cmp_gt_i32 vcc, v21, v22\n"
      "v_fmac_f32 v219, v173, v173\n"
      "v_mfma_f32_16x16x32_bf16 v[176:179], a[108:111], v[136:139], v[176:179]\n"
      "buffer_store_dword v25, v12, s[8:11], s50 offen sc1\n"
      "v_mfma_f32_16x16x32_bf16 v[176:179], a[112:115], v[140:143], v[176:179]\n"
      "v_lshlrev_b32_e64 v180, 16, v196\n"
      "v_and_b32 v183, 0xffff0000, v197\n"
      "v_mfma_f32_16x16x32_bf16 v[176:179], a[116:119], v[144:147], v[176:179]\n"
      "s_cbranch_vccnz .Lsp_wz0_11\n"
      ".Lbk_wz0_11:\n"
      "v_add_f32 v218, v218, v174\n"
      "v_mfma_f32_16x16x32_bf16 v[176:179], a[120:123], v[148:151], v[176:179]\n"
      "ds_read_b128 v[204:207], v17\n"
      "v_add_u32 v21, 1, v21\n"
      "v_mfma_f32_16x16x32_bf16 v[176:179], a[124:127], v[152:155], v[176:179]\n"
      "buffer_load_dwordx4 v0, s[8:11], s46 offen offset:1024 sc1 lds\n"
      "v_mfma_f32_16x16x32_bf16 v[176:179], a[128:131], v[156:159], v[176:179]\n"
      "v_lshlrev_b32 v182, 16, v197\n"
      "v_mfma_f32_16x16x32_bf16 v[176:179], a[132:135], v[160:163], v[176:179]\n"
      "ds_write_b32 v23, v21\n"
      "v_mfma_f32_16x16x32_bf16 v[176:179], a[136:139], v[164:167], v[176:179]\n"
      "s_add_u32 s50, s50, 24576\n"
      "v_fmac_f32 v219, v174, v174\n"
      "v_add_f32 v218, v218, v175\n"
      "v_mfma_f32_16x16x32_bf16 v[176:179], a[140:143], v[168:171], v[176:179]\n"
      "buffer_load_dwordx4 v0, s[8:11], s46 offen offset:3072 sc1 lds\n"
      "v_mfma_f32_16x16x32_bf16 v[180:183], a[144:147], v[100:103], v[180:183]\n"
      "v_and_b32 v185, 0xffff0000, v198\n"
      "v_mfma_f32_16x16x32_bf16 v[180:183], a[148:151], v[104:107], v[180:183]\n"
      "s_waitcnt lgkmcnt(1)\n"
      "v_cvt_pk_bf16_f32 v209, v174, v175\n"
      "v_add_f32 v190, v190, v206\n"
      "v_mfma_f32_16x16x32_bf16 v[180:183], a[152:155], v[108:111], v[180:183]\n"
      "v_add_f32 v189, v189, v205\n"
      "v_lshlrev_b32 v184, 16, v198\n"
      "v_mfma_f32_16x16x32_bf16 v[180:183], a[156:159], v[112:115], v[180:183]\n"
      "buffer_load_dwordx4 v0, s[8:11], s46 offen offset:2048 sc1 lds\n"
      "v_mfma_f32_16x16x32_bf16 v[180:183], a[160:163], v[116:119], v[180:183]\n"
      "v_cvt_pk_bf16_f32 v211, v178, v179\n"
      "v_add_f32 v191, v191, v207\n"
      "v_mfma_f32_16x16x32_bf16 v[180:183], a[164:167], v[120:123], v[180:183]\n"
      "v_and_b32 v187, 0xffff0000, v199\n"
      "s_add_u32 m0, s40, 0\n"
      "v_mfma_f32_16x16x32_bf16 v[180:183], a[168:171], v[124:127], v[180:183]\n"
      "v_cvt_pk_bf16_f32 v217, v190, v191\n"
      "v_add_f32 v188, v188, v204\n"
      "v_mfma_f32_16x16x32_bf16 v[180:183], a[172:175], v[128:131], v[180:183]\n"
      "buffer_load_dwordx4 v1, s[8:11], s46 offen offset:0 sc1 lds\n"
      "v_mfma_f32_16x16x32_bf16 v[180:183], a[176:179], v[132:135], v[180:183]\n"
      "ds_read_b32 v22, v24\n"
      "v_lshlrev_b32 v186, 16, v199\n"
      "v_mfma_f32_16x16x32_bf16 v[180:183], a[180:183], v[136:139], v[180:183]\n"
      "v_fmac_f32 v219, v175, v175\n"
      "v_add_f32 v218, v218, v176\n"
      "v_mfma_f32_16x16x32_bf16 v[180:183], a[184:187], v[140:143], v[180:183]\n"
      "v_fmac_f32 v219, v176, v176\n"
      "v_fmac_f32 v219, v177, v177\n"
      "v_mfma_f32_16x16x32_bf16 v[180:183], a[188:191], v[144:147], v[180:183]\n"
      "v_fmac_f32 v219, v178, v178\n"
      "v_fmac_f32 v219, v179, v179\n"
      "v_mfma_f32_16x16x32_bf16 v[180:183], a[192:195], v[148:151], v[180:183]\n"
      "v_fmac_f32 v219, v188, v188\n"
      "v_fmac_f32 v219, v189, v189\n"
      "v_mfma_f32_16x16x32_bf16 v[180:183], a[196:199], v[152:155], v[180:183]\n"
      "buffer_load_dwordx4 v[196:199], v13, s[8:11], s47 offen offset:1024 sc1\n"
      "v_mfma_f32_16x16x32_bf16 v[180:183], a[200:203], v[156:159], v[180:183]\n"
      "v_add_f32 v218, v218, v177\n"
      "v_mfma_f32_16x16x32_bf16 v[180:183], a[204:207], v[160:163], v[180:183]\n"
      "v_add_f32 v218, v218, v178\n"
      "v_add_f32 v218, v218, v179\n"
      "v_mfma_f32_16x16x32_bf16 v[180:183], a[208:211], v[164:167], v[180:183]\n"
      "s_waitcnt lgkmcnt(0)\n"
      "v_add_f32 v218, v218, v188\n"
      "v_cmp_gt_i32 vcc, v21, v22\n"
      "v_mfma_f32_16x16x32_bf16 v[180:183], a[212:215], v[168:171], v[180:183]\n"
      "v_add_f32 v218, v218, v189\n"
      "v_fmac_f32 v219, v190, v190\n"
      "v_mfma_f32_16x16x32_bf16 v[184:187], a[216:219], v[100:103], v[184:187]\n"
      "v_cvt_pk_bf16_f32 v216, v188, v189\n"
      "v_add_f32 v218, v218, v190\n"
      "v_mfma_f32_16x16x32_bf16 v[184:187], a[220:223], v[104:107], v[184:187]\n"
      "s_cbranch_vccnz .Lsp_wy1_12\n"
      ".Lbk_wy1_12:\n"
      "v_add_f32 v218, v218, v191\n"
      "v_mfma_f32_16x16x32_bf16 v[184:187], a[224:227], v[108:111], v[184:187]\n"
      "s_waitcnt vmcnt(11)\n"
      "ds_read_b128 v[108:111], v10 offset:22528\n"
      "v_lshlrev_b32 v190, 16, v201\n"
      "v_mfma_f32_16x16x32_bf16 v[184:187], a[228:231], v[112:115], v[184:187]\n"
      ".Lw_p1:\n"
      "s_cbranch_execz .Lw_pr\n"
      "s_add_u32 s38, s38, 24576\n"
      "v_lshlrev_b32 v188, 16, v200\n"
      "v_mfma_f32_16x16x32_bf16 v[184:187], a[232:235], v[116:119], v[184:187]\n"
      "ds_read_b128 v[100:103], v10 offset:20480\n"
      "v_mfma_f32_16x16x32_bf16 v[184:187], a[236:239], v[120:123], v[184:187]\n"
      "ds_read_b128 v[116:119], v10 offset:24576\n"
      "v_cvt_pk_bf16_f32 v210, v176, v177\n"
      "v_mfma_f32_16x16x32_bf16 v[184:187], a[240:243], v[124:127], v[184:187]\n"
      "ds_read_b128 v[104:107], v10 offset:21504\n"
      "v_fmac_f32 v219, v191, v191\n"
      "v_mfma_f32_16x16x32_bf16 v[184:187], a[244:247], v[128:131], v[184:187]\n"
      "v_and_b32 v189, 0xffff0000, v200\n"
      "s_sub_u32 s47, s46, s45\n"
      "v_mfma_f32_16x16x32_bf16 v[184:187], a[248:251], v[132:135], v[184:187]\n"
      "v_mfma_f32_16x16x32_bf16 v[184:187], a[252:255], v[136:139], v[184:187]\n"
      "s_waitcnt vmcnt(10) lgkmcnt(3)\n"
      "ds_read_b128 v[112:115], v10 offset:23552\n"
      "v_lshlrev_b32 v178, 16, v195\n"
      "v_mfma_f32_16x16x32_bf16 v[184:187], v[32:35], v[140:143], v[184:187]\n"
      "ds_read_b128 v[140:143], v10 offset:29696\n"
      "v_and_b32 v173, 0xffff0000, v192\n"
      "v_mfma_f32_16x16x32_bf16 v[184:187], v[36:39], v[144:147], v[184:187]\n"
      "buffer_store_dwordx4 v[208:211], v15, s[8:11], s38 offen sc1\n"
      "v_mfma_f32_16x16x32_bf16 v[184:187], v[40:43], v[148:151], v[184:187]\n"
      "ds_read_b128 v[120:123], v10 offset:25600\n"
      "v_fmac_f32 v219, v180, v180\n"
      "v_mfma_f32_16x16x32_bf16 v[184:187], v[44:47], v[152:155], v[184:187]\n"
      "ds_read2_b32 v[134:135], v8 offset0:64 offset1:96\n"
      "v_and_b32 v191, 0xffff0000, v201\n"
      "v_mfma_f32_16x16x32_bf16 v[184:187], v[48:51], v[156:159], v[184:187]\n"
      "ds_read2_b32 v[132:133], v8 offset0:0 offset1:32\n"
      "v_lshlrev_b32 v174, 16, v193\n"
      "v_mfma_f32_16x16x32_bf16 v[184:187], v[52:55], v[160:163], v[184:187]\n"
      "ds_read_b128 v[156:159], v10 offset:33792\n"
      "v_fmac_f32 v219, v181, v181\n"
      "v_mfma_f32_16x16x32_bf16 v[184:187], v[56:59], v[164:167], v[184:187]\n"
      "ds_read_b128 v[124:127], v10 offset:26624\n"
      "v_add_f32 v218, v218, v180\n"
      "v_mfma_f32_16x16x32_bf16 v[184:187], v[60:63], v[168:171], v[184:187]\n"
      "s_waitcnt lgkmcnt(5)\n"
      "ds_read_b128 v[128:131], v10 offset:27648\n"
      "v_lshlrev_b32 v172, 16, v192\n"
      "v_mfma_f32_16x16x32_bf16 v[188:191], v[64:67], v[100:103], v[188:191]\n"
      "v_lshlrev_b32 v176, 16, v194\n"
      "v_and_b32 v177, 0xffff0000, v194\n"
      "v_mfma_f32_16x16x32_bf16 v[188:191], v[68:71], v[104:107], v[188:191]\n"
      "buffer_load_dwordx2 v[200:201], v14, s[8:11], s47 offen sc1\n"
      "v_mfma_f32_16x16x32_bf16 v[188:191], v[72:75], v[108:111], v[188:191]\n"
      "v_and_b32 v175, 0xffff0000, v193\n"
      "v_and_b32 v179, 0xffff0000, v195\n"
      "v_mfma_f32_16x16x32_bf16 v[188:191], v[76:79], v[112:115], v[188:191]\n"
      "ds_read_b128 v[144:147], v10 offset:30720\n"
      "v_add_f32 v218, v218, v181\n"
      "v_mfma_f32_16x16x32_bf16 v[188:191], v[80:83], v[116:119], v[188:191]\n"
      "s_waitcnt lgkmcnt(3)\n"
      "ds_read_b128 v[136:139], v10 offset:28672\n"
      "v_fmac_f32 v219, v182, v182\n"
      "v_mfma_f32_16x16x32_bf16 v[188:191], v[84:87], v[120:123], v[188:191]\n"
      "s_waitcnt lgkmcnt(3)\n"
      "v_fmac_f32 v219, v183, v183\n"
      "v_fmac_f32 v219, v184, v184\n"
      "v_mfma_f32_16x16x32_bf16 v[188:191], v[88:91], v[124:127], v[188:191]\n"
      "s_waitcnt lgkmcnt(2)\n"
      "buffer_load_dwordx4 v[192:195], v13, s[8:11], s47 offen sc1\n"
      "v_mfma_f32_16x16x32_bf16 v[188:191], v[92:95], v[128:131], v[188:191]\n"
      "ds_read2_b32 v[170:171], v9 offset0:64 offset1:96\n"
      "v_add_f32 v218, v218, v182\n"
      "v_mfma_f32_16x16x32_bf16 v[188:191], v[96:99], v[132:135], v[188:191]\n"
      "v_add_f32 v218, v218, v183\n"
      "v_mfma_f32_16x16x32_bf16 v[172:175], a[0:3], v[100:103], v[172:175]\n"
      "ds_read_b128 v[160:163], v10 offset:34816\n"
      "v_add_u32 v21, 1, v21\n"
      "v_mfma_f32_16x16x32_bf16 v[172:175], a[4:7], v[104:107], v[172:175]\n"
      "ds_read_b128 v[148:151], v10 offset:31744\n"
      "v_add_f32 v218, v218, v184\n"
      "v_mfma_f32_16x16x32_bf16 v[172:175], a[8:11], v[108:111], v[172:175]\n"
      "ds_read2_b32 v[168:169], v9 offset0:0 offset1:32\n"
      "s_mov_b32 s46, s36\n"
      "v_mfma_f32_16x16x32_bf16 v[172:175], a[12:15], v[112:115], v[172:175]\n"
      "ds_read_b128 v[152:155], v10 offset:32768\n"
      "v_add_f32 v218, v218, v185\n"
      "v_mfma_f32_16x16x32_bf16 v[172:175], a[16:19], v[116:119], v[172:175]\n"
      "buffer_store_dwordx2 v[216:217], v16, s[8:11], s38 offen sc1\n"
      "v_mfma_f32_16x16x32_bf16 v[172:175], a[20:23], v[120:123], v[172:175]\n"
      "ds_read_b128 v[164:167], v10 offset:35840\n"
      "v_add_f32 v218, v218, v186\n"
      "v_mfma_f32_16x16x32_bf16 v[172:175], a[24:27], v[124:127], v[172:175]\n"
      "v_fmac_f32 v219, v185, v185\n"
      "v_fmac_f32 v219, v186, v186\n"
      "v_mfma_f32_16x16x32_bf16 v[172:175], a[28:31], v[128:131], v[172:175]\n"
      "v_add_f32 v218, v218, v187\n"
      "v_fmac_f32 v219, v187, v187\n"
      "v_mfma_f32_16x16x32_bf16 v[172:175], a[32:35], v[132:135], v[172:175]\n"
      "s_waitcnt lgkmcnt(3)\n"
      "ds_write_b64 v18, v[218:219]\n"
      "v_mfma_f32_16x16x32_bf16 v[172:175], a[36:39], v[136:139], v[172:175]\n"
      "s_waitcnt vmcnt(12) lgkmcnt(3)\n"
      "buffer_store_dword v25, v12, s[8:11], s50 offen sc1\n"
      "v_mfma_f32_16x16x32_bf16 v[172:175], a[40:43], v[140:143], v[172:175]\n"
      "v_cvt_pk_bf16_f32 v213, v182, v183\n"
      "v_cvt_pk_bf16_f32 v215, v186, v187\n"
      "v_mfma_f32_16x16x32_bf16 v[172:175], a[44:47], v[144:147], v[172:175]\n"
      "ds_write_b32 v23, v21\n"
      "v_mfma_f32_16x16x32_bf16 v[172:175], a[48:51], v[148:151], v[172:175]\n"
      "s_waitcnt lgkmcnt(2)\n"
      "v_cvt_pk_bf16_f32 v212, v180, v181\n"
      "v_cvt_pk_bf16_f32 v214, v184, v185\n"
      "v_mfma_f32_16x16x32_bf16 v[172:175], a[52:55], v[152:155], v[172:175]\n"
      "buffer_store_dwordx4 v[212:215], v15, s[8:11], s38 offen offset:1024 sc1\n"
      "v_mfma_f32_16x16x32_bf16 v[172:175], a[56:59], v[156:159], v[172:175]\n"
      "s_add_u32 m0, s39, 20480\n"
      "s_add_u32 s38, s38, 24576\n"
      "v_mfma_f32_16x16x32_bf16 v[172:175], a[60:63], v[160:163], v[172:175]\n"
      "s_waitcnt vmcnt(13) lgkmcnt(1)\n"
      "v_cmp_ne_u32 vcc, s21, v28\n"
      "v_mfma_f32_16x16x32_bf16 v[172:175], a[64:67], v[164:167], v[172:175]\n"
      "v_mfma_f32_16x16x32_bf16 v[172:175], a[68:71], v[168:171], v[172:175]\n"
      "v_mfma_f32_16x16x32_bf16 v[176:179], a[72:75], v[100:103], v[176:179]\n"
      "s_cmp_eq_u32 s36, s37\n"
      "s_cselect_b32 s48, 49152, s51\n"
      "v_mfma_f32_16x16x32_bf16 v[176:179], a[76:79], v[104:107], v[176:179]\n"
      "s_cbranch_vccnz .Lsp_w1_13\n"
      ".Lbk_w1_13:\n"
      "v_mfma_f32_16x16x32_bf16 v[176:179], a[80:83], v[108:111], v[176:179]\n"
      "buffer_load_dwordx4 v0, s[8:11], s46 offen offset:0 sc1 lds\n"
      "v_mfma_f32_16x16x32_bf16 v[176:179], a[84:87], v[112:115], v[176:179]\n"
      "ds_read_b32 v22, v24\n"
      "v_cvt_pk_bf16_f32 v208, v172, v173\n"
      "v_mfma_f32_16x16x32_bf16 v[176:179], a[88:91], v[116:119], v[176:179]\n"
      "v_add_f32 v218, v172, v173\n"
      "v_mul_f32 v219, v172, v172\n"
      "v_mfma_f32_16x16x32_bf16 v[176:179], a[92:95], v[120:123], v[176:179]\n"
      "s_add_u32 s36, s36, s48\n"
      "v_add_f32 v218, v218, v174\n"
      "v_mfma_f32_16x16x32_bf16 v[176:179], a[96:99], v[124:127], v[176:179]\n"
      "s_min_u32 s36, s36, s6\n"
      "v_fmac_f32 v219, v173, v173\n"
      "v_mfma_f32_16x16x32_bf16 v[176:179], a[100:103], v[128:131], v[176:179]\n"
      "buffer_load_dword v28, v11, s[8:11], s36 offen sc1\n"
      "v_mfma_f32_16x16x32_bf16 v[176:179], a[104:107], v[132:135], v[176:179]\n"
      "s_waitcnt lgkmcnt(0)\n"
      "v_cmp_gt_i32 vcc, v21, v22\n"
      "v_fmac_f32 v219, v174, v174\n"
      "v_mfma_f32_16x16x32_bf16 v[176:179], a[108:111], v[136:139], v[176:179]\n"
      "buffer_load_dwordx4 v0, s[8:11], s46 offen offset:1024 sc1 lds\n"
      "v_mfma_f32_16x16x32_bf16 v[176:179], a[112:115], v[140:143], v[176:179]\n"
      "s_waitcnt vmcnt(9)\n"
      "v_lshlrev_b32 v182, 16, v197\n"
      "v_and_b32 v185, 0xffff0000, v198\n"
      "v_mfma_f32_16x16x32_bf16 v[176:179], a[116:119], v[144:147], v[176:179]\n"
      "v_lshlrev_b32 v180, 16, v196\n"
      "v_and_b32 v183, 0xffff0000, v197\n"
      "v_mfma_f32_16x16x32_bf16 v[176:179], a[120:123], v[148:151], v[176:179]\n"
      "s_cbranch_vccnz .Lsp_wz1_14\n"
      ".Lbk_wz1_14:\n"
      "ds_read_b128 v[204:207], v17\n"
      "v_mfma_f32_16x16x32_bf16 v[176:179], a[124:127], v[152:155], v[176:179]\n"
      "buffer_load_dwordx4 v0, s[8:11], s46 offen offset:3072 sc1 lds\n"
      "v_mfma_f32_16x16x32_bf16 v[176:179], a[128:131], v[156:159], v[176:179]\n"
      "v_add_f32 v218, v218, v175\n"
      "v_mfma_f32_16x16x32_bf16 v[176:179], a[132:135], v[160:163], v[176:179]\n"
      "v_add_u32 v21, 1, v21\n"
      "v_lshlrev_b32 v184, 16, v198\n"
      "v_mfma_f32_16x16x32_bf16 v[176:179], a[136:139], v[164:167], v[176:179]\n"
      "v_and_b32 v181, 0xffff0000, v196\n"
      "v_cvt_pk_bf16_f32 v209, v174, v175\n"
      "v_mfma_f32_16x16x32_bf16 v[176:179], a[140:143], v[168:171], v[176:179]\n"
      "ds_write_b32 v23, v21\n"
      "v_mfma_f32_16x16x32_bf16 v[180:183], a[144:147], v[100:103], v[180:183]\n"
      "buffer_load_dwordx4 v0, s[8:11], s46 offen offset:2048 sc1 lds\n"
      "v_mfma_f32_16x16x32_bf16 v[180:183], a[148:151], v[104:107], v[180:183]\n"
      "s_waitcnt lgkmcnt(1)\n"
      "v_add_f32 v188, v188, v204\n"
      "v_add_f32 v190, v190, v206\n"
      "v_mfma_f32_16x16x32_bf16 v[180:183], a[152:155], v[108:111], v[180:183]\n"
      "v_add_f32 v191, v191, v207\n"
      "v_lshlrev_b32 v186, 16, v199\n"
      "v_mfma_f32_16x16x32_bf16 v[180:183], a[156:159], v[112:115], v[180:183]\n"
      "v_add_f32 v218, v218, v176\n"
      "v_fmac_f32 v219, v175, v175\n"
      "v_mfma_f32_16x16x32_bf16 v[180:183], a[160:163], v[116:119], v[180:183]\n"
      "s_add_u32 m0, s40, 20480\n"
      "v_cvt_pk_bf16_f32 v217, v190, v191\n"
      "v_add_f32 v218, v218, v177\n"
      "v_mfma_f32_16x16x32_bf16 v[180:183], a[164:167], v[120:123], v[180:183]\n"
      "s_add_u32 s50, s50, 24576\n"
      "v_cvt_pk_bf16_f32 v211, v178, v179\n"
      "v_and_b32 v187, 0xffff0000, v199\n"
      "v_mfma_f32_16x16x32_bf16 v[180:183], a[168:171], v[124:127], v[180:183]\n"
      "buffer_load_dwordx4 v1, s[8:11], s46 offen offset:0 sc1 lds\n"
      "v_mfma_f32_16x16x32_bf16 v[180:183], a[172:175], v[128:131], v[180:183]\n"
      "v_add_f32 v218, v218, v178\n"
      "v_add_f32 v189, v189, v205\n"
      "v_mfma_f32_16x16x32_bf16 v[180:183], a[176:179], v[132:135], v[180:183]\n"
      "v_fmac_f32 v219, v176, v176\n"
      "v_mfma_f32_16x16x32_bf16 v[180:183], a[180:183], v[136:139], v[180:183]\n"
      "ds_read_b32 v22, v24\n"
      "v_mfma_f32_16x16x32_bf16 v[180:183], a[184:187], v[140:143], v[180:183]\n"
      "buffer_load_dwordx4 v[196:199], v13, s[8:11], s47 offen offset:1024 sc1\n"
      "v_mfma_f32_16x16x32_bf16 v[180:183], a[188:191], v[144:147], v[180:183]\n"
      "v_fmac_f32 v219, v177, v177\n"
      "v_add_f32 v218, v218, v179\n"
      "v_mfma_f32_16x16x32_bf16 v[180:183], a[192:195], v[148:151], v[180:183]\n"
      "v_fmac_f32 v219, v178, v178\n"
      "v_fmac_f32 v219, v179, v179\n"
      "v_mfma_f32_16x16x32_bf16 v[180:183], a[196:199], v[152:155], v[180:183]\n"
      "v_fmac_f32 v219, v188, v188\n"
      "v_fmac_f32 v219, v189, v189\n"
      "v_mfma_f32_16x16x32_bf16 v[180:183], a[200:203], v[156:159], v[180:183]\n"
      "s_sub_u32 s47, s46, s45\n"
      "v_fmac_f32 v219, v190, v190\n"
      "v_add_f32 v218, v218, v188\n"
      "v_mfma_f32_16x16x32_bf16 v[180:183], a[204:207], v[160:163], v[180:183]\n"
      "v_add_f32 v218, v218, v189\n"
      "v_add_f32 v218, v218, v190\n"
      "v_mfma_f32_16x16x32_bf16 v[180:183], a[208:211], v[164:167], v[180:183]\n"
      "s_waitcnt lgkmcnt(0)\n"
      "v_cvt_pk_bf16_f32 v210, v176, v177\n"
      "v_cmp_gt_i32 vcc, v21, v22\n"
      "v_mfma_f32_16x16x32_bf16 v[180:183], a[212:215], v[168:171], v[180:183]\n"
      "v_add_f32 v218, v218, v191\n"
      "v_fmac_f32 v219, v191, v191\n"
      "v_mfma_f32_16x16x32_bf16 v[184:187], a[216:219], v[100:103], v[184:187]\n"
      "buffer_store_dwordx4 v[208:211], v15, s[8:11], s38 offen sc1\n"
      "v_mfma_f32_16x16x32_bf16 v[184:187], a[220:223], v[104:107], v[184:187]\n"
      "s_cbranch_vccnz .Lsp_wy0_15\n"
      ".Lbk_wy0_15:\n"
      "v_mfma_f32_16x16x32_bf16 v[184:187], a[224:227], v[108:111], v[184:187]\n"
      "s_waitcnt vmcnt(12)\n"
      "ds_read_b128 v[104:107], v0 offset:1024\n"
      "v_lshlrev_b32 v190, 16, v201\n"
      "v_mfma_f32_16x16x32_bf16 v[184:187], a[228:231], v[112:115], v[184:187]\n"
      "s_waitcnt vmcnt(11)\n"
      "v_lshlrev_b32 v176, 16, v194\n"
      "v_mfma_f32_16x16x32_bf16 v[184:187], a[232:235], v[116:119], v[184:187]\n"
      "ds_read_b128 v[100:103], v0 offset:0\n"
      "v_and_b32 v191, 0xffff0000, v201\n"
      "v_mfma_f32_16x16x32_bf16 v[184:187], a[236:239], v[120:123], v[184:187]\n"
      "ds_read_b128 v[120:123], v0 offset:5120\n"
      "v_lshlrev_b32 v174, 16, v193\n"
      "v_mfma_f32_16x16x32_bf16 v[184:187], a[240:243], v[124:127], v[184:187]\n"
      "v_add_f32 v218, v218, v180\n"
      "v_lshlrev_b32 v178, 16, v195\n"
      "v_mfma_f32_16x16x32_bf16 v[184:187], a[244:247], v[128:131], v[184:187]\n"
      "ds_read_b128 v[112:115], v0 offset:3072\n"
      "v_mfma_f32_16x16x32_bf16 v[184:187], a[248:251], v[132:135], v[184:187]\n"
      "ds_read_b128 v[108:111], v0 offset:2048\n"
      "v_cvt_pk_bf16_f32 v216, v188, v189\n"
      "v_mfma_f32_16x16x32_bf16 v[184:187], a[252:255], v[136:139], v[184:187]\n"
      "ds_read_b128 v[124:127], v0 offset:6144\n"
      "v_add_f32 v218, v218, v181\n"
      "v_mfma_f32_16x16x32_bf16 v[184:187], v[32:35], v[140:143], v[184:187]\n"
      "ds_read_b128 v[116:119], v0 offset:4096\n"
      "v_and_b32 v189, 0xffff0000, v200\n"
      "v_mfma_f32_16x16x32_bf16 v[184:187], v[36:39], v[144:147], v[184:187]\n"
      "ds_read_b128 v[140:143], v0 offset:9216\n"
      "s_mov_b32 s46, s36\n"
      "v_mfma_f32_16x16x32_bf16 v[184:187], v[40:43], v[148:151], v[184:187]\n"
      "ds_read2_b32 v[132:133], v2 offset0:0 offset1:32\n"
      "v_and_b32 v179, 0xffff0000, v195\n"
      "v_mfma_f32_16x16x32_bf16 v[184:187], v[44:47], v[152:155], v[184:187]\n"
      "v_add_f32 v218, v218, v182\n"
      "v_mfma_f32_16x16x32_bf16 v[184:187], v[48:51], v[156:159], v[184:187]\n"
      "ds_read_b128 v[128:131], v0 offset:7168\n"
      "v_fmac_f32 v219, v180, v180\n"
      "v_mfma_f32_16x16x32_bf16 v[184:187], v[52:55], v[160:163], v[184:187]\n"
      "ds_read_b128 v[136:139], v0 offset:8192\n"
      "v_lshlrev_b32 v188, 16, v200\n"
      "v_mfma_f32_16x16x32_bf16 v[184:187], v[56:59], v[164:167], v[184:187]\n"
      "s_waitcnt lgkmcnt(5)\n"
      "v_fmac_f32 v219, v181, v181\n"
      "v_and_b32 v175, 0xffff0000, v193\n"
      "v_mfma_f32_16x16x32_bf16 v[184:187], v[60:63], v[168:171], v[184:187]\n"
      "s_sub_u32 s20, s20, 1\n"
      "s_cbranch_scc0 .Lw_loop\n"
      "v_add_f32 v218, v218, v183\n"
      "v_fmac_f32 v219, v182, v182\n"
      "v_fmac_f32 v219, v183, v183\n"
      "s_nop 5\n"
      "v_add_f32 v218, v218, v184\n"
      "v_fmac_f32 v219, v184, v184\n"
      "v_cvt_pk_bf16_f32 v215, v186, v187\n"
      "v_fmac_f32 v219, v185, v185\n"
      "v_fmac_f32 v219, v186, v186\n"
      "v_fmac_f32 v219, v187, v187\n"
      "v_add_f32 v218, v218, v185\n"
      "v_add_f32 v218, v218, v186\n"
      "v_cvt_pk_bf16_f32 v213, v182, v183\n"
      "v_add_f32 v218, v218, v187\n"
      "ds_write_b64 v18, v[218:219]\n"
      "s_waitcnt lgkmcnt(0)\n"
      "s_barrier\n"
      "buffer_store_dwordx2 v[216:217], v16, s[8:11], s38 offen sc1\n"
      "v_cvt_pk_bf16_f32 v214, v184, v185\n"
      "v_cvt_pk_bf16_f32 v212, v180, v181\n"
      "buffer_store_dwordx4 v[212:215], v15, s[8:11], s38 offen offset:1024 sc1\n"
      "s_waitcnt vmcnt(10)\n"
      "buffer_store_dword v25, v12, s[8:11], s50 offen sc1\n"
      "s_add_u32 s50, s50, 24576\n"
      "s_add_u32 s38, s38, 24576\n"
      "s_waitcnt vmcnt(1)\n"
      "buffer_store_dword v25, v12, s[8:11], s50 offen sc1\n"
      "s_add_u32 s50, s50, 24576\n"
      "s_endpgm\n"
      ".Lv_start:\n"
      "s_mov_b64 exec, 0\n"
      "s_lshr_b32 s22, s39, 13\n"
      ".Lv_pl:\n"
      "s_cmp_eq_u32 s22, 0\n"
      "s_cbranch_scc1 .Lv_p0\n"
      "s_cmp_eq_u32 s22, 1\n"
      "s_cbranch_scc1 .Lv_p1\n"
      "s_branch .Lv_pe\n"
      ".Lv_pr:\n"
      "s_add_u32 s22, s22, 2\n"
      "s_branch .Lv_pl\n"
      ".Lv_pe:\n"
      "s_mov_b64 exec, -1\n"
      "s_add_u32 s50, s38, 4294893568\n"
      "s_add_u32 s49, s38, 4294918144\n"
      "s_add_u32 s38, s38, 4294942720\n"
      "s_cmp_eq_u32 s36, s37\n"
      "s_cselect_b32 s48, 49152, s51\n"
      "s_add_u32 s36, s36, s48\n"
      "s_min_u32 s36, s36, s6\n"
      "buffer_load_dword v28, v11, s[8:11], s36 offen sc1\n"
      "s_mov_b32 s46, s36\n"
      "s_add_u32 m0, s39, 0\n"
      "s_add_u32 s36, s36, 24576\n"
      "s_waitcnt vmcnt(0)\n"
      "v_cmp_ne_u32 vcc, s21, v28\n"
      "s_cbranch_vccnz .Lsp_v0_16\n"
      ".Lbk_v0_16:\n"
      "buffer_load_dwordx4 v0, s[8:11], s46 offen offset:0 sc1 lds\n"
      "s_min_u32 s36, s36, s6\n"
      "buffer_load_dword v28, v11, s[8:11], s36 offen sc1\n"
      "buffer_load_dwordx4 v0, s[8:11], s46 offen offset:2048 sc1 lds\n"
      "buffer_load_dwordx4 v0, s[8:11], s46 offen offset:1024 sc1 lds\n"
      "buffer_load_dwordx4 v0, s[8:11], s46 offen offset:3072 sc1 lds\n"
      "s_add_u32 m0, s40, 0\n"
      "s_nop 0\n"
      "buffer_load_dwordx4 v1, s[8:11], s46 offen offset:0 sc1 lds\n"
      "s_sub_u32 s47, s46, s45\n"
      "s_cmp_eq_u32 s36, s37\n"
      "s_cselect_b32 s48, 49152, s51\n"
      "buffer_load_dwordx4 v[192:195], v13, s[8:11], s47 offen sc1\n"
      "s_add_u32 m0, s39, 20480\n"
      "buffer_load_dword v26, v23, s[8:11], s31 offen\n"
      "s_mov_b32 s46, s36\n"
      "s_waitcnt vmcnt(6)\n"
      "v_cmp_ne_u32 vcc, s21, v28\n"
      "s_add_u32 s36, s36, s48\n"
      "s_cbranch_vccnz .Lsp_v1_17\n"
      ".Lbk_v1_17:\n"
      "s_min_u32 s36, s36, s6\n"
      "buffer_load_dword v28, v11, s[8:11], s36 offen sc1\n"
      "buffer_load_dwordx4 v0, s[8:11], s46 offen offset:1024 sc1 lds\n"
      "buffer_load_dwordx4 v0, s[8:11], s46 offen offset:0 sc1 lds\n"
      "buffer_load_dwordx4 v0, s[8:11], s46 offen offset:3072 sc1 lds\n"
      "v_add_u32 v21, 1, v21\n"
      "buffer_load_dwordx4 v0, s[8:11], s46 offen offset:2048 sc1 lds\n"
      "s_waitcnt vmcnt(7)\n"
      "ds_write_b32 v23, v21\n"
      "s_add_u32 m0, s40, 20480\n"
      "buffer_load_dwordx4 v[196:199], v13, s[8:11], s47 offen offset:1024 sc1\n"
      "buffer_load_dwordx4 v1, s[8:11], s46 offen offset:0 sc1 lds\n"
      "ds_read_b32 v22, v24\n"
      "buffer_load_dword v26, v23, s[8:11], s31 offen\n"
      "buffer_load_dword v26, v23, s[8:11], s31 offen\n"
      "s_waitcnt lgkmcnt(0)\n"
      "v_cmp_gt_i32 vcc, v21, v22\n"
      "s_sub_u32 s47, s46, s45\n"
      "s_cbranch_vccnz .Lsp_vy0_18\n"
      ".Lbk_vy0_18:\n"
      "ds_read_b128 v[100:103], v0 offset:8192\n"
      "ds_read_b128 v[112:115], v0 offset:11264\n"
      "ds_read_b128 v[104:107], v0 offset:9216\n"
      "ds_read_b128 v[108:111], v0 offset:10240\n"
      "ds_read_b128 v[124:127], v0 offset:14336\n"
      "s_waitcnt vmcnt(10)\n"
      "v_lshlrev_b32 v172, 16, v192\n"
      "v_lshlrev_b32 v176, 16, v194\n"
      "ds_read_b128 v[116:119], v0 offset:12288\n"
      "v_and_b32 v173, 0xffff0000, v192\n"
      "ds_read_b128 v[120:123], v0 offset:13312\n"
      "ds_read_b128 v[128:131], v0 offset:15360\n"
      "v_lshlrev_b32 v178, 16, v195\n"
      "ds_read2_b32 v[132:133], v2 offset0:0 offset1:32\n"
      "v_and_b32 v175, 0xffff0000, v193\n"
      "buffer_load_dword v26, v23, s[8:11], s31 offen\n"
      "ds_read_b128 v[144:147], v0 offset:2048\n"
      "v_and_b32 v179, 0xffff0000, v195\n"
      "v_lshlrev_b32 v174, 16, v193\n"
      "s_mov_b32 s20, 15\n"
      "s_waitcnt lgkmcnt(0)\n"
      "s_nop 4\n"
      "s_branch .Lv_loop\n"
      ".p2align 12\n"
      ".Lv_p0:\n"
      "s_branch .Lv_pr\n"
      ".p2align 5\n"
      ".Lv_loop:\n"
      "v_mfma_f32_16x16x32_bf16 v[188:191], v[64:67], v[100:103], 0\n"
      "ds_read2_b32 v[134:135], v2 offset0:64 offset1:96\n"
      "v_and_b32 v177, 0xffff0000, v194\n"
      "v_mfma_f32_16x16x32_bf16 v[188:191], v[68:71], v[104:107], v[188:191]\n"
      "buffer_load_dwordx4 v[192:195], v13, s[8:11], s47 offen sc1\n"
      "v_mfma_f32_16x16x32_bf16 v[188:191], v[72:75], v[108:111], v[188:191]\n"
      "ds_read2_b32 v[168:169], v3 offset0:0 offset1:32\n"
      "v_mfma_f32_16x16x32_bf16 v[188:191], v[76:79], v[112:115], v[188:191]\n"
      "ds_read_b128 v[152:155], v0 offset:4096\n"
      "v_mfma_f32_16x16x32_bf16 v[188:191], v[80:83], v[116:119], v[188:191]\n"
      "s_waitcnt lgkmcnt(3)\n"
      "v_cvt_pk_bf16_f32 v215, v186, v187\n"
      "v_mfma_f32_16x16x32_bf16 v[188:191], v[84:87], v[120:123], v[188:191]\n"
      "ds_read_b128 v[148:151], v0 offset:3072\n"
      "v_cvt_pk_bf16_f32 v212, v180, v181\n"
      "v_mfma_f32_16x16x32_bf16 v[188:191], v[88:91], v[124:127], v[188:191]\n"
      "ds_read_b128 v[136:139], v0 offset:0\n"
      "s_add_u32 s49, s49, 24576\n"
      "v_mfma_f32_16x16x32_bf16 v[188:191], v[92:95], v[128:131], v[188:191]\n"
      "s_waitcnt lgkmcnt(4)\n"
      "ds_read_b128 v[160:163], v0 offset:6144\n"
      "v_fmac_f32 v219, v183, v183\n"
      "v_mfma_f32_16x16x32_bf16 v[188:191], v[96:99], v[132:135], v[188:191]\n"
      "ds_read_b128 v[156:159], v0 offset:5120\n"
      "v_add_f32 v218, v218, v184\n"
      "v_mfma_f32_16x16x32_bf16 v[172:175], a[0:3], v[100:103], v[172:175]\n"
      "ds_read2_b32 v[170:171], v3 offset0:64 offset1:96\n"
      "v_add_f32 v218, v218, v185\n"
      "v_mfma_f32_16x16x32_bf16 v[172:175], a[4:7], v[104:107], v[172:175]\n"
      "ds_read_b128 v[140:143], v0 offset:1024\n"
      "v_cvt_pk_bf16_f32 v214, v184, v185\n"
      "v_mfma_f32_16x16x32_bf16 v[172:175], a[8:11], v[108:111], v[172:175]\n"
      "ds_read_b128 v[164:167], v0 offset:7168\n"
      "v_fmac_f32 v219, v184, v184\n"
      "v_mfma_f32_16x16x32_bf16 v[172:175], a[12:15], v[112:115], v[172:175]\n"
      "s_mov_b32 s46, s36\n"
      "v_fmac_f32 v219, v185, v185\n"
      "v_add_f32 v218, v218, v186\n"
      "v_mfma_f32_16x16x32_bf16 v[172:175], a[16:19], v[116:119], v[172:175]\n"
      "ds_write_b128 v17, v[188:191]\n"
      "v_mfma_f32_16x16x32_bf16 v[172:175], a[20:23], v[120:123], v[172:175]\n"
      "s_add_u32 s50, s50, 24576\n"
      "v_add_u32 v21, 1, v21\n"
      "v_mfma_f32_16x16x32_bf16 v[172:175], a[24:27], v[124:127], v[172:175]\n"
      "s_add_u32 m0, s39, 40960\n"
      "v_add_f32 v218, v218, v187\n"
      "v_mfma_f32_16x16x32_bf16 v[172:175], a[28:31], v[128:131], v[172:175]\n"
      "v_fmac_f32 v219, v186, v186\n"
      "v_fmac_f32 v219, v187, v187\n"
      "v_mfma_f32_16x16x32_bf16 v[172:175], a[32:35], v[132:135], v[172:175]\n"
      "s_waitcnt lgkmcnt(2)\n"
      "ds_write_b64 v18, v[218:219]\n"
      "v_mfma_f32_16x16x32_bf16 v[172:175], a[36:39], v[136:139], v[172:175]\n"
      "s_add_u32 s36, s36, 24576\n"
      "v_mfma_f32_16x16x32_bf16 v[172:175], a[40:43], v[140:143], v[172:175]\n"
      "buffer_store_dwordx4 v[212:215], v15, s[8:11], s38 offen offset:1024 sc1\n"
      "v_mfma_f32_16x16x32_bf16 v[172:175], a[44:47], v[144:147], v[172:175]\n"
      "ds_write_b32 v23, v21\n"
      "v_mfma_f32_16x16x32_bf16 v[172:175], a[48:51], v[148:151], v[172:175]\n"
      "s_waitcnt vmcnt(11) lgkmcnt(2)\n"
      "v_cmp_ne_u32 vcc, s21, v28\n"
      "v_mfma_f32_16x16x32_bf16 v[172:175], a[52:55], v[152:155], v[172:175]\n"
      "v_mfma_f32_16x16x32_bf16 v[172:175], a[56:59], v[156:159], v[172:175]\n"
      "v_mfma_f32_16x16x32_bf16 v[172:175], a[60:63], v[160:163], v[172:175]\n"
      "v_mfma_f32_16x16x32_bf16 v[172:175], a[64:67], v[164:167], v[172:175]\n"
      "s_cbranch_vccnz .Lsp_v0_19\n"
      ".Lbk_v0_19:\n"
      "v_mfma_f32_16x16x32_bf16 v[172:175], a[68:71], v[168:171], v[172:175]\n"
      "buffer_load_dwordx4 v0, s[8:11], s46 offen offset:0 sc1 lds\n"
      "v_mfma_f32_16x16x32_bf16 v[176:179], a[72:75], v[100:103], v[176:179]\n"
      "s_waitcnt vmcnt(7) lgkmcnt(0)\n"
      "s_min_u32 s36, s36, s6\n"
      "v_lshlrev_b32 v186, 16, v199\n"
      "v_mfma_f32_16x16x32_bf16 v[176:179], a[76:79], v[104:107], v[176:179]\n"
      "v_and_b32 v181, 0xffff0000, v196\n"
      "v_lshlrev_b32 v184, 16, v198\n"
      "v_mfma_f32_16x16x32_bf16 v[176:179], a[80:83], v[108:111], v[176:179]\n"
      "s_add_u32 s38, s38, 24576\n"
      "ds_read_b32 v22, v24\n"
      "v_lshlrev_b32 v180, 16, v196\n"
      "v_mfma_f32_16x16x32_bf16 v[176:179], a[84:87], v[112:115], v[176:179]\n"
      "buffer_load_dword v28, v11, s[8:11], s36 offen sc1\n"
      "v_mfma_f32_16x16x32_bf16 v[176:179], a[88:91], v[116:119], v[176:179]\n"
      "v_cvt_pk_bf16_f32 v209, v174, v175\n"
      "v_mfma_f32_16x16x32_bf16 v[176:179], a[92:95], v[120:123], v[176:179]\n"
      "buffer_load_dwordx4 v0, s[8:11], s46 offen offset:2048 sc1 lds\n"
      "v_mfma_f32_16x16x32_bf16 v[176:179], a[96:99], v[124:127], v[176:179]\n"
      "v_mul_f32 v219, v172, v172\n"
      "v_and_b32 v185, 0xffff0000, v198\n"
      "v_mfma_f32_16x16x32_bf16 v[176:179], a[100:103], v[128:131], v[176:179]\n"
      "s_waitcnt lgkmcnt(0)\n"
      "v_cmp_gt_i32 vcc, v21, v22\n"
      "v_and_b32 v183, 0xffff0000, v197\n"
      "v_mfma_f32_16x16x32_bf16 v[176:179], a[104:107], v[132:135], v[176:179]\n"
      "v_lshlrev_b32 v182, 16, v197\n"
      "v_and_b32 v187, 0xffff0000, v199\n"
      "v_mfma_f32_16x16x32_bf16 v[176:179], a[108:111], v[136:139], v[176:179]\n"
      "buffer_load_dwordx4 v[196:199], v13, s[8:11], s47 offen offset:1024 sc1\n"
      "v_mfma_f32_16x16x32_bf16 v[176:179], a[112:115], v[140:143], v[176:179]\n"
      "v_fmac_f32_e64 v219, v173, v173\n"
      "v_fmac_f32_e64 v219, v174, v174\n"
      "v_mfma_f32_16x16x32_bf16 v[176:179], a[116:119], v[144:147], v[176:179]\n"
      "s_cbranch_vccnz .Lsp_vz0_20\n"
      ".Lbk_vz0_20:\n"
      "ds_read_b128 v[204:207], v19\n"
      "v_mfma_f32_16x16x32_bf16 v[176:179], a[120:123], v[148:151], v[176:179]\n"
      "v_mfma_f32_16x16x32_bf16 v[176:179], a[124:127], v[152:155], v[176:179]\n"
      "v_add_u32 v21, 1, v21\n"
      "v_mfma_f32_16x16x32_bf16 v[176:179], a[128:131], v[156:159], v[176:179]\n"
      "buffer_load_dwordx4 v0, s[8:11], s46 offen offset:1024 sc1 lds\n"
      "v_mfma_f32_16x16x32_bf16 v[176:179], a[132:135], v[160:163], v[176:179]\n"
      "v_add_f32 v218, v172, v173\n"
      "v_mfma_f32_16x16x32_bf16 v[176:179], a[136:139], v[164:167], v[176:179]\n"
      "v_fmac_f32 v219, v175, v175\n"
      "v_mfma_f32_16x16x32_bf16 v[176:179], a[140:143], v[168:171], v[176:179]\n"
      "s_waitcnt vmcnt(10)\n"
      "ds_write_b32 v23, v21\n"
      "v_mfma_f32_16x16x32_bf16 v[180:183], a[144:147], v[100:103], v[180:183]\n"
      "s_waitcnt lgkmcnt(1)\n"
      "v_add_f32 v218, v218, v174\n"
      "v_add_f32 v204, v204, v206\n"
      "v_mfma_f32_16x16x32_bf16 v[180:183], a[148:151], v[104:107], v[180:183]\n"
      "buffer_load_dwordx4 v0, s[8:11], s46 offen offset:3072 sc1 lds\n"
      "v_mfma_f32_16x16x32_bf16 v[180:183], a[152:155], v[108:111], v[180:183]\n"
      "v_add_f32 v205, v205, v207\n"
      "v_mfma_f32_16x16x32_bf16 v[180:183], a[156:159], v[112:115], v[180:183]\n"
      "v_cvt_pk_bf16_f32 v210, v176, v177\n"
      "v_mfma_f32_16x16x32_bf16 v[180:183], a[160:163], v[116:119], v[180:183]\n"
      "v_add_f32_dpp v205, v205, v205 quad_perm:[1,0,3,2] row_mask:0xf bank_mask:0xf\n"
      "v_mfma_f32_16x16x32_bf16 v[180:183], a[164:167], v[120:123], v[180:183]\n"
      "v_fmac_f32 v219, v176, v176\n"
      "s_add_u32 m0, s40, 40960\n"
      "v_mfma_f32_16x16x32_bf16 v[180:183], a[168:171], v[124:127], v[180:183]\n"
      "buffer_load_dwordx4 v1, s[8:11], s46 offen offset:0 sc1 lds\n"
      "v_mfma_f32_16x16x32_bf16 v[180:183], a[172:175], v[128:131], v[180:183]\n"
      "v_add_f32_dpp v205, v205, v205 quad_perm:[2,3,0,1] row_mask:0xf bank_mask:0xf\n"
      "v_add_f32_dpp v204, v204, v204 quad_perm:[1,0,3,2] row_mask:0xf bank_mask:0xf\n"
      "v_mfma_f32_16x16x32_bf16 v[180:183], a[176:179], v[132:135], v[180:183]\n"
      "v_add_f32_dpp v204, v204, v204 quad_perm:[2,3,0,1] row_mask:0xf bank_mask:0xf\n"
      "v_add_f32_dpp v205, v205, v205 row_half_mirror row_mask:0xf bank_mask:0xf\n"
      "v_mfma_f32_16x16x32_bf16 v[180:183], a[180:183], v[136:139], v[180:183]\n"
      "ds_read_b32 v22, v24\n"
      "v_add_f32_dpp v204, v204, v204 row_half_mirror row_mask:0xf bank_mask:0xf\n"
      "v_mfma_f32_16x16x32_bf16 v[180:183], a[184:187], v[140:143], v[180:183]\n"
      "s_waitcnt vmcnt(10) lgkmcnt(1)\n"
      "buffer_store_dword v25, v12, s[8:11], s50 offen sc1\n"
      "v_mfma_f32_16x16x32_bf16 v[180:183], a[188:191], v[144:147], v[180:183]\n"
      "v_add_f32 v218, v218, v175\n"
      "v_add_f32 v218, v218, v176\n"
      "v_mfma_f32_16x16x32_bf16 v[180:183], a[192:195], v[148:151], v[180:183]\n"
      "v_mul_f32 v204, v27, v204\n"
      "v_mfma_f32_16x16x32_bf16 v[180:183], a[196:199], v[152:155], v[180:183]\n"
      "v_fmaak_f32 v205, v27, v205, 0x36a7c5ac\n"
      "v_add_f32 v218, v218, v177\n"
      "v_mfma_f32_16x16x32_bf16 v[180:183], a[200:203], v[156:159], v[180:183]\n"
      "buffer_store_dwordx2 v[204:205], v20, s[8:11], s49 offen sc1\n"
      "v_mfma_f32_16x16x32_bf16 v[180:183], a[204:207], v[160:163], v[180:183]\n"
      "s_waitcnt lgkmcnt(0)\n"
      "v_fmac_f32 v219, v177, v177\n"
      "v_cmp_gt_i32 vcc, v21, v22\n"
      "v_mfma_f32_16x16x32_bf16 v[180:183], a[208:211], v[164:167], v[180:183]\n"
      "s_waitcnt vmcnt(10)\n"
      "v_fmac_f32 v219, v178, v178\n"
      "v_and_b32 v175, 0xffff0000, v193\n"
      "v_mfma_f32_16x16x32_bf16 v[180:183], a[212:215], v[168:171], v[180:183]\n"
      "v_lshlrev_b32 v176, 16, v194\n"
      "v_mfma_f32_16x16x32_bf16 v[184:187], a[216:219], v[100:103], v[184:187]\n"
      "v_add_f32 v218, v218, v178\n"
      "v_cvt_pk_bf16_f32 v211, v178, v179\n"
      "v_mfma_f32_16x16x32_bf16 v[184:187], a[220:223], v[104:107], v[184:187]\n"
      "s_cbranch_vccnz .Lsp_vy1_21\n"
      ".Lbk_vy1_21:\n"
      "ds_read_b128 v[100:103], v0 offset:28672\n"
      "v_mfma_f32_16x16x32_bf16 v[184:187], a[224:227], v[108:111], v[184:187]\n"
      "v_cvt_pk_bf16_f32 v208, v172, v173\n"
      "v_mfma_f32_16x16x32_bf16 v[184:187], a[228:231], v[112:115], v[184:187]\n"
      "ds_read_b128 v[112:115], v0 offset:31744\n"
      "v_fmac_f32 v219, v179, v179\n"
      "v_mfma_f32_16x16x32_bf16 v[184:187], a[232:235], v[116:119], v[184:187]\n"
      "ds_read_b128 v[104:107], v0 offset:29696\n"
      "v_fmac_f32 v219, v180, v180\n"
      "v_mfma_f32_16x16x32_bf16 v[184:187], a[236:239], v[120:123], v[184:187]\n"
      "v_and_b32 v177, 0xffff0000, v194\n"
      "v_mfma_f32_16x16x32_bf16 v[184:187], a[240:243], v[124:127], v[184:187]\n"
      "ds_read_b128 v[108:111], v0 offset:30720\n"
      "v_fmac_f32 v219, v181, v181\n"
      "v_mfma_f32_16x16x32_bf16 v[184:187], a[244:247], v[128:131], v[184:187]\n"
      "ds_read_b128 v[116:119], v0 offset:32768\n"
      "v_add_f32 v218, v218, v179\n"
      "v_mfma_f32_16x16x32_bf16 v[184:187], a[248:251], v[132:135], v[184:187]\n"
      "v_cvt_pk_bf16_f32 v213, v182, v183\n"
      "v_mfma_f32_16x16x32_bf16 v[184:187], a[252:255], v[136:139], v[184:187]\n"
      "ds_read_b128 v[124:127], v0 offset:34816\n"
      "v_add_f32 v218, v218, v180\n"
      "v_mfma_f32_16x16x32_bf16 v[184:187], v[32:35], v[140:143], v[184:187]\n"
      "ds_read_b128 v[128:131], v0 offset:35840\n"
      "v_add_f32 v218, v218, v181\n"
      "v_mfma_f32_16x16x32_bf16 v[184:187], v[36:39], v[144:147], v[184:187]\n"
      "buffer_store_dwordx4 v[208:211], v15, s[8:11], s38 offen sc1\n"
      "v_mfma_f32_16x16x32_bf16 v[184:187], v[40:43], v[148:151], v[184:187]\n"
      "ds_read2_b32 v[132:133], v4 offset0:0 offset1:32\n"
      "v_fmac_f32 v219, v182, v182\n"
      "v_mfma_f32_16x16x32_bf16 v[184:187], v[44:47], v[152:155], v[184:187]\n"
      "v_and_b32 v173, 0xffff0000, v192\n"
      "v_mfma_f32_16x16x32_bf16 v[184:187], v[48:51], v[156:159], v[184:187]\n"
      "ds_read_b128 v[120:123], v0 offset:33792\n"
      "v_and_b32 v179, 0xffff0000, v195\n"
      "v_mfma_f32_16x16x32_bf16 v[184:187], v[52:55], v[160:163], v[184:187]\n"
      "s_sub_u32 s47, s46, s45\n"
      "ds_read2_b32 v[134:135], v4 offset0:64 offset1:96\n"
      "v_lshlrev_b32 v172, 16, v192\n"
      "v_mfma_f32_16x16x32_bf16 v[184:187], v[56:59], v[164:167], v[184:187]\n"
      "ds_read_b128 v[160:163], v0 offset:26624\n"
      "v_lshlrev_b32 v174, 16, v193\n"
      "v_mfma_f32_16x16x32_bf16 v[184:187], v[60:63], v[168:171], v[184:187]\n"
      "s_waitcnt lgkmcnt(4)\n"
      "ds_read_b128 v[144:147], v0 offset:22528\n"
      "v_lshlrev_b32 v178, 16, v195\n"
      "v_mfma_f32_16x16x32_bf16 v[188:191], v[64:67], v[100:103], 0\n"
      "v_add_f32 v218, v218, v182\n"
      "v_mfma_f32_16x16x32_bf16 v[188:191], v[68:71], v[104:107], v[188:191]\n"
      "ds_read_b128 v[148:151], v0 offset:23552\n"
      "v_cvt_pk_bf16_f32 v212, v180, v181\n"
      "v_mfma_f32_16x16x32_bf16 v[188:191], v[72:75], v[108:111], v[188:191]\n"
      "ds_read_b128 v[156:159], v0 offset:25600\n"
      "v_fmac_f32 v219, v183, v183\n"
      "v_mfma_f32_16x16x32_bf16 v[188:191], v[76:79], v[112:115], v[188:191]\n"
      "ds_read_b128 v[152:155], v0 offset:24576\n"
      "v_add_f32 v218, v218, v183\n"
      "v_mfma_f32_16x16x32_bf16 v[188:191], v[80:83], v[116:119], v[188:191]\n"
      "s_waitcnt lgkmcnt(5)\n"
      "ds_read2_b32 v[168:169], v5 offset0:0 offset1:32\n"
      "v_mfma_f32_16x16x32_bf16 v[188:191], v[84:87], v[120:123], v[188:191]\n"
      "s_cmp_eq_u32 s36, s37\n"
      "s_cselect_b32 s48, 49152, s51\n"
      "ds_read2_b32 v[170:171], v5 offset0:64 offset1:96\n"
      "v_mfma_f32_16x16x32_bf16 v[188:191], v[88:91], v[124:127], v[188:191]\n"
      "buffer_load_dwordx4 v[192:195], v13, s[8:11], s47 offen sc1\n"
      "v_mfma_f32_16x16x32_bf16 v[188:191], v[92:95], v[128:131], v[188:191]\n"
      "v_add_f32 v218, v218, v184\n"
      "v_fmac_f32 v219, v184, v184\n"
      "v_mfma_f32_16x16x32_bf16 v[188:191], v[96:99], v[132:135], v[188:191]\n"
      "ds_read_b128 v[136:139], v0 offset:20480\n"
      "v_mfma_f32_16x16x32_bf16 v[172:175], a[0:3], v[100:103], v[172:175]\n"
      "v_add_f32 v218, v218, v185\n"
      "s_add_u32 m0, s39, 61440\n"
      "v_mfma_f32_16x16x32_bf16 v[172:175], a[4:7], v[104:107], v[172:175]\n"
      "ds_read_b128 v[164:167], v0 offset:27648\n"
      "v_cvt_pk_bf16_f32 v214, v184, v185\n"
      "v_mfma_f32_16x16x32_bf16 v[172:175], a[8:11], v[108:111], v[172:175]\n"
      "ds_read_b128 v[140:143], v0 offset:21504\n"
      "v_fmac_f32 v219, v185, v185\n"
      "v_mfma_f32_16x16x32_bf16 v[172:175], a[12:15], v[112:115], v[172:175]\n"
      "v_fmac_f32 v219, v186, v186\n"
      "v_fmac_f32 v219, v187, v187\n"
      "v_mfma_f32_16x16x32_bf16 v[172:175], a[16:19], v[116:119], v[172:175]\n"
      "ds_write_b128 v17, v[188:191]\n"
      "v_mfma_f32_16x16x32_bf16 v[172:175], a[20:23], v[120:123], v[172:175]\n"
      "v_cvt_pk_bf16_f32 v215, v186, v187\n"
      "v_add_f32 v218, v218, v186\n"
      "v_mfma_f32_16x16x32_bf16 v[172:175], a[24:27], v[124:127], v[172:175]\n"
      "buffer_store_dwordx4 v[212:215], v15, s[8:11], s38 offen offset:1024 sc1\n"
      "v_mfma_f32_16x16x32_bf16 v[172:175], a[28:31], v[128:131], v[172:175]\n"
      "v_add_f32 v218, v218, v187\n"
      "v_mfma_f32_16x16x32_bf16 v[172:175], a[32:35], v[132:135], v[172:175]\n"
      "s_waitcnt lgkmcnt(2)\n"
      "ds_write_b64 v18, v[218:219]\n"
      "v_mfma_f32_16x16x32_bf16 v[172:175], a[36:39], v[136:139], v[172:175]\n"
      "s_waitcnt lgkmcnt(2)\n"
      "v_add_u32 v21, 1, v21\n"
      "s_mov_b32 s46, s36\n"
      "v_mfma_f32_16x16x32_bf16 v[172:175], a[40:43], v[140:143], v[172:175]\n"
      "s_waitcnt vmcnt(8)\n"
      "v_and_b32 v181, 0xffff0000, v196\n"
      "v_mfma_f32_16x16x32_bf16 v[172:175], a[44:47], v[144:147], v[172:175]\n"
      "ds_write_b32 v23, v21\n"
      "v_mfma_f32_16x16x32_bf16 v[172:175], a[48:51], v[148:151], v[172:175]\n"
      "v_cmp_ne_u32 vcc, s21, v28\n"
      "v_mfma_f32_16x16x32_bf16 v[172:175], a[52:55], v[152:155], v[172:175]\n"
      "v_lshlrev_b32 v186, 16, v199\n"
      "s_add_u32 s36, s36, s48\n"
      "v_mfma_f32_16x16x32_bf16 v[172:175], a[56:59], v[156:159], v[172:175]\n"
      "v_lshlrev_b32_e64 v180, 16, v196\n"
      "v_lshlrev_b32_e64 v184, 16, v198\n"
      "v_mfma_f32_16x16x32_bf16 v[172:175], a[60:63], v[160:163], v[172:175]\n"
      "s_add_u32 s49, s49, 24576\n"
      "v_and_b32 v187, 0xffff0000, v199\n"
      "v_mfma_f32_16x16x32_bf16 v[172:175], a[64:67], v[164:167], v[172:175]\n"
      "s_cbranch_vccnz .Lsp_v1_22\n"
      ".Lbk_v1_22:\n"
      "s_min_u32 s36, s36, s6\n"
      "v_mfma_f32_16x16x32_bf16 v[172:175], a[68:71], v[168:171], v[172:175]\n"
      "buffer_load_dword v28, v11, s[8:11], s36 offen sc1\n"
      "v_mfma_f32_16x16x32_bf16 v[176:179], a[72:75], v[100:103], v[176:179]\n"
      "v_mfma_f32_16x16x32_bf16 v[176:179], a[76:79], v[104:107], v[176:179]\n"
      "buffer_load_dwordx4 v0, s[8:11], s46 offen offset:1024 sc1 lds\n"
      "v_mfma_f32_16x16x32_bf16 v[176:179], a[80:83], v[108:111], v[176:179]\n"
      "ds_read_b32 v22, v24\n"
      "v_mfma_f32_16x16x32_bf16 v[176:179], a[84:87], v[112:115], v[176:179]\n"
      "v_and_b32 v183, 0xffff0000, v197\n"
      "v_mfma_f32_16x16x32_bf16 v[176:179], a[88:91], v[116:119], v[176:179]\n"
      "v_mfma_f32_16x16x32_bf16 v[176:179], a[92:95], v[120:123], v[176:179]\n"
      "buffer_load_dwordx4 v0, s[8:11], s46 offen offset:0 sc1 lds\n"
      "v_mfma_f32_16x16x32_bf16 v[176:179], a[96:99], v[124:127], v[176:179]\n"
      "v_cvt_pk_bf16_f32 v208, v172, v173\n"
      "v_mfma_f32_16x16x32_bf16 v[176:179], a[100:103], v[128:131], v[176:179]\n"
      "v_mfma_f32_16x16x32_bf16 v[176:179], a[104:107], v[132:135], v[176:179]\n"
      "v_mfma_f32_16x16x32_bf16 v[176:179], a[108:111], v[136:139], v[176:179]\n"
      "s_waitcnt lgkmcnt(0)\n"
      "v_cmp_gt_i32 vcc, v21, v22\n"
      "v_lshlrev_b32 v182, 16, v197\n"
      "v_mfma_f32_16x16x32_bf16 v[176:179], a[112:115], v[140:143], v[176:179]\n"
      "buffer_load_dwordx4 v0, s[8:11], s46 offen offset:3072 sc1 lds\n"
      "v_mfma_f32_16x16x32_bf16 v[176:179], a[116:119], v[144:147], v[176:179]\n"
      "v_mul_f32 v219, v172, v172\n"
      "v_fmac_f32 v219, v173, v173\n"
      "v_mfma_f32_16x16x32_bf16 v[176:179], a[120:123], v[148:151], v[176:179]\n"
      "s_cbranch_vccnz .Lsp_vz1_23\n"
      ".Lbk_vz1_23:\n"
      "ds_read_b128 v[204:207], v19\n"
      "v_mfma_f32_16x16x32_bf16 v[176:179], a[124:127], v[152:155], v[176:179]\n"
      "v_and_b32 v185, 0xffff0000, v198\n"
      "v_add_f32 v218, v172, v173\n"
      "v_mfma_f32_16x16x32_bf16 v[176:179], a[128:131], v[156:159], v[176:179]\n"
      "v_add_u32 v21, 1, v21\n"
      "v_mfma_f32_16x16x32_bf16 v[176:179], a[132:135], v[160:163], v[176:179]\n"
      "buffer_load_dwordx4 v0, s[8:11], s46 offen offset:2048 sc1 lds\n"
      "v_mfma_f32_16x16x32_bf16 v[176:179], a[136:139], v[164:167], v[176:179]\n"
      "s_waitcnt vmcnt(10)\n"
      "ds_write_b32 v23, v21\n"
      "v_mfma_f32_16x16x32_bf16 v[176:179], a[140:143], v[168:171], v[176:179]\n"
      "v_mfma_f32_16x16x32_bf16 v[180:183], a[144:147], v[100:103], v[180:183]\n"
      "s_add_u32 m0, s40, 61440\n"
      "v_add_f32 v218, v218, v174\n"
      "v_mfma_f32_16x16x32_bf16 v[180:183], a[148:151], v[104:107], v[180:183]\n"
      "buffer_load_dwordx4 v[196:199], v13, s[8:11], s47 offen offset:1024 sc1\n"
      "v_mfma_f32_16x16x32_bf16 v[180:183], a[152:155], v[108:111], v[180:183]\n"
      "s_waitcnt lgkmcnt(1)\n"
      "v_add_f32 v205, v205, v207\n"
      "v_add_f32 v204, v204, v206\n"
      "v_mfma_f32_16x16x32_bf16 v[180:183], a[156:159], v[112:115], v[180:183]\n"
      "s_nop 0\n"
      "v_add_f32_dpp v204, v204, v204 quad_perm:[1,0,3,2] row_mask:0xf bank_mask:0xf\n"
      "v_fmac_f32 v219, v174, v174\n"
      "v_mfma_f32_16x16x32_bf16 v[180:183], a[160:163], v[116:119], v[180:183]\n"
      "v_add_f32_dpp v205, v205, v205 quad_perm:[1,0,3,2] row_mask:0xf bank_mask:0xf\n"
      "v_add_f32 v218, v218, v175\n"
      "v_mfma_f32_16x16x32_bf16 v[180:183], a[164:167], v[120:123], v[180:183]\n"
      "buffer_load_dwordx4 v1, s[8:11], s46 offen offset:0 sc1 lds\n"
      "v_mfma_f32_16x16x32_bf16 v[180:183], a[168:171], v[124:127], v[180:183]\n"
      "v_add_f32_dpp v204, v204, v204 quad_perm:[2,3,0,1] row_mask:0xf bank_mask:0xf\n"
      "v_add_f32 v218, v218, v176\n"
      "v_mfma_f32_16x16x32_bf16 v[180:183], a[172:175], v[128:131], v[180:183]\n"
      "v_fmac_f32 v219, v175, v175\n"
      "v_mfma_f32_16x16x32_bf16 v[180:183], a[176:179], v[132:135], v[180:183]\n"
      "s_add_u32 s38, s38, 24576\n"
      "v_add_f32_dpp v205, v205, v205 quad_perm:[2,3,0,1] row_mask:0xf bank_mask:0xf\n"
      "v_mfma_f32_16x16x32_bf16 v[180:183], a[180:183], v[136:139], v[180:183]\n"
      "s_add_u32 s50, s50, 24576\n"
      "v_add_f32_dpp v205, v205, v205 row_half_mirror row_mask:0xf bank_mask:0xf\n"
      "v_fmac_f32 v219, v176, v176\n"
      "v_mfma_f32_16x16x32_bf16 v[180:183], a[184:187], v[140:143], v[180:183]\n"
      "ds_read_b32 v22, v24\n"
      "v_mfma_f32_16x16x32_bf16 v[180:183], a[188:191], v[144:147], v[180:183]\n"
      "v_add_f32_dpp v204, v204, v204 row_half_mirror row_mask:0xf bank_mask:0xf\n"
      "v_mfma_f32_16x16x32_bf16 v[180:183], a[192:195], v[148:151], v[180:183]\n"
      "s_waitcnt vmcnt(10) lgkmcnt(1)\n"
      "buffer_store_dword v25, v12, s[8:11], s50 offen sc1\n"
      "v_mfma_f32_16x16x32_bf16 v[180:183], a[196:199], v[152:155], v[180:183]\n"
      "v_fmaak_f32 v205, v27, v205, 0x36a7c5ac\n"
      "v_mul_f32 v204, v27, v204\n"
      "v_mfma_f32_16x16x32_bf16 v[180:183], a[200:203], v[156:159], v[180:183]\n"
      "buffer_store_dwordx2 v[204:205], v20, s[8:11], s49 offen sc1\n"
      "v_mfma_f32_16x16x32_bf16 v[180:183], a[204:207], v[160:163], v[180:183]\n"
      "s_waitcnt lgkmcnt(0)\n"
      "v_cvt_pk_bf16_f32 v209, v174, v175\n"
      "v_cmp_gt_i32 vcc, v21, v22\n"
      "v_mfma_f32_16x16x32_bf16 v[180:183], a[208:211], v[164:167], v[180:183]\n"
      "s_sub_u32 s47, s46, s45\n"
      "v_add_f32 v218, v218, v177\n"
      "v_fmac_f32 v219, v177, v177\n"
      "v_mfma_f32_16x16x32_bf16 v[180:183], a[212:215], v[168:171], v[180:183]\n"
      "v_cvt_pk_bf16_f32 v210, v176, v177\n"
      "v_add_f32 v218, v218, v178\n"
      "v_mfma_f32_16x16x32_bf16 v[184:187], a[216:219], v[100:103], v[184:187]\n"
      "s_cbranch_vccnz .Lsp_vy0_24\n"
      ".Lbk_vy0_24:\n"
      "v_mfma_f32_16x16x32_bf16 v[184:187], a[220:223], v[104:107], v[184:187]\n"
      "ds_read_b128 v[100:103], v10 offset:8192\n"
      "v_add_f32 v218, v218, v179\n"
      "v_mfma_f32_16x16x32_bf16 v[184:187], a[224:227], v[108:111], v[184:187]\n"
      "v_fmac_f32 v219, v178, v178\n"
      "v_fmac_f32 v219, v179, v179\n"
      "v_mfma_f32_16x16x32_bf16 v[184:187], a[228:231], v[112:115], v[184:187]\n"
      "ds_read_b128 v[112:115], v10 offset:11264\n"
      "v_fmac_f32 v219, v180, v180\n"
      "v_mfma_f32_16x16x32_bf16 v[184:187], a[232:235], v[116:119], v[184:187]\n"
      "ds_read_b128 v[104:107], v10 offset:9216\n"
      "v_fmac_f32 v219, v181, v181\n"
      "v_mfma_f32_16x16x32_bf16 v[184:187], a[236:239], v[120:123], v[184:187]\n"
      "ds_read_b128 v[108:111], v10 offset:10240\n"
      "v_mfma_f32_16x16x32_bf16 v[184:187], a[240:243], v[124:127], v[184:187]\n"
      "s_waitcnt vmcnt(10)\n"
      "ds_read_b128 v[124:127], v10 offset:14336\n"
      "v_lshlrev_b32 v172, 16, v192\n"
      "v_mfma_f32_16x16x32_bf16 v[184:187], a[244:247], v[128:131], v[184:187]\n"
      "v_cvt_pk_bf16_f32 v211, v178, v179\n"
      "v_lshlrev_b32 v176, 16, v194\n"
      "v_mfma_f32_16x16x32_bf16 v[184:187], a[248:251], v[132:135], v[184:187]\n"
      "ds_read_b128 v[116:119], v10 offset:12288\n"
      "v_add_f32 v218, v218, v180\n"
      "v_mfma_f32_16x16x32_bf16 v[184:187], a[252:255], v[136:139], v[184:187]\n"
      "v_cvt_pk_bf16_f32 v213, v182, v183\n"
      "v_and_b32 v173, 0xffff0000, v192\n"
      "v_mfma_f32_16x16x32_bf16 v[184:187], v[32:35], v[140:143], v[184:187]\n"
      "ds_read_b128 v[120:123], v10 offset:13312\n"
      "v_fmac_f32 v219, v182, v182\n"
      "v_mfma_f32_16x16x32_bf16 v[184:187], v[36:39], v[144:147], v[184:187]\n"
      "ds_read_b128 v[128:131], v10 offset:15360\n"
      "v_lshlrev_b32 v178, 16, v195\n"
      "v_mfma_f32_16x16x32_bf16 v[184:187], v[40:43], v[148:151], v[184:187]\n"
      "ds_read2_b32 v[132:133], v6 offset0:0 offset1:32\n"
      "v_and_b32 v175, 0xffff0000, v193\n"
      "v_mfma_f32_16x16x32_bf16 v[184:187], v[44:47], v[152:155], v[184:187]\n"
      "buffer_store_dwordx4 v[208:211], v15, s[8:11], s38 offen sc1\n"
      "v_mfma_f32_16x16x32_bf16 v[184:187], v[48:51], v[156:159], v[184:187]\n"
      "ds_read_b128 v[144:147], v10 offset:2048\n"
      "v_add_f32 v218, v218, v181\n"
      "v_mfma_f32_16x16x32_bf16 v[184:187], v[52:55], v[160:163], v[184:187]\n"
      "v_add_f32 v218, v218, v182\n"
      "v_and_b32 v179, 0xffff0000, v195\n"
      "v_mfma_f32_16x16x32_bf16 v[184:187], v[56:59], v[164:167], v[184:187]\n"
      "s_waitcnt lgkmcnt(4)\n"
      "v_add_f32 v218, v218, v183\n"
      "v_lshlrev_b32 v174, 16, v193\n"
      "v_mfma_f32_16x16x32_bf16 v[184:187], v[60:63], v[168:171], v[184:187]\n"
      "v_mfma_f32_16x16x32_bf16 v[188:191], v[64:67], v[100:103], 0\n"
      "ds_read2_b32 v[134:135], v6 offset0:64 offset1:96\n"
      "v_and_b32 v177, 0xffff0000, v194\n"
      "v_mfma_f32_16x16x32_bf16 v[188:191], v[68:71], v[104:107], v[188:191]\n"
      "buffer_load_dwordx4 v[192:195], v13, s[8:11], s47 offen sc1\n"
      "v_mfma_f32_16x16x32_bf16 v[188:191], v[72:75], v[108:111], v[188:191]\n"
      "ds_read2_b32 v[168:169], v7 offset0:0 offset1:32\n"
      "v_mfma_f32_16x16x32_bf16 v[188:191], v[76:79], v[112:115], v[188:191]\n"
      "ds_read_b128 v[152:155], v10 offset:4096\n"
      "v_mfma_f32_16x16x32_bf16 v[188:191], v[80:83], v[116:119], v[188:191]\n"
      "s_waitcnt lgkmcnt(3)\n"
      "v_cvt_pk_bf16_f32 v215, v186, v187\n"
      "v_mfma_f32_16x16x32_bf16 v[188:191], v[84:87], v[120:123], v[188:191]\n"
      "ds_read_b128 v[148:151], v10 offset:3072\n"
      "v_cvt_pk_bf16_f32 v212, v180, v181\n"
      "v_mfma_f32_16x16x32_bf16 v[188:191], v[88:91], v[124:127], v[188:191]\n"
      "ds_read_b128 v[136:139], v10 offset:0\n"
      "s_add_u32 s49, s49, 24576\n"
      "v_mfma_f32_16x16x32_bf16 v[188:191], v[92:95], v[128:131], v[188:191]\n"
      "s_waitcnt lgkmcnt(4)\n"
      "ds_read_b128 v[160:163], v10 offset:6144\n"
      "v_fmac_f32 v219, v183, v183\n"
      "v_mfma_f32_16x16x32_bf16 v[188:191], v[96:99], v[132:135], v[188:191]\n"
      "ds_read_b128 v[156:159], v10 offset:5120\n"
      "v_add_f32 v218, v218, v184\n"
      "v_mfma_f32_16x16x32_bf16 v[172:175], a[0:3], v[100:103], v[172:175]\n"
      "ds_read2_b32 v[170:171], v7 offset0:64 offset1:96\n"
      "v_add_f32 v218, v218, v185\n"
      "v_mfma_f32_16x16x32_bf16 v[172:175], a[4:7], v[104:107], v[172:175]\n"
      "ds_read_b128 v[140:143], v10 offset:1024\n"
      "v_cvt_pk_bf16_f32 v214, v184, v185\n"
      "v_mfma_f32_16x16x32_bf16 v[172:175], a[8:11], v[108:111], v[172:175]\n"
      "ds_read_b128 v[164:167], v10 offset:7168\n"
      "v_fmac_f32 v219, v184, v184\n"
      "v_mfma_f32_16x16x32_bf16 v[172:175], a[12:15], v[112:115], v[172:175]\n"
      "s_mov_b32 s46, s36\n"
      "v_fmac_f32 v219, v185, v185\n"
      "v_add_f32 v218, v218, v186\n"
      "v_mfma_f32_16x16x32_bf16 v[172:175], a[16:19], v[116:119], v[172:175]\n"
      "ds_write_b128 v17, v[188:191]\n"
      "v_mfma_f32_16x16x32_bf16 v[172:175], a[20:23], v[120:123], v[172:175]\n"
      "s_add_u32 s50, s50, 24576\n"
      "v_add_u32 v21, 1, v21\n"
      "v_mfma_f32_16x16x32_bf16 v[172:175], a[24:27], v[124:127], v[172:175]\n"
      "s_add_u32 m0, s39, 0\n"
      "v_add_f32 v218, v218, v187\n"
      "v_mfma_f32_16x16x32_bf16 v[172:175], a[28:31], v[128:131], v[172:175]\n"
      "v_fmac_f32 v219, v186, v186\n"
      "v_fmac_f32 v219, v187, v187\n"
      "v_mfma_f32_16x16x32_bf16 v[172:175], a[32:35], v[132:135], v[172:175]\n"
      "s_waitcnt lgkmcnt(2)\n"
      "ds_write_b64 v18, v[218:219]\n"
      "v_mfma_f32_16x16x32_bf16 v[172:175], a[36:39], v[136:139], v[172:175]\n"
      "s_add_u32 s36, s36, 24576\n"
      "v_mfma_f32_16x16x32_bf16 v[172:175], a[40:43], v[140:143], v[172:175]\n"
      "buffer_store_dwordx4 v[212:215], v15, s[8:11], s38 offen offset:1024 sc1\n"
      "v_mfma_f32_16x16x32_bf16 v[172:175], a[44:47], v[144:147], v[172:175]\n"
      "ds_write_b32 v23, v21\n"
      "v_mfma_f32_16x16x32_bf16 v[172:175], a[48:51], v[148:151], v[172:175]\n"
      "s_waitcnt vmcnt(11) lgkmcnt(2)\n"
      "v_cmp_ne_u32 vcc, s21, v28\n"
      "v_mfma_f32_16x16x32_bf16 v[172:175], a[52:55], v[152:155], v[172:175]\n"
      "v_mfma_f32_16x16x32_bf16 v[172:175], a[56:59], v[156:159], v[172:175]\n"
      "v_mfma_f32_16x16x32_bf16 v[172:175], a[60:63], v[160:163], v[172:175]\n"
      "v_mfma_f32_16x16x32_bf16 v[172:175], a[64:67], v[164:167], v[172:175]\n"
      "s_cbranch_vccnz .Lsp_v0_25\n"
      ".Lbk_v0_25:\n"
      "v_mfma_f32_16x16x32_bf16 v[172:175], a[68:71], v[168:171], v[172:175]\n"
      "buffer_load_dwordx4 v0, s[8:11], s46 offen offset:0 sc1 lds\n"
      "v_mfma_f32_16x16x32_bf16 v[176:179], a[72:75], v[100:103], v[176:179]\n"
      "s_waitcnt vmcnt(7) lgkmcnt(0)\n"
      "s_min_u32 s36, s36, s6\n"
      "v_lshlrev_b32 v186, 16, v199\n"
      "v_mfma_f32_16x16x32_bf16 v[176:179], a[76:79], v[104:107], v[176:179]\n"
      "v_and_b32 v181, 0xffff0000, v196\n"
      "v_lshlrev_b32 v184, 16, v198\n"
      "v_mfma_f32_16x16x32_bf16 v[176:179], a[80:83], v[108:111], v[176:179]\n"
      "s_add_u32 s38, s38, 24576\n"
      "ds_read_b32 v22, v24\n"
      "v_lshlrev_b32 v180, 16, v196\n"
      "v_mfma_f32_16x16x32_bf16 v[176:179], a[84:87], v[112:115], v[176:179]\n"
      "buffer_load_dword v28, v11, s[8:11], s36 offen sc1\n"
      "v_mfma_f32_16x16x32_bf16 v[176:179], a[88:91], v[116:119], v[176:179]\n"
      "v_cvt_pk_bf16_f32 v209, v174, v175\n"
      "v_mfma_f32_16x16x32_bf16 v[176:179], a[92:95], v[120:123], v[176:179]\n"
      "buffer_load_dwordx4 v0, s[8:11], s46 offen offset:2048 sc1 lds\n"
      "v_mfma_f32_16x16x32_bf16 v[176:179], a[96:99], v[124:127], v[176:179]\n"
      "v_mul_f32 v219, v172, v172\n"
      "v_and_b32 v185, 0xffff0000, v198\n"
      "v_mfma_f32_16x16x32_bf16 v[176:179], a[100:103], v[128:131], v[176:179]\n"
      "s_waitcnt lgkmcnt(0)\n"
      "v_cmp_gt_i32 vcc, v21, v22\n"
      "v_and_b32 v183, 0xffff0000, v197\n"
      "v_mfma_f32_16x16x32_bf16 v[176:179], a[104:107], v[132:135], v[176:179]\n"
      "v_lshlrev_b32 v182, 16, v197\n"
      "v_and_b32 v187, 0xffff0000, v199\n"
      "v_mfma_f32_16x16x32_bf16 v[176:179], a[108:111], v[136:139], v[176:179]\n"
      "buffer_load_dwordx4 v[196:199], v13, s[8:11], s47 offen offset:1024 sc1\n"
      "v_mfma_f32_16x16x32_bf16 v[176:179], a[112:115], v[140:143], v[176:179]\n"
      "v_fmac_f32 v219, v173, v173\n"
      "v_fmac_f32 v219, v174, v174\n"
      "v_mfma_f32_16x16x32_bf16 v[176:179], a[116:119], v[144:147], v[176:179]\n"
      "s_cbranch_vccnz .Lsp_vz0_26\n"
      ".Lbk_vz0_26:\n"
      "ds_read_b128 v[204:207], v19\n"
      "v_mfma_f32_16x16x32_bf16 v[176:179], a[120:123], v[148:151], v[176:179]\n"
      "v_mfma_f32_16x16x32_bf16 v[176:179], a[124:127], v[152:155], v[176:179]\n"
      "v_add_u32 v21, 1, v21\n"
      "v_mfma_f32_16x16x32_bf16 v[176:179], a[128:131], v[156:159], v[176:179]\n"
      "buffer_load_dwordx4 v0, s[8:11], s46 offen offset:1024 sc1 lds\n"
      "v_mfma_f32_16x16x32_bf16 v[176:179], a[132:135], v[160:163], v[176:179]\n"
      "v_add_f32 v218, v172, v173\n"
      "v_mfma_f32_16x16x32_bf16 v[176:179], a[136:139], v[164:167], v[176:179]\n"
      "v_fmac_f32 v219, v175, v175\n"
      "v_mfma_f32_16x16x32_bf16 v[176:179], a[140:143], v[168:171], v[176:179]\n"
      "s_waitcnt vmcnt(10)\n"
      "ds_write_b32 v23, v21\n"
      "v_mfma_f32_16x16x32_bf16 v[180:183], a[144:147], v[100:103], v[180:183]\n"
      "s_waitcnt lgkmcnt(1)\n"
      "v_add_f32 v218, v218, v174\n"
      "v_add_f32 v204, v204, v206\n"
      "v_mfma_f32_16x16x32_bf16 v[180:183], a[148:151], v[104:107], v[180:183]\n"
      "buffer_load_dwordx4 v0, s[8:11], s46 offen offset:3072 sc1 lds\n"
      "v_mfma_f32_16x16x32_bf16 v[180:183], a[152:155], v[108:111], v[180:183]\n"
      "v_add_f32 v205, v205, v207\n"
      "v_mfma_f32_16x16x32_bf16 v[180:183], a[156:159], v[112:115], v[180:183]\n"
      "v_cvt_pk_bf16_f32 v210, v176, v177\n"
      "v_mfma_f32_16x16x32_bf16 v[180:183], a[160:163], v[116:119], v[180:183]\n"
      "v_add_f32_dpp v205, v205, v205 quad_perm:[1,0,3,2] row_mask:0xf bank_mask:0xf\n"
      "v_mfma_f32_16x16x32_bf16 v[180:183], a[164:167], v[120:123], v[180:183]\n"
      "v_fmac_f32 v219, v176, v176\n"
      "s_add_u32 m0, s40, 0\n"
      "v_mfma_f32_16x16x32_bf16 v[180:183], a[168:171], v[124:127], v[180:183]\n"
      "buffer_load_dwordx4 v1, s[8:11], s46 offen offset:0 sc1 lds\n"
      "v_mfma_f32_16x16x32_bf16 v[180:183], a[172:175], v[128:131], v[180:183]\n"
      "v_add_f32_dpp v205, v205, v205 quad_perm:[2,3,0,1] row_mask:0xf bank_mask:0xf\n"
      "v_add_f32_dpp v204, v204, v204 quad_perm:[1,0,3,2] row_mask:0xf bank_mask:0xf\n"
      "v_mfma_f32_16x16x32_bf16 v[180:183], a[176:179], v[132:135], v[180:183]\n"
      "v_add_f32_dpp v204, v204, v204 quad_perm:[2,3,0,1] row_mask:0xf bank_mask:0xf\n"
      "v_add_f32_dpp v205, v205, v205 row_half_mirror row_mask:0xf bank_mask:0xf\n"
      "v_mfma_f32_16x16x32_bf16 v[180:183], a[180:183], v[136:139], v[180:183]\n"
      "ds_read_b32 v22, v24\n"
      "v_add_f32_dpp v204, v204, v204 row_half_mirror row_mask:0xf bank_mask:0xf\n"
      "v_mfma_f32_16x16x32_bf16 v[180:183], a[184:187], v[140:143], v[180:183]\n"
      "s_waitcnt vmcnt(10) lgkmcnt(1)\n"
      "buffer_store_dword v25, v12, s[8:11], s50 offen sc1\n"
      "v_mfma_f32_16x16x32_bf16 v[180:183], a[188:191], v[144:147], v[180:183]\n"
      "v_add_f32 v218, v218, v175\n"
      "v_add_f32 v218, v218, v176\n"
      "v_mfma_f32_16x16x32_bf16 v[180:183], a[192:195], v[148:151], v[180:183]\n"
      "v_mul_f32 v204, v27, v204\n"
      "v_mfma_f32_16x16x32_bf16 v[180:183], a[196:199], v[152:155], v[180:183]\n"
      "v_fmaak_f32 v205, v27, v205, 0x36a7c5ac\n"
      "v_add_f32 v218, v218, v177\n"
      "v_mfma_f32_16x16x32_bf16 v[180:183], a[200:203], v[156:159], v[180:183]\n"
      "buffer_store_dwordx2 v[204:205], v20, s[8:11], s49 offen sc1\n"
      "v_mfma_f32_16x16x32_bf16 v[180:183], a[204:207], v[160:163], v[180:183]\n"
      "s_waitcnt lgkmcnt(0)\n"
      "v_fmac_f32 v219, v177, v177\n"
      "v_cmp_gt_i32 vcc, v21, v22\n"
      "v_mfma_f32_16x16x32_bf16 v[180:183], a[208:211], v[164:167], v[180:183]\n"
      "s_waitcnt vmcnt(10)\n"
      "v_fmac_f32 v219, v178, v178\n"
      "v_and_b32 v175, 0xffff0000, v193\n"
      "v_mfma_f32_16x16x32_bf16 v[180:183], a[212:215], v[168:171], v[180:183]\n"
      "v_lshlrev_b32 v176, 16, v194\n"
      "v_mfma_f32_16x16x32_bf16 v[184:187], a[216:219], v[100:103], v[184:187]\n"
      "v_add_f32 v218, v218, v178\n"
      "v_cvt_pk_bf16_f32 v211, v178, v179\n"
      "v_mfma_f32_16x16x32_bf16 v[184:187], a[220:223], v[104:107], v[184:187]\n"
      "s_cbranch_vccnz .Lsp_vy1_27\n"
      ".Lbk_vy1_27:\n"
      "ds_read_b128 v[100:103], v10 offset:28672\n"
      "v_mfma_f32_16x16x32_bf16 v[184:187], a[224:227], v[108:111], v[184:187]\n"
      ".Lv_p1:\n"
      "s_cbranch_execz .Lv_pr\n"
      "v_cvt_pk_bf16_f32 v208, v172, v173\n"
      "v_mfma_f32_16x16x32_bf16 v[184:187], a[228:231], v[112:115], v[184:187]\n"
      "ds_read_b128 v[112:115], v10 offset:31744\n"
      "v_fmac_f32 v219, v179, v179\n"
      "v_mfma_f32_16x16x32_bf16 v[184:187], a[232:235], v[116:119], v[184:187]\n"
      "ds_read_b128 v[104:107], v10 offset:29696\n"
      "v_fmac_f32 v219, v180, v180\n"
      "v_mfma_f32_16x16x32_bf16 v[184:187], a[236:239], v[120:123], v[184:187]\n"
      "v_and_b32 v177, 0xffff0000, v194\n"
      "v_mfma_f32_16x16x32_bf16 v[184:187], a[240:243], v[124:127], v[184:187]\n"
      "ds_read_b128 v[108:111], v10 offset:30720\n"
      "v_fmac_f32 v219, v181, v181\n"
      "v_mfma_f32_16x16x32_bf16 v[184:187], a[244:247], v[128:131], v[184:187]\n"
      "ds_read_b128 v[116:119], v10 offset:32768\n"
      "v_add_f32 v218, v218, v179\n"
      "v_mfma_f32_16x16x32_bf16 v[184:187], a[248:251], v[132:135], v[184:187]\n"
      "v_cvt_pk_bf16_f32 v213, v182, v183\n"
      "v_mfma_f32_16x16x32_bf16 v[184:187], a[252:255], v[136:139], v[184:187]\n"
      "ds_read_b128 v[124:127], v10 offset:34816\n"
      "v_add_f32 v218, v218, v180\n"
      "v_mfma_f32_16x16x32_bf16 v[184:187], v[32:35], v[140:143], v[184:187]\n"
      "ds_read_b128 v[128:131], v10 offset:35840\n"
      "v_add_f32 v218, v218, v181\n"
      "v_mfma_f32_16x16x32_bf16 v[184:187], v[36:39], v[144:147], v[184:187]\n"
      "buffer_store_dwordx4 v[208:211], v15, s[8:11], s38 offen sc1\n"
      "v_mfma_f32_16x16x32_bf16 v[184:187], v[40:43], v[148:151], v[184:187]\n"
      "ds_read2_b32 v[132:133], v8 offset0:0 offset1:32\n"
      "v_fmac_f32 v219, v182, v182\n"
      "v_mfma_f32_16x16x32_bf16 v[184:187], v[44:47], v[152:155], v[184:187]\n"
      "v_and_b32 v173, 0xffff0000, v192\n"
      "v_mfma_f32_16x16x32_bf16 v[184:187], v[48:51], v[156:159], v[184:187]\n"
      "ds_read_b128 v[120:123], v10 offset:33792\n"
      "v_and_b32 v179, 0xffff0000, v195\n"
      "v_mfma_f32_16x16x32_bf16 v[184:187], v[52:55], v[160:163], v[184:187]\n"
      "s_sub_u32 s47, s46, s45\n"
      "ds_read2_b32 v[134:135], v8 offset0:64 offset1:96\n"
      "v_lshlrev_b32 v172, 16, v192\n"
      "v_mfma_f32_16x16x32_bf16 v[184:187], v[56:59], v[164:167], v[184:187]\n"
      "ds_read_b128 v[160:163], v10 offset:26624\n"
      "v_lshlrev_b32 v174, 16, v193\n"
      "v_mfma_f32_16x16x32_bf16 v[184:187], v[60:63], v[168:171], v[184:187]\n"
      "s_waitcnt lgkmcnt(4)\n"
      "ds_read_b128 v[144:147], v10 offset:22528\n"
      "v_lshlrev_b32 v178, 16, v195\n"
      "v_mfma_f32_16x16x32_bf16 v[188:191], v[64:67], v[100:103], 0\n"
      "v_add_f32 v218, v218, v182\n"
      "v_mfma_f32_16x16x32_bf16 v[188:191], v[68:71], v[104:107], v[188:191]\n"
      "ds_read_b128 v[148:151], v10 offset:23552\n"
      "v_cvt_pk_bf16_f32 v212, v180, v181\n"
      "v_mfma_f32_16x16x32_bf16 v[188:191], v[72:75], v[108:111], v[188:191]\n"
      "ds_read_b128 v[156:159], v10 offset:25600\n"
      "v_fmac_f32 v219, v183, v183\n"
      "v_mfma_f32_16x16x32_bf16 v[188:191], v[76:79], v[112:115], v[188:191]\n"
      "ds_read_b128 v[152:155], v10 offset:24576\n"
      "v_add_f32 v218, v218, v183\n"
      "v_mfma_f32_16x16x32_bf16 v[188:191], v[80:83], v[116:119], v[188:191]\n"
      "s_waitcnt lgkmcnt(5)\n"
      "ds_read2_b32 v[168:169], v9 offset0:0 offset1:32\n"
      "v_mfma_f32_16x16x32_bf16 v[188:191], v[84:87], v[120:123], v[188:191]\n"
      "s_cmp_eq_u32 s36, s37\n"
      "s_cselect_b32 s48, 49152, s51\n"
      "ds_read2_b32 v[170:171], v9 offset0:64 offset1:96\n"
      "v_mfma_f32_16x16x32_bf16 v[188:191], v[88:91], v[124:127], v[188:191]\n"
      "buffer_load_dwordx4 v[192:195], v13, s[8:11], s47 offen sc1\n"
      "v_mfma_f32_16x16x32_bf16 v[188:191], v[92:95], v[128:131], v[188:191]\n"
      "v_add_f32 v218, v218, v184\n"
      "v_fmac_f32 v219, v184, v184\n"
      "v_mfma_f32_16x16x32_bf16 v[188:191], v[96:99], v[132:135], v[188:191]\n"
      "ds_read_b128 v[136:139], v10 offset:20480\n"
      "v_mfma_f32_16x16x32_bf16 v[172:175], a[0:3], v[100:103], v[172:175]\n"
      "v_add_f32 v218, v218, v185\n"
      "s_add_u32 m0, s39, 20480\n"
      "v_mfma_f32_16x16x32_bf16 v[172:175], a[4:7], v[104:107], v[172:175]\n"
      "ds_read_b128 v[164:167], v10 offset:27648\n"
      "v_cvt_pk_bf16_f32 v214, v184, v185\n"
      "v_mfma_f32_16x16x32_bf16 v[172:175], a[8:11], v[108:111], v[172:175]\n"
      "ds_read_b128 v[140:143], v10 offset:21504\n"
      "v_fmac_f32 v219, v185, v185\n"
      "v_mfma_f32_16x16x32_bf16 v[172:175], a[12:15], v[112:115], v[172:175]\n"
      "v_fmac_f32 v219, v186, v186\n"
      "v_fmac_f32 v219, v187, v187\n"
      "v_mfma_f32_16x16x32_bf16 v[172:175], a[16:19], v[116:119], v[172:175]\n"
      "ds_write_b128 v17, v[188:191]\n"
      "v_mfma_f32_16x16x32_bf16 v[172:175], a[20:23], v[120:123], v[172:175]\n"
      "v_cvt_pk_bf16_f32 v215, v186, v187\n"
      "v_add_f32 v218, v218, v186\n"
      "v_mfma_f32_16x16x32_bf16 v[172:175], a[24:27], v[124:127], v[172:175]\n"
      "buffer_store_dwordx4 v[212:215], v15, s[8:11], s38 offen offset:1024 sc1\n"
      "v_mfma_f32_16x16x32_bf16 v[172:175], a[28:31], v[128:131], v[172:175]\n"
      "v_add_f32 v218, v218, v187\n"
      "v_mfma_f32_16x16x32_bf16 v[172:175], a[32:35], v[132:135], v[172:175]\n"
      "s_waitcnt lgkmcnt(2)\n"
      "ds_write_b64 v18, v[218:219]\n"
      "v_mfma_f32_16x16x32_bf16 v[172:175], a[36:39], v[136:139], v[172:175]\n"
      "s_waitcnt lgkmcnt(2)\n"
      "v_add_u32 v21, 1, v21\n"
      "s_mov_b32 s46, s36\n"
      "v_mfma_f32_16x16x32_bf16 v[172:175], a[40:43], v[140:143], v[172:175]\n"
      "s_waitcnt vmcnt(8)\n"
      "v_and_b32 v181, 0xffff0000, v196\n"
      "v_mfma_f32_16x16x32_bf16 v[172:175], a[44:47], v[144:147], v[172:175]\n"
      "ds_write_b32 v23, v21\n"
      "v_mfma_f32_16x16x32_bf16 v[172:175], a[48:51], v[148:151], v[172:175]\n"
      "v_cmp_ne_u32 vcc, s21, v28\n"
      "v_mfma_f32_16x16x32_bf16 v[172:175], a[52:55], v[152:155], v[172:175]\n"
      "v_lshlrev_b32 v186, 16, v199\n"
      "s_add_u32 s36, s36, s48\n"
      "v_mfma_f32_16x16x32_bf16 v[172:175], a[56:59], v[156:159], v[172:175]\n"
      "v_lshlrev_b32 v180, 16, v196\n"
      "v_lshlrev_b32_e64 v184, 16, v198\n"
      "v_mfma_f32_16x16x32_bf16 v[172:175], a[60:63], v[160:163], v[172:175]\n"
      "s_add_u32 s49, s49, 24576\n"
      "v_and_b32 v187, 0xffff0000, v199\n"
      "v_mfma_f32_16x16x32_bf16 v[172:175], a[64:67], v[164:167], v[172:175]\n"
      "s_cbranch_vccnz .Lsp_v1_28\n"
      ".Lbk_v1_28:\n"
      "s_min_u32 s36, s36, s6\n"
      "v_mfma_f32_16x16x32_bf16 v[172:175], a[68:71], v[168:171], v[172:175]\n"
      "buffer_load_dword v28, v11, s[8:11], s36 offen sc1\n"
      "v_mfma_f32_16x16x32_bf16 v[176:179], a[72:75], v[100:103], v[176:179]\n"
      "v_mfma_f32_16x16x32_bf16 v[176:179], a[76:79], v[104:107], v[176:179]\n"
      "buffer_load_dwordx4 v0, s[8:11], s46 offen offset:1024 sc1 lds\n"
      "v_mfma_f32_16x16x32_bf16 v[176:179], a[80:83], v[108:111], v[176:179]\n"
      "ds_read_b32 v22, v24\n"
      "v_mfma_f32_16x16x32_bf16 v[176:179], a[84:87], v[112:115], v[176:179]\n"
      "v_and_b32 v183, 0xffff0000, v197\n"
      "v_mfma_f32_16x16x32_bf16 v[176:179], a[88:91], v[116:119], v[176:179]\n"
      "v_mfma_f32_16x16x32_bf16 v[176:179], a[92:95], v[120:123], v[176:179]\n"
      "buffer_load_dwordx4 v0, s[8:11], s46 offen offset:0 sc1 lds\n"
      "v_mfma_f32_16x16x32_bf16 v[176:179], a[96:99], v[124:127], v[176:179]\n"
      "v_cvt_pk_bf16_f32 v208, v172, v173\n"
      "v_mfma_f32_16x16x32_bf16 v[176:179], a[100:103], v[128:131], v[176:179]\n"
      "v_mfma_f32_16x16x32_bf16 v[176:179], a[104:107], v[132:135], v[176:179]\n"
      "v_mfma_f32_16x16x32_bf16 v[176:179], a[108:111], v[136:139], v[176:179]\n"
      "s_waitcnt lgkmcnt(0)\n"
      "v_cmp_gt_i32 vcc, v21, v22\n"
      "v_lshlrev_b32 v182, 16, v197\n"
      "v_mfma_f32_16x16x32_bf16 v[176:179], a[112:115], v[140:143], v[176:179]\n"
      "buffer_load_dwordx4 v0, s[8:11], s46 offen offset:3072 sc1 lds\n"
      "v_mfma_f32_16x16x32_bf16 v[176:179], a[116:119], v[144:147], v[176:179]\n"
      "v_mul_f32 v219, v172, v172\n"
      "v_fmac_f32 v219, v173, v173\n"
      "v_mfma_f32_16x16x32_bf16 v[176:179], a[120:123], v[148:151], v[176:179]\n"
      "s_cbranch_vccnz .Lsp_vz1_29\n"
      ".Lbk_vz1_29:\n"
      "ds_read_b128 v[204:207], v19\n"
      "v_mfma_f32_16x16x32_bf16 v[176:179], a[124:127], v[152:155], v[176:179]\n"
      "v_and_b32 v185, 0xffff0000, v198\n"
      "v_add_f32 v218, v172, v173\n"
      "v_mfma_f32_16x16x32_bf16 v[176:179], a[128:131], v[156:159], v[176:179]\n"
      "v_add_u32 v21, 1, v21\n"
      "v_mfma_f32_16x16x32_bf16 v[176:179], a[132:135], v[160:163], v[176:179]\n"
      "buffer_load_dwordx4 v0, s[8:11], s46 offen offset:2048 sc1 lds\n"
      "v_mfma_f32_16x16x32_bf16 v[176:179], a[136:139], v[164:167], v[176:179]\n"
      "s_waitcnt vmcnt(10)\n"
      "ds_write_b32 v23, v21\n"
      "v_mfma_f32_16x16x32_bf16 v[176:179], a[140:143], v[168:171], v[176:179]\n"
      "v_mfma_f32_16x16x32_bf16 v[180:183], a[144:147], v[100:103], v[180:183]\n"
      "s_add_u32 m0, s40, 20480\n"
      "v_add_f32 v218, v218, v174\n"
      "v_mfma_f32_16x16x32_bf16 v[180:183], a[148:151], v[104:107], v[180:183]\n"
      "buffer_load_dwordx4 v[196:199], v13, s[8:11], s47 offen offset:1024 sc1\n"
      "v_mfma_f32_16x16x32_bf16 v[180:183], a[152:155], v[108:111], v[180:183]\n"
      "s_waitcnt lgkmcnt(1)\n"
      "v_add_f32 v205, v205, v207\n"
      "v_add_f32 v204, v204, v206\n"
      "v_mfma_f32_16x16x32_bf16 v[180:183], a[156:159], v[112:115], v[180:183]\n"
      "s_nop 0\n"
      "v_add_f32_dpp v204, v204, v204 quad_perm:[1,0,3,2] row_mask:0xf bank_mask:0xf\n"
      "v_fmac_f32 v219, v174, v174\n"
      "v_mfma_f32_16x16x32_bf16 v[180:183], a[160:163], v[116:119], v[180:183]\n"
      "v_add_f32_dpp v205, v205, v205 quad_perm:[1,0,3,2] row_mask:0xf bank_mask:0xf\n"
      "v_add_f32 v218, v218, v175\n"
      "v_mfma_f32_16x16x32_bf16 v[180:183], a[164:167], v[120:123], v[180:183]\n"
      "buffer_load_dwordx4 v1, s[8:11], s46 offen offset:0 sc1 lds\n"
      "v_mfma_f32_16x16x32_bf16 v[180:183], a[168:171], v[124:127], v[180:183]\n"
      "v_add_f32_dpp v204, v204, v204 quad_perm:[2,3,0,1] row_mask:0xf bank_mask:0xf\n"
      "v_add_f32 v218, v218, v176\n"
      "v_mfma_f32_16x16x32_bf16 v[180:183], a[172:175], v[128:131], v[180:183]\n"
      "v_fmac_f32 v219, v175, v175\n"
      "v_mfma_f32_16x16x32_bf16 v[180:183], a[176:179], v[132:135], v[180:183]\n"
      "s_add_u32 s38, s38, 24576\n"
      "v_add_f32_dpp v205, v205, v205 quad_perm:[2,3,0,1] row_mask:0xf bank_mask:0xf\n"
      "v_mfma_f32_16x16x32_bf16 v[180:183], a[180:183], v[136:139], v[180:183]\n"
      "s_add_u32 s50, s50, 24576\n"
      "v_add_f32_dpp v205, v205, v205 row_half_mirror row_mask:0xf bank_mask:0xf\n"
      "v_fmac_f32 v219, v176, v176\n"
      "v_mfma_f32_16x16x32_bf16 v[180:183], a[184:187], v[140:143], v[180:183]\n"
      "ds_read_b32 v22, v24\n"
      "v_mfma_f32_16x16x32_bf16 v[180:183], a[188:191], v[144:147], v[180:183]\n"
      "v_add_f32_dpp v204, v204, v204 row_half_mirror row_mask:0xf bank_mask:0xf\n"
      "v_mfma_f32_16x16x32_bf16 v[180:183], a[192:195], v[148:151], v[180:183]\n"
      "s_waitcnt vmcnt(10) lgkmcnt(1)\n"
      "buffer_store_dword v25, v12, s[8:11], s50 offen sc1\n"
      "v_mfma_f32_16x16x32_bf16 v[180:183], a[196:199], v[152:155], v[180:183]\n"
      "v_fmaak_f32 v205, v27, v205, 0x36a7c5ac\n"
      "v_mul_f32 v204, v27, v204\n"
      "v_mfma_f32_16x16x32_bf16 v[180:183], a[200:203], v[156:159], v[180:183]\n"
      "buffer_store_dwordx2 v[204:205], v20, s[8:11], s49 offen sc1\n"
      "v_mfma_f32_16x16x32_bf16 v[180:183], a[204:207], v[160:163], v[180:183]\n"
      "s_waitcnt lgkmcnt(0)\n"
      "v_cvt_pk_bf16_f32 v209, v174, v175\n"
      "v_cmp_gt_i32 vcc, v21, v22\n"
      "v_mfma_f32_16x16x32_bf16 v[180:183], a[208:211], v[164:167], v[180:183]\n"
      "s_sub_u32 s47, s46, s45\n"
      "v_add_f32 v218, v218, v177\n"
      "v_fmac_f32 v219, v177, v177\n"
      "v_mfma_f32_16x16x32_bf16 v[180:183], a[212:215], v[168:171], v[180:183]\n"
      "v_cvt_pk_bf16_f32 v210, v176, v177\n"
      "v_add_f32 v218, v218, v178\n"
      "v_mfma_f32_16x16x32_bf16 v[184:187], a[216:219], v[100:103], v[184:187]\n"
      "s_cbranch_vccnz .Lsp_vy0_30\n"
      ".Lbk_vy0_30:\n"
      "v_mfma_f32_16x16x32_bf16 v[184:187], a[220:223], v[104:107], v[184:187]\n"
      "ds_read_b128 v[100:103], v0 offset:8192\n"
      "v_add_f32 v218, v218, v179\n"
      "v_mfma_f32_16x16x32_bf16 v[184:187], a[224:227], v[108:111], v[184:187]\n"
      "v_fmac_f32 v219, v178, v178\n"
      "v_fmac_f32 v219, v179, v179\n"
      "v_mfma_f32_16x16x32_bf16 v[184:187], a[228:231], v[112:115], v[184:187]\n"
      "ds_read_b128 v[112:115], v0 offset:11264\n"
      "v_fmac_f32 v219, v180, v180\n"
      "v_mfma_f32_16x16x32_bf16 v[184:187], a[232:235], v[116:119], v[184:187]\n"
      "ds_read_b128 v[104:107], v0 offset:9216\n"
      "v_fmac_f32 v219, v181, v181\n"
      "v_mfma_f32_16x16x32_bf16 v[184:187], a[236:239], v[120:123], v[184:187]\n"
      "ds_read_b128 v[108:111], v0 offset:10240\n"
      "v_mfma_f32_16x16x32_bf16 v[184:187], a[240:243], v[124:127], v[184:187]\n"
      "s_waitcnt vmcnt(10)\n"
      "ds_read_b128 v[124:127], v0 offset:14336\n"
      "v_lshlrev_b32 v172, 16, v192\n"
      "v_mfma_f32_16x16x32_bf16 v[184:187], a[244:247], v[128:131], v[184:187]\n"
      "v_cvt_pk_bf16_f32 v211, v178, v179\n"
      "v_lshlrev_b32 v176, 16, v194\n"
      "v_mfma_f32_16x16x32_bf16 v[184:187], a[248:251], v[132:135], v[184:187]\n"
      "ds_read_b128 v[116:119], v0 offset:12288\n"
      "v_add_f32 v218, v218, v180\n"
      "v_mfma_f32_16x16x32_bf16 v[184:187], a[252:255], v[136:139], v[184:187]\n"
      "v_cvt_pk_bf16_f32 v213, v182, v183\n"
      "v_and_b32 v173, 0xffff0000, v192\n"
      "v_mfma_f32_16x16x32_bf16 v[184:187], v[32:35], v[140:143], v[184:187]\n"
      "ds_read_b128 v[120:123], v0 offset:13312\n"
      "v_fmac_f32 v219, v182, v182\n"
      "v_mfma_f32_16x16x32_bf16 v[184:187], v[36:39], v[144:147], v[184:187]\n"
      "ds_read_b128 v[128:131], v0 offset:15360\n"
      "v_lshlrev_b32 v178, 16, v195\n"
      "v_mfma_f32_16x16x32_bf16 v[184:187], v[40:43], v[148:151], v[184:187]\n"
      "ds_read2_b32 v[132:133], v2 offset0:0 offset1:32\n"
      "v_and_b32 v175, 0xffff0000, v193\n"
      "v_mfma_f32_16x16x32_bf16 v[184:187], v[44:47], v[152:155], v[184:187]\n"
      "buffer_store_dwordx4 v[208:211], v15, s[8:11], s38 offen sc1\n"
      "v_mfma_f32_16x16x32_bf16 v[184:187], v[48:51], v[156:159], v[184:187]\n"
      "ds_read_b128 v[144:147], v0 offset:2048\n"
      "v_add_f32 v218, v218, v181\n"
      "v_mfma_f32_16x16x32_bf16 v[184:187], v[52:55], v[160:163], v[184:187]\n"
      "v_add_f32 v218, v218, v182\n"
      "v_and_b32 v179, 0xffff0000, v195\n"
      "v_mfma_f32_16x16x32_bf16 v[184:187], v[56:59], v[164:167], v[184:187]\n"
      "s_waitcnt lgkmcnt(4)\n"
      "v_add_f32 v218, v218, v183\n"
      "v_lshlrev_b32 v174, 16, v193\n"
      "v_mfma_f32_16x16x32_bf16 v[184:187], v[60:63], v[168:171], v[184:187]\n"
      "s_sub_u32 s20, s20, 1\n"
      "s_cbranch_scc0 .Lv_loop\n"
      "s_nop 8\n"
      "v_cvt_pk_bf16_f32 v215, v186, v187\n"
      "v_cvt_pk_bf16_f32 v212, v180, v181\n"
      "s_add_u32 s49, s49, 24576\n"
      "v_fmac_f32 v219, v183, v183\n"
      "v_add_f32 v218, v218, v184\n"
      "v_add_f32 v218, v218, v185\n"
      "v_cvt_pk_bf16_f32 v214, v184, v185\n"
      "v_fmac_f32 v219, v184, v184\n"
      "v_fmac_f32 v219, v185, v185\n"
      "v_add_f32 v218, v218, v186\n"
      "s_add_u32 s50, s50, 24576\n"
      "v_add_f32 v218, v218, v187\n"
      "v_fmac_f32 v219, v186, v186\n"
      "v_fmac_f32 v219, v187, v187\n"
      "ds_write_b64 v18, v[218:219]\n"
      "s_waitcnt lgkmcnt(0)\n"
      "s_barrier\n"
      "buffer_store_dwordx4 v[212:215], v15, s[8:11], s38 offen offset:1024 sc1\n"
      "s_add_u32 s38, s38, 24576\n"
      "ds_read_b128 v[204:207], v19\n"
      "s_waitcnt lgkmcnt(0)\n"
      "v_add_f32 v204, v204, v206\n"
      "v_add_f32 v205, v205, v207\n"
      "s_nop 1\n"
      "v_add_f32_dpp v205, v205, v205 quad_perm:[1,0,3,2] row_mask:0xf bank_mask:0xf\n"
      "s_nop 1\n"
      "v_add_f32_dpp v205, v205, v205 quad_perm:[2,3,0,1] row_mask:0xf bank_mask:0xf\n"
      "v_add_f32_dpp v204, v204, v204 quad_perm:[1,0,3,2] row_mask:0xf bank_mask:0xf\n"
      "s_nop 1\n"
      "v_add_f32_dpp v204, v204, v204 quad_perm:[2,3,0,1] row_mask:0xf bank_mask:0xf\n"
      "v_add_f32_dpp v205, v205, v205 row_half_mirror row_mask:0xf bank_mask:0xf\n"
      "s_nop 0\n"
      "v_add_f32_dpp v204, v204, v204 row_half_mirror row_mask:0xf bank_mask:0xf\n"
      "s_waitcnt vmcnt(2)\n"
      "buffer_store_dword v25, v12, s[8:11], s50 offen sc1\n"
      "v_mul_f32 v204, v27, v204\n"
      "v_fmaak_f32 v205, v27, v205, 0x36a7c5ac\n"
      "buffer_store_dwordx2 v[204:205], v20, s[8:11], s49 offen sc1\n"
      "s_add_u32 s49, s49, 24576\n"
      "s_add_u32 s50, s50, 24576\n"
      "s_waitcnt vmcnt(0)\n"
      "buffer_store_dword v25, v12, s[8:11], s50 offen sc1\n"
      "s_add_u32 s50, s50, 24576\n"
      "s_endpgm\n"
      ".Lsp_w0_1:\n"
      "s_mov_b32 s22, s23\n"
      ".Lsl_w0_1:\n"
      "buffer_load_dword v28, v11, s[8:11], s46 offen sc1\n"
      "s_waitcnt vmcnt(0)\n"
      "v_cmp_ne_u32 vcc, s21, v28\n"
      "s_cbranch_vccz .Lbk_w0_1\n"
      "s_sleep 4\n"
      "s_sub_u32 s22, s22, 1\n"
      "s_cbranch_scc0 .Lsl_w0_1\n"
      "s_mov_b32 s23, 0\n"
      "s_branch .Lbk_w0_1\n"
      ".Lsp_w1_2:\n"
      "s_mov_b32 s22, s23\n"
      ".Lsl_w1_2:\n"
      "buffer_load_dword v28, v11, s[8:11], s46 offen sc1\n"
      "s_waitcnt vmcnt(0)\n"
      "v_cmp_ne_u32 vcc, s21, v28\n"
      "s_cbranch_vccz .Lbk_w1_2\n"
      "s_sleep 4\n"
      "s_sub_u32 s22, s22, 1\n"
      "s_cbranch_scc0 .Lsl_w1_2\n"
      "s_mov_b32 s23, 0\n"
      "s_branch .Lbk_w1_2\n"
      ".Lsp_wy0_3:\n"
      "s_lshl_b32 s22, s23, 4\n"
      ".Lsl_wy0_3:\n"
      "ds_read_b32 v22, v24\n"
      "s_waitcnt lgkmcnt(0)\n"
      "v_cmp_gt_i32 vcc, v21, v22\n"
      "s_cbranch_vccz .Lbk_wy0_3\n"
      "s_sub_u32 s22, s22, 1\n"
      "s_cbranch_scc0 .Lsl_wy0_3\n"
      "s_mov_b32 s23, 0\n"
      "s_branch .Lbk_wy0_3\n"
      ".Lsp_w0_4:\n"
      "s_mov_b32 s22, s23\n"
      "buffer_load_dword v28, v11, s[8:11], s46 offen sc1\n"
      "s_waitcnt vmcnt(0)\n"
      "v_cmp_ne_u32 vcc, s21, v28\n"
      "s_add_u32 s30, s50, 0\n"
      "buffer_store_dword v25, v12, s[8:11], s30 offen sc1\n"
      "s_add_u32 s30, s50, 24576\n"
      "buffer_store_dword v25, v12, s[8:11], s30 offen sc1\n"
      "s_cbranch_vccz .Lbk_w0_4\n"
      ".Lsl_w0_4:\n"
      "buffer_load_dword v28, v11, s[8:11], s46 offen sc1\n"
      "s_waitcnt vmcnt(0)\n"
      "v_cmp_ne_u32 vcc, s21, v28\n"
      "s_cbranch_vccz .Lbk_w0_4\n"
      "s_sleep 4\n"
      "s_sub_u32 s22, s22, 1\n"
      "s_cbranch_scc0 .Lsl_w0_4\n"
      "s_mov_b32 s23, 0\n"
      "s_branch .Lbk_w0_4\n"
      ".Lsp_wz0_5:\n"
      "s_lshl_b32 s22, s23, 4\n"
      ".Lsl_wz0_5:\n"
      "ds_read_b32 v22, v24\n"
      "s_waitcnt lgkmcnt(0)\n"
      "v_cmp_gt_i32 vcc, v21, v22\n"
      "s_cbranch_vccz .Lbk_wz0_5\n"
      "s_sub_u32 s22, s22, 1\n"
      "s_cbranch_scc0 .Lsl_wz0_5\n"
      "s_mov_b32 s23, 0\n"
      "s_branch .Lbk_wz0_5\n"
      ".Lsp_wy1_6:\n"
      "s_lshl_b32 s22, s23, 4\n"
      ".Lsl_wy1_6:\n"
      "ds_read_b32 v22, v24\n"
      "s_waitcnt lgkmcnt(0)\n"
      "v_cmp_gt_i32 vcc, v21, v22\n"
      "s_cbranch_vccz .Lbk_wy1_6\n"
      "s_sub_u32 s22, s22, 1\n"
      "s_cbranch_scc0 .Lsl_wy1_6\n"
      "s_mov_b32 s23, 0\n"
      "s_branch .Lbk_wy1_6\n"
      ".Lsp_w1_7:\n"
      "s_mov_b32 s22, s23\n"
      "buffer_load_dword v28, v11, s[8:11], s46 offen sc1\n"
      "s_waitcnt vmcnt(0)\n"
      "v_cmp_ne_u32 vcc, s21, v28\n"
      "s_add_u32 s30, s50, 24576\n"
      "buffer_store_dword v25, v12, s[8:11], s30 offen sc1\n"
      "s_cbranch_vccz .Lbk_w1_7\n"
      ".Lsl_w1_7:\n"
      "buffer_load_dword v28, v11, s[8:11], s46 offen sc1\n"
      "s_waitcnt vmcnt(0)\n"
      "v_cmp_ne_u32 vcc, s21, v28\n"
      "s_cbranch_vccz .Lbk_w1_7\n"
      "s_sleep 4\n"
      "s_sub_u32 s22, s22, 1\n"
      "s_cbranch_scc0 .Lsl_w1_7\n"
      "s_mov_b32 s23, 0\n"
      "s_branch .Lbk_w1_7\n"
      ".Lsp_wz1_8:\n"
      "s_lshl_b32 s22, s23, 4\n"
      ".Lsl_wz1_8:\n"
      "ds_read_b32 v22, v24\n"
      "s_waitcnt lgkmcnt(0)\n"
      "v_cmp_gt_i32 vcc, v21, v22\n"
      "s_cbranch_vccz .Lbk_wz1_8\n"
      "s_sub_u32 s22, s22, 1\n"
      "s_cbranch_scc0 .Lsl_wz1_8\n"
      "s_mov_b32 s23, 0\n"
      "s_branch .Lbk_wz1_8\n"
      ".Lsp_wy0_9:\n"
      "s_lshl_b32 s22, s23, 4\n"
      ".Lsl_wy0_9:\n"
      "ds_read_b32 v22, v24\n"
      "s_waitcnt lgkmcnt(0)\n"
      "v_cmp_gt_i32 vcc, v21, v22\n"
      "s_cbranch_vccz .Lbk_wy0_9\n"
      "s_sub_u32 s22, s22, 1\n"
      "s_cbranch_scc0 .Lsl_wy0_9\n"
      "s_mov_b32 s23, 0\n"
      "s_branch .Lbk_wy0_9\n"
      ".Lsp_w0_10:\n"
      "s_mov_b32 s22, s23\n"
      "buffer_load_dword v28, v11, s[8:11], s46 offen sc1\n"
      "s_waitcnt vmcnt(0)\n"
      "v_cmp_ne_u32 vcc, s21, v28\n"
      "s_add_u32 s30, s50, 0\n"
      "buffer_store_dword v25, v12, s[8:11], s30 offen sc1\n"
      "s_add_u32 s30, s50, 24576\n"
      "buffer_store_dword v25, v12, s[8:11], s30 offen sc1\n"
      "s_cbranch_vccz .Lbk_w0_10\n"
      ".Lsl_w0_10:\n"
      "buffer_load_dword v28, v11, s[8:11], s46 offen sc1\n"
      "s_waitcnt vmcnt(0)\n"
      "v_cmp_ne_u32 vcc, s21, v28\n"
      "s_cbranch_vccz .Lbk_w0_10\n"
      "s_sleep 4\n"
      "s_sub_u32 s22, s22, 1\n"
      "s_cbranch_scc0 .Lsl_w0_10\n"
      "s_mov_b32 s23, 0\n"
      "s_branch .Lbk_w0_10\n"
      ".Lsp_wz0_11:\n"
      "s_lshl_b32 s22, s23, 4\n"
      ".Lsl_wz0_11:\n"
      "ds_read_b32 v22, v24\n"
      "s_waitcnt lgkmcnt(0)\n"
      "v_cmp_gt_i32 vcc, v21, v22\n"
      "s_cbranch_vccz .Lbk_wz0_11\n"
      "s_sub_u32 s22, s22, 1\n"
      "s_cbranch_scc0 .Lsl_wz0_11\n"
      "s_mov_b32 s23, 0\n"
      "s_branch .Lbk_wz0_11\n"
      ".Lsp_wy1_12:\n"
      "s_lshl_b32 s22, s23, 4\n"
      ".Lsl_wy1_12:\n"
      "ds_read_b32 v22, v24\n"
      "s_waitcnt lgkmcnt(0)\n"
      "v_cmp_gt_i32 vcc, v21, v22\n"
      "s_cbranch_vccz .Lbk_wy1_12\n"
      "s_sub_u32 s22, s22, 1\n"
      "s_cbranch_scc0 .Lsl_wy1_12\n"
      "s_mov_b32 s23, 0\n"
      "s_branch .Lbk_wy1_12\n"
      ".Lsp_w1_13:\n"
      "s_mov_b32 s22, s23\n"
      "buffer_load_dword v28, v11, s[8:11], s46 offen sc1\n"
      "s_waitcnt vmcnt(0)\n"
      "v_cmp_ne_u32 vcc, s21, v28\n"
      "s_add_u32 s30, s50, 24576\n"
      "buffer_store_dword v25, v12, s[8:11], s30 offen sc1\n"
      "s_cbranch_vccz .Lbk_w1_13\n"
      ".Lsl_w1_13:\n"
      "buffer_load_dword v28, v11, s[8:11], s46 offen sc1\n"
      "s_waitcnt vmcnt(0)\n"
      "v_cmp_ne_u32 vcc, s21, v28\n"
      "s_cbranch_vccz .Lbk_w1_13\n"
      "s_sleep 4\n"
      "s_sub_u32 s22, s22, 1\n"
      "s_cbranch_scc0 .Lsl_w1_13\n"
      "s_mov_b32 s23, 0\n"
      "s_branch .Lbk_w1_13\n"
      ".Lsp_wz1_14:\n"
      "s_lshl_b32 s22, s23, 4\n"
      ".Lsl_wz1_14:\n"
      "ds_read_b32 v22, v24\n"
      "s_waitcnt lgkmcnt(0)\n"
      "v_cmp_gt_i32 vcc, v21, v22\n"
      "s_cbranch_vccz .Lbk_wz1_14\n"
      "s_sub_u32 s22, s22, 1\n"
      "s_cbranch_scc0 .Lsl_wz1_14\n"
      "s_mov_b32 s23, 0\n"
      "s_branch .Lbk_wz1_14\n"
      ".Lsp_wy0_15:\n"
      "s_lshl_b32 s22, s23, 4\n"
      ".Lsl_wy0_15:\n"
      "ds_read_b32 v22, v24\n"
      "s_waitcnt lgkmcnt(0)\n"
      "v_cmp_gt_i32 vcc, v21, v22\n"
      "s_cbranch_vccz .Lbk_wy0_15\n"
      "s_sub_u32 s22, s22, 1\n"
      "s_cbranch_scc0 .Lsl_wy0_15\n"
      "s_mov_b32 s23, 0\n"
      "s_branch .Lbk_wy0_15\n"
      ".Lsp_v0_16:\n"
      "s_mov_b32 s22, s23\n"
      ".Lsl_v0_16:\n"
      "buffer_load_dword v28, v11, s[8:11], s46 offen sc1\n"
      "s_waitcnt vmcnt(0)\n"
      "v_cmp_ne_u32 vcc, s21, v28\n"
      "s_cbranch_vccz .Lbk_v0_16\n"
      "s_sleep 4\n"
      "s_sub_u32 s22, s22, 1\n"
      "s_cbranch_scc0 .Lsl_v0_16\n"
      "s_mov_b32 s23, 0\n"
      "s_branch .Lbk_v0_16\n"
      ".Lsp_v1_17:\n"
      "s_mov_b32 s22, s23\n"
      ".Lsl_v1_17:\n"
      "buffer_load_dword v28, v11, s[8:11], s46 offen sc1\n"
      "s_waitcnt vmcnt(0)\n"
      "v_cmp_ne_u32 vcc, s21, v28\n"
      "s_cbranch_vccz .Lbk_v1_17\n"
      "s_sleep 4\n"
      "s_sub_u32 s22, s22, 1\n"
      "s_cbranch_scc0 .Lsl_v1_17\n"
      "s_mov_b32 s23, 0\n"
      "s_branch .Lbk_v1_17\n"
      ".Lsp_vy0_18:\n"
      "s_lshl_b32 s22, s23, 4\n"
      ".Lsl_vy0_18:\n"
      "ds_read_b32 v22, v24\n"
      "s_waitcnt lgkmcnt(0)\n"
      "v_cmp_gt_i32 vcc, v21, v22\n"
      "s_cbranch_vccz .Lbk_vy0_18\n"
      "s_sub_u32 s22, s22, 1\n"
      "s_cbranch_scc0 .Lsl_vy0_18\n"
      "s_mov_b32 s23, 0\n"
      "s_branch .Lbk_vy0_18\n"
      ".Lsp_v0_19:\n"
      "s_mov_b32 s22, s23\n"
      "buffer_load_dword v28, v11, s[8:11], s46 offen sc1\n"
      "s_waitcnt vmcnt(0)\n"
      "v_cmp_ne_u32 vcc, s21, v28\n"
      "s_add_u32 s30, s50, 0\n"
      "buffer_store_dword v25, v12, s[8:11], s30 offen sc1\n"
      "s_cbranch_vccz .Lbk_v0_19\n"
      ".Lsl_v0_19:\n"
      "buffer_load_dword v28, v11, s[8:11], s46 offen sc1\n"
      "s_waitcnt vmcnt(0)\n"
      "v_cmp_ne_u32 vcc, s21, v28\n"
      "s_cbranch_vccz .Lbk_v0_19\n"
      "s_sleep 4\n"
      "s_sub_u32 s22, s22, 1\n"
      "s_cbranch_scc0 .Lsl_v0_19\n"
      "s_mov_b32 s23, 0\n"
      "s_branch .Lbk_v0_19\n"
      ".Lsp_vz0_20:\n"
      "s_lshl_b32 s22, s23, 4\n"
      ".Lsl_vz0_20:\n"
      "ds_read_b32 v22, v24\n"
      "s_waitcnt lgkmcnt(0)\n"
      "v_cmp_gt_i32 vcc, v21, v22\n"
      "s_cbranch_vccz .Lbk_vz0_20\n"
      "s_sub_u32 s22, s22, 1\n"
      "s_cbranch_scc0 .Lsl_vz0_20\n"
      "s_mov_b32 s23, 0\n"
      "s_branch .Lbk_vz0_20\n"
      ".Lsp_vy1_21:\n"
      "s_lshl_b32 s22, s23, 4\n"
      ".Lsl_vy1_21:\n"
      "ds_read_b32 v22, v24\n"
      "s_waitcnt lgkmcnt(0)\n"
      "v_cmp_gt_i32 vcc, v21, v22\n"
      "s_cbranch_vccz .Lbk_vy1_21\n"
      "s_sub_u32 s22, s22, 1\n"
      "s_cbranch_scc0 .Lsl_vy1_21\n"
      "s_mov_b32 s23, 0\n"
      "s_branch .Lbk_vy1_21\n"
      ".Lsp_v1_22:\n"
      "s_mov_b32 s22, s23\n"
      "buffer_load_dword v28, v11, s[8:11], s46 offen sc1\n"
      "s_waitcnt vmcnt(0)\n"
      "v_cmp_ne_u32 vcc, s21, v28\n"
      "s_add_u32 s30, s50, 24576\n"
      "buffer_store_dword v25, v12, s[8:11], s30 offen sc1\n"
      "s_cbranch_vccz .Lbk_v1_22\n"
      ".Lsl_v1_22:\n"
      "buffer_load_dword v28, v11, s[8:11], s46 offen sc1\n"
      "s_waitcnt vmcnt(0)\n"
      "v_cmp_ne_u32 vcc, s21, v28\n"
      "s_cbranch_vccz .Lbk_v1_22\n"
      "s_sleep 4\n"
      "s_sub_u32 s22, s22, 1\n"
      "s_cbranch_scc0 .Lsl_v1_22\n"
      "s_mov_b32 s23, 0\n"
      "s_branch .Lbk_v1_22\n"
      ".Lsp_vz1_23:\n"
      "s_lshl_b32 s22, s23, 4\n"
      ".Lsl_vz1_23:\n"
      "ds_read_b32 v22, v24\n"
      "s_waitcnt lgkmcnt(0)\n"
      "v_cmp_gt_i32 vcc, v21, v22\n"
      "s_cbranch_vccz .Lbk_vz1_23\n"
      "s_sub_u32 s22, s22, 1\n"
      "s_cbranch_scc0 .Lsl_vz1_23\n"
      "s_mov_b32 s23, 0\n"
      "s_branch .Lbk_vz1_23\n"
      ".Lsp_vy0_24:\n"
      "s_lshl_b32 s22, s23, 4\n"
      ".Lsl_vy0_24:\n"
      "ds_read_b32 v22, v24\n"
      "s_waitcnt lgkmcnt(0)\n"
      "v_cmp_gt_i32 vcc, v21, v22\n"
      "s_cbranch_vccz .Lbk_vy0_24\n"
      "s_sub_u32 s22, s22, 1\n"
      "s_cbranch_scc0 .Lsl_vy0_24\n"
      "s_mov_b32 s23, 0\n"
      "s_branch .Lbk_vy0_24\n"
      ".Lsp_v0_25:\n"
      "s_mov_b32 s22, s23\n"
      "buffer_load_dword v28, v11, s[8:11], s46 offen sc1\n"
      "s_waitcnt vmcnt(0)\n"
      "v_cmp_ne_u32 vcc, s21, v28\n"
      "s_add_u32 s30, s50, 0\n"
      "buffer_store_dword v25, v12, s[8:11], s30 offen sc1\n"
      "s_cbranch_vccz .Lbk_v0_25\n"
      ".Lsl_v0_25:\n"
      "buffer_load_dword v28, v11, s[8:11], s46 offen sc1\n"
      "s_waitcnt vmcnt(0)\n"
      "v_cmp_ne_u32 vcc, s21, v28\n"
      "s_cbranch_vccz .Lbk_v0_25\n"
      "s_sleep 4\n"
      "s_sub_u32 s22, s22, 1\n"
      "s_cbranch_scc0 .Lsl_v0_25\n"
      "s_mov_b32 s23, 0\n"
      "s_branch .Lbk_v0_25\n"
      ".Lsp_vz0_26:\n"
      "s_lshl_b32 s22, s23, 4\n"
      ".Lsl_vz0_26:\n"
      "ds_read_b32 v22, v24\n"
      "s_waitcnt lgkmcnt(0)\n"
      "v_cmp_gt_i32 vcc, v21, v22\n"
      "s_cbranch_vccz .Lbk_vz0_26\n"
      "s_sub_u32 s22, s22, 1\n"
      "s_cbranch_scc0 .Lsl_vz0_26\n"
      "s_mov_b32 s23, 0\n"
      "s_branch .Lbk_vz0_26\n"
      ".Lsp_vy1_27:\n"
      "s_lshl_b32 s22, s23, 4\n"
      ".Lsl_vy1_27:\n"
      "ds_read_b32 v22, v24\n"
      "s_waitcnt lgkmcnt(0)\n"
      "v_cmp_gt_i32 vcc, v21, v22\n"
      "s_cbranch_vccz .Lbk_vy1_27\n"
      "s_sub_u32 s22, s22, 1\n"
      "s_cbranch_scc0 .Lsl_vy1_27\n"
      "s_mov_b32 s23, 0\n"
      "s_branch .Lbk_vy1_27\n"
      ".Lsp_v1_28:\n"
      "s_mov_b32 s22, s23\n"
      "buffer_load_dword v28, v11, s[8:11], s46 offen sc1\n"
      "s_waitcnt vmcnt(0)\n"
      "v_cmp_ne_u32 vcc, s21, v28\n"
      "s_add_u32 s30, s50, 24576\n"
      "buffer_store_dword v25, v12, s[8:11], s30 offen sc1\n"
      "s_cbranch_vccz .Lbk_v1_28\n"
      ".Lsl_v1_28:\n"
      "buffer_load_dword v28, v11, s[8:11], s46 offen sc1\n"
      "s_waitcnt vmcnt(0)\n"
      "v_cmp_ne_u32 vcc, s21, v28\n"
      "s_cbranch_vccz .Lbk_v1_28\n"
      "s_sleep 4\n"
      "s_sub_u32 s22, s22, 1\n"
      "s_cbranch_scc0 .Lsl_v1_28\n"
      "s_mov_b32 s23, 0\n"
      "s_branch .Lbk_v1_28\n"
      ".Lsp_vz1_29:\n"
      "s_lshl_b32 s22, s23, 4\n"
      ".Lsl_vz1_29:\n"
      "ds_read_b32 v22, v24\n"
      "s_waitcnt lgkmcnt(0)\n"
      "v_cmp_gt_i32 vcc, v21, v22\n"
      "s_cbranch_vccz .Lbk_vz1_29\n"
      "s_sub_u32 s22, s22, 1\n"
      "s_cbranch_scc0 .Lsl_vz1_29\n"
      "s_mov_b32 s23, 0\n"
      "s_branch .Lbk_vz1_29\n"
      ".Lsp_vy0_30:\n"
      "s_lshl_b32 s22, s23, 4\n"
      ".Lsl_vy0_30:\n"
      "ds_read_b32 v22, v24\n"
      "s_waitcnt lgkmcnt(0)\n"
      "v_cmp_gt_i32 vcc, v21, v22\n"
      "s_cbranch_vccz .Lbk_vy0_30\n"
      "s_sub_u32 s22, s22, 1\n"
      "s_cbranch_scc0 .Lsl_vy0_30\n"
      "s_mov_b32 s23, 0\n"
      "s_branch .Lbk_vy0_30\n"
      ".Lw_last:\n"
      :
      : "{s[8:11]}"(rsWS), "{s[12:15]}"(rsA), "{v0}"(v_l16), "{v1}"(v_l5), "{v2}"(v_ex[0]), "{v3}"(v_ex[1]), "{v4}"(v_ex[2]), "{v5}"(v_ex[3]), "{v6}"(v_ex[4]), "{v7}"(v_ex[5]), "{v8}"(v_ex[6]), "{v9}"(v_ex[7]), "{v10}"(v_l2), "{v11}"(v_fa), "{v12}"(v_ra), "{v13}"(v_rp), "{v14}"(v_rs), "{v15}"(v_xp), "{v16}"(v_xs), "{v17}"(v_ew), "{v18}"(v_sw), "{v19}"(v_sr), "{v20}"(v_ss), "{v22}"(v_al), "{v23}"(v_yw), "{v24}"(v_yr), "{v26}"(v_hi), "{v100}"(v_vo[0]), "{v101}"(v_vo[1]), "{v102}"(v_vo[2]), "{v103}"(v_vo[3]), "{v104}"(v_vo[4]), "{v105}"(v_vo[5]), "{v106}"(v_vo[6]), "{v107}"(v_vo[7]), "{v108}"(v_vo[8]), "{v109}"(v_vp[0]), "{v110}"(v_vp[1]), "{v111}"(v_vp[2]), "{v112}"(v_vp[3]), "{v113}"(v_vp[4]), "{v114}"(v_vp[5]), "{v115}"(v_vp[6]), "{v116}"(v_vp[7]), "{v117}"(v_vp[8]), "{v118}"(v_t0), "{v119}"(v_t1), "{v120}"(v_ts), "{v121}"(v_pw), "{s36}"(s_sx), "{s37}"(s_ev), "{s38}"(s_sy), "{s39}"(s_m4), "{s40}"(s_m5), "{s41}"(s_m9), "{s42}"(s_so), "{s43}"(s_s8), "{s44}"(s_wg), "{s45}"(s_rb)
      : "v255", "a255", "s99", "vcc", "scc", "memory");

  } else if (slot < 14) {
    const unsigned tr = ((xcd + DSH) & 7) * 3 + (slot - 5) / 3, n3 = (slot - 5) % 3, ks = unit_ks(tr), gr = unit_gr(tr);
    const u32x4 rsA = srd(Wd, 4608 * 576 * 2), rsB = srd(out, 2048 * 576 * 2), rsC = srd(X, 2048 * 576 * 2);
    const unsigned v_l16b = v_l16 + 4096, v_l8 = 1024 + lane * 8, f0 = 24 * ks + 6 * w;
    const unsigned v_fa = 147456 - f0 * 1024 + (f0 + (l8 < 5 ? l8 : 5)) * 128 + l7 / 4, v_ga = (l8 < 4 ? l8 : 4) * 4 * PW + 1536 + l7 / 4;
    const unsigned v_ra = up * 12 * PREC + l5 + 1536, v_rf = up * 33 * REC + l5 + 20480 + (4 * n3 + w) * 128;
    const unsigned v_pw = l8 < 5 ? (l8 + 7) * PREC + l8 * 4 * PW + 1536 + l7 : OOB, v_pu = ks && l8 + ks < 5 ? OOB : v_pw - 6 * PREC;
    const unsigned pf = 3 * (w >> 1) + 2 * (w & 1), sf = 3 * (w >> 1) + 1;
    const unsigned v_at = (8 * g + q) * 384 + 64 * pf + 16 * p, v_as = (8 * g + q) * 384 + 64 * sf + 32 * (w & 1) + 8 * p;
    auto vo = [&](unsigned i) { const unsigned c = 64 * i + lane; return (8 * w + c / 24) * 1152 + 384 * n3 + c % 24 * 16 - 1024 * i; };
    const unsigned v_vo0 = vo(0), v_vo1 = vo(1), v_vo2 = vo(2);
    const unsigned v_xp = (6 * n3 + pf) * 1024 + v_l16, v_xs = (6 * n3 + sf) * 1024 + (w & 1) * 512 + half;
    const unsigned v_op = i16 * 1152 + 384 * n3 + 64 * pf + 16 * g, v_os = i16 * 1152 + 384 * n3 + 64 * sf + 32 * (w & 1) + 8 * g;
    const unsigned v_sw = 147456 + i16 * 136 + (4 * w + g) * 8, v_sr = 147456 + (4 * w + g) * 136 + i16 * 8;
    const unsigned v_ss = 18432 + n3 * 512 + w * 128 + g * 32 + p * 8;
    const unsigned to0 = p < 3 ? (4 * (48 * w + (lane >> 2)) + gr) * 1152 + 384 * n3 + p * 128 : OOB;
    const unsigned v_to[3] = {to0, to0 + 64 * 1152, to0 + 128 * 1152};
    const unsigned s_ph = ks ? 6 - ks : 0, s_p0 = 0u - s_ph, r0 = gr * NREC + s_p0;
    const unsigned s_sh = WS_H + r0 * HREC + f0 * 1024, s_ps = WS_PS + r0 * PREC + (n3 * 20 + w) * PW;
    const unsigned s_x1 = WS_X1 + r0 * REC, s_xr = WS_XIN + (r0 + 33) * REC, s_or = (32 * gr + 63 - s_p0) * 18432;
    const unsigned s_k0 = ROT * 6 * gr;
    const unsigned s_m3 = w * 3072, s_m6 = w * 6144, s_so = ks * 768 * 1152;
    const unsigned s_i0 = (32 * gr + 31 - ks) * 18432, s_i5 = ks < 2 ? s_i0 - 30 * 18432 : OOB;
    const unsigned s_j0 = WS_XIN + (gr * NREC + ks) * REC, s_j5 = ks < 2 ? s_j0 + 30 * REC : OOB, s_he = s_sh + (63 + s_ph) * HREC;
    asm volatile(
      "s_buffer_load_dword s21, s[8:11], 0\n"
      "s_mov_b32 s23, 65536\n"
      "s_mov_b32 s31, 0x40000000\n"
      "buffer_load_dwordx4 v[56:59], v8, s[92:95], s46 offen\n"
      "buffer_load_dwordx2 v[60:61], v9, s[92:95], s46 offen\n"
      "s_waitcnt lgkmcnt(0)\n"
      "s_mul_i32 s21, s21, 1664525\n"
      "s_add_u32 s21, s21, 1013904223\n"
      "v_mov_b32 v18, s21\n"
      "v_mov_b32 v21, v18\n"
      "v_not_b32 v224, v18\n"
      "v_not_b32 v225, v18\n"
      "v_not_b32 v226, v18\n"
      "v_not_b32 v227, v18\n"
      "v_xor_b32 v18, v18, v19\n"
      "s_waitcnt vmcnt(0)\n"
      "buffer_store_dwordx4 v[56:59], v6, s[8:11], s48 offen sc1\n"
      "buffer_store_dwordx2 v[60:61], v7, s[8:11], s48 offen sc1\n"
      "v_lshlrev_b32 v164, 16, v56\n"
      "v_and_b32 v165, 0xffff0000, v56\n"
      "v_lshlrev_b32 v166, 16, v57\n"
      "v_and_b32 v167, 0xffff0000, v57\n"
      "v_lshlrev_b32 v168, 16, v58\n"
      "v_and_b32 v169, 0xffff0000, v58\n"
      "v_lshlrev_b32 v170, 16, v59\n"
      "v_and_b32 v171, 0xffff0000, v59\n"
      "v_lshlrev_b32 v172, 16, v60\n"
      "v_and_b32 v173, 0xffff0000, v60\n"
      "v_lshlrev_b32 v174, 16, v61\n"
      "v_and_b32 v175, 0xffff0000, v61\n"
      "v_add_f32 v62, v164, v165\n"
      "v_mul_f32 v63, v164, v164\n"
      "v_fmac_f32 v63, v165, v165\n"
      "v_add_f32 v62, v62, v166\n"
      "v_fmac_f32 v63, v166, v166\n"
      "v_add_f32 v62, v62, v167\n"
      "v_fmac_f32 v63, v167, v167\n"
      "v_add_f32 v62, v62, v168\n"
      "v_fmac_f32 v63, v168, v168\n"
      "v_add_f32 v62, v62, v169\n"
      "v_fmac_f32 v63, v169, v169\n"
      "v_add_f32 v62, v62, v170\n"
      "v_fmac_f32 v63, v170, v170\n"
      "v_add_f32 v62, v62, v171\n"
      "v_fmac_f32 v63, v171, v171\n"
      "v_add_f32 v62, v62, v172\n"
      "v_fmac_f32 v63, v172, v172\n"
      "v_add_f32 v62, v62, v173\n"
      "v_fmac_f32 v63, v173, v173\n"
      "v_add_f32 v62, v62, v174\n"
      "v_fmac_f32 v63, v174, v174\n"
      "v_add_f32 v62, v62, v175\n"
      "v_fmac_f32 v63, v175, v175\n"
      "ds_write_b64 v10, v[62:63] offset:0\n"
      "s_waitcnt lgkmcnt(0)\n"
      "s_barrier\n"
      "ds_read_b64 v[152:153], v11 offset:0\n"
      "s_waitcnt lgkmcnt(0)\n"
      "v_add_f32_dpp v152, v152, v152 row_ror:8 row_mask:0xf bank_mask:0xf\n"
      "v_add_f32_dpp v153, v153, v153 row_ror:8 row_mask:0xf bank_mask:0xf\n"
      "s_nop 0\n"
      "v_add_f32_dpp v152, v152, v152 row_ror:4 row_mask:0xf bank_mask:0xf\n"
      "v_add_f32_dpp v153, v153, v153 row_ror:4 row_mask:0xf bank_mask:0xf\n"
      "s_nop 0\n"
      "v_add_f32_dpp v152, v152, v152 row_ror:2 row_mask:0xf bank_mask:0xf\n"
      "v_add_f32_dpp v153, v153, v153 row_ror:2 row_mask:0xf bank_mask:0xf\n"
      "s_nop 0\n"
      "v_add_f32_dpp v152, v152, v152 row_ror:1 row_mask:0xf bank_mask:0xf\n"
      "v_add_f32_dpp v153, v153, v153 row_ror:1 row_mask:0xf bank_mask:0xf\n"
      "v_mul_f32 v152, 0x3ae38e39, v152\n"
      "v_add_f32 v153, 0x3afba882, v153\n"
      "v_mul_f32 v153, 0x3ae38e39, v153\n"
      "buffer_store_dwordx2 v[152:153], v12, s[8:11], s48 offen sc1\n"
      "s_add_u32 s30, s46, 4294856704\n"
      "buffer_load_dwordx4 v[64:67], v8, s[92:95], s30 offen\n"
      "buffer_load_dwordx2 v[68:69], v9, s[92:95], s30 offen\n"
      "s_add_u32 s30, s46, 4294746112\n"
      "buffer_load_dwordx4 v[72:75], v8, s[92:95], s30 offen\n"
      "buffer_load_dwordx2 v[76:77], v9, s[92:95], s30 offen\n"
      "s_waitcnt vmcnt(4)\n"
      "buffer_store_dword v18, v5, s[8:11], s48 offen sc1\n"
      "s_add_u32 s30, s46, 4294635520\n"
      "buffer_load_dwordx4 v[80:83], v8, s[92:95], s30 offen\n"
      "buffer_load_dwordx2 v[84:85], v9, s[92:95], s30 offen\n"
      "s_add_u32 s30, s46, 4294524928\n"
      "buffer_load_dwordx4 v[88:91], v8, s[92:95], s30 offen\n"
      "buffer_load_dwordx2 v[92:93], v9, s[92:95], s30 offen\n"
      "buffer_load_dwordx4 v[96:99], v8, s[92:95], s47 offen\n"
      "buffer_load_dwordx2 v[100:101], v9, s[92:95], s47 offen\n"
      "buffer_store_dword v18, v15, s[8:11], 0 offen sc1\n"
      "s_cmp_eq_u32 s45, 0\n"
      "s_cselect_b32 s30, 0x40000000, s37\n"
      "buffer_store_dword v21, v3, s[8:11], s30 offen sc1\n"
      "s_waitcnt vmcnt(11)\n"
      "s_add_u32 s30, s48, 147456\n"
      "buffer_store_dwordx4 v[64:67], v6, s[8:11], s30 offen sc1\n"
      "buffer_store_dwordx2 v[68:69], v7, s[8:11], s30 offen sc1\n"
      "v_lshlrev_b32 v164, 16, v64\n"
      "v_and_b32 v165, 0xffff0000, v64\n"
      "v_lshlrev_b32 v166, 16, v65\n"
      "v_and_b32 v167, 0xffff0000, v65\n"
      "v_lshlrev_b32 v168, 16, v66\n"
      "v_and_b32 v169, 0xffff0000, v66\n"
      "v_lshlrev_b32 v170, 16, v67\n"
      "v_and_b32 v171, 0xffff0000, v67\n"
      "v_lshlrev_b32 v172, 16, v68\n"
      "v_and_b32 v173, 0xffff0000, v68\n"
      "v_lshlrev_b32 v174, 16, v69\n"
      "v_and_b32 v175, 0xffff0000, v69\n"
      "v_add_f32 v70, v164, v165\n"
      "v_mul_f32 v71, v164, v164\n"
      "v_fmac_f32 v71, v165, v165\n"
      "v_add_f32 v70, v70, v166\n"
      "v_fmac_f32 v71, v166, v166\n"
      "v_add_f32 v70, v70, v167\n"
      "v_fmac_f32 v71, v167, v167\n"
      "v_add_f32 v70, v70, v168\n"
      "v_fmac_f32 v71, v168, v168\n"
      "v_add_f32 v70, v70, v169\n"
      "v_fmac_f32 v71, v169, v169\n"
      "v_add_f32 v70, v70, v170\n"
      "v_fmac_f32 v71, v170, v170\n"
      "v_add_f32 v70, v70, v171\n"
      "v_fmac_f32 v71, v171, v171\n"
      "v_add_f32 v70, v70, v172\n"
      "v_fmac_f32 v71, v172, v172\n"
      "v_add_f32 v70, v70, v173\n"
      "v_fmac_f32 v71, v173, v173\n"
      "v_add_f32 v70, v70, v174\n"
      "v_fmac_f32 v71, v174, v174\n"
      "v_add_f32 v70, v70, v175\n"
      "v_fmac_f32 v71, v175, v175\n"
      "ds_write_b64 v10, v[70:71] offset:2176\n"
      "s_waitcnt vmcnt(11)\n"
      "s_add_u32 s30, s48, 294912\n"
      "buffer_store_dwordx4 v[72:75], v6, s[8:11], s30 offen sc1\n"
      "buffer_store_dwordx2 v[76:77], v7, s[8:11], s30 offen sc1\n"
      "v_lshlrev_b32 v164, 16, v72\n"
      "v_and_b32 v165, 0xffff0000, v72\n"
      "v_lshlrev_b32 v166, 16, v73\n"
      "v_and_b32 v167, 0xffff0000, v73\n"
      "v_lshlrev_b32 v168, 16, v74\n"
      "v_and_b32 v169, 0xffff0000, v74\n"
      "v_lshlrev_b32 v170, 16, v75\n"
      "v_and_b32 v171, 0xffff0000, v75\n"
      "v_lshlrev_b32 v172, 16, v76\n"
      "v_and_b32 v173, 0xffff0000, v76\n"
      "v_lshlrev_b32 v174, 16, v77\n"
      "v_and_b32 v175, 0xffff0000, v77\n"
      "v_add_f32 v78, v164, v165\n"
      "v_mul_f32 v79, v164, v164\n"
      "v_fmac_f32 v79, v165, v165\n"
      "v_add_f32 v78, v78, v166\n"
      "v_fmac_f32 v79, v166, v166\n"
      "v_add_f32 v78, v78, v167\n"
      "v_fmac_f32 v79, v167, v167\n"
      "v_add_f32 v78, v78, v168\n"
      "v_fmac_f32 v79, v168, v168\n"
      "v_add_f32 v78, v78, v169\n"
      "v_fmac_f32 v79, v169, v169\n"
      "v_add_f32 v78, v78, v170\n"
      "v_fmac_f32 v79, v170, v170\n"
      "v_add_f32 v78, v78, v171\n"
      "v_fmac_f32 v79, v171, v171\n"
      "v_add_f32 v78, v78, v172\n"
      "v_fmac_f32 v79, v172, v172\n"
      "v_add_f32 v78, v78, v173\n"
      "v_fmac_f32 v79, v173, v173\n"
      "v_add_f32 v78, v78, v174\n"
      "v_fmac_f32 v79, v174, v174\n"
      "v_add_f32 v78, v78, v175\n"
      "v_fmac_f32 v79, v175, v175\n"
      "ds_write_b64 v10, v[78:79] offset:4352\n"
      "s_waitcnt vmcnt(10)\n"
      "s_add_u32 s30, s48, 442368\n"
      "buffer_store_dwordx4 v[80:83], v6, s[8:11], s30 offen sc1\n"
      "buffer_store_dwordx2 v[84:85], v7, s[8:11], s30 offen sc1\n"
      "v_lshlrev_b32 v164, 16, v80\n"
      "v_and_b32 v165, 0xffff0000, v80\n"
      "v_lshlrev_b32 v166, 16, v81\n"
      "v_and_b32 v167, 0xffff0000, v81\n"
      "v_lshlrev_b32 v168, 16, v82\n"
      "v_and_b32 v169, 0xffff0000, v82\n"
      "v_lshlrev_b32 v170, 16, v83\n"
      "v_and_b32 v171, 0xffff0000, v83\n"
      "v_lshlrev_b32 v172, 16, v84\n"
      "v_and_b32 v173, 0xffff0000, v84\n"
      "v_lshlrev_b32 v174, 16, v85\n"
      "v_and_b32 v175, 0xffff0000, v85\n"
      "v_add_f32 v86, v164, v165\n"
      "v_mul_f32 v87, v164, v164\n"
      "v_fmac_f32 v87, v165, v165\n"
      "v_add_f32 v86, v86, v166\n"
      "v_fmac_f32 v87, v166, v166\n"
      "v_add_f32 v86, v86, v167\n"
      "v_fmac_f32 v87, v167, v167\n"
      "v_add_f32 v86, v86, v168\n"
      "v_fmac_f32 v87, v168, v168\n"
      "v_add_f32 v86, v86, v169\n"
      "v_fmac_f32 v87, v169, v169\n"
      "v_add_f32 v86, v86, v170\n"
      "v_fmac_f32 v87, v170, v170\n"
      "v_add_f32 v86, v86, v171\n"
      "v_fmac_f32 v87, v171, v171\n"
      "v_add_f32 v86, v86, v172\n"
      "v_fmac_f32 v87, v172, v172\n"
      "v_add_f32 v86, v86, v173\n"
      "v_fmac_f32 v87, v173, v173\n"
      "v_add_f32 v86, v86, v174\n"
      "v_fmac_f32 v87, v174, v174\n"
      "v_add_f32 v86, v86, v175\n"
      "v_fmac_f32 v87, v175, v175\n"
      "ds_write_b64 v10, v[86:87] offset:6528\n"
      "s_waitcnt vmcnt(10)\n"
      "s_add_u32 s30, s48, 589824\n"
      "buffer_store_dwordx4 v[88:91], v6, s[8:11], s30 offen sc1\n"
      "buffer_store_dwordx2 v[92:93], v7, s[8:11], s30 offen sc1\n"
      "v_lshlrev_b32 v164, 16, v88\n"
      "v_and_b32 v165, 0xffff0000, v88\n"
      "v_lshlrev_b32 v166, 16, v89\n"
      "v_and_b32 v167, 0xffff0000, v89\n"
      "v_lshlrev_b32 v168, 16, v90\n"
      "v_and_b32 v169, 0xffff0000, v90\n"
      "v_lshlrev_b32 v170, 16, v91\n"
      "v_and_b32 v171, 0xffff0000, v91\n"
      "v_lshlrev_b32 v172, 16, v92\n"
      "v_and_b32 v173, 0xffff0000, v92\n"
      "v_lshlrev_b32 v174, 16, v93\n"
      "v_and_b32 v175, 0xffff0000, v93\n"
      "v_add_f32 v94, v164, v165\n"
      "v_mul_f32 v95, v164, v164\n"
      "v_fmac_f32 v95, v165, v165\n"
      "v_add_f32 v94, v94, v166\n"
      "v_fmac_f32 v95, v166, v166\n"
      "v_add_f32 v94, v94, v167\n"
      "v_fmac_f32 v95, v167, v167\n"
      "v_add_f32 v94, v94, v168\n"
      "v_fmac_f32 v95, v168, v168\n"
      "v_add_f32 v94, v94, v169\n"
      "v_fmac_f32 v95, v169, v169\n"
      "v_add_f32 v94, v94, v170\n"
      "v_fmac_f32 v95, v170, v170\n"
      "v_add_f32 v94, v94, v171\n"
      "v_fmac_f32 v95, v171, v171\n"
      "v_add_f32 v94, v94, v172\n"
      "v_fmac_f32 v95, v172, v172\n"
      "v_add_f32 v94, v94, v173\n"
      "v_fmac_f32 v95, v173, v173\n"
      "v_add_f32 v94, v94, v174\n"
      "v_fmac_f32 v95, v174, v174\n"
      "v_add_f32 v94, v94, v175\n"
      "v_fmac_f32 v95, v175, v175\n"
      "ds_write_b64 v10, v[94:95] offset:8704\n"
      "s_waitcnt vmcnt(10)\n"
      "buffer_store_dwordx4 v[96:99], v6, s[8:11], s49 offen sc1\n"
      "buffer_store_dwordx2 v[100:101], v7, s[8:11], s49 offen sc1\n"
      "v_lshlrev_b32 v164, 16, v96\n"
      "v_and_b32 v165, 0xffff0000, v96\n"
      "v_lshlrev_b32 v166, 16, v97\n"
      "v_and_b32 v167, 0xffff0000, v97\n"
      "v_lshlrev_b32 v168, 16, v98\n"
      "v_and_b32 v169, 0xffff0000, v98\n"
      "v_lshlrev_b32 v170, 16, v99\n"
      "v_and_b32 v171, 0xffff0000, v99\n"
      "v_lshlrev_b32 v172, 16, v100\n"
      "v_and_b32 v173, 0xffff0000, v100\n"
      "v_lshlrev_b32 v174, 16, v101\n"
      "v_and_b32 v175, 0xffff0000, v101\n"
      "v_add_f32 v102, v164, v165\n"
      "v_mul_f32 v103, v164, v164\n"
      "v_fmac_f32 v103, v165, v165\n"
      "v_add_f32 v102, v102, v166\n"
      "v_fmac_f32 v103, v166, v166\n"
      "v_add_f32 v102, v102, v167\n"
      "v_fmac_f32 v103, v167, v167\n"
      "v_add_f32 v102, v102, v168\n"
      "v_fmac_f32 v103, v168, v168\n"
      "v_add_f32 v102, v102, v169\n"
      "v_fmac_f32 v103, v169, v169\n"
      "v_add_f32 v102, v102, v170\n"
      "v_fmac_f32 v103, v170, v170\n"
      "v_add_f32 v102, v102, v171\n"
      "v_fmac_f32 v103, v171, v171\n"
      "v_add_f32 v102, v102, v172\n"
      "v_fmac_f32 v103, v172, v172\n"
      "v_add_f32 v102, v102, v173\n"
      "v_fmac_f32 v103, v173, v173\n"
      "v_add_f32 v102, v102, v174\n"
      "v_fmac_f32 v103, v174, v174\n"
      "v_add_f32 v102, v102, v175\n"
      "v_fmac_f32 v103, v175, v175\n"
      "ds_write_b64 v10, v[102:103] offset:10880\n"
      "s_waitcnt lgkmcnt(0)\n"
      "s_barrier\n"
      "ds_read_b64 v[154:155], v11 offset:2176\n"
      "ds_read_b64 v[156:157], v11 offset:4352\n"
      "ds_read_b64 v[158:159], v11 offset:6528\n"
      "ds_read_b64 v[160:161], v11 offset:8704\n"
      "ds_read_b64 v[162:163], v11 offset:10880\n"
      "s_waitcnt lgkmcnt(0)\n"
      "v_add_f32_dpp v154, v154, v154 row_ror:8 row_mask:0xf bank_mask:0xf\n"
      "v_add_f32_dpp v155, v155, v155 row_ror:8 row_mask:0xf bank_mask:0xf\n"
      "v_add_f32_dpp v156, v156, v156 row_ror:8 row_mask:0xf bank_mask:0xf\n"
      "v_add_f32_dpp v157, v157, v157 row_ror:8 row_mask:0xf bank_mask:0xf\n"
      "v_add_f32_dpp v158, v158, v158 row_ror:8 row_mask:0xf bank_mask:0xf\n"
      "v_add_f32_dpp v159, v159, v159 row_ror:8 row_mask:0xf bank_mask:0xf\n"
      "v_add_f32_dpp v160, v160, v160 row_ror:8 row_mask:0xf bank_mask:0xf\n"
      "v_add_f32_dpp v161, v161, v161 row_ror:8 row_mask:0xf bank_mask:0xf\n"
      "v_add_f32_dpp v162, v162, v162 row_ror:8 row_mask:0xf bank_mask:0xf\n"
      "v_add_f32_dpp v163, v163, v163 row_ror:8 row_mask:0xf bank_mask:0xf\n"
      "v_add_f32_dpp v154, v154, v154 row_ror:4 row_mask:0xf bank_mask:0xf\n"
      "v_add_f32_dpp v155, v155, v155 row_ror:4 row_mask:0xf bank_mask:0xf\n"
      "v_add_f32_dpp v156, v156, v156 row_ror:4 row_mask:0xf bank_mask:0xf\n"
      "v_add_f32_dpp v157, v157, v157 row_ror:4 row_mask:0xf bank_mask:0xf\n"
      "v_add_f32_dpp v158, v158, v158 row_ror:4 row_mask:0xf bank_mask:0xf\n"
      "v_add_f32_dpp v159, v159, v159 row_ror:4 row_mask:0xf bank_mask:0xf\n"
      "v_add_f32_dpp v160, v160, v160 row_ror:4 row_mask:0xf bank_mask:0xf\n"
      "v_add_f32_dpp v161, v161, v161 row_ror:4 row_mask:0xf bank_mask:0xf\n"
      "v_add_f32_dpp v162, v162, v162 row_ror:4 row_mask:0xf bank_mask:0xf\n"
      "v_add_f32_dpp v163, v163, v163 row_ror:4 row_mask:0xf bank_mask:0xf\n"
      "v_add_f32_dpp v154, v154, v154 row_ror:2 row_mask:0xf bank_mask:0xf\n"
      "v_add_f32_dpp v155, v155, v155 row_ror:2 row_mask:0xf bank_mask:0xf\n"
      "v_add_f32_dpp v156, v156, v156 row_ror:2 row_mask:0xf bank_mask:0xf\n"
      "v_add_f32_dpp v157, v157, v157 row_ror:2 row_mask:0xf bank_mask:0xf\n"
      "v_add_f32_dpp v158, v158, v158 row_ror:2 row_mask:0xf bank_mask:0xf\n"
      "v_add_f32_dpp v159, v159, v159 row_ror:2 row_mask:0xf bank_mask:0xf\n"
      "v_add_f32_dpp v160, v160, v160 row_ror:2 row_mask:0xf bank_mask:0xf\n"
      "v_add_f32_dpp v161, v161, v161 row_ror:2 row_mask:0xf bank_mask:0xf\n"
      "v_add_f32_dpp v162, v162, v162 row_ror:2 row_mask:0xf bank_mask:0xf\n"
      "v_add_f32_dpp v163, v163, v163 row_ror:2 row_mask:0xf bank_mask:0xf\n"
      "v_add_f32_dpp v154, v154, v154 row_ror:1 row_mask:0xf bank_mask:0xf\n"
      "v_add_f32_dpp v155, v155, v155 row_ror:1 row_mask:0xf bank_mask:0xf\n"
      "v_add_f32_dpp v156, v156, v156 row_ror:1 row_mask:0xf bank_mask:0xf\n"
      "v_add_f32_dpp v157, v157, v157 row_ror:1 row_mask:0xf bank_mask:0xf\n"
      "v_add_f32_dpp v158, v158, v158 row_ror:1 row_mask:0xf bank_mask:0xf\n"
      "v_add_f32_dpp v159, v159, v159 row_ror:1 row_mask:0xf bank_mask:0xf\n"
      "v_add_f32_dpp v160, v160, v160 row_ror:1 row_mask:0xf bank_mask:0xf\n"
      "v_add_f32_dpp v161, v161, v161 row_ror:1 row_mask:0xf bank_mask:0xf\n"
      "v_add_f32_dpp v162, v162, v162 row_ror:1 row_mask:0xf bank_mask:0xf\n"
      "v_add_f32_dpp v163, v163, v163 row_ror:1 row_mask:0xf bank_mask:0xf\n"
      "v_mul_f32 v154, 0x3ae38e39, v154\n"
      "v_add_f32 v155, 0x3afba882, v155\n"
      "v_mul_f32 v155, 0x3ae38e39, v155\n"
      "v_mul_f32 v156, 0x3ae38e39, v156\n"
      "v_add_f32 v157, 0x3afba882, v157\n"
      "v_mul_f32 v157, 0x3ae38e39, v157\n"
      "v_mul_f32 v158, 0x3ae38e39, v158\n"
      "v_add_f32 v159, 0x3afba882, v159\n"
      "v_mul_f32 v159, 0x3ae38e39, v159\n"
      "v_mul_f32 v160, 0x3ae38e39, v160\n"
      "v_add_f32 v161, 0x3afba882, v161\n"
      "v_mul_f32 v161, 0x3ae38e39, v161\n"
      "v_mul_f32 v162, 0x3ae38e39, v162\n"
      "v_add_f32 v163, 0x3afba882, v163\n"
      "v_mul_f32 v163, 0x3ae38e39, v163\n"
      "s_add_u32 s30, s48, 147456\n"
      "buffer_store_dwordx2 v[154:155], v12, s[8:11], s30 offen sc1\n"
      "s_add_u32 s30, s48, 294912\n"
      "buffer_store_dwordx2 v[156:157], v12, s[8:11], s30 offen sc1\n"
      "s_add_u32 s30, s48, 442368\n"
      "buffer_store_dwordx2 v[158:159], v12, s[8:11], s30 offen sc1\n"
      "s_add_u32 s30, s48, 589824\n"
      "buffer_store_dwordx2 v[160:161], v12, s[8:11], s30 offen sc1\n"
      "buffer_store_dwordx2 v[162:163], v12, s[8:11], s49 offen sc1\n"
      "s_waitcnt vmcnt(0)\n"
      "s_add_u32 s30, s48, 147456\n"
      "buffer_store_dword v18, v5, s[8:11], s30 offen sc1\n"
      "s_add_u32 s30, s48, 294912\n"
      "buffer_store_dword v18, v5, s[8:11], s30 offen sc1\n"
      "s_add_u32 s30, s48, 442368\n"
      "buffer_store_dword v18, v5, s[8:11], s30 offen sc1\n"
      "s_add_u32 s30, s48, 589824\n"
      "buffer_store_dword v18, v5, s[8:11], s30 offen sc1\n"
      "buffer_store_dword v18, v5, s[8:11], s49 offen sc1\n"
      "v_and_b32 v20, 0x40000000, v19\n"
      "v_or_b32 v5, v20, v5\n"
      "s_add_u32 s30, s37, 0\n"
      "buffer_store_dwordx4 v[224:227], v183, s[8:11], s30 offen sc1\n"
      "s_add_u32 s30, s37, 0\n"
      "buffer_store_dwordx4 v[224:227], v181, s[8:11], s30 offen sc1\n"
      "s_sleep 32\n"
      "s_sleep 32\n"
      "s_waitcnt vmcnt(0) lgkmcnt(0)\n"
      "s_mov_b32 s61, s60\n"
      "s_mov_b32 s62, 6\n"
      ".Ld_r0:\n"
      "s_mul_i32 s63, s61, 36864\n"
      "s_add_u32 s63, s63, s44\n"
      "s_mul_i32 s30, s61, 8192\n"
      "s_lshr_b32 s30, s30, 16\n"
      "s_mul_i32 s30, s30, 8\n"
      "s_sub_u32 s30, s61, s30\n"
      "s_mul_i32 s30, s30, 12288\n"
      "s_add_u32 s30, s30, 0\n"
      "s_add_u32 m0, s42, s30\n"
      "s_add_u32 s61, s61, 1\n"
      "buffer_load_dwordx4 v178, s[12:15], s63 offen offset:0 lds\n"
      "buffer_load_dwordx4 v179, s[12:15], s63 offen offset:1024 lds\n"
      "buffer_load_dwordx4 v180, s[12:15], s63 offen offset:2048 lds\n"
      "s_cmp_eq_u32 s61, 24\n"
      "s_cselect_b32 s61, 0, s61\n"
      "s_sub_u32 s62, s62, 1\n"
      "s_cbranch_scc0 .Ld_r0\n"
      "v_add_u32 v20, 49152, v176\n"
      "v_add_u32 v21, 49152, v177\n"
      "v_add_u32 v22, 98304, v176\n"
      "v_add_u32 v23, 98304, v177\n"
      "s_mov_b32 s62, 23\n"
      "s_cmp_eq_u32 s60, 6\n"
      "s_cbranch_scc1 .Ld_rs6\n"
      "s_cmp_eq_u32 s60, 12\n"
      "s_cbranch_scc1 .Ld_rs12\n"
      "s_cmp_eq_u32 s60, 18\n"
      "s_cbranch_scc1 .Ld_rs18\n"
      ".Ld_rs0:\n"
      "s_add_u32 s63, s44, 258048\n"
      "s_cmp_ge_u32 s62, 7\n"
      "s_cselect_b32 s63, s63, s31\n"
      "s_add_u32 m0, s42, 86016\n"
      "s_waitcnt vmcnt(18) lgkmcnt(0)\n"
      "s_barrier\n"
      "ds_read_b64_tr_b16 a[0:1], v176 offset:0\n"
      "ds_read_b64_tr_b16 a[2:3], v176 offset:1536\n"
      "ds_read_b64_tr_b16 a[96:97], v176 offset:8\n"
      "ds_read_b64_tr_b16 a[98:99], v176 offset:1544\n"
      "ds_read_b64_tr_b16 a[192:193], v177 offset:0\n"
      "ds_read_b64_tr_b16 a[194:195], v177 offset:1536\n"
      "buffer_load_dwordx4 v178, s[12:15], s63 offen offset:0 lds\n"
      "buffer_load_dwordx4 v179, s[12:15], s63 offen offset:1024 lds\n"
      "buffer_load_dwordx4 v180, s[12:15], s63 offen offset:2048 lds\n"
      "s_sub_u32 s62, s62, 1\n"
      ".Ld_rs1:\n"
      "s_add_u32 s63, s44, 294912\n"
      "s_cmp_ge_u32 s62, 7\n"
      "s_cselect_b32 s63, s63, s31\n"
      "s_add_u32 m0, s42, 0\n"
      "s_waitcnt vmcnt(18) lgkmcnt(0)\n"
      "s_barrier\n"
      "ds_read_b64_tr_b16 a[4:5], v176 offset:12288\n"
      "ds_read_b64_tr_b16 a[6:7], v176 offset:13824\n"
      "ds_read_b64_tr_b16 a[100:101], v176 offset:12296\n"
      "ds_read_b64_tr_b16 a[102:103], v176 offset:13832\n"
      "ds_read_b64_tr_b16 a[196:197], v177 offset:12288\n"
      "ds_read_b64_tr_b16 a[198:199], v177 offset:13824\n"
      "buffer_load_dwordx4 v178, s[12:15], s63 offen offset:0 lds\n"
      "buffer_load_dwordx4 v179, s[12:15], s63 offen offset:1024 lds\n"
      "buffer_load_dwordx4 v180, s[12:15], s63 offen offset:2048 lds\n"
      "s_sub_u32 s62, s62, 1\n"
      ".Ld_rs2:\n"
      "s_add_u32 s63, s44, 331776\n"
      "s_cmp_ge_u32 s62, 7\n"
      "s_cselect_b32 s63, s63, s31\n"
      "s_add_u32 m0, s42, 12288\n"
      "s_waitcnt vmcnt(18) lgkmcnt(0)\n"
      "s_barrier\n"
      "ds_read_b64_tr_b16 a[8:9], v176 offset:24576\n"
      "ds_read_b64_tr_b16 a[10:11], v176 offset:26112\n"
      "ds_read_b64_tr_b16 a[104:105], v176 offset:24584\n"
      "ds_read_b64_tr_b16 a[106:107], v176 offset:26120\n"
      "ds_read_b64_tr_b16 a[200:201], v177 offset:24576\n"
      "ds_read_b64_tr_b16 a[202:203], v177 offset:26112\n"
      "buffer_load_dwordx4 v178, s[12:15], s63 offen offset:0 lds\n"
      "buffer_load_dwordx4 v179, s[12:15], s63 offen offset:1024 lds\n"
      "buffer_load_dwordx4 v180, s[12:15], s63 offen offset:2048 lds\n"
      "s_sub_u32 s62, s62, 1\n"
      ".Ld_rs3:\n"
      "s_add_u32 s63, s44, 368640\n"
      "s_cmp_ge_u32 s62, 7\n"
      "s_cselect_b32 s63, s63, s31\n"
      "s_add_u32 m0, s42, 24576\n"
      "s_waitcnt vmcnt(18) lgkmcnt(0)\n"
      "s_barrier\n"
      "ds_read_b64_tr_b16 a[12:13], v176 offset:36864\n"
      "ds_read_b64_tr_b16 a[14:15], v176 offset:38400\n"
      "ds_read_b64_tr_b16 a[108:109], v176 offset:36872\n"
      "ds_read_b64_tr_b16 a[110:111], v176 offset:38408\n"
      "ds_read_b64_tr_b16 a[204:205], v177 offset:36864\n"
      "ds_read_b64_tr_b16 a[206:207], v177 offset:38400\n"
      "buffer_load_dwordx4 v178, s[12:15], s63 offen offset:0 lds\n"
      "buffer_load_dwordx4 v179, s[12:15], s63 offen offset:1024 lds\n"
      "buffer_load_dwordx4 v180, s[12:15], s63 offen offset:2048 lds\n"
      "s_sub_u32 s62, s62, 1\n"
      ".Ld_rs4:\n"
      "s_add_u32 s63, s44, 405504\n"
      "s_cmp_ge_u32 s62, 7\n"
      "s_cselect_b32 s63, s63, s31\n"
      "s_add_u32 m0, s42, 36864\n"
      "s_waitcnt vmcnt(18) lgkmcnt(0)\n"
      "s_barrier\n"
      "ds_read_b64_tr_b16 a[16:17], v20 offset:0\n"
      "ds_read_b64_tr_b16 a[18:19], v20 offset:1536\n"
      "ds_read_b64_tr_b16 a[112:113], v20 offset:8\n"
      "ds_read_b64_tr_b16 a[114:115], v20 offset:1544\n"
      "ds_read_b64_tr_b16 a[208:209], v21 offset:0\n"
      "ds_read_b64_tr_b16 a[210:211], v21 offset:1536\n"
      "buffer_load_dwordx4 v178, s[12:15], s63 offen offset:0 lds\n"
      "buffer_load_dwordx4 v179, s[12:15], s63 offen offset:1024 lds\n"
      "buffer_load_dwordx4 v180, s[12:15], s63 offen offset:2048 lds\n"
      "s_sub_u32 s62, s62, 1\n"
      ".Ld_rs5:\n"
      "s_add_u32 s63, s44, 442368\n"
      "s_cmp_ge_u32 s62, 7\n"
      "s_cselect_b32 s63, s63, s31\n"
      "s_add_u32 m0, s42, 49152\n"
      "s_waitcnt vmcnt(18) lgkmcnt(0)\n"
      "s_barrier\n"
      "ds_read_b64_tr_b16 a[20:21], v20 offset:12288\n"
      "ds_read_b64_tr_b16 a[22:23], v20 offset:13824\n"
      "ds_read_b64_tr_b16 a[116:117], v20 offset:12296\n"
      "ds_read_b64_tr_b16 a[118:119], v20 offset:13832\n"
      "ds_read_b64_tr_b16 a[212:213], v21 offset:12288\n"
      "ds_read_b64_tr_b16 a[214:215], v21 offset:13824\n"
      "buffer_load_dwordx4 v178, s[12:15], s63 offen offset:0 lds\n"
      "buffer_load_dwordx4 v179, s[12:15], s63 offen offset:1024 lds\n"
      "buffer_load_dwordx4 v180, s[12:15], s63 offen offset:2048 lds\n"
      "s_sub_u32 s62, s62, 1\n"
      "s_cbranch_scc1 .Ld_re\n"
      ".Ld_rs6:\n"
      "s_add_u32 s63, s44, 479232\n"
      "s_cmp_ge_u32 s62, 7\n"
      "s_cselect_b32 s63, s63, s31\n"
      "s_add_u32 m0, s42, 61440\n"
      "s_waitcnt vmcnt(18) lgkmcnt(0)\n"
      "s_barrier\n"
      "ds_read_b64_tr_b16 a[24:25], v20 offset:24576\n"
      "ds_read_b64_tr_b16 a[26:27], v20 offset:26112\n"
      "ds_read_b64_tr_b16 a[120:121], v20 offset:24584\n"
      "ds_read_b64_tr_b16 a[122:123], v20 offset:26120\n"
      "ds_read_b64_tr_b16 a[216:217], v21 offset:24576\n"
      "ds_read_b64_tr_b16 a[218:219], v21 offset:26112\n"
      "buffer_load_dwordx4 v178, s[12:15], s63 offen offset:0 lds\n"
      "buffer_load_dwordx4 v179, s[12:15], s63 offen offset:1024 lds\n"
      "buffer_load_dwordx4 v180, s[12:15], s63 offen offset:2048 lds\n"
      "s_sub_u32 s62, s62, 1\n"
      ".Ld_rs7:\n"
      "s_add_u32 s63, s44, 516096\n"
      "s_cmp_ge_u32 s62, 7\n"
      "s_cselect_b32 s63, s63, s31\n"
      "s_add_u32 m0, s42, 73728\n"
      "s_waitcnt vmcnt(18) lgkmcnt(0)\n"
      "s_barrier\n"
      "ds_read_b64_tr_b16 a[28:29], v20 offset:36864\n"
      "ds_read_b64_tr_b16 a[30:31], v20 offset:38400\n"
      "ds_read_b64_tr_b16 a[124:125], v20 offset:36872\n"
      "ds_read_b64_tr_b16 a[126:127], v20 offset:38408\n"
      "ds_read_b64_tr_b16 a[220:221], v21 offset:36864\n"
      "ds_read_b64_tr_b16 a[222:223], v21 offset:38400\n"
      "buffer_load_dwordx4 v178, s[12:15], s63 offen offset:0 lds\n"
      "buffer_load_dwordx4 v179, s[12:15], s63 offen offset:1024 lds\n"
      "buffer_load_dwordx4 v180, s[12:15], s63 offen offset:2048 lds\n"
      "s_sub_u32 s62, s62, 1\n"
      ".Ld_rs8:\n"
      "s_add_u32 s63, s44, 552960\n"
      "s_cmp_ge_u32 s62, 7\n"
      "s_cselect_b32 s63, s63, s31\n"
      "s_add_u32 m0, s42, 86016\n"
      "s_waitcnt vmcnt(18) lgkmcnt(0)\n"
      "s_barrier\n"
      "ds_read_b64_tr_b16 a[32:33], v176 offset:0\n"
      "ds_read_b64_tr_b16 a[34:35], v176 offset:1536\n"
      "ds_read_b64_tr_b16 a[128:129], v176 offset:8\n"
      "ds_read_b64_tr_b16 a[130:131], v176 offset:1544\n"
      "ds_read_b64_tr_b16 a[224:225], v177 offset:0\n"
      "ds_read_b64_tr_b16 a[226:227], v177 offset:1536\n"
      "buffer_load_dwordx4 v178, s[12:15], s63 offen offset:0 lds\n"
      "buffer_load_dwordx4 v179, s[12:15], s63 offen offset:1024 lds\n"
      "buffer_load_dwordx4 v180, s[12:15], s63 offen offset:2048 lds\n"
      "s_sub_u32 s62, s62, 1\n"
      ".Ld_rs9:\n"
      "s_add_u32 s63, s44, 589824\n"
      "s_cmp_ge_u32 s62, 7\n"
      "s_cselect_b32 s63, s63, s31\n"
      "s_add_u32 m0, s42, 0\n"
      "s_waitcnt vmcnt(18) lgkmcnt(0)\n"
      "s_barrier\n"
      "ds_read_b64_tr_b16 a[36:37], v176 offset:12288\n"
      "ds_read_b64_tr_b16 a[38:39], v176 offset:13824\n"
      "ds_read_b64_tr_b16 a[132:133], v176 offset:12296\n"
      "ds_read_b64_tr_b16 a[134:135], v176 offset:13832\n"
      "ds_read_b64_tr_b16 a[228:229], v177 offset:12288\n"
      "ds_read_b64_tr_b16 a[230:231], v177 offset:13824\n"
      "buffer_load_dwordx4 v178, s[12:15], s63 offen offset:0 lds\n"
      "buffer_load_dwordx4 v179, s[12:15], s63 offen offset:1024 lds\n"
      "buffer_load_dwordx4 v180, s[12:15], s63 offen offset:2048 lds\n"
      "s_sub_u32 s62, s62, 1\n"
      ".Ld_rs10:\n"
      "s_add_u32 s63, s44, 626688\n"
      "s_cmp_ge_u32 s62, 7\n"
      "s_cselect_b32 s63, s63, s31\n"
      "s_add_u32 m0, s42, 12288\n"
      "s_waitcnt vmcnt(18) lgkmcnt(0)\n"
      "s_barrier\n"
      "ds_read_b64_tr_b16 a[40:41], v176 offset:24576\n"
      "ds_read_b64_tr_b16 a[42:43], v176 offset:26112\n"
      "ds_read_b64_tr_b16 a[136:137], v176 offset:24584\n"
      "ds_read_b64_tr_b16 a[138:139], v176 offset:26120\n"
      "ds_read_b64_tr_b16 a[232:233], v177 offset:24576\n"
      "ds_read_b64_tr_b16 a[234:235], v177 offset:26112\n"
      "buffer_load_dwordx4 v178, s[12:15], s63 offen offset:0 lds\n"
      "buffer_load_dwordx4 v179, s[12:15], s63 offen offset:1024 lds\n"
      "buffer_load_dwordx4 v180, s[12:15], s63 offen offset:2048 lds\n"
      "s_sub_u32 s62, s62, 1\n"
      ".Ld_rs11:\n"
      "s_add_u32 s63, s44, 663552\n"
      "s_cmp_ge_u32 s62, 7\n"
      "s_cselect_b32 s63, s63, s31\n"
      "s_add_u32 m0, s42, 24576\n"
      "s_waitcnt vmcnt(18) lgkmcnt(0)\n"
      "s_barrier\n"
      "ds_read_b64_tr_b16 a[44:45], v176 offset:36864\n"
      "ds_read_b64_tr_b16 a[46:47], v176 offset:38400\n"
      "ds_read_b64_tr_b16 a[140:141], v176 offset:36872\n"
      "ds_read_b64_tr_b16 a[142:143], v176 offset:38408\n"
      "ds_read_b64_tr_b16 a[236:237], v177 offset:36864\n"
      "ds_read_b64_tr_b16 a[238:239], v177 offset:38400\n"
      "buffer_load_dwordx4 v178, s[12:15], s63 offen offset:0 lds\n"
      "buffer_load_dwordx4 v179, s[12:15], s63 offen offset:1024 lds\n"
      "buffer_load_dwordx4 v180, s[12:15], s63 offen offset:2048 lds\n"
      "s_sub_u32 s62, s62, 1\n"
      "s_cbranch_scc1 .Ld_re\n"
      ".Ld_rs12:\n"
      "s_add_u32 s63, s44, 700416\n"
      "s_cmp_ge_u32 s62, 7\n"
      "s_cselect_b32 s63, s63, s31\n"
      "s_add_u32 m0, s42, 36864\n"
      "s_waitcnt vmcnt(18) lgkmcnt(0)\n"
      "s_barrier\n"
      "ds_read_b64_tr_b16 a[48:49], v20 offset:0\n"
      "ds_read_b64_tr_b16 a[50:51], v20 offset:1536\n"
      "ds_read_b64_tr_b16 a[144:145], v20 offset:8\n"
      "ds_read_b64_tr_b16 a[146:147], v20 offset:1544\n"
      "ds_read_b64_tr_b16 a[240:241], v21 offset:0\n"
      "ds_read_b64_tr_b16 a[242:243], v21 offset:1536\n"
      "buffer_load_dwordx4 v178, s[12:15], s63 offen offset:0 lds\n"
      "buffer_load_dwordx4 v179, s[12:15], s63 offen offset:1024 lds\n"
      "buffer_load_dwordx4 v180, s[12:15], s63 offen offset:2048 lds\n"
      "s_sub_u32 s62, s62, 1\n"
      ".Ld_rs13:\n"
      "s_add_u32 s63, s44, 737280\n"
      "s_cmp_ge_u32 s62, 7\n"
      "s_cselect_b32 s63, s63, s31\n"
      "s_add_u32 m0, s42, 49152\n"
      "s_waitcnt vmcnt(18) lgkmcnt(0)\n"
      "s_barrier\n"
      "ds_read_b64_tr_b16 a[52:53], v20 offset:12288\n"
      "ds_read_b64_tr_b16 a[54:55], v20 offset:13824\n"
      "ds_read_b64_tr_b16 a[148:149], v20 offset:12296\n"
      "ds_read_b64_tr_b16 a[150:151], v20 offset:13832\n"
      "ds_read_b64_tr_b16 a[244:245], v21 offset:12288\n"
      "ds_read_b64_tr_b16 a[246:247], v21 offset:13824\n"
      "buffer_load_dwordx4 v178, s[12:15], s63 offen offset:0 lds\n"
      "buffer_load_dwordx4 v179, s[12:15], s63 offen offset:1024 lds\n"
      "buffer_load_dwordx4 v180, s[12:15], s63 offen offset:2048 lds\n"
      "s_sub_u32 s62, s62, 1\n"
      ".Ld_rs14:\n"
      "s_add_u32 s63, s44, 774144\n"
      "s_cmp_ge_u32 s62, 7\n"
      "s_cselect_b32 s63, s63, s31\n"
      "s_add_u32 m0, s42, 61440\n"
      "s_waitcnt vmcnt(18) lgkmcnt(0)\n"
      "s_barrier\n"
      "ds_read_b64_tr_b16 a[56:57], v20 offset:24576\n"
      "ds_read_b64_tr_b16 a[58:59], v20 offset:26112\n"
      "ds_read_b64_tr_b16 a[152:153], v20 offset:24584\n"
      "ds_read_b64_tr_b16 a[154:155], v20 offset:26120\n"
      "ds_read_b64_tr_b16 a[248:249], v21 offset:24576\n"
      "ds_read_b64_tr_b16 a[250:251], v21 offset:26112\n"
      "buffer_load_dwordx4 v178, s[12:15], s63 offen offset:0 lds\n"
      "buffer_load_dwordx4 v179, s[12:15], s63 offen offset:1024 lds\n"
      "buffer_load_dwordx4 v180, s[12:15], s63 offen offset:2048 lds\n"
      "s_sub_u32 s62, s62, 1\n"
      ".Ld_rs15:\n"
      "s_add_u32 s63, s44, 811008\n"
      "s_cmp_ge_u32 s62, 7\n"
      "s_cselect_b32 s63, s63, s31\n"
      "s_add_u32 m0, s42, 73728\n"
      "s_waitcnt vmcnt(18) lgkmcnt(0)\n"
      "s_barrier\n"
      "ds_read_b64_tr_b16 a[60:61], v20 offset:36864\n"
      "ds_read_b64_tr_b16 a[62:63], v20 offset:38400\n"
      "ds_read_b64_tr_b16 a[156:157], v20 offset:36872\n"
      "ds_read_b64_tr_b16 a[158:159], v20 offset:38408\n"
      "ds_read_b64_tr_b16 a[252:253], v21 offset:36864\n"
      "ds_read_b64_tr_b16 a[254:255], v21 offset:38400\n"
      "buffer_load_dwordx4 v178, s[12:15], s63 offen offset:0 lds\n"
      "buffer_load_dwordx4 v179, s[12:15], s63 offen offset:1024 lds\n"
      "buffer_load_dwordx4 v180, s[12:15], s63 offen offset:2048 lds\n"
      "s_sub_u32 s62, s62, 1\n"
      ".Ld_rs16:\n"
      "s_add_u32 s63, s44, 847872\n"
      "s_cmp_ge_u32 s62, 7\n"
      "s_cselect_b32 s63, s63, s31\n"
      "s_add_u32 m0, s42, 86016\n"
      "s_waitcnt vmcnt(18) lgkmcnt(0)\n"
      "s_barrier\n"
      "ds_read_b64_tr_b16 a[64:65], v176 offset:0\n"
      "ds_read_b64_tr_b16 a[66:67], v176 offset:1536\n"
      "ds_read_b64_tr_b16 a[160:161], v176 offset:8\n"
      "ds_read_b64_tr_b16 a[162:163], v176 offset:1544\n"
      "ds_read_b64_tr_b16 v[24:25], v177 offset:0\n"
      "ds_read_b64_tr_b16 v[26:27], v177 offset:1536\n"
      "buffer_load_dwordx4 v178, s[12:15], s63 offen offset:0 lds\n"
      "buffer_load_dwordx4 v179, s[12:15], s63 offen offset:1024 lds\n"
      "buffer_load_dwordx4 v180, s[12:15], s63 offen offset:2048 lds\n"
      "s_sub_u32 s62, s62, 1\n"
      ".Ld_rs17:\n"
      "s_add_u32 s63, s44, 0\n"
      "s_cmp_ge_u32 s62, 7\n"
      "s_cselect_b32 s63, s63, s31\n"
      "s_add_u32 m0, s42, 0\n"
      "s_waitcnt vmcnt(18) lgkmcnt(0)\n"
      "s_barrier\n"
      "ds_read_b64_tr_b16 a[68:69], v176 offset:12288\n"
      "ds_read_b64_tr_b16 a[70:71], v176 offset:13824\n"
      "ds_read_b64_tr_b16 a[164:165], v176 offset:12296\n"
      "ds_read_b64_tr_b16 a[166:167], v176 offset:13832\n"
      "ds_read_b64_tr_b16 v[28:29], v177 offset:12288\n"
      "ds_read_b64_tr_b16 v[30:31], v177 offset:13824\n"
      "buffer_load_dwordx4 v178, s[12:15], s63 offen offset:0 lds\n"
      "buffer_load_dwordx4 v179, s[12:15], s63 offen offset:1024 lds\n"
      "buffer_load_dwordx4 v180, s[12:15], s63 offen offset:2048 lds\n"
      "s_sub_u32 s62, s62, 1\n"
      "s_cbranch_scc1 .Ld_re\n"
      ".Ld_rs18:\n"
      "s_add_u32 s63, s44, 36864\n"
      "s_cmp_ge_u32 s62, 7\n"
      "s_cselect_b32 s63, s63, s31\n"
      "s_add_u32 m0, s42, 12288\n"
      "s_waitcnt vmcnt(18) lgkmcnt(0)\n"
      "s_barrier\n"
      "ds_read_b64_tr_b16 a[72:73], v176 offset:24576\n"
      "ds_read_b64_tr_b16 a[74:75], v176 offset:26112\n"
      "ds_read_b64_tr_b16 a[168:169], v176 offset:24584\n"
      "ds_read_b64_tr_b16 a[170:171], v176 offset:26120\n"
      "ds_read_b64_tr_b16 v[32:33], v177 offset:24576\n"
      "ds_read_b64_tr_b16 v[34:35], v177 offset:26112\n"
      "buffer_load_dwordx4 v178, s[12:15], s63 offen offset:0 lds\n"
      "buffer_load_dwordx4 v179, s[12:15], s63 offen offset:1024 lds\n"
      "buffer_load_dwordx4 v180, s[12:15], s63 offen offset:2048 lds\n"
      "s_sub_u32 s62, s62, 1\n"
      ".Ld_rs19:\n"
      "s_add_u32 s63, s44, 73728\n"
      "s_cmp_ge_u32 s62, 7\n"
      "s_cselect_b32 s63, s63, s31\n"
      "s_add_u32 m0, s42, 24576\n"
      "s_waitcnt vmcnt(18) lgkmcnt(0)\n"
      "s_barrier\n"
      "ds_read_b64_tr_b16 a[76:77], v176 offset:36864\n"
      "ds_read_b64_tr_b16 a[78:79], v176 offset:38400\n"
      "ds_read_b64_tr_b16 a[172:173], v176 offset:36872\n"
      "ds_read_b64_tr_b16 a[174:175], v176 offset:38408\n"
      "ds_read_b64_tr_b16 v[36:37], v177 offset:36864\n"
      "ds_read_b64_tr_b16 v[38:39], v177 offset:38400\n"
      "buffer_load_dwordx4 v178, s[12:15], s63 offen offset:0 lds\n"
      "buffer_load_dwordx4 v179, s[12:15], s63 offen offset:1024 lds\n"
      "buffer_load_dwordx4 v180, s[12:15], s63 offen offset:2048 lds\n"
      "s_sub_u32 s62, s62, 1\n"
      ".Ld_rs20:\n"
      "s_add_u32 s63, s44, 110592\n"
      "s_cmp_ge_u32 s62, 7\n"
      "s_cselect_b32 s63, s63, s31\n"
      "s_add_u32 m0, s42, 36864\n"
      "s_waitcnt vmcnt(18) lgkmcnt(0)\n"
      "s_barrier\n"
      "ds_read_b64_tr_b16 a[80:81], v20 offset:0\n"
      "ds_read_b64_tr_b16 a[82:83], v20 offset:1536\n"
      "ds_read_b64_tr_b16 a[176:177], v20 offset:8\n"
      "ds_read_b64_tr_b16 a[178:179], v20 offset:1544\n"
      "ds_read_b64_tr_b16 v[40:41], v21 offset:0\n"
      "ds_read_b64_tr_b16 v[42:43], v21 offset:1536\n"
      "buffer_load_dwordx4 v178, s[12:15], s63 offen offset:0 lds\n"
      "buffer_load_dwordx4 v179, s[12:15], s63 offen offset:1024 lds\n"
      "buffer_load_dwordx4 v180, s[12:15], s63 offen offset:2048 lds\n"
      "s_sub_u32 s62, s62, 1\n"
      ".Ld_rs21:\n"
      "s_add_u32 s63, s44, 147456\n"
      "s_cmp_ge_u32 s62, 7\n"
      "s_cselect_b32 s63, s63, s31\n"
      "s_add_u32 m0, s42, 49152\n"
      "s_waitcnt vmcnt(18) lgkmcnt(0)\n"
      "s_barrier\n"
      "ds_read_b64_tr_b16 a[84:85], v20 offset:12288\n"
      "ds_read_b64_tr_b16 a[86:87], v20 offset:13824\n"
      "ds_read_b64_tr_b16 a[180:181], v20 offset:12296\n"
      "ds_read_b64_tr_b16 a[182:183], v20 offset:13832\n"
      "ds_read_b64_tr_b16 v[44:45], v21 offset:12288\n"
      "ds_read_b64_tr_b16 v[46:47], v21 offset:13824\n"
      "buffer_load_dwordx4 v178, s[12:15], s63 offen offset:0 lds\n"
      "buffer_load_dwordx4 v179, s[12:15], s63 offen offset:1024 lds\n"
      "buffer_load_dwordx4 v180, s[12:15], s63 offen offset:2048 lds\n"
      "s_sub_u32 s62, s62, 1\n"
      ".Ld_rs22:\n"
      "s_add_u32 s63, s44, 184320\n"
      "s_cmp_ge_u32 s62, 7\n"
      "s_cselect_b32 s63, s63, s31\n"
      "s_add_u32 m0, s42, 61440\n"
      "s_waitcnt vmcnt(18) lgkmcnt(0)\n"
      "s_barrier\n"
      "ds_read_b64_tr_b16 a[88:89], v20 offset:24576\n"
      "ds_read_b64_tr_b16 a[90:91], v20 offset:26112\n"
      "ds_read_b64_tr_b16 a[184:185], v20 offset:24584\n"
      "ds_read_b64_tr_b16 a[186:187], v20 offset:26120\n"
      "ds_read_b64_tr_b16 v[48:49], v21 offset:24576\n"
      "ds_read_b64_tr_b16 v[50:51], v21 offset:26112\n"
      "buffer_load_dwordx4 v178, s[12:15], s63 offen offset:0 lds\n"
      "buffer_load_dwordx4 v179, s[12:15], s63 offen offset:1024 lds\n"
      "buffer_load_dwordx4 v180, s[12:15], s63 offen offset:2048 lds\n"
      "s_sub_u32 s62, s62, 1\n"
      ".Ld_rs23:\n"
      "s_add_u32 s63, s44, 221184\n"
      "s_cmp_ge_u32 s62, 7\n"
      "s_cselect_b32 s63, s63, s31\n"
      "s_add_u32 m0, s42, 73728\n"
      "s_waitcnt vmcnt(18) lgkmcnt(0)\n"
      "s_barrier\n"
      "ds_read_b64_tr_b16 a[92:93], v20 offset:36864\n"
      "ds_read_b64_tr_b16 a[94:95], v20 offset:38400\n"
      "ds_read_b64_tr_b16 a[188:189], v20 offset:36872\n"
      "ds_read_b64_tr_b16 a[190:191], v20 offset:38408\n"
      "ds_read_b64_tr_b16 v[52:53], v21 offset:36864\n"
      "ds_read_b64_tr_b16 v[54:55], v21 offset:38400\n"
      "buffer_load_dwordx4 v178, s[12:15], s63 offen offset:0 lds\n"
      "buffer_load_dwordx4 v179, s[12:15], s63 offen offset:1024 lds\n"
      "buffer_load_dwordx4 v180, s[12:15], s63 offen offset:2048 lds\n"
      "s_sub_u32 s62, s62, 1\n"
      "s_cbranch_scc0 .Ld_rs0\n"
      ".Ld_re:\n"
      "s_waitcnt vmcnt(0) lgkmcnt(0)\n"
      "v_add_u32 v22, 49152, v0\n"
      "v_add_u32 v23, 98304, v0\n"
      "s_lshr_b32 s58, s42, 10\n"
      "s_mul_i32 s58, s58, 1366\n"
      "s_waitcnt vmcnt(0) lgkmcnt(0)\n"
      "s_mov_b64 exec, 0\n"
      "s_lshr_b32 s22, s58, 12\n"
      ".Ld_pl:\n"
      "s_cmp_eq_u32 s22, 0\n"
      "s_cbranch_scc1 .Ld_p0\n"
      "s_cmp_eq_u32 s22, 1\n"
      "s_cbranch_scc1 .Ld_p1\n"
      "s_branch .Ld_pe\n"
      ".Ld_pr:\n"
      "s_add_u32 s22, s22, 4\n"
      "s_branch .Ld_pl\n"
      ".Ld_pe:\n"
      "s_mov_b64 exec, -1\n"
      "s_barrier\n"
      "v_mov_b32 v14, 0\n"
      "ds_write_b32 v16, v14\n"
      "s_waitcnt lgkmcnt(0)\n"
      "s_barrier\n"
      "s_mov_b32 s6, s50\n"
      "s_add_u32 s36, s36, 4294801408\n"
      "s_add_u32 s53, s37, 0\n"
      "s_add_u32 s52, s37, 4294894080\n"
      "s_add_u32 s37, s37, 4294894080\n"
      "s_mov_b32 s56, 0x40000000\n"
      "s_mov_b32 s57, 0x40000000\n"
      "s_mov_b32 s59, 63\n"
      "s_cmp_eq_u32 s45, 1\n"
      "s_cbranch_scc1 .Ld_in1\n"
      "s_cmp_eq_u32 s45, 2\n"
      "s_cbranch_scc1 .Ld_in2\n"
      "s_cmp_eq_u32 s45, 3\n"
      "s_cbranch_scc1 .Ld_in3\n"
      "s_cmp_eq_u32 s45, 4\n"
      "s_cbranch_scc1 .Ld_in4\n"
      "s_cmp_eq_u32 s45, 5\n"
      "s_cbranch_scc1 .Ld_in5\n"
      "s_add_u32 s36, s36, 165888\n"
      "s_min_u32 s36, s36, s6\n"
      "buffer_load_dword v20, v2, s[8:11], s36 offen sc1\n"
      "s_mov_b32 s51, s36\n"
      "s_add_u32 s36, s36, 165888\n"
      "s_add_u32 m0, s43, 0\n"
      "s_min_u32 s36, s36, s6\n"
      "s_waitcnt vmcnt(0)\n"
      "v_cmp_ne_u32 vcc, s21, v20\n"
      "s_cbranch_vccnz .Lsp_d0_1\n"
      ".Lbk_d0_1:\n"
      "buffer_load_dwordx4 v0, s[8:11], s51 offen offset:0 sc1 lds\n"
      "buffer_load_dwordx4 v0, s[8:11], s51 offen offset:1024 sc1 lds\n"
      "buffer_load_dwordx4 v0, s[8:11], s51 offen offset:3072 sc1 lds\n"
      "buffer_load_dwordx4 v0, s[8:11], s51 offen offset:2048 sc1 lds\n"
      "s_add_u32 m0, s43, 4096\n"
      "s_nop 0\n"
      "buffer_load_dwordx4 v1, s[8:11], s51 offen offset:0 sc1 lds\n"
      "buffer_load_dword v20, v2, s[8:11], s36 offen sc1\n"
      "buffer_load_dwordx4 v1, s[8:11], s51 offen offset:1024 sc1 lds\n"
      "s_mov_b32 s51, s36\n"
      "buffer_load_dwordx4 v[216:219], v6, s[8:11], s38 offen sc1\n"
      "s_add_u32 s36, s36, 165888\n"
      "s_add_u32 m0, s43, 24576\n"
      "buffer_load_dwordx2 v[220:221], v7, s[8:11], s38 offen sc1\n"
      "v_add_u32 v14, 1, v14\n"
      "s_waitcnt vmcnt(3)\n"
      "v_cmp_ne_u32 vcc, s21, v20\n"
      "s_waitcnt vmcnt(2)\n"
      "ds_write_b32 v16, v14\n"
      "s_cbranch_vccnz .Lsp_d1_2\n"
      ".Lbk_d1_2:\n"
      "buffer_load_dwordx4 v0, s[8:11], s51 offen offset:0 sc1 lds\n"
      "s_min_u32 s36, s36, s6\n"
      "buffer_load_dwordx4 v0, s[8:11], s51 offen offset:1024 sc1 lds\n"
      "ds_read_b32 v15, v17\n"
      "buffer_load_dword v20, v2, s[8:11], s36 offen sc1\n"
      "s_waitcnt lgkmcnt(0)\n"
      "v_cmp_gt_i32 vcc, v14, v15\n"
      "buffer_load_dwordx4 v0, s[8:11], s51 offen offset:2048 sc1 lds\n"
      "s_cbranch_vccnz .Lsp_dy0_3\n"
      ".Lbk_dy0_3:\n"
      "ds_read_b128 v[64:67], v0 offset:2048\n"
      "buffer_load_dwordx4 v0, s[8:11], s51 offen offset:3072 sc1 lds\n"
      "ds_read_b128 v[56:59], v0 offset:0\n"
      "s_add_u32 m0, s43, 28672\n"
      "ds_read_b128 v[76:79], v0 offset:5120\n"
      "ds_read_b128 v[60:63], v0 offset:1024\n"
      "ds_read_b128 v[80:83], v0 offset:6144\n"
      "buffer_load_dwordx4 v1, s[8:11], s51 offen offset:0 sc1 lds\n"
      "ds_read_b128 v[84:87], v0 offset:7168\n"
      "buffer_load_dwordx4 v1, s[8:11], s51 offen offset:1024 sc1 lds\n"
      "ds_read_b128 v[100:103], v0 offset:11264\n"
      "ds_read_b128 v[96:99], v0 offset:10240\n"
      "ds_read_b128 v[72:75], v0 offset:4096\n"
      "ds_read_b128 v[104:107], v0 offset:12288\n"
      "ds_read_b128 v[68:71], v0 offset:3072\n"
      "ds_read_b128 v[112:115], v0 offset:14336\n"
      "ds_read_b128 v[132:135], v0 offset:19456\n"
      "ds_read_b128 v[92:95], v0 offset:9216\n"
      "s_waitcnt vmcnt(0) lgkmcnt(0)\n"
      "s_nop 4\n"
      "s_branch .Ld_t0\n"
      ".p2align 12\n"
      ".Ld_p0:\n"
      "s_branch .Ld_pr\n"
      ".p2align 5\n"
      ".Ld_t0:\n"
      "v_mfma_f32_16x16x32_bf16 v[152:155], a[0:3], v[56:59], 0\n"
      "ds_read_b128 v[88:91], v0 offset:8192\n"
      "v_add_f32 v173, v173, v204\n"
      "v_mfma_f32_16x16x32_bf16 v[152:155], a[4:7], v[60:63], v[152:155]\n"
      "v_add_f32 v173, v173, v212\n"
      "v_add_f32 v236, v236, v170\n"
      "v_mfma_f32_16x16x32_bf16 v[152:155], a[8:11], v[64:67], v[152:155]\n"
      "s_waitcnt lgkmcnt(3)\n"
      "ds_read_b128 v[116:119], v0 offset:15360\n"
      "v_cvt_pk_bf16_f32 v229, v162, v163\n"
      "v_mfma_f32_16x16x32_bf16 v[152:155], a[12:15], v[68:71], v[152:155]\n"
      "ds_read_b128 v[108:111], v0 offset:13312\n"
      "ds_read_b128 v[124:127], v0 offset:17408\n"
      "v_mfma_f32_16x16x32_bf16 v[152:155], a[16:19], v[72:75], v[152:155]\n"
      "ds_read_b128 v[148:151], v0 offset:23552\n"
      "v_cvt_pk_bf16_f32 v228, v160, v161\n"
      "v_mfma_f32_16x16x32_bf16 v[152:155], a[20:23], v[76:79], v[152:155]\n"
      "buffer_store_dwordx2 v[228:229], v13, s[8:11], s37 offen sc1\n"
      "v_mfma_f32_16x16x32_bf16 v[152:155], a[24:27], v[80:83], v[152:155]\n"
      "ds_read_b128 v[128:131], v0 offset:18432\n"
      "v_cvt_pk_bf16_f32 v179, v170, v171\n"
      "v_mfma_f32_16x16x32_bf16 v[152:155], a[28:31], v[84:87], v[152:155]\n"
      "s_waitcnt lgkmcnt(5)\n"
      "ds_read_b128 v[120:123], v0 offset:16384\n"
      "v_add_f32 v236, v236, v171\n"
      "v_mfma_f32_16x16x32_bf16 v[152:155], a[32:35], v[88:91], v[152:155]\n"
      "ds_read_b128 v[136:139], v0 offset:20480\n"
      "v_fmac_f32 v237, v173, v173\n"
      "v_mfma_f32_16x16x32_bf16 v[152:155], a[36:39], v[92:95], v[152:155]\n"
      "buffer_store_dwordx4 v[176:179], v6, s[8:11], s56 offen sc1\n"
      "v_mfma_f32_16x16x32_bf16 v[152:155], a[40:43], v[96:99], v[152:155]\n"
      "s_mov_b32 s51, s36\n"
      "ds_read_b128 v[144:147], v0 offset:22528\n"
      "v_add_f32 v236, v236, v172\n"
      "v_mfma_f32_16x16x32_bf16 v[152:155], a[44:47], v[100:103], v[152:155]\n"
      "s_add_u32 m0, s43, 49152\n"
      "ds_read_b128 v[140:143], v0 offset:21504\n"
      "v_fmac_f32 v237, v174, v174\n"
      "v_mfma_f32_16x16x32_bf16 v[152:155], a[48:51], v[104:107], v[152:155]\n"
      "s_waitcnt lgkmcnt(5)\n"
      "v_add_f32 v175, v175, v205\n"
      "v_add_f32 v236, v236, v173\n"
      "v_mfma_f32_16x16x32_bf16 v[152:155], a[52:55], v[108:111], v[152:155]\n"
      "v_add_f32 v175, v175, v213\n"
      "v_add_f32 v236, v236, v174\n"
      "v_mfma_f32_16x16x32_bf16 v[152:155], a[56:59], v[112:115], v[152:155]\n"
      "v_add_f32 v236, v236, v175\n"
      "v_fmac_f32 v237, v175, v175\n"
      "v_mfma_f32_16x16x32_bf16 v[152:155], a[60:63], v[116:119], v[152:155]\n"
      "s_waitcnt lgkmcnt(2)\n"
      "ds_write_b64 v10, v[236:237]\n"
      "v_mfma_f32_16x16x32_bf16 v[152:155], a[64:67], v[120:123], v[152:155]\n"
      "buffer_store_dwordx4 v[176:179], v8, s[16:19], s57 offen\n"
      "v_mfma_f32_16x16x32_bf16 v[152:155], a[68:71], v[124:127], v[152:155]\n"
      "v_cvt_pk_bf16_f32 v180, v172, v173\n"
      "v_mfma_f32_16x16x32_bf16 v[152:155], a[72:75], v[128:131], v[152:155]\n"
      "s_waitcnt lgkmcnt(0)\n"
      "s_barrier\n"
      "v_mfma_f32_16x16x32_bf16 v[152:155], a[76:79], v[132:135], v[152:155]\n"
      "ds_read_b64 v[238:239], v11\n"
      "s_add_u32 s38, s38, 147456\n"
      "v_mfma_f32_16x16x32_bf16 v[152:155], a[80:83], v[136:139], v[152:155]\n"
      "v_cvt_pk_bf16_f32 v181, v174, v175\n"
      "v_mfma_f32_16x16x32_bf16 v[152:155], a[84:87], v[140:143], v[152:155]\n"
      "v_lshlrev_b32 v166, 16, v217\n"
      "v_mfma_f32_16x16x32_bf16 v[152:155], a[88:91], v[144:147], v[152:155]\n"
      "s_waitcnt vmcnt(9)\n"
      "v_cmp_ne_u32 vcc, s21, v20\n"
      "v_mfma_f32_16x16x32_bf16 v[152:155], a[92:95], v[148:151], v[152:155]\n"
      "buffer_store_dwordx2 v[180:181], v7, s[8:11], s56 offen sc1\n"
      "v_mfma_f32_16x16x32_bf16 v[156:159], a[96:99], v[56:59], 0\n"
      "v_and_b32 v167, 0xffff0000, v217\n"
      "v_mfma_f32_16x16x32_bf16 v[156:159], a[100:103], v[60:63], v[156:159]\n"
      "v_add_u32_e64 v14, 1, v14\n"
      "v_mfma_f32_16x16x32_bf16 v[156:159], a[104:107], v[64:67], v[156:159]\n"
      "s_waitcnt lgkmcnt(0)\n"
      "s_cbranch_vccnz .Lsp_d2_4\n"
      ".Lbk_d2_4:\n"
      "v_add_f32_dpp v238, v238, v238 row_ror:8 row_mask:0xf bank_mask:0xf\n"
      "v_mfma_f32_16x16x32_bf16 v[156:159], a[108:111], v[68:71], v[156:159]\n"
      "buffer_store_dwordx2 v[180:181], v9, s[16:19], s57 offen\n"
      "v_mfma_f32_16x16x32_bf16 v[156:159], a[112:115], v[72:75], v[156:159]\n"
      "v_add_f32_dpp v239, v239, v239 row_ror:8 row_mask:0xf bank_mask:0xf\n"
      "v_add_f32_dpp v238, v238, v238 row_ror:4 row_mask:0xf bank_mask:0xf\n"
      "v_mfma_f32_16x16x32_bf16 v[156:159], a[116:119], v[76:79], v[156:159]\n"
      "v_add_f32_dpp v238, v238, v238 row_ror:2 row_mask:0xf bank_mask:0xf\n"
      "v_add_f32_dpp v239, v239, v239 row_ror:4 row_mask:0xf bank_mask:0xf\n"
      "v_mfma_f32_16x16x32_bf16 v[156:159], a[120:123], v[80:83], v[156:159]\n"
      "s_add_u32 s36, s36, 165888\n"
      "v_add_f32_dpp v238, v238, v238 row_ror:1 row_mask:0xf bank_mask:0xf\n"
      "v_add_f32_dpp v239, v239, v239 row_ror:2 row_mask:0xf bank_mask:0xf\n"
      "v_mfma_f32_16x16x32_bf16 v[156:159], a[124:127], v[84:87], v[156:159]\n"
      "buffer_load_dwordx4 v0, s[8:11], s51 offen offset:2048 sc1 lds\n"
      "v_mfma_f32_16x16x32_bf16 v[156:159], a[128:131], v[88:91], v[156:159]\n"
      "s_waitcnt vmcnt(8)\n"
      "ds_write_b32 v16, v14\n"
      "v_mfma_f32_16x16x32_bf16 v[156:159], a[132:135], v[92:95], v[156:159]\n"
      "v_add_f32_dpp v239, v239, v239 row_ror:1 row_mask:0xf bank_mask:0xf\n"
      "v_mul_f32 v238, 0x3ae38e39, v238\n"
      "v_mfma_f32_16x16x32_bf16 v[156:159], a[136:139], v[96:99], v[156:159]\n"
      "v_add_f32 v239, 0x3afba882, v239\n"
      "v_mul_f32 v239, 0x3ae38e39, v239\n"
      "v_mfma_f32_16x16x32_bf16 v[156:159], a[140:143], v[100:103], v[156:159]\n"
      "buffer_store_dwordx2 v[238:239], v12, s[8:11], s56 offen sc1\n"
      "v_mfma_f32_16x16x32_bf16 v[156:159], a[144:147], v[104:107], v[156:159]\n"
      "v_mfma_f32_16x16x32_bf16 v[156:159], a[148:151], v[108:111], v[156:159]\n"
      "v_mfma_f32_16x16x32_bf16 v[156:159], a[152:155], v[112:115], v[156:159]\n"
      "v_and_b32 v171, 0xffff0000, v219\n"
      "v_mfma_f32_16x16x32_bf16 v[156:159], a[156:159], v[116:119], v[156:159]\n"
      "buffer_load_dwordx4 v0, s[8:11], s51 offen offset:0 sc1 lds\n"
      "v_mfma_f32_16x16x32_bf16 v[156:159], a[160:163], v[120:123], v[156:159]\n"
      "v_mfma_f32_16x16x32_bf16 v[156:159], a[164:167], v[124:127], v[156:159]\n"
      "ds_read_b32 v15, v17\n"
      "v_mfma_f32_16x16x32_bf16 v[156:159], a[168:171], v[128:131], v[156:159]\n"
      "v_mfma_f32_16x16x32_bf16 v[156:159], a[172:175], v[132:135], v[156:159]\n"
      "buffer_load_dwordx4 v0, s[8:11], s51 offen offset:1024 sc1 lds\n"
      "v_mfma_f32_16x16x32_bf16 v[156:159], a[176:179], v[136:139], v[156:159]\n"
      "v_and_b32 v165, 0xffff0000, v216\n"
      "v_lshlrev_b32 v164, 16, v216\n"
      "v_mfma_f32_16x16x32_bf16 v[156:159], a[180:183], v[140:143], v[156:159]\n"
      "v_add_f32 v164, v164, v152\n"
      "v_and_b32 v169, 0xffff0000, v218\n"
      "v_mfma_f32_16x16x32_bf16 v[156:159], a[184:187], v[144:147], v[156:159]\n"
      "v_mfma_f32_16x16x32_bf16 v[156:159], a[188:191], v[148:151], v[156:159]\n"
      "s_waitcnt lgkmcnt(0)\n"
      "v_cmp_gt_i32 vcc, v14, v15\n"
      "v_add_f32 v167, v167, v155\n"
      "v_mfma_f32_16x16x32_bf16 v[160:163], a[192:195], v[56:59], 0\n"
      "buffer_load_dwordx4 v0, s[8:11], s51 offen offset:3072 sc1 lds\n"
      "v_mfma_f32_16x16x32_bf16 v[160:163], a[196:199], v[60:63], v[160:163]\n"
      "v_lshlrev_b32 v168, 16, v218\n"
      "v_mfma_f32_16x16x32_bf16 v[160:163], a[200:203], v[64:67], v[160:163]\n"
      "s_add_u32 m0, s43, 53248\n"
      "s_cbranch_vccnz .Lsp_dy1_5\n"
      ".Lbk_dy1_5:\n"
      "v_mfma_f32_16x16x32_bf16 v[160:163], a[204:207], v[68:71], v[160:163]\n"
      "ds_read_b128 v[64:67], v0 offset:26624\n"
      "v_mfma_f32_16x16x32_bf16 v[160:163], a[208:211], v[72:75], v[160:163]\n"
      "buffer_load_dwordx4 v1, s[8:11], s51 offen offset:0 sc1 lds\n"
      "v_mfma_f32_16x16x32_bf16 v[160:163], a[212:215], v[76:79], v[160:163]\n"
      "ds_read_b128 v[72:75], v0 offset:28672\n"
      "s_min_u32 s36, s36, s6\n"
      "v_mfma_f32_16x16x32_bf16 v[160:163], a[216:219], v[80:83], v[160:163]\n"
      "v_add_f32 v166, v166, v154\n"
      "v_mfma_f32_16x16x32_bf16 v[160:163], a[220:223], v[84:87], v[160:163]\n"
      "ds_read_b128 v[80:83], v0 offset:30720\n"
      "v_mfma_f32_16x16x32_bf16 v[160:163], a[224:227], v[88:91], v[160:163]\n"
      "buffer_load_dwordx4 v1, s[8:11], s51 offen offset:1024 sc1 lds\n"
      "v_mfma_f32_16x16x32_bf16 v[160:163], a[228:231], v[92:95], v[160:163]\n"
      "ds_read_b128 v[60:63], v0 offset:25600\n"
      "v_mfma_f32_16x16x32_bf16 v[160:163], a[232:235], v[96:99], v[160:163]\n"
      "ds_read_b128 v[56:59], v0 offset:24576\n"
      "v_mfma_f32_16x16x32_bf16 v[160:163], a[236:239], v[100:103], v[160:163]\n"
      "ds_read_b128 v[100:103], v0 offset:35840\n"
      "v_mfma_f32_16x16x32_bf16 v[160:163], a[240:243], v[104:107], v[160:163]\n"
      "buffer_load_dword v20, v2, s[8:11], s36 offen sc1\n"
      "v_mfma_f32_16x16x32_bf16 v[160:163], a[244:247], v[108:111], v[160:163]\n"
      "ds_read_b128 v[88:91], v0 offset:32768\n"
      "v_mfma_f32_16x16x32_bf16 v[160:163], a[248:251], v[112:115], v[160:163]\n"
      "ds_read_b128 v[76:79], v0 offset:29696\n"
      "v_mfma_f32_16x16x32_bf16 v[160:163], a[252:255], v[116:119], v[160:163]\n"
      "s_waitcnt vmcnt(12) lgkmcnt(5)\n"
      "buffer_store_dword v18, v4, s[8:11], s52 offen sc1\n"
      "v_mfma_f32_16x16x32_bf16 v[160:163], v[24:27], v[120:123], v[160:163]\n"
      "ds_read_b128 v[68:71], v0 offset:27648\n"
      "s_add_u32 s52, s52, 173056\n"
      "v_mfma_f32_16x16x32_bf16 v[160:163], v[28:31], v[124:127], v[160:163]\n"
      "ds_read_b128 v[92:95], v0 offset:33792\n"
      "v_mfma_f32_16x16x32_bf16 v[160:163], v[32:35], v[128:131], v[160:163]\n"
      "v_add_f32_e64 v171, v171, v159\n"
      "v_mfma_f32_16x16x32_bf16 v[160:163], v[36:39], v[132:135], v[160:163]\n"
      "v_and_b32 v173, 0xffff0000, v220\n"
      "v_mfma_f32_16x16x32_bf16 v[160:163], v[40:43], v[136:139], v[160:163]\n"
      "v_mfma_f32_16x16x32_bf16 v[160:163], v[44:47], v[140:143], v[160:163]\n"
      "ds_read_b128 v[104:107], v0 offset:36864\n"
      "v_mfma_f32_16x16x32_bf16 v[160:163], v[48:51], v[144:147], v[160:163]\n"
      "s_waitcnt lgkmcnt(3)\n"
      "ds_read_b128 v[84:87], v0 offset:31744\n"
      "v_add_f32_e64 v165, v165, v153\n"
      "v_mfma_f32_16x16x32_bf16 v[160:163], v[52:55], v[148:151], v[160:163]\n"
      "s_sub_u32 s59, s59, 1\n"
      "s_cbranch_scc1 .Ld_out0\n"
      ".Ld_t1:\n"
      "v_mfma_f32_16x16x32_bf16 v[152:155], a[0:3], v[56:59], 0\n"
      "ds_read_b128 v[112:115], v0 offset:38912\n"
      "s_mov_b32 s51, s36\n"
      "v_mfma_f32_16x16x32_bf16 v[152:155], a[4:7], v[60:63], v[152:155]\n"
      "ds_read_b128 v[132:135], v0 offset:44032\n"
      "v_mfma_f32_16x16x32_bf16 v[152:155], a[8:11], v[64:67], v[152:155]\n"
      "s_waitcnt lgkmcnt(4)\n"
      "ds_read_b128 v[96:99], v0 offset:34816\n"
      "v_mfma_f32_16x16x32_bf16 v[152:155], a[12:15], v[68:71], v[152:155]\n"
      "s_add_u32 s37, s37, 173056\n"
      "ds_read_b128 v[108:111], v0 offset:37888\n"
      "v_mfma_f32_16x16x32_bf16 v[152:155], a[16:19], v[72:75], v[152:155]\n"
      "ds_read_b128 v[116:119], v0 offset:39936\n"
      "v_mfma_f32_16x16x32_bf16 v[152:155], a[20:23], v[76:79], v[152:155]\n"
      "ds_read_b128 v[124:127], v0 offset:41984\n"
      "v_add_f32 v169, v169, v157\n"
      "v_mfma_f32_16x16x32_bf16 v[152:155], a[24:27], v[80:83], v[152:155]\n"
      "s_waitcnt lgkmcnt(6)\n"
      "ds_read_b128 v[136:139], v0 offset:45056\n"
      "v_mfma_f32_16x16x32_bf16 v[152:155], a[28:31], v[84:87], v[152:155]\n"
      "v_mfma_f32_16x16x32_bf16 v[152:155], a[32:35], v[88:91], v[152:155]\n"
      "ds_read_b128 v[120:123], v0 offset:40960\n"
      "v_mfma_f32_16x16x32_bf16 v[152:155], a[36:39], v[92:95], v[152:155]\n"
      "s_waitcnt lgkmcnt(5)\n"
      "ds_read_b128 v[128:131], v0 offset:43008\n"
      "v_mfma_f32_16x16x32_bf16 v[152:155], a[40:43], v[96:99], v[152:155]\n"
      "ds_read_b128 v[144:147], v0 offset:47104\n"
      "v_mfma_f32_16x16x32_bf16 v[152:155], a[44:47], v[100:103], v[152:155]\n"
      "s_waitcnt vmcnt(7) lgkmcnt(5)\n"
      "buffer_store_dword v18, v5, s[8:11], s56 offen sc1\n"
      "v_mfma_f32_16x16x32_bf16 v[152:155], a[48:51], v[104:107], v[152:155]\n"
      "v_mfma_f32_16x16x32_bf16 v[152:155], a[52:55], v[108:111], v[152:155]\n"
      "ds_read_b128 v[140:143], v0 offset:46080\n"
      "v_mfma_f32_16x16x32_bf16 v[152:155], a[56:59], v[112:115], v[152:155]\n"
      "v_lshlrev_b32 v170, 16, v219\n"
      "v_mfma_f32_16x16x32_bf16 v[152:155], a[60:63], v[116:119], v[152:155]\n"
      "s_waitcnt lgkmcnt(3)\n"
      "ds_read_b128 v[148:151], v0 offset:48128\n"
      "v_add_u32 v14, 1, v14\n"
      "v_mfma_f32_16x16x32_bf16 v[152:155], a[64:67], v[120:123], v[152:155]\n"
      "v_add_f32 v168, v168, v156\n"
      "v_and_b32 v175, 0xffff0000, v221\n"
      "v_mfma_f32_16x16x32_bf16 v[152:155], a[68:71], v[124:127], v[152:155]\n"
      "s_waitcnt lgkmcnt(2)\n"
      "v_mfma_f32_16x16x32_bf16 v[152:155], a[72:75], v[128:131], v[152:155]\n"
      "s_add_u32 s36, s36, 165888\n"
      "v_mfma_f32_16x16x32_bf16 v[152:155], a[76:79], v[132:135], v[152:155]\n"
      "v_mfma_f32_16x16x32_bf16 v[152:155], a[80:83], v[136:139], v[152:155]\n"
      "s_waitcnt lgkmcnt(1)\n"
      "s_add_u32 m0, s43, 73728\n"
      "v_mfma_f32_16x16x32_bf16 v[152:155], a[84:87], v[140:143], v[152:155]\n"
      "v_add_f32 v170, v170, v158\n"
      "v_add_f32 v173, v173, v161\n"
      "v_mfma_f32_16x16x32_bf16 v[152:155], a[88:91], v[144:147], v[152:155]\n"
      "s_waitcnt lgkmcnt(0)\n"
      "v_mfma_f32_16x16x32_bf16 v[152:155], a[92:95], v[148:151], v[152:155]\n"
      "v_mfma_f32_16x16x32_bf16 v[156:159], a[96:99], v[56:59], 0\n"
      "v_lshlrev_b32 v174, 16, v221\n"
      "v_mfma_f32_16x16x32_bf16 v[156:159], a[100:103], v[60:63], v[156:159]\n"
      "v_mfma_f32_16x16x32_bf16 v[156:159], a[104:107], v[64:67], v[156:159]\n"
      "v_add_f32 v174, v174, v162\n"
      "v_mfma_f32_16x16x32_bf16 v[156:159], a[108:111], v[68:71], v[156:159]\n"
      "v_lshlrev_b32 v172, 16, v220\n"
      "v_mfma_f32_16x16x32_bf16 v[156:159], a[112:115], v[72:75], v[156:159]\n"
      "s_waitcnt vmcnt(3)\n"
      "ds_write_b32 v16, v14\n"
      "v_mfma_f32_16x16x32_bf16 v[156:159], a[116:119], v[76:79], v[156:159]\n"
      "v_mfma_f32_16x16x32_bf16 v[156:159], a[120:123], v[80:83], v[156:159]\n"
      "v_mfma_f32_16x16x32_bf16 v[156:159], a[124:127], v[84:87], v[156:159]\n"
      "v_mfma_f32_16x16x32_bf16 v[156:159], a[128:131], v[88:91], v[156:159]\n"
      "s_waitcnt vmcnt(2)\n"
      "v_cmp_ne_u32 vcc, s21, v20\n"
      "v_mfma_f32_16x16x32_bf16 v[156:159], a[132:135], v[92:95], v[156:159]\n"
      "v_add_f32 v172, v172, v160\n"
      "v_cvt_pk_bf16_f32 v224, v152, v153\n"
      "v_mfma_f32_16x16x32_bf16 v[156:159], a[136:139], v[96:99], v[156:159]\n"
      "v_mfma_f32_16x16x32_bf16 v[156:159], a[140:143], v[100:103], v[156:159]\n"
      "s_cbranch_vccnz .Lsp_d3_6\n"
      ".Lbk_d3_6:\n"
      "v_mfma_f32_16x16x32_bf16 v[156:159], a[144:147], v[104:107], v[156:159]\n"
      "buffer_load_dwordx4 v0, s[8:11], s51 offen offset:1024 sc1 lds\n"
      "v_mfma_f32_16x16x32_bf16 v[156:159], a[148:151], v[108:111], v[156:159]\n"
      "ds_read_b32 v15, v17\n"
      "v_mfma_f32_16x16x32_bf16 v[156:159], a[152:155], v[112:115], v[156:159]\n"
      "v_mfma_f32_16x16x32_bf16 v[156:159], a[156:159], v[116:119], v[156:159]\n"
      "v_mfma_f32_16x16x32_bf16 v[156:159], a[160:163], v[120:123], v[156:159]\n"
      "buffer_load_dwordx4 v0, s[8:11], s51 offen offset:0 sc1 lds\n"
      "v_mfma_f32_16x16x32_bf16 v[156:159], a[164:167], v[124:127], v[156:159]\n"
      "v_mfma_f32_16x16x32_bf16 v[156:159], a[168:171], v[128:131], v[156:159]\n"
      "v_mfma_f32_16x16x32_bf16 v[156:159], a[172:175], v[132:135], v[156:159]\n"
      "s_min_u32 s36, s36, s6\n"
      "v_mfma_f32_16x16x32_bf16 v[156:159], a[176:179], v[136:139], v[156:159]\n"
      "s_waitcnt lgkmcnt(0)\n"
      "v_cmp_gt_i32 vcc, v14, v15\n"
      "v_mfma_f32_16x16x32_bf16 v[156:159], a[180:183], v[140:143], v[156:159]\n"
      "buffer_load_dwordx4 v0, s[8:11], s51 offen offset:3072 sc1 lds\n"
      "v_mfma_f32_16x16x32_bf16 v[156:159], a[184:187], v[144:147], v[156:159]\n"
      "v_mfma_f32_16x16x32_bf16 v[156:159], a[188:191], v[148:151], v[156:159]\n"
      "s_cbranch_vccnz .Lsp_dy2_7\n"
      ".Lbk_dy2_7:\n"
      "v_add_f32 v175, v175, v163\n"
      "v_mfma_f32_16x16x32_bf16 v[160:163], a[192:195], v[56:59], 0\n"
      "v_mfma_f32_16x16x32_bf16 v[160:163], a[196:199], v[60:63], v[160:163]\n"
      "buffer_load_dword v20, v2, s[8:11], s36 offen sc1\n"
      "v_mfma_f32_16x16x32_bf16 v[160:163], a[200:203], v[64:67], v[160:163]\n"
      "v_mfma_f32_16x16x32_bf16 v[160:163], a[204:207], v[68:71], v[160:163]\n"
      "buffer_load_dwordx4 v0, s[8:11], s51 offen offset:2048 sc1 lds\n"
      "v_mfma_f32_16x16x32_bf16 v[160:163], a[208:211], v[72:75], v[160:163]\n"
      "ds_read_b128 v[68:71], v22 offset:3072\n"
      "v_mfma_f32_16x16x32_bf16 v[160:163], a[212:215], v[76:79], v[160:163]\n"
      "ds_read_b128 v[64:67], v22 offset:2048\n"
      "v_mfma_f32_16x16x32_bf16 v[160:163], a[216:219], v[80:83], v[160:163]\n"
      "ds_read_b128 v[60:63], v22 offset:1024\n"
      "s_add_u32 m0, s43, 77824\n"
      "v_mfma_f32_16x16x32_bf16 v[160:163], a[220:223], v[84:87], v[160:163]\n"
      "buffer_load_dwordx4 v1, s[8:11], s51 offen offset:1024 sc1 lds\n"
      "v_mfma_f32_16x16x32_bf16 v[160:163], a[224:227], v[88:91], v[160:163]\n"
      "ds_read_b128 v[56:59], v22 offset:0\n"
      "v_mfma_f32_16x16x32_bf16 v[160:163], a[228:231], v[92:95], v[160:163]\n"
      "v_cvt_pk_bf16_f32 v227, v158, v159\n"
      "v_mfma_f32_16x16x32_bf16 v[160:163], a[232:235], v[96:99], v[160:163]\n"
      "ds_read_b128 v[84:87], v22 offset:7168\n"
      "v_mfma_f32_16x16x32_bf16 v[160:163], a[236:239], v[100:103], v[160:163]\n"
      "ds_read_b128 v[96:99], v22 offset:10240\n"
      "v_mfma_f32_16x16x32_bf16 v[160:163], a[240:243], v[104:107], v[160:163]\n"
      "buffer_load_dwordx4 v1, s[8:11], s51 offen offset:0 sc1 lds\n"
      "v_mfma_f32_16x16x32_bf16 v[160:163], a[244:247], v[108:111], v[160:163]\n"
      "ds_read_b128 v[108:111], v22 offset:13312\n"
      "v_mfma_f32_16x16x32_bf16 v[160:163], a[248:251], v[112:115], v[160:163]\n"
      "v_cvt_pk_bf16_f32 v225, v154, v155\n"
      "v_mfma_f32_16x16x32_bf16 v[160:163], a[252:255], v[116:119], v[160:163]\n"
      "ds_read_b128 v[88:91], v22 offset:8192\n"
      "v_cvt_pk_bf16_f32 v226, v156, v157\n"
      "v_mfma_f32_16x16x32_bf16 v[160:163], v[24:27], v[120:123], v[160:163]\n"
      "ds_read_b128 v[72:75], v22 offset:4096\n"
      "v_mfma_f32_16x16x32_bf16 v[160:163], v[28:31], v[124:127], v[160:163]\n"
      "ds_read_b128 v[76:79], v22 offset:5120\n"
      "v_mfma_f32_16x16x32_bf16 v[160:163], v[32:35], v[128:131], v[160:163]\n"
      "ds_read_b128 v[104:107], v22 offset:12288\n"
      "v_mfma_f32_16x16x32_bf16 v[160:163], v[36:39], v[132:135], v[160:163]\n"
      "ds_read_b128 v[100:103], v22 offset:11264\n"
      "v_mfma_f32_16x16x32_bf16 v[160:163], v[40:43], v[136:139], v[160:163]\n"
      "ds_read_b128 v[80:83], v22 offset:6144\n"
      "v_mfma_f32_16x16x32_bf16 v[160:163], v[44:47], v[140:143], v[160:163]\n"
      "ds_read_b128 v[124:127], v22 offset:17408\n"
      "v_mfma_f32_16x16x32_bf16 v[160:163], v[48:51], v[144:147], v[160:163]\n"
      "s_waitcnt lgkmcnt(6)\n"
      "buffer_store_dwordx4 v[224:227], v0, s[8:11], s37 offen sc1\n"
      "v_mfma_f32_16x16x32_bf16 v[160:163], v[52:55], v[148:151], v[160:163]\n"
      "s_sub_u32 s59, s59, 1\n"
      "s_cbranch_scc1 .Ld_out1\n"
      ".Ld_t2:\n"
      "v_mfma_f32_16x16x32_bf16 v[152:155], a[0:3], v[56:59], 0\n"
      "v_mfma_f32_16x16x32_bf16 v[152:155], a[4:7], v[60:63], v[152:155]\n"
      "ds_read_b128 v[92:95], v22 offset:9216\n"
      "v_mfma_f32_16x16x32_bf16 v[152:155], a[8:11], v[64:67], v[152:155]\n"
      "ds_read_b128 v[132:135], v22 offset:19456\n"
      "v_cvt_pk_bf16_f32 v228, v160, v161\n"
      "v_mfma_f32_16x16x32_bf16 v[152:155], a[12:15], v[68:71], v[152:155]\n"
      "s_waitcnt lgkmcnt(3)\n"
      "ds_read_b128 v[136:139], v22 offset:20480\n"
      "v_mfma_f32_16x16x32_bf16 v[152:155], a[16:19], v[72:75], v[152:155]\n"
      "ds_read_b128 v[116:119], v22 offset:15360\n"
      "v_mfma_f32_16x16x32_bf16 v[152:155], a[20:23], v[76:79], v[152:155]\n"
      "ds_read_b128 v[120:123], v22 offset:16384\n"
      "s_mov_b32 s51, s36\n"
      "v_mfma_f32_16x16x32_bf16 v[152:155], a[24:27], v[80:83], v[152:155]\n"
      "ds_read_b128 v[112:115], v22 offset:14336\n"
      "s_add_u32 m0, s43, 98304\n"
      "v_mfma_f32_16x16x32_bf16 v[152:155], a[28:31], v[84:87], v[152:155]\n"
      "ds_read_b128 v[128:131], v22 offset:18432\n"
      "v_cvt_pk_bf16_f32 v229, v162, v163\n"
      "v_mfma_f32_16x16x32_bf16 v[152:155], a[32:35], v[88:91], v[152:155]\n"
      "s_waitcnt lgkmcnt(6)\n"
      "ds_read_b128 v[144:147], v22 offset:22528\n"
      "v_mfma_f32_16x16x32_bf16 v[152:155], a[36:39], v[92:95], v[152:155]\n"
      "ds_read_b128 v[148:151], v22 offset:23552\n"
      "v_mfma_f32_16x16x32_bf16 v[152:155], a[40:43], v[96:99], v[152:155]\n"
      "v_mfma_f32_16x16x32_bf16 v[152:155], a[44:47], v[100:103], v[152:155]\n"
      "buffer_store_dwordx2 v[228:229], v13, s[8:11], s37 offen sc1\n"
      "v_mfma_f32_16x16x32_bf16 v[152:155], a[48:51], v[104:107], v[152:155]\n"
      "ds_read_b128 v[140:143], v22 offset:21504\n"
      "v_mfma_f32_16x16x32_bf16 v[152:155], a[52:55], v[108:111], v[152:155]\n"
      "s_waitcnt lgkmcnt(4)\n"
      "v_mfma_f32_16x16x32_bf16 v[152:155], a[56:59], v[112:115], v[152:155]\n"
      "v_mfma_f32_16x16x32_bf16 v[152:155], a[60:63], v[116:119], v[152:155]\n"
      "v_mfma_f32_16x16x32_bf16 v[152:155], a[64:67], v[120:123], v[152:155]\n"
      "v_mfma_f32_16x16x32_bf16 v[152:155], a[68:71], v[124:127], v[152:155]\n"
      "s_waitcnt lgkmcnt(1)\n"
      "v_add_u32 v14, 1, v14\n"
      "v_mfma_f32_16x16x32_bf16 v[152:155], a[72:75], v[128:131], v[152:155]\n"
      "v_mfma_f32_16x16x32_bf16 v[152:155], a[76:79], v[132:135], v[152:155]\n"
      "v_mfma_f32_16x16x32_bf16 v[152:155], a[80:83], v[136:139], v[152:155]\n"
      "s_waitcnt lgkmcnt(0)\n"
      "v_mfma_f32_16x16x32_bf16 v[152:155], a[84:87], v[140:143], v[152:155]\n"
      "v_mfma_f32_16x16x32_bf16 v[152:155], a[88:91], v[144:147], v[152:155]\n"
      "v_mfma_f32_16x16x32_bf16 v[152:155], a[92:95], v[148:151], v[152:155]\n"
      "s_add_u32 s36, s36, 165888\n"
      "v_mfma_f32_16x16x32_bf16 v[156:159], a[96:99], v[56:59], 0\n"
      "s_waitcnt vmcnt(5)\n"
      "v_cmp_ne_u32 vcc, s21, v20\n"
      "v_mfma_f32_16x16x32_bf16 v[156:159], a[100:103], v[60:63], v[156:159]\n"
      "v_mfma_f32_16x16x32_bf16 v[156:159], a[104:107], v[64:67], v[156:159]\n"
      "v_mfma_f32_16x16x32_bf16 v[156:159], a[108:111], v[68:71], v[156:159]\n"
      "v_mfma_f32_16x16x32_bf16 v[156:159], a[112:115], v[72:75], v[156:159]\n"
      "v_mfma_f32_16x16x32_bf16 v[156:159], a[116:119], v[76:79], v[156:159]\n"
      "s_min_u32 s36, s36, s6\n"
      "v_mfma_f32_16x16x32_bf16 v[156:159], a[120:123], v[80:83], v[156:159]\n"
      "s_cbranch_vccnz .Lsp_d4_8\n"
      ".Lbk_d4_8:\n"
      "v_mfma_f32_16x16x32_bf16 v[156:159], a[124:127], v[84:87], v[156:159]\n"
      "buffer_load_dwordx4 v0, s[8:11], s51 offen offset:0 sc1 lds\n"
      "v_mfma_f32_16x16x32_bf16 v[156:159], a[128:131], v[88:91], v[156:159]\n"
      "s_waitcnt vmcnt(3)\n"
      "ds_write_b32 v16, v14\n"
      "v_mfma_f32_16x16x32_bf16 v[156:159], a[132:135], v[92:95], v[156:159]\n"
      "v_mfma_f32_16x16x32_bf16 v[156:159], a[136:139], v[96:99], v[156:159]\n"
      "v_mfma_f32_16x16x32_bf16 v[156:159], a[140:143], v[100:103], v[156:159]\n"
      "v_mfma_f32_16x16x32_bf16 v[156:159], a[144:147], v[104:107], v[156:159]\n"
      "buffer_load_dword v20, v2, s[8:11], s36 offen sc1\n"
      "v_mfma_f32_16x16x32_bf16 v[156:159], a[148:151], v[108:111], v[156:159]\n"
      "v_mfma_f32_16x16x32_bf16 v[156:159], a[152:155], v[112:115], v[156:159]\n"
      "v_mfma_f32_16x16x32_bf16 v[156:159], a[156:159], v[116:119], v[156:159]\n"
      "buffer_load_dwordx4 v0, s[8:11], s51 offen offset:1024 sc1 lds\n"
      "v_mfma_f32_16x16x32_bf16 v[156:159], a[160:163], v[120:123], v[156:159]\n"
      "s_add_u32 s37, s37, 106496\n"
      "v_mfma_f32_16x16x32_bf16 v[156:159], a[164:167], v[124:127], v[156:159]\n"
      "ds_read_b32 v15, v17\n"
      "v_mfma_f32_16x16x32_bf16 v[156:159], a[168:171], v[128:131], v[156:159]\n"
      "v_mfma_f32_16x16x32_bf16 v[156:159], a[172:175], v[132:135], v[156:159]\n"
      "buffer_load_dwordx4 v0, s[8:11], s51 offen offset:2048 sc1 lds\n"
      "v_mfma_f32_16x16x32_bf16 v[156:159], a[176:179], v[136:139], v[156:159]\n"
      "v_mfma_f32_16x16x32_bf16 v[156:159], a[180:183], v[140:143], v[156:159]\n"
      "v_mfma_f32_16x16x32_bf16 v[156:159], a[184:187], v[144:147], v[156:159]\n"
      "s_waitcnt lgkmcnt(0)\n"
      "v_cmp_gt_i32 vcc, v14, v15\n"
      "v_mfma_f32_16x16x32_bf16 v[156:159], a[188:191], v[148:151], v[156:159]\n"
      "buffer_load_dwordx4 v0, s[8:11], s51 offen offset:3072 sc1 lds\n"
      "v_mfma_f32_16x16x32_bf16 v[160:163], a[192:195], v[56:59], 0\n"
      "s_add_u32 m0, s43, 102400\n"
      "v_mfma_f32_16x16x32_bf16 v[160:163], a[196:199], v[60:63], v[160:163]\n"
      "s_cbranch_vccnz .Lsp_dy3_9\n"
      ".Lbk_dy3_9:\n"
      "ds_read_b128 v[56:59], v22 offset:24576\n"
      "v_mfma_f32_16x16x32_bf16 v[160:163], a[200:203], v[64:67], v[160:163]\n"
      "v_mfma_f32_16x16x32_bf16 v[160:163], a[204:207], v[68:71], v[160:163]\n"
      "buffer_load_dwordx4 v1, s[8:11], s51 offen offset:0 sc1 lds\n"
      "v_mfma_f32_16x16x32_bf16 v[160:163], a[208:211], v[72:75], v[160:163]\n"
      "ds_read_b128 v[68:71], v22 offset:27648\n"
      "v_cvt_pk_bf16_f32 v226, v156, v157\n"
      "v_mfma_f32_16x16x32_bf16 v[160:163], a[212:215], v[76:79], v[160:163]\n"
      "ds_read_b128 v[64:67], v22 offset:26624\n"
      "v_mfma_f32_16x16x32_bf16 v[160:163], a[216:219], v[80:83], v[160:163]\n"
      "v_cvt_pk_bf16_f32 v225, v154, v155\n"
      "v_mfma_f32_16x16x32_bf16 v[160:163], a[220:223], v[84:87], v[160:163]\n"
      "buffer_load_dwordx4 v1, s[8:11], s51 offen offset:1024 sc1 lds\n"
      "v_mfma_f32_16x16x32_bf16 v[160:163], a[224:227], v[88:91], v[160:163]\n"
      "v_mfma_f32_16x16x32_bf16 v[160:163], a[228:231], v[92:95], v[160:163]\n"
      "ds_read_b128 v[60:63], v22 offset:25600\n"
      "v_mfma_f32_16x16x32_bf16 v[160:163], a[232:235], v[96:99], v[160:163]\n"
      "ds_read_b128 v[84:87], v22 offset:31744\n"
      "s_mov_b32 s51, s36\n"
      "v_mfma_f32_16x16x32_bf16 v[160:163], a[236:239], v[100:103], v[160:163]\n"
      "ds_read_b128 v[100:103], v22 offset:35840\n"
      "v_mfma_f32_16x16x32_bf16 v[160:163], a[240:243], v[104:107], v[160:163]\n"
      "s_waitcnt vmcnt(7) lgkmcnt(3)\n"
      "buffer_store_dword v18, v4, s[8:11], s52 offen sc1\n"
      "v_mfma_f32_16x16x32_bf16 v[160:163], a[244:247], v[108:111], v[160:163]\n"
      "ds_read_b128 v[108:111], v22 offset:37888\n"
      "v_mfma_f32_16x16x32_bf16 v[160:163], a[248:251], v[112:115], v[160:163]\n"
      "ds_read_b128 v[88:91], v22 offset:32768\n"
      "v_mfma_f32_16x16x32_bf16 v[160:163], a[252:255], v[116:119], v[160:163]\n"
      "ds_read_b128 v[76:79], v22 offset:29696\n"
      "v_mfma_f32_16x16x32_bf16 v[160:163], v[24:27], v[120:123], v[160:163]\n"
      "ds_read_b128 v[72:75], v22 offset:28672\n"
      "v_mfma_f32_16x16x32_bf16 v[160:163], v[28:31], v[124:127], v[160:163]\n"
      "ds_read_b128 v[112:115], v22 offset:38912\n"
      "v_cvt_pk_bf16_f32 v224, v152, v153\n"
      "v_mfma_f32_16x16x32_bf16 v[160:163], v[32:35], v[128:131], v[160:163]\n"
      "ds_read_b128 v[96:99], v22 offset:34816\n"
      "v_mfma_f32_16x16x32_bf16 v[160:163], v[36:39], v[132:135], v[160:163]\n"
      "ds_read_b128 v[80:83], v22 offset:30720\n"
      "v_cvt_pk_bf16_f32 v227, v158, v159\n"
      "v_mfma_f32_16x16x32_bf16 v[160:163], v[40:43], v[136:139], v[160:163]\n"
      "buffer_store_dwordx4 v[224:227], v0, s[8:11], s37 offen sc1\n"
      "v_mfma_f32_16x16x32_bf16 v[160:163], v[44:47], v[140:143], v[160:163]\n"
      "v_mfma_f32_16x16x32_bf16 v[160:163], v[48:51], v[144:147], v[160:163]\n"
      "ds_read_b128 v[92:95], v22 offset:33792\n"
      "v_mfma_f32_16x16x32_bf16 v[160:163], v[52:55], v[148:151], v[160:163]\n"
      "s_sub_u32 s59, s59, 1\n"
      "s_cbranch_scc1 .Ld_out2\n"
      ".Ld_t3:\n"
      "v_mfma_f32_16x16x32_bf16 v[152:155], a[0:3], v[56:59], 0\n"
      "s_waitcnt lgkmcnt(3)\n"
      "v_mfma_f32_16x16x32_bf16 v[152:155], a[4:7], v[60:63], v[152:155]\n"
      "ds_read_b128 v[132:135], v22 offset:44032\n"
      "v_mfma_f32_16x16x32_bf16 v[152:155], a[8:11], v[64:67], v[152:155]\n"
      "buffer_load_dword v21, v3, s[8:11], s53 offen sc1\n"
      "v_mfma_f32_16x16x32_bf16 v[152:155], a[12:15], v[68:71], v[152:155]\n"
      "ds_read_b128 v[116:119], v22 offset:39936\n"
      "v_mfma_f32_16x16x32_bf16 v[152:155], a[16:19], v[72:75], v[152:155]\n"
      "ds_read_b128 v[104:107], v22 offset:36864\n"
      "v_mfma_f32_16x16x32_bf16 v[152:155], a[20:23], v[76:79], v[152:155]\n"
      "s_waitcnt lgkmcnt(3)\n"
      "ds_read_b128 v[120:123], v22 offset:40960\n"
      "v_cvt_pk_bf16_f32 v228, v160, v161\n"
      "v_mfma_f32_16x16x32_bf16 v[152:155], a[24:27], v[80:83], v[152:155]\n"
      "ds_read_b128 v[148:151], v22 offset:48128\n"
      "v_mfma_f32_16x16x32_bf16 v[152:155], a[28:31], v[84:87], v[152:155]\n"
      "ds_read_b128 v[128:131], v22 offset:43008\n"
      "v_cvt_pk_bf16_f32 v229, v162, v163\n"
      "v_mfma_f32_16x16x32_bf16 v[152:155], a[32:35], v[88:91], v[152:155]\n"
      "ds_read_b128 v[140:143], v22 offset:46080\n"
      "v_mfma_f32_16x16x32_bf16 v[152:155], a[36:39], v[92:95], v[152:155]\n"
      "ds_read_b128 v[124:127], v22 offset:41984\n"
      "v_mfma_f32_16x16x32_bf16 v[152:155], a[40:43], v[96:99], v[152:155]\n"
      "buffer_store_dwordx2 v[228:229], v13, s[8:11], s37 offen sc1\n"
      "v_mfma_f32_16x16x32_bf16 v[152:155], a[44:47], v[100:103], v[152:155]\n"
      "s_waitcnt lgkmcnt(5)\n"
      "ds_read_b128 v[144:147], v22 offset:47104\n"
      "v_mfma_f32_16x16x32_bf16 v[152:155], a[48:51], v[104:107], v[152:155]\n"
      "ds_read_b128 v[136:139], v22 offset:45056\n"
      "v_mfma_f32_16x16x32_bf16 v[152:155], a[52:55], v[108:111], v[152:155]\n"
      "v_mfma_f32_16x16x32_bf16 v[152:155], a[56:59], v[112:115], v[152:155]\n"
      "v_mfma_f32_16x16x32_bf16 v[152:155], a[60:63], v[116:119], v[152:155]\n"
      "s_waitcnt vmcnt(9) lgkmcnt(3)\n"
      "s_add_u32 m0, s43, 122880\n"
      "v_cmp_ne_u32 vcc, s21, v20\n"
      "v_mfma_f32_16x16x32_bf16 v[152:155], a[64:67], v[120:123], v[152:155]\n"
      "s_waitcnt lgkmcnt(2)\n"
      "v_mfma_f32_16x16x32_bf16 v[152:155], a[68:71], v[124:127], v[152:155]\n"
      "v_mfma_f32_16x16x32_bf16 v[152:155], a[72:75], v[128:131], v[152:155]\n"
      "s_cbranch_vccnz .Lsp_d5_10\n"
      ".Lbk_d5_10:\n"
      "v_mfma_f32_16x16x32_bf16 v[152:155], a[76:79], v[132:135], v[152:155]\n"
      "s_waitcnt lgkmcnt(0)\n"
      "buffer_load_dwordx4 v0, s[8:11], s51 offen offset:0 sc1 lds\n"
      "v_mfma_f32_16x16x32_bf16 v[152:155], a[80:83], v[136:139], v[152:155]\n"
      "v_mfma_f32_16x16x32_bf16 v[152:155], a[84:87], v[140:143], v[152:155]\n"
      "s_add_u32 s52, s52, 106496\n"
      "v_mfma_f32_16x16x32_bf16 v[152:155], a[88:91], v[144:147], v[152:155]\n"
      "v_mfma_f32_16x16x32_bf16 v[152:155], a[92:95], v[148:151], v[152:155]\n"
      "buffer_load_dwordx4 v0, s[8:11], s51 offen offset:1024 sc1 lds\n"
      "v_mfma_f32_16x16x32_bf16 v[156:159], a[96:99], v[56:59], 0\n"
      "v_mfma_f32_16x16x32_bf16 v[156:159], a[100:103], v[60:63], v[156:159]\n"
      "v_mfma_f32_16x16x32_bf16 v[156:159], a[104:107], v[64:67], v[156:159]\n"
      "v_add_u32 v14, 1, v14\n"
      "v_mfma_f32_16x16x32_bf16 v[156:159], a[108:111], v[68:71], v[156:159]\n"
      "buffer_load_dwordx4 v0, s[8:11], s51 offen offset:2048 sc1 lds\n"
      "v_mfma_f32_16x16x32_bf16 v[156:159], a[112:115], v[72:75], v[156:159]\n"
      "s_waitcnt vmcnt(7)\n"
      "ds_write_b32 v16, v14\n"
      "v_mfma_f32_16x16x32_bf16 v[156:159], a[116:119], v[76:79], v[156:159]\n"
      "s_add_u32 s36, s36, 165888\n"
      "s_min_u32 s36, s36, s6\n"
      "v_mfma_f32_16x16x32_bf16 v[156:159], a[120:123], v[80:83], v[156:159]\n"
      "v_mfma_f32_16x16x32_bf16 v[156:159], a[124:127], v[84:87], v[156:159]\n"
      "buffer_load_dwordx4 v0, s[8:11], s51 offen offset:3072 sc1 lds\n"
      "v_mfma_f32_16x16x32_bf16 v[156:159], a[128:131], v[88:91], v[156:159]\n"
      "v_mfma_f32_16x16x32_bf16 v[156:159], a[132:135], v[92:95], v[156:159]\n"
      "v_mfma_f32_16x16x32_bf16 v[156:159], a[136:139], v[96:99], v[156:159]\n"
      "v_mfma_f32_16x16x32_bf16 v[156:159], a[140:143], v[100:103], v[156:159]\n"
      "v_mfma_f32_16x16x32_bf16 v[156:159], a[144:147], v[104:107], v[156:159]\n"
      "s_add_u32 m0, s43, 126976\n"
      "v_mfma_f32_16x16x32_bf16 v[156:159], a[148:151], v[108:111], v[156:159]\n"
      "buffer_load_dwordx4 v1, s[8:11], s51 offen offset:0 sc1 lds\n"
      "v_mfma_f32_16x16x32_bf16 v[156:159], a[152:155], v[112:115], v[156:159]\n"
      "ds_read_b32 v15, v17\n"
      "v_mfma_f32_16x16x32_bf16 v[156:159], a[156:159], v[116:119], v[156:159]\n"
      "v_mfma_f32_16x16x32_bf16 v[156:159], a[160:163], v[120:123], v[156:159]\n"
      "v_mfma_f32_16x16x32_bf16 v[156:159], a[164:167], v[124:127], v[156:159]\n"
      "buffer_load_dwordx4 v1, s[8:11], s51 offen offset:1024 sc1 lds\n"
      "v_mfma_f32_16x16x32_bf16 v[156:159], a[168:171], v[128:131], v[156:159]\n"
      "v_mfma_f32_16x16x32_bf16 v[156:159], a[172:175], v[132:135], v[156:159]\n"
      "s_waitcnt lgkmcnt(0)\n"
      "v_cmp_gt_i32 vcc, v14, v15\n"
      "v_mfma_f32_16x16x32_bf16 v[156:159], a[176:179], v[136:139], v[156:159]\n"
      "v_mfma_f32_16x16x32_bf16 v[156:159], a[180:183], v[140:143], v[156:159]\n"
      "v_mfma_f32_16x16x32_bf16 v[156:159], a[184:187], v[144:147], v[156:159]\n"
      "s_waitcnt vmcnt(7)\n"
      "s_cbranch_vccnz .Lsp_dy4_11\n"
      ".Lbk_dy4_11:\n"
      "v_cmp_ne_u32 vcc, s21, v21\n"
      "v_mfma_f32_16x16x32_bf16 v[156:159], a[188:191], v[148:151], v[156:159]\n"
      "buffer_load_dword v20, v2, s[8:11], s36 offen sc1\n"
      "v_mfma_f32_16x16x32_bf16 v[160:163], a[192:195], v[56:59], 0\n"
      "ds_read_b128 v[56:59], v23 offset:0\n"
      "v_mfma_f32_16x16x32_bf16 v[160:163], a[196:199], v[60:63], v[160:163]\n"
      "s_cbranch_vccnz .Lsp_df_12\n"
      ".Lbk_df_12:\n"
      "v_mfma_f32_16x16x32_bf16 v[160:163], a[200:203], v[64:67], v[160:163]\n"
      "buffer_load_dwordx4 v[176:179], v0, s[8:11], s53 offen sc1\n"
      "v_mfma_f32_16x16x32_bf16 v[160:163], a[204:207], v[68:71], v[160:163]\n"
      "s_add_u32 s37, s37, 106496\n"
      "ds_read_b128 v[64:67], v23 offset:2048\n"
      "v_mfma_f32_16x16x32_bf16 v[160:163], a[208:211], v[72:75], v[160:163]\n"
      ".Ld_p1:\n"
      "s_cbranch_execz .Ld_pr\n"
      "v_mfma_f32_16x16x32_bf16 v[160:163], a[212:215], v[76:79], v[160:163]\n"
      "s_add_u32 s55, s53, 6656\n"
      "ds_read_b128 v[68:71], v23 offset:3072\n"
      "v_mfma_f32_16x16x32_bf16 v[160:163], a[216:219], v[80:83], v[160:163]\n"
      "buffer_load_dwordx2 v[180:181], v13, s[8:11], s53 offen sc1\n"
      "v_mfma_f32_16x16x32_bf16 v[160:163], a[220:223], v[84:87], v[160:163]\n"
      "s_cmp_lt_u32 s41, 32\n"
      "s_cselect_b32 s56, s39, 0x40000000\n"
      "ds_read_b128 v[80:83], v23 offset:6144\n"
      "v_mfma_f32_16x16x32_bf16 v[160:163], a[224:227], v[88:91], v[160:163]\n"
      "ds_read_b128 v[60:63], v23 offset:1024\n"
      "v_cvt_pk_bf16_f32 v224, v152, v153\n"
      "v_mfma_f32_16x16x32_bf16 v[160:163], a[228:231], v[92:95], v[160:163]\n"
      "ds_read_b128 v[72:75], v23 offset:4096\n"
      "v_mfma_f32_16x16x32_bf16 v[160:163], a[232:235], v[96:99], v[160:163]\n"
      "s_waitcnt vmcnt(9) lgkmcnt(4)\n"
      "buffer_store_dword v18, v4, s[8:11], s52 offen sc1\n"
      "v_mfma_f32_16x16x32_bf16 v[160:163], a[236:239], v[100:103], v[160:163]\n"
      "s_add_u32 s54, s53, 13312\n"
      "ds_read_b128 v[100:103], v23 offset:11264\n"
      "v_cvt_pk_bf16_f32 v225, v154, v155\n"
      "v_mfma_f32_16x16x32_bf16 v[160:163], a[240:243], v[104:107], v[160:163]\n"
      "ds_read_b128 v[96:99], v23 offset:10240\n"
      "ds_read_b128 v[76:79], v23 offset:5120\n"
      "v_mfma_f32_16x16x32_bf16 v[160:163], a[244:247], v[108:111], v[160:163]\n"
      "buffer_load_dwordx4 v[184:187], v0, s[8:11], s55 offen sc1\n"
      "v_mfma_f32_16x16x32_bf16 v[160:163], a[248:251], v[112:115], v[160:163]\n"
      "ds_read_b128 v[104:107], v23 offset:12288\n"
      "v_mfma_f32_16x16x32_bf16 v[160:163], a[252:255], v[116:119], v[160:163]\n"
      "ds_read_b128 v[116:119], v23 offset:15360\n"
      "v_cvt_pk_bf16_f32 v226, v156, v157\n"
      "v_mfma_f32_16x16x32_bf16 v[160:163], v[24:27], v[120:123], v[160:163]\n"
      "ds_read_b128 v[84:87], v23 offset:7168\n"
      "v_mfma_f32_16x16x32_bf16 v[160:163], v[28:31], v[124:127], v[160:163]\n"
      "buffer_load_dwordx2 v[196:197], v13, s[8:11], s54 offen sc1\n"
      "v_mfma_f32_16x16x32_bf16 v[160:163], v[32:35], v[128:131], v[160:163]\n"
      "ds_read_b128 v[88:91], v23 offset:8192\n"
      "v_mfma_f32_16x16x32_bf16 v[160:163], v[36:39], v[132:135], v[160:163]\n"
      "ds_read_b128 v[128:131], v23 offset:18432\n"
      "v_cvt_pk_bf16_f32 v227, v158, v159\n"
      "v_mfma_f32_16x16x32_bf16 v[160:163], v[40:43], v[136:139], v[160:163]\n"
      "ds_read_b128 v[92:95], v23 offset:9216\n"
      "s_mov_b32 s51, s36\n"
      "v_mfma_f32_16x16x32_bf16 v[160:163], v[44:47], v[140:143], v[160:163]\n"
      "buffer_load_dwordx2 v[188:189], v13, s[8:11], s55 offen sc1\n"
      "v_mfma_f32_16x16x32_bf16 v[160:163], v[48:51], v[144:147], v[160:163]\n"
      "ds_read_b128 v[120:123], v23 offset:16384\n"
      "v_mfma_f32_16x16x32_bf16 v[160:163], v[52:55], v[148:151], v[160:163]\n"
      "s_sub_u32 s59, s59, 1\n"
      "s_cbranch_scc1 .Ld_out3\n"
      ".Ld_t4:\n"
      "v_mfma_f32_16x16x32_bf16 v[152:155], a[0:3], v[56:59], 0\n"
      "s_waitcnt lgkmcnt(4)\n"
      "ds_read_b128 v[124:127], v23 offset:17408\n"
      "v_mfma_f32_16x16x32_bf16 v[152:155], a[4:7], v[60:63], v[152:155]\n"
      "buffer_load_dwordx4 v[192:195], v0, s[8:11], s54 offen sc1\n"
      "v_mfma_f32_16x16x32_bf16 v[152:155], a[8:11], v[64:67], v[152:155]\n"
      "ds_read_b128 v[112:115], v23 offset:14336\n"
      "v_mfma_f32_16x16x32_bf16 v[152:155], a[12:15], v[68:71], v[152:155]\n"
      "s_add_u32 s58, s41, 4294967264\n"
      "ds_read_b128 v[108:111], v23 offset:13312\n"
      "v_mfma_f32_16x16x32_bf16 v[152:155], a[16:19], v[72:75], v[152:155]\n"
      "ds_read_b128 v[148:151], v23 offset:23552\n"
      "v_mfma_f32_16x16x32_bf16 v[152:155], a[20:23], v[76:79], v[152:155]\n"
      "buffer_store_dwordx4 v[224:227], v0, s[8:11], s37 offen sc1\n"
      "v_mfma_f32_16x16x32_bf16 v[152:155], a[24:27], v[80:83], v[152:155]\n"
      "s_cmp_lt_u32 s58, 32\n"
      "s_cselect_b32 s57, s40, 0x40000000\n"
      "v_mfma_f32_16x16x32_bf16 v[152:155], a[28:31], v[84:87], v[152:155]\n"
      "s_waitcnt lgkmcnt(3)\n"
      "ds_read_b128 v[132:135], v23 offset:19456\n"
      "v_cvt_pk_bf16_f32 v228, v160, v161\n"
      "v_mfma_f32_16x16x32_bf16 v[152:155], a[32:35], v[88:91], v[152:155]\n"
      "s_add_u32 s41, s41, 6\n"
      "ds_read_b128 v[144:147], v23 offset:22528\n"
      "v_mfma_f32_16x16x32_bf16 v[152:155], a[36:39], v[92:95], v[152:155]\n"
      "s_add_u32 s55, s53, 19968\n"
      "s_add_u32 s54, s53, 26624\n"
      "s_add_u32 s53, s53, 599040\n"
      "v_mfma_f32_16x16x32_bf16 v[152:155], a[40:43], v[96:99], v[152:155]\n"
      "s_add_u32 s52, s52, 106496\n"
      "ds_read_b128 v[136:139], v23 offset:20480\n"
      "v_cvt_pk_bf16_f32 v229, v162, v163\n"
      "v_mfma_f32_16x16x32_bf16 v[152:155], a[44:47], v[100:103], v[152:155]\n"
      "ds_read_b128 v[140:143], v23 offset:21504\n"
      "v_mfma_f32_16x16x32_bf16 v[152:155], a[48:51], v[104:107], v[152:155]\n"
      "s_waitcnt lgkmcnt(4)\n"
      "buffer_store_dwordx2 v[228:229], v13, s[8:11], s37 offen sc1\n"
      "v_mfma_f32_16x16x32_bf16 v[152:155], a[52:55], v[108:111], v[152:155]\n"
      "v_mfma_f32_16x16x32_bf16 v[152:155], a[56:59], v[112:115], v[152:155]\n"
      "s_add_u32 s36, s36, 165888\n"
      "v_mfma_f32_16x16x32_bf16 v[152:155], a[60:63], v[116:119], v[152:155]\n"
      "v_mfma_f32_16x16x32_bf16 v[152:155], a[64:67], v[120:123], v[152:155]\n"
      "v_mfma_f32_16x16x32_bf16 v[152:155], a[68:71], v[124:127], v[152:155]\n"
      "buffer_load_dwordx4 v[200:203], v0, s[8:11], s55 offen sc1\n"
      "v_mfma_f32_16x16x32_bf16 v[152:155], a[72:75], v[128:131], v[152:155]\n"
      "s_waitcnt lgkmcnt(0)\n"
      "s_add_u32 m0, s43, 0\n"
      "v_add_u32 v14, 1, v14\n"
      "v_mfma_f32_16x16x32_bf16 v[152:155], a[76:79], v[132:135], v[152:155]\n"
      "s_add_u32 s40, s40, 4294856704\n"
      "v_mfma_f32_16x16x32_bf16 v[152:155], a[80:83], v[136:139], v[152:155]\n"
      "v_mfma_f32_16x16x32_bf16 v[152:155], a[84:87], v[140:143], v[152:155]\n"
      "buffer_load_dwordx2 v[204:205], v13, s[8:11], s55 offen sc1\n"
      "v_mfma_f32_16x16x32_bf16 v[152:155], a[88:91], v[144:147], v[152:155]\n"
      "s_waitcnt vmcnt(10)\n"
      "v_lshlrev_b32 v232, 16, v176\n"
      "v_lshlrev_b32 v233, 16, v177\n"
      "v_mfma_f32_16x16x32_bf16 v[152:155], a[92:95], v[148:151], v[152:155]\n"
      "s_min_u32 s36, s36, s6\n"
      "ds_write_b32 v16, v14\n"
      "v_mfma_f32_16x16x32_bf16 v[156:159], a[96:99], v[56:59], 0\n"
      "v_add_f32 v166, v166, v233\n"
      "v_cmp_ne_u32 vcc, s21, v20\n"
      "v_mfma_f32_16x16x32_bf16 v[156:159], a[100:103], v[60:63], v[156:159]\n"
      "buffer_load_dwordx4 v[208:211], v0, s[8:11], s54 offen sc1\n"
      "v_mfma_f32_16x16x32_bf16 v[156:159], a[104:107], v[64:67], v[156:159]\n"
      "v_cvt_pk_bf16_f32 v225, v154, v155\n"
      "v_lshlrev_b32 v235, 16, v179\n"
      "v_mfma_f32_16x16x32_bf16 v[156:159], a[108:111], v[68:71], v[156:159]\n"
      "s_waitcnt vmcnt(10)\n"
      "v_lshlrev_b32_e64 v233, 16, v181\n"
      "v_and_b32 v177, 0xffff0000, v177\n"
      "v_mfma_f32_16x16x32_bf16 v[156:159], a[112:115], v[72:75], v[156:159]\n"
      "v_lshlrev_b32_e64 v234, 16, v178\n"
      "v_mfma_f32_16x16x32_bf16 v[156:159], a[116:119], v[76:79], v[156:159]\n"
      "s_cbranch_vccnz .Lsp_d0_13\n"
      ".Lbk_d0_13:\n"
      "v_add_f32 v164, v164, v232\n"
      "v_mfma_f32_16x16x32_bf16 v[156:159], a[120:123], v[80:83], v[156:159]\n"
      "buffer_load_dwordx4 v0, s[8:11], s51 offen offset:0 sc1 lds\n"
      "v_mfma_f32_16x16x32_bf16 v[156:159], a[124:127], v[84:87], v[156:159]\n"
      "v_add_f32 v168, v168, v234\n"
      "v_mfma_f32_16x16x32_bf16 v[156:159], a[128:131], v[88:91], v[156:159]\n"
      "v_lshlrev_b32 v232, 16, v180\n"
      "v_add_f32 v172, v172, v232\n"
      "v_mfma_f32_16x16x32_bf16 v[156:159], a[132:135], v[92:95], v[156:159]\n"
      "v_and_b32 v180, 0xffff0000, v180\n"
      "v_add_f32 v174, v174, v233\n"
      "v_mfma_f32_16x16x32_bf16 v[156:159], a[136:139], v[96:99], v[156:159]\n"
      "s_waitcnt vmcnt(9) lgkmcnt(0)\n"
      "v_add_f32 v167, v167, v177\n"
      "v_lshlrev_b32 v233, 16, v185\n"
      "v_mfma_f32_16x16x32_bf16 v[156:159], a[140:143], v[100:103], v[156:159]\n"
      "buffer_load_dwordx2 v[212:213], v13, s[8:11], s54 offen sc1\n"
      "v_mfma_f32_16x16x32_bf16 v[156:159], a[144:147], v[104:107], v[156:159]\n"
      "ds_read_b32 v15, v17\n"
      "v_and_b32 v178, 0xffff0000, v178\n"
      "v_mfma_f32_16x16x32_bf16 v[156:159], a[148:151], v[108:111], v[156:159]\n"
      "v_and_b32 v176, 0xffff0000, v176\n"
      "v_lshlrev_b32 v232, 16, v184\n"
      "v_mfma_f32_16x16x32_bf16 v[156:159], a[152:155], v[112:115], v[156:159]\n"
      "v_add_f32 v166, v166, v233\n"
      "v_and_b32 v184, 0xffff0000, v184\n"
      "v_mfma_f32_16x16x32_bf16 v[156:159], a[156:159], v[116:119], v[156:159]\n"
      "buffer_load_dwordx4 v0, s[8:11], s51 offen offset:1024 sc1 lds\n"
      "v_mfma_f32_16x16x32_bf16 v[156:159], a[160:163], v[120:123], v[156:159]\n"
      "v_and_b32 v179, 0xffff0000, v179\n"
      "v_add_f32 v164, v164, v232\n"
      "v_mfma_f32_16x16x32_bf16 v[156:159], a[164:167], v[124:127], v[156:159]\n"
      "s_waitcnt vmcnt(9) lgkmcnt(0)\n"
      "v_lshlrev_b32 v233, 16, v189\n"
      "v_cmp_gt_i32 vcc, v14, v15\n"
      "v_mfma_f32_16x16x32_bf16 v[156:159], a[168:171], v[128:131], v[156:159]\n"
      "v_and_b32 v181, 0xffff0000, v181\n"
      "v_add_f32 v174, v174, v233\n"
      "v_mfma_f32_16x16x32_bf16 v[156:159], a[172:175], v[132:135], v[156:159]\n"
      "buffer_load_dwordx4 v0, s[8:11], s51 offen offset:3072 sc1 lds\n"
      "v_mfma_f32_16x16x32_bf16 v[156:159], a[176:179], v[136:139], v[156:159]\n"
      "v_lshlrev_b32 v232, 16, v188\n"
      "v_mfma_f32_16x16x32_bf16 v[156:159], a[180:183], v[140:143], v[156:159]\n"
      "s_waitcnt vmcnt(9)\n"
      "v_add_f32 v172, v172, v232\n"
      "v_lshlrev_b32 v232, 16, v192\n"
      "v_mfma_f32_16x16x32_bf16 v[156:159], a[184:187], v[144:147], v[156:159]\n"
      "v_lshlrev_b32 v234, 16, v186\n"
      "v_lshlrev_b32 v233, 16, v193\n"
      "v_mfma_f32_16x16x32_bf16 v[156:159], a[188:191], v[148:151], v[156:159]\n"
      "buffer_load_dwordx4 v0, s[8:11], s51 offen offset:2048 sc1 lds\n"
      "v_mfma_f32_16x16x32_bf16 v[160:163], a[192:195], v[56:59], 0\n"
      "v_and_b32 v185, 0xffff0000, v185\n"
      "v_add_f32 v166, v166, v233\n"
      "v_mfma_f32_16x16x32_bf16 v[160:163], a[196:199], v[60:63], v[160:163]\n"
      "s_cbranch_vccnz .Lsp_dy5_14\n"
      ".Lbk_dy5_14:\n"
      "ds_read_b128 v[60:63], v23 offset:25600\n"
      "v_mfma_f32_16x16x32_bf16 v[160:163], a[200:203], v[64:67], v[160:163]\n"
      "s_add_u32 m0, s43, 4096\n"
      "v_and_b32 v188, 0xffff0000, v188\n"
      "v_cvt_pk_bf16_f32 v227, v158, v159\n"
      "v_mfma_f32_16x16x32_bf16 v[160:163], a[204:207], v[68:71], v[160:163]\n"
      "buffer_load_dwordx4 v1, s[8:11], s51 offen offset:0 sc1 lds\n"
      "v_mfma_f32_16x16x32_bf16 v[160:163], a[208:211], v[72:75], v[160:163]\n"
      "v_add_f32 v165, v165, v176\n"
      "v_add_f32 v170, v170, v235\n"
      "v_mfma_f32_16x16x32_bf16 v[160:163], a[212:215], v[76:79], v[160:163]\n"
      "ds_read_b128 v[56:59], v23 offset:24576\n"
      "v_cvt_pk_bf16_f32 v226, v156, v157\n"
      "v_mfma_f32_16x16x32_bf16 v[160:163], a[216:219], v[80:83], v[160:163]\n"
      "ds_read_b128 v[64:67], v23 offset:26624\n"
      "v_lshlrev_b32 v233, 16, v197\n"
      "v_mfma_f32_16x16x32_bf16 v[160:163], a[220:223], v[84:87], v[160:163]\n"
      "ds_read_b128 v[80:83], v23 offset:30720\n"
      "v_add_f32 v164, v164, v232\n"
      "v_mfma_f32_16x16x32_bf16 v[160:163], a[224:227], v[88:91], v[160:163]\n"
      "buffer_load_dword v20, v2, s[8:11], s36 offen sc1\n"
      "v_mfma_f32_16x16x32_bf16 v[160:163], a[228:231], v[92:95], v[160:163]\n"
      "ds_read_b128 v[76:79], v23 offset:29696\n"
      "v_add_f32 v165, v165, v184\n"
      "v_mfma_f32_16x16x32_bf16 v[160:163], a[232:235], v[96:99], v[160:163]\n"
      "ds_read_b128 v[92:95], v23 offset:33792\n"
      "v_lshlrev_b32 v232, 16, v196\n"
      "v_mfma_f32_16x16x32_bf16 v[160:163], a[236:239], v[100:103], v[160:163]\n"
      "buffer_load_dwordx4 v1, s[8:11], s51 offen offset:1024 sc1 lds\n"
      "v_mfma_f32_16x16x32_bf16 v[160:163], a[240:243], v[104:107], v[160:163]\n"
      "ds_read_b128 v[84:87], v23 offset:31744\n"
      "v_add_f32 v172, v172, v232\n"
      "v_mfma_f32_16x16x32_bf16 v[160:163], a[244:247], v[108:111], v[160:163]\n"
      "ds_read_b128 v[68:71], v23 offset:27648\n"
      "v_add_f32 v174, v174, v233\n"
      "v_mfma_f32_16x16x32_bf16 v[160:163], a[248:251], v[112:115], v[160:163]\n"
      "s_waitcnt vmcnt(10) lgkmcnt(4)\n"
      "ds_read_b128 v[112:115], v23 offset:38912\n"
      "v_lshlrev_b32 v232, 16, v200\n"
      "v_mfma_f32_16x16x32_bf16 v[160:163], a[252:255], v[116:119], v[160:163]\n"
      "buffer_store_dword v18, v4, s[8:11], s52 offen sc1\n"
      "v_mfma_f32_16x16x32_bf16 v[160:163], v[24:27], v[120:123], v[160:163]\n"
      "ds_read_b128 v[72:75], v23 offset:28672\n"
      "v_lshlrev_b32 v235, 16, v187\n"
      "v_lshlrev_b32 v233, 16, v201\n"
      "v_mfma_f32_16x16x32_bf16 v[160:163], v[28:31], v[124:127], v[160:163]\n"
      "s_mov_b32 s51, s36\n"
      "ds_read_b128 v[124:127], v23 offset:41984\n"
      "v_add_f32 v166, v166, v233\n"
      "v_mfma_f32_16x16x32_bf16 v[160:163], v[32:35], v[128:131], v[160:163]\n"
      "ds_read_b128 v[128:131], v23 offset:43008\n"
      "v_add_f32 v164, v164, v232\n"
      "v_mfma_f32_16x16x32_bf16 v[160:163], v[36:39], v[132:135], v[160:163]\n"
      "ds_read_b128 v[88:91], v23 offset:32768\n"
      "v_add_f32 v167, v167, v185\n"
      "v_mfma_f32_16x16x32_bf16 v[160:163], v[40:43], v[136:139], v[160:163]\n"
      "ds_read_b128 v[116:119], v23 offset:39936\n"
      "ds_read_b128 v[136:139], v23 offset:45056\n"
      "v_mfma_f32_16x16x32_bf16 v[160:163], v[44:47], v[140:143], v[160:163]\n"
      "ds_read_b128 v[108:111], v23 offset:37888\n"
      "v_cvt_pk_bf16_f32 v224, v152, v153\n"
      "v_mfma_f32_16x16x32_bf16 v[160:163], v[48:51], v[144:147], v[160:163]\n"
      "s_waitcnt vmcnt(10) lgkmcnt(7)\n"
      "ds_read_b128 v[100:103], v23 offset:35840\n"
      "v_lshlrev_b32 v232, 16, v204\n"
      "v_mfma_f32_16x16x32_bf16 v[160:163], v[52:55], v[148:151], v[160:163]\n"
      "s_sub_u32 s59, s59, 1\n"
      "s_cbranch_scc1 .Ld_out4\n"
      ".Ld_t5:\n"
      "v_mfma_f32_16x16x32_bf16 v[152:155], a[0:3], v[56:59], 0\n"
      "ds_read_b128 v[96:99], v23 offset:34816\n"
      "v_and_b32 v201, 0xffff0000, v201\n"
      "v_mfma_f32_16x16x32_bf16 v[152:155], a[4:7], v[60:63], v[152:155]\n"
      "ds_read_b128 v[140:143], v23 offset:46080\n"
      "v_lshlrev_b32 v233, 16, v205\n"
      "v_mfma_f32_16x16x32_bf16 v[152:155], a[8:11], v[64:67], v[152:155]\n"
      "buffer_load_dwordx4 v[216:219], v6, s[8:11], s38 offen sc1\n"
      "v_mfma_f32_16x16x32_bf16 v[152:155], a[12:15], v[68:71], v[152:155]\n"
      "s_waitcnt lgkmcnt(4)\n"
      "s_add_u32 s37, s37, 106496\n"
      "ds_read_b128 v[104:107], v23 offset:36864\n"
      "v_and_b32 v193, 0xffff0000, v193\n"
      "v_mfma_f32_16x16x32_bf16 v[152:155], a[16:19], v[72:75], v[152:155]\n"
      "ds_read_b128 v[132:135], v23 offset:44032\n"
      "v_add_f32 v170, v170, v235\n"
      "v_mfma_f32_16x16x32_bf16 v[152:155], a[20:23], v[76:79], v[152:155]\n"
      "ds_read_b128 v[120:123], v23 offset:40960\n"
      "v_add_f32 v174, v174, v233\n"
      "v_mfma_f32_16x16x32_bf16 v[152:155], a[24:27], v[80:83], v[152:155]\n"
      "buffer_store_dwordx4 v[224:227], v0, s[8:11], s37 offen sc1\n"
      "v_mfma_f32_16x16x32_bf16 v[152:155], a[28:31], v[84:87], v[152:155]\n"
      "s_add_u32 s39, s39, 147456\n"
      "v_cvt_pk_bf16_f32 v228, v160, v161\n"
      "v_cvt_pk_bf16_f32 v229, v162, v163\n"
      "v_mfma_f32_16x16x32_bf16 v[152:155], a[32:35], v[88:91], v[152:155]\n"
      "s_add_u32 s36, s36, 165888\n"
      "ds_read_b128 v[144:147], v23 offset:47104\n"
      "v_add_f32 v168, v168, v234\n"
      "v_mfma_f32_16x16x32_bf16 v[152:155], a[36:39], v[92:95], v[152:155]\n"
      "s_waitcnt lgkmcnt(4)\n"
      "v_lshlrev_b32 v235, 16, v195\n"
      "v_lshlrev_b32 v234, 16, v194\n"
      "v_mfma_f32_16x16x32_bf16 v[152:155], a[40:43], v[96:99], v[152:155]\n"
      "s_add_u32 m0, s43, 24576\n"
      "v_add_f32 v168, v168, v234\n"
      "v_lshlrev_b32 v234, 16, v202\n"
      "v_mfma_f32_16x16x32_bf16 v[152:155], a[44:47], v[100:103], v[152:155]\n"
      "s_waitcnt lgkmcnt(2)\n"
      "ds_read_b128 v[148:151], v23 offset:48128\n"
      "v_add_f32 v171, v171, v179\n"
      "v_mfma_f32_16x16x32_bf16 v[152:155], a[48:51], v[104:107], v[152:155]\n"
      "buffer_store_dwordx2 v[228:229], v13, s[8:11], s37 offen sc1\n"
      "v_mfma_f32_16x16x32_bf16 v[152:155], a[52:55], v[108:111], v[152:155]\n"
      "v_add_f32 v169, v169, v178\n"
      "v_add_f32 v173, v173, v180\n"
      "v_add_f32 v172, v172, v232\n"
      "v_mfma_f32_16x16x32_bf16 v[152:155], a[56:59], v[112:115], v[152:155]\n"
      "s_waitcnt vmcnt(12) lgkmcnt(2)\n"
      "v_add_f32 v167, v167, v193\n"
      "v_lshlrev_b32 v232, 16, v208\n"
      "v_mfma_f32_16x16x32_bf16 v[152:155], a[60:63], v[116:119], v[152:155]\n"
      "v_and_b32 v192, 0xffff0000, v192\n"
      "v_add_f32 v168, v168, v234\n"
      "v_mfma_f32_16x16x32_bf16 v[152:155], a[64:67], v[120:123], v[152:155]\n"
      "v_and_b32 v197, 0xffff0000, v197\n"
      "v_add_f32 v167, v167, v201\n"
      "v_mfma_f32_16x16x32_bf16 v[152:155], a[68:71], v[124:127], v[152:155]\n"
      "v_add_f32 v164, v164, v232\n"
      "v_lshlrev_b32 v233, 16, v209\n"
      "v_mfma_f32_16x16x32_bf16 v[152:155], a[72:75], v[128:131], v[152:155]\n"
      "v_and_b32 v189, 0xffff0000, v189\n"
      "v_add_f32 v170, v170, v235\n"
      "v_mfma_f32_16x16x32_bf16 v[152:155], a[76:79], v[132:135], v[152:155]\n"
      "v_and_b32 v195, 0xffff0000, v195\n"
      "v_add_f32 v166, v166, v233\n"
      "v_mfma_f32_16x16x32_bf16 v[152:155], a[80:83], v[136:139], v[152:155]\n"
      "v_lshlrev_b32 v234, 16, v210\n"
      "v_mul_f32 v237, v164, v164\n"
      "v_mfma_f32_16x16x32_bf16 v[152:155], a[84:87], v[140:143], v[152:155]\n"
      "s_waitcnt lgkmcnt(0)\n"
      "buffer_load_dwordx2 v[220:221], v7, s[8:11], s38 offen sc1\n"
      "v_mfma_f32_16x16x32_bf16 v[152:155], a[88:91], v[144:147], v[152:155]\n"
      "v_and_b32 v196, 0xffff0000, v196\n"
      "v_add_f32 v165, v165, v192\n"
      "v_mfma_f32_16x16x32_bf16 v[152:155], a[92:95], v[148:151], v[152:155]\n"
      "v_and_b32 v202, 0xffff0000, v202\n"
      "v_lshlrev_b32 v235, 16, v203\n"
      "v_mfma_f32_16x16x32_bf16 v[156:159], a[96:99], v[56:59], 0\n"
      "v_and_b32 v186, 0xffff0000, v186\n"
      "v_and_b32 v208, 0xffff0000, v208\n"
      "v_mfma_f32_16x16x32_bf16 v[156:159], a[100:103], v[60:63], v[156:159]\n"
      "s_waitcnt vmcnt(11)\n"
      "v_and_b32 v200, 0xffff0000, v200\n"
      "v_lshlrev_b32 v233, 16, v213\n"
      "v_mfma_f32_16x16x32_bf16 v[156:159], a[104:107], v[64:67], v[156:159]\n"
      "v_add_u32 v14, 1, v14\n"
      "v_and_b32 v209, 0xffff0000, v209\n"
      "v_mfma_f32_16x16x32_bf16 v[156:159], a[108:111], v[68:71], v[156:159]\n"
      "v_add_f32 v175, v175, v181\n"
      "v_add_f32 v167, v167, v209\n"
      "v_mfma_f32_16x16x32_bf16 v[156:159], a[112:115], v[72:75], v[156:159]\n"
      "v_and_b32 v187, 0xffff0000, v187\n"
      "v_add_f32 v173, v173, v188\n"
      "v_mfma_f32_16x16x32_bf16 v[156:159], a[116:119], v[76:79], v[156:159]\n"
      "v_add_f32_e64 v165, v165, v200\n"
      "v_cvt_pk_bf16_f32 v177, v166, v167\n"
      "v_mfma_f32_16x16x32_bf16 v[156:159], a[120:123], v[80:83], v[156:159]\n"
      "s_waitcnt vmcnt(6)\n"
      "v_cmp_ne_u32 vcc, s21, v20\n"
      "v_and_b32 v213, 0xffff0000, v213\n"
      "v_mfma_f32_16x16x32_bf16 v[156:159], a[124:127], v[84:87], v[156:159]\n"
      "s_waitcnt vmcnt(5)\n"
      "ds_write_b32 v16, v14\n"
      "v_mfma_f32_16x16x32_bf16 v[156:159], a[128:131], v[88:91], v[156:159]\n"
      "v_add_f32_e64 v171, v171, v187\n"
      "v_add_f32_e64 v175, v175, v189\n"
      "v_mfma_f32_16x16x32_bf16 v[156:159], a[132:135], v[92:95], v[156:159]\n"
      "s_cbranch_vccnz .Lsp_d1_15\n"
      ".Lbk_d1_15:\n"
      "v_cvt_pk_bf16_f32 v225, v154, v155\n"
      "v_mfma_f32_16x16x32_bf16 v[156:159], a[136:139], v[96:99], v[156:159]\n"
      "buffer_load_dwordx4 v0, s[8:11], s51 offen offset:0 sc1 lds\n"
      "v_mfma_f32_16x16x32_bf16 v[156:159], a[140:143], v[100:103], v[156:159]\n"
      "v_add_f32 v169, v169, v186\n"
      "v_and_b32 v203, 0xffff0000, v203\n"
      "v_mfma_f32_16x16x32_bf16 v[156:159], a[144:147], v[104:107], v[156:159]\n"
      "s_min_u32 s36, s36, s6\n"
      "v_add_f32 v165, v165, v208\n"
      "v_add_f32 v168, v168, v234\n"
      "v_mfma_f32_16x16x32_bf16 v[156:159], a[148:151], v[108:111], v[156:159]\n"
      "v_and_b32 v194, 0xffff0000, v194\n"
      "v_add_f32 v170, v170, v235\n"
      "v_mfma_f32_16x16x32_bf16 v[156:159], a[152:155], v[112:115], v[156:159]\n"
      "v_add_f32 v171, v171, v195\n"
      "v_cvt_pk_bf16_f32 v224, v152, v153\n"
      "v_mfma_f32_16x16x32_bf16 v[156:159], a[156:159], v[116:119], v[156:159]\n"
      "v_add_f32 v171, v171, v203\n"
      "v_add_f32 v236, v164, v165\n"
      "v_mfma_f32_16x16x32_bf16 v[156:159], a[160:163], v[120:123], v[156:159]\n"
      "buffer_load_dwordx4 v0, s[8:11], s51 offen offset:1024 sc1 lds\n"
      "v_mfma_f32_16x16x32_bf16 v[156:159], a[164:167], v[124:127], v[156:159]\n"
      "ds_read_b32 v15, v17\n"
      "v_lshlrev_b32 v235, 16, v211\n"
      "v_mfma_f32_16x16x32_bf16 v[156:159], a[168:171], v[128:131], v[156:159]\n"
      "v_add_f32 v175, v175, v197\n"
      "v_add_f32 v236, v236, v166\n"
      "v_mfma_f32_16x16x32_bf16 v[156:159], a[172:175], v[132:135], v[156:159]\n"
      "v_add_f32 v236, v236, v167\n"
      "v_and_b32 v205, 0xffff0000, v205\n"
      "v_mfma_f32_16x16x32_bf16 v[156:159], a[176:179], v[136:139], v[156:159]\n"
      "v_and_b32 v211, 0xffff0000, v211\n"
      "v_add_f32 v236, v236, v168\n"
      "v_mfma_f32_16x16x32_bf16 v[156:159], a[180:183], v[140:143], v[156:159]\n"
      "buffer_load_dword v20, v2, s[8:11], s36 offen sc1\n"
      "v_mfma_f32_16x16x32_bf16 v[156:159], a[184:187], v[144:147], v[156:159]\n"
      "s_waitcnt lgkmcnt(0)\n"
      "v_and_b32 v204, 0xffff0000, v204\n"
      "v_cmp_gt_i32 vcc, v14, v15\n"
      "v_mfma_f32_16x16x32_bf16 v[156:159], a[188:191], v[148:151], v[156:159]\n"
      "buffer_load_dwordx4 v0, s[8:11], s51 offen offset:2048 sc1 lds\n"
      "v_mfma_f32_16x16x32_bf16 v[160:163], a[192:195], v[56:59], 0\n"
      "s_add_u32 s52, s52, 106496\n"
      "v_add_f32 v169, v169, v194\n"
      "v_lshlrev_b32 v232, 16, v212\n"
      "v_mfma_f32_16x16x32_bf16 v[160:163], a[196:199], v[60:63], v[160:163]\n"
      "v_and_b32 v212, 0xffff0000, v212\n"
      "v_fmac_f32 v237, v165, v165\n"
      "v_mfma_f32_16x16x32_bf16 v[160:163], a[200:203], v[64:67], v[160:163]\n"
      "s_cbranch_vccnz .Lsp_dy0_16\n"
      ".Lbk_dy0_16:\n"
      "ds_read_b128 v[64:67], v0 offset:2048\n"
      "v_mfma_f32_16x16x32_bf16 v[160:163], a[204:207], v[68:71], v[160:163]\n"
      "buffer_load_dwordx4 v0, s[8:11], s51 offen offset:3072 sc1 lds\n"
      "v_mfma_f32_16x16x32_bf16 v[160:163], a[208:211], v[72:75], v[160:163]\n"
      "ds_read_b128 v[56:59], v0 offset:0\n"
      "v_add_f32 v169, v169, v202\n"
      "v_mfma_f32_16x16x32_bf16 v[160:163], a[212:215], v[76:79], v[160:163]\n"
      "s_add_u32 m0, s43, 28672\n"
      "ds_read_b128 v[76:79], v0 offset:5120\n"
      "v_and_b32 v210, 0xffff0000, v210\n"
      "v_mfma_f32_16x16x32_bf16 v[160:163], a[216:219], v[80:83], v[160:163]\n"
      "ds_read_b128 v[60:63], v0 offset:1024\n"
      "v_add_f32 v169, v169, v210\n"
      "v_mfma_f32_16x16x32_bf16 v[160:163], a[220:223], v[84:87], v[160:163]\n"
      "ds_read_b128 v[80:83], v0 offset:6144\n"
      "v_fmac_f32 v237, v166, v166\n"
      "v_mfma_f32_16x16x32_bf16 v[160:163], a[224:227], v[88:91], v[160:163]\n"
      "buffer_load_dwordx4 v1, s[8:11], s51 offen offset:0 sc1 lds\n"
      "v_add_f32 v173, v173, v196\n"
      "v_mfma_f32_16x16x32_bf16 v[160:163], a[228:231], v[92:95], v[160:163]\n"
      "v_add_f32 v170, v170, v235\n"
      "v_add_f32 v236, v236, v169\n"
      "v_mfma_f32_16x16x32_bf16 v[160:163], a[232:235], v[96:99], v[160:163]\n"
      "v_add_f32 v171, v171, v211\n"
      "v_cvt_pk_bf16_f32 v176, v164, v165\n"
      "v_mfma_f32_16x16x32_bf16 v[160:163], a[236:239], v[100:103], v[160:163]\n"
      "ds_read_b128 v[84:87], v0 offset:7168\n"
      "v_cvt_pk_bf16_f32 v227, v158, v159\n"
      "v_mfma_f32_16x16x32_bf16 v[160:163], a[240:243], v[104:107], v[160:163]\n"
      "buffer_load_dwordx4 v1, s[8:11], s51 offen offset:1024 sc1 lds\n"
      "v_mfma_f32_16x16x32_bf16 v[160:163], a[244:247], v[108:111], v[160:163]\n"
      "s_add_u32 s37, s37, 106496\n"
      "ds_read_b128 v[100:103], v0 offset:11264\n"
      "v_fmac_f32 v237, v167, v167\n"
      "v_mfma_f32_16x16x32_bf16 v[160:163], a[248:251], v[112:115], v[160:163]\n"
      "ds_read_b128 v[96:99], v0 offset:10240\n"
      "v_add_f32 v172, v172, v232\n"
      "v_mfma_f32_16x16x32_bf16 v[160:163], a[252:255], v[116:119], v[160:163]\n"
      "ds_read_b128 v[72:75], v0 offset:4096\n"
      "v_fmac_f32 v237, v168, v168\n"
      "v_mfma_f32_16x16x32_bf16 v[160:163], v[24:27], v[120:123], v[160:163]\n"
      "s_waitcnt vmcnt(8) lgkmcnt(4)\n"
      "buffer_store_dword v18, v4, s[8:11], s52 offen sc1\n"
      "v_add_f32 v174, v174, v233\n"
      "v_mfma_f32_16x16x32_bf16 v[160:163], v[28:31], v[124:127], v[160:163]\n"
      "ds_read_b128 v[104:107], v0 offset:12288\n"
      "v_fmac_f32 v237, v169, v169\n"
      "v_mfma_f32_16x16x32_bf16 v[160:163], v[32:35], v[128:131], v[160:163]\n"
      "ds_read_b128 v[68:71], v0 offset:3072\n"
      "v_cvt_pk_bf16_f32 v226, v156, v157\n"
      "v_mfma_f32_16x16x32_bf16 v[160:163], v[36:39], v[132:135], v[160:163]\n"
      "ds_read_b128 v[112:115], v0 offset:14336\n"
      "v_cvt_pk_bf16_f32 v178, v168, v169\n"
      "v_fmac_f32 v237, v170, v170\n"
      "v_mfma_f32_16x16x32_bf16 v[160:163], v[40:43], v[136:139], v[160:163]\n"
      "s_add_u32 s52, s52, 106496\n"
      "ds_read_b128 v[132:135], v0 offset:19456\n"
      "v_fmac_f32 v237, v171, v171\n"
      "v_mfma_f32_16x16x32_bf16 v[160:163], v[44:47], v[140:143], v[160:163]\n"
      "ds_read_b128 v[92:95], v0 offset:9216\n"
      "v_fmac_f32 v237, v172, v172\n"
      "v_mfma_f32_16x16x32_bf16 v[160:163], v[48:51], v[144:147], v[160:163]\n"
      "buffer_store_dwordx4 v[224:227], v0, s[8:11], s37 offen sc1\n"
      "v_mfma_f32_16x16x32_bf16 v[160:163], v[52:55], v[148:151], v[160:163]\n"
      "s_sub_u32 s59, s59, 1\n"
      "s_cbranch_scc0 .Ld_t0\n"
      ".Ld_out5:\n"
      "v_add_f32 v173, v173, v204\n"
      "v_add_f32 v173, v173, v212\n"
      "s_nop 6\n"
      "v_cvt_pk_bf16_f32 v229, v162, v163\n"
      "v_cvt_pk_bf16_f32 v228, v160, v161\n"
      "buffer_store_dwordx2 v[228:229], v13, s[8:11], s37 offen sc1\n"
      "v_cvt_pk_bf16_f32 v179, v170, v171\n"
      "v_add_f32 v175, v175, v205\n"
      "v_add_f32 v175, v175, v213\n"
      "buffer_store_dwordx4 v[176:179], v8, s[16:19], s57 offen\n"
      "v_cvt_pk_bf16_f32 v180, v172, v173\n"
      "v_cvt_pk_bf16_f32 v181, v174, v175\n"
      "buffer_store_dwordx2 v[180:181], v9, s[16:19], s57 offen\n"
      "s_waitcnt vmcnt(2)\n"
      "buffer_store_dword v18, v4, s[8:11], s52 offen sc1\n"
      "s_add_u32 s52, s52, 173056\n"
      "s_add_u32 s37, s37, 173056\n"
      "s_branch .Ld_end\n"
      ".Ld_out0:\n"
      "s_add_u32 s37, s37, 173056\n"
      "v_add_f32 v169, v169, v157\n"
      "v_lshlrev_b32 v170, 16, v219\n"
      "v_add_f32 v168, v168, v156\n"
      "v_and_b32 v175, 0xffff0000, v221\n"
      "v_add_f32 v170, v170, v158\n"
      "s_nop 2\n"
      "v_add_f32 v173, v173, v161\n"
      "v_lshlrev_b32 v174, 16, v221\n"
      "v_add_f32 v174, v174, v162\n"
      "v_lshlrev_b32 v172, 16, v220\n"
      "v_add_f32 v172, v172, v160\n"
      "v_add_f32 v175, v175, v163\n"
      "buffer_load_dword v21, v3, s[8:11], s53 offen sc1\n"
      "s_waitcnt vmcnt(0)\n"
      "v_cmp_ne_u32 vcc, s21, v21\n"
      "s_cbranch_vccnz .Lsp_df_17\n"
      ".Lbk_df_17:\n"
      "buffer_load_dwordx4 v[176:179], v0, s[8:11], s53 offen sc1\n"
      "s_add_u32 s55, s53, 6656\n"
      "buffer_load_dwordx2 v[180:181], v13, s[8:11], s53 offen sc1\n"
      "s_cmp_lt_u32 s41, 32\n"
      "s_cselect_b32 s56, s39, 0x40000000\n"
      "s_add_u32 s54, s53, 13312\n"
      "buffer_load_dwordx4 v[184:187], v0, s[8:11], s55 offen sc1\n"
      "buffer_load_dwordx2 v[196:197], v13, s[8:11], s54 offen sc1\n"
      "buffer_load_dwordx2 v[188:189], v13, s[8:11], s55 offen sc1\n"
      "buffer_load_dwordx4 v[192:195], v0, s[8:11], s54 offen sc1\n"
      "s_add_u32 s58, s41, 4294967264\n"
      "s_cmp_lt_u32 s58, 32\n"
      "s_cselect_b32 s57, s40, 0x40000000\n"
      "s_add_u32 s41, s41, 6\n"
      "s_add_u32 s55, s53, 19968\n"
      "s_add_u32 s54, s53, 26624\n"
      "s_add_u32 s53, s53, 599040\n"
      "buffer_load_dwordx4 v[200:203], v0, s[8:11], s55 offen sc1\n"
      "s_add_u32 s40, s40, 4294856704\n"
      "buffer_load_dwordx2 v[204:205], v13, s[8:11], s55 offen sc1\n"
      "s_waitcnt vmcnt(7)\n"
      "v_lshlrev_b32 v232, 16, v176\n"
      "v_lshlrev_b32 v233, 16, v177\n"
      "v_add_f32 v166, v166, v233\n"
      "buffer_load_dwordx4 v[208:211], v0, s[8:11], s54 offen sc1\n"
      "v_lshlrev_b32 v235, 16, v179\n"
      "s_waitcnt vmcnt(7)\n"
      "v_lshlrev_b32 v233, 16, v181\n"
      "v_and_b32 v177, 0xffff0000, v177\n"
      "v_lshlrev_b32 v234, 16, v178\n"
      "v_add_f32 v164, v164, v232\n"
      "v_add_f32 v168, v168, v234\n"
      "v_lshlrev_b32 v232, 16, v180\n"
      "v_add_f32 v172, v172, v232\n"
      "v_and_b32 v180, 0xffff0000, v180\n"
      "v_add_f32 v174, v174, v233\n"
      "v_add_f32 v167, v167, v177\n"
      "s_waitcnt vmcnt(6)\n"
      "v_lshlrev_b32 v233, 16, v185\n"
      "buffer_load_dwordx2 v[212:213], v13, s[8:11], s54 offen sc1\n"
      "v_and_b32 v178, 0xffff0000, v178\n"
      "v_and_b32 v176, 0xffff0000, v176\n"
      "v_lshlrev_b32 v232, 16, v184\n"
      "v_add_f32 v166, v166, v233\n"
      "v_and_b32 v184, 0xffff0000, v184\n"
      "v_and_b32 v179, 0xffff0000, v179\n"
      "v_add_f32 v164, v164, v232\n"
      "s_waitcnt vmcnt(5)\n"
      "v_lshlrev_b32 v233, 16, v189\n"
      "v_and_b32 v181, 0xffff0000, v181\n"
      "v_add_f32 v174, v174, v233\n"
      "v_lshlrev_b32 v232, 16, v188\n"
      "v_add_f32 v172, v172, v232\n"
      "s_waitcnt vmcnt(4)\n"
      "v_lshlrev_b32 v232, 16, v192\n"
      "v_lshlrev_b32 v234, 16, v186\n"
      "v_lshlrev_b32 v233, 16, v193\n"
      "v_and_b32 v185, 0xffff0000, v185\n"
      "v_add_f32 v166, v166, v233\n"
      "v_and_b32 v188, 0xffff0000, v188\n"
      "v_add_f32 v165, v165, v176\n"
      "v_add_f32 v170, v170, v235\n"
      "v_lshlrev_b32 v233, 16, v197\n"
      "v_add_f32 v164, v164, v232\n"
      "v_add_f32 v165, v165, v184\n"
      "v_lshlrev_b32 v232, 16, v196\n"
      "v_add_f32 v172, v172, v232\n"
      "v_add_f32 v174, v174, v233\n"
      "s_waitcnt vmcnt(3)\n"
      "v_lshlrev_b32 v232, 16, v200\n"
      "v_lshlrev_b32 v235, 16, v187\n"
      "v_lshlrev_b32 v233, 16, v201\n"
      "v_add_f32 v166, v166, v233\n"
      "v_add_f32 v164, v164, v232\n"
      "v_add_f32 v167, v167, v185\n"
      "s_waitcnt vmcnt(2)\n"
      "v_lshlrev_b32 v232, 16, v204\n"
      "v_and_b32 v201, 0xffff0000, v201\n"
      "v_lshlrev_b32 v233, 16, v205\n"
      "v_and_b32 v193, 0xffff0000, v193\n"
      "v_add_f32 v170, v170, v235\n"
      "v_add_f32 v174, v174, v233\n"
      "s_add_u32 s39, s39, 147456\n"
      "v_add_f32 v168, v168, v234\n"
      "v_lshlrev_b32 v235, 16, v195\n"
      "v_lshlrev_b32 v234, 16, v194\n"
      "v_add_f32 v168, v168, v234\n"
      "v_lshlrev_b32 v234, 16, v202\n"
      "v_add_f32 v171, v171, v179\n"
      "v_add_f32 v169, v169, v178\n"
      "v_add_f32 v173, v173, v180\n"
      "v_add_f32 v172, v172, v232\n"
      "v_add_f32 v167, v167, v193\n"
      "s_waitcnt vmcnt(1)\n"
      "v_lshlrev_b32 v232, 16, v208\n"
      "v_and_b32 v192, 0xffff0000, v192\n"
      "v_add_f32 v168, v168, v234\n"
      "v_and_b32 v197, 0xffff0000, v197\n"
      "v_add_f32 v167, v167, v201\n"
      "v_add_f32 v164, v164, v232\n"
      "v_lshlrev_b32 v233, 16, v209\n"
      "v_and_b32 v189, 0xffff0000, v189\n"
      "v_add_f32 v170, v170, v235\n"
      "v_and_b32 v195, 0xffff0000, v195\n"
      "v_add_f32 v166, v166, v233\n"
      "v_lshlrev_b32 v234, 16, v210\n"
      "v_and_b32 v196, 0xffff0000, v196\n"
      "v_add_f32 v165, v165, v192\n"
      "v_and_b32 v202, 0xffff0000, v202\n"
      "v_lshlrev_b32 v235, 16, v203\n"
      "v_and_b32 v186, 0xffff0000, v186\n"
      "v_and_b32 v208, 0xffff0000, v208\n"
      "v_and_b32 v200, 0xffff0000, v200\n"
      "s_waitcnt vmcnt(0)\n"
      "v_lshlrev_b32 v233, 16, v213\n"
      "v_and_b32 v209, 0xffff0000, v209\n"
      "v_add_f32 v175, v175, v181\n"
      "v_add_f32 v167, v167, v209\n"
      "v_and_b32 v187, 0xffff0000, v187\n"
      "v_add_f32 v173, v173, v188\n"
      "v_add_f32 v165, v165, v200\n"
      "v_cvt_pk_bf16_f32 v177, v166, v167\n"
      "v_and_b32 v213, 0xffff0000, v213\n"
      "v_add_f32 v171, v171, v187\n"
      "v_add_f32 v175, v175, v189\n"
      "v_add_f32 v169, v169, v186\n"
      "v_and_b32 v203, 0xffff0000, v203\n"
      "v_add_f32 v165, v165, v208\n"
      "v_add_f32 v168, v168, v234\n"
      "v_and_b32 v194, 0xffff0000, v194\n"
      "v_add_f32 v170, v170, v235\n"
      "v_add_f32 v171, v171, v195\n"
      "v_add_f32 v171, v171, v203\n"
      "v_lshlrev_b32 v235, 16, v211\n"
      "v_add_f32 v175, v175, v197\n"
      "v_and_b32 v205, 0xffff0000, v205\n"
      "v_and_b32 v211, 0xffff0000, v211\n"
      "v_and_b32 v204, 0xffff0000, v204\n"
      "v_add_f32 v169, v169, v194\n"
      "v_lshlrev_b32 v232, 16, v212\n"
      "v_and_b32 v212, 0xffff0000, v212\n"
      "v_add_f32 v169, v169, v202\n"
      "v_and_b32 v210, 0xffff0000, v210\n"
      "v_add_f32 v169, v169, v210\n"
      "v_add_f32 v173, v173, v196\n"
      "v_add_f32 v170, v170, v235\n"
      "v_add_f32 v171, v171, v211\n"
      "v_cvt_pk_bf16_f32 v176, v164, v165\n"
      "v_add_f32 v172, v172, v232\n"
      "v_add_f32 v174, v174, v233\n"
      "v_cvt_pk_bf16_f32 v178, v168, v169\n"
      "v_add_f32 v173, v173, v204\n"
      "v_add_f32 v173, v173, v212\n"
      "v_cvt_pk_bf16_f32 v179, v170, v171\n"
      "v_add_f32 v175, v175, v205\n"
      "v_add_f32 v175, v175, v213\n"
      "buffer_store_dwordx4 v[176:179], v8, s[16:19], s57 offen\n"
      "v_cvt_pk_bf16_f32 v180, v172, v173\n"
      "v_cvt_pk_bf16_f32 v181, v174, v175\n"
      "buffer_store_dwordx2 v[180:181], v9, s[16:19], s57 offen\n"
      "s_branch .Ld_end\n"
      ".Ld_out1:\n"
      "s_nop 8\n"
      "v_cvt_pk_bf16_f32 v228, v160, v161\n"
      "v_cvt_pk_bf16_f32 v229, v162, v163\n"
      "buffer_store_dwordx2 v[228:229], v13, s[8:11], s37 offen sc1\n"
      "s_add_u32 s37, s37, 106496\n"
      "s_waitcnt vmcnt(0)\n"
      "buffer_store_dword v18, v4, s[8:11], s52 offen sc1\n"
      "buffer_load_dword v21, v3, s[8:11], s53 offen sc1\n"
      "s_add_u32 s52, s52, 106496\n"
      "s_waitcnt vmcnt(0)\n"
      "v_cmp_ne_u32 vcc, s21, v21\n"
      "s_cbranch_vccnz .Lsp_df_18\n"
      ".Lbk_df_18:\n"
      "buffer_load_dwordx4 v[176:179], v0, s[8:11], s53 offen sc1\n"
      "s_add_u32 s55, s53, 6656\n"
      "buffer_load_dwordx2 v[180:181], v13, s[8:11], s53 offen sc1\n"
      "s_cmp_lt_u32 s41, 32\n"
      "s_cselect_b32 s56, s39, 0x40000000\n"
      "s_add_u32 s54, s53, 13312\n"
      "buffer_load_dwordx4 v[184:187], v0, s[8:11], s55 offen sc1\n"
      "buffer_load_dwordx2 v[196:197], v13, s[8:11], s54 offen sc1\n"
      "buffer_load_dwordx2 v[188:189], v13, s[8:11], s55 offen sc1\n"
      "buffer_load_dwordx4 v[192:195], v0, s[8:11], s54 offen sc1\n"
      "s_add_u32 s58, s41, 4294967264\n"
      "s_cmp_lt_u32 s58, 32\n"
      "s_cselect_b32 s57, s40, 0x40000000\n"
      "s_add_u32 s41, s41, 6\n"
      "s_add_u32 s55, s53, 19968\n"
      "s_add_u32 s54, s53, 26624\n"
      "s_add_u32 s53, s53, 599040\n"
      "buffer_load_dwordx4 v[200:203], v0, s[8:11], s55 offen sc1\n"
      "s_add_u32 s40, s40, 4294856704\n"
      "buffer_load_dwordx2 v[204:205], v13, s[8:11], s55 offen sc1\n"
      "s_waitcnt vmcnt(7)\n"
      "v_lshlrev_b32 v232, 16, v176\n"
      "v_lshlrev_b32 v233, 16, v177\n"
      "v_add_f32 v166, v166, v233\n"
      "buffer_load_dwordx4 v[208:211], v0, s[8:11], s54 offen sc1\n"
      "v_lshlrev_b32 v235, 16, v179\n"
      "s_waitcnt vmcnt(7)\n"
      "v_lshlrev_b32 v233, 16, v181\n"
      "v_and_b32 v177, 0xffff0000, v177\n"
      "v_lshlrev_b32 v234, 16, v178\n"
      "v_add_f32 v164, v164, v232\n"
      "v_add_f32 v168, v168, v234\n"
      "v_lshlrev_b32 v232, 16, v180\n"
      "v_add_f32 v172, v172, v232\n"
      "v_and_b32 v180, 0xffff0000, v180\n"
      "v_add_f32 v174, v174, v233\n"
      "v_add_f32 v167, v167, v177\n"
      "s_waitcnt vmcnt(6)\n"
      "v_lshlrev_b32 v233, 16, v185\n"
      "buffer_load_dwordx2 v[212:213], v13, s[8:11], s54 offen sc1\n"
      "v_and_b32 v178, 0xffff0000, v178\n"
      "v_and_b32 v176, 0xffff0000, v176\n"
      "v_lshlrev_b32 v232, 16, v184\n"
      "v_add_f32 v166, v166, v233\n"
      "v_and_b32 v184, 0xffff0000, v184\n"
      "v_and_b32 v179, 0xffff0000, v179\n"
      "v_add_f32 v164, v164, v232\n"
      "s_waitcnt vmcnt(5)\n"
      "v_lshlrev_b32 v233, 16, v189\n"
      "v_and_b32 v181, 0xffff0000, v181\n"
      "v_add_f32 v174, v174, v233\n"
      "v_lshlrev_b32 v232, 16, v188\n"
      "v_add_f32 v172, v172, v232\n"
      "s_waitcnt vmcnt(4)\n"
      "v_lshlrev_b32 v232, 16, v192\n"
      "v_lshlrev_b32 v234, 16, v186\n"
      "v_lshlrev_b32 v233, 16, v193\n"
      "v_and_b32 v185, 0xffff0000, v185\n"
      "v_add_f32 v166, v166, v233\n"
      "v_and_b32 v188, 0xffff0000, v188\n"
      "v_add_f32 v165, v165, v176\n"
      "v_add_f32 v170, v170, v235\n"
      "v_lshlrev_b32 v233, 16, v197\n"
      "v_add_f32 v164, v164, v232\n"
      "v_add_f32 v165, v165, v184\n"
      "v_lshlrev_b32 v232, 16, v196\n"
      "v_add_f32 v172, v172, v232\n"
      "v_add_f32 v174, v174, v233\n"
      "s_waitcnt vmcnt(3)\n"
      "v_lshlrev_b32 v232, 16, v200\n"
      "v_lshlrev_b32 v235, 16, v187\n"
      "v_lshlrev_b32 v233, 16, v201\n"
      "v_add_f32 v166, v166, v233\n"
      "v_add_f32 v164, v164, v232\n"
      "v_add_f32 v167, v167, v185\n"
      "s_waitcnt vmcnt(2)\n"
      "v_lshlrev_b32 v232, 16, v204\n"
      "v_and_b32 v201, 0xffff0000, v201\n"
      "v_lshlrev_b32 v233, 16, v205\n"
      "v_and_b32 v193, 0xffff0000, v193\n"
      "v_add_f32 v170, v170, v235\n"
      "v_add_f32 v174, v174, v233\n"
      "s_add_u32 s39, s39, 147456\n"
      "v_add_f32 v168, v168, v234\n"
      "v_lshlrev_b32 v235, 16, v195\n"
      "v_lshlrev_b32 v234, 16, v194\n"
      "v_add_f32 v168, v168, v234\n"
      "v_lshlrev_b32 v234, 16, v202\n"
      "v_add_f32 v171, v171, v179\n"
      "v_add_f32 v169, v169, v178\n"
      "v_add_f32 v173, v173, v180\n"
      "v_add_f32 v172, v172, v232\n"
      "v_add_f32 v167, v167, v193\n"
      "s_waitcnt vmcnt(1)\n"
      "v_lshlrev_b32 v232, 16, v208\n"
      "v_and_b32 v192, 0xffff0000, v192\n"
      "v_add_f32 v168, v168, v234\n"
      "v_and_b32 v197, 0xffff0000, v197\n"
      "v_add_f32 v167, v167, v201\n"
      "v_add_f32 v164, v164, v232\n"
      "v_lshlrev_b32 v233, 16, v209\n"
      "v_and_b32 v189, 0xffff0000, v189\n"
      "v_add_f32 v170, v170, v235\n"
      "v_and_b32 v195, 0xffff0000, v195\n"
      "v_add_f32 v166, v166, v233\n"
      "v_lshlrev_b32 v234, 16, v210\n"
      "v_and_b32 v196, 0xffff0000, v196\n"
      "v_add_f32 v165, v165, v192\n"
      "v_and_b32 v202, 0xffff0000, v202\n"
      "v_lshlrev_b32 v235, 16, v203\n"
      "v_and_b32 v186, 0xffff0000, v186\n"
      "v_and_b32 v208, 0xffff0000, v208\n"
      "v_and_b32 v200, 0xffff0000, v200\n"
      "s_waitcnt vmcnt(0)\n"
      "v_lshlrev_b32 v233, 16, v213\n"
      "v_and_b32 v209, 0xffff0000, v209\n"
      "v_add_f32 v175, v175, v181\n"
      "v_add_f32 v167, v167, v209\n"
      "v_and_b32 v187, 0xffff0000, v187\n"
      "v_add_f32 v173, v173, v188\n"
      "v_add_f32 v165, v165, v200\n"
      "v_cvt_pk_bf16_f32 v177, v166, v167\n"
      "v_and_b32 v213, 0xffff0000, v213\n"
      "v_add_f32 v171, v171, v187\n"
      "v_add_f32 v175, v175, v189\n"
      "v_add_f32 v169, v169, v186\n"
      "v_and_b32 v203, 0xffff0000, v203\n"
      "v_add_f32 v165, v165, v208\n"
      "v_add_f32 v168, v168, v234\n"
      "v_and_b32 v194, 0xffff0000, v194\n"
      "v_add_f32 v170, v170, v235\n"
      "v_add_f32 v171, v171, v195\n"
      "v_add_f32 v171, v171, v203\n"
      "v_lshlrev_b32 v235, 16, v211\n"
      "v_add_f32 v175, v175, v197\n"
      "v_and_b32 v205, 0xffff0000, v205\n"
      "v_and_b32 v211, 0xffff0000, v211\n"
      "v_and_b32 v204, 0xffff0000, v204\n"
      "v_add_f32 v169, v169, v194\n"
      "v_lshlrev_b32 v232, 16, v212\n"
      "v_and_b32 v212, 0xffff0000, v212\n"
      "v_add_f32 v169, v169, v202\n"
      "v_and_b32 v210, 0xffff0000, v210\n"
      "v_add_f32 v169, v169, v210\n"
      "v_add_f32 v173, v173, v196\n"
      "v_add_f32 v170, v170, v235\n"
      "v_add_f32 v171, v171, v211\n"
      "v_cvt_pk_bf16_f32 v176, v164, v165\n"
      "v_add_f32 v172, v172, v232\n"
      "v_add_f32 v174, v174, v233\n"
      "v_cvt_pk_bf16_f32 v178, v168, v169\n"
      "v_add_f32 v173, v173, v204\n"
      "v_add_f32 v173, v173, v212\n"
      "v_cvt_pk_bf16_f32 v179, v170, v171\n"
      "v_add_f32 v175, v175, v205\n"
      "v_add_f32 v175, v175, v213\n"
      "buffer_store_dwordx4 v[176:179], v8, s[16:19], s57 offen\n"
      "v_cvt_pk_bf16_f32 v180, v172, v173\n"
      "v_cvt_pk_bf16_f32 v181, v174, v175\n"
      "buffer_store_dwordx2 v[180:181], v9, s[16:19], s57 offen\n"
      "s_branch .Ld_end\n"
      ".Ld_out2:\n"
      "buffer_load_dword v21, v3, s[8:11], s53 offen sc1\n"
      "s_nop 7\n"
      "v_cvt_pk_bf16_f32 v228, v160, v161\n"
      "v_cvt_pk_bf16_f32 v229, v162, v163\n"
      "buffer_store_dwordx2 v[228:229], v13, s[8:11], s37 offen sc1\n"
      "s_add_u32 s52, s52, 106496\n"
      "s_waitcnt vmcnt(1)\n"
      "v_cmp_ne_u32 vcc, s21, v21\n"
      "s_cbranch_vccnz .Lsp_df_19\n"
      ".Lbk_df_19:\n"
      "buffer_load_dwordx4 v[176:179], v0, s[8:11], s53 offen sc1\n"
      "s_add_u32 s37, s37, 106496\n"
      "s_add_u32 s55, s53, 6656\n"
      "buffer_load_dwordx2 v[180:181], v13, s[8:11], s53 offen sc1\n"
      "s_cmp_lt_u32 s41, 32\n"
      "s_cselect_b32 s56, s39, 0x40000000\n"
      "s_waitcnt vmcnt(2)\n"
      "buffer_store_dword v18, v4, s[8:11], s52 offen sc1\n"
      "s_add_u32 s54, s53, 13312\n"
      "buffer_load_dwordx4 v[184:187], v0, s[8:11], s55 offen sc1\n"
      "buffer_load_dwordx2 v[196:197], v13, s[8:11], s54 offen sc1\n"
      "buffer_load_dwordx2 v[188:189], v13, s[8:11], s55 offen sc1\n"
      "buffer_load_dwordx4 v[192:195], v0, s[8:11], s54 offen sc1\n"
      "s_add_u32 s58, s41, 4294967264\n"
      "s_cmp_lt_u32 s58, 32\n"
      "s_cselect_b32 s57, s40, 0x40000000\n"
      "s_add_u32 s41, s41, 6\n"
      "s_add_u32 s55, s53, 19968\n"
      "s_add_u32 s54, s53, 26624\n"
      "s_add_u32 s53, s53, 599040\n"
      "s_add_u32 s52, s52, 106496\n"
      "buffer_load_dwordx4 v[200:203], v0, s[8:11], s55 offen sc1\n"
      "s_add_u32 s40, s40, 4294856704\n"
      "buffer_load_dwordx2 v[204:205], v13, s[8:11], s55 offen sc1\n"
      "s_waitcnt vmcnt(8)\n"
      "v_lshlrev_b32 v232, 16, v176\n"
      "v_lshlrev_b32 v233, 16, v177\n"
      "v_add_f32 v166, v166, v233\n"
      "buffer_load_dwordx4 v[208:211], v0, s[8:11], s54 offen sc1\n"
      "v_lshlrev_b32 v235, 16, v179\n"
      "s_waitcnt vmcnt(8)\n"
      "v_lshlrev_b32 v233, 16, v181\n"
      "v_and_b32 v177, 0xffff0000, v177\n"
      "v_lshlrev_b32 v234, 16, v178\n"
      "v_add_f32 v164, v164, v232\n"
      "v_add_f32 v168, v168, v234\n"
      "v_lshlrev_b32 v232, 16, v180\n"
      "v_add_f32 v172, v172, v232\n"
      "v_and_b32 v180, 0xffff0000, v180\n"
      "v_add_f32 v174, v174, v233\n"
      "v_add_f32 v167, v167, v177\n"
      "s_waitcnt vmcnt(6)\n"
      "v_lshlrev_b32 v233, 16, v185\n"
      "buffer_load_dwordx2 v[212:213], v13, s[8:11], s54 offen sc1\n"
      "v_and_b32 v178, 0xffff0000, v178\n"
      "v_and_b32 v176, 0xffff0000, v176\n"
      "v_lshlrev_b32 v232, 16, v184\n"
      "v_add_f32 v166, v166, v233\n"
      "v_and_b32 v184, 0xffff0000, v184\n"
      "v_and_b32 v179, 0xffff0000, v179\n"
      "v_add_f32 v164, v164, v232\n"
      "s_waitcnt vmcnt(5)\n"
      "v_lshlrev_b32 v233, 16, v189\n"
      "v_and_b32 v181, 0xffff0000, v181\n"
      "v_add_f32 v174, v174, v233\n"
      "v_lshlrev_b32 v232, 16, v188\n"
      "v_add_f32 v172, v172, v232\n"
      "s_waitcnt vmcnt(4)\n"
      "v_lshlrev_b32 v232, 16, v192\n"
      "v_lshlrev_b32 v234, 16, v186\n"
      "v_lshlrev_b32 v233, 16, v193\n"
      "v_and_b32 v185, 0xffff0000, v185\n"
      "v_add_f32 v166, v166, v233\n"
      "v_and_b32 v188, 0xffff0000, v188\n"
      "v_add_f32 v165, v165, v176\n"
      "v_add_f32 v170, v170, v235\n"
      "v_lshlrev_b32 v233, 16, v197\n"
      "v_add_f32 v164, v164, v232\n"
      "v_add_f32 v165, v165, v184\n"
      "v_lshlrev_b32 v232, 16, v196\n"
      "v_add_f32 v172, v172, v232\n"
      "v_add_f32 v174, v174, v233\n"
      "s_waitcnt vmcnt(3)\n"
      "v_lshlrev_b32 v232, 16, v200\n"
      "v_lshlrev_b32 v235, 16, v187\n"
      "v_lshlrev_b32 v233, 16, v201\n"
      "v_add_f32 v166, v166, v233\n"
      "v_add_f32 v164, v164, v232\n"
      "v_add_f32 v167, v167, v185\n"
      "s_waitcnt vmcnt(2)\n"
      "v_lshlrev_b32 v232, 16, v204\n"
      "v_and_b32 v201, 0xffff0000, v201\n"
      "v_lshlrev_b32 v233, 16, v205\n"
      "v_and_b32 v193, 0xffff0000, v193\n"
      "v_add_f32 v170, v170, v235\n"
      "v_add_f32 v174, v174, v233\n"
      "s_add_u32 s39, s39, 147456\n"
      "v_add_f32 v168, v168, v234\n"
      "v_lshlrev_b32 v235, 16, v195\n"
      "v_lshlrev_b32 v234, 16, v194\n"
      "v_add_f32 v168, v168, v234\n"
      "v_lshlrev_b32 v234, 16, v202\n"
      "v_add_f32 v171, v171, v179\n"
      "v_add_f32 v169, v169, v178\n"
      "v_add_f32 v173, v173, v180\n"
      "v_add_f32 v172, v172, v232\n"
      "v_add_f32 v167, v167, v193\n"
      "s_waitcnt vmcnt(1)\n"
      "v_lshlrev_b32 v232, 16, v208\n"
      "v_and_b32 v192, 0xffff0000, v192\n"
      "v_add_f32 v168, v168, v234\n"
      "v_and_b32 v197, 0xffff0000, v197\n"
      "v_add_f32 v167, v167, v201\n"
      "v_add_f32 v164, v164, v232\n"
      "v_lshlrev_b32 v233, 16, v209\n"
      "v_and_b32 v189, 0xffff0000, v189\n"
      "v_add_f32 v170, v170, v235\n"
      "v_and_b32 v195, 0xffff0000, v195\n"
      "v_add_f32 v166, v166, v233\n"
      "v_lshlrev_b32 v234, 16, v210\n"
      "v_and_b32 v196, 0xffff0000, v196\n"
      "v_add_f32 v165, v165, v192\n"
      "v_and_b32 v202, 0xffff0000, v202\n"
      "v_lshlrev_b32 v235, 16, v203\n"
      "v_and_b32 v186, 0xffff0000, v186\n"
      "v_and_b32 v208, 0xffff0000, v208\n"
      "v_and_b32 v200, 0xffff0000, v200\n"
      "s_waitcnt vmcnt(0)\n"
      "v_lshlrev_b32 v233, 16, v213\n"
      "v_and_b32 v209, 0xffff0000, v209\n"
      "v_add_f32 v175, v175, v181\n"
      "v_add_f32 v167, v167, v209\n"
      "v_and_b32 v187, 0xffff0000, v187\n"
      "v_add_f32 v173, v173, v188\n"
      "v_add_f32 v165, v165, v200\n"
      "v_cvt_pk_bf16_f32 v177, v166, v167\n"
      "v_and_b32 v213, 0xffff0000, v213\n"
      "v_add_f32 v171, v171, v187\n"
      "v_add_f32 v175, v175, v189\n"
      "v_add_f32 v169, v169, v186\n"
      "v_and_b32 v203, 0xffff0000, v203\n"
      "v_add_f32 v165, v165, v208\n"
      "v_add_f32 v168, v168, v234\n"
      "v_and_b32 v194, 0xffff0000, v194\n"
      "v_add_f32 v170, v170, v235\n"
      "v_add_f32 v171, v171, v195\n"
      "v_add_f32 v171, v171, v203\n"
      "v_lshlrev_b32 v235, 16, v211\n"
      "v_add_f32 v175, v175, v197\n"
      "v_and_b32 v205, 0xffff0000, v205\n"
      "v_and_b32 v211, 0xffff0000, v211\n"
      "v_and_b32 v204, 0xffff0000, v204\n"
      "v_add_f32 v169, v169, v194\n"
      "v_lshlrev_b32 v232, 16, v212\n"
      "v_and_b32 v212, 0xffff0000, v212\n"
      "v_add_f32 v169, v169, v202\n"
      "v_and_b32 v210, 0xffff0000, v210\n"
      "v_add_f32 v169, v169, v210\n"
      "v_add_f32 v173, v173, v196\n"
      "v_add_f32 v170, v170, v235\n"
      "v_add_f32 v171, v171, v211\n"
      "v_cvt_pk_bf16_f32 v176, v164, v165\n"
      "v_add_f32 v172, v172, v232\n"
      "v_add_f32 v174, v174, v233\n"
      "v_cvt_pk_bf16_f32 v178, v168, v169\n"
      "v_add_f32 v173, v173, v204\n"
      "v_add_f32 v173, v173, v212\n"
      "v_cvt_pk_bf16_f32 v179, v170, v171\n"
      "v_add_f32 v175, v175, v205\n"
      "v_add_f32 v175, v175, v213\n"
      "buffer_store_dwordx4 v[176:179], v8, s[16:19], s57 offen\n"
      "v_cvt_pk_bf16_f32 v180, v172, v173\n"
      "v_cvt_pk_bf16_f32 v181, v174, v175\n"
      "buffer_store_dwordx2 v[180:181], v9, s[16:19], s57 offen\n"
      "s_branch .Ld_end\n"
      ".Ld_out3:\n"
      "buffer_load_dwordx4 v[192:195], v0, s[8:11], s54 offen sc1\n"
      "s_add_u32 s58, s41, 4294967264\n"
      "buffer_store_dwordx4 v[224:227], v0, s[8:11], s37 offen sc1\n"
      "s_cmp_lt_u32 s58, 32\n"
      "s_cselect_b32 s57, s40, 0x40000000\n"
      "s_nop 3\n"
      "v_cvt_pk_bf16_f32 v228, v160, v161\n"
      "s_add_u32 s41, s41, 6\n"
      "s_add_u32 s55, s53, 19968\n"
      "s_add_u32 s54, s53, 26624\n"
      "s_add_u32 s53, s53, 599040\n"
      "s_add_u32 s52, s52, 106496\n"
      "v_cvt_pk_bf16_f32 v229, v162, v163\n"
      "buffer_store_dwordx2 v[228:229], v13, s[8:11], s37 offen sc1\n"
      "buffer_load_dwordx4 v[200:203], v0, s[8:11], s55 offen sc1\n"
      "s_add_u32 s40, s40, 4294856704\n"
      "buffer_load_dwordx2 v[204:205], v13, s[8:11], s55 offen sc1\n"
      "s_waitcnt vmcnt(10)\n"
      "v_lshlrev_b32 v232, 16, v176\n"
      "v_lshlrev_b32 v233, 16, v177\n"
      "v_add_f32 v166, v166, v233\n"
      "buffer_load_dwordx4 v[208:211], v0, s[8:11], s54 offen sc1\n"
      "v_lshlrev_b32 v235, 16, v179\n"
      "s_waitcnt vmcnt(10)\n"
      "v_lshlrev_b32 v233, 16, v181\n"
      "v_and_b32 v177, 0xffff0000, v177\n"
      "v_lshlrev_b32 v234, 16, v178\n"
      "v_add_f32 v164, v164, v232\n"
      "v_add_f32 v168, v168, v234\n"
      "v_lshlrev_b32 v232, 16, v180\n"
      "v_add_f32 v172, v172, v232\n"
      "v_and_b32 v180, 0xffff0000, v180\n"
      "v_add_f32 v174, v174, v233\n"
      "v_add_f32 v167, v167, v177\n"
      "s_waitcnt vmcnt(8)\n"
      "v_lshlrev_b32 v233, 16, v185\n"
      "buffer_load_dwordx2 v[212:213], v13, s[8:11], s54 offen sc1\n"
      "v_and_b32 v178, 0xffff0000, v178\n"
      "v_and_b32 v176, 0xffff0000, v176\n"
      "v_lshlrev_b32 v232, 16, v184\n"
      "v_add_f32 v166, v166, v233\n"
      "v_and_b32 v184, 0xffff0000, v184\n"
      "v_and_b32 v179, 0xffff0000, v179\n"
      "v_add_f32 v164, v164, v232\n"
      "s_waitcnt vmcnt(7)\n"
      "v_lshlrev_b32 v233, 16, v189\n"
      "v_and_b32 v181, 0xffff0000, v181\n"
      "v_add_f32 v174, v174, v233\n"
      "v_lshlrev_b32 v232, 16, v188\n"
      "v_add_f32 v172, v172, v232\n"
      "s_waitcnt vmcnt(6)\n"
      "v_lshlrev_b32 v232, 16, v192\n"
      "v_lshlrev_b32 v234, 16, v186\n"
      "v_lshlrev_b32 v233, 16, v193\n"
      "v_and_b32 v185, 0xffff0000, v185\n"
      "v_add_f32 v166, v166, v233\n"
      "v_and_b32 v188, 0xffff0000, v188\n"
      "v_add_f32 v165, v165, v176\n"
      "v_add_f32 v170, v170, v235\n"
      "v_lshlrev_b32 v233, 16, v197\n"
      "v_add_f32 v164, v164, v232\n"
      "v_add_f32 v165, v165, v184\n"
      "v_lshlrev_b32 v232, 16, v196\n"
      "v_add_f32 v172, v172, v232\n"
      "v_add_f32 v174, v174, v233\n"
      "s_waitcnt vmcnt(3)\n"
      "v_lshlrev_b32 v232, 16, v200\n"
      "buffer_store_dword v18, v4, s[8:11], s52 offen sc1\n"
      "v_lshlrev_b32 v235, 16, v187\n"
      "v_lshlrev_b32 v233, 16, v201\n"
      "v_add_f32 v166, v166, v233\n"
      "v_add_f32 v164, v164, v232\n"
      "v_add_f32 v167, v167, v185\n"
      "s_waitcnt vmcnt(3)\n"
      "v_lshlrev_b32 v232, 16, v204\n"
      "v_and_b32 v201, 0xffff0000, v201\n"
      "v_lshlrev_b32 v233, 16, v205\n"
      "s_add_u32 s37, s37, 106496\n"
      "v_and_b32 v193, 0xffff0000, v193\n"
      "v_add_f32 v170, v170, v235\n"
      "v_add_f32 v174, v174, v233\n"
      "s_add_u32 s39, s39, 147456\n"
      "v_add_f32 v168, v168, v234\n"
      "v_lshlrev_b32 v235, 16, v195\n"
      "v_lshlrev_b32 v234, 16, v194\n"
      "v_add_f32 v168, v168, v234\n"
      "v_lshlrev_b32 v234, 16, v202\n"
      "v_add_f32 v171, v171, v179\n"
      "v_add_f32 v169, v169, v178\n"
      "v_add_f32 v173, v173, v180\n"
      "v_add_f32 v172, v172, v232\n"
      "v_add_f32 v167, v167, v193\n"
      "s_waitcnt vmcnt(2)\n"
      "v_lshlrev_b32 v232, 16, v208\n"
      "v_and_b32 v192, 0xffff0000, v192\n"
      "v_add_f32 v168, v168, v234\n"
      "v_and_b32 v197, 0xffff0000, v197\n"
      "v_add_f32 v167, v167, v201\n"
      "v_add_f32 v164, v164, v232\n"
      "v_lshlrev_b32 v233, 16, v209\n"
      "v_and_b32 v189, 0xffff0000, v189\n"
      "v_add_f32 v170, v170, v235\n"
      "v_and_b32 v195, 0xffff0000, v195\n"
      "v_add_f32 v166, v166, v233\n"
      "v_lshlrev_b32 v234, 16, v210\n"
      "v_and_b32 v196, 0xffff0000, v196\n"
      "v_add_f32 v165, v165, v192\n"
      "v_and_b32 v202, 0xffff0000, v202\n"
      "v_lshlrev_b32 v235, 16, v203\n"
      "v_and_b32 v186, 0xffff0000, v186\n"
      "v_and_b32 v208, 0xffff0000, v208\n"
      "v_and_b32 v200, 0xffff0000, v200\n"
      "s_waitcnt vmcnt(1)\n"
      "v_lshlrev_b32 v233, 16, v213\n"
      "v_and_b32 v209, 0xffff0000, v209\n"
      "v_add_f32 v175, v175, v181\n"
      "v_add_f32 v167, v167, v209\n"
      "v_and_b32 v187, 0xffff0000, v187\n"
      "v_add_f32 v173, v173, v188\n"
      "v_add_f32 v165, v165, v200\n"
      "v_cvt_pk_bf16_f32 v177, v166, v167\n"
      "v_and_b32 v213, 0xffff0000, v213\n"
      "v_add_f32 v171, v171, v187\n"
      "v_add_f32 v175, v175, v189\n"
      "v_add_f32 v169, v169, v186\n"
      "v_and_b32 v203, 0xffff0000, v203\n"
      "v_add_f32 v165, v165, v208\n"
      "v_add_f32 v168, v168, v234\n"
      "v_and_b32 v194, 0xffff0000, v194\n"
      "v_add_f32 v170, v170, v235\n"
      "v_add_f32 v171, v171, v195\n"
      "v_add_f32 v171, v171, v203\n"
      "v_lshlrev_b32 v235, 16, v211\n"
      "v_add_f32 v175, v175, v197\n"
      "v_and_b32 v205, 0xffff0000, v205\n"
      "v_and_b32 v211, 0xffff0000, v211\n"
      "v_and_b32 v204, 0xffff0000, v204\n"
      "s_add_u32 s52, s52, 106496\n"
      "v_add_f32 v169, v169, v194\n"
      "v_lshlrev_b32 v232, 16, v212\n"
      "v_and_b32 v212, 0xffff0000, v212\n"
      "v_add_f32 v169, v169, v202\n"
      "v_and_b32 v210, 0xffff0000, v210\n"
      "v_add_f32 v169, v169, v210\n"
      "v_add_f32 v173, v173, v196\n"
      "v_add_f32 v170, v170, v235\n"
      "v_add_f32 v171, v171, v211\n"
      "v_cvt_pk_bf16_f32 v176, v164, v165\n"
      "v_add_f32 v172, v172, v232\n"
      "v_add_f32 v174, v174, v233\n"
      "v_cvt_pk_bf16_f32 v178, v168, v169\n"
      "v_add_f32 v173, v173, v204\n"
      "v_add_f32 v173, v173, v212\n"
      "v_cvt_pk_bf16_f32 v179, v170, v171\n"
      "v_add_f32 v175, v175, v205\n"
      "v_add_f32 v175, v175, v213\n"
      "buffer_store_dwordx4 v[176:179], v8, s[16:19], s57 offen\n"
      "v_cvt_pk_bf16_f32 v180, v172, v173\n"
      "v_cvt_pk_bf16_f32 v181, v174, v175\n"
      "buffer_store_dwordx2 v[180:181], v9, s[16:19], s57 offen\n"
      "s_branch .Ld_end\n"
      ".Ld_out4:\n"
      "v_and_b32 v201, 0xffff0000, v201\n"
      "v_lshlrev_b32 v233, 16, v205\n"
      "s_add_u32 s37, s37, 106496\n"
      "v_and_b32 v193, 0xffff0000, v193\n"
      "v_add_f32 v170, v170, v235\n"
      "v_add_f32 v174, v174, v233\n"
      "buffer_store_dwordx4 v[224:227], v0, s[8:11], s37 offen sc1\n"
      "s_add_u32 s39, s39, 147456\n"
      "s_nop 0\n"
      "v_cvt_pk_bf16_f32 v228, v160, v161\n"
      "v_cvt_pk_bf16_f32 v229, v162, v163\n"
      "v_add_f32 v168, v168, v234\n"
      "v_lshlrev_b32 v235, 16, v195\n"
      "v_lshlrev_b32 v234, 16, v194\n"
      "v_add_f32 v168, v168, v234\n"
      "v_lshlrev_b32 v234, 16, v202\n"
      "v_add_f32 v171, v171, v179\n"
      "buffer_store_dwordx2 v[228:229], v13, s[8:11], s37 offen sc1\n"
      "v_add_f32 v169, v169, v178\n"
      "v_add_f32 v173, v173, v180\n"
      "v_add_f32 v172, v172, v232\n"
      "v_add_f32 v167, v167, v193\n"
      "s_waitcnt vmcnt(11)\n"
      "v_lshlrev_b32 v232, 16, v208\n"
      "v_and_b32 v192, 0xffff0000, v192\n"
      "v_add_f32 v168, v168, v234\n"
      "v_and_b32 v197, 0xffff0000, v197\n"
      "v_add_f32 v167, v167, v201\n"
      "v_add_f32 v164, v164, v232\n"
      "v_lshlrev_b32 v233, 16, v209\n"
      "v_and_b32 v189, 0xffff0000, v189\n"
      "v_add_f32 v170, v170, v235\n"
      "v_and_b32 v195, 0xffff0000, v195\n"
      "v_add_f32 v166, v166, v233\n"
      "v_lshlrev_b32 v234, 16, v210\n"
      "v_and_b32 v196, 0xffff0000, v196\n"
      "v_add_f32 v165, v165, v192\n"
      "v_and_b32 v202, 0xffff0000, v202\n"
      "v_lshlrev_b32 v235, 16, v203\n"
      "v_and_b32 v186, 0xffff0000, v186\n"
      "v_and_b32 v208, 0xffff0000, v208\n"
      "v_and_b32 v200, 0xffff0000, v200\n"
      "s_waitcnt vmcnt(9)\n"
      "v_lshlrev_b32 v233, 16, v213\n"
      "v_and_b32 v209, 0xffff0000, v209\n"
      "v_add_f32 v175, v175, v181\n"
      "v_add_f32 v167, v167, v209\n"
      "v_and_b32 v187, 0xffff0000, v187\n"
      "v_add_f32 v173, v173, v188\n"
      "v_add_f32 v165, v165, v200\n"
      "v_cvt_pk_bf16_f32 v177, v166, v167\n"
      "v_and_b32 v213, 0xffff0000, v213\n"
      "v_add_f32 v171, v171, v187\n"
      "v_add_f32 v175, v175, v189\n"
      "v_add_f32 v169, v169, v186\n"
      "v_and_b32 v203, 0xffff0000, v203\n"
      "v_add_f32 v165, v165, v208\n"
      "v_add_f32 v168, v168, v234\n"
      "v_and_b32 v194, 0xffff0000, v194\n"
      "v_add_f32 v170, v170, v235\n"
      "v_add_f32 v171, v171, v195\n"
      "v_add_f32 v171, v171, v203\n"
      "v_lshlrev_b32 v235, 16, v211\n"
      "v_add_f32 v175, v175, v197\n"
      "v_and_b32 v205, 0xffff0000, v205\n"
      "v_and_b32 v211, 0xffff0000, v211\n"
      "v_and_b32 v204, 0xffff0000, v204\n"
      "s_add_u32 s52, s52, 106496\n"
      "v_add_f32 v169, v169, v194\n"
      "v_lshlrev_b32 v232, 16, v212\n"
      "v_and_b32 v212, 0xffff0000, v212\n"
      "v_add_f32 v169, v169, v202\n"
      "v_and_b32 v210, 0xffff0000, v210\n"
      "v_add_f32 v169, v169, v210\n"
      "v_add_f32 v173, v173, v196\n"
      "v_add_f32 v170, v170, v235\n"
      "v_add_f32 v171, v171, v211\n"
      "v_cvt_pk_bf16_f32 v176, v164, v165\n"
      "s_add_u32 s37, s37, 106496\n"
      "v_add_f32 v172, v172, v232\n"
      "s_waitcnt vmcnt(0)\n"
      "buffer_store_dword v18, v4, s[8:11], s52 offen sc1\n"
      "v_add_f32 v174, v174, v233\n"
      "v_cvt_pk_bf16_f32 v178, v168, v169\n"
      "s_add_u32 s52, s52, 106496\n"
      "v_add_f32 v173, v173, v204\n"
      "v_add_f32 v173, v173, v212\n"
      "v_cvt_pk_bf16_f32 v179, v170, v171\n"
      "v_add_f32 v175, v175, v205\n"
      "v_add_f32 v175, v175, v213\n"
      "buffer_store_dwordx4 v[176:179], v8, s[16:19], s57 offen\n"
      "v_cvt_pk_bf16_f32 v180, v172, v173\n"
      "v_cvt_pk_bf16_f32 v181, v174, v175\n"
      "buffer_store_dwordx2 v[180:181], v9, s[16:19], s57 offen\n"
      "s_branch .Ld_end\n"
      ".Ld_in1:\n"
      "s_add_u32 s36, s36, 165888\n"
      "s_min_u32 s36, s36, s6\n"
      "s_mov_b32 s51, s36\n"
      "s_add_u32 s36, s36, 165888\n"
      "s_min_u32 s36, s36, s6\n"
      "buffer_load_dword v20, v2, s[8:11], s36 offen sc1\n"
      "s_mov_b32 s51, s36\n"
      "s_add_u32 s36, s36, 165888\n"
      "s_add_u32 m0, s43, 24576\n"
      "s_waitcnt vmcnt(0)\n"
      "v_cmp_ne_u32 vcc, s21, v20\n"
      "s_cbranch_vccnz .Lsp_d1_20\n"
      ".Lbk_d1_20:\n"
      "buffer_load_dwordx4 v0, s[8:11], s51 offen offset:0 sc1 lds\n"
      "s_min_u32 s36, s36, s6\n"
      "buffer_load_dwordx4 v0, s[8:11], s51 offen offset:1024 sc1 lds\n"
      "buffer_load_dword v20, v2, s[8:11], s36 offen sc1\n"
      "buffer_load_dwordx4 v0, s[8:11], s51 offen offset:2048 sc1 lds\n"
      "buffer_load_dwordx4 v0, s[8:11], s51 offen offset:3072 sc1 lds\n"
      "s_add_u32 m0, s43, 28672\n"
      "s_nop 0\n"
      "buffer_load_dwordx4 v1, s[8:11], s51 offen offset:0 sc1 lds\n"
      "buffer_load_dwordx4 v1, s[8:11], s51 offen offset:1024 sc1 lds\n"
      "s_mov_b32 s51, s36\n"
      "s_add_u32 m0, s43, 49152\n"
      "s_add_u32 s38, s38, 147456\n"
      "s_waitcnt vmcnt(4)\n"
      "v_cmp_ne_u32 vcc, s21, v20\n"
      "v_add_u32 v14, 1, v14\n"
      "s_cbranch_vccnz .Lsp_d2_21\n"
      ".Lbk_d2_21:\n"
      "s_add_u32 s36, s36, 165888\n"
      "buffer_load_dwordx4 v0, s[8:11], s51 offen offset:2048 sc1 lds\n"
      "s_waitcnt vmcnt(1)\n"
      "ds_write_b32 v16, v14\n"
      "buffer_load_dwordx4 v0, s[8:11], s51 offen offset:0 sc1 lds\n"
      "ds_read_b32 v15, v17\n"
      "buffer_load_dwordx4 v0, s[8:11], s51 offen offset:1024 sc1 lds\n"
      "s_waitcnt lgkmcnt(0)\n"
      "v_cmp_gt_i32 vcc, v14, v15\n"
      "buffer_load_dwordx4 v0, s[8:11], s51 offen offset:3072 sc1 lds\n"
      "s_add_u32 m0, s43, 53248\n"
      "s_cbranch_vccnz .Lsp_dy1_22\n"
      ".Lbk_dy1_22:\n"
      "ds_read_b128 v[64:67], v0 offset:26624\n"
      "buffer_load_dwordx4 v1, s[8:11], s51 offen offset:0 sc1 lds\n"
      "ds_read_b128 v[72:75], v0 offset:28672\n"
      "s_min_u32 s36, s36, s6\n"
      "ds_read_b128 v[80:83], v0 offset:30720\n"
      "buffer_load_dwordx4 v1, s[8:11], s51 offen offset:1024 sc1 lds\n"
      "ds_read_b128 v[60:63], v0 offset:25600\n"
      "ds_read_b128 v[56:59], v0 offset:24576\n"
      "ds_read_b128 v[100:103], v0 offset:35840\n"
      "buffer_load_dword v20, v2, s[8:11], s36 offen sc1\n"
      "ds_read_b128 v[88:91], v0 offset:32768\n"
      "ds_read_b128 v[76:79], v0 offset:29696\n"
      "ds_read_b128 v[68:71], v0 offset:27648\n"
      "s_add_u32 s52, s52, 173056\n"
      "ds_read_b128 v[92:95], v0 offset:33792\n"
      "ds_read_b128 v[104:107], v0 offset:36864\n"
      "ds_read_b128 v[84:87], v0 offset:31744\n"
      "s_waitcnt vmcnt(0) lgkmcnt(0)\n"
      "s_nop 4\n"
      "s_branch .Ld_t1\n"
      ".Ld_in2:\n"
      "s_add_u32 s36, s36, 165888\n"
      "s_min_u32 s36, s36, s6\n"
      "s_mov_b32 s51, s36\n"
      "s_add_u32 s36, s36, 165888\n"
      "s_min_u32 s36, s36, s6\n"
      "s_mov_b32 s51, s36\n"
      "s_add_u32 s36, s36, 165888\n"
      "s_min_u32 s36, s36, s6\n"
      "buffer_load_dword v20, v2, s[8:11], s36 offen sc1\n"
      "s_mov_b32 s51, s36\n"
      "s_add_u32 m0, s43, 49152\n"
      "s_add_u32 s38, s38, 147456\n"
      "s_waitcnt vmcnt(0)\n"
      "v_cmp_ne_u32 vcc, s21, v20\n"
      "s_cbranch_vccnz .Lsp_d2_23\n"
      ".Lbk_d2_23:\n"
      "s_add_u32 s36, s36, 165888\n"
      "buffer_load_dwordx4 v0, s[8:11], s51 offen offset:2048 sc1 lds\n"
      "buffer_load_dwordx4 v0, s[8:11], s51 offen offset:0 sc1 lds\n"
      "buffer_load_dwordx4 v0, s[8:11], s51 offen offset:1024 sc1 lds\n"
      "buffer_load_dwordx4 v0, s[8:11], s51 offen offset:3072 sc1 lds\n"
      "s_add_u32 m0, s43, 53248\n"
      "s_nop 0\n"
      "buffer_load_dwordx4 v1, s[8:11], s51 offen offset:0 sc1 lds\n"
      "s_min_u32 s36, s36, s6\n"
      "buffer_load_dwordx4 v1, s[8:11], s51 offen offset:1024 sc1 lds\n"
      "buffer_load_dword v20, v2, s[8:11], s36 offen sc1\n"
      "s_add_u32 s52, s52, 173056\n"
      "s_mov_b32 s51, s36\n"
      "s_add_u32 s37, s37, 173056\n"
      "v_add_u32 v14, 1, v14\n"
      "s_add_u32 s36, s36, 165888\n"
      "s_add_u32 m0, s43, 73728\n"
      "s_waitcnt vmcnt(1)\n"
      "ds_write_b32 v16, v14\n"
      "s_waitcnt vmcnt(0)\n"
      "v_cmp_ne_u32 vcc, s21, v20\n"
      "s_cbranch_vccnz .Lsp_d3_24\n"
      ".Lbk_d3_24:\n"
      "buffer_load_dwordx4 v0, s[8:11], s51 offen offset:1024 sc1 lds\n"
      "ds_read_b32 v15, v17\n"
      "buffer_load_dwordx4 v0, s[8:11], s51 offen offset:0 sc1 lds\n"
      "s_min_u32 s36, s36, s6\n"
      "s_waitcnt lgkmcnt(0)\n"
      "v_cmp_gt_i32 vcc, v14, v15\n"
      "buffer_load_dwordx4 v0, s[8:11], s51 offen offset:3072 sc1 lds\n"
      "s_cbranch_vccnz .Lsp_dy2_25\n"
      ".Lbk_dy2_25:\n"
      "buffer_load_dword v20, v2, s[8:11], s36 offen sc1\n"
      "buffer_load_dwordx4 v0, s[8:11], s51 offen offset:2048 sc1 lds\n"
      "ds_read_b128 v[68:71], v22 offset:3072\n"
      "ds_read_b128 v[64:67], v22 offset:2048\n"
      "ds_read_b128 v[60:63], v22 offset:1024\n"
      "s_add_u32 m0, s43, 77824\n"
      "s_nop 0\n"
      "buffer_load_dwordx4 v1, s[8:11], s51 offen offset:1024 sc1 lds\n"
      "ds_read_b128 v[56:59], v22 offset:0\n"
      "ds_read_b128 v[84:87], v22 offset:7168\n"
      "ds_read_b128 v[96:99], v22 offset:10240\n"
      "buffer_load_dwordx4 v1, s[8:11], s51 offen offset:0 sc1 lds\n"
      "ds_read_b128 v[108:111], v22 offset:13312\n"
      "ds_read_b128 v[88:91], v22 offset:8192\n"
      "ds_read_b128 v[72:75], v22 offset:4096\n"
      "ds_read_b128 v[76:79], v22 offset:5120\n"
      "ds_read_b128 v[104:107], v22 offset:12288\n"
      "ds_read_b128 v[100:103], v22 offset:11264\n"
      "ds_read_b128 v[80:83], v22 offset:6144\n"
      "ds_read_b128 v[124:127], v22 offset:17408\n"
      "s_waitcnt vmcnt(0) lgkmcnt(0)\n"
      "s_nop 4\n"
      "s_branch .Ld_t2\n"
      ".Ld_in3:\n"
      "s_add_u32 s36, s36, 165888\n"
      "s_min_u32 s36, s36, s6\n"
      "s_mov_b32 s51, s36\n"
      "s_add_u32 s36, s36, 165888\n"
      "s_min_u32 s36, s36, s6\n"
      "s_mov_b32 s51, s36\n"
      "s_add_u32 s36, s36, 165888\n"
      "s_min_u32 s36, s36, s6\n"
      "s_mov_b32 s51, s36\n"
      "s_add_u32 s38, s38, 147456\n"
      "s_add_u32 s36, s36, 165888\n"
      "s_min_u32 s36, s36, s6\n"
      "buffer_load_dword v20, v2, s[8:11], s36 offen sc1\n"
      "s_add_u32 s52, s52, 173056\n"
      "s_mov_b32 s51, s36\n"
      "s_add_u32 s37, s37, 173056\n"
      "s_add_u32 s36, s36, 165888\n"
      "s_add_u32 m0, s43, 73728\n"
      "s_waitcnt vmcnt(0)\n"
      "v_cmp_ne_u32 vcc, s21, v20\n"
      "s_cbranch_vccnz .Lsp_d3_26\n"
      ".Lbk_d3_26:\n"
      "buffer_load_dwordx4 v0, s[8:11], s51 offen offset:1024 sc1 lds\n"
      "buffer_load_dwordx4 v0, s[8:11], s51 offen offset:0 sc1 lds\n"
      "s_min_u32 s36, s36, s6\n"
      "buffer_load_dwordx4 v0, s[8:11], s51 offen offset:3072 sc1 lds\n"
      "buffer_load_dword v20, v2, s[8:11], s36 offen sc1\n"
      "buffer_load_dwordx4 v0, s[8:11], s51 offen offset:2048 sc1 lds\n"
      "s_add_u32 m0, s43, 77824\n"
      "s_nop 0\n"
      "buffer_load_dwordx4 v1, s[8:11], s51 offen offset:1024 sc1 lds\n"
      "buffer_load_dwordx4 v1, s[8:11], s51 offen offset:0 sc1 lds\n"
      "s_mov_b32 s51, s36\n"
      "s_add_u32 m0, s43, 98304\n"
      "v_add_u32 v14, 1, v14\n"
      "s_add_u32 s36, s36, 165888\n"
      "s_waitcnt vmcnt(3)\n"
      "v_cmp_ne_u32 vcc, s21, v20\n"
      "s_min_u32 s36, s36, s6\n"
      "s_cbranch_vccnz .Lsp_d4_27\n"
      ".Lbk_d4_27:\n"
      "buffer_load_dwordx4 v0, s[8:11], s51 offen offset:0 sc1 lds\n"
      "s_waitcnt vmcnt(1)\n"
      "ds_write_b32 v16, v14\n"
      "buffer_load_dword v20, v2, s[8:11], s36 offen sc1\n"
      "buffer_load_dwordx4 v0, s[8:11], s51 offen offset:1024 sc1 lds\n"
      "s_add_u32 s37, s37, 106496\n"
      "ds_read_b32 v15, v17\n"
      "buffer_load_dwordx4 v0, s[8:11], s51 offen offset:2048 sc1 lds\n"
      "s_waitcnt lgkmcnt(0)\n"
      "v_cmp_gt_i32 vcc, v14, v15\n"
      "buffer_load_dwordx4 v0, s[8:11], s51 offen offset:3072 sc1 lds\n"
      "s_add_u32 m0, s43, 102400\n"
      "s_cbranch_vccnz .Lsp_dy3_28\n"
      ".Lbk_dy3_28:\n"
      "ds_read_b128 v[56:59], v22 offset:24576\n"
      "buffer_load_dwordx4 v1, s[8:11], s51 offen offset:0 sc1 lds\n"
      "ds_read_b128 v[68:71], v22 offset:27648\n"
      "ds_read_b128 v[64:67], v22 offset:26624\n"
      "buffer_load_dwordx4 v1, s[8:11], s51 offen offset:1024 sc1 lds\n"
      "ds_read_b128 v[60:63], v22 offset:25600\n"
      "ds_read_b128 v[84:87], v22 offset:31744\n"
      "s_mov_b32 s51, s36\n"
      "ds_read_b128 v[100:103], v22 offset:35840\n"
      "ds_read_b128 v[108:111], v22 offset:37888\n"
      "ds_read_b128 v[88:91], v22 offset:32768\n"
      "ds_read_b128 v[76:79], v22 offset:29696\n"
      "ds_read_b128 v[72:75], v22 offset:28672\n"
      "ds_read_b128 v[112:115], v22 offset:38912\n"
      "ds_read_b128 v[96:99], v22 offset:34816\n"
      "ds_read_b128 v[80:83], v22 offset:30720\n"
      "ds_read_b128 v[92:95], v22 offset:33792\n"
      "s_waitcnt vmcnt(0) lgkmcnt(0)\n"
      "s_nop 4\n"
      "s_branch .Ld_t3\n"
      ".Ld_in4:\n"
      "s_add_u32 s36, s36, 165888\n"
      "s_min_u32 s36, s36, s6\n"
      "s_mov_b32 s51, s36\n"
      "s_add_u32 s36, s36, 165888\n"
      "s_min_u32 s36, s36, s6\n"
      "s_mov_b32 s51, s36\n"
      "s_add_u32 s36, s36, 165888\n"
      "s_min_u32 s36, s36, s6\n"
      "s_mov_b32 s51, s36\n"
      "s_add_u32 s38, s38, 147456\n"
      "s_add_u32 s36, s36, 165888\n"
      "s_min_u32 s36, s36, s6\n"
      "s_add_u32 s52, s52, 173056\n"
      "s_mov_b32 s51, s36\n"
      "s_add_u32 s37, s37, 173056\n"
      "s_add_u32 s36, s36, 165888\n"
      "s_min_u32 s36, s36, s6\n"
      "buffer_load_dword v20, v2, s[8:11], s36 offen sc1\n"
      "s_mov_b32 s51, s36\n"
      "s_add_u32 m0, s43, 98304\n"
      "s_add_u32 s36, s36, 165888\n"
      "s_waitcnt vmcnt(0)\n"
      "v_cmp_ne_u32 vcc, s21, v20\n"
      "s_min_u32 s36, s36, s6\n"
      "s_cbranch_vccnz .Lsp_d4_29\n"
      ".Lbk_d4_29:\n"
      "buffer_load_dwordx4 v0, s[8:11], s51 offen offset:0 sc1 lds\n"
      "buffer_load_dword v20, v2, s[8:11], s36 offen sc1\n"
      "buffer_load_dwordx4 v0, s[8:11], s51 offen offset:1024 sc1 lds\n"
      "s_add_u32 s37, s37, 106496\n"
      "buffer_load_dwordx4 v0, s[8:11], s51 offen offset:2048 sc1 lds\n"
      "buffer_load_dwordx4 v0, s[8:11], s51 offen offset:3072 sc1 lds\n"
      "s_add_u32 m0, s43, 102400\n"
      "s_nop 0\n"
      "buffer_load_dwordx4 v1, s[8:11], s51 offen offset:0 sc1 lds\n"
      "buffer_load_dwordx4 v1, s[8:11], s51 offen offset:1024 sc1 lds\n"
      "s_mov_b32 s51, s36\n"
      "s_add_u32 m0, s43, 122880\n"
      "s_waitcnt vmcnt(5)\n"
      "v_cmp_ne_u32 vcc, s21, v20\n"
      "s_cbranch_vccnz .Lsp_d5_30\n"
      ".Lbk_d5_30:\n"
      "buffer_load_dwordx4 v0, s[8:11], s51 offen offset:0 sc1 lds\n"
      "s_add_u32 s52, s52, 106496\n"
      "buffer_load_dwordx4 v0, s[8:11], s51 offen offset:1024 sc1 lds\n"
      "v_add_u32 v14, 1, v14\n"
      "buffer_load_dwordx4 v0, s[8:11], s51 offen offset:2048 sc1 lds\n"
      "s_waitcnt vmcnt(3)\n"
      "ds_write_b32 v16, v14\n"
      "s_add_u32 s36, s36, 165888\n"
      "s_min_u32 s36, s36, s6\n"
      "buffer_load_dwordx4 v0, s[8:11], s51 offen offset:3072 sc1 lds\n"
      "s_add_u32 m0, s43, 126976\n"
      "s_nop 0\n"
      "buffer_load_dwordx4 v1, s[8:11], s51 offen offset:0 sc1 lds\n"
      "ds_read_b32 v15, v17\n"
      "buffer_load_dwordx4 v1, s[8:11], s51 offen offset:1024 sc1 lds\n"
      "s_waitcnt lgkmcnt(0)\n"
      "v_cmp_gt_i32 vcc, v14, v15\n"
      "s_cbranch_vccnz .Lsp_dy4_31\n"
      ".Lbk_dy4_31:\n"
      "buffer_load_dword v20, v2, s[8:11], s36 offen sc1\n"
      "ds_read_b128 v[56:59], v23 offset:0\n"
      "s_add_u32 s37, s37, 106496\n"
      "ds_read_b128 v[64:67], v23 offset:2048\n"
      "ds_read_b128 v[68:71], v23 offset:3072\n"
      "s_cmp_lt_u32 s41, 32\n"
      "s_cselect_b32 s56, s39, 0x40000000\n"
      "ds_read_b128 v[80:83], v23 offset:6144\n"
      "ds_read_b128 v[60:63], v23 offset:1024\n"
      "ds_read_b128 v[72:75], v23 offset:4096\n"
      "ds_read_b128 v[100:103], v23 offset:11264\n"
      "ds_read_b128 v[96:99], v23 offset:10240\n"
      "ds_read_b128 v[76:79], v23 offset:5120\n"
      "ds_read_b128 v[104:107], v23 offset:12288\n"
      "ds_read_b128 v[116:119], v23 offset:15360\n"
      "ds_read_b128 v[84:87], v23 offset:7168\n"
      "ds_read_b128 v[88:91], v23 offset:8192\n"
      "ds_read_b128 v[128:131], v23 offset:18432\n"
      "ds_read_b128 v[92:95], v23 offset:9216\n"
      "s_mov_b32 s51, s36\n"
      "ds_read_b128 v[120:123], v23 offset:16384\n"
      "s_waitcnt vmcnt(0) lgkmcnt(0)\n"
      "s_nop 4\n"
      "s_branch .Ld_t4\n"
      ".Ld_in5:\n"
      "s_add_u32 s36, s36, 165888\n"
      "s_min_u32 s36, s36, s6\n"
      "s_mov_b32 s51, s36\n"
      "s_add_u32 s36, s36, 165888\n"
      "s_min_u32 s36, s36, s6\n"
      "s_mov_b32 s51, s36\n"
      "s_add_u32 s36, s36, 165888\n"
      "s_min_u32 s36, s36, s6\n"
      "s_mov_b32 s51, s36\n"
      "s_add_u32 s38, s38, 147456\n"
      "s_add_u32 s36, s36, 165888\n"
      "s_min_u32 s36, s36, s6\n"
      "s_add_u32 s52, s52, 173056\n"
      "s_mov_b32 s51, s36\n"
      "s_add_u32 s37, s37, 173056\n"
      "s_add_u32 s36, s36, 165888\n"
      "s_min_u32 s36, s36, s6\n"
      "s_mov_b32 s51, s36\n"
      "s_add_u32 s36, s36, 165888\n"
      "s_min_u32 s36, s36, s6\n"
      "buffer_load_dword v20, v2, s[8:11], s36 offen sc1\n"
      "s_add_u32 s37, s37, 106496\n"
      "s_mov_b32 s51, s36\n"
      "s_add_u32 m0, s43, 122880\n"
      "s_waitcnt vmcnt(0)\n"
      "v_cmp_ne_u32 vcc, s21, v20\n"
      "s_cbranch_vccnz .Lsp_d5_32\n"
      ".Lbk_d5_32:\n"
      "buffer_load_dwordx4 v0, s[8:11], s51 offen offset:0 sc1 lds\n"
      "s_add_u32 s52, s52, 106496\n"
      "buffer_load_dwordx4 v0, s[8:11], s51 offen offset:1024 sc1 lds\n"
      "buffer_load_dwordx4 v0, s[8:11], s51 offen offset:2048 sc1 lds\n"
      "s_add_u32 s36, s36, 165888\n"
      "s_min_u32 s36, s36, s6\n"
      "buffer_load_dwordx4 v0, s[8:11], s51 offen offset:3072 sc1 lds\n"
      "s_add_u32 m0, s43, 126976\n"
      "s_nop 0\n"
      "buffer_load_dwordx4 v1, s[8:11], s51 offen offset:0 sc1 lds\n"
      "buffer_load_dwordx4 v1, s[8:11], s51 offen offset:1024 sc1 lds\n"
      "buffer_load_dword v20, v2, s[8:11], s36 offen sc1\n"
      "s_add_u32 s37, s37, 106496\n"
      "s_cmp_lt_u32 s41, 32\n"
      "s_cselect_b32 s56, s39, 0x40000000\n"
      "s_mov_b32 s51, s36\n"
      "s_add_u32 s58, s41, 4294967264\n"
      "s_cmp_lt_u32 s58, 32\n"
      "s_cselect_b32 s57, s40, 0x40000000\n"
      "s_add_u32 s41, s41, 6\n"
      "s_add_u32 s53, s53, 599040\n"
      "s_add_u32 s52, s52, 106496\n"
      "s_add_u32 s36, s36, 165888\n"
      "s_add_u32 m0, s43, 0\n"
      "v_add_u32 v14, 1, v14\n"
      "s_add_u32 s40, s40, 4294856704\n"
      "s_min_u32 s36, s36, s6\n"
      "s_waitcnt vmcnt(1)\n"
      "ds_write_b32 v16, v14\n"
      "s_waitcnt vmcnt(0)\n"
      "v_cmp_ne_u32 vcc, s21, v20\n"
      "s_cbranch_vccnz .Lsp_d0_33\n"
      ".Lbk_d0_33:\n"
      "buffer_load_dwordx4 v0, s[8:11], s51 offen offset:0 sc1 lds\n"
      "ds_read_b32 v15, v17\n"
      "buffer_load_dwordx4 v0, s[8:11], s51 offen offset:1024 sc1 lds\n"
      "s_waitcnt lgkmcnt(0)\n"
      "v_cmp_gt_i32 vcc, v14, v15\n"
      "buffer_load_dwordx4 v0, s[8:11], s51 offen offset:3072 sc1 lds\n"
      "buffer_load_dwordx4 v0, s[8:11], s51 offen offset:2048 sc1 lds\n"
      "s_cbranch_vccnz .Lsp_dy5_34\n"
      ".Lbk_dy5_34:\n"
      "ds_read_b128 v[60:63], v23 offset:25600\n"
      "s_add_u32 m0, s43, 4096\n"
      "s_nop 0\n"
      "buffer_load_dwordx4 v1, s[8:11], s51 offen offset:0 sc1 lds\n"
      "ds_read_b128 v[56:59], v23 offset:24576\n"
      "ds_read_b128 v[64:67], v23 offset:26624\n"
      "ds_read_b128 v[80:83], v23 offset:30720\n"
      "buffer_load_dword v20, v2, s[8:11], s36 offen sc1\n"
      "ds_read_b128 v[76:79], v23 offset:29696\n"
      "ds_read_b128 v[92:95], v23 offset:33792\n"
      "buffer_load_dwordx4 v1, s[8:11], s51 offen offset:1024 sc1 lds\n"
      "ds_read_b128 v[84:87], v23 offset:31744\n"
      "ds_read_b128 v[68:71], v23 offset:27648\n"
      "ds_read_b128 v[112:115], v23 offset:38912\n"
      "ds_read_b128 v[72:75], v23 offset:28672\n"
      "s_mov_b32 s51, s36\n"
      "ds_read_b128 v[124:127], v23 offset:41984\n"
      "ds_read_b128 v[128:131], v23 offset:43008\n"
      "ds_read_b128 v[88:91], v23 offset:32768\n"
      "ds_read_b128 v[116:119], v23 offset:39936\n"
      "ds_read_b128 v[136:139], v23 offset:45056\n"
      "ds_read_b128 v[108:111], v23 offset:37888\n"
      "ds_read_b128 v[100:103], v23 offset:35840\n"
      "s_waitcnt vmcnt(0) lgkmcnt(0)\n"
      "s_nop 4\n"
      "s_branch .Ld_t5\n"
      ".Ld_end:\n"
      "s_endpgm\n"
      ".Lsp_d0_1:\n"
      "s_mov_b32 s22, s23\n"
      ".Lsl_d0_1:\n"
      "buffer_load_dword v20, v2, s[8:11], s51 offen sc1\n"
      "s_waitcnt vmcnt(0)\n"
      "v_cmp_ne_u32 vcc, s21, v20\n"
      "s_cbranch_vccz .Lbk_d0_1\n"
      "s_sleep 4\n"
      "s_sub_u32 s22, s22, 1\n"
      "s_cbranch_scc0 .Lsl_d0_1\n"
      "s_mov_b32 s23, 0\n"
      "s_branch .Lbk_d0_1\n"
      ".Lsp_d1_2:\n"
      "s_mov_b32 s22, s23\n"
      ".Lsl_d1_2:\n"
      "buffer_load_dword v20, v2, s[8:11], s51 offen sc1\n"
      "s_waitcnt vmcnt(0)\n"
      "v_cmp_ne_u32 vcc, s21, v20\n"
      "s_cbranch_vccz .Lbk_d1_2\n"
      "s_sleep 4\n"
      "s_sub_u32 s22, s22, 1\n"
      "s_cbranch_scc0 .Lsl_d1_2\n"
      "s_mov_b32 s23, 0\n"
      "s_branch .Lbk_d1_2\n"
      ".Lsp_dy0_3:\n"
      "s_lshl_b32 s22, s23, 4\n"
      ".Lsl_dy0_3:\n"
      "ds_read_b32 v15, v17\n"
      "s_waitcnt lgkmcnt(0)\n"
      "v_cmp_gt_i32 vcc, v14, v15\n"
      "s_cbranch_vccz .Lbk_dy0_3\n"
      "s_sub_u32 s22, s22, 1\n"
      "s_cbranch_scc0 .Lsl_dy0_3\n"
      "s_mov_b32 s23, 0\n"
      "s_branch .Lbk_dy0_3\n"
      ".Lsp_d2_4:\n"
      "s_mov_b32 s22, s23\n"
      "buffer_load_dword v20, v2, s[8:11], s51 offen sc1\n"
      "s_waitcnt vmcnt(0)\n"
      "v_cmp_ne_u32 vcc, s21, v20\n"
      "s_add_u32 s30, s52, 0\n"
      "buffer_store_dword v18, v4, s[8:11], s30 offen sc1\n"
      "s_cbranch_vccz .Lbk_d2_4\n"
      ".Lsl_d2_4:\n"
      "buffer_load_dword v20, v2, s[8:11], s51 offen sc1\n"
      "s_waitcnt vmcnt(0)\n"
      "v_cmp_ne_u32 vcc, s21, v20\n"
      "s_cbranch_vccz .Lbk_d2_4\n"
      "s_sleep 4\n"
      "s_sub_u32 s22, s22, 1\n"
      "s_cbranch_scc0 .Lsl_d2_4\n"
      "s_mov_b32 s23, 0\n"
      "s_branch .Lbk_d2_4\n"
      ".Lsp_dy1_5:\n"
      "s_lshl_b32 s22, s23, 4\n"
      ".Lsl_dy1_5:\n"
      "ds_read_b32 v15, v17\n"
      "s_waitcnt lgkmcnt(0)\n"
      "v_cmp_gt_i32 vcc, v14, v15\n"
      "s_cbranch_vccz .Lbk_dy1_5\n"
      "s_sub_u32 s22, s22, 1\n"
      "s_cbranch_scc0 .Lsl_dy1_5\n"
      "s_mov_b32 s23, 0\n"
      "s_branch .Lbk_dy1_5\n"
      ".Lsp_d3_6:\n"
      "s_mov_b32 s22, s23\n"
      ".Lsl_d3_6:\n"
      "buffer_load_dword v20, v2, s[8:11], s51 offen sc1\n"
      "s_waitcnt vmcnt(0)\n"
      "v_cmp_ne_u32 vcc, s21, v20\n"
      "s_cbranch_vccz .Lbk_d3_6\n"
      "s_sleep 4\n"
      "s_sub_u32 s22, s22, 1\n"
      "s_cbranch_scc0 .Lsl_d3_6\n"
      "s_mov_b32 s23, 0\n"
      "s_branch .Lbk_d3_6\n"
      ".Lsp_dy2_7:\n"
      "s_lshl_b32 s22, s23, 4\n"
      ".Lsl_dy2_7:\n"
      "ds_read_b32 v15, v17\n"
      "s_waitcnt lgkmcnt(0)\n"
      "v_cmp_gt_i32 vcc, v14, v15\n"
      "s_cbranch_vccz .Lbk_dy2_7\n"
      "s_sub_u32 s22, s22, 1\n"
      "s_cbranch_scc0 .Lsl_dy2_7\n"
      "s_mov_b32 s23, 0\n"
      "s_branch .Lbk_dy2_7\n"
      ".Lsp_d4_8:\n"
      "s_mov_b32 s22, s23\n"
      "buffer_load_dword v20, v2, s[8:11], s51 offen sc1\n"
      "s_waitcnt vmcnt(0)\n"
      "v_cmp_ne_u32 vcc, s21, v20\n"
      "s_add_u32 s30, s52, 0\n"
      "buffer_store_dword v18, v4, s[8:11], s30 offen sc1\n"
      "s_cbranch_vccz .Lbk_d4_8\n"
      ".Lsl_d4_8:\n"
      "buffer_load_dword v20, v2, s[8:11], s51 offen sc1\n"
      "s_waitcnt vmcnt(0)\n"
      "v_cmp_ne_u32 vcc, s21, v20\n"
      "s_cbranch_vccz .Lbk_d4_8\n"
      "s_sleep 4\n"
      "s_sub_u32 s22, s22, 1\n"
      "s_cbranch_scc0 .Lsl_d4_8\n"
      "s_mov_b32 s23, 0\n"
      "s_branch .Lbk_d4_8\n"
      ".Lsp_dy3_9:\n"
      "s_lshl_b32 s22, s23, 4\n"
      ".Lsl_dy3_9:\n"
      "ds_read_b32 v15, v17\n"
      "s_waitcnt lgkmcnt(0)\n"
      "v_cmp_gt_i32 vcc, v14, v15\n"
      "s_cbranch_vccz .Lbk_dy3_9\n"
      "s_sub_u32 s22, s22, 1\n"
      "s_cbranch_scc0 .Lsl_dy3_9\n"
      "s_mov_b32 s23, 0\n"
      "s_branch .Lbk_dy3_9\n"
      ".Lsp_d5_10:\n"
      "s_mov_b32 s22, s23\n"
      "buffer_load_dword v20, v2, s[8:11], s51 offen sc1\n"
      "s_waitcnt vmcnt(0)\n"
      "v_cmp_ne_u32 vcc, s21, v20\n"
      "s_add_u32 s30, s52, 106496\n"
      "buffer_store_dword v18, v4, s[8:11], s30 offen sc1\n"
      "s_cbranch_vccz .Lbk_d5_10\n"
      ".Lsl_d5_10:\n"
      "buffer_load_dword v20, v2, s[8:11], s51 offen sc1\n"
      "s_waitcnt vmcnt(0)\n"
      "v_cmp_ne_u32 vcc, s21, v20\n"
      "s_cbranch_vccz .Lbk_d5_10\n"
      "s_sleep 4\n"
      "s_sub_u32 s22, s22, 1\n"
      "s_cbranch_scc0 .Lsl_d5_10\n"
      "s_mov_b32 s23, 0\n"
      "s_branch .Lbk_d5_10\n"
      ".Lsp_dy4_11:\n"
      "s_lshl_b32 s22, s23, 4\n"
      ".Lsl_dy4_11:\n"
      "ds_read_b32 v15, v17\n"
      "s_waitcnt lgkmcnt(0)\n"
      "v_cmp_gt_i32 vcc, v14, v15\n"
      "s_cbranch_vccz .Lbk_dy4_11\n"
      "s_sub_u32 s22, s22, 1\n"
      "s_cbranch_scc0 .Lsl_dy4_11\n"
      "s_mov_b32 s23, 0\n"
      "s_branch .Lbk_dy4_11\n"
      ".Lsp_df_12:\n"
      "s_mov_b32 s22, s23\n"
      "buffer_load_dword v21, v3, s[8:11], s53 offen sc1\n"
      "s_waitcnt vmcnt(0)\n"
      "v_cmp_ne_u32 vcc, s21, v21\n"
      "s_add_u32 s30, s52, 0\n"
      "buffer_store_dword v18, v4, s[8:11], s30 offen sc1\n"
      "s_cbranch_vccz .Lbk_df_12\n"
      ".Lsl_df_12:\n"
      "buffer_load_dword v21, v3, s[8:11], s53 offen sc1\n"
      "s_waitcnt vmcnt(0)\n"
      "v_cmp_ne_u32 vcc, s21, v21\n"
      "s_cbranch_vccz .Lbk_df_12\n"
      "s_sleep 4\n"
      "s_sub_u32 s22, s22, 1\n"
      "s_cbranch_scc0 .Lsl_df_12\n"
      "s_mov_b32 s23, 0\n"
      "s_branch .Lbk_df_12\n"
      ".Lsp_d0_13:\n"
      "s_mov_b32 s22, s23\n"
      "buffer_load_dword v20, v2, s[8:11], s51 offen sc1\n"
      "s_waitcnt vmcnt(0)\n"
      "v_cmp_ne_u32 vcc, s21, v20\n"
      "s_add_u32 s30, s52, 0\n"
      "buffer_store_dword v18, v4, s[8:11], s30 offen sc1\n"
      "s_cbranch_vccz .Lbk_d0_13\n"
      ".Lsl_d0_13:\n"
      "buffer_load_dword v20, v2, s[8:11], s51 offen sc1\n"
      "s_waitcnt vmcnt(0)\n"
      "v_cmp_ne_u32 vcc, s21, v20\n"
      "s_cbranch_vccz .Lbk_d0_13\n"
      "s_sleep 4\n"
      "s_sub_u32 s22, s22, 1\n"
      "s_cbranch_scc0 .Lsl_d0_13\n"
      "s_mov_b32 s23, 0\n"
      "s_branch .Lbk_d0_13\n"
      ".Lsp_dy5_14:\n"
      "s_lshl_b32 s22, s23, 4\n"
      ".Lsl_dy5_14:\n"
      "ds_read_b32 v15, v17\n"
      "s_waitcnt lgkmcnt(0)\n"
      "v_cmp_gt_i32 vcc, v14, v15\n"
      "s_cbranch_vccz .Lbk_dy5_14\n"
      "s_sub_u32 s22, s22, 1\n"
      "s_cbranch_scc0 .Lsl_dy5_14\n"
      "s_mov_b32 s23, 0\n"
      "s_branch .Lbk_dy5_14\n"
      ".Lsp_d1_15:\n"
      "s_mov_b32 s22, s23\n"
      "buffer_load_dword v20, v2, s[8:11], s51 offen sc1\n"
      "s_waitcnt vmcnt(0)\n"
      "v_cmp_ne_u32 vcc, s21, v20\n"
      "s_add_u32 s30, s52, 106496\n"
      "buffer_store_dword v18, v4, s[8:11], s30 offen sc1\n"
      "s_cbranch_vccz .Lbk_d1_15\n"
      ".Lsl_d1_15:\n"
      "buffer_load_dword v20, v2, s[8:11], s51 offen sc1\n"
      "s_waitcnt vmcnt(0)\n"
      "v_cmp_ne_u32 vcc, s21, v20\n"
      "s_cbranch_vccz .Lbk_d1_15\n"
      "s_sleep 4\n"
      "s_sub_u32 s22, s22, 1\n"
      "s_cbranch_scc0 .Lsl_d1_15\n"
      "s_mov_b32 s23, 0\n"
      "s_branch .Lbk_d1_15\n"
      ".Lsp_dy0_16:\n"
      "s_lshl_b32 s22, s23, 4\n"
      ".Lsl_dy0_16:\n"
      "ds_read_b32 v15, v17\n"
      "s_waitcnt lgkmcnt(0)\n"
      "v_cmp_gt_i32 vcc, v14, v15\n"
      "s_cbranch_vccz .Lbk_dy0_16\n"
      "s_sub_u32 s22, s22, 1\n"
      "s_cbranch_scc0 .Lsl_dy0_16\n"
      "s_mov_b32 s23, 0\n"
      "s_branch .Lbk_dy0_16\n"
      ".Lsp_df_17:\n"
      "s_mov_b32 s22, s23\n"
      "buffer_load_dword v21, v3, s[8:11], s53 offen sc1\n"
      "s_waitcnt vmcnt(0)\n"
      "v_cmp_ne_u32 vcc, s21, v21\n"
      "s_add_u32 s30, s52, 0\n"
      "buffer_store_dword v18, v4, s[8:11], s30 offen sc1\n"
      "s_cbranch_vccz .Lbk_df_17\n"
      ".Lsl_df_17:\n"
      "buffer_load_dword v21, v3, s[8:11], s53 offen sc1\n"
      "s_waitcnt vmcnt(0)\n"
      "v_cmp_ne_u32 vcc, s21, v21\n"
      "s_cbranch_vccz .Lbk_df_17\n"
      "s_sleep 4\n"
      "s_sub_u32 s22, s22, 1\n"
      "s_cbranch_scc0 .Lsl_df_17\n"
      "s_mov_b32 s23, 0\n"
      "s_branch .Lbk_df_17\n"
      ".Lsp_df_18:\n"
      "s_mov_b32 s22, s23\n"
      "buffer_load_dword v21, v3, s[8:11], s53 offen sc1\n"
      "s_waitcnt vmcnt(0)\n"
      "v_cmp_ne_u32 vcc, s21, v21\n"
      "s_add_u32 s30, s52, 0\n"
      "buffer_store_dword v18, v4, s[8:11], s30 offen sc1\n"
      "s_cbranch_vccz .Lbk_df_18\n"
      ".Lsl_df_18:\n"
      "buffer_load_dword v21, v3, s[8:11], s53 offen sc1\n"
      "s_waitcnt vmcnt(0)\n"
      "v_cmp_ne_u32 vcc, s21, v21\n"
      "s_cbranch_vccz .Lbk_df_18\n"
      "s_sleep 4\n"
      "s_sub_u32 s22, s22, 1\n"
      "s_cbranch_scc0 .Lsl_df_18\n"
      "s_mov_b32 s23, 0\n"
      "s_branch .Lbk_df_18\n"
      ".Lsp_df_19:\n"
      "s_mov_b32 s22, s23\n"
      "buffer_load_dword v21, v3, s[8:11], s53 offen sc1\n"
      "s_waitcnt vmcnt(0)\n"
      "v_cmp_ne_u32 vcc, s21, v21\n"
      "s_add_u32 s30, s52, 0\n"
      "buffer_store_dword v18, v4, s[8:11], s30 offen sc1\n"
      "s_cbranch_vccz .Lbk_df_19\n"
      ".Lsl_df_19:\n"
      "buffer_load_dword v21, v3, s[8:11], s53 offen sc1\n"
      "s_waitcnt vmcnt(0)\n"
      "v_cmp_ne_u32 vcc, s21, v21\n"
      "s_cbranch_vccz .Lbk_df_19\n"
      "s_sleep 4\n"
      "s_sub_u32 s22, s22, 1\n"
      "s_cbranch_scc0 .Lsl_df_19\n"
      "s_mov_b32 s23, 0\n"
      "s_branch .Lbk_df_19\n"
      ".Lsp_d1_20:\n"
      "s_mov_b32 s22, s23\n"
      ".Lsl_d1_20:\n"
      "buffer_load_dword v20, v2, s[8:11], s51 offen sc1\n"
      "s_waitcnt vmcnt(0)\n"
      "v_cmp_ne_u32 vcc, s21, v20\n"
      "s_cbranch_vccz .Lbk_d1_20\n"
      "s_sleep 4\n"
      "s_sub_u32 s22, s22, 1\n"
      "s_cbranch_scc0 .Lsl_d1_20\n"
      "s_mov_b32 s23, 0\n"
      "s_branch .Lbk_d1_20\n"
      ".Lsp_d2_21:\n"
      "s_mov_b32 s22, s23\n"
      ".Lsl_d2_21:\n"
      "buffer_load_dword v20, v2, s[8:11], s51 offen sc1\n"
      "s_waitcnt vmcnt(0)\n"
      "v_cmp_ne_u32 vcc, s21, v20\n"
      "s_cbranch_vccz .Lbk_d2_21\n"
      "s_sleep 4\n"
      "s_sub_u32 s22, s22, 1\n"
      "s_cbranch_scc0 .Lsl_d2_21\n"
      "s_mov_b32 s23, 0\n"
      "s_branch .Lbk_d2_21\n"
      ".Lsp_dy1_22:\n"
      "s_lshl_b32 s22, s23, 4\n"
      ".Lsl_dy1_22:\n"
      "ds_read_b32 v15, v17\n"
      "s_waitcnt lgkmcnt(0)\n"
      "v_cmp_gt_i32 vcc, v14, v15\n"
      "s_cbranch_vccz .Lbk_dy1_22\n"
      "s_sub_u32 s22, s22, 1\n"
      "s_cbranch_scc0 .Lsl_dy1_22\n"
      "s_mov_b32 s23, 0\n"
      "s_branch .Lbk_dy1_22\n"
      ".Lsp_d2_23:\n"
      "s_mov_b32 s22, s23\n"
      ".Lsl_d2_23:\n"
      "buffer_load_dword v20, v2, s[8:11], s51 offen sc1\n"
      "s_waitcnt vmcnt(0)\n"
      "v_cmp_ne_u32 vcc, s21, v20\n"
      "s_cbranch_vccz .Lbk_d2_23\n"
      "s_sleep 4\n"
      "s_sub_u32 s22, s22, 1\n"
      "s_cbranch_scc0 .Lsl_d2_23\n"
      "s_mov_b32 s23, 0\n"
      "s_branch .Lbk_d2_23\n"
      ".Lsp_d3_24:\n"
      "s_mov_b32 s22, s23\n"
      ".Lsl_d3_24:\n"
      "buffer_load_dword v20, v2, s[8:11], s51 offen sc1\n"
      "s_waitcnt vmcnt(0)\n"
      "v_cmp_ne_u32 vcc, s21, v20\n"
      "s_cbranch_vccz .Lbk_d3_24\n"
      "s_sleep 4\n"
      "s_sub_u32 s22, s22, 1\n"
      "s_cbranch_scc0 .Lsl_d3_24\n"
      "s_mov_b32 s23, 0\n"
      "s_branch .Lbk_d3_24\n"
      ".Lsp_dy2_25:\n"
      "s_lshl_b32 s22, s23, 4\n"
      ".Lsl_dy2_25:\n"
      "ds_read_b32 v15, v17\n"
      "s_waitcnt lgkmcnt(0)\n"
      "v_cmp_gt_i32 vcc, v14, v15\n"
      "s_cbranch_vccz .Lbk_dy2_25\n"
      "s_sub_u32 s22, s22, 1\n"
      "s_cbranch_scc0 .Lsl_dy2_25\n"
      "s_mov_b32 s23, 0\n"
      "s_branch .Lbk_dy2_25\n"
      ".Lsp_d3_26:\n"
      "s_mov_b32 s22, s23\n"
      ".Lsl_d3_26:\n"
      "buffer_load_dword v20, v2, s[8:11], s51 offen sc1\n"
      "s_waitcnt vmcnt(0)\n"
      "v_cmp_ne_u32 vcc, s21, v20\n"
      "s_cbranch_vccz .Lbk_d3_26\n"
      "s_sleep 4\n"
      "s_sub_u32 s22, s22, 1\n"
      "s_cbranch_scc0 .Lsl_d3_26\n"
      "s_mov_b32 s23, 0\n"
      "s_branch .Lbk_d3_26\n"
      ".Lsp_d4_27:\n"
      "s_mov_b32 s22, s23\n"
      ".Lsl_d4_27:\n"
      "buffer_load_dword v20, v2, s[8:11], s51 offen sc1\n"
      "s_waitcnt vmcnt(0)\n"
      "v_cmp_ne_u32 vcc, s21, v20\n"
      "s_cbranch_vccz .Lbk_d4_27\n"
      "s_sleep 4\n"
      "s_sub_u32 s22, s22, 1\n"
      "s_cbranch_scc0 .Lsl_d4_27\n"
      "s_mov_b32 s23, 0\n"
      "s_branch .Lbk_d4_27\n"
      ".Lsp_dy3_28:\n"
      "s_lshl_b32 s22, s23, 4\n"
      ".Lsl_dy3_28:\n"
      "ds_read_b32 v15, v17\n"
      "s_waitcnt lgkmcnt(0)\n"
      "v_cmp_gt_i32 vcc, v14, v15\n"
      "s_cbranch_vccz .Lbk_dy3_28\n"
      "s_sub_u32 s22, s22, 1\n"
      "s_cbranch_scc0 .Lsl_dy3_28\n"
      "s_mov_b32 s23, 0\n"
      "s_branch .Lbk_dy3_28\n"
      ".Lsp_d4_29:\n"
      "s_mov_b32 s22, s23\n"
      ".Lsl_d4_29:\n"
      "buffer_load_dword v20, v2, s[8:11], s51 offen sc1\n"
      "s_waitcnt vmcnt(0)\n"
      "v_cmp_ne_u32 vcc, s21, v20\n"
      "s_cbranch_vccz .Lbk_d4_29\n"
      "s_sleep 4\n"
      "s_sub_u32 s22, s22, 1\n"
      "s_cbranch_scc0 .Lsl_d4_29\n"
      "s_mov_b32 s23, 0\n"
      "s_branch .Lbk_d4_29\n"
      ".Lsp_d5_30:\n"
      "s_mov_b32 s22, s23\n"
      ".Lsl_d5_30:\n"
      "buffer_load_dword v20, v2, s[8:11], s51 offen sc1\n"
      "s_waitcnt vmcnt(0)\n"
      "v_cmp_ne_u32 vcc, s21, v20\n"
      "s_cbranch_vccz .Lbk_d5_30\n"
      "s_sleep 4\n"
      "s_sub_u32 s22, s22, 1\n"
      "s_cbranch_scc0 .Lsl_d5_30\n"
      "s_mov_b32 s23, 0\n"
      "s_branch .Lbk_d5_30\n"
      ".Lsp_dy4_31:\n"
      "s_lshl_b32 s22, s23, 4\n"
      ".Lsl_dy4_31:\n"
      "ds_read_b32 v15, v17\n"
      "s_waitcnt lgkmcnt(0)\n"
      "v_cmp_gt_i32 vcc, v14, v15\n"
      "s_cbranch_vccz .Lbk_dy4_31\n"
      "s_sub_u32 s22, s22, 1\n"
      "s_cbranch_scc0 .Lsl_dy4_31\n"
      "s_mov_b32 s23, 0\n"
      "s_branch .Lbk_dy4_31\n"
      ".Lsp_d5_32:\n"
      "s_mov_b32 s22, s23\n"
      ".Lsl_d5_32:\n"
      "buffer_load_dword v20, v2, s[8:11], s51 offen sc1\n"
      "s_waitcnt vmcnt(0)\n"
      "v_cmp_ne_u32 vcc, s21, v20\n"
      "s_cbranch_vccz .Lbk_d5_32\n"
      "s_sleep 4\n"
      "s_sub_u32 s22, s22, 1\n"
      "s_cbranch_scc0 .Lsl_d5_32\n"
      "s_mov_b32 s23, 0\n"
      "s_branch .Lbk_d5_32\n"
      ".Lsp_d0_33:\n"
      "s_mov_b32 s22, s23\n"
      ".Lsl_d0_33:\n"
      "buffer_load_dword v20, v2, s[8:11], s51 offen sc1\n"
      "s_waitcnt vmcnt(0)\n"
      "v_cmp_ne_u32 vcc, s21, v20\n"
      "s_cbranch_vccz .Lbk_d0_33\n"
      "s_sleep 4\n"
      "s_sub_u32 s22, s22, 1\n"
      "s_cbranch_scc0 .Lsl_d0_33\n"
      "s_mov_b32 s23, 0\n"
      "s_branch .Lbk_d0_33\n"
      ".Lsp_dy5_34:\n"
      "s_lshl_b32 s22, s23, 4\n"
      ".Lsl_dy5_34:\n"
      "ds_read_b32 v15, v17\n"
      "s_waitcnt lgkmcnt(0)\n"
      "v_cmp_gt_i32 vcc, v14, v15\n"
      "s_cbranch_vccz .Lbk_dy5_34\n"
      "s_sub_u32 s22, s22, 1\n"
      "s_cbranch_scc0 .Lsl_dy5_34\n"
      "s_mov_b32 s23, 0\n"
      "s_branch .Lbk_dy5_34\n"
      ".Ld_last:\n"
      :
      : "{s[8:11]}"(rsWS), "{s[12:15]}"(rsA), "{s[16:19]}"(rsB), "{s[92:95]}"(rsC), "{v0}"(v_l16), "{v1}"(v_l16b), "{v2}"(v_fa), "{v3}"(v_ga), "{v4}"(v_ra), "{v5}"(v_rf), "{v6}"(v_xp), "{v7}"(v_xs), "{v8}"(v_op), "{v9}"(v_os), "{v10}"(v_sw), "{v11}"(v_sr), "{v12}"(v_ss), "{v13}"(v_l8), "{v15}"(v_al), "{v16}"(v_yw), "{v17}"(v_yr), "{v19}"(v_hi), "{v176}"(v_at), "{v177}"(v_as), "{v178}"(v_vo0), "{v179}"(v_vo1), "{v180}"(v_vo2), "{v181}"(v_pw), "{v183}"(v_pu), "{s36}"(s_sh), "{s37}"(s_ps), "{s38}"(s_x1), "{s39}"(s_xr), "{s40}"(s_or), "{s41}"(s_p0), "{s42}"(s_m3), "{s43}"(s_m6), "{s44}"(s_so), "{s45}"(s_ph), "{s46}"(s_i0), "{s47}"(s_i5), "{s48}"(s_j0), "{s49}"(s_j5), "{s50}"(s_he), "{s60}"(s_k0)
      : "v255", "a255", "s99", "vcc", "scc", "memory");

  } else {
    const unsigned tr = xcd * 3 + (slot - 14) / 6, j = 6 * unit_ks(tr) + (slot - 14) % 6, gr = unit_gr(tr), f = 4 * j + w;
    const u32x4 rsA = srd(w >> 1 ? Wu : Wg, 576 * 4608 * 2), rsB = srd(Wqkv, 576 * 1728 * 2), rsC = srd(Wo, 576 * 576 * 2);
    const unsigned wv = (slot - 14) * 4 + w, hf = (xcd >> 2) * 512;
    const unsigned tq2 = i16 < 15 ? (8 * wv + g) * 3456 + i16 / 5 * 1152 + hf + i16 % 5 * 128 : OOB;
    const unsigned tq3 = (lane & 7) < 6 ? (8 * wv + l8) * 3456 + (lane >> 1 & 3) * 1152 + xcd * 128 + (lane & 1) * 128 : OOB;
    const unsigned v_tq[2] = {QMAP == 2 ? tq2 : tq3, QMAP == 2 ? tq2 + 4 * 3456 : OOB};
    const unsigned s_wv = wv < PSK ? 0 : (wv - PSK) * PCN / 16;
    const unsigned v_tw = (lane & 7) < 5 ? (8 * wv + l8) * 1152 + hf + (lane & 7) * 128 : OOB, v_td = (wv * 8 + (lane & 7)) * 128;
    const unsigned v_l5 = w < 3 ? v_l16 + 16384 - w * 3072 : OOB;
    const unsigned v_fa = 19456 - w * 4096 + (lane >> 3) * 128 + (lane & 7) * 4, v_ra = up * 8 * HREC + l5 + 147456 - f * 1024 + f * 128;
    const unsigned v_st = 18432 + i16 * 32 + q * 8, v_pw = 147456 - f * 1024 + f * 128 + l8 * HREC + l7;
    const unsigned v_at = 65536 + (8 * g + q) * 256 + ((4 * w + p) ^ (4 * q)) * 16;
    const unsigned v_vo0 = (16 * (w & 1) + g) * 9216 + 256 * j + (i16 ^ (4 * g)) * 16;
    const unsigned v_vo1 = v_vo0 + 4 * 9216 - 1024, v_vo2 = v_vo1 + 4 * 9216 - 1024, v_vo3 = v_vo2 + 4 * 9216 - 1024;
    const unsigned to0 = (4 * (72 * (w & 1) + (lane >> 1)) + gr) * 9216 + 256 * j + (lane & 1) * 128;
    const unsigned v_to[3] = {to0, to0 + 128 * 9216, lane < 16 ? to0 + 256 * 9216 : OOB};
    const unsigned s_sx = WS_X1 + gr * NREC * REC + w * 4096, s_sh = WS_H + gr * NREC * HREC + f * 1024;
    const unsigned s_m4 = w * 4096, s_m5 = 16384 + w * 1024, s_k0 = ROT * ((9 * gr + 1) >> 1);
    (void)s_k0;
    asm volatile(
      "s_buffer_load_dword s21, s[8:11], 0\n"
      "s_mov_b32 s23, 65536\n"
      "s_mov_b32 s31, 0x40000000\n"
      "s_mov_b32 s41, 0\n"
      "s_sleep 16\n"
      "buffer_load_dword v58, v54, s[92:95], s41 offen\n"
      "s_waitcnt vmcnt(0)\n"
      "s_add_u32 m0, s38, 65536\n"
      "s_nop 0\n"
      "buffer_load_dwordx4 v6, s[12:15], s41 offen offset:0 lds\n"
      "buffer_load_dwordx4 v7, s[12:15], s41 offen offset:1024 lds\n"
      "buffer_load_dwordx4 v8, s[12:15], s41 offen offset:2048 lds\n"
      "buffer_load_dwordx4 v9, s[12:15], s41 offen offset:3072 lds\n"
      "s_add_u32 s41, s41, 294912\n"
      "s_add_u32 m0, s38, 81920\n"
      "s_nop 0\n"
      "buffer_load_dwordx4 v6, s[12:15], s41 offen offset:0 lds\n"
      "buffer_load_dwordx4 v7, s[12:15], s41 offen offset:1024 lds\n"
      "buffer_load_dwordx4 v8, s[12:15], s41 offen offset:2048 lds\n"
      "buffer_load_dwordx4 v9, s[12:15], s41 offen offset:3072 lds\n"
      "s_add_u32 s41, s41, 294912\n"
      "s_add_u32 m0, s38, 98304\n"
      "s_nop 0\n"
      "buffer_load_dwordx4 v6, s[12:15], s41 offen offset:0 lds\n"
      "buffer_load_dwordx4 v7, s[12:15], s41 offen offset:1024 lds\n"
      "buffer_load_dwordx4 v8, s[12:15], s41 offen offset:2048 lds\n"
      "buffer_load_dwordx4 v9, s[12:15], s41 offen offset:3072 lds\n"
      "s_add_u32 s41, s41, 294912\n"
      "s_add_u32 m0, s38, 114688\n"
      "s_nop 0\n"
      "buffer_load_dwordx4 v6, s[12:15], s41 offen offset:0 lds\n"
      "buffer_load_dwordx4 v7, s[12:15], s41 offen offset:1024 lds\n"
      "buffer_load_dwordx4 v8, s[12:15], s41 offen offset:2048 lds\n"
      "buffer_load_dwordx4 v9, s[12:15], s41 offen offset:3072 lds\n"
      "s_add_u32 s41, s41, 294912\n"
      "s_add_u32 m0, s38, 131072\n"
      "s_nop 0\n"
      "buffer_load_dwordx4 v6, s[12:15], s41 offen offset:0 lds\n"
      "buffer_load_dwordx4 v7, s[12:15], s41 offen offset:1024 lds\n"
      "buffer_load_dwordx4 v8, s[12:15], s41 offen offset:2048 lds\n"
      "buffer_load_dwordx4 v9, s[12:15], s41 offen offset:3072 lds\n"
      "s_add_u32 s41, s41, 294912\n"
      "v_mov_b32 v172, 0x3f803f80\n"
      "v_mov_b32 v173, 0x3f803f80\n"
      "v_mov_b32 v174, 0x3f803f80\n"
      "v_mov_b32 v175, 0x3f803f80\n"
      "v_add_u32 v19, 49152, v5\n"
      "s_mov_b64 exec, 0\n"
      "s_lshr_b32 s22, s38, 12\n"
      ".Lg_pl:\n"
      "s_cmp_eq_u32 s22, 0\n"
      "s_cbranch_scc1 .Lg_p0\n"
      "s_cmp_eq_u32 s22, 1\n"
      "s_cbranch_scc1 .Lg_p1\n"
      "s_branch .Lg_pe\n"
      ".Lg_pr:\n"
      "s_add_u32 s22, s22, 4\n"
      "s_branch .Lg_pl\n"
      ".Lg_pe:\n"
      "s_mov_b64 exec, -1\n"
      "s_waitcnt vmcnt(16)\n"
      "s_barrier\n"
      "ds_read_b64_tr_b16 a[0:1], v5 offset:0\n"
      "ds_read_b64_tr_b16 a[2:3], v5 offset:1024\n"
      "ds_read_b64_tr_b16 a[72:73], v5 offset:8192\n"
      "ds_read_b64_tr_b16 a[74:75], v5 offset:9216\n"
      "ds_read_b64_tr_b16 a[144:145], v5 offset:8\n"
      "ds_read_b64_tr_b16 a[146:147], v5 offset:1032\n"
      "ds_read_b64_tr_b16 a[216:217], v5 offset:8200\n"
      "ds_read_b64_tr_b16 a[218:219], v5 offset:9224\n"
      "s_add_u32 m0, s38, 147456\n"
      "s_nop 0\n"
      "buffer_load_dwordx4 v6, s[12:15], s41 offen offset:0 lds\n"
      "buffer_load_dwordx4 v7, s[12:15], s41 offen offset:1024 lds\n"
      "buffer_load_dwordx4 v8, s[12:15], s41 offen offset:2048 lds\n"
      "buffer_load_dwordx4 v9, s[12:15], s41 offen offset:3072 lds\n"
      "s_add_u32 s41, s41, 294912\n"
      "s_waitcnt vmcnt(16) lgkmcnt(0)\n"
      "s_barrier\n"
      "ds_read_b64_tr_b16 a[4:5], v5 offset:16384\n"
      "ds_read_b64_tr_b16 a[6:7], v5 offset:17408\n"
      "ds_read_b64_tr_b16 a[76:77], v5 offset:24576\n"
      "ds_read_b64_tr_b16 a[78:79], v5 offset:25600\n"
      "ds_read_b64_tr_b16 a[148:149], v5 offset:16392\n"
      "ds_read_b64_tr_b16 a[150:151], v5 offset:17416\n"
      "ds_read_b64_tr_b16 a[220:221], v5 offset:24584\n"
      "ds_read_b64_tr_b16 a[222:223], v5 offset:25608\n"
      "v_mfma_f32_16x16x32_bf16 v[140:143], a[0:3], v[172:175], 0\n"
      "v_mfma_f32_16x16x32_bf16 v[144:147], a[72:75], v[172:175], 0\n"
      "v_mfma_f32_16x16x32_bf16 v[148:151], a[144:147], v[172:175], 0\n"
      "v_mfma_f32_16x16x32_bf16 v[152:155], a[216:219], v[172:175], 0\n"
      "s_add_u32 m0, s38, 65536\n"
      "s_nop 0\n"
      "buffer_load_dwordx4 v6, s[12:15], s41 offen offset:0 lds\n"
      "buffer_load_dwordx4 v7, s[12:15], s41 offen offset:1024 lds\n"
      "buffer_load_dwordx4 v8, s[12:15], s41 offen offset:2048 lds\n"
      "buffer_load_dwordx4 v9, s[12:15], s41 offen offset:3072 lds\n"
      "s_add_u32 s41, s41, 294912\n"
      "s_waitcnt vmcnt(16) lgkmcnt(0)\n"
      "s_barrier\n"
      "ds_read_b64_tr_b16 a[8:9], v5 offset:32768\n"
      "ds_read_b64_tr_b16 a[10:11], v5 offset:33792\n"
      "ds_read_b64_tr_b16 a[80:81], v5 offset:40960\n"
      "ds_read_b64_tr_b16 a[82:83], v5 offset:41984\n"
      "ds_read_b64_tr_b16 a[152:153], v5 offset:32776\n"
      "ds_read_b64_tr_b16 a[154:155], v5 offset:33800\n"
      "ds_read_b64_tr_b16 a[224:225], v5 offset:40968\n"
      "ds_read_b64_tr_b16 a[226:227], v5 offset:41992\n"
      "v_mfma_f32_16x16x32_bf16 v[140:143], a[4:7], v[172:175], v[140:143]\n"
      "v_mfma_f32_16x16x32_bf16 v[144:147], a[76:79], v[172:175], v[144:147]\n"
      "v_mfma_f32_16x16x32_bf16 v[148:151], a[148:151], v[172:175], v[148:151]\n"
      "v_mfma_f32_16x16x32_bf16 v[152:155], a[220:223], v[172:175], v[152:155]\n"
      "s_add_u32 m0, s38, 81920\n"
      "s_nop 0\n"
      "buffer_load_dwordx4 v6, s[12:15], s41 offen offset:0 lds\n"
      "buffer_load_dwordx4 v7, s[12:15], s41 offen offset:1024 lds\n"
      "buffer_load_dwordx4 v8, s[12:15], s41 offen offset:2048 lds\n"
      "buffer_load_dwordx4 v9, s[12:15], s41 offen offset:3072 lds\n"
      "s_add_u32 s41, s41, 294912\n"
      "s_waitcnt vmcnt(16) lgkmcnt(0)\n"
      "s_barrier\n"
      "ds_read_b64_tr_b16 a[12:13], v19 offset:0\n"
      "ds_read_b64_tr_b16 a[14:15], v19 offset:1024\n"
      "ds_read_b64_tr_b16 a[84:85], v19 offset:8192\n"
      "ds_read_b64_tr_b16 a[86:87], v19 offset:9216\n"
      "ds_read_b64_tr_b16 a[156:157], v19 offset:8\n"
      "ds_read_b64_tr_b16 a[158:159], v19 offset:1032\n"
      "ds_read_b64_tr_b16 a[228:229], v19 offset:8200\n"
      "ds_read_b64_tr_b16 a[230:231], v19 offset:9224\n"
      "v_mfma_f32_16x16x32_bf16 v[140:143], a[8:11], v[172:175], v[140:143]\n"
      "v_mfma_f32_16x16x32_bf16 v[144:147], a[80:83], v[172:175], v[144:147]\n"
      "v_mfma_f32_16x16x32_bf16 v[148:151], a[152:155], v[172:175], v[148:151]\n"
      "v_mfma_f32_16x16x32_bf16 v[152:155], a[224:227], v[172:175], v[152:155]\n"
      "s_add_u32 m0, s38, 98304\n"
      "s_nop 0\n"
      "buffer_load_dwordx4 v6, s[12:15], s41 offen offset:0 lds\n"
      "buffer_load_dwordx4 v7, s[12:15], s41 offen offset:1024 lds\n"
      "buffer_load_dwordx4 v8, s[12:15], s41 offen offset:2048 lds\n"
      "buffer_load_dwordx4 v9, s[12:15], s41 offen offset:3072 lds\n"
      "s_add_u32 s41, s41, 294912\n"
      "s_waitcnt vmcnt(16) lgkmcnt(0)\n"
      "s_barrier\n"
      "ds_read_b64_tr_b16 a[16:17], v19 offset:16384\n"
      "ds_read_b64_tr_b16 a[18:19], v19 offset:17408\n"
      "ds_read_b64_tr_b16 a[88:89], v19 offset:24576\n"
      "ds_read_b64_tr_b16 a[90:91], v19 offset:25600\n"
      "ds_read_b64_tr_b16 a[160:161], v19 offset:16392\n"
      "ds_read_b64_tr_b16 a[162:163], v19 offset:17416\n"
      "ds_read_b64_tr_b16 a[232:233], v19 offset:24584\n"
      "ds_read_b64_tr_b16 a[234:235], v19 offset:25608\n"
      "v_mfma_f32_16x16x32_bf16 v[140:143], a[12:15], v[172:175], v[140:143]\n"
      "v_mfma_f32_16x16x32_bf16 v[144:147], a[84:87], v[172:175], v[144:147]\n"
      "v_mfma_f32_16x16x32_bf16 v[148:151], a[156:159], v[172:175], v[148:151]\n"
      "v_mfma_f32_16x16x32_bf16 v[152:155], a[228:231], v[172:175], v[152:155]\n"
      "s_add_u32 m0, s38, 114688\n"
      "s_nop 0\n"
      "buffer_load_dwordx4 v6, s[12:15], s41 offen offset:0 lds\n"
      "buffer_load_dwordx4 v7, s[12:15], s41 offen offset:1024 lds\n"
      "buffer_load_dwordx4 v8, s[12:15], s41 offen offset:2048 lds\n"
      "buffer_load_dwordx4 v9, s[12:15], s41 offen offset:3072 lds\n"
      "s_add_u32 s41, s41, 294912\n"
      "s_waitcnt vmcnt(16) lgkmcnt(0)\n"
      "s_barrier\n"
      "ds_read_b64_tr_b16 a[20:21], v19 offset:32768\n"
      "ds_read_b64_tr_b16 a[22:23], v19 offset:33792\n"
      "ds_read_b64_tr_b16 a[92:93], v19 offset:40960\n"
      "ds_read_b64_tr_b16 a[94:95], v19 offset:41984\n"
      "ds_read_b64_tr_b16 a[164:165], v19 offset:32776\n"
      "ds_read_b64_tr_b16 a[166:167], v19 offset:33800\n"
      "ds_read_b64_tr_b16 a[236:237], v19 offset:40968\n"
      "ds_read_b64_tr_b16 a[238:239], v19 offset:41992\n"
      "v_mfma_f32_16x16x32_bf16 v[140:143], a[16:19], v[172:175], v[140:143]\n"
      "v_mfma_f32_16x16x32_bf16 v[144:147], a[88:91], v[172:175], v[144:147]\n"
      "v_mfma_f32_16x16x32_bf16 v[148:151], a[160:163], v[172:175], v[148:151]\n"
      "v_mfma_f32_16x16x32_bf16 v[152:155], a[232:235], v[172:175], v[152:155]\n"
      "s_add_u32 m0, s38, 131072\n"
      "s_nop 0\n"
      "buffer_load_dwordx4 v6, s[12:15], s41 offen offset:0 lds\n"
      "buffer_load_dwordx4 v7, s[12:15], s41 offen offset:1024 lds\n"
      "buffer_load_dwordx4 v8, s[12:15], s41 offen offset:2048 lds\n"
      "buffer_load_dwordx4 v9, s[12:15], s41 offen offset:3072 lds\n"
      "s_add_u32 s41, s41, 294912\n"
      "s_waitcnt vmcnt(16) lgkmcnt(0)\n"
      "s_barrier\n"
      "ds_read_b64_tr_b16 a[24:25], v5 offset:0\n"
      "ds_read_b64_tr_b16 a[26:27], v5 offset:1024\n"
      "ds_read_b64_tr_b16 a[96:97], v5 offset:8192\n"
      "ds_read_b64_tr_b16 a[98:99], v5 offset:9216\n"
      "ds_read_b64_tr_b16 a[168:169], v5 offset:8\n"
      "ds_read_b64_tr_b16 a[170:171], v5 offset:1032\n"
      "ds_read_b64_tr_b16 a[240:241], v5 offset:8200\n"
      "ds_read_b64_tr_b16 a[242:243], v5 offset:9224\n"
      "v_mfma_f32_16x16x32_bf16 v[140:143], a[20:23], v[172:175], v[140:143]\n"
      "v_mfma_f32_16x16x32_bf16 v[144:147], a[92:95], v[172:175], v[144:147]\n"
      "v_mfma_f32_16x16x32_bf16 v[148:151], a[164:167], v[172:175], v[148:151]\n"
      "v_mfma_f32_16x16x32_bf16 v[152:155], a[236:239], v[172:175], v[152:155]\n"
      "s_add_u32 m0, s38, 147456\n"
      "s_nop 0\n"
      "buffer_load_dwordx4 v6, s[12:15], s41 offen offset:0 lds\n"
      "buffer_load_dwordx4 v7, s[12:15], s41 offen offset:1024 lds\n"
      "buffer_load_dwordx4 v8, s[12:15], s41 offen offset:2048 lds\n"
      "buffer_load_dwordx4 v9, s[12:15], s41 offen offset:3072 lds\n"
      "s_add_u32 s41, s41, 294912\n"
      "s_waitcnt vmcnt(16) lgkmcnt(0)\n"
      "s_barrier\n"
      "ds_read_b64_tr_b16 a[28:29], v5 offset:16384\n"
      "ds_read_b64_tr_b16 a[30:31], v5 offset:17408\n"
      "ds_read_b64_tr_b16 a[100:101], v5 offset:24576\n"
      "ds_read_b64_tr_b16 a[102:103], v5 offset:25600\n"
      "ds_read_b64_tr_b16 a[172:173], v5 offset:16392\n"
      "ds_read_b64_tr_b16 a[174:175], v5 offset:17416\n"
      "ds_read_b64_tr_b16 a[244:245], v5 offset:24584\n"
      "ds_read_b64_tr_b16 a[246:247], v5 offset:25608\n"
      "v_mfma_f32_16x16x32_bf16 v[140:143], a[24:27], v[172:175], v[140:143]\n"
      "v_mfma_f32_16x16x32_bf16 v[144:147], a[96:99], v[172:175], v[144:147]\n"
      "v_mfma_f32_16x16x32_bf16 v[148:151], a[168:171], v[172:175], v[148:151]\n"
      "v_mfma_f32_16x16x32_bf16 v[152:155], a[240:243], v[172:175], v[152:155]\n"
      "s_add_u32 m0, s38, 65536\n"
      "s_nop 0\n"
      "buffer_load_dwordx4 v6, s[12:15], s41 offen offset:0 lds\n"
      "buffer_load_dwordx4 v7, s[12:15], s41 offen offset:1024 lds\n"
      "buffer_load_dwordx4 v8, s[12:15], s41 offen offset:2048 lds\n"
      "buffer_load_dwordx4 v9, s[12:15], s41 offen offset:3072 lds\n"
      "s_add_u32 s41, s41, 294912\n"
      "s_waitcnt vmcnt(16) lgkmcnt(0)\n"
      "s_barrier\n"
      "ds_read_b64_tr_b16 a[32:33], v5 offset:32768\n"
      "ds_read_b64_tr_b16 a[34:35], v5 offset:33792\n"
      "ds_read_b64_tr_b16 a[104:105], v5 offset:40960\n"
      "ds_read_b64_tr_b16 a[106:107], v5 offset:41984\n"
      "ds_read_b64_tr_b16 a[176:177], v5 offset:32776\n"
      "ds_read_b64_tr_b16 a[178:179], v5 offset:33800\n"
      "ds_read_b64_tr_b16 a[248:249], v5 offset:40968\n"
      "ds_read_b64_tr_b16 a[250:251], v5 offset:41992\n"
      "v_mfma_f32_16x16x32_bf16 v[140:143], a[28:31], v[172:175], v[140:143]\n"
      "v_mfma_f32_16x16x32_bf16 v[144:147], a[100:103], v[172:175], v[144:147]\n"
      "v_mfma_f32_16x16x32_bf16 v[148:151], a[172:175], v[172:175], v[148:151]\n"
      "v_mfma_f32_16x16x32_bf16 v[152:155], a[244:247], v[172:175], v[152:155]\n"
      "s_add_u32 m0, s38, 81920\n"
      "s_nop 0\n"
      "buffer_load_dwordx4 v6, s[12:15], s41 offen offset:0 lds\n"
      "buffer_load_dwordx4 v7, s[12:15], s41 offen offset:1024 lds\n"
      "buffer_load_dwordx4 v8, s[12:15], s41 offen offset:2048 lds\n"
      "buffer_load_dwordx4 v9, s[12:15], s41 offen offset:3072 lds\n"
      "s_add_u32 s41, s41, 294912\n"
      "s_waitcnt vmcnt(16) lgkmcnt(0)\n"
      "s_barrier\n"
      "ds_read_b64_tr_b16 a[36:37], v19 offset:0\n"
      "ds_read_b64_tr_b16 a[38:39], v19 offset:1024\n"
      "ds_read_b64_tr_b16 a[108:109], v19 offset:8192\n"
      "ds_read_b64_tr_b16 a[110:111], v19 offset:9216\n"
      "ds_read_b64_tr_b16 a[180:181], v19 offset:8\n"
      "ds_read_b64_tr_b16 a[182:183], v19 offset:1032\n"
      "ds_read_b64_tr_b16 a[252:253], v19 offset:8200\n"
      "ds_read_b64_tr_b16 a[254:255], v19 offset:9224\n"
      "v_mfma_f32_16x16x32_bf16 v[140:143], a[32:35], v[172:175], v[140:143]\n"
      "v_mfma_f32_16x16x32_bf16 v[144:147], a[104:107], v[172:175], v[144:147]\n"
      "v_mfma_f32_16x16x32_bf16 v[148:151], a[176:179], v[172:175], v[148:151]\n"
      "v_mfma_f32_16x16x32_bf16 v[152:155], a[248:251], v[172:175], v[152:155]\n"
      "s_add_u32 m0, s38, 98304\n"
      "s_nop 0\n"
      "buffer_load_dwordx4 v6, s[12:15], s41 offen offset:0 lds\n"
      "buffer_load_dwordx4 v7, s[12:15], s41 offen offset:1024 lds\n"
      "buffer_load_dwordx4 v8, s[12:15], s41 offen offset:2048 lds\n"
      "buffer_load_dwordx4 v9, s[12:15], s41 offen offset:3072 lds\n"
      "s_add_u32 s41, s41, 294912\n"
      "s_waitcnt vmcnt(16) lgkmcnt(0)\n"
      "s_barrier\n"
      "ds_read_b64_tr_b16 a[40:41], v19 offset:16384\n"
      "ds_read_b64_tr_b16 a[42:43], v19 offset:17408\n"
      "ds_read_b64_tr_b16 a[112:113], v19 offset:24576\n"
      "ds_read_b64_tr_b16 a[114:115], v19 offset:25600\n"
      "ds_read_b64_tr_b16 a[184:185], v19 offset:16392\n"
      "ds_read_b64_tr_b16 a[186:187], v19 offset:17416\n"
      "ds_read_b64_tr_b16 v[20:21], v19 offset:24584\n"
      "ds_read_b64_tr_b16 v[22:23], v19 offset:25608\n"
      "v_mfma_f32_16x16x32_bf16 v[140:143], a[36:39], v[172:175], v[140:143]\n"
      "v_mfma_f32_16x16x32_bf16 v[144:147], a[108:111], v[172:175], v[144:147]\n"
      "v_mfma_f32_16x16x32_bf16 v[148:151], a[180:183], v[172:175], v[148:151]\n"
      "v_mfma_f32_16x16x32_bf16 v[152:155], a[252:255], v[172:175], v[152:155]\n"
      "s_add_u32 m0, s38, 114688\n"
      "s_nop 0\n"
      "buffer_load_dwordx4 v6, s[12:15], s41 offen offset:0 lds\n"
      "buffer_load_dwordx4 v7, s[12:15], s41 offen offset:1024 lds\n"
      "buffer_load_dwordx4 v8, s[12:15], s41 offen offset:2048 lds\n"
      "buffer_load_dwordx4 v9, s[12:15], s41 offen offset:3072 lds\n"
      "s_add_u32 s41, s41, 294912\n"
      "s_waitcnt vmcnt(16) lgkmcnt(0)\n"
      "s_barrier\n"
      "ds_read_b64_tr_b16 a[44:45], v19 offset:32768\n"
      "ds_read_b64_tr_b16 a[46:47], v19 offset:33792\n"
      "ds_read_b64_tr_b16 a[116:117], v19 offset:40960\n"
      "ds_read_b64_tr_b16 a[118:119], v19 offset:41984\n"
      "ds_read_b64_tr_b16 a[188:189], v19 offset:32776\n"
      "ds_read_b64_tr_b16 a[190:191], v19 offset:33800\n"
      "ds_read_b64_tr_b16 v[24:25], v19 offset:40968\n"
      "ds_read_b64_tr_b16 v[26:27], v19 offset:41992\n"
      "v_mfma_f32_16x16x32_bf16 v[140:143], a[40:43], v[172:175], v[140:143]\n"
      "v_mfma_f32_16x16x32_bf16 v[144:147], a[112:115], v[172:175], v[144:147]\n"
      "v_mfma_f32_16x16x32_bf16 v[148:151], a[184:187], v[172:175], v[148:151]\n"
      "v_mfma_f32_16x16x32_bf16 v[152:155], v[20:23], v[172:175], v[152:155]\n"
      "s_add_u32 m0, s38, 131072\n"
      "s_nop 0\n"
      "buffer_load_dwordx4 v6, s[12:15], s41 offen offset:0 lds\n"
      "buffer_load_dwordx4 v7, s[12:15], s41 offen offset:1024 lds\n"
      "buffer_load_dwordx4 v8, s[12:15], s41 offen offset:2048 lds\n"
      "buffer_load_dwordx4 v9, s[12:15], s41 offen offset:3072 lds\n"
      "s_add_u32 s41, s41, 294912\n"
      "s_waitcnt vmcnt(16) lgkmcnt(0)\n"
      "s_barrier\n"
      "ds_read_b64_tr_b16 a[48:49], v5 offset:0\n"
      "ds_read_b64_tr_b16 a[50:51], v5 offset:1024\n"
      "ds_read_b64_tr_b16 a[120:121], v5 offset:8192\n"
      "ds_read_b64_tr_b16 a[122:123], v5 offset:9216\n"
      "ds_read_b64_tr_b16 a[192:193], v5 offset:8\n"
      "ds_read_b64_tr_b16 a[194:195], v5 offset:1032\n"
      "ds_read_b64_tr_b16 v[28:29], v5 offset:8200\n"
      "ds_read_b64_tr_b16 v[30:31], v5 offset:9224\n"
      "v_mfma_f32_16x16x32_bf16 v[140:143], a[44:47], v[172:175], v[140:143]\n"
      "v_mfma_f32_16x16x32_bf16 v[144:147], a[116:119], v[172:175], v[144:147]\n"
      "v_mfma_f32_16x16x32_bf16 v[148:151], a[188:191], v[172:175], v[148:151]\n"
      "v_mfma_f32_16x16x32_bf16 v[152:155], v[24:27], v[172:175], v[152:155]\n"
      "s_add_u32 m0, s38, 147456\n"
      "s_nop 0\n"
      "buffer_load_dwordx4 v6, s[12:15], s41 offen offset:0 lds\n"
      "buffer_load_dwordx4 v7, s[12:15], s41 offen offset:1024 lds\n"
      "buffer_load_dwordx4 v8, s[12:15], s41 offen offset:2048 lds\n"
      "buffer_load_dwordx4 v9, s[12:15], s41 offen offset:3072 lds\n"
      "s_add_u32 s41, s41, 294912\n"
      "s_waitcnt vmcnt(16) lgkmcnt(0)\n"
      "s_barrier\n"
      "ds_read_b64_tr_b16 a[52:53], v5 offset:16384\n"
      "ds_read_b64_tr_b16 a[54:55], v5 offset:17408\n"
      "ds_read_b64_tr_b16 a[124:125], v5 offset:24576\n"
      "ds_read_b64_tr_b16 a[126:127], v5 offset:25600\n"
      "ds_read_b64_tr_b16 a[196:197], v5 offset:16392\n"
      "ds_read_b64_tr_b16 a[198:199], v5 offset:17416\n"
      "ds_read_b64_tr_b16 v[32:33], v5 offset:24584\n"
      "ds_read_b64_tr_b16 v[34:35], v5 offset:25608\n"
      "v_mfma_f32_16x16x32_bf16 v[140:143], a[48:51], v[172:175], v[140:143]\n"
      "v_mfma_f32_16x16x32_bf16 v[144:147], a[120:123], v[172:175], v[144:147]\n"
      "v_mfma_f32_16x16x32_bf16 v[148:151], a[192:195], v[172:175], v[148:151]\n"
      "v_mfma_f32_16x16x32_bf16 v[152:155], v[28:31], v[172:175], v[152:155]\n"
      "s_waitcnt vmcnt(12) lgkmcnt(0)\n"
      "s_barrier\n"
      "ds_read_b64_tr_b16 a[56:57], v5 offset:32768\n"
      "ds_read_b64_tr_b16 a[58:59], v5 offset:33792\n"
      "ds_read_b64_tr_b16 a[128:129], v5 offset:40960\n"
      "ds_read_b64_tr_b16 a[130:131], v5 offset:41984\n"
      "ds_read_b64_tr_b16 a[200:201], v5 offset:32776\n"
      "ds_read_b64_tr_b16 a[202:203], v5 offset:33800\n"
      "ds_read_b64_tr_b16 v[36:37], v5 offset:40968\n"
      "ds_read_b64_tr_b16 v[38:39], v5 offset:41992\n"
      "v_mfma_f32_16x16x32_bf16 v[140:143], a[52:55], v[172:175], v[140:143]\n"
      "v_mfma_f32_16x16x32_bf16 v[144:147], a[124:127], v[172:175], v[144:147]\n"
      "v_mfma_f32_16x16x32_bf16 v[148:151], a[196:199], v[172:175], v[148:151]\n"
      "v_mfma_f32_16x16x32_bf16 v[152:155], v[32:35], v[172:175], v[152:155]\n"
      "s_waitcnt vmcnt(8) lgkmcnt(0)\n"
      "s_barrier\n"
      "ds_read_b64_tr_b16 a[60:61], v19 offset:0\n"
      "ds_read_b64_tr_b16 a[62:63], v19 offset:1024\n"
      "ds_read_b64_tr_b16 a[132:133], v19 offset:8192\n"
      "ds_read_b64_tr_b16 a[134:135], v19 offset:9216\n"
      "ds_read_b64_tr_b16 a[204:205], v19 offset:8\n"
      "ds_read_b64_tr_b16 a[206:207], v19 offset:1032\n"
      "ds_read_b64_tr_b16 v[40:41], v19 offset:8200\n"
      "ds_read_b64_tr_b16 v[42:43], v19 offset:9224\n"
      "v_mfma_f32_16x16x32_bf16 v[140:143], a[56:59], v[172:175], v[140:143]\n"
      "v_mfma_f32_16x16x32_bf16 v[144:147], a[128:131], v[172:175], v[144:147]\n"
      "v_mfma_f32_16x16x32_bf16 v[148:151], a[200:203], v[172:175], v[148:151]\n"
      "v_mfma_f32_16x16x32_bf16 v[152:155], v[36:39], v[172:175], v[152:155]\n"
      "s_waitcnt vmcnt(4) lgkmcnt(0)\n"
      "s_barrier\n"
      "ds_read_b64_tr_b16 a[64:65], v19 offset:16384\n"
      "ds_read_b64_tr_b16 a[66:67], v19 offset:17408\n"
      "ds_read_b64_tr_b16 a[136:137], v19 offset:24576\n"
      "ds_read_b64_tr_b16 a[138:139], v19 offset:25600\n"
      "ds_read_b64_tr_b16 a[208:209], v19 offset:16392\n"
      "ds_read_b64_tr_b16 a[210:211], v19 offset:17416\n"
      "ds_read_b64_tr_b16 v[44:45], v19 offset:24584\n"
      "ds_read_b64_tr_b16 v[46:47], v19 offset:25608\n"
      "v_mfma_f32_16x16x32_bf16 v[140:143], a[60:63], v[172:175], v[140:143]\n"
      "v_mfma_f32_16x16x32_bf16 v[144:147], a[132:135], v[172:175], v[144:147]\n"
      "v_mfma_f32_16x16x32_bf16 v[148:151], a[204:207], v[172:175], v[148:151]\n"
      "v_mfma_f32_16x16x32_bf16 v[152:155], v[40:43], v[172:175], v[152:155]\n"
      "s_waitcnt vmcnt(0) lgkmcnt(0)\n"
      "s_barrier\n"
      "ds_read_b64_tr_b16 a[68:69], v19 offset:32768\n"
      "ds_read_b64_tr_b16 a[70:71], v19 offset:33792\n"
      "ds_read_b64_tr_b16 a[140:141], v19 offset:40960\n"
      "ds_read_b64_tr_b16 a[142:143], v19 offset:41984\n"
      "ds_read_b64_tr_b16 a[212:213], v19 offset:32776\n"
      "ds_read_b64_tr_b16 a[214:215], v19 offset:33800\n"
      "ds_read_b64_tr_b16 v[48:49], v19 offset:40968\n"
      "ds_read_b64_tr_b16 v[50:51], v19 offset:41992\n"
      "v_mfma_f32_16x16x32_bf16 v[140:143], a[64:67], v[172:175], v[140:143]\n"
      "v_mfma_f32_16x16x32_bf16 v[144:147], a[136:139], v[172:175], v[144:147]\n"
      "v_mfma_f32_16x16x32_bf16 v[148:151], a[208:211], v[172:175], v[148:151]\n"
      "v_mfma_f32_16x16x32_bf16 v[152:155], v[44:47], v[172:175], v[152:155]\n"
      "s_waitcnt lgkmcnt(0)\n"
      "v_mfma_f32_16x16x32_bf16 v[140:143], a[68:71], v[172:175], v[140:143]\n"
      "v_mfma_f32_16x16x32_bf16 v[144:147], a[140:143], v[172:175], v[144:147]\n"
      "v_mfma_f32_16x16x32_bf16 v[148:151], a[212:215], v[172:175], v[148:151]\n"
      "v_mfma_f32_16x16x32_bf16 v[152:155], v[48:51], v[172:175], v[152:155]\n"
      "s_mul_i32 s21, s21, 1664525\n"
      "s_add_u32 s21, s21, 1013904223\n"
      "v_mov_b32 v15, s21\n"
      "buffer_store_dword v15, v12, s[8:11], 0 offen sc1\n"
      "v_not_b32 v52, v15\n"
      "v_not_b32 v53, v15\n"
      "v_not_b32 v54, v15\n"
      "v_not_b32 v55, v15\n"
      "s_add_u32 s30, s37, 0\n"
      "buffer_store_dwordx4 v[52:55], v10, s[8:11], s30 offen sc1\n"
      "v_xor_b32 v15, v15, v16\n"
      "v_add_u32 v17, 40960, v0\n"
      "s_barrier\n"
      "v_mov_b32 v11, 0\n"
      "ds_write_b32 v13, v11\n"
      "s_waitcnt lgkmcnt(0)\n"
      "s_barrier\n"
      "s_add_u32 s6, s36, 1548288\n"
      "s_add_u32 s36, s36, 4294942720\n"
      "s_add_u32 s42, s37, 4294635520\n"
      "s_add_u32 s37, s37, 4294635520\n"
      "s_add_u32 s36, s36, 24576\n"
      "s_min_u32 s36, s36, s6\n"
      "buffer_load_dword v18, v2, s[8:11], s36 offen sc1\n"
      "s_mov_b32 s40, s36\n"
      "s_add_u32 s36, s36, 24576\n"
      "s_add_u32 m0, s38, 0\n"
      "s_waitcnt vmcnt(0)\n"
      "v_cmp_ne_u32 vcc, s21, v18\n"
      "s_cbranch_vccnz .Lsp_g0_1\n"
      ".Lbk_g0_1:\n"
      "buffer_load_dwordx4 v0, s[8:11], s40 offen offset:1024 sc1 lds\n"
      "buffer_load_dwordx4 v0, s[8:11], s40 offen offset:0 sc1 lds\n"
      "buffer_load_dwordx4 v0, s[8:11], s40 offen offset:2048 sc1 lds\n"
      "buffer_load_dwordx4 v0, s[8:11], s40 offen offset:3072 sc1 lds\n"
      "s_add_u32 m0, s39, 0\n"
      "s_nop 0\n"
      "buffer_load_dwordx4 v1, s[8:11], s40 offen offset:0 sc1 lds\n"
      "s_min_u32 s36, s36, s6\n"
      "buffer_load_dword v18, v2, s[8:11], s36 offen sc1\n"
      "s_mov_b32 s40, s36\n"
      "v_add_u32 v11, 1, v11\n"
      "s_add_u32 s36, s36, 24576\n"
      "s_add_u32 m0, s38, 20480\n"
      "s_waitcnt vmcnt(1)\n"
      "ds_write_b32 v13, v11\n"
      "s_waitcnt vmcnt(0)\n"
      "v_cmp_ne_u32 vcc, s21, v18\n"
      "ds_read_b32 v12, v14\n"
      "s_cbranch_vccnz .Lsp_g0_2\n"
      ".Lbk_g0_2:\n"
      "buffer_load_dwordx4 v0, s[8:11], s40 offen offset:1024 sc1 lds\n"
      "s_waitcnt lgkmcnt(0)\n"
      "v_cmp_gt_i32 vcc, v11, v12\n"
      "buffer_load_dwordx4 v0, s[8:11], s40 offen offset:0 sc1 lds\n"
      "s_cbranch_vccnz .Lsp_gy0_3\n"
      ".Lbk_gy0_3:\n"
      "ds_read_b128 v[60:63], v0 offset:2048\n"
      "buffer_load_dwordx4 v0, s[8:11], s40 offen offset:2048 sc1 lds\n"
      "ds_read_b128 v[52:55], v0 offset:0\n"
      "buffer_load_dwordx4 v0, s[8:11], s40 offen offset:3072 sc1 lds\n"
      "ds_read_b128 v[76:79], v0 offset:6144\n"
      "ds_read2st64_b64 v[168:171], v4 offset0:0 offset1:1\n"
      "s_add_u32 m0, s39, 20480\n"
      "ds_read_b128 v[72:75], v0 offset:5120\n"
      "ds_read_b128 v[68:71], v0 offset:4096\n"
      "buffer_load_dwordx4 v1, s[8:11], s40 offen offset:0 sc1 lds\n"
      "s_min_u32 s36, s36, s6\n"
      "ds_read_b128 v[56:59], v0 offset:1024\n"
      "ds_read_b128 v[64:67], v0 offset:3072\n"
      "ds_read_b128 v[88:91], v0 offset:9216\n"
      "buffer_load_dword v16, v13, s[8:11], s31 offen\n"
      "ds_read_b128 v[108:111], v0 offset:14336\n"
      "ds_read_b128 v[80:83], v0 offset:7168\n"
      "ds_read_b128 v[104:107], v0 offset:13312\n"
      "s_waitcnt lgkmcnt(8)\n"
      "v_add_f32 v169, v169, v171\n"
      "buffer_load_dword v18, v2, s[8:11], s36 offen sc1\n"
      "s_mov_b32 s20, 15\n"
      "s_waitcnt lgkmcnt(0)\n"
      "s_nop 4\n"
      "s_branch .Lg_loop\n"
      ".p2align 12\n"
      ".Lg_p0:\n"
      "s_branch .Lg_pr\n"
      ".p2align 5\n"
      ".Lg_loop:\n"
      "v_mfma_f32_16x16x32_bf16 v[124:127], a[0:3], v[52:55], 0\n"
      "s_waitcnt lgkmcnt(5)\n"
      "ds_read_b128 v[84:87], v0 offset:8192\n"
      "v_fmac_f32 v130, v176, v146\n"
      "v_mfma_f32_16x16x32_bf16 v[124:127], a[4:7], v[56:59], v[124:127]\n"
      "ds_read_b128 v[92:95], v0 offset:10240\n"
      "v_fma_f32 v163, v163, v177, v177\n"
      "v_mfma_f32_16x16x32_bf16 v[124:127], a[8:11], v[60:63], v[124:127]\n"
      "s_waitcnt lgkmcnt(5)\n"
      "ds_read_b128 v[96:99], v0 offset:11264\n"
      "v_fma_f32 v162, v162, v179, v178\n"
      "v_mfma_f32_16x16x32_bf16 v[124:127], a[12:15], v[64:67], v[124:127]\n"
      "ds_read_b128 v[100:103], v0 offset:12288\n"
      "v_mul_f32 v130, v130, v158\n"
      "v_mfma_f32_16x16x32_bf16 v[124:127], a[16:19], v[68:71], v[124:127]\n"
      "s_mov_b32 s40, s36\n"
      "v_mul_f32 v162, v134, v162\n"
      "v_fmac_f32 v137, v176, v153\n"
      "v_mfma_f32_16x16x32_bf16 v[124:127], a[20:23], v[72:75], v[124:127]\n"
      "v_rcp_f32 v163, v163\n"
      "v_mfma_f32_16x16x32_bf16 v[124:127], a[24:27], v[76:79], v[124:127]\n"
      "s_waitcnt lgkmcnt(4)\n"
      "ds_read_b128 v[112:115], v0 offset:15360\n"
      "v_fmac_f32 v138, v176, v154\n"
      "v_mfma_f32_16x16x32_bf16 v[124:127], a[28:31], v[80:83], v[124:127]\n"
      "s_waitcnt lgkmcnt(4)\n"
      "v_exp_f32 v162, v162\n"
      "v_mfma_f32_16x16x32_bf16 v[124:127], a[32:35], v[84:87], v[124:127]\n"
      "ds_read_b128 v[116:119], v0 offset:16384\n"
      "v_mul_f32 v129, v129, v157\n"
      "v_mfma_f32_16x16x32_bf16 v[124:127], a[36:39], v[88:91], v[124:127]\n"
      "s_waitcnt lgkmcnt(3)\n"
      "ds_read_b128 v[120:123], v0 offset:17408\n"
      "v_fma_f32 v162, v162, v177, v177\n"
      "v_rcp_f32 v162, v162\n"
      "v_mfma_f32_16x16x32_bf16 v[124:127], a[40:43], v[92:95], v[124:127]\n"
      "v_mul_f32 v137, v137, v161\n"
      "v_fmac_f32 v136, v176, v152\n"
      "v_mfma_f32_16x16x32_bf16 v[124:127], a[44:47], v[96:99], v[124:127]\n"
      "s_waitcnt lgkmcnt(3)\n"
      "v_mul_f32 v136, v136, v160\n"
      "v_mul_f32 v131, v131, v159\n"
      "v_mfma_f32_16x16x32_bf16 v[124:127], a[48:51], v[100:103], v[124:127]\n"
      "s_add_u32 s42, s42, 165888\n"
      "v_mul_f32 v162, v134, v162\n"
      "v_cvt_pk_bf16_f32 v166, v136, v137\n"
      "v_mfma_f32_16x16x32_bf16 v[124:127], a[52:55], v[104:107], v[124:127]\n"
      "v_fmac_f32 v128, v176, v144\n"
      "v_mul_f32 v138, v138, v162\n"
      "v_mfma_f32_16x16x32_bf16 v[124:127], a[56:59], v[108:111], v[124:127]\n"
      "s_waitcnt lgkmcnt(2)\n"
      "v_cvt_pk_bf16_f32 v165, v130, v131\n"
      "v_add_u32 v11, 1, v11\n"
      "v_mfma_f32_16x16x32_bf16 v[124:127], a[60:63], v[112:115], v[124:127]\n"
      "s_waitcnt lgkmcnt(1)\n"
      "v_mul_f32 v163, v135, v163\n"
      "v_fmac_f32 v139, v176, v155\n"
      "v_mfma_f32_16x16x32_bf16 v[124:127], a[64:67], v[116:119], v[124:127]\n"
      "s_waitcnt lgkmcnt(0)\n"
      "v_mul_f32 v128, v128, v156\n"
      "v_mul_f32 v139, v139, v163\n"
      "v_mfma_f32_16x16x32_bf16 v[124:127], a[68:71], v[120:123], v[124:127]\n"
      "s_add_u32 s37, s37, 165888\n"
      "v_cvt_pk_bf16_f32 v164, v128, v129\n"
      "v_cvt_pk_bf16_f32 v167, v138, v139\n"
      "v_mfma_f32_16x16x32_bf16 v[132:135], a[144:147], v[52:55], 0\n"
      "buffer_store_dwordx4 v[164:167], v0, s[8:11], s37 offen sc1\n"
      "v_mfma_f32_16x16x32_bf16 v[132:135], a[148:151], v[56:59], v[132:135]\n"
      "v_sub_f32 v176, -v168, v170\n"
      "v_fma_f32 v169, -v176, v176, v169\n"
      "v_mfma_f32_16x16x32_bf16 v[132:135], a[152:155], v[60:63], v[132:135]\n"
      "v_fmac_f32 v126, v176, v142\n"
      "v_fmac_f32 v124, v176, v140\n"
      "v_mfma_f32_16x16x32_bf16 v[132:135], a[156:159], v[64:67], v[132:135]\n"
      "v_mul_f32 v156, v124, v124\n"
      "v_max_f32 v177, 0x3727c5ac, v169\n"
      "v_mfma_f32_16x16x32_bf16 v[132:135], a[160:163], v[68:71], v[132:135]\n"
      "v_rsq_f32 v170, v177\n"
      "v_mfma_f32_16x16x32_bf16 v[132:135], a[164:167], v[72:75], v[132:135]\n"
      "v_fmac_f32 v125, v176, v141\n"
      "v_mul_f32 v157, v125, v125\n"
      "v_mfma_f32_16x16x32_bf16 v[132:135], a[168:171], v[76:79], v[132:135]\n"
      "v_mul_f32 v178, 0xc0135761, v170\n"
      "v_fmac_f32 v127, v176, v143\n"
      "v_mfma_f32_16x16x32_bf16 v[132:135], a[172:175], v[80:83], v[132:135]\n"
      "v_mul_f32 v171, 0x3d372713, v178\n"
      "v_mul_f32 v170, v170, v170\n"
      "v_mfma_f32_16x16x32_bf16 v[132:135], a[176:179], v[84:87], v[132:135]\n"
      "v_mul_f32 v158, v126, v126\n"
      "v_mul_f32 v159, v127, v127\n"
      "v_mfma_f32_16x16x32_bf16 v[132:135], a[180:183], v[88:91], v[132:135]\n"
      "v_mul_f32 v179, v171, v170\n"
      "v_fma_f32 v157, v157, v179, v178\n"
      "v_mfma_f32_16x16x32_bf16 v[132:135], a[184:187], v[92:95], v[132:135]\n"
      "v_fma_f32 v159, v159, v179, v178\n"
      "v_mul_f32 v157, v125, v157\n"
      "v_mfma_f32_16x16x32_bf16 v[132:135], a[188:191], v[96:99], v[132:135]\n"
      "s_add_u32 s36, s36, 24576\n"
      "v_exp_f32 v157, v157\n"
      "v_mfma_f32_16x16x32_bf16 v[132:135], a[192:195], v[100:103], v[132:135]\n"
      "v_fma_f32 v156, v156, v179, v178\n"
      "v_mul_f32 v156, v124, v156\n"
      "v_mfma_f32_16x16x32_bf16 v[132:135], a[196:199], v[104:107], v[132:135]\n"
      "v_fma_f32 v158, v158, v179, v178\n"
      "v_mul_f32 v159, v127, v159\n"
      "v_mfma_f32_16x16x32_bf16 v[132:135], a[200:203], v[108:111], v[132:135]\n"
      "v_exp_f32 v156, v156\n"
      "v_mfma_f32_16x16x32_bf16 v[132:135], a[204:207], v[112:115], v[132:135]\n"
      "s_add_u32 m0, s38, 40960\n"
      "v_exp_f32 v159, v159\n"
      "v_mfma_f32_16x16x32_bf16 v[132:135], a[208:211], v[116:119], v[132:135]\n"
      "v_mul_f32 v158, v126, v158\n"
      "v_fma_f32 v159, v159, v177, v177\n"
      "v_mfma_f32_16x16x32_bf16 v[132:135], a[212:215], v[120:123], v[132:135]\n"
      "v_rcp_f32 v159, v159\n"
      "v_mfma_f32_16x16x32_bf16 v[128:131], a[72:75], v[52:55], 0\n"
      "s_waitcnt vmcnt(3)\n"
      "ds_write_b32 v13, v11\n"
      "v_mfma_f32_16x16x32_bf16 v[128:131], a[76:79], v[56:59], v[128:131]\n"
      "v_exp_f32 v158, v158\n"
      "v_mfma_f32_16x16x32_bf16 v[128:131], a[80:83], v[60:63], v[128:131]\n"
      "v_fma_f32 v156, v156, v177, v177\n"
      "v_fmac_f32 v135, v176, v151\n"
      "v_mfma_f32_16x16x32_bf16 v[128:131], a[84:87], v[64:67], v[128:131]\n"
      "v_fmac_f32 v133, v176, v149\n"
      "v_mul_f32 v163, v135, v135\n"
      "v_mfma_f32_16x16x32_bf16 v[128:131], a[88:91], v[68:71], v[128:131]\n"
      "v_mul_f32 v161, v133, v133\n"
      "v_fma_f32 v163, v163, v179, v178\n"
      "v_fma_f32 v161, v161, v179, v178\n"
      "v_mfma_f32_16x16x32_bf16 v[128:131], a[92:95], v[72:75], v[128:131]\n"
      "v_fma_f32 v157, v157, v177, v177\n"
      "v_mul_f32 v161, v133, v161\n"
      "v_mfma_f32_16x16x32_bf16 v[128:131], a[96:99], v[76:79], v[128:131]\n"
      "v_exp_f32 v161, v161\n"
      "v_mfma_f32_16x16x32_bf16 v[128:131], a[100:103], v[80:83], v[128:131]\n"
      "s_waitcnt vmcnt(1) lgkmcnt(0)\n"
      "v_fma_f32 v161, v161, v177, v177\n"
      "v_cmp_ne_u32 vcc, s21, v18\n"
      "v_mfma_f32_16x16x32_bf16 v[128:131], a[104:107], v[84:87], v[128:131]\n"
      "v_rcp_f32 v161, v161\n"
      "v_mfma_f32_16x16x32_bf16 v[128:131], a[108:111], v[88:91], v[128:131]\n"
      "ds_read_b32 v12, v14\n"
      "v_fmac_f32 v132, v176, v148\n"
      "v_mfma_f32_16x16x32_bf16 v[128:131], a[112:115], v[92:95], v[128:131]\n"
      "s_cbranch_vccnz .Lsp_g0_4\n"
      ".Lbk_g0_4:\n"
      "v_mul_f32 v160, v132, v132\n"
      "v_mfma_f32_16x16x32_bf16 v[128:131], a[116:119], v[96:99], v[128:131]\n"
      "buffer_load_dwordx4 v0, s[8:11], s40 offen offset:1024 sc1 lds\n"
      "v_mfma_f32_16x16x32_bf16 v[128:131], a[120:123], v[100:103], v[128:131]\n"
      "v_fma_f32 v160, v160, v179, v178\n"
      "v_mul_f32 v160, v132, v160\n"
      "v_mfma_f32_16x16x32_bf16 v[128:131], a[124:127], v[104:107], v[128:131]\n"
      "v_mul_f32 v163, v135, v163\n"
      "v_mul_f32 v161, v133, v161\n"
      "v_mfma_f32_16x16x32_bf16 v[128:131], a[128:131], v[108:111], v[128:131]\n"
      "v_fma_f32 v158, v158, v177, v177\n"
      "v_fmac_f32 v134, v176, v150\n"
      "v_mfma_f32_16x16x32_bf16 v[128:131], a[132:135], v[112:115], v[128:131]\n"
      "s_waitcnt lgkmcnt(0)\n"
      "v_mul_f32 v162, v134, v134\n"
      "v_cmp_gt_i32 vcc, v11, v12\n"
      "v_mfma_f32_16x16x32_bf16 v[128:131], a[136:139], v[116:119], v[128:131]\n"
      "buffer_load_dwordx4 v0, s[8:11], s40 offen offset:0 sc1 lds\n"
      "v_mfma_f32_16x16x32_bf16 v[128:131], a[140:143], v[120:123], v[128:131]\n"
      "v_rcp_f32 v158, v158\n"
      "v_mfma_f32_16x16x32_bf16 v[136:139], a[216:219], v[52:55], 0\n"
      "v_exp_f32 v160, v160\n"
      "v_mfma_f32_16x16x32_bf16 v[136:139], a[220:223], v[56:59], v[136:139]\n"
      "s_cbranch_vccnz .Lsp_gy0_5\n"
      ".Lbk_gy0_5:\n"
      "v_fma_f32 v160, v160, v177, v177\n"
      "v_mfma_f32_16x16x32_bf16 v[136:139], a[224:227], v[60:63], v[136:139]\n"
      "ds_read_b128 v[60:63], v0 offset:22528\n"
      "buffer_load_dwordx4 v0, s[8:11], s40 offen offset:2048 sc1 lds\n"
      "v_mfma_f32_16x16x32_bf16 v[136:139], a[228:231], v[64:67], v[136:139]\n"
      "ds_read_b128 v[52:55], v0 offset:20480\n"
      "v_mul_f32 v159, v127, v159\n"
      "v_mfma_f32_16x16x32_bf16 v[136:139], a[232:235], v[68:71], v[136:139]\n"
      "v_exp_f32 v163, v163\n"
      "v_mfma_f32_16x16x32_bf16 v[136:139], a[236:239], v[72:75], v[136:139]\n"
      "v_rcp_f32 v157, v157\n"
      "v_mfma_f32_16x16x32_bf16 v[136:139], a[240:243], v[76:79], v[136:139]\n"
      "buffer_load_dwordx4 v0, s[8:11], s40 offen offset:3072 sc1 lds\n"
      "ds_read_b128 v[76:79], v0 offset:26624\n"
      "v_mfma_f32_16x16x32_bf16 v[136:139], a[244:247], v[80:83], v[136:139]\n"
      "ds_read2st64_b64 v[168:171], v4 offset0:40 offset1:41\n"
      "v_fmac_f32 v131, v176, v147\n"
      "v_mfma_f32_16x16x32_bf16 v[136:139], a[248:251], v[84:87], v[136:139]\n"
      "s_add_u32 m0, s39, 40960\n"
      "ds_read_b128 v[72:75], v0 offset:25600\n"
      "v_mul_f32 v158, v126, v158\n"
      "v_mfma_f32_16x16x32_bf16 v[136:139], a[252:255], v[88:91], v[136:139]\n"
      "v_rcp_f32 v156, v156\n"
      "v_mul_f32 v157, v125, v157\n"
      "v_mfma_f32_16x16x32_bf16 v[136:139], v[20:23], v[92:95], v[136:139]\n"
      "ds_read_b128 v[68:71], v0 offset:24576\n"
      "buffer_load_dwordx4 v1, s[8:11], s40 offen offset:0 sc1 lds\n"
      "v_mfma_f32_16x16x32_bf16 v[136:139], v[24:27], v[96:99], v[136:139]\n"
      "s_min_u32 s36, s36, s6\n"
      "ds_read_b128 v[56:59], v0 offset:21504\n"
      "v_fmac_f32 v129, v176, v145\n"
      "v_mfma_f32_16x16x32_bf16 v[136:139], v[28:31], v[100:103], v[136:139]\n"
      "ds_read_b128 v[64:67], v0 offset:23552\n"
      "v_rcp_f32 v160, v160\n"
      "v_mfma_f32_16x16x32_bf16 v[136:139], v[32:35], v[104:107], v[136:139]\n"
      "ds_read_b128 v[88:91], v0 offset:29696\n"
      "v_mul_f32 v160, v132, v160\n"
      "v_mfma_f32_16x16x32_bf16 v[136:139], v[36:39], v[108:111], v[136:139]\n"
      "s_waitcnt vmcnt(5) lgkmcnt(5)\n"
      "buffer_store_dword v15, v3, s[8:11], s42 offen sc1\n"
      "ds_read_b128 v[108:111], v0 offset:34816\n"
      "v_mfma_f32_16x16x32_bf16 v[136:139], v[40:43], v[112:115], v[136:139]\n"
      "ds_read_b128 v[80:83], v0 offset:27648\n"
      "v_mul_f32 v156, v124, v156\n"
      "v_mfma_f32_16x16x32_bf16 v[136:139], v[44:47], v[116:119], v[136:139]\n"
      "ds_read_b128 v[104:107], v0 offset:33792\n"
      "v_add_f32 v169, v169, v171\n"
      "v_mfma_f32_16x16x32_bf16 v[136:139], v[48:51], v[120:123], v[136:139]\n"
      "buffer_load_dword v18, v2, s[8:11], s36 offen sc1\n"
      "v_mfma_f32_16x16x32_bf16 v[124:127], a[0:3], v[52:55], 0\n"
      "s_waitcnt lgkmcnt(5)\n"
      "ds_read_b128 v[84:87], v0 offset:28672\n"
      "v_fmac_f32 v130, v176, v146\n"
      "v_mfma_f32_16x16x32_bf16 v[124:127], a[4:7], v[56:59], v[124:127]\n"
      "ds_read_b128 v[92:95], v0 offset:30720\n"
      "v_fma_f32 v163, v163, v177, v177\n"
      "v_mfma_f32_16x16x32_bf16 v[124:127], a[8:11], v[60:63], v[124:127]\n"
      "s_waitcnt lgkmcnt(5)\n"
      "ds_read_b128 v[96:99], v0 offset:31744\n"
      "v_fma_f32 v162, v162, v179, v178\n"
      "v_mfma_f32_16x16x32_bf16 v[124:127], a[12:15], v[64:67], v[124:127]\n"
      "ds_read_b128 v[100:103], v0 offset:32768\n"
      "v_mul_f32 v130, v130, v158\n"
      "v_mfma_f32_16x16x32_bf16 v[124:127], a[16:19], v[68:71], v[124:127]\n"
      "s_mov_b32 s40, s36\n"
      "v_mul_f32 v162, v134, v162\n"
      "v_fmac_f32 v137, v176, v153\n"
      "v_mfma_f32_16x16x32_bf16 v[124:127], a[20:23], v[72:75], v[124:127]\n"
      "v_rcp_f32 v163, v163\n"
      "v_mfma_f32_16x16x32_bf16 v[124:127], a[24:27], v[76:79], v[124:127]\n"
      "s_waitcnt lgkmcnt(4)\n"
      "ds_read_b128 v[112:115], v0 offset:35840\n"
      "v_fmac_f32 v138, v176, v154\n"
      "v_mfma_f32_16x16x32_bf16 v[124:127], a[28:31], v[80:83], v[124:127]\n"
      "s_waitcnt lgkmcnt(4)\n"
      "v_exp_f32 v162, v162\n"
      "v_mfma_f32_16x16x32_bf16 v[124:127], a[32:35], v[84:87], v[124:127]\n"
      "ds_read_b128 v[116:119], v0 offset:36864\n"
      "v_mul_f32 v129, v129, v157\n"
      "v_mfma_f32_16x16x32_bf16 v[124:127], a[36:39], v[88:91], v[124:127]\n"
      "s_waitcnt lgkmcnt(3)\n"
      "ds_read_b128 v[120:123], v0 offset:37888\n"
      "v_fma_f32 v162, v162, v177, v177\n"
      "v_rcp_f32 v162, v162\n"
      "v_mfma_f32_16x16x32_bf16 v[124:127], a[40:43], v[92:95], v[124:127]\n"
      "v_mul_f32 v137, v137, v161\n"
      "v_fmac_f32 v136, v176, v152\n"
      "v_mfma_f32_16x16x32_bf16 v[124:127], a[44:47], v[96:99], v[124:127]\n"
      "s_waitcnt lgkmcnt(3)\n"
      "v_mul_f32 v136, v136, v160\n"
      "v_mul_f32 v131, v131, v159\n"
      "v_mfma_f32_16x16x32_bf16 v[124:127], a[48:51], v[100:103], v[124:127]\n"
      "s_add_u32 s42, s42, 165888\n"
      "v_mul_f32 v162, v134, v162\n"
      "v_cvt_pk_bf16_f32 v166, v136, v137\n"
      "v_mfma_f32_16x16x32_bf16 v[124:127], a[52:55], v[104:107], v[124:127]\n"
      "v_fmac_f32 v128, v176, v144\n"
      "v_mul_f32 v138, v138, v162\n"
      "v_mfma_f32_16x16x32_bf16 v[124:127], a[56:59], v[108:111], v[124:127]\n"
      "s_waitcnt lgkmcnt(2)\n"
      "v_cvt_pk_bf16_f32 v165, v130, v131\n"
      "v_add_u32 v11, 1, v11\n"
      "v_mfma_f32_16x16x32_bf16 v[124:127], a[60:63], v[112:115], v[124:127]\n"
      "s_waitcnt lgkmcnt(1)\n"
      "v_mul_f32 v163, v135, v163\n"
      "v_fmac_f32 v139, v176, v155\n"
      "v_mfma_f32_16x16x32_bf16 v[124:127], a[64:67], v[116:119], v[124:127]\n"
      "s_waitcnt lgkmcnt(0)\n"
      "v_mul_f32 v128, v128, v156\n"
      "v_mul_f32 v139, v139, v163\n"
      "v_mfma_f32_16x16x32_bf16 v[124:127], a[68:71], v[120:123], v[124:127]\n"
      "s_add_u32 s37, s37, 165888\n"
      "v_cvt_pk_bf16_f32 v164, v128, v129\n"
      "v_cvt_pk_bf16_f32 v167, v138, v139\n"
      "v_mfma_f32_16x16x32_bf16 v[132:135], a[144:147], v[52:55], 0\n"
      "buffer_store_dwordx4 v[164:167], v0, s[8:11], s37 offen sc1\n"
      "v_mfma_f32_16x16x32_bf16 v[132:135], a[148:151], v[56:59], v[132:135]\n"
      "v_sub_f32 v176, -v168, v170\n"
      "v_fma_f32 v169, -v176, v176, v169\n"
      "v_mfma_f32_16x16x32_bf16 v[132:135], a[152:155], v[60:63], v[132:135]\n"
      "v_fmac_f32 v126, v176, v142\n"
      "v_fmac_f32 v124, v176, v140\n"
      "v_mfma_f32_16x16x32_bf16 v[132:135], a[156:159], v[64:67], v[132:135]\n"
      "v_mul_f32 v156, v124, v124\n"
      "v_max_f32 v177, 0x3727c5ac, v169\n"
      "v_mfma_f32_16x16x32_bf16 v[132:135], a[160:163], v[68:71], v[132:135]\n"
      "v_rsq_f32 v170, v177\n"
      "v_mfma_f32_16x16x32_bf16 v[132:135], a[164:167], v[72:75], v[132:135]\n"
      "v_fmac_f32 v125, v176, v141\n"
      "v_mul_f32 v157, v125, v125\n"
      "v_mfma_f32_16x16x32_bf16 v[132:135], a[168:171], v[76:79], v[132:135]\n"
      "v_mul_f32 v178, 0xc0135761, v170\n"
      "v_fmac_f32 v127, v176, v143\n"
      "v_mfma_f32_16x16x32_bf16 v[132:135], a[172:175], v[80:83], v[132:135]\n"
      "v_mul_f32 v171, 0x3d372713, v178\n"
      "v_mul_f32 v170, v170, v170\n"
      "v_mfma_f32_16x16x32_bf16 v[132:135], a[176:179], v[84:87], v[132:135]\n"
      "v_mul_f32 v158, v126, v126\n"
      "v_mul_f32 v159, v127, v127\n"
      "v_mfma_f32_16x16x32_bf16 v[132:135], a[180:183], v[88:91], v[132:135]\n"
      "v_mul_f32 v179, v171, v170\n"
      "v_fma_f32 v157, v157, v179, v178\n"
      "v_mfma_f32_16x16x32_bf16 v[132:135], a[184:187], v[92:95], v[132:135]\n"
      "v_fma_f32 v159, v159, v179, v178\n"
      "v_mul_f32 v157, v125, v157\n"
      "v_mfma_f32_16x16x32_bf16 v[132:135], a[188:191], v[96:99], v[132:135]\n"
      "s_add_u32 s36, s36, 24576\n"
      "v_exp_f32 v157, v157\n"
      "v_mfma_f32_16x16x32_bf16 v[132:135], a[192:195], v[100:103], v[132:135]\n"
      "v_fma_f32 v156, v156, v179, v178\n"
      "v_mul_f32 v156, v124, v156\n"
      "v_mfma_f32_16x16x32_bf16 v[132:135], a[196:199], v[104:107], v[132:135]\n"
      "v_fma_f32 v158, v158, v179, v178\n"
      "v_mul_f32 v159, v127, v159\n"
      "v_mfma_f32_16x16x32_bf16 v[132:135], a[200:203], v[108:111], v[132:135]\n"
      "v_exp_f32 v156, v156\n"
      "v_mfma_f32_16x16x32_bf16 v[132:135], a[204:207], v[112:115], v[132:135]\n"
      "s_add_u32 m0, s38, 61440\n"
      "v_exp_f32 v159, v159\n"
      "v_mfma_f32_16x16x32_bf16 v[132:135], a[208:211], v[116:119], v[132:135]\n"
      "v_mul_f32 v158, v126, v158\n"
      "v_fma_f32 v159, v159, v177, v177\n"
      "v_mfma_f32_16x16x32_bf16 v[132:135], a[212:215], v[120:123], v[132:135]\n"
      "v_rcp_f32 v159, v159\n"
      "v_mfma_f32_16x16x32_bf16 v[128:131], a[72:75], v[52:55], 0\n"
      "s_waitcnt vmcnt(3)\n"
      "ds_write_b32 v13, v11\n"
      "v_mfma_f32_16x16x32_bf16 v[128:131], a[76:79], v[56:59], v[128:131]\n"
      "v_exp_f32 v158, v158\n"
      "v_mfma_f32_16x16x32_bf16 v[128:131], a[80:83], v[60:63], v[128:131]\n"
      "v_fma_f32 v156, v156, v177, v177\n"
      "v_fmac_f32 v135, v176, v151\n"
      "v_mfma_f32_16x16x32_bf16 v[128:131], a[84:87], v[64:67], v[128:131]\n"
      "v_fmac_f32 v133, v176, v149\n"
      "v_mul_f32 v163, v135, v135\n"
      "v_mfma_f32_16x16x32_bf16 v[128:131], a[88:91], v[68:71], v[128:131]\n"
      "v_mul_f32 v161, v133, v133\n"
      "v_fma_f32 v163, v163, v179, v178\n"
      "v_fma_f32 v161, v161, v179, v178\n"
      "v_mfma_f32_16x16x32_bf16 v[128:131], a[92:95], v[72:75], v[128:131]\n"
      "v_fma_f32 v157, v157, v177, v177\n"
      "v_mul_f32 v161, v133, v161\n"
      "v_mfma_f32_16x16x32_bf16 v[128:131], a[96:99], v[76:79], v[128:131]\n"
      "v_exp_f32 v161, v161\n"
      "v_mfma_f32_16x16x32_bf16 v[128:131], a[100:103], v[80:83], v[128:131]\n"
      "s_waitcnt vmcnt(1) lgkmcnt(0)\n"
      "v_fma_f32 v161, v161, v177, v177\n"
      "v_cmp_ne_u32 vcc, s21, v18\n"
      "v_mfma_f32_16x16x32_bf16 v[128:131], a[104:107], v[84:87], v[128:131]\n"
      "v_rcp_f32 v161, v161\n"
      "v_mfma_f32_16x16x32_bf16 v[128:131], a[108:111], v[88:91], v[128:131]\n"
      "ds_read_b32 v12, v14\n"
      "v_fmac_f32 v132, v176, v148\n"
      "v_mfma_f32_16x16x32_bf16 v[128:131], a[112:115], v[92:95], v[128:131]\n"
      "s_cbranch_vccnz .Lsp_g0_6\n"
      ".Lbk_g0_6:\n"
      "v_mul_f32 v160, v132, v132\n"
      "v_mfma_f32_16x16x32_bf16 v[128:131], a[116:119], v[96:99], v[128:131]\n"
      "buffer_load_dwordx4 v0, s[8:11], s40 offen offset:1024 sc1 lds\n"
      "v_mfma_f32_16x16x32_bf16 v[128:131], a[120:123], v[100:103], v[128:131]\n"
      "v_fma_f32 v160, v160, v179, v178\n"
      "v_mul_f32 v160, v132, v160\n"
      "v_mfma_f32_16x16x32_bf16 v[128:131], a[124:127], v[104:107], v[128:131]\n"
      "v_mul_f32 v163, v135, v163\n"
      "v_mul_f32 v161, v133, v161\n"
      "v_mfma_f32_16x16x32_bf16 v[128:131], a[128:131], v[108:111], v[128:131]\n"
      "v_fma_f32 v158, v158, v177, v177\n"
      "v_fmac_f32 v134, v176, v150\n"
      "v_mfma_f32_16x16x32_bf16 v[128:131], a[132:135], v[112:115], v[128:131]\n"
      "s_waitcnt lgkmcnt(0)\n"
      "v_mul_f32 v162, v134, v134\n"
      "v_cmp_gt_i32 vcc, v11, v12\n"
      "v_mfma_f32_16x16x32_bf16 v[128:131], a[136:139], v[116:119], v[128:131]\n"
      "buffer_load_dwordx4 v0, s[8:11], s40 offen offset:0 sc1 lds\n"
      "v_mfma_f32_16x16x32_bf16 v[128:131], a[140:143], v[120:123], v[128:131]\n"
      "v_rcp_f32 v158, v158\n"
      "v_mfma_f32_16x16x32_bf16 v[136:139], a[216:219], v[52:55], 0\n"
      "v_exp_f32_e64 v160, v160\n"
      "v_mfma_f32_16x16x32_bf16 v[136:139], a[220:223], v[56:59], v[136:139]\n"
      "s_cbranch_vccnz .Lsp_gy0_7\n"
      ".Lbk_gy0_7:\n"
      "v_fma_f32 v160, v160, v177, v177\n"
      "v_mfma_f32_16x16x32_bf16 v[136:139], a[224:227], v[60:63], v[136:139]\n"
      "ds_read_b128 v[60:63], v17 offset:2048\n"
      "buffer_load_dwordx4 v0, s[8:11], s40 offen offset:2048 sc1 lds\n"
      "v_mfma_f32_16x16x32_bf16 v[136:139], a[228:231], v[64:67], v[136:139]\n"
      "ds_read_b128 v[52:55], v17 offset:0\n"
      "v_mul_f32 v159, v127, v159\n"
      "v_mfma_f32_16x16x32_bf16 v[136:139], a[232:235], v[68:71], v[136:139]\n"
      "v_exp_f32 v163, v163\n"
      "v_mfma_f32_16x16x32_bf16 v[136:139], a[236:239], v[72:75], v[136:139]\n"
      "v_rcp_f32 v157, v157\n"
      "v_mfma_f32_16x16x32_bf16 v[136:139], a[240:243], v[76:79], v[136:139]\n"
      "buffer_load_dwordx4 v0, s[8:11], s40 offen offset:3072 sc1 lds\n"
      "ds_read_b128 v[76:79], v17 offset:6144\n"
      "v_mfma_f32_16x16x32_bf16 v[136:139], a[244:247], v[80:83], v[136:139]\n"
      "ds_read2st64_b64 v[168:171], v4 offset0:80 offset1:81\n"
      "v_fmac_f32 v131, v176, v147\n"
      "v_mfma_f32_16x16x32_bf16 v[136:139], a[248:251], v[84:87], v[136:139]\n"
      "s_add_u32 m0, s39, 61440\n"
      "ds_read_b128 v[72:75], v17 offset:5120\n"
      "v_mul_f32 v158, v126, v158\n"
      "v_mfma_f32_16x16x32_bf16 v[136:139], a[252:255], v[88:91], v[136:139]\n"
      "v_rcp_f32 v156, v156\n"
      "v_mul_f32 v157, v125, v157\n"
      "v_mfma_f32_16x16x32_bf16 v[136:139], v[20:23], v[92:95], v[136:139]\n"
      "ds_read_b128 v[68:71], v17 offset:4096\n"
      "buffer_load_dwordx4 v1, s[8:11], s40 offen offset:0 sc1 lds\n"
      "v_mfma_f32_16x16x32_bf16 v[136:139], v[24:27], v[96:99], v[136:139]\n"
      "s_min_u32 s36, s36, s6\n"
      "ds_read_b128 v[56:59], v17 offset:1024\n"
      "v_fmac_f32 v129, v176, v145\n"
      "v_mfma_f32_16x16x32_bf16 v[136:139], v[28:31], v[100:103], v[136:139]\n"
      "ds_read_b128 v[64:67], v17 offset:3072\n"
      "v_rcp_f32 v160, v160\n"
      "v_mfma_f32_16x16x32_bf16 v[136:139], v[32:35], v[104:107], v[136:139]\n"
      "ds_read_b128 v[88:91], v17 offset:9216\n"
      "v_mul_f32 v160, v132, v160\n"
      "v_mfma_f32_16x16x32_bf16 v[136:139], v[36:39], v[108:111], v[136:139]\n"
      "s_waitcnt vmcnt(5) lgkmcnt(5)\n"
      "buffer_store_dword v15, v3, s[8:11], s42 offen sc1\n"
      "ds_read_b128 v[108:111], v17 offset:14336\n"
      "v_mfma_f32_16x16x32_bf16 v[136:139], v[40:43], v[112:115], v[136:139]\n"
      "ds_read_b128 v[80:83], v17 offset:7168\n"
      "v_mul_f32 v156, v124, v156\n"
      "v_mfma_f32_16x16x32_bf16 v[136:139], v[44:47], v[116:119], v[136:139]\n"
      "ds_read_b128 v[104:107], v17 offset:13312\n"
      "v_add_f32 v169, v169, v171\n"
      "v_mfma_f32_16x16x32_bf16 v[136:139], v[48:51], v[120:123], v[136:139]\n"
      "buffer_load_dword v18, v2, s[8:11], s36 offen sc1\n"
      "v_mfma_f32_16x16x32_bf16 v[124:127], a[0:3], v[52:55], 0\n"
      "s_waitcnt lgkmcnt(5)\n"
      "ds_read_b128 v[84:87], v17 offset:8192\n"
      "v_fmac_f32 v130, v176, v146\n"
      "v_mfma_f32_16x16x32_bf16 v[124:127], a[4:7], v[56:59], v[124:127]\n"
      "ds_read_b128 v[92:95], v17 offset:10240\n"
      "v_fma_f32 v163, v163, v177, v177\n"
      "v_mfma_f32_16x16x32_bf16 v[124:127], a[8:11], v[60:63], v[124:127]\n"
      "s_waitcnt lgkmcnt(5)\n"
      "ds_read_b128 v[96:99], v17 offset:11264\n"
      "v_fma_f32 v162, v162, v179, v178\n"
      "v_mfma_f32_16x16x32_bf16 v[124:127], a[12:15], v[64:67], v[124:127]\n"
      "ds_read_b128 v[100:103], v17 offset:12288\n"
      "v_mul_f32 v130, v130, v158\n"
      "v_mfma_f32_16x16x32_bf16 v[124:127], a[16:19], v[68:71], v[124:127]\n"
      "s_mov_b32 s40, s36\n"
      "v_mul_f32 v162, v134, v162\n"
      "v_fmac_f32 v137, v176, v153\n"
      "v_mfma_f32_16x16x32_bf16 v[124:127], a[20:23], v[72:75], v[124:127]\n"
      "v_rcp_f32 v163, v163\n"
      "v_mfma_f32_16x16x32_bf16 v[124:127], a[24:27], v[76:79], v[124:127]\n"
      "s_waitcnt lgkmcnt(4)\n"
      "ds_read_b128 v[112:115], v17 offset:15360\n"
      "v_fmac_f32 v138, v176, v154\n"
      "v_mfma_f32_16x16x32_bf16 v[124:127], a[28:31], v[80:83], v[124:127]\n"
      "s_waitcnt lgkmcnt(4)\n"
      "v_exp_f32 v162, v162\n"
      "v_mfma_f32_16x16x32_bf16 v[124:127], a[32:35], v[84:87], v[124:127]\n"
      "ds_read_b128 v[116:119], v17 offset:16384\n"
      "v_mul_f32 v129, v129, v157\n"
      "v_mfma_f32_16x16x32_bf16 v[124:127], a[36:39], v[88:91], v[124:127]\n"
      "s_waitcnt lgkmcnt(3)\n"
      "ds_read_b128 v[120:123], v17 offset:17408\n"
      "v_fma_f32 v162, v162, v177, v177\n"
      "v_rcp_f32 v162, v162\n"
      "v_mfma_f32_16x16x32_bf16 v[124:127], a[40:43], v[92:95], v[124:127]\n"
      "v_mul_f32 v137, v137, v161\n"
      "v_fmac_f32 v136, v176, v152\n"
      "v_mfma_f32_16x16x32_bf16 v[124:127], a[44:47], v[96:99], v[124:127]\n"
      "s_waitcnt lgkmcnt(3)\n"
      "v_mul_f32 v136, v136, v160\n"
      "v_mul_f32 v131, v131, v159\n"
      "v_mfma_f32_16x16x32_bf16 v[124:127], a[48:51], v[100:103], v[124:127]\n"
      "s_add_u32 s42, s42, 165888\n"
      "v_mul_f32 v162, v134, v162\n"
      "v_cvt_pk_bf16_f32 v166, v136, v137\n"
      "v_mfma_f32_16x16x32_bf16 v[124:127], a[52:55], v[104:107], v[124:127]\n"
      "v_fmac_f32 v128, v176, v144\n"
      "v_mul_f32 v138, v138, v162\n"
      "v_mfma_f32_16x16x32_bf16 v[124:127], a[56:59], v[108:111], v[124:127]\n"
      "s_waitcnt lgkmcnt(2)\n"
      "v_cvt_pk_bf16_f32 v165, v130, v131\n"
      "v_add_u32 v11, 1, v11\n"
      "v_mfma_f32_16x16x32_bf16 v[124:127], a[60:63], v[112:115], v[124:127]\n"
      "s_waitcnt lgkmcnt(1)\n"
      "v_mul_f32 v163, v135, v163\n"
      "v_fmac_f32 v139, v176, v155\n"
      "v_mfma_f32_16x16x32_bf16 v[124:127], a[64:67], v[116:119], v[124:127]\n"
      "s_waitcnt lgkmcnt(0)\n"
      "v_mul_f32 v128, v128, v156\n"
      "v_mul_f32 v139, v139, v163\n"
      "v_mfma_f32_16x16x32_bf16 v[124:127], a[68:71], v[120:123], v[124:127]\n"
      "s_add_u32 s37, s37, 165888\n"
      "v_cvt_pk_bf16_f32 v164, v128, v129\n"
      "v_cvt_pk_bf16_f32 v167, v138, v139\n"
      "v_mfma_f32_16x16x32_bf16 v[132:135], a[144:147], v[52:55], 0\n"
      "buffer_store_dwordx4 v[164:167], v0, s[8:11], s37 offen sc1\n"
      "v_mfma_f32_16x16x32_bf16 v[132:135], a[148:151], v[56:59], v[132:135]\n"
      "v_sub_f32 v176, -v168, v170\n"
      "v_fma_f32 v169, -v176, v176, v169\n"
      "v_mfma_f32_16x16x32_bf16 v[132:135], a[152:155], v[60:63], v[132:135]\n"
      "v_fmac_f32 v126, v176, v142\n"
      "v_fmac_f32 v124, v176, v140\n"
      "v_mfma_f32_16x16x32_bf16 v[132:135], a[156:159], v[64:67], v[132:135]\n"
      "v_mul_f32 v156, v124, v124\n"
      "v_max_f32 v177, 0x3727c5ac, v169\n"
      "v_mfma_f32_16x16x32_bf16 v[132:135], a[160:163], v[68:71], v[132:135]\n"
      "v_rsq_f32 v170, v177\n"
      "v_mfma_f32_16x16x32_bf16 v[132:135], a[164:167], v[72:75], v[132:135]\n"
      "v_fmac_f32 v125, v176, v141\n"
      "v_mul_f32 v157, v125, v125\n"
      "v_mfma_f32_16x16x32_bf16 v[132:135], a[168:171], v[76:79], v[132:135]\n"
      "v_mul_f32 v178, 0xc0135761, v170\n"
      "v_fmac_f32 v127, v176, v143\n"
      "v_mfma_f32_16x16x32_bf16 v[132:135], a[172:175], v[80:83], v[132:135]\n"
      "v_mul_f32 v171, 0x3d372713, v178\n"
      "v_mul_f32 v170, v170, v170\n"
      "v_mfma_f32_16x16x32_bf16 v[132:135], a[176:179], v[84:87], v[132:135]\n"
      "v_mul_f32 v158, v126, v126\n"
      "v_mul_f32 v159, v127, v127\n"
      "v_mfma_f32_16x16x32_bf16 v[132:135], a[180:183], v[88:91], v[132:135]\n"
      "v_mul_f32 v179, v171, v170\n"
      "v_fma_f32 v157, v157, v179, v178\n"
      "v_mfma_f32_16x16x32_bf16 v[132:135], a[184:187], v[92:95], v[132:135]\n"
      "v_fma_f32 v159, v159, v179, v178\n"
      "v_mul_f32 v157, v125, v157\n"
      "v_mfma_f32_16x16x32_bf16 v[132:135], a[188:191], v[96:99], v[132:135]\n"
      "s_add_u32 s36, s36, 24576\n"
      "v_exp_f32 v157, v157\n"
      "v_mfma_f32_16x16x32_bf16 v[132:135], a[192:195], v[100:103], v[132:135]\n"
      "v_fma_f32 v156, v156, v179, v178\n"
      "v_mul_f32 v156, v124, v156\n"
      "v_mfma_f32_16x16x32_bf16 v[132:135], a[196:199], v[104:107], v[132:135]\n"
      "v_fma_f32 v158, v158, v179, v178\n"
      "v_mul_f32 v159, v127, v159\n"
      "v_mfma_f32_16x16x32_bf16 v[132:135], a[200:203], v[108:111], v[132:135]\n"
      "v_exp_f32 v156, v156\n"
      "v_mfma_f32_16x16x32_bf16 v[132:135], a[204:207], v[112:115], v[132:135]\n"
      "s_add_u32 m0, s38, 0\n"
      "v_exp_f32 v159, v159\n"
      "v_mfma_f32_16x16x32_bf16 v[132:135], a[208:211], v[116:119], v[132:135]\n"
      "v_mul_f32 v158, v126, v158\n"
      "v_fma_f32 v159, v159, v177, v177\n"
      "v_mfma_f32_16x16x32_bf16 v[132:135], a[212:215], v[120:123], v[132:135]\n"
      "v_rcp_f32 v159, v159\n"
      "v_mfma_f32_16x16x32_bf16 v[128:131], a[72:75], v[52:55], 0\n"
      "s_waitcnt vmcnt(3)\n"
      "ds_write_b32 v13, v11\n"
      "v_mfma_f32_16x16x32_bf16 v[128:131], a[76:79], v[56:59], v[128:131]\n"
      "v_exp_f32 v158, v158\n"
      "v_mfma_f32_16x16x32_bf16 v[128:131], a[80:83], v[60:63], v[128:131]\n"
      "v_fma_f32 v156, v156, v177, v177\n"
      "v_fmac_f32 v135, v176, v151\n"
      "v_mfma_f32_16x16x32_bf16 v[128:131], a[84:87], v[64:67], v[128:131]\n"
      "v_fmac_f32 v133, v176, v149\n"
      "v_mul_f32 v163, v135, v135\n"
      "v_mfma_f32_16x16x32_bf16 v[128:131], a[88:91], v[68:71], v[128:131]\n"
      "v_mul_f32 v161, v133, v133\n"
      "v_fma_f32 v163, v163, v179, v178\n"
      "v_fma_f32 v161, v161, v179, v178\n"
      "v_mfma_f32_16x16x32_bf16 v[128:131], a[92:95], v[72:75], v[128:131]\n"
      "v_fma_f32 v157, v157, v177, v177\n"
      "v_mul_f32 v161, v133, v161\n"
      "v_mfma_f32_16x16x32_bf16 v[128:131], a[96:99], v[76:79], v[128:131]\n"
      "v_exp_f32 v161, v161\n"
      "v_mfma_f32_16x16x32_bf16 v[128:131], a[100:103], v[80:83], v[128:131]\n"
      "s_waitcnt vmcnt(1) lgkmcnt(0)\n"
      "v_fma_f32 v161, v161, v177, v177\n"
      "v_cmp_ne_u32 vcc, s21, v18\n"
      "v_mfma_f32_16x16x32_bf16 v[128:131], a[104:107], v[84:87], v[128:131]\n"
      "v_rcp_f32 v161, v161\n"
      "v_mfma_f32_16x16x32_bf16 v[128:131], a[108:111], v[88:91], v[128:131]\n"
      "ds_read_b32 v12, v14\n"
      "v_fmac_f32 v132, v176, v148\n"
      "v_mfma_f32_16x16x32_bf16 v[128:131], a[112:115], v[92:95], v[128:131]\n"
      "s_cbranch_vccnz .Lsp_g0_8\n"
      ".Lbk_g0_8:\n"
      "v_mul_f32 v160, v132, v132\n"
      "v_mfma_f32_16x16x32_bf16 v[128:131], a[116:119], v[96:99], v[128:131]\n"
      "buffer_load_dwordx4 v0, s[8:11], s40 offen offset:1024 sc1 lds\n"
      "v_mfma_f32_16x16x32_bf16 v[128:131], a[120:123], v[100:103], v[128:131]\n"
      "v_fma_f32 v160, v160, v179, v178\n"
      "v_mul_f32 v160, v132, v160\n"
      "v_mfma_f32_16x16x32_bf16 v[128:131], a[124:127], v[104:107], v[128:131]\n"
      "v_mul_f32 v163, v135, v163\n"
      "v_mul_f32 v161, v133, v161\n"
      "v_mfma_f32_16x16x32_bf16 v[128:131], a[128:131], v[108:111], v[128:131]\n"
      "v_fma_f32 v158, v158, v177, v177\n"
      "v_fmac_f32 v134, v176, v150\n"
      "v_mfma_f32_16x16x32_bf16 v[128:131], a[132:135], v[112:115], v[128:131]\n"
      "s_waitcnt lgkmcnt(0)\n"
      "v_mul_f32_e64 v162, v134, v134\n"
      "v_cmp_gt_i32 vcc, v11, v12\n"
      "v_mfma_f32_16x16x32_bf16 v[128:131], a[136:139], v[116:119], v[128:131]\n"
      "buffer_load_dwordx4 v0, s[8:11], s40 offen offset:0 sc1 lds\n"
      "v_mfma_f32_16x16x32_bf16 v[128:131], a[140:143], v[120:123], v[128:131]\n"
      "v_rcp_f32_e64 v158, v158\n"
      "v_mfma_f32_16x16x32_bf16 v[136:139], a[216:219], v[52:55], 0\n"
      "v_exp_f32_e64 v160, v160\n"
      "v_mfma_f32_16x16x32_bf16 v[136:139], a[220:223], v[56:59], v[136:139]\n"
      "s_cbranch_vccnz .Lsp_gy0_9\n"
      ".Lbk_gy0_9:\n"
      "v_fma_f32 v160, v160, v177, v177\n"
      "v_mfma_f32_16x16x32_bf16 v[136:139], a[224:227], v[60:63], v[136:139]\n"
      "ds_read_b128 v[60:63], v17 offset:22528\n"
      "buffer_load_dwordx4 v0, s[8:11], s40 offen offset:2048 sc1 lds\n"
      "v_mfma_f32_16x16x32_bf16 v[136:139], a[228:231], v[64:67], v[136:139]\n"
      "ds_read_b128 v[52:55], v17 offset:20480\n"
      "v_mul_f32 v159, v127, v159\n"
      "v_mfma_f32_16x16x32_bf16 v[136:139], a[232:235], v[68:71], v[136:139]\n"
      "v_exp_f32 v163, v163\n"
      "v_mfma_f32_16x16x32_bf16 v[136:139], a[236:239], v[72:75], v[136:139]\n"
      "v_rcp_f32 v157, v157\n"
      "v_mfma_f32_16x16x32_bf16 v[136:139], a[240:243], v[76:79], v[136:139]\n"
      "buffer_load_dwordx4 v0, s[8:11], s40 offen offset:3072 sc1 lds\n"
      "ds_read_b128 v[76:79], v17 offset:26624\n"
      "v_mfma_f32_16x16x32_bf16 v[136:139], a[244:247], v[80:83], v[136:139]\n"
      "ds_read2st64_b64 v[168:171], v4 offset0:120 offset1:121\n"
      "v_fmac_f32 v131, v176, v147\n"
      "v_mfma_f32_16x16x32_bf16 v[136:139], a[248:251], v[84:87], v[136:139]\n"
      "s_add_u32 m0, s39, 0\n"
      "ds_read_b128 v[72:75], v17 offset:25600\n"
      "v_mul_f32 v158, v126, v158\n"
      "v_mfma_f32_16x16x32_bf16 v[136:139], a[252:255], v[88:91], v[136:139]\n"
      "v_rcp_f32 v156, v156\n"
      "v_mul_f32 v157, v125, v157\n"
      "v_mfma_f32_16x16x32_bf16 v[136:139], v[20:23], v[92:95], v[136:139]\n"
      "ds_read_b128 v[68:71], v17 offset:24576\n"
      "buffer_load_dwordx4 v1, s[8:11], s40 offen offset:0 sc1 lds\n"
      "v_mfma_f32_16x16x32_bf16 v[136:139], v[24:27], v[96:99], v[136:139]\n"
      "s_min_u32 s36, s36, s6\n"
      "ds_read_b128 v[56:59], v17 offset:21504\n"
      "v_fmac_f32 v129, v176, v145\n"
      "v_mfma_f32_16x16x32_bf16 v[136:139], v[28:31], v[100:103], v[136:139]\n"
      ".Lg_p1:\n"
      "s_cbranch_execz .Lg_pr\n"
      "ds_read_b128 v[64:67], v17 offset:23552\n"
      "v_rcp_f32 v160, v160\n"
      "v_mfma_f32_16x16x32_bf16 v[136:139], v[32:35], v[104:107], v[136:139]\n"
      "ds_read_b128 v[88:91], v17 offset:29696\n"
      "v_mul_f32 v160, v132, v160\n"
      "v_mfma_f32_16x16x32_bf16 v[136:139], v[36:39], v[108:111], v[136:139]\n"
      "s_waitcnt vmcnt(5) lgkmcnt(5)\n"
      "buffer_store_dword v15, v3, s[8:11], s42 offen sc1\n"
      "ds_read_b128 v[108:111], v17 offset:34816\n"
      "v_mfma_f32_16x16x32_bf16 v[136:139], v[40:43], v[112:115], v[136:139]\n"
      "ds_read_b128 v[80:83], v17 offset:27648\n"
      "v_mul_f32 v156, v124, v156\n"
      "v_mfma_f32_16x16x32_bf16 v[136:139], v[44:47], v[116:119], v[136:139]\n"
      "ds_read_b128 v[104:107], v17 offset:33792\n"
      "v_add_f32 v169, v169, v171\n"
      "v_mfma_f32_16x16x32_bf16 v[136:139], v[48:51], v[120:123], v[136:139]\n"
      "buffer_load_dword v18, v2, s[8:11], s36 offen sc1\n"
      "v_mfma_f32_16x16x32_bf16 v[124:127], a[0:3], v[52:55], 0\n"
      "s_waitcnt lgkmcnt(5)\n"
      "ds_read_b128 v[84:87], v17 offset:28672\n"
      "v_fmac_f32 v130, v176, v146\n"
      "v_mfma_f32_16x16x32_bf16 v[124:127], a[4:7], v[56:59], v[124:127]\n"
      "ds_read_b128 v[92:95], v17 offset:30720\n"
      "v_fma_f32 v163, v163, v177, v177\n"
      "v_mfma_f32_16x16x32_bf16 v[124:127], a[8:11], v[60:63], v[124:127]\n"
      "s_waitcnt lgkmcnt(5)\n"
      "ds_read_b128 v[96:99], v17 offset:31744\n"
      "v_fma_f32 v162, v162, v179, v178\n"
      "v_mfma_f32_16x16x32_bf16 v[124:127], a[12:15], v[64:67], v[124:127]\n"
      "ds_read_b128 v[100:103], v17 offset:32768\n"
      "v_mul_f32 v130, v130, v158\n"
      "v_mfma_f32_16x16x32_bf16 v[124:127], a[16:19], v[68:71], v[124:127]\n"
      "s_mov_b32 s40, s36\n"
      "v_mul_f32 v162, v134, v162\n"
      "v_fmac_f32 v137, v176, v153\n"
      "v_mfma_f32_16x16x32_bf16 v[124:127], a[20:23], v[72:75], v[124:127]\n"
      "v_rcp_f32 v163, v163\n"
      "v_mfma_f32_16x16x32_bf16 v[124:127], a[24:27], v[76:79], v[124:127]\n"
      "s_waitcnt lgkmcnt(4)\n"
      "ds_read_b128 v[112:115], v17 offset:35840\n"
      "v_fmac_f32 v138, v176, v154\n"
      "v_mfma_f32_16x16x32_bf16 v[124:127], a[28:31], v[80:83], v[124:127]\n"
      "s_waitcnt lgkmcnt(4)\n"
      "v_exp_f32 v162, v162\n"
      "v_mfma_f32_16x16x32_bf16 v[124:127], a[32:35], v[84:87], v[124:127]\n"
      "ds_read_b128 v[116:119], v17 offset:36864\n"
      "v_mul_f32 v129, v129, v157\n"
      "v_mfma_f32_16x16x32_bf16 v[124:127], a[36:39], v[88:91], v[124:127]\n"
      "s_waitcnt lgkmcnt(3)\n"
      "ds_read_b128 v[120:123], v17 offset:37888\n"
      "v_fma_f32 v162, v162, v177, v177\n"
      "v_rcp_f32 v162, v162\n"
      "v_mfma_f32_16x16x32_bf16 v[124:127], a[40:43], v[92:95], v[124:127]\n"
      "v_mul_f32 v137, v137, v161\n"
      "v_fmac_f32 v136, v176, v152\n"
      "v_mfma_f32_16x16x32_bf16 v[124:127], a[44:47], v[96:99], v[124:127]\n"
      "s_waitcnt lgkmcnt(3)\n"
      "v_mul_f32 v136, v136, v160\n"
      "v_mul_f32 v131, v131, v159\n"
      "v_mfma_f32_16x16x32_bf16 v[124:127], a[48:51], v[100:103], v[124:127]\n"
      "s_add_u32 s42, s42, 165888\n"
      "v_mul_f32 v162, v134, v162\n"
      "v_cvt_pk_bf16_f32 v166, v136, v137\n"
      "v_mfma_f32_16x16x32_bf16 v[124:127], a[52:55], v[104:107], v[124:127]\n"
      "v_fmac_f32 v128, v176, v144\n"
      "v_mul_f32 v138, v138, v162\n"
      "v_mfma_f32_16x16x32_bf16 v[124:127], a[56:59], v[108:111], v[124:127]\n"
      "s_waitcnt lgkmcnt(2)\n"
      "v_cvt_pk_bf16_f32 v165, v130, v131\n"
      "v_add_u32 v11, 1, v11\n"
      "v_mfma_f32_16x16x32_bf16 v[124:127], a[60:63], v[112:115], v[124:127]\n"
      "s_waitcnt lgkmcnt(1)\n"
      "v_mul_f32 v163, v135, v163\n"
      "v_fmac_f32 v139, v176, v155\n"
      "v_mfma_f32_16x16x32_bf16 v[124:127], a[64:67], v[116:119], v[124:127]\n"
      "s_waitcnt lgkmcnt(0)\n"
      "v_mul_f32 v128, v128, v156\n"
      "v_mul_f32 v139, v139, v163\n"
      "v_mfma_f32_16x16x32_bf16 v[124:127], a[68:71], v[120:123], v[124:127]\n"
      "s_add_u32 s37, s37, 165888\n"
      "v_cvt_pk_bf16_f32 v164, v128, v129\n"
      "v_cvt_pk_bf16_f32 v167, v138, v139\n"
      "v_mfma_f32_16x16x32_bf16 v[132:135], a[144:147], v[52:55], 0\n"
      "buffer_store_dwordx4 v[164:167], v0, s[8:11], s37 offen sc1\n"
      "v_mfma_f32_16x16x32_bf16 v[132:135], a[148:151], v[56:59], v[132:135]\n"
      "v_sub_f32 v176, -v168, v170\n"
      "v_fma_f32 v169, -v176, v176, v169\n"
      "v_mfma_f32_16x16x32_bf16 v[132:135], a[152:155], v[60:63], v[132:135]\n"
      "v_fmac_f32 v126, v176, v142\n"
      "v_fmac_f32 v124, v176, v140\n"
      "v_mfma_f32_16x16x32_bf16 v[132:135], a[156:159], v[64:67], v[132:135]\n"
      "v_mul_f32 v156, v124, v124\n"
      "v_max_f32 v177, 0x3727c5ac, v169\n"
      "v_mfma_f32_16x16x32_bf16 v[132:135], a[160:163], v[68:71], v[132:135]\n"
      "v_rsq_f32 v170, v177\n"
      "v_mfma_f32_16x16x32_bf16 v[132:135], a[164:167], v[72:75], v[132:135]\n"
      "v_fmac_f32 v125, v176, v141\n"
      "v_mul_f32 v157, v125, v125\n"
      "v_mfma_f32_16x16x32_bf16 v[132:135], a[168:171], v[76:79], v[132:135]\n"
      "v_mul_f32 v178, 0xc0135761, v170\n"
      "v_fmac_f32 v127, v176, v143\n"
      "v_mfma_f32_16x16x32_bf16 v[132:135], a[172:175], v[80:83], v[132:135]\n"
      "v_mul_f32 v171, 0x3d372713, v178\n"
      "v_mul_f32 v170, v170, v170\n"
      "v_mfma_f32_16x16x32_bf16 v[132:135], a[176:179], v[84:87], v[132:135]\n"
      "v_mul_f32 v158, v126, v126\n"
      "v_mul_f32 v159, v127, v127\n"
      "v_mfma_f32_16x16x32_bf16 v[132:135], a[180:183], v[88:91], v[132:135]\n"
      "v_mul_f32 v179, v171, v170\n"
      "v_fma_f32 v157, v157, v179, v178\n"
      "v_mfma_f32_16x16x32_bf16 v[132:135], a[184:187], v[92:95], v[132:135]\n"
      "v_fma_f32 v159, v159, v179, v178\n"
      "v_mul_f32 v157, v125, v157\n"
      "v_mfma_f32_16x16x32_bf16 v[132:135], a[188:191], v[96:99], v[132:135]\n"
      "s_add_u32 s36, s36, 24576\n"
      "v_exp_f32 v157, v157\n"
      "v_mfma_f32_16x16x32_bf16 v[132:135], a[192:195], v[100:103], v[132:135]\n"
      "v_fma_f32 v156, v156, v179, v178\n"
      "v_mul_f32 v156, v124, v156\n"
      "v_mfma_f32_16x16x32_bf16 v[132:135], a[196:199], v[104:107], v[132:135]\n"
      "v_fma_f32 v158, v158, v179, v178\n"
      "v_mul_f32 v159, v127, v159\n"
      "v_mfma_f32_16x16x32_bf16 v[132:135], a[200:203], v[108:111], v[132:135]\n"
      "v_exp_f32 v156, v156\n"
      "v_mfma_f32_16x16x32_bf16 v[132:135], a[204:207], v[112:115], v[132:135]\n"
      "s_add_u32 m0, s38, 20480\n"
      "v_exp_f32 v159, v159\n"
      "v_mfma_f32_16x16x32_bf16 v[132:135], a[208:211], v[116:119], v[132:135]\n"
      "v_mul_f32 v158, v126, v158\n"
      "v_fma_f32 v159, v159, v177, v177\n"
      "v_mfma_f32_16x16x32_bf16 v[132:135], a[212:215], v[120:123], v[132:135]\n"
      "v_rcp_f32 v159, v159\n"
      "v_mfma_f32_16x16x32_bf16 v[128:131], a[72:75], v[52:55], 0\n"
      "s_waitcnt vmcnt(3)\n"
      "ds_write_b32 v13, v11\n"
      "v_mfma_f32_16x16x32_bf16 v[128:131], a[76:79], v[56:59], v[128:131]\n"
      "v_exp_f32 v158, v158\n"
      "v_mfma_f32_16x16x32_bf16 v[128:131], a[80:83], v[60:63], v[128:131]\n"
      "v_fma_f32 v156, v156, v177, v177\n"
      "v_fmac_f32 v135, v176, v151\n"
      "v_mfma_f32_16x16x32_bf16 v[128:131], a[84:87], v[64:67], v[128:131]\n"
      "v_fmac_f32 v133, v176, v149\n"
      "v_mul_f32 v163, v135, v135\n"
      "v_mfma_f32_16x16x32_bf16 v[128:131], a[88:91], v[68:71], v[128:131]\n"
      "v_mul_f32 v161, v133, v133\n"
      "v_fma_f32 v163, v163, v179, v178\n"
      "v_fma_f32 v161, v161, v179, v178\n"
      "v_mfma_f32_16x16x32_bf16 v[128:131], a[92:95], v[72:75], v[128:131]\n"
      "v_fma_f32 v157, v157, v177, v177\n"
      "v_mul_f32 v161, v133, v161\n"
      "v_mfma_f32_16x16x32_bf16 v[128:131], a[96:99], v[76:79], v[128:131]\n"
      "v_exp_f32 v161, v161\n"
      "v_mfma_f32_16x16x32_bf16 v[128:131], a[100:103], v[80:83], v[128:131]\n"
      "s_waitcnt vmcnt(1) lgkmcnt(0)\n"
      "v_fma_f32 v161, v161, v177, v177\n"
      "v_cmp_ne_u32 vcc, s21, v18\n"
      "v_mfma_f32_16x16x32_bf16 v[128:131], a[104:107], v[84:87], v[128:131]\n"
      "v_rcp_f32 v161, v161\n"
      "v_mfma_f32_16x16x32_bf16 v[128:131], a[108:111], v[88:91], v[128:131]\n"
      "ds_read_b32 v12, v14\n"
      "v_fmac_f32 v132, v176, v148\n"
      "v_mfma_f32_16x16x32_bf16 v[128:131], a[112:115], v[92:95], v[128:131]\n"
      "s_cbranch_vccnz .Lsp_g0_10\n"
      ".Lbk_g0_10:\n"
      "v_mul_f32 v160, v132, v132\n"
      "v_mfma_f32_16x16x32_bf16 v[128:131], a[116:119], v[96:99], v[128:131]\n"
      "buffer_load_dwordx4 v0, s[8:11], s40 offen offset:1024 sc1 lds\n"
      "v_mfma_f32_16x16x32_bf16 v[128:131], a[120:123], v[100:103], v[128:131]\n"
      "v_fma_f32 v160, v160, v179, v178\n"
      "v_mul_f32 v160, v132, v160\n"
      "v_mfma_f32_16x16x32_bf16 v[128:131], a[124:127], v[104:107], v[128:131]\n"
      "v_mul_f32 v163, v135, v163\n"
      "v_mul_f32 v161, v133, v161\n"
      "v_mfma_f32_16x16x32_bf16 v[128:131], a[128:131], v[108:111], v[128:131]\n"
      "v_fma_f32 v158, v158, v177, v177\n"
      "v_fmac_f32 v134, v176, v150\n"
      "v_mfma_f32_16x16x32_bf16 v[128:131], a[132:135], v[112:115], v[128:131]\n"
      "s_waitcnt lgkmcnt(0)\n"
      "v_mul_f32 v162, v134, v134\n"
      "v_cmp_gt_i32 vcc, v11, v12\n"
      "v_mfma_f32_16x16x32_bf16 v[128:131], a[136:139], v[116:119], v[128:131]\n"
      "buffer_load_dwordx4 v0, s[8:11], s40 offen offset:0 sc1 lds\n"
      "v_mfma_f32_16x16x32_bf16 v[128:131], a[140:143], v[120:123], v[128:131]\n"
      "v_rcp_f32_e64 v158, v158\n"
      "v_mfma_f32_16x16x32_bf16 v[136:139], a[216:219], v[52:55], 0\n"
      "v_exp_f32_e64 v160, v160\n"
      "v_mfma_f32_16x16x32_bf16 v[136:139], a[220:223], v[56:59], v[136:139]\n"
      "s_cbranch_vccnz .Lsp_gy0_11\n"
      ".Lbk_gy0_11:\n"
      "v_fma_f32 v160, v160, v177, v177\n"
      "v_mfma_f32_16x16x32_bf16 v[136:139], a[224:227], v[60:63], v[136:139]\n"
      "ds_read_b128 v[60:63], v0 offset:2048\n"
      "buffer_load_dwordx4 v0, s[8:11], s40 offen offset:2048 sc1 lds\n"
      "v_mfma_f32_16x16x32_bf16 v[136:139], a[228:231], v[64:67], v[136:139]\n"
      "ds_read_b128 v[52:55], v0 offset:0\n"
      "v_mul_f32 v159, v127, v159\n"
      "v_mfma_f32_16x16x32_bf16 v[136:139], a[232:235], v[68:71], v[136:139]\n"
      "v_exp_f32 v163, v163\n"
      "v_mfma_f32_16x16x32_bf16 v[136:139], a[236:239], v[72:75], v[136:139]\n"
      "v_rcp_f32 v157, v157\n"
      "v_mfma_f32_16x16x32_bf16 v[136:139], a[240:243], v[76:79], v[136:139]\n"
      "buffer_load_dwordx4 v0, s[8:11], s40 offen offset:3072 sc1 lds\n"
      "ds_read_b128 v[76:79], v0 offset:6144\n"
      "v_mfma_f32_16x16x32_bf16 v[136:139], a[244:247], v[80:83], v[136:139]\n"
      "ds_read2st64_b64 v[168:171], v4 offset0:0 offset1:1\n"
      "v_fmac_f32 v131, v176, v147\n"
      "v_mfma_f32_16x16x32_bf16 v[136:139], a[248:251], v[84:87], v[136:139]\n"
      "s_add_u32 m0, s39, 20480\n"
      "ds_read_b128 v[72:75], v0 offset:5120\n"
      "v_mul_f32 v158, v126, v158\n"
      "v_mfma_f32_16x16x32_bf16 v[136:139], a[252:255], v[88:91], v[136:139]\n"
      "v_rcp_f32 v156, v156\n"
      "v_mul_f32 v157, v125, v157\n"
      "v_mfma_f32_16x16x32_bf16 v[136:139], v[20:23], v[92:95], v[136:139]\n"
      "ds_read_b128 v[68:71], v0 offset:4096\n"
      "buffer_load_dwordx4 v1, s[8:11], s40 offen offset:0 sc1 lds\n"
      "v_mfma_f32_16x16x32_bf16 v[136:139], v[24:27], v[96:99], v[136:139]\n"
      "s_min_u32 s36, s36, s6\n"
      "ds_read_b128 v[56:59], v0 offset:1024\n"
      "v_fmac_f32 v129, v176, v145\n"
      "v_mfma_f32_16x16x32_bf16 v[136:139], v[28:31], v[100:103], v[136:139]\n"
      "ds_read_b128 v[64:67], v0 offset:3072\n"
      "v_rcp_f32 v160, v160\n"
      "v_mfma_f32_16x16x32_bf16 v[136:139], v[32:35], v[104:107], v[136:139]\n"
      "ds_read_b128 v[88:91], v0 offset:9216\n"
      "v_mul_f32 v160, v132, v160\n"
      "v_mfma_f32_16x16x32_bf16 v[136:139], v[36:39], v[108:111], v[136:139]\n"
      "s_waitcnt vmcnt(5) lgkmcnt(5)\n"
      "buffer_store_dword v15, v3, s[8:11], s42 offen sc1\n"
      "ds_read_b128 v[108:111], v0 offset:14336\n"
      "v_mfma_f32_16x16x32_bf16 v[136:139], v[40:43], v[112:115], v[136:139]\n"
      "ds_read_b128 v[80:83], v0 offset:7168\n"
      "v_mul_f32 v156, v124, v156\n"
      "v_mfma_f32_16x16x32_bf16 v[136:139], v[44:47], v[116:119], v[136:139]\n"
      "ds_read_b128 v[104:107], v0 offset:13312\n"
      "v_add_f32 v169, v169, v171\n"
      "v_mfma_f32_16x16x32_bf16 v[136:139], v[48:51], v[120:123], v[136:139]\n"
      "buffer_load_dword v18, v2, s[8:11], s36 offen sc1\n"
      "s_sub_u32 s20, s20, 1\n"
      "s_cbranch_scc0 .Lg_loop\n"
      "v_fmac_f32 v130, v176, v146\n"
      "v_fma_f32 v163, v163, v177, v177\n"
      "v_fma_f32 v162, v162, v179, v178\n"
      "v_mul_f32 v130, v130, v158\n"
      "v_mul_f32 v162, v134, v162\n"
      "s_nop 2\n"
      "v_fmac_f32 v137, v176, v153\n"
      "v_rcp_f32 v163, v163\n"
      "v_fmac_f32 v138, v176, v154\n"
      "v_exp_f32 v162, v162\n"
      "v_mul_f32 v129, v129, v157\n"
      "v_fma_f32 v162, v162, v177, v177\n"
      "v_rcp_f32 v162, v162\n"
      "v_mul_f32 v137, v137, v161\n"
      "v_fmac_f32 v136, v176, v152\n"
      "v_mul_f32 v136, v136, v160\n"
      "v_mul_f32 v131, v131, v159\n"
      "s_add_u32 s42, s42, 165888\n"
      "v_mul_f32 v162, v134, v162\n"
      "v_cvt_pk_bf16_f32 v166, v136, v137\n"
      "v_fmac_f32 v128, v176, v144\n"
      "v_mul_f32 v138, v138, v162\n"
      "v_cvt_pk_bf16_f32 v165, v130, v131\n"
      "v_mul_f32 v163, v135, v163\n"
      "v_fmac_f32 v139, v176, v155\n"
      "v_mul_f32 v128, v128, v156\n"
      "v_mul_f32 v139, v139, v163\n"
      "s_add_u32 s37, s37, 165888\n"
      "v_cvt_pk_bf16_f32 v164, v128, v129\n"
      "v_cvt_pk_bf16_f32 v167, v138, v139\n"
      "buffer_store_dwordx4 v[164:167], v0, s[8:11], s37 offen sc1\n"
      "s_waitcnt vmcnt(0)\n"
      "buffer_store_dword v15, v3, s[8:11], s42 offen sc1\n"
      "s_add_u32 s42, s42, 165888\n"
      "s_add_u32 s37, s37, 165888\n"
      "s_endpgm\n"
      ".Lsp_g0_1:\n"
      "s_mov_b32 s22, s23\n"
      ".Lsl_g0_1:\n"
      "buffer_load_dword v18, v2, s[8:11], s40 offen sc1\n"
      "s_waitcnt vmcnt(0)\n"
      "v_cmp_ne_u32 vcc, s21, v18\n"
      "s_cbranch_vccz .Lbk_g0_1\n"
      "s_sleep 4\n"
      "s_sub_u32 s22, s22, 1\n"
      "s_cbranch_scc0 .Lsl_g0_1\n"
      "s_mov_b32 s23, 0\n"
      "s_branch .Lbk_g0_1\n"
      ".Lsp_g0_2:\n"
      "s_mov_b32 s22, s23\n"
      ".Lsl_g0_2:\n"
      "buffer_load_dword v18, v2, s[8:11], s40 offen sc1\n"
      "s_waitcnt vmcnt(0)\n"
      "v_cmp_ne_u32 vcc, s21, v18\n"
      "s_cbranch_vccz .Lbk_g0_2\n"
      "s_sleep 4\n"
      "s_sub_u32 s22, s22, 1\n"
      "s_cbranch_scc0 .Lsl_g0_2\n"
      "s_mov_b32 s23, 0\n"
      "s_branch .Lbk_g0_2\n"
      ".Lsp_gy0_3:\n"
      "s_lshl_b32 s22, s23, 4\n"
      ".Lsl_gy0_3:\n"
      "ds_read_b32 v12, v14\n"
      "s_waitcnt lgkmcnt(0)\n"
      "v_cmp_gt_i32 vcc, v11, v12\n"
      "s_cbranch_vccz .Lbk_gy0_3\n"
      "s_sub_u32 s22, s22, 1\n"
      "s_cbranch_scc0 .Lsl_gy0_3\n"
      "s_mov_b32 s23, 0\n"
      "s_branch .Lbk_gy0_3\n"
      ".Lsp_g0_4:\n"
      "s_mov_b32 s22, s23\n"
      "buffer_load_dword v18, v2, s[8:11], s40 offen sc1\n"
      "s_waitcnt vmcnt(0)\n"
      "v_cmp_ne_u32 vcc, s21, v18\n"
      "s_add_u32 s30, s42, 0\n"
      "buffer_store_dword v15, v3, s[8:11], s30 offen sc1\n"
      "s_cbranch_vccz .Lbk_g0_4\n"
      ".Lsl_g0_4:\n"
      "buffer_load_dword v18, v2, s[8:11], s40 offen sc1\n"
      "s_waitcnt vmcnt(0)\n"
      "v_cmp_ne_u32 vcc, s21, v18\n"
      "s_cbranch_vccz .Lbk_g0_4\n"
      "s_sleep 4\n"
      "s_sub_u32 s22, s22, 1\n"
      "s_cbranch_scc0 .Lsl_g0_4\n"
      "s_mov_b32 s23, 0\n"
      "s_branch .Lbk_g0_4\n"
      ".Lsp_gy0_5:\n"
      "s_lshl_b32 s22, s23, 4\n"
      ".Lsl_gy0_5:\n"
      "ds_read_b32 v12, v14\n"
      "s_waitcnt lgkmcnt(0)\n"
      "v_cmp_gt_i32 vcc, v11, v12\n"
      "s_cbranch_vccz .Lbk_gy0_5\n"
      "s_sub_u32 s22, s22, 1\n"
      "s_cbranch_scc0 .Lsl_gy0_5\n"
      "s_mov_b32 s23, 0\n"
      "s_branch .Lbk_gy0_5\n"
      ".Lsp_g0_6:\n"
      "s_mov_b32 s22, s23\n"
      "buffer_load_dword v18, v2, s[8:11], s40 offen sc1\n"
      "s_waitcnt vmcnt(0)\n"
      "v_cmp_ne_u32 vcc, s21, v18\n"
      "s_add_u32 s30, s42, 0\n"
      "buffer_store_dword v15, v3, s[8:11], s30 offen sc1\n"
      "s_cbranch_vccz .Lbk_g0_6\n"
      ".Lsl_g0_6:\n"
      "buffer_load_dword v18, v2, s[8:11], s40 offen sc1\n"
      "s_waitcnt vmcnt(0)\n"
      "v_cmp_ne_u32 vcc, s21, v18\n"
      "s_cbranch_vccz .Lbk_g0_6\n"
      "s_sleep 4\n"
      "s_sub_u32 s22, s22, 1\n"
      "s_cbranch_scc0 .Lsl_g0_6\n"
      "s_mov_b32 s23, 0\n"
      "s_branch .Lbk_g0_6\n"
      ".Lsp_gy0_7:\n"
      "s_lshl_b32 s22, s23, 4\n"
      ".Lsl_gy0_7:\n"
      "ds_read_b32 v12, v14\n"
      "s_waitcnt lgkmcnt(0)\n"
      "v_cmp_gt_i32 vcc, v11, v12\n"
      "s_cbranch_vccz .Lbk_gy0_7\n"
      "s_sub_u32 s22, s22, 1\n"
      "s_cbranch_scc0 .Lsl_gy0_7\n"
      "s_mov_b32 s23, 0\n"
      "s_branch .Lbk_gy0_7\n"
      ".Lsp_g0_8:\n"
      "s_mov_b32 s22, s23\n"
      "buffer_load_dword v18, v2, s[8:11], s40 offen sc1\n"
      "s_waitcnt vmcnt(0)\n"
      "v_cmp_ne_u32 vcc, s21, v18\n"
      "s_add_u32 s30, s42, 0\n"
      "buffer_store_dword v15, v3, s[8:11], s30 offen sc1\n"
      "s_cbranch_vccz .Lbk_g0_8\n"
      ".Lsl_g0_8:\n"
      "buffer_load_dword v18, v2, s[8:11], s40 offen sc1\n"
      "s_waitcnt vmcnt(0)\n"
      "v_cmp_ne_u32 vcc, s21, v18\n"
      "s_cbranch_vccz .Lbk_g0_8\n"
      "s_sleep 4\n"
      "s_sub_u32 s22, s22, 1\n"
      "s_cbranch_scc0 .Lsl_g0_8\n"
      "s_mov_b32 s23, 0\n"
      "s_branch .Lbk_g0_8\n"
      ".Lsp_gy0_9:\n"
      "s_lshl_b32 s22, s23, 4\n"
      ".Lsl_gy0_9:\n"
      "ds_read_b32 v12, v14\n"
      "s_waitcnt lgkmcnt(0)\n"
      "v_cmp_gt_i32 vcc, v11, v12\n"
      "s_cbranch_vccz .Lbk_gy0_9\n"
      "s_sub_u32 s22, s22, 1\n"
      "s_cbranch_scc0 .Lsl_gy0_9\n"
      "s_mov_b32 s23, 0\n"
      "s_branch .Lbk_gy0_9\n"
      ".Lsp_g0_10:\n"
      "s_mov_b32 s22, s23\n"
      "buffer_load_dword v18, v2, s[8:11], s40 offen sc1\n"
      "s_waitcnt vmcnt(0)\n"
      "v_cmp_ne_u32 vcc, s21, v18\n"
      "s_add_u32 s30, s42, 0\n"
      "buffer_store_dword v15, v3, s[8:11], s30 offen sc1\n"
      "s_cbranch_vccz .Lbk_g0_10\n"
      ".Lsl_g0_10:\n"
      "buffer_load_dword v18, v2, s[8:11], s40 offen sc1\n"
      "s_waitcnt vmcnt(0)\n"
      "v_cmp_ne_u32 vcc, s21, v18\n"
      "s_cbranch_vccz .Lbk_g0_10\n"
      "s_sleep 4\n"
      "s_sub_u32 s22, s22, 1\n"
      "s_cbranch_scc0 .Lsl_g0_10\n"
      "s_mov_b32 s23, 0\n"
      "s_branch .Lbk_g0_10\n"
      ".Lsp_gy0_11:\n"
      "s_lshl_b32 s22, s23, 4\n"
      ".Lsl_gy0_11:\n"
      "ds_read_b32 v12, v14\n"
      "s_waitcnt lgkmcnt(0)\n"
      "v_cmp_gt_i32 vcc, v11, v12\n"
      "s_cbranch_vccz .Lbk_gy0_11\n"
      "s_sub_u32 s22, s22, 1\n"
      "s_cbranch_scc0 .Lsl_gy0_11\n"
      "s_mov_b32 s23, 0\n"
      "s_branch .Lbk_gy0_11\n"
      ".Lg_last:\n"
      :
      : "{s[8:11]}"(rsWS), "{s[12:15]}"(rsA), "{s[16:19]}"(rsB), "{s[92:95]}"(rsC), "{v0}"(v_l16), "{v1}"(v_l5), "{v2}"(v_fa), "{v3}"(v_ra), "{v4}"(v_st), "{v5}"(v_at), "{v6}"(v_vo0), "{v7}"(v_vo1), "{v8}"(v_vo2), "{v9}"(v_vo3), "{v10}"(v_pw), "{v12}"(v_al), "{v13}"(v_yw), "{v14}"(v_yr), "{v16}"(v_hi), "{v54}"(v_tw), "{s36}"(s_sx), "{s37}"(s_sh), "{s38}"(s_m4), "{s39}"(s_m5)
      : "v255", "a255", "s99", "vcc", "scc", "memory");

  }
}

extern "C" void kernel_launch(void* const* d_in, const int* in_sizes, int n_in, void* d_out, int out_size,
                              void* d_ws, size_t ws_size, hipStream_t stream) {
  char* ws = (char*)d_ws + ((256 - ((size_t)d_ws & 255)) & 255);
  (void)hipFuncSetAttribute((const void*)k_all, hipFuncAttributeMaxDynamicSharedMemorySize, LDS_BYTES);
  k_all<<<256, 256, LDS_BYTES, stream>>>((const u16*)d_in[0], (const u16*)d_in[1], (const u16*)d_in[2], (const u16*)d_in[4],
                                         (const u16*)d_in[5], (const u16*)d_in[6], ws, (u16*)d_out);
}
```

```cpp
#include <hip/hip_runtime.h>
#include <stdint.h>

typedef unsigned short u16;
typedef __attribute__((ext_vector_type(4))) unsigned u32x4;
constexpr unsigned WS_DBG = 65536u;
constexpr unsigned WS_XIN = 1310720u;
constexpr unsigned WS_OB = 9175040u;
constexpr unsigned WS_X1 = 17039360u;
constexpr unsigned WS_H = 26034176u;
constexpr unsigned WS_PS = 78589952u;
constexpr unsigned WS_END = 109740032u;
constexpr unsigned KSK = 0u;
constexpr unsigned ENT = 1u;
constexpr unsigned ROT = 0u;
constexpr unsigned MAP = 1u;
constexpr unsigned FLIP = 0u;
constexpr unsigned PW = 1664u;
constexpr unsigned REC = 24576u;
constexpr unsigned HREC = 165888u;
constexpr unsigned PREC = 99840u;
constexpr unsigned NREC = 80u;
constexpr unsigned OOB = 0x40000000u, NEG = 0xff61b1e6u;
constexpr int LDS_BYTES = 160 * 1024;

__device__ __forceinline__ u32x4 srd(const void* p, unsigned bytes) {
  const unsigned long long a = (unsigned long long)p;
  return u32x4{(unsigned)a, (unsigned)(a >> 32) & 0xffffu, bytes, 0x00020000u};
}

extern "C" __global__ __launch_bounds__(256, 1) __attribute__((amdgpu_waves_per_eu(1, 1))) void k_all(
    const u16* X, const u16* Wqkv, const u16* Wo, const u16* Wg, const u16* Wu, const u16* Wd, char* ws, u16* out) {
  const unsigned b = blockIdx.x, xcd = b & 7, slot = b >> 3;
  const unsigned w = __builtin_amdgcn_readfirstlane(threadIdx.x >> 6), lane = threadIdx.x & 63;
  const bool fast = ((xcd ^ FLIP) & 1) == 0;
  if (ENT) {
    constexpr unsigned NL = MAP ? 41 : 27, LR = MAP ? 9 : 6;
    const unsigned wi = slot * 4 + w, n0 = wi * NL + lane, n = n0 < 576 * LR ? n0 : 0, r = n / LR, c = n - LR * r;
    const unsigned m = (xcd * 128 + wi) * 4 + lane - NL, t = m / 144;
    unsigned oc = (wi * 8 + (lane & 7)) * 128;
    asm volatile("s_getpc_b64 vcc\n.Lent_pc:\n\ts_sub_u32 vcc_lo, vcc_lo, .Lent_pc-k_all\n\ts_subb_u32 vcc_hi, vcc_hi, 0\n\ts_mov_b32 m0, %1\n\t"
                 "v_min_u32 %0, .Lq_last-k_all, %0\n\tglobal_load_lds_dword %0, vcc" : "+v"(oc) : "s"(162816 + w * 256) : "vcc", "scc", "memory");
    unsigned oq = r * 3456 + c / (LR / 3) * 1152 + ((MAP ? xcd & 6 : xcd) + c % (LR / 3)) * 128, ox = (32 * (t & 3) + 31 - (t >> 2)) * 18432 + (m - 144 * t) * 128;
    asm volatile("" : "+v"(oq), "+v"(ox));
    const bool isq = lane < NL && (fast || !MAP);
    oq = isq ? oq : lane - NL < 4 ? ox : 0;
    asm volatile("" : "+v"(oq));
    __builtin_amdgcn_sched_barrier(0);
    const char* pa = (const char*)(isq ? Wqkv : X) + oq;
    asm volatile("global_load_lds_dword %0, off" ::"v"(pa) : "memory");
    __builtin_amdgcn_sched_barrier(0);
  }
  unsigned role, gr, part;
  if (MAP) {
    const unsigned e = xcd >> 1, sd = slot - (fast ? 10 : 0), sg = slot - (fast ? 16 : 12), i3 = sg / 3, e1 = (e - 1) & 3;
    role = fast ? (slot >= 8) + (slot >= 10) + (slot >= 16) : 2 + (slot >= 12);
    gr = role == 0 ? slot & 3 : role == 1 ? e : role == 2 ? sd / 3 + (fast ? 2 * (e & 1) : 0) : !fast ? sg & 3 : sg < 12 ? (e + 1 + sg - 3 * i3) & 3 : e1;
    part = role == 0 ? 2 * e + (slot >> 2) : role == 1 ? slot - 8 : role == 2 ? (fast ? e >> 1 : e + 2) * 3 + sd % 3
         : !fast ? 5 * e + (sg >> 2) : sg < 12 ? 20 + 4 * e + i3 : 8 + 4 * e1 + sg;
  } else {
    const unsigned td = ((xcd + 4) & 7) * 3 + (slot - 5) / 3, tg = xcd * 3 + (slot - 14) / 6;
    role = (slot >= 4) + (slot >= 5) + (slot >= 14);
    gr = role == 0 ? slot : role == 1 ? 3 - (xcd & 3) : role == 2 ? td & 3 : tg & 3;
    part = role == 0 ? xcd : role == 1 ? xcd >> 2 : role == 2 ? (td >> 2) * 3 + (slot - 5) % 3 : 6 * (tg >> 2) + (slot - 14) % 6;
  }
  const unsigned i16 = lane & 15, g = lane >> 4, q = i16 >> 2, p = lane & 3;
  const u32x4 rsWS = srd(ws, WS_END);
  const unsigned v_l16 = lane * 16, v_dbg = WS_DBG + (((role == 0 ? 0 : role == 1 ? 32 : role == 2 ? 40 : 112) + part * 4 + gr) * 4 + w) * 1024 + lane * 4;
  const unsigned yb = role & 1 ? 81920 : role ? 149760 : 61440, v_yw = yb + w * 4, v_yr = yb + p * 4;
  const unsigned half = (i16 + 16 * (g >> 1)) * 16 + (g & 1) * 8;
  const unsigned line = lane < 32 ? lane * 4 : OOB, v_al = w ? OOB : line + 128 * (1 + b);
  const unsigned up = lane >> 5, l5 = (lane & 31) * 4, v_hi = 0u - up;
  const unsigned l8 = lane >> 3, l7 = (lane & 7) * 16;
  (void)v_dbg;
  if (role == 0) {
    const unsigned hg = part, h = 4 * hg + w, par = i16 & 1;
    const u32x4 rsA = srd(Wqkv, 576 * 1728 * 2);
    const unsigned v_l5 = w < 3 || lane < 32 ? v_l16 + 16384 - w * 3072 : OOB;
    const unsigned v_fa = 20480 - w * 4096 + (lane < 48 ? lane >> 2 : 11) * 128 + p * 4, v_ra = up * 8 * REC + l5 + 20480 + h * 128;
    const unsigned v_st = 18432 + i16 * 32 + q * 8, v_tw = 61568 + w * 128 + i16 * 8, v_tr = 61568 + w * 128 + g * 32;
    const unsigned v_om = h * 512 + half, v_oe = g ? OOB : 16384 + h * 128 + i16 * 8;
    const unsigned v_pw = 20480 + h * 128 + l8 * REC + l7;
    unsigned v_vo[4], v_bc[4], v_bp[4], v_mk[4], v_sel[4] = {0, 0, 0, 0}, v_ci[4];
#pragma unroll
    for (unsigned i = 0; i < 4; ++i) {
      const unsigned kk = 4 * g + i, ka = 4 * g + 2 * (i & 1) + par;
      v_bc[i] = (i & 1) == par && kk <= i16 ? 0u : NEG;
      v_bp[i] = (i & 1) == par && kk >= i16 + 2 ? 0u : NEG;
      v_mk[i] = (i < 2 ? ka > i16 : ka <= i16) ? 0xffffu << (16 * par) : 0u;
      v_ci[i] = p == 2 ? 0x3f800000u : 0u;
    }
    v_sel[3] = lane == 16 ? 0x3f80u : lane == 17 ? 0x3f800000u : 0u;
#pragma unroll
    for (unsigned i = 0; i < 4; ++i) {
      const unsigned c = 64 * i + lane, cc = c % 30;
      v_vo[i] = c < 240 && cc < 27 ? (8 * w + c / 30) * 3456 + cc / 9 * 1152 + 144 * hg + cc % 9 * 16 - 1024 * i : OOB;
    }
    const unsigned rb = 65536 + g * 4224 + q * 480;
    const unsigned v_at = rb + 36 * w + 4 * (w & 1) + 8 * p, v_ax = rb + (p < 2 ? p : 2) * 144 + 72 * (w >> 1) + 32;
    const unsigned v_pm = (16 * g + 4 * (i16 >> 1 & 3) + 2 * (w & 1) + par) * 4, v_ms = i16 < 6 ? ~0u : 0u;
    const unsigned s_sx = WS_XIN + (gr * NREC + KSK) * REC + w * 4096, s_ev = s_sx + (32 - KSK) * REC, s_del = 0u - (gr ? NREC + 32 : 32) * REC;
    const unsigned s_kil = gr ? 0x7f7fffffu : 0xff7fffffu, s_so = WS_OB + (gr * NREC + KSK) * REC;
    const unsigned s_m4 = w * 4096, s_m5 = 16384 + w * 1024, s_ms = w * 4224, s_up = part + gr + w;
    asm volatile(
      "s_buffer_load_dword s21, s[8:11], 0\n"
      "s_mov_b32 s23, 65536\n"
      "s_mov_b32 s31, 0x40000000\n"
      "s_mov_b32 s51, 0\n"
      "s_add_u32 m0, s43, 65536\n"
      "s_nop 0\n"
      "buffer_load_dwordx4 v48, s[12:15], s51 offen offset:0 lds\n"
      "buffer_load_dwordx4 v49, s[12:15], s51 offen offset:1024 lds\n"
      "buffer_load_dwordx4 v50, s[12:15], s51 offen offset:2048 lds\n"
      "buffer_load_dwordx4 v51, s[12:15], s51 offen offset:3072 lds\n"
      "s_add_u32 s51, s51, 110592\n"
      "s_add_u32 m0, s43, 82432\n"
      "s_nop 0\n"
      "buffer_load_dwordx4 v48, s[12:15], s51 offen offset:0 lds\n"
      "buffer_load_dwordx4 v49, s[12:15], s51 offen offset:1024 lds\n"
      "buffer_load_dwordx4 v50, s[12:15], s51 offen offset:2048 lds\n"
      "buffer_load_dwordx4 v51, s[12:15], s51 offen offset:3072 lds\n"
      "s_add_u32 s51, s51, 110592\n"
      "s_add_u32 m0, s43, 99328\n"
      "s_nop 0\n"
      "buffer_load_dwordx4 v48, s[12:15], s51 offen offset:0 lds\n"
      "buffer_load_dwordx4 v49, s[12:15], s51 offen offset:1024 lds\n"
      "buffer_load_dwordx4 v50, s[12:15], s51 offen offset:2048 lds\n"
      "buffer_load_dwordx4 v51, s[12:15], s51 offen offset:3072 lds\n"
      "s_add_u32 s51, s51, 110592\n"
      "s_add_u32 s6, s36, 1572864\n"
      "s_add_u32 s36, s36, 4294942720\n"
      "s_add_u32 s48, s37, 811008\n"
      "s_mov_b32 s53, 0x7f7fffff\n"
      "v_mov_b32 v179, 0\n"
      "s_mov_b32 s54, 0xffff\n"
      "s_mov_b32 s55, 0\n"
      "v_mov_b32 v9, 0\n"
      "ds_write_b32 v11, v9\n"
      "v_mov_b32 v56, 0x3f803f80\n"
      "v_mov_b32 v57, 0x3f803f80\n"
      "v_mov_b32 v58, 0x3f803f80\n"
      "v_mov_b32 v59, 0x3f803f80\n"
      "s_mov_b64 exec, 0\n"
      "s_lshr_b32 s22, s41, 12\n"
      ".Lq_pl:\n"
      "s_cmp_eq_u32 s22, 0\n"
      "s_cbranch_scc1 .Lq_p0\n"
      "s_cmp_eq_u32 s22, 1\n"
      "s_cbranch_scc1 .Lq_p1\n"
      "s_branch .Lq_pe\n"
      ".Lq_pr:\n"
      "s_add_u32 s22, s22, 4\n"
      "s_branch .Lq_pl\n"
      ".Lq_pe:\n"
      "s_mov_b64 exec, -1\n"
      "s_waitcnt vmcnt(8) lgkmcnt(0)\n"
      "s_barrier\n"
      "ds_read_b64_tr_b16 v[60:61], v53 offset:0\n"
      "ds_read_b64_tr_b16 v[62:63], v53 offset:1920\n"
      "ds_read_b64_tr_b16 a[0:1], v52 offset:144\n"
      "ds_read_b64_tr_b16 a[2:3], v52 offset:2064\n"
      "ds_read_b64_tr_b16 a[72:73], v52 offset:288\n"
      "ds_read_b64_tr_b16 a[74:75], v52 offset:2208\n"
      "ds_read_b64_tr_b16 a[144:145], v52 offset:0\n"
      "ds_read_b64_tr_b16 a[146:147], v52 offset:1920\n"
      "v_add_u32 v52, 16896, v52\n"
      "v_add_u32 v53, 16896, v53\n"
      "s_add_u32 m0, s43, 116224\n"
      "s_nop 0\n"
      "buffer_load_dwordx4 v48, s[12:15], s51 offen offset:0 lds\n"
      "buffer_load_dwordx4 v49, s[12:15], s51 offen offset:1024 lds\n"
      "buffer_load_dwordx4 v50, s[12:15], s51 offen offset:2048 lds\n"
      "buffer_load_dwordx4 v51, s[12:15], s51 offen offset:3072 lds\n"
      "s_add_u32 s51, s51, 110592\n"
      "s_waitcnt vmcnt(8) lgkmcnt(0)\n"
      "s_barrier\n"
      "ds_read_b64_tr_b16 v[68:69], v53 offset:0\n"
      "ds_read_b64_tr_b16 v[70:71], v53 offset:1920\n"
      "ds_read_b64_tr_b16 a[4:5], v52 offset:144\n"
      "ds_read_b64_tr_b16 a[6:7], v52 offset:2064\n"
      "ds_read_b64_tr_b16 a[76:77], v52 offset:288\n"
      "ds_read_b64_tr_b16 a[78:79], v52 offset:2208\n"
      "ds_read_b64_tr_b16 a[148:149], v52 offset:0\n"
      "ds_read_b64_tr_b16 a[150:151], v52 offset:1920\n"
      "v_add_u32 v52, 16896, v52\n"
      "v_add_u32 v53, 16896, v53\n"
      "ds_bpermute_b32 v64, v54, v60\n"
      "ds_bpermute_b32 v65, v54, v61\n"
      "ds_bpermute_b32 v66, v54, v62\n"
      "ds_bpermute_b32 v67, v54, v63\n"
      "v_mfma_f32_16x16x32_bf16 v[136:139], a[0:3], v[56:59], 0\n"
      "v_mfma_f32_16x16x32_bf16 v[144:147], v[56:59], a[72:75], 0\n"
      "v_mfma_f32_16x16x32_bf16 v[148:151], a[144:147], v[56:59], 0\n"
      "s_add_u32 m0, s43, 65536\n"
      "s_nop 0\n"
      "buffer_load_dwordx4 v48, s[12:15], s51 offen offset:0 lds\n"
      "buffer_load_dwordx4 v49, s[12:15], s51 offen offset:1024 lds\n"
      "buffer_load_dwordx4 v50, s[12:15], s51 offen offset:2048 lds\n"
      "buffer_load_dwordx4 v51, s[12:15], s51 offen offset:3072 lds\n"
      "s_add_u32 s51, s51, 110592\n"
      "s_waitcnt vmcnt(8) lgkmcnt(0)\n"
      "s_barrier\n"
      "ds_read_b64_tr_b16 v[60:61], v53 offset:0\n"
      "ds_read_b64_tr_b16 v[62:63], v53 offset:1920\n"
      "ds_read_b64_tr_b16 a[8:9], v52 offset:144\n"
      "ds_read_b64_tr_b16 a[10:11], v52 offset:2064\n"
      "ds_read_b64_tr_b16 a[80:81], v52 offset:288\n"
      "ds_read_b64_tr_b16 a[82:83], v52 offset:2208\n"
      "ds_read_b64_tr_b16 a[152:153], v52 offset:0\n"
      "ds_read_b64_tr_b16 a[154:155], v52 offset:1920\n"
      "v_add_u32 v52, 16896, v52\n"
      "v_add_u32 v53, 16896, v53\n"
      "ds_bpermute_b32 v72, v54, v68\n"
      "ds_bpermute_b32 v73, v54, v69\n"
      "ds_bpermute_b32 v74, v54, v70\n"
      "ds_bpermute_b32 v75, v54, v71\n"
      "v_mfma_f32_16x16x32_bf16 v[136:139], a[4:7], v[56:59], v[136:139]\n"
      "v_mfma_f32_16x16x32_bf16 v[144:147], v[56:59], a[76:79], v[144:147]\n"
      "v_mfma_f32_16x16x32_bf16 v[148:151], a[148:151], v[56:59], v[148:151]\n"
      "s_add_u32 m0, s43, 82432\n"
      "s_nop 0\n"
      "buffer_load_dwordx4 v48, s[12:15], s51 offen offset:0 lds\n"
      "buffer_load_dwordx4 v49, s[12:15], s51 offen offset:1024 lds\n"
      "buffer_load_dwordx4 v50, s[12:15], s51 offen offset:2048 lds\n"
      "buffer_load_dwordx4 v51, s[12:15], s51 offen offset:3072 lds\n"
      "s_add_u32 s51, s51, 110592\n"
      "v_and_b32 v64, v55, v64\n"
      "v_and_b32 v65, v55, v65\n"
      "v_and_b32 v66, v55, v66\n"
      "v_and_b32 v67, v55, v67\n"
      "v_accvgpr_write_b32 a216, v64\n"
      "v_accvgpr_write_b32 a217, v65\n"
      "v_accvgpr_write_b32 a218, v66\n"
      "v_accvgpr_write_b32 a219, v67\n"
      "v_mfma_f32_16x16x32_bf16 v[140:143], v[64:67], v[56:59], 0\n"
      "s_waitcnt vmcnt(8) lgkmcnt(0)\n"
      "s_barrier\n"
      "ds_read_b64_tr_b16 v[68:69], v53 offset:0\n"
      "ds_read_b64_tr_b16 v[70:71], v53 offset:1920\n"
      "ds_read_b64_tr_b16 a[12:13], v52 offset:144\n"
      "ds_read_b64_tr_b16 a[14:15], v52 offset:2064\n"
      "ds_read_b64_tr_b16 a[84:85], v52 offset:288\n"
      "ds_read_b64_tr_b16 a[86:87], v52 offset:2208\n"
      "ds_read_b64_tr_b16 a[156:157], v52 offset:0\n"
      "ds_read_b64_tr_b16 a[158:159], v52 offset:1920\n"
      "v_add_u32 v52, 4294916608, v52\n"
      "v_add_u32 v53, 4294916608, v53\n"
      "ds_bpermute_b32 v64, v54, v60\n"
      "ds_bpermute_b32 v65, v54, v61\n"
      "ds_bpermute_b32 v66, v54, v62\n"
      "ds_bpermute_b32 v67, v54, v63\n"
      "v_mfma_f32_16x16x32_bf16 v[136:139], a[8:11], v[56:59], v[136:139]\n"
      "v_mfma_f32_16x16x32_bf16 v[144:147], v[56:59], a[80:83], v[144:147]\n"
      "v_mfma_f32_16x16x32_bf16 v[148:151], a[152:155], v[56:59], v[148:151]\n"
      "s_add_u32 m0, s43, 99328\n"
      "s_nop 0\n"
      "buffer_load_dwordx4 v48, s[12:15], s51 offen offset:0 lds\n"
      "buffer_load_dwordx4 v49, s[12:15], s51 offen offset:1024 lds\n"
      "buffer_load_dwordx4 v50, s[12:15], s51 offen offset:2048 lds\n"
      "buffer_load_dwordx4 v51, s[12:15], s51 offen offset:3072 lds\n"
      "s_add_u32 s51, s51, 110592\n"
      "v_and_b32 v72, v55, v72\n"
      "v_and_b32 v73, v55, v73\n"
      "v_and_b32 v74, v55, v74\n"
      "v_and_b32 v75, v55, v75\n"
      "v_accvgpr_write_b32 a220, v72\n"
      "v_accvgpr_write_b32 a221, v73\n"
      "v_accvgpr_write_b32 a222, v74\n"
      "v_accvgpr_write_b32 a223, v75\n"
      "v_mfma_f32_16x16x32_bf16 v[140:143], v[72:75], v[56:59], v[140:143]\n"
      "s_waitcnt vmcnt(8) lgkmcnt(0)\n"
      "s_barrier\n"
      "ds_read_b64_tr_b16 v[60:61], v53 offset:0\n"
      "ds_read_b64_tr_b16 v[62:63], v53 offset:1920\n"
      "ds_read_b64_tr_b16 a[16:17], v52 offset:144\n"
      "ds_read_b64_tr_b16 a[18:19], v52 offset:2064\n"
      "ds_read_b64_tr_b16 a[88:89], v52 offset:288\n"
      "ds_read_b64_tr_b16 a[90:91], v52 offset:2208\n"
      "ds_read_b64_tr_b16 a[160:161], v52 offset:0\n"
      "ds_read_b64_tr_b16 a[162:163], v52 offset:1920\n"
      "v_add_u32 v52, 16896, v52\n"
      "v_add_u32 v53, 16896, v53\n"
      "ds_bpermute_b32 v72, v54, v68\n"
      "ds_bpermute_b32 v73, v54, v69\n"
      "ds_bpermute_b32 v74, v54, v70\n"
      "ds_bpermute_b32 v75, v54, v71\n"
      "v_mfma_f32_16x16x32_bf16 v[136:139], a[12:15], v[56:59], v[136:139]\n"
      "v_mfma_f32_16x16x32_bf16 v[144:147], v[56:59], a[84:87], v[144:147]\n"
      "v_mfma_f32_16x16x32_bf16 v[148:151], a[156:159], v[56:59], v[148:151]\n"
      "s_add_u32 m0, s43, 116224\n"
      "s_nop 0\n"
      "buffer_load_dwordx4 v48, s[12:15], s51 offen offset:0 lds\n"
      "buffer_load_dwordx4 v49, s[12:15], s51 offen offset:1024 lds\n"
      "buffer_load_dwordx4 v50, s[12:15], s51 offen offset:2048 lds\n"
      "buffer_load_dwordx4 v51, s[12:15], s51 offen offset:3072 lds\n"
      "s_add_u32 s51, s51, 110592\n"
      "v_and_b32 v64, v55, v64\n"
      "v_and_b32 v65, v55, v65\n"
      "v_and_b32 v66, v55, v66\n"
      "v_and_b32 v67, v55, v67\n"
      "v_accvgpr_write_b32 a224, v64\n"
      "v_accvgpr_write_b32 a225, v65\n"
      "v_accvgpr_write_b32 a226, v66\n"
      "v_accvgpr_write_b32 a227, v67\n"
      "v_mfma_f32_16x16x32_bf16 v[140:143], v[64:67], v[56:59], v[140:143]\n"
      "s_waitcnt vmcnt(8) lgkmcnt(0)\n"
      "s_barrier\n"
      "ds_read_b64_tr_b16 v[68:69], v53 offset:0\n"
      "ds_read_b64_tr_b16 v[70:71], v53 offset:1920\n"
      "ds_read_b64_tr_b16 a[20:21], v52 offset:144\n"
      "ds_read_b64_tr_b16 a[22:23], v52 offset:2064\n"
      "ds_read_b64_tr_b16 a[92:93], v52 offset:288\n"
      "ds_read_b64_tr_b16 a[94:95], v52 offset:2208\n"
      "ds_read_b64_tr_b16 a[164:165], v52 offset:0\n"
      "ds_read_b64_tr_b16 a[166:167], v52 offset:1920\n"
      "v_add_u32 v52, 16896, v52\n"
      "v_add_u32 v53, 16896, v53\n"
      "ds_bpermute_b32 v64, v54, v60\n"
      "ds_bpermute_b32 v65, v54, v61\n"
      "ds_bpermute_b32 v66, v54, v62\n"
      "ds_bpermute_b32 v67, v54, v63\n"
      "v_mfma_f32_16x16x32_bf16 v[136:139], a[16:19], v[56:59], v[136:139]\n"
      "v_mfma_f32_16x16x32_bf16 v[144:147], v[56:59], a[88:91], v[144:147]\n"
      "v_mfma_f32_16x16x32_bf16 v[148:151], a[160:163], v[56:59], v[148:151]\n"
      "s_add_u32 m0, s43, 65536\n"
      "s_nop 0\n"
      "buffer_load_dwordx4 v48, s[12:15], s51 offen offset:0 lds\n"
      "buffer_load_dwordx4 v49, s[12:15], s51 offen offset:1024 lds\n"
      "buffer_load_dwordx4 v50, s[12:15], s51 offen offset:2048 lds\n"
      "buffer_load_dwordx4 v51, s[12:15], s51 offen offset:3072 lds\n"
      "s_add_u32 s51, s51, 110592\n"
      "v_and_b32 v72, v55, v72\n"
      "v_and_b32 v73, v55, v73\n"
      "v_and_b32 v74, v55, v74\n"
      "v_and_b32 v75, v55, v75\n"
      "v_accvgpr_write_b32 a228, v72\n"
      "v_accvgpr_write_b32 a229, v73\n"
      "v_accvgpr_write_b32 a230, v74\n"
      "v_accvgpr_write_b32 a231, v75\n"
      "v_mfma_f32_16x16x32_bf16 v[140:143], v[72:75], v[56:59], v[140:143]\n"
      "s_waitcnt vmcnt(8) lgkmcnt(0)\n"
      "s_barrier\n"
      "ds_read_b64_tr_b16 v[60:61], v53 offset:0\n"
      "ds_read_b64_tr_b16 v[62:63], v53 offset:1920\n"
      "ds_read_b64_tr_b16 a[24:25], v52 offset:144\n"
      "ds_read_b64_tr_b16 a[26:27], v52 offset:2064\n"
      "ds_read_b64_tr_b16 a[96:97], v52 offset:288\n"
      "ds_read_b64_tr_b16 a[98:99], v52 offset:2208\n"
      "ds_read_b64_tr_b16 a[168:169], v52 offset:0\n"
      "ds_read_b64_tr_b16 a[170:171], v52 offset:1920\n"
      "v_add_u32 v52, 16896, v52\n"
      "v_add_u32 v53, 16896, v53\n"
      "ds_bpermute_b32 v72, v54, v68\n"
      "ds_bpermute_b32 v73, v54, v69\n"
      "ds_bpermute_b32 v74, v54, v70\n"
      "ds_bpermute_b32 v75, v54, v71\n"
      "v_mfma_f32_16x16x32_bf16 v[136:139], a[20:23], v[56:59], v[136:139]\n"
      "v_mfma_f32_16x16x32_bf16 v[144:147], v[56:59], a[92:95], v[144:147]\n"
      "v_mfma_f32_16x16x32_bf16 v[148:151], a[164:167], v[56:59], v[148:151]\n"
      "s_add_u32 m0, s43, 82432\n"
      "s_nop 0\n"
      "buffer_load_dwordx4 v48, s[12:15], s51 offen offset:0 lds\n"
      "buffer_load_dwordx4 v49, s[12:15], s51 offen offset:1024 lds\n"
      "buffer_load_dwordx4 v50, s[12:15], s51 offen offset:2048 lds\n"
      "buffer_load_dwordx4 v51, s[12:15], s51 offen offset:3072 lds\n"
      "s_add_u32 s51, s51, 110592\n"
      "v_and_b32 v64, v55, v64\n"
      "v_and_b32 v65, v55, v65\n"
      "v_and_b32 v66, v55, v66\n"
      "v_and_b32 v67, v55, v67\n"
      "v_accvgpr_write_b32 a232, v64\n"
      "v_accvgpr_write_b32 a233, v65\n"
      "v_accvgpr_write_b32 a234, v66\n"
      "v_accvgpr_write_b32 a235, v67\n"
      "v_mfma_f32_16x16x32_bf16 v[140:143], v[64:67], v[56:59], v[140:143]\n"
      "s_waitcnt vmcnt(8) lgkmcnt(0)\n"
      "s_barrier\n"
      "ds_read_b64_tr_b16 v[68:69], v53 offset:0\n"
      "ds_read_b64_tr_b16 v[70:71], v53 offset:1920\n"
      "ds_read_b64_tr_b16 a[28:29], v52 offset:144\n"
      "ds_read_b64_tr_b16 a[30:31], v52 offset:2064\n"
      "ds_read_b64_tr_b16 a[100:101], v52 offset:288\n"
      "ds_read_b64_tr_b16 a[102:103], v52 offset:2208\n"
      "ds_read_b64_tr_b16 a[172:173], v52 offset:0\n"
      "ds_read_b64_tr_b16 a[174:175], v52 offset:1920\n"
      "v_add_u32 v52, 4294916608, v52\n"
      "v_add_u32 v53, 4294916608, v53\n"
      "ds_bpermute_b32 v64, v54, v60\n"
      "ds_bpermute_b32 v65, v54, v61\n"
      "ds_bpermute_b32 v66, v54, v62\n"
      "ds_bpermute_b32 v67, v54, v63\n"
      "v_mfma_f32_16x16x32_bf16 v[136:139], a[24:27], v[56:59], v[136:139]\n"
      "v_mfma_f32_16x16x32_bf16 v[144:147], v[56:59], a[96:99], v[144:147]\n"
      "v_mfma_f32_16x16x32_bf16 v[148:151], a[168:171], v[56:59], v[148:151]\n"
      "s_add_u32 m0, s43, 99328\n"
      "s_nop 0\n"
      "buffer_load_dwordx4 v48, s[12:15], s51 offen offset:0 lds\n"
      "buffer_load_dwordx4 v49, s[12:15], s51 offen offset:1024 lds\n"
      "buffer_load_dwordx4 v50, s[12:15], s51 offen offset:2048 lds\n"
      "buffer_load_dwordx4 v51, s[12:15], s51 offen offset:3072 lds\n"
      "s_add_u32 s51, s51, 110592\n"
      "v_and_b32 v72, v55, v72\n"
      "v_and_b32 v73, v55, v73\n"
      "v_and_b32 v74, v55, v74\n"
      "v_and_b32 v75, v55, v75\n"
      "v_accvgpr_write_b32 a236, v72\n"
      "v_accvgpr_write_b32 a237, v73\n"
      "v_accvgpr_write_b32 a238, v74\n"
      "v_accvgpr_write_b32 a239, v75\n"
      "v_mfma_f32_16x16x32_bf16 v[140:143], v[72:75], v[56:59], v[140:143]\n"
      "s_waitcnt vmcnt(8) lgkmcnt(0)\n"
      "s_barrier\n"
      "ds_read_b64_tr_b16 v[60:61], v53 offset:0\n"
      "ds_read_b64_tr_b16 v[62:63], v53 offset:1920\n"
      "ds_read_b64_tr_b16 a[32:33], v52 offset:144\n"
      "ds_read_b64_tr_b16 a[34:35], v52 offset:2064\n"
      "ds_read_b64_tr_b16 a[104:105], v52 offset:288\n"
      "ds_read_b64_tr_b16 a[106:107], v52 offset:2208\n"
      "ds_read_b64_tr_b16 a[176:177], v52 offset:0\n"
      "ds_read_b64_tr_b16 a[178:179], v52 offset:1920\n"
      "v_add_u32 v52, 16896, v52\n"
      "v_add_u32 v53, 16896, v53\n"
      "ds_bpermute_b32 v72, v54, v68\n"
      "ds_bpermute_b32 v73, v54, v69\n"
      "ds_bpermute_b32 v74, v54, v70\n"
      "ds_bpermute_b32 v75, v54, v71\n"
      "v_mfma_f32_16x16x32_bf16 v[136:139], a[28:31], v[56:59], v[136:139]\n"
      "v_mfma_f32_16x16x32_bf16 v[144:147], v[56:59], a[100:103], v[144:147]\n"
      "v_mfma_f32_16x16x32_bf16 v[148:151], a[172:175], v[56:59], v[148:151]\n"
      "s_add_u32 m0, s43, 116224\n"
      "s_nop 0\n"
      "buffer_load_dwordx4 v48, s[12:15], s51 offen offset:0 lds\n"
      "buffer_load_dwordx4 v49, s[12:15], s51 offen offset:1024 lds\n"
      "buffer_load_dwordx4 v50, s[12:15], s51 offen offset:2048 lds\n"
      "buffer_load_dwordx4 v51, s[12:15], s51 offen offset:3072 lds\n"
      "s_add_u32 s51, s51, 110592\n"
      "v_and_b32 v64, v55, v64\n"
      "v_and_b32 v65, v55, v65\n"
      "v_and_b32 v66, v55, v66\n"
      "v_and_b32 v67, v55, v67\n"
      "v_accvgpr_write_b32 a240, v64\n"
      "v_accvgpr_write_b32 a241, v65\n"
      "v_accvgpr_write_b32 a242, v66\n"
      "v_accvgpr_write_b32 a243, v67\n"
      "v_mfma_f32_16x16x32_bf16 v[140:143], v[64:67], v[56:59], v[140:143]\n"
      "s_waitcnt vmcnt(8) lgkmcnt(0)\n"
      "s_barrier\n"
      "ds_read_b64_tr_b16 v[68:69], v53 offset:0\n"
      "ds_read_b64_tr_b16 v[70:71], v53 offset:1920\n"
      "ds_read_b64_tr_b16 a[36:37], v52 offset:144\n"
      "ds_read_b64_tr_b16 a[38:39], v52 offset:2064\n"
      "ds_read_b64_tr_b16 a[108:109], v52 offset:288\n"
      "ds_read_b64_tr_b16 a[110:111], v52 offset:2208\n"
      "ds_read_b64_tr_b16 a[180:181], v52 offset:0\n"
      "ds_read_b64_tr_b16 a[182:183], v52 offset:1920\n"
      "v_add_u32 v52, 16896, v52\n"
      "v_add_u32 v53, 16896, v53\n"
      "ds_bpermute_b32 v64, v54, v60\n"
      "ds_bpermute_b32 v65, v54, v61\n"
      "ds_bpermute_b32 v66, v54, v62\n"
      "ds_bpermute_b32 v67, v54, v63\n"
      "v_mfma_f32_16x16x32_bf16 v[136:139], a[32:35], v[56:59], v[136:139]\n"
      "v_mfma_f32_16x16x32_bf16 v[144:147], v[56:59], a[104:107], v[144:147]\n"
      "v_mfma_f32_16x16x32_bf16 v[148:151], a[176:179], v[56:59], v[148:151]\n"
      "s_add_u32 m0, s43, 65536\n"
      "s_nop 0\n"
      "buffer_load_dwordx4 v48, s[12:15], s51 offen offset:0 lds\n"
      "buffer_load_dwordx4 v49, s[12:15], s51 offen offset:1024 lds\n"
      "buffer_load_dwordx4 v50, s[12:15], s51 offen offset:2048 lds\n"
      "buffer_load_dwordx4 v51, s[12:15], s51 offen offset:3072 lds\n"
      "s_add_u32 s51, s51, 110592\n"
      "v_and_b32 v72, v55, v72\n"
      "v_and_b32 v73, v55, v73\n"
      "v_and_b32 v74, v55, v74\n"
      "v_and_b32 v75, v55, v75\n"
      "v_accvgpr_write_b32 a244, v72\n"
      "v_accvgpr_write_b32 a245, v73\n"
      "v_accvgpr_write_b32 a246, v74\n"
      "v_accvgpr_write_b32 a247, v75\n"
      "v_mfma_f32_16x16x32_bf16 v[140:143], v[72:75], v[56:59], v[140:143]\n"
      "s_waitcnt vmcnt(8) lgkmcnt(0)\n"
      "s_barrier\n"
      "ds_read_b64_tr_b16 v[60:61], v53 offset:0\n"
      "ds_read_b64_tr_b16 v[62:63], v53 offset:1920\n"
      "ds_read_b64_tr_b16 a[40:41], v52 offset:144\n"
      "ds_read_b64_tr_b16 a[42:43], v52 offset:2064\n"
      "ds_read_b64_tr_b16 a[112:113], v52 offset:288\n"
      "ds_read_b64_tr_b16 a[114:115], v52 offset:2208\n"
      "ds_read_b64_tr_b16 a[184:185], v52 offset:0\n"
      "ds_read_b64_tr_b16 a[186:187], v52 offset:1920\n"
      "v_add_u32 v52, 16896, v52\n"
      "v_add_u32 v53, 16896, v53\n"
      "ds_bpermute_b32 v72, v54, v68\n"
      "ds_bpermute_b32 v73, v54, v69\n"
      "ds_bpermute_b32 v74, v54, v70\n"
      "ds_bpermute_b32 v75, v54, v71\n"
      "v_mfma_f32_16x16x32_bf16 v[136:139], a[36:39], v[56:59], v[136:139]\n"
      "v_mfma_f32_16x16x32_bf16 v[144:147], v[56:59], a[108:111], v[144:147]\n"
      "v_mfma_f32_16x16x32_bf16 v[148:151], a[180:183], v[56:59], v[148:151]\n"
      "s_add_u32 m0, s43, 82432\n"
      "s_nop 0\n"
      "buffer_load_dwordx4 v48, s[12:15], s51 offen offset:0 lds\n"
      "buffer_load_dwordx4 v49, s[12:15], s51 offen offset:1024 lds\n"
      "buffer_load_dwordx4 v50, s[12:15], s51 offen offset:2048 lds\n"
      "buffer_load_dwordx4 v51, s[12:15], s51 offen offset:3072 lds\n"
      "s_add_u32 s51, s51, 110592\n"
      "v_and_b32 v64, v55, v64\n"
      "v_and_b32 v65, v55, v65\n"
      "v_and_b32 v66, v55, v66\n"
      "v_and_b32 v67, v55, v67\n"
      "v_accvgpr_write_b32 a248, v64\n"
      "v_accvgpr_write_b32 a249, v65\n"
      "v_accvgpr_write_b32 a250, v66\n"
      "v_accvgpr_write_b32 a251, v67\n"
      "v_mfma_f32_16x16x32_bf16 v[140:143], v[64:67], v[56:59], v[140:143]\n"
      "s_waitcnt vmcnt(8) lgkmcnt(0)\n"
      "s_barrier\n"
      "ds_read_b64_tr_b16 v[68:69], v53 offset:0\n"
      "ds_read_b64_tr_b16 v[70:71], v53 offset:1920\n"
      "ds_read_b64_tr_b16 a[44:45], v52 offset:144\n"
      "ds_read_b64_tr_b16 a[46:47], v52 offset:2064\n"
      "ds_read_b64_tr_b16 a[116:117], v52 offset:288\n"
      "ds_read_b64_tr_b16 a[118:119], v52 offset:2208\n"
      "ds_read_b64_tr_b16 a[188:189], v52 offset:0\n"
      "ds_read_b64_tr_b16 a[190:191], v52 offset:1920\n"
      "v_add_u32 v52, 4294916608, v52\n"
      "v_add_u32 v53, 4294916608, v53\n"
      "ds_bpermute_b32 v64, v54, v60\n"
      "ds_bpermute_b32 v65, v54, v61\n"
      "ds_bpermute_b32 v66, v54, v62\n"
      "ds_bpermute_b32 v67, v54, v63\n"
      "v_mfma_f32_16x16x32_bf16 v[136:139], a[40:43], v[56:59], v[136:139]\n"
      "v_mfma_f32_16x16x32_bf16 v[144:147], v[56:59], a[112:115], v[144:147]\n"
      "v_mfma_f32_16x16x32_bf16 v[148:151], a[184:187], v[56:59], v[148:151]\n"
      "s_add_u32 m0, s43, 99328\n"
      "s_nop 0\n"
      "buffer_load_dwordx4 v48, s[12:15], s51 offen offset:0 lds\n"
      "buffer_load_dwordx4 v49, s[12:15], s51 offen offset:1024 lds\n"
      "buffer_load_dwordx4 v50, s[12:15], s51 offen offset:2048 lds\n"
      "buffer_load_dwordx4 v51, s[12:15], s51 offen offset:3072 lds\n"
      "s_add_u32 s51, s51, 110592\n"
      "v_and_b32 v72, v55, v72\n"
      "v_and_b32 v73, v55, v73\n"
      "v_and_b32 v74, v55, v74\n"
      "v_and_b32 v75, v55, v75\n"
      "v_accvgpr_write_b32 a252, v72\n"
      "v_accvgpr_write_b32 a253, v73\n"
      "v_accvgpr_write_b32 a254, v74\n"
      "v_accvgpr_write_b32 a255, v75\n"
      "v_mfma_f32_16x16x32_bf16 v[140:143], v[72:75], v[56:59], v[140:143]\n"
      "s_waitcnt vmcnt(8) lgkmcnt(0)\n"
      "s_barrier\n"
      "ds_read_b64_tr_b16 v[60:61], v53 offset:0\n"
      "ds_read_b64_tr_b16 v[62:63], v53 offset:1920\n"
      "ds_read_b64_tr_b16 a[48:49], v52 offset:144\n"
      "ds_read_b64_tr_b16 a[50:51], v52 offset:2064\n"
      "ds_read_b64_tr_b16 a[120:121], v52 offset:288\n"
      "ds_read_b64_tr_b16 a[122:123], v52 offset:2208\n"
      "ds_read_b64_tr_b16 a[192:193], v52 offset:0\n"
      "ds_read_b64_tr_b16 a[194:195], v52 offset:1920\n"
      "v_add_u32 v52, 16896, v52\n"
      "v_add_u32 v53, 16896, v53\n"
      "ds_bpermute_b32 v72, v54, v68\n"
      "ds_bpermute_b32 v73, v54, v69\n"
      "ds_bpermute_b32 v74, v54, v70\n"
      "ds_bpermute_b32 v75, v54, v71\n"
      "v_mfma_f32_16x16x32_bf16 v[136:139], a[44:47], v[56:59], v[136:139]\n"
      "v_mfma_f32_16x16x32_bf16 v[144:147], v[56:59], a[116:119], v[144:147]\n"
      "v_mfma_f32_16x16x32_bf16 v[148:151], a[188:191], v[56:59], v[148:151]\n"
      "s_add_u32 m0, s43, 116224\n"
      "s_nop 0\n"
      "buffer_load_dwordx4 v48, s[12:15], s51 offen offset:0 lds\n"
      "buffer_load_dwordx4 v49, s[12:15], s51 offen offset:1024 lds\n"
      "buffer_load_dwordx4 v50, s[12:15], s51 offen offset:2048 lds\n"
      "buffer_load_dwordx4 v51, s[12:15], s51 offen offset:3072 lds\n"
      "s_add_u32 s51, s51, 110592\n"
      "v_and_b32 v64, v55, v64\n"
      "v_and_b32 v65, v55, v65\n"
      "v_and_b32 v66, v55, v66\n"
      "v_and_b32 v67, v55, v67\n"
      "v_mov_b32 v16, v64\n"
      "v_mov_b32 v17, v65\n"
      "v_mov_b32 v18, v66\n"
      "v_mov_b32 v19, v67\n"
      "v_mfma_f32_16x16x32_bf16 v[140:143], v[64:67], v[56:59], v[140:143]\n"
      "s_add_u32 s30, s36, 24576\n"
      "buffer_load_dword v78, v2, s[8:11], s30 offen sc1\n"
      "s_add_u32 s30, s36, 49152\n"
      "buffer_load_dword v79, v2, s[8:11], s30 offen sc1\n"
      "s_waitcnt vmcnt(10) lgkmcnt(0)\n"
      "s_barrier\n"
      "ds_read_b64_tr_b16 v[68:69], v53 offset:0\n"
      "ds_read_b64_tr_b16 v[70:71], v53 offset:1920\n"
      "ds_read_b64_tr_b16 a[52:53], v52 offset:144\n"
      "ds_read_b64_tr_b16 a[54:55], v52 offset:2064\n"
      "ds_read_b64_tr_b16 a[124:125], v52 offset:288\n"
      "ds_read_b64_tr_b16 a[126:127], v52 offset:2208\n"
      "ds_read_b64_tr_b16 a[196:197], v52 offset:0\n"
      "ds_read_b64_tr_b16 a[198:199], v52 offset:1920\n"
      "v_add_u32 v52, 16896, v52\n"
      "v_add_u32 v53, 16896, v53\n"
      "ds_bpermute_b32 v64, v54, v60\n"
      "ds_bpermute_b32 v65, v54, v61\n"
      "ds_bpermute_b32 v66, v54, v62\n"
      "ds_bpermute_b32 v67, v54, v63\n"
      "v_mfma_f32_16x16x32_bf16 v[136:139], a[48:51], v[56:59], v[136:139]\n"
      "v_mfma_f32_16x16x32_bf16 v[144:147], v[56:59], a[120:123], v[144:147]\n"
      "v_mfma_f32_16x16x32_bf16 v[148:151], a[192:195], v[56:59], v[148:151]\n"
      "s_add_u32 m0, s43, 65536\n"
      "s_nop 0\n"
      "buffer_load_dwordx4 v48, s[12:15], s51 offen offset:0 lds\n"
      "buffer_load_dwordx4 v49, s[12:15], s51 offen offset:1024 lds\n"
      "buffer_load_dwordx4 v50, s[12:15], s51 offen offset:2048 lds\n"
      "buffer_load_dwordx4 v51, s[12:15], s51 offen offset:3072 lds\n"
      "s_add_u32 s51, s51, 110592\n"
      "v_and_b32 v72, v55, v72\n"
      "v_and_b32 v73, v55, v73\n"
      "v_and_b32 v74, v55, v74\n"
      "v_and_b32 v75, v55, v75\n"
      "v_mov_b32 v20, v72\n"
      "v_mov_b32 v21, v73\n"
      "v_mov_b32 v22, v74\n"
      "v_mov_b32 v23, v75\n"
      "v_mfma_f32_16x16x32_bf16 v[140:143], v[72:75], v[56:59], v[140:143]\n"
      "s_waitcnt vmcnt(10) lgkmcnt(0)\n"
      "s_barrier\n"
      "ds_read_b64_tr_b16 v[60:61], v53 offset:0\n"
      "ds_read_b64_tr_b16 v[62:63], v53 offset:1920\n"
      "ds_read_b64_tr_b16 a[56:57], v52 offset:144\n"
      "ds_read_b64_tr_b16 a[58:59], v52 offset:2064\n"
      "ds_read_b64_tr_b16 a[128:129], v52 offset:288\n"
      "ds_read_b64_tr_b16 a[130:131], v52 offset:2208\n"
      "ds_read_b64_tr_b16 a[200:201], v52 offset:0\n"
      "ds_read_b64_tr_b16 a[202:203], v52 offset:1920\n"
      "v_add_u32 v52, 16896, v52\n"
      "v_add_u32 v53, 16896, v53\n"
      "ds_bpermute_b32 v72, v54, v68\n"
      "ds_bpermute_b32 v73, v54, v69\n"
      "ds_bpermute_b32 v74, v54, v70\n"
      "ds_bpermute_b32 v75, v54, v71\n"
      "v_mfma_f32_16x16x32_bf16 v[136:139], a[52:55], v[56:59], v[136:139]\n"
      "v_mfma_f32_16x16x32_bf16 v[144:147], v[56:59], a[124:127], v[144:147]\n"
      "v_mfma_f32_16x16x32_bf16 v[148:151], a[196:199], v[56:59], v[148:151]\n"
      "s_add_u32 m0, s43, 82432\n"
      "s_nop 0\n"
      "buffer_load_dwordx4 v48, s[12:15], s51 offen offset:0 lds\n"
      "buffer_load_dwordx4 v49, s[12:15], s51 offen offset:1024 lds\n"
      "buffer_load_dwordx4 v50, s[12:15], s51 offen offset:2048 lds\n"
      "buffer_load_dwordx4 v51, s[12:15], s51 offen offset:3072 lds\n"
      "s_add_u32 s51, s51, 110592\n"
      "v_and_b32 v64, v55, v64\n"
      "v_and_b32 v65, v55, v65\n"
      "v_and_b32 v66, v55, v66\n"
      "v_and_b32 v67, v55, v67\n"
      "v_mov_b32 v24, v64\n"
      "v_mov_b32 v25, v65\n"
      "v_mov_b32 v26, v66\n"
      "v_mov_b32 v27, v67\n"
      "v_mfma_f32_16x16x32_bf16 v[140:143], v[64:67], v[56:59], v[140:143]\n"
      "s_mul_i32 s21, s21, 1664525\n"
      "s_add_u32 s21, s21, 1013904223\n"
      "v_mov_b32 v13, s21\n"
      "buffer_store_dword v13, v10, s[8:11], 0 offen sc1\n"
      "v_not_b32 v80, v13\n"
      "v_not_b32 v81, v13\n"
      "v_not_b32 v82, v13\n"
      "v_not_b32 v83, v13\n"
      "s_add_u32 s30, s40, 0\n"
      "buffer_store_dwordx4 v[80:83], v76, s[8:11], s30 offen sc1\n"
      "v_xor_b32 v13, v13, v14\n"
      "s_add_u32 s52, s40, 4294893568\n"
      "s_add_u32 s40, s40, 4294918144\n"
      "s_add_u32 s36, s36, 24576\n"
      "s_mov_b32 s45, s36\n"
      "s_min_u32 s45, s45, s6\n"
      "s_waitcnt vmcnt(11)\n"
      "v_cmp_ne_u32 vcc, s21, v78\n"
      "s_add_u32 m0, s41, 0\n"
      "s_add_u32 s36, s36, 24576\n"
      "s_mov_b32 s46, s45\n"
      "s_cbranch_vccnz .Lsp_q0_1\n"
      ".Lbk_q0_1:\n"
      "buffer_load_dwordx4 v0, s[8:11], s46 offen offset:1024 sc1 lds\n"
      "buffer_load_dwordx4 v0, s[8:11], s46 offen offset:0 sc1 lds\n"
      "buffer_load_dwordx4 v0, s[8:11], s46 offen offset:2048 sc1 lds\n"
      "s_mov_b32 s45, s36\n"
      "s_min_u32 s45, s45, s6\n"
      "buffer_load_dwordx4 v0, s[8:11], s46 offen offset:3072 sc1 lds\n"
      "s_add_u32 m0, s42, 0\n"
      "s_nop 0\n"
      "buffer_load_dwordx4 v1, s[8:11], s46 offen offset:0 sc1 lds\n"
      "s_waitcnt lgkmcnt(0)\n"
      "s_barrier\n"
      "ds_read_b64_tr_b16 v[68:69], v53 offset:0\n"
      "ds_read_b64_tr_b16 v[70:71], v53 offset:1920\n"
      "ds_read_b64_tr_b16 a[60:61], v52 offset:144\n"
      "ds_read_b64_tr_b16 a[62:63], v52 offset:2064\n"
      "ds_read_b64_tr_b16 a[132:133], v52 offset:288\n"
      "ds_read_b64_tr_b16 a[134:135], v52 offset:2208\n"
      "ds_read_b64_tr_b16 a[204:205], v52 offset:0\n"
      "ds_read_b64_tr_b16 a[206:207], v52 offset:1920\n"
      "v_add_u32 v52, 4294916608, v52\n"
      "v_add_u32 v53, 4294916608, v53\n"
      "ds_bpermute_b32 v64, v54, v60\n"
      "ds_bpermute_b32 v65, v54, v61\n"
      "ds_bpermute_b32 v66, v54, v62\n"
      "ds_bpermute_b32 v67, v54, v63\n"
      "v_mfma_f32_16x16x32_bf16 v[136:139], a[56:59], v[56:59], v[136:139]\n"
      "v_mfma_f32_16x16x32_bf16 v[144:147], v[56:59], a[128:131], v[144:147]\n"
      "v_mfma_f32_16x16x32_bf16 v[148:151], a[200:203], v[56:59], v[148:151]\n"
      "v_and_b32 v72, v55, v72\n"
      "v_and_b32 v73, v55, v73\n"
      "v_and_b32 v74, v55, v74\n"
      "v_and_b32 v75, v55, v75\n"
      "v_mov_b32 v28, v72\n"
      "v_mov_b32 v29, v73\n"
      "v_mov_b32 v30, v74\n"
      "v_mov_b32 v31, v75\n"
      "v_mfma_f32_16x16x32_bf16 v[140:143], v[72:75], v[56:59], v[140:143]\n"
      "s_waitcnt vmcnt(11) lgkmcnt(0)\n"
      "s_barrier\n"
      "ds_read_b64_tr_b16 v[60:61], v53 offset:0\n"
      "ds_read_b64_tr_b16 v[62:63], v53 offset:1920\n"
      "ds_read_b64_tr_b16 a[64:65], v52 offset:144\n"
      "ds_read_b64_tr_b16 a[66:67], v52 offset:2064\n"
      "ds_read_b64_tr_b16 a[136:137], v52 offset:288\n"
      "ds_read_b64_tr_b16 a[138:139], v52 offset:2208\n"
      "ds_read_b64_tr_b16 a[208:209], v52 offset:0\n"
      "ds_read_b64_tr_b16 a[210:211], v52 offset:1920\n"
      "v_add_u32 v52, 16896, v52\n"
      "v_add_u32 v53, 16896, v53\n"
      "ds_bpermute_b32 v72, v54, v68\n"
      "ds_bpermute_b32 v73, v54, v69\n"
      "ds_bpermute_b32 v74, v54, v70\n"
      "ds_bpermute_b32 v75, v54, v71\n"
      "v_mfma_f32_16x16x32_bf16 v[136:139], a[60:63], v[56:59], v[136:139]\n"
      "v_mfma_f32_16x16x32_bf16 v[144:147], v[56:59], a[132:135], v[144:147]\n"
      "v_mfma_f32_16x16x32_bf16 v[148:151], a[204:207], v[56:59], v[148:151]\n"
      "v_and_b32 v64, v55, v64\n"
      "v_and_b32 v65, v55, v65\n"
      "v_and_b32 v66, v55, v66\n"
      "v_and_b32 v67, v55, v67\n"
      "v_mov_b32 v32, v64\n"
      "v_mov_b32 v33, v65\n"
      "v_mov_b32 v34, v66\n"
      "v_mov_b32 v35, v67\n"
      "v_mfma_f32_16x16x32_bf16 v[140:143], v[64:67], v[56:59], v[140:143]\n"
      "s_waitcnt vmcnt(7) lgkmcnt(0)\n"
      "s_barrier\n"
      "ds_read_b64_tr_b16 v[68:69], v53 offset:0\n"
      "ds_read_b64_tr_b16 v[70:71], v53 offset:1920\n"
      "ds_read_b64_tr_b16 a[68:69], v52 offset:144\n"
      "ds_read_b64_tr_b16 a[70:71], v52 offset:2064\n"
      "ds_read_b64_tr_b16 a[140:141], v52 offset:288\n"
      "ds_read_b64_tr_b16 a[142:143], v52 offset:2208\n"
      "ds_read_b64_tr_b16 a[212:213], v52 offset:0\n"
      "ds_read_b64_tr_b16 a[214:215], v52 offset:1920\n"
      "v_add_u32 v52, 16896, v52\n"
      "v_add_u32 v53, 16896, v53\n"
      "ds_bpermute_b32 v64, v54, v60\n"
      "ds_bpermute_b32 v65, v54, v61\n"
      "ds_bpermute_b32 v66, v54, v62\n"
      "ds_bpermute_b32 v67, v54, v63\n"
      "v_mfma_f32_16x16x32_bf16 v[136:139], a[64:67], v[56:59], v[136:139]\n"
      "v_mfma_f32_16x16x32_bf16 v[144:147], v[56:59], a[136:139], v[144:147]\n"
      "v_mfma_f32_16x16x32_bf16 v[148:151], a[208:211], v[56:59], v[148:151]\n"
      "v_and_b32 v72, v55, v72\n"
      "v_and_b32 v73, v55, v73\n"
      "v_and_b32 v74, v55, v74\n"
      "v_and_b32 v75, v55, v75\n"
      "v_mov_b32 v36, v72\n"
      "v_mov_b32 v37, v73\n"
      "v_mov_b32 v38, v74\n"
      "v_mov_b32 v39, v75\n"
      "v_mfma_f32_16x16x32_bf16 v[140:143], v[72:75], v[56:59], v[140:143]\n"
      "s_waitcnt lgkmcnt(0)\n"
      "ds_bpermute_b32 v72, v54, v68\n"
      "ds_bpermute_b32 v73, v54, v69\n"
      "ds_bpermute_b32 v74, v54, v70\n"
      "ds_bpermute_b32 v75, v54, v71\n"
      "v_mfma_f32_16x16x32_bf16 v[136:139], a[68:71], v[56:59], v[136:139]\n"
      "v_mfma_f32_16x16x32_bf16 v[144:147], v[56:59], a[140:143], v[144:147]\n"
      "v_mfma_f32_16x16x32_bf16 v[148:151], a[212:215], v[56:59], v[148:151]\n"
      "v_and_b32 v64, v55, v64\n"
      "v_and_b32 v65, v55, v65\n"
      "v_and_b32 v66, v55, v66\n"
      "v_and_b32 v67, v55, v67\n"
      "v_mov_b32 v40, v64\n"
      "v_mov_b32 v41, v65\n"
      "v_mov_b32 v42, v66\n"
      "v_mov_b32 v43, v67\n"
      "v_mfma_f32_16x16x32_bf16 v[140:143], v[64:67], v[56:59], v[140:143]\n"
      "s_waitcnt lgkmcnt(0)\n"
      "v_and_b32 v72, v55, v72\n"
      "v_and_b32 v73, v55, v73\n"
      "v_and_b32 v74, v55, v74\n"
      "v_and_b32 v75, v55, v75\n"
      "v_mov_b32 v44, v72\n"
      "v_mov_b32 v45, v73\n"
      "v_mov_b32 v46, v74\n"
      "v_mov_b32 v47, v75\n"
      "v_mfma_f32_16x16x32_bf16 v[140:143], v[72:75], v[56:59], v[140:143]\n"
      "s_add_u32 s36, s36, 24576\n"
      "s_cmp_eq_u32 s36, s37\n"
      "s_cselect_b32 s47, s38, 0\n"
      "s_cselect_b32 s37, s48, s37\n"
      "s_cselect_b32 s49, s39, s53\n"
      "s_add_u32 m0, s41, 20480\n"
      "s_mov_b32 s46, s45\n"
      "s_add_u32 s45, s36, s47\n"
      "v_add_u32 v9, 1, v9\n"
      "v_cmp_ne_u32 vcc, s21, v79\n"
      "s_min_u32 s45, s45, s6\n"
      "s_cbranch_vccnz .Lsp_q1_2\n"
      ".Lbk_q1_2:\n"
      "buffer_load_dwordx4 v0, s[8:11], s46 offen offset:1024 sc1 lds\n"
      "s_waitcnt vmcnt(1)\n"
      "ds_write_b32 v11, v9\n"
      "buffer_load_dwordx4 v0, s[8:11], s46 offen offset:0 sc1 lds\n"
      "buffer_load_dwordx4 v0, s[8:11], s46 offen offset:3072 sc1 lds\n"
      "buffer_load_dword v15, v2, s[8:11], s45 offen sc1\n"
      "ds_read_b32 v10, v12\n"
      "buffer_load_dwordx4 v0, s[8:11], s46 offen offset:2048 sc1 lds\n"
      "s_waitcnt lgkmcnt(0)\n"
      "v_cmp_gt_i32 vcc, v9, v10\n"
      "s_add_u32 m0, s42, 20480\n"
      "s_nop 0\n"
      "buffer_load_dwordx4 v1, s[8:11], s46 offen offset:0 sc1 lds\n"
      "s_cbranch_vccnz .Lsp_qy0_3\n"
      ".Lbk_qy0_3:\n"
      "ds_read_b128 v[52:55], v0 offset:1024\n"
      "ds_read_b128 v[48:51], v0 offset:0\n"
      "ds_read_b128 v[56:59], v0 offset:2048\n"
      "ds_read_b128 v[68:71], v0 offset:5120\n"
      "ds_read_b128 v[64:67], v0 offset:4096\n"
      "ds_read_b128 v[60:63], v0 offset:3072\n"
      "buffer_load_dword v14, v11, s[8:11], s31 offen\n"
      "ds_read2st64_b64 v[212:215], v4 offset0:0 offset1:1\n"
      "ds_read_b128 v[72:75], v0 offset:6144\n"
      "ds_read_b128 v[76:79], v0 offset:7168\n"
      "ds_read_b128 v[88:91], v0 offset:10240\n"
      "ds_read_b128 v[108:111], v0 offset:15360\n"
      "s_mov_b32 s20, 21\n"
      "s_waitcnt lgkmcnt(0)\n"
      "s_nop 4\n"
      "s_branch .Lq_loop\n"
      ".p2align 12\n"
      ".Lq_p0:\n"
      "s_branch .Lq_pr\n"
      ".p2align 5\n"
      ".Lq_loop:\n"
      "v_mfma_f32_16x16x32_bf16 v[120:123], a[0:3], v[48:51], 0\n"
      "ds_read_b128 v[80:83], v0 offset:8192\n"
      "ds_read_b128 v[84:87], v0 offset:9216\n"
      "v_mfma_f32_16x16x32_bf16 v[120:123], a[4:7], v[52:55], v[120:123]\n"
      "ds_read_b128 v[96:99], v0 offset:12288\n"
      "v_mul_f32 v232, 0x3eae1a93, v219\n"
      "v_mfma_f32_16x16x32_bf16 v[120:123], a[8:11], v[56:59], v[120:123]\n"
      "s_waitcnt lgkmcnt(7)\n"
      "ds_read_b128 v[104:107], v0 offset:14336\n"
      "v_fma_f32 v135, -v218, v151, v135\n"
      "v_mfma_f32_16x16x32_bf16 v[120:123], a[12:15], v[60:63], v[120:123]\n"
      "v_fma_f32 v133, -v218, v149, v133\n"
      "v_fma_f32 v134, -v218, v150, v134\n"
      "v_mfma_f32_16x16x32_bf16 v[120:123], a[16:19], v[64:67], v[120:123]\n"
      "ds_read_b128 v[92:95], v0 offset:11264\n"
      "v_fma_f32 v132, -v218, v148, v132\n"
      "v_mfma_f32_16x16x32_bf16 v[120:123], a[20:23], v[68:71], v[120:123]\n"
      "s_waitcnt lgkmcnt(5)\n"
      "ds_read_b64 v[216:217], v4 offset:1024\n"
      "v_cvt_pk_bf16_f32 v176, v132, v133\n"
      "v_mfma_f32_16x16x32_bf16 v[120:123], a[24:27], v[72:75], v[120:123]\n"
      "ds_read_b128 v[100:103], v0 offset:13312\n"
      "v_cvt_pk_bf16_f32 v177, v134, v135\n"
      "v_mfma_f32_16x16x32_bf16 v[120:123], a[28:31], v[76:79], v[120:123]\n"
      "s_waitcnt lgkmcnt(5)\n"
      "v_rcp_f32 v210, v210\n"
      "v_mfma_f32_16x16x32_bf16 v[120:123], a[32:35], v[80:83], v[120:123]\n"
      "v_mul_f32 v204, v204, v210\n"
      "v_add_f32 v212, v212, v214\n"
      "v_mfma_f32_16x16x32_bf16 v[188:191], v[172:175], v[176:179], v[152:155]\n"
      "ds_read_b128 v[112:115], v0 offset:16384\n"
      "v_mfma_f32_16x16x32_bf16 v[120:123], a[36:39], v[84:87], v[120:123]\n"
      "ds_read_b128 v[116:119], v0 offset:17408\n"
      "v_mul_f32 v208, v208, v210\n"
      "v_mfma_f32_16x16x32_bf16 v[120:123], a[40:43], v[88:91], v[120:123]\n"
      "s_waitcnt lgkmcnt(4)\n"
      "s_add_u32 s36, s36, 24576\n"
      "v_mul_f32 v207, v207, v210\n"
      "v_mfma_f32_16x16x32_bf16 v[120:123], a[44:47], v[92:95], v[120:123]\n"
      "v_mul_f32 v209, v209, v210\n"
      "v_cvt_pk_bf16_f32 v230, v208, v209\n"
      "v_mfma_f32_16x16x32_bf16 v[120:123], a[48:51], v[96:99], v[120:123]\n"
      "s_waitcnt lgkmcnt(2)\n"
      "buffer_store_dwordx2 v[230:231], v8, s[8:11], s40 offen sc1\n"
      "v_mfma_f32_16x16x32_bf16 v[120:123], a[52:55], v[100:103], v[120:123]\n"
      "v_mul_f32 v206, v206, v210\n"
      "v_add_f32 v218, v212, v216\n"
      "v_mfma_f32_16x16x32_bf16 v[120:123], a[56:59], v[104:107], v[120:123]\n"
      "s_mov_b32 s46, s45\n"
      "v_add_f32 v213, v213, v215\n"
      "v_mfma_f32_16x16x32_bf16 v[120:123], a[60:63], v[108:111], v[120:123]\n"
      "s_waitcnt lgkmcnt(1)\n"
      "v_cvt_pk_bf16_f32 v229, v206, v207\n"
      "v_mfma_f32_16x16x32_bf16 v[120:123], a[64:67], v[112:115], v[120:123]\n"
      "s_waitcnt lgkmcnt(0)\n"
      "v_mul_f32 v205, v205, v210\n"
      "v_cvt_pk_bf16_f32 v228, v204, v205\n"
      "v_mfma_f32_16x16x32_bf16 v[120:123], a[68:71], v[116:119], v[120:123]\n"
      "buffer_store_dwordx2 v[228:229], v7, s[8:11], s40 offen sc1\n"
      "v_mfma_f32_16x16x32_bf16 v[124:127], a[216:219], v[48:51], 0\n"
      "v_add_f32 v213, v213, v217\n"
      "v_fma_f32 v213, -v218, v218, v213\n"
      "v_mfma_f32_16x16x32_bf16 v[124:127], a[220:223], v[52:55], v[124:127]\n"
      "v_max_f32 v213, 0x3727c5ac, v213\n"
      "s_mov_b32 s45, s36\n"
      "v_mfma_f32_16x16x32_bf16 v[124:127], a[224:227], v[56:59], v[124:127]\n"
      "v_mov_b32 v187, v185\n"
      "v_mfma_f32_16x16x32_bf16 v[124:127], a[228:231], v[60:63], v[124:127]\n"
      "v_rsq_f32 v219, v213\n"
      "v_mfma_f32_16x16x32_bf16 v[124:127], a[232:235], v[64:67], v[124:127]\n"
      "s_add_u32 m0, s41, 40960\n"
      "ds_write_b64 v5, v[218:219] offset:0\n"
      "v_mfma_f32_16x16x32_bf16 v[124:127], a[236:239], v[68:71], v[124:127]\n"
      "s_waitcnt vmcnt(5)\n"
      "v_mov_b32 v186, v184\n"
      "v_cmp_ne_u32 vcc, s21, v15\n"
      "v_mfma_f32_16x16x32_bf16 v[124:127], a[240:243], v[72:75], v[124:127]\n"
      "s_min_u32 s45, s45, s6\n"
      "v_mfma_f32_16x16x32_bf16 v[124:127], a[244:247], v[76:79], v[124:127]\n"
      "v_cndmask_b32 v233, v219, 1.0, s[54:55]\n"
      "v_add_u32 v9, 1, v9\n"
      "v_mfma_f32_16x16x32_bf16 v[124:127], a[248:251], v[80:83], v[124:127]\n"
      "v_fma_f32 v123, -v218, v139, v123\n"
      "v_fma_f32 v122, -v218, v138, v122\n"
      "v_mfma_f32_16x16x32_bf16 v[124:127], a[252:255], v[84:87], v[124:127]\n"
      "v_fma_f32 v120, -v218, v136, v120\n"
      "v_fma_f32 v121, -v218, v137, v121\n"
      "v_mfma_f32_16x16x32_bf16 v[124:127], v[16:19], v[88:91], v[124:127]\n"
      "ds_read_b128 v[224:227], v6 offset:16\n"
      "v_mul_f32 v121, v219, v121\n"
      "v_mfma_f32_16x16x32_bf16 v[124:127], v[20:23], v[92:95], v[124:127]\n"
      "v_mul_f32 v120, v219, v120\n"
      "v_mfma_f32_16x16x32_bf16 v[124:127], v[24:27], v[96:99], v[124:127]\n"
      "s_cbranch_vccnz .Lsp_q2_4\n"
      ".Lbk_q2_4:\n"
      "v_mfma_f32_16x16x32_bf16 v[124:127], v[28:31], v[100:103], v[124:127]\n"
      "buffer_load_dword v15, v2, s[8:11], s45 offen sc1\n"
      "v_mfma_f32_16x16x32_bf16 v[124:127], v[32:35], v[104:107], v[124:127]\n"
      "v_mul_f32 v123, v219, v123\n"
      "v_mul_f32 v122, v219, v122\n"
      "v_mfma_f32_16x16x32_bf16 v[124:127], v[36:39], v[108:111], v[124:127]\n"
      "s_waitcnt vmcnt(4) lgkmcnt(1)\n"
      "ds_write_b32 v11, v9\n"
      "v_mfma_f32_16x16x32_bf16 v[124:127], v[40:43], v[112:115], v[124:127]\n"
      "v_mov_b32 v182, v180\n"
      "v_mfma_f32_16x16x32_bf16 v[124:127], v[44:47], v[116:119], v[124:127]\n"
      "v_cvt_pk_bf16_f32 v173, v122, v123\n"
      "v_mfma_f32_16x16x32_bf16 v[128:131], v[48:51], a[72:75], 0\n"
      "buffer_load_dwordx4 v0, s[8:11], s46 offen offset:1024 sc1 lds\n"
      "v_mfma_f32_16x16x32_bf16 v[128:131], v[52:55], a[76:79], v[128:131]\n"
      "ds_read_b128 v[220:223], v6 offset:0\n"
      "v_cvt_pk_bf16_f32 v172, v120, v121\n"
      "v_mfma_f32_16x16x32_bf16 v[128:131], v[56:59], a[80:83], v[128:131]\n"
      "v_fma_f32 v126, -v218, v142, v126\n"
      "v_fma_f32 v124, -v218, v140, v124\n"
      "v_mfma_f32_16x16x32_bf16 v[128:131], v[60:63], a[84:87], v[128:131]\n"
      "s_add_u32 s52, s52, 24576\n"
      "v_fma_f32 v125, -v218, v141, v125\n"
      "v_mul_f32 v124, v233, v124\n"
      "v_mfma_f32_16x16x32_bf16 v[128:131], v[64:67], a[88:91], v[128:131]\n"
      "v_mul_f32 v126, v219, v126\n"
      "v_mul_f32 v125, v233, v125\n"
      "v_mfma_f32_16x16x32_bf16 v[128:131], v[68:71], a[92:95], v[128:131]\n"
      "v_fma_f32 v127, -v218, v143, v127\n"
      "v_cvt_pk_bf16_f32 v175, v124, v125\n"
      "v_mfma_f32_16x16x32_bf16 v[128:131], v[72:75], a[96:99], v[128:131]\n"
      "v_mul_f32 v127, v219, v127\n"
      "v_cvt_pk_bf16_f32 v174, v126, v127\n"
      "v_mfma_f32_16x16x32_bf16 v[128:131], v[76:79], a[100:103], v[128:131]\n"
      "buffer_load_dwordx4 v0, s[8:11], s46 offen offset:2048 sc1 lds\n"
      "v_mfma_f32_16x16x32_bf16 v[192:195], v[172:175], v[176:179], v[156:159]\n"
      "ds_read_b32 v10, v12\n"
      "v_mfma_f32_16x16x32_bf16 v[196:199], v[172:175], v[164:167], v[168:171]\n"
      "v_mov_b32 v183, v181\n"
      "v_mov_b32 v178, v175\n"
      "v_mfma_f32_16x16x32_bf16 v[128:131], v[80:83], a[104:107], v[128:131]\n"
      "buffer_load_dwordx4 v0, s[8:11], s46 offen offset:0 sc1 lds\n"
      "v_mfma_f32_16x16x32_bf16 v[128:131], v[84:87], a[108:111], v[128:131]\n"
      "v_max_f32 v235, v194, v195\n"
      "v_max_f32 v234, v192, v193\n"
      "v_mfma_f32_16x16x32_bf16 v[128:131], v[88:91], a[112:115], v[128:131]\n"
      "v_max3_f32 v234, v188, v189, v234\n"
      "v_max3_f32 v235, v190, v191, v235\n"
      "v_mfma_f32_16x16x32_bf16 v[128:131], v[92:95], a[116:119], v[128:131]\n"
      "v_max_f32 v236, v234, v235\n"
      "v_mov_b32 v237, v236\n"
      "v_mfma_f32_16x16x32_bf16 v[128:131], v[96:99], a[120:123], v[128:131]\n"
      "v_permlane16_swap_b32 v236, v237\n"
      "v_mfma_f32_16x16x32_bf16 v[128:131], v[100:103], a[124:127], v[128:131]\n"
      "v_max_f32 v236, v236, v237\n"
      "v_mov_b32 v237, v236\n"
      "v_mfma_f32_16x16x32_bf16 v[128:131], v[104:107], a[128:131], v[128:131]\n"
      "s_waitcnt lgkmcnt(0)\n"
      "v_permlane32_swap_b32 v236, v237\n"
      "v_cmp_gt_i32 vcc, v9, v10\n"
      "v_mfma_f32_16x16x32_bf16 v[128:131], v[108:111], a[132:135], v[128:131]\n"
      "buffer_load_dwordx4 v0, s[8:11], s46 offen offset:3072 sc1 lds\n"
      "v_mfma_f32_16x16x32_bf16 v[128:131], v[112:115], a[136:139], v[128:131]\n"
      "s_add_u32 m0, s42, 40960\n"
      "v_max_f32 v236, v236, v237\n"
      "v_mul_f32 v236, v236, v232\n"
      "v_mfma_f32_16x16x32_bf16 v[128:131], v[116:119], a[140:143], v[128:131]\n"
      "s_add_u32 s40, s40, 24576\n"
      "v_fma_f32 v234, v234, v232, -v236\n"
      "v_fma_f32 v235, v235, v232, -v236\n"
      "v_mfma_f32_16x16x32_bf16 v[132:135], a[144:147], v[48:51], 0\n"
      "v_exp_f32 v234, v234\n"
      "v_mfma_f32_16x16x32_bf16 v[132:135], a[148:151], v[52:55], v[132:135]\n"
      "s_cbranch_vccnz .Lsp_qy1_5\n"
      ".Lbk_qy1_5:\n"
      "ds_read_b128 v[48:51], v0 offset:20480\n"
      "v_mfma_f32_16x16x32_bf16 v[132:135], a[152:155], v[56:59], v[132:135]\n"
      "v_exp_f32 v235, v235\n"
      "v_mfma_f32_16x16x32_bf16 v[132:135], a[156:159], v[60:63], v[132:135]\n"
      "ds_read_b128 v[56:59], v0 offset:22528\n"
      "v_fma_f32 v130, -v224, v147, v130\n"
      "v_mfma_f32_16x16x32_bf16 v[132:135], a[160:163], v[64:67], v[132:135]\n"
      "ds_read_b64 v[216:217], v4 offset:21504\n"
      "v_fma_f32 v129, -v222, v146, v129\n"
      "v_mfma_f32_16x16x32_bf16 v[132:135], a[164:167], v[68:71], v[132:135]\n"
      "buffer_load_dwordx4 v1, s[8:11], s46 offen offset:0 sc1 lds\n"
      "v_mfma_f32_16x16x32_bf16 v[132:135], a[168:171], v[72:75], v[132:135]\n"
      "v_cvt_pk_bf16_f32 v234, v234, v234\n"
      "v_fma_f32 v131, -v226, v144, v131\n"
      "v_mfma_f32_16x16x32_bf16 v[132:135], a[172:175], v[76:79], v[132:135]\n"
      "ds_read_b128 v[60:63], v0 offset:23552\n"
      "v_and_b32 v200, v160, v234\n"
      "v_mfma_f32_16x16x32_bf16 v[132:135], a[176:179], v[80:83], v[132:135]\n"
      "v_cvt_pk_bf16_f32 v235, v235, v235\n"
      "v_mul_f32 v129, v223, v129\n"
      "v_mfma_f32_16x16x32_bf16 v[132:135], a[180:183], v[84:87], v[132:135]\n"
      "ds_read_b128 v[52:55], v0 offset:21504\n"
      "v_mul_f32 v131, v227, v131\n"
      "v_mfma_f32_16x16x32_bf16 v[132:135], a[184:187], v[88:91], v[132:135]\n"
      "ds_read_b128 v[88:91], v0 offset:30720\n"
      "v_mul_f32 v130, v225, v130\n"
      "v_cvt_pk_bf16_f32 v185, v198, v199\n"
      "v_mfma_f32_16x16x32_bf16 v[132:135], a[188:191], v[92:95], v[132:135]\n"
      "v_cvt_pk_bf16_f32 v184, v196, v197\n"
      "v_and_b32 v201, v161, v235\n"
      "v_fma_f32 v128, -v220, v145, v128\n"
      "v_mfma_f32_16x16x32_bf16 v[132:135], a[192:195], v[96:99], v[132:135]\n"
      "ds_read_b128 v[68:71], v0 offset:25600\n"
      "v_and_b32 v203, v163, v235\n"
      "v_mfma_f32_16x16x32_bf16 v[132:135], a[196:199], v[100:103], v[132:135]\n"
      "v_mul_f32 v128, v221, v128\n"
      "v_and_b32 v202, v162, v234\n"
      "v_mfma_f32_16x16x32_bf16 v[132:135], a[200:203], v[104:107], v[132:135]\n"
      "v_cvt_pk_bf16_f32 v180, v128, v129\n"
      "v_cvt_pk_bf16_f32 v181, v130, v131\n"
      "v_mfma_f32_16x16x32_bf16 v[208:211], v[184:187], v[200:203], 0\n"
      "ds_read_b128 v[64:67], v0 offset:24576\n"
      "ds_read_b128 v[72:75], v0 offset:26624\n"
      "v_mfma_f32_16x16x32_bf16 v[204:207], v[180:183], v[200:203], 0\n"
      "s_waitcnt vmcnt(6) lgkmcnt(4)\n"
      "buffer_store_dword v13, v3, s[8:11], s52 offen sc1\n"
      "v_mfma_f32_16x16x32_bf16 v[132:135], a[204:207], v[108:111], v[132:135]\n"
      "ds_read2st64_b64 v[212:215], v4 offset0:40 offset1:41\n"
      "v_mfma_f32_16x16x32_bf16 v[132:135], a[208:211], v[112:115], v[132:135]\n"
      "v_rcp_f32 v210, v210\n"
      "v_mfma_f32_16x16x32_bf16 v[132:135], a[212:215], v[116:119], v[132:135]\n"
      "ds_read_b128 v[76:79], v0 offset:27648\n"
      "v_mul_f32 v209, v209, v210\n"
      "v_mfma_f32_16x16x32_bf16 v[120:123], a[0:3], v[48:51], 0\n"
      "ds_read_b128 v[80:83], v0 offset:28672\n"
      "ds_read_b128 v[100:103], v0 offset:33792\n"
      "v_mfma_f32_16x16x32_bf16 v[120:123], a[4:7], v[52:55], v[120:123]\n"
      "ds_read_b128 v[92:95], v0 offset:31744\n"
      "v_mul_f32 v208, v208, v210\n"
      "v_mfma_f32_16x16x32_bf16 v[120:123], a[8:11], v[56:59], v[120:123]\n"
      "ds_read_b128 v[84:87], v0 offset:29696\n"
      "v_fma_f32 v134, -v218, v150, v134\n"
      "v_mfma_f32_16x16x32_bf16 v[120:123], a[12:15], v[60:63], v[120:123]\n"
      "s_waitcnt lgkmcnt(6)\n"
      "ds_read_b128 v[104:107], v0 offset:34816\n"
      "v_fma_f32 v132, -v218, v148, v132\n"
      "v_mfma_f32_16x16x32_bf16 v[120:123], a[16:19], v[64:67], v[120:123]\n"
      "s_mov_b32 s50, s49\n"
      "ds_read_b128 v[96:99], v0 offset:32768\n"
      "v_mfma_f32_16x16x32_bf16 v[120:123], a[20:23], v[68:71], v[120:123]\n"
      "ds_read_b128 v[108:111], v0 offset:35840\n"
      "v_fma_f32 v133, -v218, v149, v133\n"
      "v_mfma_f32_16x16x32_bf16 v[120:123], a[24:27], v[72:75], v[120:123]\n"
      "s_waitcnt lgkmcnt(7)\n"
      "v_fma_f32 v135, -v218, v151, v135\n"
      "v_cvt_pk_bf16_f32 v177, v134, v135\n"
      "v_mfma_f32_16x16x32_bf16 v[120:123], a[28:31], v[76:79], v[120:123]\n"
      "s_waitcnt lgkmcnt(5)\n"
      "ds_read_b128 v[112:115], v0 offset:36864\n"
      "v_cvt_pk_bf16_f32 v176, v132, v133\n"
      "v_mfma_f32_16x16x32_bf16 v[120:123], a[32:35], v[80:83], v[120:123]\n"
      "v_mov_b32 v187, v185\n"
      "v_mul_f32 v232, 0x3eae1a93, v219\n"
      "v_mfma_f32_16x16x32_bf16 v[188:191], v[172:175], v[176:179], v[152:155]\n"
      "s_waitcnt lgkmcnt(4)\n"
      "ds_read_b128 v[116:119], v0 offset:37888\n"
      "v_mul_f32 v207, v207, v210\n"
      "v_mfma_f32_16x16x32_bf16 v[120:123], a[36:39], v[84:87], v[120:123]\n"
      "v_mul_f32 v204, v204, v210\n"
      "v_mfma_f32_16x16x32_bf16 v[120:123], a[40:43], v[88:91], v[120:123]\n"
      "v_cvt_pk_bf16_f32 v230, v208, v209\n"
      "v_add_f32 v213, v213, v215\n"
      "v_mfma_f32_16x16x32_bf16 v[120:123], a[44:47], v[92:95], v[120:123]\n"
      "s_waitcnt lgkmcnt(2)\n"
      "v_mul_f32 v205, v205, v210\n"
      "v_mfma_f32_16x16x32_bf16 v[120:123], a[48:51], v[96:99], v[120:123]\n"
      "buffer_store_dwordx2 v[230:231], v8, s[8:11], s40 offen sc1\n"
      "v_mfma_f32_16x16x32_bf16 v[120:123], a[52:55], v[100:103], v[120:123]\n"
      "v_add_f32 v212, v212, v214\n"
      "v_mfma_f32_16x16x32_bf16 v[120:123], a[56:59], v[104:107], v[120:123]\n"
      "v_cvt_pk_bf16_f32 v228, v204, v205\n"
      "v_mul_f32 v206, v206, v210\n"
      "v_mfma_f32_16x16x32_bf16 v[120:123], a[60:63], v[108:111], v[120:123]\n"
      "s_waitcnt vmcnt(7) lgkmcnt(0)\n"
      "v_cvt_pk_bf16_f32 v229, v206, v207\n"
      "v_cmp_ne_u32 vcc, s21, v15\n"
      "v_mfma_f32_16x16x32_bf16 v[120:123], a[64:67], v[112:115], v[120:123]\n"
      "s_add_u32 m0, s41, 0\n"
      "v_mov_b32 v182, v180\n"
      "v_mfma_f32_16x16x32_bf16 v[120:123], a[68:71], v[116:119], v[120:123]\n"
      "buffer_store_dwordx2 v[228:229], v7, s[8:11], s40 offen sc1\n"
      "v_mfma_f32_16x16x32_bf16 v[124:127], a[216:219], v[48:51], 0\n"
      "v_add_f32 v218, v212, v216\n"
      "v_add_f32_e64 v213, v213, v217\n"
      "v_mfma_f32_16x16x32_bf16 v[124:127], a[220:223], v[52:55], v[124:127]\n"
      "s_add_u32 s36, s36, 24576\n"
      "v_fma_f32 v213, -v218, v218, v213\n"
      "v_mfma_f32_16x16x32_bf16 v[124:127], a[224:227], v[56:59], v[124:127]\n"
      "s_mov_b32 s46, s45\n"
      "s_cbranch_vccnz .Lsp_q0_6\n"
      ".Lbk_q0_6:\n"
      "v_mfma_f32_16x16x32_bf16 v[124:127], a[228:231], v[60:63], v[124:127]\n"
      "buffer_load_dwordx4 v0, s[8:11], s46 offen offset:1024 sc1 lds\n"
      "v_mfma_f32_16x16x32_bf16 v[124:127], a[232:235], v[64:67], v[124:127]\n"
      "v_max_f32 v213, 0x3727c5ac, v213\n"
      "v_fma_f32 v120, -v218, v136, v120\n"
      "v_mfma_f32_16x16x32_bf16 v[124:127], a[236:239], v[68:71], v[124:127]\n"
      "v_mov_b32 v186, v184\n"
      "v_mfma_f32_16x16x32_bf16 v[124:127], a[240:243], v[72:75], v[124:127]\n"
      "v_add_u32 v9, 1, v9\n"
      "v_mfma_f32_16x16x32_bf16 v[124:127], a[244:247], v[76:79], v[124:127]\n"
      "s_add_u32 s40, s40, 24576\n"
      "v_fma_f32 v123, -v218, v139, v123\n"
      "v_mfma_f32_16x16x32_bf16 v[124:127], a[248:251], v[80:83], v[124:127]\n"
      "v_rsq_f32 v219, v213\n"
      "v_mfma_f32_16x16x32_bf16 v[124:127], a[252:255], v[84:87], v[124:127]\n"
      "v_mfma_f32_16x16x32_bf16 v[124:127], v[16:19], v[88:91], v[124:127]\n"
      "v_fma_f32 v121, -v218, v137, v121\n"
      "v_fma_f32 v122, -v218, v138, v122\n"
      "v_mfma_f32_16x16x32_bf16 v[124:127], v[20:23], v[92:95], v[124:127]\n"
      "buffer_load_dwordx4 v0, s[8:11], s46 offen offset:0 sc1 lds\n"
      "v_mfma_f32_16x16x32_bf16 v[124:127], v[24:27], v[96:99], v[124:127]\n"
      "v_mul_f32 v120, v219, v120\n"
      "v_mul_f32 v122, v219, v122\n"
      "v_mfma_f32_16x16x32_bf16 v[124:127], v[28:31], v[100:103], v[124:127]\n"
      "ds_write_b64 v5, v[218:219] offset:512\n"
      "v_mfma_f32_16x16x32_bf16 v[124:127], v[32:35], v[104:107], v[124:127]\n"
      "v_mul_f32 v121, v219, v121\n"
      "v_mul_f32 v123, v219, v123\n"
      "v_mfma_f32_16x16x32_bf16 v[124:127], v[36:39], v[108:111], v[124:127]\n"
      "buffer_load_dwordx4 v0, s[8:11], s46 offen offset:2048 sc1 lds\n"
      "v_mfma_f32_16x16x32_bf16 v[124:127], v[40:43], v[112:115], v[124:127]\n"
      "s_mov_b32 s45, s36\n"
      "ds_read_b128 v[224:227], v6 offset:528\n"
      "v_mfma_f32_16x16x32_bf16 v[124:127], v[44:47], v[116:119], v[124:127]\n"
      "s_min_u32 s45, s45, s6\n"
      "v_cndmask_b32 v233, v219, 1.0, s[54:55]\n"
      "v_mfma_f32_16x16x32_bf16 v[128:131], v[48:51], a[72:75], 0\n"
      "s_add_u32 s52, s52, 24576\n"
      "v_cvt_pk_bf16_f32 v172, v120, v121\n"
      "v_cvt_pk_bf16_f32 v173, v122, v123\n"
      "v_mfma_f32_16x16x32_bf16 v[128:131], v[52:55], a[76:79], v[128:131]\n"
      "buffer_load_dwordx4 v0, s[8:11], s46 offen offset:3072 sc1 lds\n"
      "v_mfma_f32_16x16x32_bf16 v[128:131], v[56:59], a[80:83], v[128:131]\n"
      "v_fma_f32 v126, -v218, v142, v126\n"
      "v_fma_f32 v124, -v218, v140, v124\n"
      "v_mul_f32 v126, v219, v126\n"
      "v_mfma_f32_16x16x32_bf16 v[128:131], v[60:63], a[84:87], v[128:131]\n"
      "v_fma_f32 v125, -v218, v141, v125\n"
      "v_mul_f32 v125, v233, v125\n"
      "v_mfma_f32_16x16x32_bf16 v[128:131], v[64:67], a[88:91], v[128:131]\n"
      "s_waitcnt vmcnt(7) lgkmcnt(1)\n"
      "ds_write_b32 v11, v9\n"
      "v_mfma_f32_16x16x32_bf16 v[128:131], v[68:71], a[92:95], v[128:131]\n"
      "v_fma_f32 v127, -v218, v143, v127\n"
      "v_mul_f32 v124, v233, v124\n"
      "v_mul_f32 v127, v219, v127\n"
      "v_mfma_f32_16x16x32_bf16 v[128:131], v[72:75], a[96:99], v[128:131]\n"
      "v_cvt_pk_bf16_f32 v174, v126, v127\n"
      "v_cvt_pk_bf16_f32 v175, v124, v125\n"
      "v_mfma_f32_16x16x32_bf16 v[128:131], v[76:79], a[100:103], v[128:131]\n"
      "buffer_load_dword v15, v2, s[8:11], s45 offen sc1\n"
      "v_mfma_f32_16x16x32_bf16 v[192:195], v[172:175], v[176:179], v[156:159]\n"
      "v_mov_b32 v183, v181\n"
      "v_mfma_f32_16x16x32_bf16 v[196:199], v[172:175], v[164:167], v[168:171]\n"
      "ds_read_b128 v[220:223], v6 offset:512\n"
      "s_add_u32 m0, s42, 0\n"
      "v_mfma_f32_16x16x32_bf16 v[128:131], v[80:83], a[104:107], v[128:131]\n"
      "buffer_load_dwordx4 v1, s[8:11], s46 offen offset:0 sc1 lds\n"
      "v_mfma_f32_16x16x32_bf16 v[128:131], v[84:87], a[108:111], v[128:131]\n"
      "v_max_f32 v235, v194, v195\n"
      "v_max3_f32 v235, v190, v191, v235\n"
      "v_mfma_f32_16x16x32_bf16 v[128:131], v[88:91], a[112:115], v[128:131]\n"
      "v_max_f32 v234, v192, v193\n"
      "v_max3_f32 v234, v188, v189, v234\n"
      "v_mfma_f32_16x16x32_bf16 v[128:131], v[92:95], a[116:119], v[128:131]\n"
      "ds_read_b32 v10, v12\n"
      "v_max_f32 v236, v234, v235\n"
      "v_mfma_f32_16x16x32_bf16 v[128:131], v[96:99], a[120:123], v[128:131]\n"
      "v_mov_b32 v237, v236\n"
      "v_cvt_pk_bf16_f32 v185, v198, v199\n"
      "v_mfma_f32_16x16x32_bf16 v[128:131], v[100:103], a[124:127], v[128:131]\n"
      "v_permlane16_swap_b32 v236, v237\n"
      "v_mfma_f32_16x16x32_bf16 v[128:131], v[104:107], a[128:131], v[128:131]\n"
      "v_max_f32 v236, v236, v237\n"
      "v_mov_b32 v237, v236\n"
      "v_mfma_f32_16x16x32_bf16 v[128:131], v[108:111], a[132:135], v[128:131]\n"
      "v_permlane32_swap_b32 v236, v237\n"
      "v_mfma_f32_16x16x32_bf16 v[128:131], v[112:115], a[136:139], v[128:131]\n"
      "s_waitcnt lgkmcnt(0)\n"
      "v_cmp_gt_i32 vcc, v9, v10\n"
      "v_max_f32 v236, v236, v237\n"
      "v_mfma_f32_16x16x32_bf16 v[128:131], v[116:119], a[140:143], v[128:131]\n"
      "v_mul_f32_e64 v236, v236, v232\n"
      "v_fma_f32 v235, v235, v232, -v236\n"
      "v_mfma_f32_16x16x32_bf16 v[132:135], a[144:147], v[48:51], 0\n"
      "v_exp_f32_e64 v235, v235\n"
      "v_mfma_f32_16x16x32_bf16 v[132:135], a[148:151], v[52:55], v[132:135]\n"
      "s_cbranch_vccnz .Lsp_qy2_7\n"
      ".Lbk_qy2_7:\n"
      "v_cvt_pk_bf16_f32 v235, v235, v235\n"
      "v_mfma_f32_16x16x32_bf16 v[132:135], a[152:155], v[56:59], v[132:135]\n"
      "v_fma_f32 v130, -v224, v147, v130\n"
      "v_mul_f32 v130, v225, v130\n"
      "v_mfma_f32_16x16x32_bf16 v[132:135], a[156:159], v[60:63], v[132:135]\n"
      "v_fma_f32 v129, -v222, v146, v129\n"
      "v_cvt_pk_bf16_f32 v184, v196, v197\n"
      "v_mfma_f32_16x16x32_bf16 v[132:135], a[160:163], v[64:67], v[132:135]\n"
      "v_fma_f32 v234, v234, v232, -v236\n"
      "v_fma_f32 v131, -v226, v144, v131\n"
      "v_mfma_f32_16x16x32_bf16 v[132:135], a[164:167], v[68:71], v[132:135]\n"
      "ds_read_b128 v[52:55], v0 offset:41984\n"
      "ds_read_b128 v[64:67], v0 offset:45056\n"
      "v_mfma_f32_16x16x32_bf16 v[132:135], a[168:171], v[72:75], v[132:135]\n"
      "ds_read_b128 v[56:59], v0 offset:43008\n"
      "v_mul_f32 v129, v223, v129\n"
      "v_mfma_f32_16x16x32_bf16 v[132:135], a[172:175], v[76:79], v[132:135]\n"
      "v_exp_f32 v234, v234\n"
      "v_mfma_f32_16x16x32_bf16 v[132:135], a[176:179], v[80:83], v[132:135]\n"
      "v_fma_f32 v128, -v220, v145, v128\n"
      "v_and_b32 v203, v163, v235\n"
      "v_mfma_f32_16x16x32_bf16 v[132:135], a[180:183], v[84:87], v[132:135]\n"
      "v_cvt_pk_bf16_f32 v234, v234, v234\n"
      "v_and_b32 v201, v161, v235\n"
      "v_mfma_f32_16x16x32_bf16 v[132:135], a[184:187], v[88:91], v[132:135]\n"
      "ds_read_b128 v[48:51], v0 offset:40960\n"
      "v_and_b32 v202, v162, v234\n"
      "v_mfma_f32_16x16x32_bf16 v[132:135], a[188:191], v[92:95], v[132:135]\n"
      "ds_read_b128 v[60:63], v0 offset:44032\n"
      "v_mul_f32 v128, v221, v128\n"
      "v_mfma_f32_16x16x32_bf16 v[132:135], a[192:195], v[96:99], v[132:135]\n"
      "ds_read_b128 v[76:79], v0 offset:48128\n"
      "v_cvt_pk_bf16_f32 v180, v128, v129\n"
      "v_mfma_f32_16x16x32_bf16 v[132:135], a[196:199], v[100:103], v[132:135]\n"
      "ds_read_b128 v[100:103], v0 offset:54272\n"
      "v_and_b32 v200, v160, v234\n"
      "v_mfma_f32_16x16x32_bf16 v[132:135], a[200:203], v[104:107], v[132:135]\n"
      "v_mul_f32 v131, v227, v131\n"
      "v_cvt_pk_bf16_f32 v181, v130, v131\n"
      "v_mfma_f32_16x16x32_bf16 v[208:211], v[184:187], v[200:203], 0\n"
      "s_waitcnt vmcnt(6) lgkmcnt(4)\n"
      "buffer_store_dword v13, v3, s[8:11], s52 offen sc1\n"
      "v_mfma_f32_16x16x32_bf16 v[204:207], v[180:183], v[200:203], 0\n"
      "ds_read_b128 v[84:87], v0 offset:50176\n"
      "ds_read_b128 v[72:75], v0 offset:47104\n"
      "v_mfma_f32_16x16x32_bf16 v[132:135], a[204:207], v[108:111], v[132:135]\n"
      "ds_read_b128 v[68:71], v0 offset:46080\n"
      "v_mov_b32 v178, v175\n"
      "v_mfma_f32_16x16x32_bf16 v[132:135], a[208:211], v[112:115], v[132:135]\n"
      "v_rcp_f32 v210, v210\n"
      "v_mfma_f32_16x16x32_bf16 v[132:135], a[212:215], v[116:119], v[132:135]\n"
      "s_waitcnt lgkmcnt(4)\n"
      "ds_read_b128 v[92:95], v0 offset:52224\n"
      "v_mul_f32 v205, v205, v210\n"
      "v_mfma_f32_16x16x32_bf16 v[120:123], a[0:3], v[48:51], 0\n"
      "s_add_u32 s36, s36, 24576\n"
      "ds_read_b128 v[80:83], v0 offset:49152\n"
      "v_mov_b32 v182, v180\n"
      "v_mfma_f32_16x16x32_bf16 v[120:123], a[4:7], v[52:55], v[120:123]\n"
      "ds_read_b128 v[96:99], v0 offset:53248\n"
      "v_mfma_f32_16x16x32_bf16 v[120:123], a[8:11], v[56:59], v[120:123]\n"
      "ds_read_b128 v[88:91], v0 offset:51200\n"
      "v_fma_f32 v132, -v218, v148, v132\n"
      "v_mfma_f32_16x16x32_bf16 v[120:123], a[12:15], v[60:63], v[120:123]\n"
      "s_cmp_eq_u32 s36, s37\n"
      "v_fma_f32 v133, -v218, v149, v133\n"
      "v_cvt_pk_bf16_f32 v176, v132, v133\n"
      "v_mfma_f32_16x16x32_bf16 v[120:123], a[16:19], v[64:67], v[120:123]\n"
      "s_waitcnt lgkmcnt(4)\n"
      "ds_read2st64_b64 v[212:215], v4 offset0:80 offset1:81\n"
      "s_cselect_b32 s47, s38, 0\n"
      "v_mfma_f32_16x16x32_bf16 v[120:123], a[20:23], v[68:71], v[120:123]\n"
      "s_cselect_b32 s37, s48, s37\n"
      "ds_read_b128 v[112:115], v0 offset:57344\n"
      "v_fma_f32 v135, -v218, v151, v135\n"
      "v_mfma_f32_16x16x32_bf16 v[120:123], a[24:27], v[72:75], v[120:123]\n"
      "s_cselect_b32 s49, s39, s53\n"
      "v_fma_f32 v134, -v218, v150, v134\n"
      "v_mov_b32 v183, v181\n"
      "v_mfma_f32_16x16x32_bf16 v[120:123], a[28:31], v[76:79], v[120:123]\n"
      "s_waitcnt lgkmcnt(4)\n"
      "ds_read_b128 v[104:107], v0 offset:55296\n"
      "v_cvt_pk_bf16_f32 v177, v134, v135\n"
      "v_mfma_f32_16x16x32_bf16 v[120:123], a[32:35], v[80:83], v[120:123]\n"
      "ds_read_b128 v[108:111], v0 offset:56320\n"
      "v_mul_f32 v206, v206, v210\n"
      "v_mfma_f32_16x16x32_bf16 v[188:191], v[172:175], v[176:179], v[152:155]\n"
      "ds_read_b64 v[216:217], v4 offset:41984\n"
      "v_mul_f32 v207, v207, v210\n"
      "v_mfma_f32_16x16x32_bf16 v[120:123], a[36:39], v[84:87], v[120:123]\n"
      "s_waitcnt lgkmcnt(5)\n"
      "s_add_u32 m0, s41, 20480\n"
      "ds_read_b128 v[116:119], v0 offset:58368\n"
      "v_mfma_f32_16x16x32_bf16 v[120:123], a[40:43], v[88:91], v[120:123]\n"
      "s_mov_b32 s46, s45\n"
      "s_add_u32 s45, s36, s47\n"
      "v_mul_f32 v209, v209, v210\n"
      "v_mfma_f32_16x16x32_bf16 v[120:123], a[44:47], v[92:95], v[120:123]\n"
      "v_cvt_pk_bf16_f32 v229, v206, v207\n"
      "v_mfma_f32_16x16x32_bf16 v[120:123], a[48:51], v[96:99], v[120:123]\n"
      "s_waitcnt lgkmcnt(4)\n"
      "v_add_f32 v213, v213, v215\n"
      "v_mfma_f32_16x16x32_bf16 v[120:123], a[52:55], v[100:103], v[120:123]\n"
      "s_waitcnt lgkmcnt(3)\n"
      "v_mul_f32 v232, 0x3eae1a93, v219\n"
      "v_mul_f32 v204, v204, v210\n"
      "v_mfma_f32_16x16x32_bf16 v[120:123], a[56:59], v[104:107], v[120:123]\n"
      "s_waitcnt lgkmcnt(2)\n"
      "v_mul_f32 v208, v208, v210\n"
      "v_cvt_pk_bf16_f32 v230, v208, v209\n"
      "v_mfma_f32_16x16x32_bf16 v[120:123], a[60:63], v[108:111], v[120:123]\n"
      "v_mfma_f32_16x16x32_bf16 v[120:123], a[64:67], v[112:115], v[120:123]\n"
      "s_waitcnt lgkmcnt(0)\n"
      "buffer_store_dwordx2 v[230:231], v8, s[8:11], s40 offen sc1\n"
      "v_mfma_f32_16x16x32_bf16 v[120:123], a[68:71], v[116:119], v[120:123]\n"
      "v_cvt_pk_bf16_f32 v228, v204, v205\n"
      "v_mfma_f32_16x16x32_bf16 v[124:127], a[216:219], v[48:51], 0\n"
      "v_add_f32 v212, v212, v214\n"
      "v_add_f32 v213, v213, v217\n"
      "v_mfma_f32_16x16x32_bf16 v[124:127], a[220:223], v[52:55], v[124:127]\n"
      "v_add_f32 v218, v212, v216\n"
      "v_fma_f32 v213, -v218, v218, v213\n"
      "v_mfma_f32_16x16x32_bf16 v[124:127], a[224:227], v[56:59], v[124:127]\n"
      "buffer_store_dwordx2 v[228:229], v7, s[8:11], s40 offen sc1\n"
      "v_mfma_f32_16x16x32_bf16 v[124:127], a[228:231], v[60:63], v[124:127]\n"
      "s_add_u32 s52, s52, 24576\n"
      "v_fma_f32 v123, -v218, v139, v123\n"
      "v_add_u32 v9, 1, v9\n"
      "v_mfma_f32_16x16x32_bf16 v[124:127], a[232:235], v[64:67], v[124:127]\n"
      "v_fma_f32 v122, -v218, v138, v122\n"
      "v_mfma_f32_16x16x32_bf16 v[124:127], a[236:239], v[68:71], v[124:127]\n"
      "s_waitcnt vmcnt(4)\n"
      "v_max_f32 v213, 0x3727c5ac, v213\n"
      "v_cmp_ne_u32 vcc, s21, v15\n"
      "v_mfma_f32_16x16x32_bf16 v[124:127], a[240:243], v[72:75], v[124:127]\n"
      "v_rsq_f32 v219, v213\n"
      "v_mfma_f32_16x16x32_bf16 v[124:127], a[244:247], v[76:79], v[124:127]\n"
      "v_mov_b32 v186, v184\n"
      "v_fma_f32 v120, -v218, v136, v120\n"
      "v_mfma_f32_16x16x32_bf16 v[124:127], a[248:251], v[80:83], v[124:127]\n"
      "s_min_u32 s45, s45, s6\n"
      "ds_write_b64 v5, v[218:219] offset:1024\n"
      "v_mfma_f32_16x16x32_bf16 v[124:127], a[252:255], v[84:87], v[124:127]\n"
      "s_cbranch_vccnz .Lsp_q1_8\n"
      ".Lbk_q1_8:\n"
      "v_mfma_f32_16x16x32_bf16 v[124:127], v[16:19], v[88:91], v[124:127]\n"
      "buffer_load_dwordx4 v0, s[8:11], s46 offen offset:1024 sc1 lds\n"
      "v_mfma_f32_16x16x32_bf16 v[124:127], v[20:23], v[92:95], v[124:127]\n"
      "v_cndmask_b32 v233, v219, 1.0, s[54:55]\n"
      "v_mul_f32 v123, v219, v123\n"
      "v_mfma_f32_16x16x32_bf16 v[124:127], v[24:27], v[96:99], v[124:127]\n"
      "ds_read_b128 v[220:223], v6 offset:1024\n"
      "v_mul_f32 v120, v219, v120\n"
      "v_mfma_f32_16x16x32_bf16 v[124:127], v[28:31], v[100:103], v[124:127]\n"
      "v_mul_f32 v122, v219, v122\n"
      "v_fma_f32 v121, -v218, v137, v121\n"
      "v_mfma_f32_16x16x32_bf16 v[124:127], v[32:35], v[104:107], v[124:127]\n"
      "s_waitcnt vmcnt(4)\n"
      "ds_write_b32 v11, v9\n"
      "v_mfma_f32_16x16x32_bf16 v[124:127], v[36:39], v[108:111], v[124:127]\n"
      "buffer_load_dwordx4 v0, s[8:11], s46 offen offset:0 sc1 lds\n"
      "v_mfma_f32_16x16x32_bf16 v[124:127], v[40:43], v[112:115], v[124:127]\n"
      "v_mfma_f32_16x16x32_bf16 v[124:127], v[44:47], v[116:119], v[124:127]\n"
      "v_cvt_pk_bf16_f32 v173, v122, v123\n"
      "v_mfma_f32_16x16x32_bf16 v[128:131], v[48:51], a[72:75], 0\n"
      "v_mul_f32 v121, v219, v121\n"
      "v_cvt_pk_bf16_f32 v172, v120, v121\n"
      "v_mfma_f32_16x16x32_bf16 v[128:131], v[52:55], a[76:79], v[128:131]\n"
      "buffer_load_dwordx4 v0, s[8:11], s46 offen offset:3072 sc1 lds\n"
      "v_mfma_f32_16x16x32_bf16 v[128:131], v[56:59], a[80:83], v[128:131]\n"
      "v_fma_f32 v125, -v218, v141, v125\n"
      "v_fma_f32 v124, -v218, v140, v124\n"
      "v_mfma_f32_16x16x32_bf16 v[128:131], v[60:63], a[84:87], v[128:131]\n"
      "v_fma_f32 v127, -v218, v143, v127\n"
      "v_mul_f32 v125, v233, v125\n"
      "v_mfma_f32_16x16x32_bf16 v[128:131], v[64:67], a[88:91], v[128:131]\n"
      "v_mul_f32 v124, v233, v124\n"
      "v_mul_f32 v127, v219, v127\n"
      "v_mfma_f32_16x16x32_bf16 v[128:131], v[68:71], a[92:95], v[128:131]\n"
      "v_fma_f32 v126, -v218, v142, v126\n"
      "v_mul_f32 v126, v219, v126\n"
      "v_mfma_f32_16x16x32_bf16 v[128:131], v[72:75], a[96:99], v[128:131]\n"
      "v_cvt_pk_bf16_f32 v175, v124, v125\n"
      "v_cvt_pk_bf16_f32 v174, v126, v127\n"
      "v_mfma_f32_16x16x32_bf16 v[128:131], v[76:79], a[100:103], v[128:131]\n"
      "buffer_load_dword v15, v2, s[8:11], s45 offen sc1\n"
      "v_mfma_f32_16x16x32_bf16 v[192:195], v[172:175], v[176:179], v[156:159]\n"
      "ds_read_b128 v[224:227], v6 offset:1040\n"
      "v_mov_b32 v187, v185\n"
      "v_mfma_f32_16x16x32_bf16 v[196:199], v[172:175], v[164:167], v[168:171]\n"
      "ds_read_b32 v10, v12\n"
      "v_mfma_f32_16x16x32_bf16 v[128:131], v[80:83], a[104:107], v[128:131]\n"
      "buffer_load_dwordx4 v0, s[8:11], s46 offen offset:2048 sc1 lds\n"
      "v_mfma_f32_16x16x32_bf16 v[128:131], v[84:87], a[108:111], v[128:131]\n"
      "v_med3_f32 v234, v192, v193, s50\n"
      "v_med3_f32 v235, v194, v195, s50\n"
      "v_mfma_f32_16x16x32_bf16 v[128:131], v[88:91], a[112:115], v[128:131]\n"
      "v_cvt_pk_bf16_f32 v185, v198, v199\n"
      "v_max3_f32 v234, v188, v189, v234\n"
      "v_mfma_f32_16x16x32_bf16 v[128:131], v[92:95], a[116:119], v[128:131]\n"
      "v_max3_f32 v235, v190, v191, v235\n"
      "v_max_f32 v236, v234, v235\n"
      "v_mfma_f32_16x16x32_bf16 v[128:131], v[96:99], a[120:123], v[128:131]\n"
      "s_waitcnt lgkmcnt(0)\n"
      "v_mov_b32 v237, v236\n"
      "v_cmp_gt_i32 vcc, v9, v10\n"
      "v_mfma_f32_16x16x32_bf16 v[128:131], v[100:103], a[124:127], v[128:131]\n"
      "s_add_u32 m0, s42, 20480\n"
      "v_permlane16_swap_b32 v236, v237\n"
      "v_mfma_f32_16x16x32_bf16 v[128:131], v[104:107], a[128:131], v[128:131]\n"
      "v_max_f32 v236, v236, v237\n"
      "v_mov_b32 v237, v236\n"
      "v_mfma_f32_16x16x32_bf16 v[128:131], v[108:111], a[132:135], v[128:131]\n"
      "buffer_load_dwordx4 v1, s[8:11], s46 offen offset:0 sc1 lds\n"
      "v_mfma_f32_16x16x32_bf16 v[128:131], v[112:115], a[136:139], v[128:131]\n"
      ".Lq_p1:\n"
      "s_cbranch_execz .Lq_pr\n"
      "v_permlane32_swap_b32 v236, v237\n"
      "v_mfma_f32_16x16x32_bf16 v[128:131], v[116:119], a[140:143], v[128:131]\n"
      "v_max_f32 v236, v236, v237\n"
      "v_mul_f32_e64 v236, v236, v232\n"
      "v_mfma_f32_16x16x32_bf16 v[132:135], a[144:147], v[48:51], 0\n"
      "s_cbranch_vccnz .Lsp_qy0_9\n"
      ".Lbk_qy0_9:\n"
      "v_fma_f32 v234, v234, v232, -v236\n"
      "v_mfma_f32_16x16x32_bf16 v[132:135], a[148:151], v[52:55], v[132:135]\n"
      "ds_read_b128 v[52:55], v0 offset:1024\n"
      "ds_read_b128 v[48:51], v0 offset:0\n"
      "v_mfma_f32_16x16x32_bf16 v[132:135], a[152:155], v[56:59], v[132:135]\n"
      "v_fma_f32 v131, -v226, v144, v131\n"
      "v_fma_f32 v235, v235, v232, -v236\n"
      "v_mfma_f32_16x16x32_bf16 v[132:135], a[156:159], v[60:63], v[132:135]\n"
      "ds_read_b128 v[56:59], v0 offset:2048\n"
      "v_fma_f32 v128, -v220, v145, v128\n"
      "v_mfma_f32_16x16x32_bf16 v[132:135], a[160:163], v[64:67], v[132:135]\n"
      "v_exp_f32 v234, v234\n"
      "v_mfma_f32_16x16x32_bf16 v[132:135], a[164:167], v[68:71], v[132:135]\n"
      "v_exp_f32 v235, v235\n"
      "v_mfma_f32_16x16x32_bf16 v[132:135], a[168:171], v[72:75], v[132:135]\n"
      "v_cvt_pk_bf16_f32 v234, v234, v234\n"
      "v_fma_f32 v129, -v222, v146, v129\n"
      "v_mfma_f32_16x16x32_bf16 v[132:135], a[172:175], v[76:79], v[132:135]\n"
      "v_mul_f32 v129, v223, v129\n"
      "v_fma_f32 v130, -v224, v147, v130\n"
      "v_mfma_f32_16x16x32_bf16 v[132:135], a[176:179], v[80:83], v[132:135]\n"
      "v_cvt_pk_bf16_f32 v235, v235, v235\n"
      "v_and_b32 v200, v160, v234\n"
      "v_mfma_f32_16x16x32_bf16 v[132:135], a[180:183], v[84:87], v[132:135]\n"
      "v_cvt_pk_bf16_f32 v184, v196, v197\n"
      "v_and_b32 v201, v161, v235\n"
      "v_mfma_f32_16x16x32_bf16 v[132:135], a[184:187], v[88:91], v[132:135]\n"
      "ds_read_b128 v[68:71], v0 offset:5120\n"
      "v_mul_f32 v130, v225, v130\n"
      "v_mfma_f32_16x16x32_bf16 v[132:135], a[188:191], v[92:95], v[132:135]\n"
      "v_and_b32 v203, v163, v235\n"
      "v_mul_f32_e64 v131, v227, v131\n"
      "v_mfma_f32_16x16x32_bf16 v[132:135], a[192:195], v[96:99], v[132:135]\n"
      "ds_read_b128 v[64:67], v0 offset:4096\n"
      "v_mul_f32_e64 v128, v221, v128\n"
      "v_mfma_f32_16x16x32_bf16 v[132:135], a[196:199], v[100:103], v[132:135]\n"
      "v_and_b32_e64 v202, v162, v234\n"
      "v_cvt_pk_bf16_f32 v181, v130, v131\n"
      "v_mfma_f32_16x16x32_bf16 v[132:135], a[200:203], v[104:107], v[132:135]\n"
      "s_add_u32 s40, s40, 24576\n"
      "ds_read_b128 v[60:63], v0 offset:3072\n"
      "v_cvt_pk_bf16_f32 v180, v128, v129\n"
      "v_mfma_f32_16x16x32_bf16 v[208:211], v[184:187], v[200:203], 0\n"
      "s_waitcnt vmcnt(6) lgkmcnt(3)\n"
      "buffer_store_dword v13, v3, s[8:11], s52 offen sc1\n"
      "ds_read2st64_b64 v[212:215], v4 offset0:0 offset1:1\n"
      "v_mfma_f32_16x16x32_bf16 v[204:207], v[180:183], v[200:203], 0\n"
      "ds_read_b128 v[72:75], v0 offset:6144\n"
      "v_mfma_f32_16x16x32_bf16 v[132:135], a[204:207], v[108:111], v[132:135]\n"
      "ds_read_b128 v[76:79], v0 offset:7168\n"
      "v_mov_b32_e64 v178, v175\n"
      "v_mfma_f32_16x16x32_bf16 v[132:135], a[208:211], v[112:115], v[132:135]\n"
      "ds_read_b128 v[88:91], v0 offset:10240\n"
      "ds_read_b128 v[108:111], v0 offset:15360\n"
      "v_mfma_f32_16x16x32_bf16 v[132:135], a[212:215], v[116:119], v[132:135]\n"
      "s_sub_u32 s20, s20, 1\n"
      "s_cbranch_scc0 .Lq_loop\n"
      ".Lq_tail:\n"
      "v_mul_f32 v232, 0x3eae1a93, v219\n"
      "s_nop 7\n"
      "v_fma_f32 v135, -v218, v151, v135\n"
      "v_fma_f32 v133, -v218, v149, v133\n"
      "v_fma_f32 v134, -v218, v150, v134\n"
      "v_fma_f32 v132, -v218, v148, v132\n"
      "v_cvt_pk_bf16_f32 v176, v132, v133\n"
      "v_cvt_pk_bf16_f32 v177, v134, v135\n"
      "v_rcp_f32 v210, v210\n"
      "s_nop 0\n"
      "v_mul_f32 v204, v204, v210\n"
      "v_mfma_f32_16x16x32_bf16 v[188:191], v[172:175], v[176:179], v[152:155]\n"
      "v_mul_f32 v208, v208, v210\n"
      "v_mul_f32 v207, v207, v210\n"
      "v_mul_f32 v209, v209, v210\n"
      "v_cvt_pk_bf16_f32 v230, v208, v209\n"
      "buffer_store_dwordx2 v[230:231], v8, s[8:11], s40 offen sc1\n"
      "v_mul_f32 v206, v206, v210\n"
      "v_cvt_pk_bf16_f32 v229, v206, v207\n"
      "v_mul_f32 v205, v205, v210\n"
      "v_cvt_pk_bf16_f32 v228, v204, v205\n"
      "buffer_store_dwordx2 v[228:229], v7, s[8:11], s40 offen sc1\n"
      "v_mov_b32 v187, v185\n"
      "v_mov_b32 v186, v184\n"
      "v_mov_b32 v182, v180\n"
      "s_add_u32 s52, s52, 24576\n"
      "v_mov_b32 v183, v181\n"
      "s_add_u32 s40, s40, 24576\n"
      "s_waitcnt vmcnt(0)\n"
      "buffer_store_dword v13, v3, s[8:11], s52 offen sc1\n"
      "s_add_u32 s52, s52, 24576\n"
      "s_cmp_lg_u32 s44, 0\n"
      "s_cbranch_scc1 .Lq_done\n"
      "v_lshlrev_b32 v48, 3, v0\n"
      "s_mov_b32 s22, s23\n"
      ".Lq_up:\n"
      "s_mov_b32 s30, 128\n"
      "buffer_load_dword v49, v48, s[8:11], s30 offen sc1\n"
      "s_mov_b32 s30, 8320\n"
      "buffer_load_dword v50, v48, s[8:11], s30 offen sc1\n"
      "s_mov_b32 s30, 16512\n"
      "buffer_load_dword v51, v48, s[8:11], s30 offen sc1\n"
      "s_mov_b32 s30, 24704\n"
      "buffer_load_dword v52, v48, s[8:11], s30 offen sc1\n"
      "s_waitcnt vmcnt(0)\n"
      "v_xor_b32 v49, s21, v49\n"
      "v_xor_b32 v50, s21, v50\n"
      "v_xor_b32 v51, s21, v51\n"
      "v_xor_b32 v52, s21, v52\n"
      "v_or3_b32 v49, v49, v50, v51\n"
      "v_or_b32 v49, v49, v52\n"
      "v_cmp_ne_u32 vcc, 0, v49\n"
      "s_cbranch_vccz .Lq_ok\n"
      "s_sleep 4\n"
      "s_sub_u32 s22, s22, 1\n"
      "s_cbranch_scc0 .Lq_up\n"
      ".Lq_ok:\n"
      "v_lshrrev_b32 v48, 2, v0\n"
      "v_and_b32 v48, 0x7c, v48\n"
      "v_mov_b32 v49, s21\n"
      "buffer_store_dword v49, v48, s[8:11], 0 offen sc1\n"
      ".Lq_done:\n"
      "s_endpgm\n"
      ".Lsp_q0_1:\n"
      "s_mov_b32 s22, s23\n"
      ".Lsl_q0_1:\n"
      "buffer_load_dword v15, v2, s[8:11], s46 offen sc1\n"
      "s_waitcnt vmcnt(0)\n"
      "v_cmp_ne_u32 vcc, s21, v15\n"
      "s_cbranch_vccz .Lbk_q0_1\n"
      "s_sleep 4\n"
      "s_sub_u32 s22, s22, 1\n"
      "s_cbranch_scc0 .Lsl_q0_1\n"
      "s_mov_b32 s23, 0\n"
      "s_branch .Lbk_q0_1\n"
      ".Lsp_q1_2:\n"
      "s_mov_b32 s22, s23\n"
      ".Lsl_q1_2:\n"
      "buffer_load_dword v15, v2, s[8:11], s46 offen sc1\n"
      "s_waitcnt vmcnt(0)\n"
      "v_cmp_ne_u32 vcc, s21, v15\n"
      "s_cbranch_vccz .Lbk_q1_2\n"
      "s_sleep 4\n"
      "s_sub_u32 s22, s22, 1\n"
      "s_cbranch_scc0 .Lsl_q1_2\n"
      "s_mov_b32 s23, 0\n"
      "s_branch .Lbk_q1_2\n"
      ".Lsp_qy0_3:\n"
      "s_lshl_b32 s22, s23, 4\n"
      ".Lsl_qy0_3:\n"
      "ds_read_b32 v10, v12\n"
      "s_waitcnt lgkmcnt(0)\n"
      "v_cmp_gt_i32 vcc, v9, v10\n"
      "s_cbranch_vccz .Lbk_qy0_3\n"
      "s_sub_u32 s22, s22, 1\n"
      "s_cbranch_scc0 .Lsl_qy0_3\n"
      "s_mov_b32 s23, 0\n"
      "s_branch .Lbk_qy0_3\n"
      ".Lsp_q2_4:\n"
      "s_mov_b32 s22, s23\n"
      "buffer_load_dword v15, v2, s[8:11], s46 offen sc1\n"
      "s_waitcnt vmcnt(0)\n"
      "v_cmp_ne_u32 vcc, s21, v15\n"
      "s_add_u32 s30, s52, 24576\n"
      "buffer_store_dword v13, v3, s[8:11], s30 offen sc1\n"
      "s_cbranch_vccz .Lbk_q2_4\n"
      ".Lsl_q2_4:\n"
      "buffer_load_dword v15, v2, s[8:11], s46 offen sc1\n"
      "s_waitcnt vmcnt(0)\n"
      "v_cmp_ne_u32 vcc, s21, v15\n"
      "s_cbranch_vccz .Lbk_q2_4\n"
      "s_sleep 4\n"
      "s_sub_u32 s22, s22, 1\n"
      "s_cbranch_scc0 .Lsl_q2_4\n"
      "s_mov_b32 s23, 0\n"
      "s_branch .Lbk_q2_4\n"
      ".Lsp_qy1_5:\n"
      "s_lshl_b32 s22, s23, 4\n"
      ".Lsl_qy1_5:\n"
      "ds_read_b32 v10, v12\n"
      "s_waitcnt lgkmcnt(0)\n"
      "v_cmp_gt_i32 vcc, v9, v10\n"
      "s_cbranch_vccz .Lbk_qy1_5\n"
      "s_sub_u32 s22, s22, 1\n"
      "s_cbranch_scc0 .Lsl_qy1_5\n"
      "s_mov_b32 s23, 0\n"
      "s_branch .Lbk_qy1_5\n"
      ".Lsp_q0_6:\n"
      "s_mov_b32 s22, s23\n"
      "buffer_load_dword v15, v2, s[8:11], s46 offen sc1\n"
      "s_waitcnt vmcnt(0)\n"
      "v_cmp_ne_u32 vcc, s21, v15\n"
      "s_add_u32 s30, s52, 24576\n"
      "buffer_store_dword v13, v3, s[8:11], s30 offen sc1\n"
      "s_cbranch_vccz .Lbk_q0_6\n"
      ".Lsl_q0_6:\n"
      "buffer_load_dword v15, v2, s[8:11], s46 offen sc1\n"
      "s_waitcnt vmcnt(0)\n"
      "v_cmp_ne_u32 vcc, s21, v15\n"
      "s_cbranch_vccz .Lbk_q0_6\n"
      "s_sleep 4\n"
      "s_sub_u32 s22, s22, 1\n"
      "s_cbranch_scc0 .Lsl_q0_6\n"
      "s_mov_b32 s23, 0\n"
      "s_branch .Lbk_q0_6\n"
      ".Lsp_qy2_7:\n"
      "s_lshl_b32 s22, s23, 4\n"
      ".Lsl_qy2_7:\n"
      "ds_read_b32 v10, v12\n"
      "s_waitcnt lgkmcnt(0)\n"
      "v_cmp_gt_i32 vcc, v9, v10\n"
      "s_cbranch_vccz .Lbk_qy2_7\n"
      "s_sub_u32 s22, s22, 1\n"
      "s_cbranch_scc0 .Lsl_qy2_7\n"
      "s_mov_b32 s23, 0\n"
      "s_branch .Lbk_qy2_7\n"
      ".Lsp_q1_8:\n"
      "s_mov_b32 s22, s23\n"
      "buffer_load_dword v15, v2, s[8:11], s46 offen sc1\n"
      "s_waitcnt vmcnt(0)\n"
      "v_cmp_ne_u32 vcc, s21, v15\n"
      "s_add_u32 s30, s52, 0\n"
      "buffer_store_dword v13, v3, s[8:11], s30 offen sc1\n"
      "s_cbranch_vccz .Lbk_q1_8\n"
      ".Lsl_q1_8:\n"
      "buffer_load_dword v15, v2, s[8:11], s46 offen sc1\n"
      "s_waitcnt vmcnt(0)\n"
      "v_cmp_ne_u32 vcc, s21, v15\n"
      "s_cbranch_vccz .Lbk_q1_8\n"
      "s_sleep 4\n"
      "s_sub_u32 s22, s22, 1\n"
      "s_cbranch_scc0 .Lsl_q1_8\n"
      "s_mov_b32 s23, 0\n"
      "s_branch .Lbk_q1_8\n"
      ".Lsp_qy0_9:\n"
      "s_lshl_b32 s22, s23, 4\n"
      ".Lsl_qy0_9:\n"
      "ds_read_b32 v10, v12\n"
      "s_waitcnt lgkmcnt(0)\n"
      "v_cmp_gt_i32 vcc, v9, v10\n"
      "s_cbranch_vccz .Lbk_qy0_9\n"
      "s_sub_u32 s22, s22, 1\n"
      "s_cbranch_scc0 .Lsl_qy0_9\n"
      "s_mov_b32 s23, 0\n"
      "s_branch .Lbk_qy0_9\n"
      ".Lq_last:\n"
      :
      : "{s[8:11]}"(rsWS), "{s[12:15]}"(rsA), "{v0}"(v_l16), "{v1}"(v_l5), "{v2}"(v_fa), "{v3}"(v_ra), "{v4}"(v_st), "{v5}"(v_tw), "{v6}"(v_tr), "{v7}"(v_om), "{v8}"(v_oe), "{v10}"(v_al), "{v11}"(v_yw), "{v12}"(v_yr), "{v14}"(v_hi), "{v152}"(v_bc[0]), "{v153}"(v_bc[1]), "{v154}"(v_bc[2]), "{v155}"(v_bc[3]), "{v156}"(v_bp[0]), "{v157}"(v_bp[1]), "{v158}"(v_bp[2]), "{v159}"(v_bp[3]), "{v160}"(v_mk[0]), "{v161}"(v_mk[1]), "{v162}"(v_mk[2]), "{v163}"(v_mk[3]), "{v164}"(v_sel[0]), "{v165}"(v_sel[1]), "{v166}"(v_sel[2]), "{v167}"(v_sel[3]), "{v168}"(v_ci[0]), "{v169}"(v_ci[1]), "{v170}"(v_ci[2]), "{v171}"(v_ci[3]), "{v48}"(v_vo[0]), "{v49}"(v_vo[1]), "{v50}"(v_vo[2]), "{v51}"(v_vo[3]), "{v52}"(v_at), "{v53}"(v_ax), "{v54}"(v_pm), "{v55}"(v_ms), "{v76}"(v_pw), "{s36}"(s_sx), "{s37}"(s_ev), "{s38}"(s_del), "{s39}"(s_kil), "{s40}"(s_so), "{s41}"(s_m4), "{s42}"(s_m5), "{s43}"(s_ms), "{s44}"(s_up)
      : "v255", "a255", "s99", "vcc", "scc", "memory");

  } else if (role == 1) {
    const unsigned c = part, o = w & 1;
    const u32x4 rsA = srd(Wo, 576 * 576 * 2);
    const unsigned v_l5 = v_l16 + 16384 - w * 3072, e0 = 16384 + o * 2048 + g * 512 + i16 * 8, e1 = e0 + 2048 - o * 4096;
    const unsigned v_ex[8] = {e0, e1, e0 + 20480, e1 + 20480, e0 + 40960, e1 + 40960, e0 + 61440, e1 + 61440}, v_l2 = v_l16 + 40960;
    const unsigned v_fa = 20480 - w * 4096 + (8 * w + (lane >> 3)) * 128 + (lane & 7) * 4, v_ra = up * 8 * REC + l5 + 19456 + (4 * c + w) * 128;
    const unsigned v_xp = (9 * c + 2 * w) * 1024 + v_l16, v_xs = (9 * c + 8) * 1024 + (w >> 1) * 512 + half;
    const unsigned v_rp = v_xp + WS_XIN, v_rs = v_xs + WS_XIN;
    const unsigned v_ew = 82944 + (w >> 1) * 1024 + v_l16;
    const unsigned v_sw = 86016 + i16 * 144 + (4 * w + g) * 8, v_sr = 86016 + (8 * (w >> 1) + l8) * 144 + l7;
    const unsigned v_ss = 18432 + c * 512 + (w >> 1) * 256 + l8 * 32 + p * 8;
    const unsigned v_pw = 19456 + (4 * c + w) * 128 + l8 * REC + l7;
    unsigned v_vo[9], v_vp[9];
#pragma unroll
    for (unsigned j = 0; j < 9; ++j) {
      const unsigned ch = 64 * j + lane, r = ch / 36, x = 576 * c + ch % 36 * 16 - 1024 * (j & 3);
      v_vo[j] = (20 * o + r) * 1152 + x;
      v_vp[j] = (36 * (4 * o + (r >> 2)) + 16 + (r & 3)) * 1152 + x;
    }
    const unsigned ro = (8 * g + q) * 576;
    const unsigned v_t0 = ro + o * 18432 + 128 * w + 16 * p, v_t1 = v_t0 + 18432 - 2 * o * 18432, v_ts = ro + o * 18432 + 512 + 32 * (w >> 1) + 8 * p;
    const unsigned s_sx = WS_OB + (gr * NREC + KSK) * REC + w * 4096, s_ev = s_sx + (31 - KSK) * REC, s_sy = WS_X1 + (gr * NREC + KSK) * REC;
    const unsigned s_m4 = w * 4096, s_m5 = 16384 + w * 1024, s_m9 = w * 9216, s_so = (w >> 1) * 8 * 41472;
    const unsigned s_s8 = s_so, s_wg = o, s_rb = WS_OB + w * 4096;
    asm volatile(
      "s_buffer_load_dword s21, s[8:11], 0\n"
      "s_mov_b32 s23, 65536\n"
      "s_mov_b32 s31, 0x40000000\n"
      "s_add_u32 m0, s41, 0\n"
      "s_nop 0\n"
      "buffer_load_dwordx4 v100, s[12:15], s42 offen offset:0 lds\n"
      "buffer_load_dwordx4 v101, s[12:15], s42 offen offset:1024 lds\n"
      "buffer_load_dwordx4 v102, s[12:15], s42 offen offset:2048 lds\n"
      "buffer_load_dwordx4 v103, s[12:15], s42 offen offset:3072 lds\n"
      "s_add_u32 m0, s41, 4096\n"
      "s_nop 0\n"
      "buffer_load_dwordx4 v104, s[12:15], s42 offen offset:0 lds\n"
      "buffer_load_dwordx4 v105, s[12:15], s42 offen offset:1024 lds\n"
      "buffer_load_dwordx4 v106, s[12:15], s42 offen offset:2048 lds\n"
      "buffer_load_dwordx4 v107, s[12:15], s42 offen offset:3072 lds\n"
      "s_add_u32 m0, s41, 8192\n"
      "s_nop 0\n"
      "buffer_load_dwordx4 v108, s[12:15], s42 offen offset:0 lds\n"
      "s_add_u32 s42, s42, 41472\n"
      "s_add_u32 m0, s41, 36864\n"
      "s_nop 0\n"
      "buffer_load_dwordx4 v100, s[12:15], s42 offen offset:0 lds\n"
      "buffer_load_dwordx4 v101, s[12:15], s42 offen offset:1024 lds\n"
      "buffer_load_dwordx4 v102, s[12:15], s42 offen offset:2048 lds\n"
      "buffer_load_dwordx4 v103, s[12:15], s42 offen offset:3072 lds\n"
      "s_add_u32 m0, s41, 40960\n"
      "s_nop 0\n"
      "buffer_load_dwordx4 v104, s[12:15], s42 offen offset:0 lds\n"
      "buffer_load_dwordx4 v105, s[12:15], s42 offen offset:1024 lds\n"
      "buffer_load_dwordx4 v106, s[12:15], s42 offen offset:2048 lds\n"
      "buffer_load_dwordx4 v107, s[12:15], s42 offen offset:3072 lds\n"
      "s_add_u32 m0, s41, 45056\n"
      "s_nop 0\n"
      "buffer_load_dwordx4 v108, s[12:15], s42 offen offset:0 lds\n"
      "s_add_u32 s42, s42, 41472\n"
      "s_add_u32 m0, s41, 73728\n"
      "s_nop 0\n"
      "buffer_load_dwordx4 v100, s[12:15], s42 offen offset:0 lds\n"
      "buffer_load_dwordx4 v101, s[12:15], s42 offen offset:1024 lds\n"
      "buffer_load_dwordx4 v102, s[12:15], s42 offen offset:2048 lds\n"
      "buffer_load_dwordx4 v103, s[12:15], s42 offen offset:3072 lds\n"
      "s_add_u32 m0, s41, 77824\n"
      "s_nop 0\n"
      "buffer_load_dwordx4 v104, s[12:15], s42 offen offset:0 lds\n"
      "buffer_load_dwordx4 v105, s[12:15], s42 offen offset:1024 lds\n"
      "buffer_load_dwordx4 v106, s[12:15], s42 offen offset:2048 lds\n"
      "buffer_load_dwordx4 v107, s[12:15], s42 offen offset:3072 lds\n"
      "s_add_u32 m0, s41, 81920\n"
      "s_nop 0\n"
      "buffer_load_dwordx4 v108, s[12:15], s42 offen offset:0 lds\n"
      "s_add_u32 s42, s42, 41472\n"
      "s_waitcnt vmcnt(18)\n"
      "s_barrier\n"
      "ds_read_b64_tr_b16 v[64:65], v120 offset:0\n"
      "ds_read_b64_tr_b16 v[66:67], v120 offset:2304\n"
      "ds_read_b64_tr_b16 a[0:1], v118 offset:0\n"
      "ds_read_b64_tr_b16 a[2:3], v118 offset:2304\n"
      "ds_read_b64_tr_b16 a[72:73], v118 offset:8\n"
      "ds_read_b64_tr_b16 a[74:75], v118 offset:2312\n"
      "ds_read_b64_tr_b16 a[144:145], v118 offset:64\n"
      "ds_read_b64_tr_b16 a[146:147], v118 offset:2368\n"
      "ds_read_b64_tr_b16 a[216:217], v118 offset:72\n"
      "ds_read_b64_tr_b16 a[218:219], v118 offset:2376\n"
      "ds_read_b64_tr_b16 a[36:37], v119 offset:0\n"
      "ds_read_b64_tr_b16 a[38:39], v119 offset:2304\n"
      "ds_read_b64_tr_b16 a[108:109], v119 offset:8\n"
      "ds_read_b64_tr_b16 a[110:111], v119 offset:2312\n"
      "ds_read_b64_tr_b16 a[180:181], v119 offset:64\n"
      "ds_read_b64_tr_b16 a[182:183], v119 offset:2368\n"
      "ds_read_b64_tr_b16 a[252:253], v119 offset:72\n"
      "ds_read_b64_tr_b16 a[254:255], v119 offset:2376\n"
      "v_add_u32 v118, 36864, v118\n"
      "v_add_u32 v119, 36864, v119\n"
      "v_add_u32 v120, 36864, v120\n"
      "s_add_u32 m0, s41, 110592\n"
      "s_nop 0\n"
      "buffer_load_dwordx4 v100, s[12:15], s42 offen offset:0 lds\n"
      "buffer_load_dwordx4 v101, s[12:15], s42 offen offset:1024 lds\n"
      "buffer_load_dwordx4 v102, s[12:15], s42 offen offset:2048 lds\n"
      "buffer_load_dwordx4 v103, s[12:15], s42 offen offset:3072 lds\n"
      "s_add_u32 m0, s41, 114688\n"
      "s_nop 0\n"
      "buffer_load_dwordx4 v104, s[12:15], s42 offen offset:0 lds\n"
      "buffer_load_dwordx4 v105, s[12:15], s42 offen offset:1024 lds\n"
      "buffer_load_dwordx4 v106, s[12:15], s42 offen offset:2048 lds\n"
      "buffer_load_dwordx4 v107, s[12:15], s42 offen offset:3072 lds\n"
      "s_add_u32 m0, s41, 118784\n"
      "s_nop 0\n"
      "buffer_load_dwordx4 v108, s[12:15], s42 offen offset:0 lds\n"
      "s_add_u32 s42, s42, 41472\n"
      "s_waitcnt vmcnt(18) lgkmcnt(0)\n"
      "s_barrier\n"
      "ds_read_b64_tr_b16 v[68:69], v120 offset:0\n"
      "ds_read_b64_tr_b16 v[70:71], v120 offset:2304\n"
      "ds_read_b64_tr_b16 a[4:5], v118 offset:0\n"
      "ds_read_b64_tr_b16 a[6:7], v118 offset:2304\n"
      "ds_read_b64_tr_b16 a[76:77], v118 offset:8\n"
      "ds_read_b64_tr_b16 a[78:79], v118 offset:2312\n"
      "ds_read_b64_tr_b16 a[148:149], v118 offset:64\n"
      "ds_read_b64_tr_b16 a[150:151], v118 offset:2368\n"
      "ds_read_b64_tr_b16 a[220:221], v118 offset:72\n"
      "ds_read_b64_tr_b16 a[222:223], v118 offset:2376\n"
      "ds_read_b64_tr_b16 a[40:41], v119 offset:0\n"
      "ds_read_b64_tr_b16 a[42:43], v119 offset:2304\n"
      "ds_read_b64_tr_b16 a[112:113], v119 offset:8\n"
      "ds_read_b64_tr_b16 a[114:115], v119 offset:2312\n"
      "ds_read_b64_tr_b16 a[184:185], v119 offset:64\n"
      "ds_read_b64_tr_b16 a[186:187], v119 offset:2368\n"
      "ds_read_b64_tr_b16 v[32:33], v119 offset:72\n"
      "ds_read_b64_tr_b16 v[34:35], v119 offset:2376\n"
      "v_add_u32 v118, 36864, v118\n"
      "v_add_u32 v119, 36864, v119\n"
      "v_add_u32 v120, 36864, v120\n"
      "s_add_u32 m0, s41, 0\n"
      "s_nop 0\n"
      "buffer_load_dwordx4 v100, s[12:15], s42 offen offset:0 lds\n"
      "buffer_load_dwordx4 v101, s[12:15], s42 offen offset:1024 lds\n"
      "buffer_load_dwordx4 v102, s[12:15], s42 offen offset:2048 lds\n"
      "buffer_load_dwordx4 v103, s[12:15], s42 offen offset:3072 lds\n"
      "s_add_u32 m0, s41, 4096\n"
      "s_nop 0\n"
      "buffer_load_dwordx4 v104, s[12:15], s42 offen offset:0 lds\n"
      "buffer_load_dwordx4 v105, s[12:15], s42 offen offset:1024 lds\n"
      "buffer_load_dwordx4 v106, s[12:15], s42 offen offset:2048 lds\n"
      "buffer_load_dwordx4 v107, s[12:15], s42 offen offset:3072 lds\n"
      "s_add_u32 m0, s41, 8192\n"
      "s_nop 0\n"
      "buffer_load_dwordx4 v108, s[12:15], s42 offen offset:0 lds\n"
      "s_add_u32 s42, s42, 41472\n"
      "s_waitcnt vmcnt(18) lgkmcnt(0)\n"
      "s_barrier\n"
      "ds_read_b64_tr_b16 v[72:73], v120 offset:0\n"
      "ds_read_b64_tr_b16 v[74:75], v120 offset:2304\n"
      "ds_read_b64_tr_b16 a[8:9], v118 offset:0\n"
      "ds_read_b64_tr_b16 a[10:11], v118 offset:2304\n"
      "ds_read_b64_tr_b16 a[80:81], v118 offset:8\n"
      "ds_read_b64_tr_b16 a[82:83], v118 offset:2312\n"
      "ds_read_b64_tr_b16 a[152:153], v118 offset:64\n"
      "ds_read_b64_tr_b16 a[154:155], v118 offset:2368\n"
      "ds_read_b64_tr_b16 a[224:225], v118 offset:72\n"
      "ds_read_b64_tr_b16 a[226:227], v118 offset:2376\n"
      "ds_read_b64_tr_b16 a[44:45], v119 offset:0\n"
      "ds_read_b64_tr_b16 a[46:47], v119 offset:2304\n"
      "ds_read_b64_tr_b16 a[116:117], v119 offset:8\n"
      "ds_read_b64_tr_b16 a[118:119], v119 offset:2312\n"
      "ds_read_b64_tr_b16 a[188:189], v119 offset:64\n"
      "ds_read_b64_tr_b16 a[190:191], v119 offset:2368\n"
      "ds_read_b64_tr_b16 v[36:37], v119 offset:72\n"
      "ds_read_b64_tr_b16 v[38:39], v119 offset:2376\n"
      "v_add_u32 v118, 36864, v118\n"
      "v_add_u32 v119, 36864, v119\n"
      "v_add_u32 v120, 36864, v120\n"
      "s_add_u32 m0, s41, 36864\n"
      "s_nop 0\n"
      "buffer_load_dwordx4 v100, s[12:15], s42 offen offset:0 lds\n"
      "buffer_load_dwordx4 v101, s[12:15], s42 offen offset:1024 lds\n"
      "buffer_load_dwordx4 v102, s[12:15], s42 offen offset:2048 lds\n"
      "buffer_load_dwordx4 v103, s[12:15], s42 offen offset:3072 lds\n"
      "s_add_u32 m0, s41, 40960\n"
      "s_nop 0\n"
      "buffer_load_dwordx4 v104, s[12:15], s42 offen offset:0 lds\n"
      "buffer_load_dwordx4 v105, s[12:15], s42 offen offset:1024 lds\n"
      "buffer_load_dwordx4 v106, s[12:15], s42 offen offset:2048 lds\n"
      "buffer_load_dwordx4 v107, s[12:15], s42 offen offset:3072 lds\n"
      "s_add_u32 m0, s41, 45056\n"
      "s_nop 0\n"
      "buffer_load_dwordx4 v108, s[12:15], s42 offen offset:0 lds\n"
      "s_add_u32 s42, s42, 41472\n"
      "s_waitcnt vmcnt(18) lgkmcnt(0)\n"
      "s_barrier\n"
      "ds_read_b64_tr_b16 v[76:77], v120 offset:0\n"
      "ds_read_b64_tr_b16 v[78:79], v120 offset:2304\n"
      "ds_read_b64_tr_b16 a[12:13], v118 offset:0\n"
      "ds_read_b64_tr_b16 a[14:15], v118 offset:2304\n"
      "ds_read_b64_tr_b16 a[84:85], v118 offset:8\n"
      "ds_read_b64_tr_b16 a[86:87], v118 offset:2312\n"
      "ds_read_b64_tr_b16 a[156:157], v118 offset:64\n"
      "ds_read_b64_tr_b16 a[158:159], v118 offset:2368\n"
      "ds_read_b64_tr_b16 a[228:229], v118 offset:72\n"
      "ds_read_b64_tr_b16 a[230:231], v118 offset:2376\n"
      "ds_read_b64_tr_b16 a[48:49], v119 offset:0\n"
      "ds_read_b64_tr_b16 a[50:51], v119 offset:2304\n"
      "ds_read_b64_tr_b16 a[120:121], v119 offset:8\n"
      "ds_read_b64_tr_b16 a[122:123], v119 offset:2312\n"
      "ds_read_b64_tr_b16 a[192:193], v119 offset:64\n"
      "ds_read_b64_tr_b16 a[194:195], v119 offset:2368\n"
      "ds_read_b64_tr_b16 v[40:41], v119 offset:72\n"
      "ds_read_b64_tr_b16 v[42:43], v119 offset:2376\n"
      "v_add_u32 v118, 4294856704, v118\n"
      "v_add_u32 v119, 4294856704, v119\n"
      "v_add_u32 v120, 4294856704, v120\n"
      "s_add_u32 m0, s41, 73728\n"
      "s_nop 0\n"
      "buffer_load_dwordx4 v100, s[12:15], s42 offen offset:0 lds\n"
      "buffer_load_dwordx4 v101, s[12:15], s42 offen offset:1024 lds\n"
      "buffer_load_dwordx4 v102, s[12:15], s42 offen offset:2048 lds\n"
      "buffer_load_dwordx4 v103, s[12:15], s42 offen offset:3072 lds\n"
      "s_add_u32 m0, s41, 77824\n"
      "s_nop 0\n"
      "buffer_load_dwordx4 v104, s[12:15], s42 offen offset:0 lds\n"
      "buffer_load_dwordx4 v105, s[12:15], s42 offen offset:1024 lds\n"
      "buffer_load_dwordx4 v106, s[12:15], s42 offen offset:2048 lds\n"
      "buffer_load_dwordx4 v107, s[12:15], s42 offen offset:3072 lds\n"
      "s_add_u32 m0, s41, 81920\n"
      "s_nop 0\n"
      "buffer_load_dwordx4 v108, s[12:15], s42 offen offset:0 lds\n"
      "s_add_u32 s42, s42, 41472\n"
      "s_waitcnt vmcnt(18) lgkmcnt(0)\n"
      "s_barrier\n"
      "ds_read_b64_tr_b16 v[80:81], v120 offset:0\n"
      "ds_read_b64_tr_b16 v[82:83], v120 offset:2304\n"
      "ds_read_b64_tr_b16 a[16:17], v118 offset:0\n"
      "ds_read_b64_tr_b16 a[18:19], v118 offset:2304\n"
      "ds_read_b64_tr_b16 a[88:89], v118 offset:8\n"
      "ds_read_b64_tr_b16 a[90:91], v118 offset:2312\n"
      "ds_read_b64_tr_b16 a[160:161], v118 offset:64\n"
      "ds_read_b64_tr_b16 a[162:163], v118 offset:2368\n"
      "ds_read_b64_tr_b16 a[232:233], v118 offset:72\n"
      "ds_read_b64_tr_b16 a[234:235], v118 offset:2376\n"
      "ds_read_b64_tr_b16 a[52:53], v119 offset:0\n"
      "ds_read_b64_tr_b16 a[54:55], v119 offset:2304\n"
      "ds_read_b64_tr_b16 a[124:125], v119 offset:8\n"
      "ds_read_b64_tr_b16 a[126:127], v119 offset:2312\n"
      "ds_read_b64_tr_b16 a[196:197], v119 offset:64\n"
      "ds_read_b64_tr_b16 a[198:199], v119 offset:2368\n"
      "ds_read_b64_tr_b16 v[44:45], v119 offset:72\n"
      "ds_read_b64_tr_b16 v[46:47], v119 offset:2376\n"
      "v_add_u32 v118, 36864, v118\n"
      "v_add_u32 v119, 36864, v119\n"
      "v_add_u32 v120, 36864, v120\n"
      "s_add_u32 m0, s41, 110592\n"
      "s_nop 0\n"
      "buffer_load_dwordx4 v100, s[12:15], s42 offen offset:0 lds\n"
      "buffer_load_dwordx4 v101, s[12:15], s42 offen offset:1024 lds\n"
      "buffer_load_dwordx4 v102, s[12:15], s42 offen offset:2048 lds\n"
      "buffer_load_dwordx4 v103, s[12:15], s42 offen offset:3072 lds\n"
      "s_add_u32 m0, s41, 114688\n"
      "s_nop 0\n"
      "buffer_load_dwordx4 v104, s[12:15], s42 offen offset:0 lds\n"
      "buffer_load_dwordx4 v105, s[12:15], s42 offen offset:1024 lds\n"
      "buffer_load_dwordx4 v106, s[12:15], s42 offen offset:2048 lds\n"
      "buffer_load_dwordx4 v107, s[12:15], s42 offen offset:3072 lds\n"
      "s_add_u32 m0, s41, 118784\n"
      "s_nop 0\n"
      "buffer_load_dwordx4 v108, s[12:15], s42 offen offset:0 lds\n"
      "s_add_u32 s42, s42, 41472\n"
      "s_waitcnt vmcnt(18) lgkmcnt(0)\n"
      "s_barrier\n"
      "ds_read_b64_tr_b16 v[84:85], v120 offset:0\n"
      "ds_read_b64_tr_b16 v[86:87], v120 offset:2304\n"
      "ds_read_b64_tr_b16 a[20:21], v118 offset:0\n"
      "ds_read_b64_tr_b16 a[22:23], v118 offset:2304\n"
      "ds_read_b64_tr_b16 a[92:93], v118 offset:8\n"
      "ds_read_b64_tr_b16 a[94:95], v118 offset:2312\n"
      "ds_read_b64_tr_b16 a[164:165], v118 offset:64\n"
      "ds_read_b64_tr_b16 a[166:167], v118 offset:2368\n"
      "ds_read_b64_tr_b16 a[236:237], v118 offset:72\n"
      "ds_read_b64_tr_b16 a[238:239], v118 offset:2376\n"
      "ds_read_b64_tr_b16 a[56:57], v119 offset:0\n"
      "ds_read_b64_tr_b16 a[58:59], v119 offset:2304\n"
      "ds_read_b64_tr_b16 a[128:129], v119 offset:8\n"
      "ds_read_b64_tr_b16 a[130:131], v119 offset:2312\n"
      "ds_read_b64_tr_b16 a[200:201], v119 offset:64\n"
      "ds_read_b64_tr_b16 a[202:203], v119 offset:2368\n"
      "ds_read_b64_tr_b16 v[48:49], v119 offset:72\n"
      "ds_read_b64_tr_b16 v[50:51], v119 offset:2376\n"
      "v_add_u32 v118, 36864, v118\n"
      "v_add_u32 v119, 36864, v119\n"
      "v_add_u32 v120, 36864, v120\n"
      "s_add_u32 m0, s41, 0\n"
      "s_nop 0\n"
      "buffer_load_dwordx4 v109, s[12:15], s43 offen offset:0 lds\n"
      "buffer_load_dwordx4 v110, s[12:15], s43 offen offset:1024 lds\n"
      "buffer_load_dwordx4 v111, s[12:15], s43 offen offset:2048 lds\n"
      "buffer_load_dwordx4 v112, s[12:15], s43 offen offset:3072 lds\n"
      "s_add_u32 m0, s41, 4096\n"
      "s_nop 0\n"
      "buffer_load_dwordx4 v113, s[12:15], s43 offen offset:0 lds\n"
      "buffer_load_dwordx4 v114, s[12:15], s43 offen offset:1024 lds\n"
      "buffer_load_dwordx4 v115, s[12:15], s43 offen offset:2048 lds\n"
      "buffer_load_dwordx4 v116, s[12:15], s43 offen offset:3072 lds\n"
      "s_add_u32 m0, s41, 8192\n"
      "s_nop 0\n"
      "buffer_load_dwordx4 v117, s[12:15], s43 offen offset:0 lds\n"
      "s_add_u32 s42, s42, 41472\n"
      "s_waitcnt vmcnt(18) lgkmcnt(0)\n"
      "s_barrier\n"
      "ds_read_b64_tr_b16 v[88:89], v120 offset:0\n"
      "ds_read_b64_tr_b16 v[90:91], v120 offset:2304\n"
      "ds_read_b64_tr_b16 a[24:25], v118 offset:0\n"
      "ds_read_b64_tr_b16 a[26:27], v118 offset:2304\n"
      "ds_read_b64_tr_b16 a[96:97], v118 offset:8\n"
      "ds_read_b64_tr_b16 a[98:99], v118 offset:2312\n"
      "ds_read_b64_tr_b16 a[168:169], v118 offset:64\n"
      "ds_read_b64_tr_b16 a[170:171], v118 offset:2368\n"
      "ds_read_b64_tr_b16 a[240:241], v118 offset:72\n"
      "ds_read_b64_tr_b16 a[242:243], v118 offset:2376\n"
      "ds_read_b64_tr_b16 a[60:61], v119 offset:0\n"
      "ds_read_b64_tr_b16 a[62:63], v119 offset:2304\n"
      "ds_read_b64_tr_b16 a[132:133], v119 offset:8\n"
      "ds_read_b64_tr_b16 a[134:135], v119 offset:2312\n"
      "ds_read_b64_tr_b16 a[204:205], v119 offset:64\n"
      "ds_read_b64_tr_b16 a[206:207], v119 offset:2368\n"
      "ds_read_b64_tr_b16 v[52:53], v119 offset:72\n"
      "ds_read_b64_tr_b16 v[54:55], v119 offset:2376\n"
      "v_add_u32 v118, 36864, v118\n"
      "v_add_u32 v119, 36864, v119\n"
      "v_add_u32 v120, 36864, v120\n"
      "s_waitcnt vmcnt(9) lgkmcnt(0)\n"
      "s_barrier\n"
      "ds_read_b64_tr_b16 v[92:93], v120 offset:0\n"
      "ds_read_b64_tr_b16 v[94:95], v120 offset:2304\n"
      "ds_read_b64_tr_b16 a[28:29], v118 offset:0\n"
      "ds_read_b64_tr_b16 a[30:31], v118 offset:2304\n"
      "ds_read_b64_tr_b16 a[100:101], v118 offset:8\n"
      "ds_read_b64_tr_b16 a[102:103], v118 offset:2312\n"
      "ds_read_b64_tr_b16 a[172:173], v118 offset:64\n"
      "ds_read_b64_tr_b16 a[174:175], v118 offset:2368\n"
      "ds_read_b64_tr_b16 a[244:245], v118 offset:72\n"
      "ds_read_b64_tr_b16 a[246:247], v118 offset:2376\n"
      "ds_read_b64_tr_b16 a[64:65], v119 offset:0\n"
      "ds_read_b64_tr_b16 a[66:67], v119 offset:2304\n"
      "ds_read_b64_tr_b16 a[136:137], v119 offset:8\n"
      "ds_read_b64_tr_b16 a[138:139], v119 offset:2312\n"
      "ds_read_b64_tr_b16 a[208:209], v119 offset:64\n"
      "ds_read_b64_tr_b16 a[210:211], v119 offset:2368\n"
      "ds_read_b64_tr_b16 v[56:57], v119 offset:72\n"
      "ds_read_b64_tr_b16 v[58:59], v119 offset:2376\n"
      "v_add_u32 v118, 4294856704, v118\n"
      "v_add_u32 v119, 4294856704, v119\n"
      "v_add_u32 v120, 4294856704, v120\n"
      "s_waitcnt vmcnt(0) lgkmcnt(0)\n"
      "s_barrier\n"
      "ds_read_b64_tr_b16 v[96:97], v120 offset:0\n"
      "ds_read_b64_tr_b16 v[98:99], v120 offset:2304\n"
      "ds_read_b64_tr_b16 a[32:33], v118 offset:0\n"
      "ds_read_b64_tr_b16 a[34:35], v118 offset:2304\n"
      "ds_read_b64_tr_b16 a[104:105], v118 offset:8\n"
      "ds_read_b64_tr_b16 a[106:107], v118 offset:2312\n"
      "ds_read_b64_tr_b16 a[176:177], v118 offset:64\n"
      "ds_read_b64_tr_b16 a[178:179], v118 offset:2368\n"
      "ds_read_b64_tr_b16 a[248:249], v118 offset:72\n"
      "ds_read_b64_tr_b16 a[250:251], v118 offset:2376\n"
      "ds_read_b64_tr_b16 a[68:69], v119 offset:0\n"
      "ds_read_b64_tr_b16 a[70:71], v119 offset:2304\n"
      "ds_read_b64_tr_b16 a[140:141], v119 offset:8\n"
      "ds_read_b64_tr_b16 a[142:143], v119 offset:2312\n"
      "ds_read_b64_tr_b16 a[212:213], v119 offset:64\n"
      "ds_read_b64_tr_b16 a[214:215], v119 offset:2368\n"
      "ds_read_b64_tr_b16 v[60:61], v119 offset:72\n"
      "ds_read_b64_tr_b16 v[62:63], v119 offset:2376\n"
      "v_add_u32 v118, 36864, v118\n"
      "v_add_u32 v119, 36864, v119\n"
      "v_add_u32 v120, 36864, v120\n"
      "s_waitcnt lgkmcnt(0)\n"
      "s_mul_i32 s21, s21, 1664525\n"
      "s_add_u32 s21, s21, 1013904223\n"
      "v_mov_b32 v25, s21\n"
      "buffer_store_dword v25, v22, s[8:11], 0 offen sc1\n"
      "v_not_b32 v124, v25\n"
      "v_not_b32 v125, v25\n"
      "v_not_b32 v126, v25\n"
      "v_not_b32 v127, v25\n"
      "s_add_u32 s30, s38, 0\n"
      "buffer_store_dwordx4 v[124:127], v121, s[8:11], s30 offen sc1\n"
      "v_xor_b32 v25, v25, v26\n"
      "v_mov_b32 v27, 0x3ae38e39\n"
      "s_barrier\n"
      "v_mov_b32 v21, 0\n"
      "ds_write_b32 v23, v21\n"
      "s_waitcnt lgkmcnt(0)\n"
      "s_barrier\n"
      "s_add_u32 s6, s36, 1572864\n"
      "s_add_u32 s36, s36, 4294942720\n"
      "s_mov_b32 s51, 24576\n"
      "s_waitcnt vmcnt(0) lgkmcnt(0)\n"
      "s_cmp_lg_u32 s44, 0\n"
      "s_cbranch_scc1 .Lv_start\n"
      "s_mov_b64 exec, 0\n"
      "s_lshr_b32 s22, s39, 13\n"
      ".Lw_pl:\n"
      "s_cmp_eq_u32 s22, 0\n"
      "s_cbranch_scc1 .Lw_p0\n"
      "s_cmp_eq_u32 s22, 1\n"
      "s_cbranch_scc1 .Lw_p1\n"
      "s_branch .Lw_pe\n"
      ".Lw_pr:\n"
      "s_add_u32 s22, s22, 2\n"
      "s_branch .Lw_pl\n"
      ".Lw_pe:\n"
      "s_mov_b64 exec, -1\n"
      "s_add_u32 s50, s38, 4294918144\n"
      "s_add_u32 s49, s38, 4294918144\n"
      "s_add_u32 s38, s38, 4294942720\n"
      "s_cmp_eq_u32 s36, s37\n"
      "s_cselect_b32 s48, 49152, s51\n"
      "s_add_u32 s36, s36, s48\n"
      "s_min_u32 s36, s36, s6\n"
      "buffer_load_dword v28, v11, s[8:11], s36 offen sc1\n"
      "s_mov_b32 s46, s36\n"
      "s_add_u32 s36, s36, 24576\n"
      "s_add_u32 m0, s39, 0\n"
      "s_waitcnt vmcnt(0)\n"
      "v_cmp_ne_u32 vcc, s21, v28\n"
      "s_min_u32 s36, s36, s6\n"
      "s_cbranch_vccnz .Lsp_w0_1\n"
      ".Lbk_w0_1:\n"
      "buffer_load_dword v28, v11, s[8:11], s36 offen sc1\n"
      "buffer_load_dwordx4 v0, s[8:11], s46 offen offset:0 sc1 lds\n"
      "buffer_load_dwordx4 v0, s[8:11], s46 offen offset:1024 sc1 lds\n"
      "buffer_load_dwordx4 v0, s[8:11], s46 offen offset:3072 sc1 lds\n"
      "buffer_load_dwordx4 v0, s[8:11], s46 offen offset:2048 sc1 lds\n"
      "s_add_u32 m0, s40, 0\n"
      "s_nop 0\n"
      "buffer_load_dwordx4 v1, s[8:11], s46 offen offset:0 sc1 lds\n"
      "s_sub_u32 s47, s46, s45\n"
      "buffer_load_dwordx2 v[200:201], v14, s[8:11], s47 offen sc1\n"
      "buffer_load_dwordx4 v[192:195], v13, s[8:11], s47 offen sc1\n"
      "s_mov_b32 s46, s36\n"
      "buffer_load_dword v26, v23, s[8:11], s31 offen\n"
      "buffer_load_dword v26, v23, s[8:11], s31 offen\n"
      "buffer_load_dword v26, v23, s[8:11], s31 offen\n"
      "s_add_u32 m0, s39, 20480\n"
      "s_waitcnt vmcnt(10)\n"
      "v_cmp_ne_u32 vcc, s21, v28\n"
      "s_cmp_eq_u32 s36, s37\n"
      "s_cselect_b32 s48, 49152, s51\n"
      "s_cbranch_vccnz .Lsp_w1_2\n"
      ".Lbk_w1_2:\n"
      "buffer_load_dwordx4 v0, s[8:11], s46 offen offset:0 sc1 lds\n"
      "s_add_u32 s36, s36, s48\n"
      "s_min_u32 s36, s36, s6\n"
      "buffer_load_dword v28, v11, s[8:11], s36 offen sc1\n"
      "buffer_load_dwordx4 v0, s[8:11], s46 offen offset:1024 sc1 lds\n"
      "buffer_load_dwordx4 v0, s[8:11], s46 offen offset:3072 sc1 lds\n"
      "v_add_u32 v21, 1, v21\n"
      "s_waitcnt vmcnt(9)\n"
      "ds_write_b32 v23, v21\n"
      "buffer_load_dwordx4 v0, s[8:11], s46 offen offset:2048 sc1 lds\n"
      "s_add_u32 m0, s40, 20480\n"
      "s_nop 0\n"
      "buffer_load_dwordx4 v1, s[8:11], s46 offen offset:0 sc1 lds\n"
      "ds_read_b32 v22, v24\n"
      "buffer_load_dwordx4 v[196:199], v13, s[8:11], s47 offen offset:1024 sc1\n"
      "s_sub_u32 s47, s46, s45\n"
      "s_waitcnt lgkmcnt(0)\n"
      "v_cmp_gt_i32 vcc, v21, v22\n"
      "buffer_load_dword v26, v23, s[8:11], s31 offen\n"
      "s_cbranch_vccnz .Lsp_wy0_3\n"
      ".Lbk_wy0_3:\n"
      "ds_read_b128 v[104:107], v0 offset:1024\n"
      "s_waitcnt vmcnt(12)\n"
      "v_lshlrev_b32 v190, 16, v201\n"
      "s_waitcnt vmcnt(11)\n"
      "v_lshlrev_b32 v176, 16, v194\n"
      "ds_read_b128 v[100:103], v0 offset:0\n"
      "v_and_b32 v191, 0xffff0000, v201\n"
      "ds_read_b128 v[120:123], v0 offset:5120\n"
      "v_lshlrev_b32 v174, 16, v193\n"
      "v_lshlrev_b32 v178, 16, v195\n"
      "ds_read_b128 v[112:115], v0 offset:3072\n"
      "ds_read_b128 v[108:111], v0 offset:2048\n"
      "ds_read_b128 v[124:127], v0 offset:6144\n"
      "ds_read_b128 v[116:119], v0 offset:4096\n"
      "v_and_b32 v189, 0xffff0000, v200\n"
      "ds_read_b128 v[140:143], v0 offset:9216\n"
      "s_mov_b32 s46, s36\n"
      "ds_read2_b32 v[132:133], v2 offset0:0 offset1:32\n"
      "v_and_b32 v179, 0xffff0000, v195\n"
      "ds_read_b128 v[128:131], v0 offset:7168\n"
      "ds_read_b128 v[136:139], v0 offset:8192\n"
      "v_lshlrev_b32 v188, 16, v200\n"
      "v_and_b32 v175, 0xffff0000, v193\n"
      "s_mov_b32 s20, 31\n"
      "s_waitcnt lgkmcnt(0)\n"
      "s_nop 4\n"
      "s_branch .Lw_loop\n"
      ".p2align 12\n"
      ".Lw_p0:\n"
      "s_branch .Lw_pr\n"
      ".p2align 5\n"
      ".Lw_loop:\n"
      "v_mfma_f32_16x16x32_bf16 v[188:191], v[64:67], v[100:103], v[188:191]\n"
      "ds_read2_b32 v[134:135], v2 offset0:64 offset1:96\n"
      "v_and_b32 v173, 0xffff0000, v192\n"
      "v_mfma_f32_16x16x32_bf16 v[188:191], v[68:71], v[104:107], v[188:191]\n"
      "s_add_u32 s36, s36, 24576\n"
      "ds_read_b128 v[156:159], v0 offset:13312\n"
      "v_add_f32 v218, v218, v183\n"
      "v_mfma_f32_16x16x32_bf16 v[188:191], v[72:75], v[108:111], v[188:191]\n"
      "v_fmac_f32 v219, v182, v182\n"
      "v_fmac_f32 v219, v183, v183\n"
      "v_mfma_f32_16x16x32_bf16 v[188:191], v[76:79], v[112:115], v[188:191]\n"
      "s_waitcnt lgkmcnt(3)\n"
      "v_add_f32 v218, v218, v184\n"
      "v_fmac_f32 v219, v184, v184\n"
      "v_mfma_f32_16x16x32_bf16 v[188:191], v[80:83], v[116:119], v[188:191]\n"
      "buffer_load_dwordx2 v[200:201], v14, s[8:11], s47 offen sc1\n"
      "v_mfma_f32_16x16x32_bf16 v[188:191], v[84:87], v[120:123], v[188:191]\n"
      "ds_read_b128 v[148:151], v0 offset:11264\n"
      "v_and_b32 v177, 0xffff0000, v194\n"
      "v_mfma_f32_16x16x32_bf16 v[188:191], v[88:91], v[124:127], v[188:191]\n"
      "ds_read_b128 v[160:163], v0 offset:14336\n"
      "v_cvt_pk_bf16_f32 v215, v186, v187\n"
      "v_mfma_f32_16x16x32_bf16 v[188:191], v[92:95], v[128:131], v[188:191]\n"
      "s_waitcnt lgkmcnt(3)\n"
      "v_fmac_f32 v219, v185, v185\n"
      "v_lshlrev_b32 v172, 16, v192\n"
      "v_mfma_f32_16x16x32_bf16 v[188:191], v[96:99], v[132:135], v[188:191]\n"
      "buffer_load_dwordx4 v[192:195], v13, s[8:11], s47 offen sc1\n"
      "v_mfma_f32_16x16x32_bf16 v[172:175], a[0:3], v[100:103], v[172:175]\n"
      "v_fmac_f32 v219, v186, v186\n"
      "v_fmac_f32 v219, v187, v187\n"
      "v_mfma_f32_16x16x32_bf16 v[172:175], a[4:7], v[104:107], v[172:175]\n"
      "ds_read2_b32 v[170:171], v3 offset0:64 offset1:96\n"
      "v_add_u32 v21, 1, v21\n"
      "v_mfma_f32_16x16x32_bf16 v[172:175], a[8:11], v[108:111], v[172:175]\n"
      "ds_read_b128 v[144:147], v0 offset:10240\n"
      "v_add_f32 v218, v218, v185\n"
      "v_mfma_f32_16x16x32_bf16 v[172:175], a[12:15], v[112:115], v[172:175]\n"
      "ds_read2_b32 v[168:169], v3 offset0:0 offset1:32\n"
      "v_mfma_f32_16x16x32_bf16 v[172:175], a[16:19], v[116:119], v[172:175]\n"
      "s_add_u32 m0, s39, 40960\n"
      "ds_read_b128 v[164:167], v0 offset:15360\n"
      "v_add_f32 v218, v218, v186\n"
      "v_mfma_f32_16x16x32_bf16 v[172:175], a[20:23], v[120:123], v[172:175]\n"
      "ds_read_b128 v[152:155], v0 offset:12288\n"
      "v_mfma_f32_16x16x32_bf16 v[172:175], a[24:27], v[124:127], v[172:175]\n"
      "v_mfma_f32_16x16x32_bf16 v[172:175], a[28:31], v[128:131], v[172:175]\n"
      "v_cvt_pk_bf16_f32 v213, v182, v183\n"
      "v_add_f32 v218, v218, v187\n"
      "v_mfma_f32_16x16x32_bf16 v[172:175], a[32:35], v[132:135], v[172:175]\n"
      "ds_write_b64 v18, v[218:219]\n"
      "v_mfma_f32_16x16x32_bf16 v[172:175], a[36:39], v[136:139], v[172:175]\n"
      "v_mfma_f32_16x16x32_bf16 v[172:175], a[40:43], v[140:143], v[172:175]\n"
      "s_waitcnt lgkmcnt(3)\n"
      "buffer_store_dwordx2 v[216:217], v16, s[8:11], s38 offen sc1\n"
      "v_mfma_f32_16x16x32_bf16 v[172:175], a[44:47], v[144:147], v[172:175]\n"
      "ds_write_b32 v23, v21\n"
      "v_mfma_f32_16x16x32_bf16 v[172:175], a[48:51], v[148:151], v[172:175]\n"
      "s_waitcnt lgkmcnt(2)\n"
      "v_cvt_pk_bf16_f32 v214, v184, v185\n"
      "v_mfma_f32_16x16x32_bf16 v[172:175], a[52:55], v[152:155], v[172:175]\n"
      "v_cvt_pk_bf16_f32 v212, v180, v181\n"
      "v_mfma_f32_16x16x32_bf16 v[172:175], a[56:59], v[156:159], v[172:175]\n"
      "buffer_store_dwordx4 v[212:215], v15, s[8:11], s38 offen offset:1024 sc1\n"
      "v_mfma_f32_16x16x32_bf16 v[172:175], a[60:63], v[160:163], v[172:175]\n"
      "v_mfma_f32_16x16x32_bf16 v[172:175], a[64:67], v[164:167], v[172:175]\n"
      "s_waitcnt vmcnt(10) lgkmcnt(1)\n"
      "v_cmp_ne_u32 vcc, s21, v28\n"
      "v_mfma_f32_16x16x32_bf16 v[172:175], a[68:71], v[168:171], v[172:175]\n"
      "v_mfma_f32_16x16x32_bf16 v[176:179], a[72:75], v[100:103], v[176:179]\n"
      "v_mfma_f32_16x16x32_bf16 v[176:179], a[76:79], v[104:107], v[176:179]\n"
      "s_min_u32 s36, s36, s6\n"
      "s_cbranch_vccnz .Lsp_w0_4\n"
      ".Lbk_w0_4:\n"
      "v_mfma_f32_16x16x32_bf16 v[176:179], a[80:83], v[108:111], v[176:179]\n"
      "buffer_load_dword v28, v11, s[8:11], s36 offen sc1\n"
      "v_mfma_f32_16x16x32_bf16 v[176:179], a[84:87], v[112:115], v[176:179]\n"
      "ds_read_b32 v22, v24\n"
      "v_mfma_f32_16x16x32_bf16 v[176:179], a[88:91], v[116:119], v[176:179]\n"
      "v_cvt_pk_bf16_f32 v208, v172, v173\n"
      "v_mfma_f32_16x16x32_bf16 v[176:179], a[92:95], v[120:123], v[176:179]\n"
      "buffer_load_dwordx4 v0, s[8:11], s46 offen offset:0 sc1 lds\n"
      "v_mfma_f32_16x16x32_bf16 v[176:179], a[96:99], v[124:127], v[176:179]\n"
      "v_add_f32 v218, v172, v173\n"
      "v_mfma_f32_16x16x32_bf16 v[176:179], a[100:103], v[128:131], v[176:179]\n"
      "s_waitcnt vmcnt(7) lgkmcnt(1)\n"
      "v_and_b32 v181, 0xffff0000, v196\n"
      "v_mul_f32 v219, v172, v172\n"
      "v_mfma_f32_16x16x32_bf16 v[176:179], a[104:107], v[132:135], v[176:179]\n"
      "s_waitcnt lgkmcnt(0)\n"
      "v_cmp_gt_i32 vcc, v21, v22\n"
      "v_fmac_f32_e64 v219, v173, v173\n"
      "v_mfma_f32_16x16x32_bf16 v[176:179], a[108:111], v[136:139], v[176:179]\n"
      "buffer_store_dword v25, v12, s[8:11], s50 offen sc1\n"
      "v_mfma_f32_16x16x32_bf16 v[176:179], a[112:115], v[140:143], v[176:179]\n"
      "v_lshlrev_b32_e64 v180, 16, v196\n"
      "v_and_b32 v183, 0xffff0000, v197\n"
      "v_mfma_f32_16x16x32_bf16 v[176:179], a[116:119], v[144:147], v[176:179]\n"
      "s_cbranch_vccnz .Lsp_wz0_5\n"
      ".Lbk_wz0_5:\n"
      "v_add_f32 v218, v218, v174\n"
      "v_mfma_f32_16x16x32_bf16 v[176:179], a[120:123], v[148:151], v[176:179]\n"
      "ds_read_b128 v[204:207], v17\n"
      "v_add_u32 v21, 1, v21\n"
      "v_mfma_f32_16x16x32_bf16 v[176:179], a[124:127], v[152:155], v[176:179]\n"
      "buffer_load_dwordx4 v0, s[8:11], s46 offen offset:1024 sc1 lds\n"
      "v_mfma_f32_16x16x32_bf16 v[176:179], a[128:131], v[156:159], v[176:179]\n"
      "v_lshlrev_b32 v182, 16, v197\n"
      "v_mfma_f32_16x16x32_bf16 v[176:179], a[132:135], v[160:163], v[176:179]\n"
      "ds_write_b32 v23, v21\n"
      "v_mfma_f32_16x16x32_bf16 v[176:179], a[136:139], v[164:167], v[176:179]\n"
      "s_add_u32 s50, s50, 24576\n"
      "v_fmac_f32 v219, v174, v174\n"
      "v_add_f32 v218, v218, v175\n"
      "v_mfma_f32_16x16x32_bf16 v[176:179], a[140:143], v[168:171], v[176:179]\n"
      "buffer_load_dwordx4 v0, s[8:11], s46 offen offset:3072 sc1 lds\n"
      "v_mfma_f32_16x16x32_bf16 v[180:183], a[144:147], v[100:103], v[180:183]\n"
      "v_and_b32 v185, 0xffff0000, v198\n"
      "v_mfma_f32_16x16x32_bf16 v[180:183], a[148:151], v[104:107], v[180:183]\n"
      "s_waitcnt lgkmcnt(1)\n"
      "v_cvt_pk_bf16_f32 v209, v174, v175\n"
      "v_add_f32 v190, v190, v206\n"
      "v_mfma_f32_16x16x32_bf16 v[180:183], a[152:155], v[108:111], v[180:183]\n"
      "v_add_f32 v189, v189, v205\n"
      "v_lshlrev_b32 v184, 16, v198\n"
      "v_mfma_f32_16x16x32_bf16 v[180:183], a[156:159], v[112:115], v[180:183]\n"
      "buffer_load_dwordx4 v0, s[8:11], s46 offen offset:2048 sc1 lds\n"
      "v_mfma_f32_16x16x32_bf16 v[180:183], a[160:163], v[116:119], v[180:183]\n"
      "v_cvt_pk_bf16_f32 v211, v178, v179\n"
      "v_add_f32 v191, v191, v207\n"
      "v_mfma_f32_16x16x32_bf16 v[180:183], a[164:167], v[120:123], v[180:183]\n"
      "v_and_b32 v187, 0xffff0000, v199\n"
      "s_add_u32 m0, s40, 40960\n"
      "v_mfma_f32_16x16x32_bf16 v[180:183], a[168:171], v[124:127], v[180:183]\n"
      "v_cvt_pk_bf16_f32 v217, v190, v191\n"
      "v_add_f32 v188, v188, v204\n"
      "v_mfma_f32_16x16x32_bf16 v[180:183], a[172:175], v[128:131], v[180:183]\n"
      "buffer_load_dwordx4 v1, s[8:11], s46 offen offset:0 sc1 lds\n"
      "v_mfma_f32_16x16x32_bf16 v[180:183], a[176:179], v[132:135], v[180:183]\n"
      "ds_read_b32 v22, v24\n"
      "v_lshlrev_b32 v186, 16, v199\n"
      "v_mfma_f32_16x16x32_bf16 v[180:183], a[180:183], v[136:139], v[180:183]\n"
      "v_fmac_f32 v219, v175, v175\n"
      "v_add_f32 v218, v218, v176\n"
      "v_mfma_f32_16x16x32_bf16 v[180:183], a[184:187], v[140:143], v[180:183]\n"
      "v_fmac_f32 v219, v176, v176\n"
      "v_fmac_f32 v219, v177, v177\n"
      "v_mfma_f32_16x16x32_bf16 v[180:183], a[188:191], v[144:147], v[180:183]\n"
      "v_fmac_f32 v219, v178, v178\n"
      "v_fmac_f32 v219, v179, v179\n"
      "v_mfma_f32_16x16x32_bf16 v[180:183], a[192:195], v[148:151], v[180:183]\n"
      "v_fmac_f32 v219, v188, v188\n"
      "v_fmac_f32 v219, v189, v189\n"
      "v_mfma_f32_16x16x32_bf16 v[180:183], a[196:199], v[152:155], v[180:183]\n"
      "buffer_load_dwordx4 v[196:199], v13, s[8:11], s47 offen offset:1024 sc1\n"
      "v_mfma_f32_16x16x32_bf16 v[180:183], a[200:203], v[156:159], v[180:183]\n"
      "v_add_f32 v218, v218, v177\n"
      "v_mfma_f32_16x16x32_bf16 v[180:183], a[204:207], v[160:163], v[180:183]\n"
      "v_add_f32 v218, v218, v178\n"
      "v_add_f32 v218, v218, v179\n"
      "v_mfma_f32_16x16x32_bf16 v[180:183], a[208:211], v[164:167], v[180:183]\n"
      "s_waitcnt lgkmcnt(0)\n"
      "v_add_f32 v218, v218, v188\n"
      "v_cmp_gt_i32 vcc, v21, v22\n"
      "v_mfma_f32_16x16x32_bf16 v[180:183], a[212:215], v[168:171], v[180:183]\n"
      "v_add_f32 v218, v218, v189\n"
      "v_fmac_f32 v219, v190, v190\n"
      "v_mfma_f32_16x16x32_bf16 v[184:187], a[216:219], v[100:103], v[184:187]\n"
      "v_cvt_pk_bf16_f32 v216, v188, v189\n"
      "v_add_f32 v218, v218, v190\n"
      "v_mfma_f32_16x16x32_bf16 v[184:187], a[220:223], v[104:107], v[184:187]\n"
      "s_cbranch_vccnz .Lsp_wy1_6\n"
      ".Lbk_wy1_6:\n"
      "v_add_f32 v218, v218, v191\n"
      "v_mfma_f32_16x16x32_bf16 v[184:187], a[224:227], v[108:111], v[184:187]\n"
      "s_waitcnt vmcnt(11)\n"
      "ds_read_b128 v[108:111], v0 offset:22528\n"
      "v_lshlrev_b32 v190, 16, v201\n"
      "v_mfma_f32_16x16x32_bf16 v[184:187], a[228:231], v[112:115], v[184:187]\n"
      "s_add_u32 s38, s38, 24576\n"
      "v_lshlrev_b32 v188, 16, v200\n"
      "v_mfma_f32_16x16x32_bf16 v[184:187], a[232:235], v[116:119], v[184:187]\n"
      "ds_read_b128 v[100:103], v0 offset:20480\n"
      "v_mfma_f32_16x16x32_bf16 v[184:187], a[236:239], v[120:123], v[184:187]\n"
      "ds_read_b128 v[116:119], v0 offset:24576\n"
      "v_cvt_pk_bf16_f32 v210, v176, v177\n"
      "v_mfma_f32_16x16x32_bf16 v[184:187], a[240:243], v[124:127], v[184:187]\n"
      "ds_read_b128 v[104:107], v0 offset:21504\n"
      "v_fmac_f32 v219, v191, v191\n"
      "v_mfma_f32_16x16x32_bf16 v[184:187], a[244:247], v[128:131], v[184:187]\n"
      "v_and_b32 v189, 0xffff0000, v200\n"
      "s_sub_u32 s47, s46, s45\n"
      "v_mfma_f32_16x16x32_bf16 v[184:187], a[248:251], v[132:135], v[184:187]\n"
      "v_mfma_f32_16x16x32_bf16 v[184:187], a[252:255], v[136:139], v[184:187]\n"
      "s_waitcnt vmcnt(10) lgkmcnt(3)\n"
      "ds_read_b128 v[112:115], v0 offset:23552\n"
      "v_lshlrev_b32 v178, 16, v195\n"
      "v_mfma_f32_16x16x32_bf16 v[184:187], v[32:35], v[140:143], v[184:187]\n"
      "ds_read_b128 v[140:143], v0 offset:29696\n"
      "v_and_b32 v173, 0xffff0000, v192\n"
      "v_mfma_f32_16x16x32_bf16 v[184:187], v[36:39], v[144:147], v[184:187]\n"
      "buffer_store_dwordx4 v[208:211], v15, s[8:11], s38 offen sc1\n"
      "v_mfma_f32_16x16x32_bf16 v[184:187], v[40:43], v[148:151], v[184:187]\n"
      "ds_read_b128 v[120:123], v0 offset:25600\n"
      "v_fmac_f32 v219, v180, v180\n"
      "v_mfma_f32_16x16x32_bf16 v[184:187], v[44:47], v[152:155], v[184:187]\n"
      "ds_read2_b32 v[134:135], v4 offset0:64 offset1:96\n"
      "v_and_b32 v191, 0xffff0000, v201\n"
      "v_mfma_f32_16x16x32_bf16 v[184:187], v[48:51], v[156:159], v[184:187]\n"
      "ds_read2_b32 v[132:133], v4 offset0:0 offset1:32\n"
      "v_lshlrev_b32 v174, 16, v193\n"
      "v_mfma_f32_16x16x32_bf16 v[184:187], v[52:55], v[160:163], v[184:187]\n"
      "ds_read_b128 v[156:159], v0 offset:33792\n"
      "v_fmac_f32 v219, v181, v181\n"
      "v_mfma_f32_16x16x32_bf16 v[184:187], v[56:59], v[164:167], v[184:187]\n"
      "ds_read_b128 v[124:127], v0 offset:26624\n"
      "v_add_f32 v218, v218, v180\n"
      "v_mfma_f32_16x16x32_bf16 v[184:187], v[60:63], v[168:171], v[184:187]\n"
      "s_waitcnt lgkmcnt(5)\n"
      "ds_read_b128 v[128:131], v0 offset:27648\n"
      "v_lshlrev_b32 v172, 16, v192\n"
      "v_mfma_f32_16x16x32_bf16 v[188:191], v[64:67], v[100:103], v[188:191]\n"
      "v_lshlrev_b32 v176, 16, v194\n"
      "v_and_b32 v177, 0xffff0000, v194\n"
      "v_mfma_f32_16x16x32_bf16 v[188:191], v[68:71], v[104:107], v[188:191]\n"
      "buffer_load_dwordx2 v[200:201], v14, s[8:11], s47 offen sc1\n"
      "v_mfma_f32_16x16x32_bf16 v[188:191], v[72:75], v[108:111], v[188:191]\n"
      "v_and_b32 v175, 0xffff0000, v193\n"
      "v_and_b32 v179, 0xffff0000, v195\n"
      "v_mfma_f32_16x16x32_bf16 v[188:191], v[76:79], v[112:115], v[188:191]\n"
      "ds_read_b128 v[144:147], v0 offset:30720\n"
      "v_add_f32 v218, v218, v181\n"
      "v_mfma_f32_16x16x32_bf16 v[188:191], v[80:83], v[116:119], v[188:191]\n"
      "s_waitcnt lgkmcnt(3)\n"
      "ds_read_b128 v[136:139], v0 offset:28672\n"
      "v_fmac_f32 v219, v182, v182\n"
      "v_mfma_f32_16x16x32_bf16 v[188:191], v[84:87], v[120:123], v[188:191]\n"
      "s_waitcnt lgkmcnt(3)\n"
      "v_fmac_f32 v219, v183, v183\n"
      "v_fmac_f32 v219, v184, v184\n"
      "v_mfma_f32_16x16x32_bf16 v[188:191], v[88:91], v[124:127], v[188:191]\n"
      "s_waitcnt lgkmcnt(2)\n"
      "buffer_load_dwordx4 v[192:195], v13, s[8:11], s47 offen sc1\n"
      "v_mfma_f32_16x16x32_bf16 v[188:191], v[92:95], v[128:131], v[188:191]\n"
      "ds_read2_b32 v[170:171], v5 offset0:64 offset1:96\n"
      "v_add_f32 v218, v218, v182\n"
      "v_mfma_f32_16x16x32_bf16 v[188:191], v[96:99], v[132:135], v[188:191]\n"
      "v_add_f32 v218, v218, v183\n"
      "v_mfma_f32_16x16x32_bf16 v[172:175], a[0:3], v[100:103], v[172:175]\n"
      "ds_read_b128 v[160:163], v0 offset:34816\n"
      "v_add_u32 v21, 1, v21\n"
      "v_mfma_f32_16x16x32_bf16 v[172:175], a[4:7], v[104:107], v[172:175]\n"
      "ds_read_b128 v[148:151], v0 offset:31744\n"
      "v_add_f32 v218, v218, v184\n"
      "v_mfma_f32_16x16x32_bf16 v[172:175], a[8:11], v[108:111], v[172:175]\n"
      "ds_read2_b32 v[168:169], v5 offset0:0 offset1:32\n"
      "s_mov_b32 s46, s36\n"
      "v_mfma_f32_16x16x32_bf16 v[172:175], a[12:15], v[112:115], v[172:175]\n"
      "ds_read_b128 v[152:155], v0 offset:32768\n"
      "v_add_f32 v218, v218, v185\n"
      "v_mfma_f32_16x16x32_bf16 v[172:175], a[16:19], v[116:119], v[172:175]\n"
      "buffer_store_dwordx2 v[216:217], v16, s[8:11], s38 offen sc1\n"
      "v_mfma_f32_16x16x32_bf16 v[172:175], a[20:23], v[120:123], v[172:175]\n"
      "ds_read_b128 v[164:167], v0 offset:35840\n"
      "v_add_f32 v218, v218, v186\n"
      "v_mfma_f32_16x16x32_bf16 v[172:175], a[24:27], v[124:127], v[172:175]\n"
      "v_fmac_f32 v219, v185, v185\n"
      "v_fmac_f32 v219, v186, v186\n"
      "v_mfma_f32_16x16x32_bf16 v[172:175], a[28:31], v[128:131], v[172:175]\n"
      "v_add_f32 v218, v218, v187\n"
      "v_fmac_f32 v219, v187, v187\n"
      "v_mfma_f32_16x16x32_bf16 v[172:175], a[32:35], v[132:135], v[172:175]\n"
      "s_waitcnt lgkmcnt(3)\n"
      "ds_write_b64 v18, v[218:219]\n"
      "v_mfma_f32_16x16x32_bf16 v[172:175], a[36:39], v[136:139], v[172:175]\n"
      "s_waitcnt vmcnt(12) lgkmcnt(3)\n"
      "buffer_store_dword v25, v12, s[8:11], s50 offen sc1\n"
      "v_mfma_f32_16x16x32_bf16 v[172:175], a[40:43], v[140:143], v[172:175]\n"
      "v_cvt_pk_bf16_f32 v213, v182, v183\n"
      "v_cvt_pk_bf16_f32 v215, v186, v187\n"
      "v_mfma_f32_16x16x32_bf16 v[172:175], a[44:47], v[144:147], v[172:175]\n"
      "ds_write_b32 v23, v21\n"
      "v_mfma_f32_16x16x32_bf16 v[172:175], a[48:51], v[148:151], v[172:175]\n"
      "s_waitcnt lgkmcnt(2)\n"
      "v_cvt_pk_bf16_f32 v212, v180, v181\n"
      "v_cvt_pk_bf16_f32 v214, v184, v185\n"
      "v_mfma_f32_16x16x32_bf16 v[172:175], a[52:55], v[152:155], v[172:175]\n"
      "buffer_store_dwordx4 v[212:215], v15, s[8:11], s38 offen offset:1024 sc1\n"
      "v_mfma_f32_16x16x32_bf16 v[172:175], a[56:59], v[156:159], v[172:175]\n"
      "s_add_u32 m0, s39, 61440\n"
      "s_add_u32 s38, s38, 24576\n"
      "v_mfma_f32_16x16x32_bf16 v[172:175], a[60:63], v[160:163], v[172:175]\n"
      "s_waitcnt vmcnt(13) lgkmcnt(1)\n"
      "v_cmp_ne_u32 vcc, s21, v28\n"
      "v_mfma_f32_16x16x32_bf16 v[172:175], a[64:67], v[164:167], v[172:175]\n"
      "v_mfma_f32_16x16x32_bf16 v[172:175], a[68:71], v[168:171], v[172:175]\n"
      "v_mfma_f32_16x16x32_bf16 v[176:179], a[72:75], v[100:103], v[176:179]\n"
      "s_cmp_eq_u32 s36, s37\n"
      "s_cselect_b32 s48, 49152, s51\n"
      "v_mfma_f32_16x16x32_bf16 v[176:179], a[76:79], v[104:107], v[176:179]\n"
      "s_cbranch_vccnz .Lsp_w1_7\n"
      ".Lbk_w1_7:\n"
      "v_mfma_f32_16x16x32_bf16 v[176:179], a[80:83], v[108:111], v[176:179]\n"
      "buffer_load_dwordx4 v0, s[8:11], s46 offen offset:0 sc1 lds\n"
      "v_mfma_f32_16x16x32_bf16 v[176:179], a[84:87], v[112:115], v[176:179]\n"
      "ds_read_b32 v22, v24\n"
      "v_cvt_pk_bf16_f32 v208, v172, v173\n"
      "v_mfma_f32_16x16x32_bf16 v[176:179], a[88:91], v[116:119], v[176:179]\n"
      "v_add_f32 v218, v172, v173\n"
      "v_mul_f32 v219, v172, v172\n"
      "v_mfma_f32_16x16x32_bf16 v[176:179], a[92:95], v[120:123], v[176:179]\n"
      "s_add_u32 s36, s36, s48\n"
      "v_add_f32 v218, v218, v174\n"
      "v_mfma_f32_16x16x32_bf16 v[176:179], a[96:99], v[124:127], v[176:179]\n"
      "s_min_u32 s36, s36, s6\n"
      "v_fmac_f32 v219, v173, v173\n"
      "v_mfma_f32_16x16x32_bf16 v[176:179], a[100:103], v[128:131], v[176:179]\n"
      "buffer_load_dword v28, v11, s[8:11], s36 offen sc1\n"
      "v_mfma_f32_16x16x32_bf16 v[176:179], a[104:107], v[132:135], v[176:179]\n"
      "s_waitcnt lgkmcnt(0)\n"
      "v_cmp_gt_i32 vcc, v21, v22\n"
      "v_fmac_f32 v219, v174, v174\n"
      "v_mfma_f32_16x16x32_bf16 v[176:179], a[108:111], v[136:139], v[176:179]\n"
      "buffer_load_dwordx4 v0, s[8:11], s46 offen offset:1024 sc1 lds\n"
      "v_mfma_f32_16x16x32_bf16 v[176:179], a[112:115], v[140:143], v[176:179]\n"
      "s_waitcnt vmcnt(9)\n"
      "v_lshlrev_b32 v182, 16, v197\n"
      "v_and_b32 v185, 0xffff0000, v198\n"
      "v_mfma_f32_16x16x32_bf16 v[176:179], a[116:119], v[144:147], v[176:179]\n"
      "v_lshlrev_b32 v180, 16, v196\n"
      "v_and_b32 v183, 0xffff0000, v197\n"
      "v_mfma_f32_16x16x32_bf16 v[176:179], a[120:123], v[148:151], v[176:179]\n"
      "s_cbranch_vccnz .Lsp_wz1_8\n"
      ".Lbk_wz1_8:\n"
      "ds_read_b128 v[204:207], v17\n"
      "v_mfma_f32_16x16x32_bf16 v[176:179], a[124:127], v[152:155], v[176:179]\n"
      "buffer_load_dwordx4 v0, s[8:11], s46 offen offset:3072 sc1 lds\n"
      "v_mfma_f32_16x16x32_bf16 v[176:179], a[128:131], v[156:159], v[176:179]\n"
      "v_add_f32 v218, v218, v175\n"
      "v_mfma_f32_16x16x32_bf16 v[176:179], a[132:135], v[160:163], v[176:179]\n"
      "v_add_u32 v21, 1, v21\n"
      "v_lshlrev_b32 v184, 16, v198\n"
      "v_mfma_f32_16x16x32_bf16 v[176:179], a[136:139], v[164:167], v[176:179]\n"
      "v_and_b32 v181, 0xffff0000, v196\n"
      "v_cvt_pk_bf16_f32 v209, v174, v175\n"
      "v_mfma_f32_16x16x32_bf16 v[176:179], a[140:143], v[168:171], v[176:179]\n"
      "ds_write_b32 v23, v21\n"
      "v_mfma_f32_16x16x32_bf16 v[180:183], a[144:147], v[100:103], v[180:183]\n"
      "buffer_load_dwordx4 v0, s[8:11], s46 offen offset:2048 sc1 lds\n"
      "v_mfma_f32_16x16x32_bf16 v[180:183], a[148:151], v[104:107], v[180:183]\n"
      "s_waitcnt lgkmcnt(1)\n"
      "v_add_f32 v188, v188, v204\n"
      "v_add_f32 v190, v190, v206\n"
      "v_mfma_f32_16x16x32_bf16 v[180:183], a[152:155], v[108:111], v[180:183]\n"
      "v_add_f32 v191, v191, v207\n"
      "v_lshlrev_b32 v186, 16, v199\n"
      "v_mfma_f32_16x16x32_bf16 v[180:183], a[156:159], v[112:115], v[180:183]\n"
      "v_add_f32 v218, v218, v176\n"
      "v_fmac_f32 v219, v175, v175\n"
      "v_mfma_f32_16x16x32_bf16 v[180:183], a[160:163], v[116:119], v[180:183]\n"
      "s_add_u32 m0, s40, 61440\n"
      "v_cvt_pk_bf16_f32 v217, v190, v191\n"
      "v_add_f32 v218, v218, v177\n"
      "v_mfma_f32_16x16x32_bf16 v[180:183], a[164:167], v[120:123], v[180:183]\n"
      "s_add_u32 s50, s50, 24576\n"
      "v_cvt_pk_bf16_f32 v211, v178, v179\n"
      "v_and_b32 v187, 0xffff0000, v199\n"
      "v_mfma_f32_16x16x32_bf16 v[180:183], a[168:171], v[124:127], v[180:183]\n"
      "buffer_load_dwordx4 v1, s[8:11], s46 offen offset:0 sc1 lds\n"
      "v_mfma_f32_16x16x32_bf16 v[180:183], a[172:175], v[128:131], v[180:183]\n"
      "v_add_f32 v218, v218, v178\n"
      "v_add_f32 v189, v189, v205\n"
      "v_mfma_f32_16x16x32_bf16 v[180:183], a[176:179], v[132:135], v[180:183]\n"
      "v_fmac_f32 v219, v176, v176\n"
      "v_mfma_f32_16x16x32_bf16 v[180:183], a[180:183], v[136:139], v[180:183]\n"
      "ds_read_b32 v22, v24\n"
      "v_mfma_f32_16x16x32_bf16 v[180:183], a[184:187], v[140:143], v[180:183]\n"
      "buffer_load_dwordx4 v[196:199], v13, s[8:11], s47 offen offset:1024 sc1\n"
      "v_mfma_f32_16x16x32_bf16 v[180:183], a[188:191], v[144:147], v[180:183]\n"
      "v_fmac_f32 v219, v177, v177\n"
      "v_add_f32 v218, v218, v179\n"
      "v_mfma_f32_16x16x32_bf16 v[180:183], a[192:195], v[148:151], v[180:183]\n"
      "v_fmac_f32 v219, v178, v178\n"
      "v_fmac_f32 v219, v179, v179\n"
      "v_mfma_f32_16x16x32_bf16 v[180:183], a[196:199], v[152:155], v[180:183]\n"
      "v_fmac_f32 v219, v188, v188\n"
      "v_fmac_f32 v219, v189, v189\n"
      "v_mfma_f32_16x16x32_bf16 v[180:183], a[200:203], v[156:159], v[180:183]\n"
      "s_sub_u32 s47, s46, s45\n"
      "v_fmac_f32 v219, v190, v190\n"
      "v_add_f32 v218, v218, v188\n"
      "v_mfma_f32_16x16x32_bf16 v[180:183], a[204:207], v[160:163], v[180:183]\n"
      "v_add_f32 v218, v218, v189\n"
      "v_add_f32 v218, v218, v190\n"
      "v_mfma_f32_16x16x32_bf16 v[180:183], a[208:211], v[164:167], v[180:183]\n"
      "s_waitcnt lgkmcnt(0)\n"
      "v_cvt_pk_bf16_f32 v210, v176, v177\n"
      "v_cmp_gt_i32 vcc, v21, v22\n"
      "v_mfma_f32_16x16x32_bf16 v[180:183], a[212:215], v[168:171], v[180:183]\n"
      "v_add_f32 v218, v218, v191\n"
      "v_fmac_f32 v219, v191, v191\n"
      "v_mfma_f32_16x16x32_bf16 v[184:187], a[216:219], v[100:103], v[184:187]\n"
      "buffer_store_dwordx4 v[208:211], v15, s[8:11], s38 offen sc1\n"
      "v_mfma_f32_16x16x32_bf16 v[184:187], a[220:223], v[104:107], v[184:187]\n"
      "s_cbranch_vccnz .Lsp_wy0_9\n"
      ".Lbk_wy0_9:\n"
      "v_mfma_f32_16x16x32_bf16 v[184:187], a[224:227], v[108:111], v[184:187]\n"
      "s_waitcnt vmcnt(12)\n"
      "ds_read_b128 v[104:107], v10 offset:1024\n"
      "v_lshlrev_b32 v190, 16, v201\n"
      "v_mfma_f32_16x16x32_bf16 v[184:187], a[228:231], v[112:115], v[184:187]\n"
      "s_waitcnt vmcnt(11)\n"
      "v_lshlrev_b32 v176, 16, v194\n"
      "v_mfma_f32_16x16x32_bf16 v[184:187], a[232:235], v[116:119], v[184:187]\n"
      "ds_read_b128 v[100:103], v10 offset:0\n"
      "v_and_b32 v191, 0xffff0000, v201\n"
      "v_mfma_f32_16x16x32_bf16 v[184:187], a[236:239], v[120:123], v[184:187]\n"
      "ds_read_b128 v[120:123], v10 offset:5120\n"
      "v_lshlrev_b32 v174, 16, v193\n"
      "v_mfma_f32_16x16x32_bf16 v[184:187], a[240:243], v[124:127], v[184:187]\n"
      "v_add_f32 v218, v218, v180\n"
      "v_lshlrev_b32 v178, 16, v195\n"
      "v_mfma_f32_16x16x32_bf16 v[184:187], a[244:247], v[128:131], v[184:187]\n"
      "ds_read_b128 v[112:115], v10 offset:3072\n"
      "v_mfma_f32_16x16x32_bf16 v[184:187], a[248:251], v[132:135], v[184:187]\n"
      "ds_read_b128 v[108:111], v10 offset:2048\n"
      "v_cvt_pk_bf16_f32 v216, v188, v189\n"
      "v_mfma_f32_16x16x32_bf16 v[184:187], a[252:255], v[136:139], v[184:187]\n"
      "ds_read_b128 v[124:127], v10 offset:6144\n"
      "v_add_f32 v218, v218, v181\n"
      "v_mfma_f32_16x16x32_bf16 v[184:187], v[32:35], v[140:143], v[184:187]\n"
      "ds_read_b128 v[116:119], v10 offset:4096\n"
      "v_and_b32 v189, 0xffff0000, v200\n"
      "v_mfma_f32_16x16x32_bf16 v[184:187], v[36:39], v[144:147], v[184:187]\n"
      "ds_read_b128 v[140:143], v10 offset:9216\n"
      "s_mov_b32 s46, s36\n"
      "v_mfma_f32_16x16x32_bf16 v[184:187], v[40:43], v[148:151], v[184:187]\n"
      "ds_read2_b32 v[132:133], v6 offset0:0 offset1:32\n"
      "v_and_b32 v179, 0xffff0000, v195\n"
      "v_mfma_f32_16x16x32_bf16 v[184:187], v[44:47], v[152:155], v[184:187]\n"
      "v_add_f32 v218, v218, v182\n"
      "v_mfma_f32_16x16x32_bf16 v[184:187], v[48:51], v[156:159], v[184:187]\n"
      "ds_read_b128 v[128:131], v10 offset:7168\n"
      "v_fmac_f32 v219, v180, v180\n"
      "v_mfma_f32_16x16x32_bf16 v[184:187], v[52:55], v[160:163], v[184:187]\n"
      "ds_read_b128 v[136:139], v10 offset:8192\n"
      "v_lshlrev_b32 v188, 16, v200\n"
      "v_mfma_f32_16x16x32_bf16 v[184:187], v[56:59], v[164:167], v[184:187]\n"
      "s_waitcnt lgkmcnt(5)\n"
      "v_fmac_f32 v219, v181, v181\n"
      "v_and_b32 v175, 0xffff0000, v193\n"
      "v_mfma_f32_16x16x32_bf16 v[184:187], v[60:63], v[168:171], v[184:187]\n"
      "s_sub_u32 s20, s20, 1\n"
      "s_cbranch_scc1 .Lw_tail\n"
      "v_mfma_f32_16x16x32_bf16 v[188:191], v[64:67], v[100:103], v[188:191]\n"
      "ds_read2_b32 v[134:135], v6 offset0:64 offset1:96\n"
      "v_and_b32 v173, 0xffff0000, v192\n"
      "v_mfma_f32_16x16x32_bf16 v[188:191], v[68:71], v[104:107], v[188:191]\n"
      "s_add_u32 s36, s36, 24576\n"
      "ds_read_b128 v[156:159], v10 offset:13312\n"
      "v_add_f32 v218, v218, v183\n"
      "v_mfma_f32_16x16x32_bf16 v[188:191], v[72:75], v[108:111], v[188:191]\n"
      "v_fmac_f32 v219, v182, v182\n"
      "v_fmac_f32 v219, v183, v183\n"
      "v_mfma_f32_16x16x32_bf16 v[188:191], v[76:79], v[112:115], v[188:191]\n"
      "s_waitcnt lgkmcnt(3)\n"
      "v_add_f32 v218, v218, v184\n"
      "v_fmac_f32 v219, v184, v184\n"
      "v_mfma_f32_16x16x32_bf16 v[188:191], v[80:83], v[116:119], v[188:191]\n"
      "buffer_load_dwordx2 v[200:201], v14, s[8:11], s47 offen sc1\n"
      "v_mfma_f32_16x16x32_bf16 v[188:191], v[84:87], v[120:123], v[188:191]\n"
      "ds_read_b128 v[148:151], v10 offset:11264\n"
      "v_and_b32 v177, 0xffff0000, v194\n"
      "v_mfma_f32_16x16x32_bf16 v[188:191], v[88:91], v[124:127], v[188:191]\n"
      "ds_read_b128 v[160:163], v10 offset:14336\n"
      "v_cvt_pk_bf16_f32 v215, v186, v187\n"
      "v_mfma_f32_16x16x32_bf16 v[188:191], v[92:95], v[128:131], v[188:191]\n"
      "s_waitcnt lgkmcnt(3)\n"
      "v_fmac_f32 v219, v185, v185\n"
      "v_lshlrev_b32 v172, 16, v192\n"
      "v_mfma_f32_16x16x32_bf16 v[188:191], v[96:99], v[132:135], v[188:191]\n"
      "buffer_load_dwordx4 v[192:195], v13, s[8:11], s47 offen sc1\n"
      "v_mfma_f32_16x16x32_bf16 v[172:175], a[0:3], v[100:103], v[172:175]\n"
      "v_fmac_f32 v219, v186, v186\n"
      "v_fmac_f32 v219, v187, v187\n"
      "v_mfma_f32_16x16x32_bf16 v[172:175], a[4:7], v[104:107], v[172:175]\n"
      "ds_read2_b32 v[170:171], v7 offset0:64 offset1:96\n"
      "v_add_u32 v21, 1, v21\n"
      "v_mfma_f32_16x16x32_bf16 v[172:175], a[8:11], v[108:111], v[172:175]\n"
      "ds_read_b128 v[144:147], v10 offset:10240\n"
      "v_add_f32 v218, v218, v185\n"
      "v_mfma_f32_16x16x32_bf16 v[172:175], a[12:15], v[112:115], v[172:175]\n"
      "ds_read2_b32 v[168:169], v7 offset0:0 offset1:32\n"
      "v_mfma_f32_16x16x32_bf16 v[172:175], a[16:19], v[116:119], v[172:175]\n"
      "s_add_u32 m0, s39, 0\n"
      "ds_read_b128 v[164:167], v10 offset:15360\n"
      "v_add_f32 v218, v218, v186\n"
      "v_mfma_f32_16x16x32_bf16 v[172:175], a[20:23], v[120:123], v[172:175]\n"
      "ds_read_b128 v[152:155], v10 offset:12288\n"
      "v_mfma_f32_16x16x32_bf16 v[172:175], a[24:27], v[124:127], v[172:175]\n"
      "v_mfma_f32_16x16x32_bf16 v[172:175], a[28:31], v[128:131], v[172:175]\n"
      "v_cvt_pk_bf16_f32 v213, v182, v183\n"
      "v_add_f32_e64 v218, v218, v187\n"
      "v_mfma_f32_16x16x32_bf16 v[172:175], a[32:35], v[132:135], v[172:175]\n"
      "ds_write_b64 v18, v[218:219]\n"
      "v_mfma_f32_16x16x32_bf16 v[172:175], a[36:39], v[136:139], v[172:175]\n"
      "v_mfma_f32_16x16x32_bf16 v[172:175], a[40:43], v[140:143], v[172:175]\n"
      "s_waitcnt lgkmcnt(3)\n"
      "buffer_store_dwordx2 v[216:217], v16, s[8:11], s38 offen sc1\n"
      "v_mfma_f32_16x16x32_bf16 v[172:175], a[44:47], v[144:147], v[172:175]\n"
      "ds_write_b32 v23, v21\n"
      "v_mfma_f32_16x16x32_bf16 v[172:175], a[48:51], v[148:151], v[172:175]\n"
      "s_waitcnt lgkmcnt(2)\n"
      "v_cvt_pk_bf16_f32 v214, v184, v185\n"
      "v_mfma_f32_16x16x32_bf16 v[172:175], a[52:55], v[152:155], v[172:175]\n"
      "v_cvt_pk_bf16_f32 v212, v180, v181\n"
      "v_mfma_f32_16x16x32_bf16 v[172:175], a[56:59], v[156:159], v[172:175]\n"
      "buffer_store_dwordx4 v[212:215], v15, s[8:11], s38 offen offset:1024 sc1\n"
      "v_mfma_f32_16x16x32_bf16 v[172:175], a[60:63], v[160:163], v[172:175]\n"
      "v_mfma_f32_16x16x32_bf16 v[172:175], a[64:67], v[164:167], v[172:175]\n"
      "s_waitcnt vmcnt(10) lgkmcnt(1)\n"
      "v_cmp_ne_u32 vcc, s21, v28\n"
      "v_mfma_f32_16x16x32_bf16 v[172:175], a[68:71], v[168:171], v[172:175]\n"
      "v_mfma_f32_16x16x32_bf16 v[176:179], a[72:75], v[100:103], v[176:179]\n"
      "v_mfma_f32_16x16x32_bf16 v[176:179], a[76:79], v[104:107], v[176:179]\n"
      "s_min_u32 s36, s36, s6\n"
      "s_cbranch_vccnz .Lsp_w0_10\n"
      ".Lbk_w0_10:\n"
      "v_mfma_f32_16x16x32_bf16 v[176:179], a[80:83], v[108:111], v[176:179]\n"
      "buffer_load_dword v28, v11, s[8:11], s36 offen sc1\n"
      "v_mfma_f32_16x16x32_bf16 v[176:179], a[84:87], v[112:115], v[176:179]\n"
      "ds_read_b32 v22, v24\n"
      "v_mfma_f32_16x16x32_bf16 v[176:179], a[88:91], v[116:119], v[176:179]\n"
      "v_cvt_pk_bf16_f32 v208, v172, v173\n"
      "v_mfma_f32_16x16x32_bf16 v[176:179], a[92:95], v[120:123], v[176:179]\n"
      "buffer_load_dwordx4 v0, s[8:11], s46 offen offset:0 sc1 lds\n"
      "v_mfma_f32_16x16x32_bf16 v[176:179], a[96:99], v[124:127], v[176:179]\n"
      "v_add_f32 v218, v172, v173\n"
      "v_mfma_f32_16x16x32_bf16 v[176:179], a[100:103], v[128:131], v[176:179]\n"
      "s_waitcnt vmcnt(7) lgkmcnt(1)\n"
      "v_and_b32 v181, 0xffff0000, v196\n"
      "v_mul_f32 v219, v172, v172\n"
      "v_mfma_f32_16x16x32_bf16 v[176:179], a[104:107], v[132:135], v[176:179]\n"
      "s_waitcnt lgkmcnt(0)\n"
      "v_cmp_gt_i32 vcc, v21, v22\n"
      "v_fmac_f32 v219, v173, v173\n"
      "v_mfma_f32_16x16x32_bf16 v[176:179], a[108:111], v[136:139], v[176:179]\n"
      "buffer_store_dword v25, v12, s[8:11], s50 offen sc1\n"
      "v_mfma_f32_16x16x32_bf16 v[176:179], a[112:115], v[140:143], v[176:179]\n"
      "v_lshlrev_b32 v180, 16, v196\n"
      "v_and_b32 v183, 0xffff0000, v197\n"
      "v_mfma_f32_16x16x32_bf16 v[176:179], a[116:119], v[144:147], v[176:179]\n"
      "s_cbranch_vccnz .Lsp_wz0_11\n"
      ".Lbk_wz0_11:\n"
      "v_add_f32 v218, v218, v174\n"
      "v_mfma_f32_16x16x32_bf16 v[176:179], a[120:123], v[148:151], v[176:179]\n"
      "ds_read_b128 v[204:207], v17\n"
      "v_add_u32 v21, 1, v21\n"
      "v_mfma_f32_16x16x32_bf16 v[176:179], a[124:127], v[152:155], v[176:179]\n"
      "buffer_load_dwordx4 v0, s[8:11], s46 offen offset:1024 sc1 lds\n"
      "v_mfma_f32_16x16x32_bf16 v[176:179], a[128:131], v[156:159], v[176:179]\n"
      "v_lshlrev_b32 v182, 16, v197\n"
      "v_mfma_f32_16x16x32_bf16 v[176:179], a[132:135], v[160:163], v[176:179]\n"
      "ds_write_b32 v23, v21\n"
      "v_mfma_f32_16x16x32_bf16 v[176:179], a[136:139], v[164:167], v[176:179]\n"
      "s_add_u32 s50, s50, 24576\n"
      "v_fmac_f32 v219, v174, v174\n"
      "v_add_f32 v218, v218, v175\n"
      "v_mfma_f32_16x16x32_bf16 v[176:179], a[140:143], v[168:171], v[176:179]\n"
      "buffer_load_dwordx4 v0, s[8:11], s46 offen offset:3072 sc1 lds\n"
      "v_mfma_f32_16x16x32_bf16 v[180:183], a[144:147], v[100:103], v[180:183]\n"
      "v_and_b32 v185, 0xffff0000, v198\n"
      "v_mfma_f32_16x16x32_bf16 v[180:183], a[148:151], v[104:107], v[180:183]\n"
      "s_waitcnt lgkmcnt(1)\n"
      "v_cvt_pk_bf16_f32 v209, v174, v175\n"
      "v_add_f32 v190, v190, v206\n"
      "v_mfma_f32_16x16x32_bf16 v[180:183], a[152:155], v[108:111], v[180:183]\n"
      "v_add_f32 v189, v189, v205\n"
      "v_lshlrev_b32 v184, 16, v198\n"
      "v_mfma_f32_16x16x32_bf16 v[180:183], a[156:159], v[112:115], v[180:183]\n"
      "buffer_load_dwordx4 v0, s[8:11], s46 offen offset:2048 sc1 lds\n"
      "v_mfma_f32_16x16x32_bf16 v[180:183], a[160:163], v[116:119], v[180:183]\n"
      "v_cvt_pk_bf16_f32 v211, v178, v179\n"
      "v_add_f32 v191, v191, v207\n"
      "v_mfma_f32_16x16x32_bf16 v[180:183], a[164:167], v[120:123], v[180:183]\n"
      "v_and_b32 v187, 0xffff0000, v199\n"
      "s_add_u32 m0, s40, 0\n"
      "v_mfma_f32_16x16x32_bf16 v[180:183], a[168:171], v[124:127], v[180:183]\n"
      "v_cvt_pk_bf16_f32 v217, v190, v191\n"
      "v_add_f32 v188, v188, v204\n"
      "v_mfma_f32_16x16x32_bf16 v[180:183], a[172:175], v[128:131], v[180:183]\n"
      "buffer_load_dwordx4 v1, s[8:11], s46 offen offset:0 sc1 lds\n"
      "v_mfma_f32_16x16x32_bf16 v[180:183], a[176:179], v[132:135], v[180:183]\n"
      "ds_read_b32 v22, v24\n"
      "v_lshlrev_b32 v186, 16, v199\n"
      "v_mfma_f32_16x16x32_bf16 v[180:183], a[180:183], v[136:139], v[180:183]\n"
      "v_fmac_f32 v219, v175, v175\n"
      "v_add_f32 v218, v218, v176\n"
      "v_mfma_f32_16x16x32_bf16 v[180:183], a[184:187], v[140:143], v[180:183]\n"
      "v_fmac_f32 v219, v176, v176\n"
      "v_fmac_f32 v219, v177, v177\n"
      "v_mfma_f32_16x16x32_bf16 v[180:183], a[188:191], v[144:147], v[180:183]\n"
      "v_fmac_f32 v219, v178, v178\n"
      "v_fmac_f32 v219, v179, v179\n"
      "v_mfma_f32_16x16x32_bf16 v[180:183], a[192:195], v[148:151], v[180:183]\n"
      "v_fmac_f32 v219, v188, v188\n"
      "v_fmac_f32 v219, v189, v189\n"
      "v_mfma_f32_16x16x32_bf16 v[180:183], a[196:199], v[152:155], v[180:183]\n"
      "buffer_load_dwordx4 v[196:199], v13, s[8:11], s47 offen offset:1024 sc1\n"
      "v_mfma_f32_16x16x32_bf16 v[180:183], a[200:203], v[156:159], v[180:183]\n"
      "v_add_f32 v218, v218, v177\n"
      "v_mfma_f32_16x16x32_bf16 v[180:183], a[204:207], v[160:163], v[180:183]\n"
      "v_add_f32 v218, v218, v178\n"
      "v_add_f32 v218, v218, v179\n"
      "v_mfma_f32_16x16x32_bf16 v[180:183], a[208:211], v[164:167], v[180:183]\n"
      "s_waitcnt lgkmcnt(0)\n"
      "v_add_f32 v218, v218, v188\n"
      "v_cmp_gt_i32 vcc, v21, v22\n"
      "v_mfma_f32_16x16x32_bf16 v[180:183], a[212:215], v[168:171], v[180:183]\n"
      "v_add_f32 v218, v218, v189\n"
      "v_fmac_f32 v219, v190, v190\n"
      "v_mfma_f32_16x16x32_bf16 v[184:187], a[216:219], v[100:103], v[184:187]\n"
      "v_cvt_pk_bf16_f32 v216, v188, v189\n"
      "v_add_f32 v218, v218, v190\n"
      "v_mfma_f32_16x16x32_bf16 v[184:187], a[220:223], v[104:107], v[184:187]\n"
      "s_cbranch_vccnz .Lsp_wy1_12\n"
      ".Lbk_wy1_12:\n"
      "v_add_f32 v218, v218, v191\n"
      "v_mfma_f32_16x16x32_bf16 v[184:187], a[224:227], v[108:111], v[184:187]\n"
      ".Lw_p1:\n"
      "s_cbranch_execz .Lw_pr\n"
      "s_waitcnt vmcnt(11)\n"
      "ds_read_b128 v[108:111], v10 offset:22528\n"
      "v_lshlrev_b32 v190, 16, v201\n"
      "v_mfma_f32_16x16x32_bf16 v[184:187], a[228:231], v[112:115], v[184:187]\n"
      "s_add_u32 s38, s38, 24576\n"
      "v_lshlrev_b32 v188, 16, v200\n"
      "v_mfma_f32_16x16x32_bf16 v[184:187], a[232:235], v[116:119], v[184:187]\n"
      "ds_read_b128 v[100:103], v10 offset:20480\n"
      "v_mfma_f32_16x16x32_bf16 v[184:187], a[236:239], v[120:123], v[184:187]\n"
      "ds_read_b128 v[116:119], v10 offset:24576\n"
      "v_cvt_pk_bf16_f32 v210, v176, v177\n"
      "v_mfma_f32_16x16x32_bf16 v[184:187], a[240:243], v[124:127], v[184:187]\n"
      "ds_read_b128 v[104:107], v10 offset:21504\n"
      "v_fmac_f32 v219, v191, v191\n"
      "v_mfma_f32_16x16x32_bf16 v[184:187], a[244:247], v[128:131], v[184:187]\n"
      "v_and_b32 v189, 0xffff0000, v200\n"
      "s_sub_u32 s47, s46, s45\n"
      "v_mfma_f32_16x16x32_bf16 v[184:187], a[248:251], v[132:135], v[184:187]\n"
      "v_mfma_f32_16x16x32_bf16 v[184:187], a[252:255], v[136:139], v[184:187]\n"
      "s_waitcnt vmcnt(10) lgkmcnt(3)\n"
      "ds_read_b128 v[112:115], v10 offset:23552\n"
      "v_lshlrev_b32 v178, 16, v195\n"
      "v_mfma_f32_16x16x32_bf16 v[184:187], v[32:35], v[140:143], v[184:187]\n"
      "ds_read_b128 v[140:143], v10 offset:29696\n"
      "v_and_b32 v173, 0xffff0000, v192\n"
      "v_mfma_f32_16x16x32_bf16 v[184:187], v[36:39], v[144:147], v[184:187]\n"
      "buffer_store_dwordx4 v[208:211], v15, s[8:11], s38 offen sc1\n"
      "v_mfma_f32_16x16x32_bf16 v[184:187], v[40:43], v[148:151], v[184:187]\n"
      "ds_read_b128 v[120:123], v10 offset:25600\n"
      "v_fmac_f32 v219, v180, v180\n"
      "v_mfma_f32_16x16x32_bf16 v[184:187], v[44:47], v[152:155], v[184:187]\n"
      "ds_read2_b32 v[134:135], v8 offset0:64 offset1:96\n"
      "v_and_b32 v191, 0xffff0000, v201\n"
      "v_mfma_f32_16x16x32_bf16 v[184:187], v[48:51], v[156:159], v[184:187]\n"
      "ds_read2_b32 v[132:133], v8 offset0:0 offset1:32\n"
      "v_lshlrev_b32 v174, 16, v193\n"
      "v_mfma_f32_16x16x32_bf16 v[184:187], v[52:55], v[160:163], v[184:187]\n"
      "ds_read_b128 v[156:159], v10 offset:33792\n"
      "v_fmac_f32 v219, v181, v181\n"
      "v_mfma_f32_16x16x32_bf16 v[184:187], v[56:59], v[164:167], v[184:187]\n"
      "ds_read_b128 v[124:127], v10 offset:26624\n"
      "v_add_f32 v218, v218, v180\n"
      "v_mfma_f32_16x16x32_bf16 v[184:187], v[60:63], v[168:171], v[184:187]\n"
      "s_waitcnt lgkmcnt(5)\n"
      "ds_read_b128 v[128:131], v10 offset:27648\n"
      "v_lshlrev_b32 v172, 16, v192\n"
      "v_mfma_f32_16x16x32_bf16 v[188:191], v[64:67], v[100:103], v[188:191]\n"
      "v_lshlrev_b32 v176, 16, v194\n"
      "v_and_b32 v177, 0xffff0000, v194\n"
      "v_mfma_f32_16x16x32_bf16 v[188:191], v[68:71], v[104:107], v[188:191]\n"
      "buffer_load_dwordx2 v[200:201], v14, s[8:11], s47 offen sc1\n"
      "v_mfma_f32_16x16x32_bf16 v[188:191], v[72:75], v[108:111], v[188:191]\n"
      "v_and_b32 v175, 0xffff0000, v193\n"
      "v_and_b32 v179, 0xffff0000, v195\n"
      "v_mfma_f32_16x16x32_bf16 v[188:191], v[76:79], v[112:115], v[188:191]\n"
      "ds_read_b128 v[144:147], v10 offset:30720\n"
      "v_add_f32 v218, v218, v181\n"
      "v_mfma_f32_16x16x32_bf16 v[188:191], v[80:83], v[116:119], v[188:191]\n"
      "s_waitcnt lgkmcnt(3)\n"
      "ds_read_b128 v[136:139], v10 offset:28672\n"
      "v_fmac_f32 v219, v182, v182\n"
      "v_mfma_f32_16x16x32_bf16 v[188:191], v[84:87], v[120:123], v[188:191]\n"
      "s_waitcnt lgkmcnt(3)\n"
      "v_fmac_f32 v219, v183, v183\n"
      "v_fmac_f32 v219, v184, v184\n"
      "v_mfma_f32_16x16x32_bf16 v[188:191], v[88:91], v[124:127], v[188:191]\n"
      "s_waitcnt lgkmcnt(2)\n"
      "buffer_load_dwordx4 v[192:195], v13, s[8:11], s47 offen sc1\n"
      "v_mfma_f32_16x16x32_bf16 v[188:191], v[92:95], v[128:131], v[188:191]\n"
      "ds_read2_b32 v[170:171], v9 offset0:64 offset1:96\n"
      "v_add_f32 v218, v218, v182\n"
      "v_mfma_f32_16x16x32_bf16 v[188:191], v[96:99], v[132:135], v[188:191]\n"
      "v_add_f32 v218, v218, v183\n"
      "v_mfma_f32_16x16x32_bf16 v[172:175], a[0:3], v[100:103], v[172:175]\n"
      "ds_read_b128 v[160:163], v10 offset:34816\n"
      "v_add_u32 v21, 1, v21\n"
      "v_mfma_f32_16x16x32_bf16 v[172:175], a[4:7], v[104:107], v[172:175]\n"
      "ds_read_b128 v[148:151], v10 offset:31744\n"
      "v_add_f32 v218, v218, v184\n"
      "v_mfma_f32_16x16x32_bf16 v[172:175], a[8:11], v[108:111], v[172:175]\n"
      "ds_read2_b32 v[168:169], v9 offset0:0 offset1:32\n"
      "s_mov_b32 s46, s36\n"
      "v_mfma_f32_16x16x32_bf16 v[172:175], a[12:15], v[112:115], v[172:175]\n"
      "ds_read_b128 v[152:155], v10 offset:32768\n"
      "v_add_f32 v218, v218, v185\n"
      "v_mfma_f32_16x16x32_bf16 v[172:175], a[16:19], v[116:119], v[172:175]\n"
      "buffer_store_dwordx2 v[216:217], v16, s[8:11], s38 offen sc1\n"
      "v_mfma_f32_16x16x32_bf16 v[172:175], a[20:23], v[120:123], v[172:175]\n"
      "ds_read_b128 v[164:167], v10 offset:35840\n"
      "v_add_f32 v218, v218, v186\n"
      "v_mfma_f32_16x16x32_bf16 v[172:175], a[24:27], v[124:127], v[172:175]\n"
      "v_fmac_f32 v219, v185, v185\n"
      "v_fmac_f32 v219, v186, v186\n"
      "v_mfma_f32_16x16x32_bf16 v[172:175], a[28:31], v[128:131], v[172:175]\n"
      "v_add_f32 v218, v218, v187\n"
      "v_fmac_f32 v219, v187, v187\n"
      "v_mfma_f32_16x16x32_bf16 v[172:175], a[32:35], v[132:135], v[172:175]\n"
      "s_waitcnt lgkmcnt(3)\n"
      "ds_write_b64 v18, v[218:219]\n"
      "v_mfma_f32_16x16x32_bf16 v[172:175], a[36:39], v[136:139], v[172:175]\n"
      "s_waitcnt vmcnt(12) lgkmcnt(3)\n"
      "buffer_store_dword v25, v12, s[8:11], s50 offen sc1\n"
      "v_mfma_f32_16x16x32_bf16 v[172:175], a[40:43], v[140:143], v[172:175]\n"
      "v_cvt_pk_bf16_f32 v213, v182, v183\n"
      "v_cvt_pk_bf16_f32 v215, v186, v187\n"
      "v_mfma_f32_16x16x32_bf16 v[172:175], a[44:47], v[144:147], v[172:175]\n"
      "ds_write_b32 v23, v21\n"
      "v_mfma_f32_16x16x32_bf16 v[172:175], a[48:51], v[148:151], v[172:175]\n"
      "s_waitcnt lgkmcnt(2)\n"
      "v_cvt_pk_bf16_f32 v212, v180, v181\n"
      "v_cvt_pk_bf16_f32 v214, v184, v185\n"
      "v_mfma_f32_16x16x32_bf16 v[172:175], a[52:55], v[152:155], v[172:175]\n"
      "buffer_store_dwordx4 v[212:215], v15, s[8:11], s38 offen offset:1024 sc1\n"
      "v_mfma_f32_16x16x32_bf16 v[172:175], a[56:59], v[156:159], v[172:175]\n"
      "s_add_u32 m0, s39, 20480\n"
      "s_add_u32 s38, s38, 24576\n"
      "v_mfma_f32_16x16x32_bf16 v[172:175], a[60:63], v[160:163], v[172:175]\n"
      "s_waitcnt vmcnt(13) lgkmcnt(1)\n"
      "v_cmp_ne_u32 vcc, s21, v28\n"
      "v_mfma_f32_16x16x32_bf16 v[172:175], a[64:67], v[164:167], v[172:175]\n"
      "v_mfma_f32_16x16x32_bf16 v[172:175], a[68:71], v[168:171], v[172:175]\n"
      "v_mfma_f32_16x16x32_bf16 v[176:179], a[72:75], v[100:103], v[176:179]\n"
      "s_cmp_eq_u32 s36, s37\n"
      "s_cselect_b32 s48, 49152, s51\n"
      "v_mfma_f32_16x16x32_bf16 v[176:179], a[76:79], v[104:107], v[176:179]\n"
      "s_cbranch_vccnz .Lsp_w1_13\n"
      ".Lbk_w1_13:\n"
      "v_mfma_f32_16x16x32_bf16 v[176:179], a[80:83], v[108:111], v[176:179]\n"
      "buffer_load_dwordx4 v0, s[8:11], s46 offen offset:0 sc1 lds\n"
      "v_mfma_f32_16x16x32_bf16 v[176:179], a[84:87], v[112:115], v[176:179]\n"
      "ds_read_b32 v22, v24\n"
      "v_cvt_pk_bf16_f32 v208, v172, v173\n"
      "v_mfma_f32_16x16x32_bf16 v[176:179], a[88:91], v[116:119], v[176:179]\n"
      "v_add_f32 v218, v172, v173\n"
      "v_mul_f32 v219, v172, v172\n"
      "v_mfma_f32_16x16x32_bf16 v[176:179], a[92:95], v[120:123], v[176:179]\n"
      "s_add_u32 s36, s36, s48\n"
      "v_add_f32 v218, v218, v174\n"
      "v_mfma_f32_16x16x32_bf16 v[176:179], a[96:99], v[124:127], v[176:179]\n"
      "s_min_u32 s36, s36, s6\n"
      "v_fmac_f32 v219, v173, v173\n"
      "v_mfma_f32_16x16x32_bf16 v[176:179], a[100:103], v[128:131], v[176:179]\n"
      "buffer_load_dword v28, v11, s[8:11], s36 offen sc1\n"
      "v_mfma_f32_16x16x32_bf16 v[176:179], a[104:107], v[132:135], v[176:179]\n"
      "s_waitcnt lgkmcnt(0)\n"
      "v_cmp_gt_i32 vcc, v21, v22\n"
      "v_fmac_f32_e64 v219, v174, v174\n"
      "v_mfma_f32_16x16x32_bf16 v[176:179], a[108:111], v[136:139], v[176:179]\n"
      "buffer_load_dwordx4 v0, s[8:11], s46 offen offset:1024 sc1 lds\n"
      "v_mfma_f32_16x16x32_bf16 v[176:179], a[112:115], v[140:143], v[176:179]\n"
      "s_waitcnt vmcnt(9)\n"
      "v_lshlrev_b32_e64 v182, 16, v197\n"
      "v_and_b32 v185, 0xffff0000, v198\n"
      "v_mfma_f32_16x16x32_bf16 v[176:179], a[116:119], v[144:147], v[176:179]\n"
      "v_lshlrev_b32_e64 v180, 16, v196\n"
      "v_and_b32 v183, 0xffff0000, v197\n"
      "v_mfma_f32_16x16x32_bf16 v[176:179], a[120:123], v[148:151], v[176:179]\n"
      "s_cbranch_vccnz .Lsp_wz1_14\n"
      ".Lbk_wz1_14:\n"
      "ds_read_b128 v[204:207], v17\n"
      "v_mfma_f32_16x16x32_bf16 v[176:179], a[124:127], v[152:155], v[176:179]\n"
      "buffer_load_dwordx4 v0, s[8:11], s46 offen offset:3072 sc1 lds\n"
      "v_mfma_f32_16x16x32_bf16 v[176:179], a[128:131], v[156:159], v[176:179]\n"
      "v_add_f32 v218, v218, v175\n"
      "v_mfma_f32_16x16x32_bf16 v[176:179], a[132:135], v[160:163], v[176:179]\n"
      "v_add_u32 v21, 1, v21\n"
      "v_lshlrev_b32 v184, 16, v198\n"
      "v_mfma_f32_16x16x32_bf16 v[176:179], a[136:139], v[164:167], v[176:179]\n"
      "v_and_b32 v181, 0xffff0000, v196\n"
      "v_cvt_pk_bf16_f32 v209, v174, v175\n"
      "v_mfma_f32_16x16x32_bf16 v[176:179], a[140:143], v[168:171], v[176:179]\n"
      "ds_write_b32 v23, v21\n"
      "v_mfma_f32_16x16x32_bf16 v[180:183], a[144:147], v[100:103], v[180:183]\n"
      "buffer_load_dwordx4 v0, s[8:11], s46 offen offset:2048 sc1 lds\n"
      "v_mfma_f32_16x16x32_bf16 v[180:183], a[148:151], v[104:107], v[180:183]\n"
      "s_waitcnt lgkmcnt(1)\n"
      "v_add_f32 v188, v188, v204\n"
      "v_add_f32 v190, v190, v206\n"
      "v_mfma_f32_16x16x32_bf16 v[180:183], a[152:155], v[108:111], v[180:183]\n"
      "v_add_f32 v191, v191, v207\n"
      "v_lshlrev_b32 v186, 16, v199\n"
      "v_mfma_f32_16x16x32_bf16 v[180:183], a[156:159], v[112:115], v[180:183]\n"
      "v_add_f32 v218, v218, v176\n"
      "v_fmac_f32 v219, v175, v175\n"
      "v_mfma_f32_16x16x32_bf16 v[180:183], a[160:163], v[116:119], v[180:183]\n"
      "s_add_u32 m0, s40, 20480\n"
      "v_cvt_pk_bf16_f32 v217, v190, v191\n"
      "v_add_f32 v218, v218, v177\n"
      "v_mfma_f32_16x16x32_bf16 v[180:183], a[164:167], v[120:123], v[180:183]\n"
      "s_add_u32 s50, s50, 24576\n"
      "v_cvt_pk_bf16_f32 v211, v178, v179\n"
      "v_and_b32 v187, 0xffff0000, v199\n"
      "v_mfma_f32_16x16x32_bf16 v[180:183], a[168:171], v[124:127], v[180:183]\n"
      "buffer_load_dwordx4 v1, s[8:11], s46 offen offset:0 sc1 lds\n"
      "v_mfma_f32_16x16x32_bf16 v[180:183], a[172:175], v[128:131], v[180:183]\n"
      "v_add_f32 v218, v218, v178\n"
      "v_add_f32 v189, v189, v205\n"
      "v_mfma_f32_16x16x32_bf16 v[180:183], a[176:179], v[132:135], v[180:183]\n"
      "v_fmac_f32 v219, v176, v176\n"
      "v_mfma_f32_16x16x32_bf16 v[180:183], a[180:183], v[136:139], v[180:183]\n"
      "ds_read_b32 v22, v24\n"
      "v_mfma_f32_16x16x32_bf16 v[180:183], a[184:187], v[140:143], v[180:183]\n"
      "buffer_load_dwordx4 v[196:199], v13, s[8:11], s47 offen offset:1024 sc1\n"
      "v_mfma_f32_16x16x32_bf16 v[180:183], a[188:191], v[144:147], v[180:183]\n"
      "v_fmac_f32 v219, v177, v177\n"
      "v_add_f32 v218, v218, v179\n"
      "v_mfma_f32_16x16x32_bf16 v[180:183], a[192:195], v[148:151], v[180:183]\n"
      "v_fmac_f32 v219, v178, v178\n"
      "v_fmac_f32 v219, v179, v179\n"
      "v_mfma_f32_16x16x32_bf16 v[180:183], a[196:199], v[152:155], v[180:183]\n"
      "v_fmac_f32 v219, v188, v188\n"
      "v_fmac_f32 v219, v189, v189\n"
      "v_mfma_f32_16x16x32_bf16 v[180:183], a[200:203], v[156:159], v[180:183]\n"
      "s_sub_u32 s47, s46, s45\n"
      "v_fmac_f32 v219, v190, v190\n"
      "v_add_f32 v218, v218, v188\n"
      "v_mfma_f32_16x16x32_bf16 v[180:183], a[204:207], v[160:163], v[180:183]\n"
      "v_add_f32 v218, v218, v189\n"
      "v_add_f32 v218, v218, v190\n"
      "v_mfma_f32_16x16x32_bf16 v[180:183], a[208:211], v[164:167], v[180:183]\n"
      "s_waitcnt lgkmcnt(0)\n"
      "v_cvt_pk_bf16_f32 v210, v176, v177\n"
      "v_cmp_gt_i32 vcc, v21, v22\n"
      "v_mfma_f32_16x16x32_bf16 v[180:183], a[212:215], v[168:171], v[180:183]\n"
      "v_add_f32 v218, v218, v191\n"
      "v_fmac_f32 v219, v191, v191\n"
      "v_mfma_f32_16x16x32_bf16 v[184:187], a[216:219], v[100:103], v[184:187]\n"
      "buffer_store_dwordx4 v[208:211], v15, s[8:11], s38 offen sc1\n"
      "v_mfma_f32_16x16x32_bf16 v[184:187], a[220:223], v[104:107], v[184:187]\n"
      "s_cbranch_vccnz .Lsp_wy0_15\n"
      ".Lbk_wy0_15:\n"
      "v_mfma_f32_16x16x32_bf16 v[184:187], a[224:227], v[108:111], v[184:187]\n"
      "s_waitcnt vmcnt(12)\n"
      "ds_read_b128 v[104:107], v0 offset:1024\n"
      "v_lshlrev_b32 v190, 16, v201\n"
      "v_mfma_f32_16x16x32_bf16 v[184:187], a[228:231], v[112:115], v[184:187]\n"
      "s_waitcnt vmcnt(11)\n"
      "v_lshlrev_b32 v176, 16, v194\n"
      "v_mfma_f32_16x16x32_bf16 v[184:187], a[232:235], v[116:119], v[184:187]\n"
      "ds_read_b128 v[100:103], v0 offset:0\n"
      "v_and_b32 v191, 0xffff0000, v201\n"
      "v_mfma_f32_16x16x32_bf16 v[184:187], a[236:239], v[120:123], v[184:187]\n"
      "ds_read_b128 v[120:123], v0 offset:5120\n"
      "v_lshlrev_b32 v174, 16, v193\n"
      "v_mfma_f32_16x16x32_bf16 v[184:187], a[240:243], v[124:127], v[184:187]\n"
      "v_add_f32 v218, v218, v180\n"
      "v_lshlrev_b32 v178, 16, v195\n"
      "v_mfma_f32_16x16x32_bf16 v[184:187], a[244:247], v[128:131], v[184:187]\n"
      "ds_read_b128 v[112:115], v0 offset:3072\n"
      "v_mfma_f32_16x16x32_bf16 v[184:187], a[248:251], v[132:135], v[184:187]\n"
      "ds_read_b128 v[108:111], v0 offset:2048\n"
      "v_cvt_pk_bf16_f32 v216, v188, v189\n"
      "v_mfma_f32_16x16x32_bf16 v[184:187], a[252:255], v[136:139], v[184:187]\n"
      "ds_read_b128 v[124:127], v0 offset:6144\n"
      "v_add_f32 v218, v218, v181\n"
      "v_mfma_f32_16x16x32_bf16 v[184:187], v[32:35], v[140:143], v[184:187]\n"
      "ds_read_b128 v[116:119], v0 offset:4096\n"
      "v_and_b32 v189, 0xffff0000, v200\n"
      "v_mfma_f32_16x16x32_bf16 v[184:187], v[36:39], v[144:147], v[184:187]\n"
      "ds_read_b128 v[140:143], v0 offset:9216\n"
      "s_mov_b32 s46, s36\n"
      "v_mfma_f32_16x16x32_bf16 v[184:187], v[40:43], v[148:151], v[184:187]\n"
      "ds_read2_b32 v[132:133], v2 offset0:0 offset1:32\n"
      "v_and_b32 v179, 0xffff0000, v195\n"
      "v_mfma_f32_16x16x32_bf16 v[184:187], v[44:47], v[152:155], v[184:187]\n"
      "v_add_f32 v218, v218, v182\n"
      "v_mfma_f32_16x16x32_bf16 v[184:187], v[48:51], v[156:159], v[184:187]\n"
      "ds_read_b128 v[128:131], v0 offset:7168\n"
      "v_fmac_f32 v219, v180, v180\n"
      "v_mfma_f32_16x16x32_bf16 v[184:187], v[52:55], v[160:163], v[184:187]\n"
      "ds_read_b128 v[136:139], v0 offset:8192\n"
      "v_lshlrev_b32 v188, 16, v200\n"
      "v_mfma_f32_16x16x32_bf16 v[184:187], v[56:59], v[164:167], v[184:187]\n"
      "s_waitcnt lgkmcnt(5)\n"
      "v_fmac_f32 v219, v181, v181\n"
      "v_and_b32 v175, 0xffff0000, v193\n"
      "v_mfma_f32_16x16x32_bf16 v[184:187], v[60:63], v[168:171], v[184:187]\n"
      "s_sub_u32 s20, s20, 1\n"
      "s_cbranch_scc0 .Lw_loop\n"
      ".Lw_tail:\n"
      "v_add_f32 v218, v218, v183\n"
      "v_fmac_f32 v219, v182, v182\n"
      "v_fmac_f32 v219, v183, v183\n"
      "s_nop 5\n"
      "v_add_f32 v218, v218, v184\n"
      "v_fmac_f32 v219, v184, v184\n"
      "v_cvt_pk_bf16_f32 v215, v186, v187\n"
      "v_fmac_f32 v219, v185, v185\n"
      "v_fmac_f32 v219, v186, v186\n"
      "v_fmac_f32 v219, v187, v187\n"
      "v_add_f32 v218, v218, v185\n"
      "v_add_f32 v218, v218, v186\n"
      "v_cvt_pk_bf16_f32 v213, v182, v183\n"
      "v_add_f32 v218, v218, v187\n"
      "ds_write_b64 v18, v[218:219]\n"
      "s_waitcnt lgkmcnt(0)\n"
      "s_barrier\n"
      "buffer_store_dwordx2 v[216:217], v16, s[8:11], s38 offen sc1\n"
      "v_cvt_pk_bf16_f32 v214, v184, v185\n"
      "v_cvt_pk_bf16_f32 v212, v180, v181\n"
      "buffer_store_dwordx4 v[212:215], v15, s[8:11], s38 offen offset:1024 sc1\n"
      "s_waitcnt vmcnt(10)\n"
      "buffer_store_dword v25, v12, s[8:11], s50 offen sc1\n"
      "s_add_u32 s50, s50, 24576\n"
      "s_add_u32 s38, s38, 24576\n"
      "s_waitcnt vmcnt(1)\n"
      "buffer_store_dword v25, v12, s[8:11], s50 offen sc1\n"
      "s_add_u32 s50, s50, 24576\n"
      "s_endpgm\n"
      ".Lv_start:\n"
      "s_mov_b64 exec, 0\n"
      "s_lshr_b32 s22, s39, 13\n"
      ".Lv_pl:\n"
      "s_cmp_eq_u32 s22, 0\n"
      "s_cbranch_scc1 .Lv_p0\n"
      "s_cmp_eq_u32 s22, 1\n"
      "s_cbranch_scc1 .Lv_p1\n"
      "s_branch .Lv_pe\n"
      ".Lv_pr:\n"
      "s_add_u32 s22, s22, 2\n"
      "s_branch .Lv_pl\n"
      ".Lv_pe:\n"
      "s_mov_b64 exec, -1\n"
      "s_add_u32 s50, s38, 4294893568\n"
      "s_add_u32 s49, s38, 4294918144\n"
      "s_add_u32 s38, s38, 4294942720\n"
      "s_cmp_eq_u32 s36, s37\n"
      "s_cselect_b32 s48, 49152, s51\n"
      "s_add_u32 s36, s36, s48\n"
      "s_min_u32 s36, s36, s6\n"
      "buffer_load_dword v28, v11, s[8:11], s36 offen sc1\n"
      "s_mov_b32 s46, s36\n"
      "s_add_u32 m0, s39, 0\n"
      "s_add_u32 s36, s36, 24576\n"
      "s_waitcnt vmcnt(0)\n"
      "v_cmp_ne_u32 vcc, s21, v28\n"
      "s_cbranch_vccnz .Lsp_v0_16\n"
      ".Lbk_v0_16:\n"
      "buffer_load_dwordx4 v0, s[8:11], s46 offen offset:0 sc1 lds\n"
      "s_min_u32 s36, s36, s6\n"
      "buffer_load_dword v28, v11, s[8:11], s36 offen sc1\n"
      "buffer_load_dwordx4 v0, s[8:11], s46 offen offset:2048 sc1 lds\n"
      "buffer_load_dwordx4 v0, s[8:11], s46 offen offset:1024 sc1 lds\n"
      "buffer_load_dwordx4 v0, s[8:11], s46 offen offset:3072 sc1 lds\n"
      "s_add_u32 m0, s40, 0\n"
      "s_nop 0\n"
      "buffer_load_dwordx4 v1, s[8:11], s46 offen offset:0 sc1 lds\n"
      "s_sub_u32 s47, s46, s45\n"
      "s_cmp_eq_u32 s36, s37\n"
      "s_cselect_b32 s48, 49152, s51\n"
      "buffer_load_dwordx4 v[192:195], v13, s[8:11], s47 offen sc1\n"
      "s_add_u32 m0, s39, 20480\n"
      "buffer_load_dword v26, v23, s[8:11], s31 offen\n"
      "s_mov_b32 s46, s36\n"
      "s_waitcnt vmcnt(6)\n"
      "v_cmp_ne_u32 vcc, s21, v28\n"
      "s_add_u32 s36, s36, s48\n"
      "s_cbranch_vccnz .Lsp_v1_17\n"
      ".Lbk_v1_17:\n"
      "s_min_u32 s36, s36, s6\n"
      "buffer_load_dword v28, v11, s[8:11], s36 offen sc1\n"
      "buffer_load_dwordx4 v0, s[8:11], s46 offen offset:1024 sc1 lds\n"
      "buffer_load_dwordx4 v0, s[8:11], s46 offen offset:0 sc1 lds\n"
      "buffer_load_dwordx4 v0, s[8:11], s46 offen offset:3072 sc1 lds\n"
      "v_add_u32 v21, 1, v21\n"
      "buffer_load_dwordx4 v0, s[8:11], s46 offen offset:2048 sc1 lds\n"
      "s_waitcnt vmcnt(7)\n"
      "ds_write_b32 v23, v21\n"
      "s_add_u32 m0, s40, 20480\n"
      "buffer_load_dwordx4 v[196:199], v13, s[8:11], s47 offen offset:1024 sc1\n"
      "buffer_load_dwordx4 v1, s[8:11], s46 offen offset:0 sc1 lds\n"
      "ds_read_b32 v22, v24\n"
      "buffer_load_dword v26, v23, s[8:11], s31 offen\n"
      "buffer_load_dword v26, v23, s[8:11], s31 offen\n"
      "s_waitcnt lgkmcnt(0)\n"
      "v_cmp_gt_i32 vcc, v21, v22\n"
      "s_sub_u32 s47, s46, s45\n"
      "s_cbranch_vccnz .Lsp_vy0_18\n"
      ".Lbk_vy0_18:\n"
      "ds_read_b128 v[100:103], v0 offset:8192\n"
      "ds_read_b128 v[112:115], v0 offset:11264\n"
      "ds_read_b128 v[104:107], v0 offset:9216\n"
      "ds_read_b128 v[108:111], v0 offset:10240\n"
      "ds_read_b128 v[124:127], v0 offset:14336\n"
      "s_waitcnt vmcnt(10)\n"
      "v_lshlrev_b32 v172, 16, v192\n"
      "v_lshlrev_b32 v176, 16, v194\n"
      "ds_read_b128 v[116:119], v0 offset:12288\n"
      "v_and_b32 v173, 0xffff0000, v192\n"
      "ds_read_b128 v[120:123], v0 offset:13312\n"
      "ds_read_b128 v[128:131], v0 offset:15360\n"
      "v_lshlrev_b32 v178, 16, v195\n"
      "ds_read2_b32 v[132:133], v2 offset0:0 offset1:32\n"
      "v_and_b32 v175, 0xffff0000, v193\n"
      "buffer_load_dword v26, v23, s[8:11], s31 offen\n"
      "ds_read_b128 v[144:147], v0 offset:2048\n"
      "v_and_b32 v179, 0xffff0000, v195\n"
      "v_lshlrev_b32 v174, 16, v193\n"
      "s_mov_b32 s20, 31\n"
      "s_waitcnt lgkmcnt(0)\n"
      "s_nop 4\n"
      "s_branch .Lv_loop\n"
      ".p2align 12\n"
      ".Lv_p0:\n"
      "s_branch .Lv_pr\n"
      ".p2align 5\n"
      ".Lv_loop:\n"
      "v_mfma_f32_16x16x32_bf16 v[188:191], v[64:67], v[100:103], 0\n"
      "ds_read2_b32 v[134:135], v2 offset0:64 offset1:96\n"
      "v_and_b32 v177, 0xffff0000, v194\n"
      "v_mfma_f32_16x16x32_bf16 v[188:191], v[68:71], v[104:107], v[188:191]\n"
      "buffer_load_dwordx4 v[192:195], v13, s[8:11], s47 offen sc1\n"
      "v_mfma_f32_16x16x32_bf16 v[188:191], v[72:75], v[108:111], v[188:191]\n"
      "ds_read2_b32 v[168:169], v3 offset0:0 offset1:32\n"
      "v_mfma_f32_16x16x32_bf16 v[188:191], v[76:79], v[112:115], v[188:191]\n"
      "ds_read_b128 v[152:155], v0 offset:4096\n"
      "v_mfma_f32_16x16x32_bf16 v[188:191], v[80:83], v[116:119], v[188:191]\n"
      "s_waitcnt lgkmcnt(3)\n"
      "v_cvt_pk_bf16_f32 v215, v186, v187\n"
      "v_mfma_f32_16x16x32_bf16 v[188:191], v[84:87], v[120:123], v[188:191]\n"
      "ds_read_b128 v[148:151], v0 offset:3072\n"
      "v_cvt_pk_bf16_f32 v212, v180, v181\n"
      "v_mfma_f32_16x16x32_bf16 v[188:191], v[88:91], v[124:127], v[188:191]\n"
      "ds_read_b128 v[136:139], v0 offset:0\n"
      "s_add_u32 s49, s49, 24576\n"
      "v_mfma_f32_16x16x32_bf16 v[188:191], v[92:95], v[128:131], v[188:191]\n"
      "s_waitcnt lgkmcnt(4)\n"
      "ds_read_b128 v[160:163], v0 offset:6144\n"
      "v_fmac_f32 v219, v183, v183\n"
      "v_mfma_f32_16x16x32_bf16 v[188:191], v[96:99], v[132:135], v[188:191]\n"
      "ds_read_b128 v[156:159], v0 offset:5120\n"
      "v_add_f32 v218, v218, v184\n"
      "v_mfma_f32_16x16x32_bf16 v[172:175], a[0:3], v[100:103], v[172:175]\n"
      "ds_read2_b32 v[170:171], v3 offset0:64 offset1:96\n"
      "v_add_f32 v218, v218, v185\n"
      "v_mfma_f32_16x16x32_bf16 v[172:175], a[4:7], v[104:107], v[172:175]\n"
      "ds_read_b128 v[140:143], v0 offset:1024\n"
      "v_cvt_pk_bf16_f32 v214, v184, v185\n"
      "v_mfma_f32_16x16x32_bf16 v[172:175], a[8:11], v[108:111], v[172:175]\n"
      "ds_read_b128 v[164:167], v0 offset:7168\n"
      "v_fmac_f32 v219, v184, v184\n"
      "v_mfma_f32_16x16x32_bf16 v[172:175], a[12:15], v[112:115], v[172:175]\n"
      "s_mov_b32 s46, s36\n"
      "v_fmac_f32 v219, v185, v185\n"
      "v_add_f32 v218, v218, v186\n"
      "v_mfma_f32_16x16x32_bf16 v[172:175], a[16:19], v[116:119], v[172:175]\n"
      "ds_write_b128 v17, v[188:191]\n"
      "v_mfma_f32_16x16x32_bf16 v[172:175], a[20:23], v[120:123], v[172:175]\n"
      "s_add_u32 s50, s50, 24576\n"
      "v_add_u32 v21, 1, v21\n"
      "v_mfma_f32_16x16x32_bf16 v[172:175], a[24:27], v[124:127], v[172:175]\n"
      "s_add_u32 m0, s39, 40960\n"
      "v_add_f32 v218, v218, v187\n"
      "v_mfma_f32_16x16x32_bf16 v[172:175], a[28:31], v[128:131], v[172:175]\n"
      "v_fmac_f32 v219, v186, v186\n"
      "v_fmac_f32 v219, v187, v187\n"
      "v_mfma_f32_16x16x32_bf16 v[172:175], a[32:35], v[132:135], v[172:175]\n"
      "s_waitcnt lgkmcnt(2)\n"
      "ds_write_b64 v18, v[218:219]\n"
      "v_mfma_f32_16x16x32_bf16 v[172:175], a[36:39], v[136:139], v[172:175]\n"
      "s_add_u32 s36, s36, 24576\n"
      "v_mfma_f32_16x16x32_bf16 v[172:175], a[40:43], v[140:143], v[172:175]\n"
      "buffer_store_dwordx4 v[212:215], v15, s[8:11], s38 offen offset:1024 sc1\n"
      "v_mfma_f32_16x16x32_bf16 v[172:175], a[44:47], v[144:147], v[172:175]\n"
      "ds_write_b32 v23, v21\n"
      "v_mfma_f32_16x16x32_bf16 v[172:175], a[48:51], v[148:151], v[172:175]\n"
      "s_waitcnt vmcnt(11) lgkmcnt(2)\n"
      "v_cmp_ne_u32 vcc, s21, v28\n"
      "v_mfma_f32_16x16x32_bf16 v[172:175], a[52:55], v[152:155], v[172:175]\n"
      "v_mfma_f32_16x16x32_bf16 v[172:175], a[56:59], v[156:159], v[172:175]\n"
      "v_mfma_f32_16x16x32_bf16 v[172:175], a[60:63], v[160:163], v[172:175]\n"
      "v_mfma_f32_16x16x32_bf16 v[172:175], a[64:67], v[164:167], v[172:175]\n"
      "s_cbranch_vccnz .Lsp_v0_19\n"
      ".Lbk_v0_19:\n"
      "v_mfma_f32_16x16x32_bf16 v[172:175], a[68:71], v[168:171], v[172:175]\n"
      "buffer_load_dwordx4 v0, s[8:11], s46 offen offset:0 sc1 lds\n"
      "v_mfma_f32_16x16x32_bf16 v[176:179], a[72:75], v[100:103], v[176:179]\n"
      "s_waitcnt vmcnt(7) lgkmcnt(0)\n"
      "s_min_u32 s36, s36, s6\n"
      "v_lshlrev_b32 v186, 16, v199\n"
      "v_mfma_f32_16x16x32_bf16 v[176:179], a[76:79], v[104:107], v[176:179]\n"
      "v_and_b32 v181, 0xffff0000, v196\n"
      "v_lshlrev_b32 v184, 16, v198\n"
      "v_mfma_f32_16x16x32_bf16 v[176:179], a[80:83], v[108:111], v[176:179]\n"
      "s_add_u32 s38, s38, 24576\n"
      "ds_read_b32 v22, v24\n"
      "v_lshlrev_b32 v180, 16, v196\n"
      "v_mfma_f32_16x16x32_bf16 v[176:179], a[84:87], v[112:115], v[176:179]\n"
      "buffer_load_dword v28, v11, s[8:11], s36 offen sc1\n"
      "v_mfma_f32_16x16x32_bf16 v[176:179], a[88:91], v[116:119], v[176:179]\n"
      "v_cvt_pk_bf16_f32 v209, v174, v175\n"
      "v_mfma_f32_16x16x32_bf16 v[176:179], a[92:95], v[120:123], v[176:179]\n"
      "buffer_load_dwordx4 v0, s[8:11], s46 offen offset:2048 sc1 lds\n"
      "v_mfma_f32_16x16x32_bf16 v[176:179], a[96:99], v[124:127], v[176:179]\n"
      "v_mul_f32 v219, v172, v172\n"
      "v_and_b32 v185, 0xffff0000, v198\n"
      "v_mfma_f32_16x16x32_bf16 v[176:179], a[100:103], v[128:131], v[176:179]\n"
      "s_waitcnt lgkmcnt(0)\n"
      "v_cmp_gt_i32 vcc, v21, v22\n"
      "v_and_b32 v183, 0xffff0000, v197\n"
      "v_mfma_f32_16x16x32_bf16 v[176:179], a[104:107], v[132:135], v[176:179]\n"
      "v_lshlrev_b32 v182, 16, v197\n"
      "v_and_b32 v187, 0xffff0000, v199\n"
      "v_mfma_f32_16x16x32_bf16 v[176:179], a[108:111], v[136:139], v[176:179]\n"
      "buffer_load_dwordx4 v[196:199], v13, s[8:11], s47 offen offset:1024 sc1\n"
      "v_mfma_f32_16x16x32_bf16 v[176:179], a[112:115], v[140:143], v[176:179]\n"
      "v_fmac_f32_e64 v219, v173, v173\n"
      "v_fmac_f32_e64 v219, v174, v174\n"
      "v_mfma_f32_16x16x32_bf16 v[176:179], a[116:119], v[144:147], v[176:179]\n"
      "s_cbranch_vccnz .Lsp_vz0_20\n"
      ".Lbk_vz0_20:\n"
      "ds_read_b128 v[204:207], v19\n"
      "v_mfma_f32_16x16x32_bf16 v[176:179], a[120:123], v[148:151], v[176:179]\n"
      "v_mfma_f32_16x16x32_bf16 v[176:179], a[124:127], v[152:155], v[176:179]\n"
      "v_add_u32 v21, 1, v21\n"
      "v_mfma_f32_16x16x32_bf16 v[176:179], a[128:131], v[156:159], v[176:179]\n"
      "buffer_load_dwordx4 v0, s[8:11], s46 offen offset:1024 sc1 lds\n"
      "v_mfma_f32_16x16x32_bf16 v[176:179], a[132:135], v[160:163], v[176:179]\n"
      "v_add_f32 v218, v172, v173\n"
      "v_mfma_f32_16x16x32_bf16 v[176:179], a[136:139], v[164:167], v[176:179]\n"
      "v_fmac_f32 v219, v175, v175\n"
      "v_mfma_f32_16x16x32_bf16 v[176:179], a[140:143], v[168:171], v[176:179]\n"
      "s_waitcnt vmcnt(10)\n"
      "ds_write_b32 v23, v21\n"
      "v_mfma_f32_16x16x32_bf16 v[180:183], a[144:147], v[100:103], v[180:183]\n"
      "s_waitcnt lgkmcnt(1)\n"
      "v_add_f32 v218, v218, v174\n"
      "v_add_f32 v204, v204, v206\n"
      "v_mfma_f32_16x16x32_bf16 v[180:183], a[148:151], v[104:107], v[180:183]\n"
      "buffer_load_dwordx4 v0, s[8:11], s46 offen offset:3072 sc1 lds\n"
      "v_mfma_f32_16x16x32_bf16 v[180:183], a[152:155], v[108:111], v[180:183]\n"
      "v_add_f32 v205, v205, v207\n"
      "v_mfma_f32_16x16x32_bf16 v[180:183], a[156:159], v[112:115], v[180:183]\n"
      "v_cvt_pk_bf16_f32 v210, v176, v177\n"
      "v_mfma_f32_16x16x32_bf16 v[180:183], a[160:163], v[116:119], v[180:183]\n"
      "v_add_f32_dpp v205, v205, v205 quad_perm:[1,0,3,2] row_mask:0xf bank_mask:0xf\n"
      "v_mfma_f32_16x16x32_bf16 v[180:183], a[164:167], v[120:123], v[180:183]\n"
      "v_fmac_f32 v219, v176, v176\n"
      "s_add_u32 m0, s40, 40960\n"
      "v_mfma_f32_16x16x32_bf16 v[180:183], a[168:171], v[124:127], v[180:183]\n"
      "buffer_load_dwordx4 v1, s[8:11], s46 offen offset:0 sc1 lds\n"
      "v_mfma_f32_16x16x32_bf16 v[180:183], a[172:175], v[128:131], v[180:183]\n"
      "v_add_f32_dpp v205, v205, v205 quad_perm:[2,3,0,1] row_mask:0xf bank_mask:0xf\n"
      "v_add_f32_dpp v204, v204, v204 quad_perm:[1,0,3,2] row_mask:0xf bank_mask:0xf\n"
      "v_mfma_f32_16x16x32_bf16 v[180:183], a[176:179], v[132:135], v[180:183]\n"
      "v_add_f32_dpp v204, v204, v204 quad_perm:[2,3,0,1] row_mask:0xf bank_mask:0xf\n"
      "v_add_f32_dpp v205, v205, v205 row_half_mirror row_mask:0xf bank_mask:0xf\n"
      "v_mfma_f32_16x16x32_bf16 v[180:183], a[180:183], v[136:139], v[180:183]\n"
      "ds_read_b32 v22, v24\n"
      "v_add_f32_dpp v204, v204, v204 row_half_mirror row_mask:0xf bank_mask:0xf\n"
      "v_mfma_f32_16x16x32_bf16 v[180:183], a[184:187], v[140:143], v[180:183]\n"
      "s_waitcnt vmcnt(10) lgkmcnt(1)\n"
      "buffer_store_dword v25, v12, s[8:11], s50 offen sc1\n"
      "v_mfma_f32_16x16x32_bf16 v[180:183], a[188:191], v[144:147], v[180:183]\n"
      "v_add_f32 v218, v218, v175\n"
      "v_add_f32 v218, v218, v176\n"
      "v_mfma_f32_16x16x32_bf16 v[180:183], a[192:195], v[148:151], v[180:183]\n"
      "v_mul_f32 v204, v27, v204\n"
      "v_mfma_f32_16x16x32_bf16 v[180:183], a[196:199], v[152:155], v[180:183]\n"
      "v_fmaak_f32 v205, v27, v205, 0x36a7c5ac\n"
      "v_add_f32 v218, v218, v177\n"
      "v_mfma_f32_16x16x32_bf16 v[180:183], a[200:203], v[156:159], v[180:183]\n"
      "buffer_store_dwordx2 v[204:205], v20, s[8:11], s49 offen sc1\n"
      "v_mfma_f32_16x16x32_bf16 v[180:183], a[204:207], v[160:163], v[180:183]\n"
      "s_waitcnt lgkmcnt(0)\n"
      "v_fmac_f32 v219, v177, v177\n"
      "v_cmp_gt_i32 vcc, v21, v22\n"
      "v_mfma_f32_16x16x32_bf16 v[180:183], a[208:211], v[164:167], v[180:183]\n"
      "s_waitcnt vmcnt(10)\n"
      "v_fmac_f32 v219, v178, v178\n"
      "v_and_b32 v175, 0xffff0000, v193\n"
      "v_mfma_f32_16x16x32_bf16 v[180:183], a[212:215], v[168:171], v[180:183]\n"
      "v_lshlrev_b32 v176, 16, v194\n"
      "v_mfma_f32_16x16x32_bf16 v[184:187], a[216:219], v[100:103], v[184:187]\n"
      "v_add_f32 v218, v218, v178\n"
      "v_cvt_pk_bf16_f32 v211, v178, v179\n"
      "v_mfma_f32_16x16x32_bf16 v[184:187], a[220:223], v[104:107], v[184:187]\n"
      "s_cbranch_vccnz .Lsp_vy1_21\n"
      ".Lbk_vy1_21:\n"
      "ds_read_b128 v[100:103], v0 offset:28672\n"
      "v_mfma_f32_16x16x32_bf16 v[184:187], a[224:227], v[108:111], v[184:187]\n"
      "v_cvt_pk_bf16_f32 v208, v172, v173\n"
      "v_mfma_f32_16x16x32_bf16 v[184:187], a[228:231], v[112:115], v[184:187]\n"
      "ds_read_b128 v[112:115], v0 offset:31744\n"
      "v_fmac_f32 v219, v179, v179\n"
      "v_mfma_f32_16x16x32_bf16 v[184:187], a[232:235], v[116:119], v[184:187]\n"
      "ds_read_b128 v[104:107], v0 offset:29696\n"
      "v_fmac_f32 v219, v180, v180\n"
      "v_mfma_f32_16x16x32_bf16 v[184:187], a[236:239], v[120:123], v[184:187]\n"
      "v_and_b32 v177, 0xffff0000, v194\n"
      "v_mfma_f32_16x16x32_bf16 v[184:187], a[240:243], v[124:127], v[184:187]\n"
      "ds_read_b128 v[108:111], v0 offset:30720\n"
      "v_fmac_f32 v219, v181, v181\n"
      "v_mfma_f32_16x16x32_bf16 v[184:187], a[244:247], v[128:131], v[184:187]\n"
      "ds_read_b128 v[116:119], v0 offset:32768\n"
      "v_add_f32 v218, v218, v179\n"
      "v_mfma_f32_16x16x32_bf16 v[184:187], a[248:251], v[132:135], v[184:187]\n"
      "v_cvt_pk_bf16_f32 v213, v182, v183\n"
      "v_mfma_f32_16x16x32_bf16 v[184:187], a[252:255], v[136:139], v[184:187]\n"
      "ds_read_b128 v[124:127], v0 offset:34816\n"
      "v_add_f32 v218, v218, v180\n"
      "v_mfma_f32_16x16x32_bf16 v[184:187], v[32:35], v[140:143], v[184:187]\n"
      "ds_read_b128 v[128:131], v0 offset:35840\n"
      "v_add_f32 v218, v218, v181\n"
      "v_mfma_f32_16x16x32_bf16 v[184:187], v[36:39], v[144:147], v[184:187]\n"
      "buffer_store_dwordx4 v[208:211], v15, s[8:11], s38 offen sc1\n"
      "v_mfma_f32_16x16x32_bf16 v[184:187], v[40:43], v[148:151], v[184:187]\n"
      "ds_read2_b32 v[132:133], v4 offset0:0 offset1:32\n"
      "v_fmac_f32 v219, v182, v182\n"
      "v_mfma_f32_16x16x32_bf16 v[184:187], v[44:47], v[152:155], v[184:187]\n"
      "v_and_b32 v173, 0xffff0000, v192\n"
      "v_mfma_f32_16x16x32_bf16 v[184:187], v[48:51], v[156:159], v[184:187]\n"
      "ds_read_b128 v[120:123], v0 offset:33792\n"
      "v_and_b32 v179, 0xffff0000, v195\n"
      "v_mfma_f32_16x16x32_bf16 v[184:187], v[52:55], v[160:163], v[184:187]\n"
      "s_sub_u32 s47, s46, s45\n"
      "ds_read2_b32 v[134:135], v4 offset0:64 offset1:96\n"
      "v_lshlrev_b32 v172, 16, v192\n"
      "v_mfma_f32_16x16x32_bf16 v[184:187], v[56:59], v[164:167], v[184:187]\n"
      "ds_read_b128 v[160:163], v0 offset:26624\n"
      "v_lshlrev_b32 v174, 16, v193\n"
      "v_mfma_f32_16x16x32_bf16 v[184:187], v[60:63], v[168:171], v[184:187]\n"
      "s_waitcnt lgkmcnt(4)\n"
      "ds_read_b128 v[144:147], v0 offset:22528\n"
      "v_lshlrev_b32 v178, 16, v195\n"
      "v_mfma_f32_16x16x32_bf16 v[188:191], v[64:67], v[100:103], 0\n"
      "v_add_f32 v218, v218, v182\n"
      "v_mfma_f32_16x16x32_bf16 v[188:191], v[68:71], v[104:107], v[188:191]\n"
      "ds_read_b128 v[148:151], v0 offset:23552\n"
      "v_cvt_pk_bf16_f32 v212, v180, v181\n"
      "v_mfma_f32_16x16x32_bf16 v[188:191], v[72:75], v[108:111], v[188:191]\n"
      "ds_read_b128 v[156:159], v0 offset:25600\n"
      "v_fmac_f32 v219, v183, v183\n"
      "v_mfma_f32_16x16x32_bf16 v[188:191], v[76:79], v[112:115], v[188:191]\n"
      "ds_read_b128 v[152:155], v0 offset:24576\n"
      "v_add_f32 v218, v218, v183\n"
      "v_mfma_f32_16x16x32_bf16 v[188:191], v[80:83], v[116:119], v[188:191]\n"
      "s_waitcnt lgkmcnt(5)\n"
      "ds_read2_b32 v[168:169], v5 offset0:0 offset1:32\n"
      "v_mfma_f32_16x16x32_bf16 v[188:191], v[84:87], v[120:123], v[188:191]\n"
      "s_cmp_eq_u32 s36, s37\n"
      "s_cselect_b32 s48, 49152, s51\n"
      "ds_read2_b32 v[170:171], v5 offset0:64 offset1:96\n"
      "v_mfma_f32_16x16x32_bf16 v[188:191], v[88:91], v[124:127], v[188:191]\n"
      "buffer_load_dwordx4 v[192:195], v13, s[8:11], s47 offen sc1\n"
      "v_mfma_f32_16x16x32_bf16 v[188:191], v[92:95], v[128:131], v[188:191]\n"
      "v_add_f32 v218, v218, v184\n"
      "v_fmac_f32 v219, v184, v184\n"
      "v_mfma_f32_16x16x32_bf16 v[188:191], v[96:99], v[132:135], v[188:191]\n"
      "ds_read_b128 v[136:139], v0 offset:20480\n"
      "v_mfma_f32_16x16x32_bf16 v[172:175], a[0:3], v[100:103], v[172:175]\n"
      "v_add_f32 v218, v218, v185\n"
      "s_add_u32 m0, s39, 61440\n"
      "v_mfma_f32_16x16x32_bf16 v[172:175], a[4:7], v[104:107], v[172:175]\n"
      "ds_read_b128 v[164:167], v0 offset:27648\n"
      "v_cvt_pk_bf16_f32 v214, v184, v185\n"
      "v_mfma_f32_16x16x32_bf16 v[172:175], a[8:11], v[108:111], v[172:175]\n"
      "ds_read_b128 v[140:143], v0 offset:21504\n"
      "v_fmac_f32 v219, v185, v185\n"
      "v_mfma_f32_16x16x32_bf16 v[172:175], a[12:15], v[112:115], v[172:175]\n"
      "v_fmac_f32 v219, v186, v186\n"
      "v_fmac_f32 v219, v187, v187\n"
      "v_mfma_f32_16x16x32_bf16 v[172:175], a[16:19], v[116:119], v[172:175]\n"
      "ds_write_b128 v17, v[188:191]\n"
      "v_mfma_f32_16x16x32_bf16 v[172:175], a[20:23], v[120:123], v[172:175]\n"
      "v_cvt_pk_bf16_f32 v215, v186, v187\n"
      "v_add_f32 v218, v218, v186\n"
      "v_mfma_f32_16x16x32_bf16 v[172:175], a[24:27], v[124:127], v[172:175]\n"
      "buffer_store_dwordx4 v[212:215], v15, s[8:11], s38 offen offset:1024 sc1\n"
      "v_mfma_f32_16x16x32_bf16 v[172:175], a[28:31], v[128:131], v[172:175]\n"
      "v_add_f32 v218, v218, v187\n"
      "v_mfma_f32_16x16x32_bf16 v[172:175], a[32:35], v[132:135], v[172:175]\n"
      "s_waitcnt lgkmcnt(2)\n"
      "ds_write_b64 v18, v[218:219]\n"
      "v_mfma_f32_16x16x32_bf16 v[172:175], a[36:39], v[136:139], v[172:175]\n"
      "s_waitcnt lgkmcnt(2)\n"
      "v_add_u32 v21, 1, v21\n"
      "s_mov_b32 s46, s36\n"
      "v_mfma_f32_16x16x32_bf16 v[172:175], a[40:43], v[140:143], v[172:175]\n"
      "s_waitcnt vmcnt(8)\n"
      "v_and_b32 v181, 0xffff0000, v196\n"
      "v_mfma_f32_16x16x32_bf16 v[172:175], a[44:47], v[144:147], v[172:175]\n"
      "ds_write_b32 v23, v21\n"
      "v_mfma_f32_16x16x32_bf16 v[172:175], a[48:51], v[148:151], v[172:175]\n"
      "v_cmp_ne_u32 vcc, s21, v28\n"
      "v_mfma_f32_16x16x32_bf16 v[172:175], a[52:55], v[152:155], v[172:175]\n"
      "v_lshlrev_b32 v186, 16, v199\n"
      "s_add_u32 s36, s36, s48\n"
      "v_mfma_f32_16x16x32_bf16 v[172:175], a[56:59], v[156:159], v[172:175]\n"
      "v_lshlrev_b32_e64 v180, 16, v196\n"
      "v_lshlrev_b32_e64 v184, 16, v198\n"
      "v_mfma_f32_16x16x32_bf16 v[172:175], a[60:63], v[160:163], v[172:175]\n"
      "s_add_u32 s49, s49, 24576\n"
      "v_and_b32 v187, 0xffff0000, v199\n"
      "v_mfma_f32_16x16x32_bf16 v[172:175], a[64:67], v[164:167], v[172:175]\n"
      "s_cbranch_vccnz .Lsp_v1_22\n"
      ".Lbk_v1_22:\n"
      "s_min_u32 s36, s36, s6\n"
      "v_mfma_f32_16x16x32_bf16 v[172:175], a[68:71], v[168:171], v[172:175]\n"
      "buffer_load_dword v28, v11, s[8:11], s36 offen sc1\n"
      "v_mfma_f32_16x16x32_bf16 v[176:179], a[72:75], v[100:103], v[176:179]\n"
      "v_mfma_f32_16x16x32_bf16 v[176:179], a[76:79], v[104:107], v[176:179]\n"
      "buffer_load_dwordx4 v0, s[8:11], s46 offen offset:1024 sc1 lds\n"
      "v_mfma_f32_16x16x32_bf16 v[176:179], a[80:83], v[108:111], v[176:179]\n"
      "ds_read_b32 v22, v24\n"
      "v_mfma_f32_16x16x32_bf16 v[176:179], a[84:87], v[112:115], v[176:179]\n"
      "v_and_b32 v183, 0xffff0000, v197\n"
      "v_mfma_f32_16x16x32_bf16 v[176:179], a[88:91], v[116:119], v[176:179]\n"
      "v_mfma_f32_16x16x32_bf16 v[176:179], a[92:95], v[120:123], v[176:179]\n"
      "buffer_load_dwordx4 v0, s[8:11], s46 offen offset:0 sc1 lds\n"
      "v_mfma_f32_16x16x32_bf16 v[176:179], a[96:99], v[124:127], v[176:179]\n"
      "v_cvt_pk_bf16_f32 v208, v172, v173\n"
      "v_mfma_f32_16x16x32_bf16 v[176:179], a[100:103], v[128:131], v[176:179]\n"
      "v_mfma_f32_16x16x32_bf16 v[176:179], a[104:107], v[132:135], v[176:179]\n"
      "v_mfma_f32_16x16x32_bf16 v[176:179], a[108:111], v[136:139], v[176:179]\n"
      "s_waitcnt lgkmcnt(0)\n"
      "v_cmp_gt_i32 vcc, v21, v22\n"
      "v_lshlrev_b32 v182, 16, v197\n"
      "v_mfma_f32_16x16x32_bf16 v[176:179], a[112:115], v[140:143], v[176:179]\n"
      "buffer_load_dwordx4 v0, s[8:11], s46 offen offset:3072 sc1 lds\n"
      "v_mfma_f32_16x16x32_bf16 v[176:179], a[116:119], v[144:147], v[176:179]\n"
      "v_mul_f32 v219, v172, v172\n"
      "v_fmac_f32 v219, v173, v173\n"
      "v_mfma_f32_16x16x32_bf16 v[176:179], a[120:123], v[148:151], v[176:179]\n"
      "s_cbranch_vccnz .Lsp_vz1_23\n"
      ".Lbk_vz1_23:\n"
      "ds_read_b128 v[204:207], v19\n"
      "v_mfma_f32_16x16x32_bf16 v[176:179], a[124:127], v[152:155], v[176:179]\n"
      "v_and_b32 v185, 0xffff0000, v198\n"
      "v_add_f32 v218, v172, v173\n"
      "v_mfma_f32_16x16x32_bf16 v[176:179], a[128:131], v[156:159], v[176:179]\n"
      "v_add_u32 v21, 1, v21\n"
      "v_mfma_f32_16x16x32_bf16 v[176:179], a[132:135], v[160:163], v[176:179]\n"
      "buffer_load_dwordx4 v0, s[8:11], s46 offen offset:2048 sc1 lds\n"
      "v_mfma_f32_16x16x32_bf16 v[176:179], a[136:139], v[164:167], v[176:179]\n"
      "s_waitcnt vmcnt(10)\n"
      "ds_write_b32 v23, v21\n"
      "v_mfma_f32_16x16x32_bf16 v[176:179], a[140:143], v[168:171], v[176:179]\n"
      "v_mfma_f32_16x16x32_bf16 v[180:183], a[144:147], v[100:103], v[180:183]\n"
      "s_add_u32 m0, s40, 61440\n"
      "v_add_f32 v218, v218, v174\n"
      "v_mfma_f32_16x16x32_bf16 v[180:183], a[148:151], v[104:107], v[180:183]\n"
      "buffer_load_dwordx4 v[196:199], v13, s[8:11], s47 offen offset:1024 sc1\n"
      "v_mfma_f32_16x16x32_bf16 v[180:183], a[152:155], v[108:111], v[180:183]\n"
      "s_waitcnt lgkmcnt(1)\n"
      "v_add_f32 v205, v205, v207\n"
      "v_add_f32 v204, v204, v206\n"
      "v_mfma_f32_16x16x32_bf16 v[180:183], a[156:159], v[112:115], v[180:183]\n"
      "s_nop 0\n"
      "v_add_f32_dpp v204, v204, v204 quad_perm:[1,0,3,2] row_mask:0xf bank_mask:0xf\n"
      "v_fmac_f32 v219, v174, v174\n"
      "v_mfma_f32_16x16x32_bf16 v[180:183], a[160:163], v[116:119], v[180:183]\n"
      "v_add_f32_dpp v205, v205, v205 quad_perm:[1,0,3,2] row_mask:0xf bank_mask:0xf\n"
      "v_add_f32 v218, v218, v175\n"
      "v_mfma_f32_16x16x32_bf16 v[180:183], a[164:167], v[120:123], v[180:183]\n"
      "buffer_load_dwordx4 v1, s[8:11], s46 offen offset:0 sc1 lds\n"
      "v_mfma_f32_16x16x32_bf16 v[180:183], a[168:171], v[124:127], v[180:183]\n"
      "v_add_f32_dpp v204, v204, v204 quad_perm:[2,3,0,1] row_mask:0xf bank_mask:0xf\n"
      "v_add_f32 v218, v218, v176\n"
      "v_mfma_f32_16x16x32_bf16 v[180:183], a[172:175], v[128:131], v[180:183]\n"
      "v_fmac_f32 v219, v175, v175\n"
      "v_mfma_f32_16x16x32_bf16 v[180:183], a[176:179], v[132:135], v[180:183]\n"
      "s_add_u32 s38, s38, 24576\n"
      "v_add_f32_dpp v205, v205, v205 quad_perm:[2,3,0,1] row_mask:0xf bank_mask:0xf\n"
      "v_mfma_f32_16x16x32_bf16 v[180:183], a[180:183], v[136:139], v[180:183]\n"
      "s_add_u32 s50, s50, 24576\n"
      "v_add_f32_dpp v205, v205, v205 row_half_mirror row_mask:0xf bank_mask:0xf\n"
      "v_fmac_f32 v219, v176, v176\n"
      "v_mfma_f32_16x16x32_bf16 v[180:183], a[184:187], v[140:143], v[180:183]\n"
      "ds_read_b32 v22, v24\n"
      "v_mfma_f32_16x16x32_bf16 v[180:183], a[188:191], v[144:147], v[180:183]\n"
      "v_add_f32_dpp v204, v204, v204 row_half_mirror row_mask:0xf bank_mask:0xf\n"
      "v_mfma_f32_16x16x32_bf16 v[180:183], a[192:195], v[148:151], v[180:183]\n"
      "s_waitcnt vmcnt(10) lgkmcnt(1)\n"
      "buffer_store_dword v25, v12, s[8:11], s50 offen sc1\n"
      "v_mfma_f32_16x16x32_bf16 v[180:183], a[196:199], v[152:155], v[180:183]\n"
      "v_fmaak_f32 v205, v27, v205, 0x36a7c5ac\n"
      "v_mul_f32 v204, v27, v204\n"
      "v_mfma_f32_16x16x32_bf16 v[180:183], a[200:203], v[156:159], v[180:183]\n"
      "buffer_store_dwordx2 v[204:205], v20, s[8:11], s49 offen sc1\n"
      "v_mfma_f32_16x16x32_bf16 v[180:183], a[204:207], v[160:163], v[180:183]\n"
      "s_waitcnt lgkmcnt(0)\n"
      "v_cvt_pk_bf16_f32 v209, v174, v175\n"
      "v_cmp_gt_i32 vcc, v21, v22\n"
      "v_mfma_f32_16x16x32_bf16 v[180:183], a[208:211], v[164:167], v[180:183]\n"
      "s_sub_u32 s47, s46, s45\n"
      "v_add_f32 v218, v218, v177\n"
      "v_fmac_f32 v219, v177, v177\n"
      "v_mfma_f32_16x16x32_bf16 v[180:183], a[212:215], v[168:171], v[180:183]\n"
      "v_cvt_pk_bf16_f32 v210, v176, v177\n"
      "v_add_f32 v218, v218, v178\n"
      "v_mfma_f32_16x16x32_bf16 v[184:187], a[216:219], v[100:103], v[184:187]\n"
      "s_cbranch_vccnz .Lsp_vy0_24\n"
      ".Lbk_vy0_24:\n"
      "v_mfma_f32_16x16x32_bf16 v[184:187], a[220:223], v[104:107], v[184:187]\n"
      "ds_read_b128 v[100:103], v10 offset:8192\n"
      "v_add_f32 v218, v218, v179\n"
      "v_mfma_f32_16x16x32_bf16 v[184:187], a[224:227], v[108:111], v[184:187]\n"
      "v_fmac_f32 v219, v178, v178\n"
      "v_fmac_f32 v219, v179, v179\n"
      "v_mfma_f32_16x16x32_bf16 v[184:187], a[228:231], v[112:115], v[184:187]\n"
      "ds_read_b128 v[112:115], v10 offset:11264\n"
      "v_fmac_f32 v219, v180, v180\n"
      "v_mfma_f32_16x16x32_bf16 v[184:187], a[232:235], v[116:119], v[184:187]\n"
      "ds_read_b128 v[104:107], v10 offset:9216\n"
      "v_fmac_f32 v219, v181, v181\n"
      "v_mfma_f32_16x16x32_bf16 v[184:187], a[236:239], v[120:123], v[184:187]\n"
      "ds_read_b128 v[108:111], v10 offset:10240\n"
      "v_mfma_f32_16x16x32_bf16 v[184:187], a[240:243], v[124:127], v[184:187]\n"
      "s_waitcnt vmcnt(10)\n"
      "ds_read_b128 v[124:127], v10 offset:14336\n"
      "v_lshlrev_b32 v172, 16, v192\n"
      "v_mfma_f32_16x16x32_bf16 v[184:187], a[244:247], v[128:131], v[184:187]\n"
      "v_cvt_pk_bf16_f32 v211, v178, v179\n"
      "v_lshlrev_b32 v176, 16, v194\n"
      "v_mfma_f32_16x16x32_bf16 v[184:187], a[248:251], v[132:135], v[184:187]\n"
      "ds_read_b128 v[116:119], v10 offset:12288\n"
      "v_add_f32 v218, v218, v180\n"
      "v_mfma_f32_16x16x32_bf16 v[184:187], a[252:255], v[136:139], v[184:187]\n"
      "v_cvt_pk_bf16_f32 v213, v182, v183\n"
      "v_and_b32 v173, 0xffff0000, v192\n"
      "v_mfma_f32_16x16x32_bf16 v[184:187], v[32:35], v[140:143], v[184:187]\n"
      "ds_read_b128 v[120:123], v10 offset:13312\n"
      "v_fmac_f32 v219, v182, v182\n"
      "v_mfma_f32_16x16x32_bf16 v[184:187], v[36:39], v[144:147], v[184:187]\n"
      "ds_read_b128 v[128:131], v10 offset:15360\n"
      "v_lshlrev_b32 v178, 16, v195\n"
      "v_mfma_f32_16x16x32_bf16 v[184:187], v[40:43], v[148:151], v[184:187]\n"
      "ds_read2_b32 v[132:133], v6 offset0:0 offset1:32\n"
      "v_and_b32 v175, 0xffff0000, v193\n"
      "v_mfma_f32_16x16x32_bf16 v[184:187], v[44:47], v[152:155], v[184:187]\n"
      "buffer_store_dwordx4 v[208:211], v15, s[8:11], s38 offen sc1\n"
      "v_mfma_f32_16x16x32_bf16 v[184:187], v[48:51], v[156:159], v[184:187]\n"
      "ds_read_b128 v[144:147], v10 offset:2048\n"
      "v_add_f32 v218, v218, v181\n"
      "v_mfma_f32_16x16x32_bf16 v[184:187], v[52:55], v[160:163], v[184:187]\n"
      "v_add_f32 v218, v218, v182\n"
      "v_and_b32 v179, 0xffff0000, v195\n"
      "v_mfma_f32_16x16x32_bf16 v[184:187], v[56:59], v[164:167], v[184:187]\n"
      "s_waitcnt lgkmcnt(4)\n"
      "v_add_f32 v218, v218, v183\n"
      "v_lshlrev_b32 v174, 16, v193\n"
      "v_mfma_f32_16x16x32_bf16 v[184:187], v[60:63], v[168:171], v[184:187]\n"
      "s_sub_u32 s20, s20, 1\n"
      "s_cbranch_scc1 .Lv_tail\n"
      "v_mfma_f32_16x16x32_bf16 v[188:191], v[64:67], v[100:103], 0\n"
      "ds_read2_b32 v[134:135], v6 offset0:64 offset1:96\n"
      "v_and_b32 v177, 0xffff0000, v194\n"
      "v_mfma_f32_16x16x32_bf16 v[188:191], v[68:71], v[104:107], v[188:191]\n"
      "buffer_load_dwordx4 v[192:195], v13, s[8:11], s47 offen sc1\n"
      "v_mfma_f32_16x16x32_bf16 v[188:191], v[72:75], v[108:111], v[188:191]\n"
      "ds_read2_b32 v[168:169], v7 offset0:0 offset1:32\n"
      "v_mfma_f32_16x16x32_bf16 v[188:191], v[76:79], v[112:115], v[188:191]\n"
      "ds_read_b128 v[152:155], v10 offset:4096\n"
      "v_mfma_f32_16x16x32_bf16 v[188:191], v[80:83], v[116:119], v[188:191]\n"
      "s_waitcnt lgkmcnt(3)\n"
      "v_cvt_pk_bf16_f32 v215, v186, v187\n"
      "v_mfma_f32_16x16x32_bf16 v[188:191], v[84:87], v[120:123], v[188:191]\n"
      "ds_read_b128 v[148:151], v10 offset:3072\n"
      "v_cvt_pk_bf16_f32 v212, v180, v181\n"
      "v_mfma_f32_16x16x32_bf16 v[188:191], v[88:91], v[124:127], v[188:191]\n"
      "ds_read_b128 v[136:139], v10 offset:0\n"
      "s_add_u32 s49, s49, 24576\n"
      "v_mfma_f32_16x16x32_bf16 v[188:191], v[92:95], v[128:131], v[188:191]\n"
      "s_waitcnt lgkmcnt(4)\n"
      "ds_read_b128 v[160:163], v10 offset:6144\n"
      "v_fmac_f32 v219, v183, v183\n"
      "v_mfma_f32_16x16x32_bf16 v[188:191], v[96:99], v[132:135], v[188:191]\n"
      "ds_read_b128 v[156:159], v10 offset:5120\n"
      "v_add_f32 v218, v218, v184\n"
      "v_mfma_f32_16x16x32_bf16 v[172:175], a[0:3], v[100:103], v[172:175]\n"
      "ds_read2_b32 v[170:171], v7 offset0:64 offset1:96\n"
      "v_add_f32 v218, v218, v185\n"
      "v_mfma_f32_16x16x32_bf16 v[172:175], a[4:7], v[104:107], v[172:175]\n"
      "ds_read_b128 v[140:143], v10 offset:1024\n"
      "v_cvt_pk_bf16_f32 v214, v184, v185\n"
      "v_mfma_f32_16x16x32_bf16 v[172:175], a[8:11], v[108:111], v[172:175]\n"
      "ds_read_b128 v[164:167], v10 offset:7168\n"
      "v_fmac_f32 v219, v184, v184\n"
      "v_mfma_f32_16x16x32_bf16 v[172:175], a[12:15], v[112:115], v[172:175]\n"
      "s_mov_b32 s46, s36\n"
      "v_fmac_f32 v219, v185, v185\n"
      "v_add_f32 v218, v218, v186\n"
      "v_mfma_f32_16x16x32_bf16 v[172:175], a[16:19], v[116:119], v[172:175]\n"
      "ds_write_b128 v17, v[188:191]\n"
      "v_mfma_f32_16x16x32_bf16 v[172:175], a[20:23], v[120:123], v[172:175]\n"
      "s_add_u32 s50, s50, 24576\n"
      "v_add_u32 v21, 1, v21\n"
      "v_mfma_f32_16x16x32_bf16 v[172:175], a[24:27], v[124:127], v[172:175]\n"
      "s_add_u32 m0, s39, 0\n"
      "v_add_f32 v218, v218, v187\n"
      "v_mfma_f32_16x16x32_bf16 v[172:175], a[28:31], v[128:131], v[172:175]\n"
      "v_fmac_f32 v219, v186, v186\n"
      "v_fmac_f32_e64 v219, v187, v187\n"
      "v_mfma_f32_16x16x32_bf16 v[172:175], a[32:35], v[132:135], v[172:175]\n"
      "s_waitcnt lgkmcnt(2)\n"
      "ds_write_b64 v18, v[218:219]\n"
      "v_mfma_f32_16x16x32_bf16 v[172:175], a[36:39], v[136:139], v[172:175]\n"
      "s_add_u32 s36, s36, 24576\n"
      "v_mfma_f32_16x16x32_bf16 v[172:175], a[40:43], v[140:143], v[172:175]\n"
      "buffer_store_dwordx4 v[212:215], v15, s[8:11], s38 offen offset:1024 sc1\n"
      "v_mfma_f32_16x16x32_bf16 v[172:175], a[44:47], v[144:147], v[172:175]\n"
      "ds_write_b32 v23, v21\n"
      "v_mfma_f32_16x16x32_bf16 v[172:175], a[48:51], v[148:151], v[172:175]\n"
      "s_waitcnt vmcnt(11) lgkmcnt(2)\n"
      "v_cmp_ne_u32 vcc, s21, v28\n"
      "v_mfma_f32_16x16x32_bf16 v[172:175], a[52:55], v[152:155], v[172:175]\n"
      "v_mfma_f32_16x16x32_bf16 v[172:175], a[56:59], v[156:159], v[172:175]\n"
      "v_mfma_f32_16x16x32_bf16 v[172:175], a[60:63], v[160:163], v[172:175]\n"
      "v_mfma_f32_16x16x32_bf16 v[172:175], a[64:67], v[164:167], v[172:175]\n"
      "s_cbranch_vccnz .Lsp_v0_25\n"
      ".Lbk_v0_25:\n"
      "v_mfma_f32_16x16x32_bf16 v[172:175], a[68:71], v[168:171], v[172:175]\n"
      "buffer_load_dwordx4 v0, s[8:11], s46 offen offset:0 sc1 lds\n"
      "v_mfma_f32_16x16x32_bf16 v[176:179], a[72:75], v[100:103], v[176:179]\n"
      "s_waitcnt vmcnt(7) lgkmcnt(0)\n"
      "s_min_u32 s36, s36, s6\n"
      "v_lshlrev_b32 v186, 16, v199\n"
      "v_mfma_f32_16x16x32_bf16 v[176:179], a[76:79], v[104:107], v[176:179]\n"
      "v_and_b32 v181, 0xffff0000, v196\n"
      "v_lshlrev_b32 v184, 16, v198\n"
      "v_mfma_f32_16x16x32_bf16 v[176:179], a[80:83], v[108:111], v[176:179]\n"
      "s_add_u32 s38, s38, 24576\n"
      "ds_read_b32 v22, v24\n"
      "v_lshlrev_b32 v180, 16, v196\n"
      "v_mfma_f32_16x16x32_bf16 v[176:179], a[84:87], v[112:115], v[176:179]\n"
      "buffer_load_dword v28, v11, s[8:11], s36 offen sc1\n"
      "v_mfma_f32_16x16x32_bf16 v[176:179], a[88:91], v[116:119], v[176:179]\n"
      "v_cvt_pk_bf16_f32 v209, v174, v175\n"
      "v_mfma_f32_16x16x32_bf16 v[176:179], a[92:95], v[120:123], v[176:179]\n"
      "buffer_load_dwordx4 v0, s[8:11], s46 offen offset:2048 sc1 lds\n"
      "v_mfma_f32_16x16x32_bf16 v[176:179], a[96:99], v[124:127], v[176:179]\n"
      "v_mul_f32_e64 v219, v172, v172\n"
      "v_and_b32 v185, 0xffff0000, v198\n"
      "v_mfma_f32_16x16x32_bf16 v[176:179], a[100:103], v[128:131], v[176:179]\n"
      "s_waitcnt lgkmcnt(0)\n"
      "v_cmp_gt_i32 vcc, v21, v22\n"
      "v_and_b32 v183, 0xffff0000, v197\n"
      "v_mfma_f32_16x16x32_bf16 v[176:179], a[104:107], v[132:135], v[176:179]\n"
      "v_lshlrev_b32_e64 v182, 16, v197\n"
      "v_and_b32 v187, 0xffff0000, v199\n"
      "v_mfma_f32_16x16x32_bf16 v[176:179], a[108:111], v[136:139], v[176:179]\n"
      "buffer_load_dwordx4 v[196:199], v13, s[8:11], s47 offen offset:1024 sc1\n"
      "v_mfma_f32_16x16x32_bf16 v[176:179], a[112:115], v[140:143], v[176:179]\n"
      "v_fmac_f32_e64 v219, v173, v173\n"
      "v_fmac_f32_e64 v219, v174, v174\n"
      "v_mfma_f32_16x16x32_bf16 v[176:179], a[116:119], v[144:147], v[176:179]\n"
      "s_cbranch_vccnz .Lsp_vz0_26\n"
      ".Lbk_vz0_26:\n"
      "ds_read_b128 v[204:207], v19\n"
      "v_mfma_f32_16x16x32_bf16 v[176:179], a[120:123], v[148:151], v[176:179]\n"
      "v_mfma_f32_16x16x32_bf16 v[176:179], a[124:127], v[152:155], v[176:179]\n"
      "v_add_u32 v21, 1, v21\n"
      "v_mfma_f32_16x16x32_bf16 v[176:179], a[128:131], v[156:159], v[176:179]\n"
      "buffer_load_dwordx4 v0, s[8:11], s46 offen offset:1024 sc1 lds\n"
      "v_mfma_f32_16x16x32_bf16 v[176:179], a[132:135], v[160:163], v[176:179]\n"
      "v_add_f32 v218, v172, v173\n"
      "v_mfma_f32_16x16x32_bf16 v[176:179], a[136:139], v[164:167], v[176:179]\n"
      "v_fmac_f32 v219, v175, v175\n"
      "v_mfma_f32_16x16x32_bf16 v[176:179], a[140:143], v[168:171], v[176:179]\n"
      "s_waitcnt vmcnt(10)\n"
      "ds_write_b32 v23, v21\n"
      "v_mfma_f32_16x16x32_bf16 v[180:183], a[144:147], v[100:103], v[180:183]\n"
      "s_waitcnt lgkmcnt(1)\n"
      "v_add_f32 v218, v218, v174\n"
      "v_add_f32 v204, v204, v206\n"
      "v_mfma_f32_16x16x32_bf16 v[180:183], a[148:151], v[104:107], v[180:183]\n"
      "buffer_load_dwordx4 v0, s[8:11], s46 offen offset:3072 sc1 lds\n"
      "v_mfma_f32_16x16x32_bf16 v[180:183], a[152:155], v[108:111], v[180:183]\n"
      "v_add_f32 v205, v205, v207\n"
      "v_mfma_f32_16x16x32_bf16 v[180:183], a[156:159], v[112:115], v[180:183]\n"
      "v_cvt_pk_bf16_f32 v210, v176, v177\n"
      "v_mfma_f32_16x16x32_bf16 v[180:183], a[160:163], v[116:119], v[180:183]\n"
      "v_add_f32_dpp v205, v205, v205 quad_perm:[1,0,3,2] row_mask:0xf bank_mask:0xf\n"
      "v_mfma_f32_16x16x32_bf16 v[180:183], a[164:167], v[120:123], v[180:183]\n"
      "v_fmac_f32 v219, v176, v176\n"
      "s_add_u32 m0, s40, 0\n"
      "v_mfma_f32_16x16x32_bf16 v[180:183], a[168:171], v[124:127], v[180:183]\n"
      "buffer_load_dwordx4 v1, s[8:11], s46 offen offset:0 sc1 lds\n"
      "v_mfma_f32_16x16x32_bf16 v[180:183], a[172:175], v[128:131], v[180:183]\n"
      "v_add_f32_dpp v205, v205, v205 quad_perm:[2,3,0,1] row_mask:0xf bank_mask:0xf\n"
      "v_add_f32_dpp v204, v204, v204 quad_perm:[1,0,3,2] row_mask:0xf bank_mask:0xf\n"
      "v_mfma_f32_16x16x32_bf16 v[180:183], a[176:179], v[132:135], v[180:183]\n"
      "v_add_f32_dpp v204, v204, v204 quad_perm:[2,3,0,1] row_mask:0xf bank_mask:0xf\n"
      "v_add_f32_dpp v205, v205, v205 row_half_mirror row_mask:0xf bank_mask:0xf\n"
      "v_mfma_f32_16x16x32_bf16 v[180:183], a[180:183], v[136:139], v[180:183]\n"
      "ds_read_b32 v22, v24\n"
      "v_add_f32_dpp v204, v204, v204 row_half_mirror row_mask:0xf bank_mask:0xf\n"
      "v_mfma_f32_16x16x32_bf16 v[180:183], a[184:187], v[140:143], v[180:183]\n"
      "s_waitcnt vmcnt(10) lgkmcnt(1)\n"
      "buffer_store_dword v25, v12, s[8:11], s50 offen sc1\n"
      "v_mfma_f32_16x16x32_bf16 v[180:183], a[188:191], v[144:147], v[180:183]\n"
      "v_add_f32 v218, v218, v175\n"
      "v_add_f32 v218, v218, v176\n"
      "v_mfma_f32_16x16x32_bf16 v[180:183], a[192:195], v[148:151], v[180:183]\n"
      "v_mul_f32 v204, v27, v204\n"
      "v_mfma_f32_16x16x32_bf16 v[180:183], a[196:199], v[152:155], v[180:183]\n"
      "v_fmaak_f32 v205, v27, v205, 0x36a7c5ac\n"
      "v_add_f32 v218, v218, v177\n"
      "v_mfma_f32_16x16x32_bf16 v[180:183], a[200:203], v[156:159], v[180:183]\n"
      "buffer_store_dwordx2 v[204:205], v20, s[8:11], s49 offen sc1\n"
      "v_mfma_f32_16x16x32_bf16 v[180:183], a[204:207], v[160:163], v[180:183]\n"
      "s_waitcnt lgkmcnt(0)\n"
      "v_fmac_f32 v219, v177, v177\n"
      "v_cmp_gt_i32 vcc, v21, v22\n"
      "v_mfma_f32_16x16x32_bf16 v[180:183], a[208:211], v[164:167], v[180:183]\n"
      "s_waitcnt vmcnt(10)\n"
      "v_fmac_f32 v219, v178, v178\n"
      "v_and_b32 v175, 0xffff0000, v193\n"
      "v_mfma_f32_16x16x32_bf16 v[180:183], a[212:215], v[168:171], v[180:183]\n"
      "v_lshlrev_b32 v176, 16, v194\n"
      "v_mfma_f32_16x16x32_bf16 v[184:187], a[216:219], v[100:103], v[184:187]\n"
      ".Lv_p1:\n"
      "s_cbranch_execz .Lv_pr\n"
      "v_add_f32 v218, v218, v178\n"
      "v_cvt_pk_bf16_f32 v211, v178, v179\n"
      "v_mfma_f32_16x16x32_bf16 v[184:187], a[220:223], v[104:107], v[184:187]\n"
      "s_cbranch_vccnz .Lsp_vy1_27\n"
      ".Lbk_vy1_27:\n"
      "ds_read_b128 v[100:103], v10 offset:28672\n"
      "v_mfma_f32_16x16x32_bf16 v[184:187], a[224:227], v[108:111], v[184:187]\n"
      "v_cvt_pk_bf16_f32 v208, v172, v173\n"
      "v_mfma_f32_16x16x32_bf16 v[184:187], a[228:231], v[112:115], v[184:187]\n"
      "ds_read_b128 v[112:115], v10 offset:31744\n"
      "v_fmac_f32 v219, v179, v179\n"
      "v_mfma_f32_16x16x32_bf16 v[184:187], a[232:235], v[116:119], v[184:187]\n"
      "ds_read_b128 v[104:107], v10 offset:29696\n"
      "v_fmac_f32 v219, v180, v180\n"
      "v_mfma_f32_16x16x32_bf16 v[184:187], a[236:239], v[120:123], v[184:187]\n"
      "v_and_b32 v177, 0xffff0000, v194\n"
      "v_mfma_f32_16x16x32_bf16 v[184:187], a[240:243], v[124:127], v[184:187]\n"
      "ds_read_b128 v[108:111], v10 offset:30720\n"
      "v_fmac_f32 v219, v181, v181\n"
      "v_mfma_f32_16x16x32_bf16 v[184:187], a[244:247], v[128:131], v[184:187]\n"
      "ds_read_b128 v[116:119], v10 offset:32768\n"
      "v_add_f32 v218, v218, v179\n"
      "v_mfma_f32_16x16x32_bf16 v[184:187], a[248:251], v[132:135], v[184:187]\n"
      "v_cvt_pk_bf16_f32 v213, v182, v183\n"
      "v_mfma_f32_16x16x32_bf16 v[184:187], a[252:255], v[136:139], v[184:187]\n"
      "ds_read_b128 v[124:127], v10 offset:34816\n"
      "v_add_f32 v218, v218, v180\n"
      "v_mfma_f32_16x16x32_bf16 v[184:187], v[32:35], v[140:143], v[184:187]\n"
      "ds_read_b128 v[128:131], v10 offset:35840\n"
      "v_add_f32 v218, v218, v181\n"
      "v_mfma_f32_16x16x32_bf16 v[184:187], v[36:39], v[144:147], v[184:187]\n"
      "buffer_store_dwordx4 v[208:211], v15, s[8:11], s38 offen sc1\n"
      "v_mfma_f32_16x16x32_bf16 v[184:187], v[40:43], v[148:151], v[184:187]\n"
      "ds_read2_b32 v[132:133], v8 offset0:0 offset1:32\n"
      "v_fmac_f32 v219, v182, v182\n"
      "v_mfma_f32_16x16x32_bf16 v[184:187], v[44:47], v[152:155], v[184:187]\n"
      "v_and_b32 v173, 0xffff0000, v192\n"
      "v_mfma_f32_16x16x32_bf16 v[184:187], v[48:51], v[156:159], v[184:187]\n"
      "ds_read_b128 v[120:123], v10 offset:33792\n"
      "v_and_b32 v179, 0xffff0000, v195\n"
      "v_mfma_f32_16x16x32_bf16 v[184:187], v[52:55], v[160:163], v[184:187]\n"
      "s_sub_u32 s47, s46, s45\n"
      "ds_read2_b32 v[134:135], v8 offset0:64 offset1:96\n"
      "v_lshlrev_b32 v172, 16, v192\n"
      "v_mfma_f32_16x16x32_bf16 v[184:187], v[56:59], v[164:167], v[184:187]\n"
      "ds_read_b128 v[160:163], v10 offset:26624\n"
      "v_lshlrev_b32 v174, 16, v193\n"
      "v_mfma_f32_16x16x32_bf16 v[184:187], v[60:63], v[168:171], v[184:187]\n"
      "s_waitcnt lgkmcnt(4)\n"
      "ds_read_b128 v[144:147], v10 offset:22528\n"
      "v_lshlrev_b32 v178, 16, v195\n"
      "v_mfma_f32_16x16x32_bf16 v[188:191], v[64:67], v[100:103], 0\n"
      "v_add_f32 v218, v218, v182\n"
      "v_mfma_f32_16x16x32_bf16 v[188:191], v[68:71], v[104:107], v[188:191]\n"
      "ds_read_b128 v[148:151], v10 offset:23552\n"
      "v_cvt_pk_bf16_f32 v212, v180, v181\n"
      "v_mfma_f32_16x16x32_bf16 v[188:191], v[72:75], v[108:111], v[188:191]\n"
      "ds_read_b128 v[156:159], v10 offset:25600\n"
      "v_fmac_f32 v219, v183, v183\n"
      "v_mfma_f32_16x16x32_bf16 v[188:191], v[76:79], v[112:115], v[188:191]\n"
      "ds_read_b128 v[152:155], v10 offset:24576\n"
      "v_add_f32 v218, v218, v183\n"
      "v_mfma_f32_16x16x32_bf16 v[188:191], v[80:83], v[116:119], v[188:191]\n"
      "s_waitcnt lgkmcnt(5)\n"
      "ds_read2_b32 v[168:169], v9 offset0:0 offset1:32\n"
      "v_mfma_f32_16x16x32_bf16 v[188:191], v[84:87], v[120:123], v[188:191]\n"
      "s_cmp_eq_u32 s36, s37\n"
      "s_cselect_b32 s48, 49152, s51\n"
      "ds_read2_b32 v[170:171], v9 offset0:64 offset1:96\n"
      "v_mfma_f32_16x16x32_bf16 v[188:191], v[88:91], v[124:127], v[188:191]\n"
      "buffer_load_dwordx4 v[192:195], v13, s[8:11], s47 offen sc1\n"
      "v_mfma_f32_16x16x32_bf16 v[188:191], v[92:95], v[128:131], v[188:191]\n"
      "v_add_f32 v218, v218, v184\n"
      "v_fmac_f32 v219, v184, v184\n"
      "v_mfma_f32_16x16x32_bf16 v[188:191], v[96:99], v[132:135], v[188:191]\n"
      "ds_read_b128 v[136:139], v10 offset:20480\n"
      "v_mfma_f32_16x16x32_bf16 v[172:175], a[0:3], v[100:103], v[172:175]\n"
      "v_add_f32 v218, v218, v185\n"
      "s_add_u32 m0, s39, 20480\n"
      "v_mfma_f32_16x16x32_bf16 v[172:175], a[4:7], v[104:107], v[172:175]\n"
      "ds_read_b128 v[164:167], v10 offset:27648\n"
      "v_cvt_pk_bf16_f32 v214, v184, v185\n"
      "v_mfma_f32_16x16x32_bf16 v[172:175], a[8:11], v[108:111], v[172:175]\n"
      "ds_read_b128 v[140:143], v10 offset:21504\n"
      "v_fmac_f32 v219, v185, v185\n"
      "v_mfma_f32_16x16x32_bf16 v[172:175], a[12:15], v[112:115], v[172:175]\n"
      "v_fmac_f32 v219, v186, v186\n"
      "v_fmac_f32 v219, v187, v187\n"
      "v_mfma_f32_16x16x32_bf16 v[172:175], a[16:19], v[116:119], v[172:175]\n"
      "ds_write_b128 v17, v[188:191]\n"
      "v_mfma_f32_16x16x32_bf16 v[172:175], a[20:23], v[120:123], v[172:175]\n"
      "v_cvt_pk_bf16_f32 v215, v186, v187\n"
      "v_add_f32 v218, v218, v186\n"
      "v_mfma_f32_16x16x32_bf16 v[172:175], a[24:27], v[124:127], v[172:175]\n"
      "buffer_store_dwordx4 v[212:215], v15, s[8:11], s38 offen offset:1024 sc1\n"
      "v_mfma_f32_16x16x32_bf16 v[172:175], a[28:31], v[128:131], v[172:175]\n"
      "v_add_f32 v218, v218, v187\n"
      "v_mfma_f32_16x16x32_bf16 v[172:175], a[32:35], v[132:135], v[172:175]\n"
      "s_waitcnt lgkmcnt(2)\n"
      "ds_write_b64 v18, v[218:219]\n"
      "v_mfma_f32_16x16x32_bf16 v[172:175], a[36:39], v[136:139], v[172:175]\n"
      "s_waitcnt lgkmcnt(2)\n"
      "v_add_u32 v21, 1, v21\n"
      "s_mov_b32 s46, s36\n"
      "v_mfma_f32_16x16x32_bf16 v[172:175], a[40:43], v[140:143], v[172:175]\n"
      "s_waitcnt vmcnt(8)\n"
      "v_and_b32 v181, 0xffff0000, v196\n"
      "v_mfma_f32_16x16x32_bf16 v[172:175], a[44:47], v[144:147], v[172:175]\n"
      "ds_write_b32 v23, v21\n"
      "v_mfma_f32_16x16x32_bf16 v[172:175], a[48:51], v[148:151], v[172:175]\n"
      "v_cmp_ne_u32 vcc, s21, v28\n"
      "v_mfma_f32_16x16x32_bf16 v[172:175], a[52:55], v[152:155], v[172:175]\n"
      "v_lshlrev_b32 v186, 16, v199\n"
      "s_add_u32 s36, s36, s48\n"
      "v_mfma_f32_16x16x32_bf16 v[172:175], a[56:59], v[156:159], v[172:175]\n"
      "v_lshlrev_b32_e64 v180, 16, v196\n"
      "v_lshlrev_b32_e64 v184, 16, v198\n"
      "v_mfma_f32_16x16x32_bf16 v[172:175], a[60:63], v[160:163], v[172:175]\n"
      "s_add_u32 s49, s49, 24576\n"
      "v_and_b32 v187, 0xffff0000, v199\n"
      "v_mfma_f32_16x16x32_bf16 v[172:175], a[64:67], v[164:167], v[172:175]\n"
      "s_cbranch_vccnz .Lsp_v1_28\n"
      ".Lbk_v1_28:\n"
      "s_min_u32 s36, s36, s6\n"
      "v_mfma_f32_16x16x32_bf16 v[172:175], a[68:71], v[168:171], v[172:175]\n"
      "buffer_load_dword v28, v11, s[8:11], s36 offen sc1\n"
      "v_mfma_f32_16x16x32_bf16 v[176:179], a[72:75], v[100:103], v[176:179]\n"
      "v_mfma_f32_16x16x32_bf16 v[176:179], a[76:79], v[104:107], v[176:179]\n"
      "buffer_load_dwordx4 v0, s[8:11], s46 offen offset:1024 sc1 lds\n"
      "v_mfma_f32_16x16x32_bf16 v[176:179], a[80:83], v[108:111], v[176:179]\n"
      "ds_read_b32 v22, v24\n"
      "v_mfma_f32_16x16x32_bf16 v[176:179], a[84:87], v[112:115], v[176:179]\n"
      "v_and_b32 v183, 0xffff0000, v197\n"
      "v_mfma_f32_16x16x32_bf16 v[176:179], a[88:91], v[116:119], v[176:179]\n"
      "v_mfma_f32_16x16x32_bf16 v[176:179], a[92:95], v[120:123], v[176:179]\n"
      "buffer_load_dwordx4 v0, s[8:11], s46 offen offset:0 sc1 lds\n"
      "v_mfma_f32_16x16x32_bf16 v[176:179], a[96:99], v[124:127], v[176:179]\n"
      "v_cvt_pk_bf16_f32 v208, v172, v173\n"
      "v_mfma_f32_16x16x32_bf16 v[176:179], a[100:103], v[128:131], v[176:179]\n"
      "v_mfma_f32_16x16x32_bf16 v[176:179], a[104:107], v[132:135], v[176:179]\n"
      "v_mfma_f32_16x16x32_bf16 v[176:179], a[108:111], v[136:139], v[176:179]\n"
      "s_waitcnt lgkmcnt(0)\n"
      "v_cmp_gt_i32 vcc, v21, v22\n"
      "v_lshlrev_b32 v182, 16, v197\n"
      "v_mfma_f32_16x16x32_bf16 v[176:179], a[112:115], v[140:143], v[176:179]\n"
      "buffer_load_dwordx4 v0, s[8:11], s46 offen offset:3072 sc1 lds\n"
      "v_mfma_f32_16x16x32_bf16 v[176:179], a[116:119], v[144:147], v[176:179]\n"
      "v_mul_f32 v219, v172, v172\n"
      "v_fmac_f32 v219, v173, v173\n"
      "v_mfma_f32_16x16x32_bf16 v[176:179], a[120:123], v[148:151], v[176:179]\n"
      "s_cbranch_vccnz .Lsp_vz1_29\n"
      ".Lbk_vz1_29:\n"
      "ds_read_b128 v[204:207], v19\n"
      "v_mfma_f32_16x16x32_bf16 v[176:179], a[124:127], v[152:155], v[176:179]\n"
      "v_and_b32 v185, 0xffff0000, v198\n"
      "v_add_f32 v218, v172, v173\n"
      "v_mfma_f32_16x16x32_bf16 v[176:179], a[128:131], v[156:159], v[176:179]\n"
      "v_add_u32 v21, 1, v21\n"
      "v_mfma_f32_16x16x32_bf16 v[176:179], a[132:135], v[160:163], v[176:179]\n"
      "buffer_load_dwordx4 v0, s[8:11], s46 offen offset:2048 sc1 lds\n"
      "v_mfma_f32_16x16x32_bf16 v[176:179], a[136:139], v[164:167], v[176:179]\n"
      "s_waitcnt vmcnt(10)\n"
      "ds_write_b32 v23, v21\n"
      "v_mfma_f32_16x16x32_bf16 v[176:179], a[140:143], v[168:171], v[176:179]\n"
      "v_mfma_f32_16x16x32_bf16 v[180:183], a[144:147], v[100:103], v[180:183]\n"
      "s_add_u32 m0, s40, 20480\n"
      "v_add_f32 v218, v218, v174\n"
      "v_mfma_f32_16x16x32_bf16 v[180:183], a[148:151], v[104:107], v[180:183]\n"
      "buffer_load_dwordx4 v[196:199], v13, s[8:11], s47 offen offset:1024 sc1\n"
      "v_mfma_f32_16x16x32_bf16 v[180:183], a[152:155], v[108:111], v[180:183]\n"
      "s_waitcnt lgkmcnt(1)\n"
      "v_add_f32 v205, v205, v207\n"
      "v_add_f32 v204, v204, v206\n"
      "v_mfma_f32_16x16x32_bf16 v[180:183], a[156:159], v[112:115], v[180:183]\n"
      "s_nop 0\n"
      "v_add_f32_dpp v204, v204, v204 quad_perm:[1,0,3,2] row_mask:0xf bank_mask:0xf\n"
      "v_fmac_f32 v219, v174, v174\n"
      "v_mfma_f32_16x16x32_bf16 v[180:183], a[160:163], v[116:119], v[180:183]\n"
      "v_add_f32_dpp v205, v205, v205 quad_perm:[1,0,3,2] row_mask:0xf bank_mask:0xf\n"
      "v_add_f32 v218, v218, v175\n"
      "v_mfma_f32_16x16x32_bf16 v[180:183], a[164:167], v[120:123], v[180:183]\n"
      "buffer_load_dwordx4 v1, s[8:11], s46 offen offset:0 sc1 lds\n"
      "v_mfma_f32_16x16x32_bf16 v[180:183], a[168:171], v[124:127], v[180:183]\n"
      "v_add_f32_dpp v204, v204, v204 quad_perm:[2,3,0,1] row_mask:0xf bank_mask:0xf\n"
      "v_add_f32 v218, v218, v176\n"
      "v_mfma_f32_16x16x32_bf16 v[180:183], a[172:175], v[128:131], v[180:183]\n"
      "v_fmac_f32 v219, v175, v175\n"
      "v_mfma_f32_16x16x32_bf16 v[180:183], a[176:179], v[132:135], v[180:183]\n"
      "s_add_u32 s38, s38, 24576\n"
      "v_add_f32_dpp v205, v205, v205 quad_perm:[2,3,0,1] row_mask:0xf bank_mask:0xf\n"
      "v_mfma_f32_16x16x32_bf16 v[180:183], a[180:183], v[136:139], v[180:183]\n"
      "s_add_u32 s50, s50, 24576\n"
      "v_add_f32_dpp v205, v205, v205 row_half_mirror row_mask:0xf bank_mask:0xf\n"
      "v_fmac_f32 v219, v176, v176\n"
      "v_mfma_f32_16x16x32_bf16 v[180:183], a[184:187], v[140:143], v[180:183]\n"
      "ds_read_b32 v22, v24\n"
      "v_mfma_f32_16x16x32_bf16 v[180:183], a[188:191], v[144:147], v[180:183]\n"
      "v_add_f32_dpp v204, v204, v204 row_half_mirror row_mask:0xf bank_mask:0xf\n"
      "v_mfma_f32_16x16x32_bf16 v[180:183], a[192:195], v[148:151], v[180:183]\n"
      "s_waitcnt vmcnt(10) lgkmcnt(1)\n"
      "buffer_store_dword v25, v12, s[8:11], s50 offen sc1\n"
      "v_mfma_f32_16x16x32_bf16 v[180:183], a[196:199], v[152:155], v[180:183]\n"
      "v_fmaak_f32 v205, v27, v205, 0x36a7c5ac\n"
      "v_mul_f32 v204, v27, v204\n"
      "v_mfma_f32_16x16x32_bf16 v[180:183], a[200:203], v[156:159], v[180:183]\n"
      "buffer_store_dwordx2 v[204:205], v20, s[8:11], s49 offen sc1\n"
      "v_mfma_f32_16x16x32_bf16 v[180:183], a[204:207], v[160:163], v[180:183]\n"
      "s_waitcnt lgkmcnt(0)\n"
      "v_cvt_pk_bf16_f32 v209, v174, v175\n"
      "v_cmp_gt_i32 vcc, v21, v22\n"
      "v_mfma_f32_16x16x32_bf16 v[180:183], a[208:211], v[164:167], v[180:183]\n"
      "s_sub_u32 s47, s46, s45\n"
      "v_add_f32 v218, v218, v177\n"
      "v_fmac_f32 v219, v177, v177\n"
      "v_mfma_f32_16x16x32_bf16 v[180:183], a[212:215], v[168:171], v[180:183]\n"
      "v_cvt_pk_bf16_f32 v210, v176, v177\n"
      "v_add_f32 v218, v218, v178\n"
      "v_mfma_f32_16x16x32_bf16 v[184:187], a[216:219], v[100:103], v[184:187]\n"
      "s_cbranch_vccnz .Lsp_vy0_30\n"
      ".Lbk_vy0_30:\n"
      "v_mfma_f32_16x16x32_bf16 v[184:187], a[220:223], v[104:107], v[184:187]\n"
      "ds_read_b128 v[100:103], v0 offset:8192\n"
      "v_add_f32 v218, v218, v179\n"
      "v_mfma_f32_16x16x32_bf16 v[184:187], a[224:227], v[108:111], v[184:187]\n"
      "v_fmac_f32 v219, v178, v178\n"
      "v_fmac_f32 v219, v179, v179\n"
      "v_mfma_f32_16x16x32_bf16 v[184:187], a[228:231], v[112:115], v[184:187]\n"
      "ds_read_b128 v[112:115], v0 offset:11264\n"
      "v_fmac_f32 v219, v180, v180\n"
      "v_mfma_f32_16x16x32_bf16 v[184:187], a[232:235], v[116:119], v[184:187]\n"
      "ds_read_b128 v[104:107], v0 offset:9216\n"
      "v_fmac_f32 v219, v181, v181\n"
      "v_mfma_f32_16x16x32_bf16 v[184:187], a[236:239], v[120:123], v[184:187]\n"
      "ds_read_b128 v[108:111], v0 offset:10240\n"
      "v_mfma_f32_16x16x32_bf16 v[184:187], a[240:243], v[124:127], v[184:187]\n"
      "s_waitcnt vmcnt(10)\n"
      "ds_read_b128 v[124:127], v0 offset:14336\n"
      "v_lshlrev_b32 v172, 16, v192\n"
      "v_mfma_f32_16x16x32_bf16 v[184:187], a[244:247], v[128:131], v[184:187]\n"
      "v_cvt_pk_bf16_f32 v211, v178, v179\n"
      "v_lshlrev_b32 v176, 16, v194\n"
      "v_mfma_f32_16x16x32_bf16 v[184:187], a[248:251], v[132:135], v[184:187]\n"
      "ds_read_b128 v[116:119], v0 offset:12288\n"
      "v_add_f32 v218, v218, v180\n"
      "v_mfma_f32_16x16x32_bf16 v[184:187], a[252:255], v[136:139], v[184:187]\n"
      "v_cvt_pk_bf16_f32 v213, v182, v183\n"
      "v_and_b32 v173, 0xffff0000, v192\n"
      "v_mfma_f32_16x16x32_bf16 v[184:187], v[32:35], v[140:143], v[184:187]\n"
      "ds_read_b128 v[120:123], v0 offset:13312\n"
      "v_fmac_f32 v219, v182, v182\n"
      "v_mfma_f32_16x16x32_bf16 v[184:187], v[36:39], v[144:147], v[184:187]\n"
      "ds_read_b128 v[128:131], v0 offset:15360\n"
      "v_lshlrev_b32 v178, 16, v195\n"
      "v_mfma_f32_16x16x32_bf16 v[184:187], v[40:43], v[148:151], v[184:187]\n"
      "ds_read2_b32 v[132:133], v2 offset0:0 offset1:32\n"
      "v_and_b32 v175, 0xffff0000, v193\n"
      "v_mfma_f32_16x16x32_bf16 v[184:187], v[44:47], v[152:155], v[184:187]\n"
      "buffer_store_dwordx4 v[208:211], v15, s[8:11], s38 offen sc1\n"
      "v_mfma_f32_16x16x32_bf16 v[184:187], v[48:51], v[156:159], v[184:187]\n"
      "ds_read_b128 v[144:147], v0 offset:2048\n"
      "v_add_f32 v218, v218, v181\n"
      "v_mfma_f32_16x16x32_bf16 v[184:187], v[52:55], v[160:163], v[184:187]\n"
      "v_add_f32 v218, v218, v182\n"
      "v_and_b32 v179, 0xffff0000, v195\n"
      "v_mfma_f32_16x16x32_bf16 v[184:187], v[56:59], v[164:167], v[184:187]\n"
      "s_waitcnt lgkmcnt(4)\n"
      "v_add_f32 v218, v218, v183\n"
      "v_lshlrev_b32 v174, 16, v193\n"
      "v_mfma_f32_16x16x32_bf16 v[184:187], v[60:63], v[168:171], v[184:187]\n"
      "s_sub_u32 s20, s20, 1\n"
      "s_cbranch_scc0 .Lv_loop\n"
      ".Lv_tail:\n"
      "s_nop 8\n"
      "v_cvt_pk_bf16_f32 v215, v186, v187\n"
      "v_cvt_pk_bf16_f32 v212, v180, v181\n"
      "s_add_u32 s49, s49, 24576\n"
      "v_fmac_f32 v219, v183, v183\n"
      "v_add_f32 v218, v218, v184\n"
      "v_add_f32 v218, v218, v185\n"
      "v_cvt_pk_bf16_f32 v214, v184, v185\n"
      "v_fmac_f32 v219, v184, v184\n"
      "v_fmac_f32 v219, v185, v185\n"
      "v_add_f32 v218, v218, v186\n"
      "s_add_u32 s50, s50, 24576\n"
      "v_add_f32 v218, v218, v187\n"
      "v_fmac_f32 v219, v186, v186\n"
      "v_fmac_f32 v219, v187, v187\n"
      "ds_write_b64 v18, v[218:219]\n"
      "s_waitcnt lgkmcnt(0)\n"
      "s_barrier\n"
      "buffer_store_dwordx4 v[212:215], v15, s[8:11], s38 offen offset:1024 sc1\n"
      "s_add_u32 s38, s38, 24576\n"
      "ds_read_b128 v[204:207], v19\n"
      "s_waitcnt lgkmcnt(0)\n"
      "v_add_f32 v204, v204, v206\n"
      "v_add_f32 v205, v205, v207\n"
      "s_nop 1\n"
      "v_add_f32_dpp v205, v205, v205 quad_perm:[1,0,3,2] row_mask:0xf bank_mask:0xf\n"
      "s_nop 1\n"
      "v_add_f32_dpp v205, v205, v205 quad_perm:[2,3,0,1] row_mask:0xf bank_mask:0xf\n"
      "v_add_f32_dpp v204, v204, v204 quad_perm:[1,0,3,2] row_mask:0xf bank_mask:0xf\n"
      "s_nop 1\n"
      "v_add_f32_dpp v204, v204, v204 quad_perm:[2,3,0,1] row_mask:0xf bank_mask:0xf\n"
      "v_add_f32_dpp v205, v205, v205 row_half_mirror row_mask:0xf bank_mask:0xf\n"
      "s_nop 0\n"
      "v_add_f32_dpp v204, v204, v204 row_half_mirror row_mask:0xf bank_mask:0xf\n"
      "s_waitcnt vmcnt(2)\n"
      "buffer_store_dword v25, v12, s[8:11], s50 offen sc1\n"
      "v_mul_f32 v204, v27, v204\n"
      "v_fmaak_f32 v205, v27, v205, 0x36a7c5ac\n"
      "buffer_store_dwordx2 v[204:205], v20, s[8:11], s49 offen sc1\n"
      "s_add_u32 s49, s49, 24576\n"
      "s_add_u32 s50, s50, 24576\n"
      "s_waitcnt vmcnt(0)\n"
      "buffer_store_dword v25, v12, s[8:11], s50 offen sc1\n"
      "s_add_u32 s50, s50, 24576\n"
      "s_endpgm\n"
      ".Lsp_w0_1:\n"
      "s_mov_b32 s22, s23\n"
      ".Lsl_w0_1:\n"
      "buffer_load_dword v28, v11, s[8:11], s46 offen sc1\n"
      "s_waitcnt vmcnt(0)\n"
      "v_cmp_ne_u32 vcc, s21, v28\n"
      "s_cbranch_vccz .Lbk_w0_1\n"
      "s_sleep 4\n"
      "s_sub_u32 s22, s22, 1\n"
      "s_cbranch_scc0 .Lsl_w0_1\n"
      "s_mov_b32 s23, 0\n"
      "s_branch .Lbk_w0_1\n"
      ".Lsp_w1_2:\n"
      "s_mov_b32 s22, s23\n"
      ".Lsl_w1_2:\n"
      "buffer_load_dword v28, v11, s[8:11], s46 offen sc1\n"
      "s_waitcnt vmcnt(0)\n"
      "v_cmp_ne_u32 vcc, s21, v28\n"
      "s_cbranch_vccz .Lbk_w1_2\n"
      "s_sleep 4\n"
      "s_sub_u32 s22, s22, 1\n"
      "s_cbranch_scc0 .Lsl_w1_2\n"
      "s_mov_b32 s23, 0\n"
      "s_branch .Lbk_w1_2\n"
      ".Lsp_wy0_3:\n"
      "s_lshl_b32 s22, s23, 4\n"
      ".Lsl_wy0_3:\n"
      "ds_read_b32 v22, v24\n"
      "s_waitcnt lgkmcnt(0)\n"
      "v_cmp_gt_i32 vcc, v21, v22\n"
      "s_cbranch_vccz .Lbk_wy0_3\n"
      "s_sub_u32 s22, s22, 1\n"
      "s_cbranch_scc0 .Lsl_wy0_3\n"
      "s_mov_b32 s23, 0\n"
      "s_branch .Lbk_wy0_3\n"
      ".Lsp_w0_4:\n"
      "s_mov_b32 s22, s23\n"
      "buffer_load_dword v28, v11, s[8:11], s46 offen sc1\n"
      "s_waitcnt vmcnt(0)\n"
      "v_cmp_ne_u32 vcc, s21, v28\n"
      "s_add_u32 s30, s50, 0\n"
      "buffer_store_dword v25, v12, s[8:11], s30 offen sc1\n"
      "s_add_u32 s30, s50, 24576\n"
      "buffer_store_dword v25, v12, s[8:11], s30 offen sc1\n"
      "s_cbranch_vccz .Lbk_w0_4\n"
      ".Lsl_w0_4:\n"
      "buffer_load_dword v28, v11, s[8:11], s46 offen sc1\n"
      "s_waitcnt vmcnt(0)\n"
      "v_cmp_ne_u32 vcc, s21, v28\n"
      "s_cbranch_vccz .Lbk_w0_4\n"
      "s_sleep 4\n"
      "s_sub_u32 s22, s22, 1\n"
      "s_cbranch_scc0 .Lsl_w0_4\n"
      "s_mov_b32 s23, 0\n"
      "s_branch .Lbk_w0_4\n"
      ".Lsp_wz0_5:\n"
      "s_lshl_b32 s22, s23, 4\n"
      ".Lsl_wz0_5:\n"
      "ds_read_b32 v22, v24\n"
      "s_waitcnt lgkmcnt(0)\n"
      "v_cmp_gt_i32 vcc, v21, v22\n"
      "s_cbranch_vccz .Lbk_wz0_5\n"
      "s_sub_u32 s22, s22, 1\n"
      "s_cbranch_scc0 .Lsl_wz0_5\n"
      "s_mov_b32 s23, 0\n"
      "s_branch .Lbk_wz0_5\n"
      ".Lsp_wy1_6:\n"
      "s_lshl_b32 s22, s23, 4\n"
      ".Lsl_wy1_6:\n"
      "ds_read_b32 v22, v24\n"
      "s_waitcnt lgkmcnt(0)\n"
      "v_cmp_gt_i32 vcc, v21, v22\n"
      "s_cbranch_vccz .Lbk_wy1_6\n"
      "s_sub_u32 s22, s22, 1\n"
      "s_cbranch_scc0 .Lsl_wy1_6\n"
      "s_mov_b32 s23, 0\n"
      "s_branch .Lbk_wy1_6\n"
      ".Lsp_w1_7:\n"
      "s_mov_b32 s22, s23\n"
      "buffer_load_dword v28, v11, s[8:11], s46 offen sc1\n"
      "s_waitcnt vmcnt(0)\n"
      "v_cmp_ne_u32 vcc, s21, v28\n"
      "s_add_u32 s30, s50, 24576\n"
      "buffer_store_dword v25, v12, s[8:11], s30 offen sc1\n"
      "s_cbranch_vccz .Lbk_w1_7\n"
      ".Lsl_w1_7:\n"
      "buffer_load_dword v28, v11, s[8:11], s46 offen sc1\n"
      "s_waitcnt vmcnt(0)\n"
      "v_cmp_ne_u32 vcc, s21, v28\n"
      "s_cbranch_vccz .Lbk_w1_7\n"
      "s_sleep 4\n"
      "s_sub_u32 s22, s22, 1\n"
      "s_cbranch_scc0 .Lsl_w1_7\n"
      "s_mov_b32 s23, 0\n"
      "s_branch .Lbk_w1_7\n"
      ".Lsp_wz1_8:\n"
      "s_lshl_b32 s22, s23, 4\n"
      ".Lsl_wz1_8:\n"
      "ds_read_b32 v22, v24\n"
      "s_waitcnt lgkmcnt(0)\n"
      "v_cmp_gt_i32 vcc, v21, v22\n"
      "s_cbranch_vccz .Lbk_wz1_8\n"
      "s_sub_u32 s22, s22, 1\n"
      "s_cbranch_scc0 .Lsl_wz1_8\n"
      "s_mov_b32 s23, 0\n"
      "s_branch .Lbk_wz1_8\n"
      ".Lsp_wy0_9:\n"
      "s_lshl_b32 s22, s23, 4\n"
      ".Lsl_wy0_9:\n"
      "ds_read_b32 v22, v24\n"
      "s_waitcnt lgkmcnt(0)\n"
      "v_cmp_gt_i32 vcc, v21, v22\n"
      "s_cbranch_vccz .Lbk_wy0_9\n"
      "s_sub_u32 s22, s22, 1\n"
      "s_cbranch_scc0 .Lsl_wy0_9\n"
      "s_mov_b32 s23, 0\n"
      "s_branch .Lbk_wy0_9\n"
      ".Lsp_w0_10:\n"
      "s_mov_b32 s22, s23\n"
      "buffer_load_dword v28, v11, s[8:11], s46 offen sc1\n"
      "s_waitcnt vmcnt(0)\n"
      "v_cmp_ne_u32 vcc, s21, v28\n"
      "s_add_u32 s30, s50, 0\n"
      "buffer_store_dword v25, v12, s[8:11], s30 offen sc1\n"
      "s_add_u32 s30, s50, 24576\n"
      "buffer_store_dword v25, v12, s[8:11], s30 offen sc1\n"
      "s_cbranch_vccz .Lbk_w0_10\n"
      ".Lsl_w0_10:\n"
      "buffer_load_dword v28, v11, s[8:11], s46 offen sc1\n"
      "s_waitcnt vmcnt(0)\n"
      "v_cmp_ne_u32 vcc, s21, v28\n"
      "s_cbranch_vccz .Lbk_w0_10\n"
      "s_sleep 4\n"
      "s_sub_u32 s22, s22, 1\n"
      "s_cbranch_scc0 .Lsl_w0_10\n"
      "s_mov_b32 s23, 0\n"
      "s_branch .Lbk_w0_10\n"
      ".Lsp_wz0_11:\n"
      "s_lshl_b32 s22, s23, 4\n"
      ".Lsl_wz0_11:\n"
      "ds_read_b32 v22, v24\n"
      "s_waitcnt lgkmcnt(0)\n"
      "v_cmp_gt_i32 vcc, v21, v22\n"
      "s_cbranch_vccz .Lbk_wz0_11\n"
      "s_sub_u32 s22, s22, 1\n"
      "s_cbranch_scc0 .Lsl_wz0_11\n"
      "s_mov_b32 s23, 0\n"
      "s_branch .Lbk_wz0_11\n"
      ".Lsp_wy1_12:\n"
      "s_lshl_b32 s22, s23, 4\n"
      ".Lsl_wy1_12:\n"
      "ds_read_b32 v22, v24\n"
      "s_waitcnt lgkmcnt(0)\n"
      "v_cmp_gt_i32 vcc, v21, v22\n"
      "s_cbranch_vccz .Lbk_wy1_12\n"
      "s_sub_u32 s22, s22, 1\n"
      "s_cbranch_scc0 .Lsl_wy1_12\n"
      "s_mov_b32 s23, 0\n"
      "s_branch .Lbk_wy1_12\n"
      ".Lsp_w1_13:\n"
      "s_mov_b32 s22, s23\n"
      "buffer_load_dword v28, v11, s[8:11], s46 offen sc1\n"
      "s_waitcnt vmcnt(0)\n"
      "v_cmp_ne_u32 vcc, s21, v28\n"
      "s_add_u32 s30, s50, 24576\n"
      "buffer_store_dword v25, v12, s[8:11], s30 offen sc1\n"
      "s_cbranch_vccz .Lbk_w1_13\n"
      ".Lsl_w1_13:\n"
      "buffer_load_dword v28, v11, s[8:11], s46 offen sc1\n"
      "s_waitcnt vmcnt(0)\n"
      "v_cmp_ne_u32 vcc, s21, v28\n"
      "s_cbranch_vccz .Lbk_w1_13\n"
      "s_sleep 4\n"
      "s_sub_u32 s22, s22, 1\n"
      "s_cbranch_scc0 .Lsl_w1_13\n"
      "s_mov_b32 s23, 0\n"
      "s_branch .Lbk_w1_13\n"
      ".Lsp_wz1_14:\n"
      "s_lshl_b32 s22, s23, 4\n"
      ".Lsl_wz1_14:\n"
      "ds_read_b32 v22, v24\n"
      "s_waitcnt lgkmcnt(0)\n"
      "v_cmp_gt_i32 vcc, v21, v22\n"
      "s_cbranch_vccz .Lbk_wz1_14\n"
      "s_sub_u32 s22, s22, 1\n"
      "s_cbranch_scc0 .Lsl_wz1_14\n"
      "s_mov_b32 s23, 0\n"
      "s_branch .Lbk_wz1_14\n"
      ".Lsp_wy0_15:\n"
      "s_lshl_b32 s22, s23, 4\n"
      ".Lsl_wy0_15:\n"
      "ds_read_b32 v22, v24\n"
      "s_waitcnt lgkmcnt(0)\n"
      "v_cmp_gt_i32 vcc, v21, v22\n"
      "s_cbranch_vccz .Lbk_wy0_15\n"
      "s_sub_u32 s22, s22, 1\n"
      "s_cbranch_scc0 .Lsl_wy0_15\n"
      "s_mov_b32 s23, 0\n"
      "s_branch .Lbk_wy0_15\n"
      ".Lsp_v0_16:\n"
      "s_mov_b32 s22, s23\n"
      ".Lsl_v0_16:\n"
      "buffer_load_dword v28, v11, s[8:11], s46 offen sc1\n"
      "s_waitcnt vmcnt(0)\n"
      "v_cmp_ne_u32 vcc, s21, v28\n"
      "s_cbranch_vccz .Lbk_v0_16\n"
      "s_sleep 4\n"
      "s_sub_u32 s22, s22, 1\n"
      "s_cbranch_scc0 .Lsl_v0_16\n"
      "s_mov_b32 s23, 0\n"
      "s_branch .Lbk_v0_16\n"
      ".Lsp_v1_17:\n"
      "s_mov_b32 s22, s23\n"
      ".Lsl_v1_17:\n"
      "buffer_load_dword v28, v11, s[8:11], s46 offen sc1\n"
      "s_waitcnt vmcnt(0)\n"
      "v_cmp_ne_u32 vcc, s21, v28\n"
      "s_cbranch_vccz .Lbk_v1_17\n"
      "s_sleep 4\n"
      "s_sub_u32 s22, s22, 1\n"
      "s_cbranch_scc0 .Lsl_v1_17\n"
      "s_mov_b32 s23, 0\n"
      "s_branch .Lbk_v1_17\n"
      ".Lsp_vy0_18:\n"
      "s_lshl_b32 s22, s23, 4\n"
      ".Lsl_vy0_18:\n"
      "ds_read_b32 v22, v24\n"
      "s_waitcnt lgkmcnt(0)\n"
      "v_cmp_gt_i32 vcc, v21, v22\n"
      "s_cbranch_vccz .Lbk_vy0_18\n"
      "s_sub_u32 s22, s22, 1\n"
      "s_cbranch_scc0 .Lsl_vy0_18\n"
      "s_mov_b32 s23, 0\n"
      "s_branch .Lbk_vy0_18\n"
      ".Lsp_v0_19:\n"
      "s_mov_b32 s22, s23\n"
      "buffer_load_dword v28, v11, s[8:11], s46 offen sc1\n"
      "s_waitcnt vmcnt(0)\n"
      "v_cmp_ne_u32 vcc, s21, v28\n"
      "s_add_u32 s30, s50, 0\n"
      "buffer_store_dword v25, v12, s[8:11], s30 offen sc1\n"
      "s_cbranch_vccz .Lbk_v0_19\n"
      ".Lsl_v0_19:\n"
      "buffer_load_dword v28, v11, s[8:11], s46 offen sc1\n"
      "s_waitcnt vmcnt(0)\n"
      "v_cmp_ne_u32 vcc, s21, v28\n"
      "s_cbranch_vccz .Lbk_v0_19\n"
      "s_sleep 4\n"
      "s_sub_u32 s22, s22, 1\n"
      "s_cbranch_scc0 .Lsl_v0_19\n"
      "s_mov_b32 s23, 0\n"
      "s_branch .Lbk_v0_19\n"
      ".Lsp_vz0_20:\n"
      "s_lshl_b32 s22, s23, 4\n"
      ".Lsl_vz0_20:\n"
      "ds_read_b32 v22, v24\n"
      "s_waitcnt lgkmcnt(0)\n"
      "v_cmp_gt_i32 vcc, v21, v22\n"
      "s_cbranch_vccz .Lbk_vz0_20\n"
      "s_sub_u32 s22, s22, 1\n"
      "s_cbranch_scc0 .Lsl_vz0_20\n"
      "s_mov_b32 s23, 0\n"
      "s_branch .Lbk_vz0_20\n"
      ".Lsp_vy1_21:\n"
      "s_lshl_b32 s22, s23, 4\n"
      ".Lsl_vy1_21:\n"
      "ds_read_b32 v22, v24\n"
      "s_waitcnt lgkmcnt(0)\n"
      "v_cmp_gt_i32 vcc, v21, v22\n"
      "s_cbranch_vccz .Lbk_vy1_21\n"
      "s_sub_u32 s22, s22, 1\n"
      "s_cbranch_scc0 .Lsl_vy1_21\n"
      "s_mov_b32 s23, 0\n"
      "s_branch .Lbk_vy1_21\n"
      ".Lsp_v1_22:\n"
      "s_mov_b32 s22, s23\n"
      "buffer_load_dword v28, v11, s[8:11], s46 offen sc1\n"
      "s_waitcnt vmcnt(0)\n"
      "v_cmp_ne_u32 vcc, s21, v28\n"
      "s_add_u32 s30, s50, 24576\n"
      "buffer_store_dword v25, v12, s[8:11], s30 offen sc1\n"
      "s_cbranch_vccz .Lbk_v1_22\n"
      ".Lsl_v1_22:\n"
      "buffer_load_dword v28, v11, s[8:11], s46 offen sc1\n"
      "s_waitcnt vmcnt(0)\n"
      "v_cmp_ne_u32 vcc, s21, v28\n"
      "s_cbranch_vccz .Lbk_v1_22\n"
      "s_sleep 4\n"
      "s_sub_u32 s22, s22, 1\n"
      "s_cbranch_scc0 .Lsl_v1_22\n"
      "s_mov_b32 s23, 0\n"
      "s_branch .Lbk_v1_22\n"
      ".Lsp_vz1_23:\n"
      "s_lshl_b32 s22, s23, 4\n"
      ".Lsl_vz1_23:\n"
      "ds_read_b32 v22, v24\n"
      "s_waitcnt lgkmcnt(0)\n"
      "v_cmp_gt_i32 vcc, v21, v22\n"
      "s_cbranch_vccz .Lbk_vz1_23\n"
      "s_sub_u32 s22, s22, 1\n"
      "s_cbranch_scc0 .Lsl_vz1_23\n"
      "s_mov_b32 s23, 0\n"
      "s_branch .Lbk_vz1_23\n"
      ".Lsp_vy0_24:\n"
      "s_lshl_b32 s22, s23, 4\n"
      ".Lsl_vy0_24:\n"
      "ds_read_b32 v22, v24\n"
      "s_waitcnt lgkmcnt(0)\n"
      "v_cmp_gt_i32 vcc, v21, v22\n"
      "s_cbranch_vccz .Lbk_vy0_24\n"
      "s_sub_u32 s22, s22, 1\n"
      "s_cbranch_scc0 .Lsl_vy0_24\n"
      "s_mov_b32 s23, 0\n"
      "s_branch .Lbk_vy0_24\n"
      ".Lsp_v0_25:\n"
      "s_mov_b32 s22, s23\n"
      "buffer_load_dword v28, v11, s[8:11], s46 offen sc1\n"
      "s_waitcnt vmcnt(0)\n"
      "v_cmp_ne_u32 vcc, s21, v28\n"
      "s_add_u32 s30, s50, 0\n"
      "buffer_store_dword v25, v12, s[8:11], s30 offen sc1\n"
      "s_cbranch_vccz .Lbk_v0_25\n"
      ".Lsl_v0_25:\n"
      "buffer_load_dword v28, v11, s[8:11], s46 offen sc1\n"
      "s_waitcnt vmcnt(0)\n"
      "v_cmp_ne_u32 vcc, s21, v28\n"
      "s_cbranch_vccz .Lbk_v0_25\n"
      "s_sleep 4\n"
      "s_sub_u32 s22, s22, 1\n"
      "s_cbranch_scc0 .Lsl_v0_25\n"
      "s_mov_b32 s23, 0\n"
      "s_branch .Lbk_v0_25\n"
      ".Lsp_vz0_26:\n"
      "s_lshl_b32 s22, s23, 4\n"
      ".Lsl_vz0_26:\n"
      "ds_read_b32 v22, v24\n"
      "s_waitcnt lgkmcnt(0)\n"
      "v_cmp_gt_i32 vcc, v21, v22\n"
      "s_cbranch_vccz .Lbk_vz0_26\n"
      "s_sub_u32 s22, s22, 1\n"
      "s_cbranch_scc0 .Lsl_vz0_26\n"
      "s_mov_b32 s23, 0\n"
      "s_branch .Lbk_vz0_26\n"
      ".Lsp_vy1_27:\n"
      "s_lshl_b32 s22, s23, 4\n"
      ".Lsl_vy1_27:\n"
      "ds_read_b32 v22, v24\n"
      "s_waitcnt lgkmcnt(0)\n"
      "v_cmp_gt_i32 vcc, v21, v22\n"
      "s_cbranch_vccz .Lbk_vy1_27\n"
      "s_sub_u32 s22, s22, 1\n"
      "s_cbranch_scc0 .Lsl_vy1_27\n"
      "s_mov_b32 s23, 0\n"
      "s_branch .Lbk_vy1_27\n"
      ".Lsp_v1_28:\n"
      "s_mov_b32 s22, s23\n"
      "buffer_load_dword v28, v11, s[8:11], s46 offen sc1\n"
      "s_waitcnt vmcnt(0)\n"
      "v_cmp_ne_u32 vcc, s21, v28\n"
      "s_add_u32 s30, s50, 24576\n"
      "buffer_store_dword v25, v12, s[8:11], s30 offen sc1\n"
      "s_cbranch_vccz .Lbk_v1_28\n"
      ".Lsl_v1_28:\n"
      "buffer_load_dword v28, v11, s[8:11], s46 offen sc1\n"
      "s_waitcnt vmcnt(0)\n"
      "v_cmp_ne_u32 vcc, s21, v28\n"
      "s_cbranch_vccz .Lbk_v1_28\n"
      "s_sleep 4\n"
      "s_sub_u32 s22, s22, 1\n"
      "s_cbranch_scc0 .Lsl_v1_28\n"
      "s_mov_b32 s23, 0\n"
      "s_branch .Lbk_v1_28\n"
      ".Lsp_vz1_29:\n"
      "s_lshl_b32 s22, s23, 4\n"
      ".Lsl_vz1_29:\n"
      "ds_read_b32 v22, v24\n"
      "s_waitcnt lgkmcnt(0)\n"
      "v_cmp_gt_i32 vcc, v21, v22\n"
      "s_cbranch_vccz .Lbk_vz1_29\n"
      "s_sub_u32 s22, s22, 1\n"
      "s_cbranch_scc0 .Lsl_vz1_29\n"
      "s_mov_b32 s23, 0\n"
      "s_branch .Lbk_vz1_29\n"
      ".Lsp_vy0_30:\n"
      "s_lshl_b32 s22, s23, 4\n"
      ".Lsl_vy0_30:\n"
      "ds_read_b32 v22, v24\n"
      "s_waitcnt lgkmcnt(0)\n"
      "v_cmp_gt_i32 vcc, v21, v22\n"
      "s_cbranch_vccz .Lbk_vy0_30\n"
      "s_sub_u32 s22, s22, 1\n"
      "s_cbranch_scc0 .Lsl_vy0_30\n"
      "s_mov_b32 s23, 0\n"
      "s_branch .Lbk_vy0_30\n"
      ".Lw_last:\n"
      :
      : "{s[8:11]}"(rsWS), "{s[12:15]}"(rsA), "{v0}"(v_l16), "{v1}"(v_l5), "{v2}"(v_ex[0]), "{v3}"(v_ex[1]), "{v4}"(v_ex[2]), "{v5}"(v_ex[3]), "{v6}"(v_ex[4]), "{v7}"(v_ex[5]), "{v8}"(v_ex[6]), "{v9}"(v_ex[7]), "{v10}"(v_l2), "{v11}"(v_fa), "{v12}"(v_ra), "{v13}"(v_rp), "{v14}"(v_rs), "{v15}"(v_xp), "{v16}"(v_xs), "{v17}"(v_ew), "{v18}"(v_sw), "{v19}"(v_sr), "{v20}"(v_ss), "{v22}"(v_al), "{v23}"(v_yw), "{v24}"(v_yr), "{v26}"(v_hi), "{v100}"(v_vo[0]), "{v101}"(v_vo[1]), "{v102}"(v_vo[2]), "{v103}"(v_vo[3]), "{v104}"(v_vo[4]), "{v105}"(v_vo[5]), "{v106}"(v_vo[6]), "{v107}"(v_vo[7]), "{v108}"(v_vo[8]), "{v109}"(v_vp[0]), "{v110}"(v_vp[1]), "{v111}"(v_vp[2]), "{v112}"(v_vp[3]), "{v113}"(v_vp[4]), "{v114}"(v_vp[5]), "{v115}"(v_vp[6]), "{v116}"(v_vp[7]), "{v117}"(v_vp[8]), "{v118}"(v_t0), "{v119}"(v_t1), "{v120}"(v_ts), "{v121}"(v_pw), "{s36}"(s_sx), "{s37}"(s_ev), "{s38}"(s_sy), "{s39}"(s_m4), "{s40}"(s_m5), "{s41}"(s_m9), "{s42}"(s_so), "{s43}"(s_s8), "{s44}"(s_wg), "{s45}"(s_rb)
      : "v255", "a255", "s99", "vcc", "scc", "memory");

  } else if (role == 2) {
    const unsigned ks = part / 3, n3 = part % 3;
    const u32x4 rsA = srd(Wd, 4608 * 576 * 2), rsB = srd(out, 2048 * 576 * 2), rsC = srd(X, 2048 * 576 * 2);
    const unsigned v_l16b = v_l16 + 4096, v_l8 = 1024 + lane * 8, f0 = 24 * ks + 6 * w;
    const unsigned v_fa = 147456 - f0 * 1024 + (f0 + (l8 < 5 ? l8 : 5)) * 128 + l7 / 4, v_ga = (l8 < 4 ? l8 : 4) * 4 * PW + 1536 + l7 / 4;
    const unsigned v_ra = up * 12 * PREC + l5 + 1536, v_rf = up * 33 * REC + l5 + 20480 + (4 * n3 + w) * 128;
    const unsigned v_pw = l8 < 5 ? (l8 + 7) * PREC + l8 * 4 * PW + 1536 + l7 : OOB, v_pu = ks && l8 + ks < 5 ? OOB : v_pw - 6 * PREC;
    const unsigned pf = 3 * (w >> 1) + 2 * (w & 1), sf = 3 * (w >> 1) + 1;
    const unsigned v_at = (8 * g + q) * 384 + 64 * pf + 16 * p, v_as = (8 * g + q) * 384 + 64 * sf + 32 * (w & 1) + 8 * p;
    auto vo = [&](unsigned i) { const unsigned c = 64 * i + lane; return (8 * w + c / 24) * 1152 + 384 * n3 + c % 24 * 16 - 1024 * i; };
    const unsigned v_vo0 = vo(0), v_vo1 = vo(1), v_vo2 = vo(2);
    const unsigned v_xp = (6 * n3 + pf) * 1024 + v_l16, v_xs = (6 * n3 + sf) * 1024 + (w & 1) * 512 + half;
    const unsigned v_op = i16 * 1152 + 384 * n3 + 64 * pf + 16 * g, v_os = i16 * 1152 + 384 * n3 + 64 * sf + 32 * (w & 1) + 8 * g;
    const unsigned v_sw = 147456 + i16 * 136 + (4 * w + g) * 8, v_sr = 147456 + (4 * w + g) * 136 + i16 * 8;
    const unsigned v_ss = 18432 + n3 * 512 + w * 128 + g * 32 + p * 8;
    const unsigned s_ph = ks ? 6 - ks : 0, s_p0 = 0u - s_ph, r0 = gr * NREC + s_p0;
    const unsigned s_sh = WS_H + r0 * HREC + f0 * 1024, s_ps = WS_PS + r0 * PREC + (n3 * 20 + w) * PW;
    const unsigned s_x1 = WS_X1 + r0 * REC, s_xr = WS_XIN + (r0 + 33) * REC, s_or = (32 * gr + 63 - s_p0) * 18432;
    const unsigned s_k0 = ROT * 6 * gr;
    const unsigned s_m3 = w * 3072, s_m6 = w * 6144, s_so = ks * 768 * 1152;
    const unsigned s_i0 = (32 * gr + 31 - ks) * 18432, s_i5 = ks < 2 ? s_i0 - 30 * 18432 : OOB;
    const unsigned s_j0 = WS_XIN + (gr * NREC + ks) * REC, s_j5 = ks < 2 ? s_j0 + 30 * REC : OOB, s_he = s_sh + (63 + s_ph) * HREC;
    asm volatile(
      "s_buffer_load_dword s21, s[8:11], 0\n"
      "s_mov_b32 s23, 65536\n"
      "s_mov_b32 s31, 0x40000000\n"
      "buffer_load_dwordx4 v[56:59], v8, s[92:95], s46 offen\n"
      "buffer_load_dwordx2 v[60:61], v9, s[92:95], s46 offen\n"
      "s_waitcnt lgkmcnt(0)\n"
      "s_mul_i32 s21, s21, 1664525\n"
      "s_add_u32 s21, s21, 1013904223\n"
      "v_mov_b32 v18, s21\n"
      "v_mov_b32 v21, v18\n"
      "v_not_b32 v224, v18\n"
      "v_not_b32 v225, v18\n"
      "v_not_b32 v226, v18\n"
      "v_not_b32 v227, v18\n"
      "v_xor_b32 v18, v18, v19\n"
      "s_waitcnt vmcnt(0)\n"
      "buffer_store_dwordx4 v[56:59], v6, s[8:11], s48 offen sc1\n"
      "buffer_store_dwordx2 v[60:61], v7, s[8:11], s48 offen sc1\n"
      "v_lshlrev_b32 v164, 16, v56\n"
      "v_and_b32 v165, 0xffff0000, v56\n"
      "v_lshlrev_b32 v166, 16, v57\n"
      "v_and_b32 v167, 0xffff0000, v57\n"
      "v_lshlrev_b32 v168, 16, v58\n"
      "v_and_b32 v169, 0xffff0000, v58\n"
      "v_lshlrev_b32 v170, 16, v59\n"
      "v_and_b32 v171, 0xffff0000, v59\n"
      "v_lshlrev_b32 v172, 16, v60\n"
      "v_and_b32 v173, 0xffff0000, v60\n"
      "v_lshlrev_b32 v174, 16, v61\n"
      "v_and_b32 v175, 0xffff0000, v61\n"
      "v_add_f32 v62, v164, v165\n"
      "v_mul_f32 v63, v164, v164\n"
      "v_fmac_f32 v63, v165, v165\n"
      "v_add_f32 v62, v62, v166\n"
      "v_fmac_f32 v63, v166, v166\n"
      "v_add_f32 v62, v62, v167\n"
      "v_fmac_f32 v63, v167, v167\n"
      "v_add_f32 v62, v62, v168\n"
      "v_fmac_f32 v63, v168, v168\n"
      "v_add_f32 v62, v62, v169\n"
      "v_fmac_f32 v63, v169, v169\n"
      "v_add_f32 v62, v62, v170\n"
      "v_fmac_f32 v63, v170, v170\n"
      "v_add_f32 v62, v62, v171\n"
      "v_fmac_f32 v63, v171, v171\n"
      "v_add_f32 v62, v62, v172\n"
      "v_fmac_f32 v63, v172, v172\n"
      "v_add_f32 v62, v62, v173\n"
      "v_fmac_f32 v63, v173, v173\n"
      "v_add_f32 v62, v62, v174\n"
      "v_fmac_f32 v63, v174, v174\n"
      "v_add_f32 v62, v62, v175\n"
      "v_fmac_f32 v63, v175, v175\n"
      "ds_write_b64 v10, v[62:63] offset:0\n"
      "s_waitcnt lgkmcnt(0)\n"
      "s_barrier\n"
      "ds_read_b64 v[152:153], v11 offset:0\n"
      "s_waitcnt lgkmcnt(0)\n"
      "v_add_f32_dpp v152, v152, v152 row_ror:8 row_mask:0xf bank_mask:0xf\n"
      "v_add_f32_dpp v153, v153, v153 row_ror:8 row_mask:0xf bank_mask:0xf\n"
      "s_nop 0\n"
      "v_add_f32_dpp v152, v152, v152 row_ror:4 row_mask:0xf bank_mask:0xf\n"
      "v_add_f32_dpp v153, v153, v153 row_ror:4 row_mask:0xf bank_mask:0xf\n"
      "s_nop 0\n"
      "v_add_f32_dpp v152, v152, v152 row_ror:2 row_mask:0xf bank_mask:0xf\n"
      "v_add_f32_dpp v153, v153, v153 row_ror:2 row_mask:0xf bank_mask:0xf\n"
      "s_nop 0\n"
      "v_add_f32_dpp v152, v152, v152 row_ror:1 row_mask:0xf bank_mask:0xf\n"
      "v_add_f32_dpp v153, v153, v153 row_ror:1 row_mask:0xf bank_mask:0xf\n"
      "v_mul_f32 v152, 0x3ae38e39, v152\n"
      "v_add_f32 v153, 0x3afba882, v153\n"
      "v_mul_f32 v153, 0x3ae38e39, v153\n"
      "buffer_store_dwordx2 v[152:153], v12, s[8:11], s48 offen sc1\n"
      "s_add_u32 s30, s46, 4294856704\n"
      "buffer_load_dwordx4 v[64:67], v8, s[92:95], s30 offen\n"
      "buffer_load_dwordx2 v[68:69], v9, s[92:95], s30 offen\n"
      "s_add_u32 s30, s46, 4294746112\n"
      "buffer_load_dwordx4 v[72:75], v8, s[92:95], s30 offen\n"
      "buffer_load_dwordx2 v[76:77], v9, s[92:95], s30 offen\n"
      "s_waitcnt vmcnt(4)\n"
      "buffer_store_dword v18, v5, s[8:11], s48 offen sc1\n"
      "s_add_u32 s30, s46, 4294635520\n"
      "buffer_load_dwordx4 v[80:83], v8, s[92:95], s30 offen\n"
      "buffer_load_dwordx2 v[84:85], v9, s[92:95], s30 offen\n"
      "s_add_u32 s30, s46, 4294524928\n"
      "buffer_load_dwordx4 v[88:91], v8, s[92:95], s30 offen\n"
      "buffer_load_dwordx2 v[92:93], v9, s[92:95], s30 offen\n"
      "buffer_load_dwordx4 v[96:99], v8, s[92:95], s47 offen\n"
      "buffer_load_dwordx2 v[100:101], v9, s[92:95], s47 offen\n"
      "buffer_store_dword v18, v15, s[8:11], 0 offen sc1\n"
      "s_cmp_eq_u32 s45, 0\n"
      "s_cselect_b32 s30, 0x40000000, s37\n"
      "buffer_store_dword v21, v3, s[8:11], s30 offen sc1\n"
      "s_waitcnt vmcnt(11)\n"
      "s_add_u32 s30, s48, 147456\n"
      "buffer_store_dwordx4 v[64:67], v6, s[8:11], s30 offen sc1\n"
      "buffer_store_dwordx2 v[68:69], v7, s[8:11], s30 offen sc1\n"
      "v_lshlrev_b32 v164, 16, v64\n"
      "v_and_b32 v165, 0xffff0000, v64\n"
      "v_lshlrev_b32 v166, 16, v65\n"
      "v_and_b32 v167, 0xffff0000, v65\n"
      "v_lshlrev_b32 v168, 16, v66\n"
      "v_and_b32 v169, 0xffff0000, v66\n"
      "v_lshlrev_b32 v170, 16, v67\n"
      "v_and_b32 v171, 0xffff0000, v67\n"
      "v_lshlrev_b32 v172, 16, v68\n"
      "v_and_b32 v173, 0xffff0000, v68\n"
      "v_lshlrev_b32 v174, 16, v69\n"
      "v_and_b32 v175, 0xffff0000, v69\n"
      "v_add_f32 v70, v164, v165\n"
      "v_mul_f32 v71, v164, v164\n"
      "v_fmac_f32 v71, v165, v165\n"
      "v_add_f32 v70, v70, v166\n"
      "v_fmac_f32 v71, v166, v166\n"
      "v_add_f32 v70, v70, v167\n"
      "v_fmac_f32 v71, v167, v167\n"
      "v_add_f32 v70, v70, v168\n"
      "v_fmac_f32 v71, v168, v168\n"
      "v_add_f32 v70, v70, v169\n"
      "v_fmac_f32 v71, v169, v169\n"
      "v_add_f32 v70, v70, v170\n"
      "v_fmac_f32 v71, v170, v170\n"
      "v_add_f32 v70, v70, v171\n"
      "v_fmac_f32 v71, v171, v171\n"
      "v_add_f32 v70, v70, v172\n"
      "v_fmac_f32 v71, v172, v172\n"
      "v_add_f32 v70, v70, v173\n"
      "v_fmac_f32 v71, v173, v173\n"
      "v_add_f32 v70, v70, v174\n"
      "v_fmac_f32 v71, v174, v174\n"
      "v_add_f32 v70, v70, v175\n"
      "v_fmac_f32 v71, v175, v175\n"
      "ds_write_b64 v10, v[70:71] offset:2176\n"
      "s_waitcnt vmcnt(11)\n"
      "s_add_u32 s30, s48, 294912\n"
      "buffer_store_dwordx4 v[72:75], v6, s[8:11], s30 offen sc1\n"
      "buffer_store_dwordx2 v[76:77], v7, s[8:11], s30 offen sc1\n"
      "v_lshlrev_b32 v164, 16, v72\n"
      "v_and_b32 v165, 0xffff0000, v72\n"
      "v_lshlrev_b32 v166, 16, v73\n"
      "v_and_b32 v167, 0xffff0000, v73\n"
      "v_lshlrev_b32 v168, 16, v74\n"
      "v_and_b32 v169, 0xffff0000, v74\n"
      "v_lshlrev_b32 v170, 16, v75\n"
      "v_and_b32 v171, 0xffff0000, v75\n"
      "v_lshlrev_b32 v172, 16, v76\n"
      "v_and_b32 v173, 0xffff0000, v76\n"
      "v_lshlrev_b32 v174, 16, v77\n"
      "v_and_b32 v175, 0xffff0000, v77\n"
      "v_add_f32 v78, v164, v165\n"
      "v_mul_f32 v79, v164, v164\n"
      "v_fmac_f32 v79, v165, v165\n"
      "v_add_f32 v78, v78, v166\n"
      "v_fmac_f32 v79, v166, v166\n"
      "v_add_f32 v78, v78, v167\n"
      "v_fmac_f32 v79, v167, v167\n"
      "v_add_f32 v78, v78, v168\n"
      "v_fmac_f32 v79, v168, v168\n"
      "v_add_f32 v78, v78, v169\n"
      "v_fmac_f32 v79, v169, v169\n"
      "v_add_f32 v78, v78, v170\n"
      "v_fmac_f32 v79, v170, v170\n"
      "v_add_f32 v78, v78, v171\n"
      "v_fmac_f32 v79, v171, v171\n"
      "v_add_f32 v78, v78, v172\n"
      "v_fmac_f32 v79, v172, v172\n"
      "v_add_f32 v78, v78, v173\n"
      "v_fmac_f32 v79, v173, v173\n"
      "v_add_f32 v78, v78, v174\n"
      "v_fmac_f32 v79, v174, v174\n"
      "v_add_f32 v78, v78, v175\n"
      "v_fmac_f32 v79, v175, v175\n"
      "ds_write_b64 v10, v[78:79] offset:4352\n"
      "s_waitcnt vmcnt(10)\n"
      "s_add_u32 s30, s48, 442368\n"
      "buffer_store_dwordx4 v[80:83], v6, s[8:11], s30 offen sc1\n"
      "buffer_store_dwordx2 v[84:85], v7, s[8:11], s30 offen sc1\n"
      "v_lshlrev_b32 v164, 16, v80\n"
      "v_and_b32 v165, 0xffff0000, v80\n"
      "v_lshlrev_b32 v166, 16, v81\n"
      "v_and_b32 v167, 0xffff0000, v81\n"
      "v_lshlrev_b32 v168, 16, v82\n"
      "v_and_b32 v169, 0xffff0000, v82\n"
      "v_lshlrev_b32 v170, 16, v83\n"
      "v_and_b32 v171, 0xffff0000, v83\n"
      "v_lshlrev_b32 v172, 16, v84\n"
      "v_and_b32 v173, 0xffff0000, v84\n"
      "v_lshlrev_b32 v174, 16, v85\n"
      "v_and_b32 v175, 0xffff0000, v85\n"
      "v_add_f32 v86, v164, v165\n"
      "v_mul_f32 v87, v164, v164\n"
      "v_fmac_f32 v87, v165, v165\n"
      "v_add_f32 v86, v86, v166\n"
      "v_fmac_f32 v87, v166, v166\n"
      "v_add_f32 v86, v86, v167\n"
      "v_fmac_f32 v87, v167, v167\n"
      "v_add_f32 v86, v86, v168\n"
      "v_fmac_f32 v87, v168, v168\n"
      "v_add_f32 v86, v86, v169\n"
      "v_fmac_f32 v87, v169, v169\n"
      "v_add_f32 v86, v86, v170\n"
      "v_fmac_f32 v87, v170, v170\n"
      "v_add_f32 v86, v86, v171\n"
      "v_fmac_f32 v87, v171, v171\n"
      "v_add_f32 v86, v86, v172\n"
      "v_fmac_f32 v87, v172, v172\n"
      "v_add_f32 v86, v86, v173\n"
      "v_fmac_f32 v87, v173, v173\n"
      "v_add_f32 v86, v86, v174\n"
      "v_fmac_f32 v87, v174, v174\n"
      "v_add_f32 v86, v86, v175\n"
      "v_fmac_f32 v87, v175, v175\n"
      "ds_write_b64 v10, v[86:87] offset:6528\n"
      "s_waitcnt vmcnt(10)\n"
      "s_add_u32 s30, s48, 589824\n"
      "buffer_store_dwordx4 v[88:91], v6, s[8:11], s30 offen sc1\n"
      "buffer_store_dwordx2 v[92:93], v7, s[8:11], s30 offen sc1\n"
      "v_lshlrev_b32 v164, 16, v88\n"
      "v_and_b32 v165, 0xffff0000, v88\n"
      "v_lshlrev_b32 v166, 16, v89\n"
      "v_and_b32 v167, 0xffff0000, v89\n"
      "v_lshlrev_b32 v168, 16, v90\n"
      "v_and_b32 v169, 0xffff0000, v90\n"
      "v_lshlrev_b32 v170, 16, v91\n"
      "v_and_b32 v171, 0xffff0000, v91\n"
      "v_lshlrev_b32 v172, 16, v92\n"
      "v_and_b32 v173, 0xffff0000, v92\n"
      "v_lshlrev_b32 v174, 16, v93\n"
      "v_and_b32 v175, 0xffff0000, v93\n"
      "v_add_f32 v94, v164, v165\n"
      "v_mul_f32 v95, v164, v164\n"
      "v_fmac_f32 v95, v165, v165\n"
      "v_add_f32 v94, v94, v166\n"
      "v_fmac_f32 v95, v166, v166\n"
      "v_add_f32 v94, v94, v167\n"
      "v_fmac_f32 v95, v167, v167\n"
      "v_add_f32 v94, v94, v168\n"
      "v_fmac_f32 v95, v168, v168\n"
      "v_add_f32 v94, v94, v169\n"
      "v_fmac_f32 v95, v169, v169\n"
      "v_add_f32 v94, v94, v170\n"
      "v_fmac_f32 v95, v170, v170\n"
      "v_add_f32 v94, v94, v171\n"
      "v_fmac_f32 v95, v171, v171\n"
      "v_add_f32 v94, v94, v172\n"
      "v_fmac_f32 v95, v172, v172\n"
      "v_add_f32 v94, v94, v173\n"
      "v_fmac_f32 v95, v173, v173\n"
      "v_add_f32 v94, v94, v174\n"
      "v_fmac_f32 v95, v174, v174\n"
      "v_add_f32 v94, v94, v175\n"
      "v_fmac_f32 v95, v175, v175\n"
      "ds_write_b64 v10, v[94:95] offset:8704\n"
      "s_waitcnt vmcnt(10)\n"
      "buffer_store_dwordx4 v[96:99], v6, s[8:11], s49 offen sc1\n"
      "buffer_store_dwordx2 v[100:101], v7, s[8:11], s49 offen sc1\n"
      "v_lshlrev_b32 v164, 16, v96\n"
      "v_and_b32 v165, 0xffff0000, v96\n"
      "v_lshlrev_b32 v166, 16, v97\n"
      "v_and_b32 v167, 0xffff0000, v97\n"
      "v_lshlrev_b32 v168, 16, v98\n"
      "v_and_b32 v169, 0xffff0000, v98\n"
      "v_lshlrev_b32 v170, 16, v99\n"
      "v_and_b32 v171, 0xffff0000, v99\n"
      "v_lshlrev_b32 v172, 16, v100\n"
      "v_and_b32 v173, 0xffff0000, v100\n"
      "v_lshlrev_b32 v174, 16, v101\n"
      "v_and_b32 v175, 0xffff0000, v101\n"
      "v_add_f32 v102, v164, v165\n"
      "v_mul_f32 v103, v164, v164\n"
      "v_fmac_f32 v103, v165, v165\n"
      "v_add_f32 v102, v102, v166\n"
      "v_fmac_f32 v103, v166, v166\n"
      "v_add_f32 v102, v102, v167\n"
      "v_fmac_f32 v103, v167, v167\n"
      "v_add_f32 v102, v102, v168\n"
      "v_fmac_f32 v103, v168, v168\n"
      "v_add_f32 v102, v102, v169\n"
      "v_fmac_f32 v103, v169, v169\n"
      "v_add_f32 v102, v102, v170\n"
      "v_fmac_f32 v103, v170, v170\n"
      "v_add_f32 v102, v102, v171\n"
      "v_fmac_f32 v103, v171, v171\n"
      "v_add_f32 v102, v102, v172\n"
      "v_fmac_f32 v103, v172, v172\n"
      "v_add_f32 v102, v102, v173\n"
      "v_fmac_f32 v103, v173, v173\n"
      "v_add_f32 v102, v102, v174\n"
      "v_fmac_f32 v103, v174, v174\n"
      "v_add_f32 v102, v102, v175\n"
      "v_fmac_f32 v103, v175, v175\n"
      "ds_write_b64 v10, v[102:103] offset:10880\n"
      "s_waitcnt lgkmcnt(0)\n"
      "s_barrier\n"
      "ds_read_b64 v[154:155], v11 offset:2176\n"
      "ds_read_b64 v[156:157], v11 offset:4352\n"
      "ds_read_b64 v[158:159], v11 offset:6528\n"
      "ds_read_b64 v[160:161], v11 offset:8704\n"
      "ds_read_b64 v[162:163], v11 offset:10880\n"
      "s_waitcnt lgkmcnt(0)\n"
      "v_add_f32_dpp v154, v154, v154 row_ror:8 row_mask:0xf bank_mask:0xf\n"
      "v_add_f32_dpp v155, v155, v155 row_ror:8 row_mask:0xf bank_mask:0xf\n"
      "v_add_f32_dpp v156, v156, v156 row_ror:8 row_mask:0xf bank_mask:0xf\n"
      "v_add_f32_dpp v157, v157, v157 row_ror:8 row_mask:0xf bank_mask:0xf\n"
      "v_add_f32_dpp v158, v158, v158 row_ror:8 row_mask:0xf bank_mask:0xf\n"
      "v_add_f32_dpp v159, v159, v159 row_ror:8 row_mask:0xf bank_mask:0xf\n"
      "v_add_f32_dpp v160, v160, v160 row_ror:8 row_mask:0xf bank_mask:0xf\n"
      "v_add_f32_dpp v161, v161, v161 row_ror:8 row_mask:0xf bank_mask:0xf\n"
      "v_add_f32_dpp v162, v162, v162 row_ror:8 row_mask:0xf bank_mask:0xf\n"
      "v_add_f32_dpp v163, v163, v163 row_ror:8 row_mask:0xf bank_mask:0xf\n"
      "v_add_f32_dpp v154, v154, v154 row_ror:4 row_mask:0xf bank_mask:0xf\n"
      "v_add_f32_dpp v155, v155, v155 row_ror:4 row_mask:0xf bank_mask:0xf\n"
      "v_add_f32_dpp v156, v156, v156 row_ror:4 row_mask:0xf bank_mask:0xf\n"
      "v_add_f32_dpp v157, v157, v157 row_ror:4 row_mask:0xf bank_mask:0xf\n"
      "v_add_f32_dpp v158, v158, v158 row_ror:4 row_mask:0xf bank_mask:0xf\n"
      "v_add_f32_dpp v159, v159, v159 row_ror:4 row_mask:0xf bank_mask:0xf\n"
      "v_add_f32_dpp v160, v160, v160 row_ror:4 row_mask:0xf bank_mask:0xf\n"
      "v_add_f32_dpp v161, v161, v161 row_ror:4 row_mask:0xf bank_mask:0xf\n"
      "v_add_f32_dpp v162, v162, v162 row_ror:4 row_mask:0xf bank_mask:0xf\n"
      "v_add_f32_dpp v163, v163, v163 row_ror:4 row_mask:0xf bank_mask:0xf\n"
      "v_add_f32_dpp v154, v154, v154 row_ror:2 row_mask:0xf bank_mask:0xf\n"
      "v_add_f32_dpp v155, v155, v155 row_ror:2 row_mask:0xf bank_mask:0xf\n"
      "v_add_f32_dpp v156, v156, v156 row_ror:2 row_mask:0xf bank_mask:0xf\n"
      "v_add_f32_dpp v157, v157, v157 row_ror:2 row_mask:0xf bank_mask:0xf\n"
      "v_add_f32_dpp v158, v158, v158 row_ror:2 row_mask:0xf bank_mask:0xf\n"
      "v_add_f32_dpp v159, v159, v159 row_ror:2 row_mask:0xf bank_mask:0xf\n"
      "v_add_f32_dpp v160, v160, v160 row_ror:2 row_mask:0xf bank_mask:0xf\n"
      "v_add_f32_dpp v161, v161, v161 row_ror:2 row_mask:0xf bank_mask:0xf\n"
      "v_add_f32_dpp v162, v162, v162 row_ror:2 row_mask:0xf bank_mask:0xf\n"
      "v_add_f32_dpp v163, v163, v163 row_ror:2 row_mask:0xf bank_mask:0xf\n"
      "v_add_f32_dpp v154, v154, v154 row_ror:1 row_mask:0xf bank_mask:0xf\n"
      "v_add_f32_dpp v155, v155, v155 row_ror:1 row_mask:0xf bank_mask:0xf\n"
      "v_add_f32_dpp v156, v156, v156 row_ror:1 row_mask:0xf bank_mask:0xf\n"
      "v_add_f32_dpp v157, v157, v157 row_ror:1 row_mask:0xf bank_mask:0xf\n"
      "v_add_f32_dpp v158, v158, v158 row_ror:1 row_mask:0xf bank_mask:0xf\n"
      "v_add_f32_dpp v159, v159, v159 row_ror:1 row_mask:0xf bank_mask:0xf\n"
      "v_add_f32_dpp v160, v160, v160 row_ror:1 row_mask:0xf bank_mask:0xf\n"
      "v_add_f32_dpp v161, v161, v161 row_ror:1 row_mask:0xf bank_mask:0xf\n"
      "v_add_f32_dpp v162, v162, v162 row_ror:1 row_mask:0xf bank_mask:0xf\n"
      "v_add_f32_dpp v163, v163, v163 row_ror:1 row_mask:0xf bank_mask:0xf\n"
      "v_mul_f32 v154, 0x3ae38e39, v154\n"
      "v_add_f32 v155, 0x3afba882, v155\n"
      "v_mul_f32 v155, 0x3ae38e39, v155\n"
      "v_mul_f32 v156, 0x3ae38e39, v156\n"
      "v_add_f32 v157, 0x3afba882, v157\n"
      "v_mul_f32 v157, 0x3ae38e39, v157\n"
      "v_mul_f32 v158, 0x3ae38e39, v158\n"
      "v_add_f32 v159, 0x3afba882, v159\n"
      "v_mul_f32 v159, 0x3ae38e39, v159\n"
      "v_mul_f32 v160, 0x3ae38e39, v160\n"
      "v_add_f32 v161, 0x3afba882, v161\n"
      "v_mul_f32 v161, 0x3ae38e39, v161\n"
      "v_mul_f32 v162, 0x3ae38e39, v162\n"
      "v_add_f32 v163, 0x3afba882, v163\n"
      "v_mul_f32 v163, 0x3ae38e39, v163\n"
      "s_add_u32 s30, s48, 147456\n"
      "buffer_store_dwordx2 v[154:155], v12, s[8:11], s30 offen sc1\n"
      "s_add_u32 s30, s48, 294912\n"
      "buffer_store_dwordx2 v[156:157], v12, s[8:11], s30 offen sc1\n"
      "s_add_u32 s30, s48, 442368\n"
      "buffer_store_dwordx2 v[158:159], v12, s[8:11], s30 offen sc1\n"
      "s_add_u32 s30, s48, 589824\n"
      "buffer_store_dwordx2 v[160:161], v12, s[8:11], s30 offen sc1\n"
      "buffer_store_dwordx2 v[162:163], v12, s[8:11], s49 offen sc1\n"
      "s_waitcnt vmcnt(0)\n"
      "s_add_u32 s30, s48, 147456\n"
      "buffer_store_dword v18, v5, s[8:11], s30 offen sc1\n"
      "s_add_u32 s30, s48, 294912\n"
      "buffer_store_dword v18, v5, s[8:11], s30 offen sc1\n"
      "s_add_u32 s30, s48, 442368\n"
      "buffer_store_dword v18, v5, s[8:11], s30 offen sc1\n"
      "s_add_u32 s30, s48, 589824\n"
      "buffer_store_dword v18, v5, s[8:11], s30 offen sc1\n"
      "buffer_store_dword v18, v5, s[8:11], s49 offen sc1\n"
      "v_and_b32 v20, 0x40000000, v19\n"
      "v_or_b32 v5, v20, v5\n"
      "s_add_u32 s30, s37, 0\n"
      "buffer_store_dwordx4 v[224:227], v183, s[8:11], s30 offen sc1\n"
      "s_add_u32 s30, s37, 0\n"
      "buffer_store_dwordx4 v[224:227], v181, s[8:11], s30 offen sc1\n"
      "s_sleep 32\n"
      "s_waitcnt vmcnt(0) lgkmcnt(0)\n"
      "s_mov_b32 s61, s60\n"
      "s_mov_b32 s62, 6\n"
      ".Ld_r0:\n"
      "s_mul_i32 s63, s61, 36864\n"
      "s_add_u32 s63, s63, s44\n"
      "s_mul_i32 s30, s61, 8192\n"
      "s_lshr_b32 s30, s30, 16\n"
      "s_mul_i32 s30, s30, 8\n"
      "s_sub_u32 s30, s61, s30\n"
      "s_mul_i32 s30, s30, 12288\n"
      "s_add_u32 s30, s30, 0\n"
      "s_add_u32 m0, s42, s30\n"
      "s_add_u32 s61, s61, 1\n"
      "buffer_load_dwordx4 v178, s[12:15], s63 offen offset:0 lds\n"
      "buffer_load_dwordx4 v179, s[12:15], s63 offen offset:1024 lds\n"
      "buffer_load_dwordx4 v180, s[12:15], s63 offen offset:2048 lds\n"
      "s_cmp_eq_u32 s61, 24\n"
      "s_cselect_b32 s61, 0, s61\n"
      "s_sub_u32 s62, s62, 1\n"
      "s_cbranch_scc0 .Ld_r0\n"
      "v_add_u32 v20, 49152, v176\n"
      "v_add_u32 v21, 49152, v177\n"
      "v_add_u32 v22, 98304, v176\n"
      "v_add_u32 v23, 98304, v177\n"
      "s_mov_b32 s62, 23\n"
      "s_cmp_eq_u32 s60, 6\n"
      "s_cbranch_scc1 .Ld_rs6\n"
      "s_cmp_eq_u32 s60, 12\n"
      "s_cbranch_scc1 .Ld_rs12\n"
      "s_cmp_eq_u32 s60, 18\n"
      "s_cbranch_scc1 .Ld_rs18\n"
      ".Ld_rs0:\n"
      "s_add_u32 s63, s44, 258048\n"
      "s_cmp_ge_u32 s62, 7\n"
      "s_cselect_b32 s63, s63, s31\n"
      "s_add_u32 m0, s42, 86016\n"
      "s_waitcnt vmcnt(18) lgkmcnt(0)\n"
      "s_barrier\n"
      "ds_read_b64_tr_b16 a[0:1], v176 offset:0\n"
      "ds_read_b64_tr_b16 a[2:3], v176 offset:1536\n"
      "ds_read_b64_tr_b16 a[96:97], v176 offset:8\n"
      "ds_read_b64_tr_b16 a[98:99], v176 offset:1544\n"
      "ds_read_b64_tr_b16 a[192:193], v177 offset:0\n"
      "ds_read_b64_tr_b16 a[194:195], v177 offset:1536\n"
      "buffer_load_dwordx4 v178, s[12:15], s63 offen offset:0 lds\n"
      "buffer_load_dwordx4 v179, s[12:15], s63 offen offset:1024 lds\n"
      "buffer_load_dwordx4 v180, s[12:15], s63 offen offset:2048 lds\n"
      "s_sub_u32 s62, s62, 1\n"
      ".Ld_rs1:\n"
      "s_add_u32 s63, s44, 294912\n"
      "s_cmp_ge_u32 s62, 7\n"
      "s_cselect_b32 s63, s63, s31\n"
      "s_add_u32 m0, s42, 0\n"
      "s_waitcnt vmcnt(18) lgkmcnt(0)\n"
      "s_barrier\n"
      "ds_read_b64_tr_b16 a[4:5], v176 offset:12288\n"
      "ds_read_b64_tr_b16 a[6:7], v176 offset:13824\n"
      "ds_read_b64_tr_b16 a[100:101], v176 offset:12296\n"
      "ds_read_b64_tr_b16 a[102:103], v176 offset:13832\n"
      "ds_read_b64_tr_b16 a[196:197], v177 offset:12288\n"
      "ds_read_b64_tr_b16 a[198:199], v177 offset:13824\n"
      "buffer_load_dwordx4 v178, s[12:15], s63 offen offset:0 lds\n"
      "buffer_load_dwordx4 v179, s[12:15], s63 offen offset:1024 lds\n"
      "buffer_load_dwordx4 v180, s[12:15], s63 offen offset:2048 lds\n"
      "s_sub_u32 s62, s62, 1\n"
      ".Ld_rs2:\n"
      "s_add_u32 s63, s44, 331776\n"
      "s_cmp_ge_u32 s62, 7\n"
      "s_cselect_b32 s63, s63, s31\n"
      "s_add_u32 m0, s42, 12288\n"
      "s_waitcnt vmcnt(18) lgkmcnt(0)\n"
      "s_barrier\n"
      "ds_read_b64_tr_b16 a[8:9], v176 offset:24576\n"
      "ds_read_b64_tr_b16 a[10:11], v176 offset:26112\n"
      "ds_read_b64_tr_b16 a[104:105], v176 offset:24584\n"
      "ds_read_b64_tr_b16 a[106:107], v176 offset:26120\n"
      "ds_read_b64_tr_b16 a[200:201], v177 offset:24576\n"
      "ds_read_b64_tr_b16 a[202:203], v177 offset:26112\n"
      "buffer_load_dwordx4 v178, s[12:15], s63 offen offset:0 lds\n"
      "buffer_load_dwordx4 v179, s[12:15], s63 offen offset:1024 lds\n"
      "buffer_load_dwordx4 v180, s[12:15], s63 offen offset:2048 lds\n"
      "s_sub_u32 s62, s62, 1\n"
      ".Ld_rs3:\n"
      "s_add_u32 s63, s44, 368640\n"
      "s_cmp_ge_u32 s62, 7\n"
      "s_cselect_b32 s63, s63, s31\n"
      "s_add_u32 m0, s42, 24576\n"
      "s_waitcnt vmcnt(18) lgkmcnt(0)\n"
      "s_barrier\n"
      "ds_read_b64_tr_b16 a[12:13], v176 offset:36864\n"
      "ds_read_b64_tr_b16 a[14:15], v176 offset:38400\n"
      "ds_read_b64_tr_b16 a[108:109], v176 offset:36872\n"
      "ds_read_b64_tr_b16 a[110:111], v176 offset:38408\n"
      "ds_read_b64_tr_b16 a[204:205], v177 offset:36864\n"
      "ds_read_b64_tr_b16 a[206:207], v177 offset:38400\n"
      "buffer_load_dwordx4 v178, s[12:15], s63 offen offset:0 lds\n"
      "buffer_load_dwordx4 v179, s[12:15], s63 offen offset:1024 lds\n"
      "buffer_load_dwordx4 v180, s[12:15], s63 offen offset:2048 lds\n"
      "s_sub_u32 s62, s62, 1\n"
      ".Ld_rs4:\n"
      "s_add_u32 s63, s44, 405504\n"
      "s_cmp_ge_u32 s62, 7\n"
      "s_cselect_b32 s63, s63, s31\n"
      "s_add_u32 m0, s42, 36864\n"
      "s_waitcnt vmcnt(18) lgkmcnt(0)\n"
      "s_barrier\n"
      "ds_read_b64_tr_b16 a[16:17], v20 offset:0\n"
      "ds_read_b64_tr_b16 a[18:19], v20 offset:1536\n"
      "ds_read_b64_tr_b16 a[112:113], v20 offset:8\n"
      "ds_read_b64_tr_b16 a[114:115], v20 offset:1544\n"
      "ds_read_b64_tr_b16 a[208:209], v21 offset:0\n"
      "ds_read_b64_tr_b16 a[210:211], v21 offset:1536\n"
      "buffer_load_dwordx4 v178, s[12:15], s63 offen offset:0 lds\n"
      "buffer_load_dwordx4 v179, s[12:15], s63 offen offset:1024 lds\n"
      "buffer_load_dwordx4 v180, s[12:15], s63 offen offset:2048 lds\n"
      "s_sub_u32 s62, s62, 1\n"
      ".Ld_rs5:\n"
      "s_add_u32 s63, s44, 442368\n"
      "s_cmp_ge_u32 s62, 7\n"
      "s_cselect_b32 s63, s63, s31\n"
      "s_add_u32 m0, s42, 49152\n"
      "s_waitcnt vmcnt(18) lgkmcnt(0)\n"
      "s_barrier\n"
      "ds_read_b64_tr_b16 a[20:21], v20 offset:12288\n"
      "ds_read_b64_tr_b16 a[22:23], v20 offset:13824\n"
      "ds_read_b64_tr_b16 a[116:117], v20 offset:12296\n"
      "ds_read_b64_tr_b16 a[118:119], v20 offset:13832\n"
      "ds_read_b64_tr_b16 a[212:213], v21 offset:12288\n"
      "ds_read_b64_tr_b16 a[214:215], v21 offset:13824\n"
      "buffer_load_dwordx4 v178, s[12:15], s63 offen offset:0 lds\n"
      "buffer_load_dwordx4 v179, s[12:15], s63 offen offset:1024 lds\n"
      "buffer_load_dwordx4 v180, s[12:15], s63 offen offset:2048 lds\n"
      "s_sub_u32 s62, s62, 1\n"
      "s_cbranch_scc1 .Ld_re\n"
      ".Ld_rs6:\n"
      "s_add_u32 s63, s44, 479232\n"
      "s_cmp_ge_u32 s62, 7\n"
      "s_cselect_b32 s63, s63, s31\n"
      "s_add_u32 m0, s42, 61440\n"
      "s_waitcnt vmcnt(18) lgkmcnt(0)\n"
      "s_barrier\n"
      "ds_read_b64_tr_b16 a[24:25], v20 offset:24576\n"
      "ds_read_b64_tr_b16 a[26:27], v20 offset:26112\n"
      "ds_read_b64_tr_b16 a[120:121], v20 offset:24584\n"
      "ds_read_b64_tr_b16 a[122:123], v20 offset:26120\n"
      "ds_read_b64_tr_b16 a[216:217], v21 offset:24576\n"
      "ds_read_b64_tr_b16 a[218:219], v21 offset:26112\n"
      "buffer_load_dwordx4 v178, s[12:15], s63 offen offset:0 lds\n"
      "buffer_load_dwordx4 v179, s[12:15], s63 offen offset:1024 lds\n"
      "buffer_load_dwordx4 v180, s[12:15], s63 offen offset:2048 lds\n"
      "s_sub_u32 s62, s62, 1\n"
      ".Ld_rs7:\n"
      "s_add_u32 s63, s44, 516096\n"
      "s_cmp_ge_u32 s62, 7\n"
      "s_cselect_b32 s63, s63, s31\n"
      "s_add_u32 m0, s42, 73728\n"
      "s_waitcnt vmcnt(18) lgkmcnt(0)\n"
      "s_barrier\n"
      "ds_read_b64_tr_b16 a[28:29], v20 offset:36864\n"
      "ds_read_b64_tr_b16 a[30:31], v20 offset:38400\n"
      "ds_read_b64_tr_b16 a[124:125], v20 offset:36872\n"
      "ds_read_b64_tr_b16 a[126:127], v20 offset:38408\n"
      "ds_read_b64_tr_b16 a[220:221], v21 offset:36864\n"
      "ds_read_b64_tr_b16 a[222:223], v21 offset:38400\n"
      "buffer_load_dwordx4 v178, s[12:15], s63 offen offset:0 lds\n"
      "buffer_load_dwordx4 v179, s[12:15], s63 offen offset:1024 lds\n"
      "buffer_load_dwordx4 v180, s[12:15], s63 offen offset:2048 lds\n"
      "s_sub_u32 s62, s62, 1\n"
      ".Ld_rs8:\n"
      "s_add_u32 s63, s44, 552960\n"
      "s_cmp_ge_u32 s62, 7\n"
      "s_cselect_b32 s63, s63, s31\n"
      "s_add_u32 m0, s42, 86016\n"
      "s_waitcnt vmcnt(18) lgkmcnt(0)\n"
      "s_barrier\n"
      "ds_read_b64_tr_b16 a[32:33], v176 offset:0\n"
      "ds_read_b64_tr_b16 a[34:35], v176 offset:1536\n"
      "ds_read_b64_tr_b16 a[128:129], v176 offset:8\n"
      "ds_read_b64_tr_b16 a[130:131], v176 offset:1544\n"
      "ds_read_b64_tr_b16 a[224:225], v177 offset:0\n"
      "ds_read_b64_tr_b16 a[226:227], v177 offset:1536\n"
      "buffer_load_dwordx4 v178, s[12:15], s63 offen offset:0 lds\n"
      "buffer_load_dwordx4 v179, s[12:15], s63 offen offset:1024 lds\n"
      "buffer_load_dwordx4 v180, s[12:15], s63 offen offset:2048 lds\n"
      "s_sub_u32 s62, s62, 1\n"
      ".Ld_rs9:\n"
      "s_add_u32 s63, s44, 589824\n"
      "s_cmp_ge_u32 s62, 7\n"
      "s_cselect_b32 s63, s63, s31\n"
      "s_add_u32 m0, s42, 0\n"
      "s_waitcnt vmcnt(18) lgkmcnt(0)\n"
      "s_barrier\n"
      "ds_read_b64_tr_b16 a[36:37], v176 offset:12288\n"
      "ds_read_b64_tr_b16 a[38:39], v176 offset:13824\n"
      "ds_read_b64_tr_b16 a[132:133], v176 offset:12296\n"
      "ds_read_b64_tr_b16 a[134:135], v176 offset:13832\n"
      "ds_read_b64_tr_b16 a[228:229], v177 offset:12288\n"
      "ds_read_b64_tr_b16 a[230:231], v177 offset:13824\n"
      "buffer_load_dwordx4 v178, s[12:15], s63 offen offset:0 lds\n"
      "buffer_load_dwordx4 v179, s[12:15], s63 offen offset:1024 lds\n"
      "buffer_load_dwordx4 v180, s[12:15], s63 offen offset:2048 lds\n"
      "s_sub_u32 s62, s62, 1\n"
      ".Ld_rs10:\n"
      "s_add_u32 s63, s44, 626688\n"
      "s_cmp_ge_u32 s62, 7\n"
      "s_cselect_b32 s63, s63, s31\n"
      "s_add_u32 m0, s42, 12288\n"
      "s_waitcnt vmcnt(18) lgkmcnt(0)\n"
      "s_barrier\n"
      "ds_read_b64_tr_b16 a[40:41], v176 offset:24576\n"
      "ds_read_b64_tr_b16 a[42:43], v176 offset:26112\n"
      "ds_read_b64_tr_b16 a[136:137], v176 offset:24584\n"
      "ds_read_b64_tr_b16 a[138:139], v176 offset:26120\n"
      "ds_read_b64_tr_b16 a[232:233], v177 offset:24576\n"
      "ds_read_b64_tr_b16 a[234:235], v177 offset:26112\n"
      "buffer_load_dwordx4 v178, s[12:15], s63 offen offset:0 lds\n"
      "buffer_load_dwordx4 v179, s[12:15], s63 offen offset:1024 lds\n"
      "buffer_load_dwordx4 v180, s[12:15], s63 offen offset:2048 lds\n"
      "s_sub_u32 s62, s62, 1\n"
      ".Ld_rs11:\n"
      "s_add_u32 s63, s44, 663552\n"
      "s_cmp_ge_u32 s62, 7\n"
      "s_cselect_b32 s63, s63, s31\n"
      "s_add_u32 m0, s42, 24576\n"
      "s_waitcnt vmcnt(18) lgkmcnt(0)\n"
      "s_barrier\n"
      "ds_read_b64_tr_b16 a[44:45], v176 offset:36864\n"
      "ds_read_b64_tr_b16 a[46:47], v176 offset:38400\n"
      "ds_read_b64_tr_b16 a[140:141], v176 offset:36872\n"
      "ds_read_b64_tr_b16 a[142:143], v176 offset:38408\n"
      "ds_read_b64_tr_b16 a[236:237], v177 offset:36864\n"
      "ds_read_b64_tr_b16 a[238:239], v177 offset:38400\n"
      "buffer_load_dwordx4 v178, s[12:15], s63 offen offset:0 lds\n"
      "buffer_load_dwordx4 v179, s[12:15], s63 offen offset:1024 lds\n"
      "buffer_load_dwordx4 v180, s[12:15], s63 offen offset:2048 lds\n"
      "s_sub_u32 s62, s62, 1\n"
      "s_cbranch_scc1 .Ld_re\n"
      ".Ld_rs12:\n"
      "s_add_u32 s63, s44, 700416\n"
      "s_cmp_ge_u32 s62, 7\n"
      "s_cselect_b32 s63, s63, s31\n"
      "s_add_u32 m0, s42, 36864\n"
      "s_waitcnt vmcnt(18) lgkmcnt(0)\n"
      "s_barrier\n"
      "ds_read_b64_tr_b16 a[48:49], v20 offset:0\n"
      "ds_read_b64_tr_b16 a[50:51], v20 offset:1536\n"
      "ds_read_b64_tr_b16 a[144:145], v20 offset:8\n"
      "ds_read_b64_tr_b16 a[146:147], v20 offset:1544\n"
      "ds_read_b64_tr_b16 a[240:241], v21 offset:0\n"
      "ds_read_b64_tr_b16 a[242:243], v21 offset:1536\n"
      "buffer_load_dwordx4 v178, s[12:15], s63 offen offset:0 lds\n"
      "buffer_load_dwordx4 v179, s[12:15], s63 offen offset:1024 lds\n"
      "buffer_load_dwordx4 v180, s[12:15], s63 offen offset:2048 lds\n"
      "s_sub_u32 s62, s62, 1\n"
      ".Ld_rs13:\n"
      "s_add_u32 s63, s44, 737280\n"
      "s_cmp_ge_u32 s62, 7\n"
      "s_cselect_b32 s63, s63, s31\n"
      "s_add_u32 m0, s42, 49152\n"
      "s_waitcnt vmcnt(18) lgkmcnt(0)\n"
      "s_barrier\n"
      "ds_read_b64_tr_b16 a[52:53], v20 offset:12288\n"
      "ds_read_b64_tr_b16 a[54:55], v20 offset:13824\n"
      "ds_read_b64_tr_b16 a[148:149], v20 offset:12296\n"
      "ds_read_b64_tr_b16 a[150:151], v20 offset:13832\n"
      "ds_read_b64_tr_b16 a[244:245], v21 offset:12288\n"
      "ds_read_b64_tr_b16 a[246:247], v21 offset:13824\n"
      "buffer_load_dwordx4 v178, s[12:15], s63 offen offset:0 lds\n"
      "buffer_load_dwordx4 v179, s[12:15], s63 offen offset:1024 lds\n"
      "buffer_load_dwordx4 v180, s[12:15], s63 offen offset:2048 lds\n"
      "s_sub_u32 s62, s62, 1\n"
      ".Ld_rs14:\n"
      "s_add_u32 s63, s44, 774144\n"
      "s_cmp_ge_u32 s62, 7\n"
      "s_cselect_b32 s63, s63, s31\n"
      "s_add_u32 m0, s42, 61440\n"
      "s_waitcnt vmcnt(18) lgkmcnt(0)\n"
      "s_barrier\n"
      "ds_read_b64_tr_b16 a[56:57], v20 offset:24576\n"
      "ds_read_b64_tr_b16 a[58:59], v20 offset:26112\n"
      "ds_read_b64_tr_b16 a[152:153], v20 offset:24584\n"
      "ds_read_b64_tr_b16 a[154:155], v20 offset:26120\n"
      "ds_read_b64_tr_b16 a[248:249], v21 offset:24576\n"
      "ds_read_b64_tr_b16 a[250:251], v21 offset:26112\n"
      "buffer_load_dwordx4 v178, s[12:15], s63 offen offset:0 lds\n"
      "buffer_load_dwordx4 v179, s[12:15], s63 offen offset:1024 lds\n"
      "buffer_load_dwordx4 v180, s[12:15], s63 offen offset:2048 lds\n"
      "s_sub_u32 s62, s62, 1\n"
      ".Ld_rs15:\n"
      "s_add_u32 s63, s44, 811008\n"
      "s_cmp_ge_u32 s62, 7\n"
      "s_cselect_b32 s63, s63, s31\n"
      "s_add_u32 m0, s42, 73728\n"
      "s_waitcnt vmcnt(18) lgkmcnt(0)\n"
      "s_barrier\n"
      "ds_read_b64_tr_b16 a[60:61], v20 offset:36864\n"
      "ds_read_b64_tr_b16 a[62:63], v20 offset:38400\n"
      "ds_read_b64_tr_b16 a[156:157], v20 offset:36872\n"
      "ds_read_b64_tr_b16 a[158:159], v20 offset:38408\n"
      "ds_read_b64_tr_b16 a[252:253], v21 offset:36864\n"
      "ds_read_b64_tr_b16 a[254:255], v21 offset:38400\n"
      "buffer_load_dwordx4 v178, s[12:15], s63 offen offset:0 lds\n"
      "buffer_load_dwordx4 v179, s[12:15], s63 offen offset:1024 lds\n"
      "buffer_load_dwordx4 v180, s[12:15], s63 offen offset:2048 lds\n"
      "s_sub_u32 s62, s62, 1\n"
      ".Ld_rs16:\n"
      "s_add_u32 s63, s44, 847872\n"
      "s_cmp_ge_u32 s62, 7\n"
      "s_cselect_b32 s63, s63, s31\n"
      "s_add_u32 m0, s42, 86016\n"
      "s_waitcnt vmcnt(18) lgkmcnt(0)\n"
      "s_barrier\n"
      "ds_read_b64_tr_b16 a[64:65], v176 offset:0\n"
      "ds_read_b64_tr_b16 a[66:67], v176 offset:1536\n"
      "ds_read_b64_tr_b16 a[160:161], v176 offset:8\n"
      "ds_read_b64_tr_b16 a[162:163], v176 offset:1544\n"
      "ds_read_b64_tr_b16 v[24:25], v177 offset:0\n"
      "ds_read_b64_tr_b16 v[26:27], v177 offset:1536\n"
      "buffer_load_dwordx4 v178, s[12:15], s63 offen offset:0 lds\n"
      "buffer_load_dwordx4 v179, s[12:15], s63 offen offset:1024 lds\n"
      "buffer_load_dwordx4 v180, s[12:15], s63 offen offset:2048 lds\n"
      "s_sub_u32 s62, s62, 1\n"
      ".Ld_rs17:\n"
      "s_add_u32 s63, s44, 0\n"
      "s_cmp_ge_u32 s62, 7\n"
      "s_cselect_b32 s63, s63, s31\n"
      "s_add_u32 m0, s42, 0\n"
      "s_waitcnt vmcnt(18) lgkmcnt(0)\n"
      "s_barrier\n"
      "ds_read_b64_tr_b16 a[68:69], v176 offset:12288\n"
      "ds_read_b64_tr_b16 a[70:71], v176 offset:13824\n"
      "ds_read_b64_tr_b16 a[164:165], v176 offset:12296\n"
      "ds_read_b64_tr_b16 a[166:167], v176 offset:13832\n"
      "ds_read_b64_tr_b16 v[28:29], v177 offset:12288\n"
      "ds_read_b64_tr_b16 v[30:31], v177 offset:13824\n"
      "buffer_load_dwordx4 v178, s[12:15], s63 offen offset:0 lds\n"
      "buffer_load_dwordx4 v179, s[12:15], s63 offen offset:1024 lds\n"
      "buffer_load_dwordx4 v180, s[12:15], s63 offen offset:2048 lds\n"
      "s_sub_u32 s62, s62, 1\n"
      "s_cbranch_scc1 .Ld_re\n"
      ".Ld_rs18:\n"
      "s_add_u32 s63, s44, 36864\n"
      "s_cmp_ge_u32 s62, 7\n"
      "s_cselect_b32 s63, s63, s31\n"
      "s_add_u32 m0, s42, 12288\n"
      "s_waitcnt vmcnt(18) lgkmcnt(0)\n"
      "s_barrier\n"
      "ds_read_b64_tr_b16 a[72:73], v176 offset:24576\n"
      "ds_read_b64_tr_b16 a[74:75], v176 offset:26112\n"
      "ds_read_b64_tr_b16 a[168:169], v176 offset:24584\n"
      "ds_read_b64_tr_b16 a[170:171], v176 offset:26120\n"
      "ds_read_b64_tr_b16 v[32:33], v177 offset:24576\n"
      "ds_read_b64_tr_b16 v[34:35], v177 offset:26112\n"
      "buffer_load_dwordx4 v178, s[12:15], s63 offen offset:0 lds\n"
      "buffer_load_dwordx4 v179, s[12:15], s63 offen offset:1024 lds\n"
      "buffer_load_dwordx4 v180, s[12:15], s63 offen offset:2048 lds\n"
      "s_sub_u32 s62, s62, 1\n"
      ".Ld_rs19:\n"
      "s_add_u32 s63, s44, 73728\n"
      "s_cmp_ge_u32 s62, 7\n"
      "s_cselect_b32 s63, s63, s31\n"
      "s_add_u32 m0, s42, 24576\n"
      "s_waitcnt vmcnt(18) lgkmcnt(0)\n"
      "s_barrier\n"
      "ds_read_b64_tr_b16 a[76:77], v176 offset:36864\n"
      "ds_read_b64_tr_b16 a[78:79], v176 offset:38400\n"
      "ds_read_b64_tr_b16 a[172:173], v176 offset:36872\n"
      "ds_read_b64_tr_b16 a[174:175], v176 offset:38408\n"
      "ds_read_b64_tr_b16 v[36:37], v177 offset:36864\n"
      "ds_read_b64_tr_b16 v[38:39], v177 offset:38400\n"
      "buffer_load_dwordx4 v178, s[12:15], s63 offen offset:0 lds\n"
      "buffer_load_dwordx4 v179, s[12:15], s63 offen offset:1024 lds\n"
      "buffer_load_dwordx4 v180, s[12:15], s63 offen offset:2048 lds\n"
      "s_sub_u32 s62, s62, 1\n"
      ".Ld_rs20:\n"
      "s_add_u32 s63, s44, 110592\n"
      "s_cmp_ge_u32 s62, 7\n"
      "s_cselect_b32 s63, s63, s31\n"
      "s_add_u32 m0, s42, 36864\n"
      "s_waitcnt vmcnt(18) lgkmcnt(0)\n"
      "s_barrier\n"
      "ds_read_b64_tr_b16 a[80:81], v20 offset:0\n"
      "ds_read_b64_tr_b16 a[82:83], v20 offset:1536\n"
      "ds_read_b64_tr_b16 a[176:177], v20 offset:8\n"
      "ds_read_b64_tr_b16 a[178:179], v20 offset:1544\n"
      "ds_read_b64_tr_b16 v[40:41], v21 offset:0\n"
      "ds_read_b64_tr_b16 v[42:43], v21 offset:1536\n"
      "buffer_load_dwordx4 v178, s[12:15], s63 offen offset:0 lds\n"
      "buffer_load_dwordx4 v179, s[12:15], s63 offen offset:1024 lds\n"
      "buffer_load_dwordx4 v180, s[12:15], s63 offen offset:2048 lds\n"
      "s_sub_u32 s62, s62, 1\n"
      ".Ld_rs21:\n"
      "s_add_u32 s63, s44, 147456\n"
      "s_cmp_ge_u32 s62, 7\n"
      "s_cselect_b32 s63, s63, s31\n"
      "s_add_u32 m0, s42, 49152\n"
      "s_waitcnt vmcnt(18) lgkmcnt(0)\n"
      "s_barrier\n"
      "ds_read_b64_tr_b16 a[84:85], v20 offset:12288\n"
      "ds_read_b64_tr_b16 a[86:87], v20 offset:13824\n"
      "ds_read_b64_tr_b16 a[180:181], v20 offset:12296\n"
      "ds_read_b64_tr_b16 a[182:183], v20 offset:13832\n"
      "ds_read_b64_tr_b16 v[44:45], v21 offset:12288\n"
      "ds_read_b64_tr_b16 v[46:47], v21 offset:13824\n"
      "buffer_load_dwordx4 v178, s[12:15], s63 offen offset:0 lds\n"
      "buffer_load_dwordx4 v179, s[12:15], s63 offen offset:1024 lds\n"
      "buffer_load_dwordx4 v180, s[12:15], s63 offen offset:2048 lds\n"
      "s_sub_u32 s62, s62, 1\n"
      ".Ld_rs22:\n"
      "s_add_u32 s63, s44, 184320\n"
      "s_cmp_ge_u32 s62, 7\n"
      "s_cselect_b32 s63, s63, s31\n"
      "s_add_u32 m0, s42, 61440\n"
      "s_waitcnt vmcnt(18) lgkmcnt(0)\n"
      "s_barrier\n"
      "ds_read_b64_tr_b16 a[88:89], v20 offset:24576\n"
      "ds_read_b64_tr_b16 a[90:91], v20 offset:26112\n"
      "ds_read_b64_tr_b16 a[184:185], v20 offset:24584\n"
      "ds_read_b64_tr_b16 a[186:187], v20 offset:26120\n"
      "ds_read_b64_tr_b16 v[48:49], v21 offset:24576\n"
      "ds_read_b64_tr_b16 v[50:51], v21 offset:26112\n"
      "buffer_load_dwordx4 v178, s[12:15], s63 offen offset:0 lds\n"
      "buffer_load_dwordx4 v179, s[12:15], s63 offen offset:1024 lds\n"
      "buffer_load_dwordx4 v180, s[12:15], s63 offen offset:2048 lds\n"
      "s_sub_u32 s62, s62, 1\n"
      ".Ld_rs23:\n"
      "s_add_u32 s63, s44, 221184\n"
      "s_cmp_ge_u32 s62, 7\n"
      "s_cselect_b32 s63, s63, s31\n"
      "s_add_u32 m0, s42, 73728\n"
      "s_waitcnt vmcnt(18) lgkmcnt(0)\n"
      "s_barrier\n"
      "ds_read_b64_tr_b16 a[92:93], v20 offset:36864\n"
      "ds_read_b64_tr_b16 a[94:95], v20 offset:38400\n"
      "ds_read_b64_tr_b16 a[188:189], v20 offset:36872\n"
      "ds_read_b64_tr_b16 a[190:191], v20 offset:38408\n"
      "ds_read_b64_tr_b16 v[52:53], v21 offset:36864\n"
      "ds_read_b64_tr_b16 v[54:55], v21 offset:38400\n"
      "buffer_load_dwordx4 v178, s[12:15], s63 offen offset:0 lds\n"
      "buffer_load_dwordx4 v179, s[12:15], s63 offen offset:1024 lds\n"
      "buffer_load_dwordx4 v180, s[12:15], s63 offen offset:2048 lds\n"
      "s_sub_u32 s62, s62, 1\n"
      "s_cbranch_scc0 .Ld_rs0\n"
      ".Ld_re:\n"
      "s_waitcnt vmcnt(0) lgkmcnt(0)\n"
      "v_add_u32 v22, 49152, v0\n"
      "v_add_u32 v23, 98304, v0\n"
      "s_lshr_b32 s58, s42, 10\n"
      "s_mul_i32 s58, s58, 1366\n"
      "s_waitcnt vmcnt(0) lgkmcnt(0)\n"
      "s_mov_b64 exec, 0\n"
      "s_lshr_b32 s22, s58, 12\n"
      ".Ld_pl:\n"
      "s_cmp_eq_u32 s22, 0\n"
      "s_cbranch_scc1 .Ld_p0\n"
      "s_cmp_eq_u32 s22, 1\n"
      "s_cbranch_scc1 .Ld_p1\n"
      "s_branch .Ld_pe\n"
      ".Ld_pr:\n"
      "s_add_u32 s22, s22, 4\n"
      "s_branch .Ld_pl\n"
      ".Ld_pe:\n"
      "s_mov_b64 exec, -1\n"
      "s_barrier\n"
      "v_mov_b32 v14, 0\n"
      "ds_write_b32 v16, v14\n"
      "s_waitcnt lgkmcnt(0)\n"
      "s_barrier\n"
      "s_mov_b32 s6, s50\n"
      "s_add_u32 s36, s36, 4294801408\n"
      "s_add_u32 s53, s37, 0\n"
      "s_add_u32 s52, s37, 4294787584\n"
      "s_add_u32 s37, s37, 4294894080\n"
      "s_mov_b32 s56, 0x40000000\n"
      "s_mov_b32 s57, 0x40000000\n"
      "s_mov_b32 s59, 63\n"
      "s_cmp_eq_u32 s45, 1\n"
      "s_cbranch_scc1 .Ld_in1\n"
      "s_cmp_eq_u32 s45, 2\n"
      "s_cbranch_scc1 .Ld_in2\n"
      "s_cmp_eq_u32 s45, 3\n"
      "s_cbranch_scc1 .Ld_in3\n"
      "s_cmp_eq_u32 s45, 4\n"
      "s_cbranch_scc1 .Ld_in4\n"
      "s_cmp_eq_u32 s45, 5\n"
      "s_cbranch_scc1 .Ld_in5\n"
      "s_add_u32 s36, s36, 165888\n"
      "s_min_u32 s36, s36, s6\n"
      "buffer_load_dword v20, v2, s[8:11], s36 offen sc1\n"
      "s_mov_b32 s51, s36\n"
      "s_add_u32 s36, s36, 165888\n"
      "s_min_u32 s36, s36, s6\n"
      "s_add_u32 m0, s43, 0\n"
      "s_waitcnt vmcnt(0)\n"
      "v_cmp_ne_u32 vcc, s21, v20\n"
      "s_cbranch_vccnz .Lsp_d0_1\n"
      ".Lbk_d0_1:\n"
      "buffer_load_dwordx4 v0, s[8:11], s51 offen offset:1024 sc1 lds\n"
      "buffer_load_dwordx4 v0, s[8:11], s51 offen offset:2048 sc1 lds\n"
      "buffer_load_dwordx4 v0, s[8:11], s51 offen offset:0 sc1 lds\n"
      "buffer_load_dword v20, v2, s[8:11], s36 offen sc1\n"
      "buffer_load_dwordx4 v0, s[8:11], s51 offen offset:3072 sc1 lds\n"
      "s_add_u32 m0, s43, 4096\n"
      "s_nop 0\n"
      "buffer_load_dwordx4 v1, s[8:11], s51 offen offset:0 sc1 lds\n"
      "buffer_load_dwordx4 v1, s[8:11], s51 offen offset:1024 sc1 lds\n"
      "s_mov_b32 s51, s36\n"
      "buffer_load_dwordx4 v[216:219], v6, s[8:11], s38 offen sc1\n"
      "s_add_u32 m0, s43, 24576\n"
      "s_add_u32 s36, s36, 165888\n"
      "v_add_u32 v14, 1, v14\n"
      "buffer_load_dwordx2 v[220:221], v7, s[8:11], s38 offen sc1\n"
      "s_waitcnt vmcnt(5)\n"
      "v_cmp_ne_u32 vcc, s21, v20\n"
      "s_cbranch_vccnz .Lsp_d1_2\n"
      ".Lbk_d1_2:\n"
      "buffer_load_dwordx4 v0, s[8:11], s51 offen offset:0 sc1 lds\n"
      "buffer_load_dwordx4 v0, s[8:11], s51 offen offset:1024 sc1 lds\n"
      "s_waitcnt vmcnt(4)\n"
      "ds_write_b32 v16, v14\n"
      "buffer_load_dwordx4 v0, s[8:11], s51 offen offset:2048 sc1 lds\n"
      "buffer_load_dwordx4 v0, s[8:11], s51 offen offset:3072 sc1 lds\n"
      "s_min_u32 s36, s36, s6\n"
      "ds_read_b32 v15, v17\n"
      "buffer_load_dword v20, v2, s[8:11], s36 offen sc1\n"
      "s_add_u32 m0, s43, 28672\n"
      "s_waitcnt lgkmcnt(0)\n"
      "v_cmp_gt_i32 vcc, v14, v15\n"
      "buffer_load_dwordx4 v1, s[8:11], s51 offen offset:0 sc1 lds\n"
      "s_cbranch_vccnz .Lsp_dy0_3\n"
      ".Lbk_dy0_3:\n"
      "ds_read_b128 v[56:59], v0 offset:0\n"
      "ds_read_b128 v[60:63], v0 offset:1024\n"
      "buffer_load_dwordx4 v1, s[8:11], s51 offen offset:1024 sc1 lds\n"
      "ds_read_b128 v[72:75], v0 offset:4096\n"
      "ds_read_b128 v[68:71], v0 offset:3072\n"
      "ds_read_b128 v[92:95], v0 offset:9216\n"
      "ds_read_b128 v[76:79], v0 offset:5120\n"
      "ds_read_b128 v[64:67], v0 offset:2048\n"
      "ds_read_b128 v[80:83], v0 offset:6144\n"
      "ds_read_b128 v[104:107], v0 offset:12288\n"
      "ds_read_b128 v[88:91], v0 offset:8192\n"
      "s_mov_b32 s51, s36\n"
      "ds_read_b128 v[84:87], v0 offset:7168\n"
      "ds_read_b128 v[112:115], v0 offset:14336\n"
      "ds_read_b128 v[100:103], v0 offset:11264\n"
      "ds_read_b128 v[96:99], v0 offset:10240\n"
      "s_waitcnt vmcnt(0) lgkmcnt(0)\n"
      "s_nop 4\n"
      "s_branch .Ld_t0\n"
      ".p2align 12\n"
      ".Ld_p0:\n"
      "s_branch .Ld_pr\n"
      ".p2align 5\n"
      ".Ld_t0:\n"
      "v_mfma_f32_16x16x32_bf16 v[152:155], a[0:3], v[56:59], 0\n"
      "ds_read_b128 v[136:139], v0 offset:20480\n"
      "v_add_f32 v171, v171, v211\n"
      "v_mfma_f32_16x16x32_bf16 v[152:155], a[4:7], v[60:63], v[152:155]\n"
      "s_waitcnt lgkmcnt(4)\n"
      "ds_read_b128 v[120:123], v0 offset:16384\n"
      "v_fmac_f32 v237, v171, v171\n"
      "v_mfma_f32_16x16x32_bf16 v[152:155], a[8:11], v[64:67], v[152:155]\n"
      "ds_read_b128 v[108:111], v0 offset:13312\n"
      "v_add_f32 v174, v174, v233\n"
      "v_mfma_f32_16x16x32_bf16 v[152:155], a[12:15], v[68:71], v[152:155]\n"
      "v_add_f32 v175, v175, v205\n"
      "v_cvt_pk_bf16_f32 v228, v160, v161\n"
      "v_mfma_f32_16x16x32_bf16 v[152:155], a[16:19], v[72:75], v[152:155]\n"
      "ds_read_b128 v[124:127], v0 offset:17408\n"
      "v_cvt_pk_bf16_f32 v229, v162, v163\n"
      "v_mfma_f32_16x16x32_bf16 v[152:155], a[20:23], v[76:79], v[152:155]\n"
      "buffer_store_dwordx2 v[228:229], v13, s[8:11], s37 offen sc1\n"
      "v_mfma_f32_16x16x32_bf16 v[152:155], a[24:27], v[80:83], v[152:155]\n"
      "s_add_u32 s38, s38, 147456\n"
      "ds_read_b128 v[116:119], v0 offset:15360\n"
      "v_add_f32 v175, v175, v213\n"
      "v_mfma_f32_16x16x32_bf16 v[152:155], a[28:31], v[84:87], v[152:155]\n"
      "v_cvt_pk_bf16_f32 v179, v170, v171\n"
      "s_add_u32 s36, s36, 165888\n"
      "v_mfma_f32_16x16x32_bf16 v[152:155], a[32:35], v[88:91], v[152:155]\n"
      "ds_read_b128 v[128:131], v0 offset:18432\n"
      "v_cvt_pk_bf16_f32 v178, v168, v169\n"
      "v_mfma_f32_16x16x32_bf16 v[152:155], a[36:39], v[92:95], v[152:155]\n"
      "s_waitcnt lgkmcnt(3)\n"
      "buffer_store_dwordx4 v[176:179], v6, s[8:11], s56 offen sc1\n"
      "v_mfma_f32_16x16x32_bf16 v[152:155], a[40:43], v[96:99], v[152:155]\n"
      "ds_read_b128 v[140:143], v0 offset:21504\n"
      "v_fmac_f32 v237, v172, v172\n"
      "v_mfma_f32_16x16x32_bf16 v[152:155], a[44:47], v[100:103], v[152:155]\n"
      "ds_read_b128 v[132:135], v0 offset:19456\n"
      "v_add_f32 v236, v236, v171\n"
      "v_mfma_f32_16x16x32_bf16 v[152:155], a[48:51], v[104:107], v[152:155]\n"
      "v_add_f32 v236, v236, v172\n"
      "v_add_f32 v236, v236, v173\n"
      "v_mfma_f32_16x16x32_bf16 v[152:155], a[52:55], v[108:111], v[152:155]\n"
      "v_cvt_pk_bf16_f32 v181, v174, v175\n"
      "v_add_f32 v236, v236, v174\n"
      "v_mfma_f32_16x16x32_bf16 v[152:155], a[56:59], v[112:115], v[152:155]\n"
      "s_waitcnt lgkmcnt(3)\n"
      "ds_read_b128 v[144:147], v0 offset:22528\n"
      "v_and_b32 v165, 0xffff0000, v216\n"
      "v_mfma_f32_16x16x32_bf16 v[152:155], a[60:63], v[116:119], v[152:155]\n"
      "s_min_u32 s36, s36, s6\n"
      "ds_read_b128 v[148:151], v0 offset:23552\n"
      "v_fmac_f32 v237, v173, v173\n"
      "v_mfma_f32_16x16x32_bf16 v[152:155], a[64:67], v[120:123], v[152:155]\n"
      "buffer_store_dwordx2 v[180:181], v7, s[8:11], s56 offen sc1\n"
      "v_mfma_f32_16x16x32_bf16 v[152:155], a[68:71], v[124:127], v[152:155]\n"
      "s_waitcnt lgkmcnt(3)\n"
      "v_fmac_f32 v237, v174, v174\n"
      "v_add_u32 v14, 1, v14\n"
      "v_mfma_f32_16x16x32_bf16 v[152:155], a[72:75], v[128:131], v[152:155]\n"
      "s_waitcnt lgkmcnt(2)\n"
      "v_fmac_f32 v237, v175, v175\n"
      "v_add_f32 v236, v236, v175\n"
      "v_mfma_f32_16x16x32_bf16 v[152:155], a[76:79], v[132:135], v[152:155]\n"
      "ds_write_b64 v10, v[236:237]\n"
      "v_mfma_f32_16x16x32_bf16 v[152:155], a[80:83], v[136:139], v[152:155]\n"
      "buffer_store_dwordx4 v[176:179], v8, s[16:19], s57 offen\n"
      "v_mfma_f32_16x16x32_bf16 v[152:155], a[84:87], v[140:143], v[152:155]\n"
      "s_waitcnt lgkmcnt(2)\n"
      "v_mfma_f32_16x16x32_bf16 v[152:155], a[88:91], v[144:147], v[152:155]\n"
      "s_waitcnt lgkmcnt(0)\n"
      "s_barrier\n"
      "ds_read_b64 v[238:239], v11\n"
      "v_mfma_f32_16x16x32_bf16 v[152:155], a[92:95], v[148:151], v[152:155]\n"
      "s_waitcnt vmcnt(8)\n"
      "s_add_u32 m0, s43, 49152\n"
      "v_cmp_ne_u32 vcc, s21, v20\n"
      "v_mfma_f32_16x16x32_bf16 v[156:159], a[96:99], v[56:59], 0\n"
      "v_mfma_f32_16x16x32_bf16 v[156:159], a[100:103], v[60:63], v[156:159]\n"
      "v_mfma_f32_16x16x32_bf16 v[156:159], a[104:107], v[64:67], v[156:159]\n"
      "s_cbranch_vccnz .Lsp_d2_4\n"
      ".Lbk_d2_4:\n"
      "v_mfma_f32_16x16x32_bf16 v[156:159], a[108:111], v[68:71], v[156:159]\n"
      "buffer_load_dwordx4 v0, s[8:11], s51 offen offset:0 sc1 lds\n"
      "v_mfma_f32_16x16x32_bf16 v[156:159], a[112:115], v[72:75], v[156:159]\n"
      "v_and_b32 v167, 0xffff0000, v217\n"
      "v_mfma_f32_16x16x32_bf16 v[156:159], a[116:119], v[76:79], v[156:159]\n"
      "s_waitcnt lgkmcnt(0)\n"
      "v_add_f32_dpp v238, v238, v238 row_ror:8 row_mask:0xf bank_mask:0xf\n"
      "v_add_f32_dpp v239, v239, v239 row_ror:8 row_mask:0xf bank_mask:0xf\n"
      "v_mfma_f32_16x16x32_bf16 v[156:159], a[120:123], v[80:83], v[156:159]\n"
      "v_add_f32_dpp v238, v238, v238 row_ror:4 row_mask:0xf bank_mask:0xf\n"
      "v_add_f32_dpp v239, v239, v239 row_ror:4 row_mask:0xf bank_mask:0xf\n"
      "v_mfma_f32_16x16x32_bf16 v[156:159], a[124:127], v[84:87], v[156:159]\n"
      "buffer_load_dwordx4 v0, s[8:11], s51 offen offset:3072 sc1 lds\n"
      "v_mfma_f32_16x16x32_bf16 v[156:159], a[128:131], v[88:91], v[156:159]\n"
      "s_waitcnt vmcnt(8)\n"
      "ds_write_b32 v16, v14\n"
      "v_mfma_f32_16x16x32_bf16 v[156:159], a[132:135], v[92:95], v[156:159]\n"
      "v_add_f32_dpp v238, v238, v238 row_ror:2 row_mask:0xf bank_mask:0xf\n"
      "v_mfma_f32_16x16x32_bf16 v[156:159], a[136:139], v[96:99], v[156:159]\n"
      "v_add_f32_dpp v239, v239, v239 row_ror:2 row_mask:0xf bank_mask:0xf\n"
      "v_add_f32_dpp v238, v238, v238 row_ror:1 row_mask:0xf bank_mask:0xf\n"
      "v_mfma_f32_16x16x32_bf16 v[156:159], a[140:143], v[100:103], v[156:159]\n"
      "buffer_load_dword v20, v2, s[8:11], s36 offen sc1\n"
      "v_mfma_f32_16x16x32_bf16 v[156:159], a[144:147], v[104:107], v[156:159]\n"
      "v_add_f32_dpp v239, v239, v239 row_ror:1 row_mask:0xf bank_mask:0xf\n"
      "v_add_f32 v239, 0x3afba882, v239\n"
      "v_mfma_f32_16x16x32_bf16 v[156:159], a[148:151], v[108:111], v[156:159]\n"
      "s_add_u32 s52, s52, 106496\n"
      "v_mul_f32 v239, 0x3ae38e39, v239\n"
      "v_mul_f32 v238, 0x3ae38e39, v238\n"
      "v_mfma_f32_16x16x32_bf16 v[156:159], a[152:155], v[112:115], v[156:159]\n"
      "buffer_store_dwordx2 v[238:239], v12, s[8:11], s56 offen sc1\n"
      "v_mfma_f32_16x16x32_bf16 v[156:159], a[156:159], v[116:119], v[156:159]\n"
      "v_lshlrev_b32 v166, 16, v217\n"
      "v_lshlrev_b32 v164, 16, v216\n"
      "v_mfma_f32_16x16x32_bf16 v[156:159], a[160:163], v[120:123], v[156:159]\n"
      "v_mfma_f32_16x16x32_bf16 v[156:159], a[164:167], v[124:127], v[156:159]\n"
      "buffer_load_dwordx4 v0, s[8:11], s51 offen offset:1024 sc1 lds\n"
      "v_mfma_f32_16x16x32_bf16 v[156:159], a[168:171], v[128:131], v[156:159]\n"
      "ds_read_b32 v15, v17\n"
      "v_mfma_f32_16x16x32_bf16 v[156:159], a[172:175], v[132:135], v[156:159]\n"
      "v_add_f32 v164, v164, v152\n"
      "v_lshlrev_b32 v170, 16, v219\n"
      "v_mfma_f32_16x16x32_bf16 v[156:159], a[176:179], v[136:139], v[156:159]\n"
      "v_lshlrev_b32 v168, 16, v218\n"
      "v_mfma_f32_16x16x32_bf16 v[156:159], a[180:183], v[140:143], v[156:159]\n"
      "buffer_load_dwordx4 v0, s[8:11], s51 offen offset:2048 sc1 lds\n"
      "v_mfma_f32_16x16x32_bf16 v[156:159], a[184:187], v[144:147], v[156:159]\n"
      "s_add_u32 m0, s43, 53248\n"
      "v_mfma_f32_16x16x32_bf16 v[156:159], a[188:191], v[148:151], v[156:159]\n"
      "s_waitcnt lgkmcnt(0)\n"
      "v_cmp_gt_i32 vcc, v14, v15\n"
      "v_mfma_f32_16x16x32_bf16 v[160:163], a[192:195], v[56:59], 0\n"
      "v_mfma_f32_16x16x32_bf16 v[160:163], a[196:199], v[60:63], v[160:163]\n"
      "buffer_load_dwordx4 v1, s[8:11], s51 offen offset:0 sc1 lds\n"
      "v_mfma_f32_16x16x32_bf16 v[160:163], a[200:203], v[64:67], v[160:163]\n"
      "s_cbranch_vccnz .Lsp_dy1_5\n"
      ".Lbk_dy1_5:\n"
      "v_mfma_f32_16x16x32_bf16 v[160:163], a[204:207], v[68:71], v[160:163]\n"
      "v_mfma_f32_16x16x32_bf16 v[160:163], a[208:211], v[72:75], v[160:163]\n"
      "ds_read_b128 v[72:75], v0 offset:28672\n"
      "v_mfma_f32_16x16x32_bf16 v[160:163], a[212:215], v[76:79], v[160:163]\n"
      "buffer_load_dwordx4 v1, s[8:11], s51 offen offset:1024 sc1 lds\n"
      "v_mfma_f32_16x16x32_bf16 v[160:163], a[216:219], v[80:83], v[160:163]\n"
      "v_mfma_f32_16x16x32_bf16 v[160:163], a[220:223], v[84:87], v[160:163]\n"
      "ds_read_b128 v[80:83], v0 offset:30720\n"
      "v_mfma_f32_16x16x32_bf16 v[160:163], a[224:227], v[88:91], v[160:163]\n"
      "ds_read_b128 v[84:87], v0 offset:31744\n"
      "v_mfma_f32_16x16x32_bf16 v[160:163], a[228:231], v[92:95], v[160:163]\n"
      "ds_read_b128 v[68:71], v0 offset:27648\n"
      "v_mfma_f32_16x16x32_bf16 v[160:163], a[232:235], v[96:99], v[160:163]\n"
      "s_waitcnt vmcnt(11)\n"
      "buffer_store_dword v18, v4, s[8:11], s52 offen sc1\n"
      "v_mfma_f32_16x16x32_bf16 v[160:163], a[236:239], v[100:103], v[160:163]\n"
      "v_add_f32 v170, v170, v158\n"
      "v_mfma_f32_16x16x32_bf16 v[160:163], a[240:243], v[104:107], v[160:163]\n"
      "ds_read_b128 v[104:107], v0 offset:36864\n"
      "v_add_f32 v166, v166, v154\n"
      "v_mfma_f32_16x16x32_bf16 v[160:163], a[244:247], v[108:111], v[160:163]\n"
      "ds_read_b128 v[56:59], v0 offset:24576\n"
      "v_mfma_f32_16x16x32_bf16 v[160:163], a[248:251], v[112:115], v[160:163]\n"
      "v_mfma_f32_16x16x32_bf16 v[160:163], a[252:255], v[116:119], v[160:163]\n"
      "ds_read_b128 v[60:63], v0 offset:25600\n"
      "v_add_f32 v167, v167, v155\n"
      "v_mfma_f32_16x16x32_bf16 v[160:163], v[24:27], v[120:123], v[160:163]\n"
      "ds_read_b128 v[116:119], v0 offset:39936\n"
      "v_mfma_f32_16x16x32_bf16 v[160:163], v[28:31], v[124:127], v[160:163]\n"
      "ds_read_b128 v[92:95], v0 offset:33792\n"
      "v_mfma_f32_16x16x32_bf16 v[160:163], v[32:35], v[128:131], v[160:163]\n"
      "ds_read_b128 v[64:67], v0 offset:26624\n"
      "v_add_f32 v168, v168, v156\n"
      "v_mfma_f32_16x16x32_bf16 v[160:163], v[36:39], v[132:135], v[160:163]\n"
      "v_add_f32 v165, v165, v153\n"
      "v_mfma_f32_16x16x32_bf16 v[160:163], v[40:43], v[136:139], v[160:163]\n"
      "ds_read_b128 v[76:79], v0 offset:29696\n"
      "v_mfma_f32_16x16x32_bf16 v[160:163], v[44:47], v[140:143], v[160:163]\n"
      "ds_read_b128 v[100:103], v0 offset:35840\n"
      "v_mfma_f32_16x16x32_bf16 v[160:163], v[48:51], v[144:147], v[160:163]\n"
      "s_waitcnt lgkmcnt(5)\n"
      "ds_read_b128 v[96:99], v0 offset:34816\n"
      "v_mfma_f32_16x16x32_bf16 v[160:163], v[52:55], v[148:151], v[160:163]\n"
      "s_sub_u32 s59, s59, 1\n"
      "s_cbranch_scc1 .Ld_out0\n"
      ".Ld_t1:\n"
      "v_mfma_f32_16x16x32_bf16 v[152:155], a[0:3], v[56:59], 0\n"
      "ds_read_b128 v[88:91], v0 offset:32768\n"
      "v_mfma_f32_16x16x32_bf16 v[152:155], a[4:7], v[60:63], v[152:155]\n"
      "s_waitcnt lgkmcnt(4)\n"
      "ds_read_b128 v[112:115], v0 offset:38912\n"
      "v_and_b32 v171, 0xffff0000, v219\n"
      "v_mfma_f32_16x16x32_bf16 v[152:155], a[8:11], v[64:67], v[152:155]\n"
      "ds_read_b128 v[128:131], v0 offset:43008\n"
      "v_lshlrev_b32 v174, 16, v221\n"
      "v_mfma_f32_16x16x32_bf16 v[152:155], a[12:15], v[68:71], v[152:155]\n"
      "ds_read_b128 v[124:127], v0 offset:41984\n"
      "v_mfma_f32_16x16x32_bf16 v[152:155], a[16:19], v[72:75], v[152:155]\n"
      "s_waitcnt lgkmcnt(5)\n"
      "ds_read_b128 v[132:135], v0 offset:44032\n"
      "v_and_b32 v169, 0xffff0000, v218\n"
      "v_mfma_f32_16x16x32_bf16 v[152:155], a[20:23], v[76:79], v[152:155]\n"
      "ds_read_b128 v[108:111], v0 offset:37888\n"
      "v_mfma_f32_16x16x32_bf16 v[152:155], a[24:27], v[80:83], v[152:155]\n"
      "s_mov_b32 s51, s36\n"
      "v_add_f32 v169, v169, v157\n"
      "v_and_b32 v175, 0xffff0000, v221\n"
      "v_mfma_f32_16x16x32_bf16 v[152:155], a[28:31], v[84:87], v[152:155]\n"
      "s_waitcnt lgkmcnt(5)\n"
      "ds_read_b128 v[140:143], v0 offset:46080\n"
      "v_mfma_f32_16x16x32_bf16 v[152:155], a[32:35], v[88:91], v[152:155]\n"
      "ds_read_b128 v[120:123], v0 offset:40960\n"
      "v_mfma_f32_16x16x32_bf16 v[152:155], a[36:39], v[92:95], v[152:155]\n"
      "ds_read_b128 v[144:147], v0 offset:47104\n"
      "v_mfma_f32_16x16x32_bf16 v[152:155], a[40:43], v[96:99], v[152:155]\n"
      "v_lshlrev_b32 v172, 16, v220\n"
      "v_mfma_f32_16x16x32_bf16 v[152:155], a[44:47], v[100:103], v[152:155]\n"
      "ds_read_b128 v[136:139], v0 offset:45056\n"
      "v_mfma_f32_16x16x32_bf16 v[152:155], a[48:51], v[104:107], v[152:155]\n"
      "s_waitcnt lgkmcnt(4)\n"
      "buffer_store_dwordx2 v[180:181], v9, s[16:19], s57 offen\n"
      "v_mfma_f32_16x16x32_bf16 v[152:155], a[52:55], v[108:111], v[152:155]\n"
      "v_add_f32 v172, v172, v160\n"
      "v_mfma_f32_16x16x32_bf16 v[152:155], a[56:59], v[112:115], v[152:155]\n"
      "s_add_u32 s37, s37, 173056\n"
      "ds_read_b128 v[148:151], v0 offset:48128\n"
      "v_mfma_f32_16x16x32_bf16 v[152:155], a[60:63], v[116:119], v[152:155]\n"
      "s_waitcnt lgkmcnt(3)\n"
      "v_add_u32 v14, 1, v14\n"
      "v_mfma_f32_16x16x32_bf16 v[152:155], a[64:67], v[120:123], v[152:155]\n"
      "v_mfma_f32_16x16x32_bf16 v[152:155], a[68:71], v[124:127], v[152:155]\n"
      "v_mfma_f32_16x16x32_bf16 v[152:155], a[72:75], v[128:131], v[152:155]\n"
      "s_waitcnt vmcnt(6) lgkmcnt(1)\n"
      "buffer_store_dword v18, v5, s[8:11], s56 offen sc1\n"
      "v_mfma_f32_16x16x32_bf16 v[152:155], a[76:79], v[132:135], v[152:155]\n"
      "s_add_u32 m0, s43, 73728\n"
      "v_mfma_f32_16x16x32_bf16 v[152:155], a[80:83], v[136:139], v[152:155]\n"
      "v_cmp_ne_u32 vcc, s21, v20\n"
      "v_mfma_f32_16x16x32_bf16 v[152:155], a[84:87], v[140:143], v[152:155]\n"
      "v_add_f32 v174, v174, v162\n"
      "v_mfma_f32_16x16x32_bf16 v[152:155], a[88:91], v[144:147], v[152:155]\n"
      "s_waitcnt lgkmcnt(0)\n"
      "v_add_f32 v171, v171, v159\n"
      "v_mfma_f32_16x16x32_bf16 v[152:155], a[92:95], v[148:151], v[152:155]\n"
      "s_cbranch_vccnz .Lsp_d3_6\n"
      ".Lbk_d3_6:\n"
      "v_mfma_f32_16x16x32_bf16 v[156:159], a[96:99], v[56:59], 0\n"
      "buffer_load_dwordx4 v0, s[8:11], s51 offen offset:0 sc1 lds\n"
      "v_mfma_f32_16x16x32_bf16 v[156:159], a[100:103], v[60:63], v[156:159]\n"
      "s_add_u32 s36, s36, 165888\n"
      "s_min_u32 s36, s36, s6\n"
      "v_mfma_f32_16x16x32_bf16 v[156:159], a[104:107], v[64:67], v[156:159]\n"
      "v_mfma_f32_16x16x32_bf16 v[156:159], a[108:111], v[68:71], v[156:159]\n"
      "v_mfma_f32_16x16x32_bf16 v[156:159], a[112:115], v[72:75], v[156:159]\n"
      "buffer_load_dwordx4 v0, s[8:11], s51 offen offset:1024 sc1 lds\n"
      "v_mfma_f32_16x16x32_bf16 v[156:159], a[116:119], v[76:79], v[156:159]\n"
      "v_cvt_pk_bf16_f32 v224, v152, v153\n"
      "v_mfma_f32_16x16x32_bf16 v[156:159], a[120:123], v[80:83], v[156:159]\n"
      "v_mfma_f32_16x16x32_bf16 v[156:159], a[124:127], v[84:87], v[156:159]\n"
      "v_mfma_f32_16x16x32_bf16 v[156:159], a[128:131], v[88:91], v[156:159]\n"
      "buffer_load_dword v20, v2, s[8:11], s36 offen sc1\n"
      "v_mfma_f32_16x16x32_bf16 v[156:159], a[132:135], v[92:95], v[156:159]\n"
      "s_waitcnt vmcnt(6)\n"
      "ds_write_b32 v16, v14\n"
      "v_mfma_f32_16x16x32_bf16 v[156:159], a[136:139], v[96:99], v[156:159]\n"
      "v_and_b32 v173, 0xffff0000, v220\n"
      "v_mfma_f32_16x16x32_bf16 v[156:159], a[140:143], v[100:103], v[156:159]\n"
      "v_add_f32 v173, v173, v161\n"
      "v_mfma_f32_16x16x32_bf16 v[156:159], a[144:147], v[104:107], v[156:159]\n"
      "buffer_load_dwordx4 v0, s[8:11], s51 offen offset:3072 sc1 lds\n"
      "v_mfma_f32_16x16x32_bf16 v[156:159], a[148:151], v[108:111], v[156:159]\n"
      "v_mfma_f32_16x16x32_bf16 v[156:159], a[152:155], v[112:115], v[156:159]\n"
      "v_mfma_f32_16x16x32_bf16 v[156:159], a[156:159], v[116:119], v[156:159]\n"
      "v_mfma_f32_16x16x32_bf16 v[156:159], a[160:163], v[120:123], v[156:159]\n"
      "buffer_load_dwordx4 v0, s[8:11], s51 offen offset:2048 sc1 lds\n"
      "v_mfma_f32_16x16x32_bf16 v[156:159], a[164:167], v[124:127], v[156:159]\n"
      "v_mfma_f32_16x16x32_bf16 v[156:159], a[168:171], v[128:131], v[156:159]\n"
      "s_add_u32 m0, s43, 77824\n"
      "ds_read_b32 v15, v17\n"
      "v_mfma_f32_16x16x32_bf16 v[156:159], a[172:175], v[132:135], v[156:159]\n"
      "v_mfma_f32_16x16x32_bf16 v[156:159], a[176:179], v[136:139], v[156:159]\n"
      "buffer_load_dwordx4 v1, s[8:11], s51 offen offset:0 sc1 lds\n"
      "v_mfma_f32_16x16x32_bf16 v[156:159], a[180:183], v[140:143], v[156:159]\n"
      "v_mfma_f32_16x16x32_bf16 v[156:159], a[184:187], v[144:147], v[156:159]\n"
      "v_mfma_f32_16x16x32_bf16 v[156:159], a[188:191], v[148:151], v[156:159]\n"
      "s_waitcnt lgkmcnt(0)\n"
      "v_add_f32_e64 v175, v175, v163\n"
      "v_cmp_gt_i32 vcc, v14, v15\n"
      "v_mfma_f32_16x16x32_bf16 v[160:163], a[192:195], v[56:59], 0\n"
      "v_mfma_f32_16x16x32_bf16 v[160:163], a[196:199], v[60:63], v[160:163]\n"
      "buffer_load_dwordx4 v1, s[8:11], s51 offen offset:1024 sc1 lds\n"
      "v_mfma_f32_16x16x32_bf16 v[160:163], a[200:203], v[64:67], v[160:163]\n"
      "s_cbranch_vccnz .Lsp_dy2_7\n"
      ".Lbk_dy2_7:\n"
      "ds_read_b128 v[60:63], v22 offset:1024\n"
      "v_mfma_f32_16x16x32_bf16 v[160:163], a[204:207], v[68:71], v[160:163]\n"
      "ds_read_b128 v[64:67], v22 offset:2048\n"
      "v_mfma_f32_16x16x32_bf16 v[160:163], a[208:211], v[72:75], v[160:163]\n"
      "v_mfma_f32_16x16x32_bf16 v[160:163], a[212:215], v[76:79], v[160:163]\n"
      "v_mfma_f32_16x16x32_bf16 v[160:163], a[216:219], v[80:83], v[160:163]\n"
      "v_mfma_f32_16x16x32_bf16 v[160:163], a[220:223], v[84:87], v[160:163]\n"
      "v_cvt_pk_bf16_f32 v226, v156, v157\n"
      "v_mfma_f32_16x16x32_bf16 v[160:163], a[224:227], v[88:91], v[160:163]\n"
      "s_mov_b32 s51, s36\n"
      "ds_read_b128 v[84:87], v22 offset:7168\n"
      "v_mfma_f32_16x16x32_bf16 v[160:163], a[228:231], v[92:95], v[160:163]\n"
      "ds_read_b128 v[80:83], v22 offset:6144\n"
      "v_mfma_f32_16x16x32_bf16 v[160:163], a[232:235], v[96:99], v[160:163]\n"
      "ds_read_b128 v[68:71], v22 offset:3072\n"
      "v_mfma_f32_16x16x32_bf16 v[160:163], a[236:239], v[100:103], v[160:163]\n"
      "v_mfma_f32_16x16x32_bf16 v[160:163], a[240:243], v[104:107], v[160:163]\n"
      "v_cvt_pk_bf16_f32 v227, v158, v159\n"
      "v_mfma_f32_16x16x32_bf16 v[160:163], a[244:247], v[108:111], v[160:163]\n"
      "ds_read_b128 v[56:59], v22 offset:0\n"
      "v_mfma_f32_16x16x32_bf16 v[160:163], a[248:251], v[112:115], v[160:163]\n"
      "ds_read_b128 v[72:75], v22 offset:4096\n"
      "v_mfma_f32_16x16x32_bf16 v[160:163], a[252:255], v[116:119], v[160:163]\n"
      "ds_read_b128 v[116:119], v22 offset:15360\n"
      "v_cvt_pk_bf16_f32 v225, v154, v155\n"
      "v_mfma_f32_16x16x32_bf16 v[160:163], v[24:27], v[120:123], v[160:163]\n"
      "ds_read_b128 v[76:79], v22 offset:5120\n"
      "v_mfma_f32_16x16x32_bf16 v[160:163], v[28:31], v[124:127], v[160:163]\n"
      "ds_read_b128 v[104:107], v22 offset:12288\n"
      "v_mfma_f32_16x16x32_bf16 v[160:163], v[32:35], v[128:131], v[160:163]\n"
      "ds_read_b128 v[88:91], v22 offset:8192\n"
      "v_mfma_f32_16x16x32_bf16 v[160:163], v[36:39], v[132:135], v[160:163]\n"
      "buffer_store_dwordx4 v[224:227], v0, s[8:11], s37 offen sc1\n"
      "v_mfma_f32_16x16x32_bf16 v[160:163], v[40:43], v[136:139], v[160:163]\n"
      "ds_read_b128 v[112:115], v22 offset:14336\n"
      "v_mfma_f32_16x16x32_bf16 v[160:163], v[44:47], v[140:143], v[160:163]\n"
      "ds_read_b128 v[108:111], v22 offset:13312\n"
      "v_mfma_f32_16x16x32_bf16 v[160:163], v[48:51], v[144:147], v[160:163]\n"
      "s_waitcnt lgkmcnt(5)\n"
      "ds_read_b128 v[92:95], v22 offset:9216\n"
      "v_mfma_f32_16x16x32_bf16 v[160:163], v[52:55], v[148:151], v[160:163]\n"
      "s_sub_u32 s59, s59, 1\n"
      "s_cbranch_scc1 .Ld_out1\n"
      ".Ld_t2:\n"
      "v_mfma_f32_16x16x32_bf16 v[152:155], a[0:3], v[56:59], 0\n"
      "ds_read_b128 v[100:103], v22 offset:11264\n"
      "v_mfma_f32_16x16x32_bf16 v[152:155], a[4:7], v[60:63], v[152:155]\n"
      "ds_read_b128 v[96:99], v22 offset:10240\n"
      "v_mfma_f32_16x16x32_bf16 v[152:155], a[8:11], v[64:67], v[152:155]\n"
      "v_cvt_pk_bf16_f32 v228, v160, v161\n"
      "v_cvt_pk_bf16_f32 v229, v162, v163\n"
      "v_mfma_f32_16x16x32_bf16 v[152:155], a[12:15], v[68:71], v[152:155]\n"
      "ds_read_b128 v[120:123], v22 offset:16384\n"
      "v_mfma_f32_16x16x32_bf16 v[152:155], a[16:19], v[72:75], v[152:155]\n"
      "s_waitcnt lgkmcnt(4)\n"
      "buffer_store_dwordx2 v[228:229], v13, s[8:11], s37 offen sc1\n"
      "v_mfma_f32_16x16x32_bf16 v[152:155], a[20:23], v[76:79], v[152:155]\n"
      "s_add_u32 m0, s43, 98304\n"
      "v_mfma_f32_16x16x32_bf16 v[152:155], a[24:27], v[80:83], v[152:155]\n"
      "ds_read_b128 v[128:131], v22 offset:18432\n"
      "v_mfma_f32_16x16x32_bf16 v[152:155], a[28:31], v[84:87], v[152:155]\n"
      "ds_read_b128 v[148:151], v22 offset:23552\n"
      "s_add_u32 s37, s37, 106496\n"
      "v_mfma_f32_16x16x32_bf16 v[152:155], a[32:35], v[88:91], v[152:155]\n"
      "s_waitcnt lgkmcnt(3)\n"
      "ds_read_b128 v[136:139], v22 offset:20480\n"
      "v_mfma_f32_16x16x32_bf16 v[152:155], a[36:39], v[92:95], v[152:155]\n"
      "ds_read_b128 v[124:127], v22 offset:17408\n"
      "v_mfma_f32_16x16x32_bf16 v[152:155], a[40:43], v[96:99], v[152:155]\n"
      "ds_read_b128 v[144:147], v22 offset:22528\n"
      "v_add_u32_e64 v14, 1, v14\n"
      "v_mfma_f32_16x16x32_bf16 v[152:155], a[44:47], v[100:103], v[152:155]\n"
      "ds_read_b128 v[132:135], v22 offset:19456\n"
      "v_mfma_f32_16x16x32_bf16 v[152:155], a[48:51], v[104:107], v[152:155]\n"
      "v_mfma_f32_16x16x32_bf16 v[152:155], a[52:55], v[108:111], v[152:155]\n"
      "ds_read_b128 v[140:143], v22 offset:21504\n"
      "v_mfma_f32_16x16x32_bf16 v[152:155], a[56:59], v[112:115], v[152:155]\n"
      "v_mfma_f32_16x16x32_bf16 v[152:155], a[60:63], v[116:119], v[152:155]\n"
      "s_waitcnt lgkmcnt(4)\n"
      "v_mfma_f32_16x16x32_bf16 v[152:155], a[64:67], v[120:123], v[152:155]\n"
      "s_waitcnt lgkmcnt(3)\n"
      "v_mfma_f32_16x16x32_bf16 v[152:155], a[68:71], v[124:127], v[152:155]\n"
      "v_mfma_f32_16x16x32_bf16 v[152:155], a[72:75], v[128:131], v[152:155]\n"
      "s_waitcnt vmcnt(6) lgkmcnt(1)\n"
      "v_cmp_ne_u32 vcc, s21, v20\n"
      "v_mfma_f32_16x16x32_bf16 v[152:155], a[76:79], v[132:135], v[152:155]\n"
      "v_mfma_f32_16x16x32_bf16 v[152:155], a[80:83], v[136:139], v[152:155]\n"
      "s_waitcnt lgkmcnt(0)\n"
      "v_mfma_f32_16x16x32_bf16 v[152:155], a[84:87], v[140:143], v[152:155]\n"
      "v_mfma_f32_16x16x32_bf16 v[152:155], a[88:91], v[144:147], v[152:155]\n"
      "s_cbranch_vccnz .Lsp_d4_8\n"
      ".Lbk_d4_8:\n"
      "v_mfma_f32_16x16x32_bf16 v[152:155], a[92:95], v[148:151], v[152:155]\n"
      "buffer_load_dwordx4 v0, s[8:11], s51 offen offset:2048 sc1 lds\n"
      "v_mfma_f32_16x16x32_bf16 v[156:159], a[96:99], v[56:59], 0\n"
      "s_add_u32 s36, s36, 165888\n"
      "v_mfma_f32_16x16x32_bf16 v[156:159], a[100:103], v[60:63], v[156:159]\n"
      "v_mfma_f32_16x16x32_bf16 v[156:159], a[104:107], v[64:67], v[156:159]\n"
      "v_mfma_f32_16x16x32_bf16 v[156:159], a[108:111], v[68:71], v[156:159]\n"
      "v_mfma_f32_16x16x32_bf16 v[156:159], a[112:115], v[72:75], v[156:159]\n"
      "buffer_load_dwordx4 v0, s[8:11], s51 offen offset:1024 sc1 lds\n"
      "v_mfma_f32_16x16x32_bf16 v[156:159], a[116:119], v[76:79], v[156:159]\n"
      "v_mfma_f32_16x16x32_bf16 v[156:159], a[120:123], v[80:83], v[156:159]\n"
      "s_min_u32 s36, s36, s6\n"
      "v_mfma_f32_16x16x32_bf16 v[156:159], a[124:127], v[84:87], v[156:159]\n"
      "s_waitcnt vmcnt(4)\n"
      "ds_write_b32 v16, v14\n"
      "v_mfma_f32_16x16x32_bf16 v[156:159], a[128:131], v[88:91], v[156:159]\n"
      "buffer_load_dword v20, v2, s[8:11], s36 offen sc1\n"
      "v_mfma_f32_16x16x32_bf16 v[156:159], a[132:135], v[92:95], v[156:159]\n"
      "v_mfma_f32_16x16x32_bf16 v[156:159], a[136:139], v[96:99], v[156:159]\n"
      "v_mfma_f32_16x16x32_bf16 v[156:159], a[140:143], v[100:103], v[156:159]\n"
      "buffer_load_dwordx4 v0, s[8:11], s51 offen offset:0 sc1 lds\n"
      "v_mfma_f32_16x16x32_bf16 v[156:159], a[144:147], v[104:107], v[156:159]\n"
      "s_add_u32 s52, s52, 173056\n"
      "v_mfma_f32_16x16x32_bf16 v[156:159], a[148:151], v[108:111], v[156:159]\n"
      "v_mfma_f32_16x16x32_bf16 v[156:159], a[152:155], v[112:115], v[156:159]\n"
      "v_mfma_f32_16x16x32_bf16 v[156:159], a[156:159], v[116:119], v[156:159]\n"
      "buffer_load_dwordx4 v0, s[8:11], s51 offen offset:3072 sc1 lds\n"
      "v_mfma_f32_16x16x32_bf16 v[156:159], a[160:163], v[120:123], v[156:159]\n"
      "s_add_u32 m0, s43, 102400\n"
      "ds_read_b32 v15, v17\n"
      "v_mfma_f32_16x16x32_bf16 v[156:159], a[164:167], v[124:127], v[156:159]\n"
      "v_mfma_f32_16x16x32_bf16 v[156:159], a[168:171], v[128:131], v[156:159]\n"
      "v_mfma_f32_16x16x32_bf16 v[156:159], a[172:175], v[132:135], v[156:159]\n"
      "buffer_load_dwordx4 v1, s[8:11], s51 offen offset:0 sc1 lds\n"
      "v_mfma_f32_16x16x32_bf16 v[156:159], a[176:179], v[136:139], v[156:159]\n"
      "v_mfma_f32_16x16x32_bf16 v[156:159], a[180:183], v[140:143], v[156:159]\n"
      "s_waitcnt lgkmcnt(0)\n"
      "v_cmp_gt_i32 vcc, v14, v15\n"
      "v_mfma_f32_16x16x32_bf16 v[156:159], a[184:187], v[144:147], v[156:159]\n"
      "v_mfma_f32_16x16x32_bf16 v[156:159], a[188:191], v[148:151], v[156:159]\n"
      "buffer_load_dwordx4 v1, s[8:11], s51 offen offset:1024 sc1 lds\n"
      "v_mfma_f32_16x16x32_bf16 v[160:163], a[192:195], v[56:59], 0\n"
      "s_cbranch_vccnz .Lsp_dy3_9\n"
      ".Lbk_dy3_9:\n"
      "v_mfma_f32_16x16x32_bf16 v[160:163], a[196:199], v[60:63], v[160:163]\n"
      "v_mfma_f32_16x16x32_bf16 v[160:163], a[200:203], v[64:67], v[160:163]\n"
      "ds_read_b128 v[64:67], v22 offset:26624\n"
      "v_mfma_f32_16x16x32_bf16 v[160:163], a[204:207], v[68:71], v[160:163]\n"
      "v_mfma_f32_16x16x32_bf16 v[160:163], a[208:211], v[72:75], v[160:163]\n"
      "ds_read_b128 v[56:59], v22 offset:24576\n"
      "v_mfma_f32_16x16x32_bf16 v[160:163], a[212:215], v[76:79], v[160:163]\n"
      "s_mov_b32 s51, s36\n"
      "v_mfma_f32_16x16x32_bf16 v[160:163], a[216:219], v[80:83], v[160:163]\n"
      "v_cvt_pk_bf16_f32 v224, v152, v153\n"
      "v_mfma_f32_16x16x32_bf16 v[160:163], a[220:223], v[84:87], v[160:163]\n"
      "ds_read_b128 v[72:75], v22 offset:28672\n"
      "v_mfma_f32_16x16x32_bf16 v[160:163], a[224:227], v[88:91], v[160:163]\n"
      "s_waitcnt vmcnt(7)\n"
      "buffer_store_dword v18, v4, s[8:11], s52 offen sc1\n"
      "v_mfma_f32_16x16x32_bf16 v[160:163], a[228:231], v[92:95], v[160:163]\n"
      "v_mfma_f32_16x16x32_bf16 v[160:163], a[232:235], v[96:99], v[160:163]\n"
      "ds_read_b128 v[68:71], v22 offset:27648\n"
      "v_cvt_pk_bf16_f32 v226, v156, v157\n"
      "v_mfma_f32_16x16x32_bf16 v[160:163], a[236:239], v[100:103], v[160:163]\n"
      "ds_read_b128 v[60:63], v22 offset:25600\n"
      "v_mfma_f32_16x16x32_bf16 v[160:163], a[240:243], v[104:107], v[160:163]\n"
      "buffer_load_dword v21, v3, s[8:11], s53 offen sc1\n"
      "v_mfma_f32_16x16x32_bf16 v[160:163], a[244:247], v[108:111], v[160:163]\n"
      "ds_read_b128 v[84:87], v22 offset:31744\n"
      "v_mfma_f32_16x16x32_bf16 v[160:163], a[248:251], v[112:115], v[160:163]\n"
      "ds_read_b128 v[80:83], v22 offset:30720\n"
      "v_mfma_f32_16x16x32_bf16 v[160:163], a[252:255], v[116:119], v[160:163]\n"
      "ds_read_b128 v[88:91], v22 offset:32768\n"
      "v_cvt_pk_bf16_f32 v227, v158, v159\n"
      "v_mfma_f32_16x16x32_bf16 v[160:163], v[24:27], v[120:123], v[160:163]\n"
      "ds_read_b128 v[112:115], v22 offset:38912\n"
      "v_cvt_pk_bf16_f32 v225, v154, v155\n"
      "v_mfma_f32_16x16x32_bf16 v[160:163], v[28:31], v[124:127], v[160:163]\n"
      "ds_read_b128 v[104:107], v22 offset:36864\n"
      "v_mfma_f32_16x16x32_bf16 v[160:163], v[32:35], v[128:131], v[160:163]\n"
      "ds_read_b128 v[76:79], v22 offset:29696\n"
      "v_mfma_f32_16x16x32_bf16 v[160:163], v[36:39], v[132:135], v[160:163]\n"
      "ds_read_b128 v[100:103], v22 offset:35840\n"
      "v_mfma_f32_16x16x32_bf16 v[160:163], v[40:43], v[136:139], v[160:163]\n"
      "ds_read_b128 v[116:119], v22 offset:39936\n"
      "v_mfma_f32_16x16x32_bf16 v[160:163], v[44:47], v[140:143], v[160:163]\n"
      "ds_read_b128 v[136:139], v22 offset:45056\n"
      "v_mfma_f32_16x16x32_bf16 v[160:163], v[48:51], v[144:147], v[160:163]\n"
      "s_waitcnt lgkmcnt(6)\n"
      "ds_read_b128 v[132:135], v22 offset:44032\n"
      "v_mfma_f32_16x16x32_bf16 v[160:163], v[52:55], v[148:151], v[160:163]\n"
      "s_sub_u32 s59, s59, 1\n"
      "s_cbranch_scc1 .Ld_out2\n"
      ".Ld_t3:\n"
      "v_mfma_f32_16x16x32_bf16 v[152:155], a[0:3], v[56:59], 0\n"
      "ds_read_b128 v[92:95], v22 offset:33792\n"
      "v_mfma_f32_16x16x32_bf16 v[152:155], a[4:7], v[60:63], v[152:155]\n"
      "ds_read_b128 v[124:127], v22 offset:41984\n"
      "v_mfma_f32_16x16x32_bf16 v[152:155], a[8:11], v[64:67], v[152:155]\n"
      "s_add_u32 s36, s36, 165888\n"
      "ds_read_b128 v[96:99], v22 offset:34816\n"
      "v_mfma_f32_16x16x32_bf16 v[152:155], a[12:15], v[68:71], v[152:155]\n"
      "buffer_store_dwordx4 v[224:227], v0, s[8:11], s37 offen sc1\n"
      "v_mfma_f32_16x16x32_bf16 v[152:155], a[16:19], v[72:75], v[152:155]\n"
      "s_waitcnt lgkmcnt(4)\n"
      "v_mfma_f32_16x16x32_bf16 v[152:155], a[20:23], v[76:79], v[152:155]\n"
      "ds_read_b128 v[108:111], v22 offset:37888\n"
      "v_cvt_pk_bf16_f32 v229, v162, v163\n"
      "v_mfma_f32_16x16x32_bf16 v[152:155], a[24:27], v[80:83], v[152:155]\n"
      "ds_read_b128 v[128:131], v22 offset:43008\n"
      "v_mfma_f32_16x16x32_bf16 v[152:155], a[28:31], v[84:87], v[152:155]\n"
      "ds_read_b128 v[120:123], v22 offset:40960\n"
      "v_mfma_f32_16x16x32_bf16 v[152:155], a[32:35], v[88:91], v[152:155]\n"
      "s_waitcnt lgkmcnt(4)\n"
      "ds_read_b128 v[148:151], v22 offset:48128\n"
      "v_cvt_pk_bf16_f32 v228, v160, v161\n"
      "v_mfma_f32_16x16x32_bf16 v[152:155], a[36:39], v[92:95], v[152:155]\n"
      "s_waitcnt lgkmcnt(4)\n"
      "v_mfma_f32_16x16x32_bf16 v[152:155], a[40:43], v[96:99], v[152:155]\n"
      "buffer_store_dwordx2 v[228:229], v13, s[8:11], s37 offen sc1\n"
      "v_mfma_f32_16x16x32_bf16 v[152:155], a[44:47], v[100:103], v[152:155]\n"
      "ds_read_b128 v[140:143], v22 offset:46080\n"
      "v_mfma_f32_16x16x32_bf16 v[152:155], a[48:51], v[104:107], v[152:155]\n"
      "s_waitcnt lgkmcnt(4)\n"
      "v_mfma_f32_16x16x32_bf16 v[152:155], a[52:55], v[108:111], v[152:155]\n"
      "s_waitcnt vmcnt(8) lgkmcnt(3)\n"
      "v_cmp_ne_u32 vcc, s21, v20\n"
      "v_mfma_f32_16x16x32_bf16 v[152:155], a[56:59], v[112:115], v[152:155]\n"
      "ds_read_b128 v[144:147], v22 offset:47104\n"
      "v_mfma_f32_16x16x32_bf16 v[152:155], a[60:63], v[116:119], v[152:155]\n"
      "s_waitcnt lgkmcnt(2)\n"
      "v_add_u32 v14, 1, v14\n"
      "v_mfma_f32_16x16x32_bf16 v[152:155], a[64:67], v[120:123], v[152:155]\n"
      "v_mfma_f32_16x16x32_bf16 v[152:155], a[68:71], v[124:127], v[152:155]\n"
      "v_mfma_f32_16x16x32_bf16 v[152:155], a[72:75], v[128:131], v[152:155]\n"
      "s_add_u32 m0, s43, 122880\n"
      "v_mfma_f32_16x16x32_bf16 v[152:155], a[76:79], v[132:135], v[152:155]\n"
      "v_mfma_f32_16x16x32_bf16 v[152:155], a[80:83], v[136:139], v[152:155]\n"
      "s_waitcnt lgkmcnt(1)\n"
      "s_cbranch_vccnz .Lsp_d5_10\n"
      ".Lbk_d5_10:\n"
      "v_mfma_f32_16x16x32_bf16 v[152:155], a[84:87], v[140:143], v[152:155]\n"
      "s_waitcnt lgkmcnt(0)\n"
      "buffer_load_dwordx4 v0, s[8:11], s51 offen offset:2048 sc1 lds\n"
      "v_mfma_f32_16x16x32_bf16 v[152:155], a[88:91], v[144:147], v[152:155]\n"
      "v_mfma_f32_16x16x32_bf16 v[152:155], a[92:95], v[148:151], v[152:155]\n"
      "v_mfma_f32_16x16x32_bf16 v[156:159], a[96:99], v[56:59], 0\n"
      "v_mfma_f32_16x16x32_bf16 v[156:159], a[100:103], v[60:63], v[156:159]\n"
      "buffer_load_dwordx4 v0, s[8:11], s51 offen offset:0 sc1 lds\n"
      "v_mfma_f32_16x16x32_bf16 v[156:159], a[104:107], v[64:67], v[156:159]\n"
      "v_mfma_f32_16x16x32_bf16 v[156:159], a[108:111], v[68:71], v[156:159]\n"
      "s_waitcnt vmcnt(6)\n"
      "ds_write_b32 v16, v14\n"
      "v_mfma_f32_16x16x32_bf16 v[156:159], a[112:115], v[72:75], v[156:159]\n"
      "v_mfma_f32_16x16x32_bf16 v[156:159], a[116:119], v[76:79], v[156:159]\n"
      "buffer_load_dwordx4 v0, s[8:11], s51 offen offset:1024 sc1 lds\n"
      "v_mfma_f32_16x16x32_bf16 v[156:159], a[120:123], v[80:83], v[156:159]\n"
      "v_mfma_f32_16x16x32_bf16 v[156:159], a[124:127], v[84:87], v[156:159]\n"
      "v_mfma_f32_16x16x32_bf16 v[156:159], a[128:131], v[88:91], v[156:159]\n"
      "s_min_u32 s36, s36, s6\n"
      "v_mfma_f32_16x16x32_bf16 v[156:159], a[132:135], v[92:95], v[156:159]\n"
      "buffer_load_dwordx4 v0, s[8:11], s51 offen offset:3072 sc1 lds\n"
      "v_mfma_f32_16x16x32_bf16 v[156:159], a[136:139], v[96:99], v[156:159]\n"
      "s_add_u32 m0, s43, 126976\n"
      "v_mfma_f32_16x16x32_bf16 v[156:159], a[140:143], v[100:103], v[156:159]\n"
      "v_mfma_f32_16x16x32_bf16 v[156:159], a[144:147], v[104:107], v[156:159]\n"
      "ds_read_b32 v15, v17\n"
      "v_mfma_f32_16x16x32_bf16 v[156:159], a[148:151], v[108:111], v[156:159]\n"
      "v_mfma_f32_16x16x32_bf16 v[156:159], a[152:155], v[112:115], v[156:159]\n"
      "buffer_load_dword v20, v2, s[8:11], s36 offen sc1\n"
      "v_mfma_f32_16x16x32_bf16 v[156:159], a[156:159], v[116:119], v[156:159]\n"
      "s_add_u32 s52, s52, 106496\n"
      "v_mfma_f32_16x16x32_bf16 v[156:159], a[160:163], v[120:123], v[156:159]\n"
      "buffer_load_dwordx4 v1, s[8:11], s51 offen offset:0 sc1 lds\n"
      "v_mfma_f32_16x16x32_bf16 v[156:159], a[164:167], v[124:127], v[156:159]\n"
      "s_waitcnt lgkmcnt(0)\n"
      "v_cmp_gt_i32 vcc, v14, v15\n"
      "v_mfma_f32_16x16x32_bf16 v[156:159], a[168:171], v[128:131], v[156:159]\n"
      "v_cvt_pk_bf16_f32 v224, v152, v153\n"
      "v_mfma_f32_16x16x32_bf16 v[156:159], a[172:175], v[132:135], v[156:159]\n"
      "v_mfma_f32_16x16x32_bf16 v[156:159], a[176:179], v[136:139], v[156:159]\n"
      "buffer_load_dwordx4 v1, s[8:11], s51 offen offset:1024 sc1 lds\n"
      "v_mfma_f32_16x16x32_bf16 v[156:159], a[180:183], v[140:143], v[156:159]\n"
      "v_cvt_pk_bf16_f32 v225, v154, v155\n"
      "v_mfma_f32_16x16x32_bf16 v[156:159], a[184:187], v[144:147], v[156:159]\n"
      "s_waitcnt vmcnt(9)\n"
      "s_cbranch_vccnz .Lsp_dy4_11\n"
      ".Lbk_dy4_11:\n"
      "v_cmp_ne_u32 vcc, s21, v21\n"
      "v_mfma_f32_16x16x32_bf16 v[156:159], a[188:191], v[148:151], v[156:159]\n"
      "s_cmp_lt_u32 s41, 32\n"
      "s_cselect_b32 s56, s39, 0x40000000\n"
      "v_mfma_f32_16x16x32_bf16 v[160:163], a[192:195], v[56:59], 0\n"
      ".Ld_p1:\n"
      "s_cbranch_execz .Ld_pr\n"
      "v_mfma_f32_16x16x32_bf16 v[160:163], a[196:199], v[60:63], v[160:163]\n"
      "s_cbranch_vccnz .Lsp_df_12\n"
      ".Lbk_df_12:\n"
      "buffer_load_dwordx4 v[176:179], v0, s[8:11], s53 offen sc1\n"
      "v_mfma_f32_16x16x32_bf16 v[160:163], a[200:203], v[64:67], v[160:163]\n"
      "s_add_u32 s55, s53, 6656\n"
      "ds_read_b128 v[56:59], v23 offset:0\n"
      "v_mfma_f32_16x16x32_bf16 v[160:163], a[204:207], v[68:71], v[160:163]\n"
      "ds_read_b128 v[60:63], v23 offset:1024\n"
      "v_cvt_pk_bf16_f32 v226, v156, v157\n"
      "v_mfma_f32_16x16x32_bf16 v[160:163], a[208:211], v[72:75], v[160:163]\n"
      "ds_read_b128 v[64:67], v23 offset:2048\n"
      "v_mfma_f32_16x16x32_bf16 v[160:163], a[212:215], v[76:79], v[160:163]\n"
      "buffer_load_dwordx2 v[180:181], v13, s[8:11], s53 offen sc1\n"
      "v_mfma_f32_16x16x32_bf16 v[160:163], a[216:219], v[80:83], v[160:163]\n"
      "ds_read_b128 v[76:79], v23 offset:5120\n"
      "v_mfma_f32_16x16x32_bf16 v[160:163], a[220:223], v[84:87], v[160:163]\n"
      "s_add_u32 s37, s37, 106496\n"
      "ds_read_b128 v[68:71], v23 offset:3072\n"
      "v_mfma_f32_16x16x32_bf16 v[160:163], a[224:227], v[88:91], v[160:163]\n"
      "buffer_load_dwordx4 v[184:187], v0, s[8:11], s55 offen sc1\n"
      "v_mfma_f32_16x16x32_bf16 v[160:163], a[228:231], v[92:95], v[160:163]\n"
      "ds_read_b128 v[72:75], v23 offset:4096\n"
      "v_mfma_f32_16x16x32_bf16 v[160:163], a[232:235], v[96:99], v[160:163]\n"
      "ds_read_b128 v[96:99], v23 offset:10240\n"
      "s_add_u32 s54, s53, 13312\n"
      "v_mfma_f32_16x16x32_bf16 v[160:163], a[236:239], v[100:103], v[160:163]\n"
      "ds_read_b128 v[88:91], v23 offset:8192\n"
      "v_cvt_pk_bf16_f32 v227, v158, v159\n"
      "v_mfma_f32_16x16x32_bf16 v[160:163], a[240:243], v[104:107], v[160:163]\n"
      "ds_read_b128 v[100:103], v23 offset:11264\n"
      "s_mov_b32 s51, s36\n"
      "v_mfma_f32_16x16x32_bf16 v[160:163], a[244:247], v[108:111], v[160:163]\n"
      "buffer_load_dwordx2 v[188:189], v13, s[8:11], s55 offen sc1\n"
      "v_mfma_f32_16x16x32_bf16 v[160:163], a[248:251], v[112:115], v[160:163]\n"
      "ds_read_b128 v[92:95], v23 offset:9216\n"
      "v_mfma_f32_16x16x32_bf16 v[160:163], a[252:255], v[116:119], v[160:163]\n"
      "ds_read_b128 v[116:119], v23 offset:15360\n"
      "v_mfma_f32_16x16x32_bf16 v[160:163], v[24:27], v[120:123], v[160:163]\n"
      "s_add_u32 s36, s36, 165888\n"
      "ds_read_b128 v[84:87], v23 offset:7168\n"
      "v_mfma_f32_16x16x32_bf16 v[160:163], v[28:31], v[124:127], v[160:163]\n"
      "s_waitcnt vmcnt(11) lgkmcnt(5)\n"
      "buffer_store_dword v18, v4, s[8:11], s52 offen sc1\n"
      "v_mfma_f32_16x16x32_bf16 v[160:163], v[32:35], v[128:131], v[160:163]\n"
      "v_mfma_f32_16x16x32_bf16 v[160:163], v[36:39], v[132:135], v[160:163]\n"
      "s_add_u32 s55, s53, 19968\n"
      "v_mfma_f32_16x16x32_bf16 v[160:163], v[40:43], v[136:139], v[160:163]\n"
      "buffer_load_dwordx2 v[196:197], v13, s[8:11], s54 offen sc1\n"
      "v_mfma_f32_16x16x32_bf16 v[160:163], v[44:47], v[140:143], v[160:163]\n"
      "ds_read_b128 v[80:83], v23 offset:6144\n"
      "s_add_u32 s58, s41, 4294967264\n"
      "v_mfma_f32_16x16x32_bf16 v[160:163], v[48:51], v[144:147], v[160:163]\n"
      "s_min_u32 s36, s36, s6\n"
      "ds_read_b128 v[120:123], v23 offset:16384\n"
      "v_mfma_f32_16x16x32_bf16 v[160:163], v[52:55], v[148:151], v[160:163]\n"
      "s_sub_u32 s59, s59, 1\n"
      "s_cbranch_scc1 .Ld_out3\n"
      ".Ld_t4:\n"
      "v_mfma_f32_16x16x32_bf16 v[152:155], a[0:3], v[56:59], 0\n"
      "buffer_load_dwordx4 v[192:195], v0, s[8:11], s54 offen sc1\n"
      "v_mfma_f32_16x16x32_bf16 v[152:155], a[4:7], v[60:63], v[152:155]\n"
      "ds_read_b128 v[108:111], v23 offset:13312\n"
      "s_add_u32 s39, s39, 147456\n"
      "v_mfma_f32_16x16x32_bf16 v[152:155], a[8:11], v[64:67], v[152:155]\n"
      "s_cmp_lt_u32 s58, 32\n"
      "s_cselect_b32 s57, s40, 0x40000000\n"
      "s_add_u32 s52, s52, 106496\n"
      "v_mfma_f32_16x16x32_bf16 v[152:155], a[12:15], v[68:71], v[152:155]\n"
      "ds_read_b128 v[104:107], v23 offset:12288\n"
      "v_mfma_f32_16x16x32_bf16 v[152:155], a[16:19], v[72:75], v[152:155]\n"
      "buffer_store_dwordx4 v[224:227], v0, s[8:11], s37 offen sc1\n"
      "v_mfma_f32_16x16x32_bf16 v[152:155], a[20:23], v[76:79], v[152:155]\n"
      "s_waitcnt lgkmcnt(2)\n"
      "ds_read_b128 v[132:135], v23 offset:19456\n"
      "v_cvt_pk_bf16_f32 v229, v162, v163\n"
      "v_mfma_f32_16x16x32_bf16 v[152:155], a[24:27], v[80:83], v[152:155]\n"
      "ds_read_b128 v[112:115], v23 offset:14336\n"
      "v_mfma_f32_16x16x32_bf16 v[152:155], a[28:31], v[84:87], v[152:155]\n"
      "ds_read_b128 v[128:131], v23 offset:18432\n"
      "v_add_u32 v14, 1, v14\n"
      "v_mfma_f32_16x16x32_bf16 v[152:155], a[32:35], v[88:91], v[152:155]\n"
      "ds_read_b128 v[140:143], v23 offset:21504\n"
      "v_cvt_pk_bf16_f32 v228, v160, v161\n"
      "v_mfma_f32_16x16x32_bf16 v[152:155], a[36:39], v[92:95], v[152:155]\n"
      "ds_read_b128 v[124:127], v23 offset:17408\n"
      "v_mfma_f32_16x16x32_bf16 v[152:155], a[40:43], v[96:99], v[152:155]\n"
      "buffer_store_dwordx2 v[228:229], v13, s[8:11], s37 offen sc1\n"
      "v_mfma_f32_16x16x32_bf16 v[152:155], a[44:47], v[100:103], v[152:155]\n"
      "s_waitcnt lgkmcnt(5)\n"
      "ds_read_b128 v[144:147], v23 offset:22528\n"
      "v_mfma_f32_16x16x32_bf16 v[152:155], a[48:51], v[104:107], v[152:155]\n"
      "s_add_u32 m0, s43, 0\n"
      "ds_read_b128 v[136:139], v23 offset:20480\n"
      "v_mfma_f32_16x16x32_bf16 v[152:155], a[52:55], v[108:111], v[152:155]\n"
      "s_waitcnt lgkmcnt(5)\n"
      "s_add_u32 s54, s53, 26624\n"
      "v_mfma_f32_16x16x32_bf16 v[152:155], a[56:59], v[112:115], v[152:155]\n"
      "buffer_load_dwordx4 v[200:203], v0, s[8:11], s55 offen sc1\n"
      "v_mfma_f32_16x16x32_bf16 v[152:155], a[60:63], v[116:119], v[152:155]\n"
      "ds_read_b128 v[148:151], v23 offset:23552\n"
      "s_add_u32 s40, s40, 4294856704\n"
      "v_mfma_f32_16x16x32_bf16 v[152:155], a[64:67], v[120:123], v[152:155]\n"
      "s_waitcnt lgkmcnt(3)\n"
      "s_add_u32 s41, s41, 6\n"
      "v_mfma_f32_16x16x32_bf16 v[152:155], a[68:71], v[124:127], v[152:155]\n"
      "v_mfma_f32_16x16x32_bf16 v[152:155], a[72:75], v[128:131], v[152:155]\n"
      "buffer_load_dwordx4 v[208:211], v0, s[8:11], s54 offen sc1\n"
      "v_mfma_f32_16x16x32_bf16 v[152:155], a[76:79], v[132:135], v[152:155]\n"
      "s_waitcnt lgkmcnt(1)\n"
      "v_mfma_f32_16x16x32_bf16 v[152:155], a[80:83], v[136:139], v[152:155]\n"
      "v_mfma_f32_16x16x32_bf16 v[152:155], a[84:87], v[140:143], v[152:155]\n"
      "s_add_u32 s53, s53, 599040\n"
      "v_mfma_f32_16x16x32_bf16 v[152:155], a[88:91], v[144:147], v[152:155]\n"
      "s_waitcnt lgkmcnt(0)\n"
      "buffer_load_dwordx2 v[212:213], v13, s[8:11], s54 offen sc1\n"
      "v_mfma_f32_16x16x32_bf16 v[152:155], a[92:95], v[148:151], v[152:155]\n"
      "s_waitcnt vmcnt(14)\n"
      "v_cmp_ne_u32 vcc, s21, v20\n"
      "v_mfma_f32_16x16x32_bf16 v[156:159], a[96:99], v[56:59], 0\n"
      "v_mfma_f32_16x16x32_bf16 v[156:159], a[100:103], v[60:63], v[156:159]\n"
      "s_waitcnt vmcnt(12)\n"
      "ds_write_b32 v16, v14\n"
      "v_mfma_f32_16x16x32_bf16 v[156:159], a[104:107], v[64:67], v[156:159]\n"
      "v_mfma_f32_16x16x32_bf16 v[156:159], a[108:111], v[68:71], v[156:159]\n"
      "buffer_load_dwordx2 v[204:205], v13, s[8:11], s55 offen sc1\n"
      "v_mfma_f32_16x16x32_bf16 v[156:159], a[112:115], v[72:75], v[156:159]\n"
      "s_cbranch_vccnz .Lsp_d0_13\n"
      ".Lbk_d0_13:\n"
      "v_mfma_f32_16x16x32_bf16 v[156:159], a[116:119], v[76:79], v[156:159]\n"
      "s_waitcnt vmcnt(12)\n"
      "v_lshlrev_b32 v232, 16, v176\n"
      "v_lshlrev_b32 v233, 16, v177\n"
      "v_mfma_f32_16x16x32_bf16 v[156:159], a[120:123], v[80:83], v[156:159]\n"
      "v_and_b32 v176, 0xffff0000, v176\n"
      "v_add_f32 v166, v166, v233\n"
      "v_mfma_f32_16x16x32_bf16 v[156:159], a[124:127], v[84:87], v[156:159]\n"
      "buffer_load_dwordx4 v0, s[8:11], s51 offen offset:1024 sc1 lds\n"
      "v_mfma_f32_16x16x32_bf16 v[156:159], a[128:131], v[88:91], v[156:159]\n"
      "v_lshlrev_b32 v235, 16, v179\n"
      "v_lshlrev_b32 v234, 16, v178\n"
      "v_mfma_f32_16x16x32_bf16 v[156:159], a[132:135], v[92:95], v[156:159]\n"
      "s_waitcnt vmcnt(12) lgkmcnt(0)\n"
      "v_add_f32 v164, v164, v232\n"
      "v_lshlrev_b32 v232, 16, v180\n"
      "v_mfma_f32_16x16x32_bf16 v[156:159], a[136:139], v[96:99], v[156:159]\n"
      "v_lshlrev_b32 v233, 16, v181\n"
      "v_and_b32 v177, 0xffff0000, v177\n"
      "v_mfma_f32_16x16x32_bf16 v[156:159], a[140:143], v[100:103], v[156:159]\n"
      "buffer_load_dwordx4 v0, s[8:11], s51 offen offset:2048 sc1 lds\n"
      "v_mfma_f32_16x16x32_bf16 v[156:159], a[144:147], v[104:107], v[156:159]\n"
      "v_add_f32 v174, v174, v233\n"
      "v_add_f32 v165, v165, v176\n"
      "v_mfma_f32_16x16x32_bf16 v[156:159], a[148:151], v[108:111], v[156:159]\n"
      "v_add_f32 v167, v167, v177\n"
      "v_and_b32 v181, 0xffff0000, v181\n"
      "v_mfma_f32_16x16x32_bf16 v[156:159], a[152:155], v[112:115], v[156:159]\n"
      "s_waitcnt vmcnt(12)\n"
      "v_lshlrev_b32 v233, 16, v185\n"
      "v_cvt_pk_bf16_f32 v225, v154, v155\n"
      "v_mfma_f32_16x16x32_bf16 v[156:159], a[156:159], v[116:119], v[156:159]\n"
      "buffer_load_dwordx4 v0, s[8:11], s51 offen offset:0 sc1 lds\n"
      "v_mfma_f32_16x16x32_bf16 v[156:159], a[160:163], v[120:123], v[156:159]\n"
      "ds_read_b32 v15, v17\n"
      "v_add_f32 v172, v172, v232\n"
      "v_mfma_f32_16x16x32_bf16 v[156:159], a[164:167], v[124:127], v[156:159]\n"
      "v_and_b32 v180, 0xffff0000, v180\n"
      "v_add_f32 v166, v166, v233\n"
      "v_mfma_f32_16x16x32_bf16 v[156:159], a[168:171], v[128:131], v[156:159]\n"
      "v_add_f32 v168, v168, v234\n"
      "v_lshlrev_b32 v234, 16, v186\n"
      "v_mfma_f32_16x16x32_bf16 v[156:159], a[172:175], v[132:135], v[156:159]\n"
      "buffer_load_dword v20, v2, s[8:11], s36 offen sc1\n"
      "v_mfma_f32_16x16x32_bf16 v[156:159], a[176:179], v[136:139], v[156:159]\n"
      "s_waitcnt vmcnt(13)\n"
      "v_lshlrev_b32 v232, 16, v184\n"
      "v_lshlrev_b32 v233, 16, v189\n"
      "v_mfma_f32_16x16x32_bf16 v[156:159], a[180:183], v[140:143], v[156:159]\n"
      "s_waitcnt lgkmcnt(0)\n"
      "v_and_b32 v179, 0xffff0000, v179\n"
      "v_cmp_gt_i32 vcc, v14, v15\n"
      "v_mfma_f32_16x16x32_bf16 v[156:159], a[184:187], v[144:147], v[156:159]\n"
      "buffer_load_dwordx4 v0, s[8:11], s51 offen offset:3072 sc1 lds\n"
      "v_mfma_f32_16x16x32_bf16 v[156:159], a[188:191], v[148:151], v[156:159]\n"
      "v_and_b32 v184, 0xffff0000, v184\n"
      "v_and_b32 v185, 0xffff0000, v185\n"
      "v_mfma_f32_16x16x32_bf16 v[160:163], a[192:195], v[56:59], 0\n"
      "s_add_u32 m0, s43, 4096\n"
      "v_add_f32 v175, v175, v181\n"
      "v_add_f32_e64 v168, v168, v234\n"
      "v_mfma_f32_16x16x32_bf16 v[160:163], a[196:199], v[60:63], v[160:163]\n"
      "s_cbranch_vccnz .Lsp_dy5_14\n"
      ".Lbk_dy5_14:\n"
      "ds_read_b128 v[60:63], v23 offset:25600\n"
      "v_mfma_f32_16x16x32_bf16 v[160:163], a[200:203], v[64:67], v[160:163]\n"
      "buffer_load_dwordx4 v1, s[8:11], s51 offen offset:0 sc1 lds\n"
      "v_mfma_f32_16x16x32_bf16 v[160:163], a[204:207], v[68:71], v[160:163]\n"
      "v_add_f32 v164, v164, v232\n"
      "v_cvt_pk_bf16_f32 v226, v156, v157\n"
      "v_mfma_f32_16x16x32_bf16 v[160:163], a[208:211], v[72:75], v[160:163]\n"
      "v_add_f32 v165, v165, v184\n"
      "v_add_f32 v174, v174, v233\n"
      "v_mfma_f32_16x16x32_bf16 v[160:163], a[212:215], v[76:79], v[160:163]\n"
      "buffer_load_dwordx4 v1, s[8:11], s51 offen offset:1024 sc1 lds\n"
      "v_mfma_f32_16x16x32_bf16 v[160:163], a[216:219], v[80:83], v[160:163]\n"
      "s_waitcnt vmcnt(13)\n"
      "v_lshlrev_b32 v232, 16, v188\n"
      "v_lshlrev_b32 v233, 16, v193\n"
      "v_mfma_f32_16x16x32_bf16 v[160:163], a[220:223], v[84:87], v[160:163]\n"
      "ds_read_b128 v[76:79], v23 offset:29696\n"
      "v_and_b32 v193, 0xffff0000, v193\n"
      "v_mfma_f32_16x16x32_bf16 v[160:163], a[224:227], v[88:91], v[160:163]\n"
      "ds_read_b128 v[64:67], v23 offset:26624\n"
      "v_cvt_pk_bf16_f32 v224, v152, v153\n"
      "v_mfma_f32_16x16x32_bf16 v[160:163], a[228:231], v[92:95], v[160:163]\n"
      "ds_read_b128 v[92:95], v23 offset:33792\n"
      "v_add_f32 v170, v170, v235\n"
      "v_mfma_f32_16x16x32_bf16 v[160:163], a[232:235], v[96:99], v[160:163]\n"
      "ds_read_b128 v[72:75], v23 offset:28672\n"
      "v_add_f32 v172, v172, v232\n"
      "v_mfma_f32_16x16x32_bf16 v[160:163], a[236:239], v[100:103], v[160:163]\n"
      "ds_read_b128 v[84:87], v23 offset:31744\n"
      "v_lshlrev_b32 v232, 16, v192\n"
      "v_mfma_f32_16x16x32_bf16 v[160:163], a[240:243], v[104:107], v[160:163]\n"
      "ds_read_b128 v[96:99], v23 offset:34816\n"
      "v_lshlrev_b32 v235, 16, v187\n"
      "v_mfma_f32_16x16x32_bf16 v[160:163], a[244:247], v[108:111], v[160:163]\n"
      "ds_read_b128 v[80:83], v23 offset:30720\n"
      "v_add_f32 v166, v166, v233\n"
      "v_mfma_f32_16x16x32_bf16 v[160:163], a[248:251], v[112:115], v[160:163]\n"
      "s_waitcnt vmcnt(11) lgkmcnt(6)\n"
      "buffer_store_dword v18, v4, s[8:11], s52 offen sc1\n"
      "v_mfma_f32_16x16x32_bf16 v[160:163], a[252:255], v[116:119], v[160:163]\n"
      "ds_read_b128 v[56:59], v23 offset:24576\n"
      "v_add_f32 v170, v170, v235\n"
      "v_mfma_f32_16x16x32_bf16 v[160:163], v[24:27], v[120:123], v[160:163]\n"
      "ds_read_b128 v[108:111], v23 offset:37888\n"
      "v_lshlrev_b32 v235, 16, v195\n"
      "v_mfma_f32_16x16x32_bf16 v[160:163], v[28:31], v[124:127], v[160:163]\n"
      "s_add_u32 s37, s37, 106496\n"
      "ds_read_b128 v[88:91], v23 offset:32768\n"
      "v_lshlrev_b32 v233, 16, v197\n"
      "v_mfma_f32_16x16x32_bf16 v[160:163], v[32:35], v[128:131], v[160:163]\n"
      "ds_read_b128 v[68:71], v23 offset:27648\n"
      "v_lshlrev_b32 v234, 16, v194\n"
      "v_mfma_f32_16x16x32_bf16 v[160:163], v[36:39], v[132:135], v[160:163]\n"
      "ds_read_b128 v[100:103], v23 offset:35840\n"
      "v_cvt_pk_bf16_f32 v227, v158, v159\n"
      "v_add_f32 v164, v164, v232\n"
      "v_mfma_f32_16x16x32_bf16 v[160:163], v[40:43], v[136:139], v[160:163]\n"
      "ds_read_b128 v[104:107], v23 offset:36864\n"
      "v_lshlrev_b32 v232, 16, v196\n"
      "v_mfma_f32_16x16x32_bf16 v[160:163], v[44:47], v[140:143], v[160:163]\n"
      "s_mov_b32 s51, s36\n"
      "ds_read_b128 v[128:131], v23 offset:43008\n"
      "v_and_b32 v178, 0xffff0000, v178\n"
      "v_add_f32_e64 v171, v171, v179\n"
      "v_mfma_f32_16x16x32_bf16 v[160:163], v[48:51], v[144:147], v[160:163]\n"
      "s_waitcnt lgkmcnt(6)\n"
      "v_and_b32 v192, 0xffff0000, v192\n"
      "v_add_f32_e64 v168, v168, v234\n"
      "v_mfma_f32_16x16x32_bf16 v[160:163], v[52:55], v[148:151], v[160:163]\n"
      "s_sub_u32 s59, s59, 1\n"
      "s_cbranch_scc1 .Ld_out4\n"
      ".Ld_t5:\n"
      "v_mfma_f32_16x16x32_bf16 v[152:155], a[0:3], v[56:59], 0\n"
      "s_add_u32 s52, s52, 106496\n"
      "ds_read_b128 v[112:115], v23 offset:38912\n"
      "v_add_f32 v174, v174, v233\n"
      "v_mfma_f32_16x16x32_bf16 v[152:155], a[4:7], v[60:63], v[152:155]\n"
      "s_waitcnt vmcnt(11) lgkmcnt(4)\n"
      "ds_read_b128 v[136:139], v23 offset:45056\n"
      "v_lshlrev_b32 v233, 16, v201\n"
      "v_mfma_f32_16x16x32_bf16 v[152:155], a[8:11], v[64:67], v[152:155]\n"
      "buffer_load_dwordx4 v[216:219], v6, s[8:11], s38 offen sc1\n"
      "v_mfma_f32_16x16x32_bf16 v[152:155], a[12:15], v[68:71], v[152:155]\n"
      "s_add_u32 m0, s43, 24576\n"
      "ds_read_b128 v[140:143], v23 offset:46080\n"
      "v_add_f32 v170, v170, v235\n"
      "v_mfma_f32_16x16x32_bf16 v[152:155], a[16:19], v[72:75], v[152:155]\n"
      "ds_read_b128 v[144:147], v23 offset:47104\n"
      "v_and_b32 v186, 0xffff0000, v186\n"
      "v_add_f32 v172, v172, v232\n"
      "v_mfma_f32_16x16x32_bf16 v[152:155], a[20:23], v[76:79], v[152:155]\n"
      "ds_read_b128 v[116:119], v23 offset:39936\n"
      "v_cvt_pk_bf16_f32 v229, v162, v163\n"
      "v_mfma_f32_16x16x32_bf16 v[152:155], a[24:27], v[80:83], v[152:155]\n"
      "buffer_store_dwordx4 v[224:227], v0, s[8:11], s37 offen sc1\n"
      "v_mfma_f32_16x16x32_bf16 v[152:155], a[28:31], v[84:87], v[152:155]\n"
      "ds_read_b128 v[132:135], v23 offset:44032\n"
      "v_add_f32 v166, v166, v233\n"
      "v_mfma_f32_16x16x32_bf16 v[152:155], a[32:35], v[88:91], v[152:155]\n"
      "ds_read_b128 v[120:123], v23 offset:40960\n"
      "v_add_f32 v165, v165, v192\n"
      "v_mfma_f32_16x16x32_bf16 v[152:155], a[36:39], v[92:95], v[152:155]\n"
      "s_add_u32 s36, s36, 165888\n"
      "ds_read_b128 v[124:127], v23 offset:41984\n"
      "v_add_u32 v14, 1, v14\n"
      "v_mfma_f32_16x16x32_bf16 v[152:155], a[40:43], v[96:99], v[152:155]\n"
      "s_waitcnt lgkmcnt(5)\n"
      "v_and_b32 v201, 0xffff0000, v201\n"
      "v_lshlrev_b32 v232, 16, v200\n"
      "v_mfma_f32_16x16x32_bf16 v[152:155], a[44:47], v[100:103], v[152:155]\n"
      "ds_read_b128 v[148:151], v23 offset:48128\n"
      "v_cvt_pk_bf16_f32 v228, v160, v161\n"
      "v_mfma_f32_16x16x32_bf16 v[152:155], a[48:51], v[104:107], v[152:155]\n"
      "buffer_store_dwordx2 v[228:229], v13, s[8:11], s37 offen sc1\n"
      "v_mfma_f32_16x16x32_bf16 v[152:155], a[52:55], v[108:111], v[152:155]\n"
      "s_waitcnt vmcnt(11) lgkmcnt(4)\n"
      "v_add_f32 v164, v164, v232\n"
      "v_lshlrev_b32 v233, 16, v205\n"
      "v_lshlrev_b32 v232, 16, v204\n"
      "v_mfma_f32_16x16x32_bf16 v[152:155], a[56:59], v[112:115], v[152:155]\n"
      "v_add_f32 v172, v172, v232\n"
      "v_add_f32 v174, v174, v233\n"
      "v_mfma_f32_16x16x32_bf16 v[152:155], a[60:63], v[116:119], v[152:155]\n"
      "s_waitcnt lgkmcnt(2)\n"
      "v_and_b32 v204, 0xffff0000, v204\n"
      "v_and_b32 v188, 0xffff0000, v188\n"
      "v_mfma_f32_16x16x32_bf16 v[152:155], a[64:67], v[120:123], v[152:155]\n"
      "s_waitcnt lgkmcnt(1)\n"
      "v_and_b32 v189, 0xffff0000, v189\n"
      "v_lshlrev_b32 v232, 16, v208\n"
      "v_mfma_f32_16x16x32_bf16 v[152:155], a[68:71], v[124:127], v[152:155]\n"
      "v_add_f32 v175, v175, v189\n"
      "v_lshlrev_b32 v234, 16, v202\n"
      "v_mfma_f32_16x16x32_bf16 v[152:155], a[72:75], v[128:131], v[152:155]\n"
      "v_add_f32 v173, v173, v180\n"
      "v_lshlrev_b32 v233, 16, v209\n"
      "v_mfma_f32_16x16x32_bf16 v[152:155], a[76:79], v[132:135], v[152:155]\n"
      "v_add_f32 v164, v164, v232\n"
      "v_mul_f32 v237, v164, v164\n"
      "v_mfma_f32_16x16x32_bf16 v[152:155], a[80:83], v[136:139], v[152:155]\n"
      "buffer_load_dwordx2 v[220:221], v7, s[8:11], s38 offen sc1\n"
      "v_mfma_f32_16x16x32_bf16 v[152:155], a[84:87], v[140:143], v[152:155]\n"
      "v_add_f32 v167, v167, v185\n"
      "v_lshlrev_b32 v235, 16, v203\n"
      "v_mfma_f32_16x16x32_bf16 v[152:155], a[88:91], v[144:147], v[152:155]\n"
      "s_waitcnt lgkmcnt(0)\n"
      "v_add_f32 v173, v173, v188\n"
      "v_and_b32 v208, 0xffff0000, v208\n"
      "v_mfma_f32_16x16x32_bf16 v[152:155], a[92:95], v[148:151], v[152:155]\n"
      "s_waitcnt vmcnt(8)\n"
      "v_cmp_ne_u32 vcc, s21, v20\n"
      "v_add_f32_e64 v169, v169, v178\n"
      "v_mfma_f32_16x16x32_bf16 v[156:159], a[96:99], v[56:59], 0\n"
      "v_add_f32_e64 v167, v167, v193\n"
      "v_and_b32 v209, 0xffff0000, v209\n"
      "v_mfma_f32_16x16x32_bf16 v[156:159], a[100:103], v[60:63], v[156:159]\n"
      "v_and_b32 v187, 0xffff0000, v187\n"
      "v_lshlrev_b32_e64 v232, 16, v212\n"
      "v_mfma_f32_16x16x32_bf16 v[156:159], a[104:107], v[64:67], v[156:159]\n"
      "s_cbranch_vccnz .Lsp_d1_15\n"
      ".Lbk_d1_15:\n"
      "v_and_b32 v205, 0xffff0000, v205\n"
      "v_mfma_f32_16x16x32_bf16 v[156:159], a[108:111], v[68:71], v[156:159]\n"
      "buffer_load_dwordx4 v0, s[8:11], s51 offen offset:0 sc1 lds\n"
      "v_mfma_f32_16x16x32_bf16 v[156:159], a[112:115], v[72:75], v[156:159]\n"
      "v_add_f32 v168, v168, v234\n"
      "v_add_f32 v166, v166, v233\n"
      "v_mfma_f32_16x16x32_bf16 v[156:159], a[116:119], v[76:79], v[156:159]\n"
      "v_lshlrev_b32 v233, 16, v213\n"
      "v_cvt_pk_bf16_f32 v225, v154, v155\n"
      "v_mfma_f32_16x16x32_bf16 v[156:159], a[120:123], v[80:83], v[156:159]\n"
      "v_and_b32 v200, 0xffff0000, v200\n"
      "v_add_f32 v165, v165, v200\n"
      "v_mfma_f32_16x16x32_bf16 v[156:159], a[124:127], v[84:87], v[156:159]\n"
      "buffer_load_dwordx4 v0, s[8:11], s51 offen offset:1024 sc1 lds\n"
      "v_add_f32 v171, v171, v187\n"
      "v_mfma_f32_16x16x32_bf16 v[156:159], a[128:131], v[88:91], v[156:159]\n"
      "v_add_f32 v165, v165, v208\n"
      "v_lshlrev_b32 v234, 16, v210\n"
      "v_add_f32 v167, v167, v201\n"
      "v_mfma_f32_16x16x32_bf16 v[156:159], a[132:135], v[92:95], v[156:159]\n"
      "s_waitcnt vmcnt(7)\n"
      "ds_write_b32 v16, v14\n"
      "v_mfma_f32_16x16x32_bf16 v[156:159], a[136:139], v[96:99], v[156:159]\n"
      "v_and_b32 v194, 0xffff0000, v194\n"
      "v_and_b32 v210, 0xffff0000, v210\n"
      "v_mfma_f32_16x16x32_bf16 v[156:159], a[140:143], v[100:103], v[156:159]\n"
      "buffer_load_dwordx4 v0, s[8:11], s51 offen offset:2048 sc1 lds\n"
      "v_mfma_f32_16x16x32_bf16 v[156:159], a[144:147], v[104:107], v[156:159]\n"
      "v_cvt_pk_bf16_f32 v224, v152, v153\n"
      "v_and_b32 v213, 0xffff0000, v213\n"
      "v_mfma_f32_16x16x32_bf16 v[156:159], a[148:151], v[108:111], v[156:159]\n"
      "v_fmac_f32 v237, v165, v165\n"
      "v_add_f32 v169, v169, v186\n"
      "v_mfma_f32_16x16x32_bf16 v[156:159], a[152:155], v[112:115], v[156:159]\n"
      "v_add_f32 v169, v169, v194\n"
      "v_add_f32 v170, v170, v235\n"
      "v_mfma_f32_16x16x32_bf16 v[156:159], a[156:159], v[116:119], v[156:159]\n"
      "buffer_load_dwordx4 v0, s[8:11], s51 offen offset:3072 sc1 lds\n"
      "v_mfma_f32_16x16x32_bf16 v[156:159], a[160:163], v[120:123], v[156:159]\n"
      "s_add_u32 s37, s37, 106496\n"
      "v_and_b32 v197, 0xffff0000, v197\n"
      "v_add_f32 v167, v167, v209\n"
      "v_mfma_f32_16x16x32_bf16 v[156:159], a[164:167], v[124:127], v[156:159]\n"
      "v_and_b32 v195, 0xffff0000, v195\n"
      "v_add_f32 v236, v164, v165\n"
      "v_mfma_f32_16x16x32_bf16 v[156:159], a[168:171], v[128:131], v[156:159]\n"
      "s_min_u32 s36, s36, s6\n"
      "ds_read_b32 v15, v17\n"
      "v_and_b32 v202, 0xffff0000, v202\n"
      "v_mfma_f32_16x16x32_bf16 v[156:159], a[172:175], v[132:135], v[156:159]\n"
      "buffer_load_dword v20, v2, s[8:11], s36 offen sc1\n"
      "v_mfma_f32_16x16x32_bf16 v[156:159], a[176:179], v[136:139], v[156:159]\n"
      "v_fmac_f32 v237, v166, v166\n"
      "v_lshlrev_b32 v235, 16, v211\n"
      "v_mfma_f32_16x16x32_bf16 v[156:159], a[180:183], v[140:143], v[156:159]\n"
      "s_add_u32 m0, s43, 28672\n"
      "v_and_b32 v196, 0xffff0000, v196\n"
      "v_add_f32 v175, v175, v197\n"
      "v_mfma_f32_16x16x32_bf16 v[156:159], a[184:187], v[144:147], v[156:159]\n"
      "v_add_f32 v170, v170, v235\n"
      "v_add_f32 v172, v172, v232\n"
      "v_mfma_f32_16x16x32_bf16 v[156:159], a[188:191], v[148:151], v[156:159]\n"
      "s_waitcnt lgkmcnt(0)\n"
      "v_and_b32 v212, 0xffff0000, v212\n"
      "v_cmp_gt_i32 vcc, v14, v15\n"
      "v_mfma_f32_16x16x32_bf16 v[160:163], a[192:195], v[56:59], 0\n"
      "buffer_load_dwordx4 v1, s[8:11], s51 offen offset:0 sc1 lds\n"
      "v_mfma_f32_16x16x32_bf16 v[160:163], a[196:199], v[60:63], v[160:163]\n"
      "v_add_f32_e64 v168, v168, v234\n"
      "v_add_f32_e64 v169, v169, v202\n"
      "v_mfma_f32_16x16x32_bf16 v[160:163], a[200:203], v[64:67], v[160:163]\n"
      "s_cbranch_vccnz .Lsp_dy0_16\n"
      ".Lbk_dy0_16:\n"
      "ds_read_b128 v[56:59], v0 offset:0\n"
      "v_mfma_f32_16x16x32_bf16 v[160:163], a[204:207], v[68:71], v[160:163]\n"
      "ds_read_b128 v[60:63], v0 offset:1024\n"
      "v_fmac_f32 v237, v167, v167\n"
      "v_mfma_f32_16x16x32_bf16 v[160:163], a[208:211], v[72:75], v[160:163]\n"
      "buffer_load_dwordx4 v1, s[8:11], s51 offen offset:1024 sc1 lds\n"
      "v_add_f32 v173, v173, v196\n"
      "v_mfma_f32_16x16x32_bf16 v[160:163], a[212:215], v[76:79], v[160:163]\n"
      "v_add_f32 v173, v173, v204\n"
      "v_cvt_pk_bf16_f32 v226, v156, v157\n"
      "v_mfma_f32_16x16x32_bf16 v[160:163], a[216:219], v[80:83], v[160:163]\n"
      "ds_read_b128 v[72:75], v0 offset:4096\n"
      "v_add_f32 v171, v171, v195\n"
      "v_mfma_f32_16x16x32_bf16 v[160:163], a[220:223], v[84:87], v[160:163]\n"
      "v_and_b32 v211, 0xffff0000, v211\n"
      "v_cvt_pk_bf16_f32 v227, v158, v159\n"
      "v_mfma_f32_16x16x32_bf16 v[160:163], a[224:227], v[88:91], v[160:163]\n"
      "ds_read_b128 v[68:71], v0 offset:3072\n"
      "v_add_f32 v173, v173, v212\n"
      "v_mfma_f32_16x16x32_bf16 v[160:163], a[228:231], v[92:95], v[160:163]\n"
      "buffer_store_dwordx4 v[224:227], v0, s[8:11], s37 offen sc1\n"
      "v_mfma_f32_16x16x32_bf16 v[160:163], a[232:235], v[96:99], v[160:163]\n"
      "ds_read_b128 v[92:95], v0 offset:9216\n"
      "v_cvt_pk_bf16_f32 v176, v164, v165\n"
      "v_mfma_f32_16x16x32_bf16 v[160:163], a[236:239], v[100:103], v[160:163]\n"
      "ds_read_b128 v[76:79], v0 offset:5120\n"
      "v_add_f32 v169, v169, v210\n"
      "v_mfma_f32_16x16x32_bf16 v[160:163], a[240:243], v[104:107], v[160:163]\n"
      "ds_read_b128 v[64:67], v0 offset:2048\n"
      "v_add_f32 v236, v236, v166\n"
      "v_mfma_f32_16x16x32_bf16 v[160:163], a[244:247], v[108:111], v[160:163]\n"
      "ds_read_b128 v[80:83], v0 offset:6144\n"
      "ds_read_b128 v[104:107], v0 offset:12288\n"
      "v_mfma_f32_16x16x32_bf16 v[160:163], a[248:251], v[112:115], v[160:163]\n"
      "v_add_f32 v236, v236, v167\n"
      "v_and_b32 v203, 0xffff0000, v203\n"
      "v_mfma_f32_16x16x32_bf16 v[160:163], a[252:255], v[116:119], v[160:163]\n"
      "ds_read_b128 v[88:91], v0 offset:8192\n"
      "v_add_f32 v236, v236, v168\n"
      "v_mfma_f32_16x16x32_bf16 v[160:163], v[24:27], v[120:123], v[160:163]\n"
      "s_waitcnt vmcnt(9) lgkmcnt(6)\n"
      "buffer_store_dword v18, v4, s[8:11], s52 offen sc1\n"
      "v_mfma_f32_16x16x32_bf16 v[160:163], v[28:31], v[124:127], v[160:163]\n"
      "s_mov_b32 s51, s36\n"
      "ds_read_b128 v[84:87], v0 offset:7168\n"
      "v_add_f32 v236, v236, v169\n"
      "v_mfma_f32_16x16x32_bf16 v[160:163], v[32:35], v[128:131], v[160:163]\n"
      "v_fmac_f32 v237, v168, v168\n"
      "v_fmac_f32 v237, v169, v169\n"
      "v_mfma_f32_16x16x32_bf16 v[160:163], v[36:39], v[132:135], v[160:163]\n"
      "ds_read_b128 v[112:115], v0 offset:14336\n"
      "v_cvt_pk_bf16_f32 v180, v172, v173\n"
      "v_mfma_f32_16x16x32_bf16 v[160:163], v[40:43], v[136:139], v[160:163]\n"
      "ds_read_b128 v[100:103], v0 offset:11264\n"
      "v_fmac_f32 v237, v170, v170\n"
      "v_mfma_f32_16x16x32_bf16 v[160:163], v[44:47], v[140:143], v[160:163]\n"
      "ds_read_b128 v[96:99], v0 offset:10240\n"
      "v_add_f32 v236, v236, v170\n"
      "v_mfma_f32_16x16x32_bf16 v[160:163], v[48:51], v[144:147], v[160:163]\n"
      "v_add_f32 v171, v171, v203\n"
      "v_cvt_pk_bf16_f32 v177, v166, v167\n"
      "v_mfma_f32_16x16x32_bf16 v[160:163], v[52:55], v[148:151], v[160:163]\n"
      "s_sub_u32 s59, s59, 1\n"
      "s_cbranch_scc0 .Ld_t0\n"
      ".Ld_out5:\n"
      "v_add_f32 v171, v171, v211\n"
      "v_add_f32 v174, v174, v233\n"
      "v_add_f32 v175, v175, v205\n"
      "s_nop 5\n"
      "v_cvt_pk_bf16_f32 v228, v160, v161\n"
      "v_cvt_pk_bf16_f32 v229, v162, v163\n"
      "buffer_store_dwordx2 v[228:229], v13, s[8:11], s37 offen sc1\n"
      "v_add_f32 v175, v175, v213\n"
      "v_cvt_pk_bf16_f32 v179, v170, v171\n"
      "v_cvt_pk_bf16_f32 v178, v168, v169\n"
      "v_cvt_pk_bf16_f32 v181, v174, v175\n"
      "buffer_store_dwordx4 v[176:179], v8, s[16:19], s57 offen\n"
      "s_add_u32 s52, s52, 106496\n"
      "s_waitcnt vmcnt(1)\n"
      "buffer_store_dword v18, v4, s[8:11], s52 offen sc1\n"
      "buffer_store_dwordx2 v[180:181], v9, s[16:19], s57 offen\n"
      "s_add_u32 s37, s37, 173056\n"
      "s_add_u32 s52, s52, 173056\n"
      "s_branch .Ld_end\n"
      ".Ld_out0:\n"
      "v_and_b32 v171, 0xffff0000, v219\n"
      "v_lshlrev_b32 v174, 16, v221\n"
      "v_and_b32 v169, 0xffff0000, v218\n"
      "v_add_f32 v169, v169, v157\n"
      "v_and_b32 v175, 0xffff0000, v221\n"
      "v_lshlrev_b32 v172, 16, v220\n"
      "buffer_store_dwordx2 v[180:181], v9, s[16:19], s57 offen\n"
      "s_nop 1\n"
      "v_add_f32 v172, v172, v160\n"
      "s_add_u32 s37, s37, 173056\n"
      "v_add_f32 v174, v174, v162\n"
      "v_add_f32 v171, v171, v159\n"
      "v_and_b32 v173, 0xffff0000, v220\n"
      "v_add_f32 v173, v173, v161\n"
      "v_add_f32 v175, v175, v163\n"
      "s_add_u32 s52, s52, 173056\n"
      "buffer_load_dword v21, v3, s[8:11], s53 offen sc1\n"
      "s_waitcnt vmcnt(0)\n"
      "v_cmp_ne_u32 vcc, s21, v21\n"
      "s_cmp_lt_u32 s41, 32\n"
      "s_cselect_b32 s56, s39, 0x40000000\n"
      "s_cbranch_vccnz .Lsp_df_17\n"
      ".Lbk_df_17:\n"
      "buffer_load_dwordx4 v[176:179], v0, s[8:11], s53 offen sc1\n"
      "s_add_u32 s55, s53, 6656\n"
      "buffer_load_dwordx2 v[180:181], v13, s[8:11], s53 offen sc1\n"
      "buffer_load_dwordx4 v[184:187], v0, s[8:11], s55 offen sc1\n"
      "s_add_u32 s54, s53, 13312\n"
      "buffer_load_dwordx2 v[188:189], v13, s[8:11], s55 offen sc1\n"
      "s_add_u32 s55, s53, 19968\n"
      "buffer_load_dwordx2 v[196:197], v13, s[8:11], s54 offen sc1\n"
      "s_add_u32 s58, s41, 4294967264\n"
      "buffer_load_dwordx4 v[192:195], v0, s[8:11], s54 offen sc1\n"
      "s_add_u32 s39, s39, 147456\n"
      "s_cmp_lt_u32 s58, 32\n"
      "s_cselect_b32 s57, s40, 0x40000000\n"
      "s_add_u32 s54, s53, 26624\n"
      "buffer_load_dwordx4 v[200:203], v0, s[8:11], s55 offen sc1\n"
      "s_add_u32 s40, s40, 4294856704\n"
      "s_add_u32 s41, s41, 6\n"
      "buffer_load_dwordx4 v[208:211], v0, s[8:11], s54 offen sc1\n"
      "s_add_u32 s53, s53, 599040\n"
      "buffer_load_dwordx2 v[212:213], v13, s[8:11], s54 offen sc1\n"
      "buffer_load_dwordx2 v[204:205], v13, s[8:11], s55 offen sc1\n"
      "s_waitcnt vmcnt(9)\n"
      "v_lshlrev_b32 v232, 16, v176\n"
      "v_lshlrev_b32 v233, 16, v177\n"
      "v_and_b32 v176, 0xffff0000, v176\n"
      "v_add_f32 v166, v166, v233\n"
      "v_lshlrev_b32 v235, 16, v179\n"
      "v_lshlrev_b32 v234, 16, v178\n"
      "v_add_f32 v164, v164, v232\n"
      "s_waitcnt vmcnt(8)\n"
      "v_lshlrev_b32 v232, 16, v180\n"
      "v_lshlrev_b32 v233, 16, v181\n"
      "v_and_b32 v177, 0xffff0000, v177\n"
      "v_add_f32 v174, v174, v233\n"
      "v_add_f32 v165, v165, v176\n"
      "v_add_f32 v167, v167, v177\n"
      "v_and_b32 v181, 0xffff0000, v181\n"
      "s_waitcnt vmcnt(7)\n"
      "v_lshlrev_b32 v233, 16, v185\n"
      "v_add_f32 v172, v172, v232\n"
      "v_and_b32 v180, 0xffff0000, v180\n"
      "v_add_f32 v166, v166, v233\n"
      "v_add_f32 v168, v168, v234\n"
      "v_lshlrev_b32 v234, 16, v186\n"
      "v_lshlrev_b32 v232, 16, v184\n"
      "s_waitcnt vmcnt(6)\n"
      "v_lshlrev_b32 v233, 16, v189\n"
      "v_and_b32 v179, 0xffff0000, v179\n"
      "v_and_b32 v184, 0xffff0000, v184\n"
      "v_and_b32 v185, 0xffff0000, v185\n"
      "v_add_f32 v175, v175, v181\n"
      "v_add_f32 v168, v168, v234\n"
      "v_add_f32 v164, v164, v232\n"
      "v_add_f32 v165, v165, v184\n"
      "v_add_f32 v174, v174, v233\n"
      "v_lshlrev_b32 v232, 16, v188\n"
      "s_waitcnt vmcnt(4)\n"
      "v_lshlrev_b32 v233, 16, v193\n"
      "v_and_b32 v193, 0xffff0000, v193\n"
      "v_add_f32 v170, v170, v235\n"
      "v_add_f32 v172, v172, v232\n"
      "v_lshlrev_b32 v232, 16, v192\n"
      "v_lshlrev_b32 v235, 16, v187\n"
      "v_add_f32 v166, v166, v233\n"
      "v_add_f32 v170, v170, v235\n"
      "v_lshlrev_b32 v235, 16, v195\n"
      "v_lshlrev_b32 v233, 16, v197\n"
      "v_lshlrev_b32 v234, 16, v194\n"
      "v_add_f32 v164, v164, v232\n"
      "v_lshlrev_b32 v232, 16, v196\n"
      "v_and_b32 v178, 0xffff0000, v178\n"
      "v_add_f32 v171, v171, v179\n"
      "v_and_b32 v192, 0xffff0000, v192\n"
      "v_add_f32 v168, v168, v234\n"
      "v_add_f32 v174, v174, v233\n"
      "s_waitcnt vmcnt(3)\n"
      "v_lshlrev_b32 v233, 16, v201\n"
      "v_add_f32 v170, v170, v235\n"
      "v_and_b32 v186, 0xffff0000, v186\n"
      "v_add_f32 v172, v172, v232\n"
      "v_add_f32 v166, v166, v233\n"
      "v_add_f32 v165, v165, v192\n"
      "v_and_b32 v201, 0xffff0000, v201\n"
      "v_lshlrev_b32 v232, 16, v200\n"
      "v_add_f32 v164, v164, v232\n"
      "s_waitcnt vmcnt(0)\n"
      "v_lshlrev_b32 v233, 16, v205\n"
      "v_lshlrev_b32 v232, 16, v204\n"
      "v_add_f32 v172, v172, v232\n"
      "v_add_f32 v174, v174, v233\n"
      "v_and_b32 v204, 0xffff0000, v204\n"
      "v_and_b32 v188, 0xffff0000, v188\n"
      "v_and_b32 v189, 0xffff0000, v189\n"
      "v_lshlrev_b32 v232, 16, v208\n"
      "v_add_f32 v175, v175, v189\n"
      "v_lshlrev_b32 v234, 16, v202\n"
      "v_add_f32 v173, v173, v180\n"
      "v_lshlrev_b32 v233, 16, v209\n"
      "v_add_f32 v164, v164, v232\n"
      "v_add_f32 v167, v167, v185\n"
      "v_lshlrev_b32 v235, 16, v203\n"
      "v_add_f32 v173, v173, v188\n"
      "v_and_b32 v208, 0xffff0000, v208\n"
      "v_add_f32 v169, v169, v178\n"
      "v_add_f32 v167, v167, v193\n"
      "v_and_b32 v209, 0xffff0000, v209\n"
      "v_and_b32 v187, 0xffff0000, v187\n"
      "v_lshlrev_b32 v232, 16, v212\n"
      "v_and_b32 v205, 0xffff0000, v205\n"
      "v_add_f32 v168, v168, v234\n"
      "v_add_f32 v166, v166, v233\n"
      "v_lshlrev_b32 v233, 16, v213\n"
      "v_and_b32 v200, 0xffff0000, v200\n"
      "v_add_f32 v165, v165, v200\n"
      "v_add_f32 v171, v171, v187\n"
      "v_add_f32 v165, v165, v208\n"
      "v_lshlrev_b32 v234, 16, v210\n"
      "v_add_f32 v167, v167, v201\n"
      "v_and_b32 v194, 0xffff0000, v194\n"
      "v_and_b32 v210, 0xffff0000, v210\n"
      "v_and_b32 v213, 0xffff0000, v213\n"
      "v_add_f32 v169, v169, v186\n"
      "v_add_f32 v169, v169, v194\n"
      "v_add_f32 v170, v170, v235\n"
      "v_and_b32 v197, 0xffff0000, v197\n"
      "v_add_f32 v167, v167, v209\n"
      "v_and_b32 v195, 0xffff0000, v195\n"
      "v_and_b32 v202, 0xffff0000, v202\n"
      "v_lshlrev_b32 v235, 16, v211\n"
      "v_and_b32 v196, 0xffff0000, v196\n"
      "v_add_f32 v175, v175, v197\n"
      "v_add_f32 v170, v170, v235\n"
      "v_add_f32 v172, v172, v232\n"
      "v_and_b32 v212, 0xffff0000, v212\n"
      "v_add_f32 v168, v168, v234\n"
      "v_add_f32 v169, v169, v202\n"
      "v_add_f32 v173, v173, v196\n"
      "v_add_f32 v173, v173, v204\n"
      "v_add_f32 v171, v171, v195\n"
      "v_and_b32 v211, 0xffff0000, v211\n"
      "v_add_f32 v173, v173, v212\n"
      "v_cvt_pk_bf16_f32 v176, v164, v165\n"
      "v_add_f32 v169, v169, v210\n"
      "v_and_b32 v203, 0xffff0000, v203\n"
      "v_cvt_pk_bf16_f32 v180, v172, v173\n"
      "v_add_f32 v171, v171, v203\n"
      "v_cvt_pk_bf16_f32 v177, v166, v167\n"
      "v_add_f32 v171, v171, v211\n"
      "v_add_f32 v174, v174, v233\n"
      "v_add_f32 v175, v175, v205\n"
      "v_add_f32 v175, v175, v213\n"
      "v_cvt_pk_bf16_f32 v179, v170, v171\n"
      "v_cvt_pk_bf16_f32 v178, v168, v169\n"
      "v_cvt_pk_bf16_f32 v181, v174, v175\n"
      "buffer_store_dwordx4 v[176:179], v8, s[16:19], s57 offen\n"
      "buffer_store_dwordx2 v[180:181], v9, s[16:19], s57 offen\n"
      "s_branch .Ld_end\n"
      ".Ld_out1:\n"
      "s_nop 8\n"
      "v_cvt_pk_bf16_f32 v228, v160, v161\n"
      "v_cvt_pk_bf16_f32 v229, v162, v163\n"
      "buffer_store_dwordx2 v[228:229], v13, s[8:11], s37 offen sc1\n"
      "s_add_u32 s37, s37, 106496\n"
      "s_add_u32 s52, s52, 173056\n"
      "s_waitcnt vmcnt(0)\n"
      "buffer_store_dword v18, v4, s[8:11], s52 offen sc1\n"
      "buffer_load_dword v21, v3, s[8:11], s53 offen sc1\n"
      "s_add_u32 s52, s52, 106496\n"
      "s_waitcnt vmcnt(0)\n"
      "v_cmp_ne_u32 vcc, s21, v21\n"
      "s_cmp_lt_u32 s41, 32\n"
      "s_cselect_b32 s56, s39, 0x40000000\n"
      "s_cbranch_vccnz .Lsp_df_18\n"
      ".Lbk_df_18:\n"
      "buffer_load_dwordx4 v[176:179], v0, s[8:11], s53 offen sc1\n"
      "s_add_u32 s55, s53, 6656\n"
      "buffer_load_dwordx2 v[180:181], v13, s[8:11], s53 offen sc1\n"
      "buffer_load_dwordx4 v[184:187], v0, s[8:11], s55 offen sc1\n"
      "s_add_u32 s54, s53, 13312\n"
      "buffer_load_dwordx2 v[188:189], v13, s[8:11], s55 offen sc1\n"
      "s_add_u32 s55, s53, 19968\n"
      "buffer_load_dwordx2 v[196:197], v13, s[8:11], s54 offen sc1\n"
      "s_add_u32 s58, s41, 4294967264\n"
      "buffer_load_dwordx4 v[192:195], v0, s[8:11], s54 offen sc1\n"
      "s_add_u32 s39, s39, 147456\n"
      "s_cmp_lt_u32 s58, 32\n"
      "s_cselect_b32 s57, s40, 0x40000000\n"
      "s_add_u32 s54, s53, 26624\n"
      "buffer_load_dwordx4 v[200:203], v0, s[8:11], s55 offen sc1\n"
      "s_add_u32 s40, s40, 4294856704\n"
      "s_add_u32 s41, s41, 6\n"
      "buffer_load_dwordx4 v[208:211], v0, s[8:11], s54 offen sc1\n"
      "s_add_u32 s53, s53, 599040\n"
      "buffer_load_dwordx2 v[212:213], v13, s[8:11], s54 offen sc1\n"
      "buffer_load_dwordx2 v[204:205], v13, s[8:11], s55 offen sc1\n"
      "s_waitcnt vmcnt(9)\n"
      "v_lshlrev_b32 v232, 16, v176\n"
      "v_lshlrev_b32 v233, 16, v177\n"
      "v_and_b32 v176, 0xffff0000, v176\n"
      "v_add_f32 v166, v166, v233\n"
      "v_lshlrev_b32 v235, 16, v179\n"
      "v_lshlrev_b32 v234, 16, v178\n"
      "v_add_f32 v164, v164, v232\n"
      "s_waitcnt vmcnt(8)\n"
      "v_lshlrev_b32 v232, 16, v180\n"
      "v_lshlrev_b32 v233, 16, v181\n"
      "v_and_b32 v177, 0xffff0000, v177\n"
      "v_add_f32 v174, v174, v233\n"
      "v_add_f32 v165, v165, v176\n"
      "v_add_f32 v167, v167, v177\n"
      "v_and_b32 v181, 0xffff0000, v181\n"
      "s_waitcnt vmcnt(7)\n"
      "v_lshlrev_b32 v233, 16, v185\n"
      "v_add_f32 v172, v172, v232\n"
      "v_and_b32 v180, 0xffff0000, v180\n"
      "v_add_f32 v166, v166, v233\n"
      "v_add_f32 v168, v168, v234\n"
      "v_lshlrev_b32 v234, 16, v186\n"
      "v_lshlrev_b32 v232, 16, v184\n"
      "s_waitcnt vmcnt(6)\n"
      "v_lshlrev_b32 v233, 16, v189\n"
      "v_and_b32 v179, 0xffff0000, v179\n"
      "v_and_b32 v184, 0xffff0000, v184\n"
      "v_and_b32 v185, 0xffff0000, v185\n"
      "v_add_f32 v175, v175, v181\n"
      "v_add_f32 v168, v168, v234\n"
      "v_add_f32 v164, v164, v232\n"
      "v_add_f32 v165, v165, v184\n"
      "v_add_f32 v174, v174, v233\n"
      "v_lshlrev_b32 v232, 16, v188\n"
      "s_waitcnt vmcnt(4)\n"
      "v_lshlrev_b32 v233, 16, v193\n"
      "v_and_b32 v193, 0xffff0000, v193\n"
      "v_add_f32 v170, v170, v235\n"
      "v_add_f32 v172, v172, v232\n"
      "v_lshlrev_b32 v232, 16, v192\n"
      "v_lshlrev_b32 v235, 16, v187\n"
      "v_add_f32 v166, v166, v233\n"
      "v_add_f32 v170, v170, v235\n"
      "v_lshlrev_b32 v235, 16, v195\n"
      "v_lshlrev_b32 v233, 16, v197\n"
      "v_lshlrev_b32 v234, 16, v194\n"
      "v_add_f32 v164, v164, v232\n"
      "v_lshlrev_b32 v232, 16, v196\n"
      "v_and_b32 v178, 0xffff0000, v178\n"
      "v_add_f32 v171, v171, v179\n"
      "v_and_b32 v192, 0xffff0000, v192\n"
      "v_add_f32 v168, v168, v234\n"
      "v_add_f32 v174, v174, v233\n"
      "s_waitcnt vmcnt(3)\n"
      "v_lshlrev_b32 v233, 16, v201\n"
      "v_add_f32 v170, v170, v235\n"
      "v_and_b32 v186, 0xffff0000, v186\n"
      "v_add_f32 v172, v172, v232\n"
      "v_add_f32 v166, v166, v233\n"
      "v_add_f32 v165, v165, v192\n"
      "v_and_b32 v201, 0xffff0000, v201\n"
      "v_lshlrev_b32 v232, 16, v200\n"
      "v_add_f32 v164, v164, v232\n"
      "s_waitcnt vmcnt(0)\n"
      "v_lshlrev_b32 v233, 16, v205\n"
      "v_lshlrev_b32 v232, 16, v204\n"
      "v_add_f32 v172, v172, v232\n"
      "v_add_f32 v174, v174, v233\n"
      "v_and_b32 v204, 0xffff0000, v204\n"
      "v_and_b32 v188, 0xffff0000, v188\n"
      "v_and_b32 v189, 0xffff0000, v189\n"
      "v_lshlrev_b32 v232, 16, v208\n"
      "v_add_f32 v175, v175, v189\n"
      "v_lshlrev_b32 v234, 16, v202\n"
      "v_add_f32 v173, v173, v180\n"
      "v_lshlrev_b32 v233, 16, v209\n"
      "v_add_f32 v164, v164, v232\n"
      "v_add_f32 v167, v167, v185\n"
      "v_lshlrev_b32 v235, 16, v203\n"
      "v_add_f32 v173, v173, v188\n"
      "v_and_b32 v208, 0xffff0000, v208\n"
      "v_add_f32 v169, v169, v178\n"
      "v_add_f32 v167, v167, v193\n"
      "v_and_b32 v209, 0xffff0000, v209\n"
      "v_and_b32 v187, 0xffff0000, v187\n"
      "v_lshlrev_b32 v232, 16, v212\n"
      "v_and_b32 v205, 0xffff0000, v205\n"
      "v_add_f32 v168, v168, v234\n"
      "v_add_f32 v166, v166, v233\n"
      "v_lshlrev_b32 v233, 16, v213\n"
      "v_and_b32 v200, 0xffff0000, v200\n"
      "v_add_f32 v165, v165, v200\n"
      "v_add_f32 v171, v171, v187\n"
      "v_add_f32 v165, v165, v208\n"
      "v_lshlrev_b32 v234, 16, v210\n"
      "v_add_f32 v167, v167, v201\n"
      "v_and_b32 v194, 0xffff0000, v194\n"
      "v_and_b32 v210, 0xffff0000, v210\n"
      "v_and_b32 v213, 0xffff0000, v213\n"
      "v_add_f32 v169, v169, v186\n"
      "v_add_f32 v169, v169, v194\n"
      "v_add_f32 v170, v170, v235\n"
      "v_and_b32 v197, 0xffff0000, v197\n"
      "v_add_f32 v167, v167, v209\n"
      "v_and_b32 v195, 0xffff0000, v195\n"
      "v_and_b32 v202, 0xffff0000, v202\n"
      "v_lshlrev_b32 v235, 16, v211\n"
      "v_and_b32 v196, 0xffff0000, v196\n"
      "v_add_f32 v175, v175, v197\n"
      "v_add_f32 v170, v170, v235\n"
      "v_add_f32 v172, v172, v232\n"
      "v_and_b32 v212, 0xffff0000, v212\n"
      "v_add_f32 v168, v168, v234\n"
      "v_add_f32 v169, v169, v202\n"
      "v_add_f32 v173, v173, v196\n"
      "v_add_f32 v173, v173, v204\n"
      "v_add_f32 v171, v171, v195\n"
      "v_and_b32 v211, 0xffff0000, v211\n"
      "v_add_f32 v173, v173, v212\n"
      "v_cvt_pk_bf16_f32 v176, v164, v165\n"
      "v_add_f32 v169, v169, v210\n"
      "v_and_b32 v203, 0xffff0000, v203\n"
      "v_cvt_pk_bf16_f32 v180, v172, v173\n"
      "v_add_f32 v171, v171, v203\n"
      "v_cvt_pk_bf16_f32 v177, v166, v167\n"
      "v_add_f32 v171, v171, v211\n"
      "v_add_f32 v174, v174, v233\n"
      "v_add_f32 v175, v175, v205\n"
      "v_add_f32 v175, v175, v213\n"
      "v_cvt_pk_bf16_f32 v179, v170, v171\n"
      "v_cvt_pk_bf16_f32 v178, v168, v169\n"
      "v_cvt_pk_bf16_f32 v181, v174, v175\n"
      "buffer_store_dwordx4 v[176:179], v8, s[16:19], s57 offen\n"
      "buffer_store_dwordx2 v[180:181], v9, s[16:19], s57 offen\n"
      "s_branch .Ld_end\n"
      ".Ld_out2:\n"
      "buffer_store_dwordx4 v[224:227], v0, s[8:11], s37 offen sc1\n"
      "s_nop 7\n"
      "v_cvt_pk_bf16_f32 v229, v162, v163\n"
      "v_cvt_pk_bf16_f32 v228, v160, v161\n"
      "buffer_store_dwordx2 v[228:229], v13, s[8:11], s37 offen sc1\n"
      "s_add_u32 s52, s52, 106496\n"
      "s_waitcnt vmcnt(2)\n"
      "v_cmp_ne_u32 vcc, s21, v21\n"
      "s_cmp_lt_u32 s41, 32\n"
      "s_cselect_b32 s56, s39, 0x40000000\n"
      "s_cbranch_vccnz .Lsp_df_19\n"
      ".Lbk_df_19:\n"
      "buffer_load_dwordx4 v[176:179], v0, s[8:11], s53 offen sc1\n"
      "s_add_u32 s55, s53, 6656\n"
      "buffer_load_dwordx2 v[180:181], v13, s[8:11], s53 offen sc1\n"
      "s_add_u32 s37, s37, 106496\n"
      "buffer_load_dwordx4 v[184:187], v0, s[8:11], s55 offen sc1\n"
      "s_add_u32 s54, s53, 13312\n"
      "buffer_load_dwordx2 v[188:189], v13, s[8:11], s55 offen sc1\n"
      "s_waitcnt vmcnt(4)\n"
      "buffer_store_dword v18, v4, s[8:11], s52 offen sc1\n"
      "s_add_u32 s55, s53, 19968\n"
      "buffer_load_dwordx2 v[196:197], v13, s[8:11], s54 offen sc1\n"
      "s_add_u32 s58, s41, 4294967264\n"
      "buffer_load_dwordx4 v[192:195], v0, s[8:11], s54 offen sc1\n"
      "s_add_u32 s39, s39, 147456\n"
      "s_cmp_lt_u32 s58, 32\n"
      "s_cselect_b32 s57, s40, 0x40000000\n"
      "s_add_u32 s52, s52, 106496\n"
      "s_add_u32 s54, s53, 26624\n"
      "buffer_load_dwordx4 v[200:203], v0, s[8:11], s55 offen sc1\n"
      "s_add_u32 s40, s40, 4294856704\n"
      "s_add_u32 s41, s41, 6\n"
      "buffer_load_dwordx4 v[208:211], v0, s[8:11], s54 offen sc1\n"
      "s_add_u32 s53, s53, 599040\n"
      "buffer_load_dwordx2 v[212:213], v13, s[8:11], s54 offen sc1\n"
      "buffer_load_dwordx2 v[204:205], v13, s[8:11], s55 offen sc1\n"
      "s_waitcnt vmcnt(10)\n"
      "v_lshlrev_b32 v232, 16, v176\n"
      "v_lshlrev_b32 v233, 16, v177\n"
      "v_and_b32 v176, 0xffff0000, v176\n"
      "v_add_f32 v166, v166, v233\n"
      "v_lshlrev_b32 v235, 16, v179\n"
      "v_lshlrev_b32 v234, 16, v178\n"
      "v_add_f32 v164, v164, v232\n"
      "s_waitcnt vmcnt(9)\n"
      "v_lshlrev_b32 v232, 16, v180\n"
      "v_lshlrev_b32 v233, 16, v181\n"
      "v_and_b32 v177, 0xffff0000, v177\n"
      "v_add_f32 v174, v174, v233\n"
      "v_add_f32 v165, v165, v176\n"
      "v_add_f32 v167, v167, v177\n"
      "v_and_b32 v181, 0xffff0000, v181\n"
      "s_waitcnt vmcnt(8)\n"
      "v_lshlrev_b32 v233, 16, v185\n"
      "v_add_f32 v172, v172, v232\n"
      "v_and_b32 v180, 0xffff0000, v180\n"
      "v_add_f32 v166, v166, v233\n"
      "v_add_f32 v168, v168, v234\n"
      "v_lshlrev_b32 v234, 16, v186\n"
      "v_lshlrev_b32 v232, 16, v184\n"
      "s_waitcnt vmcnt(7)\n"
      "v_lshlrev_b32 v233, 16, v189\n"
      "v_and_b32 v179, 0xffff0000, v179\n"
      "v_and_b32 v184, 0xffff0000, v184\n"
      "v_and_b32 v185, 0xffff0000, v185\n"
      "v_add_f32 v175, v175, v181\n"
      "v_add_f32 v168, v168, v234\n"
      "v_add_f32 v164, v164, v232\n"
      "v_add_f32 v165, v165, v184\n"
      "v_add_f32 v174, v174, v233\n"
      "v_lshlrev_b32 v232, 16, v188\n"
      "s_waitcnt vmcnt(4)\n"
      "v_lshlrev_b32 v233, 16, v193\n"
      "v_and_b32 v193, 0xffff0000, v193\n"
      "v_add_f32 v170, v170, v235\n"
      "v_add_f32 v172, v172, v232\n"
      "v_lshlrev_b32 v232, 16, v192\n"
      "v_lshlrev_b32 v235, 16, v187\n"
      "v_add_f32 v166, v166, v233\n"
      "v_add_f32 v170, v170, v235\n"
      "v_lshlrev_b32 v235, 16, v195\n"
      "v_lshlrev_b32 v233, 16, v197\n"
      "v_lshlrev_b32 v234, 16, v194\n"
      "v_add_f32 v164, v164, v232\n"
      "v_lshlrev_b32 v232, 16, v196\n"
      "v_and_b32 v178, 0xffff0000, v178\n"
      "v_add_f32 v171, v171, v179\n"
      "v_and_b32 v192, 0xffff0000, v192\n"
      "v_add_f32 v168, v168, v234\n"
      "v_add_f32 v174, v174, v233\n"
      "s_waitcnt vmcnt(3)\n"
      "v_lshlrev_b32 v233, 16, v201\n"
      "v_add_f32 v170, v170, v235\n"
      "v_and_b32 v186, 0xffff0000, v186\n"
      "v_add_f32 v172, v172, v232\n"
      "v_add_f32 v166, v166, v233\n"
      "v_add_f32 v165, v165, v192\n"
      "v_and_b32 v201, 0xffff0000, v201\n"
      "v_lshlrev_b32 v232, 16, v200\n"
      "v_add_f32 v164, v164, v232\n"
      "s_waitcnt vmcnt(0)\n"
      "v_lshlrev_b32 v233, 16, v205\n"
      "v_lshlrev_b32 v232, 16, v204\n"
      "v_add_f32 v172, v172, v232\n"
      "v_add_f32 v174, v174, v233\n"
      "v_and_b32 v204, 0xffff0000, v204\n"
      "v_and_b32 v188, 0xffff0000, v188\n"
      "v_and_b32 v189, 0xffff0000, v189\n"
      "v_lshlrev_b32 v232, 16, v208\n"
      "v_add_f32 v175, v175, v189\n"
      "v_lshlrev_b32 v234, 16, v202\n"
      "v_add_f32 v173, v173, v180\n"
      "v_lshlrev_b32 v233, 16, v209\n"
      "v_add_f32 v164, v164, v232\n"
      "v_add_f32 v167, v167, v185\n"
      "v_lshlrev_b32 v235, 16, v203\n"
      "v_add_f32 v173, v173, v188\n"
      "v_and_b32 v208, 0xffff0000, v208\n"
      "v_add_f32 v169, v169, v178\n"
      "v_add_f32 v167, v167, v193\n"
      "v_and_b32 v209, 0xffff0000, v209\n"
      "v_and_b32 v187, 0xffff0000, v187\n"
      "v_lshlrev_b32 v232, 16, v212\n"
      "v_and_b32 v205, 0xffff0000, v205\n"
      "v_add_f32 v168, v168, v234\n"
      "v_add_f32 v166, v166, v233\n"
      "v_lshlrev_b32 v233, 16, v213\n"
      "v_and_b32 v200, 0xffff0000, v200\n"
      "v_add_f32 v165, v165, v200\n"
      "v_add_f32 v171, v171, v187\n"
      "v_add_f32 v165, v165, v208\n"
      "v_lshlrev_b32 v234, 16, v210\n"
      "v_add_f32 v167, v167, v201\n"
      "v_and_b32 v194, 0xffff0000, v194\n"
      "v_and_b32 v210, 0xffff0000, v210\n"
      "v_and_b32 v213, 0xffff0000, v213\n"
      "v_add_f32 v169, v169, v186\n"
      "v_add_f32 v169, v169, v194\n"
      "v_add_f32 v170, v170, v235\n"
      "v_and_b32 v197, 0xffff0000, v197\n"
      "v_add_f32 v167, v167, v209\n"
      "v_and_b32 v195, 0xffff0000, v195\n"
      "v_and_b32 v202, 0xffff0000, v202\n"
      "v_lshlrev_b32 v235, 16, v211\n"
      "v_and_b32 v196, 0xffff0000, v196\n"
      "v_add_f32 v175, v175, v197\n"
      "v_add_f32 v170, v170, v235\n"
      "v_add_f32 v172, v172, v232\n"
      "v_and_b32 v212, 0xffff0000, v212\n"
      "v_add_f32 v168, v168, v234\n"
      "v_add_f32 v169, v169, v202\n"
      "v_add_f32 v173, v173, v196\n"
      "v_add_f32 v173, v173, v204\n"
      "v_add_f32 v171, v171, v195\n"
      "v_and_b32 v211, 0xffff0000, v211\n"
      "v_add_f32 v173, v173, v212\n"
      "v_cvt_pk_bf16_f32 v176, v164, v165\n"
      "v_add_f32 v169, v169, v210\n"
      "v_and_b32 v203, 0xffff0000, v203\n"
      "v_cvt_pk_bf16_f32 v180, v172, v173\n"
      "v_add_f32 v171, v171, v203\n"
      "v_cvt_pk_bf16_f32 v177, v166, v167\n"
      "v_add_f32 v171, v171, v211\n"
      "v_add_f32 v174, v174, v233\n"
      "v_add_f32 v175, v175, v205\n"
      "v_add_f32 v175, v175, v213\n"
      "v_cvt_pk_bf16_f32 v179, v170, v171\n"
      "v_cvt_pk_bf16_f32 v178, v168, v169\n"
      "v_cvt_pk_bf16_f32 v181, v174, v175\n"
      "buffer_store_dwordx4 v[176:179], v8, s[16:19], s57 offen\n"
      "buffer_store_dwordx2 v[180:181], v9, s[16:19], s57 offen\n"
      "s_branch .Ld_end\n"
      ".Ld_out3:\n"
      "buffer_load_dwordx4 v[192:195], v0, s[8:11], s54 offen sc1\n"
      "s_add_u32 s39, s39, 147456\n"
      "s_cmp_lt_u32 s58, 32\n"
      "s_cselect_b32 s57, s40, 0x40000000\n"
      "s_add_u32 s52, s52, 106496\n"
      "buffer_store_dwordx4 v[224:227], v0, s[8:11], s37 offen sc1\n"
      "s_nop 2\n"
      "v_cvt_pk_bf16_f32 v229, v162, v163\n"
      "v_cvt_pk_bf16_f32 v228, v160, v161\n"
      "buffer_store_dwordx2 v[228:229], v13, s[8:11], s37 offen sc1\n"
      "s_add_u32 s54, s53, 26624\n"
      "buffer_load_dwordx4 v[200:203], v0, s[8:11], s55 offen sc1\n"
      "s_add_u32 s40, s40, 4294856704\n"
      "s_add_u32 s41, s41, 6\n"
      "buffer_load_dwordx4 v[208:211], v0, s[8:11], s54 offen sc1\n"
      "s_add_u32 s53, s53, 599040\n"
      "buffer_load_dwordx2 v[212:213], v13, s[8:11], s54 offen sc1\n"
      "buffer_load_dwordx2 v[204:205], v13, s[8:11], s55 offen sc1\n"
      "s_waitcnt vmcnt(12)\n"
      "v_lshlrev_b32 v232, 16, v176\n"
      "v_lshlrev_b32 v233, 16, v177\n"
      "v_and_b32 v176, 0xffff0000, v176\n"
      "v_add_f32 v166, v166, v233\n"
      "v_lshlrev_b32 v235, 16, v179\n"
      "v_lshlrev_b32 v234, 16, v178\n"
      "v_add_f32 v164, v164, v232\n"
      "s_waitcnt vmcnt(11)\n"
      "v_lshlrev_b32 v232, 16, v180\n"
      "v_lshlrev_b32 v233, 16, v181\n"
      "v_and_b32 v177, 0xffff0000, v177\n"
      "v_add_f32 v174, v174, v233\n"
      "v_add_f32 v165, v165, v176\n"
      "v_add_f32 v167, v167, v177\n"
      "v_and_b32 v181, 0xffff0000, v181\n"
      "s_waitcnt vmcnt(10)\n"
      "v_lshlrev_b32 v233, 16, v185\n"
      "v_add_f32 v172, v172, v232\n"
      "v_and_b32 v180, 0xffff0000, v180\n"
      "v_add_f32 v166, v166, v233\n"
      "v_add_f32 v168, v168, v234\n"
      "v_lshlrev_b32 v234, 16, v186\n"
      "v_lshlrev_b32 v232, 16, v184\n"
      "s_waitcnt vmcnt(9)\n"
      "v_lshlrev_b32 v233, 16, v189\n"
      "v_and_b32 v179, 0xffff0000, v179\n"
      "v_and_b32 v184, 0xffff0000, v184\n"
      "v_and_b32 v185, 0xffff0000, v185\n"
      "v_add_f32 v175, v175, v181\n"
      "v_add_f32 v168, v168, v234\n"
      "v_add_f32 v164, v164, v232\n"
      "v_add_f32 v165, v165, v184\n"
      "v_add_f32 v174, v174, v233\n"
      "v_lshlrev_b32 v232, 16, v188\n"
      "s_waitcnt vmcnt(6)\n"
      "v_lshlrev_b32 v233, 16, v193\n"
      "v_and_b32 v193, 0xffff0000, v193\n"
      "v_add_f32 v170, v170, v235\n"
      "v_add_f32 v172, v172, v232\n"
      "v_lshlrev_b32 v232, 16, v192\n"
      "v_lshlrev_b32 v235, 16, v187\n"
      "v_add_f32 v166, v166, v233\n"
      "s_waitcnt vmcnt(4)\n"
      "buffer_store_dword v18, v4, s[8:11], s52 offen sc1\n"
      "v_add_f32 v170, v170, v235\n"
      "v_lshlrev_b32 v235, 16, v195\n"
      "s_add_u32 s37, s37, 106496\n"
      "v_lshlrev_b32 v233, 16, v197\n"
      "v_lshlrev_b32 v234, 16, v194\n"
      "v_add_f32 v164, v164, v232\n"
      "v_lshlrev_b32 v232, 16, v196\n"
      "v_and_b32 v178, 0xffff0000, v178\n"
      "v_add_f32 v171, v171, v179\n"
      "v_and_b32 v192, 0xffff0000, v192\n"
      "v_add_f32 v168, v168, v234\n"
      "s_add_u32 s52, s52, 106496\n"
      "v_add_f32 v174, v174, v233\n"
      "s_waitcnt vmcnt(4)\n"
      "v_lshlrev_b32 v233, 16, v201\n"
      "v_add_f32 v170, v170, v235\n"
      "v_and_b32 v186, 0xffff0000, v186\n"
      "v_add_f32 v172, v172, v232\n"
      "v_add_f32 v166, v166, v233\n"
      "v_add_f32 v165, v165, v192\n"
      "v_and_b32 v201, 0xffff0000, v201\n"
      "v_lshlrev_b32 v232, 16, v200\n"
      "v_add_f32 v164, v164, v232\n"
      "s_waitcnt vmcnt(1)\n"
      "v_lshlrev_b32 v233, 16, v205\n"
      "v_lshlrev_b32 v232, 16, v204\n"
      "v_add_f32 v172, v172, v232\n"
      "v_add_f32 v174, v174, v233\n"
      "v_and_b32 v204, 0xffff0000, v204\n"
      "v_and_b32 v188, 0xffff0000, v188\n"
      "v_and_b32 v189, 0xffff0000, v189\n"
      "v_lshlrev_b32 v232, 16, v208\n"
      "v_add_f32 v175, v175, v189\n"
      "v_lshlrev_b32 v234, 16, v202\n"
      "v_add_f32 v173, v173, v180\n"
      "v_lshlrev_b32 v233, 16, v209\n"
      "v_add_f32 v164, v164, v232\n"
      "v_add_f32 v167, v167, v185\n"
      "v_lshlrev_b32 v235, 16, v203\n"
      "v_add_f32 v173, v173, v188\n"
      "v_and_b32 v208, 0xffff0000, v208\n"
      "v_add_f32 v169, v169, v178\n"
      "v_add_f32 v167, v167, v193\n"
      "v_and_b32 v209, 0xffff0000, v209\n"
      "v_and_b32 v187, 0xffff0000, v187\n"
      "v_lshlrev_b32 v232, 16, v212\n"
      "v_and_b32 v205, 0xffff0000, v205\n"
      "v_add_f32 v168, v168, v234\n"
      "v_add_f32 v166, v166, v233\n"
      "v_lshlrev_b32 v233, 16, v213\n"
      "v_and_b32 v200, 0xffff0000, v200\n"
      "v_add_f32 v165, v165, v200\n"
      "v_add_f32 v171, v171, v187\n"
      "v_add_f32 v165, v165, v208\n"
      "v_lshlrev_b32 v234, 16, v210\n"
      "v_add_f32 v167, v167, v201\n"
      "v_and_b32 v194, 0xffff0000, v194\n"
      "v_and_b32 v210, 0xffff0000, v210\n"
      "v_and_b32 v213, 0xffff0000, v213\n"
      "v_add_f32 v169, v169, v186\n"
      "v_add_f32 v169, v169, v194\n"
      "v_add_f32 v170, v170, v235\n"
      "v_and_b32 v197, 0xffff0000, v197\n"
      "v_add_f32 v167, v167, v209\n"
      "v_and_b32 v195, 0xffff0000, v195\n"
      "v_and_b32 v202, 0xffff0000, v202\n"
      "v_lshlrev_b32 v235, 16, v211\n"
      "v_and_b32 v196, 0xffff0000, v196\n"
      "v_add_f32 v175, v175, v197\n"
      "v_add_f32 v170, v170, v235\n"
      "v_add_f32 v172, v172, v232\n"
      "v_and_b32 v212, 0xffff0000, v212\n"
      "v_add_f32 v168, v168, v234\n"
      "v_add_f32 v169, v169, v202\n"
      "v_add_f32 v173, v173, v196\n"
      "v_add_f32 v173, v173, v204\n"
      "v_add_f32 v171, v171, v195\n"
      "v_and_b32 v211, 0xffff0000, v211\n"
      "v_add_f32 v173, v173, v212\n"
      "v_cvt_pk_bf16_f32 v176, v164, v165\n"
      "v_add_f32 v169, v169, v210\n"
      "v_and_b32 v203, 0xffff0000, v203\n"
      "v_cvt_pk_bf16_f32 v180, v172, v173\n"
      "v_add_f32 v171, v171, v203\n"
      "v_cvt_pk_bf16_f32 v177, v166, v167\n"
      "v_add_f32 v171, v171, v211\n"
      "v_add_f32 v174, v174, v233\n"
      "v_add_f32 v175, v175, v205\n"
      "v_add_f32 v175, v175, v213\n"
      "v_cvt_pk_bf16_f32 v179, v170, v171\n"
      "v_cvt_pk_bf16_f32 v178, v168, v169\n"
      "v_cvt_pk_bf16_f32 v181, v174, v175\n"
      "buffer_store_dwordx4 v[176:179], v8, s[16:19], s57 offen\n"
      "buffer_store_dwordx2 v[180:181], v9, s[16:19], s57 offen\n"
      "s_branch .Ld_end\n"
      ".Ld_out4:\n"
      "s_add_u32 s52, s52, 106496\n"
      "v_add_f32 v174, v174, v233\n"
      "s_waitcnt vmcnt(11)\n"
      "v_lshlrev_b32 v233, 16, v201\n"
      "v_add_f32 v170, v170, v235\n"
      "v_and_b32 v186, 0xffff0000, v186\n"
      "v_add_f32 v172, v172, v232\n"
      "s_nop 1\n"
      "v_cvt_pk_bf16_f32 v229, v162, v163\n"
      "buffer_store_dwordx4 v[224:227], v0, s[8:11], s37 offen sc1\n"
      "v_add_f32 v166, v166, v233\n"
      "v_add_f32 v165, v165, v192\n"
      "v_and_b32 v201, 0xffff0000, v201\n"
      "v_lshlrev_b32 v232, 16, v200\n"
      "v_cvt_pk_bf16_f32 v228, v160, v161\n"
      "buffer_store_dwordx2 v[228:229], v13, s[8:11], s37 offen sc1\n"
      "v_add_f32 v164, v164, v232\n"
      "s_waitcnt vmcnt(10)\n"
      "v_lshlrev_b32 v233, 16, v205\n"
      "v_lshlrev_b32 v232, 16, v204\n"
      "v_add_f32 v172, v172, v232\n"
      "v_add_f32 v174, v174, v233\n"
      "v_and_b32 v204, 0xffff0000, v204\n"
      "v_and_b32 v188, 0xffff0000, v188\n"
      "v_and_b32 v189, 0xffff0000, v189\n"
      "v_lshlrev_b32 v232, 16, v208\n"
      "v_add_f32 v175, v175, v189\n"
      "v_lshlrev_b32 v234, 16, v202\n"
      "v_add_f32 v173, v173, v180\n"
      "v_lshlrev_b32 v233, 16, v209\n"
      "v_add_f32 v164, v164, v232\n"
      "v_add_f32 v167, v167, v185\n"
      "v_lshlrev_b32 v235, 16, v203\n"
      "v_add_f32 v173, v173, v188\n"
      "v_and_b32 v208, 0xffff0000, v208\n"
      "v_add_f32 v169, v169, v178\n"
      "v_add_f32 v167, v167, v193\n"
      "v_and_b32 v209, 0xffff0000, v209\n"
      "v_and_b32 v187, 0xffff0000, v187\n"
      "v_lshlrev_b32 v232, 16, v212\n"
      "v_and_b32 v205, 0xffff0000, v205\n"
      "v_add_f32 v168, v168, v234\n"
      "v_add_f32 v166, v166, v233\n"
      "v_lshlrev_b32 v233, 16, v213\n"
      "v_and_b32 v200, 0xffff0000, v200\n"
      "v_add_f32 v165, v165, v200\n"
      "v_add_f32 v171, v171, v187\n"
      "v_add_f32 v165, v165, v208\n"
      "v_lshlrev_b32 v234, 16, v210\n"
      "v_add_f32 v167, v167, v201\n"
      "v_and_b32 v194, 0xffff0000, v194\n"
      "v_and_b32 v210, 0xffff0000, v210\n"
      "v_and_b32 v213, 0xffff0000, v213\n"
      "v_add_f32 v169, v169, v186\n"
      "v_add_f32 v169, v169, v194\n"
      "v_add_f32 v170, v170, v235\n"
      "s_add_u32 s37, s37, 106496\n"
      "v_and_b32 v197, 0xffff0000, v197\n"
      "v_add_f32 v167, v167, v209\n"
      "v_and_b32 v195, 0xffff0000, v195\n"
      "v_and_b32 v202, 0xffff0000, v202\n"
      "v_lshlrev_b32 v235, 16, v211\n"
      "v_and_b32 v196, 0xffff0000, v196\n"
      "v_add_f32 v175, v175, v197\n"
      "v_add_f32 v170, v170, v235\n"
      "v_add_f32 v172, v172, v232\n"
      "v_and_b32 v212, 0xffff0000, v212\n"
      "v_add_f32 v168, v168, v234\n"
      "v_add_f32 v169, v169, v202\n"
      "v_add_f32 v173, v173, v196\n"
      "v_add_f32 v173, v173, v204\n"
      "v_add_f32 v171, v171, v195\n"
      "v_and_b32 v211, 0xffff0000, v211\n"
      "v_add_f32 v173, v173, v212\n"
      "v_cvt_pk_bf16_f32 v176, v164, v165\n"
      "v_add_f32 v169, v169, v210\n"
      "v_and_b32 v203, 0xffff0000, v203\n"
      "s_waitcnt vmcnt(0)\n"
      "buffer_store_dword v18, v4, s[8:11], s52 offen sc1\n"
      "v_cvt_pk_bf16_f32 v180, v172, v173\n"
      "v_add_f32 v171, v171, v203\n"
      "v_cvt_pk_bf16_f32 v177, v166, v167\n"
      "v_add_f32 v171, v171, v211\n"
      "v_add_f32 v174, v174, v233\n"
      "v_add_f32 v175, v175, v205\n"
      "v_add_f32 v175, v175, v213\n"
      "v_cvt_pk_bf16_f32 v179, v170, v171\n"
      "v_cvt_pk_bf16_f32 v178, v168, v169\n"
      "v_cvt_pk_bf16_f32 v181, v174, v175\n"
      "buffer_store_dwordx4 v[176:179], v8, s[16:19], s57 offen\n"
      "s_add_u32 s52, s52, 106496\n"
      "buffer_store_dwordx2 v[180:181], v9, s[16:19], s57 offen\n"
      "s_branch .Ld_end\n"
      ".Ld_in1:\n"
      "s_add_u32 s36, s36, 165888\n"
      "s_min_u32 s36, s36, s6\n"
      "s_mov_b32 s51, s36\n"
      "s_add_u32 s36, s36, 165888\n"
      "s_min_u32 s36, s36, s6\n"
      "buffer_load_dword v20, v2, s[8:11], s36 offen sc1\n"
      "s_mov_b32 s51, s36\n"
      "s_add_u32 m0, s43, 24576\n"
      "s_add_u32 s36, s36, 165888\n"
      "s_waitcnt vmcnt(0)\n"
      "v_cmp_ne_u32 vcc, s21, v20\n"
      "s_cbranch_vccnz .Lsp_d1_20\n"
      ".Lbk_d1_20:\n"
      "buffer_load_dwordx4 v0, s[8:11], s51 offen offset:0 sc1 lds\n"
      "buffer_load_dwordx4 v0, s[8:11], s51 offen offset:1024 sc1 lds\n"
      "buffer_load_dwordx4 v0, s[8:11], s51 offen offset:2048 sc1 lds\n"
      "buffer_load_dwordx4 v0, s[8:11], s51 offen offset:3072 sc1 lds\n"
      "s_min_u32 s36, s36, s6\n"
      "buffer_load_dword v20, v2, s[8:11], s36 offen sc1\n"
      "s_add_u32 m0, s43, 28672\n"
      "s_nop 0\n"
      "buffer_load_dwordx4 v1, s[8:11], s51 offen offset:0 sc1 lds\n"
      "buffer_load_dwordx4 v1, s[8:11], s51 offen offset:1024 sc1 lds\n"
      "s_mov_b32 s51, s36\n"
      "s_add_u32 s38, s38, 147456\n"
      "s_add_u32 s36, s36, 165888\n"
      "s_min_u32 s36, s36, s6\n"
      "v_add_u32 v14, 1, v14\n"
      "s_add_u32 m0, s43, 49152\n"
      "s_waitcnt vmcnt(2)\n"
      "v_cmp_ne_u32 vcc, s21, v20\n"
      "s_cbranch_vccnz .Lsp_d2_21\n"
      ".Lbk_d2_21:\n"
      "buffer_load_dwordx4 v0, s[8:11], s51 offen offset:0 sc1 lds\n"
      "buffer_load_dwordx4 v0, s[8:11], s51 offen offset:3072 sc1 lds\n"
      "s_waitcnt vmcnt(2)\n"
      "ds_write_b32 v16, v14\n"
      "buffer_load_dword v20, v2, s[8:11], s36 offen sc1\n"
      "s_add_u32 s52, s52, 106496\n"
      "buffer_load_dwordx4 v0, s[8:11], s51 offen offset:1024 sc1 lds\n"
      "ds_read_b32 v15, v17\n"
      "buffer_load_dwordx4 v0, s[8:11], s51 offen offset:2048 sc1 lds\n"
      "s_add_u32 m0, s43, 53248\n"
      "s_waitcnt lgkmcnt(0)\n"
      "v_cmp_gt_i32 vcc, v14, v15\n"
      "buffer_load_dwordx4 v1, s[8:11], s51 offen offset:0 sc1 lds\n"
      "s_cbranch_vccnz .Lsp_dy1_22\n"
      ".Lbk_dy1_22:\n"
      "ds_read_b128 v[72:75], v0 offset:28672\n"
      "buffer_load_dwordx4 v1, s[8:11], s51 offen offset:1024 sc1 lds\n"
      "ds_read_b128 v[80:83], v0 offset:30720\n"
      "ds_read_b128 v[84:87], v0 offset:31744\n"
      "ds_read_b128 v[68:71], v0 offset:27648\n"
      "ds_read_b128 v[104:107], v0 offset:36864\n"
      "ds_read_b128 v[56:59], v0 offset:24576\n"
      "ds_read_b128 v[60:63], v0 offset:25600\n"
      "ds_read_b128 v[116:119], v0 offset:39936\n"
      "ds_read_b128 v[92:95], v0 offset:33792\n"
      "ds_read_b128 v[64:67], v0 offset:26624\n"
      "ds_read_b128 v[76:79], v0 offset:29696\n"
      "ds_read_b128 v[100:103], v0 offset:35840\n"
      "ds_read_b128 v[96:99], v0 offset:34816\n"
      "s_waitcnt vmcnt(0) lgkmcnt(0)\n"
      "s_nop 4\n"
      "s_branch .Ld_t1\n"
      ".Ld_in2:\n"
      "s_add_u32 s36, s36, 165888\n"
      "s_min_u32 s36, s36, s6\n"
      "s_mov_b32 s51, s36\n"
      "s_add_u32 s36, s36, 165888\n"
      "s_min_u32 s36, s36, s6\n"
      "s_mov_b32 s51, s36\n"
      "s_add_u32 s36, s36, 165888\n"
      "s_min_u32 s36, s36, s6\n"
      "buffer_load_dword v20, v2, s[8:11], s36 offen sc1\n"
      "s_mov_b32 s51, s36\n"
      "s_add_u32 s38, s38, 147456\n"
      "s_add_u32 s36, s36, 165888\n"
      "s_min_u32 s36, s36, s6\n"
      "s_add_u32 m0, s43, 49152\n"
      "s_waitcnt vmcnt(0)\n"
      "v_cmp_ne_u32 vcc, s21, v20\n"
      "s_cbranch_vccnz .Lsp_d2_23\n"
      ".Lbk_d2_23:\n"
      "buffer_load_dwordx4 v0, s[8:11], s51 offen offset:0 sc1 lds\n"
      "buffer_load_dwordx4 v0, s[8:11], s51 offen offset:3072 sc1 lds\n"
      "buffer_load_dword v20, v2, s[8:11], s36 offen sc1\n"
      "s_add_u32 s52, s52, 106496\n"
      "buffer_load_dwordx4 v0, s[8:11], s51 offen offset:1024 sc1 lds\n"
      "buffer_load_dwordx4 v0, s[8:11], s51 offen offset:2048 sc1 lds\n"
      "s_add_u32 m0, s43, 53248\n"
      "s_nop 0\n"
      "buffer_load_dwordx4 v1, s[8:11], s51 offen offset:0 sc1 lds\n"
      "buffer_load_dwordx4 v1, s[8:11], s51 offen offset:1024 sc1 lds\n"
      "s_mov_b32 s51, s36\n"
      "s_add_u32 s37, s37, 173056\n"
      "v_add_u32 v14, 1, v14\n"
      "s_add_u32 m0, s43, 73728\n"
      "s_waitcnt vmcnt(4)\n"
      "v_cmp_ne_u32 vcc, s21, v20\n"
      "s_cbranch_vccnz .Lsp_d3_24\n"
      ".Lbk_d3_24:\n"
      "buffer_load_dwordx4 v0, s[8:11], s51 offen offset:0 sc1 lds\n"
      "s_add_u32 s36, s36, 165888\n"
      "s_min_u32 s36, s36, s6\n"
      "buffer_load_dwordx4 v0, s[8:11], s51 offen offset:1024 sc1 lds\n"
      "buffer_load_dword v20, v2, s[8:11], s36 offen sc1\n"
      "s_waitcnt vmcnt(3)\n"
      "ds_write_b32 v16, v14\n"
      "buffer_load_dwordx4 v0, s[8:11], s51 offen offset:3072 sc1 lds\n"
      "buffer_load_dwordx4 v0, s[8:11], s51 offen offset:2048 sc1 lds\n"
      "s_add_u32 m0, s43, 77824\n"
      "ds_read_b32 v15, v17\n"
      "buffer_load_dwordx4 v1, s[8:11], s51 offen offset:0 sc1 lds\n"
      "s_waitcnt lgkmcnt(0)\n"
      "v_cmp_gt_i32 vcc, v14, v15\n"
      "buffer_load_dwordx4 v1, s[8:11], s51 offen offset:1024 sc1 lds\n"
      "s_cbranch_vccnz .Lsp_dy2_25\n"
      ".Lbk_dy2_25:\n"
      "ds_read_b128 v[60:63], v22 offset:1024\n"
      "ds_read_b128 v[64:67], v22 offset:2048\n"
      "s_mov_b32 s51, s36\n"
      "ds_read_b128 v[84:87], v22 offset:7168\n"
      "ds_read_b128 v[80:83], v22 offset:6144\n"
      "ds_read_b128 v[68:71], v22 offset:3072\n"
      "ds_read_b128 v[56:59], v22 offset:0\n"
      "ds_read_b128 v[72:75], v22 offset:4096\n"
      "ds_read_b128 v[116:119], v22 offset:15360\n"
      "ds_read_b128 v[76:79], v22 offset:5120\n"
      "ds_read_b128 v[104:107], v22 offset:12288\n"
      "ds_read_b128 v[88:91], v22 offset:8192\n"
      "ds_read_b128 v[112:115], v22 offset:14336\n"
      "ds_read_b128 v[108:111], v22 offset:13312\n"
      "ds_read_b128 v[92:95], v22 offset:9216\n"
      "s_waitcnt vmcnt(0) lgkmcnt(0)\n"
      "s_nop 4\n"
      "s_branch .Ld_t2\n"
      ".Ld_in3:\n"
      "s_add_u32 s36, s36, 165888\n"
      "s_min_u32 s36, s36, s6\n"
      "s_mov_b32 s51, s36\n"
      "s_add_u32 s36, s36, 165888\n"
      "s_min_u32 s36, s36, s6\n"
      "s_mov_b32 s51, s36\n"
      "s_add_u32 s36, s36, 165888\n"
      "s_min_u32 s36, s36, s6\n"
      "s_mov_b32 s51, s36\n"
      "s_add_u32 s38, s38, 147456\n"
      "s_add_u32 s36, s36, 165888\n"
      "s_min_u32 s36, s36, s6\n"
      "buffer_load_dword v20, v2, s[8:11], s36 offen sc1\n"
      "s_add_u32 s52, s52, 106496\n"
      "s_mov_b32 s51, s36\n"
      "s_add_u32 s37, s37, 173056\n"
      "s_add_u32 m0, s43, 73728\n"
      "s_waitcnt vmcnt(0)\n"
      "v_cmp_ne_u32 vcc, s21, v20\n"
      "s_cbranch_vccnz .Lsp_d3_26\n"
      ".Lbk_d3_26:\n"
      "buffer_load_dwordx4 v0, s[8:11], s51 offen offset:0 sc1 lds\n"
      "s_add_u32 s36, s36, 165888\n"
      "s_min_u32 s36, s36, s6\n"
      "buffer_load_dwordx4 v0, s[8:11], s51 offen offset:1024 sc1 lds\n"
      "buffer_load_dword v20, v2, s[8:11], s36 offen sc1\n"
      "buffer_load_dwordx4 v0, s[8:11], s51 offen offset:3072 sc1 lds\n"
      "buffer_load_dwordx4 v0, s[8:11], s51 offen offset:2048 sc1 lds\n"
      "s_add_u32 m0, s43, 77824\n"
      "s_nop 0\n"
      "buffer_load_dwordx4 v1, s[8:11], s51 offen offset:0 sc1 lds\n"
      "buffer_load_dwordx4 v1, s[8:11], s51 offen offset:1024 sc1 lds\n"
      "s_mov_b32 s51, s36\n"
      "s_add_u32 m0, s43, 98304\n"
      "s_add_u32 s37, s37, 106496\n"
      "v_add_u32 v14, 1, v14\n"
      "s_waitcnt vmcnt(4)\n"
      "v_cmp_ne_u32 vcc, s21, v20\n"
      "s_cbranch_vccnz .Lsp_d4_27\n"
      ".Lbk_d4_27:\n"
      "buffer_load_dwordx4 v0, s[8:11], s51 offen offset:2048 sc1 lds\n"
      "s_add_u32 s36, s36, 165888\n"
      "buffer_load_dwordx4 v0, s[8:11], s51 offen offset:1024 sc1 lds\n"
      "s_min_u32 s36, s36, s6\n"
      "s_waitcnt vmcnt(2)\n"
      "ds_write_b32 v16, v14\n"
      "buffer_load_dword v20, v2, s[8:11], s36 offen sc1\n"
      "buffer_load_dwordx4 v0, s[8:11], s51 offen offset:0 sc1 lds\n"
      "s_add_u32 s52, s52, 173056\n"
      "buffer_load_dwordx4 v0, s[8:11], s51 offen offset:3072 sc1 lds\n"
      "s_add_u32 m0, s43, 102400\n"
      "ds_read_b32 v15, v17\n"
      "buffer_load_dwordx4 v1, s[8:11], s51 offen offset:0 sc1 lds\n"
      "s_waitcnt lgkmcnt(0)\n"
      "v_cmp_gt_i32 vcc, v14, v15\n"
      "buffer_load_dwordx4 v1, s[8:11], s51 offen offset:1024 sc1 lds\n"
      "s_cbranch_vccnz .Lsp_dy3_28\n"
      ".Lbk_dy3_28:\n"
      "ds_read_b128 v[64:67], v22 offset:26624\n"
      "ds_read_b128 v[56:59], v22 offset:24576\n"
      "s_mov_b32 s51, s36\n"
      "ds_read_b128 v[72:75], v22 offset:28672\n"
      "ds_read_b128 v[68:71], v22 offset:27648\n"
      "ds_read_b128 v[60:63], v22 offset:25600\n"
      "ds_read_b128 v[84:87], v22 offset:31744\n"
      "ds_read_b128 v[80:83], v22 offset:30720\n"
      "ds_read_b128 v[88:91], v22 offset:32768\n"
      "ds_read_b128 v[112:115], v22 offset:38912\n"
      "ds_read_b128 v[104:107], v22 offset:36864\n"
      "ds_read_b128 v[76:79], v22 offset:29696\n"
      "ds_read_b128 v[100:103], v22 offset:35840\n"
      "ds_read_b128 v[116:119], v22 offset:39936\n"
      "ds_read_b128 v[136:139], v22 offset:45056\n"
      "ds_read_b128 v[132:135], v22 offset:44032\n"
      "s_waitcnt vmcnt(0) lgkmcnt(0)\n"
      "s_nop 4\n"
      "s_branch .Ld_t3\n"
      ".Ld_in4:\n"
      "s_add_u32 s36, s36, 165888\n"
      "s_min_u32 s36, s36, s6\n"
      "s_mov_b32 s51, s36\n"
      "s_add_u32 s36, s36, 165888\n"
      "s_min_u32 s36, s36, s6\n"
      "s_mov_b32 s51, s36\n"
      "s_add_u32 s36, s36, 165888\n"
      "s_min_u32 s36, s36, s6\n"
      "s_mov_b32 s51, s36\n"
      "s_add_u32 s38, s38, 147456\n"
      "s_add_u32 s36, s36, 165888\n"
      "s_min_u32 s36, s36, s6\n"
      "s_add_u32 s52, s52, 106496\n"
      "s_mov_b32 s51, s36\n"
      "s_add_u32 s37, s37, 173056\n"
      "s_add_u32 s36, s36, 165888\n"
      "s_min_u32 s36, s36, s6\n"
      "buffer_load_dword v20, v2, s[8:11], s36 offen sc1\n"
      "s_mov_b32 s51, s36\n"
      "s_add_u32 m0, s43, 98304\n"
      "s_add_u32 s37, s37, 106496\n"
      "s_waitcnt vmcnt(0)\n"
      "v_cmp_ne_u32 vcc, s21, v20\n"
      "s_cbranch_vccnz .Lsp_d4_29\n"
      ".Lbk_d4_29:\n"
      "buffer_load_dwordx4 v0, s[8:11], s51 offen offset:2048 sc1 lds\n"
      "s_add_u32 s36, s36, 165888\n"
      "buffer_load_dwordx4 v0, s[8:11], s51 offen offset:1024 sc1 lds\n"
      "s_min_u32 s36, s36, s6\n"
      "buffer_load_dword v20, v2, s[8:11], s36 offen sc1\n"
      "buffer_load_dwordx4 v0, s[8:11], s51 offen offset:0 sc1 lds\n"
      "s_add_u32 s52, s52, 173056\n"
      "buffer_load_dwordx4 v0, s[8:11], s51 offen offset:3072 sc1 lds\n"
      "s_add_u32 m0, s43, 102400\n"
      "s_nop 0\n"
      "buffer_load_dwordx4 v1, s[8:11], s51 offen offset:0 sc1 lds\n"
      "buffer_load_dwordx4 v1, s[8:11], s51 offen offset:1024 sc1 lds\n"
      "s_mov_b32 s51, s36\n"
      "s_add_u32 s36, s36, 165888\n"
      "s_waitcnt vmcnt(4)\n"
      "v_cmp_ne_u32 vcc, s21, v20\n"
      "v_add_u32 v14, 1, v14\n"
      "s_add_u32 m0, s43, 122880\n"
      "s_cbranch_vccnz .Lsp_d5_30\n"
      ".Lbk_d5_30:\n"
      "buffer_load_dwordx4 v0, s[8:11], s51 offen offset:2048 sc1 lds\n"
      "buffer_load_dwordx4 v0, s[8:11], s51 offen offset:0 sc1 lds\n"
      "s_waitcnt vmcnt(2)\n"
      "ds_write_b32 v16, v14\n"
      "buffer_load_dwordx4 v0, s[8:11], s51 offen offset:1024 sc1 lds\n"
      "s_min_u32 s36, s36, s6\n"
      "buffer_load_dwordx4 v0, s[8:11], s51 offen offset:3072 sc1 lds\n"
      "s_add_u32 m0, s43, 126976\n"
      "ds_read_b32 v15, v17\n"
      "buffer_load_dword v20, v2, s[8:11], s36 offen sc1\n"
      "s_add_u32 s52, s52, 106496\n"
      "buffer_load_dwordx4 v1, s[8:11], s51 offen offset:0 sc1 lds\n"
      "s_waitcnt lgkmcnt(0)\n"
      "v_cmp_gt_i32 vcc, v14, v15\n"
      "buffer_load_dwordx4 v1, s[8:11], s51 offen offset:1024 sc1 lds\n"
      "s_cbranch_vccnz .Lsp_dy4_31\n"
      ".Lbk_dy4_31:\n"
      "s_cmp_lt_u32 s41, 32\n"
      "s_cselect_b32 s56, s39, 0x40000000\n"
      "ds_read_b128 v[56:59], v23 offset:0\n"
      "ds_read_b128 v[60:63], v23 offset:1024\n"
      "ds_read_b128 v[64:67], v23 offset:2048\n"
      "ds_read_b128 v[76:79], v23 offset:5120\n"
      "s_add_u32 s37, s37, 106496\n"
      "ds_read_b128 v[68:71], v23 offset:3072\n"
      "ds_read_b128 v[72:75], v23 offset:4096\n"
      "ds_read_b128 v[96:99], v23 offset:10240\n"
      "ds_read_b128 v[88:91], v23 offset:8192\n"
      "ds_read_b128 v[100:103], v23 offset:11264\n"
      "s_mov_b32 s51, s36\n"
      "ds_read_b128 v[92:95], v23 offset:9216\n"
      "ds_read_b128 v[116:119], v23 offset:15360\n"
      "s_add_u32 s36, s36, 165888\n"
      "ds_read_b128 v[84:87], v23 offset:7168\n"
      "ds_read_b128 v[80:83], v23 offset:6144\n"
      "s_add_u32 s58, s41, 4294967264\n"
      "s_min_u32 s36, s36, s6\n"
      "ds_read_b128 v[120:123], v23 offset:16384\n"
      "s_waitcnt vmcnt(0) lgkmcnt(0)\n"
      "s_nop 4\n"
      "s_branch .Ld_t4\n"
      ".Ld_in5:\n"
      "s_add_u32 s36, s36, 165888\n"
      "s_min_u32 s36, s36, s6\n"
      "s_mov_b32 s51, s36\n"
      "s_add_u32 s36, s36, 165888\n"
      "s_min_u32 s36, s36, s6\n"
      "s_mov_b32 s51, s36\n"
      "s_add_u32 s36, s36, 165888\n"
      "s_min_u32 s36, s36, s6\n"
      "s_mov_b32 s51, s36\n"
      "s_add_u32 s38, s38, 147456\n"
      "s_add_u32 s36, s36, 165888\n"
      "s_min_u32 s36, s36, s6\n"
      "s_add_u32 s52, s52, 106496\n"
      "s_mov_b32 s51, s36\n"
      "s_add_u32 s37, s37, 173056\n"
      "s_add_u32 s36, s36, 165888\n"
      "s_min_u32 s36, s36, s6\n"
      "s_mov_b32 s51, s36\n"
      "s_add_u32 s37, s37, 106496\n"
      "s_add_u32 s36, s36, 165888\n"
      "s_min_u32 s36, s36, s6\n"
      "buffer_load_dword v20, v2, s[8:11], s36 offen sc1\n"
      "s_add_u32 s52, s52, 173056\n"
      "s_mov_b32 s51, s36\n"
      "s_add_u32 s36, s36, 165888\n"
      "s_waitcnt vmcnt(0)\n"
      "v_cmp_ne_u32 vcc, s21, v20\n"
      "s_add_u32 m0, s43, 122880\n"
      "s_cbranch_vccnz .Lsp_d5_32\n"
      ".Lbk_d5_32:\n"
      "buffer_load_dwordx4 v0, s[8:11], s51 offen offset:2048 sc1 lds\n"
      "buffer_load_dwordx4 v0, s[8:11], s51 offen offset:0 sc1 lds\n"
      "buffer_load_dwordx4 v0, s[8:11], s51 offen offset:1024 sc1 lds\n"
      "s_min_u32 s36, s36, s6\n"
      "buffer_load_dwordx4 v0, s[8:11], s51 offen offset:3072 sc1 lds\n"
      "s_add_u32 m0, s43, 126976\n"
      "buffer_load_dword v20, v2, s[8:11], s36 offen sc1\n"
      "s_add_u32 s52, s52, 106496\n"
      "buffer_load_dwordx4 v1, s[8:11], s51 offen offset:0 sc1 lds\n"
      "buffer_load_dwordx4 v1, s[8:11], s51 offen offset:1024 sc1 lds\n"
      "s_cmp_lt_u32 s41, 32\n"
      "s_cselect_b32 s56, s39, 0x40000000\n"
      "s_add_u32 s37, s37, 106496\n"
      "s_mov_b32 s51, s36\n"
      "s_add_u32 s36, s36, 165888\n"
      "s_add_u32 s58, s41, 4294967264\n"
      "s_min_u32 s36, s36, s6\n"
      "s_add_u32 s39, s39, 147456\n"
      "s_cmp_lt_u32 s58, 32\n"
      "s_cselect_b32 s57, s40, 0x40000000\n"
      "s_add_u32 s52, s52, 106496\n"
      "v_add_u32 v14, 1, v14\n"
      "s_add_u32 m0, s43, 0\n"
      "s_add_u32 s40, s40, 4294856704\n"
      "s_add_u32 s41, s41, 6\n"
      "s_add_u32 s53, s53, 599040\n"
      "s_waitcnt vmcnt(2)\n"
      "v_cmp_ne_u32 vcc, s21, v20\n"
      "s_waitcnt vmcnt(0)\n"
      "ds_write_b32 v16, v14\n"
      "s_cbranch_vccnz .Lsp_d0_33\n"
      ".Lbk_d0_33:\n"
      "buffer_load_dwordx4 v0, s[8:11], s51 offen offset:1024 sc1 lds\n"
      "buffer_load_dwordx4 v0, s[8:11], s51 offen offset:2048 sc1 lds\n"
      "buffer_load_dwordx4 v0, s[8:11], s51 offen offset:0 sc1 lds\n"
      "ds_read_b32 v15, v17\n"
      "buffer_load_dword v20, v2, s[8:11], s36 offen sc1\n"
      "s_waitcnt lgkmcnt(0)\n"
      "v_cmp_gt_i32 vcc, v14, v15\n"
      "buffer_load_dwordx4 v0, s[8:11], s51 offen offset:3072 sc1 lds\n"
      "s_add_u32 m0, s43, 4096\n"
      "s_cbranch_vccnz .Lsp_dy5_34\n"
      ".Lbk_dy5_34:\n"
      "ds_read_b128 v[60:63], v23 offset:25600\n"
      "buffer_load_dwordx4 v1, s[8:11], s51 offen offset:0 sc1 lds\n"
      "buffer_load_dwordx4 v1, s[8:11], s51 offen offset:1024 sc1 lds\n"
      "ds_read_b128 v[76:79], v23 offset:29696\n"
      "ds_read_b128 v[64:67], v23 offset:26624\n"
      "ds_read_b128 v[92:95], v23 offset:33792\n"
      "ds_read_b128 v[72:75], v23 offset:28672\n"
      "ds_read_b128 v[84:87], v23 offset:31744\n"
      "ds_read_b128 v[96:99], v23 offset:34816\n"
      "ds_read_b128 v[80:83], v23 offset:30720\n"
      "ds_read_b128 v[56:59], v23 offset:24576\n"
      "ds_read_b128 v[108:111], v23 offset:37888\n"
      "s_add_u32 s37, s37, 106496\n"
      "ds_read_b128 v[88:91], v23 offset:32768\n"
      "ds_read_b128 v[68:71], v23 offset:27648\n"
      "ds_read_b128 v[100:103], v23 offset:35840\n"
      "ds_read_b128 v[104:107], v23 offset:36864\n"
      "s_mov_b32 s51, s36\n"
      "ds_read_b128 v[128:131], v23 offset:43008\n"
      "s_waitcnt vmcnt(0) lgkmcnt(0)\n"
      "s_nop 4\n"
      "s_branch .Ld_t5\n"
      ".Ld_end:\n"
      "s_endpgm\n"
      ".Lsp_d0_1:\n"
      "s_mov_b32 s22, s23\n"
      ".Lsl_d0_1:\n"
      "buffer_load_dword v20, v2, s[8:11], s51 offen sc1\n"
      "s_waitcnt vmcnt(0)\n"
      "v_cmp_ne_u32 vcc, s21, v20\n"
      "s_cbranch_vccz .Lbk_d0_1\n"
      "s_sleep 4\n"
      "s_sub_u32 s22, s22, 1\n"
      "s_cbranch_scc0 .Lsl_d0_1\n"
      "s_mov_b32 s23, 0\n"
      "s_branch .Lbk_d0_1\n"
      ".Lsp_d1_2:\n"
      "s_mov_b32 s22, s23\n"
      ".Lsl_d1_2:\n"
      "buffer_load_dword v20, v2, s[8:11], s51 offen sc1\n"
      "s_waitcnt vmcnt(0)\n"
      "v_cmp_ne_u32 vcc, s21, v20\n"
      "s_cbranch_vccz .Lbk_d1_2\n"
      "s_sleep 4\n"
      "s_sub_u32 s22, s22, 1\n"
      "s_cbranch_scc0 .Lsl_d1_2\n"
      "s_mov_b32 s23, 0\n"
      "s_branch .Lbk_d1_2\n"
      ".Lsp_dy0_3:\n"
      "s_lshl_b32 s22, s23, 4\n"
      ".Lsl_dy0_3:\n"
      "ds_read_b32 v15, v17\n"
      "s_waitcnt lgkmcnt(0)\n"
      "v_cmp_gt_i32 vcc, v14, v15\n"
      "s_cbranch_vccz .Lbk_dy0_3\n"
      "s_sub_u32 s22, s22, 1\n"
      "s_cbranch_scc0 .Lsl_dy0_3\n"
      "s_mov_b32 s23, 0\n"
      "s_branch .Lbk_dy0_3\n"
      ".Lsp_d2_4:\n"
      "s_mov_b32 s22, s23\n"
      "buffer_load_dword v20, v2, s[8:11], s51 offen sc1\n"
      "s_waitcnt vmcnt(0)\n"
      "v_cmp_ne_u32 vcc, s21, v20\n"
      "s_add_u32 s30, s52, 106496\n"
      "buffer_store_dword v18, v4, s[8:11], s30 offen sc1\n"
      "s_cbranch_vccz .Lbk_d2_4\n"
      ".Lsl_d2_4:\n"
      "buffer_load_dword v20, v2, s[8:11], s51 offen sc1\n"
      "s_waitcnt vmcnt(0)\n"
      "v_cmp_ne_u32 vcc, s21, v20\n"
      "s_cbranch_vccz .Lbk_d2_4\n"
      "s_sleep 4\n"
      "s_sub_u32 s22, s22, 1\n"
      "s_cbranch_scc0 .Lsl_d2_4\n"
      "s_mov_b32 s23, 0\n"
      "s_branch .Lbk_d2_4\n"
      ".Lsp_dy1_5:\n"
      "s_lshl_b32 s22, s23, 4\n"
      ".Lsl_dy1_5:\n"
      "ds_read_b32 v15, v17\n"
      "s_waitcnt lgkmcnt(0)\n"
      "v_cmp_gt_i32 vcc, v14, v15\n"
      "s_cbranch_vccz .Lbk_dy1_5\n"
      "s_sub_u32 s22, s22, 1\n"
      "s_cbranch_scc0 .Lsl_dy1_5\n"
      "s_mov_b32 s23, 0\n"
      "s_branch .Lbk_dy1_5\n"
      ".Lsp_d3_6:\n"
      "s_mov_b32 s22, s23\n"
      ".Lsl_d3_6:\n"
      "buffer_load_dword v20, v2, s[8:11], s51 offen sc1\n"
      "s_waitcnt vmcnt(0)\n"
      "v_cmp_ne_u32 vcc, s21, v20\n"
      "s_cbranch_vccz .Lbk_d3_6\n"
      "s_sleep 4\n"
      "s_sub_u32 s22, s22, 1\n"
      "s_cbranch_scc0 .Lsl_d3_6\n"
      "s_mov_b32 s23, 0\n"
      "s_branch .Lbk_d3_6\n"
      ".Lsp_dy2_7:\n"
      "s_lshl_b32 s22, s23, 4\n"
      ".Lsl_dy2_7:\n"
      "ds_read_b32 v15, v17\n"
      "s_waitcnt lgkmcnt(0)\n"
      "v_cmp_gt_i32 vcc, v14, v15\n"
      "s_cbranch_vccz .Lbk_dy2_7\n"
      "s_sub_u32 s22, s22, 1\n"
      "s_cbranch_scc0 .Lsl_dy2_7\n"
      "s_mov_b32 s23, 0\n"
      "s_branch .Lbk_dy2_7\n"
      ".Lsp_d4_8:\n"
      "s_mov_b32 s22, s23\n"
      "buffer_load_dword v20, v2, s[8:11], s51 offen sc1\n"
      "s_waitcnt vmcnt(0)\n"
      "v_cmp_ne_u32 vcc, s21, v20\n"
      "s_add_u32 s30, s52, 173056\n"
      "buffer_store_dword v18, v4, s[8:11], s30 offen sc1\n"
      "s_cbranch_vccz .Lbk_d4_8\n"
      ".Lsl_d4_8:\n"
      "buffer_load_dword v20, v2, s[8:11], s51 offen sc1\n"
      "s_waitcnt vmcnt(0)\n"
      "v_cmp_ne_u32 vcc, s21, v20\n"
      "s_cbranch_vccz .Lbk_d4_8\n"
      "s_sleep 4\n"
      "s_sub_u32 s22, s22, 1\n"
      "s_cbranch_scc0 .Lsl_d4_8\n"
      "s_mov_b32 s23, 0\n"
      "s_branch .Lbk_d4_8\n"
      ".Lsp_dy3_9:\n"
      "s_lshl_b32 s22, s23, 4\n"
      ".Lsl_dy3_9:\n"
      "ds_read_b32 v15, v17\n"
      "s_waitcnt lgkmcnt(0)\n"
      "v_cmp_gt_i32 vcc, v14, v15\n"
      "s_cbranch_vccz .Lbk_dy3_9\n"
      "s_sub_u32 s22, s22, 1\n"
      "s_cbranch_scc0 .Lsl_dy3_9\n"
      "s_mov_b32 s23, 0\n"
      "s_branch .Lbk_dy3_9\n"
      ".Lsp_d5_10:\n"
      "s_mov_b32 s22, s23\n"
      "buffer_load_dword v20, v2, s[8:11], s51 offen sc1\n"
      "s_waitcnt vmcnt(0)\n"
      "v_cmp_ne_u32 vcc, s21, v20\n"
      "s_add_u32 s30, s52, 106496\n"
      "buffer_store_dword v18, v4, s[8:11], s30 offen sc1\n"
      "s_cbranch_vccz .Lbk_d5_10\n"
      ".Lsl_d5_10:\n"
      "buffer_load_dword v20, v2, s[8:11], s51 offen sc1\n"
      "s_waitcnt vmcnt(0)\n"
      "v_cmp_ne_u32 vcc, s21, v20\n"
      "s_cbranch_vccz .Lbk_d5_10\n"
      "s_sleep 4\n"
      "s_sub_u32 s22, s22, 1\n"
      "s_cbranch_scc0 .Lsl_d5_10\n"
      "s_mov_b32 s23, 0\n"
      "s_branch .Lbk_d5_10\n"
      ".Lsp_dy4_11:\n"
      "s_lshl_b32 s22, s23, 4\n"
      ".Lsl_dy4_11:\n"
      "ds_read_b32 v15, v17\n"
      "s_waitcnt lgkmcnt(0)\n"
      "v_cmp_gt_i32 vcc, v14, v15\n"
      "s_cbranch_vccz .Lbk_dy4_11\n"
      "s_sub_u32 s22, s22, 1\n"
      "s_cbranch_scc0 .Lsl_dy4_11\n"
      "s_mov_b32 s23, 0\n"
      "s_branch .Lbk_dy4_11\n"
      ".Lsp_df_12:\n"
      "s_mov_b32 s22, s23\n"
      "buffer_load_dword v21, v3, s[8:11], s53 offen sc1\n"
      "s_waitcnt vmcnt(0)\n"
      "v_cmp_ne_u32 vcc, s21, v21\n"
      "s_add_u32 s30, s52, 0\n"
      "buffer_store_dword v18, v4, s[8:11], s30 offen sc1\n"
      "s_cbranch_vccz .Lbk_df_12\n"
      ".Lsl_df_12:\n"
      "buffer_load_dword v21, v3, s[8:11], s53 offen sc1\n"
      "s_waitcnt vmcnt(0)\n"
      "v_cmp_ne_u32 vcc, s21, v21\n"
      "s_cbranch_vccz .Lbk_df_12\n"
      "s_sleep 4\n"
      "s_sub_u32 s22, s22, 1\n"
      "s_cbranch_scc0 .Lsl_df_12\n"
      "s_mov_b32 s23, 0\n"
      "s_branch .Lbk_df_12\n"
      ".Lsp_d0_13:\n"
      "s_mov_b32 s22, s23\n"
      "buffer_load_dword v20, v2, s[8:11], s51 offen sc1\n"
      "s_waitcnt vmcnt(0)\n"
      "v_cmp_ne_u32 vcc, s21, v20\n"
      "s_add_u32 s30, s52, 0\n"
      "buffer_store_dword v18, v4, s[8:11], s30 offen sc1\n"
      "s_cbranch_vccz .Lbk_d0_13\n"
      ".Lsl_d0_13:\n"
      "buffer_load_dword v20, v2, s[8:11], s51 offen sc1\n"
      "s_waitcnt vmcnt(0)\n"
      "v_cmp_ne_u32 vcc, s21, v20\n"
      "s_cbranch_vccz .Lbk_d0_13\n"
      "s_sleep 4\n"
      "s_sub_u32 s22, s22, 1\n"
      "s_cbranch_scc0 .Lsl_d0_13\n"
      "s_mov_b32 s23, 0\n"
      "s_branch .Lbk_d0_13\n"
      ".Lsp_dy5_14:\n"
      "s_lshl_b32 s22, s23, 4\n"
      ".Lsl_dy5_14:\n"
      "ds_read_b32 v15, v17\n"
      "s_waitcnt lgkmcnt(0)\n"
      "v_cmp_gt_i32 vcc, v14, v15\n"
      "s_cbranch_vccz .Lbk_dy5_14\n"
      "s_sub_u32 s22, s22, 1\n"
      "s_cbranch_scc0 .Lsl_dy5_14\n"
      "s_mov_b32 s23, 0\n"
      "s_branch .Lbk_dy5_14\n"
      ".Lsp_d1_15:\n"
      "s_mov_b32 s22, s23\n"
      "buffer_load_dword v20, v2, s[8:11], s51 offen sc1\n"
      "s_waitcnt vmcnt(0)\n"
      "v_cmp_ne_u32 vcc, s21, v20\n"
      "s_add_u32 s30, s52, 0\n"
      "buffer_store_dword v18, v4, s[8:11], s30 offen sc1\n"
      "s_cbranch_vccz .Lbk_d1_15\n"
      ".Lsl_d1_15:\n"
      "buffer_load_dword v20, v2, s[8:11], s51 offen sc1\n"
      "s_waitcnt vmcnt(0)\n"
      "v_cmp_ne_u32 vcc, s21, v20\n"
      "s_cbranch_vccz .Lbk_d1_15\n"
      "s_sleep 4\n"
      "s_sub_u32 s22, s22, 1\n"
      "s_cbranch_scc0 .Lsl_d1_15\n"
      "s_mov_b32 s23, 0\n"
      "s_branch .Lbk_d1_15\n"
      ".Lsp_dy0_16:\n"
      "s_lshl_b32 s22, s23, 4\n"
      ".Lsl_dy0_16:\n"
      "ds_read_b32 v15, v17\n"
      "s_waitcnt lgkmcnt(0)\n"
      "v_cmp_gt_i32 vcc, v14, v15\n"
      "s_cbranch_vccz .Lbk_dy0_16\n"
      "s_sub_u32 s22, s22, 1\n"
      "s_cbranch_scc0 .Lsl_dy0_16\n"
      "s_mov_b32 s23, 0\n"
      "s_branch .Lbk_dy0_16\n"
      ".Lsp_df_17:\n"
      "s_mov_b32 s22, s23\n"
      "buffer_load_dword v21, v3, s[8:11], s53 offen sc1\n"
      "s_waitcnt vmcnt(0)\n"
      "v_cmp_ne_u32 vcc, s21, v21\n"
      "s_add_u32 s30, s52, 0\n"
      "buffer_store_dword v18, v4, s[8:11], s30 offen sc1\n"
      "s_cbranch_vccz .Lbk_df_17\n"
      ".Lsl_df_17:\n"
      "buffer_load_dword v21, v3, s[8:11], s53 offen sc1\n"
      "s_waitcnt vmcnt(0)\n"
      "v_cmp_ne_u32 vcc, s21, v21\n"
      "s_cbranch_vccz .Lbk_df_17\n"
      "s_sleep 4\n"
      "s_sub_u32 s22, s22, 1\n"
      "s_cbranch_scc0 .Lsl_df_17\n"
      "s_mov_b32 s23, 0\n"
      "s_branch .Lbk_df_17\n"
      ".Lsp_df_18:\n"
      "s_mov_b32 s22, s23\n"
      "buffer_load_dword v21, v3, s[8:11], s53 offen sc1\n"
      "s_waitcnt vmcnt(0)\n"
      "v_cmp_ne_u32 vcc, s21, v21\n"
      "s_add_u32 s30, s52, 0\n"
      "buffer_store_dword v18, v4, s[8:11], s30 offen sc1\n"
      "s_cbranch_vccz .Lbk_df_18\n"
      ".Lsl_df_18:\n"
      "buffer_load_dword v21, v3, s[8:11], s53 offen sc1\n"
      "s_waitcnt vmcnt(0)\n"
      "v_cmp_ne_u32 vcc, s21, v21\n"
      "s_cbranch_vccz .Lbk_df_18\n"
      "s_sleep 4\n"
      "s_sub_u32 s22, s22, 1\n"
      "s_cbranch_scc0 .Lsl_df_18\n"
      "s_mov_b32 s23, 0\n"
      "s_branch .Lbk_df_18\n"
      ".Lsp_df_19:\n"
      "s_mov_b32 s22, s23\n"
      "buffer_load_dword v21, v3, s[8:11], s53 offen sc1\n"
      "s_waitcnt vmcnt(0)\n"
      "v_cmp_ne_u32 vcc, s21, v21\n"
      "s_add_u32 s30, s52, 0\n"
      "buffer_store_dword v18, v4, s[8:11], s30 offen sc1\n"
      "s_cbranch_vccz .Lbk_df_19\n"
      ".Lsl_df_19:\n"
      "buffer_load_dword v21, v3, s[8:11], s53 offen sc1\n"
      "s_waitcnt vmcnt(0)\n"
      "v_cmp_ne_u32 vcc, s21, v21\n"
      "s_cbranch_vccz .Lbk_df_19\n"
      "s_sleep 4\n"
      "s_sub_u32 s22, s22, 1\n"
      "s_cbranch_scc0 .Lsl_df_19\n"
      "s_mov_b32 s23, 0\n"
      "s_branch .Lbk_df_19\n"
      ".Lsp_d1_20:\n"
      "s_mov_b32 s22, s23\n"
      ".Lsl_d1_20:\n"
      "buffer_load_dword v20, v2, s[8:11], s51 offen sc1\n"
      "s_waitcnt vmcnt(0)\n"
      "v_cmp_ne_u32 vcc, s21, v20\n"
      "s_cbranch_vccz .Lbk_d1_20\n"
      "s_sleep 4\n"
      "s_sub_u32 s22, s22, 1\n"
      "s_cbranch_scc0 .Lsl_d1_20\n"
      "s_mov_b32 s23, 0\n"
      "s_branch .Lbk_d1_20\n"
      ".Lsp_d2_21:\n"
      "s_mov_b32 s22, s23\n"
      ".Lsl_d2_21:\n"
      "buffer_load_dword v20, v2, s[8:11], s51 offen sc1\n"
      "s_waitcnt vmcnt(0)\n"
      "v_cmp_ne_u32 vcc, s21, v20\n"
      "s_cbranch_vccz .Lbk_d2_21\n"
      "s_sleep 4\n"
      "s_sub_u32 s22, s22, 1\n"
      "s_cbranch_scc0 .Lsl_d2_21\n"
      "s_mov_b32 s23, 0\n"
      "s_branch .Lbk_d2_21\n"
      ".Lsp_dy1_22:\n"
      "s_lshl_b32 s22, s23, 4\n"
      ".Lsl_dy1_22:\n"
      "ds_read_b32 v15, v17\n"
      "s_waitcnt lgkmcnt(0)\n"
      "v_cmp_gt_i32 vcc, v14, v15\n"
      "s_cbranch_vccz .Lbk_dy1_22\n"
      "s_sub_u32 s22, s22, 1\n"
      "s_cbranch_scc0 .Lsl_dy1_22\n"
      "s_mov_b32 s23, 0\n"
      "s_branch .Lbk_dy1_22\n"
      ".Lsp_d2_23:\n"
      "s_mov_b32 s22, s23\n"
      ".Lsl_d2_23:\n"
      "buffer_load_dword v20, v2, s[8:11], s51 offen sc1\n"
      "s_waitcnt vmcnt(0)\n"
      "v_cmp_ne_u32 vcc, s21, v20\n"
      "s_cbranch_vccz .Lbk_d2_23\n"
      "s_sleep 4\n"
      "s_sub_u32 s22, s22, 1\n"
      "s_cbranch_scc0 .Lsl_d2_23\n"
      "s_mov_b32 s23, 0\n"
      "s_branch .Lbk_d2_23\n"
      ".Lsp_d3_24:\n"
      "s_mov_b32 s22, s23\n"
      ".Lsl_d3_24:\n"
      "buffer_load_dword v20, v2, s[8:11], s51 offen sc1\n"
      "s_waitcnt vmcnt(0)\n"
      "v_cmp_ne_u32 vcc, s21, v20\n"
      "s_cbranch_vccz .Lbk_d3_24\n"
      "s_sleep 4\n"
      "s_sub_u32 s22, s22, 1\n"
      "s_cbranch_scc0 .Lsl_d3_24\n"
      "s_mov_b32 s23, 0\n"
      "s_branch .Lbk_d3_24\n"
      ".Lsp_dy2_25:\n"
      "s_lshl_b32 s22, s23, 4\n"
      ".Lsl_dy2_25:\n"
      "ds_read_b32 v15, v17\n"
      "s_waitcnt lgkmcnt(0)\n"
      "v_cmp_gt_i32 vcc, v14, v15\n"
      "s_cbranch_vccz .Lbk_dy2_25\n"
      "s_sub_u32 s22, s22, 1\n"
      "s_cbranch_scc0 .Lsl_dy2_25\n"
      "s_mov_b32 s23, 0\n"
      "s_branch .Lbk_dy2_25\n"
      ".Lsp_d3_26:\n"
      "s_mov_b32 s22, s23\n"
      ".Lsl_d3_26:\n"
      "buffer_load_dword v20, v2, s[8:11], s51 offen sc1\n"
      "s_waitcnt vmcnt(0)\n"
      "v_cmp_ne_u32 vcc, s21, v20\n"
      "s_cbranch_vccz .Lbk_d3_26\n"
      "s_sleep 4\n"
      "s_sub_u32 s22, s22, 1\n"
      "s_cbranch_scc0 .Lsl_d3_26\n"
      "s_mov_b32 s23, 0\n"
      "s_branch .Lbk_d3_26\n"
      ".Lsp_d4_27:\n"
      "s_mov_b32 s22, s23\n"
      ".Lsl_d4_27:\n"
      "buffer_load_dword v20, v2, s[8:11], s51 offen sc1\n"
      "s_waitcnt vmcnt(0)\n"
      "v_cmp_ne_u32 vcc, s21, v20\n"
      "s_cbranch_vccz .Lbk_d4_27\n"
      "s_sleep 4\n"
      "s_sub_u32 s22, s22, 1\n"
      "s_cbranch_scc0 .Lsl_d4_27\n"
      "s_mov_b32 s23, 0\n"
      "s_branch .Lbk_d4_27\n"
      ".Lsp_dy3_28:\n"
      "s_lshl_b32 s22, s23, 4\n"
      ".Lsl_dy3_28:\n"
      "ds_read_b32 v15, v17\n"
      "s_waitcnt lgkmcnt(0)\n"
      "v_cmp_gt_i32 vcc, v14, v15\n"
      "s_cbranch_vccz .Lbk_dy3_28\n"
      "s_sub_u32 s22, s22, 1\n"
      "s_cbranch_scc0 .Lsl_dy3_28\n"
      "s_mov_b32 s23, 0\n"
      "s_branch .Lbk_dy3_28\n"
      ".Lsp_d4_29:\n"
      "s_mov_b32 s22, s23\n"
      ".Lsl_d4_29:\n"
      "buffer_load_dword v20, v2, s[8:11], s51 offen sc1\n"
      "s_waitcnt vmcnt(0)\n"
      "v_cmp_ne_u32 vcc, s21, v20\n"
      "s_cbranch_vccz .Lbk_d4_29\n"
      "s_sleep 4\n"
      "s_sub_u32 s22, s22, 1\n"
      "s_cbranch_scc0 .Lsl_d4_29\n"
      "s_mov_b32 s23, 0\n"
      "s_branch .Lbk_d4_29\n"
      ".Lsp_d5_30:\n"
      "s_mov_b32 s22, s23\n"
      ".Lsl_d5_30:\n"
      "buffer_load_dword v20, v2, s[8:11], s51 offen sc1\n"
      "s_waitcnt vmcnt(0)\n"
      "v_cmp_ne_u32 vcc, s21, v20\n"
      "s_cbranch_vccz .Lbk_d5_30\n"
      "s_sleep 4\n"
      "s_sub_u32 s22, s22, 1\n"
      "s_cbranch_scc0 .Lsl_d5_30\n"
      "s_mov_b32 s23, 0\n"
      "s_branch .Lbk_d5_30\n"
      ".Lsp_dy4_31:\n"
      "s_lshl_b32 s22, s23, 4\n"
      ".Lsl_dy4_31:\n"
      "ds_read_b32 v15, v17\n"
      "s_waitcnt lgkmcnt(0)\n"
      "v_cmp_gt_i32 vcc, v14, v15\n"
      "s_cbranch_vccz .Lbk_dy4_31\n"
      "s_sub_u32 s22, s22, 1\n"
      "s_cbranch_scc0 .Lsl_dy4_31\n"
      "s_mov_b32 s23, 0\n"
      "s_branch .Lbk_dy4_31\n"
      ".Lsp_d5_32:\n"
      "s_mov_b32 s22, s23\n"
      ".Lsl_d5_32:\n"
      "buffer_load_dword v20, v2, s[8:11], s51 offen sc1\n"
      "s_waitcnt vmcnt(0)\n"
      "v_cmp_ne_u32 vcc, s21, v20\n"
      "s_cbranch_vccz .Lbk_d5_32\n"
      "s_sleep 4\n"
      "s_sub_u32 s22, s22, 1\n"
      "s_cbranch_scc0 .Lsl_d5_32\n"
      "s_mov_b32 s23, 0\n"
      "s_branch .Lbk_d5_32\n"
      ".Lsp_d0_33:\n"
      "s_mov_b32 s22, s23\n"
      ".Lsl_d0_33:\n"
      "buffer_load_dword v20, v2, s[8:11], s51 offen sc1\n"
      "s_waitcnt vmcnt(0)\n"
      "v_cmp_ne_u32 vcc, s21, v20\n"
      "s_cbranch_vccz .Lbk_d0_33\n"
      "s_sleep 4\n"
      "s_sub_u32 s22, s22, 1\n"
      "s_cbranch_scc0 .Lsl_d0_33\n"
      "s_mov_b32 s23, 0\n"
      "s_branch .Lbk_d0_33\n"
      ".Lsp_dy5_34:\n"
      "s_lshl_b32 s22, s23, 4\n"
      ".Lsl_dy5_34:\n"
      "ds_read_b32 v15, v17\n"
      "s_waitcnt lgkmcnt(0)\n"
      "v_cmp_gt_i32 vcc, v14, v15\n"
      "s_cbranch_vccz .Lbk_dy5_34\n"
      "s_sub_u32 s22, s22, 1\n"
      "s_cbranch_scc0 .Lsl_dy5_34\n"
      "s_mov_b32 s23, 0\n"
      "s_branch .Lbk_dy5_34\n"
      ".Ld_last:\n"
      :
      : "{s[8:11]}"(rsWS), "{s[12:15]}"(rsA), "{s[16:19]}"(rsB), "{s[92:95]}"(rsC), "{v0}"(v_l16), "{v1}"(v_l16b), "{v2}"(v_fa), "{v3}"(v_ga), "{v4}"(v_ra), "{v5}"(v_rf), "{v6}"(v_xp), "{v7}"(v_xs), "{v8}"(v_op), "{v9}"(v_os), "{v10}"(v_sw), "{v11}"(v_sr), "{v12}"(v_ss), "{v13}"(v_l8), "{v15}"(v_al), "{v16}"(v_yw), "{v17}"(v_yr), "{v19}"(v_hi), "{v176}"(v_at), "{v177}"(v_as), "{v178}"(v_vo0), "{v179}"(v_vo1), "{v180}"(v_vo2), "{v181}"(v_pw), "{v183}"(v_pu), "{s36}"(s_sh), "{s37}"(s_ps), "{s38}"(s_x1), "{s39}"(s_xr), "{s40}"(s_or), "{s41}"(s_p0), "{s42}"(s_m3), "{s43}"(s_m6), "{s44}"(s_so), "{s45}"(s_ph), "{s46}"(s_i0), "{s47}"(s_i5), "{s48}"(s_j0), "{s49}"(s_j5), "{s50}"(s_he), "{s60}"(s_k0)
      : "v255", "a255", "s99", "vcc", "scc", "memory");

  } else {
    const unsigned j = part, f = 4 * j + w;
    const u32x4 rsA = srd(w >> 1 ? Wu : Wg, 576 * 4608 * 2), rsC = srd(Wo, 576 * 576 * 2);
    const unsigned wv = (slot - (MAP ? 16 : 14)) * 4 + w, tw0 = (wv * 81 + lane) * 128;
    const unsigned v_tw[2] = {MAP ? (fast ? tw0 : OOB) : (lane & 7) < 5 ? (8 * wv + l8) * 1152 + (xcd >> 2) * 512 + (lane & 7) * 128 : OOB,
                              MAP && fast && lane < 17 ? tw0 + 8192 : OOB};
    const unsigned v_l5 = w < 3 ? v_l16 + 16384 - w * 3072 : OOB;
    const unsigned v_fa = 19456 - w * 4096 + (lane >> 3) * 128 + (lane & 7) * 4, v_ra = up * 8 * HREC + l5 + 147456 - f * 1024 + f * 128;
    const unsigned v_st = 18432 + i16 * 32 + q * 8, v_pw = 147456 - f * 1024 + f * 128 + l8 * HREC + l7;
    const unsigned v_at = 65536 + (8 * g + q) * 256 + ((4 * w + p) ^ (4 * q)) * 16;
    const unsigned v_vo0 = (16 * (w & 1) + g) * 9216 + 256 * j + (i16 ^ (4 * g)) * 16;
    const unsigned v_vo1 = v_vo0 + 4 * 9216 - 1024, v_vo2 = v_vo1 + 4 * 9216 - 1024, v_vo3 = v_vo2 + 4 * 9216 - 1024;
    const unsigned s_sx = WS_X1 + gr * NREC * REC + w * 4096, s_sh = WS_H + gr * NREC * HREC + f * 1024;
    const unsigned s_m4 = w * 4096, s_m5 = 16384 + w * 1024;
    asm volatile(
      "s_buffer_load_dword s21, s[8:11], 0\n"
      "s_mov_b32 s23, 65536\n"
      "s_mov_b32 s31, 0x40000000\n"
      "s_mov_b32 s41, 0\n"
      "s_sleep 16\n"
      "buffer_load_dword v58, v54, s[92:95], s41 offen\n"
      "buffer_load_dword v59, v55, s[92:95], s41 offen\n"
      "s_waitcnt vmcnt(0)\n"
      "s_add_u32 m0, s38, 65536\n"
      "s_nop 0\n"
      "buffer_load_dwordx4 v6, s[12:15], s41 offen offset:0 lds\n"
      "buffer_load_dwordx4 v7, s[12:15], s41 offen offset:1024 lds\n"
      "buffer_load_dwordx4 v8, s[12:15], s41 offen offset:2048 lds\n"
      "buffer_load_dwordx4 v9, s[12:15], s41 offen offset:3072 lds\n"
      "s_add_u32 s41, s41, 294912\n"
      "s_add_u32 m0, s38, 81920\n"
      "s_nop 0\n"
      "buffer_load_dwordx4 v6, s[12:15], s41 offen offset:0 lds\n"
      "buffer_load_dwordx4 v7, s[12:15], s41 offen offset:1024 lds\n"
      "buffer_load_dwordx4 v8, s[12:15], s41 offen offset:2048 lds\n"
      "buffer_load_dwordx4 v9, s[12:15], s41 offen offset:3072 lds\n"
      "s_add_u32 s41, s41, 294912\n"
      "s_add_u32 m0, s38, 98304\n"
      "s_nop 0\n"
      "buffer_load_dwordx4 v6, s[12:15], s41 offen offset:0 lds\n"
      "buffer_load_dwordx4 v7, s[12:15], s41 offen offset:1024 lds\n"
      "buffer_load_dwordx4 v8, s[12:15], s41 offen offset:2048 lds\n"
      "buffer_load_dwordx4 v9, s[12:15], s41 offen offset:3072 lds\n"
      "s_add_u32 s41, s41, 294912\n"
      "s_add_u32 m0, s38, 114688\n"
      "s_nop 0\n"
      "buffer_load_dwordx4 v6, s[12:15], s41 offen offset:0 lds\n"
      "buffer_load_dwordx4 v7, s[12:15], s41 offen offset:1024 lds\n"
      "buffer_load_dwordx4 v8, s[12:15], s41 offen offset:2048 lds\n"
      "buffer_load_dwordx4 v9, s[12:15], s41 offen offset:3072 lds\n"
      "s_add_u32 s41, s41, 294912\n"
      "s_add_u32 m0, s38, 131072\n"
      "s_nop 0\n"
      "buffer_load_dwordx4 v6, s[12:15], s41 offen offset:0 lds\n"
      "buffer_load_dwordx4 v7, s[12:15], s41 offen offset:1024 lds\n"
      "buffer_load_dwordx4 v8, s[12:15], s41 offen offset:2048 lds\n"
      "buffer_load_dwordx4 v9, s[12:15], s41 offen offset:3072 lds\n"
      "s_add_u32 s41, s41, 294912\n"
      "v_mov_b32 v172, 0x3f803f80\n"
      "v_mov_b32 v173, 0x3f803f80\n"
      "v_mov_b32 v174, 0x3f803f80\n"
      "v_mov_b32 v175, 0x3f803f80\n"
      "v_add_u32 v19, 49152, v5\n"
      "s_mov_b64 exec, 0\n"
      "s_lshr_b32 s22, s38, 12\n"
      ".Lg_pl:\n"
      "s_cmp_eq_u32 s22, 0\n"
      "s_cbranch_scc1 .Lg_p0\n"
      "s_cmp_eq_u32 s22, 1\n"
      "s_cbranch_scc1 .Lg_p1\n"
      "s_branch .Lg_pe\n"
      ".Lg_pr:\n"
      "s_add_u32 s22, s22, 4\n"
      "s_branch .Lg_pl\n"
      ".Lg_pe:\n"
      "s_mov_b64 exec, -1\n"
      "s_waitcnt vmcnt(16)\n"
      "s_barrier\n"
      "ds_read_b64_tr_b16 a[0:1], v5 offset:0\n"
      "ds_read_b64_tr_b16 a[2:3], v5 offset:1024\n"
      "ds_read_b64_tr_b16 a[72:73], v5 offset:8192\n"
      "ds_read_b64_tr_b16 a[74:75], v5 offset:9216\n"
      "ds_read_b64_tr_b16 a[144:145], v5 offset:8\n"
      "ds_read_b64_tr_b16 a[146:147], v5 offset:1032\n"
      "ds_read_b64_tr_b16 a[216:217], v5 offset:8200\n"
      "ds_read_b64_tr_b16 a[218:219], v5 offset:9224\n"
      "s_add_u32 m0, s38, 147456\n"
      "s_nop 0\n"
      "buffer_load_dwordx4 v6, s[12:15], s41 offen offset:0 lds\n"
      "buffer_load_dwordx4 v7, s[12:15], s41 offen offset:1024 lds\n"
      "buffer_load_dwordx4 v8, s[12:15], s41 offen offset:2048 lds\n"
      "buffer_load_dwordx4 v9, s[12:15], s41 offen offset:3072 lds\n"
      "s_add_u32 s41, s41, 294912\n"
      "s_waitcnt vmcnt(16) lgkmcnt(0)\n"
      "s_barrier\n"
      "ds_read_b64_tr_b16 a[4:5], v5 offset:16384\n"
      "ds_read_b64_tr_b16 a[6:7], v5 offset:17408\n"
      "ds_read_b64_tr_b16 a[76:77], v5 offset:24576\n"
      "ds_read_b64_tr_b16 a[78:79], v5 offset:25600\n"
      "ds_read_b64_tr_b16 a[148:149], v5 offset:16392\n"
      "ds_read_b64_tr_b16 a[150:151], v5 offset:17416\n"
      "ds_read_b64_tr_b16 a[220:221], v5 offset:24584\n"
      "ds_read_b64_tr_b16 a[222:223], v5 offset:25608\n"
      "v_mfma_f32_16x16x32_bf16 v[140:143], a[0:3], v[172:175], 0\n"
      "v_mfma_f32_16x16x32_bf16 v[144:147], a[72:75], v[172:175], 0\n"
      "v_mfma_f32_16x16x32_bf16 v[148:151], a[144:147], v[172:175], 0\n"
      "v_mfma_f32_16x16x32_bf16 v[152:155], a[216:219], v[172:175], 0\n"
      "s_add_u32 m0, s38, 65536\n"
      "s_nop 0\n"
      "buffer_load_dwordx4 v6, s[12:15], s41 offen offset:0 lds\n"
      "buffer_load_dwordx4 v7, s[12:15], s41 offen offset:1024 lds\n"
      "buffer_load_dwordx4 v8, s[12:15], s41 offen offset:2048 lds\n"
      "buffer_load_dwordx4 v9, s[12:15], s41 offen offset:3072 lds\n"
      "s_add_u32 s41, s41, 294912\n"
      "s_waitcnt vmcnt(16) lgkmcnt(0)\n"
      "s_barrier\n"
      "ds_read_b64_tr_b16 a[8:9], v5 offset:32768\n"
      "ds_read_b64_tr_b16 a[10:11], v5 offset:33792\n"
      "ds_read_b64_tr_b16 a[80:81], v5 offset:40960\n"
      "ds_read_b64_tr_b16 a[82:83], v5 offset:41984\n"
      "ds_read_b64_tr_b16 a[152:153], v5 offset:32776\n"
      "ds_read_b64_tr_b16 a[154:155], v5 offset:33800\n"
      "ds_read_b64_tr_b16 a[224:225], v5 offset:40968\n"
      "ds_read_b64_tr_b16 a[226:227], v5 offset:41992\n"
      "v_mfma_f32_16x16x32_bf16 v[140:143], a[4:7], v[172:175], v[140:143]\n"
      "v_mfma_f32_16x16x32_bf16 v[144:147], a[76:79], v[172:175], v[144:147]\n"
      "v_mfma_f32_16x16x32_bf16 v[148:151], a[148:151], v[172:175], v[148:151]\n"
      "v_mfma_f32_16x16x32_bf16 v[152:155], a[220:223], v[172:175], v[152:155]\n"
      "s_add_u32 m0, s38, 81920\n"
      "s_nop 0\n"
      "buffer_load_dwordx4 v6, s[12:15], s41 offen offset:0 lds\n"
      "buffer_load_dwordx4 v7, s[12:15], s41 offen offset:1024 lds\n"
      "buffer_load_dwordx4 v8, s[12:15], s41 offen offset:2048 lds\n"
      "buffer_load_dwordx4 v9, s[12:15], s41 offen offset:3072 lds\n"
      "s_add_u32 s41, s41, 294912\n"
      "s_waitcnt vmcnt(16) lgkmcnt(0)\n"
      "s_barrier\n"
      "ds_read_b64_tr_b16 a[12:13], v19 offset:0\n"
      "ds_read_b64_tr_b16 a[14:15], v19 offset:1024\n"
      "ds_read_b64_tr_b16 a[84:85], v19 offset:8192\n"
      "ds_read_b64_tr_b16 a[86:87], v19 offset:9216\n"
      "ds_read_b64_tr_b16 a[156:157], v19 offset:8\n"
      "ds_read_b64_tr_b16 a[158:159], v19 offset:1032\n"
      "ds_read_b64_tr_b16 a[228:229], v19 offset:8200\n"
      "ds_read_b64_tr_b16 a[230:231], v19 offset:9224\n"
      "v_mfma_f32_16x16x32_bf16 v[140:143], a[8:11], v[172:175], v[140:143]\n"
      "v_mfma_f32_16x16x32_bf16 v[144:147], a[80:83], v[172:175], v[144:147]\n"
      "v_mfma_f32_16x16x32_bf16 v[148:151], a[152:155], v[172:175], v[148:151]\n"
      "v_mfma_f32_16x16x32_bf16 v[152:155], a[224:227], v[172:175], v[152:155]\n"
      "s_add_u32 m0, s38, 98304\n"
      "s_nop 0\n"
      "buffer_load_dwordx4 v6, s[12:15], s41 offen offset:0 lds\n"
      "buffer_load_dwordx4 v7, s[12:15], s41 offen offset:1024 lds\n"
      "buffer_load_dwordx4 v8, s[12:15], s41 offen offset:2048 lds\n"
      "buffer_load_dwordx4 v9, s[12:15], s41 offen offset:3072 lds\n"
      "s_add_u32 s41, s41, 294912\n"
      "s_waitcnt vmcnt(16) lgkmcnt(0)\n"
      "s_barrier\n"
      "ds_read_b64_tr_b16 a[16:17], v19 offset:16384\n"
      "ds_read_b64_tr_b16 a[18:19], v19 offset:17408\n"
      "ds_read_b64_tr_b16 a[88:89], v19 offset:24576\n"
      "ds_read_b64_tr_b16 a[90:91], v19 offset:25600\n"
      "ds_read_b64_tr_b16 a[160:161], v19 offset:16392\n"
      "ds_read_b64_tr_b16 a[162:163], v19 offset:17416\n"
      "ds_read_b64_tr_b16 a[232:233], v19 offset:24584\n"
      "ds_read_b64_tr_b16 a[234:235], v19 offset:25608\n"
      "v_mfma_f32_16x16x32_bf16 v[140:143], a[12:15], v[172:175], v[140:143]\n"
      "v_mfma_f32_16x16x32_bf16 v[144:147], a[84:87], v[172:175], v[144:147]\n"
      "v_mfma_f32_16x16x32_bf16 v[148:151], a[156:159], v[172:175], v[148:151]\n"
      "v_mfma_f32_16x16x32_bf16 v[152:155], a[228:231], v[172:175], v[152:155]\n"
      "s_add_u32 m0, s38, 114688\n"
      "s_nop 0\n"
      "buffer_load_dwordx4 v6, s[12:15], s41 offen offset:0 lds\n"
      "buffer_load_dwordx4 v7, s[12:15], s41 offen offset:1024 lds\n"
      "buffer_load_dwordx4 v8, s[12:15], s41 offen offset:2048 lds\n"
      "buffer_load_dwordx4 v9, s[12:15], s41 offen offset:3072 lds\n"
      "s_add_u32 s41, s41, 294912\n"
      "s_waitcnt vmcnt(16) lgkmcnt(0)\n"
      "s_barrier\n"
      "ds_read_b64_tr_b16 a[20:21], v19 offset:32768\n"
      "ds_read_b64_tr_b16 a[22:23], v19 offset:33792\n"
      "ds_read_b64_tr_b16 a[92:93], v19 offset:40960\n"
      "ds_read_b64_tr_b16 a[94:95], v19 offset:41984\n"
      "ds_read_b64_tr_b16 a[164:165], v19 offset:32776\n"
      "ds_read_b64_tr_b16 a[166:167], v19 offset:33800\n"
      "ds_read_b64_tr_b16 a[236:237], v19 offset:40968\n"
      "ds_read_b64_tr_b16 a[238:239], v19 offset:41992\n"
      "v_mfma_f32_16x16x32_bf16 v[140:143], a[16:19], v[172:175], v[140:143]\n"
      "v_mfma_f32_16x16x32_bf16 v[144:147], a[88:91], v[172:175], v[144:147]\n"
      "v_mfma_f32_16x16x32_bf16 v[148:151], a[160:163], v[172:175], v[148:151]\n"
      "v_mfma_f32_16x16x32_bf16 v[152:155], a[232:235], v[172:175], v[152:155]\n"
      "s_add_u32 m0, s38, 131072\n"
      "s_nop 0\n"
      "buffer_load_dwordx4 v6, s[12:15], s41 offen offset:0 lds\n"
      "buffer_load_dwordx4 v7, s[12:15], s41 offen offset:1024 lds\n"
      "buffer_load_dwordx4 v8, s[12:15], s41 offen offset:2048 lds\n"
      "buffer_load_dwordx4 v9, s[12:15], s41 offen offset:3072 lds\n"
      "s_add_u32 s41, s41, 294912\n"
      "s_waitcnt vmcnt(16) lgkmcnt(0)\n"
      "s_barrier\n"
      "ds_read_b64_tr_b16 a[24:25], v5 offset:0\n"
      "ds_read_b64_tr_b16 a[26:27], v5 offset:1024\n"
      "ds_read_b64_tr_b16 a[96:97], v5 offset:8192\n"
      "ds_read_b64_tr_b16 a[98:99], v5 offset:9216\n"
      "ds_read_b64_tr_b16 a[168:169], v5 offset:8\n"
      "ds_read_b64_tr_b16 a[170:171], v5 offset:1032\n"
      "ds_read_b64_tr_b16 a[240:241], v5 offset:8200\n"
      "ds_read_b64_tr_b16 a[242:243], v5 offset:9224\n"
      "v_mfma_f32_16x16x32_bf16 v[140:143], a[20:23], v[172:175], v[140:143]\n"
      "v_mfma_f32_16x16x32_bf16 v[144:147], a[92:95], v[172:175], v[144:147]\n"
      "v_mfma_f32_16x16x32_bf16 v[148:151], a[164:167], v[172:175], v[148:151]\n"
      "v_mfma_f32_16x16x32_bf16 v[152:155], a[236:239], v[172:175], v[152:155]\n"
      "s_add_u32 m0, s38, 147456\n"
      "s_nop 0\n"
      "buffer_load_dwordx4 v6, s[12:15], s41 offen offset:0 lds\n"
      "buffer_load_dwordx4 v7, s[12:15], s41 offen offset:1024 lds\n"
      "buffer_load_dwordx4 v8, s[12:15], s41 offen offset:2048 lds\n"
      "buffer_load_dwordx4 v9, s[12:15], s41 offen offset:3072 lds\n"
      "s_add_u32 s41, s41, 294912\n"
      "s_waitcnt vmcnt(16) lgkmcnt(0)\n"
      "s_barrier\n"
      "ds_read_b64_tr_b16 a[28:29], v5 offset:16384\n"
      "ds_read_b64_tr_b16 a[30:31], v5 offset:17408\n"
      "ds_read_b64_tr_b16 a[100:101], v5 offset:24576\n"
      "ds_read_b64_tr_b16 a[102:103], v5 offset:25600\n"
      "ds_read_b64_tr_b16 a[172:173], v5 offset:16392\n"
      "ds_read_b64_tr_b16 a[174:175], v5 offset:17416\n"
      "ds_read_b64_tr_b16 a[244:245], v5 offset:24584\n"
      "ds_read_b64_tr_b16 a[246:247], v5 offset:25608\n"
      "v_mfma_f32_16x16x32_bf16 v[140:143], a[24:27], v[172:175], v[140:143]\n"
      "v_mfma_f32_16x16x32_bf16 v[144:147], a[96:99], v[172:175], v[144:147]\n"
      "v_mfma_f32_16x16x32_bf16 v[148:151], a[168:171], v[172:175], v[148:151]\n"
      "v_mfma_f32_16x16x32_bf16 v[152:155], a[240:243], v[172:175], v[152:155]\n"
      "s_add_u32 m0, s38, 65536\n"
      "s_nop 0\n"
      "buffer_load_dwordx4 v6, s[12:15], s41 offen offset:0 lds\n"
      "buffer_load_dwordx4 v7, s[12:15], s41 offen offset:1024 lds\n"
      "buffer_load_dwordx4 v8, s[12:15], s41 offen offset:2048 lds\n"
      "buffer_load_dwordx4 v9, s[12:15], s41 offen offset:3072 lds\n"
      "s_add_u32 s41, s41, 294912\n"
      "s_waitcnt vmcnt(16) lgkmcnt(0)\n"
      "s_barrier\n"
      "ds_read_b64_tr_b16 a[32:33], v5 offset:32768\n"
      "ds_read_b64_tr_b16 a[34:35], v5 offset:33792\n"
      "ds_read_b64_tr_b16 a[104:105], v5 offset:40960\n"
      "ds_read_b64_tr_b16 a[106:107], v5 offset:41984\n"
      "ds_read_b64_tr_b16 a[176:177], v5 offset:32776\n"
      "ds_read_b64_tr_b16 a[178:179], v5 offset:33800\n"
      "ds_read_b64_tr_b16 a[248:249], v5 offset:40968\n"
      "ds_read_b64_tr_b16 a[250:251], v5 offset:41992\n"
      "v_mfma_f32_16x16x32_bf16 v[140:143], a[28:31], v[172:175], v[140:143]\n"
      "v_mfma_f32_16x16x32_bf16 v[144:147], a[100:103], v[172:175], v[144:147]\n"
      "v_mfma_f32_16x16x32_bf16 v[148:151], a[172:175], v[172:175], v[148:151]\n"
      "v_mfma_f32_16x16x32_bf16 v[152:155], a[244:247], v[172:175], v[152:155]\n"
      "s_add_u32 m0, s38, 81920\n"
      "s_nop 0\n"
      "buffer_load_dwordx4 v6, s[12:15], s41 offen offset:0 lds\n"
      "buffer_load_dwordx4 v7, s[12:15], s41 offen offset:1024 lds\n"
      "buffer_load_dwordx4 v8, s[12:15], s41 offen offset:2048 lds\n"
      "buffer_load_dwordx4 v9, s[12:15], s41 offen offset:3072 lds\n"
      "s_add_u32 s41, s41, 294912\n"
      "s_waitcnt vmcnt(16) lgkmcnt(0)\n"
      "s_barrier\n"
      "ds_read_b64_tr_b16 a[36:37], v19 offset:0\n"
      "ds_read_b64_tr_b16 a[38:39], v19 offset:1024\n"
      "ds_read_b64_tr_b16 a[108:109], v19 offset:8192\n"
      "ds_read_b64_tr_b16 a[110:111], v19 offset:9216\n"
      "ds_read_b64_tr_b16 a[180:181], v19 offset:8\n"
      "ds_read_b64_tr_b16 a[182:183], v19 offset:1032\n"
      "ds_read_b64_tr_b16 a[252:253], v19 offset:8200\n"
      "ds_read_b64_tr_b16 a[254:255], v19 offset:9224\n"
      "v_mfma_f32_16x16x32_bf16 v[140:143], a[32:35], v[172:175], v[140:143]\n"
      "v_mfma_f32_16x16x32_bf16 v[144:147], a[104:107], v[172:175], v[144:147]\n"
      "v_mfma_f32_16x16x32_bf16 v[148:151], a[176:179], v[172:175], v[148:151]\n"
      "v_mfma_f32_16x16x32_bf16 v[152:155], a[248:251], v[172:175], v[152:155]\n"
      "s_add_u32 m0, s38, 98304\n"
      "s_nop 0\n"
      "buffer_load_dwordx4 v6, s[12:15], s41 offen offset:0 lds\n"
      "buffer_load_dwordx4 v7, s[12:15], s41 offen offset:1024 lds\n"
      "buffer_load_dwordx4 v8, s[12:15], s41 offen offset:2048 lds\n"
      "buffer_load_dwordx4 v9, s[12:15], s41 offen offset:3072 lds\n"
      "s_add_u32 s41, s41, 294912\n"
      "s_waitcnt vmcnt(16) lgkmcnt(0)\n"
      "s_barrier\n"
      "ds_read_b64_tr_b16 a[40:41], v19 offset:16384\n"
      "ds_read_b64_tr_b16 a[42:43], v19 offset:17408\n"
      "ds_read_b64_tr_b16 a[112:113], v19 offset:24576\n"
      "ds_read_b64_tr_b16 a[114:115], v19 offset:25600\n"
      "ds_read_b64_tr_b16 a[184:185], v19 offset:16392\n"
      "ds_read_b64_tr_b16 a[186:187], v19 offset:17416\n"
      "ds_read_b64_tr_b16 v[20:21], v19 offset:24584\n"
      "ds_read_b64_tr_b16 v[22:23], v19 offset:25608\n"
      "v_mfma_f32_16x16x32_bf16 v[140:143], a[36:39], v[172:175], v[140:143]\n"
      "v_mfma_f32_16x16x32_bf16 v[144:147], a[108:111], v[172:175], v[144:147]\n"
      "v_mfma_f32_16x16x32_bf16 v[148:151], a[180:183], v[172:175], v[148:151]\n"
      "v_mfma_f32_16x16x32_bf16 v[152:155], a[252:255], v[172:175], v[152:155]\n"
      "s_add_u32 m0, s38, 114688\n"
      "s_nop 0\n"
      "buffer_load_dwordx4 v6, s[12:15], s41 offen offset:0 lds\n"
      "buffer_load_dwordx4 v7, s[12:15], s41 offen offset:1024 lds\n"
      "buffer_load_dwordx4 v8, s[12:15], s41 offen offset:2048 lds\n"
      "buffer_load_dwordx4 v9, s[12:15], s41 offen offset:3072 lds\n"
      "s_add_u32 s41, s41, 294912\n"
      "s_waitcnt vmcnt(16) lgkmcnt(0)\n"
      "s_barrier\n"
      "ds_read_b64_tr_b16 a[44:45], v19 offset:32768\n"
      "ds_read_b64_tr_b16 a[46:47], v19 offset:33792\n"
      "ds_read_b64_tr_b16 a[116:117], v19 offset:40960\n"
      "ds_read_b64_tr_b16 a[118:119], v19 offset:41984\n"
      "ds_read_b64_tr_b16 a[188:189], v19 offset:32776\n"
      "ds_read_b64_tr_b16 a[190:191], v19 offset:33800\n"
      "ds_read_b64_tr_b16 v[24:25], v19 offset:40968\n"
      "ds_read_b64_tr_b16 v[26:27], v19 offset:41992\n"
      "v_mfma_f32_16x16x32_bf16 v[140:143], a[40:43], v[172:175], v[140:143]\n"
      "v_mfma_f32_16x16x32_bf16 v[144:147], a[112:115], v[172:175], v[144:147]\n"
      "v_mfma_f32_16x16x32_bf16 v[148:151], a[184:187], v[172:175], v[148:151]\n"
      "v_mfma_f32_16x16x32_bf16 v[152:155], v[20:23], v[172:175], v[152:155]\n"
      "s_add_u32 m0, s38, 131072\n"
      "s_nop 0\n"
      "buffer_load_dwordx4 v6, s[12:15], s41 offen offset:0 lds\n"
      "buffer_load_dwordx4 v7, s[12:15], s41 offen offset:1024 lds\n"
      "buffer_load_dwordx4 v8, s[12:15], s41 offen offset:2048 lds\n"
      "buffer_load_dwordx4 v9, s[12:15], s41 offen offset:3072 lds\n"
      "s_add_u32 s41, s41, 294912\n"
      "s_waitcnt vmcnt(16) lgkmcnt(0)\n"
      "s_barrier\n"
      "ds_read_b64_tr_b16 a[48:49], v5 offset:0\n"
      "ds_read_b64_tr_b16 a[50:51], v5 offset:1024\n"
      "ds_read_b64_tr_b16 a[120:121], v5 offset:8192\n"
      "ds_read_b64_tr_b16 a[122:123], v5 offset:9216\n"
      "ds_read_b64_tr_b16 a[192:193], v5 offset:8\n"
      "ds_read_b64_tr_b16 a[194:195], v5 offset:1032\n"
      "ds_read_b64_tr_b16 v[28:29], v5 offset:8200\n"
      "ds_read_b64_tr_b16 v[30:31], v5 offset:9224\n"
      "v_mfma_f32_16x16x32_bf16 v[140:143], a[44:47], v[172:175], v[140:143]\n"
      "v_mfma_f32_16x16x32_bf16 v[144:147], a[116:119], v[172:175], v[144:147]\n"
      "v_mfma_f32_16x16x32_bf16 v[148:151], a[188:191], v[172:175], v[148:151]\n"
      "v_mfma_f32_16x16x32_bf16 v[152:155], v[24:27], v[172:175], v[152:155]\n"
      "s_add_u32 m0, s38, 147456\n"
      "s_nop 0\n"
      "buffer_load_dwordx4 v6, s[12:15], s41 offen offset:0 lds\n"
      "buffer_load_dwordx4 v7, s[12:15], s41 offen offset:1024 lds\n"
      "buffer_load_dwordx4 v8, s[12:15], s41 offen offset:2048 lds\n"
      "buffer_load_dwordx4 v9, s[12:15], s41 offen offset:3072 lds\n"
      "s_add_u32 s41, s41, 294912\n"
      "s_waitcnt vmcnt(16) lgkmcnt(0)\n"
      "s_barrier\n"
      "ds_read_b64_tr_b16 a[52:53], v5 offset:16384\n"
      "ds_read_b64_tr_b16 a[54:55], v5 offset:17408\n"
      "ds_read_b64_tr_b16 a[124:125], v5 offset:24576\n"
      "ds_read_b64_tr_b16 a[126:127], v5 offset:25600\n"
      "ds_read_b64_tr_b16 a[196:197], v5 offset:16392\n"
      "ds_read_b64_tr_b16 a[198:199], v5 offset:17416\n"
      "ds_read_b64_tr_b16 v[32:33], v5 offset:24584\n"
      "ds_read_b64_tr_b16 v[34:35], v5 offset:25608\n"
      "v_mfma_f32_16x16x32_bf16 v[140:143], a[48:51], v[172:175], v[140:143]\n"
      "v_mfma_f32_16x16x32_bf16 v[144:147], a[120:123], v[172:175], v[144:147]\n"
      "v_mfma_f32_16x16x32_bf16 v[148:151], a[192:195], v[172:175], v[148:151]\n"
      "v_mfma_f32_16x16x32_bf16 v[152:155], v[28:31], v[172:175], v[152:155]\n"
      "s_waitcnt vmcnt(12) lgkmcnt(0)\n"
      "s_barrier\n"
      "ds_read_b64_tr_b16 a[56:57], v5 offset:32768\n"
      "ds_read_b64_tr_b16 a[58:59], v5 offset:33792\n"
      "ds_read_b64_tr_b16 a[128:129], v5 offset:40960\n"
      "ds_read_b64_tr_b16 a[130:131], v5 offset:41984\n"
      "ds_read_b64_tr_b16 a[200:201], v5 offset:32776\n"
      "ds_read_b64_tr_b16 a[202:203], v5 offset:33800\n"
      "ds_read_b64_tr_b16 v[36:37], v5 offset:40968\n"
      "ds_read_b64_tr_b16 v[38:39], v5 offset:41992\n"
      "v_mfma_f32_16x16x32_bf16 v[140:143], a[52:55], v[172:175], v[140:143]\n"
      "v_mfma_f32_16x16x32_bf16 v[144:147], a[124:127], v[172:175], v[144:147]\n"
      "v_mfma_f32_16x16x32_bf16 v[148:151], a[196:199], v[172:175], v[148:151]\n"
      "v_mfma_f32_16x16x32_bf16 v[152:155], v[32:35], v[172:175], v[152:155]\n"
      "s_waitcnt vmcnt(8) lgkmcnt(0)\n"
      "s_barrier\n"
      "ds_read_b64_tr_b16 a[60:61], v19 offset:0\n"
      "ds_read_b64_tr_b16 a[62:63], v19 offset:1024\n"
      "ds_read_b64_tr_b16 a[132:133], v19 offset:8192\n"
      "ds_read_b64_tr_b16 a[134:135], v19 offset:9216\n"
      "ds_read_b64_tr_b16 a[204:205], v19 offset:8\n"
      "ds_read_b64_tr_b16 a[206:207], v19 offset:1032\n"
      "ds_read_b64_tr_b16 v[40:41], v19 offset:8200\n"
      "ds_read_b64_tr_b16 v[42:43], v19 offset:9224\n"
      "v_mfma_f32_16x16x32_bf16 v[140:143], a[56:59], v[172:175], v[140:143]\n"
      "v_mfma_f32_16x16x32_bf16 v[144:147], a[128:131], v[172:175], v[144:147]\n"
      "v_mfma_f32_16x16x32_bf16 v[148:151], a[200:203], v[172:175], v[148:151]\n"
      "v_mfma_f32_16x16x32_bf16 v[152:155], v[36:39], v[172:175], v[152:155]\n"
      "s_waitcnt vmcnt(4) lgkmcnt(0)\n"
      "s_barrier\n"
      "ds_read_b64_tr_b16 a[64:65], v19 offset:16384\n"
      "ds_read_b64_tr_b16 a[66:67], v19 offset:17408\n"
      "ds_read_b64_tr_b16 a[136:137], v19 offset:24576\n"
      "ds_read_b64_tr_b16 a[138:139], v19 offset:25600\n"
      "ds_read_b64_tr_b16 a[208:209], v19 offset:16392\n"
      "ds_read_b64_tr_b16 a[210:211], v19 offset:17416\n"
      "ds_read_b64_tr_b16 v[44:45], v19 offset:24584\n"
      "ds_read_b64_tr_b16 v[46:47], v19 offset:25608\n"
      "v_mfma_f32_16x16x32_bf16 v[140:143], a[60:63], v[172:175], v[140:143]\n"
      "v_mfma_f32_16x16x32_bf16 v[144:147], a[132:135], v[172:175], v[144:147]\n"
      "v_mfma_f32_16x16x32_bf16 v[148:151], a[204:207], v[172:175], v[148:151]\n"
      "v_mfma_f32_16x16x32_bf16 v[152:155], v[40:43], v[172:175], v[152:155]\n"
      "s_waitcnt vmcnt(0) lgkmcnt(0)\n"
      "s_barrier\n"
      "ds_read_b64_tr_b16 a[68:69], v19 offset:32768\n"
      "ds_read_b64_tr_b16 a[70:71], v19 offset:33792\n"
      "ds_read_b64_tr_b16 a[140:141], v19 offset:40960\n"
      "ds_read_b64_tr_b16 a[142:143], v19 offset:41984\n"
      "ds_read_b64_tr_b16 a[212:213], v19 offset:32776\n"
      "ds_read_b64_tr_b16 a[214:215], v19 offset:33800\n"
      "ds_read_b64_tr_b16 v[48:49], v19 offset:40968\n"
      "ds_read_b64_tr_b16 v[50:51], v19 offset:41992\n"
      "v_mfma_f32_16x16x32_bf16 v[140:143], a[64:67], v[172:175], v[140:143]\n"
      "v_mfma_f32_16x16x32_bf16 v[144:147], a[136:139], v[172:175], v[144:147]\n"
      "v_mfma_f32_16x16x32_bf16 v[148:151], a[208:211], v[172:175], v[148:151]\n"
      "v_mfma_f32_16x16x32_bf16 v[152:155], v[44:47], v[172:175], v[152:155]\n"
      "s_waitcnt lgkmcnt(0)\n"
      "v_mfma_f32_16x16x32_bf16 v[140:143], a[68:71], v[172:175], v[140:143]\n"
      "v_mfma_f32_16x16x32_bf16 v[144:147], a[140:143], v[172:175], v[144:147]\n"
      "v_mfma_f32_16x16x32_bf16 v[148:151], a[212:215], v[172:175], v[148:151]\n"
      "v_mfma_f32_16x16x32_bf16 v[152:155], v[48:51], v[172:175], v[152:155]\n"
      "s_mul_i32 s21, s21, 1664525\n"
      "s_add_u32 s21, s21, 1013904223\n"
      "v_mov_b32 v15, s21\n"
      "buffer_store_dword v15, v12, s[8:11], 0 offen sc1\n"
      "v_not_b32 v52, v15\n"
      "v_not_b32 v53, v15\n"
      "v_not_b32 v54, v15\n"
      "v_not_b32 v55, v15\n"
      "s_add_u32 s30, s37, 0\n"
      "buffer_store_dwordx4 v[52:55], v10, s[8:11], s30 offen sc1\n"
      "v_xor_b32 v15, v15, v16\n"
      "v_add_u32 v17, 40960, v0\n"
      "s_barrier\n"
      "v_mov_b32 v11, 0\n"
      "ds_write_b32 v13, v11\n"
      "s_waitcnt lgkmcnt(0)\n"
      "s_barrier\n"
      "s_add_u32 s6, s36, 1548288\n"
      "s_add_u32 s36, s36, 4294942720\n"
      "s_add_u32 s42, s37, 4294635520\n"
      "s_add_u32 s37, s37, 4294635520\n"
      "s_add_u32 s36, s36, 24576\n"
      "s_min_u32 s36, s36, s6\n"
      "buffer_load_dword v18, v2, s[8:11], s36 offen sc1\n"
      "s_mov_b32 s40, s36\n"
      "s_add_u32 s36, s36, 24576\n"
      "s_add_u32 m0, s38, 0\n"
      "s_waitcnt vmcnt(0)\n"
      "v_cmp_ne_u32 vcc, s21, v18\n"
      "s_cbranch_vccnz .Lsp_g0_1\n"
      ".Lbk_g0_1:\n"
      "buffer_load_dwordx4 v0, s[8:11], s40 offen offset:1024 sc1 lds\n"
      "buffer_load_dwordx4 v0, s[8:11], s40 offen offset:0 sc1 lds\n"
      "buffer_load_dwordx4 v0, s[8:11], s40 offen offset:2048 sc1 lds\n"
      "buffer_load_dwordx4 v0, s[8:11], s40 offen offset:3072 sc1 lds\n"
      "s_add_u32 m0, s39, 0\n"
      "s_nop 0\n"
      "buffer_load_dwordx4 v1, s[8:11], s40 offen offset:0 sc1 lds\n"
      "s_min_u32 s36, s36, s6\n"
      "buffer_load_dword v18, v2, s[8:11], s36 offen sc1\n"
      "s_mov_b32 s40, s36\n"
      "v_add_u32 v11, 1, v11\n"
      "s_add_u32 s36, s36, 24576\n"
      "s_add_u32 m0, s38, 20480\n"
      "s_waitcnt vmcnt(1)\n"
      "ds_write_b32 v13, v11\n"
      "s_waitcnt vmcnt(0)\n"
      "v_cmp_ne_u32 vcc, s21, v18\n"
      "ds_read_b32 v12, v14\n"
      "s_cbranch_vccnz .Lsp_g0_2\n"
      ".Lbk_g0_2:\n"
      "buffer_load_dwordx4 v0, s[8:11], s40 offen offset:1024 sc1 lds\n"
      "s_waitcnt lgkmcnt(0)\n"
      "v_cmp_gt_i32 vcc, v11, v12\n"
      "buffer_load_dwordx4 v0, s[8:11], s40 offen offset:0 sc1 lds\n"
      "s_cbranch_vccnz .Lsp_gy0_3\n"
      ".Lbk_gy0_3:\n"
      "ds_read_b128 v[60:63], v0 offset:2048\n"
      "buffer_load_dwordx4 v0, s[8:11], s40 offen offset:2048 sc1 lds\n"
      "ds_read_b128 v[52:55], v0 offset:0\n"
      "buffer_load_dwordx4 v0, s[8:11], s40 offen offset:3072 sc1 lds\n"
      "ds_read_b128 v[76:79], v0 offset:6144\n"
      "ds_read2st64_b64 v[168:171], v4 offset0:0 offset1:1\n"
      "s_add_u32 m0, s39, 20480\n"
      "ds_read_b128 v[72:75], v0 offset:5120\n"
      "ds_read_b128 v[68:71], v0 offset:4096\n"
      "buffer_load_dwordx4 v1, s[8:11], s40 offen offset:0 sc1 lds\n"
      "s_min_u32 s36, s36, s6\n"
      "ds_read_b128 v[56:59], v0 offset:1024\n"
      "ds_read_b128 v[64:67], v0 offset:3072\n"
      "ds_read_b128 v[88:91], v0 offset:9216\n"
      "buffer_load_dword v16, v13, s[8:11], s31 offen\n"
      "ds_read_b128 v[108:111], v0 offset:14336\n"
      "ds_read_b128 v[80:83], v0 offset:7168\n"
      "ds_read_b128 v[104:107], v0 offset:13312\n"
      "s_waitcnt lgkmcnt(8)\n"
      "v_add_f32 v169, v169, v171\n"
      "buffer_load_dword v18, v2, s[8:11], s36 offen sc1\n"
      "s_mov_b32 s20, 15\n"
      "s_waitcnt lgkmcnt(0)\n"
      "s_nop 4\n"
      "s_branch .Lg_loop\n"
      ".p2align 12\n"
      ".Lg_p0:\n"
      "s_branch .Lg_pr\n"
      ".p2align 5\n"
      ".Lg_loop:\n"
      "v_mfma_f32_16x16x32_bf16 v[124:127], a[0:3], v[52:55], 0\n"
      "s_waitcnt lgkmcnt(5)\n"
      "ds_read_b128 v[84:87], v0 offset:8192\n"
      "v_fmac_f32 v130, v176, v146\n"
      "v_mfma_f32_16x16x32_bf16 v[124:127], a[4:7], v[56:59], v[124:127]\n"
      "ds_read_b128 v[92:95], v0 offset:10240\n"
      "v_fma_f32 v163, v163, v177, v177\n"
      "v_mfma_f32_16x16x32_bf16 v[124:127], a[8:11], v[60:63], v[124:127]\n"
      "s_waitcnt lgkmcnt(5)\n"
      "ds_read_b128 v[96:99], v0 offset:11264\n"
      "v_fma_f32 v162, v162, v179, v178\n"
      "v_mfma_f32_16x16x32_bf16 v[124:127], a[12:15], v[64:67], v[124:127]\n"
      "ds_read_b128 v[100:103], v0 offset:12288\n"
      "v_mul_f32 v130, v130, v158\n"
      "v_mfma_f32_16x16x32_bf16 v[124:127], a[16:19], v[68:71], v[124:127]\n"
      "s_mov_b32 s40, s36\n"
      "v_mul_f32 v162, v134, v162\n"
      "v_fmac_f32 v137, v176, v153\n"
      "v_mfma_f32_16x16x32_bf16 v[124:127], a[20:23], v[72:75], v[124:127]\n"
      "v_rcp_f32 v163, v163\n"
      "v_mfma_f32_16x16x32_bf16 v[124:127], a[24:27], v[76:79], v[124:127]\n"
      "s_waitcnt lgkmcnt(4)\n"
      "ds_read_b128 v[112:115], v0 offset:15360\n"
      "v_fmac_f32 v138, v176, v154\n"
      "v_mfma_f32_16x16x32_bf16 v[124:127], a[28:31], v[80:83], v[124:127]\n"
      "s_waitcnt lgkmcnt(4)\n"
      "v_exp_f32 v162, v162\n"
      "v_mfma_f32_16x16x32_bf16 v[124:127], a[32:35], v[84:87], v[124:127]\n"
      "ds_read_b128 v[116:119], v0 offset:16384\n"
      "v_mul_f32 v129, v129, v157\n"
      "v_mfma_f32_16x16x32_bf16 v[124:127], a[36:39], v[88:91], v[124:127]\n"
      "s_waitcnt lgkmcnt(3)\n"
      "ds_read_b128 v[120:123], v0 offset:17408\n"
      "v_fma_f32 v162, v162, v177, v177\n"
      "v_rcp_f32 v162, v162\n"
      "v_mfma_f32_16x16x32_bf16 v[124:127], a[40:43], v[92:95], v[124:127]\n"
      "v_mul_f32 v137, v137, v161\n"
      "v_fmac_f32 v136, v176, v152\n"
      "v_mfma_f32_16x16x32_bf16 v[124:127], a[44:47], v[96:99], v[124:127]\n"
      "s_waitcnt lgkmcnt(3)\n"
      "v_mul_f32 v136, v136, v160\n"
      "v_mul_f32 v131, v131, v159\n"
      "v_mfma_f32_16x16x32_bf16 v[124:127], a[48:51], v[100:103], v[124:127]\n"
      "s_add_u32 s42, s42, 165888\n"
      "v_mul_f32 v162, v134, v162\n"
      "v_cvt_pk_bf16_f32 v166, v136, v137\n"
      "v_mfma_f32_16x16x32_bf16 v[124:127], a[52:55], v[104:107], v[124:127]\n"
      "v_fmac_f32 v128, v176, v144\n"
      "v_mul_f32 v138, v138, v162\n"
      "v_mfma_f32_16x16x32_bf16 v[124:127], a[56:59], v[108:111], v[124:127]\n"
      "s_waitcnt lgkmcnt(2)\n"
      "v_cvt_pk_bf16_f32 v165, v130, v131\n"
      "v_add_u32 v11, 1, v11\n"
      "v_mfma_f32_16x16x32_bf16 v[124:127], a[60:63], v[112:115], v[124:127]\n"
      "s_waitcnt lgkmcnt(1)\n"
      "v_mul_f32 v163, v135, v163\n"
      "v_fmac_f32 v139, v176, v155\n"
      "v_mfma_f32_16x16x32_bf16 v[124:127], a[64:67], v[116:119], v[124:127]\n"
      "s_waitcnt lgkmcnt(0)\n"
      "v_mul_f32 v128, v128, v156\n"
      "v_mul_f32 v139, v139, v163\n"
      "v_mfma_f32_16x16x32_bf16 v[124:127], a[68:71], v[120:123], v[124:127]\n"
      "s_add_u32 s37, s37, 165888\n"
      "v_cvt_pk_bf16_f32 v164, v128, v129\n"
      "v_cvt_pk_bf16_f32 v167, v138, v139\n"
      "v_mfma_f32_16x16x32_bf16 v[132:135], a[144:147], v[52:55], 0\n"
      "buffer_store_dwordx4 v[164:167], v0, s[8:11], s37 offen sc1\n"
      "v_mfma_f32_16x16x32_bf16 v[132:135], a[148:151], v[56:59], v[132:135]\n"
      "v_sub_f32 v176, -v168, v170\n"
      "v_fma_f32 v169, -v176, v176, v169\n"
      "v_mfma_f32_16x16x32_bf16 v[132:135], a[152:155], v[60:63], v[132:135]\n"
      "v_fmac_f32 v126, v176, v142\n"
      "v_fmac_f32 v124, v176, v140\n"
      "v_mfma_f32_16x16x32_bf16 v[132:135], a[156:159], v[64:67], v[132:135]\n"
      "v_mul_f32 v156, v124, v124\n"
      "v_max_f32 v177, 0x3727c5ac, v169\n"
      "v_mfma_f32_16x16x32_bf16 v[132:135], a[160:163], v[68:71], v[132:135]\n"
      "v_rsq_f32 v170, v177\n"
      "v_mfma_f32_16x16x32_bf16 v[132:135], a[164:167], v[72:75], v[132:135]\n"
      "v_fmac_f32 v125, v176, v141\n"
      "v_mul_f32 v157, v125, v125\n"
      "v_mfma_f32_16x16x32_bf16 v[132:135], a[168:171], v[76:79], v[132:135]\n"
      "v_mul_f32 v178, 0xc0135761, v170\n"
      "v_fmac_f32 v127, v176, v143\n"
      "v_mfma_f32_16x16x32_bf16 v[132:135], a[172:175], v[80:83], v[132:135]\n"
      "v_mul_f32 v171, 0x3d372713, v178\n"
      "v_mul_f32 v170, v170, v170\n"
      "v_mfma_f32_16x16x32_bf16 v[132:135], a[176:179], v[84:87], v[132:135]\n"
      "v_mul_f32 v158, v126, v126\n"
      "v_mul_f32 v159, v127, v127\n"
      "v_mfma_f32_16x16x32_bf16 v[132:135], a[180:183], v[88:91], v[132:135]\n"
      "v_mul_f32 v179, v171, v170\n"
      "v_fma_f32 v157, v157, v179, v178\n"
      "v_mfma_f32_16x16x32_bf16 v[132:135], a[184:187], v[92:95], v[132:135]\n"
      "v_fma_f32 v159, v159, v179, v178\n"
      "v_mul_f32 v157, v125, v157\n"
      "v_mfma_f32_16x16x32_bf16 v[132:135], a[188:191], v[96:99], v[132:135]\n"
      "s_add_u32 s36, s36, 24576\n"
      "v_exp_f32 v157, v157\n"
      "v_mfma_f32_16x16x32_bf16 v[132:135], a[192:195], v[100:103], v[132:135]\n"
      "v_fma_f32 v156, v156, v179, v178\n"
      "v_mul_f32 v156, v124, v156\n"
      "v_mfma_f32_16x16x32_bf16 v[132:135], a[196:199], v[104:107], v[132:135]\n"
      "v_fma_f32 v158, v158, v179, v178\n"
      "v_mul_f32 v159, v127, v159\n"
      "v_mfma_f32_16x16x32_bf16 v[132:135], a[200:203], v[108:111], v[132:135]\n"
      "v_exp_f32 v156, v156\n"
      "v_mfma_f32_16x16x32_bf16 v[132:135], a[204:207], v[112:115], v[132:135]\n"
      "s_add_u32 m0, s38, 40960\n"
      "v_exp_f32 v159, v159\n"
      "v_mfma_f32_16x16x32_bf16 v[132:135], a[208:211], v[116:119], v[132:135]\n"
      "v_mul_f32 v158, v126, v158\n"
      "v_fma_f32 v159, v159, v177, v177\n"
      "v_mfma_f32_16x16x32_bf16 v[132:135], a[212:215], v[120:123], v[132:135]\n"
      "v_rcp_f32 v159, v159\n"
      "v_mfma_f32_16x16x32_bf16 v[128:131], a[72:75], v[52:55], 0\n"
      "s_waitcnt vmcnt(3)\n"
      "ds_write_b32 v13, v11\n"
      "v_mfma_f32_16x16x32_bf16 v[128:131], a[76:79], v[56:59], v[128:131]\n"
      "v_exp_f32 v158, v158\n"
      "v_mfma_f32_16x16x32_bf16 v[128:131], a[80:83], v[60:63], v[128:131]\n"
      "v_fma_f32 v156, v156, v177, v177\n"
      "v_fmac_f32 v135, v176, v151\n"
      "v_mfma_f32_16x16x32_bf16 v[128:131], a[84:87], v[64:67], v[128:131]\n"
      "v_fmac_f32 v133, v176, v149\n"
      "v_mul_f32 v163, v135, v135\n"
      "v_mfma_f32_16x16x32_bf16 v[128:131], a[88:91], v[68:71], v[128:131]\n"
      "v_mul_f32 v161, v133, v133\n"
      "v_fma_f32 v163, v163, v179, v178\n"
      "v_fma_f32 v161, v161, v179, v178\n"
      "v_mfma_f32_16x16x32_bf16 v[128:131], a[92:95], v[72:75], v[128:131]\n"
      "v_fma_f32 v157, v157, v177, v177\n"
      "v_mul_f32 v161, v133, v161\n"
      "v_mfma_f32_16x16x32_bf16 v[128:131], a[96:99], v[76:79], v[128:131]\n"
      "v_exp_f32 v161, v161\n"
      "v_mfma_f32_16x16x32_bf16 v[128:131], a[100:103], v[80:83], v[128:131]\n"
      "s_waitcnt vmcnt(1) lgkmcnt(0)\n"
      "v_fma_f32 v161, v161, v177, v177\n"
      "v_cmp_ne_u32 vcc, s21, v18\n"
      "v_mfma_f32_16x16x32_bf16 v[128:131], a[104:107], v[84:87], v[128:131]\n"
      "v_rcp_f32 v161, v161\n"
      "v_mfma_f32_16x16x32_bf16 v[128:131], a[108:111], v[88:91], v[128:131]\n"
      "ds_read_b32 v12, v14\n"
      "v_fmac_f32 v132, v176, v148\n"
      "v_mfma_f32_16x16x32_bf16 v[128:131], a[112:115], v[92:95], v[128:131]\n"
      "s_cbranch_vccnz .Lsp_g0_4\n"
      ".Lbk_g0_4:\n"
      "v_mul_f32 v160, v132, v132\n"
      "v_mfma_f32_16x16x32_bf16 v[128:131], a[116:119], v[96:99], v[128:131]\n"
      "buffer_load_dwordx4 v0, s[8:11], s40 offen offset:1024 sc1 lds\n"
      "v_mfma_f32_16x16x32_bf16 v[128:131], a[120:123], v[100:103], v[128:131]\n"
      "v_fma_f32 v160, v160, v179, v178\n"
      "v_mul_f32 v160, v132, v160\n"
      "v_mfma_f32_16x16x32_bf16 v[128:131], a[124:127], v[104:107], v[128:131]\n"
      "v_mul_f32 v163, v135, v163\n"
      "v_mul_f32 v161, v133, v161\n"
      "v_mfma_f32_16x16x32_bf16 v[128:131], a[128:131], v[108:111], v[128:131]\n"
      "v_fma_f32 v158, v158, v177, v177\n"
      "v_fmac_f32 v134, v176, v150\n"
      "v_mfma_f32_16x16x32_bf16 v[128:131], a[132:135], v[112:115], v[128:131]\n"
      "s_waitcnt lgkmcnt(0)\n"
      "v_mul_f32 v162, v134, v134\n"
      "v_cmp_gt_i32 vcc, v11, v12\n"
      "v_mfma_f32_16x16x32_bf16 v[128:131], a[136:139], v[116:119], v[128:131]\n"
      "buffer_load_dwordx4 v0, s[8:11], s40 offen offset:0 sc1 lds\n"
      "v_mfma_f32_16x16x32_bf16 v[128:131], a[140:143], v[120:123], v[128:131]\n"
      "v_rcp_f32 v158, v158\n"
      "v_mfma_f32_16x16x32_bf16 v[136:139], a[216:219], v[52:55], 0\n"
      "v_exp_f32 v160, v160\n"
      "v_mfma_f32_16x16x32_bf16 v[136:139], a[220:223], v[56:59], v[136:139]\n"
      "s_cbranch_vccnz .Lsp_gy0_5\n"
      ".Lbk_gy0_5:\n"
      "v_fma_f32 v160, v160, v177, v177\n"
      "v_mfma_f32_16x16x32_bf16 v[136:139], a[224:227], v[60:63], v[136:139]\n"
      "ds_read_b128 v[60:63], v0 offset:22528\n"
      "buffer_load_dwordx4 v0, s[8:11], s40 offen offset:2048 sc1 lds\n"
      "v_mfma_f32_16x16x32_bf16 v[136:139], a[228:231], v[64:67], v[136:139]\n"
      "ds_read_b128 v[52:55], v0 offset:20480\n"
      "v_mul_f32 v159, v127, v159\n"
      "v_mfma_f32_16x16x32_bf16 v[136:139], a[232:235], v[68:71], v[136:139]\n"
      "v_exp_f32 v163, v163\n"
      "v_mfma_f32_16x16x32_bf16 v[136:139], a[236:239], v[72:75], v[136:139]\n"
      "v_rcp_f32 v157, v157\n"
      "v_mfma_f32_16x16x32_bf16 v[136:139], a[240:243], v[76:79], v[136:139]\n"
      "buffer_load_dwordx4 v0, s[8:11], s40 offen offset:3072 sc1 lds\n"
      "ds_read_b128 v[76:79], v0 offset:26624\n"
      "v_mfma_f32_16x16x32_bf16 v[136:139], a[244:247], v[80:83], v[136:139]\n"
      "ds_read2st64_b64 v[168:171], v4 offset0:40 offset1:41\n"
      "v_fmac_f32 v131, v176, v147\n"
      "v_mfma_f32_16x16x32_bf16 v[136:139], a[248:251], v[84:87], v[136:139]\n"
      "s_add_u32 m0, s39, 40960\n"
      "ds_read_b128 v[72:75], v0 offset:25600\n"
      "v_mul_f32 v158, v126, v158\n"
      "v_mfma_f32_16x16x32_bf16 v[136:139], a[252:255], v[88:91], v[136:139]\n"
      "v_rcp_f32 v156, v156\n"
      "v_mul_f32 v157, v125, v157\n"
      "v_mfma_f32_16x16x32_bf16 v[136:139], v[20:23], v[92:95], v[136:139]\n"
      "ds_read_b128 v[68:71], v0 offset:24576\n"
      "buffer_load_dwordx4 v1, s[8:11], s40 offen offset:0 sc1 lds\n"
      "v_mfma_f32_16x16x32_bf16 v[136:139], v[24:27], v[96:99], v[136:139]\n"
      "s_min_u32 s36, s36, s6\n"
      "ds_read_b128 v[56:59], v0 offset:21504\n"
      "v_fmac_f32 v129, v176, v145\n"
      "v_mfma_f32_16x16x32_bf16 v[136:139], v[28:31], v[100:103], v[136:139]\n"
      "ds_read_b128 v[64:67], v0 offset:23552\n"
      "v_rcp_f32 v160, v160\n"
      "v_mfma_f32_16x16x32_bf16 v[136:139], v[32:35], v[104:107], v[136:139]\n"
      "ds_read_b128 v[88:91], v0 offset:29696\n"
      "v_mul_f32 v160, v132, v160\n"
      "v_mfma_f32_16x16x32_bf16 v[136:139], v[36:39], v[108:111], v[136:139]\n"
      "s_waitcnt vmcnt(5) lgkmcnt(5)\n"
      "buffer_store_dword v15, v3, s[8:11], s42 offen sc1\n"
      "ds_read_b128 v[108:111], v0 offset:34816\n"
      "v_mfma_f32_16x16x32_bf16 v[136:139], v[40:43], v[112:115], v[136:139]\n"
      "ds_read_b128 v[80:83], v0 offset:27648\n"
      "v_mul_f32 v156, v124, v156\n"
      "v_mfma_f32_16x16x32_bf16 v[136:139], v[44:47], v[116:119], v[136:139]\n"
      "ds_read_b128 v[104:107], v0 offset:33792\n"
      "v_add_f32 v169, v169, v171\n"
      "v_mfma_f32_16x16x32_bf16 v[136:139], v[48:51], v[120:123], v[136:139]\n"
      "buffer_load_dword v18, v2, s[8:11], s36 offen sc1\n"
      "v_mfma_f32_16x16x32_bf16 v[124:127], a[0:3], v[52:55], 0\n"
      "s_waitcnt lgkmcnt(5)\n"
      "ds_read_b128 v[84:87], v0 offset:28672\n"
      "v_fmac_f32 v130, v176, v146\n"
      "v_mfma_f32_16x16x32_bf16 v[124:127], a[4:7], v[56:59], v[124:127]\n"
      "ds_read_b128 v[92:95], v0 offset:30720\n"
      "v_fma_f32 v163, v163, v177, v177\n"
      "v_mfma_f32_16x16x32_bf16 v[124:127], a[8:11], v[60:63], v[124:127]\n"
      "s_waitcnt lgkmcnt(5)\n"
      "ds_read_b128 v[96:99], v0 offset:31744\n"
      "v_fma_f32 v162, v162, v179, v178\n"
      "v_mfma_f32_16x16x32_bf16 v[124:127], a[12:15], v[64:67], v[124:127]\n"
      "ds_read_b128 v[100:103], v0 offset:32768\n"
      "v_mul_f32 v130, v130, v158\n"
      "v_mfma_f32_16x16x32_bf16 v[124:127], a[16:19], v[68:71], v[124:127]\n"
      "s_mov_b32 s40, s36\n"
      "v_mul_f32 v162, v134, v162\n"
      "v_fmac_f32 v137, v176, v153\n"
      "v_mfma_f32_16x16x32_bf16 v[124:127], a[20:23], v[72:75], v[124:127]\n"
      "v_rcp_f32 v163, v163\n"
      "v_mfma_f32_16x16x32_bf16 v[124:127], a[24:27], v[76:79], v[124:127]\n"
      "s_waitcnt lgkmcnt(4)\n"
      "ds_read_b128 v[112:115], v0 offset:35840\n"
      "v_fmac_f32 v138, v176, v154\n"
      "v_mfma_f32_16x16x32_bf16 v[124:127], a[28:31], v[80:83], v[124:127]\n"
      "s_waitcnt lgkmcnt(4)\n"
      "v_exp_f32 v162, v162\n"
      "v_mfma_f32_16x16x32_bf16 v[124:127], a[32:35], v[84:87], v[124:127]\n"
      "ds_read_b128 v[116:119], v0 offset:36864\n"
      "v_mul_f32 v129, v129, v157\n"
      "v_mfma_f32_16x16x32_bf16 v[124:127], a[36:39], v[88:91], v[124:127]\n"
      "s_waitcnt lgkmcnt(3)\n"
      "ds_read_b128 v[120:123], v0 offset:37888\n"
      "v_fma_f32 v162, v162, v177, v177\n"
      "v_rcp_f32 v162, v162\n"
      "v_mfma_f32_16x16x32_bf16 v[124:127], a[40:43], v[92:95], v[124:127]\n"
      "v_mul_f32 v137, v137, v161\n"
      "v_fmac_f32 v136, v176, v152\n"
      "v_mfma_f32_16x16x32_bf16 v[124:127], a[44:47], v[96:99], v[124:127]\n"
      "s_waitcnt lgkmcnt(3)\n"
      "v_mul_f32 v136, v136, v160\n"
      "v_mul_f32 v131, v131, v159\n"
      "v_mfma_f32_16x16x32_bf16 v[124:127], a[48:51], v[100:103], v[124:127]\n"
      "s_add_u32 s42, s42, 165888\n"
      "v_mul_f32 v162, v134, v162\n"
      "v_cvt_pk_bf16_f32 v166, v136, v137\n"
      "v_mfma_f32_16x16x32_bf16 v[124:127], a[52:55], v[104:107], v[124:127]\n"
      "v_fmac_f32 v128, v176, v144\n"
      "v_mul_f32 v138, v138, v162\n"
      "v_mfma_f32_16x16x32_bf16 v[124:127], a[56:59], v[108:111], v[124:127]\n"
      "s_waitcnt lgkmcnt(2)\n"
      "v_cvt_pk_bf16_f32 v165, v130, v131\n"
      "v_add_u32 v11, 1, v11\n"
      "v_mfma_f32_16x16x32_bf16 v[124:127], a[60:63], v[112:115], v[124:127]\n"
      "s_waitcnt lgkmcnt(1)\n"
      "v_mul_f32 v163, v135, v163\n"
      "v_fmac_f32 v139, v176, v155\n"
      "v_mfma_f32_16x16x32_bf16 v[124:127], a[64:67], v[116:119], v[124:127]\n"
      "s_waitcnt lgkmcnt(0)\n"
      "v_mul_f32 v128, v128, v156\n"
      "v_mul_f32 v139, v139, v163\n"
      "v_mfma_f32_16x16x32_bf16 v[124:127], a[68:71], v[120:123], v[124:127]\n"
      "s_add_u32 s37, s37, 165888\n"
      "v_cvt_pk_bf16_f32 v164, v128, v129\n"
      "v_cvt_pk_bf16_f32 v167, v138, v139\n"
      "v_mfma_f32_16x16x32_bf16 v[132:135], a[144:147], v[52:55], 0\n"
      "buffer_store_dwordx4 v[164:167], v0, s[8:11], s37 offen sc1\n"
      "v_mfma_f32_16x16x32_bf16 v[132:135], a[148:151], v[56:59], v[132:135]\n"
      "v_sub_f32 v176, -v168, v170\n"
      "v_fma_f32 v169, -v176, v176, v169\n"
      "v_mfma_f32_16x16x32_bf16 v[132:135], a[152:155], v[60:63], v[132:135]\n"
      "v_fmac_f32 v126, v176, v142\n"
      "v_fmac_f32 v124, v176, v140\n"
      "v_mfma_f32_16x16x32_bf16 v[132:135], a[156:159], v[64:67], v[132:135]\n"
      "v_mul_f32 v156, v124, v124\n"
      "v_max_f32 v177, 0x3727c5ac, v169\n"
      "v_mfma_f32_16x16x32_bf16 v[132:135], a[160:163], v[68:71], v[132:135]\n"
      "v_rsq_f32 v170, v177\n"
      "v_mfma_f32_16x16x32_bf16 v[132:135], a[164:167], v[72:75], v[132:135]\n"
      "v_fmac_f32 v125, v176, v141\n"
      "v_mul_f32 v157, v125, v125\n"
      "v_mfma_f32_16x16x32_bf16 v[132:135], a[168:171], v[76:79], v[132:135]\n"
      "v_mul_f32 v178, 0xc0135761, v170\n"
      "v_fmac_f32 v127, v176, v143\n"
      "v_mfma_f32_16x16x32_bf16 v[132:135], a[172:175], v[80:83], v[132:135]\n"
      "v_mul_f32 v171, 0x3d372713, v178\n"
      "v_mul_f32 v170, v170, v170\n"
      "v_mfma_f32_16x16x32_bf16 v[132:135], a[176:179], v[84:87], v[132:135]\n"
      "v_mul_f32 v158, v126, v126\n"
      "v_mul_f32 v159, v127, v127\n"
      "v_mfma_f32_16x16x32_bf16 v[132:135], a[180:183], v[88:91], v[132:135]\n"
      "v_mul_f32 v179, v171, v170\n"
      "v_fma_f32 v157, v157, v179, v178\n"
      "v_mfma_f32_16x16x32_bf16 v[132:135], a[184:187], v[92:95], v[132:135]\n"
      "v_fma_f32 v159, v159, v179, v178\n"
      "v_mul_f32 v157, v125, v157\n"
      "v_mfma_f32_16x16x32_bf16 v[132:135], a[188:191], v[96:99], v[132:135]\n"
      "s_add_u32 s36, s36, 24576\n"
      "v_exp_f32 v157, v157\n"
      "v_mfma_f32_16x16x32_bf16 v[132:135], a[192:195], v[100:103], v[132:135]\n"
      "v_fma_f32 v156, v156, v179, v178\n"
      "v_mul_f32 v156, v124, v156\n"
      "v_mfma_f32_16x16x32_bf16 v[132:135], a[196:199], v[104:107], v[132:135]\n"
      "v_fma_f32 v158, v158, v179, v178\n"
      "v_mul_f32 v159, v127, v159\n"
      "v_mfma_f32_16x16x32_bf16 v[132:135], a[200:203], v[108:111], v[132:135]\n"
      "v_exp_f32 v156, v156\n"
      "v_mfma_f32_16x16x32_bf16 v[132:135], a[204:207], v[112:115], v[132:135]\n"
      "s_add_u32 m0, s38, 61440\n"
      "v_exp_f32 v159, v159\n"
      "v_mfma_f32_16x16x32_bf16 v[132:135], a[208:211], v[116:119], v[132:135]\n"
      "v_mul_f32 v158, v126, v158\n"
      "v_fma_f32 v159, v159, v177, v177\n"
      "v_mfma_f32_16x16x32_bf16 v[132:135], a[212:215], v[120:123], v[132:135]\n"
      "v_rcp_f32 v159, v159\n"
      "v_mfma_f32_16x16x32_bf16 v[128:131], a[72:75], v[52:55], 0\n"
      "s_waitcnt vmcnt(3)\n"
      "ds_write_b32 v13, v11\n"
      "v_mfma_f32_16x16x32_bf16 v[128:131], a[76:79], v[56:59], v[128:131]\n"
      "v_exp_f32 v158, v158\n"
      "v_mfma_f32_16x16x32_bf16 v[128:131], a[80:83], v[60:63], v[128:131]\n"
      "v_fma_f32 v156, v156, v177, v177\n"
      "v_fmac_f32 v135, v176, v151\n"
      "v_mfma_f32_16x16x32_bf16 v[128:131], a[84:87], v[64:67], v[128:131]\n"
      "v_fmac_f32 v133, v176, v149\n"
      "v_mul_f32 v163, v135, v135\n"
      "v_mfma_f32_16x16x32_bf16 v[128:131], a[88:91], v[68:71], v[128:131]\n"
      "v_mul_f32 v161, v133, v133\n"
      "v_fma_f32 v163, v163, v179, v178\n"
      "v_fma_f32 v161, v161, v179, v178\n"
      "v_mfma_f32_16x16x32_bf16 v[128:131], a[92:95], v[72:75], v[128:131]\n"
      "v_fma_f32 v157, v157, v177, v177\n"
      "v_mul_f32 v161, v133, v161\n"
      "v_mfma_f32_16x16x32_bf16 v[128:131], a[96:99], v[76:79], v[128:131]\n"
      "v_exp_f32 v161, v161\n"
      "v_mfma_f32_16x16x32_bf16 v[128:131], a[100:103], v[80:83], v[128:131]\n"
      "s_waitcnt vmcnt(1) lgkmcnt(0)\n"
      "v_fma_f32 v161, v161, v177, v177\n"
      "v_cmp_ne_u32 vcc, s21, v18\n"
      "v_mfma_f32_16x16x32_bf16 v[128:131], a[104:107], v[84:87], v[128:131]\n"
      "v_rcp_f32 v161, v161\n"
      "v_mfma_f32_16x16x32_bf16 v[128:131], a[108:111], v[88:91], v[128:131]\n"
      "ds_read_b32 v12, v14\n"
      "v_fmac_f32 v132, v176, v148\n"
      "v_mfma_f32_16x16x32_bf16 v[128:131], a[112:115], v[92:95], v[128:131]\n"
      "s_cbranch_vccnz .Lsp_g0_6\n"
      ".Lbk_g0_6:\n"
      "v_mul_f32 v160, v132, v132\n"
      "v_mfma_f32_16x16x32_bf16 v[128:131], a[116:119], v[96:99], v[128:131]\n"
      "buffer_load_dwordx4 v0, s[8:11], s40 offen offset:1024 sc1 lds\n"
      "v_mfma_f32_16x16x32_bf16 v[128:131], a[120:123], v[100:103], v[128:131]\n"
      "v_fma_f32 v160, v160, v179, v178\n"
      "v_mul_f32 v160, v132, v160\n"
      "v_mfma_f32_16x16x32_bf16 v[128:131], a[124:127], v[104:107], v[128:131]\n"
      "v_mul_f32 v163, v135, v163\n"
      "v_mul_f32 v161, v133, v161\n"
      "v_mfma_f32_16x16x32_bf16 v[128:131], a[128:131], v[108:111], v[128:131]\n"
      "v_fma_f32 v158, v158, v177, v177\n"
      "v_fmac_f32 v134, v176, v150\n"
      "v_mfma_f32_16x16x32_bf16 v[128:131], a[132:135], v[112:115], v[128:131]\n"
      "s_waitcnt lgkmcnt(0)\n"
      "v_mul_f32 v162, v134, v134\n"
      "v_cmp_gt_i32 vcc, v11, v12\n"
      "v_mfma_f32_16x16x32_bf16 v[128:131], a[136:139], v[116:119], v[128:131]\n"
      "buffer_load_dwordx4 v0, s[8:11], s40 offen offset:0 sc1 lds\n"
      "v_mfma_f32_16x16x32_bf16 v[128:131], a[140:143], v[120:123], v[128:131]\n"
      "v_rcp_f32 v158, v158\n"
      "v_mfma_f32_16x16x32_bf16 v[136:139], a[216:219], v[52:55], 0\n"
      "v_exp_f32_e64 v160, v160\n"
      "v_mfma_f32_16x16x32_bf16 v[136:139], a[220:223], v[56:59], v[136:139]\n"
      "s_cbranch_vccnz .Lsp_gy0_7\n"
      ".Lbk_gy0_7:\n"
      "v_fma_f32 v160, v160, v177, v177\n"
      "v_mfma_f32_16x16x32_bf16 v[136:139], a[224:227], v[60:63], v[136:139]\n"
      "ds_read_b128 v[60:63], v17 offset:2048\n"
      "buffer_load_dwordx4 v0, s[8:11], s40 offen offset:2048 sc1 lds\n"
      "v_mfma_f32_16x16x32_bf16 v[136:139], a[228:231], v[64:67], v[136:139]\n"
      "ds_read_b128 v[52:55], v17 offset:0\n"
      "v_mul_f32 v159, v127, v159\n"
      "v_mfma_f32_16x16x32_bf16 v[136:139], a[232:235], v[68:71], v[136:139]\n"
      "v_exp_f32 v163, v163\n"
      "v_mfma_f32_16x16x32_bf16 v[136:139], a[236:239], v[72:75], v[136:139]\n"
      "v_rcp_f32 v157, v157\n"
      "v_mfma_f32_16x16x32_bf16 v[136:139], a[240:243], v[76:79], v[136:139]\n"
      "buffer_load_dwordx4 v0, s[8:11], s40 offen offset:3072 sc1 lds\n"
      "ds_read_b128 v[76:79], v17 offset:6144\n"
      "v_mfma_f32_16x16x32_bf16 v[136:139], a[244:247], v[80:83], v[136:139]\n"
      "ds_read2st64_b64 v[168:171], v4 offset0:80 offset1:81\n"
      "v_fmac_f32 v131, v176, v147\n"
      "v_mfma_f32_16x16x32_bf16 v[136:139], a[248:251], v[84:87], v[136:139]\n"
      "s_add_u32 m0, s39, 61440\n"
      "ds_read_b128 v[72:75], v17 offset:5120\n"
      "v_mul_f32 v158, v126, v158\n"
      "v_mfma_f32_16x16x32_bf16 v[136:139], a[252:255], v[88:91], v[136:139]\n"
      "v_rcp_f32 v156, v156\n"
      "v_mul_f32 v157, v125, v157\n"
      "v_mfma_f32_16x16x32_bf16 v[136:139], v[20:23], v[92:95], v[136:139]\n"
      "ds_read_b128 v[68:71], v17 offset:4096\n"
      "buffer_load_dwordx4 v1, s[8:11], s40 offen offset:0 sc1 lds\n"
      "v_mfma_f32_16x16x32_bf16 v[136:139], v[24:27], v[96:99], v[136:139]\n"
      "s_min_u32 s36, s36, s6\n"
      "ds_read_b128 v[56:59], v17 offset:1024\n"
      "v_fmac_f32 v129, v176, v145\n"
      "v_mfma_f32_16x16x32_bf16 v[136:139], v[28:31], v[100:103], v[136:139]\n"
      "ds_read_b128 v[64:67], v17 offset:3072\n"
      "v_rcp_f32 v160, v160\n"
      "v_mfma_f32_16x16x32_bf16 v[136:139], v[32:35], v[104:107], v[136:139]\n"
      "ds_read_b128 v[88:91], v17 offset:9216\n"
      "v_mul_f32 v160, v132, v160\n"
      "v_mfma_f32_16x16x32_bf16 v[136:139], v[36:39], v[108:111], v[136:139]\n"
      "s_waitcnt vmcnt(5) lgkmcnt(5)\n"
      "buffer_store_dword v15, v3, s[8:11], s42 offen sc1\n"
      "ds_read_b128 v[108:111], v17 offset:14336\n"
      "v_mfma_f32_16x16x32_bf16 v[136:139], v[40:43], v[112:115], v[136:139]\n"
      "ds_read_b128 v[80:83], v17 offset:7168\n"
      "v_mul_f32 v156, v124, v156\n"
      "v_mfma_f32_16x16x32_bf16 v[136:139], v[44:47], v[116:119], v[136:139]\n"
      "ds_read_b128 v[104:107], v17 offset:13312\n"
      "v_add_f32 v169, v169, v171\n"
      "v_mfma_f32_16x16x32_bf16 v[136:139], v[48:51], v[120:123], v[136:139]\n"
      "buffer_load_dword v18, v2, s[8:11], s36 offen sc1\n"
      "v_mfma_f32_16x16x32_bf16 v[124:127], a[0:3], v[52:55], 0\n"
      "s_waitcnt lgkmcnt(5)\n"
      "ds_read_b128 v[84:87], v17 offset:8192\n"
      "v_fmac_f32 v130, v176, v146\n"
      "v_mfma_f32_16x16x32_bf16 v[124:127], a[4:7], v[56:59], v[124:127]\n"
      "ds_read_b128 v[92:95], v17 offset:10240\n"
      "v_fma_f32 v163, v163, v177, v177\n"
      "v_mfma_f32_16x16x32_bf16 v[124:127], a[8:11], v[60:63], v[124:127]\n"
      "s_waitcnt lgkmcnt(5)\n"
      "ds_read_b128 v[96:99], v17 offset:11264\n"
      "v_fma_f32 v162, v162, v179, v178\n"
      "v_mfma_f32_16x16x32_bf16 v[124:127], a[12:15], v[64:67], v[124:127]\n"
      "ds_read_b128 v[100:103], v17 offset:12288\n"
      "v_mul_f32 v130, v130, v158\n"
      "v_mfma_f32_16x16x32_bf16 v[124:127], a[16:19], v[68:71], v[124:127]\n"
      "s_mov_b32 s40, s36\n"
      "v_mul_f32 v162, v134, v162\n"
      "v_fmac_f32 v137, v176, v153\n"
      "v_mfma_f32_16x16x32_bf16 v[124:127], a[20:23], v[72:75], v[124:127]\n"
      "v_rcp_f32 v163, v163\n"
      "v_mfma_f32_16x16x32_bf16 v[124:127], a[24:27], v[76:79], v[124:127]\n"
      "s_waitcnt lgkmcnt(4)\n"
      "ds_read_b128 v[112:115], v17 offset:15360\n"
      "v_fmac_f32 v138, v176, v154\n"
      "v_mfma_f32_16x16x32_bf16 v[124:127], a[28:31], v[80:83], v[124:127]\n"
      "s_waitcnt lgkmcnt(4)\n"
      "v_exp_f32 v162, v162\n"
      "v_mfma_f32_16x16x32_bf16 v[124:127], a[32:35], v[84:87], v[124:127]\n"
      "ds_read_b128 v[116:119], v17 offset:16384\n"
      "v_mul_f32 v129, v129, v157\n"
      "v_mfma_f32_16x16x32_bf16 v[124:127], a[36:39], v[88:91], v[124:127]\n"
      "s_waitcnt lgkmcnt(3)\n"
      "ds_read_b128 v[120:123], v17 offset:17408\n"
      "v_fma_f32 v162, v162, v177, v177\n"
      "v_rcp_f32 v162, v162\n"
      "v_mfma_f32_16x16x32_bf16 v[124:127], a[40:43], v[92:95], v[124:127]\n"
      "v_mul_f32 v137, v137, v161\n"
      "v_fmac_f32 v136, v176, v152\n"
      "v_mfma_f32_16x16x32_bf16 v[124:127], a[44:47], v[96:99], v[124:127]\n"
      "s_waitcnt lgkmcnt(3)\n"
      "v_mul_f32 v136, v136, v160\n"
      "v_mul_f32 v131, v131, v159\n"
      "v_mfma_f32_16x16x32_bf16 v[124:127], a[48:51], v[100:103], v[124:127]\n"
      "s_add_u32 s42, s42, 165888\n"
      "v_mul_f32 v162, v134, v162\n"
      "v_cvt_pk_bf16_f32 v166, v136, v137\n"
      "v_mfma_f32_16x16x32_bf16 v[124:127], a[52:55], v[104:107], v[124:127]\n"
      "v_fmac_f32 v128, v176, v144\n"
      "v_mul_f32 v138, v138, v162\n"
      "v_mfma_f32_16x16x32_bf16 v[124:127], a[56:59], v[108:111], v[124:127]\n"
      "s_waitcnt lgkmcnt(2)\n"
      "v_cvt_pk_bf16_f32 v165, v130, v131\n"
      "v_add_u32 v11, 1, v11\n"
      "v_mfma_f32_16x16x32_bf16 v[124:127], a[60:63], v[112:115], v[124:127]\n"
      "s_waitcnt lgkmcnt(1)\n"
      "v_mul_f32 v163, v135, v163\n"
      "v_fmac_f32 v139, v176, v155\n"
      "v_mfma_f32_16x16x32_bf16 v[124:127], a[64:67], v[116:119], v[124:127]\n"
      "s_waitcnt lgkmcnt(0)\n"
      "v_mul_f32 v128, v128, v156\n"
      "v_mul_f32 v139, v139, v163\n"
      "v_mfma_f32_16x16x32_bf16 v[124:127], a[68:71], v[120:123], v[124:127]\n"
      "s_add_u32 s37, s37, 165888\n"
      "v_cvt_pk_bf16_f32 v164, v128, v129\n"
      "v_cvt_pk_bf16_f32 v167, v138, v139\n"
      "v_mfma_f32_16x16x32_bf16 v[132:135], a[144:147], v[52:55], 0\n"
      "buffer_store_dwordx4 v[164:167], v0, s[8:11], s37 offen sc1\n"
      "v_mfma_f32_16x16x32_bf16 v[132:135], a[148:151], v[56:59], v[132:135]\n"
      "v_sub_f32 v176, -v168, v170\n"
      "v_fma_f32 v169, -v176, v176, v169\n"
      "v_mfma_f32_16x16x32_bf16 v[132:135], a[152:155], v[60:63], v[132:135]\n"
      "v_fmac_f32 v126, v176, v142\n"
      "v_fmac_f32 v124, v176, v140\n"
      "v_mfma_f32_16x16x32_bf16 v[132:135], a[156:159], v[64:67], v[132:135]\n"
      "v_mul_f32 v156, v124, v124\n"
      "v_max_f32 v177, 0x3727c5ac, v169\n"
      "v_mfma_f32_16x16x32_bf16 v[132:135], a[160:163], v[68:71], v[132:135]\n"
      "v_rsq_f32 v170, v177\n"
      "v_mfma_f32_16x16x32_bf16 v[132:135], a[164:167], v[72:75], v[132:135]\n"
      "v_fmac_f32 v125, v176, v141\n"
      "v_mul_f32 v157, v125, v125\n"
      "v_mfma_f32_16x16x32_bf16 v[132:135], a[168:171], v[76:79], v[132:135]\n"
      "v_mul_f32 v178, 0xc0135761, v170\n"
      "v_fmac_f32 v127, v176, v143\n"
      "v_mfma_f32_16x16x32_bf16 v[132:135], a[172:175], v[80:83], v[132:135]\n"
      "v_mul_f32 v171, 0x3d372713, v178\n"
      "v_mul_f32 v170, v170, v170\n"
      "v_mfma_f32_16x16x32_bf16 v[132:135], a[176:179], v[84:87], v[132:135]\n"
      "v_mul_f32 v158, v126, v126\n"
      "v_mul_f32 v159, v127, v127\n"
      "v_mfma_f32_16x16x32_bf16 v[132:135], a[180:183], v[88:91], v[132:135]\n"
      "v_mul_f32 v179, v171, v170\n"
      "v_fma_f32 v157, v157, v179, v178\n"
      "v_mfma_f32_16x16x32_bf16 v[132:135], a[184:187], v[92:95], v[132:135]\n"
      "v_fma_f32 v159, v159, v179, v178\n"
      "v_mul_f32 v157, v125, v157\n"
      "v_mfma_f32_16x16x32_bf16 v[132:135], a[188:191], v[96:99], v[132:135]\n"
      "s_add_u32 s36, s36, 24576\n"
      "v_exp_f32 v157, v157\n"
      "v_mfma_f32_16x16x32_bf16 v[132:135], a[192:195], v[100:103], v[132:135]\n"
      "v_fma_f32 v156, v156, v179, v178\n"
      "v_mul_f32 v156, v124, v156\n"
      "v_mfma_f32_16x16x32_bf16 v[132:135], a[196:199], v[104:107], v[132:135]\n"
      "v_fma_f32 v158, v158, v179, v178\n"
      "v_mul_f32 v159, v127, v159\n"
      "v_mfma_f32_16x16x32_bf16 v[132:135], a[200:203], v[108:111], v[132:135]\n"
      "v_exp_f32 v156, v156\n"
      "v_mfma_f32_16x16x32_bf16 v[132:135], a[204:207], v[112:115], v[132:135]\n"
      "s_add_u32 m0, s38, 0\n"
      "v_exp_f32 v159, v159\n"
      "v_mfma_f32_16x16x32_bf16 v[132:135], a[208:211], v[116:119], v[132:135]\n"
      "v_mul_f32 v158, v126, v158\n"
      "v_fma_f32 v159, v159, v177, v177\n"
      "v_mfma_f32_16x16x32_bf16 v[132:135], a[212:215], v[120:123], v[132:135]\n"
      "v_rcp_f32 v159, v159\n"
      "v_mfma_f32_16x16x32_bf16 v[128:131], a[72:75], v[52:55], 0\n"
      "s_waitcnt vmcnt(3)\n"
      "ds_write_b32 v13, v11\n"
      "v_mfma_f32_16x16x32_bf16 v[128:131], a[76:79], v[56:59], v[128:131]\n"
      "v_exp_f32 v158, v158\n"
      "v_mfma_f32_16x16x32_bf16 v[128:131], a[80:83], v[60:63], v[128:131]\n"
      "v_fma_f32 v156, v156, v177, v177\n"
      "v_fmac_f32 v135, v176, v151\n"
      "v_mfma_f32_16x16x32_bf16 v[128:131], a[84:87], v[64:67], v[128:131]\n"
      "v_fmac_f32 v133, v176, v149\n"
      "v_mul_f32 v163, v135, v135\n"
      "v_mfma_f32_16x16x32_bf16 v[128:131], a[88:91], v[68:71], v[128:131]\n"
      "v_mul_f32 v161, v133, v133\n"
      "v_fma_f32 v163, v163, v179, v178\n"
      "v_fma_f32 v161, v161, v179, v178\n"
      "v_mfma_f32_16x16x32_bf16 v[128:131], a[92:95], v[72:75], v[128:131]\n"
      "v_fma_f32 v157, v157, v177, v177\n"
      "v_mul_f32 v161, v133, v161\n"
      "v_mfma_f32_16x16x32_bf16 v[128:131], a[96:99], v[76:79], v[128:131]\n"
      "v_exp_f32 v161, v161\n"
      "v_mfma_f32_16x16x32_bf16 v[128:131], a[100:103], v[80:83], v[128:131]\n"
      "s_waitcnt vmcnt(1) lgkmcnt(0)\n"
      "v_fma_f32 v161, v161, v177, v177\n"
      "v_cmp_ne_u32 vcc, s21, v18\n"
      "v_mfma_f32_16x16x32_bf16 v[128:131], a[104:107], v[84:87], v[128:131]\n"
      "v_rcp_f32 v161, v161\n"
      "v_mfma_f32_16x16x32_bf16 v[128:131], a[108:111], v[88:91], v[128:131]\n"
      "ds_read_b32 v12, v14\n"
      "v_fmac_f32 v132, v176, v148\n"
      "v_mfma_f32_16x16x32_bf16 v[128:131], a[112:115], v[92:95], v[128:131]\n"
      "s_cbranch_vccnz .Lsp_g0_8\n"
      ".Lbk_g0_8:\n"
      "v_mul_f32 v160, v132, v132\n"
      "v_mfma_f32_16x16x32_bf16 v[128:131], a[116:119], v[96:99], v[128:131]\n"
      "buffer_load_dwordx4 v0, s[8:11], s40 offen offset:1024 sc1 lds\n"
      "v_mfma_f32_16x16x32_bf16 v[128:131], a[120:123], v[100:103], v[128:131]\n"
      "v_fma_f32 v160, v160, v179, v178\n"
      "v_mul_f32 v160, v132, v160\n"
      "v_mfma_f32_16x16x32_bf16 v[128:131], a[124:127], v[104:107], v[128:131]\n"
      "v_mul_f32 v163, v135, v163\n"
      "v_mul_f32 v161, v133, v161\n"
      "v_mfma_f32_16x16x32_bf16 v[128:131], a[128:131], v[108:111], v[128:131]\n"
      "v_fma_f32 v158, v158, v177, v177\n"
      "v_fmac_f32 v134, v176, v150\n"
      "v_mfma_f32_16x16x32_bf16 v[128:131], a[132:135], v[112:115], v[128:131]\n"
      "s_waitcnt lgkmcnt(0)\n"
      "v_mul_f32_e64 v162, v134, v134\n"
      "v_cmp_gt_i32 vcc, v11, v12\n"
      "v_mfma_f32_16x16x32_bf16 v[128:131], a[136:139], v[116:119], v[128:131]\n"
      "buffer_load_dwordx4 v0, s[8:11], s40 offen offset:0 sc1 lds\n"
      "v_mfma_f32_16x16x32_bf16 v[128:131], a[140:143], v[120:123], v[128:131]\n"
      "v_rcp_f32_e64 v158, v158\n"
      "v_mfma_f32_16x16x32_bf16 v[136:139], a[216:219], v[52:55], 0\n"
      "v_exp_f32_e64 v160, v160\n"
      "v_mfma_f32_16x16x32_bf16 v[136:139], a[220:223], v[56:59], v[136:139]\n"
      "s_cbranch_vccnz .Lsp_gy0_9\n"
      ".Lbk_gy0_9:\n"
      "v_fma_f32 v160, v160, v177, v177\n"
      "v_mfma_f32_16x16x32_bf16 v[136:139], a[224:227], v[60:63], v[136:139]\n"
      "ds_read_b128 v[60:63], v17 offset:22528\n"
      "buffer_load_dwordx4 v0, s[8:11], s40 offen offset:2048 sc1 lds\n"
      "v_mfma_f32_16x16x32_bf16 v[136:139], a[228:231], v[64:67], v[136:139]\n"
      "ds_read_b128 v[52:55], v17 offset:20480\n"
      "v_mul_f32 v159, v127, v159\n"
      "v_mfma_f32_16x16x32_bf16 v[136:139], a[232:235], v[68:71], v[136:139]\n"
      "v_exp_f32 v163, v163\n"
      "v_mfma_f32_16x16x32_bf16 v[136:139], a[236:239], v[72:75], v[136:139]\n"
      "v_rcp_f32 v157, v157\n"
      "v_mfma_f32_16x16x32_bf16 v[136:139], a[240:243], v[76:79], v[136:139]\n"
      "buffer_load_dwordx4 v0, s[8:11], s40 offen offset:3072 sc1 lds\n"
      "ds_read_b128 v[76:79], v17 offset:26624\n"
      "v_mfma_f32_16x16x32_bf16 v[136:139], a[244:247], v[80:83], v[136:139]\n"
      "ds_read2st64_b64 v[168:171], v4 offset0:120 offset1:121\n"
      "v_fmac_f32 v131, v176, v147\n"
      "v_mfma_f32_16x16x32_bf16 v[136:139], a[248:251], v[84:87], v[136:139]\n"
      "s_add_u32 m0, s39, 0\n"
      "ds_read_b128 v[72:75], v17 offset:25600\n"
      "v_mul_f32 v158, v126, v158\n"
      "v_mfma_f32_16x16x32_bf16 v[136:139], a[252:255], v[88:91], v[136:139]\n"
      "v_rcp_f32 v156, v156\n"
      "v_mul_f32 v157, v125, v157\n"
      "v_mfma_f32_16x16x32_bf16 v[136:139], v[20:23], v[92:95], v[136:139]\n"
      "ds_read_b128 v[68:71], v17 offset:24576\n"
      "buffer_load_dwordx4 v1, s[8:11], s40 offen offset:0 sc1 lds\n"
      "v_mfma_f32_16x16x32_bf16 v[136:139], v[24:27], v[96:99], v[136:139]\n"
      "s_min_u32 s36, s36, s6\n"
      "ds_read_b128 v[56:59], v17 offset:21504\n"
      "v_fmac_f32 v129, v176, v145\n"
      "v_mfma_f32_16x16x32_bf16 v[136:139], v[28:31], v[100:103], v[136:139]\n"
      ".Lg_p1:\n"
      "s_cbranch_execz .Lg_pr\n"
      "ds_read_b128 v[64:67], v17 offset:23552\n"
      "v_rcp_f32 v160, v160\n"
      "v_mfma_f32_16x16x32_bf16 v[136:139], v[32:35], v[104:107], v[136:139]\n"
      "ds_read_b128 v[88:91], v17 offset:29696\n"
      "v_mul_f32 v160, v132, v160\n"
      "v_mfma_f32_16x16x32_bf16 v[136:139], v[36:39], v[108:111], v[136:139]\n"
      "s_waitcnt vmcnt(5) lgkmcnt(5)\n"
      "buffer_store_dword v15, v3, s[8:11], s42 offen sc1\n"
      "ds_read_b128 v[108:111], v17 offset:34816\n"
      "v_mfma_f32_16x16x32_bf16 v[136:139], v[40:43], v[112:115], v[136:139]\n"
      "ds_read_b128 v[80:83], v17 offset:27648\n"
      "v_mul_f32 v156, v124, v156\n"
      "v_mfma_f32_16x16x32_bf16 v[136:139], v[44:47], v[116:119], v[136:139]\n"
      "ds_read_b128 v[104:107], v17 offset:33792\n"
      "v_add_f32 v169, v169, v171\n"
      "v_mfma_f32_16x16x32_bf16 v[136:139], v[48:51], v[120:123], v[136:139]\n"
      "buffer_load_dword v18, v2, s[8:11], s36 offen sc1\n"
      "v_mfma_f32_16x16x32_bf16 v[124:127], a[0:3], v[52:55], 0\n"
      "s_waitcnt lgkmcnt(5)\n"
      "ds_read_b128 v[84:87], v17 offset:28672\n"
      "v_fmac_f32 v130, v176, v146\n"
      "v_mfma_f32_16x16x32_bf16 v[124:127], a[4:7], v[56:59], v[124:127]\n"
      "ds_read_b128 v[92:95], v17 offset:30720\n"
      "v_fma_f32 v163, v163, v177, v177\n"
      "v_mfma_f32_16x16x32_bf16 v[124:127], a[8:11], v[60:63], v[124:127]\n"
      "s_waitcnt lgkmcnt(5)\n"
      "ds_read_b128 v[96:99], v17 offset:31744\n"
      "v_fma_f32 v162, v162, v179, v178\n"
      "v_mfma_f32_16x16x32_bf16 v[124:127], a[12:15], v[64:67], v[124:127]\n"
      "ds_read_b128 v[100:103], v17 offset:32768\n"
      "v_mul_f32 v130, v130, v158\n"
      "v_mfma_f32_16x16x32_bf16 v[124:127], a[16:19], v[68:71], v[124:127]\n"
      "s_mov_b32 s40, s36\n"
      "v_mul_f32 v162, v134, v162\n"
      "v_fmac_f32 v137, v176, v153\n"
      "v_mfma_f32_16x16x32_bf16 v[124:127], a[20:23], v[72:75], v[124:127]\n"
      "v_rcp_f32 v163, v163\n"
      "v_mfma_f32_16x16x32_bf16 v[124:127], a[24:27], v[76:79], v[124:127]\n"
      "s_waitcnt lgkmcnt(4)\n"
      "ds_read_b128 v[112:115], v17 offset:35840\n"
      "v_fmac_f32 v138, v176, v154\n"
      "v_mfma_f32_16x16x32_bf16 v[124:127], a[28:31], v[80:83], v[124:127]\n"
      "s_waitcnt lgkmcnt(4)\n"
      "v_exp_f32 v162, v162\n"
      "v_mfma_f32_16x16x32_bf16 v[124:127], a[32:35], v[84:87], v[124:127]\n"
      "ds_read_b128 v[116:119], v17 offset:36864\n"
      "v_mul_f32 v129, v129, v157\n"
      "v_mfma_f32_16x16x32_bf16 v[124:127], a[36:39], v[88:91], v[124:127]\n"
      "s_waitcnt lgkmcnt(3)\n"
      "ds_read_b128 v[120:123], v17 offset:37888\n"
      "v_fma_f32 v162, v162, v177, v177\n"
      "v_rcp_f32 v162, v162\n"
      "v_mfma_f32_16x16x32_bf16 v[124:127], a[40:43], v[92:95], v[124:127]\n"
      "v_mul_f32 v137, v137, v161\n"
      "v_fmac_f32 v136, v176, v152\n"
      "v_mfma_f32_16x16x32_bf16 v[124:127], a[44:47], v[96:99], v[124:127]\n"
      "s_waitcnt lgkmcnt(3)\n"
      "v_mul_f32 v136, v136, v160\n"
      "v_mul_f32 v131, v131, v159\n"
      "v_mfma_f32_16x16x32_bf16 v[124:127], a[48:51], v[100:103], v[124:127]\n"
      "s_add_u32 s42, s42, 165888\n"
      "v_mul_f32 v162, v134, v162\n"
      "v_cvt_pk_bf16_f32 v166, v136, v137\n"
      "v_mfma_f32_16x16x32_bf16 v[124:127], a[52:55], v[104:107], v[124:127]\n"
      "v_fmac_f32 v128, v176, v144\n"
      "v_mul_f32 v138, v138, v162\n"
      "v_mfma_f32_16x16x32_bf16 v[124:127], a[56:59], v[108:111], v[124:127]\n"
      "s_waitcnt lgkmcnt(2)\n"
      "v_cvt_pk_bf16_f32 v165, v130, v131\n"
      "v_add_u32 v11, 1, v11\n"
      "v_mfma_f32_16x16x32_bf16 v[124:127], a[60:63], v[112:115], v[124:127]\n"
      "s_waitcnt lgkmcnt(1)\n"
      "v_mul_f32 v163, v135, v163\n"
      "v_fmac_f32 v139, v176, v155\n"
      "v_mfma_f32_16x16x32_bf16 v[124:127], a[64:67], v[116:119], v[124:127]\n"
      "s_waitcnt lgkmcnt(0)\n"
      "v_mul_f32 v128, v128, v156\n"
      "v_mul_f32 v139, v139, v163\n"
      "v_mfma_f32_16x16x32_bf16 v[124:127], a[68:71], v[120:123], v[124:127]\n"
      "s_add_u32 s37, s37, 165888\n"
      "v_cvt_pk_bf16_f32 v164, v128, v129\n"
      "v_cvt_pk_bf16_f32 v167, v138, v139\n"
      "v_mfma_f32_16x16x32_bf16 v[132:135], a[144:147], v[52:55], 0\n"
      "buffer_store_dwordx4 v[164:167], v0, s[8:11], s37 offen sc1\n"
      "v_mfma_f32_16x16x32_bf16 v[132:135], a[148:151], v[56:59], v[132:135]\n"
      "v_sub_f32 v176, -v168, v170\n"
      "v_fma_f32 v169, -v176, v176, v169\n"
      "v_mfma_f32_16x16x32_bf16 v[132:135], a[152:155], v[60:63], v[132:135]\n"
      "v_fmac_f32 v126, v176, v142\n"
      "v_fmac_f32 v124, v176, v140\n"
      "v_mfma_f32_16x16x32_bf16 v[132:135], a[156:159], v[64:67], v[132:135]\n"
      "v_mul_f32 v156, v124, v124\n"
      "v_max_f32 v177, 0x3727c5ac, v169\n"
      "v_mfma_f32_16x16x32_bf16 v[132:135], a[160:163], v[68:71], v[132:135]\n"
      "v_rsq_f32 v170, v177\n"
      "v_mfma_f32_16x16x32_bf16 v[132:135], a[164:167], v[72:75], v[132:135]\n"
      "v_fmac_f32 v125, v176, v141\n"
      "v_mul_f32 v157, v125, v125\n"
      "v_mfma_f32_16x16x32_bf16 v[132:135], a[168:171], v[76:79], v[132:135]\n"
      "v_mul_f32 v178, 0xc0135761, v170\n"
      "v_fmac_f32 v127, v176, v143\n"
      "v_mfma_f32_16x16x32_bf16 v[132:135], a[172:175], v[80:83], v[132:135]\n"
      "v_mul_f32 v171, 0x3d372713, v178\n"
      "v_mul_f32 v170, v170, v170\n"
      "v_mfma_f32_16x16x32_bf16 v[132:135], a[176:179], v[84:87], v[132:135]\n"
      "v_mul_f32 v158, v126, v126\n"
      "v_mul_f32 v159, v127, v127\n"
      "v_mfma_f32_16x16x32_bf16 v[132:135], a[180:183], v[88:91], v[132:135]\n"
      "v_mul_f32 v179, v171, v170\n"
      "v_fma_f32 v157, v157, v179, v178\n"
      "v_mfma_f32_16x16x32_bf16 v[132:135], a[184:187], v[92:95], v[132:135]\n"
      "v_fma_f32 v159, v159, v179, v178\n"
      "v_mul_f32 v157, v125, v157\n"
      "v_mfma_f32_16x16x32_bf16 v[132:135], a[188:191], v[96:99], v[132:135]\n"
      "s_add_u32 s36, s36, 24576\n"
      "v_exp_f32 v157, v157\n"
      "v_mfma_f32_16x16x32_bf16 v[132:135], a[192:195], v[100:103], v[132:135]\n"
      "v_fma_f32 v156, v156, v179, v178\n"
      "v_mul_f32 v156, v124, v156\n"
      "v_mfma_f32_16x16x32_bf16 v[132:135], a[196:199], v[104:107], v[132:135]\n"
      "v_fma_f32 v158, v158, v179, v178\n"
      "v_mul_f32 v159, v127, v159\n"
      "v_mfma_f32_16x16x32_bf16 v[132:135], a[200:203], v[108:111], v[132:135]\n"
      "v_exp_f32 v156, v156\n"
      "v_mfma_f32_16x16x32_bf16 v[132:135], a[204:207], v[112:115], v[132:135]\n"
      "s_add_u32 m0, s38, 20480\n"
      "v_exp_f32 v159, v159\n"
      "v_mfma_f32_16x16x32_bf16 v[132:135], a[208:211], v[116:119], v[132:135]\n"
      "v_mul_f32 v158, v126, v158\n"
      "v_fma_f32 v159, v159, v177, v177\n"
      "v_mfma_f32_16x16x32_bf16 v[132:135], a[212:215], v[120:123], v[132:135]\n"
      "v_rcp_f32 v159, v159\n"
      "v_mfma_f32_16x16x32_bf16 v[128:131], a[72:75], v[52:55], 0\n"
      "s_waitcnt vmcnt(3)\n"
      "ds_write_b32 v13, v11\n"
      "v_mfma_f32_16x16x32_bf16 v[128:131], a[76:79], v[56:59], v[128:131]\n"
      "v_exp_f32 v158, v158\n"
      "v_mfma_f32_16x16x32_bf16 v[128:131], a[80:83], v[60:63], v[128:131]\n"
      "v_fma_f32 v156, v156, v177, v177\n"
      "v_fmac_f32 v135, v176, v151\n"
      "v_mfma_f32_16x16x32_bf16 v[128:131], a[84:87], v[64:67], v[128:131]\n"
      "v_fmac_f32 v133, v176, v149\n"
      "v_mul_f32 v163, v135, v135\n"
      "v_mfma_f32_16x16x32_bf16 v[128:131], a[88:91], v[68:71], v[128:131]\n"
      "v_mul_f32 v161, v133, v133\n"
      "v_fma_f32 v163, v163, v179, v178\n"
      "v_fma_f32 v161, v161, v179, v178\n"
      "v_mfma_f32_16x16x32_bf16 v[128:131], a[92:95], v[72:75], v[128:131]\n"
      "v_fma_f32 v157, v157, v177, v177\n"
      "v_mul_f32 v161, v133, v161\n"
      "v_mfma_f32_16x16x32_bf16 v[128:131], a[96:99], v[76:79], v[128:131]\n"
      "v_exp_f32 v161, v161\n"
      "v_mfma_f32_16x16x32_bf16 v[128:131], a[100:103], v[80:83], v[128:131]\n"
      "s_waitcnt vmcnt(1) lgkmcnt(0)\n"
      "v_fma_f32 v161, v161, v177, v177\n"
      "v_cmp_ne_u32 vcc, s21, v18\n"
      "v_mfma_f32_16x16x32_bf16 v[128:131], a[104:107], v[84:87], v[128:131]\n"
      "v_rcp_f32 v161, v161\n"
      "v_mfma_f32_16x16x32_bf16 v[128:131], a[108:111], v[88:91], v[128:131]\n"
      "ds_read_b32 v12, v14\n"
      "v_fmac_f32 v132, v176, v148\n"
      "v_mfma_f32_16x16x32_bf16 v[128:131], a[112:115], v[92:95], v[128:131]\n"
      "s_cbranch_vccnz .Lsp_g0_10\n"
      ".Lbk_g0_10:\n"
      "v_mul_f32 v160, v132, v132\n"
      "v_mfma_f32_16x16x32_bf16 v[128:131], a[116:119], v[96:99], v[128:131]\n"
      "buffer_load_dwordx4 v0, s[8:11], s40 offen offset:1024 sc1 lds\n"
      "v_mfma_f32_16x16x32_bf16 v[128:131], a[120:123], v[100:103], v[128:131]\n"
      "v_fma_f32 v160, v160, v179, v178\n"
      "v_mul_f32 v160, v132, v160\n"
      "v_mfma_f32_16x16x32_bf16 v[128:131], a[124:127], v[104:107], v[128:131]\n"
      "v_mul_f32 v163, v135, v163\n"
      "v_mul_f32 v161, v133, v161\n"
      "v_mfma_f32_16x16x32_bf16 v[128:131], a[128:131], v[108:111], v[128:131]\n"
      "v_fma_f32 v158, v158, v177, v177\n"
      "v_fmac_f32 v134, v176, v150\n"
      "v_mfma_f32_16x16x32_bf16 v[128:131], a[132:135], v[112:115], v[128:131]\n"
      "s_waitcnt lgkmcnt(0)\n"
      "v_mul_f32 v162, v134, v134\n"
      "v_cmp_gt_i32 vcc, v11, v12\n"
      "v_mfma_f32_16x16x32_bf16 v[128:131], a[136:139], v[116:119], v[128:131]\n"
      "buffer_load_dwordx4 v0, s[8:11], s40 offen offset:0 sc1 lds\n"
      "v_mfma_f32_16x16x32_bf16 v[128:131], a[140:143], v[120:123], v[128:131]\n"
      "v_rcp_f32_e64 v158, v158\n"
      "v_mfma_f32_16x16x32_bf16 v[136:139], a[216:219], v[52:55], 0\n"
      "v_exp_f32_e64 v160, v160\n"
      "v_mfma_f32_16x16x32_bf16 v[136:139], a[220:223], v[56:59], v[136:139]\n"
      "s_cbranch_vccnz .Lsp_gy0_11\n"
      ".Lbk_gy0_11:\n"
      "v_fma_f32 v160, v160, v177, v177\n"
      "v_mfma_f32_16x16x32_bf16 v[136:139], a[224:227], v[60:63], v[136:139]\n"
      "ds_read_b128 v[60:63], v0 offset:2048\n"
      "buffer_load_dwordx4 v0, s[8:11], s40 offen offset:2048 sc1 lds\n"
      "v_mfma_f32_16x16x32_bf16 v[136:139], a[228:231], v[64:67], v[136:139]\n"
      "ds_read_b128 v[52:55], v0 offset:0\n"
      "v_mul_f32 v159, v127, v159\n"
      "v_mfma_f32_16x16x32_bf16 v[136:139], a[232:235], v[68:71], v[136:139]\n"
      "v_exp_f32 v163, v163\n"
      "v_mfma_f32_16x16x32_bf16 v[136:139], a[236:239], v[72:75], v[136:139]\n"
      "v_rcp_f32 v157, v157\n"
      "v_mfma_f32_16x16x32_bf16 v[136:139], a[240:243], v[76:79], v[136:139]\n"
      "buffer_load_dwordx4 v0, s[8:11], s40 offen offset:3072 sc1 lds\n"
      "ds_read_b128 v[76:79], v0 offset:6144\n"
      "v_mfma_f32_16x16x32_bf16 v[136:139], a[244:247], v[80:83], v[136:139]\n"
      "ds_read2st64_b64 v[168:171], v4 offset0:0 offset1:1\n"
      "v_fmac_f32 v131, v176, v147\n"
      "v_mfma_f32_16x16x32_bf16 v[136:139], a[248:251], v[84:87], v[136:139]\n"
      "s_add_u32 m0, s39, 20480\n"
      "ds_read_b128 v[72:75], v0 offset:5120\n"
      "v_mul_f32 v158, v126, v158\n"
      "v_mfma_f32_16x16x32_bf16 v[136:139], a[252:255], v[88:91], v[136:139]\n"
      "v_rcp_f32 v156, v156\n"
      "v_mul_f32 v157, v125, v157\n"
      "v_mfma_f32_16x16x32_bf16 v[136:139], v[20:23], v[92:95], v[136:139]\n"
      "ds_read_b128 v[68:71], v0 offset:4096\n"
      "buffer_load_dwordx4 v1, s[8:11], s40 offen offset:0 sc1 lds\n"
      "v_mfma_f32_16x16x32_bf16 v[136:139], v[24:27], v[96:99], v[136:139]\n"
      "s_min_u32 s36, s36, s6\n"
      "ds_read_b128 v[56:59], v0 offset:1024\n"
      "v_fmac_f32 v129, v176, v145\n"
      "v_mfma_f32_16x16x32_bf16 v[136:139], v[28:31], v[100:103], v[136:139]\n"
      "ds_read_b128 v[64:67], v0 offset:3072\n"
      "v_rcp_f32 v160, v160\n"
      "v_mfma_f32_16x16x32_bf16 v[136:139], v[32:35], v[104:107], v[136:139]\n"
      "ds_read_b128 v[88:91], v0 offset:9216\n"
      "v_mul_f32 v160, v132, v160\n"
      "v_mfma_f32_16x16x32_bf16 v[136:139], v[36:39], v[108:111], v[136:139]\n"
      "s_waitcnt vmcnt(5) lgkmcnt(5)\n"
      "buffer_store_dword v15, v3, s[8:11], s42 offen sc1\n"
      "ds_read_b128 v[108:111], v0 offset:14336\n"
      "v_mfma_f32_16x16x32_bf16 v[136:139], v[40:43], v[112:115], v[136:139]\n"
      "ds_read_b128 v[80:83], v0 offset:7168\n"
      "v_mul_f32 v156, v124, v156\n"
      "v_mfma_f32_16x16x32_bf16 v[136:139], v[44:47], v[116:119], v[136:139]\n"
      "ds_read_b128 v[104:107], v0 offset:13312\n"
      "v_add_f32 v169, v169, v171\n"
      "v_mfma_f32_16x16x32_bf16 v[136:139], v[48:51], v[120:123], v[136:139]\n"
      "buffer_load_dword v18, v2, s[8:11], s36 offen sc1\n"
      "s_sub_u32 s20, s20, 1\n"
      "s_cbranch_scc0 .Lg_loop\n"
      ".Lg_tail:\n"
      "v_fmac_f32 v130, v176, v146\n"
      "v_fma_f32 v163, v163, v177, v177\n"
      "v_fma_f32 v162, v162, v179, v178\n"
      "v_mul_f32 v130, v130, v158\n"
      "v_mul_f32 v162, v134, v162\n"
      "s_nop 2\n"
      "v_fmac_f32 v137, v176, v153\n"
      "v_rcp_f32 v163, v163\n"
      "v_fmac_f32 v138, v176, v154\n"
      "v_exp_f32 v162, v162\n"
      "v_mul_f32 v129, v129, v157\n"
      "v_fma_f32 v162, v162, v177, v177\n"
      "v_rcp_f32 v162, v162\n"
      "v_mul_f32 v137, v137, v161\n"
      "v_fmac_f32 v136, v176, v152\n"
      "v_mul_f32 v136, v136, v160\n"
      "v_mul_f32 v131, v131, v159\n"
      "s_add_u32 s42, s42, 165888\n"
      "v_mul_f32 v162, v134, v162\n"
      "v_cvt_pk_bf16_f32 v166, v136, v137\n"
      "v_fmac_f32 v128, v176, v144\n"
      "v_mul_f32 v138, v138, v162\n"
      "v_cvt_pk_bf16_f32 v165, v130, v131\n"
      "v_mul_f32 v163, v135, v163\n"
      "v_fmac_f32 v139, v176, v155\n"
      "v_mul_f32 v128, v128, v156\n"
      "v_mul_f32 v139, v139, v163\n"
      "s_add_u32 s37, s37, 165888\n"
      "v_cvt_pk_bf16_f32 v164, v128, v129\n"
      "v_cvt_pk_bf16_f32 v167, v138, v139\n"
      "buffer_store_dwordx4 v[164:167], v0, s[8:11], s37 offen sc1\n"
      "s_waitcnt vmcnt(0)\n"
      "buffer_store_dword v15, v3, s[8:11], s42 offen sc1\n"
      "s_add_u32 s42, s42, 165888\n"
      "s_add_u32 s37, s37, 165888\n"
      "s_endpgm\n"
      ".Lsp_g0_1:\n"
      "s_mov_b32 s22, s23\n"
      ".Lsl_g0_1:\n"
      "buffer_load_dword v18, v2, s[8:11], s40 offen sc1\n"
      "s_waitcnt vmcnt(0)\n"
      "v_cmp_ne_u32 vcc, s21, v18\n"
      "s_cbranch_vccz .Lbk_g0_1\n"
      "s_sleep 4\n"
      "s_sub_u32 s22, s22, 1\n"
      "s_cbranch_scc0 .Lsl_g0_1\n"
      "s_mov_b32 s23, 0\n"
      "s_branch .Lbk_g0_1\n"
      ".Lsp_g0_2:\n"
      "s_mov_b32 s22, s23\n"
      ".Lsl_g0_2:\n"
      "buffer_load_dword v18, v2, s[8:11], s40 offen sc1\n"
      "s_waitcnt vmcnt(0)\n"
      "v_cmp_ne_u32 vcc, s21, v18\n"
      "s_cbranch_vccz .Lbk_g0_2\n"
      "s_sleep 4\n"
      "s_sub_u32 s22, s22, 1\n"
      "s_cbranch_scc0 .Lsl_g0_2\n"
      "s_mov_b32 s23, 0\n"
      "s_branch .Lbk_g0_2\n"
      ".Lsp_gy0_3:\n"
      "s_lshl_b32 s22, s23, 4\n"
      ".Lsl_gy0_3:\n"
      "ds_read_b32 v12, v14\n"
      "s_waitcnt lgkmcnt(0)\n"
      "v_cmp_gt_i32 vcc, v11, v12\n"
      "s_cbranch_vccz .Lbk_gy0_3\n"
      "s_sub_u32 s22, s22, 1\n"
      "s_cbranch_scc0 .Lsl_gy0_3\n"
      "s_mov_b32 s23, 0\n"
      "s_branch .Lbk_gy0_3\n"
      ".Lsp_g0_4:\n"
      "s_mov_b32 s22, s23\n"
      "buffer_load_dword v18, v2, s[8:11], s40 offen sc1\n"
      "s_waitcnt vmcnt(0)\n"
      "v_cmp_ne_u32 vcc, s21, v18\n"
      "s_add_u32 s30, s42, 0\n"
      "buffer_store_dword v15, v3, s[8:11], s30 offen sc1\n"
      "s_cbranch_vccz .Lbk_g0_4\n"
      ".Lsl_g0_4:\n"
      "buffer_load_dword v18, v2, s[8:11], s40 offen sc1\n"
      "s_waitcnt vmcnt(0)\n"
      "v_cmp_ne_u32 vcc, s21, v18\n"
      "s_cbranch_vccz .Lbk_g0_4\n"
      "s_sleep 4\n"
      "s_sub_u32 s22, s22, 1\n"
      "s_cbranch_scc0 .Lsl_g0_4\n"
      "s_mov_b32 s23, 0\n"
      "s_branch .Lbk_g0_4\n"
      ".Lsp_gy0_5:\n"
      "s_lshl_b32 s22, s23, 4\n"
      ".Lsl_gy0_5:\n"
      "ds_read_b32 v12, v14\n"
      "s_waitcnt lgkmcnt(0)\n"
      "v_cmp_gt_i32 vcc, v11, v12\n"
      "s_cbranch_vccz .Lbk_gy0_5\n"
      "s_sub_u32 s22, s22, 1\n"
      "s_cbranch_scc0 .Lsl_gy0_5\n"
      "s_mov_b32 s23, 0\n"
      "s_branch .Lbk_gy0_5\n"
      ".Lsp_g0_6:\n"
      "s_mov_b32 s22, s23\n"
      "buffer_load_dword v18, v2, s[8:11], s40 offen sc1\n"
      "s_waitcnt vmcnt(0)\n"
      "v_cmp_ne_u32 vcc, s21, v18\n"
      "s_add_u32 s30, s42, 0\n"
      "buffer_store_dword v15, v3, s[8:11], s30 offen sc1\n"
      "s_cbranch_vccz .Lbk_g0_6\n"
      ".Lsl_g0_6:\n"
      "buffer_load_dword v18, v2, s[8:11], s40 offen sc1\n"
      "s_waitcnt vmcnt(0)\n"
      "v_cmp_ne_u32 vcc, s21, v18\n"
      "s_cbranch_vccz .Lbk_g0_6\n"
      "s_sleep 4\n"
      "s_sub_u32 s22, s22, 1\n"
      "s_cbranch_scc0 .Lsl_g0_6\n"
      "s_mov_b32 s23, 0\n"
      "s_branch .Lbk_g0_6\n"
      ".Lsp_gy0_7:\n"
      "s_lshl_b32 s22, s23, 4\n"
      ".Lsl_gy0_7:\n"
      "ds_read_b32 v12, v14\n"
      "s_waitcnt lgkmcnt(0)\n"
      "v_cmp_gt_i32 vcc, v11, v12\n"
      "s_cbranch_vccz .Lbk_gy0_7\n"
      "s_sub_u32 s22, s22, 1\n"
      "s_cbranch_scc0 .Lsl_gy0_7\n"
      "s_mov_b32 s23, 0\n"
      "s_branch .Lbk_gy0_7\n"
      ".Lsp_g0_8:\n"
      "s_mov_b32 s22, s23\n"
      "buffer_load_dword v18, v2, s[8:11], s40 offen sc1\n"
      "s_waitcnt vmcnt(0)\n"
      "v_cmp_ne_u32 vcc, s21, v18\n"
      "s_add_u32 s30, s42, 0\n"
      "buffer_store_dword v15, v3, s[8:11], s30 offen sc1\n"
      "s_cbranch_vccz .Lbk_g0_8\n"
      ".Lsl_g0_8:\n"
      "buffer_load_dword v18, v2, s[8:11], s40 offen sc1\n"
      "s_waitcnt vmcnt(0)\n"
      "v_cmp_ne_u32 vcc, s21, v18\n"
      "s_cbranch_vccz .Lbk_g0_8\n"
      "s_sleep 4\n"
      "s_sub_u32 s22, s22, 1\n"
      "s_cbranch_scc0 .Lsl_g0_8\n"
      "s_mov_b32 s23, 0\n"
      "s_branch .Lbk_g0_8\n"
      ".Lsp_gy0_9:\n"
      "s_lshl_b32 s22, s23, 4\n"
      ".Lsl_gy0_9:\n"
      "ds_read_b32 v12, v14\n"
      "s_waitcnt lgkmcnt(0)\n"
      "v_cmp_gt_i32 vcc, v11, v12\n"
      "s_cbranch_vccz .Lbk_gy0_9\n"
      "s_sub_u32 s22, s22, 1\n"
      "s_cbranch_scc0 .Lsl_gy0_9\n"
      "s_mov_b32 s23, 0\n"
      "s_branch .Lbk_gy0_9\n"
      ".Lsp_g0_10:\n"
      "s_mov_b32 s22, s23\n"
      "buffer_load_dword v18, v2, s[8:11], s40 offen sc1\n"
      "s_waitcnt vmcnt(0)\n"
      "v_cmp_ne_u32 vcc, s21, v18\n"
      "s_add_u32 s30, s42, 0\n"
      "buffer_store_dword v15, v3, s[8:11], s30 offen sc1\n"
      "s_cbranch_vccz .Lbk_g0_10\n"
      ".Lsl_g0_10:\n"
      "buffer_load_dword v18, v2, s[8:11], s40 offen sc1\n"
      "s_waitcnt vmcnt(0)\n"
      "v_cmp_ne_u32 vcc, s21, v18\n"
      "s_cbranch_vccz .Lbk_g0_10\n"
      "s_sleep 4\n"
      "s_sub_u32 s22, s22, 1\n"
      "s_cbranch_scc0 .Lsl_g0_10\n"
      "s_mov_b32 s23, 0\n"
      "s_branch .Lbk_g0_10\n"
      ".Lsp_gy0_11:\n"
      "s_lshl_b32 s22, s23, 4\n"
      ".Lsl_gy0_11:\n"
      "ds_read_b32 v12, v14\n"
      "s_waitcnt lgkmcnt(0)\n"
      "v_cmp_gt_i32 vcc, v11, v12\n"
      "s_cbranch_vccz .Lbk_gy0_11\n"
      "s_sub_u32 s22, s22, 1\n"
      "s_cbranch_scc0 .Lsl_gy0_11\n"
      "s_mov_b32 s23, 0\n"
      "s_branch .Lbk_gy0_11\n"
      ".Lg_last:\n"
      :
      : "{s[8:11]}"(rsWS), "{s[12:15]}"(rsA), "{s[92:95]}"(rsC), "{v0}"(v_l16), "{v1}"(v_l5), "{v2}"(v_fa), "{v3}"(v_ra), "{v4}"(v_st), "{v5}"(v_at), "{v6}"(v_vo0), "{v7}"(v_vo1), "{v8}"(v_vo2), "{v9}"(v_vo3), "{v10}"(v_pw), "{v12}"(v_al), "{v13}"(v_yw), "{v14}"(v_yr), "{v16}"(v_hi), "{v54}"(v_tw[0]), "{v55}"(v_tw[1]), "{s36}"(s_sx), "{s37}"(s_sh), "{s38}"(s_m4), "{s39}"(s_m5)
      : "v255", "a255", "s99", "vcc", "scc", "memory");

  }
}

extern "C" void kernel_launch(void* const* d_in, const int* in_sizes, int n_in, void* d_out, int out_size,
                              void* d_ws, size_t ws_size, hipStream_t stream) {
  char* ws = (char*)d_ws + ((256 - ((size_t)d_ws & 255)) & 255);
  (void)hipFuncSetAttribute((const void*)k_all, hipFuncAttributeMaxDynamicSharedMemorySize, LDS_BYTES);
  k_all<<<256, 256, LDS_BYTES, stream>>>((const u16*)d_in[0], (const u16*)d_in[1], (const u16*)d_in[2], (const u16*)d_in[4],
                                         (const u16*)d_in[5], (const u16*)d_in[6], ws, (u16*)d_out);
}
```

```cpp
#include <hip/hip_runtime.h>
#include <stdint.h>

typedef unsigned short u16;
typedef __attribute__((ext_vector_type(4))) unsigned u32x4;
constexpr unsigned WS_DBG = 65536u;
constexpr unsigned WS_XIN = 1310720u;
constexpr unsigned WS_OB = 9175040u;
constexpr unsigned WS_X1 = 17039360u;
constexpr unsigned WS_H = 26034176u;
constexpr unsigned WS_PS = 78589952u;
constexpr unsigned WS_END = 109740032u;
constexpr unsigned KSK = 0u;
constexpr unsigned ENT = 1u;
constexpr unsigned ROT = 0u;
constexpr unsigned MAP = 1u;
constexpr unsigned FLIP = 0u;
constexpr unsigned PW = 1664u;
constexpr unsigned REC = 24576u;
constexpr unsigned HREC = 165888u;
constexpr unsigned PREC = 99840u;
constexpr unsigned NREC = 80u;
constexpr unsigned OOB = 0x40000000u, NEG = 0xff61b1e6u;
constexpr int LDS_BYTES = 160 * 1024;

__device__ __forceinline__ u32x4 srd(const void* p, unsigned bytes) {
  const unsigned long long a = (unsigned long long)p;
  return u32x4{(unsigned)a, (unsigned)(a >> 32) & 0xffffu, bytes, 0x00020000u};
}

extern "C" __global__ __launch_bounds__(256, 1) __attribute__((amdgpu_waves_per_eu(1, 1))) void k_all(
    const u16* X, const u16* Wqkv, const u16* Wo, const u16* Wg, const u16* Wu, const u16* Wd, char* ws, u16* out) {
  const unsigned b = blockIdx.x, xcd = b & 7, slot = b >> 3;
  const unsigned w = __builtin_amdgcn_readfirstlane(threadIdx.x >> 6), lane = threadIdx.x & 63;
  const bool fast = ((xcd ^ FLIP) & 1) == 0;
  if (ENT) {
    constexpr unsigned NL = MAP ? 41 : 27, LR = MAP ? 9 : 6;
    const unsigned wi = slot * 4 + w, n0 = wi * NL + lane, n = n0 < 576 * LR ? n0 : 0, r = n / LR, c = n - LR * r;
    const unsigned m = (xcd * 128 + wi) * 4 + lane - NL, t = m / 144;
    unsigned oc = (wi * 8 + (lane & 7)) * 128;
    asm volatile("s_getpc_b64 vcc\n.Lent_pc:\n\ts_sub_u32 vcc_lo, vcc_lo, .Lent_pc-k_all\n\ts_subb_u32 vcc_hi, vcc_hi, 0\n\ts_mov_b32 m0, %1\n\t"
                 "v_min_u32 %0, .Lq_last-k_all, %0\n\tglobal_load_lds_dword %0, vcc" : "+v"(oc) : "s"(162816 + w * 256) : "vcc", "scc", "memory");
    unsigned oq = r * 3456 + c / (LR / 3) * 1152 + ((MAP ? xcd & 6 : xcd) + c % (LR / 3)) * 128, ox = (32 * (t & 3) + 31 - (t >> 2)) * 18432 + (m - 144 * t) * 128;
    asm volatile("" : "+v"(oq), "+v"(ox));
    const bool isq = lane < NL && (fast || !MAP);
    oq = isq ? oq : lane - NL < 4 ? ox : 0;
    asm volatile("" : "+v"(oq));
    __builtin_amdgcn_sched_barrier(0);
    const char* pa = (const char*)(isq ? Wqkv : X) + oq;
    asm volatile("global_load_lds_dword %0, off" ::"v"(pa) : "memory");
    __builtin_amdgcn_sched_barrier(0);
  }
  unsigned role, gr, part;
  if (MAP) {
    const unsigned e = xcd >> 1, sd = slot - (fast ? 10 : 0), sg = slot - (fast ? 16 : 12), i3 = sg / 3, e1 = (e - 1) & 3;
    role = fast ? (slot >= 8) + (slot >= 10) + (slot >= 16) : 2 + (slot >= 12);
    gr = role == 0 ? slot & 3 : role == 1 ? e : role == 2 ? sd / 3 + (fast ? 2 * (e & 1) : 0) : !fast ? sg & 3 : sg < 12 ? (e + 1 + sg - 3 * i3) & 3 : e1;
    part = role == 0 ? 2 * e + (slot >> 2) : role == 1 ? slot - 8 : role == 2 ? (fast ? e >> 1 : e + 2) * 3 + sd % 3
         : !fast ? 5 * e + (sg >> 2) : sg < 12 ? 20 + 4 * e + i3 : 8 + 4 * e1 + sg;
  } else {
    const unsigned td = ((xcd + 4) & 7) * 3 + (slot - 5) / 3, tg = xcd * 3 + (slot - 14) / 6;
    role = (slot >= 4) + (slot >= 5) + (slot >= 14);
    gr = role == 0 ? slot : role == 1 ? 3 - (xcd & 3) : role == 2 ? td & 3 : tg & 3;
    part = role == 0 ? xcd : role == 1 ? xcd >> 2 : role == 2 ? (td >> 2) * 3 + (slot - 5) % 3 : 6 * (tg >> 2) + (slot - 14) % 6;
  }
  const unsigned i16 = lane & 15, g = lane >> 4, q = i16 >> 2, p = lane & 3;
  const u32x4 rsWS = srd(ws, WS_END);
  const unsigned v_l16 = lane * 16, v_dbg = WS_DBG + (((role == 0 ? 0 : role == 1 ? 32 : role == 2 ? 40 : 112) + part * 4 + gr) * 4 + w) * 1024 + lane * 4;
  const unsigned yb = role & 1 ? 81920 : role ? 149760 : 61440, v_yw = yb + w * 4, v_yr = yb + p * 4;
  const unsigned half = (i16 + 16 * (g >> 1)) * 16 + (g & 1) * 8;
  const unsigned line = lane < 32 ? lane * 4 : OOB, v_al = w ? OOB : line + 128 * (1 + b);
  const unsigned up = lane >> 5, l5 = (lane & 31) * 4, v_hi = 0u - up;
  const unsigned l8 = lane >> 3, l7 = (lane & 7) * 16;
  (void)v_dbg;
  if (role == 0) {
    const unsigned hg = part, h = 4 * hg + w, par = i16 & 1;
    const u32x4 rsA = srd(Wqkv, 576 * 1728 * 2);
    const unsigned v_l5 = w < 3 || lane < 32 ? v_l16 + 16384 - w * 3072 : OOB;
    const unsigned v_fa = 20480 - w * 4096 + (lane < 48 ? lane >> 2 : 11) * 128 + p * 4, v_ra = up * 8 * REC + l5 + 20480 + h * 128;
    const unsigned v_st = 18432 + i16 * 32 + q * 8, v_tw = 61568 + w * 128 + i16 * 8, v_tr = 61568 + w * 128 + g * 32;
    const unsigned v_om = h * 512 + half, v_oe = g ? OOB : 16384 + h * 128 + i16 * 8;
    const unsigned v_pw = 20480 + h * 128 + l8 * REC + l7;
    unsigned v_vo[4], v_bc[4], v_bp[4], v_mk[4], v_sel[4] = {0, 0, 0, 0}, v_ci[4];
#pragma unroll
    for (unsigned i = 0; i < 4; ++i) {
      const unsigned kk = 4 * g + i, ka = 4 * g + 2 * (i & 1) + par;
      v_bc[i] = (i & 1) == par && kk <= i16 ? 0u : NEG;
      v_bp[i] = (i & 1) == par && kk >= i16 + 2 ? 0u : NEG;
      v_mk[i] = (i < 2 ? ka > i16 : ka <= i16) ? 0xffffu << (16 * par) : 0u;
      v_ci[i] = p == 2 ? 0x3f800000u : 0u;
    }
    v_sel[3] = lane == 16 ? 0x3f80u : lane == 17 ? 0x3f800000u : 0u;
#pragma unroll
    for (unsigned i = 0; i < 4; ++i) {
      const unsigned c = 64 * i + lane, cc = c % 30;
      v_vo[i] = c < 240 && cc < 27 ? (8 * w + c / 30) * 3456 + cc / 9 * 1152 + 144 * hg + cc % 9 * 16 - 1024 * i : OOB;
    }
    const unsigned rb = 65536 + g * 4224 + q * 480;
    const unsigned v_at = rb + 36 * w + 4 * (w & 1) + 8 * p, v_ax = rb + (p < 2 ? p : 2) * 144 + 72 * (w >> 1) + 32;
    const unsigned v_pm = (16 * g + 4 * (i16 >> 1 & 3) + 2 * (w & 1) + par) * 4, v_ms = i16 < 6 ? ~0u : 0u;
    const unsigned s_sx = WS_XIN + (gr * NREC + KSK) * REC + w * 4096, s_ev = s_sx + (32 - KSK) * REC, s_del = 0u - (gr ? NREC + 32 : 32) * REC;
    const unsigned s_kil = gr ? 0x7f7fffffu : 0xff7fffffu, s_so = WS_OB + (gr * NREC + KSK) * REC;
    const unsigned s_m4 = w * 4096, s_m5 = 16384 + w * 1024, s_ms = w * 4224, s_up = part + gr + w;
    asm volatile(
      "s_buffer_load_dword s21, s[8:11], 0\n"
      "s_mov_b32 s23, 65536\n"
      "s_mov_b32 s31, 0x40000000\n"
      "s_mov_b32 s51, 0\n"
      "s_add_u32 m0, s43, 65536\n"
      "s_nop 0\n"
      "buffer_load_dwordx4 v48, s[12:15], s51 offen offset:0 lds\n"
      "buffer_load_dwordx4 v49, s[12:15], s51 offen offset:1024 lds\n"
      "buffer_load_dwordx4 v50, s[12:15], s51 offen offset:2048 lds\n"
      "buffer_load_dwordx4 v51, s[12:15], s51 offen offset:3072 lds\n"
      "s_add_u32 s51, s51, 110592\n"
      "s_add_u32 m0, s43, 82432\n"
      "s_nop 0\n"
      "buffer_load_dwordx4 v48, s[12:15], s51 offen offset:0 lds\n"
      "buffer_load_dwordx4 v49, s[12:15], s51 offen offset:1024 lds\n"
      "buffer_load_dwordx4 v50, s[12:15], s51 offen offset:2048 lds\n"
      "buffer_load_dwordx4 v51, s[12:15], s51 offen offset:3072 lds\n"
      "s_add_u32 s51, s51, 110592\n"
      "s_add_u32 m0, s43, 99328\n"
      "s_nop 0\n"
      "buffer_load_dwordx4 v48, s[12:15], s51 offen offset:0 lds\n"
      "buffer_load_dwordx4 v49, s[12:15], s51 offen offset:1024 lds\n"
      "buffer_load_dwordx4 v50, s[12:15], s51 offen offset:2048 lds\n"
      "buffer_load_dwordx4 v51, s[12:15], s51 offen offset:3072 lds\n"
      "s_add_u32 s51, s51, 110592\n"
      "s_add_u32 s6, s36, 1572864\n"
      "s_add_u32 s36, s36, 4294942720\n"
      "s_add_u32 s48, s37, 811008\n"
      "s_mov_b32 s53, 0x7f7fffff\n"
      "v_mov_b32 v179, 0\n"
      "s_mov_b32 s54, 0xffff\n"
      "s_mov_b32 s55, 0\n"
      "v_mov_b32 v9, 0\n"
      "ds_write_b32 v11, v9\n"
      "v_mov_b32 v56, 0x3f803f80\n"
      "v_mov_b32 v57, 0x3f803f80\n"
      "v_mov_b32 v58, 0x3f803f80\n"
      "v_mov_b32 v59, 0x3f803f80\n"
      "s_mov_b64 exec, 0\n"
      "s_lshr_b32 s22, s41, 12\n"
      ".Lq_pl:\n"
      "s_cmp_eq_u32 s22, 0\n"
      "s_cbranch_scc1 .Lq_p0\n"
      "s_cmp_eq_u32 s22, 1\n"
      "s_cbranch_scc1 .Lq_p1\n"
      "s_branch .Lq_pe\n"
      ".Lq_pr:\n"
      "s_add_u32 s22, s22, 4\n"
      "s_branch .Lq_pl\n"
      ".Lq_pe:\n"
      "s_mov_b64 exec, -1\n"
      "s_waitcnt vmcnt(8) lgkmcnt(0)\n"
      "s_barrier\n"
      "ds_read_b64_tr_b16 v[60:61], v53 offset:0\n"
      "ds_read_b64_tr_b16 v[62:63], v53 offset:1920\n"
      "ds_read_b64_tr_b16 a[0:1], v52 offset:144\n"
      "ds_read_b64_tr_b16 a[2:3], v52 offset:2064\n"
      "ds_read_b64_tr_b16 a[72:73], v52 offset:288\n"
      "ds_read_b64_tr_b16 a[74:75], v52 offset:2208\n"
      "ds_read_b64_tr_b16 a[144:145], v52 offset:0\n"
      "ds_read_b64_tr_b16 a[146:147], v52 offset:1920\n"
      "v_add_u32 v52, 16896, v52\n"
      "v_add_u32 v53, 16896, v53\n"
      "s_add_u32 m0, s43, 116224\n"
      "s_nop 0\n"
      "buffer_load_dwordx4 v48, s[12:15], s51 offen offset:0 lds\n"
      "buffer_load_dwordx4 v49, s[12:15], s51 offen offset:1024 lds\n"
      "buffer_load_dwordx4 v50, s[12:15], s51 offen offset:2048 lds\n"
      "buffer_load_dwordx4 v51, s[12:15], s51 offen offset:3072 lds\n"
      "s_add_u32 s51, s51, 110592\n"
      "s_waitcnt vmcnt(8) lgkmcnt(0)\n"
      "s_barrier\n"
      "ds_read_b64_tr_b16 v[68:69], v53 offset:0\n"
      "ds_read_b64_tr_b16 v[70:71], v53 offset:1920\n"
      "ds_read_b64_tr_b16 a[4:5], v52 offset:144\n"
      "ds_read_b64_tr_b16 a[6:7], v52 offset:2064\n"
      "ds_read_b64_tr_b16 a[76:77], v52 offset:288\n"
      "ds_read_b64_tr_b16 a[78:79], v52 offset:2208\n"
      "ds_read_b64_tr_b16 a[148:149], v52 offset:0\n"
      "ds_read_b64_tr_b16 a[150:151], v52 offset:1920\n"
      "v_add_u32 v52, 16896, v52\n"
      "v_add_u32 v53, 16896, v53\n"
      "ds_bpermute_b32 v64, v54, v60\n"
      "ds_bpermute_b32 v65, v54, v61\n"
      "ds_bpermute_b32 v66, v54, v62\n"
      "ds_bpermute_b32 v67, v54, v63\n"
      "v_mfma_f32_16x16x32_bf16 v[136:139], a[0:3], v[56:59], 0\n"
      "v_mfma_f32_16x16x32_bf16 v[144:147], v[56:59], a[72:75], 0\n"
      "v_mfma_f32_16x16x32_bf16 v[148:151], a[144:147], v[56:59], 0\n"
      "s_add_u32 m0, s43, 65536\n"
      "s_nop 0\n"
      "buffer_load_dwordx4 v48, s[12:15], s51 offen offset:0 lds\n"
      "buffer_load_dwordx4 v49, s[12:15], s51 offen offset:1024 lds\n"
      "buffer_load_dwordx4 v50, s[12:15], s51 offen offset:2048 lds\n"
      "buffer_load_dwordx4 v51, s[12:15], s51 offen offset:3072 lds\n"
      "s_add_u32 s51, s51, 110592\n"
      "s_waitcnt vmcnt(8) lgkmcnt(0)\n"
      "s_barrier\n"
      "ds_read_b64_tr_b16 v[60:61], v53 offset:0\n"
      "ds_read_b64_tr_b16 v[62:63], v53 offset:1920\n"
      "ds_read_b64_tr_b16 a[8:9], v52 offset:144\n"
      "ds_read_b64_tr_b16 a[10:11], v52 offset:2064\n"
      "ds_read_b64_tr_b16 a[80:81], v52 offset:288\n"
      "ds_read_b64_tr_b16 a[82:83], v52 offset:2208\n"
      "ds_read_b64_tr_b16 a[152:153], v52 offset:0\n"
      "ds_read_b64_tr_b16 a[154:155], v52 offset:1920\n"
      "v_add_u32 v52, 16896, v52\n"
      "v_add_u32 v53, 16896, v53\n"
      "ds_bpermute_b32 v72, v54, v68\n"
      "ds_bpermute_b32 v73, v54, v69\n"
      "ds_bpermute_b32 v74, v54, v70\n"
      "ds_bpermute_b32 v75, v54, v71\n"
      "v_mfma_f32_16x16x32_bf16 v[136:139], a[4:7], v[56:59], v[136:139]\n"
      "v_mfma_f32_16x16x32_bf16 v[144:147], v[56:59], a[76:79], v[144:147]\n"
      "v_mfma_f32_16x16x32_bf16 v[148:151], a[148:151], v[56:59], v[148:151]\n"
      "s_add_u32 m0, s43, 82432\n"
      "s_nop 0\n"
      "buffer_load_dwordx4 v48, s[12:15], s51 offen offset:0 lds\n"
      "buffer_load_dwordx4 v49, s[12:15], s51 offen offset:1024 lds\n"
      "buffer_load_dwordx4 v50, s[12:15], s51 offen offset:2048 lds\n"
      "buffer_load_dwordx4 v51, s[12:15], s51 offen offset:3072 lds\n"
      "s_add_u32 s51, s51, 110592\n"
      "v_and_b32 v64, v55, v64\n"
      "v_and_b32 v65, v55, v65\n"
      "v_and_b32 v66, v55, v66\n"
      "v_and_b32 v67, v55, v67\n"
      "v_accvgpr_write_b32 a216, v64\n"
      "v_accvgpr_write_b32 a217, v65\n"
      "v_accvgpr_write_b32 a218, v66\n"
      "v_accvgpr_write_b32 a219, v67\n"
      "v_mfma_f32_16x16x32_bf16 v[140:143], v[64:67], v[56:59], 0\n"
      "s_waitcnt vmcnt(8) lgkmcnt(0)\n"
      "s_barrier\n"
      "ds_read_b64_tr_b16 v[68:69], v53 offset:0\n"
      "ds_read_b64_tr_b16 v[70:71], v53 offset:1920\n"
      "ds_read_b64_tr_b16 a[12:13], v52 offset:144\n"
      "ds_read_b64_tr_b16 a[14:15], v52 offset:2064\n"
      "ds_read_b64_tr_b16 a[84:85], v52 offset:288\n"
      "ds_read_b64_tr_b16 a[86:87], v52 offset:2208\n"
      "ds_read_b64_tr_b16 a[156:157], v52 offset:0\n"
      "ds_read_b64_tr_b16 a[158:159], v52 offset:1920\n"
      "v_add_u32 v52, 4294916608, v52\n"
      "v_add_u32 v53, 4294916608, v53\n"
      "ds_bpermute_b32 v64, v54, v60\n"
      "ds_bpermute_b32 v65, v54, v61\n"
      "ds_bpermute_b32 v66, v54, v62\n"
      "ds_bpermute_b32 v67, v54, v63\n"
      "v_mfma_f32_16x16x32_bf16 v[136:139], a[8:11], v[56:59], v[136:139]\n"
      "v_mfma_f32_16x16x32_bf16 v[144:147], v[56:59], a[80:83], v[144:147]\n"
      "v_mfma_f32_16x16x32_bf16 v[148:151], a[152:155], v[56:59], v[148:151]\n"
      "s_add_u32 m0, s43, 99328\n"
      "s_nop 0\n"
      "buffer_load_dwordx4 v48, s[12:15], s51 offen offset:0 lds\n"
      "buffer_load_dwordx4 v49, s[12:15], s51 offen offset:1024 lds\n"
      "buffer_load_dwordx4 v50, s[12:15], s51 offen offset:2048 lds\n"
      "buffer_load_dwordx4 v51, s[12:15], s51 offen offset:3072 lds\n"
      "s_add_u32 s51, s51, 110592\n"
      "v_and_b32 v72, v55, v72\n"
      "v_and_b32 v73, v55, v73\n"
      "v_and_b32 v74, v55, v74\n"
      "v_and_b32 v75, v55, v75\n"
      "v_accvgpr_write_b32 a220, v72\n"
      "v_accvgpr_write_b32 a221, v73\n"
      "v_accvgpr_write_b32 a222, v74\n"
      "v_accvgpr_write_b32 a223, v75\n"
      "v_mfma_f32_16x16x32_bf16 v[140:143], v[72:75], v[56:59], v[140:143]\n"
      "s_waitcnt vmcnt(8) lgkmcnt(0)\n"
      "s_barrier\n"
      "ds_read_b64_tr_b16 v[60:61], v53 offset:0\n"
      "ds_read_b64_tr_b16 v[62:63], v53 offset:1920\n"
      "ds_read_b64_tr_b16 a[16:17], v52 offset:144\n"
      "ds_read_b64_tr_b16 a[18:19], v52 offset:2064\n"
      "ds_read_b64_tr_b16 a[88:89], v52 offset:288\n"
      "ds_read_b64_tr_b16 a[90:91], v52 offset:2208\n"
      "ds_read_b64_tr_b16 a[160:161], v52 offset:0\n"
      "ds_read_b64_tr_b16 a[162:163], v52 offset:1920\n"
      "v_add_u32 v52, 16896, v52\n"
      "v_add_u32 v53, 16896, v53\n"
      "ds_bpermute_b32 v72, v54, v68\n"
      "ds_bpermute_b32 v73, v54, v69\n"
      "ds_bpermute_b32 v74, v54, v70\n"
      "ds_bpermute_b32 v75, v54, v71\n"
      "v_mfma_f32_16x16x32_bf16 v[136:139], a[12:15], v[56:59], v[136:139]\n"
      "v_mfma_f32_16x16x32_bf16 v[144:147], v[56:59], a[84:87], v[144:147]\n"
      "v_mfma_f32_16x16x32_bf16 v[148:151], a[156:159], v[56:59], v[148:151]\n"
      "s_add_u32 m0, s43, 116224\n"
      "s_nop 0\n"
      "buffer_load_dwordx4 v48, s[12:15], s51 offen offset:0 lds\n"
      "buffer_load_dwordx4 v49, s[12:15], s51 offen offset:1024 lds\n"
      "buffer_load_dwordx4 v50, s[12:15], s51 offen offset:2048 lds\n"
      "buffer_load_dwordx4 v51, s[12:15], s51 offen offset:3072 lds\n"
      "s_add_u32 s51, s51, 110592\n"
      "v_and_b32 v64, v55, v64\n"
      "v_and_b32 v65, v55, v65\n"
      "v_and_b32 v66, v55, v66\n"
      "v_and_b32 v67, v55, v67\n"
      "v_accvgpr_write_b32 a224, v64\n"
      "v_accvgpr_write_b32 a225, v65\n"
      "v_accvgpr_write_b32 a226, v66\n"
      "v_accvgpr_write_b32 a227, v67\n"
      "v_mfma_f32_16x16x32_bf16 v[140:143], v[64:67], v[56:59], v[140:143]\n"
      "s_waitcnt vmcnt(8) lgkmcnt(0)\n"
      "s_barrier\n"
      "ds_read_b64_tr_b16 v[68:69], v53 offset:0\n"
      "ds_read_b64_tr_b16 v[70:71], v53 offset:1920\n"
      "ds_read_b64_tr_b16 a[20:21], v52 offset:144\n"
      "ds_read_b64_tr_b16 a[22:23], v52 offset:2064\n"
      "ds_read_b64_tr_b16 a[92:93], v52 offset:288\n"
      "ds_read_b64_tr_b16 a[94:95], v52 offset:2208\n"
      "ds_read_b64_tr_b16 a[164:165], v52 offset:0\n"
      "ds_read_b64_tr_b16 a[166:167], v52 offset:1920\n"
      "v_add_u32 v52, 16896, v52\n"
      "v_add_u32 v53, 16896, v53\n"
      "ds_bpermute_b32 v64, v54, v60\n"
      "ds_bpermute_b32 v65, v54, v61\n"
      "ds_bpermute_b32 v66, v54, v62\n"
      "ds_bpermute_b32 v67, v54, v63\n"
      "v_mfma_f32_16x16x32_bf16 v[136:139], a[16:19], v[56:59], v[136:139]\n"
      "v_mfma_f32_16x16x32_bf16 v[144:147], v[56:59], a[88:91], v[144:147]\n"
      "v_mfma_f32_16x16x32_bf16 v[148:151], a[160:163], v[56:59], v[148:151]\n"
      "s_add_u32 m0, s43, 65536\n"
      "s_nop 0\n"
      "buffer_load_dwordx4 v48, s[12:15], s51 offen offset:0 lds\n"
      "buffer_load_dwordx4 v49, s[12:15], s51 offen offset:1024 lds\n"
      "buffer_load_dwordx4 v50, s[12:15], s51 offen offset:2048 lds\n"
      "buffer_load_dwordx4 v51, s[12:15], s51 offen offset:3072 lds\n"
      "s_add_u32 s51, s51, 110592\n"
      "v_and_b32 v72, v55, v72\n"
      "v_and_b32 v73, v55, v73\n"
      "v_and_b32 v74, v55, v74\n"
      "v_and_b32 v75, v55, v75\n"
      "v_accvgpr_write_b32 a228, v72\n"
      "v_accvgpr_write_b32 a229, v73\n"
      "v_accvgpr_write_b32 a230, v74\n"
      "v_accvgpr_write_b32 a231, v75\n"
      "v_mfma_f32_16x16x32_bf16 v[140:143], v[72:75], v[56:59], v[140:143]\n"
      "s_waitcnt vmcnt(8) lgkmcnt(0)\n"
      "s_barrier\n"
      "ds_read_b64_tr_b16 v[60:61], v53 offset:0\n"
      "ds_read_b64_tr_b16 v[62:63], v53 offset:1920\n"
      "ds_read_b64_tr_b16 a[24:25], v52 offset:144\n"
      "ds_read_b64_tr_b16 a[26:27], v52 offset:2064\n"
      "ds_read_b64_tr_b16 a[96:97], v52 offset:288\n"
      "ds_read_b64_tr_b16 a[98:99], v52 offset:2208\n"
      "ds_read_b64_tr_b16 a[168:169], v52 offset:0\n"
      "ds_read_b64_tr_b16 a[170:171], v52 offset:1920\n"
      "v_add_u32 v52, 16896, v52\n"
      "v_add_u32 v53, 16896, v53\n"
      "ds_bpermute_b32 v72, v54, v68\n"
      "ds_bpermute_b32 v73, v54, v69\n"
      "ds_bpermute_b32 v74, v54, v70\n"
      "ds_bpermute_b32 v75, v54, v71\n"
      "v_mfma_f32_16x16x32_bf16 v[136:139], a[20:23], v[56:59], v[136:139]\n"
      "v_mfma_f32_16x16x32_bf16 v[144:147], v[56:59], a[92:95], v[144:147]\n"
      "v_mfma_f32_16x16x32_bf16 v[148:151], a[164:167], v[56:59], v[148:151]\n"
      "s_add_u32 m0, s43, 82432\n"
      "s_nop 0\n"
      "buffer_load_dwordx4 v48, s[12:15], s51 offen offset:0 lds\n"
      "buffer_load_dwordx4 v49, s[12:15], s51 offen offset:1024 lds\n"
      "buffer_load_dwordx4 v50, s[12:15], s51 offen offset:2048 lds\n"
      "buffer_load_dwordx4 v51, s[12:15], s51 offen offset:3072 lds\n"
      "s_add_u32 s51, s51, 110592\n"
      "v_and_b32 v64, v55, v64\n"
      "v_and_b32 v65, v55, v65\n"
      "v_and_b32 v66, v55, v66\n"
      "v_and_b32 v67, v55, v67\n"
      "v_accvgpr_write_b32 a232, v64\n"
      "v_accvgpr_write_b32 a233, v65\n"
      "v_accvgpr_write_b32 a234, v66\n"
      "v_accvgpr_write_b32 a235, v67\n"
      "v_mfma_f32_16x16x32_bf16 v[140:143], v[64:67], v[56:59], v[140:143]\n"
      "s_waitcnt vmcnt(8) lgkmcnt(0)\n"
      "s_barrier\n"
      "ds_read_b64_tr_b16 v[68:69], v53 offset:0\n"
      "ds_read_b64_tr_b16 v[70:71], v53 offset:1920\n"
      "ds_read_b64_tr_b16 a[28:29], v52 offset:144\n"
      "ds_read_b64_tr_b16 a[30:31], v52 offset:2064\n"
      "ds_read_b64_tr_b16 a[100:101], v52 offset:288\n"
      "ds_read_b64_tr_b16 a[102:103], v52 offset:2208\n"
      "ds_read_b64_tr_b16 a[172:173], v52 offset:0\n"
      "ds_read_b64_tr_b16 a[174:175], v52 offset:1920\n"
      "v_add_u32 v52, 4294916608, v52\n"
      "v_add_u32 v53, 4294916608, v53\n"
      "ds_bpermute_b32 v64, v54, v60\n"
      "ds_bpermute_b32 v65, v54, v61\n"
      "ds_bpermute_b32 v66, v54, v62\n"
      "ds_bpermute_b32 v67, v54, v63\n"
      "v_mfma_f32_16x16x32_bf16 v[136:139], a[24:27], v[56:59], v[136:139]\n"
      "v_mfma_f32_16x16x32_bf16 v[144:147], v[56:59], a[96:99], v[144:147]\n"
      "v_mfma_f32_16x16x32_bf16 v[148:151], a[168:171], v[56:59], v[148:151]\n"
      "s_add_u32 m0, s43, 99328\n"
      "s_nop 0\n"
      "buffer_load_dwordx4 v48, s[12:15], s51 offen offset:0 lds\n"
      "buffer_load_dwordx4 v49, s[12:15], s51 offen offset:1024 lds\n"
      "buffer_load_dwordx4 v50, s[12:15], s51 offen offset:2048 lds\n"
      "buffer_load_dwordx4 v51, s[12:15], s51 offen offset:3072 lds\n"
      "s_add_u32 s51, s51, 110592\n"
      "v_and_b32 v72, v55, v72\n"
      "v_and_b32 v73, v55, v73\n"
      "v_and_b32 v74, v55, v74\n"
      "v_and_b32 v75, v55, v75\n"
      "v_accvgpr_write_b32 a236, v72\n"
      "v_accvgpr_write_b32 a237, v73\n"
      "v_accvgpr_write_b32 a238, v74\n"
      "v_accvgpr_write_b32 a239, v75\n"
      "v_mfma_f32_16x16x32_bf16 v[140:143], v[72:75], v[56:59], v[140:143]\n"
      "s_waitcnt vmcnt(8) lgkmcnt(0)\n"
      "s_barrier\n"
      "ds_read_b64_tr_b16 v[60:61], v53 offset:0\n"
      "ds_read_b64_tr_b16 v[62:63], v53 offset:1920\n"
      "ds_read_b64_tr_b16 a[32:33], v52 offset:144\n"
      "ds_read_b64_tr_b16 a[34:35], v52 offset:2064\n"
      "ds_read_b64_tr_b16 a[104:105], v52 offset:288\n"
      "ds_read_b64_tr_b16 a[106:107], v52 offset:2208\n"
      "ds_read_b64_tr_b16 a[176:177], v52 offset:0\n"
      "ds_read_b64_tr_b16 a[178:179], v52 offset:1920\n"
      "v_add_u32 v52, 16896, v52\n"
      "v_add_u32 v53, 16896, v53\n"
      "ds_bpermute_b32 v72, v54, v68\n"
      "ds_bpermute_b32 v73, v54, v69\n"
      "ds_bpermute_b32 v74, v54, v70\n"
      "ds_bpermute_b32 v75, v54, v71\n"
      "v_mfma_f32_16x16x32_bf16 v[136:139], a[28:31], v[56:59], v[136:139]\n"
      "v_mfma_f32_16x16x32_bf16 v[144:147], v[56:59], a[100:103], v[144:147]\n"
      "v_mfma_f32_16x16x32_bf16 v[148:151], a[172:175], v[56:59], v[148:151]\n"
      "s_add_u32 m0, s43, 116224\n"
      "s_nop 0\n"
      "buffer_load_dwordx4 v48, s[12:15], s51 offen offset:0 lds\n"
      "buffer_load_dwordx4 v49, s[12:15], s51 offen offset:1024 lds\n"
      "buffer_load_dwordx4 v50, s[12:15], s51 offen offset:2048 lds\n"
      "buffer_load_dwordx4 v51, s[12:15], s51 offen offset:3072 lds\n"
      "s_add_u32 s51, s51, 110592\n"
      "v_and_b32 v64, v55, v64\n"
      "v_and_b32 v65, v55, v65\n"
      "v_and_b32 v66, v55, v66\n"
      "v_and_b32 v67, v55, v67\n"
      "v_accvgpr_write_b32 a240, v64\n"
      "v_accvgpr_write_b32 a241, v65\n"
      "v_accvgpr_write_b32 a242, v66\n"
      "v_accvgpr_write_b32 a243, v67\n"
      "v_mfma_f32_16x16x32_bf16 v[140:143], v[64:67], v[56:59], v[140:143]\n"
      "s_waitcnt vmcnt(8) lgkmcnt(0)\n"
      "s_barrier\n"
      "ds_read_b64_tr_b16 v[68:69], v53 offset:0\n"
      "ds_read_b64_tr_b16 v[70:71], v53 offset:1920\n"
      "ds_read_b64_tr_b16 a[36:37], v52 offset:144\n"
      "ds_read_b64_tr_b16 a[38:39], v52 offset:2064\n"
      "ds_read_b64_tr_b16 a[108:109], v52 offset:288\n"
      "ds_read_b64_tr_b16 a[110:111], v52 offset:2208\n"
      "ds_read_b64_tr_b16 a[180:181], v52 offset:0\n"
      "ds_read_b64_tr_b16 a[182:183], v52 offset:1920\n"
      "v_add_u32 v52, 16896, v52\n"
      "v_add_u32 v53, 16896, v53\n"
      "ds_bpermute_b32 v64, v54, v60\n"
      "ds_bpermute_b32 v65, v54, v61\n"
      "ds_bpermute_b32 v66, v54, v62\n"
      "ds_bpermute_b32 v67, v54, v63\n"
      "v_mfma_f32_16x16x32_bf16 v[136:139], a[32:35], v[56:59], v[136:139]\n"
      "v_mfma_f32_16x16x32_bf16 v[144:147], v[56:59], a[104:107], v[144:147]\n"
      "v_mfma_f32_16x16x32_bf16 v[148:151], a[176:179], v[56:59], v[148:151]\n"
      "s_add_u32 m0, s43, 65536\n"
      "s_nop 0\n"
      "buffer_load_dwordx4 v48, s[12:15], s51 offen offset:0 lds\n"
      "buffer_load_dwordx4 v49, s[12:15], s51 offen offset:1024 lds\n"
      "buffer_load_dwordx4 v50, s[12:15], s51 offen offset:2048 lds\n"
      "buffer_load_dwordx4 v51, s[12:15], s51 offen offset:3072 lds\n"
      "s_add_u32 s51, s51, 110592\n"
      "v_and_b32 v72, v55, v72\n"
      "v_and_b32 v73, v55, v73\n"
      "v_and_b32 v74, v55, v74\n"
      "v_and_b32 v75, v55, v75\n"
      "v_accvgpr_write_b32 a244, v72\n"
      "v_accvgpr_write_b32 a245, v73\n"
      "v_accvgpr_write_b32 a246, v74\n"
      "v_accvgpr_write_b32 a247, v75\n"
      "v_mfma_f32_16x16x32_bf16 v[140:143], v[72:75], v[56:59], v[140:143]\n"
      "s_waitcnt vmcnt(8) lgkmcnt(0)\n"
      "s_barrier\n"
      "ds_read_b64_tr_b16 v[60:61], v53 offset:0\n"
      "ds_read_b64_tr_b16 v[62:63], v53 offset:1920\n"
      "ds_read_b64_tr_b16 a[40:41], v52 offset:144\n"
      "ds_read_b64_tr_b16 a[42:43], v52 offset:2064\n"
      "ds_read_b64_tr_b16 a[112:113], v52 offset:288\n"
      "ds_read_b64_tr_b16 a[114:115], v52 offset:2208\n"
      "ds_read_b64_tr_b16 a[184:185], v52 offset:0\n"
      "ds_read_b64_tr_b16 a[186:187], v52 offset:1920\n"
      "v_add_u32 v52, 16896, v52\n"
      "v_add_u32 v53, 16896, v53\n"
      "ds_bpermute_b32 v72, v54, v68\n"
      "ds_bpermute_b32 v73, v54, v69\n"
      "ds_bpermute_b32 v74, v54, v70\n"
      "ds_bpermute_b32 v75, v54, v71\n"
      "v_mfma_f32_16x16x32_bf16 v[136:139], a[36:39], v[56:59], v[136:139]\n"
      "v_mfma_f32_16x16x32_bf16 v[144:147], v[56:59], a[108:111], v[144:147]\n"
      "v_mfma_f32_16x16x32_bf16 v[148:151], a[180:183], v[56:59], v[148:151]\n"
      "s_add_u32 m0, s43, 82432\n"
      "s_nop 0\n"
      "buffer_load_dwordx4 v48, s[12:15], s51 offen offset:0 lds\n"
      "buffer_load_dwordx4 v49, s[12:15], s51 offen offset:1024 lds\n"
      "buffer_load_dwordx4 v50, s[12:15], s51 offen offset:2048 lds\n"
      "buffer_load_dwordx4 v51, s[12:15], s51 offen offset:3072 lds\n"
      "s_add_u32 s51, s51, 110592\n"
      "v_and_b32 v64, v55, v64\n"
      "v_and_b32 v65, v55, v65\n"
      "v_and_b32 v66, v55, v66\n"
      "v_and_b32 v67, v55, v67\n"
      "v_accvgpr_write_b32 a248, v64\n"
      "v_accvgpr_write_b32 a249, v65\n"
      "v_accvgpr_write_b32 a250, v66\n"
      "v_accvgpr_write_b32 a251, v67\n"
      "v_mfma_f32_16x16x32_bf16 v[140:143], v[64:67], v[56:59], v[140:143]\n"
      "s_waitcnt vmcnt(8) lgkmcnt(0)\n"
      "s_barrier\n"
      "ds_read_b64_tr_b16 v[68:69], v53 offset:0\n"
      "ds_read_b64_tr_b16 v[70:71], v53 offset:1920\n"
      "ds_read_b64_tr_b16 a[44:45], v52 offset:144\n"
      "ds_read_b64_tr_b16 a[46:47], v52 offset:2064\n"
      "ds_read_b64_tr_b16 a[116:117], v52 offset:288\n"
      "ds_read_b64_tr_b16 a[118:119], v52 offset:2208\n"
      "ds_read_b64_tr_b16 a[188:189], v52 offset:0\n"
      "ds_read_b64_tr_b16 a[190:191], v52 offset:1920\n"
      "v_add_u32 v52, 4294916608, v52\n"
      "v_add_u32 v53, 4294916608, v53\n"
      "ds_bpermute_b32 v64, v54, v60\n"
      "ds_bpermute_b32 v65, v54, v61\n"
      "ds_bpermute_b32 v66, v54, v62\n"
      "ds_bpermute_b32 v67, v54, v63\n"
      "v_mfma_f32_16x16x32_bf16 v[136:139], a[40:43], v[56:59], v[136:139]\n"
      "v_mfma_f32_16x16x32_bf16 v[144:147], v[56:59], a[112:115], v[144:147]\n"
      "v_mfma_f32_16x16x32_bf16 v[148:151], a[184:187], v[56:59], v[148:151]\n"
      "s_add_u32 m0, s43, 99328\n"
      "s_nop 0\n"
      "buffer_load_dwordx4 v48, s[12:15], s51 offen offset:0 lds\n"
      "buffer_load_dwordx4 v49, s[12:15], s51 offen offset:1024 lds\n"
      "buffer_load_dwordx4 v50, s[12:15], s51 offen offset:2048 lds\n"
      "buffer_load_dwordx4 v51, s[12:15], s51 offen offset:3072 lds\n"
      "s_add_u32 s51, s51, 110592\n"
      "v_and_b32 v72, v55, v72\n"
      "v_and_b32 v73, v55, v73\n"
      "v_and_b32 v74, v55, v74\n"
      "v_and_b32 v75, v55, v75\n"
      "v_accvgpr_write_b32 a252, v72\n"
      "v_accvgpr_write_b32 a253, v73\n"
      "v_accvgpr_write_b32 a254, v74\n"
      "v_accvgpr_write_b32 a255, v75\n"
      "v_mfma_f32_16x16x32_bf16 v[140:143], v[72:75], v[56:59], v[140:143]\n"
      "s_waitcnt vmcnt(8) lgkmcnt(0)\n"
      "s_barrier\n"
      "ds_read_b64_tr_b16 v[60:61], v53 offset:0\n"
      "ds_read_b64_tr_b16 v[62:63], v53 offset:1920\n"
      "ds_read_b64_tr_b16 a[48:49], v52 offset:144\n"
      "ds_read_b64_tr_b16 a[50:51], v52 offset:2064\n"
      "ds_read_b64_tr_b16 a[120:121], v52 offset:288\n"
      "ds_read_b64_tr_b16 a[122:123], v52 offset:2208\n"
      "ds_read_b64_tr_b16 a[192:193], v52 offset:0\n"
      "ds_read_b64_tr_b16 a[194:195], v52 offset:1920\n"
      "v_add_u32 v52, 16896, v52\n"
      "v_add_u32 v53, 16896, v53\n"
      "ds_bpermute_b32 v72, v54, v68\n"
      "ds_bpermute_b32 v73, v54, v69\n"
      "ds_bpermute_b32 v74, v54, v70\n"
      "ds_bpermute_b32 v75, v54, v71\n"
      "v_mfma_f32_16x16x32_bf16 v[136:139], a[44:47], v[56:59], v[136:139]\n"
      "v_mfma_f32_16x16x32_bf16 v[144:147], v[56:59], a[116:119], v[144:147]\n"
      "v_mfma_f32_16x16x32_bf16 v[148:151], a[188:191], v[56:59], v[148:151]\n"
      "s_add_u32 m0, s43, 116224\n"
      "s_nop 0\n"
      "buffer_load_dwordx4 v48, s[12:15], s51 offen offset:0 lds\n"
      "buffer_load_dwordx4 v49, s[12:15], s51 offen offset:1024 lds\n"
      "buffer_load_dwordx4 v50, s[12:15], s51 offen offset:2048 lds\n"
      "buffer_load_dwordx4 v51, s[12:15], s51 offen offset:3072 lds\n"
      "s_add_u32 s51, s51, 110592\n"
      "v_and_b32 v64, v55, v64\n"
      "v_and_b32 v65, v55, v65\n"
      "v_and_b32 v66, v55, v66\n"
      "v_and_b32 v67, v55, v67\n"
      "v_mov_b32 v16, v64\n"
      "v_mov_b32 v17, v65\n"
      "v_mov_b32 v18, v66\n"
      "v_mov_b32 v19, v67\n"
      "v_mfma_f32_16x16x32_bf16 v[140:143], v[64:67], v[56:59], v[140:143]\n"
      "s_add_u32 s30, s36, 24576\n"
      "buffer_load_dword v78, v2, s[8:11], s30 offen sc1\n"
      "s_add_u32 s30, s36, 49152\n"
      "buffer_load_dword v79, v2, s[8:11], s30 offen sc1\n"
      "s_waitcnt vmcnt(10) lgkmcnt(0)\n"
      "s_barrier\n"
      "ds_read_b64_tr_b16 v[68:69], v53 offset:0\n"
      "ds_read_b64_tr_b16 v[70:71], v53 offset:1920\n"
      "ds_read_b64_tr_b16 a[52:53], v52 offset:144\n"
      "ds_read_b64_tr_b16 a[54:55], v52 offset:2064\n"
      "ds_read_b64_tr_b16 a[124:125], v52 offset:288\n"
      "ds_read_b64_tr_b16 a[126:127], v52 offset:2208\n"
      "ds_read_b64_tr_b16 a[196:197], v52 offset:0\n"
      "ds_read_b64_tr_b16 a[198:199], v52 offset:1920\n"
      "v_add_u32 v52, 16896, v52\n"
      "v_add_u32 v53, 16896, v53\n"
      "ds_bpermute_b32 v64, v54, v60\n"
      "ds_bpermute_b32 v65, v54, v61\n"
      "ds_bpermute_b32 v66, v54, v62\n"
      "ds_bpermute_b32 v67, v54, v63\n"
      "v_mfma_f32_16x16x32_bf16 v[136:139], a[48:51], v[56:59], v[136:139]\n"
      "v_mfma_f32_16x16x32_bf16 v[144:147], v[56:59], a[120:123], v[144:147]\n"
      "v_mfma_f32_16x16x32_bf16 v[148:151], a[192:195], v[56:59], v[148:151]\n"
      "s_add_u32 m0, s43, 65536\n"
      "s_nop 0\n"
      "buffer_load_dwordx4 v48, s[12:15], s51 offen offset:0 lds\n"
      "buffer_load_dwordx4 v49, s[12:15], s51 offen offset:1024 lds\n"
      "buffer_load_dwordx4 v50, s[12:15], s51 offen offset:2048 lds\n"
      "buffer_load_dwordx4 v51, s[12:15], s51 offen offset:3072 lds\n"
      "s_add_u32 s51, s51, 110592\n"
      "v_and_b32 v72, v55, v72\n"
      "v_and_b32 v73, v55, v73\n"
      "v_and_b32 v74, v55, v74\n"
      "v_and_b32 v75, v55, v75\n"
      "v_mov_b32 v20, v72\n"
      "v_mov_b32 v21, v73\n"
      "v_mov_b32 v22, v74\n"
      "v_mov_b32 v23, v75\n"
      "v_mfma_f32_16x16x32_bf16 v[140:143], v[72:75], v[56:59], v[140:143]\n"
      "s_waitcnt vmcnt(10) lgkmcnt(0)\n"
      "s_barrier\n"
      "ds_read_b64_tr_b16 v[60:61], v53 offset:0\n"
      "ds_read_b64_tr_b16 v[62:63], v53 offset:1920\n"
      "ds_read_b64_tr_b16 a[56:57], v52 offset:144\n"
      "ds_read_b64_tr_b16 a[58:59], v52 offset:2064\n"
      "ds_read_b64_tr_b16 a[128:129], v52 offset:288\n"
      "ds_read_b64_tr_b16 a[130:131], v52 offset:2208\n"
      "ds_read_b64_tr_b16 a[200:201], v52 offset:0\n"
      "ds_read_b64_tr_b16 a[202:203], v52 offset:1920\n"
      "v_add_u32 v52, 16896, v52\n"
      "v_add_u32 v53, 16896, v53\n"
      "ds_bpermute_b32 v72, v54, v68\n"
      "ds_bpermute_b32 v73, v54, v69\n"
      "ds_bpermute_b32 v74, v54, v70\n"
      "ds_bpermute_b32 v75, v54, v71\n"
      "v_mfma_f32_16x16x32_bf16 v[136:139], a[52:55], v[56:59], v[136:139]\n"
      "v_mfma_f32_16x16x32_bf16 v[144:147], v[56:59], a[124:127], v[144:147]\n"
      "v_mfma_f32_16x16x32_bf16 v[148:151], a[196:199], v[56:59], v[148:151]\n"
      "s_add_u32 m0, s43, 82432\n"
      "s_nop 0\n"
      "buffer_load_dwordx4 v48, s[12:15], s51 offen offset:0 lds\n"
      "buffer_load_dwordx4 v49, s[12:15], s51 offen offset:1024 lds\n"
      "buffer_load_dwordx4 v50, s[12:15], s51 offen offset:2048 lds\n"
      "buffer_load_dwordx4 v51, s[12:15], s51 offen offset:3072 lds\n"
      "s_add_u32 s51, s51, 110592\n"
      "v_and_b32 v64, v55, v64\n"
      "v_and_b32 v65, v55, v65\n"
      "v_and_b32 v66, v55, v66\n"
      "v_and_b32 v67, v55, v67\n"
      "v_mov_b32 v24, v64\n"
      "v_mov_b32 v25, v65\n"
      "v_mov_b32 v26, v66\n"
      "v_mov_b32 v27, v67\n"
      "v_mfma_f32_16x16x32_bf16 v[140:143], v[64:67], v[56:59], v[140:143]\n"
      "s_mul_i32 s21, s21, 1664525\n"
      "s_add_u32 s21, s21, 1013904223\n"
      "v_mov_b32 v13, s21\n"
      "buffer_store_dword v13, v10, s[8:11], 0 offen sc1\n"
      "v_not_b32 v80, v13\n"
      "v_not_b32 v81, v13\n"
      "v_not_b32 v82, v13\n"
      "v_not_b32 v83, v13\n"
      "s_add_u32 s30, s40, 0\n"
      "buffer_store_dwordx4 v[80:83], v76, s[8:11], s30 offen sc1\n"
      "v_xor_b32 v13, v13, v14\n"
      "s_add_u32 s52, s40, 4294893568\n"
      "s_add_u32 s40, s40, 4294918144\n"
      "s_add_u32 s36, s36, 24576\n"
      "s_mov_b32 s45, s36\n"
      "s_min_u32 s45, s45, s6\n"
      "s_waitcnt vmcnt(11)\n"
      "v_cmp_ne_u32 vcc, s21, v78\n"
      "s_add_u32 m0, s41, 0\n"
      "s_add_u32 s36, s36, 24576\n"
      "s_mov_b32 s46, s45\n"
      "s_cbranch_vccnz .Lsp_q0_1\n"
      ".Lbk_q0_1:\n"
      "buffer_load_dwordx4 v0, s[8:11], s46 offen offset:1024 sc1 lds\n"
      "buffer_load_dwordx4 v0, s[8:11], s46 offen offset:0 sc1 lds\n"
      "buffer_load_dwordx4 v0, s[8:11], s46 offen offset:2048 sc1 lds\n"
      "s_mov_b32 s45, s36\n"
      "s_min_u32 s45, s45, s6\n"
      "buffer_load_dwordx4 v0, s[8:11], s46 offen offset:3072 sc1 lds\n"
      "s_add_u32 m0, s42, 0\n"
      "s_nop 0\n"
      "buffer_load_dwordx4 v1, s[8:11], s46 offen offset:0 sc1 lds\n"
      "s_waitcnt lgkmcnt(0)\n"
      "s_barrier\n"
      "ds_read_b64_tr_b16 v[68:69], v53 offset:0\n"
      "ds_read_b64_tr_b16 v[70:71], v53 offset:1920\n"
      "ds_read_b64_tr_b16 a[60:61], v52 offset:144\n"
      "ds_read_b64_tr_b16 a[62:63], v52 offset:2064\n"
      "ds_read_b64_tr_b16 a[132:133], v52 offset:288\n"
      "ds_read_b64_tr_b16 a[134:135], v52 offset:2208\n"
      "ds_read_b64_tr_b16 a[204:205], v52 offset:0\n"
      "ds_read_b64_tr_b16 a[206:207], v52 offset:1920\n"
      "v_add_u32 v52, 4294916608, v52\n"
      "v_add_u32 v53, 4294916608, v53\n"
      "ds_bpermute_b32 v64, v54, v60\n"
      "ds_bpermute_b32 v65, v54, v61\n"
      "ds_bpermute_b32 v66, v54, v62\n"
      "ds_bpermute_b32 v67, v54, v63\n"
      "v_mfma_f32_16x16x32_bf16 v[136:139], a[56:59], v[56:59], v[136:139]\n"
      "v_mfma_f32_16x16x32_bf16 v[144:147], v[56:59], a[128:131], v[144:147]\n"
      "v_mfma_f32_16x16x32_bf16 v[148:151], a[200:203], v[56:59], v[148:151]\n"
      "v_and_b32 v72, v55, v72\n"
      "v_and_b32 v73, v55, v73\n"
      "v_and_b32 v74, v55, v74\n"
      "v_and_b32 v75, v55, v75\n"
      "v_mov_b32 v28, v72\n"
      "v_mov_b32 v29, v73\n"
      "v_mov_b32 v30, v74\n"
      "v_mov_b32 v31, v75\n"
      "v_mfma_f32_16x16x32_bf16 v[140:143], v[72:75], v[56:59], v[140:143]\n"
      "s_waitcnt vmcnt(11) lgkmcnt(0)\n"
      "s_barrier\n"
      "ds_read_b64_tr_b16 v[60:61], v53 offset:0\n"
      "ds_read_b64_tr_b16 v[62:63], v53 offset:1920\n"
      "ds_read_b64_tr_b16 a[64:65], v52 offset:144\n"
      "ds_read_b64_tr_b16 a[66:67], v52 offset:2064\n"
      "ds_read_b64_tr_b16 a[136:137], v52 offset:288\n"
      "ds_read_b64_tr_b16 a[138:139], v52 offset:2208\n"
      "ds_read_b64_tr_b16 a[208:209], v52 offset:0\n"
      "ds_read_b64_tr_b16 a[210:211], v52 offset:1920\n"
      "v_add_u32 v52, 16896, v52\n"
      "v_add_u32 v53, 16896, v53\n"
      "ds_bpermute_b32 v72, v54, v68\n"
      "ds_bpermute_b32 v73, v54, v69\n"
      "ds_bpermute_b32 v74, v54, v70\n"
      "ds_bpermute_b32 v75, v54, v71\n"
      "v_mfma_f32_16x16x32_bf16 v[136:139], a[60:63], v[56:59], v[136:139]\n"
      "v_mfma_f32_16x16x32_bf16 v[144:147], v[56:59], a[132:135], v[144:147]\n"
      "v_mfma_f32_16x16x32_bf16 v[148:151], a[204:207], v[56:59], v[148:151]\n"
      "v_and_b32 v64, v55, v64\n"
      "v_and_b32 v65, v55, v65\n"
      "v_and_b32 v66, v55, v66\n"
      "v_and_b32 v67, v55, v67\n"
      "v_mov_b32 v32, v64\n"
      "v_mov_b32 v33, v65\n"
      "v_mov_b32 v34, v66\n"
      "v_mov_b32 v35, v67\n"
      "v_mfma_f32_16x16x32_bf16 v[140:143], v[64:67], v[56:59], v[140:143]\n"
      "s_waitcnt vmcnt(7) lgkmcnt(0)\n"
      "s_barrier\n"
      "ds_read_b64_tr_b16 v[68:69], v53 offset:0\n"
      "ds_read_b64_tr_b16 v[70:71], v53 offset:1920\n"
      "ds_read_b64_tr_b16 a[68:69], v52 offset:144\n"
      "ds_read_b64_tr_b16 a[70:71], v52 offset:2064\n"
      "ds_read_b64_tr_b16 a[140:141], v52 offset:288\n"
      "ds_read_b64_tr_b16 a[142:143], v52 offset:2208\n"
      "ds_read_b64_tr_b16 a[212:213], v52 offset:0\n"
      "ds_read_b64_tr_b16 a[214:215], v52 offset:1920\n"
      "v_add_u32 v52, 16896, v52\n"
      "v_add_u32 v53, 16896, v53\n"
      "ds_bpermute_b32 v64, v54, v60\n"
      "ds_bpermute_b32 v65, v54, v61\n"
      "ds_bpermute_b32 v66, v54, v62\n"
      "ds_bpermute_b32 v67, v54, v63\n"
      "v_mfma_f32_16x16x32_bf16 v[136:139], a[64:67], v[56:59], v[136:139]\n"
      "v_mfma_f32_16x16x32_bf16 v[144:147], v[56:59], a[136:139], v[144:147]\n"
      "v_mfma_f32_16x16x32_bf16 v[148:151], a[208:211], v[56:59], v[148:151]\n"
      "v_and_b32 v72, v55, v72\n"
      "v_and_b32 v73, v55, v73\n"
      "v_and_b32 v74, v55, v74\n"
      "v_and_b32 v75, v55, v75\n"
      "v_mov_b32 v36, v72\n"
      "v_mov_b32 v37, v73\n"
      "v_mov_b32 v38, v74\n"
      "v_mov_b32 v39, v75\n"
      "v_mfma_f32_16x16x32_bf16 v[140:143], v[72:75], v[56:59], v[140:143]\n"
      "s_waitcnt lgkmcnt(0)\n"
      "ds_bpermute_b32 v72, v54, v68\n"
      "ds_bpermute_b32 v73, v54, v69\n"
      "ds_bpermute_b32 v74, v54, v70\n"
      "ds_bpermute_b32 v75, v54, v71\n"
      "v_mfma_f32_16x16x32_bf16 v[136:139], a[68:71], v[56:59], v[136:139]\n"
      "v_mfma_f32_16x16x32_bf16 v[144:147], v[56:59], a[140:143], v[144:147]\n"
      "v_mfma_f32_16x16x32_bf16 v[148:151], a[212:215], v[56:59], v[148:151]\n"
      "v_and_b32 v64, v55, v64\n"
      "v_and_b32 v65, v55, v65\n"
      "v_and_b32 v66, v55, v66\n"
      "v_and_b32 v67, v55, v67\n"
      "v_mov_b32 v40, v64\n"
      "v_mov_b32 v41, v65\n"
      "v_mov_b32 v42, v66\n"
      "v_mov_b32 v43, v67\n"
      "v_mfma_f32_16x16x32_bf16 v[140:143], v[64:67], v[56:59], v[140:143]\n"
      "s_waitcnt lgkmcnt(0)\n"
      "v_and_b32 v72, v55, v72\n"
      "v_and_b32 v73, v55, v73\n"
      "v_and_b32 v74, v55, v74\n"
      "v_and_b32 v75, v55, v75\n"
      "v_mov_b32 v44, v72\n"
      "v_mov_b32 v45, v73\n"
      "v_mov_b32 v46, v74\n"
      "v_mov_b32 v47, v75\n"
      "v_mfma_f32_16x16x32_bf16 v[140:143], v[72:75], v[56:59], v[140:143]\n"
      "s_add_u32 s36, s36, 24576\n"
      "s_cmp_eq_u32 s36, s37\n"
      "s_cselect_b32 s47, s38, 0\n"
      "s_cselect_b32 s37, s48, s37\n"
      "s_cselect_b32 s49, s39, s53\n"
      "s_add_u32 m0, s41, 20480\n"
      "s_mov_b32 s46, s45\n"
      "s_add_u32 s45, s36, s47\n"
      "v_add_u32 v9, 1, v9\n"
      "v_cmp_ne_u32 vcc, s21, v79\n"
      "s_min_u32 s45, s45, s6\n"
      "s_cbranch_vccnz .Lsp_q1_2\n"
      ".Lbk_q1_2:\n"
      "buffer_load_dwordx4 v0, s[8:11], s46 offen offset:1024 sc1 lds\n"
      "s_waitcnt vmcnt(1)\n"
      "ds_write_b32 v11, v9\n"
      "buffer_load_dwordx4 v0, s[8:11], s46 offen offset:0 sc1 lds\n"
      "buffer_load_dwordx4 v0, s[8:11], s46 offen offset:3072 sc1 lds\n"
      "buffer_load_dword v15, v2, s[8:11], s45 offen sc1\n"
      "ds_read_b32 v10, v12\n"
      "buffer_load_dwordx4 v0, s[8:11], s46 offen offset:2048 sc1 lds\n"
      "s_waitcnt lgkmcnt(0)\n"
      "v_cmp_gt_i32 vcc, v9, v10\n"
      "s_add_u32 m0, s42, 20480\n"
      "s_nop 0\n"
      "buffer_load_dwordx4 v1, s[8:11], s46 offen offset:0 sc1 lds\n"
      "s_cbranch_vccnz .Lsp_qy0_3\n"
      ".Lbk_qy0_3:\n"
      "ds_read_b128 v[52:55], v0 offset:1024\n"
      "ds_read_b128 v[48:51], v0 offset:0\n"
      "ds_read_b128 v[56:59], v0 offset:2048\n"
      "ds_read_b128 v[68:71], v0 offset:5120\n"
      "ds_read_b128 v[64:67], v0 offset:4096\n"
      "ds_read_b128 v[60:63], v0 offset:3072\n"
      "buffer_load_dword v14, v11, s[8:11], s31 offen\n"
      "ds_read2st64_b64 v[212:215], v4 offset0:0 offset1:1\n"
      "ds_read_b128 v[72:75], v0 offset:6144\n"
      "ds_read_b128 v[76:79], v0 offset:7168\n"
      "ds_read_b128 v[88:91], v0 offset:10240\n"
      "ds_read_b128 v[108:111], v0 offset:15360\n"
      "s_mov_b32 s20, 21\n"
      "s_waitcnt lgkmcnt(0)\n"
      "s_nop 4\n"
      "s_branch .Lq_loop\n"
      ".p2align 12\n"
      ".Lq_p0:\n"
      "s_branch .Lq_pr\n"
      ".p2align 5\n"
      ".Lq_loop:\n"
      "v_mfma_f32_16x16x32_bf16 v[120:123], a[0:3], v[48:51], 0\n"
      "ds_read_b128 v[80:83], v0 offset:8192\n"
      "ds_read_b128 v[84:87], v0 offset:9216\n"
      "v_mfma_f32_16x16x32_bf16 v[120:123], a[4:7], v[52:55], v[120:123]\n"
      "ds_read_b128 v[96:99], v0 offset:12288\n"
      "v_mul_f32 v232, 0x3eae1a93, v219\n"
      "v_mfma_f32_16x16x32_bf16 v[120:123], a[8:11], v[56:59], v[120:123]\n"
      "s_waitcnt lgkmcnt(7)\n"
      "ds_read_b128 v[104:107], v0 offset:14336\n"
      "v_fma_f32 v135, -v218, v151, v135\n"
      "v_mfma_f32_16x16x32_bf16 v[120:123], a[12:15], v[60:63], v[120:123]\n"
      "v_fma_f32 v133, -v218, v149, v133\n"
      "v_fma_f32 v134, -v218, v150, v134\n"
      "v_mfma_f32_16x16x32_bf16 v[120:123], a[16:19], v[64:67], v[120:123]\n"
      "ds_read_b128 v[92:95], v0 offset:11264\n"
      "v_fma_f32 v132, -v218, v148, v132\n"
      "v_mfma_f32_16x16x32_bf16 v[120:123], a[20:23], v[68:71], v[120:123]\n"
      "s_waitcnt lgkmcnt(5)\n"
      "ds_read_b64 v[216:217], v4 offset:1024\n"
      "v_cvt_pk_bf16_f32 v176, v132, v133\n"
      "v_mfma_f32_16x16x32_bf16 v[120:123], a[24:27], v[72:75], v[120:123]\n"
      "ds_read_b128 v[100:103], v0 offset:13312\n"
      "v_cvt_pk_bf16_f32 v177, v134, v135\n"
      "v_mfma_f32_16x16x32_bf16 v[120:123], a[28:31], v[76:79], v[120:123]\n"
      "s_waitcnt lgkmcnt(5)\n"
      "v_rcp_f32 v210, v210\n"
      "v_mfma_f32_16x16x32_bf16 v[120:123], a[32:35], v[80:83], v[120:123]\n"
      "v_mul_f32 v204, v204, v210\n"
      "v_add_f32 v212, v212, v214\n"
      "v_mfma_f32_16x16x32_bf16 v[188:191], v[172:175], v[176:179], v[152:155]\n"
      "ds_read_b128 v[112:115], v0 offset:16384\n"
      "v_mfma_f32_16x16x32_bf16 v[120:123], a[36:39], v[84:87], v[120:123]\n"
      "ds_read_b128 v[116:119], v0 offset:17408\n"
      "v_mul_f32 v208, v208, v210\n"
      "v_mfma_f32_16x16x32_bf16 v[120:123], a[40:43], v[88:91], v[120:123]\n"
      "s_waitcnt lgkmcnt(4)\n"
      "s_add_u32 s36, s36, 24576\n"
      "v_mul_f32 v207, v207, v210\n"
      "v_mfma_f32_16x16x32_bf16 v[120:123], a[44:47], v[92:95], v[120:123]\n"
      "v_mul_f32 v209, v209, v210\n"
      "v_cvt_pk_bf16_f32 v230, v208, v209\n"
      "v_mfma_f32_16x16x32_bf16 v[120:123], a[48:51], v[96:99], v[120:123]\n"
      "s_waitcnt lgkmcnt(2)\n"
      "buffer_store_dwordx2 v[230:231], v8, s[8:11], s40 offen sc1\n"
      "v_mfma_f32_16x16x32_bf16 v[120:123], a[52:55], v[100:103], v[120:123]\n"
      "v_mul_f32 v206, v206, v210\n"
      "v_add_f32 v218, v212, v216\n"
      "v_mfma_f32_16x16x32_bf16 v[120:123], a[56:59], v[104:107], v[120:123]\n"
      "s_mov_b32 s46, s45\n"
      "v_add_f32 v213, v213, v215\n"
      "v_mfma_f32_16x16x32_bf16 v[120:123], a[60:63], v[108:111], v[120:123]\n"
      "s_waitcnt lgkmcnt(1)\n"
      "v_cvt_pk_bf16_f32 v229, v206, v207\n"
      "v_mfma_f32_16x16x32_bf16 v[120:123], a[64:67], v[112:115], v[120:123]\n"
      "s_waitcnt lgkmcnt(0)\n"
      "v_mul_f32 v205, v205, v210\n"
      "v_cvt_pk_bf16_f32 v228, v204, v205\n"
      "v_mfma_f32_16x16x32_bf16 v[120:123], a[68:71], v[116:119], v[120:123]\n"
      "buffer_store_dwordx2 v[228:229], v7, s[8:11], s40 offen sc1\n"
      "v_mfma_f32_16x16x32_bf16 v[124:127], a[216:219], v[48:51], 0\n"
      "v_add_f32 v213, v213, v217\n"
      "v_fma_f32 v213, -v218, v218, v213\n"
      "v_mfma_f32_16x16x32_bf16 v[124:127], a[220:223], v[52:55], v[124:127]\n"
      "v_max_f32 v213, 0x3727c5ac, v213\n"
      "s_mov_b32 s45, s36\n"
      "v_mfma_f32_16x16x32_bf16 v[124:127], a[224:227], v[56:59], v[124:127]\n"
      "v_mov_b32 v187, v185\n"
      "v_mfma_f32_16x16x32_bf16 v[124:127], a[228:231], v[60:63], v[124:127]\n"
      "v_rsq_f32 v219, v213\n"
      "v_mfma_f32_16x16x32_bf16 v[124:127], a[232:235], v[64:67], v[124:127]\n"
      "s_add_u32 m0, s41, 40960\n"
      "ds_write_b64 v5, v[218:219] offset:0\n"
      "v_mfma_f32_16x16x32_bf16 v[124:127], a[236:239], v[68:71], v[124:127]\n"
      "s_waitcnt vmcnt(5)\n"
      "v_mov_b32 v186, v184\n"
      "v_cmp_ne_u32 vcc, s21, v15\n"
      "v_mfma_f32_16x16x32_bf16 v[124:127], a[240:243], v[72:75], v[124:127]\n"
      "s_min_u32 s45, s45, s6\n"
      "v_mfma_f32_16x16x32_bf16 v[124:127], a[244:247], v[76:79], v[124:127]\n"
      "v_cndmask_b32 v233, v219, 1.0, s[54:55]\n"
      "v_add_u32 v9, 1, v9\n"
      "v_mfma_f32_16x16x32_bf16 v[124:127], a[248:251], v[80:83], v[124:127]\n"
      "v_fma_f32 v123, -v218, v139, v123\n"
      "v_fma_f32 v122, -v218, v138, v122\n"
      "v_mfma_f32_16x16x32_bf16 v[124:127], a[252:255], v[84:87], v[124:127]\n"
      "v_fma_f32 v120, -v218, v136, v120\n"
      "v_fma_f32 v121, -v218, v137, v121\n"
      "v_mfma_f32_16x16x32_bf16 v[124:127], v[16:19], v[88:91], v[124:127]\n"
      "ds_read_b128 v[224:227], v6 offset:16\n"
      "v_mul_f32 v121, v219, v121\n"
      "v_mfma_f32_16x16x32_bf16 v[124:127], v[20:23], v[92:95], v[124:127]\n"
      "v_mul_f32 v120, v219, v120\n"
      "v_mfma_f32_16x16x32_bf16 v[124:127], v[24:27], v[96:99], v[124:127]\n"
      "s_cbranch_vccnz .Lsp_q2_4\n"
      ".Lbk_q2_4:\n"
      "v_mfma_f32_16x16x32_bf16 v[124:127], v[28:31], v[100:103], v[124:127]\n"
      "buffer_load_dword v15, v2, s[8:11], s45 offen sc1\n"
      "v_mfma_f32_16x16x32_bf16 v[124:127], v[32:35], v[104:107], v[124:127]\n"
      "v_mul_f32 v123, v219, v123\n"
      "v_mul_f32 v122, v219, v122\n"
      "v_mfma_f32_16x16x32_bf16 v[124:127], v[36:39], v[108:111], v[124:127]\n"
      "s_waitcnt vmcnt(4) lgkmcnt(1)\n"
      "ds_write_b32 v11, v9\n"
      "v_mfma_f32_16x16x32_bf16 v[124:127], v[40:43], v[112:115], v[124:127]\n"
      "v_mov_b32 v182, v180\n"
      "v_mfma_f32_16x16x32_bf16 v[124:127], v[44:47], v[116:119], v[124:127]\n"
      "v_cvt_pk_bf16_f32 v173, v122, v123\n"
      "v_mfma_f32_16x16x32_bf16 v[128:131], v[48:51], a[72:75], 0\n"
      "buffer_load_dwordx4 v0, s[8:11], s46 offen offset:1024 sc1 lds\n"
      "v_mfma_f32_16x16x32_bf16 v[128:131], v[52:55], a[76:79], v[128:131]\n"
      "ds_read_b128 v[220:223], v6 offset:0\n"
      "v_cvt_pk_bf16_f32 v172, v120, v121\n"
      "v_mfma_f32_16x16x32_bf16 v[128:131], v[56:59], a[80:83], v[128:131]\n"
      "v_fma_f32 v126, -v218, v142, v126\n"
      "v_fma_f32 v124, -v218, v140, v124\n"
      "v_mfma_f32_16x16x32_bf16 v[128:131], v[60:63], a[84:87], v[128:131]\n"
      "s_add_u32 s52, s52, 24576\n"
      "v_fma_f32 v125, -v218, v141, v125\n"
      "v_mul_f32 v124, v233, v124\n"
      "v_mfma_f32_16x16x32_bf16 v[128:131], v[64:67], a[88:91], v[128:131]\n"
      "v_mul_f32 v126, v219, v126\n"
      "v_mul_f32 v125, v233, v125\n"
      "v_mfma_f32_16x16x32_bf16 v[128:131], v[68:71], a[92:95], v[128:131]\n"
      "v_fma_f32 v127, -v218, v143, v127\n"
      "v_cvt_pk_bf16_f32 v175, v124, v125\n"
      "v_mfma_f32_16x16x32_bf16 v[128:131], v[72:75], a[96:99], v[128:131]\n"
      "v_mul_f32 v127, v219, v127\n"
      "v_cvt_pk_bf16_f32 v174, v126, v127\n"
      "v_mfma_f32_16x16x32_bf16 v[128:131], v[76:79], a[100:103], v[128:131]\n"
      "buffer_load_dwordx4 v0, s[8:11], s46 offen offset:2048 sc1 lds\n"
      "v_mfma_f32_16x16x32_bf16 v[192:195], v[172:175], v[176:179], v[156:159]\n"
      "ds_read_b32 v10, v12\n"
      "v_mfma_f32_16x16x32_bf16 v[196:199], v[172:175], v[164:167], v[168:171]\n"
      "v_mov_b32 v183, v181\n"
      "v_mov_b32 v178, v175\n"
      "v_mfma_f32_16x16x32_bf16 v[128:131], v[80:83], a[104:107], v[128:131]\n"
      "buffer_load_dwordx4 v0, s[8:11], s46 offen offset:0 sc1 lds\n"
      "v_mfma_f32_16x16x32_bf16 v[128:131], v[84:87], a[108:111], v[128:131]\n"
      "v_max_f32 v235, v194, v195\n"
      "v_max_f32 v234, v192, v193\n"
      "v_mfma_f32_16x16x32_bf16 v[128:131], v[88:91], a[112:115], v[128:131]\n"
      "v_max3_f32 v234, v188, v189, v234\n"
      "v_max3_f32 v235, v190, v191, v235\n"
      "v_mfma_f32_16x16x32_bf16 v[128:131], v[92:95], a[116:119], v[128:131]\n"
      "v_max_f32 v236, v234, v235\n"
      "v_mov_b32 v237, v236\n"
      "v_mfma_f32_16x16x32_bf16 v[128:131], v[96:99], a[120:123], v[128:131]\n"
      "v_permlane16_swap_b32 v236, v237\n"
      "v_mfma_f32_16x16x32_bf16 v[128:131], v[100:103], a[124:127], v[128:131]\n"
      "v_max_f32 v236, v236, v237\n"
      "v_mov_b32 v237, v236\n"
      "v_mfma_f32_16x16x32_bf16 v[128:131], v[104:107], a[128:131], v[128:131]\n"
      "s_waitcnt lgkmcnt(0)\n"
      "v_permlane32_swap_b32 v236, v237\n"
      "v_cmp_gt_i32 vcc, v9, v10\n"
      "v_mfma_f32_16x16x32_bf16 v[128:131], v[108:111], a[132:135], v[128:131]\n"
      "buffer_load_dwordx4 v0, s[8:11], s46 offen offset:3072 sc1 lds\n"
      "v_mfma_f32_16x16x32_bf16 v[128:131], v[112:115], a[136:139], v[128:131]\n"
      "s_add_u32 m0, s42, 40960\n"
      "v_max_f32 v236, v236, v237\n"
      "v_mul_f32 v236, v236, v232\n"
      "v_mfma_f32_16x16x32_bf16 v[128:131], v[116:119], a[140:143], v[128:131]\n"
      "s_add_u32 s40, s40, 24576\n"
      "v_fma_f32 v234, v234, v232, -v236\n"
      "v_fma_f32 v235, v235, v232, -v236\n"
      "v_mfma_f32_16x16x32_bf16 v[132:135], a[144:147], v[48:51], 0\n"
      "v_exp_f32 v234, v234\n"
      "v_mfma_f32_16x16x32_bf16 v[132:135], a[148:151], v[52:55], v[132:135]\n"
      "s_cbranch_vccnz .Lsp_qy1_5\n"
      ".Lbk_qy1_5:\n"
      "ds_read_b128 v[48:51], v0 offset:20480\n"
      "v_mfma_f32_16x16x32_bf16 v[132:135], a[152:155], v[56:59], v[132:135]\n"
      "v_exp_f32 v235, v235\n"
      "v_mfma_f32_16x16x32_bf16 v[132:135], a[156:159], v[60:63], v[132:135]\n"
      "ds_read_b128 v[56:59], v0 offset:22528\n"
      "v_fma_f32 v130, -v224, v147, v130\n"
      "v_mfma_f32_16x16x32_bf16 v[132:135], a[160:163], v[64:67], v[132:135]\n"
      "ds_read_b64 v[216:217], v4 offset:21504\n"
      "v_fma_f32 v129, -v222, v146, v129\n"
      "v_mfma_f32_16x16x32_bf16 v[132:135], a[164:167], v[68:71], v[132:135]\n"
      "buffer_load_dwordx4 v1, s[8:11], s46 offen offset:0 sc1 lds\n"
      "v_mfma_f32_16x16x32_bf16 v[132:135], a[168:171], v[72:75], v[132:135]\n"
      "v_cvt_pk_bf16_f32 v234, v234, v234\n"
      "v_fma_f32 v131, -v226, v144, v131\n"
      "v_mfma_f32_16x16x32_bf16 v[132:135], a[172:175], v[76:79], v[132:135]\n"
      "ds_read_b128 v[60:63], v0 offset:23552\n"
      "v_and_b32 v200, v160, v234\n"
      "v_mfma_f32_16x16x32_bf16 v[132:135], a[176:179], v[80:83], v[132:135]\n"
      "v_cvt_pk_bf16_f32 v235, v235, v235\n"
      "v_mul_f32 v129, v223, v129\n"
      "v_mfma_f32_16x16x32_bf16 v[132:135], a[180:183], v[84:87], v[132:135]\n"
      "ds_read_b128 v[52:55], v0 offset:21504\n"
      "v_mul_f32 v131, v227, v131\n"
      "v_mfma_f32_16x16x32_bf16 v[132:135], a[184:187], v[88:91], v[132:135]\n"
      "ds_read_b128 v[88:91], v0 offset:30720\n"
      "v_mul_f32 v130, v225, v130\n"
      "v_cvt_pk_bf16_f32 v185, v198, v199\n"
      "v_mfma_f32_16x16x32_bf16 v[132:135], a[188:191], v[92:95], v[132:135]\n"
      "v_cvt_pk_bf16_f32 v184, v196, v197\n"
      "v_and_b32 v201, v161, v235\n"
      "v_fma_f32 v128, -v220, v145, v128\n"
      "v_mfma_f32_16x16x32_bf16 v[132:135], a[192:195], v[96:99], v[132:135]\n"
      "ds_read_b128 v[68:71], v0 offset:25600\n"
      "v_and_b32 v203, v163, v235\n"
      "v_mfma_f32_16x16x32_bf16 v[132:135], a[196:199], v[100:103], v[132:135]\n"
      "v_mul_f32 v128, v221, v128\n"
      "v_and_b32 v202, v162, v234\n"
      "v_mfma_f32_16x16x32_bf16 v[132:135], a[200:203], v[104:107], v[132:135]\n"
      "v_cvt_pk_bf16_f32 v180, v128, v129\n"
      "v_cvt_pk_bf16_f32 v181, v130, v131\n"
      "v_mfma_f32_16x16x32_bf16 v[208:211], v[184:187], v[200:203], 0\n"
      "ds_read_b128 v[64:67], v0 offset:24576\n"
      "ds_read_b128 v[72:75], v0 offset:26624\n"
      "v_mfma_f32_16x16x32_bf16 v[204:207], v[180:183], v[200:203], 0\n"
      "s_waitcnt vmcnt(6) lgkmcnt(4)\n"
      "buffer_store_dword v13, v3, s[8:11], s52 offen sc1\n"
      "v_mfma_f32_16x16x32_bf16 v[132:135], a[204:207], v[108:111], v[132:135]\n"
      "ds_read2st64_b64 v[212:215], v4 offset0:40 offset1:41\n"
      "v_mfma_f32_16x16x32_bf16 v[132:135], a[208:211], v[112:115], v[132:135]\n"
      "v_rcp_f32 v210, v210\n"
      "v_mfma_f32_16x16x32_bf16 v[132:135], a[212:215], v[116:119], v[132:135]\n"
      "ds_read_b128 v[76:79], v0 offset:27648\n"
      "v_mul_f32 v209, v209, v210\n"
      "v_mfma_f32_16x16x32_bf16 v[120:123], a[0:3], v[48:51], 0\n"
      "ds_read_b128 v[80:83], v0 offset:28672\n"
      "ds_read_b128 v[100:103], v0 offset:33792\n"
      "v_mfma_f32_16x16x32_bf16 v[120:123], a[4:7], v[52:55], v[120:123]\n"
      "ds_read_b128 v[92:95], v0 offset:31744\n"
      "v_mul_f32 v208, v208, v210\n"
      "v_mfma_f32_16x16x32_bf16 v[120:123], a[8:11], v[56:59], v[120:123]\n"
      "ds_read_b128 v[84:87], v0 offset:29696\n"
      "v_fma_f32 v134, -v218, v150, v134\n"
      "v_mfma_f32_16x16x32_bf16 v[120:123], a[12:15], v[60:63], v[120:123]\n"
      "s_waitcnt lgkmcnt(6)\n"
      "ds_read_b128 v[104:107], v0 offset:34816\n"
      "v_fma_f32 v132, -v218, v148, v132\n"
      "v_mfma_f32_16x16x32_bf16 v[120:123], a[16:19], v[64:67], v[120:123]\n"
      "s_mov_b32 s50, s49\n"
      "ds_read_b128 v[96:99], v0 offset:32768\n"
      "v_mfma_f32_16x16x32_bf16 v[120:123], a[20:23], v[68:71], v[120:123]\n"
      "ds_read_b128 v[108:111], v0 offset:35840\n"
      "v_fma_f32 v133, -v218, v149, v133\n"
      "v_mfma_f32_16x16x32_bf16 v[120:123], a[24:27], v[72:75], v[120:123]\n"
      "s_waitcnt lgkmcnt(7)\n"
      "v_fma_f32 v135, -v218, v151, v135\n"
      "v_cvt_pk_bf16_f32 v177, v134, v135\n"
      "v_mfma_f32_16x16x32_bf16 v[120:123], a[28:31], v[76:79], v[120:123]\n"
      "s_waitcnt lgkmcnt(5)\n"
      "ds_read_b128 v[112:115], v0 offset:36864\n"
      "v_cvt_pk_bf16_f32 v176, v132, v133\n"
      "v_mfma_f32_16x16x32_bf16 v[120:123], a[32:35], v[80:83], v[120:123]\n"
      "v_mov_b32 v187, v185\n"
      "v_mul_f32 v232, 0x3eae1a93, v219\n"
      "v_mfma_f32_16x16x32_bf16 v[188:191], v[172:175], v[176:179], v[152:155]\n"
      "s_waitcnt lgkmcnt(4)\n"
      "ds_read_b128 v[116:119], v0 offset:37888\n"
      "v_mul_f32 v207, v207, v210\n"
      "v_mfma_f32_16x16x32_bf16 v[120:123], a[36:39], v[84:87], v[120:123]\n"
      "v_mul_f32 v204, v204, v210\n"
      "v_mfma_f32_16x16x32_bf16 v[120:123], a[40:43], v[88:91], v[120:123]\n"
      "v_cvt_pk_bf16_f32 v230, v208, v209\n"
      "v_add_f32 v213, v213, v215\n"
      "v_mfma_f32_16x16x32_bf16 v[120:123], a[44:47], v[92:95], v[120:123]\n"
      "s_waitcnt lgkmcnt(2)\n"
      "v_mul_f32 v205, v205, v210\n"
      "v_mfma_f32_16x16x32_bf16 v[120:123], a[48:51], v[96:99], v[120:123]\n"
      "buffer_store_dwordx2 v[230:231], v8, s[8:11], s40 offen sc1\n"
      "v_mfma_f32_16x16x32_bf16 v[120:123], a[52:55], v[100:103], v[120:123]\n"
      "v_add_f32 v212, v212, v214\n"
      "v_mfma_f32_16x16x32_bf16 v[120:123], a[56:59], v[104:107], v[120:123]\n"
      "v_cvt_pk_bf16_f32 v228, v204, v205\n"
      "v_mul_f32 v206, v206, v210\n"
      "v_mfma_f32_16x16x32_bf16 v[120:123], a[60:63], v[108:111], v[120:123]\n"
      "s_waitcnt vmcnt(7) lgkmcnt(0)\n"
      "v_cvt_pk_bf16_f32 v229, v206, v207\n"
      "v_cmp_ne_u32 vcc, s21, v15\n"
      "v_mfma_f32_16x16x32_bf16 v[120:123], a[64:67], v[112:115], v[120:123]\n"
      "s_add_u32 m0, s41, 0\n"
      "v_mov_b32 v182, v180\n"
      "v_mfma_f32_16x16x32_bf16 v[120:123], a[68:71], v[116:119], v[120:123]\n"
      "buffer_store_dwordx2 v[228:229], v7, s[8:11], s40 offen sc1\n"
      "v_mfma_f32_16x16x32_bf16 v[124:127], a[216:219], v[48:51], 0\n"
      "v_add_f32 v218, v212, v216\n"
      "v_add_f32_e64 v213, v213, v217\n"
      "v_mfma_f32_16x16x32_bf16 v[124:127], a[220:223], v[52:55], v[124:127]\n"
      "s_add_u32 s36, s36, 24576\n"
      "v_fma_f32 v213, -v218, v218, v213\n"
      "v_mfma_f32_16x16x32_bf16 v[124:127], a[224:227], v[56:59], v[124:127]\n"
      "s_mov_b32 s46, s45\n"
      "s_cbranch_vccnz .Lsp_q0_6\n"
      ".Lbk_q0_6:\n"
      "v_mfma_f32_16x16x32_bf16 v[124:127], a[228:231], v[60:63], v[124:127]\n"
      "buffer_load_dwordx4 v0, s[8:11], s46 offen offset:1024 sc1 lds\n"
      "v_mfma_f32_16x16x32_bf16 v[124:127], a[232:235], v[64:67], v[124:127]\n"
      "v_max_f32 v213, 0x3727c5ac, v213\n"
      "v_fma_f32 v120, -v218, v136, v120\n"
      "v_mfma_f32_16x16x32_bf16 v[124:127], a[236:239], v[68:71], v[124:127]\n"
      "v_mov_b32 v186, v184\n"
      "v_mfma_f32_16x16x32_bf16 v[124:127], a[240:243], v[72:75], v[124:127]\n"
      "v_add_u32 v9, 1, v9\n"
      "v_mfma_f32_16x16x32_bf16 v[124:127], a[244:247], v[76:79], v[124:127]\n"
      "s_add_u32 s40, s40, 24576\n"
      "v_fma_f32 v123, -v218, v139, v123\n"
      "v_mfma_f32_16x16x32_bf16 v[124:127], a[248:251], v[80:83], v[124:127]\n"
      "v_rsq_f32 v219, v213\n"
      "v_mfma_f32_16x16x32_bf16 v[124:127], a[252:255], v[84:87], v[124:127]\n"
      "v_mfma_f32_16x16x32_bf16 v[124:127], v[16:19], v[88:91], v[124:127]\n"
      "v_fma_f32 v121, -v218, v137, v121\n"
      "v_fma_f32 v122, -v218, v138, v122\n"
      "v_mfma_f32_16x16x32_bf16 v[124:127], v[20:23], v[92:95], v[124:127]\n"
      "buffer_load_dwordx4 v0, s[8:11], s46 offen offset:0 sc1 lds\n"
      "v_mfma_f32_16x16x32_bf16 v[124:127], v[24:27], v[96:99], v[124:127]\n"
      "v_mul_f32 v120, v219, v120\n"
      "v_mul_f32 v122, v219, v122\n"
      "v_mfma_f32_16x16x32_bf16 v[124:127], v[28:31], v[100:103], v[124:127]\n"
      "ds_write_b64 v5, v[218:219] offset:512\n"
      "v_mfma_f32_16x16x32_bf16 v[124:127], v[32:35], v[104:107], v[124:127]\n"
      "v_mul_f32 v121, v219, v121\n"
      "v_mul_f32 v123, v219, v123\n"
      "v_mfma_f32_16x16x32_bf16 v[124:127], v[36:39], v[108:111], v[124:127]\n"
      "buffer_load_dwordx4 v0, s[8:11], s46 offen offset:2048 sc1 lds\n"
      "v_mfma_f32_16x16x32_bf16 v[124:127], v[40:43], v[112:115], v[124:127]\n"
      "s_mov_b32 s45, s36\n"
      "ds_read_b128 v[224:227], v6 offset:528\n"
      "v_mfma_f32_16x16x32_bf16 v[124:127], v[44:47], v[116:119], v[124:127]\n"
      "s_min_u32 s45, s45, s6\n"
      "v_cndmask_b32 v233, v219, 1.0, s[54:55]\n"
      "v_mfma_f32_16x16x32_bf16 v[128:131], v[48:51], a[72:75], 0\n"
      "s_add_u32 s52, s52, 24576\n"
      "v_cvt_pk_bf16_f32 v172, v120, v121\n"
      "v_cvt_pk_bf16_f32 v173, v122, v123\n"
      "v_mfma_f32_16x16x32_bf16 v[128:131], v[52:55], a[76:79], v[128:131]\n"
      "buffer_load_dwordx4 v0, s[8:11], s46 offen offset:3072 sc1 lds\n"
      "v_mfma_f32_16x16x32_bf16 v[128:131], v[56:59], a[80:83], v[128:131]\n"
      "v_fma_f32 v126, -v218, v142, v126\n"
      "v_fma_f32 v124, -v218, v140, v124\n"
      "v_mul_f32 v126, v219, v126\n"
      "v_mfma_f32_16x16x32_bf16 v[128:131], v[60:63], a[84:87], v[128:131]\n"
      "v_fma_f32 v125, -v218, v141, v125\n"
      "v_mul_f32 v125, v233, v125\n"
      "v_mfma_f32_16x16x32_bf16 v[128:131], v[64:67], a[88:91], v[128:131]\n"
      "s_waitcnt vmcnt(7) lgkmcnt(1)\n"
      "ds_write_b32 v11, v9\n"
      "v_mfma_f32_16x16x32_bf16 v[128:131], v[68:71], a[92:95], v[128:131]\n"
      "v_fma_f32 v127, -v218, v143, v127\n"
      "v_mul_f32 v124, v233, v124\n"
      "v_mul_f32 v127, v219, v127\n"
      "v_mfma_f32_16x16x32_bf16 v[128:131], v[72:75], a[96:99], v[128:131]\n"
      "v_cvt_pk_bf16_f32 v174, v126, v127\n"
      "v_cvt_pk_bf16_f32 v175, v124, v125\n"
      "v_mfma_f32_16x16x32_bf16 v[128:131], v[76:79], a[100:103], v[128:131]\n"
      "buffer_load_dword v15, v2, s[8:11], s45 offen sc1\n"
      "v_mfma_f32_16x16x32_bf16 v[192:195], v[172:175], v[176:179], v[156:159]\n"
      "v_mov_b32 v183, v181\n"
      "v_mfma_f32_16x16x32_bf16 v[196:199], v[172:175], v[164:167], v[168:171]\n"
      "ds_read_b128 v[220:223], v6 offset:512\n"
      "s_add_u32 m0, s42, 0\n"
      "v_mfma_f32_16x16x32_bf16 v[128:131], v[80:83], a[104:107], v[128:131]\n"
      "buffer_load_dwordx4 v1, s[8:11], s46 offen offset:0 sc1 lds\n"
      "v_mfma_f32_16x16x32_bf16 v[128:131], v[84:87], a[108:111], v[128:131]\n"
      "v_max_f32 v235, v194, v195\n"
      "v_max3_f32 v235, v190, v191, v235\n"
      "v_mfma_f32_16x16x32_bf16 v[128:131], v[88:91], a[112:115], v[128:131]\n"
      "v_max_f32 v234, v192, v193\n"
      "v_max3_f32 v234, v188, v189, v234\n"
      "v_mfma_f32_16x16x32_bf16 v[128:131], v[92:95], a[116:119], v[128:131]\n"
      "ds_read_b32 v10, v12\n"
      "v_max_f32 v236, v234, v235\n"
      "v_mfma_f32_16x16x32_bf16 v[128:131], v[96:99], a[120:123], v[128:131]\n"
      "v_mov_b32 v237, v236\n"
      "v_cvt_pk_bf16_f32 v185, v198, v199\n"
      "v_mfma_f32_16x16x32_bf16 v[128:131], v[100:103], a[124:127], v[128:131]\n"
      "v_permlane16_swap_b32 v236, v237\n"
      "v_mfma_f32_16x16x32_bf16 v[128:131], v[104:107], a[128:131], v[128:131]\n"
      "v_max_f32 v236, v236, v237\n"
      "v_mov_b32 v237, v236\n"
      "v_mfma_f32_16x16x32_bf16 v[128:131], v[108:111], a[132:135], v[128:131]\n"
      "v_permlane32_swap_b32 v236, v237\n"
      "v_mfma_f32_16x16x32_bf16 v[128:131], v[112:115], a[136:139], v[128:131]\n"
      "s_waitcnt lgkmcnt(0)\n"
      "v_cmp_gt_i32 vcc, v9, v10\n"
      "v_max_f32 v236, v236, v237\n"
      "v_mfma_f32_16x16x32_bf16 v[128:131], v[116:119], a[140:143], v[128:131]\n"
      "v_mul_f32_e64 v236, v236, v232\n"
      "v_fma_f32 v235, v235, v232, -v236\n"
      "v_mfma_f32_16x16x32_bf16 v[132:135], a[144:147], v[48:51], 0\n"
      "v_exp_f32_e64 v235, v235\n"
      "v_mfma_f32_16x16x32_bf16 v[132:135], a[148:151], v[52:55], v[132:135]\n"
      "s_cbranch_vccnz .Lsp_qy2_7\n"
      ".Lbk_qy2_7:\n"
      "v_cvt_pk_bf16_f32 v235, v235, v235\n"
      "v_mfma_f32_16x16x32_bf16 v[132:135], a[152:155], v[56:59], v[132:135]\n"
      "v_fma_f32 v130, -v224, v147, v130\n"
      "v_mul_f32 v130, v225, v130\n"
      "v_mfma_f32_16x16x32_bf16 v[132:135], a[156:159], v[60:63], v[132:135]\n"
      "v_fma_f32 v129, -v222, v146, v129\n"
      "v_cvt_pk_bf16_f32 v184, v196, v197\n"
      "v_mfma_f32_16x16x32_bf16 v[132:135], a[160:163], v[64:67], v[132:135]\n"
      "v_fma_f32 v234, v234, v232, -v236\n"
      "v_fma_f32 v131, -v226, v144, v131\n"
      "v_mfma_f32_16x16x32_bf16 v[132:135], a[164:167], v[68:71], v[132:135]\n"
      "ds_read_b128 v[52:55], v0 offset:41984\n"
      "ds_read_b128 v[64:67], v0 offset:45056\n"
      "v_mfma_f32_16x16x32_bf16 v[132:135], a[168:171], v[72:75], v[132:135]\n"
      "ds_read_b128 v[56:59], v0 offset:43008\n"
      "v_mul_f32 v129, v223, v129\n"
      "v_mfma_f32_16x16x32_bf16 v[132:135], a[172:175], v[76:79], v[132:135]\n"
      "v_exp_f32 v234, v234\n"
      "v_mfma_f32_16x16x32_bf16 v[132:135], a[176:179], v[80:83], v[132:135]\n"
      "v_fma_f32 v128, -v220, v145, v128\n"
      "v_and_b32 v203, v163, v235\n"
      "v_mfma_f32_16x16x32_bf16 v[132:135], a[180:183], v[84:87], v[132:135]\n"
      "v_cvt_pk_bf16_f32 v234, v234, v234\n"
      "v_and_b32 v201, v161, v235\n"
      "v_mfma_f32_16x16x32_bf16 v[132:135], a[184:187], v[88:91], v[132:135]\n"
      "ds_read_b128 v[48:51], v0 offset:40960\n"
      "v_and_b32 v202, v162, v234\n"
      "v_mfma_f32_16x16x32_bf16 v[132:135], a[188:191], v[92:95], v[132:135]\n"
      "ds_read_b128 v[60:63], v0 offset:44032\n"
      "v_mul_f32 v128, v221, v128\n"
      "v_mfma_f32_16x16x32_bf16 v[132:135], a[192:195], v[96:99], v[132:135]\n"
      "ds_read_b128 v[76:79], v0 offset:48128\n"
      "v_cvt_pk_bf16_f32 v180, v128, v129\n"
      "v_mfma_f32_16x16x32_bf16 v[132:135], a[196:199], v[100:103], v[132:135]\n"
      "ds_read_b128 v[100:103], v0 offset:54272\n"
      "v_and_b32 v200, v160, v234\n"
      "v_mfma_f32_16x16x32_bf16 v[132:135], a[200:203], v[104:107], v[132:135]\n"
      "v_mul_f32 v131, v227, v131\n"
      "v_cvt_pk_bf16_f32 v181, v130, v131\n"
      "v_mfma_f32_16x16x32_bf16 v[208:211], v[184:187], v[200:203], 0\n"
      "s_waitcnt vmcnt(6) lgkmcnt(4)\n"
      "buffer_store_dword v13, v3, s[8:11], s52 offen sc1\n"
      "v_mfma_f32_16x16x32_bf16 v[204:207], v[180:183], v[200:203], 0\n"
      "ds_read_b128 v[84:87], v0 offset:50176\n"
      "ds_read_b128 v[72:75], v0 offset:47104\n"
      "v_mfma_f32_16x16x32_bf16 v[132:135], a[204:207], v[108:111], v[132:135]\n"
      "ds_read_b128 v[68:71], v0 offset:46080\n"
      "v_mov_b32 v178, v175\n"
      "v_mfma_f32_16x16x32_bf16 v[132:135], a[208:211], v[112:115], v[132:135]\n"
      "v_rcp_f32 v210, v210\n"
      "v_mfma_f32_16x16x32_bf16 v[132:135], a[212:215], v[116:119], v[132:135]\n"
      "s_waitcnt lgkmcnt(4)\n"
      "ds_read_b128 v[92:95], v0 offset:52224\n"
      "v_mul_f32 v205, v205, v210\n"
      "v_mfma_f32_16x16x32_bf16 v[120:123], a[0:3], v[48:51], 0\n"
      "s_add_u32 s36, s36, 24576\n"
      "ds_read_b128 v[80:83], v0 offset:49152\n"
      "v_mov_b32 v182, v180\n"
      "v_mfma_f32_16x16x32_bf16 v[120:123], a[4:7], v[52:55], v[120:123]\n"
      "ds_read_b128 v[96:99], v0 offset:53248\n"
      "v_mfma_f32_16x16x32_bf16 v[120:123], a[8:11], v[56:59], v[120:123]\n"
      "ds_read_b128 v[88:91], v0 offset:51200\n"
      "v_fma_f32 v132, -v218, v148, v132\n"
      "v_mfma_f32_16x16x32_bf16 v[120:123], a[12:15], v[60:63], v[120:123]\n"
      "s_cmp_eq_u32 s36, s37\n"
      "v_fma_f32 v133, -v218, v149, v133\n"
      "v_cvt_pk_bf16_f32 v176, v132, v133\n"
      "v_mfma_f32_16x16x32_bf16 v[120:123], a[16:19], v[64:67], v[120:123]\n"
      "s_waitcnt lgkmcnt(4)\n"
      "ds_read2st64_b64 v[212:215], v4 offset0:80 offset1:81\n"
      "s_cselect_b32 s47, s38, 0\n"
      "v_mfma_f32_16x16x32_bf16 v[120:123], a[20:23], v[68:71], v[120:123]\n"
      "s_cselect_b32 s37, s48, s37\n"
      "ds_read_b128 v[112:115], v0 offset:57344\n"
      "v_fma_f32 v135, -v218, v151, v135\n"
      "v_mfma_f32_16x16x32_bf16 v[120:123], a[24:27], v[72:75], v[120:123]\n"
      "s_cselect_b32 s49, s39, s53\n"
      "v_fma_f32 v134, -v218, v150, v134\n"
      "v_mov_b32 v183, v181\n"
      "v_mfma_f32_16x16x32_bf16 v[120:123], a[28:31], v[76:79], v[120:123]\n"
      "s_waitcnt lgkmcnt(4)\n"
      "ds_read_b128 v[104:107], v0 offset:55296\n"
      "v_cvt_pk_bf16_f32 v177, v134, v135\n"
      "v_mfma_f32_16x16x32_bf16 v[120:123], a[32:35], v[80:83], v[120:123]\n"
      "ds_read_b128 v[108:111], v0 offset:56320\n"
      "v_mul_f32 v206, v206, v210\n"
      "v_mfma_f32_16x16x32_bf16 v[188:191], v[172:175], v[176:179], v[152:155]\n"
      "ds_read_b64 v[216:217], v4 offset:41984\n"
      "v_mul_f32 v207, v207, v210\n"
      "v_mfma_f32_16x16x32_bf16 v[120:123], a[36:39], v[84:87], v[120:123]\n"
      "s_waitcnt lgkmcnt(5)\n"
      "s_add_u32 m0, s41, 20480\n"
      "ds_read_b128 v[116:119], v0 offset:58368\n"
      "v_mfma_f32_16x16x32_bf16 v[120:123], a[40:43], v[88:91], v[120:123]\n"
      "s_mov_b32 s46, s45\n"
      "s_add_u32 s45, s36, s47\n"
      "v_mul_f32 v209, v209, v210\n"
      "v_mfma_f32_16x16x32_bf16 v[120:123], a[44:47], v[92:95], v[120:123]\n"
      "v_cvt_pk_bf16_f32 v229, v206, v207\n"
      "v_mfma_f32_16x16x32_bf16 v[120:123], a[48:51], v[96:99], v[120:123]\n"
      "s_waitcnt lgkmcnt(4)\n"
      "v_add_f32 v213, v213, v215\n"
      "v_mfma_f32_16x16x32_bf16 v[120:123], a[52:55], v[100:103], v[120:123]\n"
      "s_waitcnt lgkmcnt(3)\n"
      "v_mul_f32 v232, 0x3eae1a93, v219\n"
      "v_mul_f32 v204, v204, v210\n"
      "v_mfma_f32_16x16x32_bf16 v[120:123], a[56:59], v[104:107], v[120:123]\n"
      "s_waitcnt lgkmcnt(2)\n"
      "v_mul_f32 v208, v208, v210\n"
      "v_cvt_pk_bf16_f32 v230, v208, v209\n"
      "v_mfma_f32_16x16x32_bf16 v[120:123], a[60:63], v[108:111], v[120:123]\n"
      "v_mfma_f32_16x16x32_bf16 v[120:123], a[64:67], v[112:115], v[120:123]\n"
      "s_waitcnt lgkmcnt(0)\n"
      "buffer_store_dwordx2 v[230:231], v8, s[8:11], s40 offen sc1\n"
      "v_mfma_f32_16x16x32_bf16 v[120:123], a[68:71], v[116:119], v[120:123]\n"
      "v_cvt_pk_bf16_f32 v228, v204, v205\n"
      "v_mfma_f32_16x16x32_bf16 v[124:127], a[216:219], v[48:51], 0\n"
      "v_add_f32 v212, v212, v214\n"
      "v_add_f32 v213, v213, v217\n"
      "v_mfma_f32_16x16x32_bf16 v[124:127], a[220:223], v[52:55], v[124:127]\n"
      "v_add_f32 v218, v212, v216\n"
      "v_fma_f32 v213, -v218, v218, v213\n"
      "v_mfma_f32_16x16x32_bf16 v[124:127], a[224:227], v[56:59], v[124:127]\n"
      "buffer_store_dwordx2 v[228:229], v7, s[8:11], s40 offen sc1\n"
      "v_mfma_f32_16x16x32_bf16 v[124:127], a[228:231], v[60:63], v[124:127]\n"
      "s_add_u32 s52, s52, 24576\n"
      "v_fma_f32 v123, -v218, v139, v123\n"
      "v_add_u32 v9, 1, v9\n"
      "v_mfma_f32_16x16x32_bf16 v[124:127], a[232:235], v[64:67], v[124:127]\n"
      "v_fma_f32 v122, -v218, v138, v122\n"
      "v_mfma_f32_16x16x32_bf16 v[124:127], a[236:239], v[68:71], v[124:127]\n"
      "s_waitcnt vmcnt(4)\n"
      "v_max_f32 v213, 0x3727c5ac, v213\n"
      "v_cmp_ne_u32 vcc, s21, v15\n"
      "v_mfma_f32_16x16x32_bf16 v[124:127], a[240:243], v[72:75], v[124:127]\n"
      "v_rsq_f32 v219, v213\n"
      "v_mfma_f32_16x16x32_bf16 v[124:127], a[244:247], v[76:79], v[124:127]\n"
      "v_mov_b32 v186, v184\n"
      "v_fma_f32 v120, -v218, v136, v120\n"
      "v_mfma_f32_16x16x32_bf16 v[124:127], a[248:251], v[80:83], v[124:127]\n"
      "s_min_u32 s45, s45, s6\n"
      "ds_write_b64 v5, v[218:219] offset:1024\n"
      "v_mfma_f32_16x16x32_bf16 v[124:127], a[252:255], v[84:87], v[124:127]\n"
      "s_cbranch_vccnz .Lsp_q1_8\n"
      ".Lbk_q1_8:\n"
      "v_mfma_f32_16x16x32_bf16 v[124:127], v[16:19], v[88:91], v[124:127]\n"
      "buffer_load_dwordx4 v0, s[8:11], s46 offen offset:1024 sc1 lds\n"
      "v_mfma_f32_16x16x32_bf16 v[124:127], v[20:23], v[92:95], v[124:127]\n"
      "v_cndmask_b32 v233, v219, 1.0, s[54:55]\n"
      "v_mul_f32 v123, v219, v123\n"
      "v_mfma_f32_16x16x32_bf16 v[124:127], v[24:27], v[96:99], v[124:127]\n"
      "ds_read_b128 v[220:223], v6 offset:1024\n"
      "v_mul_f32 v120, v219, v120\n"
      "v_mfma_f32_16x16x32_bf16 v[124:127], v[28:31], v[100:103], v[124:127]\n"
      "v_mul_f32 v122, v219, v122\n"
      "v_fma_f32 v121, -v218, v137, v121\n"
      "v_mfma_f32_16x16x32_bf16 v[124:127], v[32:35], v[104:107], v[124:127]\n"
      "s_waitcnt vmcnt(4)\n"
      "ds_write_b32 v11, v9\n"
      "v_mfma_f32_16x16x32_bf16 v[124:127], v[36:39], v[108:111], v[124:127]\n"
      "buffer_load_dwordx4 v0, s[8:11], s46 offen offset:0 sc1 lds\n"
      "v_mfma_f32_16x16x32_bf16 v[124:127], v[40:43], v[112:115], v[124:127]\n"
      "v_mfma_f32_16x16x32_bf16 v[124:127], v[44:47], v[116:119], v[124:127]\n"
      "v_cvt_pk_bf16_f32 v173, v122, v123\n"
      "v_mfma_f32_16x16x32_bf16 v[128:131], v[48:51], a[72:75], 0\n"
      "v_mul_f32 v121, v219, v121\n"
      "v_cvt_pk_bf16_f32 v172, v120, v121\n"
      "v_mfma_f32_16x16x32_bf16 v[128:131], v[52:55], a[76:79], v[128:131]\n"
      "buffer_load_dwordx4 v0, s[8:11], s46 offen offset:3072 sc1 lds\n"
      "v_mfma_f32_16x16x32_bf16 v[128:131], v[56:59], a[80:83], v[128:131]\n"
      "v_fma_f32 v125, -v218, v141, v125\n"
      "v_fma_f32 v124, -v218, v140, v124\n"
      "v_mfma_f32_16x16x32_bf16 v[128:131], v[60:63], a[84:87], v[128:131]\n"
      "v_fma_f32 v127, -v218, v143, v127\n"
      "v_mul_f32 v125, v233, v125\n"
      "v_mfma_f32_16x16x32_bf16 v[128:131], v[64:67], a[88:91], v[128:131]\n"
      "v_mul_f32 v124, v233, v124\n"
      "v_mul_f32 v127, v219, v127\n"
      "v_mfma_f32_16x16x32_bf16 v[128:131], v[68:71], a[92:95], v[128:131]\n"
      "v_fma_f32 v126, -v218, v142, v126\n"
      "v_mul_f32 v126, v219, v126\n"
      "v_mfma_f32_16x16x32_bf16 v[128:131], v[72:75], a[96:99], v[128:131]\n"
      "v_cvt_pk_bf16_f32 v175, v124, v125\n"
      "v_cvt_pk_bf16_f32 v174, v126, v127\n"
      "v_mfma_f32_16x16x32_bf16 v[128:131], v[76:79], a[100:103], v[128:131]\n"
      "buffer_load_dword v15, v2, s[8:11], s45 offen sc1\n"
      "v_mfma_f32_16x16x32_bf16 v[192:195], v[172:175], v[176:179], v[156:159]\n"
      "ds_read_b128 v[224:227], v6 offset:1040\n"
      "v_mov_b32 v187, v185\n"
      "v_mfma_f32_16x16x32_bf16 v[196:199], v[172:175], v[164:167], v[168:171]\n"
      "ds_read_b32 v10, v12\n"
      "v_mfma_f32_16x16x32_bf16 v[128:131], v[80:83], a[104:107], v[128:131]\n"
      "buffer_load_dwordx4 v0, s[8:11], s46 offen offset:2048 sc1 lds\n"
      "v_mfma_f32_16x16x32_bf16 v[128:131], v[84:87], a[108:111], v[128:131]\n"
      "v_med3_f32 v234, v192, v193, s50\n"
      "v_med3_f32 v235, v194, v195, s50\n"
      "v_mfma_f32_16x16x32_bf16 v[128:131], v[88:91], a[112:115], v[128:131]\n"
      "v_cvt_pk_bf16_f32 v185, v198, v199\n"
      "v_max3_f32 v234, v188, v189, v234\n"
      "v_mfma_f32_16x16x32_bf16 v[128:131], v[92:95], a[116:119], v[128:131]\n"
      "v_max3_f32 v235, v190, v191, v235\n"
      "v_max_f32 v236, v234, v235\n"
      "v_mfma_f32_16x16x32_bf16 v[128:131], v[96:99], a[120:123], v[128:131]\n"
      "s_waitcnt lgkmcnt(0)\n"
      "v_mov_b32 v237, v236\n"
      "v_cmp_gt_i32 vcc, v9, v10\n"
      "v_mfma_f32_16x16x32_bf16 v[128:131], v[100:103], a[124:127], v[128:131]\n"
      "s_add_u32 m0, s42, 20480\n"
      "v_permlane16_swap_b32 v236, v237\n"
      "v_mfma_f32_16x16x32_bf16 v[128:131], v[104:107], a[128:131], v[128:131]\n"
      "v_max_f32 v236, v236, v237\n"
      "v_mov_b32 v237, v236\n"
      "v_mfma_f32_16x16x32_bf16 v[128:131], v[108:111], a[132:135], v[128:131]\n"
      "buffer_load_dwordx4 v1, s[8:11], s46 offen offset:0 sc1 lds\n"
      "v_mfma_f32_16x16x32_bf16 v[128:131], v[112:115], a[136:139], v[128:131]\n"
      ".Lq_p1:\n"
      "s_cbranch_execz .Lq_pr\n"
      "v_permlane32_swap_b32 v236, v237\n"
      "v_mfma_f32_16x16x32_bf16 v[128:131], v[116:119], a[140:143], v[128:131]\n"
      "v_max_f32 v236, v236, v237\n"
      "v_mul_f32_e64 v236, v236, v232\n"
      "v_mfma_f32_16x16x32_bf16 v[132:135], a[144:147], v[48:51], 0\n"
      "s_cbranch_vccnz .Lsp_qy0_9\n"
      ".Lbk_qy0_9:\n"
      "v_fma_f32 v234, v234, v232, -v236\n"
      "v_mfma_f32_16x16x32_bf16 v[132:135], a[148:151], v[52:55], v[132:135]\n"
      "ds_read_b128 v[52:55], v0 offset:1024\n"
      "ds_read_b128 v[48:51], v0 offset:0\n"
      "v_mfma_f32_16x16x32_bf16 v[132:135], a[152:155], v[56:59], v[132:135]\n"
      "v_fma_f32 v131, -v226, v144, v131\n"
      "v_fma_f32 v235, v235, v232, -v236\n"
      "v_mfma_f32_16x16x32_bf16 v[132:135], a[156:159], v[60:63], v[132:135]\n"
      "ds_read_b128 v[56:59], v0 offset:2048\n"
      "v_fma_f32 v128, -v220, v145, v128\n"
      "v_mfma_f32_16x16x32_bf16 v[132:135], a[160:163], v[64:67], v[132:135]\n"
      "v_exp_f32 v234, v234\n"
      "v_mfma_f32_16x16x32_bf16 v[132:135], a[164:167], v[68:71], v[132:135]\n"
      "v_exp_f32 v235, v235\n"
      "v_mfma_f32_16x16x32_bf16 v[132:135], a[168:171], v[72:75], v[132:135]\n"
      "v_cvt_pk_bf16_f32 v234, v234, v234\n"
      "v_fma_f32 v129, -v222, v146, v129\n"
      "v_mfma_f32_16x16x32_bf16 v[132:135], a[172:175], v[76:79], v[132:135]\n"
      "v_mul_f32 v129, v223, v129\n"
      "v_fma_f32 v130, -v224, v147, v130\n"
      "v_mfma_f32_16x16x32_bf16 v[132:135], a[176:179], v[80:83], v[132:135]\n"
      "v_cvt_pk_bf16_f32 v235, v235, v235\n"
      "v_and_b32 v200, v160, v234\n"
      "v_mfma_f32_16x16x32_bf16 v[132:135], a[180:183], v[84:87], v[132:135]\n"
      "v_cvt_pk_bf16_f32 v184, v196, v197\n"
      "v_and_b32 v201, v161, v235\n"
      "v_mfma_f32_16x16x32_bf16 v[132:135], a[184:187], v[88:91], v[132:135]\n"
      "ds_read_b128 v[68:71], v0 offset:5120\n"
      "v_mul_f32 v130, v225, v130\n"
      "v_mfma_f32_16x16x32_bf16 v[132:135], a[188:191], v[92:95], v[132:135]\n"
      "v_and_b32 v203, v163, v235\n"
      "v_mul_f32_e64 v131, v227, v131\n"
      "v_mfma_f32_16x16x32_bf16 v[132:135], a[192:195], v[96:99], v[132:135]\n"
      "ds_read_b128 v[64:67], v0 offset:4096\n"
      "v_mul_f32_e64 v128, v221, v128\n"
      "v_mfma_f32_16x16x32_bf16 v[132:135], a[196:199], v[100:103], v[132:135]\n"
      "v_and_b32_e64 v202, v162, v234\n"
      "v_cvt_pk_bf16_f32 v181, v130, v131\n"
      "v_mfma_f32_16x16x32_bf16 v[132:135], a[200:203], v[104:107], v[132:135]\n"
      "s_add_u32 s40, s40, 24576\n"
      "ds_read_b128 v[60:63], v0 offset:3072\n"
      "v_cvt_pk_bf16_f32 v180, v128, v129\n"
      "v_mfma_f32_16x16x32_bf16 v[208:211], v[184:187], v[200:203], 0\n"
      "s_waitcnt vmcnt(6) lgkmcnt(3)\n"
      "buffer_store_dword v13, v3, s[8:11], s52 offen sc1\n"
      "ds_read2st64_b64 v[212:215], v4 offset0:0 offset1:1\n"
      "v_mfma_f32_16x16x32_bf16 v[204:207], v[180:183], v[200:203], 0\n"
      "ds_read_b128 v[72:75], v0 offset:6144\n"
      "v_mfma_f32_16x16x32_bf16 v[132:135], a[204:207], v[108:111], v[132:135]\n"
      "ds_read_b128 v[76:79], v0 offset:7168\n"
      "v_mov_b32_e64 v178, v175\n"
      "v_mfma_f32_16x16x32_bf16 v[132:135], a[208:211], v[112:115], v[132:135]\n"
      "ds_read_b128 v[88:91], v0 offset:10240\n"
      "ds_read_b128 v[108:111], v0 offset:15360\n"
      "v_mfma_f32_16x16x32_bf16 v[132:135], a[212:215], v[116:119], v[132:135]\n"
      "s_sub_u32 s20, s20, 1\n"
      "s_cbranch_scc0 .Lq_loop\n"
      ".Lq_tail:\n"
      "v_mul_f32 v232, 0x3eae1a93, v219\n"
      "s_nop 7\n"
      "v_fma_f32 v135, -v218, v151, v135\n"
      "v_fma_f32 v133, -v218, v149, v133\n"
      "v_fma_f32 v134, -v218, v150, v134\n"
      "v_fma_f32 v132, -v218, v148, v132\n"
      "v_cvt_pk_bf16_f32 v176, v132, v133\n"
      "v_cvt_pk_bf16_f32 v177, v134, v135\n"
      "v_rcp_f32 v210, v210\n"
      "s_nop 0\n"
      "v_mul_f32 v204, v204, v210\n"
      "v_mfma_f32_16x16x32_bf16 v[188:191], v[172:175], v[176:179], v[152:155]\n"
      "v_mul_f32 v208, v208, v210\n"
      "v_mul_f32 v207, v207, v210\n"
      "v_mul_f32 v209, v209, v210\n"
      "v_cvt_pk_bf16_f32 v230, v208, v209\n"
      "buffer_store_dwordx2 v[230:231], v8, s[8:11], s40 offen sc1\n"
      "v_mul_f32 v206, v206, v210\n"
      "v_cvt_pk_bf16_f32 v229, v206, v207\n"
      "v_mul_f32 v205, v205, v210\n"
      "v_cvt_pk_bf16_f32 v228, v204, v205\n"
      "buffer_store_dwordx2 v[228:229], v7, s[8:11], s40 offen sc1\n"
      "v_mov_b32 v187, v185\n"
      "v_mov_b32 v186, v184\n"
      "v_mov_b32 v182, v180\n"
      "s_add_u32 s52, s52, 24576\n"
      "v_mov_b32 v183, v181\n"
      "s_add_u32 s40, s40, 24576\n"
      "s_waitcnt vmcnt(0)\n"
      "buffer_store_dword v13, v3, s[8:11], s52 offen sc1\n"
      "s_add_u32 s52, s52, 24576\n"
      "s_cmp_lg_u32 s44, 0\n"
      "s_cbranch_scc1 .Lq_done\n"
      "v_lshlrev_b32 v48, 3, v0\n"
      "s_mov_b32 s22, s23\n"
      ".Lq_up:\n"
      "s_mov_b32 s30, 128\n"
      "buffer_load_dword v49, v48, s[8:11], s30 offen sc1\n"
      "s_mov_b32 s30, 8320\n"
      "buffer_load_dword v50, v48, s[8:11], s30 offen sc1\n"
      "s_mov_b32 s30, 16512\n"
      "buffer_load_dword v51, v48, s[8:11], s30 offen sc1\n"
      "s_mov_b32 s30, 24704\n"
      "buffer_load_dword v52, v48, s[8:11], s30 offen sc1\n"
      "s_waitcnt vmcnt(0)\n"
      "v_xor_b32 v49, s21, v49\n"
      "v_xor_b32 v50, s21, v50\n"
      "v_xor_b32 v51, s21, v51\n"
      "v_xor_b32 v52, s21, v52\n"
      "v_or3_b32 v49, v49, v50, v51\n"
      "v_or_b32 v49, v49, v52\n"
      "v_cmp_ne_u32 vcc, 0, v49\n"
      "s_cbranch_vccz .Lq_ok\n"
      "s_sleep 4\n"
      "s_sub_u32 s22, s22, 1\n"
      "s_cbranch_scc0 .Lq_up\n"
      ".Lq_ok:\n"
      "v_lshrrev_b32 v48, 2, v0\n"
      "v_and_b32 v48, 0x7c, v48\n"
      "v_mov_b32 v49, s21\n"
      "buffer_store_dword v49, v48, s[8:11], 0 offen sc1\n"
      ".Lq_done:\n"
      "s_endpgm\n"
      ".Lsp_q0_1:\n"
      "s_mov_b32 s22, s23\n"
      ".Lsl_q0_1:\n"
      "buffer_load_dword v15, v2, s[8:11], s46 offen sc1\n"
      "s_waitcnt vmcnt(0)\n"
      "v_cmp_ne_u32 vcc, s21, v15\n"
      "s_cbranch_vccz .Lbk_q0_1\n"
      "s_sleep 4\n"
      "s_sub_u32 s22, s22, 1\n"
      "s_cbranch_scc0 .Lsl_q0_1\n"
      "s_mov_b32 s23, 0\n"
      "s_branch .Lbk_q0_1\n"
      ".Lsp_q1_2:\n"
      "s_mov_b32 s22, s23\n"
      ".Lsl_q1_2:\n"
      "buffer_load_dword v15, v2, s[8:11], s46 offen sc1\n"
      "s_waitcnt vmcnt(0)\n"
      "v_cmp_ne_u32 vcc, s21, v15\n"
      "s_cbranch_vccz .Lbk_q1_2\n"
      "s_sleep 4\n"
      "s_sub_u32 s22, s22, 1\n"
      "s_cbranch_scc0 .Lsl_q1_2\n"
      "s_mov_b32 s23, 0\n"
      "s_branch .Lbk_q1_2\n"
      ".Lsp_qy0_3:\n"
      "s_lshl_b32 s22, s23, 4\n"
      ".Lsl_qy0_3:\n"
      "ds_read_b32 v10, v12\n"
      "s_waitcnt lgkmcnt(0)\n"
      "v_cmp_gt_i32 vcc, v9, v10\n"
      "s_cbranch_vccz .Lbk_qy0_3\n"
      "s_sub_u32 s22, s22, 1\n"
      "s_cbranch_scc0 .Lsl_qy0_3\n"
      "s_mov_b32 s23, 0\n"
      "s_branch .Lbk_qy0_3\n"
      ".Lsp_q2_4:\n"
      "s_mov_b32 s22, s23\n"
      "buffer_load_dword v15, v2, s[8:11], s46 offen sc1\n"
      "s_waitcnt vmcnt(0)\n"
      "v_cmp_ne_u32 vcc, s21, v15\n"
      "s_add_u32 s30, s52, 24576\n"
      "buffer_store_dword v13, v3, s[8:11], s30 offen sc1\n"
      "s_cbranch_vccz .Lbk_q2_4\n"
      ".Lsl_q2_4:\n"
      "buffer_load_dword v15, v2, s[8:11], s46 offen sc1\n"
      "s_waitcnt vmcnt(0)\n"
      "v_cmp_ne_u32 vcc, s21, v15\n"
      "s_cbranch_vccz .Lbk_q2_4\n"
      "s_sleep 4\n"
      "s_sub_u32 s22, s22, 1\n"
      "s_cbranch_scc0 .Lsl_q2_4\n"
      "s_mov_b32 s23, 0\n"
      "s_branch .Lbk_q2_4\n"
      ".Lsp_qy1_5:\n"
      "s_lshl_b32 s22, s23, 4\n"
      ".Lsl_qy1_5:\n"
      "ds_read_b32 v10, v12\n"
      "s_waitcnt lgkmcnt(0)\n"
      "v_cmp_gt_i32 vcc, v9, v10\n"
      "s_cbranch_vccz .Lbk_qy1_5\n"
      "s_sub_u32 s22, s22, 1\n"
      "s_cbranch_scc0 .Lsl_qy1_5\n"
      "s_mov_b32 s23, 0\n"
      "s_branch .Lbk_qy1_5\n"
      ".Lsp_q0_6:\n"
      "s_mov_b32 s22, s23\n"
      "buffer_load_dword v15, v2, s[8:11], s46 offen sc1\n"
      "s_waitcnt vmcnt(0)\n"
      "v_cmp_ne_u32 vcc, s21, v15\n"
      "s_add_u32 s30, s52, 24576\n"
      "buffer_store_dword v13, v3, s[8:11], s30 offen sc1\n"
      "s_cbranch_vccz .Lbk_q0_6\n"
      ".Lsl_q0_6:\n"
      "buffer_load_dword v15, v2, s[8:11], s46 offen sc1\n"
      "s_waitcnt vmcnt(0)\n"
      "v_cmp_ne_u32 vcc, s21, v15\n"
      "s_cbranch_vccz .Lbk_q0_6\n"
      "s_sleep 4\n"
      "s_sub_u32 s22, s22, 1\n"
      "s_cbranch_scc0 .Lsl_q0_6\n"
      "s_mov_b32 s23, 0\n"
      "s_branch .Lbk_q0_6\n"
      ".Lsp_qy2_7:\n"
      "s_lshl_b32 s22, s23, 4\n"
      ".Lsl_qy2_7:\n"
      "ds_read_b32 v10, v12\n"
      "s_waitcnt lgkmcnt(0)\n"
      "v_cmp_gt_i32 vcc, v9, v10\n"
      "s_cbranch_vccz .Lbk_qy2_7\n"
      "s_sub_u32 s22, s22, 1\n"
      "s_cbranch_scc0 .Lsl_qy2_7\n"
      "s_mov_b32 s23, 0\n"
      "s_branch .Lbk_qy2_7\n"
      ".Lsp_q1_8:\n"
      "s_mov_b32 s22, s23\n"
      "buffer_load_dword v15, v2, s[8:11], s46 offen sc1\n"
      "s_waitcnt vmcnt(0)\n"
      "v_cmp_ne_u32 vcc, s21, v15\n"
      "s_add_u32 s30, s52, 0\n"
      "buffer_store_dword v13, v3, s[8:11], s30 offen sc1\n"
      "s_cbranch_vccz .Lbk_q1_8\n"
      ".Lsl_q1_8:\n"
      "buffer_load_dword v15, v2, s[8:11], s46 offen sc1\n"
      "s_waitcnt vmcnt(0)\n"
      "v_cmp_ne_u32 vcc, s21, v15\n"
      "s_cbranch_vccz .Lbk_q1_8\n"
      "s_sleep 4\n"
      "s_sub_u32 s22, s22, 1\n"
      "s_cbranch_scc0 .Lsl_q1_8\n"
      "s_mov_b32 s23, 0\n"
      "s_branch .Lbk_q1_8\n"
      ".Lsp_qy0_9:\n"
      "s_lshl_b32 s22, s23, 4\n"
      ".Lsl_qy0_9:\n"
      "ds_read_b32 v10, v12\n"
      "s_waitcnt lgkmcnt(0)\n"
      "v_cmp_gt_i32 vcc, v9, v10\n"
      "s_cbranch_vccz .Lbk_qy0_9\n"
      "s_sub_u32 s22, s22, 1\n"
      "s_cbranch_scc0 .Lsl_qy0_9\n"
      "s_mov_b32 s23, 0\n"
      "s_branch .Lbk_qy0_9\n"
      ".Lq_last:\n"
      :
      : "{s[8:11]}"(rsWS), "{s[12:15]}"(rsA), "{v0}"(v_l16), "{v1}"(v_l5), "{v2}"(v_fa), "{v3}"(v_ra), "{v4}"(v_st), "{v5}"(v_tw), "{v6}"(v_tr), "{v7}"(v_om), "{v8}"(v_oe), "{v10}"(v_al), "{v11}"(v_yw), "{v12}"(v_yr), "{v14}"(v_hi), "{v152}"(v_bc[0]), "{v153}"(v_bc[1]), "{v154}"(v_bc[2]), "{v155}"(v_bc[3]), "{v156}"(v_bp[0]), "{v157}"(v_bp[1]), "{v158}"(v_bp[2]), "{v159}"(v_bp[3]), "{v160}"(v_mk[0]), "{v161}"(v_mk[1]), "{v162}"(v_mk[2]), "{v163}"(v_mk[3]), "{v164}"(v_sel[0]), "{v165}"(v_sel[1]), "{v166}"(v_sel[2]), "{v167}"(v_sel[3]), "{v168}"(v_ci[0]), "{v169}"(v_ci[1]), "{v170}"(v_ci[2]), "{v171}"(v_ci[3]), "{v48}"(v_vo[0]), "{v49}"(v_vo[1]), "{v50}"(v_vo[2]), "{v51}"(v_vo[3]), "{v52}"(v_at), "{v53}"(v_ax), "{v54}"(v_pm), "{v55}"(v_ms), "{v76}"(v_pw), "{s36}"(s_sx), "{s37}"(s_ev), "{s38}"(s_del), "{s39}"(s_kil), "{s40}"(s_so), "{s41}"(s_m4), "{s42}"(s_m5), "{s43}"(s_ms), "{s44}"(s_up)
      : "v255", "a255", "s99", "vcc", "scc", "memory");

  } else if (role == 1) {
    const unsigned c = part, o = w & 1;
    const u32x4 rsA = srd(Wo, 576 * 576 * 2);
    const unsigned v_l5 = v_l16 + 16384 - w * 3072, e0 = 16384 + o * 2048 + g * 512 + i16 * 8, e1 = e0 + 2048 - o * 4096;
    const unsigned v_ex[8] = {e0, e1, e0 + 20480, e1 + 20480, e0 + 40960, e1 + 40960, e0 + 61440, e1 + 61440}, v_l2 = v_l16 + 40960;
    const unsigned v_fa = 20480 - w * 4096 + (8 * w + (lane >> 3)) * 128 + (lane & 7) * 4, v_ra = up * 8 * REC + l5 + 19456 + (4 * c + w) * 128;
    const unsigned v_xp = (9 * c + 2 * w) * 1024 + v_l16, v_xs = (9 * c + 8) * 1024 + (w >> 1) * 512 + half;
    const unsigned v_rp = v_xp + WS_XIN, v_rs = v_xs + WS_XIN;
    const unsigned v_ew = 82944 + (w >> 1) * 1024 + lane * 4;
    const unsigned v_sw = 86016 + i16 * 144 + (4 * w + g) * 8, v_sr = 86016 + (8 * (w >> 1) + l8) * 144 + l7;
    const unsigned v_ss = 18432 + c * 512 + (w >> 1) * 256 + l8 * 32 + p * 8;
    const unsigned v_pw = 19456 + (4 * c + w) * 128 + l8 * REC + l7;
    unsigned v_vo[9], v_vp[9];
#pragma unroll
    for (unsigned j = 0; j < 9; ++j) {
      const unsigned ch = 64 * j + lane, r = ch / 36, x = 576 * c + ch % 36 * 16 - 1024 * (j & 3);
      v_vo[j] = (20 * o + r) * 1152 + x;
      v_vp[j] = (36 * (4 * o + (r >> 2)) + 16 + (r & 3)) * 1152 + x;
    }
    const unsigned ro = (8 * g + q) * 576;
    const unsigned v_t0 = ro + o * 18432 + 128 * w + 16 * p, v_t1 = v_t0 + 18432 - 2 * o * 18432, v_ts = ro + o * 18432 + 512 + 32 * (w >> 1) + 8 * p;
    const unsigned s_sx = WS_OB + (gr * NREC + KSK) * REC + w * 4096, s_ev = s_sx + (31 - KSK) * REC, s_sy = WS_X1 + (gr * NREC + KSK) * REC;
    const unsigned s_m4 = w * 4096, s_m5 = 16384 + w * 1024, s_m9 = w * 9216, s_so = (w >> 1) * 8 * 41472;
    const unsigned s_s8 = s_so, s_wg = o, s_rb = WS_OB + w * 4096;
    asm volatile(
      "s_buffer_load_dword s21, s[8:11], 0\n"
      "s_mov_b32 s23, 65536\n"
      "s_mov_b32 s31, 0x40000000\n"
      "s_add_u32 m0, s41, 0\n"
      "s_nop 0\n"
      "buffer_load_dwordx4 v100, s[12:15], s42 offen offset:0 lds\n"
      "buffer_load_dwordx4 v101, s[12:15], s42 offen offset:1024 lds\n"
      "buffer_load_dwordx4 v102, s[12:15], s42 offen offset:2048 lds\n"
      "buffer_load_dwordx4 v103, s[12:15], s42 offen offset:3072 lds\n"
      "s_add_u32 m0, s41, 4096\n"
      "s_nop 0\n"
      "buffer_load_dwordx4 v104, s[12:15], s42 offen offset:0 lds\n"
      "buffer_load_dwordx4 v105, s[12:15], s42 offen offset:1024 lds\n"
      "buffer_load_dwordx4 v106, s[12:15], s42 offen offset:2048 lds\n"
      "buffer_load_dwordx4 v107, s[12:15], s42 offen offset:3072 lds\n"
      "s_add_u32 m0, s41, 8192\n"
      "s_nop 0\n"
      "buffer_load_dwordx4 v108, s[12:15], s42 offen offset:0 lds\n"
      "s_add_u32 s42, s42, 41472\n"
      "s_add_u32 m0, s41, 36864\n"
      "s_nop 0\n"
      "buffer_load_dwordx4 v100, s[12:15], s42 offen offset:0 lds\n"
      "buffer_load_dwordx4 v101, s[12:15], s42 offen offset:1024 lds\n"
      "buffer_load_dwordx4 v102, s[12:15], s42 offen offset:2048 lds\n"
      "buffer_load_dwordx4 v103, s[12:15], s42 offen offset:3072 lds\n"
      "s_add_u32 m0, s41, 40960\n"
      "s_nop 0\n"
      "buffer_load_dwordx4 v104, s[12:15], s42 offen offset:0 lds\n"
      "buffer_load_dwordx4 v105, s[12:15], s42 offen offset:1024 lds\n"
      "buffer_load_dwordx4 v106, s[12:15], s42 offen offset:2048 lds\n"
      "buffer_load_dwordx4 v107, s[12:15], s42 offen offset:3072 lds\n"
      "s_add_u32 m0, s41, 45056\n"
      "s_nop 0\n"
      "buffer_load_dwordx4 v108, s[12:15], s42 offen offset:0 lds\n"
      "s_add_u32 s42, s42, 41472\n"
      "s_add_u32 m0, s41, 73728\n"
      "s_nop 0\n"
      "buffer_load_dwordx4 v100, s[12:15], s42 offen offset:0 lds\n"
      "buffer_load_dwordx4 v101, s[12:15], s42 offen offset:1024 lds\n"
      "buffer_load_dwordx4 v102, s[12:15], s42 offen offset:2048 lds\n"
      "buffer_load_dwordx4 v103, s[12:15], s42 offen offset:3072 lds\n"
      "s_add_u32 m0, s41, 77824\n"
      "s_nop 0\n"
      "buffer_load_dwordx4 v104, s[12:15], s42 offen offset:0 lds\n"
      "buffer_load_dwordx4 v105, s[12:15], s42 offen offset:1024 lds\n"
      "buffer_load_dwordx4 v106, s[12:15], s42 offen offset:2048 lds\n"
      "buffer_load_dwordx4 v107, s[12:15], s42 offen offset:3072 lds\n"
      "s_add_u32 m0, s41, 81920\n"
      "s_nop 0\n"
      "buffer_load_dwordx4 v108, s[12:15], s42 offen offset:0 lds\n"
      "s_add_u32 s42, s42, 41472\n"
      "s_waitcnt vmcnt(18)\n"
      "s_barrier\n"
      "ds_read_b64_tr_b16 v[64:65], v120 offset:0\n"
      "ds_read_b64_tr_b16 v[66:67], v120 offset:2304\n"
      "ds_read_b64_tr_b16 a[0:1], v118 offset:0\n"
      "ds_read_b64_tr_b16 a[2:3], v118 offset:2304\n"
      "ds_read_b64_tr_b16 a[72:73], v118 offset:8\n"
      "ds_read_b64_tr_b16 a[74:75], v118 offset:2312\n"
      "ds_read_b64_tr_b16 a[144:145], v118 offset:64\n"
      "ds_read_b64_tr_b16 a[146:147], v118 offset:2368\n"
      "ds_read_b64_tr_b16 a[216:217], v118 offset:72\n"
      "ds_read_b64_tr_b16 a[218:219], v118 offset:2376\n"
      "ds_read_b64_tr_b16 a[36:37], v119 offset:0\n"
      "ds_read_b64_tr_b16 a[38:39], v119 offset:2304\n"
      "ds_read_b64_tr_b16 a[108:109], v119 offset:8\n"
      "ds_read_b64_tr_b16 a[110:111], v119 offset:2312\n"
      "ds_read_b64_tr_b16 a[180:181], v119 offset:64\n"
      "ds_read_b64_tr_b16 a[182:183], v119 offset:2368\n"
      "ds_read_b64_tr_b16 a[252:253], v119 offset:72\n"
      "ds_read_b64_tr_b16 a[254:255], v119 offset:2376\n"
      "v_add_u32 v118, 36864, v118\n"
      "v_add_u32 v119, 36864, v119\n"
      "v_add_u32 v120, 36864, v120\n"
      "s_add_u32 m0, s41, 110592\n"
      "s_nop 0\n"
      "buffer_load_dwordx4 v100, s[12:15], s42 offen offset:0 lds\n"
      "buffer_load_dwordx4 v101, s[12:15], s42 offen offset:1024 lds\n"
      "buffer_load_dwordx4 v102, s[12:15], s42 offen offset:2048 lds\n"
      "buffer_load_dwordx4 v103, s[12:15], s42 offen offset:3072 lds\n"
      "s_add_u32 m0, s41, 114688\n"
      "s_nop 0\n"
      "buffer_load_dwordx4 v104, s[12:15], s42 offen offset:0 lds\n"
      "buffer_load_dwordx4 v105, s[12:15], s42 offen offset:1024 lds\n"
      "buffer_load_dwordx4 v106, s[12:15], s42 offen offset:2048 lds\n"
      "buffer_load_dwordx4 v107, s[12:15], s42 offen offset:3072 lds\n"
      "s_add_u32 m0, s41, 118784\n"
      "s_nop 0\n"
      "buffer_load_dwordx4 v108, s[12:15], s42 offen offset:0 lds\n"
      "s_add_u32 s42, s42, 41472\n"
      "s_waitcnt vmcnt(18) lgkmcnt(0)\n"
      "s_barrier\n"
      "ds_read_b64_tr_b16 v[68:69], v120 offset:0\n"
      "ds_read_b64_tr_b16 v[70:71], v120 offset:2304\n"
      "ds_read_b64_tr_b16 a[4:5], v118 offset:0\n"
      "ds_read_b64_tr_b16 a[6:7], v118 offset:2304\n"
      "ds_read_b64_tr_b16 a[76:77], v118 offset:8\n"
      "ds_read_b64_tr_b16 a[78:79], v118 offset:2312\n"
      "ds_read_b64_tr_b16 a[148:149], v118 offset:64\n"
      "ds_read_b64_tr_b16 a[150:151], v118 offset:2368\n"
      "ds_read_b64_tr_b16 a[220:221], v118 offset:72\n"
      "ds_read_b64_tr_b16 a[222:223], v118 offset:2376\n"
      "ds_read_b64_tr_b16 a[40:41], v119 offset:0\n"
      "ds_read_b64_tr_b16 a[42:43], v119 offset:2304\n"
      "ds_read_b64_tr_b16 a[112:113], v119 offset:8\n"
      "ds_read_b64_tr_b16 a[114:115], v119 offset:2312\n"
      "ds_read_b64_tr_b16 a[184:185], v119 offset:64\n"
      "ds_read_b64_tr_b16 a[186:187], v119 offset:2368\n"
      "ds_read_b64_tr_b16 v[32:33], v119 offset:72\n"
      "ds_read_b64_tr_b16 v[34:35], v119 offset:2376\n"
      "v_add_u32 v118, 36864, v118\n"
      "v_add_u32 v119, 36864, v119\n"
      "v_add_u32 v120, 36864, v120\n"
      "s_add_u32 m0, s41, 0\n"
      "s_nop 0\n"
      "buffer_load_dwordx4 v100, s[12:15], s42 offen offset:0 lds\n"
      "buffer_load_dwordx4 v101, s[12:15], s42 offen offset:1024 lds\n"
      "buffer_load_dwordx4 v102, s[12:15], s42 offen offset:2048 lds\n"
      "buffer_load_dwordx4 v103, s[12:15], s42 offen offset:3072 lds\n"
      "s_add_u32 m0, s41, 4096\n"
      "s_nop 0\n"
      "buffer_load_dwordx4 v104, s[12:15], s42 offen offset:0 lds\n"
      "buffer_load_dwordx4 v105, s[12:15], s42 offen offset:1024 lds\n"
      "buffer_load_dwordx4 v106, s[12:15], s42 offen offset:2048 lds\n"
      "buffer_load_dwordx4 v107, s[12:15], s42 offen offset:3072 lds\n"
      "s_add_u32 m0, s41, 8192\n"
      "s_nop 0\n"
      "buffer_load_dwordx4 v108, s[12:15], s42 offen offset:0 lds\n"
      "s_add_u32 s42, s42, 41472\n"
      "s_waitcnt vmcnt(18) lgkmcnt(0)\n"
      "s_barrier\n"
      "ds_read_b64_tr_b16 v[72:73], v120 offset:0\n"
      "ds_read_b64_tr_b16 v[74:75], v120 offset:2304\n"
      "ds_read_b64_tr_b16 a[8:9], v118 offset:0\n"
      "ds_read_b64_tr_b16 a[10:11], v118 offset:2304\n"
      "ds_read_b64_tr_b16 a[80:81], v118 offset:8\n"
      "ds_read_b64_tr_b16 a[82:83], v118 offset:2312\n"
      "ds_read_b64_tr_b16 a[152:153], v118 offset:64\n"
      "ds_read_b64_tr_b16 a[154:155], v118 offset:2368\n"
      "ds_read_b64_tr_b16 a[224:225], v118 offset:72\n"
      "ds_read_b64_tr_b16 a[226:227], v118 offset:2376\n"
      "ds_read_b64_tr_b16 a[44:45], v119 offset:0\n"
      "ds_read_b64_tr_b16 a[46:47], v119 offset:2304\n"
      "ds_read_b64_tr_b16 a[116:117], v119 offset:8\n"
      "ds_read_b64_tr_b16 a[118:119], v119 offset:2312\n"
      "ds_read_b64_tr_b16 a[188:189], v119 offset:64\n"
      "ds_read_b64_tr_b16 a[190:191], v119 offset:2368\n"
      "ds_read_b64_tr_b16 v[36:37], v119 offset:72\n"
      "ds_read_b64_tr_b16 v[38:39], v119 offset:2376\n"
      "v_add_u32 v118, 36864, v118\n"
      "v_add_u32 v119, 36864, v119\n"
      "v_add_u32 v120, 36864, v120\n"
      "s_add_u32 m0, s41, 36864\n"
      "s_nop 0\n"
      "buffer_load_dwordx4 v100, s[12:15], s42 offen offset:0 lds\n"
      "buffer_load_dwordx4 v101, s[12:15], s42 offen offset:1024 lds\n"
      "buffer_load_dwordx4 v102, s[12:15], s42 offen offset:2048 lds\n"
      "buffer_load_dwordx4 v103, s[12:15], s42 offen offset:3072 lds\n"
      "s_add_u32 m0, s41, 40960\n"
      "s_nop 0\n"
      "buffer_load_dwordx4 v104, s[12:15], s42 offen offset:0 lds\n"
      "buffer_load_dwordx4 v105, s[12:15], s42 offen offset:1024 lds\n"
      "buffer_load_dwordx4 v106, s[12:15], s42 offen offset:2048 lds\n"
      "buffer_load_dwordx4 v107, s[12:15], s42 offen offset:3072 lds\n"
      "s_add_u32 m0, s41, 45056\n"
      "s_nop 0\n"
      "buffer_load_dwordx4 v108, s[12:15], s42 offen offset:0 lds\n"
      "s_add_u32 s42, s42, 41472\n"
      "s_waitcnt vmcnt(18) lgkmcnt(0)\n"
      "s_barrier\n"
      "ds_read_b64_tr_b16 v[76:77], v120 offset:0\n"
      "ds_read_b64_tr_b16 v[78:79], v120 offset:2304\n"
      "ds_read_b64_tr_b16 a[12:13], v118 offset:0\n"
      "ds_read_b64_tr_b16 a[14:15], v118 offset:2304\n"
      "ds_read_b64_tr_b16 a[84:85], v118 offset:8\n"
      "ds_read_b64_tr_b16 a[86:87], v118 offset:2312\n"
      "ds_read_b64_tr_b16 a[156:157], v118 offset:64\n"
      "ds_read_b64_tr_b16 a[158:159], v118 offset:2368\n"
      "ds_read_b64_tr_b16 a[228:229], v118 offset:72\n"
      "ds_read_b64_tr_b16 a[230:231], v118 offset:2376\n"
      "ds_read_b64_tr_b16 a[48:49], v119 offset:0\n"
      "ds_read_b64_tr_b16 a[50:51], v119 offset:2304\n"
      "ds_read_b64_tr_b16 a[120:121], v119 offset:8\n"
      "ds_read_b64_tr_b16 a[122:123], v119 offset:2312\n"
      "ds_read_b64_tr_b16 a[192:193], v119 offset:64\n"
      "ds_read_b64_tr_b16 a[194:195], v119 offset:2368\n"
      "ds_read_b64_tr_b16 v[40:41], v119 offset:72\n"
      "ds_read_b64_tr_b16 v[42:43], v119 offset:2376\n"
      "v_add_u32 v118, 4294856704, v118\n"
      "v_add_u32 v119, 4294856704, v119\n"
      "v_add_u32 v120, 4294856704, v120\n"
      "s_add_u32 m0, s41, 73728\n"
      "s_nop 0\n"
      "buffer_load_dwordx4 v100, s[12:15], s42 offen offset:0 lds\n"
      "buffer_load_dwordx4 v101, s[12:15], s42 offen offset:1024 lds\n"
      "buffer_load_dwordx4 v102, s[12:15], s42 offen offset:2048 lds\n"
      "buffer_load_dwordx4 v103, s[12:15], s42 offen offset:3072 lds\n"
      "s_add_u32 m0, s41, 77824\n"
      "s_nop 0\n"
      "buffer_load_dwordx4 v104, s[12:15], s42 offen offset:0 lds\n"
      "buffer_load_dwordx4 v105, s[12:15], s42 offen offset:1024 lds\n"
      "buffer_load_dwordx4 v106, s[12:15], s42 offen offset:2048 lds\n"
      "buffer_load_dwordx4 v107, s[12:15], s42 offen offset:3072 lds\n"
      "s_add_u32 m0, s41, 81920\n"
      "s_nop 0\n"
      "buffer_load_dwordx4 v108, s[12:15], s42 offen offset:0 lds\n"
      "s_add_u32 s42, s42, 41472\n"
      "s_waitcnt vmcnt(18) lgkmcnt(0)\n"
      "s_barrier\n"
      "ds_read_b64_tr_b16 v[80:81], v120 offset:0\n"
      "ds_read_b64_tr_b16 v[82:83], v120 offset:2304\n"
      "ds_read_b64_tr_b16 a[16:17], v118 offset:0\n"
      "ds_read_b64_tr_b16 a[18:19], v118 offset:2304\n"
      "ds_read_b64_tr_b16 a[88:89], v118 offset:8\n"
      "ds_read_b64_tr_b16 a[90:91], v118 offset:2312\n"
      "ds_read_b64_tr_b16 a[160:161], v118 offset:64\n"
      "ds_read_b64_tr_b16 a[162:163], v118 offset:2368\n"
      "ds_read_b64_tr_b16 a[232:233], v118 offset:72\n"
      "ds_read_b64_tr_b16 a[234:235], v118 offset:2376\n"
      "ds_read_b64_tr_b16 a[52:53], v119 offset:0\n"
      "ds_read_b64_tr_b16 a[54:55], v119 offset:2304\n"
      "ds_read_b64_tr_b16 a[124:125], v119 offset:8\n"
      "ds_read_b64_tr_b16 a[126:127], v119 offset:2312\n"
      "ds_read_b64_tr_b16 a[196:197], v119 offset:64\n"
      "ds_read_b64_tr_b16 a[198:199], v119 offset:2368\n"
      "ds_read_b64_tr_b16 v[44:45], v119 offset:72\n"
      "ds_read_b64_tr_b16 v[46:47], v119 offset:2376\n"
      "v_add_u32 v118, 36864, v118\n"
      "v_add_u32 v119, 36864, v119\n"
      "v_add_u32 v120, 36864, v120\n"
      "s_add_u32 m0, s41, 110592\n"
      "s_nop 0\n"
      "buffer_load_dwordx4 v100, s[12:15], s42 offen offset:0 lds\n"
      "buffer_load_dwordx4 v101, s[12:15], s42 offen offset:1024 lds\n"
      "buffer_load_dwordx4 v102, s[12:15], s42 offen offset:2048 lds\n"
      "buffer_load_dwordx4 v103, s[12:15], s42 offen offset:3072 lds\n"
      "s_add_u32 m0, s41, 114688\n"
      "s_nop 0\n"
      "buffer_load_dwordx4 v104, s[12:15], s42 offen offset:0 lds\n"
      "buffer_load_dwordx4 v105, s[12:15], s42 offen offset:1024 lds\n"
      "buffer_load_dwordx4 v106, s[12:15], s42 offen offset:2048 lds\n"
      "buffer_load_dwordx4 v107, s[12:15], s42 offen offset:3072 lds\n"
      "s_add_u32 m0, s41, 118784\n"
      "s_nop 0\n"
      "buffer_load_dwordx4 v108, s[12:15], s42 offen offset:0 lds\n"
      "s_add_u32 s42, s42, 41472\n"
      "s_waitcnt vmcnt(18) lgkmcnt(0)\n"
      "s_barrier\n"
      "ds_read_b64_tr_b16 v[84:85], v120 offset:0\n"
      "ds_read_b64_tr_b16 v[86:87], v120 offset:2304\n"
      "ds_read_b64_tr_b16 a[20:21], v118 offset:0\n"
      "ds_read_b64_tr_b16 a[22:23], v118 offset:2304\n"
      "ds_read_b64_tr_b16 a[92:93], v118 offset:8\n"
      "ds_read_b64_tr_b16 a[94:95], v118 offset:2312\n"
      "ds_read_b64_tr_b16 a[164:165], v118 offset:64\n"
      "ds_read_b64_tr_b16 a[166:167], v118 offset:2368\n"
      "ds_read_b64_tr_b16 a[236:237], v118 offset:72\n"
      "ds_read_b64_tr_b16 a[238:239], v118 offset:2376\n"
      "ds_read_b64_tr_b16 a[56:57], v119 offset:0\n"
      "ds_read_b64_tr_b16 a[58:59], v119 offset:2304\n"
      "ds_read_b64_tr_b16 a[128:129], v119 offset:8\n"
      "ds_read_b64_tr_b16 a[130:131], v119 offset:2312\n"
      "ds_read_b64_tr_b16 a[200:201], v119 offset:64\n"
      "ds_read_b64_tr_b16 a[202:203], v119 offset:2368\n"
      "ds_read_b64_tr_b16 v[48:49], v119 offset:72\n"
      "ds_read_b64_tr_b16 v[50:51], v119 offset:2376\n"
      "v_add_u32 v118, 36864, v118\n"
      "v_add_u32 v119, 36864, v119\n"
      "v_add_u32 v120, 36864, v120\n"
      "s_add_u32 m0, s41, 0\n"
      "s_nop 0\n"
      "buffer_load_dwordx4 v109, s[12:15], s43 offen offset:0 lds\n"
      "buffer_load_dwordx4 v110, s[12:15], s43 offen offset:1024 lds\n"
      "buffer_load_dwordx4 v111, s[12:15], s43 offen offset:2048 lds\n"
      "buffer_load_dwordx4 v112, s[12:15], s43 offen offset:3072 lds\n"
      "s_add_u32 m0, s41, 4096\n"
      "s_nop 0\n"
      "buffer_load_dwordx4 v113, s[12:15], s43 offen offset:0 lds\n"
      "buffer_load_dwordx4 v114, s[12:15], s43 offen offset:1024 lds\n"
      "buffer_load_dwordx4 v115, s[12:15], s43 offen offset:2048 lds\n"
      "buffer_load_dwordx4 v116, s[12:15], s43 offen offset:3072 lds\n"
      "s_add_u32 m0, s41, 8192\n"
      "s_nop 0\n"
      "buffer_load_dwordx4 v117, s[12:15], s43 offen offset:0 lds\n"
      "s_add_u32 s42, s42, 41472\n"
      "s_waitcnt vmcnt(18) lgkmcnt(0)\n"
      "s_barrier\n"
      "ds_read_b64_tr_b16 v[88:89], v120 offset:0\n"
      "ds_read_b64_tr_b16 v[90:91], v120 offset:2304\n"
      "ds_read_b64_tr_b16 a[24:25], v118 offset:0\n"
      "ds_read_b64_tr_b16 a[26:27], v118 offset:2304\n"
      "ds_read_b64_tr_b16 a[96:97], v118 offset:8\n"
      "ds_read_b64_tr_b16 a[98:99], v118 offset:2312\n"
      "ds_read_b64_tr_b16 a[168:169], v118 offset:64\n"
      "ds_read_b64_tr_b16 a[170:171], v118 offset:2368\n"
      "ds_read_b64_tr_b16 a[240:241], v118 offset:72\n"
      "ds_read_b64_tr_b16 a[242:243], v118 offset:2376\n"
      "ds_read_b64_tr_b16 a[60:61], v119 offset:0\n"
      "ds_read_b64_tr_b16 a[62:63], v119 offset:2304\n"
      "ds_read_b64_tr_b16 a[132:133], v119 offset:8\n"
      "ds_read_b64_tr_b16 a[134:135], v119 offset:2312\n"
      "ds_read_b64_tr_b16 a[204:205], v119 offset:64\n"
      "ds_read_b64_tr_b16 a[206:207], v119 offset:2368\n"
      "ds_read_b64_tr_b16 v[52:53], v119 offset:72\n"
      "ds_read_b64_tr_b16 v[54:55], v119 offset:2376\n"
      "v_add_u32 v118, 36864, v118\n"
      "v_add_u32 v119, 36864, v119\n"
      "v_add_u32 v120, 36864, v120\n"
      "s_waitcnt vmcnt(9) lgkmcnt(0)\n"
      "s_barrier\n"
      "ds_read_b64_tr_b16 v[92:93], v120 offset:0\n"
      "ds_read_b64_tr_b16 v[94:95], v120 offset:2304\n"
      "ds_read_b64_tr_b16 a[28:29], v118 offset:0\n"
      "ds_read_b64_tr_b16 a[30:31], v118 offset:2304\n"
      "ds_read_b64_tr_b16 a[100:101], v118 offset:8\n"
      "ds_read_b64_tr_b16 a[102:103], v118 offset:2312\n"
      "ds_read_b64_tr_b16 a[172:173], v118 offset:64\n"
      "ds_read_b64_tr_b16 a[174:175], v118 offset:2368\n"
      "ds_read_b64_tr_b16 a[244:245], v118 offset:72\n"
      "ds_read_b64_tr_b16 a[246:247], v118 offset:2376\n"
      "ds_read_b64_tr_b16 a[64:65], v119 offset:0\n"
      "ds_read_b64_tr_b16 a[66:67], v119 offset:2304\n"
      "ds_read_b64_tr_b16 a[136:137], v119 offset:8\n"
      "ds_read_b64_tr_b16 a[138:139], v119 offset:2312\n"
      "ds_read_b64_tr_b16 a[208:209], v119 offset:64\n"
      "ds_read_b64_tr_b16 a[210:211], v119 offset:2368\n"
      "ds_read_b64_tr_b16 v[56:57], v119 offset:72\n"
      "ds_read_b64_tr_b16 v[58:59], v119 offset:2376\n"
      "v_add_u32 v118, 4294856704, v118\n"
      "v_add_u32 v119, 4294856704, v119\n"
      "v_add_u32 v120, 4294856704, v120\n"
      "s_waitcnt vmcnt(0) lgkmcnt(0)\n"
      "s_barrier\n"
      "ds_read_b64_tr_b16 v[96:97], v120 offset:0\n"
      "ds_read_b64_tr_b16 v[98:99], v120 offset:2304\n"
      "ds_read_b64_tr_b16 a[32:33], v118 offset:0\n"
      "ds_read_b64_tr_b16 a[34:35], v118 offset:2304\n"
      "ds_read_b64_tr_b16 a[104:105], v118 offset:8\n"
      "ds_read_b64_tr_b16 a[106:107], v118 offset:2312\n"
      "ds_read_b64_tr_b16 a[176:177], v118 offset:64\n"
      "ds_read_b64_tr_b16 a[178:179], v118 offset:2368\n"
      "ds_read_b64_tr_b16 a[248:249], v118 offset:72\n"
      "ds_read_b64_tr_b16 a[250:251], v118 offset:2376\n"
      "ds_read_b64_tr_b16 a[68:69], v119 offset:0\n"
      "ds_read_b64_tr_b16 a[70:71], v119 offset:2304\n"
      "ds_read_b64_tr_b16 a[140:141], v119 offset:8\n"
      "ds_read_b64_tr_b16 a[142:143], v119 offset:2312\n"
      "ds_read_b64_tr_b16 a[212:213], v119 offset:64\n"
      "ds_read_b64_tr_b16 a[214:215], v119 offset:2368\n"
      "ds_read_b64_tr_b16 v[60:61], v119 offset:72\n"
      "ds_read_b64_tr_b16 v[62:63], v119 offset:2376\n"
      "v_add_u32 v118, 36864, v118\n"
      "v_add_u32 v119, 36864, v119\n"
      "v_add_u32 v120, 36864, v120\n"
      "s_waitcnt lgkmcnt(0)\n"
      "s_getpc_b64 vcc\n"
      ".Lw_ct:\n"
      "s_add_u32 vcc_lo, vcc_lo, .Lw_p0-.Lw_ct\n"
      "s_addc_u32 vcc_hi, vcc_hi, 0\n"
      "s_lshl_b32 s30, s39, 1\n"
      "v_lshl_add_u32 v130, v0, 3, s30\n"
      "v_min_u32 v130, .Lw_last-.Lw_p0-4, v130\n"
      "global_load_dword v130, v130, vcc\n"
      "s_mul_i32 s21, s21, 1664525\n"
      "s_add_u32 s21, s21, 1013904223\n"
      "v_mov_b32 v25, s21\n"
      "buffer_store_dword v25, v22, s[8:11], 0 offen sc1\n"
      "v_not_b32 v124, v25\n"
      "v_not_b32 v125, v25\n"
      "v_not_b32 v126, v25\n"
      "v_not_b32 v127, v25\n"
      "s_add_u32 s30, s38, 0\n"
      "buffer_store_dwordx4 v[124:127], v121, s[8:11], s30 offen sc1\n"
      "v_xor_b32 v25, v25, v26\n"
      "v_mov_b32 v27, 0x3ae38e39\n"
      "s_barrier\n"
      "v_mov_b32 v21, 0\n"
      "ds_write_b32 v23, v21\n"
      "s_waitcnt lgkmcnt(0)\n"
      "s_barrier\n"
      "s_add_u32 s6, s36, 1572864\n"
      "s_add_u32 s36, s36, 4294942720\n"
      "s_mov_b32 s51, 24576\n"
      "s_waitcnt vmcnt(0) lgkmcnt(0)\n"
      "s_cmp_lg_u32 s44, 0\n"
      "s_cbranch_scc1 .Lv_start\n"
      "s_mov_b64 exec, 0\n"
      "s_lshr_b32 s22, s39, 13\n"
      ".Lw_pl:\n"
      "s_cmp_eq_u32 s22, 0\n"
      "s_cbranch_scc1 .Lw_p0\n"
      "s_cmp_eq_u32 s22, 1\n"
      "s_cbranch_scc1 .Lw_p1\n"
      "s_branch .Lw_pe\n"
      ".Lw_pr:\n"
      "s_add_u32 s22, s22, 2\n"
      "s_branch .Lw_pl\n"
      ".Lw_pe:\n"
      "s_mov_b64 exec, -1\n"
      "s_add_u32 s50, s38, 4294893568\n"
      "s_add_u32 s49, s38, 4294893568\n"
      "s_add_u32 s38, s38, 4294942720\n"
      "s_cmp_eq_u32 s36, s37\n"
      "s_cselect_b32 s48, 49152, s51\n"
      "s_add_u32 s36, s36, s48\n"
      "s_min_u32 s36, s36, s6\n"
      "buffer_load_dword v28, v11, s[8:11], s36 offen sc1\n"
      "s_mov_b32 s46, s36\n"
      "s_add_u32 s36, s36, 24576\n"
      "s_min_u32 s36, s36, s6\n"
      "s_waitcnt vmcnt(0)\n"
      "v_cmp_ne_u32 vcc, s21, v28\n"
      "s_add_u32 m0, s39, 0\n"
      "s_cbranch_vccnz .Lsp_w0_1\n"
      ".Lbk_w0_1:\n"
      "buffer_load_dword v28, v11, s[8:11], s36 offen sc1\n"
      "buffer_load_dwordx4 v0, s[8:11], s46 offen offset:0 sc1 lds\n"
      "buffer_load_dwordx4 v0, s[8:11], s46 offen offset:1024 sc1 lds\n"
      "buffer_load_dwordx4 v0, s[8:11], s46 offen offset:3072 sc1 lds\n"
      "buffer_load_dwordx4 v0, s[8:11], s46 offen offset:2048 sc1 lds\n"
      "s_add_u32 m0, s40, 0\n"
      "s_nop 0\n"
      "buffer_load_dwordx4 v1, s[8:11], s46 offen offset:0 sc1 lds\n"
      "s_sub_u32 s47, s46, s45\n"
      "buffer_load_dwordx2 v[200:201], v14, s[8:11], s47 offen sc1\n"
      "buffer_load_dwordx4 v[192:195], v13, s[8:11], s47 offen sc1\n"
      "buffer_load_dword v26, v23, s[8:11], s31 offen\n"
      "s_add_u32 m0, s39, 20480\n"
      "s_mov_b32 s46, s36\n"
      "s_waitcnt vmcnt(8)\n"
      "v_cmp_ne_u32 vcc, s21, v28\n"
      "buffer_load_dword v26, v23, s[8:11], s31 offen\n"
      "s_cmp_eq_u32 s36, s37\n"
      "s_cselect_b32 s48, 49152, s51\n"
      "s_add_u32 s36, s36, s48\n"
      "s_cbranch_vccnz .Lsp_w1_2\n"
      ".Lbk_w1_2:\n"
      "s_min_u32 s36, s36, s6\n"
      "buffer_load_dword v28, v11, s[8:11], s36 offen sc1\n"
      "buffer_load_dwordx4 v0, s[8:11], s46 offen offset:0 sc1 lds\n"
      "buffer_load_dwordx4 v0, s[8:11], s46 offen offset:1024 sc1 lds\n"
      "buffer_load_dwordx4 v0, s[8:11], s46 offen offset:3072 sc1 lds\n"
      "v_add_u32 v21, 1, v21\n"
      "s_waitcnt vmcnt(8)\n"
      "ds_write_b32 v23, v21\n"
      "buffer_load_dwordx4 v0, s[8:11], s46 offen offset:2048 sc1 lds\n"
      "s_add_u32 m0, s40, 20480\n"
      "buffer_load_dwordx4 v[196:199], v13, s[8:11], s47 offen offset:1024 sc1\n"
      "buffer_load_dwordx4 v1, s[8:11], s46 offen offset:0 sc1 lds\n"
      "ds_read_b32 v22, v24\n"
      "s_waitcnt lgkmcnt(0)\n"
      "v_cmp_gt_i32 vcc, v21, v22\n"
      "s_waitcnt vmcnt(10)\n"
      "v_and_b32 v189, 0xffff0000, v200\n"
      "v_lshlrev_b32 v188, 16, v200\n"
      "s_cbranch_vccnz .Lsp_wy0_3\n"
      ".Lbk_wy0_3:\n"
      "ds_read_b128 v[104:107], v0 offset:1024\n"
      "v_lshlrev_b32 v190, 16, v201\n"
      "ds_read_b128 v[100:103], v0 offset:0\n"
      "ds_read_b128 v[120:123], v0 offset:5120\n"
      "s_waitcnt vmcnt(9)\n"
      "v_lshlrev_b32 v178, 16, v195\n"
      "ds_read_b128 v[112:115], v0 offset:3072\n"
      "ds_read_b128 v[116:119], v0 offset:4096\n"
      "v_lshlrev_b32 v174, 16, v193\n"
      "ds_read_b128 v[124:127], v0 offset:6144\n"
      "s_sub_u32 s47, s46, s45\n"
      "ds_read_b128 v[108:111], v0 offset:2048\n"
      "v_and_b32 v191, 0xffff0000, v201\n"
      "v_and_b32 v177, 0xffff0000, v194\n"
      "ds_read_b128 v[136:139], v0 offset:8192\n"
      "v_lshlrev_b32 v176, 16, v194\n"
      "ds_read_b128 v[128:131], v0 offset:7168\n"
      "v_lshlrev_b32 v172, 16, v192\n"
      "v_and_b32 v175, 0xffff0000, v193\n"
      "buffer_load_dwordx2 v[200:201], v14, s[8:11], s47 offen sc1\n"
      "ds_read2_b32 v[132:133], v2 offset0:0 offset1:32\n"
      "v_and_b32 v173, 0xffff0000, v192\n"
      "ds_read2_b32 v[134:135], v2 offset0:64 offset1:96\n"
      "s_mov_b32 s20, 31\n"
      "s_waitcnt lgkmcnt(0)\n"
      "s_nop 4\n"
      "s_branch .Lw_loop\n"
      ".p2align 12\n"
      ".Lw_p0:\n"
      "s_branch .Lw_pr\n"
      ".p2align 5\n"
      ".Lw_loop:\n"
      "v_mfma_f32_16x16x32_bf16 v[188:191], v[64:67], v[100:103], v[188:191]\n"
      "ds_read_b128 v[140:143], v0 offset:9216\n"
      "v_and_b32 v179, 0xffff0000, v195\n"
      "v_mfma_f32_16x16x32_bf16 v[188:191], v[68:71], v[104:107], v[188:191]\n"
      "buffer_load_dwordx4 v[192:195], v13, s[8:11], s47 offen sc1\n"
      "v_mfma_f32_16x16x32_bf16 v[188:191], v[72:75], v[108:111], v[188:191]\n"
      "v_add_f32 v218, v218, v182\n"
      "v_mfma_f32_16x16x32_bf16 v[188:191], v[76:79], v[112:115], v[188:191]\n"
      "ds_read_b128 v[156:159], v0 offset:13312\n"
      "v_fmac_f32 v219, v184, v184\n"
      "v_mfma_f32_16x16x32_bf16 v[188:191], v[80:83], v[116:119], v[188:191]\n"
      "ds_read_b128 v[164:167], v0 offset:15360\n"
      "v_cvt_pk_bf16_f32 v213, v182, v183\n"
      "v_mfma_f32_16x16x32_bf16 v[188:191], v[84:87], v[120:123], v[188:191]\n"
      "ds_read_b128 v[144:147], v0 offset:10240\n"
      "v_mfma_f32_16x16x32_bf16 v[188:191], v[88:91], v[124:127], v[188:191]\n"
      "s_waitcnt lgkmcnt(4)\n"
      "v_cvt_pk_bf16_f32 v215, v186, v187\n"
      "v_add_f32 v218, v218, v183\n"
      "v_mfma_f32_16x16x32_bf16 v[188:191], v[92:95], v[128:131], v[188:191]\n"
      "buffer_store_dwordx4 v[208:211], v15, s[8:11], s38 offen sc1\n"
      "v_mfma_f32_16x16x32_bf16 v[188:191], v[96:99], v[132:135], v[188:191]\n"
      "v_cvt_pk_bf16_f32 v212, v180, v181\n"
      "v_add_f32 v218, v218, v184\n"
      "v_mfma_f32_16x16x32_bf16 v[172:175], a[0:3], v[100:103], v[172:175]\n"
      "s_add_u32 s50, s50, 24576\n"
      "v_cvt_pk_bf16_f32 v214, v184, v185\n"
      "v_add_f32 v218, v218, v185\n"
      "v_mfma_f32_16x16x32_bf16 v[172:175], a[4:7], v[104:107], v[172:175]\n"
      "s_mov_b32 s46, s36\n"
      "v_add_f32 v218, v218, v186\n"
      "v_mfma_f32_16x16x32_bf16 v[172:175], a[8:11], v[108:111], v[172:175]\n"
      "ds_read_b128 v[148:151], v0 offset:11264\n"
      "v_add_f32 v218, v218, v187\n"
      "v_mfma_f32_16x16x32_bf16 v[172:175], a[12:15], v[112:115], v[172:175]\n"
      "v_fmac_f32 v219, v185, v185\n"
      "v_add_u32 v21, 1, v21\n"
      "v_mfma_f32_16x16x32_bf16 v[172:175], a[16:19], v[116:119], v[172:175]\n"
      "s_add_u32 s36, s36, 24576\n"
      "ds_read_b128 v[152:155], v0 offset:12288\n"
      "v_fmac_f32 v219, v186, v186\n"
      "v_mfma_f32_16x16x32_bf16 v[172:175], a[20:23], v[120:123], v[172:175]\n"
      "buffer_store_dwordx2 v[216:217], v16, s[8:11], s38 offen sc1\n"
      "v_mfma_f32_16x16x32_bf16 v[172:175], a[24:27], v[124:127], v[172:175]\n"
      "ds_read_b128 v[160:163], v0 offset:14336\n"
      "v_mfma_f32_16x16x32_bf16 v[172:175], a[28:31], v[128:131], v[172:175]\n"
      "ds_read2_b32 v[170:171], v3 offset0:64 offset1:96\n"
      "v_mfma_f32_16x16x32_bf16 v[172:175], a[32:35], v[132:135], v[172:175]\n"
      "ds_read2_b32 v[168:169], v3 offset0:0 offset1:32\n"
      "s_min_u32 s36, s36, s6\n"
      "v_mfma_f32_16x16x32_bf16 v[172:175], a[36:39], v[136:139], v[172:175]\n"
      "s_waitcnt lgkmcnt(4)\n"
      "v_mfma_f32_16x16x32_bf16 v[172:175], a[40:43], v[140:143], v[172:175]\n"
      "buffer_store_dwordx4 v[212:215], v15, s[8:11], s38 offen offset:1024 sc1\n"
      "v_mfma_f32_16x16x32_bf16 v[172:175], a[44:47], v[144:147], v[172:175]\n"
      "ds_write_b32 v18, v218\n"
      "v_mfma_f32_16x16x32_bf16 v[172:175], a[48:51], v[148:151], v[172:175]\n"
      "s_waitcnt vmcnt(11) lgkmcnt(4)\n"
      "v_fmac_f32_e64 v219, v187, v187\n"
      "v_cmp_ne_u32 vcc, s21, v28\n"
      "v_mfma_f32_16x16x32_bf16 v[172:175], a[52:55], v[152:155], v[172:175]\n"
      "ds_write_b32 v18, v219 offset:4\n"
      "v_mfma_f32_16x16x32_bf16 v[172:175], a[56:59], v[156:159], v[172:175]\n"
      "s_waitcnt lgkmcnt(3)\n"
      "s_add_u32 m0, s39, 40960\n"
      "v_mfma_f32_16x16x32_bf16 v[172:175], a[60:63], v[160:163], v[172:175]\n"
      "ds_write_b32 v23, v21\n"
      "v_mfma_f32_16x16x32_bf16 v[172:175], a[64:67], v[164:167], v[172:175]\n"
      "s_waitcnt lgkmcnt(3)\n"
      "s_cbranch_vccnz .Lsp_w0_4\n"
      ".Lbk_w0_4:\n"
      "v_mfma_f32_16x16x32_bf16 v[172:175], a[68:71], v[168:171], v[172:175]\n"
      "v_mfma_f32_16x16x32_bf16 v[176:179], a[72:75], v[100:103], v[176:179]\n"
      "buffer_load_dword v28, v11, s[8:11], s36 offen sc1\n"
      "v_mfma_f32_16x16x32_bf16 v[176:179], a[76:79], v[104:107], v[176:179]\n"
      "s_waitcnt vmcnt(7) lgkmcnt(2)\n"
      "v_lshlrev_b32 v186, 16, v199\n"
      "v_lshlrev_b32 v184, 16, v198\n"
      "v_mfma_f32_16x16x32_bf16 v[176:179], a[80:83], v[108:111], v[176:179]\n"
      "v_lshlrev_b32 v180, 16, v196\n"
      "v_and_b32 v183, 0xffff0000, v197\n"
      "v_mfma_f32_16x16x32_bf16 v[176:179], a[84:87], v[112:115], v[176:179]\n"
      "buffer_store_dword v25, v12, s[8:11], s50 offen sc1\n"
      "v_mfma_f32_16x16x32_bf16 v[176:179], a[88:91], v[116:119], v[176:179]\n"
      "v_lshlrev_b32 v182, 16, v197\n"
      "v_and_b32 v181, 0xffff0000, v196\n"
      "v_mfma_f32_16x16x32_bf16 v[176:179], a[92:95], v[120:123], v[176:179]\n"
      "v_and_b32 v187, 0xffff0000, v199\n"
      "v_and_b32 v185, 0xffff0000, v198\n"
      "v_mfma_f32_16x16x32_bf16 v[176:179], a[96:99], v[124:127], v[176:179]\n"
      "buffer_load_dwordx4 v[196:199], v13, s[8:11], s47 offen offset:1024 sc1\n"
      "v_mfma_f32_16x16x32_bf16 v[176:179], a[100:103], v[128:131], v[176:179]\n"
      "ds_read_b32 v22, v24\n"
      "v_mul_f32 v219, v172, v172\n"
      "v_mfma_f32_16x16x32_bf16 v[176:179], a[104:107], v[132:135], v[176:179]\n"
      "v_mfma_f32_16x16x32_bf16 v[176:179], a[108:111], v[136:139], v[176:179]\n"
      "v_mfma_f32_16x16x32_bf16 v[176:179], a[112:115], v[140:143], v[176:179]\n"
      "buffer_load_dwordx4 v0, s[8:11], s46 offen offset:0 sc1 lds\n"
      "v_mfma_f32_16x16x32_bf16 v[176:179], a[116:119], v[144:147], v[176:179]\n"
      "v_add_f32 v218, v172, v173\n"
      "v_fmac_f32 v219, v173, v173\n"
      "v_mfma_f32_16x16x32_bf16 v[176:179], a[120:123], v[148:151], v[176:179]\n"
      "s_waitcnt lgkmcnt(0)\n"
      "v_cmp_gt_i32 vcc, v21, v22\n"
      "v_mfma_f32_16x16x32_bf16 v[176:179], a[124:127], v[152:155], v[176:179]\n"
      "v_mfma_f32_16x16x32_bf16 v[176:179], a[128:131], v[156:159], v[176:179]\n"
      "buffer_load_dwordx4 v0, s[8:11], s46 offen offset:1024 sc1 lds\n"
      "v_mfma_f32_16x16x32_bf16 v[176:179], a[132:135], v[160:163], v[176:179]\n"
      "s_cbranch_vccnz .Lsp_wz0_5\n"
      ".Lbk_wz0_5:\n"
      "ds_read2st64_b32 v[204:205], v17 offset1:1\n"
      "v_mfma_f32_16x16x32_bf16 v[176:179], a[136:139], v[164:167], v[176:179]\n"
      "ds_read2st64_b32 v[206:207], v17 offset0:2 offset1:3\n"
      "v_add_u32 v21, 1, v21\n"
      "v_mfma_f32_16x16x32_bf16 v[176:179], a[140:143], v[168:171], v[176:179]\n"
      "s_waitcnt vmcnt(10)\n"
      "ds_write_b32 v23, v21\n"
      "v_mfma_f32_16x16x32_bf16 v[180:183], a[144:147], v[100:103], v[180:183]\n"
      "buffer_load_dwordx4 v0, s[8:11], s46 offen offset:3072 sc1 lds\n"
      "v_mfma_f32_16x16x32_bf16 v[180:183], a[148:151], v[104:107], v[180:183]\n"
      "v_add_f32 v218, v218, v174\n"
      "v_fmac_f32 v219, v174, v174\n"
      "v_mfma_f32_16x16x32_bf16 v[180:183], a[152:155], v[108:111], v[180:183]\n"
      "v_fmac_f32 v219, v175, v175\n"
      "v_fmac_f32 v219, v176, v176\n"
      "v_mfma_f32_16x16x32_bf16 v[180:183], a[156:159], v[112:115], v[180:183]\n"
      "v_add_f32 v218, v218, v175\n"
      "v_add_f32 v218, v218, v176\n"
      "v_mfma_f32_16x16x32_bf16 v[180:183], a[160:163], v[116:119], v[180:183]\n"
      "buffer_load_dwordx4 v0, s[8:11], s46 offen offset:2048 sc1 lds\n"
      "v_mfma_f32_16x16x32_bf16 v[180:183], a[164:167], v[120:123], v[180:183]\n"
      "s_add_u32 m0, s40, 40960\n"
      "v_add_f32 v218, v218, v177\n"
      "v_cvt_pk_bf16_f32 v209, v174, v175\n"
      "v_mfma_f32_16x16x32_bf16 v[180:183], a[168:171], v[124:127], v[180:183]\n"
      "s_waitcnt lgkmcnt(0)\n"
      "v_add_f32 v188, v188, v204\n"
      "v_add_f32 v190, v190, v206\n"
      "v_mfma_f32_16x16x32_bf16 v[180:183], a[172:175], v[128:131], v[180:183]\n"
      "v_cvt_pk_bf16_f32 v208, v172, v173\n"
      "v_fmac_f32 v219, v177, v177\n"
      "v_mfma_f32_16x16x32_bf16 v[180:183], a[176:179], v[132:135], v[180:183]\n"
      "buffer_load_dwordx4 v1, s[8:11], s46 offen offset:0 sc1 lds\n"
      "v_mfma_f32_16x16x32_bf16 v[180:183], a[180:183], v[136:139], v[180:183]\n"
      "s_add_u32 s38, s38, 24576\n"
      "v_cvt_pk_bf16_f32 v210, v176, v177\n"
      "v_mfma_f32_16x16x32_bf16 v[180:183], a[184:187], v[140:143], v[180:183]\n"
      "ds_read_b32 v22, v24\n"
      "v_add_f32 v218, v218, v178\n"
      "v_mfma_f32_16x16x32_bf16 v[180:183], a[188:191], v[144:147], v[180:183]\n"
      "v_add_f32 v218, v218, v179\n"
      "v_add_f32 v218, v218, v188\n"
      "v_mfma_f32_16x16x32_bf16 v[180:183], a[192:195], v[148:151], v[180:183]\n"
      "s_add_u32 s50, s50, 24576\n"
      "v_fmac_f32 v219, v178, v178\n"
      "v_fmac_f32 v219, v179, v179\n"
      "v_mfma_f32_16x16x32_bf16 v[180:183], a[196:199], v[152:155], v[180:183]\n"
      "v_add_f32 v191, v191, v207\n"
      "v_cvt_pk_bf16_f32 v211, v178, v179\n"
      "v_mfma_f32_16x16x32_bf16 v[180:183], a[200:203], v[156:159], v[180:183]\n"
      "v_fmac_f32 v219, v188, v188\n"
      "v_add_f32 v189, v189, v205\n"
      "v_mfma_f32_16x16x32_bf16 v[180:183], a[204:207], v[160:163], v[180:183]\n"
      "s_waitcnt lgkmcnt(0)\n"
      "v_fmac_f32 v219, v189, v189\n"
      "v_cmp_gt_i32 vcc, v21, v22\n"
      "v_mfma_f32_16x16x32_bf16 v[180:183], a[208:211], v[164:167], v[180:183]\n"
      "v_fmac_f32 v219, v190, v190\n"
      "v_add_f32 v218, v218, v189\n"
      "v_mfma_f32_16x16x32_bf16 v[180:183], a[212:215], v[168:171], v[180:183]\n"
      "s_waitcnt vmcnt(11)\n"
      "v_lshlrev_b32 v172, 16, v192\n"
      "v_mfma_f32_16x16x32_bf16 v[184:187], a[216:219], v[100:103], v[184:187]\n"
      "v_and_b32 v175, 0xffff0000, v193\n"
      "v_and_b32 v177, 0xffff0000, v194\n"
      "v_mfma_f32_16x16x32_bf16 v[184:187], a[220:223], v[104:107], v[184:187]\n"
      "s_cbranch_vccnz .Lsp_wy1_6\n"
      ".Lbk_wy1_6:\n"
      "v_add_f32 v218, v218, v190\n"
      "v_mfma_f32_16x16x32_bf16 v[184:187], a[224:227], v[108:111], v[184:187]\n"
      "ds_read_b128 v[104:107], v0 offset:21504\n"
      "v_cvt_pk_bf16_f32 v216, v188, v189\n"
      "v_mfma_f32_16x16x32_bf16 v[184:187], a[228:231], v[112:115], v[184:187]\n"
      "v_add_f32 v218, v218, v191\n"
      "v_add_f32 v218, v218, v180\n"
      "v_mfma_f32_16x16x32_bf16 v[184:187], a[232:235], v[116:119], v[184:187]\n"
      "ds_read_b128 v[116:119], v0 offset:24576\n"
      "v_cvt_pk_bf16_f32 v217, v190, v191\n"
      "v_mfma_f32_16x16x32_bf16 v[184:187], a[236:239], v[120:123], v[184:187]\n"
      "ds_read_b128 v[108:111], v0 offset:22528\n"
      "v_lshlrev_b32 v188, 16, v200\n"
      "v_mfma_f32_16x16x32_bf16 v[184:187], a[240:243], v[124:127], v[184:187]\n"
      "ds_read_b128 v[100:103], v0 offset:20480\n"
      "v_fmac_f32 v219, v191, v191\n"
      "v_mfma_f32_16x16x32_bf16 v[184:187], a[244:247], v[128:131], v[184:187]\n"
      "v_lshlrev_b32 v190, 16, v201\n"
      "v_and_b32 v191, 0xffff0000, v201\n"
      "v_mfma_f32_16x16x32_bf16 v[184:187], a[248:251], v[132:135], v[184:187]\n"
      "ds_read2_b32 v[134:135], v4 offset0:64 offset1:96\n"
      "v_lshlrev_b32 v174, 16, v193\n"
      "v_mfma_f32_16x16x32_bf16 v[184:187], a[252:255], v[136:139], v[184:187]\n"
      "s_sub_u32 s47, s46, s45\n"
      "v_and_b32 v189, 0xffff0000, v200\n"
      "v_fmac_f32 v219, v180, v180\n"
      "v_mfma_f32_16x16x32_bf16 v[184:187], v[32:35], v[140:143], v[184:187]\n"
      "buffer_store_dwordx4 v[208:211], v15, s[8:11], s38 offen sc1\n"
      "v_mfma_f32_16x16x32_bf16 v[184:187], v[36:39], v[144:147], v[184:187]\n"
      "ds_read_b128 v[136:139], v0 offset:28672\n"
      "v_fmac_f32 v219, v181, v181\n"
      "v_mfma_f32_16x16x32_bf16 v[184:187], v[40:43], v[148:151], v[184:187]\n"
      "ds_read2_b32 v[132:133], v4 offset0:0 offset1:32\n"
      "v_lshlrev_b32 v178, 16, v195\n"
      "v_mfma_f32_16x16x32_bf16 v[184:187], v[44:47], v[152:155], v[184:187]\n"
      "ds_read_b128 v[112:115], v0 offset:23552\n"
      "v_add_f32 v218, v218, v181\n"
      "v_mfma_f32_16x16x32_bf16 v[184:187], v[48:51], v[156:159], v[184:187]\n"
      "ds_read_b128 v[120:123], v0 offset:25600\n"
      "v_and_b32 v173, 0xffff0000, v192\n"
      "v_mfma_f32_16x16x32_bf16 v[184:187], v[52:55], v[160:163], v[184:187]\n"
      "ds_read_b128 v[124:127], v0 offset:26624\n"
      "v_lshlrev_b32 v176, 16, v194\n"
      "v_mfma_f32_16x16x32_bf16 v[184:187], v[56:59], v[164:167], v[184:187]\n"
      "buffer_load_dwordx2 v[200:201], v14, s[8:11], s47 offen sc1\n"
      "v_mfma_f32_16x16x32_bf16 v[184:187], v[60:63], v[168:171], v[184:187]\n"
      "s_waitcnt lgkmcnt(5)\n"
      "ds_read_b128 v[128:131], v0 offset:27648\n"
      "v_add_f32 v218, v218, v182\n"
      "v_mfma_f32_16x16x32_bf16 v[188:191], v[64:67], v[100:103], v[188:191]\n"
      "ds_read_b128 v[140:143], v0 offset:29696\n"
      "v_cvt_pk_bf16_f32 v213, v182, v183\n"
      "v_mfma_f32_16x16x32_bf16 v[188:191], v[68:71], v[104:107], v[188:191]\n"
      "ds_read_b128 v[152:155], v0 offset:32768\n"
      "v_mfma_f32_16x16x32_bf16 v[188:191], v[72:75], v[108:111], v[188:191]\n"
      "s_waitcnt lgkmcnt(5)\n"
      "buffer_store_dwordx2 v[216:217], v16, s[8:11], s38 offen sc1\n"
      "v_mfma_f32_16x16x32_bf16 v[188:191], v[76:79], v[112:115], v[188:191]\n"
      "ds_read_b128 v[144:147], v0 offset:30720\n"
      "v_cvt_pk_bf16_f32 v212, v180, v181\n"
      "v_mfma_f32_16x16x32_bf16 v[188:191], v[80:83], v[116:119], v[188:191]\n"
      "s_waitcnt lgkmcnt(4)\n"
      "ds_read_b128 v[164:167], v0 offset:35840\n"
      "v_add_f32 v218, v218, v183\n"
      "v_mfma_f32_16x16x32_bf16 v[188:191], v[84:87], v[120:123], v[188:191]\n"
      "v_fmac_f32 v219, v182, v182\n"
      "v_add_f32 v218, v218, v184\n"
      "v_mfma_f32_16x16x32_bf16 v[188:191], v[88:91], v[124:127], v[188:191]\n"
      "s_waitcnt lgkmcnt(4)\n"
      "ds_read_b128 v[148:151], v0 offset:31744\n"
      "v_add_f32 v218, v218, v185\n"
      "v_mfma_f32_16x16x32_bf16 v[188:191], v[92:95], v[128:131], v[188:191]\n"
      "ds_read_b128 v[160:163], v0 offset:34816\n"
      "v_and_b32 v179, 0xffff0000, v195\n"
      "v_mfma_f32_16x16x32_bf16 v[188:191], v[96:99], v[132:135], v[188:191]\n"
      "buffer_load_dwordx4 v[192:195], v13, s[8:11], s47 offen sc1\n"
      "v_mfma_f32_16x16x32_bf16 v[172:175], a[0:3], v[100:103], v[172:175]\n"
      "v_cvt_pk_bf16_f32 v215, v186, v187\n"
      "v_fmac_f32 v219, v183, v183\n"
      "v_mfma_f32_16x16x32_bf16 v[172:175], a[4:7], v[104:107], v[172:175]\n"
      "v_cvt_pk_bf16_f32 v214, v184, v185\n"
      "v_mfma_f32_16x16x32_bf16 v[172:175], a[8:11], v[108:111], v[172:175]\n"
      "ds_read2_b32 v[168:169], v5 offset0:0 offset1:32\n"
      "v_mfma_f32_16x16x32_bf16 v[172:175], a[12:15], v[112:115], v[172:175]\n"
      "ds_read_b128 v[156:159], v0 offset:33792\n"
      "v_mfma_f32_16x16x32_bf16 v[172:175], a[16:19], v[116:119], v[172:175]\n"
      "buffer_store_dwordx4 v[212:215], v15, s[8:11], s38 offen offset:1024 sc1\n"
      "v_mfma_f32_16x16x32_bf16 v[172:175], a[20:23], v[120:123], v[172:175]\n"
      "v_fmac_f32 v219, v184, v184\n"
      "v_fmac_f32 v219, v185, v185\n"
      "v_mfma_f32_16x16x32_bf16 v[172:175], a[24:27], v[124:127], v[172:175]\n"
      "ds_read2_b32 v[170:171], v5 offset0:64 offset1:96\n"
      "v_add_f32 v218, v218, v186\n"
      "v_mfma_f32_16x16x32_bf16 v[172:175], a[28:31], v[128:131], v[172:175]\n"
      "v_fmac_f32 v219, v186, v186\n"
      "v_add_f32 v218, v218, v187\n"
      "v_mfma_f32_16x16x32_bf16 v[172:175], a[32:35], v[132:135], v[172:175]\n"
      "ds_write_b32 v18, v218\n"
      "v_mfma_f32_16x16x32_bf16 v[172:175], a[36:39], v[136:139], v[172:175]\n"
      "s_waitcnt lgkmcnt(3)\n"
      "s_add_u32 m0, s39, 61440\n"
      "v_add_u32 v21, 1, v21\n"
      "v_mfma_f32_16x16x32_bf16 v[172:175], a[40:43], v[140:143], v[172:175]\n"
      "s_mov_b32 s46, s36\n"
      "v_mfma_f32_16x16x32_bf16 v[172:175], a[44:47], v[144:147], v[172:175]\n"
      "v_fmac_f32 v219, v187, v187\n"
      "v_mfma_f32_16x16x32_bf16 v[172:175], a[48:51], v[148:151], v[172:175]\n"
      "ds_write_b32 v18, v219 offset:4\n"
      "v_mfma_f32_16x16x32_bf16 v[172:175], a[52:55], v[152:155], v[172:175]\n"
      "s_waitcnt vmcnt(10) lgkmcnt(2)\n"
      "v_and_b32 v181, 0xffff0000, v196\n"
      "v_mfma_f32_16x16x32_bf16 v[172:175], a[56:59], v[156:159], v[172:175]\n"
      "ds_write_b32 v23, v21\n"
      "v_mfma_f32_16x16x32_bf16 v[172:175], a[60:63], v[160:163], v[172:175]\n"
      "v_lshlrev_b32 v186, 16, v199\n"
      "v_cmp_ne_u32 vcc, s21, v28\n"
      "v_mfma_f32_16x16x32_bf16 v[172:175], a[64:67], v[164:167], v[172:175]\n"
      "buffer_store_dword v25, v12, s[8:11], s50 offen sc1\n"
      "v_mfma_f32_16x16x32_bf16 v[172:175], a[68:71], v[168:171], v[172:175]\n"
      "s_cmp_eq_u32 s36, s37\n"
      "s_cselect_b32 s48, 49152, s51\n"
      "s_add_u32 s36, s36, s48\n"
      "v_mfma_f32_16x16x32_bf16 v[176:179], a[72:75], v[100:103], v[176:179]\n"
      "s_cbranch_vccnz .Lsp_w1_7\n"
      ".Lbk_w1_7:\n"
      "s_min_u32 s36, s36, s6\n"
      "v_mfma_f32_16x16x32_bf16 v[176:179], a[76:79], v[104:107], v[176:179]\n"
      "buffer_load_dword v28, v11, s[8:11], s36 offen sc1\n"
      "v_mfma_f32_16x16x32_bf16 v[176:179], a[80:83], v[108:111], v[176:179]\n"
      "v_and_b32 v183, 0xffff0000, v197\n"
      "v_mul_f32 v219, v172, v172\n"
      "v_mfma_f32_16x16x32_bf16 v[176:179], a[84:87], v[112:115], v[176:179]\n"
      "buffer_load_dwordx4 v0, s[8:11], s46 offen offset:0 sc1 lds\n"
      "v_mfma_f32_16x16x32_bf16 v[176:179], a[88:91], v[116:119], v[176:179]\n"
      "v_mfma_f32_16x16x32_bf16 v[176:179], a[92:95], v[120:123], v[176:179]\n"
      "v_fmac_f32 v219, v173, v173\n"
      "v_mfma_f32_16x16x32_bf16 v[176:179], a[96:99], v[124:127], v[176:179]\n"
      "v_lshlrev_b32 v184, 16, v198\n"
      "v_and_b32 v185, 0xffff0000, v198\n"
      "v_mfma_f32_16x16x32_bf16 v[176:179], a[100:103], v[128:131], v[176:179]\n"
      "ds_read_b32 v22, v24\n"
      "v_fmac_f32 v219, v174, v174\n"
      "v_mfma_f32_16x16x32_bf16 v[176:179], a[104:107], v[132:135], v[176:179]\n"
      "buffer_load_dwordx4 v0, s[8:11], s46 offen offset:1024 sc1 lds\n"
      "v_mfma_f32_16x16x32_bf16 v[176:179], a[108:111], v[136:139], v[176:179]\n"
      "v_lshlrev_b32 v182, 16, v197\n"
      "v_add_f32 v218, v172, v173\n"
      "v_mfma_f32_16x16x32_bf16 v[176:179], a[112:115], v[140:143], v[176:179]\n"
      "v_add_f32 v218, v218, v174\n"
      "v_fmac_f32 v219, v175, v175\n"
      "v_mfma_f32_16x16x32_bf16 v[176:179], a[116:119], v[144:147], v[176:179]\n"
      "v_cvt_pk_bf16_f32 v208, v172, v173\n"
      "v_mfma_f32_16x16x32_bf16 v[176:179], a[120:123], v[148:151], v[176:179]\n"
      "s_waitcnt lgkmcnt(0)\n"
      "v_and_b32 v187, 0xffff0000, v199\n"
      "v_cmp_gt_i32 vcc, v21, v22\n"
      "v_mfma_f32_16x16x32_bf16 v[176:179], a[124:127], v[152:155], v[176:179]\n"
      "buffer_load_dwordx4 v0, s[8:11], s46 offen offset:3072 sc1 lds\n"
      "v_mfma_f32_16x16x32_bf16 v[176:179], a[128:131], v[156:159], v[176:179]\n"
      "v_lshlrev_b32 v180, 16, v196\n"
      "v_add_f32 v218, v218, v175\n"
      "v_mfma_f32_16x16x32_bf16 v[176:179], a[132:135], v[160:163], v[176:179]\n"
      "s_cbranch_vccnz .Lsp_wz1_8\n"
      ".Lbk_wz1_8:\n"
      "ds_read2st64_b32 v[204:205], v17 offset1:1\n"
      "v_mfma_f32_16x16x32_bf16 v[176:179], a[136:139], v[164:167], v[176:179]\n"
      "ds_read2st64_b32 v[206:207], v17 offset0:2 offset1:3\n"
      "v_add_u32 v21, 1, v21\n"
      "v_mfma_f32_16x16x32_bf16 v[176:179], a[140:143], v[168:171], v[176:179]\n"
      "s_waitcnt vmcnt(10)\n"
      "ds_write_b32 v23, v21\n"
      "v_mfma_f32_16x16x32_bf16 v[180:183], a[144:147], v[100:103], v[180:183]\n"
      "buffer_load_dwordx4 v0, s[8:11], s46 offen offset:2048 sc1 lds\n"
      "v_mfma_f32_16x16x32_bf16 v[180:183], a[148:151], v[104:107], v[180:183]\n"
      "v_mfma_f32_16x16x32_bf16 v[180:183], a[152:155], v[108:111], v[180:183]\n"
      "s_add_u32 m0, s40, 61440\n"
      "v_add_f32 v218, v218, v176\n"
      "v_mfma_f32_16x16x32_bf16 v[180:183], a[156:159], v[112:115], v[180:183]\n"
      "v_fmac_f32 v219, v176, v176\n"
      "v_cvt_pk_bf16_f32 v209, v174, v175\n"
      "v_mfma_f32_16x16x32_bf16 v[180:183], a[160:163], v[116:119], v[180:183]\n"
      "buffer_load_dwordx4 v[196:199], v13, s[8:11], s47 offen offset:1024 sc1\n"
      "v_mfma_f32_16x16x32_bf16 v[180:183], a[164:167], v[120:123], v[180:183]\n"
      "s_waitcnt lgkmcnt(1)\n"
      "v_cvt_pk_bf16_f32 v211, v178, v179\n"
      "v_add_f32 v190, v190, v206\n"
      "v_mfma_f32_16x16x32_bf16 v[180:183], a[168:171], v[124:127], v[180:183]\n"
      "v_add_f32 v218, v218, v177\n"
      "v_add_f32 v218, v218, v178\n"
      "v_mfma_f32_16x16x32_bf16 v[180:183], a[172:175], v[128:131], v[180:183]\n"
      "v_add_f32 v218, v218, v179\n"
      "v_add_f32 v189, v189, v205\n"
      "v_mfma_f32_16x16x32_bf16 v[180:183], a[176:179], v[132:135], v[180:183]\n"
      "buffer_load_dwordx4 v1, s[8:11], s46 offen offset:0 sc1 lds\n"
      "v_mfma_f32_16x16x32_bf16 v[180:183], a[180:183], v[136:139], v[180:183]\n"
      "v_add_f32 v188, v188, v204\n"
      "v_fmac_f32 v219, v177, v177\n"
      "v_mfma_f32_16x16x32_bf16 v[180:183], a[184:187], v[140:143], v[180:183]\n"
      "ds_read_b32 v22, v24\n"
      "v_fmac_f32 v219, v178, v178\n"
      "v_mfma_f32_16x16x32_bf16 v[180:183], a[188:191], v[144:147], v[180:183]\n"
      "v_cvt_pk_bf16_f32 v210, v176, v177\n"
      "v_fmac_f32 v219, v179, v179\n"
      "v_mfma_f32_16x16x32_bf16 v[180:183], a[192:195], v[148:151], v[180:183]\n"
      "v_add_f32 v191, v191, v207\n"
      "v_fmac_f32 v219, v188, v188\n"
      "v_mfma_f32_16x16x32_bf16 v[180:183], a[196:199], v[152:155], v[180:183]\n"
      "v_add_f32 v218, v218, v188\n"
      "v_mfma_f32_16x16x32_bf16 v[180:183], a[200:203], v[156:159], v[180:183]\n"
      "v_add_f32 v218, v218, v189\n"
      "v_add_f32 v218, v218, v190\n"
      "v_mfma_f32_16x16x32_bf16 v[180:183], a[204:207], v[160:163], v[180:183]\n"
      "s_waitcnt lgkmcnt(0)\n"
      "v_fmac_f32 v219, v189, v189\n"
      "v_cmp_gt_i32 vcc, v21, v22\n"
      "v_mfma_f32_16x16x32_bf16 v[180:183], a[208:211], v[164:167], v[180:183]\n"
      "v_cvt_pk_bf16_f32 v216, v188, v189\n"
      "v_fmac_f32 v219, v190, v190\n"
      "v_mfma_f32_16x16x32_bf16 v[180:183], a[212:215], v[168:171], v[180:183]\n"
      "s_waitcnt vmcnt(11)\n"
      "v_and_b32 v189, 0xffff0000, v200\n"
      "v_fmac_f32 v219, v191, v191\n"
      "v_mfma_f32_16x16x32_bf16 v[184:187], a[216:219], v[100:103], v[184:187]\n"
      "s_add_u32 s38, s38, 24576\n"
      "v_cvt_pk_bf16_f32 v217, v190, v191\n"
      "v_lshlrev_b32_e64 v188, 16, v200\n"
      "v_mfma_f32_16x16x32_bf16 v[184:187], a[220:223], v[104:107], v[184:187]\n"
      "s_cbranch_vccnz .Lsp_wy0_9\n"
      ".Lbk_wy0_9:\n"
      "v_mfma_f32_16x16x32_bf16 v[184:187], a[224:227], v[108:111], v[184:187]\n"
      "ds_read_b128 v[104:107], v10 offset:1024\n"
      "v_add_f32 v218, v218, v191\n"
      "v_mfma_f32_16x16x32_bf16 v[184:187], a[228:231], v[112:115], v[184:187]\n"
      "v_fmac_f32 v219, v180, v180\n"
      "v_lshlrev_b32 v190, 16, v201\n"
      "v_mfma_f32_16x16x32_bf16 v[184:187], a[232:235], v[116:119], v[184:187]\n"
      "ds_read_b128 v[100:103], v10 offset:0\n"
      "v_add_f32 v218, v218, v180\n"
      "v_mfma_f32_16x16x32_bf16 v[184:187], a[236:239], v[120:123], v[184:187]\n"
      "s_waitcnt vmcnt(9)\n"
      "ds_read_b128 v[120:123], v10 offset:5120\n"
      "v_lshlrev_b32 v178, 16, v195\n"
      "v_mfma_f32_16x16x32_bf16 v[184:187], a[240:243], v[124:127], v[184:187]\n"
      "ds_read_b128 v[112:115], v10 offset:3072\n"
      "v_fmac_f32 v219, v181, v181\n"
      "v_mfma_f32_16x16x32_bf16 v[184:187], a[244:247], v[128:131], v[184:187]\n"
      "ds_read_b128 v[116:119], v10 offset:4096\n"
      "v_lshlrev_b32 v174, 16, v193\n"
      "v_mfma_f32_16x16x32_bf16 v[184:187], a[248:251], v[132:135], v[184:187]\n"
      "ds_read_b128 v[124:127], v10 offset:6144\n"
      "v_fmac_f32 v219, v182, v182\n"
      "v_mfma_f32_16x16x32_bf16 v[184:187], a[252:255], v[136:139], v[184:187]\n"
      "s_sub_u32 s47, s46, s45\n"
      "ds_read_b128 v[108:111], v10 offset:2048\n"
      "v_mfma_f32_16x16x32_bf16 v[184:187], v[32:35], v[140:143], v[184:187]\n"
      "v_and_b32 v191, 0xffff0000, v201\n"
      "v_and_b32 v177, 0xffff0000, v194\n"
      "v_mfma_f32_16x16x32_bf16 v[184:187], v[36:39], v[144:147], v[184:187]\n"
      "ds_read_b128 v[136:139], v10 offset:8192\n"
      "v_lshlrev_b32 v176, 16, v194\n"
      "v_mfma_f32_16x16x32_bf16 v[184:187], v[40:43], v[148:151], v[184:187]\n"
      "ds_read_b128 v[128:131], v10 offset:7168\n"
      "v_lshlrev_b32 v172, 16, v192\n"
      "v_mfma_f32_16x16x32_bf16 v[184:187], v[44:47], v[152:155], v[184:187]\n"
      "v_fmac_f32 v219, v183, v183\n"
      "v_and_b32 v175, 0xffff0000, v193\n"
      "v_mfma_f32_16x16x32_bf16 v[184:187], v[48:51], v[156:159], v[184:187]\n"
      "buffer_load_dwordx2 v[200:201], v14, s[8:11], s47 offen sc1\n"
      "v_mfma_f32_16x16x32_bf16 v[184:187], v[52:55], v[160:163], v[184:187]\n"
      "ds_read2_b32 v[132:133], v6 offset0:0 offset1:32\n"
      "v_and_b32 v173, 0xffff0000, v192\n"
      "v_mfma_f32_16x16x32_bf16 v[184:187], v[56:59], v[164:167], v[184:187]\n"
      "s_waitcnt lgkmcnt(3)\n"
      "ds_read2_b32 v[134:135], v6 offset0:64 offset1:96\n"
      "v_add_f32 v218, v218, v181\n"
      "v_mfma_f32_16x16x32_bf16 v[184:187], v[60:63], v[168:171], v[184:187]\n"
      "s_sub_u32 s20, s20, 1\n"
      "s_cbranch_scc1 .Lw_tail\n"
      "v_mfma_f32_16x16x32_bf16 v[188:191], v[64:67], v[100:103], v[188:191]\n"
      "ds_read_b128 v[140:143], v10 offset:9216\n"
      "v_and_b32 v179, 0xffff0000, v195\n"
      "v_mfma_f32_16x16x32_bf16 v[188:191], v[68:71], v[104:107], v[188:191]\n"
      "buffer_load_dwordx4 v[192:195], v13, s[8:11], s47 offen sc1\n"
      "v_mfma_f32_16x16x32_bf16 v[188:191], v[72:75], v[108:111], v[188:191]\n"
      "v_add_f32 v218, v218, v182\n"
      "v_mfma_f32_16x16x32_bf16 v[188:191], v[76:79], v[112:115], v[188:191]\n"
      "ds_read_b128 v[156:159], v10 offset:13312\n"
      "v_fmac_f32 v219, v184, v184\n"
      "v_mfma_f32_16x16x32_bf16 v[188:191], v[80:83], v[116:119], v[188:191]\n"
      "ds_read_b128 v[164:167], v10 offset:15360\n"
      "v_cvt_pk_bf16_f32 v213, v182, v183\n"
      "v_mfma_f32_16x16x32_bf16 v[188:191], v[84:87], v[120:123], v[188:191]\n"
      "ds_read_b128 v[144:147], v10 offset:10240\n"
      "v_mfma_f32_16x16x32_bf16 v[188:191], v[88:91], v[124:127], v[188:191]\n"
      "s_waitcnt lgkmcnt(4)\n"
      "v_cvt_pk_bf16_f32 v215, v186, v187\n"
      "v_add_f32 v218, v218, v183\n"
      "v_mfma_f32_16x16x32_bf16 v[188:191], v[92:95], v[128:131], v[188:191]\n"
      "buffer_store_dwordx4 v[208:211], v15, s[8:11], s38 offen sc1\n"
      "v_mfma_f32_16x16x32_bf16 v[188:191], v[96:99], v[132:135], v[188:191]\n"
      "v_cvt_pk_bf16_f32 v212, v180, v181\n"
      "v_add_f32 v218, v218, v184\n"
      "v_mfma_f32_16x16x32_bf16 v[172:175], a[0:3], v[100:103], v[172:175]\n"
      "s_add_u32 s50, s50, 24576\n"
      "v_cvt_pk_bf16_f32 v214, v184, v185\n"
      "v_add_f32 v218, v218, v185\n"
      "v_mfma_f32_16x16x32_bf16 v[172:175], a[4:7], v[104:107], v[172:175]\n"
      "s_mov_b32 s46, s36\n"
      "v_add_f32 v218, v218, v186\n"
      "v_mfma_f32_16x16x32_bf16 v[172:175], a[8:11], v[108:111], v[172:175]\n"
      "ds_read_b128 v[148:151], v10 offset:11264\n"
      "v_add_f32 v218, v218, v187\n"
      "v_mfma_f32_16x16x32_bf16 v[172:175], a[12:15], v[112:115], v[172:175]\n"
      "v_fmac_f32 v219, v185, v185\n"
      "v_add_u32 v21, 1, v21\n"
      "v_mfma_f32_16x16x32_bf16 v[172:175], a[16:19], v[116:119], v[172:175]\n"
      "s_add_u32 s36, s36, 24576\n"
      "ds_read_b128 v[152:155], v10 offset:12288\n"
      "v_fmac_f32 v219, v186, v186\n"
      "v_mfma_f32_16x16x32_bf16 v[172:175], a[20:23], v[120:123], v[172:175]\n"
      "buffer_store_dwordx2 v[216:217], v16, s[8:11], s38 offen sc1\n"
      "v_mfma_f32_16x16x32_bf16 v[172:175], a[24:27], v[124:127], v[172:175]\n"
      "ds_read_b128 v[160:163], v10 offset:14336\n"
      "v_mfma_f32_16x16x32_bf16 v[172:175], a[28:31], v[128:131], v[172:175]\n"
      "ds_read2_b32 v[170:171], v7 offset0:64 offset1:96\n"
      "v_mfma_f32_16x16x32_bf16 v[172:175], a[32:35], v[132:135], v[172:175]\n"
      "ds_read2_b32 v[168:169], v7 offset0:0 offset1:32\n"
      "s_min_u32 s36, s36, s6\n"
      "v_mfma_f32_16x16x32_bf16 v[172:175], a[36:39], v[136:139], v[172:175]\n"
      "s_waitcnt lgkmcnt(4)\n"
      "v_mfma_f32_16x16x32_bf16 v[172:175], a[40:43], v[140:143], v[172:175]\n"
      "buffer_store_dwordx4 v[212:215], v15, s[8:11], s38 offen offset:1024 sc1\n"
      "v_mfma_f32_16x16x32_bf16 v[172:175], a[44:47], v[144:147], v[172:175]\n"
      "ds_write_b32 v18, v218\n"
      "v_mfma_f32_16x16x32_bf16 v[172:175], a[48:51], v[148:151], v[172:175]\n"
      "s_waitcnt vmcnt(11) lgkmcnt(4)\n"
      "v_fmac_f32 v219, v187, v187\n"
      "v_cmp_ne_u32 vcc, s21, v28\n"
      "v_mfma_f32_16x16x32_bf16 v[172:175], a[52:55], v[152:155], v[172:175]\n"
      "ds_write_b32 v18, v219 offset:4\n"
      "v_mfma_f32_16x16x32_bf16 v[172:175], a[56:59], v[156:159], v[172:175]\n"
      "s_waitcnt lgkmcnt(3)\n"
      "s_add_u32 m0, s39, 0\n"
      "v_mfma_f32_16x16x32_bf16 v[172:175], a[60:63], v[160:163], v[172:175]\n"
      "ds_write_b32 v23, v21\n"
      "v_mfma_f32_16x16x32_bf16 v[172:175], a[64:67], v[164:167], v[172:175]\n"
      "s_waitcnt lgkmcnt(3)\n"
      "s_cbranch_vccnz .Lsp_w0_10\n"
      ".Lbk_w0_10:\n"
      "v_mfma_f32_16x16x32_bf16 v[172:175], a[68:71], v[168:171], v[172:175]\n"
      "v_mfma_f32_16x16x32_bf16 v[176:179], a[72:75], v[100:103], v[176:179]\n"
      "buffer_load_dword v28, v11, s[8:11], s36 offen sc1\n"
      "v_mfma_f32_16x16x32_bf16 v[176:179], a[76:79], v[104:107], v[176:179]\n"
      "s_waitcnt vmcnt(7) lgkmcnt(2)\n"
      "v_lshlrev_b32 v186, 16, v199\n"
      "v_lshlrev_b32 v184, 16, v198\n"
      "v_mfma_f32_16x16x32_bf16 v[176:179], a[80:83], v[108:111], v[176:179]\n"
      "v_lshlrev_b32 v180, 16, v196\n"
      "v_and_b32 v183, 0xffff0000, v197\n"
      "v_mfma_f32_16x16x32_bf16 v[176:179], a[84:87], v[112:115], v[176:179]\n"
      "buffer_store_dword v25, v12, s[8:11], s50 offen sc1\n"
      "v_mfma_f32_16x16x32_bf16 v[176:179], a[88:91], v[116:119], v[176:179]\n"
      "v_lshlrev_b32 v182, 16, v197\n"
      "v_and_b32 v181, 0xffff0000, v196\n"
      "v_mfma_f32_16x16x32_bf16 v[176:179], a[92:95], v[120:123], v[176:179]\n"
      "v_and_b32 v187, 0xffff0000, v199\n"
      "v_and_b32 v185, 0xffff0000, v198\n"
      "v_mfma_f32_16x16x32_bf16 v[176:179], a[96:99], v[124:127], v[176:179]\n"
      "buffer_load_dwordx4 v[196:199], v13, s[8:11], s47 offen offset:1024 sc1\n"
      "v_mfma_f32_16x16x32_bf16 v[176:179], a[100:103], v[128:131], v[176:179]\n"
      "ds_read_b32 v22, v24\n"
      "v_mul_f32 v219, v172, v172\n"
      "v_mfma_f32_16x16x32_bf16 v[176:179], a[104:107], v[132:135], v[176:179]\n"
      "v_mfma_f32_16x16x32_bf16 v[176:179], a[108:111], v[136:139], v[176:179]\n"
      "v_mfma_f32_16x16x32_bf16 v[176:179], a[112:115], v[140:143], v[176:179]\n"
      "buffer_load_dwordx4 v0, s[8:11], s46 offen offset:0 sc1 lds\n"
      "v_mfma_f32_16x16x32_bf16 v[176:179], a[116:119], v[144:147], v[176:179]\n"
      "v_add_f32 v218, v172, v173\n"
      "v_fmac_f32 v219, v173, v173\n"
      "v_mfma_f32_16x16x32_bf16 v[176:179], a[120:123], v[148:151], v[176:179]\n"
      "s_waitcnt lgkmcnt(0)\n"
      "v_cmp_gt_i32 vcc, v21, v22\n"
      "v_mfma_f32_16x16x32_bf16 v[176:179], a[124:127], v[152:155], v[176:179]\n"
      "v_mfma_f32_16x16x32_bf16 v[176:179], a[128:131], v[156:159], v[176:179]\n"
      "buffer_load_dwordx4 v0, s[8:11], s46 offen offset:1024 sc1 lds\n"
      "v_mfma_f32_16x16x32_bf16 v[176:179], a[132:135], v[160:163], v[176:179]\n"
      "s_cbranch_vccnz .Lsp_wz0_11\n"
      ".Lbk_wz0_11:\n"
      "ds_read2st64_b32 v[204:205], v17 offset1:1\n"
      "v_mfma_f32_16x16x32_bf16 v[176:179], a[136:139], v[164:167], v[176:179]\n"
      "ds_read2st64_b32 v[206:207], v17 offset0:2 offset1:3\n"
      "v_add_u32 v21, 1, v21\n"
      "v_mfma_f32_16x16x32_bf16 v[176:179], a[140:143], v[168:171], v[176:179]\n"
      "s_waitcnt vmcnt(10)\n"
      "ds_write_b32 v23, v21\n"
      "v_mfma_f32_16x16x32_bf16 v[180:183], a[144:147], v[100:103], v[180:183]\n"
      "buffer_load_dwordx4 v0, s[8:11], s46 offen offset:3072 sc1 lds\n"
      "v_mfma_f32_16x16x32_bf16 v[180:183], a[148:151], v[104:107], v[180:183]\n"
      "v_add_f32 v218, v218, v174\n"
      "v_fmac_f32 v219, v174, v174\n"
      "v_mfma_f32_16x16x32_bf16 v[180:183], a[152:155], v[108:111], v[180:183]\n"
      "v_fmac_f32 v219, v175, v175\n"
      "v_fmac_f32 v219, v176, v176\n"
      "v_mfma_f32_16x16x32_bf16 v[180:183], a[156:159], v[112:115], v[180:183]\n"
      "v_add_f32 v218, v218, v175\n"
      "v_add_f32 v218, v218, v176\n"
      "v_mfma_f32_16x16x32_bf16 v[180:183], a[160:163], v[116:119], v[180:183]\n"
      "buffer_load_dwordx4 v0, s[8:11], s46 offen offset:2048 sc1 lds\n"
      "v_mfma_f32_16x16x32_bf16 v[180:183], a[164:167], v[120:123], v[180:183]\n"
      "s_add_u32 m0, s40, 0\n"
      "v_add_f32 v218, v218, v177\n"
      "v_cvt_pk_bf16_f32 v209, v174, v175\n"
      "v_mfma_f32_16x16x32_bf16 v[180:183], a[168:171], v[124:127], v[180:183]\n"
      "s_waitcnt lgkmcnt(0)\n"
      "v_add_f32 v188, v188, v204\n"
      "v_add_f32 v190, v190, v206\n"
      "v_mfma_f32_16x16x32_bf16 v[180:183], a[172:175], v[128:131], v[180:183]\n"
      "v_cvt_pk_bf16_f32 v208, v172, v173\n"
      "v_fmac_f32 v219, v177, v177\n"
      "v_mfma_f32_16x16x32_bf16 v[180:183], a[176:179], v[132:135], v[180:183]\n"
      "buffer_load_dwordx4 v1, s[8:11], s46 offen offset:0 sc1 lds\n"
      "v_mfma_f32_16x16x32_bf16 v[180:183], a[180:183], v[136:139], v[180:183]\n"
      "s_add_u32 s38, s38, 24576\n"
      "v_cvt_pk_bf16_f32 v210, v176, v177\n"
      "v_mfma_f32_16x16x32_bf16 v[180:183], a[184:187], v[140:143], v[180:183]\n"
      "ds_read_b32 v22, v24\n"
      "v_add_f32 v218, v218, v178\n"
      "v_mfma_f32_16x16x32_bf16 v[180:183], a[188:191], v[144:147], v[180:183]\n"
      "v_add_f32 v218, v218, v179\n"
      "v_add_f32 v218, v218, v188\n"
      "v_mfma_f32_16x16x32_bf16 v[180:183], a[192:195], v[148:151], v[180:183]\n"
      "s_add_u32 s50, s50, 24576\n"
      "v_fmac_f32 v219, v178, v178\n"
      "v_fmac_f32 v219, v179, v179\n"
      "v_mfma_f32_16x16x32_bf16 v[180:183], a[196:199], v[152:155], v[180:183]\n"
      "v_add_f32 v191, v191, v207\n"
      "v_cvt_pk_bf16_f32 v211, v178, v179\n"
      "v_mfma_f32_16x16x32_bf16 v[180:183], a[200:203], v[156:159], v[180:183]\n"
      "v_fmac_f32 v219, v188, v188\n"
      "v_add_f32 v189, v189, v205\n"
      "v_mfma_f32_16x16x32_bf16 v[180:183], a[204:207], v[160:163], v[180:183]\n"
      "s_waitcnt lgkmcnt(0)\n"
      "v_fmac_f32 v219, v189, v189\n"
      "v_cmp_gt_i32 vcc, v21, v22\n"
      "v_mfma_f32_16x16x32_bf16 v[180:183], a[208:211], v[164:167], v[180:183]\n"
      ".Lw_p1:\n"
      "s_cbranch_execz .Lw_pr\n"
      "v_fmac_f32 v219, v190, v190\n"
      "v_add_f32 v218, v218, v189\n"
      "v_mfma_f32_16x16x32_bf16 v[180:183], a[212:215], v[168:171], v[180:183]\n"
      "s_waitcnt vmcnt(11)\n"
      "v_lshlrev_b32 v172, 16, v192\n"
      "v_mfma_f32_16x16x32_bf16 v[184:187], a[216:219], v[100:103], v[184:187]\n"
      "v_and_b32 v175, 0xffff0000, v193\n"
      "v_and_b32 v177, 0xffff0000, v194\n"
      "v_mfma_f32_16x16x32_bf16 v[184:187], a[220:223], v[104:107], v[184:187]\n"
      "s_cbranch_vccnz .Lsp_wy1_12\n"
      ".Lbk_wy1_12:\n"
      "v_add_f32 v218, v218, v190\n"
      "v_mfma_f32_16x16x32_bf16 v[184:187], a[224:227], v[108:111], v[184:187]\n"
      "ds_read_b128 v[104:107], v10 offset:21504\n"
      "v_cvt_pk_bf16_f32 v216, v188, v189\n"
      "v_mfma_f32_16x16x32_bf16 v[184:187], a[228:231], v[112:115], v[184:187]\n"
      "v_add_f32 v218, v218, v191\n"
      "v_add_f32 v218, v218, v180\n"
      "v_mfma_f32_16x16x32_bf16 v[184:187], a[232:235], v[116:119], v[184:187]\n"
      "ds_read_b128 v[116:119], v10 offset:24576\n"
      "v_cvt_pk_bf16_f32 v217, v190, v191\n"
      "v_mfma_f32_16x16x32_bf16 v[184:187], a[236:239], v[120:123], v[184:187]\n"
      "ds_read_b128 v[108:111], v10 offset:22528\n"
      "v_lshlrev_b32 v188, 16, v200\n"
      "v_mfma_f32_16x16x32_bf16 v[184:187], a[240:243], v[124:127], v[184:187]\n"
      "ds_read_b128 v[100:103], v10 offset:20480\n"
      "v_fmac_f32 v219, v191, v191\n"
      "v_mfma_f32_16x16x32_bf16 v[184:187], a[244:247], v[128:131], v[184:187]\n"
      "v_lshlrev_b32 v190, 16, v201\n"
      "v_and_b32 v191, 0xffff0000, v201\n"
      "v_mfma_f32_16x16x32_bf16 v[184:187], a[248:251], v[132:135], v[184:187]\n"
      "ds_read2_b32 v[134:135], v8 offset0:64 offset1:96\n"
      "v_lshlrev_b32 v174, 16, v193\n"
      "v_mfma_f32_16x16x32_bf16 v[184:187], a[252:255], v[136:139], v[184:187]\n"
      "s_sub_u32 s47, s46, s45\n"
      "v_and_b32 v189, 0xffff0000, v200\n"
      "v_fmac_f32 v219, v180, v180\n"
      "v_mfma_f32_16x16x32_bf16 v[184:187], v[32:35], v[140:143], v[184:187]\n"
      "buffer_store_dwordx4 v[208:211], v15, s[8:11], s38 offen sc1\n"
      "v_mfma_f32_16x16x32_bf16 v[184:187], v[36:39], v[144:147], v[184:187]\n"
      "ds_read_b128 v[136:139], v10 offset:28672\n"
      "v_fmac_f32 v219, v181, v181\n"
      "v_mfma_f32_16x16x32_bf16 v[184:187], v[40:43], v[148:151], v[184:187]\n"
      "ds_read2_b32 v[132:133], v8 offset0:0 offset1:32\n"
      "v_lshlrev_b32 v178, 16, v195\n"
      "v_mfma_f32_16x16x32_bf16 v[184:187], v[44:47], v[152:155], v[184:187]\n"
      "ds_read_b128 v[112:115], v10 offset:23552\n"
      "v_add_f32 v218, v218, v181\n"
      "v_mfma_f32_16x16x32_bf16 v[184:187], v[48:51], v[156:159], v[184:187]\n"
      "ds_read_b128 v[120:123], v10 offset:25600\n"
      "v_and_b32 v173, 0xffff0000, v192\n"
      "v_mfma_f32_16x16x32_bf16 v[184:187], v[52:55], v[160:163], v[184:187]\n"
      "ds_read_b128 v[124:127], v10 offset:26624\n"
      "v_lshlrev_b32 v176, 16, v194\n"
      "v_mfma_f32_16x16x32_bf16 v[184:187], v[56:59], v[164:167], v[184:187]\n"
      "buffer_load_dwordx2 v[200:201], v14, s[8:11], s47 offen sc1\n"
      "v_mfma_f32_16x16x32_bf16 v[184:187], v[60:63], v[168:171], v[184:187]\n"
      "s_waitcnt lgkmcnt(5)\n"
      "ds_read_b128 v[128:131], v10 offset:27648\n"
      "v_add_f32 v218, v218, v182\n"
      "v_mfma_f32_16x16x32_bf16 v[188:191], v[64:67], v[100:103], v[188:191]\n"
      "ds_read_b128 v[140:143], v10 offset:29696\n"
      "v_cvt_pk_bf16_f32 v213, v182, v183\n"
      "v_mfma_f32_16x16x32_bf16 v[188:191], v[68:71], v[104:107], v[188:191]\n"
      "ds_read_b128 v[152:155], v10 offset:32768\n"
      "v_mfma_f32_16x16x32_bf16 v[188:191], v[72:75], v[108:111], v[188:191]\n"
      "s_waitcnt lgkmcnt(5)\n"
      "buffer_store_dwordx2 v[216:217], v16, s[8:11], s38 offen sc1\n"
      "v_mfma_f32_16x16x32_bf16 v[188:191], v[76:79], v[112:115], v[188:191]\n"
      "ds_read_b128 v[144:147], v10 offset:30720\n"
      "v_cvt_pk_bf16_f32 v212, v180, v181\n"
      "v_mfma_f32_16x16x32_bf16 v[188:191], v[80:83], v[116:119], v[188:191]\n"
      "s_waitcnt lgkmcnt(4)\n"
      "ds_read_b128 v[164:167], v10 offset:35840\n"
      "v_add_f32 v218, v218, v183\n"
      "v_mfma_f32_16x16x32_bf16 v[188:191], v[84:87], v[120:123], v[188:191]\n"
      "v_fmac_f32 v219, v182, v182\n"
      "v_add_f32 v218, v218, v184\n"
      "v_mfma_f32_16x16x32_bf16 v[188:191], v[88:91], v[124:127], v[188:191]\n"
      "s_waitcnt lgkmcnt(4)\n"
      "ds_read_b128 v[148:151], v10 offset:31744\n"
      "v_add_f32 v218, v218, v185\n"
      "v_mfma_f32_16x16x32_bf16 v[188:191], v[92:95], v[128:131], v[188:191]\n"
      "ds_read_b128 v[160:163], v10 offset:34816\n"
      "v_and_b32 v179, 0xffff0000, v195\n"
      "v_mfma_f32_16x16x32_bf16 v[188:191], v[96:99], v[132:135], v[188:191]\n"
      "buffer_load_dwordx4 v[192:195], v13, s[8:11], s47 offen sc1\n"
      "v_mfma_f32_16x16x32_bf16 v[172:175], a[0:3], v[100:103], v[172:175]\n"
      "v_cvt_pk_bf16_f32 v215, v186, v187\n"
      "v_fmac_f32 v219, v183, v183\n"
      "v_mfma_f32_16x16x32_bf16 v[172:175], a[4:7], v[104:107], v[172:175]\n"
      "v_cvt_pk_bf16_f32 v214, v184, v185\n"
      "v_mfma_f32_16x16x32_bf16 v[172:175], a[8:11], v[108:111], v[172:175]\n"
      "ds_read2_b32 v[168:169], v9 offset0:0 offset1:32\n"
      "v_mfma_f32_16x16x32_bf16 v[172:175], a[12:15], v[112:115], v[172:175]\n"
      "ds_read_b128 v[156:159], v10 offset:33792\n"
      "v_mfma_f32_16x16x32_bf16 v[172:175], a[16:19], v[116:119], v[172:175]\n"
      "buffer_store_dwordx4 v[212:215], v15, s[8:11], s38 offen offset:1024 sc1\n"
      "v_mfma_f32_16x16x32_bf16 v[172:175], a[20:23], v[120:123], v[172:175]\n"
      "v_fmac_f32 v219, v184, v184\n"
      "v_fmac_f32 v219, v185, v185\n"
      "v_mfma_f32_16x16x32_bf16 v[172:175], a[24:27], v[124:127], v[172:175]\n"
      "ds_read2_b32 v[170:171], v9 offset0:64 offset1:96\n"
      "v_add_f32 v218, v218, v186\n"
      "v_mfma_f32_16x16x32_bf16 v[172:175], a[28:31], v[128:131], v[172:175]\n"
      "v_fmac_f32 v219, v186, v186\n"
      "v_add_f32 v218, v218, v187\n"
      "v_mfma_f32_16x16x32_bf16 v[172:175], a[32:35], v[132:135], v[172:175]\n"
      "ds_write_b32 v18, v218\n"
      "v_mfma_f32_16x16x32_bf16 v[172:175], a[36:39], v[136:139], v[172:175]\n"
      "s_waitcnt lgkmcnt(3)\n"
      "s_add_u32 m0, s39, 20480\n"
      "v_add_u32 v21, 1, v21\n"
      "v_mfma_f32_16x16x32_bf16 v[172:175], a[40:43], v[140:143], v[172:175]\n"
      "s_mov_b32 s46, s36\n"
      "v_mfma_f32_16x16x32_bf16 v[172:175], a[44:47], v[144:147], v[172:175]\n"
      "v_fmac_f32 v219, v187, v187\n"
      "v_mfma_f32_16x16x32_bf16 v[172:175], a[48:51], v[148:151], v[172:175]\n"
      "ds_write_b32 v18, v219 offset:4\n"
      "v_mfma_f32_16x16x32_bf16 v[172:175], a[52:55], v[152:155], v[172:175]\n"
      "s_waitcnt vmcnt(10) lgkmcnt(2)\n"
      "v_and_b32 v181, 0xffff0000, v196\n"
      "v_mfma_f32_16x16x32_bf16 v[172:175], a[56:59], v[156:159], v[172:175]\n"
      "ds_write_b32 v23, v21\n"
      "v_mfma_f32_16x16x32_bf16 v[172:175], a[60:63], v[160:163], v[172:175]\n"
      "v_lshlrev_b32 v186, 16, v199\n"
      "v_cmp_ne_u32 vcc, s21, v28\n"
      "v_mfma_f32_16x16x32_bf16 v[172:175], a[64:67], v[164:167], v[172:175]\n"
      "buffer_store_dword v25, v12, s[8:11], s50 offen sc1\n"
      "v_mfma_f32_16x16x32_bf16 v[172:175], a[68:71], v[168:171], v[172:175]\n"
      "s_cmp_eq_u32 s36, s37\n"
      "s_cselect_b32 s48, 49152, s51\n"
      "s_add_u32 s36, s36, s48\n"
      "v_mfma_f32_16x16x32_bf16 v[176:179], a[72:75], v[100:103], v[176:179]\n"
      "s_cbranch_vccnz .Lsp_w1_13\n"
      ".Lbk_w1_13:\n"
      "s_min_u32 s36, s36, s6\n"
      "v_mfma_f32_16x16x32_bf16 v[176:179], a[76:79], v[104:107], v[176:179]\n"
      "buffer_load_dword v28, v11, s[8:11], s36 offen sc1\n"
      "v_mfma_f32_16x16x32_bf16 v[176:179], a[80:83], v[108:111], v[176:179]\n"
      "v_and_b32 v183, 0xffff0000, v197\n"
      "v_mul_f32 v219, v172, v172\n"
      "v_mfma_f32_16x16x32_bf16 v[176:179], a[84:87], v[112:115], v[176:179]\n"
      "buffer_load_dwordx4 v0, s[8:11], s46 offen offset:0 sc1 lds\n"
      "v_mfma_f32_16x16x32_bf16 v[176:179], a[88:91], v[116:119], v[176:179]\n"
      "v_mfma_f32_16x16x32_bf16 v[176:179], a[92:95], v[120:123], v[176:179]\n"
      "v_fmac_f32 v219, v173, v173\n"
      "v_mfma_f32_16x16x32_bf16 v[176:179], a[96:99], v[124:127], v[176:179]\n"
      "v_lshlrev_b32 v184, 16, v198\n"
      "v_and_b32 v185, 0xffff0000, v198\n"
      "v_mfma_f32_16x16x32_bf16 v[176:179], a[100:103], v[128:131], v[176:179]\n"
      "ds_read_b32 v22, v24\n"
      "v_fmac_f32 v219, v174, v174\n"
      "v_mfma_f32_16x16x32_bf16 v[176:179], a[104:107], v[132:135], v[176:179]\n"
      "buffer_load_dwordx4 v0, s[8:11], s46 offen offset:1024 sc1 lds\n"
      "v_mfma_f32_16x16x32_bf16 v[176:179], a[108:111], v[136:139], v[176:179]\n"
      "v_lshlrev_b32 v182, 16, v197\n"
      "v_add_f32 v218, v172, v173\n"
      "v_mfma_f32_16x16x32_bf16 v[176:179], a[112:115], v[140:143], v[176:179]\n"
      "v_add_f32 v218, v218, v174\n"
      "v_fmac_f32 v219, v175, v175\n"
      "v_mfma_f32_16x16x32_bf16 v[176:179], a[116:119], v[144:147], v[176:179]\n"
      "v_cvt_pk_bf16_f32 v208, v172, v173\n"
      "v_mfma_f32_16x16x32_bf16 v[176:179], a[120:123], v[148:151], v[176:179]\n"
      "s_waitcnt lgkmcnt(0)\n"
      "v_and_b32 v187, 0xffff0000, v199\n"
      "v_cmp_gt_i32 vcc, v21, v22\n"
      "v_mfma_f32_16x16x32_bf16 v[176:179], a[124:127], v[152:155], v[176:179]\n"
      "buffer_load_dwordx4 v0, s[8:11], s46 offen offset:3072 sc1 lds\n"
      "v_mfma_f32_16x16x32_bf16 v[176:179], a[128:131], v[156:159], v[176:179]\n"
      "v_lshlrev_b32 v180, 16, v196\n"
      "v_add_f32 v218, v218, v175\n"
      "v_mfma_f32_16x16x32_bf16 v[176:179], a[132:135], v[160:163], v[176:179]\n"
      "s_cbranch_vccnz .Lsp_wz1_14\n"
      ".Lbk_wz1_14:\n"
      "ds_read2st64_b32 v[204:205], v17 offset1:1\n"
      "v_mfma_f32_16x16x32_bf16 v[176:179], a[136:139], v[164:167], v[176:179]\n"
      "ds_read2st64_b32 v[206:207], v17 offset0:2 offset1:3\n"
      "v_add_u32 v21, 1, v21\n"
      "v_mfma_f32_16x16x32_bf16 v[176:179], a[140:143], v[168:171], v[176:179]\n"
      "s_waitcnt vmcnt(10)\n"
      "ds_write_b32 v23, v21\n"
      "v_mfma_f32_16x16x32_bf16 v[180:183], a[144:147], v[100:103], v[180:183]\n"
      "buffer_load_dwordx4 v0, s[8:11], s46 offen offset:2048 sc1 lds\n"
      "v_mfma_f32_16x16x32_bf16 v[180:183], a[148:151], v[104:107], v[180:183]\n"
      "v_mfma_f32_16x16x32_bf16 v[180:183], a[152:155], v[108:111], v[180:183]\n"
      "s_add_u32 m0, s40, 20480\n"
      "v_add_f32 v218, v218, v176\n"
      "v_mfma_f32_16x16x32_bf16 v[180:183], a[156:159], v[112:115], v[180:183]\n"
      "v_fmac_f32 v219, v176, v176\n"
      "v_cvt_pk_bf16_f32 v209, v174, v175\n"
      "v_mfma_f32_16x16x32_bf16 v[180:183], a[160:163], v[116:119], v[180:183]\n"
      "buffer_load_dwordx4 v[196:199], v13, s[8:11], s47 offen offset:1024 sc1\n"
      "v_mfma_f32_16x16x32_bf16 v[180:183], a[164:167], v[120:123], v[180:183]\n"
      "s_waitcnt lgkmcnt(1)\n"
      "v_cvt_pk_bf16_f32 v211, v178, v179\n"
      "v_add_f32 v190, v190, v206\n"
      "v_mfma_f32_16x16x32_bf16 v[180:183], a[168:171], v[124:127], v[180:183]\n"
      "v_add_f32 v218, v218, v177\n"
      "v_add_f32 v218, v218, v178\n"
      "v_mfma_f32_16x16x32_bf16 v[180:183], a[172:175], v[128:131], v[180:183]\n"
      "v_add_f32 v218, v218, v179\n"
      "v_add_f32 v189, v189, v205\n"
      "v_mfma_f32_16x16x32_bf16 v[180:183], a[176:179], v[132:135], v[180:183]\n"
      "buffer_load_dwordx4 v1, s[8:11], s46 offen offset:0 sc1 lds\n"
      "v_mfma_f32_16x16x32_bf16 v[180:183], a[180:183], v[136:139], v[180:183]\n"
      "v_add_f32 v188, v188, v204\n"
      "v_fmac_f32 v219, v177, v177\n"
      "v_mfma_f32_16x16x32_bf16 v[180:183], a[184:187], v[140:143], v[180:183]\n"
      "ds_read_b32 v22, v24\n"
      "v_fmac_f32 v219, v178, v178\n"
      "v_mfma_f32_16x16x32_bf16 v[180:183], a[188:191], v[144:147], v[180:183]\n"
      "v_cvt_pk_bf16_f32 v210, v176, v177\n"
      "v_fmac_f32 v219, v179, v179\n"
      "v_mfma_f32_16x16x32_bf16 v[180:183], a[192:195], v[148:151], v[180:183]\n"
      "v_add_f32 v191, v191, v207\n"
      "v_fmac_f32 v219, v188, v188\n"
      "v_mfma_f32_16x16x32_bf16 v[180:183], a[196:199], v[152:155], v[180:183]\n"
      "v_add_f32 v218, v218, v188\n"
      "v_mfma_f32_16x16x32_bf16 v[180:183], a[200:203], v[156:159], v[180:183]\n"
      "v_add_f32 v218, v218, v189\n"
      "v_add_f32 v218, v218, v190\n"
      "v_mfma_f32_16x16x32_bf16 v[180:183], a[204:207], v[160:163], v[180:183]\n"
      "s_waitcnt lgkmcnt(0)\n"
      "v_fmac_f32 v219, v189, v189\n"
      "v_cmp_gt_i32 vcc, v21, v22\n"
      "v_mfma_f32_16x16x32_bf16 v[180:183], a[208:211], v[164:167], v[180:183]\n"
      "v_cvt_pk_bf16_f32 v216, v188, v189\n"
      "v_fmac_f32 v219, v190, v190\n"
      "v_mfma_f32_16x16x32_bf16 v[180:183], a[212:215], v[168:171], v[180:183]\n"
      "s_waitcnt vmcnt(11)\n"
      "v_and_b32 v189, 0xffff0000, v200\n"
      "v_fmac_f32 v219, v191, v191\n"
      "v_mfma_f32_16x16x32_bf16 v[184:187], a[216:219], v[100:103], v[184:187]\n"
      "s_add_u32 s38, s38, 24576\n"
      "v_cvt_pk_bf16_f32 v217, v190, v191\n"
      "v_lshlrev_b32_e64 v188, 16, v200\n"
      "v_mfma_f32_16x16x32_bf16 v[184:187], a[220:223], v[104:107], v[184:187]\n"
      "s_cbranch_vccnz .Lsp_wy0_15\n"
      ".Lbk_wy0_15:\n"
      "v_mfma_f32_16x16x32_bf16 v[184:187], a[224:227], v[108:111], v[184:187]\n"
      "ds_read_b128 v[104:107], v0 offset:1024\n"
      "v_add_f32 v218, v218, v191\n"
      "v_mfma_f32_16x16x32_bf16 v[184:187], a[228:231], v[112:115], v[184:187]\n"
      "v_fmac_f32 v219, v180, v180\n"
      "v_lshlrev_b32 v190, 16, v201\n"
      "v_mfma_f32_16x16x32_bf16 v[184:187], a[232:235], v[116:119], v[184:187]\n"
      "ds_read_b128 v[100:103], v0 offset:0\n"
      "v_add_f32 v218, v218, v180\n"
      "v_mfma_f32_16x16x32_bf16 v[184:187], a[236:239], v[120:123], v[184:187]\n"
      "s_waitcnt vmcnt(9)\n"
      "ds_read_b128 v[120:123], v0 offset:5120\n"
      "v_lshlrev_b32 v178, 16, v195\n"
      "v_mfma_f32_16x16x32_bf16 v[184:187], a[240:243], v[124:127], v[184:187]\n"
      "ds_read_b128 v[112:115], v0 offset:3072\n"
      "v_fmac_f32 v219, v181, v181\n"
      "v_mfma_f32_16x16x32_bf16 v[184:187], a[244:247], v[128:131], v[184:187]\n"
      "ds_read_b128 v[116:119], v0 offset:4096\n"
      "v_lshlrev_b32 v174, 16, v193\n"
      "v_mfma_f32_16x16x32_bf16 v[184:187], a[248:251], v[132:135], v[184:187]\n"
      "ds_read_b128 v[124:127], v0 offset:6144\n"
      "v_fmac_f32 v219, v182, v182\n"
      "v_mfma_f32_16x16x32_bf16 v[184:187], a[252:255], v[136:139], v[184:187]\n"
      "s_sub_u32 s47, s46, s45\n"
      "ds_read_b128 v[108:111], v0 offset:2048\n"
      "v_mfma_f32_16x16x32_bf16 v[184:187], v[32:35], v[140:143], v[184:187]\n"
      "v_and_b32 v191, 0xffff0000, v201\n"
      "v_and_b32 v177, 0xffff0000, v194\n"
      "v_mfma_f32_16x16x32_bf16 v[184:187], v[36:39], v[144:147], v[184:187]\n"
      "ds_read_b128 v[136:139], v0 offset:8192\n"
      "v_lshlrev_b32 v176, 16, v194\n"
      "v_mfma_f32_16x16x32_bf16 v[184:187], v[40:43], v[148:151], v[184:187]\n"
      "ds_read_b128 v[128:131], v0 offset:7168\n"
      "v_lshlrev_b32 v172, 16, v192\n"
      "v_mfma_f32_16x16x32_bf16 v[184:187], v[44:47], v[152:155], v[184:187]\n"
      "v_fmac_f32 v219, v183, v183\n"
      "v_and_b32 v175, 0xffff0000, v193\n"
      "v_mfma_f32_16x16x32_bf16 v[184:187], v[48:51], v[156:159], v[184:187]\n"
      "buffer_load_dwordx2 v[200:201], v14, s[8:11], s47 offen sc1\n"
      "v_mfma_f32_16x16x32_bf16 v[184:187], v[52:55], v[160:163], v[184:187]\n"
      "ds_read2_b32 v[132:133], v2 offset0:0 offset1:32\n"
      "v_and_b32 v173, 0xffff0000, v192\n"
      "v_mfma_f32_16x16x32_bf16 v[184:187], v[56:59], v[164:167], v[184:187]\n"
      "s_waitcnt lgkmcnt(3)\n"
      "ds_read2_b32 v[134:135], v2 offset0:64 offset1:96\n"
      "v_add_f32 v218, v218, v181\n"
      "v_mfma_f32_16x16x32_bf16 v[184:187], v[60:63], v[168:171], v[184:187]\n"
      "s_sub_u32 s20, s20, 1\n"
      "s_cbranch_scc0 .Lw_loop\n"
      ".Lw_tail:\n"
      "v_add_f32 v218, v218, v182\n"
      "s_nop 7\n"
      "v_fmac_f32 v219, v184, v184\n"
      "v_cvt_pk_bf16_f32 v213, v182, v183\n"
      "v_cvt_pk_bf16_f32 v215, v186, v187\n"
      "v_add_f32 v218, v218, v183\n"
      "buffer_store_dwordx4 v[208:211], v15, s[8:11], s38 offen sc1\n"
      "v_cvt_pk_bf16_f32 v212, v180, v181\n"
      "v_add_f32 v218, v218, v184\n"
      "s_add_u32 s50, s50, 24576\n"
      "v_cvt_pk_bf16_f32 v214, v184, v185\n"
      "v_add_f32 v218, v218, v185\n"
      "v_add_f32 v218, v218, v186\n"
      "v_add_f32 v218, v218, v187\n"
      "v_fmac_f32 v219, v185, v185\n"
      "v_fmac_f32 v219, v186, v186\n"
      "buffer_store_dwordx2 v[216:217], v16, s[8:11], s38 offen sc1\n"
      "buffer_store_dwordx4 v[212:215], v15, s[8:11], s38 offen offset:1024 sc1\n"
      "ds_write_b32 v18, v218\n"
      "v_fmac_f32 v219, v187, v187\n"
      "ds_write_b32 v18, v219 offset:4\n"
      "s_waitcnt lgkmcnt(0)\n"
      "s_barrier\n"
      "s_waitcnt vmcnt(12)\n"
      "buffer_store_dword v25, v12, s[8:11], s50 offen sc1\n"
      "s_add_u32 s38, s38, 24576\n"
      "s_add_u32 s50, s50, 24576\n"
      "s_waitcnt vmcnt(1)\n"
      "buffer_store_dword v25, v12, s[8:11], s50 offen sc1\n"
      "s_add_u32 s50, s50, 24576\n"
      "s_endpgm\n"
      ".Lv_start:\n"
      "s_mov_b64 exec, 0\n"
      "s_lshr_b32 s22, s39, 13\n"
      ".Lv_pl:\n"
      "s_cmp_eq_u32 s22, 0\n"
      "s_cbranch_scc1 .Lv_p0\n"
      "s_cmp_eq_u32 s22, 1\n"
      "s_cbranch_scc1 .Lv_p1\n"
      "s_branch .Lv_pe\n"
      ".Lv_pr:\n"
      "s_add_u32 s22, s22, 2\n"
      "s_branch .Lv_pl\n"
      ".Lv_pe:\n"
      "s_mov_b64 exec, -1\n"
      "s_add_u32 s50, s38, 4294893568\n"
      "s_add_u32 s49, s38, 4294918144\n"
      "s_add_u32 s38, s38, 4294942720\n"
      "s_cmp_eq_u32 s36, s37\n"
      "s_cselect_b32 s48, 49152, s51\n"
      "s_add_u32 s36, s36, s48\n"
      "s_min_u32 s36, s36, s6\n"
      "buffer_load_dword v28, v11, s[8:11], s36 offen sc1\n"
      "s_mov_b32 s46, s36\n"
      "s_add_u32 s36, s36, 24576\n"
      "s_min_u32 s36, s36, s6\n"
      "s_add_u32 m0, s39, 0\n"
      "s_waitcnt vmcnt(0)\n"
      "v_cmp_ne_u32 vcc, s21, v28\n"
      "s_cbranch_vccnz .Lsp_v0_16\n"
      ".Lbk_v0_16:\n"
      "buffer_load_dword v28, v11, s[8:11], s36 offen sc1\n"
      "buffer_load_dwordx4 v0, s[8:11], s46 offen offset:3072 sc1 lds\n"
      "buffer_load_dwordx4 v0, s[8:11], s46 offen offset:0 sc1 lds\n"
      "buffer_load_dwordx4 v0, s[8:11], s46 offen offset:1024 sc1 lds\n"
      "buffer_load_dwordx4 v0, s[8:11], s46 offen offset:2048 sc1 lds\n"
      "s_add_u32 m0, s40, 0\n"
      "s_nop 0\n"
      "buffer_load_dwordx4 v1, s[8:11], s46 offen offset:0 sc1 lds\n"
      "s_sub_u32 s47, s46, s45\n"
      "buffer_load_dwordx4 v[192:195], v13, s[8:11], s47 offen sc1\n"
      "s_mov_b32 s46, s36\n"
      "s_cmp_eq_u32 s36, s37\n"
      "s_cselect_b32 s48, 49152, s51\n"
      "buffer_load_dword v26, v23, s[8:11], s31 offen\n"
      "s_add_u32 s36, s36, s48\n"
      "s_waitcnt vmcnt(7)\n"
      "v_cmp_ne_u32 vcc, s21, v28\n"
      "s_min_u32 s36, s36, s6\n"
      "s_cbranch_vccnz .Lsp_v1_17\n"
      ".Lbk_v1_17:\n"
      "buffer_load_dword v28, v11, s[8:11], s36 offen sc1\n"
      "s_add_u32 m0, s39, 20480\n"
      "s_nop 0\n"
      "buffer_load_dwordx4 v0, s[8:11], s46 offen offset:0 sc1 lds\n"
      "buffer_load_dwordx4 v0, s[8:11], s46 offen offset:1024 sc1 lds\n"
      "buffer_load_dwordx4 v0, s[8:11], s46 offen offset:2048 sc1 lds\n"
      "buffer_load_dwordx4 v[196:199], v13, s[8:11], s47 offen offset:1024 sc1\n"
      "v_add_u32 v21, 1, v21\n"
      "s_waitcnt vmcnt(7)\n"
      "ds_write_b32 v23, v21\n"
      "buffer_load_dwordx4 v0, s[8:11], s46 offen offset:3072 sc1 lds\n"
      "s_add_u32 m0, s40, 20480\n"
      "s_nop 0\n"
      "buffer_load_dwordx4 v1, s[8:11], s46 offen offset:0 sc1 lds\n"
      "ds_read_b32 v22, v24\n"
      "buffer_load_dword v26, v23, s[8:11], s31 offen\n"
      "s_waitcnt lgkmcnt(0)\n"
      "v_cmp_gt_i32 vcc, v21, v22\n"
      "buffer_load_dword v26, v23, s[8:11], s31 offen\n"
      "s_cbranch_vccnz .Lsp_vy0_18\n"
      ".Lbk_vy0_18:\n"
      "s_sub_u32 s47, s46, s45\n"
      "s_waitcnt vmcnt(10)\n"
      "v_lshlrev_b32 v174, 16, v193\n"
      "v_and_b32 v179, 0xffff0000, v195\n"
      "ds_read_b128 v[100:103], v0 offset:8192\n"
      "ds_read_b128 v[108:111], v0 offset:10240\n"
      "v_and_b32 v173, 0xffff0000, v192\n"
      "ds_read_b128 v[120:123], v0 offset:13312\n"
      "ds_read_b128 v[128:131], v0 offset:15360\n"
      "v_lshlrev_b32 v178, 16, v195\n"
      "ds_read_b128 v[104:107], v0 offset:9216\n"
      "v_lshlrev_b32 v172, 16, v192\n"
      "buffer_load_dword v26, v23, s[8:11], s31 offen\n"
      "s_mov_b32 s46, s36\n"
      "ds_read_b128 v[112:115], v0 offset:11264\n"
      "ds_read2_b32 v[134:135], v2 offset0:64 offset1:96\n"
      "ds_read2_b32 v[132:133], v2 offset0:0 offset1:32\n"
      "s_add_u32 s36, s36, 24576\n"
      "ds_read_b128 v[116:119], v0 offset:12288\n"
      "ds_read_b128 v[124:127], v0 offset:14336\n"
      "ds_read_b128 v[160:163], v0 offset:6144\n"
      "v_and_b32 v177, 0xffff0000, v194\n"
      "s_mov_b32 s20, 31\n"
      "s_waitcnt lgkmcnt(0)\n"
      "s_nop 4\n"
      "s_branch .Lv_loop\n"
      ".p2align 12\n"
      ".Lv_p0:\n"
      "s_branch .Lv_pr\n"
      ".p2align 5\n"
      ".Lv_loop:\n"
      "v_mfma_f32_16x16x32_bf16 v[188:191], v[64:67], v[100:103], 0\n"
      "ds_read_b128 v[140:143], v0 offset:1024\n"
      "v_and_b32 v175, 0xffff0000, v193\n"
      "v_mfma_f32_16x16x32_bf16 v[188:191], v[68:71], v[104:107], v[188:191]\n"
      "ds_read2_b32 v[168:169], v3 offset0:0 offset1:32\n"
      "v_lshlrev_b32 v176, 16, v194\n"
      "v_mfma_f32_16x16x32_bf16 v[188:191], v[72:75], v[108:111], v[188:191]\n"
      "s_waitcnt lgkmcnt(5)\n"
      "ds_read_b128 v[164:167], v0 offset:7168\n"
      "v_add_f32 v218, v218, v184\n"
      "v_mfma_f32_16x16x32_bf16 v[188:191], v[76:79], v[112:115], v[188:191]\n"
      "s_waitcnt lgkmcnt(5)\n"
      "ds_read_b128 v[144:147], v0 offset:2048\n"
      "v_add_f32 v218, v218, v185\n"
      "v_mfma_f32_16x16x32_bf16 v[188:191], v[80:83], v[116:119], v[188:191]\n"
      "buffer_load_dwordx4 v[192:195], v13, s[8:11], s47 offen sc1\n"
      "v_mfma_f32_16x16x32_bf16 v[188:191], v[84:87], v[120:123], v[188:191]\n"
      "s_waitcnt lgkmcnt(4)\n"
      "ds_read2_b32 v[170:171], v3 offset0:64 offset1:96\n"
      "v_fmac_f32 v219, v183, v183\n"
      "v_mfma_f32_16x16x32_bf16 v[188:191], v[88:91], v[124:127], v[188:191]\n"
      "s_min_u32 s36, s36, s6\n"
      "ds_read_b128 v[136:139], v0 offset:0\n"
      "v_fmac_f32 v219, v184, v184\n"
      "v_mfma_f32_16x16x32_bf16 v[188:191], v[92:95], v[128:131], v[188:191]\n"
      "ds_read_b128 v[148:151], v0 offset:3072\n"
      "v_mfma_f32_16x16x32_bf16 v[188:191], v[96:99], v[132:135], v[188:191]\n"
      "v_cvt_pk_bf16_f32 v215, v186, v187\n"
      "v_add_f32 v218, v218, v186\n"
      "v_mfma_f32_16x16x32_bf16 v[172:175], a[0:3], v[100:103], v[172:175]\n"
      "ds_read_b128 v[152:155], v0 offset:4096\n"
      "v_add_f32 v218, v218, v187\n"
      "v_mfma_f32_16x16x32_bf16 v[172:175], a[4:7], v[104:107], v[172:175]\n"
      "ds_read_b128 v[156:159], v0 offset:5120\n"
      "v_fmac_f32 v219, v185, v185\n"
      "v_mfma_f32_16x16x32_bf16 v[172:175], a[8:11], v[108:111], v[172:175]\n"
      "s_add_u32 m0, s39, 40960\n"
      "v_cvt_pk_bf16_f32 v214, v184, v185\n"
      "v_mfma_f32_16x16x32_bf16 v[172:175], a[12:15], v[112:115], v[172:175]\n"
      "ds_write_b32 v18, v218\n"
      "v_mfma_f32_16x16x32_bf16 v[172:175], a[16:19], v[116:119], v[172:175]\n"
      "s_waitcnt vmcnt(10) lgkmcnt(4)\n"
      "v_fmac_f32_e64 v219, v186, v186\n"
      "v_cmp_ne_u32 vcc, s21, v28\n"
      "v_mfma_f32_16x16x32_bf16 v[172:175], a[20:23], v[120:123], v[172:175]\n"
      "ds_write_b32 v17, v190 offset:512\n"
      "v_mfma_f32_16x16x32_bf16 v[172:175], a[24:27], v[124:127], v[172:175]\n"
      "buffer_store_dwordx4 v[212:215], v15, s[8:11], s38 offen offset:1024 sc1\n"
      "v_mfma_f32_16x16x32_bf16 v[172:175], a[28:31], v[128:131], v[172:175]\n"
      "ds_write_b32 v17, v188 offset:0\n"
      "v_mfma_f32_16x16x32_bf16 v[172:175], a[32:35], v[132:135], v[172:175]\n"
      "v_fmac_f32_e64 v219, v187, v187\n"
      "v_mfma_f32_16x16x32_bf16 v[172:175], a[36:39], v[136:139], v[172:175]\n"
      "ds_write_b32 v17, v191 offset:768\n"
      "v_mfma_f32_16x16x32_bf16 v[172:175], a[40:43], v[140:143], v[172:175]\n"
      "s_cbranch_vccnz .Lsp_v0_19\n"
      ".Lbk_v0_19:\n"
      "v_add_u32 v21, 1, v21\n"
      "v_mfma_f32_16x16x32_bf16 v[172:175], a[44:47], v[144:147], v[172:175]\n"
      "s_waitcnt lgkmcnt(3)\n"
      "ds_write_b32 v18, v219 offset:4\n"
      "v_mfma_f32_16x16x32_bf16 v[172:175], a[48:51], v[148:151], v[172:175]\n"
      "buffer_load_dword v28, v11, s[8:11], s36 offen sc1\n"
      "v_mfma_f32_16x16x32_bf16 v[172:175], a[52:55], v[152:155], v[172:175]\n"
      "ds_write_b32 v17, v189 offset:256\n"
      "v_mfma_f32_16x16x32_bf16 v[172:175], a[56:59], v[156:159], v[172:175]\n"
      "s_waitcnt vmcnt(8) lgkmcnt(3)\n"
      "v_lshlrev_b32 v186, 16, v199\n"
      "v_and_b32 v185, 0xffff0000, v198\n"
      "v_mfma_f32_16x16x32_bf16 v[172:175], a[60:63], v[160:163], v[172:175]\n"
      "ds_write_b32 v23, v21\n"
      "v_mfma_f32_16x16x32_bf16 v[172:175], a[64:67], v[164:167], v[172:175]\n"
      "v_and_b32 v181, 0xffff0000, v196\n"
      "v_and_b32 v187, 0xffff0000, v199\n"
      "v_mfma_f32_16x16x32_bf16 v[172:175], a[68:71], v[168:171], v[172:175]\n"
      "buffer_load_dwordx4 v0, s[8:11], s46 offen offset:3072 sc1 lds\n"
      "v_mfma_f32_16x16x32_bf16 v[176:179], a[72:75], v[100:103], v[176:179]\n"
      "v_lshlrev_b32 v182, 16, v197\n"
      "s_add_u32 s38, s38, 24576\n"
      "v_mfma_f32_16x16x32_bf16 v[176:179], a[76:79], v[104:107], v[176:179]\n"
      "v_lshlrev_b32 v184, 16, v198\n"
      "v_mfma_f32_16x16x32_bf16 v[176:179], a[80:83], v[108:111], v[176:179]\n"
      "v_cvt_pk_bf16_f32 v208, v172, v173\n"
      "v_mfma_f32_16x16x32_bf16 v[176:179], a[84:87], v[112:115], v[176:179]\n"
      "v_mul_f32 v219, v172, v172\n"
      "v_mfma_f32_16x16x32_bf16 v[176:179], a[88:91], v[116:119], v[176:179]\n"
      "v_lshlrev_b32 v180, 16, v196\n"
      "v_and_b32 v183, 0xffff0000, v197\n"
      "v_mfma_f32_16x16x32_bf16 v[176:179], a[92:95], v[120:123], v[176:179]\n"
      "v_fmac_f32 v219, v173, v173\n"
      "v_mfma_f32_16x16x32_bf16 v[176:179], a[96:99], v[124:127], v[176:179]\n"
      "buffer_load_dwordx4 v[196:199], v13, s[8:11], s47 offen offset:1024 sc1\n"
      "v_mfma_f32_16x16x32_bf16 v[176:179], a[100:103], v[128:131], v[176:179]\n"
      "ds_read_b32 v22, v24\n"
      "v_mfma_f32_16x16x32_bf16 v[176:179], a[104:107], v[132:135], v[176:179]\n"
      "v_fmac_f32 v219, v174, v174\n"
      "v_mfma_f32_16x16x32_bf16 v[176:179], a[108:111], v[136:139], v[176:179]\n"
      "v_fmac_f32 v219, v175, v175\n"
      "v_mfma_f32_16x16x32_bf16 v[176:179], a[112:115], v[140:143], v[176:179]\n"
      "buffer_load_dwordx4 v0, s[8:11], s46 offen offset:0 sc1 lds\n"
      "v_mfma_f32_16x16x32_bf16 v[176:179], a[116:119], v[144:147], v[176:179]\n"
      "v_mfma_f32_16x16x32_bf16 v[176:179], a[120:123], v[148:151], v[176:179]\n"
      "s_waitcnt lgkmcnt(0)\n"
      "v_cmp_gt_i32 vcc, v21, v22\n"
      "v_add_f32_e64 v218, v172, v173\n"
      "v_mfma_f32_16x16x32_bf16 v[176:179], a[124:127], v[152:155], v[176:179]\n"
      "v_add_f32_e64 v218, v218, v174\n"
      "v_add_f32_e64 v218, v218, v175\n"
      "v_mfma_f32_16x16x32_bf16 v[176:179], a[128:131], v[156:159], v[176:179]\n"
      "buffer_load_dwordx4 v0, s[8:11], s46 offen offset:1024 sc1 lds\n"
      "v_mfma_f32_16x16x32_bf16 v[176:179], a[132:135], v[160:163], v[176:179]\n"
      "s_cbranch_vccnz .Lsp_vz0_20\n"
      ".Lbk_vz0_20:\n"
      "ds_read_b128 v[204:207], v19\n"
      "v_mfma_f32_16x16x32_bf16 v[176:179], a[136:139], v[164:167], v[176:179]\n"
      "v_cvt_pk_bf16_f32 v209, v174, v175\n"
      "v_add_u32 v21, 1, v21\n"
      "v_mfma_f32_16x16x32_bf16 v[176:179], a[140:143], v[168:171], v[176:179]\n"
      "s_waitcnt vmcnt(10)\n"
      "ds_write_b32 v23, v21\n"
      "v_mfma_f32_16x16x32_bf16 v[180:183], a[144:147], v[100:103], v[180:183]\n"
      "v_mfma_f32_16x16x32_bf16 v[180:183], a[148:151], v[104:107], v[180:183]\n"
      "buffer_load_dwordx4 v0, s[8:11], s46 offen offset:2048 sc1 lds\n"
      "v_mfma_f32_16x16x32_bf16 v[180:183], a[152:155], v[108:111], v[180:183]\n"
      "s_add_u32 s49, s49, 24576\n"
      "s_add_u32 m0, s40, 40960\n"
      "v_mfma_f32_16x16x32_bf16 v[180:183], a[156:159], v[112:115], v[180:183]\n"
      "v_mfma_f32_16x16x32_bf16 v[180:183], a[160:163], v[116:119], v[180:183]\n"
      "s_waitcnt lgkmcnt(1)\n"
      "v_add_f32 v204, v204, v206\n"
      "v_cvt_pk_bf16_f32 v210, v176, v177\n"
      "v_mfma_f32_16x16x32_bf16 v[180:183], a[164:167], v[120:123], v[180:183]\n"
      "buffer_load_dwordx4 v1, s[8:11], s46 offen offset:0 sc1 lds\n"
      "v_mfma_f32_16x16x32_bf16 v[180:183], a[168:171], v[124:127], v[180:183]\n"
      "v_add_f32 v205, v205, v207\n"
      "v_add_f32_dpp v204, v204, v204 quad_perm:[1,0,3,2] row_mask:0xf bank_mask:0xf\n"
      "v_mfma_f32_16x16x32_bf16 v[180:183], a[172:175], v[128:131], v[180:183]\n"
      "v_fmac_f32 v219, v176, v176\n"
      "v_mfma_f32_16x16x32_bf16 v[180:183], a[176:179], v[132:135], v[180:183]\n"
      "v_add_f32_dpp v204, v204, v204 quad_perm:[2,3,0,1] row_mask:0xf bank_mask:0xf\n"
      "v_cvt_pk_bf16_f32 v211, v178, v179\n"
      "v_mfma_f32_16x16x32_bf16 v[180:183], a[180:183], v[136:139], v[180:183]\n"
      "buffer_store_dwordx4 v[208:211], v15, s[8:11], s38 offen sc1\n"
      "v_mfma_f32_16x16x32_bf16 v[180:183], a[184:187], v[140:143], v[180:183]\n"
      "ds_read_b32 v22, v24\n"
      "v_add_f32_dpp v205, v205, v205 quad_perm:[1,0,3,2] row_mask:0xf bank_mask:0xf\n"
      "v_mfma_f32_16x16x32_bf16 v[180:183], a[188:191], v[144:147], v[180:183]\n"
      "s_add_u32 s50, s50, 24576\n"
      "v_add_f32_dpp v205, v205, v205 quad_perm:[2,3,0,1] row_mask:0xf bank_mask:0xf\n"
      "v_fmac_f32 v219, v177, v177\n"
      "v_mfma_f32_16x16x32_bf16 v[180:183], a[192:195], v[148:151], v[180:183]\n"
      "v_add_f32_dpp v205, v205, v205 row_half_mirror row_mask:0xf bank_mask:0xf\n"
      "v_fmac_f32 v219, v178, v178\n"
      "v_mfma_f32_16x16x32_bf16 v[180:183], a[196:199], v[152:155], v[180:183]\n"
      "v_add_f32_dpp v204, v204, v204 row_half_mirror row_mask:0xf bank_mask:0xf\n"
      "v_mfma_f32_16x16x32_bf16 v[180:183], a[200:203], v[156:159], v[180:183]\n"
      "v_mul_f32 v204, v27, v204\n"
      "v_fmaak_f32 v205, v27, v205, 0x36a7c5ac\n"
      "v_mfma_f32_16x16x32_bf16 v[180:183], a[204:207], v[160:163], v[180:183]\n"
      "s_waitcnt vmcnt(12) lgkmcnt(1)\n"
      "buffer_store_dword v25, v12, s[8:11], s50 offen sc1\n"
      "v_mfma_f32_16x16x32_bf16 v[180:183], a[208:211], v[164:167], v[180:183]\n"
      "s_waitcnt lgkmcnt(0)\n"
      "v_cmp_gt_i32 vcc, v21, v22\n"
      "v_add_f32 v218, v218, v176\n"
      "v_mfma_f32_16x16x32_bf16 v[180:183], a[212:215], v[168:171], v[180:183]\n"
      "v_add_f32 v218, v218, v177\n"
      "v_add_f32_e64 v218, v218, v178\n"
      "v_mfma_f32_16x16x32_bf16 v[184:187], a[216:219], v[100:103], v[184:187]\n"
      "buffer_store_dwordx2 v[204:205], v20, s[8:11], s49 offen sc1\n"
      "v_mfma_f32_16x16x32_bf16 v[184:187], a[220:223], v[104:107], v[184:187]\n"
      "s_sub_u32 s47, s46, s45\n"
      "s_cbranch_vccnz .Lsp_vy1_21\n"
      ".Lbk_vy1_21:\n"
      "v_mfma_f32_16x16x32_bf16 v[184:187], a[224:227], v[108:111], v[184:187]\n"
      "s_waitcnt vmcnt(11)\n"
      "ds_read_b128 v[104:107], v0 offset:29696\n"
      "v_and_b32 v177, 0xffff0000, v194\n"
      "v_mfma_f32_16x16x32_bf16 v[184:187], a[228:231], v[112:115], v[184:187]\n"
      "v_fmac_f32 v219, v179, v179\n"
      "v_mfma_f32_16x16x32_bf16 v[184:187], a[232:235], v[116:119], v[184:187]\n"
      "v_fmac_f32 v219, v180, v180\n"
      "v_add_f32 v218, v218, v179\n"
      "v_mfma_f32_16x16x32_bf16 v[184:187], a[236:239], v[120:123], v[184:187]\n"
      "v_cvt_pk_bf16_f32 v212, v180, v181\n"
      "v_and_b32 v179, 0xffff0000, v195\n"
      "v_mfma_f32_16x16x32_bf16 v[184:187], a[240:243], v[124:127], v[184:187]\n"
      "ds_read_b128 v[120:123], v0 offset:33792\n"
      "v_lshlrev_b32 v174, 16, v193\n"
      "v_mfma_f32_16x16x32_bf16 v[184:187], a[244:247], v[128:131], v[184:187]\n"
      "ds_read_b128 v[112:115], v0 offset:31744\n"
      "v_add_f32 v218, v218, v180\n"
      "v_mfma_f32_16x16x32_bf16 v[184:187], a[248:251], v[132:135], v[184:187]\n"
      "ds_read_b128 v[100:103], v0 offset:28672\n"
      "v_add_f32 v218, v218, v181\n"
      "v_mfma_f32_16x16x32_bf16 v[184:187], a[252:255], v[136:139], v[184:187]\n"
      "ds_read_b128 v[124:127], v0 offset:34816\n"
      "v_mfma_f32_16x16x32_bf16 v[184:187], v[32:35], v[140:143], v[184:187]\n"
      "ds_read_b128 v[108:111], v0 offset:30720\n"
      "v_and_b32 v175, 0xffff0000, v193\n"
      "v_mfma_f32_16x16x32_bf16 v[184:187], v[36:39], v[144:147], v[184:187]\n"
      "ds_read_b128 v[116:119], v0 offset:32768\n"
      "v_fmac_f32 v219, v181, v181\n"
      "v_mfma_f32_16x16x32_bf16 v[184:187], v[40:43], v[148:151], v[184:187]\n"
      "v_add_f32 v218, v218, v182\n"
      "v_mfma_f32_16x16x32_bf16 v[184:187], v[44:47], v[152:155], v[184:187]\n"
      "ds_read2_b32 v[132:133], v4 offset0:0 offset1:32\n"
      "v_and_b32 v173, 0xffff0000, v192\n"
      "v_mfma_f32_16x16x32_bf16 v[184:187], v[48:51], v[156:159], v[184:187]\n"
      "ds_read_b128 v[128:131], v0 offset:35840\n"
      "v_lshlrev_b32 v178, 16, v195\n"
      "v_mfma_f32_16x16x32_bf16 v[184:187], v[52:55], v[160:163], v[184:187]\n"
      "ds_read_b128 v[156:159], v0 offset:25600\n"
      "v_lshlrev_b32 v172, 16, v192\n"
      "v_mfma_f32_16x16x32_bf16 v[184:187], v[56:59], v[164:167], v[184:187]\n"
      "ds_read_b128 v[144:147], v0 offset:22528\n"
      "v_lshlrev_b32 v176, 16, v194\n"
      "v_mfma_f32_16x16x32_bf16 v[184:187], v[60:63], v[168:171], v[184:187]\n"
      "s_waitcnt lgkmcnt(5)\n"
      "buffer_load_dwordx4 v[192:195], v13, s[8:11], s47 offen sc1\n"
      "v_mfma_f32_16x16x32_bf16 v[188:191], v[64:67], v[100:103], 0\n"
      "ds_read2_b32 v[134:135], v4 offset0:64 offset1:96\n"
      "v_fmac_f32 v219, v182, v182\n"
      "v_mfma_f32_16x16x32_bf16 v[188:191], v[68:71], v[104:107], v[188:191]\n"
      "ds_read_b128 v[148:151], v0 offset:23552\n"
      "v_add_f32 v218, v218, v183\n"
      "v_mfma_f32_16x16x32_bf16 v[188:191], v[72:75], v[108:111], v[188:191]\n"
      "s_mov_b32 s46, s36\n"
      "v_cvt_pk_bf16_f32 v213, v182, v183\n"
      "v_cvt_pk_bf16_f32 v215, v186, v187\n"
      "v_mfma_f32_16x16x32_bf16 v[188:191], v[76:79], v[112:115], v[188:191]\n"
      "s_waitcnt lgkmcnt(4)\n"
      "ds_read_b128 v[160:163], v0 offset:26624\n"
      "v_fmac_f32 v219, v183, v183\n"
      "v_mfma_f32_16x16x32_bf16 v[188:191], v[80:83], v[116:119], v[188:191]\n"
      "ds_read_b128 v[140:143], v0 offset:21504\n"
      "v_fmac_f32 v219, v184, v184\n"
      "v_mfma_f32_16x16x32_bf16 v[188:191], v[84:87], v[120:123], v[188:191]\n"
      "ds_read2_b32 v[168:169], v5 offset0:0 offset1:32\n"
      "v_add_f32 v218, v218, v184\n"
      "v_mfma_f32_16x16x32_bf16 v[188:191], v[88:91], v[124:127], v[188:191]\n"
      "ds_read_b128 v[152:155], v0 offset:24576\n"
      "v_fmac_f32 v219, v185, v185\n"
      "v_mfma_f32_16x16x32_bf16 v[188:191], v[92:95], v[128:131], v[188:191]\n"
      "s_waitcnt lgkmcnt(5)\n"
      "ds_read_b128 v[136:139], v0 offset:20480\n"
      "v_add_f32 v218, v218, v185\n"
      "v_mfma_f32_16x16x32_bf16 v[188:191], v[96:99], v[132:135], v[188:191]\n"
      "s_cmp_eq_u32 s36, s37\n"
      "s_cselect_b32 s48, 49152, s51\n"
      "ds_read_b128 v[164:167], v0 offset:27648\n"
      "v_mfma_f32_16x16x32_bf16 v[172:175], a[0:3], v[100:103], v[172:175]\n"
      "ds_read2_b32 v[170:171], v5 offset0:64 offset1:96\n"
      "v_cvt_pk_bf16_f32 v214, v184, v185\n"
      "v_mfma_f32_16x16x32_bf16 v[172:175], a[4:7], v[104:107], v[172:175]\n"
      "v_fmac_f32 v219, v186, v186\n"
      "v_add_f32 v218, v218, v186\n"
      "v_mfma_f32_16x16x32_bf16 v[172:175], a[8:11], v[108:111], v[172:175]\n"
      "ds_write_b32 v17, v189 offset:256\n"
      "v_mfma_f32_16x16x32_bf16 v[172:175], a[12:15], v[112:115], v[172:175]\n"
      "v_add_f32 v218, v218, v187\n"
      "v_add_u32 v21, 1, v21\n"
      "v_mfma_f32_16x16x32_bf16 v[172:175], a[16:19], v[116:119], v[172:175]\n"
      "buffer_store_dwordx4 v[212:215], v15, s[8:11], s38 offen offset:1024 sc1\n"
      "v_mfma_f32_16x16x32_bf16 v[172:175], a[20:23], v[120:123], v[172:175]\n"
      "ds_write_b32 v17, v190 offset:512\n"
      "v_mfma_f32_16x16x32_bf16 v[172:175], a[24:27], v[124:127], v[172:175]\n"
      "v_fmac_f32 v219, v187, v187\n"
      "v_mfma_f32_16x16x32_bf16 v[172:175], a[28:31], v[128:131], v[172:175]\n"
      "s_add_u32 s36, s36, s48\n"
      "ds_write_b32 v18, v218\n"
      "v_mfma_f32_16x16x32_bf16 v[172:175], a[32:35], v[132:135], v[172:175]\n"
      "s_waitcnt vmcnt(11) lgkmcnt(3)\n"
      "v_cmp_ne_u32 vcc, s21, v28\n"
      "v_mfma_f32_16x16x32_bf16 v[172:175], a[36:39], v[136:139], v[172:175]\n"
      "ds_write_b32 v17, v188 offset:0\n"
      "v_mfma_f32_16x16x32_bf16 v[172:175], a[40:43], v[140:143], v[172:175]\n"
      "s_waitcnt vmcnt(9) lgkmcnt(3)\n"
      "s_min_u32 s36, s36, s6\n"
      "v_lshlrev_b32 v182, 16, v197\n"
      "v_mfma_f32_16x16x32_bf16 v[172:175], a[44:47], v[144:147], v[172:175]\n"
      "ds_write_b32 v17, v191 offset:768\n"
      "v_mfma_f32_16x16x32_bf16 v[172:175], a[48:51], v[148:151], v[172:175]\n"
      "s_cbranch_vccnz .Lsp_v1_22\n"
      ".Lbk_v1_22:\n"
      "v_and_b32 v181, 0xffff0000, v196\n"
      "v_mfma_f32_16x16x32_bf16 v[172:175], a[52:55], v[152:155], v[172:175]\n"
      "ds_write_b32 v18, v219 offset:4\n"
      "v_mfma_f32_16x16x32_bf16 v[172:175], a[56:59], v[156:159], v[172:175]\n"
      "buffer_load_dword v28, v11, s[8:11], s36 offen sc1\n"
      "v_mfma_f32_16x16x32_bf16 v[172:175], a[60:63], v[160:163], v[172:175]\n"
      "s_add_u32 m0, s39, 61440\n"
      "ds_write_b32 v23, v21\n"
      "v_mfma_f32_16x16x32_bf16 v[172:175], a[64:67], v[164:167], v[172:175]\n"
      "v_and_b32 v187, 0xffff0000, v199\n"
      "v_and_b32 v183, 0xffff0000, v197\n"
      "v_mfma_f32_16x16x32_bf16 v[172:175], a[68:71], v[168:171], v[172:175]\n"
      "buffer_load_dwordx4 v0, s[8:11], s46 offen offset:0 sc1 lds\n"
      "v_mfma_f32_16x16x32_bf16 v[176:179], a[72:75], v[100:103], v[176:179]\n"
      "s_add_u32 s49, s49, 24576\n"
      "v_mfma_f32_16x16x32_bf16 v[176:179], a[76:79], v[104:107], v[176:179]\n"
      "v_mfma_f32_16x16x32_bf16 v[176:179], a[80:83], v[108:111], v[176:179]\n"
      "v_cvt_pk_bf16_f32 v209, v174, v175\n"
      "v_mfma_f32_16x16x32_bf16 v[176:179], a[84:87], v[112:115], v[176:179]\n"
      "buffer_load_dwordx4 v0, s[8:11], s46 offen offset:1024 sc1 lds\n"
      "v_mfma_f32_16x16x32_bf16 v[176:179], a[88:91], v[116:119], v[176:179]\n"
      "v_add_f32 v218, v172, v173\n"
      "v_mfma_f32_16x16x32_bf16 v[176:179], a[92:95], v[120:123], v[176:179]\n"
      "v_add_f32 v218, v218, v174\n"
      "v_lshlrev_b32 v180, 16, v196\n"
      "v_mfma_f32_16x16x32_bf16 v[176:179], a[96:99], v[124:127], v[176:179]\n"
      "ds_read_b32 v22, v24\n"
      "v_lshlrev_b32 v186, 16, v199\n"
      "v_mfma_f32_16x16x32_bf16 v[176:179], a[100:103], v[128:131], v[176:179]\n"
      "buffer_load_dwordx4 v0, s[8:11], s46 offen offset:2048 sc1 lds\n"
      "v_mfma_f32_16x16x32_bf16 v[176:179], a[104:107], v[132:135], v[176:179]\n"
      "v_mul_f32 v219, v172, v172\n"
      "v_fmac_f32 v219, v173, v173\n"
      "v_mfma_f32_16x16x32_bf16 v[176:179], a[108:111], v[136:139], v[176:179]\n"
      "v_lshlrev_b32 v184, 16, v198\n"
      "v_mfma_f32_16x16x32_bf16 v[176:179], a[112:115], v[140:143], v[176:179]\n"
      "v_fmac_f32_e64 v219, v174, v174\n"
      "v_mfma_f32_16x16x32_bf16 v[176:179], a[116:119], v[144:147], v[176:179]\n"
      "s_waitcnt lgkmcnt(0)\n"
      "v_cmp_gt_i32 vcc, v21, v22\n"
      "v_add_f32_e64 v218, v218, v175\n"
      "v_mfma_f32_16x16x32_bf16 v[176:179], a[120:123], v[148:151], v[176:179]\n"
      "v_mfma_f32_16x16x32_bf16 v[176:179], a[124:127], v[152:155], v[176:179]\n"
      "v_and_b32 v185, 0xffff0000, v198\n"
      "v_cvt_pk_bf16_f32 v208, v172, v173\n"
      "v_mfma_f32_16x16x32_bf16 v[176:179], a[128:131], v[156:159], v[176:179]\n"
      "buffer_load_dwordx4 v[196:199], v13, s[8:11], s47 offen offset:1024 sc1\n"
      "v_mfma_f32_16x16x32_bf16 v[176:179], a[132:135], v[160:163], v[176:179]\n"
      "s_cbranch_vccnz .Lsp_vz1_23\n"
      ".Lbk_vz1_23:\n"
      "ds_read_b128 v[204:207], v19\n"
      "v_mfma_f32_16x16x32_bf16 v[176:179], a[136:139], v[164:167], v[176:179]\n"
      "v_add_u32 v21, 1, v21\n"
      "v_mfma_f32_16x16x32_bf16 v[176:179], a[140:143], v[168:171], v[176:179]\n"
      "s_waitcnt vmcnt(10)\n"
      "ds_write_b32 v23, v21\n"
      "v_mfma_f32_16x16x32_bf16 v[180:183], a[144:147], v[100:103], v[180:183]\n"
      "v_fmac_f32 v219, v175, v175\n"
      "v_mfma_f32_16x16x32_bf16 v[180:183], a[148:151], v[104:107], v[180:183]\n"
      "buffer_load_dwordx4 v0, s[8:11], s46 offen offset:3072 sc1 lds\n"
      "v_mfma_f32_16x16x32_bf16 v[180:183], a[152:155], v[108:111], v[180:183]\n"
      "v_cvt_pk_bf16_f32 v210, v176, v177\n"
      "v_fmac_f32 v219, v176, v176\n"
      "v_mfma_f32_16x16x32_bf16 v[180:183], a[156:159], v[112:115], v[180:183]\n"
      "s_add_u32 m0, s40, 61440\n"
      "v_fmac_f32 v219, v177, v177\n"
      "v_mfma_f32_16x16x32_bf16 v[180:183], a[160:163], v[116:119], v[180:183]\n"
      "s_waitcnt lgkmcnt(1)\n"
      "v_add_f32 v204, v204, v206\n"
      "v_add_f32 v205, v205, v207\n"
      "v_mfma_f32_16x16x32_bf16 v[180:183], a[164:167], v[120:123], v[180:183]\n"
      "v_add_f32_dpp v204, v204, v204 quad_perm:[1,0,3,2] row_mask:0xf bank_mask:0xf\n"
      "v_fmac_f32 v219, v178, v178\n"
      "v_mfma_f32_16x16x32_bf16 v[180:183], a[168:171], v[124:127], v[180:183]\n"
      "buffer_load_dwordx4 v1, s[8:11], s46 offen offset:0 sc1 lds\n"
      "v_mfma_f32_16x16x32_bf16 v[180:183], a[172:175], v[128:131], v[180:183]\n"
      "v_add_f32_dpp v205, v205, v205 quad_perm:[1,0,3,2] row_mask:0xf bank_mask:0xf\n"
      "v_add_f32_dpp v204, v204, v204 quad_perm:[2,3,0,1] row_mask:0xf bank_mask:0xf\n"
      "v_mfma_f32_16x16x32_bf16 v[180:183], a[176:179], v[132:135], v[180:183]\n"
      "v_add_f32_dpp v204, v204, v204 row_half_mirror row_mask:0xf bank_mask:0xf\n"
      "v_cvt_pk_bf16_f32 v211, v178, v179\n"
      "v_mfma_f32_16x16x32_bf16 v[180:183], a[180:183], v[136:139], v[180:183]\n"
      "s_add_u32 s38, s38, 24576\n"
      "v_mul_f32 v204, v27, v204\n"
      "v_add_f32_dpp v205, v205, v205 quad_perm:[2,3,0,1] row_mask:0xf bank_mask:0xf\n"
      "v_mfma_f32_16x16x32_bf16 v[180:183], a[184:187], v[140:143], v[180:183]\n"
      "ds_read_b32 v22, v24\n"
      "v_add_f32 v218, v218, v176\n"
      "v_mfma_f32_16x16x32_bf16 v[180:183], a[188:191], v[144:147], v[180:183]\n"
      "v_add_f32_dpp v205, v205, v205 row_half_mirror row_mask:0xf bank_mask:0xf\n"
      "v_fmaak_f32 v205, v27, v205, 0x36a7c5ac\n"
      "v_mfma_f32_16x16x32_bf16 v[180:183], a[192:195], v[148:151], v[180:183]\n"
      "buffer_store_dwordx2 v[204:205], v20, s[8:11], s49 offen sc1\n"
      "v_mfma_f32_16x16x32_bf16 v[180:183], a[196:199], v[152:155], v[180:183]\n"
      "s_add_u32 s50, s50, 24576\n"
      "v_add_f32 v218, v218, v177\n"
      "v_mfma_f32_16x16x32_bf16 v[180:183], a[200:203], v[156:159], v[180:183]\n"
      "v_add_f32 v218, v218, v178\n"
      "v_mfma_f32_16x16x32_bf16 v[180:183], a[204:207], v[160:163], v[180:183]\n"
      "s_waitcnt lgkmcnt(0)\n"
      "v_add_f32 v218, v218, v179\n"
      "v_cmp_gt_i32 vcc, v21, v22\n"
      "v_mfma_f32_16x16x32_bf16 v[180:183], a[208:211], v[164:167], v[180:183]\n"
      "s_waitcnt vmcnt(10)\n"
      "buffer_store_dword v25, v12, s[8:11], s50 offen sc1\n"
      "v_mfma_f32_16x16x32_bf16 v[180:183], a[212:215], v[168:171], v[180:183]\n"
      "v_fmac_f32 v219, v179, v179\n"
      "v_mfma_f32_16x16x32_bf16 v[184:187], a[216:219], v[100:103], v[184:187]\n"
      "s_cbranch_vccnz .Lsp_vy0_24\n"
      ".Lbk_vy0_24:\n"
      "s_sub_u32 s47, s46, s45\n"
      "v_mfma_f32_16x16x32_bf16 v[184:187], a[220:223], v[104:107], v[184:187]\n"
      "s_waitcnt vmcnt(10)\n"
      "v_lshlrev_b32 v174, 16, v193\n"
      "v_and_b32 v179, 0xffff0000, v195\n"
      "v_mfma_f32_16x16x32_bf16 v[184:187], a[224:227], v[108:111], v[184:187]\n"
      "ds_read_b128 v[100:103], v10 offset:8192\n"
      "v_cvt_pk_bf16_f32 v213, v182, v183\n"
      "v_mfma_f32_16x16x32_bf16 v[184:187], a[228:231], v[112:115], v[184:187]\n"
      "ds_read_b128 v[108:111], v10 offset:10240\n"
      "v_and_b32 v173, 0xffff0000, v192\n"
      "v_mfma_f32_16x16x32_bf16 v[184:187], a[232:235], v[116:119], v[184:187]\n"
      "v_fmac_f32 v219, v180, v180\n"
      "v_mfma_f32_16x16x32_bf16 v[184:187], a[236:239], v[120:123], v[184:187]\n"
      "ds_read_b128 v[120:123], v10 offset:13312\n"
      "v_add_f32 v218, v218, v180\n"
      "v_mfma_f32_16x16x32_bf16 v[184:187], a[240:243], v[124:127], v[184:187]\n"
      "v_add_f32 v218, v218, v181\n"
      "v_add_f32 v218, v218, v182\n"
      "v_mfma_f32_16x16x32_bf16 v[184:187], a[244:247], v[128:131], v[184:187]\n"
      "ds_read_b128 v[128:131], v10 offset:15360\n"
      "v_lshlrev_b32 v178, 16, v195\n"
      "v_mfma_f32_16x16x32_bf16 v[184:187], a[248:251], v[132:135], v[184:187]\n"
      "v_mfma_f32_16x16x32_bf16 v[184:187], a[252:255], v[136:139], v[184:187]\n"
      "ds_read_b128 v[104:107], v10 offset:9216\n"
      "v_lshlrev_b32 v172, 16, v192\n"
      "v_mfma_f32_16x16x32_bf16 v[184:187], v[32:35], v[140:143], v[184:187]\n"
      "buffer_store_dwordx4 v[208:211], v15, s[8:11], s38 offen sc1\n"
      "v_mfma_f32_16x16x32_bf16 v[184:187], v[36:39], v[144:147], v[184:187]\n"
      "s_mov_b32 s46, s36\n"
      "ds_read_b128 v[112:115], v10 offset:11264\n"
      "v_fmac_f32 v219, v181, v181\n"
      "v_mfma_f32_16x16x32_bf16 v[184:187], v[40:43], v[148:151], v[184:187]\n"
      "ds_read2_b32 v[134:135], v6 offset0:64 offset1:96\n"
      "v_cvt_pk_bf16_f32 v212, v180, v181\n"
      "v_mfma_f32_16x16x32_bf16 v[184:187], v[44:47], v[152:155], v[184:187]\n"
      "ds_read2_b32 v[132:133], v6 offset0:0 offset1:32\n"
      "s_add_u32 s36, s36, 24576\n"
      "v_mfma_f32_16x16x32_bf16 v[184:187], v[48:51], v[156:159], v[184:187]\n"
      "ds_read_b128 v[116:119], v10 offset:12288\n"
      "v_fmac_f32 v219, v182, v182\n"
      "v_mfma_f32_16x16x32_bf16 v[184:187], v[52:55], v[160:163], v[184:187]\n"
      "ds_read_b128 v[124:127], v10 offset:14336\n"
      "v_add_f32 v218, v218, v183\n"
      "v_mfma_f32_16x16x32_bf16 v[184:187], v[56:59], v[164:167], v[184:187]\n"
      "s_waitcnt lgkmcnt(5)\n"
      "ds_read_b128 v[160:163], v10 offset:6144\n"
      "v_and_b32 v177, 0xffff0000, v194\n"
      "v_mfma_f32_16x16x32_bf16 v[184:187], v[60:63], v[168:171], v[184:187]\n"
      "s_sub_u32 s20, s20, 1\n"
      "s_cbranch_scc1 .Lv_tail\n"
      "v_mfma_f32_16x16x32_bf16 v[188:191], v[64:67], v[100:103], 0\n"
      "ds_read_b128 v[140:143], v10 offset:1024\n"
      "v_and_b32 v175, 0xffff0000, v193\n"
      "v_mfma_f32_16x16x32_bf16 v[188:191], v[68:71], v[104:107], v[188:191]\n"
      "ds_read2_b32 v[168:169], v7 offset0:0 offset1:32\n"
      "v_lshlrev_b32 v176, 16, v194\n"
      "v_mfma_f32_16x16x32_bf16 v[188:191], v[72:75], v[108:111], v[188:191]\n"
      "s_waitcnt lgkmcnt(5)\n"
      "ds_read_b128 v[164:167], v10 offset:7168\n"
      "v_add_f32 v218, v218, v184\n"
      "v_mfma_f32_16x16x32_bf16 v[188:191], v[76:79], v[112:115], v[188:191]\n"
      "s_waitcnt lgkmcnt(5)\n"
      "ds_read_b128 v[144:147], v10 offset:2048\n"
      "v_add_f32 v218, v218, v185\n"
      "v_mfma_f32_16x16x32_bf16 v[188:191], v[80:83], v[116:119], v[188:191]\n"
      "buffer_load_dwordx4 v[192:195], v13, s[8:11], s47 offen sc1\n"
      "v_mfma_f32_16x16x32_bf16 v[188:191], v[84:87], v[120:123], v[188:191]\n"
      "s_waitcnt lgkmcnt(4)\n"
      "ds_read2_b32 v[170:171], v7 offset0:64 offset1:96\n"
      "v_fmac_f32 v219, v183, v183\n"
      "v_mfma_f32_16x16x32_bf16 v[188:191], v[88:91], v[124:127], v[188:191]\n"
      "s_min_u32 s36, s36, s6\n"
      "ds_read_b128 v[136:139], v10 offset:0\n"
      "v_fmac_f32 v219, v184, v184\n"
      "v_mfma_f32_16x16x32_bf16 v[188:191], v[92:95], v[128:131], v[188:191]\n"
      "ds_read_b128 v[148:151], v10 offset:3072\n"
      "v_mfma_f32_16x16x32_bf16 v[188:191], v[96:99], v[132:135], v[188:191]\n"
      "v_cvt_pk_bf16_f32 v215, v186, v187\n"
      "v_add_f32 v218, v218, v186\n"
      "v_mfma_f32_16x16x32_bf16 v[172:175], a[0:3], v[100:103], v[172:175]\n"
      "ds_read_b128 v[152:155], v10 offset:4096\n"
      "v_add_f32 v218, v218, v187\n"
      "v_mfma_f32_16x16x32_bf16 v[172:175], a[4:7], v[104:107], v[172:175]\n"
      "ds_read_b128 v[156:159], v10 offset:5120\n"
      "v_fmac_f32 v219, v185, v185\n"
      "v_mfma_f32_16x16x32_bf16 v[172:175], a[8:11], v[108:111], v[172:175]\n"
      "s_add_u32 m0, s39, 0\n"
      "v_cvt_pk_bf16_f32 v214, v184, v185\n"
      "v_mfma_f32_16x16x32_bf16 v[172:175], a[12:15], v[112:115], v[172:175]\n"
      "ds_write_b32 v18, v218\n"
      "v_mfma_f32_16x16x32_bf16 v[172:175], a[16:19], v[116:119], v[172:175]\n"
      "s_waitcnt vmcnt(10) lgkmcnt(4)\n"
      "v_fmac_f32 v219, v186, v186\n"
      "v_cmp_ne_u32 vcc, s21, v28\n"
      "v_mfma_f32_16x16x32_bf16 v[172:175], a[20:23], v[120:123], v[172:175]\n"
      "ds_write_b32 v17, v190 offset:512\n"
      "v_mfma_f32_16x16x32_bf16 v[172:175], a[24:27], v[124:127], v[172:175]\n"
      "buffer_store_dwordx4 v[212:215], v15, s[8:11], s38 offen offset:1024 sc1\n"
      "v_mfma_f32_16x16x32_bf16 v[172:175], a[28:31], v[128:131], v[172:175]\n"
      "ds_write_b32 v17, v188 offset:0\n"
      "v_mfma_f32_16x16x32_bf16 v[172:175], a[32:35], v[132:135], v[172:175]\n"
      "v_fmac_f32_e64 v219, v187, v187\n"
      "v_mfma_f32_16x16x32_bf16 v[172:175], a[36:39], v[136:139], v[172:175]\n"
      "ds_write_b32 v17, v191 offset:768\n"
      "v_mfma_f32_16x16x32_bf16 v[172:175], a[40:43], v[140:143], v[172:175]\n"
      "s_cbranch_vccnz .Lsp_v0_25\n"
      ".Lbk_v0_25:\n"
      "v_add_u32 v21, 1, v21\n"
      "v_mfma_f32_16x16x32_bf16 v[172:175], a[44:47], v[144:147], v[172:175]\n"
      "s_waitcnt lgkmcnt(3)\n"
      "ds_write_b32 v18, v219 offset:4\n"
      "v_mfma_f32_16x16x32_bf16 v[172:175], a[48:51], v[148:151], v[172:175]\n"
      "buffer_load_dword v28, v11, s[8:11], s36 offen sc1\n"
      "v_mfma_f32_16x16x32_bf16 v[172:175], a[52:55], v[152:155], v[172:175]\n"
      "ds_write_b32 v17, v189 offset:256\n"
      "v_mfma_f32_16x16x32_bf16 v[172:175], a[56:59], v[156:159], v[172:175]\n"
      "s_waitcnt vmcnt(8) lgkmcnt(3)\n"
      "v_lshlrev_b32 v186, 16, v199\n"
      "v_and_b32 v185, 0xffff0000, v198\n"
      "v_mfma_f32_16x16x32_bf16 v[172:175], a[60:63], v[160:163], v[172:175]\n"
      "ds_write_b32 v23, v21\n"
      "v_mfma_f32_16x16x32_bf16 v[172:175], a[64:67], v[164:167], v[172:175]\n"
      "v_and_b32 v181, 0xffff0000, v196\n"
      "v_and_b32 v187, 0xffff0000, v199\n"
      "v_mfma_f32_16x16x32_bf16 v[172:175], a[68:71], v[168:171], v[172:175]\n"
      "buffer_load_dwordx4 v0, s[8:11], s46 offen offset:3072 sc1 lds\n"
      "v_mfma_f32_16x16x32_bf16 v[176:179], a[72:75], v[100:103], v[176:179]\n"
      "v_lshlrev_b32 v182, 16, v197\n"
      "s_add_u32 s38, s38, 24576\n"
      "v_mfma_f32_16x16x32_bf16 v[176:179], a[76:79], v[104:107], v[176:179]\n"
      "v_lshlrev_b32 v184, 16, v198\n"
      "v_mfma_f32_16x16x32_bf16 v[176:179], a[80:83], v[108:111], v[176:179]\n"
      "v_cvt_pk_bf16_f32 v208, v172, v173\n"
      "v_mfma_f32_16x16x32_bf16 v[176:179], a[84:87], v[112:115], v[176:179]\n"
      "v_mul_f32 v219, v172, v172\n"
      "v_mfma_f32_16x16x32_bf16 v[176:179], a[88:91], v[116:119], v[176:179]\n"
      "v_lshlrev_b32 v180, 16, v196\n"
      "v_and_b32 v183, 0xffff0000, v197\n"
      "v_mfma_f32_16x16x32_bf16 v[176:179], a[92:95], v[120:123], v[176:179]\n"
      "v_fmac_f32 v219, v173, v173\n"
      "v_mfma_f32_16x16x32_bf16 v[176:179], a[96:99], v[124:127], v[176:179]\n"
      "buffer_load_dwordx4 v[196:199], v13, s[8:11], s47 offen offset:1024 sc1\n"
      "v_mfma_f32_16x16x32_bf16 v[176:179], a[100:103], v[128:131], v[176:179]\n"
      "ds_read_b32 v22, v24\n"
      "v_mfma_f32_16x16x32_bf16 v[176:179], a[104:107], v[132:135], v[176:179]\n"
      "v_fmac_f32 v219, v174, v174\n"
      "v_mfma_f32_16x16x32_bf16 v[176:179], a[108:111], v[136:139], v[176:179]\n"
      "v_fmac_f32 v219, v175, v175\n"
      "v_mfma_f32_16x16x32_bf16 v[176:179], a[112:115], v[140:143], v[176:179]\n"
      "buffer_load_dwordx4 v0, s[8:11], s46 offen offset:0 sc1 lds\n"
      "v_mfma_f32_16x16x32_bf16 v[176:179], a[116:119], v[144:147], v[176:179]\n"
      "v_mfma_f32_16x16x32_bf16 v[176:179], a[120:123], v[148:151], v[176:179]\n"
      "s_waitcnt lgkmcnt(0)\n"
      "v_cmp_gt_i32 vcc, v21, v22\n"
      "v_add_f32_e64 v218, v172, v173\n"
      "v_mfma_f32_16x16x32_bf16 v[176:179], a[124:127], v[152:155], v[176:179]\n"
      "v_add_f32_e64 v218, v218, v174\n"
      "v_add_f32_e64 v218, v218, v175\n"
      "v_mfma_f32_16x16x32_bf16 v[176:179], a[128:131], v[156:159], v[176:179]\n"
      "buffer_load_dwordx4 v0, s[8:11], s46 offen offset:1024 sc1 lds\n"
      "v_mfma_f32_16x16x32_bf16 v[176:179], a[132:135], v[160:163], v[176:179]\n"
      "s_cbranch_vccnz .Lsp_vz0_26\n"
      ".Lbk_vz0_26:\n"
      "ds_read_b128 v[204:207], v19\n"
      "v_mfma_f32_16x16x32_bf16 v[176:179], a[136:139], v[164:167], v[176:179]\n"
      "v_cvt_pk_bf16_f32 v209, v174, v175\n"
      "v_add_u32 v21, 1, v21\n"
      "v_mfma_f32_16x16x32_bf16 v[176:179], a[140:143], v[168:171], v[176:179]\n"
      "s_waitcnt vmcnt(10)\n"
      "ds_write_b32 v23, v21\n"
      "v_mfma_f32_16x16x32_bf16 v[180:183], a[144:147], v[100:103], v[180:183]\n"
      "v_mfma_f32_16x16x32_bf16 v[180:183], a[148:151], v[104:107], v[180:183]\n"
      "buffer_load_dwordx4 v0, s[8:11], s46 offen offset:2048 sc1 lds\n"
      "v_mfma_f32_16x16x32_bf16 v[180:183], a[152:155], v[108:111], v[180:183]\n"
      "s_add_u32 s49, s49, 24576\n"
      "s_add_u32 m0, s40, 0\n"
      "v_mfma_f32_16x16x32_bf16 v[180:183], a[156:159], v[112:115], v[180:183]\n"
      "v_mfma_f32_16x16x32_bf16 v[180:183], a[160:163], v[116:119], v[180:183]\n"
      "s_waitcnt lgkmcnt(1)\n"
      "v_add_f32 v204, v204, v206\n"
      "v_cvt_pk_bf16_f32 v210, v176, v177\n"
      "v_mfma_f32_16x16x32_bf16 v[180:183], a[164:167], v[120:123], v[180:183]\n"
      "buffer_load_dwordx4 v1, s[8:11], s46 offen offset:0 sc1 lds\n"
      "v_mfma_f32_16x16x32_bf16 v[180:183], a[168:171], v[124:127], v[180:183]\n"
      "v_add_f32 v205, v205, v207\n"
      "v_add_f32_dpp v204, v204, v204 quad_perm:[1,0,3,2] row_mask:0xf bank_mask:0xf\n"
      "v_mfma_f32_16x16x32_bf16 v[180:183], a[172:175], v[128:131], v[180:183]\n"
      "v_fmac_f32 v219, v176, v176\n"
      "v_mfma_f32_16x16x32_bf16 v[180:183], a[176:179], v[132:135], v[180:183]\n"
      "v_add_f32_dpp v204, v204, v204 quad_perm:[2,3,0,1] row_mask:0xf bank_mask:0xf\n"
      "v_cvt_pk_bf16_f32 v211, v178, v179\n"
      "v_mfma_f32_16x16x32_bf16 v[180:183], a[180:183], v[136:139], v[180:183]\n"
      "buffer_store_dwordx4 v[208:211], v15, s[8:11], s38 offen sc1\n"
      "v_mfma_f32_16x16x32_bf16 v[180:183], a[184:187], v[140:143], v[180:183]\n"
      "ds_read_b32 v22, v24\n"
      "v_add_f32_dpp v205, v205, v205 quad_perm:[1,0,3,2] row_mask:0xf bank_mask:0xf\n"
      "v_mfma_f32_16x16x32_bf16 v[180:183], a[188:191], v[144:147], v[180:183]\n"
      "s_add_u32 s50, s50, 24576\n"
      "v_add_f32_dpp v205, v205, v205 quad_perm:[2,3,0,1] row_mask:0xf bank_mask:0xf\n"
      "v_fmac_f32 v219, v177, v177\n"
      "v_mfma_f32_16x16x32_bf16 v[180:183], a[192:195], v[148:151], v[180:183]\n"
      "v_add_f32_dpp v205, v205, v205 row_half_mirror row_mask:0xf bank_mask:0xf\n"
      "v_fmac_f32 v219, v178, v178\n"
      "v_mfma_f32_16x16x32_bf16 v[180:183], a[196:199], v[152:155], v[180:183]\n"
      ".Lv_p1:\n"
      "s_cbranch_execz .Lv_pr\n"
      "v_add_f32_dpp v204, v204, v204 row_half_mirror row_mask:0xf bank_mask:0xf\n"
      "v_mfma_f32_16x16x32_bf16 v[180:183], a[200:203], v[156:159], v[180:183]\n"
      "v_mul_f32 v204, v27, v204\n"
      "v_fmaak_f32 v205, v27, v205, 0x36a7c5ac\n"
      "v_mfma_f32_16x16x32_bf16 v[180:183], a[204:207], v[160:163], v[180:183]\n"
      "s_waitcnt vmcnt(12) lgkmcnt(1)\n"
      "buffer_store_dword v25, v12, s[8:11], s50 offen sc1\n"
      "v_mfma_f32_16x16x32_bf16 v[180:183], a[208:211], v[164:167], v[180:183]\n"
      "s_waitcnt lgkmcnt(0)\n"
      "v_cmp_gt_i32 vcc, v21, v22\n"
      "v_add_f32 v218, v218, v176\n"
      "v_mfma_f32_16x16x32_bf16 v[180:183], a[212:215], v[168:171], v[180:183]\n"
      "v_add_f32 v218, v218, v177\n"
      "v_add_f32_e64 v218, v218, v178\n"
      "v_mfma_f32_16x16x32_bf16 v[184:187], a[216:219], v[100:103], v[184:187]\n"
      "buffer_store_dwordx2 v[204:205], v20, s[8:11], s49 offen sc1\n"
      "v_mfma_f32_16x16x32_bf16 v[184:187], a[220:223], v[104:107], v[184:187]\n"
      "s_sub_u32 s47, s46, s45\n"
      "s_cbranch_vccnz .Lsp_vy1_27\n"
      ".Lbk_vy1_27:\n"
      "v_mfma_f32_16x16x32_bf16 v[184:187], a[224:227], v[108:111], v[184:187]\n"
      "s_waitcnt vmcnt(11)\n"
      "ds_read_b128 v[104:107], v10 offset:29696\n"
      "v_and_b32 v177, 0xffff0000, v194\n"
      "v_mfma_f32_16x16x32_bf16 v[184:187], a[228:231], v[112:115], v[184:187]\n"
      "v_fmac_f32 v219, v179, v179\n"
      "v_mfma_f32_16x16x32_bf16 v[184:187], a[232:235], v[116:119], v[184:187]\n"
      "v_fmac_f32 v219, v180, v180\n"
      "v_add_f32 v218, v218, v179\n"
      "v_mfma_f32_16x16x32_bf16 v[184:187], a[236:239], v[120:123], v[184:187]\n"
      "v_cvt_pk_bf16_f32 v212, v180, v181\n"
      "v_and_b32 v179, 0xffff0000, v195\n"
      "v_mfma_f32_16x16x32_bf16 v[184:187], a[240:243], v[124:127], v[184:187]\n"
      "ds_read_b128 v[120:123], v10 offset:33792\n"
      "v_lshlrev_b32 v174, 16, v193\n"
      "v_mfma_f32_16x16x32_bf16 v[184:187], a[244:247], v[128:131], v[184:187]\n"
      "ds_read_b128 v[112:115], v10 offset:31744\n"
      "v_add_f32 v218, v218, v180\n"
      "v_mfma_f32_16x16x32_bf16 v[184:187], a[248:251], v[132:135], v[184:187]\n"
      "ds_read_b128 v[100:103], v10 offset:28672\n"
      "v_add_f32 v218, v218, v181\n"
      "v_mfma_f32_16x16x32_bf16 v[184:187], a[252:255], v[136:139], v[184:187]\n"
      "ds_read_b128 v[124:127], v10 offset:34816\n"
      "v_mfma_f32_16x16x32_bf16 v[184:187], v[32:35], v[140:143], v[184:187]\n"
      "ds_read_b128 v[108:111], v10 offset:30720\n"
      "v_and_b32 v175, 0xffff0000, v193\n"
      "v_mfma_f32_16x16x32_bf16 v[184:187], v[36:39], v[144:147], v[184:187]\n"
      "ds_read_b128 v[116:119], v10 offset:32768\n"
      "v_fmac_f32 v219, v181, v181\n"
      "v_mfma_f32_16x16x32_bf16 v[184:187], v[40:43], v[148:151], v[184:187]\n"
      "v_add_f32 v218, v218, v182\n"
      "v_mfma_f32_16x16x32_bf16 v[184:187], v[44:47], v[152:155], v[184:187]\n"
      "ds_read2_b32 v[132:133], v8 offset0:0 offset1:32\n"
      "v_and_b32 v173, 0xffff0000, v192\n"
      "v_mfma_f32_16x16x32_bf16 v[184:187], v[48:51], v[156:159], v[184:187]\n"
      "ds_read_b128 v[128:131], v10 offset:35840\n"
      "v_lshlrev_b32 v178, 16, v195\n"
      "v_mfma_f32_16x16x32_bf16 v[184:187], v[52:55], v[160:163], v[184:187]\n"
      "ds_read_b128 v[156:159], v10 offset:25600\n"
      "v_lshlrev_b32 v172, 16, v192\n"
      "v_mfma_f32_16x16x32_bf16 v[184:187], v[56:59], v[164:167], v[184:187]\n"
      "ds_read_b128 v[144:147], v10 offset:22528\n"
      "v_lshlrev_b32 v176, 16, v194\n"
      "v_mfma_f32_16x16x32_bf16 v[184:187], v[60:63], v[168:171], v[184:187]\n"
      "s_waitcnt lgkmcnt(5)\n"
      "buffer_load_dwordx4 v[192:195], v13, s[8:11], s47 offen sc1\n"
      "v_mfma_f32_16x16x32_bf16 v[188:191], v[64:67], v[100:103], 0\n"
      "ds_read2_b32 v[134:135], v8 offset0:64 offset1:96\n"
      "v_fmac_f32 v219, v182, v182\n"
      "v_mfma_f32_16x16x32_bf16 v[188:191], v[68:71], v[104:107], v[188:191]\n"
      "ds_read_b128 v[148:151], v10 offset:23552\n"
      "v_add_f32 v218, v218, v183\n"
      "v_mfma_f32_16x16x32_bf16 v[188:191], v[72:75], v[108:111], v[188:191]\n"
      "s_mov_b32 s46, s36\n"
      "v_cvt_pk_bf16_f32 v213, v182, v183\n"
      "v_cvt_pk_bf16_f32 v215, v186, v187\n"
      "v_mfma_f32_16x16x32_bf16 v[188:191], v[76:79], v[112:115], v[188:191]\n"
      "s_waitcnt lgkmcnt(4)\n"
      "ds_read_b128 v[160:163], v10 offset:26624\n"
      "v_fmac_f32 v219, v183, v183\n"
      "v_mfma_f32_16x16x32_bf16 v[188:191], v[80:83], v[116:119], v[188:191]\n"
      "ds_read_b128 v[140:143], v10 offset:21504\n"
      "v_fmac_f32 v219, v184, v184\n"
      "v_mfma_f32_16x16x32_bf16 v[188:191], v[84:87], v[120:123], v[188:191]\n"
      "ds_read2_b32 v[168:169], v9 offset0:0 offset1:32\n"
      "v_add_f32 v218, v218, v184\n"
      "v_mfma_f32_16x16x32_bf16 v[188:191], v[88:91], v[124:127], v[188:191]\n"
      "ds_read_b128 v[152:155], v10 offset:24576\n"
      "v_fmac_f32 v219, v185, v185\n"
      "v_mfma_f32_16x16x32_bf16 v[188:191], v[92:95], v[128:131], v[188:191]\n"
      "s_waitcnt lgkmcnt(5)\n"
      "ds_read_b128 v[136:139], v10 offset:20480\n"
      "v_add_f32 v218, v218, v185\n"
      "v_mfma_f32_16x16x32_bf16 v[188:191], v[96:99], v[132:135], v[188:191]\n"
      "s_cmp_eq_u32 s36, s37\n"
      "s_cselect_b32 s48, 49152, s51\n"
      "ds_read_b128 v[164:167], v10 offset:27648\n"
      "v_mfma_f32_16x16x32_bf16 v[172:175], a[0:3], v[100:103], v[172:175]\n"
      "ds_read2_b32 v[170:171], v9 offset0:64 offset1:96\n"
      "v_cvt_pk_bf16_f32 v214, v184, v185\n"
      "v_mfma_f32_16x16x32_bf16 v[172:175], a[4:7], v[104:107], v[172:175]\n"
      "v_fmac_f32 v219, v186, v186\n"
      "v_add_f32 v218, v218, v186\n"
      "v_mfma_f32_16x16x32_bf16 v[172:175], a[8:11], v[108:111], v[172:175]\n"
      "ds_write_b32 v17, v189 offset:256\n"
      "v_mfma_f32_16x16x32_bf16 v[172:175], a[12:15], v[112:115], v[172:175]\n"
      "v_add_f32 v218, v218, v187\n"
      "v_add_u32 v21, 1, v21\n"
      "v_mfma_f32_16x16x32_bf16 v[172:175], a[16:19], v[116:119], v[172:175]\n"
      "buffer_store_dwordx4 v[212:215], v15, s[8:11], s38 offen offset:1024 sc1\n"
      "v_mfma_f32_16x16x32_bf16 v[172:175], a[20:23], v[120:123], v[172:175]\n"
      "ds_write_b32 v17, v190 offset:512\n"
      "v_mfma_f32_16x16x32_bf16 v[172:175], a[24:27], v[124:127], v[172:175]\n"
      "v_fmac_f32 v219, v187, v187\n"
      "v_mfma_f32_16x16x32_bf16 v[172:175], a[28:31], v[128:131], v[172:175]\n"
      "s_add_u32 s36, s36, s48\n"
      "ds_write_b32 v18, v218\n"
      "v_mfma_f32_16x16x32_bf16 v[172:175], a[32:35], v[132:135], v[172:175]\n"
      "s_waitcnt vmcnt(11) lgkmcnt(3)\n"
      "v_cmp_ne_u32 vcc, s21, v28\n"
      "v_mfma_f32_16x16x32_bf16 v[172:175], a[36:39], v[136:139], v[172:175]\n"
      "ds_write_b32 v17, v188 offset:0\n"
      "v_mfma_f32_16x16x32_bf16 v[172:175], a[40:43], v[140:143], v[172:175]\n"
      "s_waitcnt vmcnt(9) lgkmcnt(3)\n"
      "s_min_u32 s36, s36, s6\n"
      "v_lshlrev_b32 v182, 16, v197\n"
      "v_mfma_f32_16x16x32_bf16 v[172:175], a[44:47], v[144:147], v[172:175]\n"
      "ds_write_b32 v17, v191 offset:768\n"
      "v_mfma_f32_16x16x32_bf16 v[172:175], a[48:51], v[148:151], v[172:175]\n"
      "s_cbranch_vccnz .Lsp_v1_28\n"
      ".Lbk_v1_28:\n"
      "v_and_b32 v181, 0xffff0000, v196\n"
      "v_mfma_f32_16x16x32_bf16 v[172:175], a[52:55], v[152:155], v[172:175]\n"
      "ds_write_b32 v18, v219 offset:4\n"
      "v_mfma_f32_16x16x32_bf16 v[172:175], a[56:59], v[156:159], v[172:175]\n"
      "buffer_load_dword v28, v11, s[8:11], s36 offen sc1\n"
      "v_mfma_f32_16x16x32_bf16 v[172:175], a[60:63], v[160:163], v[172:175]\n"
      "s_add_u32 m0, s39, 20480\n"
      "ds_write_b32 v23, v21\n"
      "v_mfma_f32_16x16x32_bf16 v[172:175], a[64:67], v[164:167], v[172:175]\n"
      "v_and_b32 v187, 0xffff0000, v199\n"
      "v_and_b32 v183, 0xffff0000, v197\n"
      "v_mfma_f32_16x16x32_bf16 v[172:175], a[68:71], v[168:171], v[172:175]\n"
      "buffer_load_dwordx4 v0, s[8:11], s46 offen offset:0 sc1 lds\n"
      "v_mfma_f32_16x16x32_bf16 v[176:179], a[72:75], v[100:103], v[176:179]\n"
      "s_add_u32 s49, s49, 24576\n"
      "v_mfma_f32_16x16x32_bf16 v[176:179], a[76:79], v[104:107], v[176:179]\n"
      "v_mfma_f32_16x16x32_bf16 v[176:179], a[80:83], v[108:111], v[176:179]\n"
      "v_cvt_pk_bf16_f32 v209, v174, v175\n"
      "v_mfma_f32_16x16x32_bf16 v[176:179], a[84:87], v[112:115], v[176:179]\n"
      "buffer_load_dwordx4 v0, s[8:11], s46 offen offset:1024 sc1 lds\n"
      "v_mfma_f32_16x16x32_bf16 v[176:179], a[88:91], v[116:119], v[176:179]\n"
      "v_add_f32 v218, v172, v173\n"
      "v_mfma_f32_16x16x32_bf16 v[176:179], a[92:95], v[120:123], v[176:179]\n"
      "v_add_f32 v218, v218, v174\n"
      "v_lshlrev_b32 v180, 16, v196\n"
      "v_mfma_f32_16x16x32_bf16 v[176:179], a[96:99], v[124:127], v[176:179]\n"
      "ds_read_b32 v22, v24\n"
      "v_lshlrev_b32 v186, 16, v199\n"
      "v_mfma_f32_16x16x32_bf16 v[176:179], a[100:103], v[128:131], v[176:179]\n"
      "buffer_load_dwordx4 v0, s[8:11], s46 offen offset:2048 sc1 lds\n"
      "v_mfma_f32_16x16x32_bf16 v[176:179], a[104:107], v[132:135], v[176:179]\n"
      "v_mul_f32 v219, v172, v172\n"
      "v_fmac_f32 v219, v173, v173\n"
      "v_mfma_f32_16x16x32_bf16 v[176:179], a[108:111], v[136:139], v[176:179]\n"
      "v_lshlrev_b32 v184, 16, v198\n"
      "v_mfma_f32_16x16x32_bf16 v[176:179], a[112:115], v[140:143], v[176:179]\n"
      "v_fmac_f32_e64 v219, v174, v174\n"
      "v_mfma_f32_16x16x32_bf16 v[176:179], a[116:119], v[144:147], v[176:179]\n"
      "s_waitcnt lgkmcnt(0)\n"
      "v_cmp_gt_i32 vcc, v21, v22\n"
      "v_add_f32_e64 v218, v218, v175\n"
      "v_mfma_f32_16x16x32_bf16 v[176:179], a[120:123], v[148:151], v[176:179]\n"
      "v_mfma_f32_16x16x32_bf16 v[176:179], a[124:127], v[152:155], v[176:179]\n"
      "v_and_b32 v185, 0xffff0000, v198\n"
      "v_cvt_pk_bf16_f32 v208, v172, v173\n"
      "v_mfma_f32_16x16x32_bf16 v[176:179], a[128:131], v[156:159], v[176:179]\n"
      "buffer_load_dwordx4 v[196:199], v13, s[8:11], s47 offen offset:1024 sc1\n"
      "v_mfma_f32_16x16x32_bf16 v[176:179], a[132:135], v[160:163], v[176:179]\n"
      "s_cbranch_vccnz .Lsp_vz1_29\n"
      ".Lbk_vz1_29:\n"
      "ds_read_b128 v[204:207], v19\n"
      "v_mfma_f32_16x16x32_bf16 v[176:179], a[136:139], v[164:167], v[176:179]\n"
      "v_add_u32 v21, 1, v21\n"
      "v_mfma_f32_16x16x32_bf16 v[176:179], a[140:143], v[168:171], v[176:179]\n"
      "s_waitcnt vmcnt(10)\n"
      "ds_write_b32 v23, v21\n"
      "v_mfma_f32_16x16x32_bf16 v[180:183], a[144:147], v[100:103], v[180:183]\n"
      "v_fmac_f32 v219, v175, v175\n"
      "v_mfma_f32_16x16x32_bf16 v[180:183], a[148:151], v[104:107], v[180:183]\n"
      "buffer_load_dwordx4 v0, s[8:11], s46 offen offset:3072 sc1 lds\n"
      "v_mfma_f32_16x16x32_bf16 v[180:183], a[152:155], v[108:111], v[180:183]\n"
      "v_cvt_pk_bf16_f32 v210, v176, v177\n"
      "v_fmac_f32 v219, v176, v176\n"
      "v_mfma_f32_16x16x32_bf16 v[180:183], a[156:159], v[112:115], v[180:183]\n"
      "s_add_u32 m0, s40, 20480\n"
      "v_fmac_f32 v219, v177, v177\n"
      "v_mfma_f32_16x16x32_bf16 v[180:183], a[160:163], v[116:119], v[180:183]\n"
      "s_waitcnt lgkmcnt(1)\n"
      "v_add_f32 v204, v204, v206\n"
      "v_add_f32 v205, v205, v207\n"
      "v_mfma_f32_16x16x32_bf16 v[180:183], a[164:167], v[120:123], v[180:183]\n"
      "v_add_f32_dpp v204, v204, v204 quad_perm:[1,0,3,2] row_mask:0xf bank_mask:0xf\n"
      "v_fmac_f32 v219, v178, v178\n"
      "v_mfma_f32_16x16x32_bf16 v[180:183], a[168:171], v[124:127], v[180:183]\n"
      "buffer_load_dwordx4 v1, s[8:11], s46 offen offset:0 sc1 lds\n"
      "v_mfma_f32_16x16x32_bf16 v[180:183], a[172:175], v[128:131], v[180:183]\n"
      "v_add_f32_dpp v205, v205, v205 quad_perm:[1,0,3,2] row_mask:0xf bank_mask:0xf\n"
      "v_add_f32_dpp v204, v204, v204 quad_perm:[2,3,0,1] row_mask:0xf bank_mask:0xf\n"
      "v_mfma_f32_16x16x32_bf16 v[180:183], a[176:179], v[132:135], v[180:183]\n"
      "v_add_f32_dpp v204, v204, v204 row_half_mirror row_mask:0xf bank_mask:0xf\n"
      "v_cvt_pk_bf16_f32 v211, v178, v179\n"
      "v_mfma_f32_16x16x32_bf16 v[180:183], a[180:183], v[136:139], v[180:183]\n"
      "s_add_u32 s38, s38, 24576\n"
      "v_mul_f32 v204, v27, v204\n"
      "v_add_f32_dpp v205, v205, v205 quad_perm:[2,3,0,1] row_mask:0xf bank_mask:0xf\n"
      "v_mfma_f32_16x16x32_bf16 v[180:183], a[184:187], v[140:143], v[180:183]\n"
      "ds_read_b32 v22, v24\n"
      "v_add_f32 v218, v218, v176\n"
      "v_mfma_f32_16x16x32_bf16 v[180:183], a[188:191], v[144:147], v[180:183]\n"
      "v_add_f32_dpp v205, v205, v205 row_half_mirror row_mask:0xf bank_mask:0xf\n"
      "v_fmaak_f32 v205, v27, v205, 0x36a7c5ac\n"
      "v_mfma_f32_16x16x32_bf16 v[180:183], a[192:195], v[148:151], v[180:183]\n"
      "buffer_store_dwordx2 v[204:205], v20, s[8:11], s49 offen sc1\n"
      "v_mfma_f32_16x16x32_bf16 v[180:183], a[196:199], v[152:155], v[180:183]\n"
      "s_add_u32 s50, s50, 24576\n"
      "v_add_f32 v218, v218, v177\n"
      "v_mfma_f32_16x16x32_bf16 v[180:183], a[200:203], v[156:159], v[180:183]\n"
      "v_add_f32 v218, v218, v178\n"
      "v_mfma_f32_16x16x32_bf16 v[180:183], a[204:207], v[160:163], v[180:183]\n"
      "s_waitcnt lgkmcnt(0)\n"
      "v_add_f32 v218, v218, v179\n"
      "v_cmp_gt_i32 vcc, v21, v22\n"
      "v_mfma_f32_16x16x32_bf16 v[180:183], a[208:211], v[164:167], v[180:183]\n"
      "s_waitcnt vmcnt(10)\n"
      "buffer_store_dword v25, v12, s[8:11], s50 offen sc1\n"
      "v_mfma_f32_16x16x32_bf16 v[180:183], a[212:215], v[168:171], v[180:183]\n"
      "v_fmac_f32 v219, v179, v179\n"
      "v_mfma_f32_16x16x32_bf16 v[184:187], a[216:219], v[100:103], v[184:187]\n"
      "s_cbranch_vccnz .Lsp_vy0_30\n"
      ".Lbk_vy0_30:\n"
      "s_sub_u32 s47, s46, s45\n"
      "v_mfma_f32_16x16x32_bf16 v[184:187], a[220:223], v[104:107], v[184:187]\n"
      "s_waitcnt vmcnt(10)\n"
      "v_lshlrev_b32 v174, 16, v193\n"
      "v_and_b32 v179, 0xffff0000, v195\n"
      "v_mfma_f32_16x16x32_bf16 v[184:187], a[224:227], v[108:111], v[184:187]\n"
      "ds_read_b128 v[100:103], v0 offset:8192\n"
      "v_cvt_pk_bf16_f32 v213, v182, v183\n"
      "v_mfma_f32_16x16x32_bf16 v[184:187], a[228:231], v[112:115], v[184:187]\n"
      "ds_read_b128 v[108:111], v0 offset:10240\n"
      "v_and_b32 v173, 0xffff0000, v192\n"
      "v_mfma_f32_16x16x32_bf16 v[184:187], a[232:235], v[116:119], v[184:187]\n"
      "v_fmac_f32 v219, v180, v180\n"
      "v_mfma_f32_16x16x32_bf16 v[184:187], a[236:239], v[120:123], v[184:187]\n"
      "ds_read_b128 v[120:123], v0 offset:13312\n"
      "v_add_f32 v218, v218, v180\n"
      "v_mfma_f32_16x16x32_bf16 v[184:187], a[240:243], v[124:127], v[184:187]\n"
      "v_add_f32 v218, v218, v181\n"
      "v_add_f32 v218, v218, v182\n"
      "v_mfma_f32_16x16x32_bf16 v[184:187], a[244:247], v[128:131], v[184:187]\n"
      "ds_read_b128 v[128:131], v0 offset:15360\n"
      "v_lshlrev_b32 v178, 16, v195\n"
      "v_mfma_f32_16x16x32_bf16 v[184:187], a[248:251], v[132:135], v[184:187]\n"
      "v_mfma_f32_16x16x32_bf16 v[184:187], a[252:255], v[136:139], v[184:187]\n"
      "ds_read_b128 v[104:107], v0 offset:9216\n"
      "v_lshlrev_b32 v172, 16, v192\n"
      "v_mfma_f32_16x16x32_bf16 v[184:187], v[32:35], v[140:143], v[184:187]\n"
      "buffer_store_dwordx4 v[208:211], v15, s[8:11], s38 offen sc1\n"
      "v_mfma_f32_16x16x32_bf16 v[184:187], v[36:39], v[144:147], v[184:187]\n"
      "s_mov_b32 s46, s36\n"
      "ds_read_b128 v[112:115], v0 offset:11264\n"
      "v_fmac_f32 v219, v181, v181\n"
      "v_mfma_f32_16x16x32_bf16 v[184:187], v[40:43], v[148:151], v[184:187]\n"
      "ds_read2_b32 v[134:135], v2 offset0:64 offset1:96\n"
      "v_cvt_pk_bf16_f32 v212, v180, v181\n"
      "v_mfma_f32_16x16x32_bf16 v[184:187], v[44:47], v[152:155], v[184:187]\n"
      "ds_read2_b32 v[132:133], v2 offset0:0 offset1:32\n"
      "s_add_u32 s36, s36, 24576\n"
      "v_mfma_f32_16x16x32_bf16 v[184:187], v[48:51], v[156:159], v[184:187]\n"
      "ds_read_b128 v[116:119], v0 offset:12288\n"
      "v_fmac_f32 v219, v182, v182\n"
      "v_mfma_f32_16x16x32_bf16 v[184:187], v[52:55], v[160:163], v[184:187]\n"
      "ds_read_b128 v[124:127], v0 offset:14336\n"
      "v_add_f32 v218, v218, v183\n"
      "v_mfma_f32_16x16x32_bf16 v[184:187], v[56:59], v[164:167], v[184:187]\n"
      "s_waitcnt lgkmcnt(5)\n"
      "ds_read_b128 v[160:163], v0 offset:6144\n"
      "v_and_b32 v177, 0xffff0000, v194\n"
      "v_mfma_f32_16x16x32_bf16 v[184:187], v[60:63], v[168:171], v[184:187]\n"
      "s_sub_u32 s20, s20, 1\n"
      "s_cbranch_scc0 .Lv_loop\n"
      ".Lv_tail:\n"
      "s_nop 8\n"
      "v_add_f32 v218, v218, v184\n"
      "v_add_f32 v218, v218, v185\n"
      "v_fmac_f32 v219, v183, v183\n"
      "v_fmac_f32 v219, v184, v184\n"
      "v_cvt_pk_bf16_f32 v215, v186, v187\n"
      "v_add_f32 v218, v218, v186\n"
      "v_add_f32 v218, v218, v187\n"
      "v_fmac_f32 v219, v185, v185\n"
      "v_cvt_pk_bf16_f32 v214, v184, v185\n"
      "ds_write_b32 v18, v218\n"
      "v_fmac_f32 v219, v186, v186\n"
      "buffer_store_dwordx4 v[212:215], v15, s[8:11], s38 offen offset:1024 sc1\n"
      "v_fmac_f32 v219, v187, v187\n"
      "ds_write_b32 v18, v219 offset:4\n"
      "s_waitcnt lgkmcnt(0)\n"
      "s_barrier\n"
      "s_add_u32 s38, s38, 24576\n"
      "ds_read_b128 v[204:207], v19\n"
      "s_add_u32 s49, s49, 24576\n"
      "s_waitcnt lgkmcnt(0)\n"
      "v_add_f32 v204, v204, v206\n"
      "v_add_f32 v205, v205, v207\n"
      "s_nop 0\n"
      "v_add_f32_dpp v204, v204, v204 quad_perm:[1,0,3,2] row_mask:0xf bank_mask:0xf\n"
      "s_nop 1\n"
      "v_add_f32_dpp v204, v204, v204 quad_perm:[2,3,0,1] row_mask:0xf bank_mask:0xf\n"
      "v_add_f32_dpp v205, v205, v205 quad_perm:[1,0,3,2] row_mask:0xf bank_mask:0xf\n"
      "s_add_u32 s50, s50, 24576\n"
      "s_nop 0\n"
      "v_add_f32_dpp v205, v205, v205 quad_perm:[2,3,0,1] row_mask:0xf bank_mask:0xf\n"
      "s_nop 1\n"
      "v_add_f32_dpp v205, v205, v205 row_half_mirror row_mask:0xf bank_mask:0xf\n"
      "v_add_f32_dpp v204, v204, v204 row_half_mirror row_mask:0xf bank_mask:0xf\n"
      "v_mul_f32 v204, v27, v204\n"
      "v_fmaak_f32 v205, v27, v205, 0x36a7c5ac\n"
      "s_waitcnt vmcnt(3)\n"
      "buffer_store_dword v25, v12, s[8:11], s50 offen sc1\n"
      "buffer_store_dwordx2 v[204:205], v20, s[8:11], s49 offen sc1\n"
      "s_add_u32 s49, s49, 24576\n"
      "s_add_u32 s50, s50, 24576\n"
      "s_waitcnt vmcnt(0)\n"
      "buffer_store_dword v25, v12, s[8:11], s50 offen sc1\n"
      "s_add_u32 s50, s50, 24576\n"
      "s_endpgm\n"
      ".Lsp_w0_1:\n"
      "s_mov_b32 s22, s23\n"
      ".Lsl_w0_1:\n"
      "buffer_load_dword v28, v11, s[8:11], s46 offen sc1\n"
      "s_waitcnt vmcnt(0)\n"
      "v_cmp_ne_u32 vcc, s21, v28\n"
      "s_cbranch_vccz .Lbk_w0_1\n"
      "s_sleep 4\n"
      "s_sub_u32 s22, s22, 1\n"
      "s_cbranch_scc0 .Lsl_w0_1\n"
      "s_mov_b32 s23, 0\n"
      "s_branch .Lbk_w0_1\n"
      ".Lsp_w1_2:\n"
      "s_mov_b32 s22, s23\n"
      ".Lsl_w1_2:\n"
      "buffer_load_dword v28, v11, s[8:11], s46 offen sc1\n"
      "s_waitcnt vmcnt(0)\n"
      "v_cmp_ne_u32 vcc, s21, v28\n"
      "s_cbranch_vccz .Lbk_w1_2\n"
      "s_sleep 4\n"
      "s_sub_u32 s22, s22, 1\n"
      "s_cbranch_scc0 .Lsl_w1_2\n"
      "s_mov_b32 s23, 0\n"
      "s_branch .Lbk_w1_2\n"
      ".Lsp_wy0_3:\n"
      "s_lshl_b32 s22, s23, 4\n"
      ".Lsl_wy0_3:\n"
      "ds_read_b32 v22, v24\n"
      "s_waitcnt lgkmcnt(0)\n"
      "v_cmp_gt_i32 vcc, v21, v22\n"
      "s_cbranch_vccz .Lbk_wy0_3\n"
      "s_sub_u32 s22, s22, 1\n"
      "s_cbranch_scc0 .Lsl_wy0_3\n"
      "s_mov_b32 s23, 0\n"
      "s_branch .Lbk_wy0_3\n"
      ".Lsp_w0_4:\n"
      "s_mov_b32 s22, s23\n"
      "buffer_load_dword v28, v11, s[8:11], s46 offen sc1\n"
      "s_waitcnt vmcnt(0)\n"
      "v_cmp_ne_u32 vcc, s21, v28\n"
      "s_add_u32 s30, s50, 0\n"
      "buffer_store_dword v25, v12, s[8:11], s30 offen sc1\n"
      "s_add_u32 s30, s50, 24576\n"
      "buffer_store_dword v25, v12, s[8:11], s30 offen sc1\n"
      "s_cbranch_vccz .Lbk_w0_4\n"
      ".Lsl_w0_4:\n"
      "buffer_load_dword v28, v11, s[8:11], s46 offen sc1\n"
      "s_waitcnt vmcnt(0)\n"
      "v_cmp_ne_u32 vcc, s21, v28\n"
      "s_cbranch_vccz .Lbk_w0_4\n"
      "s_sleep 4\n"
      "s_sub_u32 s22, s22, 1\n"
      "s_cbranch_scc0 .Lsl_w0_4\n"
      "s_mov_b32 s23, 0\n"
      "s_branch .Lbk_w0_4\n"
      ".Lsp_wz0_5:\n"
      "s_lshl_b32 s22, s23, 4\n"
      ".Lsl_wz0_5:\n"
      "ds_read_b32 v22, v24\n"
      "s_waitcnt lgkmcnt(0)\n"
      "v_cmp_gt_i32 vcc, v21, v22\n"
      "s_cbranch_vccz .Lbk_wz0_5\n"
      "s_sub_u32 s22, s22, 1\n"
      "s_cbranch_scc0 .Lsl_wz0_5\n"
      "s_mov_b32 s23, 0\n"
      "s_branch .Lbk_wz0_5\n"
      ".Lsp_wy1_6:\n"
      "s_lshl_b32 s22, s23, 4\n"
      ".Lsl_wy1_6:\n"
      "ds_read_b32 v22, v24\n"
      "s_waitcnt lgkmcnt(0)\n"
      "v_cmp_gt_i32 vcc, v21, v22\n"
      "s_cbranch_vccz .Lbk_wy1_6\n"
      "s_sub_u32 s22, s22, 1\n"
      "s_cbranch_scc0 .Lsl_wy1_6\n"
      "s_mov_b32 s23, 0\n"
      "s_branch .Lbk_wy1_6\n"
      ".Lsp_w1_7:\n"
      "s_mov_b32 s22, s23\n"
      "buffer_load_dword v28, v11, s[8:11], s46 offen sc1\n"
      "s_waitcnt vmcnt(0)\n"
      "v_cmp_ne_u32 vcc, s21, v28\n"
      "s_add_u32 s30, s50, 24576\n"
      "buffer_store_dword v25, v12, s[8:11], s30 offen sc1\n"
      "s_cbranch_vccz .Lbk_w1_7\n"
      ".Lsl_w1_7:\n"
      "buffer_load_dword v28, v11, s[8:11], s46 offen sc1\n"
      "s_waitcnt vmcnt(0)\n"
      "v_cmp_ne_u32 vcc, s21, v28\n"
      "s_cbranch_vccz .Lbk_w1_7\n"
      "s_sleep 4\n"
      "s_sub_u32 s22, s22, 1\n"
      "s_cbranch_scc0 .Lsl_w1_7\n"
      "s_mov_b32 s23, 0\n"
      "s_branch .Lbk_w1_7\n"
      ".Lsp_wz1_8:\n"
      "s_lshl_b32 s22, s23, 4\n"
      ".Lsl_wz1_8:\n"
      "ds_read_b32 v22, v24\n"
      "s_waitcnt lgkmcnt(0)\n"
      "v_cmp_gt_i32 vcc, v21, v22\n"
      "s_cbranch_vccz .Lbk_wz1_8\n"
      "s_sub_u32 s22, s22, 1\n"
      "s_cbranch_scc0 .Lsl_wz1_8\n"
      "s_mov_b32 s23, 0\n"
      "s_branch .Lbk_wz1_8\n"
      ".Lsp_wy0_9:\n"
      "s_lshl_b32 s22, s23, 4\n"
      ".Lsl_wy0_9:\n"
      "ds_read_b32 v22, v24\n"
      "s_waitcnt lgkmcnt(0)\n"
      "v_cmp_gt_i32 vcc, v21, v22\n"
      "s_cbranch_vccz .Lbk_wy0_9\n"
      "s_sub_u32 s22, s22, 1\n"
      "s_cbranch_scc0 .Lsl_wy0_9\n"
      "s_mov_b32 s23, 0\n"
      "s_branch .Lbk_wy0_9\n"
      ".Lsp_w0_10:\n"
      "s_mov_b32 s22, s23\n"
      "buffer_load_dword v28, v11, s[8:11], s46 offen sc1\n"
      "s_waitcnt vmcnt(0)\n"
      "v_cmp_ne_u32 vcc, s21, v28\n"
      "s_add_u32 s30, s50, 0\n"
      "buffer_store_dword v25, v12, s[8:11], s30 offen sc1\n"
      "s_add_u32 s30, s50, 24576\n"
      "buffer_store_dword v25, v12, s[8:11], s30 offen sc1\n"
      "s_cbranch_vccz .Lbk_w0_10\n"
      ".Lsl_w0_10:\n"
      "buffer_load_dword v28, v11, s[8:11], s46 offen sc1\n"
      "s_waitcnt vmcnt(0)\n"
      "v_cmp_ne_u32 vcc, s21, v28\n"
      "s_cbranch_vccz .Lbk_w0_10\n"
      "s_sleep 4\n"
      "s_sub_u32 s22, s22, 1\n"
      "s_cbranch_scc0 .Lsl_w0_10\n"
      "s_mov_b32 s23, 0\n"
      "s_branch .Lbk_w0_10\n"
      ".Lsp_wz0_11:\n"
      "s_lshl_b32 s22, s23, 4\n"
      ".Lsl_wz0_11:\n"
      "ds_read_b32 v22, v24\n"
      "s_waitcnt lgkmcnt(0)\n"
      "v_cmp_gt_i32 vcc, v21, v22\n"
      "s_cbranch_vccz .Lbk_wz0_11\n"
      "s_sub_u32 s22, s22, 1\n"
      "s_cbranch_scc0 .Lsl_wz0_11\n"
      "s_mov_b32 s23, 0\n"
      "s_branch .Lbk_wz0_11\n"
      ".Lsp_wy1_12:\n"
      "s_lshl_b32 s22, s23, 4\n"
      ".Lsl_wy1_12:\n"
      "ds_read_b32 v22, v24\n"
      "s_waitcnt lgkmcnt(0)\n"
      "v_cmp_gt_i32 vcc, v21, v22\n"
      "s_cbranch_vccz .Lbk_wy1_12\n"
      "s_sub_u32 s22, s22, 1\n"
      "s_cbranch_scc0 .Lsl_wy1_12\n"
      "s_mov_b32 s23, 0\n"
      "s_branch .Lbk_wy1_12\n"
      ".Lsp_w1_13:\n"
      "s_mov_b32 s22, s23\n"
      "buffer_load_dword v28, v11, s[8:11], s46 offen sc1\n"
      "s_waitcnt vmcnt(0)\n"
      "v_cmp_ne_u32 vcc, s21, v28\n"
      "s_add_u32 s30, s50, 24576\n"
      "buffer_store_dword v25, v12, s[8:11], s30 offen sc1\n"
      "s_cbranch_vccz .Lbk_w1_13\n"
      ".Lsl_w1_13:\n"
      "buffer_load_dword v28, v11, s[8:11], s46 offen sc1\n"
      "s_waitcnt vmcnt(0)\n"
      "v_cmp_ne_u32 vcc, s21, v28\n"
      "s_cbranch_vccz .Lbk_w1_13\n"
      "s_sleep 4\n"
      "s_sub_u32 s22, s22, 1\n"
      "s_cbranch_scc0 .Lsl_w1_13\n"
      "s_mov_b32 s23, 0\n"
      "s_branch .Lbk_w1_13\n"
      ".Lsp_wz1_14:\n"
      "s_lshl_b32 s22, s23, 4\n"
      ".Lsl_wz1_14:\n"
      "ds_read_b32 v22, v24\n"
      "s_waitcnt lgkmcnt(0)\n"
      "v_cmp_gt_i32 vcc, v21, v22\n"
      "s_cbranch_vccz .Lbk_wz1_14\n"
      "s_sub_u32 s22, s22, 1\n"
      "s_cbranch_scc0 .Lsl_wz1_14\n"
      "s_mov_b32 s23, 0\n"
      "s_branch .Lbk_wz1_14\n"
      ".Lsp_wy0_15:\n"
      "s_lshl_b32 s22, s23, 4\n"
      ".Lsl_wy0_15:\n"
      "ds_read_b32 v22, v24\n"
      "s_waitcnt lgkmcnt(0)\n"
      "v_cmp_gt_i32 vcc, v21, v22\n"
      "s_cbranch_vccz .Lbk_wy0_15\n"
      "s_sub_u32 s22, s22, 1\n"
      "s_cbranch_scc0 .Lsl_wy0_15\n"
      "s_mov_b32 s23, 0\n"
      "s_branch .Lbk_wy0_15\n"
      ".Lsp_v0_16:\n"
      "s_mov_b32 s22, s23\n"
      ".Lsl_v0_16:\n"
      "buffer_load_dword v28, v11, s[8:11], s46 offen sc1\n"
      "s_waitcnt vmcnt(0)\n"
      "v_cmp_ne_u32 vcc, s21, v28\n"
      "s_cbranch_vccz .Lbk_v0_16\n"
      "s_sleep 4\n"
      "s_sub_u32 s22, s22, 1\n"
      "s_cbranch_scc0 .Lsl_v0_16\n"
      "s_mov_b32 s23, 0\n"
      "s_branch .Lbk_v0_16\n"
      ".Lsp_v1_17:\n"
      "s_mov_b32 s22, s23\n"
      ".Lsl_v1_17:\n"
      "buffer_load_dword v28, v11, s[8:11], s46 offen sc1\n"
      "s_waitcnt vmcnt(0)\n"
      "v_cmp_ne_u32 vcc, s21, v28\n"
      "s_cbranch_vccz .Lbk_v1_17\n"
      "s_sleep 4\n"
      "s_sub_u32 s22, s22, 1\n"
      "s_cbranch_scc0 .Lsl_v1_17\n"
      "s_mov_b32 s23, 0\n"
      "s_branch .Lbk_v1_17\n"
      ".Lsp_vy0_18:\n"
      "s_lshl_b32 s22, s23, 4\n"
      ".Lsl_vy0_18:\n"
      "ds_read_b32 v22, v24\n"
      "s_waitcnt lgkmcnt(0)\n"
      "v_cmp_gt_i32 vcc, v21, v22\n"
      "s_cbranch_vccz .Lbk_vy0_18\n"
      "s_sub_u32 s22, s22, 1\n"
      "s_cbranch_scc0 .Lsl_vy0_18\n"
      "s_mov_b32 s23, 0\n"
      "s_branch .Lbk_vy0_18\n"
      ".Lsp_v0_19:\n"
      "s_mov_b32 s22, s23\n"
      "buffer_load_dword v28, v11, s[8:11], s46 offen sc1\n"
      "s_waitcnt vmcnt(0)\n"
      "v_cmp_ne_u32 vcc, s21, v28\n"
      "s_add_u32 s30, s50, 24576\n"
      "buffer_store_dword v25, v12, s[8:11], s30 offen sc1\n"
      "s_cbranch_vccz .Lbk_v0_19\n"
      ".Lsl_v0_19:\n"
      "buffer_load_dword v28, v11, s[8:11], s46 offen sc1\n"
      "s_waitcnt vmcnt(0)\n"
      "v_cmp_ne_u32 vcc, s21, v28\n"
      "s_cbranch_vccz .Lbk_v0_19\n"
      "s_sleep 4\n"
      "s_sub_u32 s22, s22, 1\n"
      "s_cbranch_scc0 .Lsl_v0_19\n"
      "s_mov_b32 s23, 0\n"
      "s_branch .Lbk_v0_19\n"
      ".Lsp_vz0_20:\n"
      "s_lshl_b32 s22, s23, 4\n"
      ".Lsl_vz0_20:\n"
      "ds_read_b32 v22, v24\n"
      "s_waitcnt lgkmcnt(0)\n"
      "v_cmp_gt_i32 vcc, v21, v22\n"
      "s_cbranch_vccz .Lbk_vz0_20\n"
      "s_sub_u32 s22, s22, 1\n"
      "s_cbranch_scc0 .Lsl_vz0_20\n"
      "s_mov_b32 s23, 0\n"
      "s_branch .Lbk_vz0_20\n"
      ".Lsp_vy1_21:\n"
      "s_lshl_b32 s22, s23, 4\n"
      ".Lsl_vy1_21:\n"
      "ds_read_b32 v22, v24\n"
      "s_waitcnt lgkmcnt(0)\n"
      "v_cmp_gt_i32 vcc, v21, v22\n"
      "s_cbranch_vccz .Lbk_vy1_21\n"
      "s_sub_u32 s22, s22, 1\n"
      "s_cbranch_scc0 .Lsl_vy1_21\n"
      "s_mov_b32 s23, 0\n"
      "s_branch .Lbk_vy1_21\n"
      ".Lsp_v1_22:\n"
      "s_mov_b32 s22, s23\n"
      "buffer_load_dword v28, v11, s[8:11], s46 offen sc1\n"
      "s_waitcnt vmcnt(0)\n"
      "v_cmp_ne_u32 vcc, s21, v28\n"
      "s_add_u32 s30, s50, 24576\n"
      "buffer_store_dword v25, v12, s[8:11], s30 offen sc1\n"
      "s_cbranch_vccz .Lbk_v1_22\n"
      ".Lsl_v1_22:\n"
      "buffer_load_dword v28, v11, s[8:11], s46 offen sc1\n"
      "s_waitcnt vmcnt(0)\n"
      "v_cmp_ne_u32 vcc, s21, v28\n"
      "s_cbranch_vccz .Lbk_v1_22\n"
      "s_sleep 4\n"
      "s_sub_u32 s22, s22, 1\n"
      "s_cbranch_scc0 .Lsl_v1_22\n"
      "s_mov_b32 s23, 0\n"
      "s_branch .Lbk_v1_22\n"
      ".Lsp_vz1_23:\n"
      "s_lshl_b32 s22, s23, 4\n"
      ".Lsl_vz1_23:\n"
      "ds_read_b32 v22, v24\n"
      "s_waitcnt lgkmcnt(0)\n"
      "v_cmp_gt_i32 vcc, v21, v22\n"
      "s_cbranch_vccz .Lbk_vz1_23\n"
      "s_sub_u32 s22, s22, 1\n"
      "s_cbranch_scc0 .Lsl_vz1_23\n"
      "s_mov_b32 s23, 0\n"
      "s_branch .Lbk_vz1_23\n"
      ".Lsp_vy0_24:\n"
      "s_lshl_b32 s22, s23, 4\n"
      ".Lsl_vy0_24:\n"
      "ds_read_b32 v22, v24\n"
      "s_waitcnt lgkmcnt(0)\n"
      "v_cmp_gt_i32 vcc, v21, v22\n"
      "s_cbranch_vccz .Lbk_vy0_24\n"
      "s_sub_u32 s22, s22, 1\n"
      "s_cbranch_scc0 .Lsl_vy0_24\n"
      "s_mov_b32 s23, 0\n"
      "s_branch .Lbk_vy0_24\n"
      ".Lsp_v0_25:\n"
      "s_mov_b32 s22, s23\n"
      "buffer_load_dword v28, v11, s[8:11], s46 offen sc1\n"
      "s_waitcnt vmcnt(0)\n"
      "v_cmp_ne_u32 vcc, s21, v28\n"
      "s_add_u32 s30, s50, 24576\n"
      "buffer_store_dword v25, v12, s[8:11], s30 offen sc1\n"
      "s_cbranch_vccz .Lbk_v0_25\n"
      ".Lsl_v0_25:\n"
      "buffer_load_dword v28, v11, s[8:11], s46 offen sc1\n"
      "s_waitcnt vmcnt(0)\n"
      "v_cmp_ne_u32 vcc, s21, v28\n"
      "s_cbranch_vccz .Lbk_v0_25\n"
      "s_sleep 4\n"
      "s_sub_u32 s22, s22, 1\n"
      "s_cbranch_scc0 .Lsl_v0_25\n"
      "s_mov_b32 s23, 0\n"
      "s_branch .Lbk_v0_25\n"
      ".Lsp_vz0_26:\n"
      "s_lshl_b32 s22, s23, 4\n"
      ".Lsl_vz0_26:\n"
      "ds_read_b32 v22, v24\n"
      "s_waitcnt lgkmcnt(0)\n"
      "v_cmp_gt_i32 vcc, v21, v22\n"
      "s_cbranch_vccz .Lbk_vz0_26\n"
      "s_sub_u32 s22, s22, 1\n"
      "s_cbranch_scc0 .Lsl_vz0_26\n"
      "s_mov_b32 s23, 0\n"
      "s_branch .Lbk_vz0_26\n"
      ".Lsp_vy1_27:\n"
      "s_lshl_b32 s22, s23, 4\n"
      ".Lsl_vy1_27:\n"
      "ds_read_b32 v22, v24\n"
      "s_waitcnt lgkmcnt(0)\n"
      "v_cmp_gt_i32 vcc, v21, v22\n"
      "s_cbranch_vccz .Lbk_vy1_27\n"
      "s_sub_u32 s22, s22, 1\n"
      "s_cbranch_scc0 .Lsl_vy1_27\n"
      "s_mov_b32 s23, 0\n"
      "s_branch .Lbk_vy1_27\n"
      ".Lsp_v1_28:\n"
      "s_mov_b32 s22, s23\n"
      "buffer_load_dword v28, v11, s[8:11], s46 offen sc1\n"
      "s_waitcnt vmcnt(0)\n"
      "v_cmp_ne_u32 vcc, s21, v28\n"
      "s_add_u32 s30, s50, 24576\n"
      "buffer_store_dword v25, v12, s[8:11], s30 offen sc1\n"
      "s_cbranch_vccz .Lbk_v1_28\n"
      ".Lsl_v1_28:\n"
      "buffer_load_dword v28, v11, s[8:11], s46 offen sc1\n"
      "s_waitcnt vmcnt(0)\n"
      "v_cmp_ne_u32 vcc, s21, v28\n"
      "s_cbranch_vccz .Lbk_v1_28\n"
      "s_sleep 4\n"
      "s_sub_u32 s22, s22, 1\n"
      "s_cbranch_scc0 .Lsl_v1_28\n"
      "s_mov_b32 s23, 0\n"
      "s_branch .Lbk_v1_28\n"
      ".Lsp_vz1_29:\n"
      "s_lshl_b32 s22, s23, 4\n"
      ".Lsl_vz1_29:\n"
      "ds_read_b32 v22, v24\n"
      "s_waitcnt lgkmcnt(0)\n"
      "v_cmp_gt_i32 vcc, v21, v22\n"
      "s_cbranch_vccz .Lbk_vz1_29\n"
      "s_sub_u32 s22, s22, 1\n"
      "s_cbranch_scc0 .Lsl_vz1_29\n"
      "s_mov_b32 s23, 0\n"
      "s_branch .Lbk_vz1_29\n"
      ".Lsp_vy0_30:\n"
      "s_lshl_b32 s22, s23, 4\n"
      ".Lsl_vy0_30:\n"
      "ds_read_b32 v22, v24\n"
      "s_waitcnt lgkmcnt(0)\n"
      "v_cmp_gt_i32 vcc, v21, v22\n"
      "s_cbranch_vccz .Lbk_vy0_30\n"
      "s_sub_u32 s22, s22, 1\n"
      "s_cbranch_scc0 .Lsl_vy0_30\n"
      "s_mov_b32 s23, 0\n"
      "s_branch .Lbk_vy0_30\n"
      ".Lw_last:\n"
      :
      : "{s[8:11]}"(rsWS), "{s[12:15]}"(rsA), "{v0}"(v_l16), "{v1}"(v_l5), "{v2}"(v_ex[0]), "{v3}"(v_ex[1]), "{v4}"(v_ex[2]), "{v5}"(v_ex[3]), "{v6}"(v_ex[4]), "{v7}"(v_ex[5]), "{v8}"(v_ex[6]), "{v9}"(v_ex[7]), "{v10}"(v_l2), "{v11}"(v_fa), "{v12}"(v_ra), "{v13}"(v_rp), "{v14}"(v_rs), "{v15}"(v_xp), "{v16}"(v_xs), "{v17}"(v_ew), "{v18}"(v_sw), "{v19}"(v_sr), "{v20}"(v_ss), "{v22}"(v_al), "{v23}"(v_yw), "{v24}"(v_yr), "{v26}"(v_hi), "{v100}"(v_vo[0]), "{v101}"(v_vo[1]), "{v102}"(v_vo[2]), "{v103}"(v_vo[3]), "{v104}"(v_vo[4]), "{v105}"(v_vo[5]), "{v106}"(v_vo[6]), "{v107}"(v_vo[7]), "{v108}"(v_vo[8]), "{v109}"(v_vp[0]), "{v110}"(v_vp[1]), "{v111}"(v_vp[2]), "{v112}"(v_vp[3]), "{v113}"(v_vp[4]), "{v114}"(v_vp[5]), "{v115}"(v_vp[6]), "{v116}"(v_vp[7]), "{v117}"(v_vp[8]), "{v118}"(v_t0), "{v119}"(v_t1), "{v120}"(v_ts), "{v121}"(v_pw), "{s36}"(s_sx), "{s37}"(s_ev), "{s38}"(s_sy), "{s39}"(s_m4), "{s40}"(s_m5), "{s41}"(s_m9), "{s42}"(s_so), "{s43}"(s_s8), "{s44}"(s_wg), "{s45}"(s_rb)
      : "v255", "a255", "s99", "vcc", "scc", "memory");

  } else if (role == 2) {
    const unsigned ks = part / 3, n3 = part % 3;
    const u32x4 rsA = srd(Wd, 4608 * 576 * 2), rsB = srd(out, 2048 * 576 * 2), rsC = srd(X, 2048 * 576 * 2);
    const unsigned v_l16b = v_l16 + 4096, v_l8 = 1024 + lane * 8, f0 = 24 * ks + 6 * w;
    const unsigned v_fa = 147456 - f0 * 1024 + (f0 + (l8 < 5 ? l8 : 5)) * 128 + l7 / 4, v_ga = (l8 < 4 ? l8 : 4) * 4 * PW + 1536 + l7 / 4;
    const unsigned v_ra = up * 6 * PREC + l5 + 1536, v_rf = up * 33 * REC + l5 + 20480 + (4 * n3 + w) * 128;
    const unsigned v_pw = l8 < 5 ? (l8 + 7) * PREC + l8 * 4 * PW + 1536 + l7 : OOB, v_pu = ks && l8 + ks < 5 ? OOB : v_pw - 6 * PREC;
    const unsigned pf = 3 * (w >> 1) + 2 * (w & 1), sf = 3 * (w >> 1) + 1;
    const unsigned v_at = (8 * g + q) * 384 + 64 * pf + 16 * p, v_as = (8 * g + q) * 384 + 64 * sf + 32 * (w & 1) + 8 * p;
    auto vo = [&](unsigned i) { const unsigned c = 64 * i + lane; return (8 * w + c / 24) * 1152 + 384 * n3 + c % 24 * 16 - 1024 * i; };
    const unsigned v_vo0 = vo(0), v_vo1 = vo(1), v_vo2 = vo(2);
    const unsigned v_xp = (6 * n3 + pf) * 1024 + v_l16, v_xs = (6 * n3 + sf) * 1024 + (w & 1) * 512 + half;
    const unsigned v_op = i16 * 1152 + 384 * n3 + 64 * pf + 16 * g, v_os = i16 * 1152 + 384 * n3 + 64 * sf + 32 * (w & 1) + 8 * g;
    const unsigned v_sw = 147456 + i16 * 136 + (4 * w + g) * 8, v_sr = 147456 + (4 * w + g) * 136 + i16 * 8;
    const unsigned v_ss = 18432 + n3 * 512 + w * 128 + g * 32 + p * 8;
    const unsigned s_ph = ks ? 6 - ks : 0, s_p0 = 0u - s_ph, r0 = gr * NREC + s_p0;
    const unsigned s_sh = WS_H + r0 * HREC + f0 * 1024, s_ps = WS_PS + r0 * PREC + (n3 * 20 + w) * PW;
    const unsigned s_x1 = WS_X1 + r0 * REC, s_xr = WS_XIN + (r0 + 33) * REC, s_or = (32 * gr + 63 - s_p0) * 18432;
    const unsigned s_k0 = ROT * 6 * gr;
    const unsigned s_m3 = w * 3072, s_m6 = w * 6144, s_so = ks * 768 * 1152;
    const unsigned s_i0 = (32 * gr + 31 - ks) * 18432, s_i5 = ks < 2 ? s_i0 - 30 * 18432 : OOB;
    const unsigned s_j0 = WS_XIN + (gr * NREC + ks) * REC, s_j5 = ks < 2 ? s_j0 + 30 * REC : OOB, s_he = s_sh + (63 + s_ph) * HREC;
    asm volatile(
      "s_buffer_load_dword s21, s[8:11], 0\n"
      "s_mov_b32 s23, 65536\n"
      "s_mov_b32 s31, 0x40000000\n"
      "buffer_load_dwordx4 v[56:59], v8, s[92:95], s46 offen\n"
      "buffer_load_dwordx2 v[60:61], v9, s[92:95], s46 offen\n"
      "s_waitcnt lgkmcnt(0)\n"
      "s_mul_i32 s21, s21, 1664525\n"
      "s_add_u32 s21, s21, 1013904223\n"
      "v_mov_b32 v18, s21\n"
      "v_mov_b32 v21, v18\n"
      "v_not_b32 v224, v18\n"
      "v_not_b32 v225, v18\n"
      "v_not_b32 v226, v18\n"
      "v_not_b32 v227, v18\n"
      "v_xor_b32 v18, v18, v19\n"
      "s_waitcnt vmcnt(0)\n"
      "buffer_store_dwordx4 v[56:59], v6, s[8:11], s48 offen sc1\n"
      "buffer_store_dwordx2 v[60:61], v7, s[8:11], s48 offen sc1\n"
      "v_lshlrev_b32 v164, 16, v56\n"
      "v_and_b32 v165, 0xffff0000, v56\n"
      "v_lshlrev_b32 v166, 16, v57\n"
      "v_and_b32 v167, 0xffff0000, v57\n"
      "v_lshlrev_b32 v168, 16, v58\n"
      "v_and_b32 v169, 0xffff0000, v58\n"
      "v_lshlrev_b32 v170, 16, v59\n"
      "v_and_b32 v171, 0xffff0000, v59\n"
      "v_lshlrev_b32 v172, 16, v60\n"
      "v_and_b32 v173, 0xffff0000, v60\n"
      "v_lshlrev_b32 v174, 16, v61\n"
      "v_and_b32 v175, 0xffff0000, v61\n"
      "v_add_f32 v62, v164, v165\n"
      "v_mul_f32 v63, v164, v164\n"
      "v_fmac_f32 v63, v165, v165\n"
      "v_add_f32 v62, v62, v166\n"
      "v_fmac_f32 v63, v166, v166\n"
      "v_add_f32 v62, v62, v167\n"
      "v_fmac_f32 v63, v167, v167\n"
      "v_add_f32 v62, v62, v168\n"
      "v_fmac_f32 v63, v168, v168\n"
      "v_add_f32 v62, v62, v169\n"
      "v_fmac_f32 v63, v169, v169\n"
      "v_add_f32 v62, v62, v170\n"
      "v_fmac_f32 v63, v170, v170\n"
      "v_add_f32 v62, v62, v171\n"
      "v_fmac_f32 v63, v171, v171\n"
      "v_add_f32 v62, v62, v172\n"
      "v_fmac_f32 v63, v172, v172\n"
      "v_add_f32 v62, v62, v173\n"
      "v_fmac_f32 v63, v173, v173\n"
      "v_add_f32 v62, v62, v174\n"
      "v_fmac_f32 v63, v174, v174\n"
      "v_add_f32 v62, v62, v175\n"
      "v_fmac_f32 v63, v175, v175\n"
      "ds_write_b64 v10, v[62:63] offset:0\n"
      "s_waitcnt lgkmcnt(0)\n"
      "s_barrier\n"
      "ds_read_b64 v[152:153], v11 offset:0\n"
      "s_waitcnt lgkmcnt(0)\n"
      "v_add_f32_dpp v152, v152, v152 row_ror:8 row_mask:0xf bank_mask:0xf\n"
      "v_add_f32_dpp v153, v153, v153 row_ror:8 row_mask:0xf bank_mask:0xf\n"
      "s_nop 0\n"
      "v_add_f32_dpp v152, v152, v152 row_ror:4 row_mask:0xf bank_mask:0xf\n"
      "v_add_f32_dpp v153, v153, v153 row_ror:4 row_mask:0xf bank_mask:0xf\n"
      "s_nop 0\n"
      "v_add_f32_dpp v152, v152, v152 row_ror:2 row_mask:0xf bank_mask:0xf\n"
      "v_add_f32_dpp v153, v153, v153 row_ror:2 row_mask:0xf bank_mask:0xf\n"
      "s_nop 0\n"
      "v_add_f32_dpp v152, v152, v152 row_ror:1 row_mask:0xf bank_mask:0xf\n"
      "v_add_f32_dpp v153, v153, v153 row_ror:1 row_mask:0xf bank_mask:0xf\n"
      "v_mul_f32 v152, 0x3ae38e39, v152\n"
      "v_add_f32 v153, 0x3afba882, v153\n"
      "v_mul_f32 v153, 0x3ae38e39, v153\n"
      "buffer_store_dwordx2 v[152:153], v12, s[8:11], s48 offen sc1\n"
      "s_add_u32 s30, s46, 4294856704\n"
      "buffer_load_dwordx4 v[64:67], v8, s[92:95], s30 offen\n"
      "buffer_load_dwordx2 v[68:69], v9, s[92:95], s30 offen\n"
      "s_add_u32 s30, s46, 4294746112\n"
      "buffer_load_dwordx4 v[72:75], v8, s[92:95], s30 offen\n"
      "buffer_load_dwordx2 v[76:77], v9, s[92:95], s30 offen\n"
      "s_waitcnt vmcnt(4)\n"
      "buffer_store_dword v18, v5, s[8:11], s48 offen sc1\n"
      "s_add_u32 s30, s46, 4294635520\n"
      "buffer_load_dwordx4 v[80:83], v8, s[92:95], s30 offen\n"
      "buffer_load_dwordx2 v[84:85], v9, s[92:95], s30 offen\n"
      "s_add_u32 s30, s46, 4294524928\n"
      "buffer_load_dwordx4 v[88:91], v8, s[92:95], s30 offen\n"
      "buffer_load_dwordx2 v[92:93], v9, s[92:95], s30 offen\n"
      "buffer_load_dwordx4 v[96:99], v8, s[92:95], s47 offen\n"
      "buffer_load_dwordx2 v[100:101], v9, s[92:95], s47 offen\n"
      "buffer_store_dword v18, v15, s[8:11], 0 offen sc1\n"
      "s_cmp_eq_u32 s45, 0\n"
      "s_cselect_b32 s30, 0x40000000, s37\n"
      "buffer_store_dword v21, v3, s[8:11], s30 offen sc1\n"
      "s_waitcnt vmcnt(11)\n"
      "s_add_u32 s30, s48, 147456\n"
      "buffer_store_dwordx4 v[64:67], v6, s[8:11], s30 offen sc1\n"
      "buffer_store_dwordx2 v[68:69], v7, s[8:11], s30 offen sc1\n"
      "v_lshlrev_b32 v164, 16, v64\n"
      "v_and_b32 v165, 0xffff0000, v64\n"
      "v_lshlrev_b32 v166, 16, v65\n"
      "v_and_b32 v167, 0xffff0000, v65\n"
      "v_lshlrev_b32 v168, 16, v66\n"
      "v_and_b32 v169, 0xffff0000, v66\n"
      "v_lshlrev_b32 v170, 16, v67\n"
      "v_and_b32 v171, 0xffff0000, v67\n"
      "v_lshlrev_b32 v172, 16, v68\n"
      "v_and_b32 v173, 0xffff0000, v68\n"
      "v_lshlrev_b32 v174, 16, v69\n"
      "v_and_b32 v175, 0xffff0000, v69\n"
      "v_add_f32 v70, v164, v165\n"
      "v_mul_f32 v71, v164, v164\n"
      "v_fmac_f32 v71, v165, v165\n"
      "v_add_f32 v70, v70, v166\n"
      "v_fmac_f32 v71, v166, v166\n"
      "v_add_f32 v70, v70, v167\n"
      "v_fmac_f32 v71, v167, v167\n"
      "v_add_f32 v70, v70, v168\n"
      "v_fmac_f32 v71, v168, v168\n"
      "v_add_f32 v70, v70, v169\n"
      "v_fmac_f32 v71, v169, v169\n"
      "v_add_f32 v70, v70, v170\n"
      "v_fmac_f32 v71, v170, v170\n"
      "v_add_f32 v70, v70, v171\n"
      "v_fmac_f32 v71, v171, v171\n"
      "v_add_f32 v70, v70, v172\n"
      "v_fmac_f32 v71, v172, v172\n"
      "v_add_f32 v70, v70, v173\n"
      "v_fmac_f32 v71, v173, v173\n"
      "v_add_f32 v70, v70, v174\n"
      "v_fmac_f32 v71, v174, v174\n"
      "v_add_f32 v70, v70, v175\n"
      "v_fmac_f32 v71, v175, v175\n"
      "ds_write_b64 v10, v[70:71] offset:2176\n"
      "s_waitcnt vmcnt(11)\n"
      "s_add_u32 s30, s48, 294912\n"
      "buffer_store_dwordx4 v[72:75], v6, s[8:11], s30 offen sc1\n"
      "buffer_store_dwordx2 v[76:77], v7, s[8:11], s30 offen sc1\n"
      "v_lshlrev_b32 v164, 16, v72\n"
      "v_and_b32 v165, 0xffff0000, v72\n"
      "v_lshlrev_b32 v166, 16, v73\n"
      "v_and_b32 v167, 0xffff0000, v73\n"
      "v_lshlrev_b32 v168, 16, v74\n"
      "v_and_b32 v169, 0xffff0000, v74\n"
      "v_lshlrev_b32 v170, 16, v75\n"
      "v_and_b32 v171, 0xffff0000, v75\n"
      "v_lshlrev_b32 v172, 16, v76\n"
      "v_and_b32 v173, 0xffff0000, v76\n"
      "v_lshlrev_b32 v174, 16, v77\n"
      "v_and_b32 v175, 0xffff0000, v77\n"
      "v_add_f32 v78, v164, v165\n"
      "v_mul_f32 v79, v164, v164\n"
      "v_fmac_f32 v79, v165, v165\n"
      "v_add_f32 v78, v78, v166\n"
      "v_fmac_f32 v79, v166, v166\n"
      "v_add_f32 v78, v78, v167\n"
      "v_fmac_f32 v79, v167, v167\n"
      "v_add_f32 v78, v78, v168\n"
      "v_fmac_f32 v79, v168, v168\n"
      "v_add_f32 v78, v78, v169\n"
      "v_fmac_f32 v79, v169, v169\n"
      "v_add_f32 v78, v78, v170\n"
      "v_fmac_f32 v79, v170, v170\n"
      "v_add_f32 v78, v78, v171\n"
      "v_fmac_f32 v79, v171, v171\n"
      "v_add_f32 v78, v78, v172\n"
      "v_fmac_f32 v79, v172, v172\n"
      "v_add_f32 v78, v78, v173\n"
      "v_fmac_f32 v79, v173, v173\n"
      "v_add_f32 v78, v78, v174\n"
      "v_fmac_f32 v79, v174, v174\n"
      "v_add_f32 v78, v78, v175\n"
      "v_fmac_f32 v79, v175, v175\n"
      "ds_write_b64 v10, v[78:79] offset:4352\n"
      "s_waitcnt vmcnt(10)\n"
      "s_add_u32 s30, s48, 442368\n"
      "buffer_store_dwordx4 v[80:83], v6, s[8:11], s30 offen sc1\n"
      "buffer_store_dwordx2 v[84:85], v7, s[8:11], s30 offen sc1\n"
      "v_lshlrev_b32 v164, 16, v80\n"
      "v_and_b32 v165, 0xffff0000, v80\n"
      "v_lshlrev_b32 v166, 16, v81\n"
      "v_and_b32 v167, 0xffff0000, v81\n"
      "v_lshlrev_b32 v168, 16, v82\n"
      "v_and_b32 v169, 0xffff0000, v82\n"
      "v_lshlrev_b32 v170, 16, v83\n"
      "v_and_b32 v171, 0xffff0000, v83\n"
      "v_lshlrev_b32 v172, 16, v84\n"
      "v_and_b32 v173, 0xffff0000, v84\n"
      "v_lshlrev_b32 v174, 16, v85\n"
      "v_and_b32 v175, 0xffff0000, v85\n"
      "v_add_f32 v86, v164, v165\n"
      "v_mul_f32 v87, v164, v164\n"
      "v_fmac_f32 v87, v165, v165\n"
      "v_add_f32 v86, v86, v166\n"
      "v_fmac_f32 v87, v166, v166\n"
      "v_add_f32 v86, v86, v167\n"
      "v_fmac_f32 v87, v167, v167\n"
      "v_add_f32 v86, v86, v168\n"
      "v_fmac_f32 v87, v168, v168\n"
      "v_add_f32 v86, v86, v169\n"
      "v_fmac_f32 v87, v169, v169\n"
      "v_add_f32 v86, v86, v170\n"
      "v_fmac_f32 v87, v170, v170\n"
      "v_add_f32 v86, v86, v171\n"
      "v_fmac_f32 v87, v171, v171\n"
      "v_add_f32 v86, v86, v172\n"
      "v_fmac_f32 v87, v172, v172\n"
      "v_add_f32 v86, v86, v173\n"
      "v_fmac_f32 v87, v173, v173\n"
      "v_add_f32 v86, v86, v174\n"
      "v_fmac_f32 v87, v174, v174\n"
      "v_add_f32 v86, v86, v175\n"
      "v_fmac_f32 v87, v175, v175\n"
      "ds_write_b64 v10, v[86:87] offset:6528\n"
      "s_waitcnt vmcnt(10)\n"
      "s_add_u32 s30, s48, 589824\n"
      "buffer_store_dwordx4 v[88:91], v6, s[8:11], s30 offen sc1\n"
      "buffer_store_dwordx2 v[92:93], v7, s[8:11], s30 offen sc1\n"
      "v_lshlrev_b32 v164, 16, v88\n"
      "v_and_b32 v165, 0xffff0000, v88\n"
      "v_lshlrev_b32 v166, 16, v89\n"
      "v_and_b32 v167, 0xffff0000, v89\n"
      "v_lshlrev_b32 v168, 16, v90\n"
      "v_and_b32 v169, 0xffff0000, v90\n"
      "v_lshlrev_b32 v170, 16, v91\n"
      "v_and_b32 v171, 0xffff0000, v91\n"
      "v_lshlrev_b32 v172, 16, v92\n"
      "v_and_b32 v173, 0xffff0000, v92\n"
      "v_lshlrev_b32 v174, 16, v93\n"
      "v_and_b32 v175, 0xffff0000, v93\n"
      "v_add_f32 v94, v164, v165\n"
      "v_mul_f32 v95, v164, v164\n"
      "v_fmac_f32 v95, v165, v165\n"
      "v_add_f32 v94, v94, v166\n"
      "v_fmac_f32 v95, v166, v166\n"
      "v_add_f32 v94, v94, v167\n"
      "v_fmac_f32 v95, v167, v167\n"
      "v_add_f32 v94, v94, v168\n"
      "v_fmac_f32 v95, v168, v168\n"
      "v_add_f32 v94, v94, v169\n"
      "v_fmac_f32 v95, v169, v169\n"
      "v_add_f32 v94, v94, v170\n"
      "v_fmac_f32 v95, v170, v170\n"
      "v_add_f32 v94, v94, v171\n"
      "v_fmac_f32 v95, v171, v171\n"
      "v_add_f32 v94, v94, v172\n"
      "v_fmac_f32 v95, v172, v172\n"
      "v_add_f32 v94, v94, v173\n"
      "v_fmac_f32 v95, v173, v173\n"
      "v_add_f32 v94, v94, v174\n"
      "v_fmac_f32 v95, v174, v174\n"
      "v_add_f32 v94, v94, v175\n"
      "v_fmac_f32 v95, v175, v175\n"
      "ds_write_b64 v10, v[94:95] offset:8704\n"
      "s_waitcnt vmcnt(10)\n"
      "buffer_store_dwordx4 v[96:99], v6, s[8:11], s49 offen sc1\n"
      "buffer_store_dwordx2 v[100:101], v7, s[8:11], s49 offen sc1\n"
      "v_lshlrev_b32 v164, 16, v96\n"
      "v_and_b32 v165, 0xffff0000, v96\n"
      "v_lshlrev_b32 v166, 16, v97\n"
      "v_and_b32 v167, 0xffff0000, v97\n"
      "v_lshlrev_b32 v168, 16, v98\n"
      "v_and_b32 v169, 0xffff0000, v98\n"
      "v_lshlrev_b32 v170, 16, v99\n"
      "v_and_b32 v171, 0xffff0000, v99\n"
      "v_lshlrev_b32 v172, 16, v100\n"
      "v_and_b32 v173, 0xffff0000, v100\n"
      "v_lshlrev_b32 v174, 16, v101\n"
      "v_and_b32 v175, 0xffff0000, v101\n"
      "v_add_f32 v102, v164, v165\n"
      "v_mul_f32 v103, v164, v164\n"
      "v_fmac_f32 v103, v165, v165\n"
      "v_add_f32 v102, v102, v166\n"
      "v_fmac_f32 v103, v166, v166\n"
      "v_add_f32 v102, v102, v167\n"
      "v_fmac_f32 v103, v167, v167\n"
      "v_add_f32 v102, v102, v168\n"
      "v_fmac_f32 v103, v168, v168\n"
      "v_add_f32 v102, v102, v169\n"
      "v_fmac_f32 v103, v169, v169\n"
      "v_add_f32 v102, v102, v170\n"
      "v_fmac_f32 v103, v170, v170\n"
      "v_add_f32 v102, v102, v171\n"
      "v_fmac_f32 v103, v171, v171\n"
      "v_add_f32 v102, v102, v172\n"
      "v_fmac_f32 v103, v172, v172\n"
      "v_add_f32 v102, v102, v173\n"
      "v_fmac_f32 v103, v173, v173\n"
      "v_add_f32 v102, v102, v174\n"
      "v_fmac_f32 v103, v174, v174\n"
      "v_add_f32 v102, v102, v175\n"
      "v_fmac_f32 v103, v175, v175\n"
      "ds_write_b64 v10, v[102:103] offset:10880\n"
      "s_waitcnt lgkmcnt(0)\n"
      "s_barrier\n"
      "ds_read_b64 v[154:155], v11 offset:2176\n"
      "ds_read_b64 v[156:157], v11 offset:4352\n"
      "ds_read_b64 v[158:159], v11 offset:6528\n"
      "ds_read_b64 v[160:161], v11 offset:8704\n"
      "ds_read_b64 v[162:163], v11 offset:10880\n"
      "s_waitcnt lgkmcnt(0)\n"
      "v_add_f32_dpp v154, v154, v154 row_ror:8 row_mask:0xf bank_mask:0xf\n"
      "v_add_f32_dpp v155, v155, v155 row_ror:8 row_mask:0xf bank_mask:0xf\n"
      "v_add_f32_dpp v156, v156, v156 row_ror:8 row_mask:0xf bank_mask:0xf\n"
      "v_add_f32_dpp v157, v157, v157 row_ror:8 row_mask:0xf bank_mask:0xf\n"
      "v_add_f32_dpp v158, v158, v158 row_ror:8 row_mask:0xf bank_mask:0xf\n"
      "v_add_f32_dpp v159, v159, v159 row_ror:8 row_mask:0xf bank_mask:0xf\n"
      "v_add_f32_dpp v160, v160, v160 row_ror:8 row_mask:0xf bank_mask:0xf\n"
      "v_add_f32_dpp v161, v161, v161 row_ror:8 row_mask:0xf bank_mask:0xf\n"
      "v_add_f32_dpp v162, v162, v162 row_ror:8 row_mask:0xf bank_mask:0xf\n"
      "v_add_f32_dpp v163, v163, v163 row_ror:8 row_mask:0xf bank_mask:0xf\n"
      "v_add_f32_dpp v154, v154, v154 row_ror:4 row_mask:0xf bank_mask:0xf\n"
      "v_add_f32_dpp v155, v155, v155 row_ror:4 row_mask:0xf bank_mask:0xf\n"
      "v_add_f32_dpp v156, v156, v156 row_ror:4 row_mask:0xf bank_mask:0xf\n"
      "v_add_f32_dpp v157, v157, v157 row_ror:4 row_mask:0xf bank_mask:0xf\n"
      "v_add_f32_dpp v158, v158, v158 row_ror:4 row_mask:0xf bank_mask:0xf\n"
      "v_add_f32_dpp v159, v159, v159 row_ror:4 row_mask:0xf bank_mask:0xf\n"
      "v_add_f32_dpp v160, v160, v160 row_ror:4 row_mask:0xf bank_mask:0xf\n"
      "v_add_f32_dpp v161, v161, v161 row_ror:4 row_mask:0xf bank_mask:0xf\n"
      "v_add_f32_dpp v162, v162, v162 row_ror:4 row_mask:0xf bank_mask:0xf\n"
      "v_add_f32_dpp v163, v163, v163 row_ror:4 row_mask:0xf bank_mask:0xf\n"
      "v_add_f32_dpp v154, v154, v154 row_ror:2 row_mask:0xf bank_mask:0xf\n"
      "v_add_f32_dpp v155, v155, v155 row_ror:2 row_mask:0xf bank_mask:0xf\n"
      "v_add_f32_dpp v156, v156, v156 row_ror:2 row_mask:0xf bank_mask:0xf\n"
      "v_add_f32_dpp v157, v157, v157 row_ror:2 row_mask:0xf bank_mask:0xf\n"
      "v_add_f32_dpp v158, v158, v158 row_ror:2 row_mask:0xf bank_mask:0xf\n"
      "v_add_f32_dpp v159, v159, v159 row_ror:2 row_mask:0xf bank_mask:0xf\n"
      "v_add_f32_dpp v160, v160, v160 row_ror:2 row_mask:0xf bank_mask:0xf\n"
      "v_add_f32_dpp v161, v161, v161 row_ror:2 row_mask:0xf bank_mask:0xf\n"
      "v_add_f32_dpp v162, v162, v162 row_ror:2 row_mask:0xf bank_mask:0xf\n"
      "v_add_f32_dpp v163, v163, v163 row_ror:2 row_mask:0xf bank_mask:0xf\n"
      "v_add_f32_dpp v154, v154, v154 row_ror:1 row_mask:0xf bank_mask:0xf\n"
      "v_add_f32_dpp v155, v155, v155 row_ror:1 row_mask:0xf bank_mask:0xf\n"
      "v_add_f32_dpp v156, v156, v156 row_ror:1 row_mask:0xf bank_mask:0xf\n"
      "v_add_f32_dpp v157, v157, v157 row_ror:1 row_mask:0xf bank_mask:0xf\n"
      "v_add_f32_dpp v158, v158, v158 row_ror:1 row_mask:0xf bank_mask:0xf\n"
      "v_add_f32_dpp v159, v159, v159 row_ror:1 row_mask:0xf bank_mask:0xf\n"
      "v_add_f32_dpp v160, v160, v160 row_ror:1 row_mask:0xf bank_mask:0xf\n"
      "v_add_f32_dpp v161, v161, v161 row_ror:1 row_mask:0xf bank_mask:0xf\n"
      "v_add_f32_dpp v162, v162, v162 row_ror:1 row_mask:0xf bank_mask:0xf\n"
      "v_add_f32_dpp v163, v163, v163 row_ror:1 row_mask:0xf bank_mask:0xf\n"
      "v_mul_f32 v154, 0x3ae38e39, v154\n"
      "v_add_f32 v155, 0x3afba882, v155\n"
      "v_mul_f32 v155, 0x3ae38e39, v155\n"
      "v_mul_f32 v156, 0x3ae38e39, v156\n"
      "v_add_f32 v157, 0x3afba882, v157\n"
      "v_mul_f32 v157, 0x3ae38e39, v157\n"
      "v_mul_f32 v158, 0x3ae38e39, v158\n"
      "v_add_f32 v159, 0x3afba882, v159\n"
      "v_mul_f32 v159, 0x3ae38e39, v159\n"
      "v_mul_f32 v160, 0x3ae38e39, v160\n"
      "v_add_f32 v161, 0x3afba882, v161\n"
      "v_mul_f32 v161, 0x3ae38e39, v161\n"
      "v_mul_f32 v162, 0x3ae38e39, v162\n"
      "v_add_f32 v163, 0x3afba882, v163\n"
      "v_mul_f32 v163, 0x3ae38e39, v163\n"
      "s_add_u32 s30, s48, 147456\n"
      "buffer_store_dwordx2 v[154:155], v12, s[8:11], s30 offen sc1\n"
      "s_add_u32 s30, s48, 294912\n"
      "buffer_store_dwordx2 v[156:157], v12, s[8:11], s30 offen sc1\n"
      "s_add_u32 s30, s48, 442368\n"
      "buffer_store_dwordx2 v[158:159], v12, s[8:11], s30 offen sc1\n"
      "s_add_u32 s30, s48, 589824\n"
      "buffer_store_dwordx2 v[160:161], v12, s[8:11], s30 offen sc1\n"
      "buffer_store_dwordx2 v[162:163], v12, s[8:11], s49 offen sc1\n"
      "s_waitcnt vmcnt(0)\n"
      "s_add_u32 s30, s48, 147456\n"
      "buffer_store_dword v18, v5, s[8:11], s30 offen sc1\n"
      "s_add_u32 s30, s48, 294912\n"
      "buffer_store_dword v18, v5, s[8:11], s30 offen sc1\n"
      "s_add_u32 s30, s48, 442368\n"
      "buffer_store_dword v18, v5, s[8:11], s30 offen sc1\n"
      "s_add_u32 s30, s48, 589824\n"
      "buffer_store_dword v18, v5, s[8:11], s30 offen sc1\n"
      "buffer_store_dword v18, v5, s[8:11], s49 offen sc1\n"
      "v_and_b32 v20, 0x40000000, v19\n"
      "v_or_b32 v5, v20, v5\n"
      "s_add_u32 s30, s37, 0\n"
      "buffer_store_dwordx4 v[224:227], v183, s[8:11], s30 offen sc1\n"
      "s_add_u32 s30, s37, 0\n"
      "buffer_store_dwordx4 v[224:227], v181, s[8:11], s30 offen sc1\n"
      "s_sleep 32\n"
      "s_waitcnt vmcnt(0) lgkmcnt(0)\n"
      "s_mov_b32 s61, s60\n"
      "s_mov_b32 s62, 6\n"
      ".Ld_r0:\n"
      "s_mul_i32 s63, s61, 36864\n"
      "s_add_u32 s63, s63, s44\n"
      "s_mul_i32 s30, s61, 8192\n"
      "s_lshr_b32 s30, s30, 16\n"
      "s_mul_i32 s30, s30, 8\n"
      "s_sub_u32 s30, s61, s30\n"
      "s_mul_i32 s30, s30, 12288\n"
      "s_add_u32 s30, s30, 0\n"
      "s_add_u32 m0, s42, s30\n"
      "s_add_u32 s61, s61, 1\n"
      "buffer_load_dwordx4 v178, s[12:15], s63 offen offset:0 lds\n"
      "buffer_load_dwordx4 v179, s[12:15], s63 offen offset:1024 lds\n"
      "buffer_load_dwordx4 v180, s[12:15], s63 offen offset:2048 lds\n"
      "s_cmp_eq_u32 s61, 24\n"
      "s_cselect_b32 s61, 0, s61\n"
      "s_sub_u32 s62, s62, 1\n"
      "s_cbranch_scc0 .Ld_r0\n"
      "v_add_u32 v20, 49152, v176\n"
      "v_add_u32 v21, 49152, v177\n"
      "v_add_u32 v22, 98304, v176\n"
      "v_add_u32 v23, 98304, v177\n"
      "s_mov_b32 s62, 23\n"
      "s_cmp_eq_u32 s60, 6\n"
      "s_cbranch_scc1 .Ld_rs6\n"
      "s_cmp_eq_u32 s60, 12\n"
      "s_cbranch_scc1 .Ld_rs12\n"
      "s_cmp_eq_u32 s60, 18\n"
      "s_cbranch_scc1 .Ld_rs18\n"
      ".Ld_rs0:\n"
      "s_add_u32 s63, s44, 258048\n"
      "s_cmp_ge_u32 s62, 7\n"
      "s_cselect_b32 s63, s63, s31\n"
      "s_add_u32 m0, s42, 86016\n"
      "s_waitcnt vmcnt(18) lgkmcnt(0)\n"
      "s_barrier\n"
      "ds_read_b64_tr_b16 a[0:1], v176 offset:0\n"
      "ds_read_b64_tr_b16 a[2:3], v176 offset:1536\n"
      "ds_read_b64_tr_b16 a[96:97], v176 offset:8\n"
      "ds_read_b64_tr_b16 a[98:99], v176 offset:1544\n"
      "ds_read_b64_tr_b16 a[192:193], v177 offset:0\n"
      "ds_read_b64_tr_b16 a[194:195], v177 offset:1536\n"
      "buffer_load_dwordx4 v178, s[12:15], s63 offen offset:0 lds\n"
      "buffer_load_dwordx4 v179, s[12:15], s63 offen offset:1024 lds\n"
      "buffer_load_dwordx4 v180, s[12:15], s63 offen offset:2048 lds\n"
      "s_sub_u32 s62, s62, 1\n"
      ".Ld_rs1:\n"
      "s_add_u32 s63, s44, 294912\n"
      "s_cmp_ge_u32 s62, 7\n"
      "s_cselect_b32 s63, s63, s31\n"
      "s_add_u32 m0, s42, 0\n"
      "s_waitcnt vmcnt(18) lgkmcnt(0)\n"
      "s_barrier\n"
      "ds_read_b64_tr_b16 a[4:5], v176 offset:12288\n"
      "ds_read_b64_tr_b16 a[6:7], v176 offset:13824\n"
      "ds_read_b64_tr_b16 a[100:101], v176 offset:12296\n"
      "ds_read_b64_tr_b16 a[102:103], v176 offset:13832\n"
      "ds_read_b64_tr_b16 a[196:197], v177 offset:12288\n"
      "ds_read_b64_tr_b16 a[198:199], v177 offset:13824\n"
      "buffer_load_dwordx4 v178, s[12:15], s63 offen offset:0 lds\n"
      "buffer_load_dwordx4 v179, s[12:15], s63 offen offset:1024 lds\n"
      "buffer_load_dwordx4 v180, s[12:15], s63 offen offset:2048 lds\n"
      "s_sub_u32 s62, s62, 1\n"
      ".Ld_rs2:\n"
      "s_add_u32 s63, s44, 331776\n"
      "s_cmp_ge_u32 s62, 7\n"
      "s_cselect_b32 s63, s63, s31\n"
      "s_add_u32 m0, s42, 12288\n"
      "s_waitcnt vmcnt(18) lgkmcnt(0)\n"
      "s_barrier\n"
      "ds_read_b64_tr_b16 a[8:9], v176 offset:24576\n"
      "ds_read_b64_tr_b16 a[10:11], v176 offset:26112\n"
      "ds_read_b64_tr_b16 a[104:105], v176 offset:24584\n"
      "ds_read_b64_tr_b16 a[106:107], v176 offset:26120\n"
      "ds_read_b64_tr_b16 a[200:201], v177 offset:24576\n"
      "ds_read_b64_tr_b16 a[202:203], v177 offset:26112\n"
      "buffer_load_dwordx4 v178, s[12:15], s63 offen offset:0 lds\n"
      "buffer_load_dwordx4 v179, s[12:15], s63 offen offset:1024 lds\n"
      "buffer_load_dwordx4 v180, s[12:15], s63 offen offset:2048 lds\n"
      "s_sub_u32 s62, s62, 1\n"
      ".Ld_rs3:\n"
      "s_add_u32 s63, s44, 368640\n"
      "s_cmp_ge_u32 s62, 7\n"
      "s_cselect_b32 s63, s63, s31\n"
      "s_add_u32 m0, s42, 24576\n"
      "s_waitcnt vmcnt(18) lgkmcnt(0)\n"
      "s_barrier\n"
      "ds_read_b64_tr_b16 a[12:13], v176 offset:36864\n"
      "ds_read_b64_tr_b16 a[14:15], v176 offset:38400\n"
      "ds_read_b64_tr_b16 a[108:109], v176 offset:36872\n"
      "ds_read_b64_tr_b16 a[110:111], v176 offset:38408\n"
      "ds_read_b64_tr_b16 a[204:205], v177 offset:36864\n"
      "ds_read_b64_tr_b16 a[206:207], v177 offset:38400\n"
      "buffer_load_dwordx4 v178, s[12:15], s63 offen offset:0 lds\n"
      "buffer_load_dwordx4 v179, s[12:15], s63 offen offset:1024 lds\n"
      "buffer_load_dwordx4 v180, s[12:15], s63 offen offset:2048 lds\n"
      "s_sub_u32 s62, s62, 1\n"
      ".Ld_rs4:\n"
      "s_add_u32 s63, s44, 405504\n"
      "s_cmp_ge_u32 s62, 7\n"
      "s_cselect_b32 s63, s63, s31\n"
      "s_add_u32 m0, s42, 36864\n"
      "s_waitcnt vmcnt(18) lgkmcnt(0)\n"
      "s_barrier\n"
      "ds_read_b64_tr_b16 a[16:17], v20 offset:0\n"
      "ds_read_b64_tr_b16 a[18:19], v20 offset:1536\n"
      "ds_read_b64_tr_b16 a[112:113], v20 offset:8\n"
      "ds_read_b64_tr_b16 a[114:115], v20 offset:1544\n"
      "ds_read_b64_tr_b16 a[208:209], v21 offset:0\n"
      "ds_read_b64_tr_b16 a[210:211], v21 offset:1536\n"
      "buffer_load_dwordx4 v178, s[12:15], s63 offen offset:0 lds\n"
      "buffer_load_dwordx4 v179, s[12:15], s63 offen offset:1024 lds\n"
      "buffer_load_dwordx4 v180, s[12:15], s63 offen offset:2048 lds\n"
      "s_sub_u32 s62, s62, 1\n"
      ".Ld_rs5:\n"
      "s_add_u32 s63, s44, 442368\n"
      "s_cmp_ge_u32 s62, 7\n"
      "s_cselect_b32 s63, s63, s31\n"
      "s_add_u32 m0, s42, 49152\n"
      "s_waitcnt vmcnt(18) lgkmcnt(0)\n"
      "s_barrier\n"
      "ds_read_b64_tr_b16 a[20:21], v20 offset:12288\n"
      "ds_read_b64_tr_b16 a[22:23], v20 offset:13824\n"
      "ds_read_b64_tr_b16 a[116:117], v20 offset:12296\n"
      "ds_read_b64_tr_b16 a[118:119], v20 offset:13832\n"
      "ds_read_b64_tr_b16 a[212:213], v21 offset:12288\n"
      "ds_read_b64_tr_b16 a[214:215], v21 offset:13824\n"
      "buffer_load_dwordx4 v178, s[12:15], s63 offen offset:0 lds\n"
      "buffer_load_dwordx4 v179, s[12:15], s63 offen offset:1024 lds\n"
      "buffer_load_dwordx4 v180, s[12:15], s63 offen offset:2048 lds\n"
      "s_sub_u32 s62, s62, 1\n"
      "s_cbranch_scc1 .Ld_re\n"
      ".Ld_rs6:\n"
      "s_add_u32 s63, s44, 479232\n"
      "s_cmp_ge_u32 s62, 7\n"
      "s_cselect_b32 s63, s63, s31\n"
      "s_add_u32 m0, s42, 61440\n"
      "s_waitcnt vmcnt(18) lgkmcnt(0)\n"
      "s_barrier\n"
      "ds_read_b64_tr_b16 a[24:25], v20 offset:24576\n"
      "ds_read_b64_tr_b16 a[26:27], v20 offset:26112\n"
      "ds_read_b64_tr_b16 a[120:121], v20 offset:24584\n"
      "ds_read_b64_tr_b16 a[122:123], v20 offset:26120\n"
      "ds_read_b64_tr_b16 a[216:217], v21 offset:24576\n"
      "ds_read_b64_tr_b16 a[218:219], v21 offset:26112\n"
      "buffer_load_dwordx4 v178, s[12:15], s63 offen offset:0 lds\n"
      "buffer_load_dwordx4 v179, s[12:15], s63 offen offset:1024 lds\n"
      "buffer_load_dwordx4 v180, s[12:15], s63 offen offset:2048 lds\n"
      "s_sub_u32 s62, s62, 1\n"
      ".Ld_rs7:\n"
      "s_add_u32 s63, s44, 516096\n"
      "s_cmp_ge_u32 s62, 7\n"
      "s_cselect_b32 s63, s63, s31\n"
      "s_add_u32 m0, s42, 73728\n"
      "s_waitcnt vmcnt(18) lgkmcnt(0)\n"
      "s_barrier\n"
      "ds_read_b64_tr_b16 a[28:29], v20 offset:36864\n"
      "ds_read_b64_tr_b16 a[30:31], v20 offset:38400\n"
      "ds_read_b64_tr_b16 a[124:125], v20 offset:36872\n"
      "ds_read_b64_tr_b16 a[126:127], v20 offset:38408\n"
      "ds_read_b64_tr_b16 a[220:221], v21 offset:36864\n"
      "ds_read_b64_tr_b16 a[222:223], v21 offset:38400\n"
      "buffer_load_dwordx4 v178, s[12:15], s63 offen offset:0 lds\n"
      "buffer_load_dwordx4 v179, s[12:15], s63 offen offset:1024 lds\n"
      "buffer_load_dwordx4 v180, s[12:15], s63 offen offset:2048 lds\n"
      "s_sub_u32 s62, s62, 1\n"
      ".Ld_rs8:\n"
      "s_add_u32 s63, s44, 552960\n"
      "s_cmp_ge_u32 s62, 7\n"
      "s_cselect_b32 s63, s63, s31\n"
      "s_add_u32 m0, s42, 86016\n"
      "s_waitcnt vmcnt(18) lgkmcnt(0)\n"
      "s_barrier\n"
      "ds_read_b64_tr_b16 a[32:33], v176 offset:0\n"
      "ds_read_b64_tr_b16 a[34:35], v176 offset:1536\n"
      "ds_read_b64_tr_b16 a[128:129], v176 offset:8\n"
      "ds_read_b64_tr_b16 a[130:131], v176 offset:1544\n"
      "ds_read_b64_tr_b16 a[224:225], v177 offset:0\n"
      "ds_read_b64_tr_b16 a[226:227], v177 offset:1536\n"
      "buffer_load_dwordx4 v178, s[12:15], s63 offen offset:0 lds\n"
      "buffer_load_dwordx4 v179, s[12:15], s63 offen offset:1024 lds\n"
      "buffer_load_dwordx4 v180, s[12:15], s63 offen offset:2048 lds\n"
      "s_sub_u32 s62, s62, 1\n"
      ".Ld_rs9:\n"
      "s_add_u32 s63, s44, 589824\n"
      "s_cmp_ge_u32 s62, 7\n"
      "s_cselect_b32 s63, s63, s31\n"
      "s_add_u32 m0, s42, 0\n"
      "s_waitcnt vmcnt(18) lgkmcnt(0)\n"
      "s_barrier\n"
      "ds_read_b64_tr_b16 a[36:37], v176 offset:12288\n"
      "ds_read_b64_tr_b16 a[38:39], v176 offset:13824\n"
      "ds_read_b64_tr_b16 a[132:133], v176 offset:12296\n"
      "ds_read_b64_tr_b16 a[134:135], v176 offset:13832\n"
      "ds_read_b64_tr_b16 a[228:229], v177 offset:12288\n"
      "ds_read_b64_tr_b16 a[230:231], v177 offset:13824\n"
      "buffer_load_dwordx4 v178, s[12:15], s63 offen offset:0 lds\n"
      "buffer_load_dwordx4 v179, s[12:15], s63 offen offset:1024 lds\n"
      "buffer_load_dwordx4 v180, s[12:15], s63 offen offset:2048 lds\n"
      "s_sub_u32 s62, s62, 1\n"
      ".Ld_rs10:\n"
      "s_add_u32 s63, s44, 626688\n"
      "s_cmp_ge_u32 s62, 7\n"
      "s_cselect_b32 s63, s63, s31\n"
      "s_add_u32 m0, s42, 12288\n"
      "s_waitcnt vmcnt(18) lgkmcnt(0)\n"
      "s_barrier\n"
      "ds_read_b64_tr_b16 a[40:41], v176 offset:24576\n"
      "ds_read_b64_tr_b16 a[42:43], v176 offset:26112\n"
      "ds_read_b64_tr_b16 a[136:137], v176 offset:24584\n"
      "ds_read_b64_tr_b16 a[138:139], v176 offset:26120\n"
      "ds_read_b64_tr_b16 a[232:233], v177 offset:24576\n"
      "ds_read_b64_tr_b16 a[234:235], v177 offset:26112\n"
      "buffer_load_dwordx4 v178, s[12:15], s63 offen offset:0 lds\n"
      "buffer_load_dwordx4 v179, s[12:15], s63 offen offset:1024 lds\n"
      "buffer_load_dwordx4 v180, s[12:15], s63 offen offset:2048 lds\n"
      "s_sub_u32 s62, s62, 1\n"
      ".Ld_rs11:\n"
      "s_add_u32 s63, s44, 663552\n"
      "s_cmp_ge_u32 s62, 7\n"
      "s_cselect_b32 s63, s63, s31\n"
      "s_add_u32 m0, s42, 24576\n"
      "s_waitcnt vmcnt(18) lgkmcnt(0)\n"
      "s_barrier\n"
      "ds_read_b64_tr_b16 a[44:45], v176 offset:36864\n"
      "ds_read_b64_tr_b16 a[46:47], v176 offset:38400\n"
      "ds_read_b64_tr_b16 a[140:141], v176 offset:36872\n"
      "ds_read_b64_tr_b16 a[142:143], v176 offset:38408\n"
      "ds_read_b64_tr_b16 a[236:237], v177 offset:36864\n"
      "ds_read_b64_tr_b16 a[238:239], v177 offset:38400\n"
      "buffer_load_dwordx4 v178, s[12:15], s63 offen offset:0 lds\n"
      "buffer_load_dwordx4 v179, s[12:15], s63 offen offset:1024 lds\n"
      "buffer_load_dwordx4 v180, s[12:15], s63 offen offset:2048 lds\n"
      "s_sub_u32 s62, s62, 1\n"
      "s_cbranch_scc1 .Ld_re\n"
      ".Ld_rs12:\n"
      "s_add_u32 s63, s44, 700416\n"
      "s_cmp_ge_u32 s62, 7\n"
      "s_cselect_b32 s63, s63, s31\n"
      "s_add_u32 m0, s42, 36864\n"
      "s_waitcnt vmcnt(18) lgkmcnt(0)\n"
      "s_barrier\n"
      "ds_read_b64_tr_b16 a[48:49], v20 offset:0\n"
      "ds_read_b64_tr_b16 a[50:51], v20 offset:1536\n"
      "ds_read_b64_tr_b16 a[144:145], v20 offset:8\n"
      "ds_read_b64_tr_b16 a[146:147], v20 offset:1544\n"
      "ds_read_b64_tr_b16 a[240:241], v21 offset:0\n"
      "ds_read_b64_tr_b16 a[242:243], v21 offset:1536\n"
      "buffer_load_dwordx4 v178, s[12:15], s63 offen offset:0 lds\n"
      "buffer_load_dwordx4 v179, s[12:15], s63 offen offset:1024 lds\n"
      "buffer_load_dwordx4 v180, s[12:15], s63 offen offset:2048 lds\n"
      "s_sub_u32 s62, s62, 1\n"
      ".Ld_rs13:\n"
      "s_add_u32 s63, s44, 737280\n"
      "s_cmp_ge_u32 s62, 7\n"
      "s_cselect_b32 s63, s63, s31\n"
      "s_add_u32 m0, s42, 49152\n"
      "s_waitcnt vmcnt(18) lgkmcnt(0)\n"
      "s_barrier\n"
      "ds_read_b64_tr_b16 a[52:53], v20 offset:12288\n"
      "ds_read_b64_tr_b16 a[54:55], v20 offset:13824\n"
      "ds_read_b64_tr_b16 a[148:149], v20 offset:12296\n"
      "ds_read_b64_tr_b16 a[150:151], v20 offset:13832\n"
      "ds_read_b64_tr_b16 a[244:245], v21 offset:12288\n"
      "ds_read_b64_tr_b16 a[246:247], v21 offset:13824\n"
      "buffer_load_dwordx4 v178, s[12:15], s63 offen offset:0 lds\n"
      "buffer_load_dwordx4 v179, s[12:15], s63 offen offset:1024 lds\n"
      "buffer_load_dwordx4 v180, s[12:15], s63 offen offset:2048 lds\n"
      "s_sub_u32 s62, s62, 1\n"
      ".Ld_rs14:\n"
      "s_add_u32 s63, s44, 774144\n"
      "s_cmp_ge_u32 s62, 7\n"
      "s_cselect_b32 s63, s63, s31\n"
      "s_add_u32 m0, s42, 61440\n"
      "s_waitcnt vmcnt(18) lgkmcnt(0)\n"
      "s_barrier\n"
      "ds_read_b64_tr_b16 a[56:57], v20 offset:24576\n"
      "ds_read_b64_tr_b16 a[58:59], v20 offset:26112\n"
      "ds_read_b64_tr_b16 a[152:153], v20 offset:24584\n"
      "ds_read_b64_tr_b16 a[154:155], v20 offset:26120\n"
      "ds_read_b64_tr_b16 a[248:249], v21 offset:24576\n"
      "ds_read_b64_tr_b16 a[250:251], v21 offset:26112\n"
      "buffer_load_dwordx4 v178, s[12:15], s63 offen offset:0 lds\n"
      "buffer_load_dwordx4 v179, s[12:15], s63 offen offset:1024 lds\n"
      "buffer_load_dwordx4 v180, s[12:15], s63 offen offset:2048 lds\n"
      "s_sub_u32 s62, s62, 1\n"
      ".Ld_rs15:\n"
      "s_add_u32 s63, s44, 811008\n"
      "s_cmp_ge_u32 s62, 7\n"
      "s_cselect_b32 s63, s63, s31\n"
      "s_add_u32 m0, s42, 73728\n"
      "s_waitcnt vmcnt(18) lgkmcnt(0)\n"
      "s_barrier\n"
      "ds_read_b64_tr_b16 a[60:61], v20 offset:36864\n"
      "ds_read_b64_tr_b16 a[62:63], v20 offset:38400\n"
      "ds_read_b64_tr_b16 a[156:157], v20 offset:36872\n"
      "ds_read_b64_tr_b16 a[158:159], v20 offset:38408\n"
      "ds_read_b64_tr_b16 a[252:253], v21 offset:36864\n"
      "ds_read_b64_tr_b16 a[254:255], v21 offset:38400\n"
      "buffer_load_dwordx4 v178, s[12:15], s63 offen offset:0 lds\n"
      "buffer_load_dwordx4 v179, s[12:15], s63 offen offset:1024 lds\n"
      "buffer_load_dwordx4 v180, s[12:15], s63 offen offset:2048 lds\n"
      "s_sub_u32 s62, s62, 1\n"
      ".Ld_rs16:\n"
      "s_add_u32 s63, s44, 847872\n"
      "s_cmp_ge_u32 s62, 7\n"
      "s_cselect_b32 s63, s63, s31\n"
      "s_add_u32 m0, s42, 86016\n"
      "s_waitcnt vmcnt(18) lgkmcnt(0)\n"
      "s_barrier\n"
      "ds_read_b64_tr_b16 a[64:65], v176 offset:0\n"
      "ds_read_b64_tr_b16 a[66:67], v176 offset:1536\n"
      "ds_read_b64_tr_b16 a[160:161], v176 offset:8\n"
      "ds_read_b64_tr_b16 a[162:163], v176 offset:1544\n"
      "ds_read_b64_tr_b16 v[24:25], v177 offset:0\n"
      "ds_read_b64_tr_b16 v[26:27], v177 offset:1536\n"
      "buffer_load_dwordx4 v178, s[12:15], s63 offen offset:0 lds\n"
      "buffer_load_dwordx4 v179, s[12:15], s63 offen offset:1024 lds\n"
      "buffer_load_dwordx4 v180, s[12:15], s63 offen offset:2048 lds\n"
      "s_sub_u32 s62, s62, 1\n"
      ".Ld_rs17:\n"
      "s_add_u32 s63, s44, 0\n"
      "s_cmp_ge_u32 s62, 7\n"
      "s_cselect_b32 s63, s63, s31\n"
      "s_add_u32 m0, s42, 0\n"
      "s_waitcnt vmcnt(18) lgkmcnt(0)\n"
      "s_barrier\n"
      "ds_read_b64_tr_b16 a[68:69], v176 offset:12288\n"
      "ds_read_b64_tr_b16 a[70:71], v176 offset:13824\n"
      "ds_read_b64_tr_b16 a[164:165], v176 offset:12296\n"
      "ds_read_b64_tr_b16 a[166:167], v176 offset:13832\n"
      "ds_read_b64_tr_b16 v[28:29], v177 offset:12288\n"
      "ds_read_b64_tr_b16 v[30:31], v177 offset:13824\n"
      "buffer_load_dwordx4 v178, s[12:15], s63 offen offset:0 lds\n"
      "buffer_load_dwordx4 v179, s[12:15], s63 offen offset:1024 lds\n"
      "buffer_load_dwordx4 v180, s[12:15], s63 offen offset:2048 lds\n"
      "s_sub_u32 s62, s62, 1\n"
      "s_cbranch_scc1 .Ld_re\n"
      ".Ld_rs18:\n"
      "s_add_u32 s63, s44, 36864\n"
      "s_cmp_ge_u32 s62, 7\n"
      "s_cselect_b32 s63, s63, s31\n"
      "s_add_u32 m0, s42, 12288\n"
      "s_waitcnt vmcnt(18) lgkmcnt(0)\n"
      "s_barrier\n"
      "ds_read_b64_tr_b16 a[72:73], v176 offset:24576\n"
      "ds_read_b64_tr_b16 a[74:75], v176 offset:26112\n"
      "ds_read_b64_tr_b16 a[168:169], v176 offset:24584\n"
      "ds_read_b64_tr_b16 a[170:171], v176 offset:26120\n"
      "ds_read_b64_tr_b16 v[32:33], v177 offset:24576\n"
      "ds_read_b64_tr_b16 v[34:35], v177 offset:26112\n"
      "buffer_load_dwordx4 v178, s[12:15], s63 offen offset:0 lds\n"
      "buffer_load_dwordx4 v179, s[12:15], s63 offen offset:1024 lds\n"
      "buffer_load_dwordx4 v180, s[12:15], s63 offen offset:2048 lds\n"
      "s_sub_u32 s62, s62, 1\n"
      ".Ld_rs19:\n"
      "s_add_u32 s63, s44, 73728\n"
      "s_cmp_ge_u32 s62, 7\n"
      "s_cselect_b32 s63, s63, s31\n"
      "s_add_u32 m0, s42, 24576\n"
      "s_waitcnt vmcnt(18) lgkmcnt(0)\n"
      "s_barrier\n"
      "ds_read_b64_tr_b16 a[76:77], v176 offset:36864\n"
      "ds_read_b64_tr_b16 a[78:79], v176 offset:38400\n"
      "ds_read_b64_tr_b16 a[172:173], v176 offset:36872\n"
      "ds_read_b64_tr_b16 a[174:175], v176 offset:38408\n"
      "ds_read_b64_tr_b16 v[36:37], v177 offset:36864\n"
      "ds_read_b64_tr_b16 v[38:39], v177 offset:38400\n"
      "buffer_load_dwordx4 v178, s[12:15], s63 offen offset:0 lds\n"
      "buffer_load_dwordx4 v179, s[12:15], s63 offen offset:1024 lds\n"
      "buffer_load_dwordx4 v180, s[12:15], s63 offen offset:2048 lds\n"
      "s_sub_u32 s62, s62, 1\n"
      ".Ld_rs20:\n"
      "s_add_u32 s63, s44, 110592\n"
      "s_cmp_ge_u32 s62, 7\n"
      "s_cselect_b32 s63, s63, s31\n"
      "s_add_u32 m0, s42, 36864\n"
      "s_waitcnt vmcnt(18) lgkmcnt(0)\n"
      "s_barrier\n"
      "ds_read_b64_tr_b16 a[80:81], v20 offset:0\n"
      "ds_read_b64_tr_b16 a[82:83], v20 offset:1536\n"
      "ds_read_b64_tr_b16 a[176:177], v20 offset:8\n"
      "ds_read_b64_tr_b16 a[178:179], v20 offset:1544\n"
      "ds_read_b64_tr_b16 v[40:41], v21 offset:0\n"
      "ds_read_b64_tr_b16 v[42:43], v21 offset:1536\n"
      "buffer_load_dwordx4 v178, s[12:15], s63 offen offset:0 lds\n"
      "buffer_load_dwordx4 v179, s[12:15], s63 offen offset:1024 lds\n"
      "buffer_load_dwordx4 v180, s[12:15], s63 offen offset:2048 lds\n"
      "s_sub_u32 s62, s62, 1\n"
      ".Ld_rs21:\n"
      "s_add_u32 s63, s44, 147456\n"
      "s_cmp_ge_u32 s62, 7\n"
      "s_cselect_b32 s63, s63, s31\n"
      "s_add_u32 m0, s42, 49152\n"
      "s_waitcnt vmcnt(18) lgkmcnt(0)\n"
      "s_barrier\n"
      "ds_read_b64_tr_b16 a[84:85], v20 offset:12288\n"
      "ds_read_b64_tr_b16 a[86:87], v20 offset:13824\n"
      "ds_read_b64_tr_b16 a[180:181], v20 offset:12296\n"
      "ds_read_b64_tr_b16 a[182:183], v20 offset:13832\n"
      "ds_read_b64_tr_b16 v[44:45], v21 offset:12288\n"
      "ds_read_b64_tr_b16 v[46:47], v21 offset:13824\n"
      "buffer_load_dwordx4 v178, s[12:15], s63 offen offset:0 lds\n"
      "buffer_load_dwordx4 v179, s[12:15], s63 offen offset:1024 lds\n"
      "buffer_load_dwordx4 v180, s[12:15], s63 offen offset:2048 lds\n"
      "s_sub_u32 s62, s62, 1\n"
      ".Ld_rs22:\n"
      "s_add_u32 s63, s44, 184320\n"
      "s_cmp_ge_u32 s62, 7\n"
      "s_cselect_b32 s63, s63, s31\n"
      "s_add_u32 m0, s42, 61440\n"
      "s_waitcnt vmcnt(18) lgkmcnt(0)\n"
      "s_barrier\n"
      "ds_read_b64_tr_b16 a[88:89], v20 offset:24576\n"
      "ds_read_b64_tr_b16 a[90:91], v20 offset:26112\n"
      "ds_read_b64_tr_b16 a[184:185], v20 offset:24584\n"
      "ds_read_b64_tr_b16 a[186:187], v20 offset:26120\n"
      "ds_read_b64_tr_b16 v[48:49], v21 offset:24576\n"
      "ds_read_b64_tr_b16 v[50:51], v21 offset:26112\n"
      "buffer_load_dwordx4 v178, s[12:15], s63 offen offset:0 lds\n"
      "buffer_load_dwordx4 v179, s[12:15], s63 offen offset:1024 lds\n"
      "buffer_load_dwordx4 v180, s[12:15], s63 offen offset:2048 lds\n"
      "s_sub_u32 s62, s62, 1\n"
      ".Ld_rs23:\n"
      "s_add_u32 s63, s44, 221184\n"
      "s_cmp_ge_u32 s62, 7\n"
      "s_cselect_b32 s63, s63, s31\n"
      "s_add_u32 m0, s42, 73728\n"
      "s_waitcnt vmcnt(18) lgkmcnt(0)\n"
      "s_barrier\n"
      "ds_read_b64_tr_b16 a[92:93], v20 offset:36864\n"
      "ds_read_b64_tr_b16 a[94:95], v20 offset:38400\n"
      "ds_read_b64_tr_b16 a[188:189], v20 offset:36872\n"
      "ds_read_b64_tr_b16 a[190:191], v20 offset:38408\n"
      "ds_read_b64_tr_b16 v[52:53], v21 offset:36864\n"
      "ds_read_b64_tr_b16 v[54:55], v21 offset:38400\n"
      "buffer_load_dwordx4 v178, s[12:15], s63 offen offset:0 lds\n"
      "buffer_load_dwordx4 v179, s[12:15], s63 offen offset:1024 lds\n"
      "buffer_load_dwordx4 v180, s[12:15], s63 offen offset:2048 lds\n"
      "s_sub_u32 s62, s62, 1\n"
      "s_cbranch_scc0 .Ld_rs0\n"
      ".Ld_re:\n"
      "s_waitcnt vmcnt(0) lgkmcnt(0)\n"
      "v_add_u32 v22, 49152, v0\n"
      "v_add_u32 v23, 98304, v0\n"
      "s_lshr_b32 s58, s42, 10\n"
      "s_mul_i32 s58, s58, 1366\n"
      "s_and_b32 s58, s58, 0x7000\n"
      "s_getpc_b64 vcc\n"
      ".Ld_ct:\n"
      "s_add_u32 vcc_lo, vcc_lo, .Ld_p0-.Ld_ct\n"
      "s_addc_u32 vcc_hi, vcc_hi, 0\n"
      "s_lshl_b32 s30, s58, 1\n"
      "v_lshl_add_u32 v56, v0, 3, s30\n"
      "v_min_u32 v56, .Ld_last-.Ld_p0-4, v56\n"
      "global_load_dword v56, v56, vcc\n"
      "s_waitcnt vmcnt(0) lgkmcnt(0)\n"
      "s_mov_b64 exec, 0\n"
      "s_lshr_b32 s22, s58, 12\n"
      ".Ld_pl:\n"
      "s_cmp_eq_u32 s22, 0\n"
      "s_cbranch_scc1 .Ld_p0\n"
      "s_cmp_eq_u32 s22, 1\n"
      "s_cbranch_scc1 .Ld_p1\n"
      "s_branch .Ld_pe\n"
      ".Ld_pr:\n"
      "s_add_u32 s22, s22, 4\n"
      "s_branch .Ld_pl\n"
      ".Ld_pe:\n"
      "s_mov_b64 exec, -1\n"
      "s_barrier\n"
      "v_mov_b32 v14, 0\n"
      "ds_write_b32 v16, v14\n"
      "s_waitcnt lgkmcnt(0)\n"
      "s_barrier\n"
      "s_mov_b32 s6, s50\n"
      "s_add_u32 s36, s36, 4294801408\n"
      "s_add_u32 s53, s37, 0\n"
      "s_add_u32 s52, s37, 4294787584\n"
      "s_add_u32 s37, s37, 4294894080\n"
      "s_mov_b32 s56, 0x40000000\n"
      "s_mov_b32 s57, 0x40000000\n"
      "s_mov_b32 s59, 63\n"
      "s_cmp_eq_u32 s45, 1\n"
      "s_cbranch_scc1 .Ld_in1\n"
      "s_cmp_eq_u32 s45, 2\n"
      "s_cbranch_scc1 .Ld_in2\n"
      "s_cmp_eq_u32 s45, 3\n"
      "s_cbranch_scc1 .Ld_in3\n"
      "s_cmp_eq_u32 s45, 4\n"
      "s_cbranch_scc1 .Ld_in4\n"
      "s_cmp_eq_u32 s45, 5\n"
      "s_cbranch_scc1 .Ld_in5\n"
      "s_add_u32 s36, s36, 165888\n"
      "s_min_u32 s36, s36, s6\n"
      "buffer_load_dword v20, v2, s[8:11], s36 offen sc1\n"
      "s_add_u32 m0, s43, 0\n"
      "s_mov_b32 s51, s36\n"
      "s_add_u32 s36, s36, 165888\n"
      "s_min_u32 s36, s36, s6\n"
      "s_waitcnt vmcnt(0)\n"
      "v_cmp_ne_u32 vcc, s21, v20\n"
      "s_cbranch_vccnz .Lsp_d0_1\n"
      ".Lbk_d0_1:\n"
      "buffer_load_dwordx4 v0, s[8:11], s51 offen offset:0 sc1 lds\n"
      "buffer_load_dwordx4 v0, s[8:11], s51 offen offset:1024 sc1 lds\n"
      "buffer_load_dword v20, v2, s[8:11], s36 offen sc1\n"
      "buffer_load_dwordx4 v0, s[8:11], s51 offen offset:2048 sc1 lds\n"
      "buffer_load_dwordx4 v0, s[8:11], s51 offen offset:3072 sc1 lds\n"
      "s_add_u32 m0, s43, 4096\n"
      "s_nop 0\n"
      "buffer_load_dwordx4 v1, s[8:11], s51 offen offset:1024 sc1 lds\n"
      "buffer_load_dwordx4 v1, s[8:11], s51 offen offset:0 sc1 lds\n"
      "s_mov_b32 s51, s36\n"
      "s_add_u32 m0, s43, 24576\n"
      "v_add_u32 v14, 1, v14\n"
      "s_add_u32 s36, s36, 165888\n"
      "buffer_load_dwordx4 v[216:219], v6, s[8:11], s38 offen sc1\n"
      "s_waitcnt vmcnt(5)\n"
      "v_cmp_ne_u32 vcc, s21, v20\n"
      "buffer_load_dwordx2 v[220:221], v7, s[8:11], s38 offen sc1\n"
      "s_cbranch_vccnz .Lsp_d1_2\n"
      ".Lbk_d1_2:\n"
      "buffer_load_dwordx4 v0, s[8:11], s51 offen offset:0 sc1 lds\n"
      "buffer_load_dwordx4 v0, s[8:11], s51 offen offset:1024 sc1 lds\n"
      "s_waitcnt vmcnt(4)\n"
      "ds_write_b32 v16, v14\n"
      "buffer_load_dwordx4 v0, s[8:11], s51 offen offset:2048 sc1 lds\n"
      "s_min_u32 s36, s36, s6\n"
      "buffer_load_dword v20, v2, s[8:11], s36 offen sc1\n"
      "buffer_load_dwordx4 v0, s[8:11], s51 offen offset:3072 sc1 lds\n"
      "ds_read_b32 v15, v17\n"
      "s_add_u32 m0, s43, 28672\n"
      "s_nop 0\n"
      "buffer_load_dwordx4 v1, s[8:11], s51 offen offset:0 sc1 lds\n"
      "s_waitcnt lgkmcnt(0)\n"
      "v_cmp_gt_i32 vcc, v14, v15\n"
      "buffer_load_dwordx4 v1, s[8:11], s51 offen offset:1024 sc1 lds\n"
      "s_cbranch_vccnz .Lsp_dy0_3\n"
      ".Lbk_dy0_3:\n"
      "ds_read_b128 v[56:59], v0 offset:0\n"
      "ds_read_b128 v[60:63], v0 offset:1024\n"
      "ds_read_b128 v[84:87], v0 offset:7168\n"
      "ds_read_b128 v[72:75], v0 offset:4096\n"
      "ds_read_b128 v[88:91], v0 offset:8192\n"
      "ds_read_b128 v[68:71], v0 offset:3072\n"
      "ds_read_b128 v[104:107], v0 offset:12288\n"
      "ds_read_b128 v[96:99], v0 offset:10240\n"
      "ds_read_b128 v[80:83], v0 offset:6144\n"
      "ds_read_b128 v[64:67], v0 offset:2048\n"
      "ds_read_b128 v[92:95], v0 offset:9216\n"
      "ds_read_b128 v[76:79], v0 offset:5120\n"
      "ds_read_b128 v[132:135], v0 offset:19456\n"
      "ds_read_b128 v[100:103], v0 offset:11264\n"
      "s_waitcnt vmcnt(0) lgkmcnt(0)\n"
      "s_nop 4\n"
      "s_branch .Ld_t0\n"
      ".p2align 12\n"
      ".Ld_p0:\n"
      "s_branch .Ld_pr\n"
      ".p2align 5\n"
      ".Ld_t0:\n"
      "v_mfma_f32_16x16x32_bf16 v[152:155], a[0:3], v[56:59], 0\n"
      "ds_read_b128 v[108:111], v0 offset:13312\n"
      "ds_read_b128 v[120:123], v0 offset:16384\n"
      "v_mfma_f32_16x16x32_bf16 v[152:155], a[4:7], v[60:63], v[152:155]\n"
      "s_waitcnt lgkmcnt(6)\n"
      "ds_read_b128 v[116:119], v0 offset:15360\n"
      "v_add_f32 v236, v236, v168\n"
      "v_mfma_f32_16x16x32_bf16 v[152:155], a[8:11], v[64:67], v[152:155]\n"
      "v_cvt_pk_bf16_f32 v228, v160, v161\n"
      "v_cvt_pk_bf16_f32 v229, v162, v163\n"
      "v_mfma_f32_16x16x32_bf16 v[152:155], a[12:15], v[68:71], v[152:155]\n"
      "buffer_store_dwordx2 v[228:229], v13, s[8:11], s37 offen sc1\n"
      "v_mfma_f32_16x16x32_bf16 v[152:155], a[16:19], v[72:75], v[152:155]\n"
      "s_waitcnt lgkmcnt(4)\n"
      "v_cvt_pk_bf16_f32 v178, v168, v169\n"
      "v_add_f32 v175, v175, v213\n"
      "v_mfma_f32_16x16x32_bf16 v[152:155], a[20:23], v[76:79], v[152:155]\n"
      "ds_read_b128 v[112:115], v0 offset:14336\n"
      "v_add_f32 v171, v171, v211\n"
      "v_mfma_f32_16x16x32_bf16 v[152:155], a[24:27], v[80:83], v[152:155]\n"
      "ds_read_b128 v[124:127], v0 offset:17408\n"
      "v_add_f32 v236, v236, v169\n"
      "v_mfma_f32_16x16x32_bf16 v[152:155], a[28:31], v[84:87], v[152:155]\n"
      "v_add_f32 v173, v173, v204\n"
      "v_cvt_pk_bf16_f32 v179, v170, v171\n"
      "v_mfma_f32_16x16x32_bf16 v[152:155], a[32:35], v[88:91], v[152:155]\n"
      "ds_read_b128 v[128:131], v0 offset:18432\n"
      "v_add_f32 v173, v173, v212\n"
      "v_mfma_f32_16x16x32_bf16 v[152:155], a[36:39], v[92:95], v[152:155]\n"
      "buffer_store_dwordx4 v[176:179], v6, s[8:11], s56 offen sc1\n"
      "v_mfma_f32_16x16x32_bf16 v[152:155], a[40:43], v[96:99], v[152:155]\n"
      "s_waitcnt lgkmcnt(3)\n"
      "ds_read_b128 v[148:151], v0 offset:23552\n"
      "v_cvt_pk_bf16_f32 v180, v172, v173\n"
      "v_mfma_f32_16x16x32_bf16 v[152:155], a[44:47], v[100:103], v[152:155]\n"
      "ds_read_b128 v[144:147], v0 offset:22528\n"
      "v_fmac_f32 v237, v168, v168\n"
      "v_mfma_f32_16x16x32_bf16 v[152:155], a[48:51], v[104:107], v[152:155]\n"
      "s_add_u32 m0, s43, 49152\n"
      "ds_read_b128 v[136:139], v0 offset:20480\n"
      "v_fmac_f32 v237, v169, v169\n"
      "v_mfma_f32_16x16x32_bf16 v[152:155], a[52:55], v[108:111], v[152:155]\n"
      "s_waitcnt lgkmcnt(4)\n"
      "s_mov_b32 s51, s36\n"
      "ds_read_b128 v[140:143], v0 offset:21504\n"
      "v_mfma_f32_16x16x32_bf16 v[152:155], a[56:59], v[112:115], v[152:155]\n"
      "v_cvt_pk_bf16_f32 v181, v174, v175\n"
      "v_fmac_f32 v237, v170, v170\n"
      "v_mfma_f32_16x16x32_bf16 v[152:155], a[60:63], v[116:119], v[152:155]\n"
      "buffer_store_dwordx2 v[180:181], v7, s[8:11], s56 offen sc1\n"
      "v_mfma_f32_16x16x32_bf16 v[152:155], a[64:67], v[120:123], v[152:155]\n"
      "v_add_u32 v14, 1, v14\n"
      "v_mfma_f32_16x16x32_bf16 v[152:155], a[68:71], v[124:127], v[152:155]\n"
      "s_waitcnt lgkmcnt(3)\n"
      "v_add_f32 v236, v236, v170\n"
      "v_fmac_f32 v237, v171, v171\n"
      "v_mfma_f32_16x16x32_bf16 v[152:155], a[72:75], v[128:131], v[152:155]\n"
      "v_fmac_f32 v237, v172, v172\n"
      "v_fmac_f32 v237, v173, v173\n"
      "v_mfma_f32_16x16x32_bf16 v[152:155], a[76:79], v[132:135], v[152:155]\n"
      "s_waitcnt lgkmcnt(1)\n"
      "buffer_store_dwordx2 v[180:181], v9, s[16:19], s57 offen\n"
      "v_mfma_f32_16x16x32_bf16 v[152:155], a[80:83], v[136:139], v[152:155]\n"
      "s_waitcnt lgkmcnt(0)\n"
      "v_add_f32 v236, v236, v171\n"
      "v_fmac_f32 v237, v174, v174\n"
      "v_mfma_f32_16x16x32_bf16 v[152:155], a[84:87], v[140:143], v[152:155]\n"
      "v_add_f32 v236, v236, v172\n"
      "v_fmac_f32 v237, v175, v175\n"
      "v_mfma_f32_16x16x32_bf16 v[152:155], a[88:91], v[144:147], v[152:155]\n"
      "v_add_f32 v236, v236, v173\n"
      "v_add_f32 v236, v236, v174\n"
      "v_mfma_f32_16x16x32_bf16 v[152:155], a[92:95], v[148:151], v[152:155]\n"
      "buffer_store_dwordx4 v[176:179], v8, s[16:19], s57 offen\n"
      "v_mfma_f32_16x16x32_bf16 v[156:159], a[96:99], v[56:59], 0\n"
      "s_waitcnt vmcnt(15)\n"
      "v_add_f32 v236, v236, v175\n"
      "v_lshlrev_b32 v166, 16, v217\n"
      "v_mfma_f32_16x16x32_bf16 v[156:159], a[100:103], v[60:63], v[156:159]\n"
      "s_waitcnt vmcnt(10)\n"
      "v_cmp_ne_u32 vcc, s21, v20\n"
      "v_mfma_f32_16x16x32_bf16 v[156:159], a[104:107], v[64:67], v[156:159]\n"
      "ds_write_b64 v10, v[236:237]\n"
      "v_mfma_f32_16x16x32_bf16 v[156:159], a[108:111], v[68:71], v[156:159]\n"
      "s_add_u32 s36, s36, 165888\n"
      "v_mfma_f32_16x16x32_bf16 v[156:159], a[112:115], v[72:75], v[156:159]\n"
      "s_min_u32 s36, s36, s6\n"
      "s_cbranch_vccnz .Lsp_d2_4\n"
      ".Lbk_d2_4:\n"
      "v_mfma_f32_16x16x32_bf16 v[156:159], a[116:119], v[76:79], v[156:159]\n"
      "s_waitcnt vmcnt(7)\n"
      "ds_write_b32 v16, v14\n"
      "v_mfma_f32_16x16x32_bf16 v[156:159], a[120:123], v[80:83], v[156:159]\n"
      "buffer_load_dword v20, v2, s[8:11], s36 offen sc1\n"
      "v_mfma_f32_16x16x32_bf16 v[156:159], a[124:127], v[84:87], v[156:159]\n"
      "v_and_b32 v167, 0xffff0000, v217\n"
      "v_lshlrev_b32 v164, 16, v216\n"
      "v_mfma_f32_16x16x32_bf16 v[156:159], a[128:131], v[88:91], v[156:159]\n"
      "v_mfma_f32_16x16x32_bf16 v[156:159], a[132:135], v[92:95], v[156:159]\n"
      "buffer_load_dwordx4 v0, s[8:11], s51 offen offset:3072 sc1 lds\n"
      "v_mfma_f32_16x16x32_bf16 v[156:159], a[136:139], v[96:99], v[156:159]\n"
      "s_add_u32 s52, s52, 106496\n"
      "v_mfma_f32_16x16x32_bf16 v[156:159], a[140:143], v[100:103], v[156:159]\n"
      "v_mfma_f32_16x16x32_bf16 v[156:159], a[144:147], v[104:107], v[156:159]\n"
      "s_add_u32 s37, s37, 173056\n"
      "v_and_b32 v165, 0xffff0000, v216\n"
      "v_mfma_f32_16x16x32_bf16 v[156:159], a[148:151], v[108:111], v[156:159]\n"
      "buffer_load_dwordx4 v0, s[8:11], s51 offen offset:1024 sc1 lds\n"
      "v_mfma_f32_16x16x32_bf16 v[156:159], a[152:155], v[112:115], v[156:159]\n"
      "ds_read_b32 v15, v17\n"
      "v_mfma_f32_16x16x32_bf16 v[156:159], a[156:159], v[116:119], v[156:159]\n"
      "v_mfma_f32_16x16x32_bf16 v[156:159], a[160:163], v[120:123], v[156:159]\n"
      "v_lshlrev_b32 v170, 16, v219\n"
      "v_mfma_f32_16x16x32_bf16 v[156:159], a[164:167], v[124:127], v[156:159]\n"
      "buffer_load_dwordx4 v0, s[8:11], s51 offen offset:2048 sc1 lds\n"
      "v_mfma_f32_16x16x32_bf16 v[156:159], a[168:171], v[128:131], v[156:159]\n"
      "v_mfma_f32_16x16x32_bf16 v[156:159], a[172:175], v[132:135], v[156:159]\n"
      "s_waitcnt lgkmcnt(0)\n"
      "v_cmp_gt_i32 vcc, v14, v15\n"
      "v_mfma_f32_16x16x32_bf16 v[156:159], a[176:179], v[136:139], v[156:159]\n"
      "v_add_f32 v165, v165, v153\n"
      "v_mfma_f32_16x16x32_bf16 v[156:159], a[180:183], v[140:143], v[156:159]\n"
      "buffer_load_dwordx4 v0, s[8:11], s51 offen offset:0 sc1 lds\n"
      "v_mfma_f32_16x16x32_bf16 v[156:159], a[184:187], v[144:147], v[156:159]\n"
      "v_add_f32 v164, v164, v152\n"
      "s_add_u32 m0, s43, 53248\n"
      "v_mfma_f32_16x16x32_bf16 v[156:159], a[188:191], v[148:151], v[156:159]\n"
      "s_cbranch_vccnz .Lsp_dy1_5\n"
      ".Lbk_dy1_5:\n"
      "ds_read_b64 v[238:239], v11\n"
      "v_mfma_f32_16x16x32_bf16 v[160:163], a[192:195], v[56:59], 0\n"
      "v_lshlrev_b32 v168, 16, v218\n"
      "v_mfma_f32_16x16x32_bf16 v[160:163], a[196:199], v[60:63], v[160:163]\n"
      "buffer_load_dwordx4 v1, s[8:11], s51 offen offset:0 sc1 lds\n"
      "v_mfma_f32_16x16x32_bf16 v[160:163], a[200:203], v[64:67], v[160:163]\n"
      "v_mfma_f32_16x16x32_bf16 v[160:163], a[204:207], v[68:71], v[160:163]\n"
      "ds_read_b128 v[56:59], v0 offset:24576\n"
      "v_mfma_f32_16x16x32_bf16 v[160:163], a[208:211], v[72:75], v[160:163]\n"
      "ds_read_b128 v[64:67], v0 offset:26624\n"
      "v_mfma_f32_16x16x32_bf16 v[160:163], a[212:215], v[76:79], v[160:163]\n"
      "buffer_load_dwordx4 v1, s[8:11], s51 offen offset:1024 sc1 lds\n"
      "v_mfma_f32_16x16x32_bf16 v[160:163], a[216:219], v[80:83], v[160:163]\n"
      "s_waitcnt lgkmcnt(2)\n"
      "ds_read_b128 v[68:71], v0 offset:27648\n"
      "v_add_f32_dpp v238, v238, v238 row_ror:8 row_mask:0xf bank_mask:0xf\n"
      "v_mfma_f32_16x16x32_bf16 v[160:163], a[220:223], v[84:87], v[160:163]\n"
      "ds_read_b128 v[80:83], v0 offset:30720\n"
      "v_add_f32_dpp v238, v238, v238 row_ror:4 row_mask:0xf bank_mask:0xf\n"
      "v_mfma_f32_16x16x32_bf16 v[160:163], a[224:227], v[88:91], v[160:163]\n"
      "ds_read_b128 v[60:63], v0 offset:25600\n"
      "v_add_f32_dpp v238, v238, v238 row_ror:2 row_mask:0xf bank_mask:0xf\n"
      "v_mfma_f32_16x16x32_bf16 v[160:163], a[228:231], v[92:95], v[160:163]\n"
      "ds_read_b128 v[92:95], v0 offset:33792\n"
      "v_add_f32_dpp v239, v239, v239 row_ror:8 row_mask:0xf bank_mask:0xf\n"
      "v_mfma_f32_16x16x32_bf16 v[160:163], a[232:235], v[96:99], v[160:163]\n"
      "ds_read_b128 v[84:87], v0 offset:31744\n"
      "v_add_f32_dpp v239, v239, v239 row_ror:4 row_mask:0xf bank_mask:0xf\n"
      "v_mfma_f32_16x16x32_bf16 v[160:163], a[236:239], v[100:103], v[160:163]\n"
      "s_waitcnt vmcnt(11) lgkmcnt(5)\n"
      "buffer_store_dword v18, v4, s[8:11], s52 offen sc1\n"
      "v_mfma_f32_16x16x32_bf16 v[160:163], a[240:243], v[104:107], v[160:163]\n"
      "ds_read_b128 v[72:75], v0 offset:28672\n"
      "v_add_f32_dpp v239, v239, v239 row_ror:2 row_mask:0xf bank_mask:0xf\n"
      "v_mfma_f32_16x16x32_bf16 v[160:163], a[244:247], v[108:111], v[160:163]\n"
      "ds_read_b128 v[76:79], v0 offset:29696\n"
      "v_add_f32_dpp v239, v239, v239 row_ror:1 row_mask:0xf bank_mask:0xf\n"
      "v_mfma_f32_16x16x32_bf16 v[160:163], a[248:251], v[112:115], v[160:163]\n"
      "v_add_f32_dpp v238, v238, v238 row_ror:1 row_mask:0xf bank_mask:0xf\n"
      "v_add_f32 v168, v168, v156\n"
      "v_mfma_f32_16x16x32_bf16 v[160:163], a[252:255], v[116:119], v[160:163]\n"
      "ds_read_b128 v[100:103], v0 offset:35840\n"
      "v_mul_f32 v238, 0x3ae38e39, v238\n"
      "v_mfma_f32_16x16x32_bf16 v[160:163], v[24:27], v[120:123], v[160:163]\n"
      "v_and_b32 v169, 0xffff0000, v218\n"
      "v_mfma_f32_16x16x32_bf16 v[160:163], v[28:31], v[124:127], v[160:163]\n"
      "ds_read_b128 v[96:99], v0 offset:34816\n"
      "v_and_b32 v175, 0xffff0000, v221\n"
      "v_mfma_f32_16x16x32_bf16 v[160:163], v[32:35], v[128:131], v[160:163]\n"
      "ds_read_b128 v[116:119], v0 offset:39936\n"
      "v_lshlrev_b32 v174, 16, v221\n"
      "v_mfma_f32_16x16x32_bf16 v[160:163], v[36:39], v[132:135], v[160:163]\n"
      "ds_read_b128 v[120:123], v0 offset:40960\n"
      "v_add_f32 v167, v167, v155\n"
      "v_mfma_f32_16x16x32_bf16 v[160:163], v[40:43], v[136:139], v[160:163]\n"
      "ds_read_b128 v[108:111], v0 offset:37888\n"
      "v_and_b32 v171, 0xffff0000, v219\n"
      "v_mfma_f32_16x16x32_bf16 v[160:163], v[44:47], v[140:143], v[160:163]\n"
      "ds_read_b128 v[88:91], v0 offset:32768\n"
      "v_add_f32 v166, v166, v154\n"
      "v_mfma_f32_16x16x32_bf16 v[160:163], v[48:51], v[144:147], v[160:163]\n"
      "v_add_f32 v239, 0x3afba882, v239\n"
      "v_mul_f32 v239, 0x3ae38e39, v239\n"
      "v_mfma_f32_16x16x32_bf16 v[160:163], v[52:55], v[148:151], v[160:163]\n"
      "s_sub_u32 s59, s59, 1\n"
      "s_cbranch_scc1 .Ld_out0\n"
      ".Ld_t1:\n"
      "v_mfma_f32_16x16x32_bf16 v[152:155], a[0:3], v[56:59], 0\n"
      "s_waitcnt lgkmcnt(4)\n"
      "buffer_store_dwordx2 v[238:239], v12, s[8:11], s56 offen sc1\n"
      "v_mfma_f32_16x16x32_bf16 v[152:155], a[4:7], v[60:63], v[152:155]\n"
      "ds_read_b128 v[104:107], v0 offset:36864\n"
      "v_mfma_f32_16x16x32_bf16 v[152:155], a[8:11], v[64:67], v[152:155]\n"
      "ds_read_b128 v[140:143], v0 offset:46080\n"
      "v_mfma_f32_16x16x32_bf16 v[152:155], a[12:15], v[68:71], v[152:155]\n"
      "ds_read_b128 v[112:115], v0 offset:38912\n"
      "v_mfma_f32_16x16x32_bf16 v[152:155], a[16:19], v[72:75], v[152:155]\n"
      "ds_read_b128 v[124:127], v0 offset:41984\n"
      "v_mfma_f32_16x16x32_bf16 v[152:155], a[20:23], v[76:79], v[152:155]\n"
      "ds_read_b128 v[132:135], v0 offset:44032\n"
      "v_mfma_f32_16x16x32_bf16 v[152:155], a[24:27], v[80:83], v[152:155]\n"
      "s_mov_b32 s51, s36\n"
      "v_add_f32 v174, v174, v162\n"
      "s_add_u32 s36, s36, 165888\n"
      "v_mfma_f32_16x16x32_bf16 v[152:155], a[28:31], v[84:87], v[152:155]\n"
      "s_waitcnt lgkmcnt(5)\n"
      "ds_read_b128 v[148:151], v0 offset:48128\n"
      "v_mfma_f32_16x16x32_bf16 v[152:155], a[32:35], v[88:91], v[152:155]\n"
      "ds_read_b128 v[128:131], v0 offset:43008\n"
      "s_add_u32 m0, s43, 73728\n"
      "v_mfma_f32_16x16x32_bf16 v[152:155], a[36:39], v[92:95], v[152:155]\n"
      "ds_read_b128 v[136:139], v0 offset:45056\n"
      "v_mfma_f32_16x16x32_bf16 v[152:155], a[40:43], v[96:99], v[152:155]\n"
      "v_mfma_f32_16x16x32_bf16 v[152:155], a[44:47], v[100:103], v[152:155]\n"
      "s_waitcnt lgkmcnt(4)\n"
      "v_add_f32 v170, v170, v158\n"
      "v_mfma_f32_16x16x32_bf16 v[152:155], a[48:51], v[104:107], v[152:155]\n"
      "v_mfma_f32_16x16x32_bf16 v[152:155], a[52:55], v[108:111], v[152:155]\n"
      "s_waitcnt vmcnt(8) lgkmcnt(3)\n"
      "v_lshlrev_b32 v172, 16, v220\n"
      "v_cmp_ne_u32 vcc, s21, v20\n"
      "v_mfma_f32_16x16x32_bf16 v[152:155], a[56:59], v[112:115], v[152:155]\n"
      "ds_read_b128 v[144:147], v0 offset:47104\n"
      "s_add_u32 s38, s38, 147456\n"
      "v_mfma_f32_16x16x32_bf16 v[152:155], a[60:63], v[116:119], v[152:155]\n"
      "s_min_u32 s36, s36, s6\n"
      "v_add_f32 v171, v171, v159\n"
      "v_mfma_f32_16x16x32_bf16 v[152:155], a[64:67], v[120:123], v[152:155]\n"
      "v_mfma_f32_16x16x32_bf16 v[152:155], a[68:71], v[124:127], v[152:155]\n"
      "s_waitcnt lgkmcnt(1)\n"
      "v_mfma_f32_16x16x32_bf16 v[152:155], a[72:75], v[128:131], v[152:155]\n"
      "v_add_f32 v169, v169, v157\n"
      "v_add_f32 v172, v172, v160\n"
      "v_mfma_f32_16x16x32_bf16 v[152:155], a[76:79], v[132:135], v[152:155]\n"
      "v_mfma_f32_16x16x32_bf16 v[152:155], a[80:83], v[136:139], v[152:155]\n"
      "s_cbranch_vccnz .Lsp_d3_6\n"
      ".Lbk_d3_6:\n"
      "v_mfma_f32_16x16x32_bf16 v[152:155], a[84:87], v[140:143], v[152:155]\n"
      "s_waitcnt lgkmcnt(0)\n"
      "v_mfma_f32_16x16x32_bf16 v[152:155], a[88:91], v[144:147], v[152:155]\n"
      "v_mfma_f32_16x16x32_bf16 v[152:155], a[92:95], v[148:151], v[152:155]\n"
      "buffer_load_dwordx4 v0, s[8:11], s51 offen offset:2048 sc1 lds\n"
      "v_mfma_f32_16x16x32_bf16 v[156:159], a[96:99], v[56:59], 0\n"
      "v_mfma_f32_16x16x32_bf16 v[156:159], a[100:103], v[60:63], v[156:159]\n"
      "v_add_u32 v14, 1, v14\n"
      "v_mfma_f32_16x16x32_bf16 v[156:159], a[104:107], v[64:67], v[156:159]\n"
      "v_cvt_pk_bf16_f32 v225, v154, v155\n"
      "v_mfma_f32_16x16x32_bf16 v[156:159], a[108:111], v[68:71], v[156:159]\n"
      "v_add_f32 v175, v175, v163\n"
      "v_mfma_f32_16x16x32_bf16 v[156:159], a[112:115], v[72:75], v[156:159]\n"
      "buffer_load_dwordx4 v0, s[8:11], s51 offen offset:1024 sc1 lds\n"
      "v_mfma_f32_16x16x32_bf16 v[156:159], a[116:119], v[76:79], v[156:159]\n"
      "v_mfma_f32_16x16x32_bf16 v[156:159], a[120:123], v[80:83], v[156:159]\n"
      "v_mfma_f32_16x16x32_bf16 v[156:159], a[124:127], v[84:87], v[156:159]\n"
      "v_and_b32 v173, 0xffff0000, v220\n"
      "v_mfma_f32_16x16x32_bf16 v[156:159], a[128:131], v[88:91], v[156:159]\n"
      "buffer_load_dword v20, v2, s[8:11], s36 offen sc1\n"
      "v_mfma_f32_16x16x32_bf16 v[156:159], a[132:135], v[92:95], v[156:159]\n"
      "s_waitcnt vmcnt(5)\n"
      "ds_write_b32 v16, v14\n"
      "v_mfma_f32_16x16x32_bf16 v[156:159], a[136:139], v[96:99], v[156:159]\n"
      "buffer_load_dwordx4 v0, s[8:11], s51 offen offset:0 sc1 lds\n"
      "v_mfma_f32_16x16x32_bf16 v[156:159], a[140:143], v[100:103], v[156:159]\n"
      "v_add_f32 v173, v173, v161\n"
      "v_mfma_f32_16x16x32_bf16 v[156:159], a[144:147], v[104:107], v[156:159]\n"
      "v_mfma_f32_16x16x32_bf16 v[156:159], a[148:151], v[108:111], v[156:159]\n"
      "v_mfma_f32_16x16x32_bf16 v[156:159], a[152:155], v[112:115], v[156:159]\n"
      "v_mfma_f32_16x16x32_bf16 v[156:159], a[156:159], v[116:119], v[156:159]\n"
      "buffer_load_dwordx4 v0, s[8:11], s51 offen offset:3072 sc1 lds\n"
      "v_mfma_f32_16x16x32_bf16 v[156:159], a[160:163], v[120:123], v[156:159]\n"
      "s_add_u32 m0, s43, 77824\n"
      "v_mfma_f32_16x16x32_bf16 v[156:159], a[164:167], v[124:127], v[156:159]\n"
      "v_mfma_f32_16x16x32_bf16 v[156:159], a[168:171], v[128:131], v[156:159]\n"
      "ds_read_b32 v15, v17\n"
      "v_mfma_f32_16x16x32_bf16 v[156:159], a[172:175], v[132:135], v[156:159]\n"
      "s_add_u32 s52, s52, 173056\n"
      "v_mfma_f32_16x16x32_bf16 v[156:159], a[176:179], v[136:139], v[156:159]\n"
      "buffer_load_dwordx4 v1, s[8:11], s51 offen offset:0 sc1 lds\n"
      "v_mfma_f32_16x16x32_bf16 v[156:159], a[180:183], v[140:143], v[156:159]\n"
      "v_mfma_f32_16x16x32_bf16 v[156:159], a[184:187], v[144:147], v[156:159]\n"
      "v_cvt_pk_bf16_f32 v224, v152, v153\n"
      "v_mfma_f32_16x16x32_bf16 v[156:159], a[188:191], v[148:151], v[156:159]\n"
      "s_waitcnt lgkmcnt(0)\n"
      "v_cmp_gt_i32 vcc, v14, v15\n"
      "v_mfma_f32_16x16x32_bf16 v[160:163], a[192:195], v[56:59], 0\n"
      "buffer_load_dwordx4 v1, s[8:11], s51 offen offset:1024 sc1 lds\n"
      "v_mfma_f32_16x16x32_bf16 v[160:163], a[196:199], v[60:63], v[160:163]\n"
      "v_mfma_f32_16x16x32_bf16 v[160:163], a[200:203], v[64:67], v[160:163]\n"
      "s_cbranch_vccnz .Lsp_dy2_7\n"
      ".Lbk_dy2_7:\n"
      "v_mfma_f32_16x16x32_bf16 v[160:163], a[204:207], v[68:71], v[160:163]\n"
      "ds_read_b128 v[68:71], v22 offset:3072\n"
      "v_mfma_f32_16x16x32_bf16 v[160:163], a[208:211], v[72:75], v[160:163]\n"
      "ds_read_b128 v[60:63], v22 offset:1024\n"
      "v_mfma_f32_16x16x32_bf16 v[160:163], a[212:215], v[76:79], v[160:163]\n"
      "s_waitcnt vmcnt(7)\n"
      "buffer_store_dword v18, v5, s[8:11], s56 offen sc1\n"
      "v_mfma_f32_16x16x32_bf16 v[160:163], a[216:219], v[80:83], v[160:163]\n"
      "v_cvt_pk_bf16_f32 v227, v158, v159\n"
      "v_mfma_f32_16x16x32_bf16 v[160:163], a[220:223], v[84:87], v[160:163]\n"
      "ds_read_b128 v[80:83], v22 offset:6144\n"
      "v_cvt_pk_bf16_f32 v226, v156, v157\n"
      "v_mfma_f32_16x16x32_bf16 v[160:163], a[224:227], v[88:91], v[160:163]\n"
      "ds_read_b128 v[72:75], v22 offset:4096\n"
      "v_mfma_f32_16x16x32_bf16 v[160:163], a[228:231], v[92:95], v[160:163]\n"
      "ds_read_b128 v[76:79], v22 offset:5120\n"
      "v_mfma_f32_16x16x32_bf16 v[160:163], a[232:235], v[96:99], v[160:163]\n"
      "ds_read_b128 v[96:99], v22 offset:10240\n"
      "v_mfma_f32_16x16x32_bf16 v[160:163], a[236:239], v[100:103], v[160:163]\n"
      "v_mfma_f32_16x16x32_bf16 v[160:163], a[240:243], v[104:107], v[160:163]\n"
      "buffer_store_dwordx4 v[224:227], v0, s[8:11], s37 offen sc1\n"
      "v_mfma_f32_16x16x32_bf16 v[160:163], a[244:247], v[108:111], v[160:163]\n"
      "v_mfma_f32_16x16x32_bf16 v[160:163], a[248:251], v[112:115], v[160:163]\n"
      "ds_read_b128 v[84:87], v22 offset:7168\n"
      "v_mfma_f32_16x16x32_bf16 v[160:163], a[252:255], v[116:119], v[160:163]\n"
      "v_mfma_f32_16x16x32_bf16 v[160:163], v[24:27], v[120:123], v[160:163]\n"
      "ds_read_b128 v[56:59], v22 offset:0\n"
      "v_mfma_f32_16x16x32_bf16 v[160:163], v[28:31], v[124:127], v[160:163]\n"
      "ds_read_b128 v[92:95], v22 offset:9216\n"
      "s_mov_b32 s51, s36\n"
      "v_mfma_f32_16x16x32_bf16 v[160:163], v[32:35], v[128:131], v[160:163]\n"
      "ds_read_b128 v[64:67], v22 offset:2048\n"
      "v_mfma_f32_16x16x32_bf16 v[160:163], v[36:39], v[132:135], v[160:163]\n"
      "ds_read_b128 v[100:103], v22 offset:11264\n"
      "v_mfma_f32_16x16x32_bf16 v[160:163], v[40:43], v[136:139], v[160:163]\n"
      "ds_read_b128 v[132:135], v22 offset:19456\n"
      "v_mfma_f32_16x16x32_bf16 v[160:163], v[44:47], v[140:143], v[160:163]\n"
      "v_mfma_f32_16x16x32_bf16 v[160:163], v[48:51], v[144:147], v[160:163]\n"
      "s_waitcnt lgkmcnt(4)\n"
      "ds_read_b128 v[112:115], v22 offset:14336\n"
      "s_add_u32 m0, s43, 98304\n"
      "v_mfma_f32_16x16x32_bf16 v[160:163], v[52:55], v[148:151], v[160:163]\n"
      "s_sub_u32 s59, s59, 1\n"
      "s_cbranch_scc1 .Ld_out1\n"
      ".Ld_t2:\n"
      "v_mfma_f32_16x16x32_bf16 v[152:155], a[0:3], v[56:59], 0\n"
      "ds_read_b128 v[88:91], v22 offset:8192\n"
      "v_mfma_f32_16x16x32_bf16 v[152:155], a[4:7], v[60:63], v[152:155]\n"
      "s_waitcnt lgkmcnt(4)\n"
      "ds_read_b128 v[120:123], v22 offset:16384\n"
      "v_mfma_f32_16x16x32_bf16 v[152:155], a[8:11], v[64:67], v[152:155]\n"
      "s_add_u32 s36, s36, 165888\n"
      "ds_read_b128 v[104:107], v22 offset:12288\n"
      "v_cvt_pk_bf16_f32 v229, v162, v163\n"
      "v_mfma_f32_16x16x32_bf16 v[152:155], a[12:15], v[68:71], v[152:155]\n"
      "ds_read_b128 v[148:151], v22 offset:23552\n"
      "v_cvt_pk_bf16_f32 v228, v160, v161\n"
      "v_mfma_f32_16x16x32_bf16 v[152:155], a[16:19], v[72:75], v[152:155]\n"
      "buffer_store_dwordx2 v[228:229], v13, s[8:11], s37 offen sc1\n"
      "v_mfma_f32_16x16x32_bf16 v[152:155], a[20:23], v[76:79], v[152:155]\n"
      "ds_read_b128 v[108:111], v22 offset:13312\n"
      "v_mfma_f32_16x16x32_bf16 v[152:155], a[24:27], v[80:83], v[152:155]\n"
      "ds_read_b128 v[116:119], v22 offset:15360\n"
      "v_mfma_f32_16x16x32_bf16 v[152:155], a[28:31], v[84:87], v[152:155]\n"
      "s_waitcnt lgkmcnt(5)\n"
      "ds_read_b128 v[124:127], v22 offset:17408\n"
      "v_mfma_f32_16x16x32_bf16 v[152:155], a[32:35], v[88:91], v[152:155]\n"
      "ds_read_b128 v[140:143], v22 offset:21504\n"
      "v_mfma_f32_16x16x32_bf16 v[152:155], a[36:39], v[92:95], v[152:155]\n"
      "ds_read_b128 v[128:131], v22 offset:18432\n"
      "v_mfma_f32_16x16x32_bf16 v[152:155], a[40:43], v[96:99], v[152:155]\n"
      "v_mfma_f32_16x16x32_bf16 v[152:155], a[44:47], v[100:103], v[152:155]\n"
      "s_waitcnt lgkmcnt(5)\n"
      "ds_read_b128 v[136:139], v22 offset:20480\n"
      "v_mfma_f32_16x16x32_bf16 v[152:155], a[48:51], v[104:107], v[152:155]\n"
      "s_waitcnt lgkmcnt(5)\n"
      "v_mfma_f32_16x16x32_bf16 v[152:155], a[52:55], v[108:111], v[152:155]\n"
      "v_mfma_f32_16x16x32_bf16 v[152:155], a[56:59], v[112:115], v[152:155]\n"
      "s_waitcnt lgkmcnt(3)\n"
      "ds_read_b128 v[144:147], v22 offset:22528\n"
      "v_mfma_f32_16x16x32_bf16 v[152:155], a[60:63], v[116:119], v[152:155]\n"
      "v_add_u32 v14, 1, v14\n"
      "v_mfma_f32_16x16x32_bf16 v[152:155], a[64:67], v[120:123], v[152:155]\n"
      "s_waitcnt vmcnt(7) lgkmcnt(2)\n"
      "v_cmp_ne_u32 vcc, s21, v20\n"
      "v_mfma_f32_16x16x32_bf16 v[152:155], a[68:71], v[124:127], v[152:155]\n"
      "v_mfma_f32_16x16x32_bf16 v[152:155], a[72:75], v[128:131], v[152:155]\n"
      "v_mfma_f32_16x16x32_bf16 v[152:155], a[76:79], v[132:135], v[152:155]\n"
      "s_waitcnt lgkmcnt(1)\n"
      "v_mfma_f32_16x16x32_bf16 v[152:155], a[80:83], v[136:139], v[152:155]\n"
      "v_mfma_f32_16x16x32_bf16 v[152:155], a[84:87], v[140:143], v[152:155]\n"
      "s_waitcnt lgkmcnt(0)\n"
      "s_cbranch_vccnz .Lsp_d4_8\n"
      ".Lbk_d4_8:\n"
      "v_mfma_f32_16x16x32_bf16 v[152:155], a[88:91], v[144:147], v[152:155]\n"
      "v_mfma_f32_16x16x32_bf16 v[152:155], a[92:95], v[148:151], v[152:155]\n"
      "s_min_u32 s36, s36, s6\n"
      "v_mfma_f32_16x16x32_bf16 v[156:159], a[96:99], v[56:59], 0\n"
      "buffer_load_dwordx4 v0, s[8:11], s51 offen offset:0 sc1 lds\n"
      "v_mfma_f32_16x16x32_bf16 v[156:159], a[100:103], v[60:63], v[156:159]\n"
      "v_mfma_f32_16x16x32_bf16 v[156:159], a[104:107], v[64:67], v[156:159]\n"
      "v_mfma_f32_16x16x32_bf16 v[156:159], a[108:111], v[68:71], v[156:159]\n"
      "v_mfma_f32_16x16x32_bf16 v[156:159], a[112:115], v[72:75], v[156:159]\n"
      "buffer_load_dwordx4 v0, s[8:11], s51 offen offset:2048 sc1 lds\n"
      "v_mfma_f32_16x16x32_bf16 v[156:159], a[116:119], v[76:79], v[156:159]\n"
      "s_waitcnt vmcnt(5)\n"
      "ds_write_b32 v16, v14\n"
      "v_mfma_f32_16x16x32_bf16 v[156:159], a[120:123], v[80:83], v[156:159]\n"
      "v_mfma_f32_16x16x32_bf16 v[156:159], a[124:127], v[84:87], v[156:159]\n"
      "v_mfma_f32_16x16x32_bf16 v[156:159], a[128:131], v[88:91], v[156:159]\n"
      "v_mfma_f32_16x16x32_bf16 v[156:159], a[132:135], v[92:95], v[156:159]\n"
      "buffer_load_dword v20, v2, s[8:11], s36 offen sc1\n"
      "v_mfma_f32_16x16x32_bf16 v[156:159], a[136:139], v[96:99], v[156:159]\n"
      "v_mfma_f32_16x16x32_bf16 v[156:159], a[140:143], v[100:103], v[156:159]\n"
      "v_cvt_pk_bf16_f32 v225, v154, v155\n"
      "v_mfma_f32_16x16x32_bf16 v[156:159], a[144:147], v[104:107], v[156:159]\n"
      "buffer_load_dwordx4 v0, s[8:11], s51 offen offset:3072 sc1 lds\n"
      "v_mfma_f32_16x16x32_bf16 v[156:159], a[148:151], v[108:111], v[156:159]\n"
      "v_mfma_f32_16x16x32_bf16 v[156:159], a[152:155], v[112:115], v[156:159]\n"
      "v_mfma_f32_16x16x32_bf16 v[156:159], a[156:159], v[116:119], v[156:159]\n"
      "ds_read_b32 v15, v17\n"
      "v_mfma_f32_16x16x32_bf16 v[156:159], a[160:163], v[120:123], v[156:159]\n"
      "buffer_load_dwordx4 v0, s[8:11], s51 offen offset:1024 sc1 lds\n"
      "v_mfma_f32_16x16x32_bf16 v[156:159], a[164:167], v[124:127], v[156:159]\n"
      "s_add_u32 m0, s43, 102400\n"
      "v_mfma_f32_16x16x32_bf16 v[156:159], a[168:171], v[128:131], v[156:159]\n"
      "v_mfma_f32_16x16x32_bf16 v[156:159], a[172:175], v[132:135], v[156:159]\n"
      "v_mfma_f32_16x16x32_bf16 v[156:159], a[176:179], v[136:139], v[156:159]\n"
      "buffer_load_dwordx4 v1, s[8:11], s51 offen offset:1024 sc1 lds\n"
      "v_mfma_f32_16x16x32_bf16 v[156:159], a[180:183], v[140:143], v[156:159]\n"
      "v_mfma_f32_16x16x32_bf16 v[156:159], a[184:187], v[144:147], v[156:159]\n"
      "s_waitcnt lgkmcnt(0)\n"
      "v_cmp_gt_i32 vcc, v14, v15\n"
      "v_mfma_f32_16x16x32_bf16 v[156:159], a[188:191], v[148:151], v[156:159]\n"
      "v_mfma_f32_16x16x32_bf16 v[160:163], a[192:195], v[56:59], 0\n"
      "buffer_load_dwordx4 v1, s[8:11], s51 offen offset:0 sc1 lds\n"
      "v_mfma_f32_16x16x32_bf16 v[160:163], a[196:199], v[60:63], v[160:163]\n"
      "s_cbranch_vccnz .Lsp_dy3_9\n"
      ".Lbk_dy3_9:\n"
      "ds_read_b128 v[60:63], v22 offset:25600\n"
      "v_mfma_f32_16x16x32_bf16 v[160:163], a[200:203], v[64:67], v[160:163]\n"
      "v_mfma_f32_16x16x32_bf16 v[160:163], a[204:207], v[68:71], v[160:163]\n"
      "v_cvt_pk_bf16_f32 v227, v158, v159\n"
      "v_mfma_f32_16x16x32_bf16 v[160:163], a[208:211], v[72:75], v[160:163]\n"
      "ds_read_b128 v[64:67], v22 offset:26624\n"
      "v_mfma_f32_16x16x32_bf16 v[160:163], a[212:215], v[76:79], v[160:163]\n"
      "s_mov_b32 s51, s36\n"
      "ds_read_b128 v[72:75], v22 offset:28672\n"
      "v_mfma_f32_16x16x32_bf16 v[160:163], a[216:219], v[80:83], v[160:163]\n"
      "ds_read_b128 v[68:71], v22 offset:27648\n"
      "v_mfma_f32_16x16x32_bf16 v[160:163], a[220:223], v[84:87], v[160:163]\n"
      "v_cvt_pk_bf16_f32 v224, v152, v153\n"
      "v_mfma_f32_16x16x32_bf16 v[160:163], a[224:227], v[88:91], v[160:163]\n"
      "s_waitcnt vmcnt(7) lgkmcnt(3)\n"
      "buffer_store_dword v18, v4, s[8:11], s52 offen sc1\n"
      "v_mfma_f32_16x16x32_bf16 v[160:163], a[228:231], v[92:95], v[160:163]\n"
      "ds_read_b128 v[92:95], v22 offset:33792\n"
      "v_mfma_f32_16x16x32_bf16 v[160:163], a[232:235], v[96:99], v[160:163]\n"
      "ds_read_b128 v[56:59], v22 offset:24576\n"
      "v_mfma_f32_16x16x32_bf16 v[160:163], a[236:239], v[100:103], v[160:163]\n"
      "buffer_load_dword v21, v3, s[8:11], s53 offen sc1\n"
      "v_mfma_f32_16x16x32_bf16 v[160:163], a[240:243], v[104:107], v[160:163]\n"
      "ds_read_b128 v[100:103], v22 offset:35840\n"
      "v_mfma_f32_16x16x32_bf16 v[160:163], a[244:247], v[108:111], v[160:163]\n"
      "ds_read_b128 v[80:83], v22 offset:30720\n"
      "v_mfma_f32_16x16x32_bf16 v[160:163], a[248:251], v[112:115], v[160:163]\n"
      "ds_read_b128 v[112:115], v22 offset:38912\n"
      "v_mfma_f32_16x16x32_bf16 v[160:163], a[252:255], v[116:119], v[160:163]\n"
      "s_add_u32 s37, s37, 106496\n"
      "ds_read_b128 v[88:91], v22 offset:32768\n"
      "v_mfma_f32_16x16x32_bf16 v[160:163], v[24:27], v[120:123], v[160:163]\n"
      "ds_read_b128 v[96:99], v22 offset:34816\n"
      "v_cvt_pk_bf16_f32 v226, v156, v157\n"
      "v_mfma_f32_16x16x32_bf16 v[160:163], v[28:31], v[124:127], v[160:163]\n"
      "ds_read_b128 v[76:79], v22 offset:29696\n"
      "v_mfma_f32_16x16x32_bf16 v[160:163], v[32:35], v[128:131], v[160:163]\n"
      "v_mfma_f32_16x16x32_bf16 v[160:163], v[36:39], v[132:135], v[160:163]\n"
      "buffer_store_dwordx4 v[224:227], v0, s[8:11], s37 offen sc1\n"
      "v_mfma_f32_16x16x32_bf16 v[160:163], v[40:43], v[136:139], v[160:163]\n"
      "ds_read_b128 v[116:119], v22 offset:39936\n"
      "v_mfma_f32_16x16x32_bf16 v[160:163], v[44:47], v[140:143], v[160:163]\n"
      "ds_read_b128 v[84:87], v22 offset:31744\n"
      "v_mfma_f32_16x16x32_bf16 v[160:163], v[48:51], v[144:147], v[160:163]\n"
      "s_waitcnt lgkmcnt(4)\n"
      "ds_read_b128 v[104:107], v22 offset:36864\n"
      "v_mfma_f32_16x16x32_bf16 v[160:163], v[52:55], v[148:151], v[160:163]\n"
      "s_sub_u32 s59, s59, 1\n"
      "s_cbranch_scc1 .Ld_out2\n"
      ".Ld_t3:\n"
      "v_mfma_f32_16x16x32_bf16 v[152:155], a[0:3], v[56:59], 0\n"
      "ds_read_b128 v[120:123], v22 offset:40960\n"
      "v_mfma_f32_16x16x32_bf16 v[152:155], a[4:7], v[60:63], v[152:155]\n"
      "ds_read_b128 v[108:111], v22 offset:37888\n"
      "v_mfma_f32_16x16x32_bf16 v[152:155], a[8:11], v[64:67], v[152:155]\n"
      "ds_read_b128 v[132:135], v22 offset:44032\n"
      "v_mfma_f32_16x16x32_bf16 v[152:155], a[12:15], v[68:71], v[152:155]\n"
      "ds_read_b128 v[136:139], v22 offset:45056\n"
      "v_cvt_pk_bf16_f32 v229, v162, v163\n"
      "v_mfma_f32_16x16x32_bf16 v[152:155], a[16:19], v[72:75], v[152:155]\n"
      "s_waitcnt lgkmcnt(5)\n"
      "v_mfma_f32_16x16x32_bf16 v[152:155], a[20:23], v[76:79], v[152:155]\n"
      "ds_read_b128 v[124:127], v22 offset:41984\n"
      "v_mfma_f32_16x16x32_bf16 v[152:155], a[24:27], v[80:83], v[152:155]\n"
      "s_add_u32 m0, s43, 122880\n"
      "v_cvt_pk_bf16_f32 v228, v160, v161\n"
      "v_mfma_f32_16x16x32_bf16 v[152:155], a[28:31], v[84:87], v[152:155]\n"
      "buffer_store_dwordx2 v[228:229], v13, s[8:11], s37 offen sc1\n"
      "v_mfma_f32_16x16x32_bf16 v[152:155], a[32:35], v[88:91], v[152:155]\n"
      "v_mfma_f32_16x16x32_bf16 v[152:155], a[36:39], v[92:95], v[152:155]\n"
      "ds_read_b128 v[128:131], v22 offset:43008\n"
      "v_mfma_f32_16x16x32_bf16 v[152:155], a[40:43], v[96:99], v[152:155]\n"
      "ds_read_b128 v[144:147], v22 offset:47104\n"
      "v_mfma_f32_16x16x32_bf16 v[152:155], a[44:47], v[100:103], v[152:155]\n"
      "s_waitcnt lgkmcnt(3)\n"
      "ds_read_b128 v[148:151], v22 offset:48128\n"
      "v_mfma_f32_16x16x32_bf16 v[152:155], a[48:51], v[104:107], v[152:155]\n"
      "ds_read_b128 v[140:143], v22 offset:46080\n"
      "v_mfma_f32_16x16x32_bf16 v[152:155], a[52:55], v[108:111], v[152:155]\n"
      "v_mfma_f32_16x16x32_bf16 v[152:155], a[56:59], v[112:115], v[152:155]\n"
      "s_waitcnt vmcnt(8) lgkmcnt(4)\n"
      "v_cmp_ne_u32 vcc, s21, v20\n"
      "v_mfma_f32_16x16x32_bf16 v[152:155], a[60:63], v[116:119], v[152:155]\n"
      "v_mfma_f32_16x16x32_bf16 v[152:155], a[64:67], v[120:123], v[152:155]\n"
      "s_add_u32 s36, s36, 165888\n"
      "s_add_u32 s52, s52, 106496\n"
      "v_mfma_f32_16x16x32_bf16 v[152:155], a[68:71], v[124:127], v[152:155]\n"
      "s_waitcnt lgkmcnt(2)\n"
      "v_mfma_f32_16x16x32_bf16 v[152:155], a[72:75], v[128:131], v[152:155]\n"
      "s_cbranch_vccnz .Lsp_d5_10\n"
      ".Lbk_d5_10:\n"
      "v_mfma_f32_16x16x32_bf16 v[152:155], a[76:79], v[132:135], v[152:155]\n"
      "buffer_load_dwordx4 v0, s[8:11], s51 offen offset:1024 sc1 lds\n"
      "v_mfma_f32_16x16x32_bf16 v[152:155], a[80:83], v[136:139], v[152:155]\n"
      "s_waitcnt lgkmcnt(0)\n"
      "v_mfma_f32_16x16x32_bf16 v[152:155], a[84:87], v[140:143], v[152:155]\n"
      "v_add_u32 v14, 1, v14\n"
      "v_mfma_f32_16x16x32_bf16 v[152:155], a[88:91], v[144:147], v[152:155]\n"
      "v_mfma_f32_16x16x32_bf16 v[152:155], a[92:95], v[148:151], v[152:155]\n"
      "buffer_load_dwordx4 v0, s[8:11], s51 offen offset:0 sc1 lds\n"
      "v_mfma_f32_16x16x32_bf16 v[156:159], a[96:99], v[56:59], 0\n"
      "v_mfma_f32_16x16x32_bf16 v[156:159], a[100:103], v[60:63], v[156:159]\n"
      "v_mfma_f32_16x16x32_bf16 v[156:159], a[104:107], v[64:67], v[156:159]\n"
      "v_mfma_f32_16x16x32_bf16 v[156:159], a[108:111], v[68:71], v[156:159]\n"
      "buffer_load_dwordx4 v0, s[8:11], s51 offen offset:2048 sc1 lds\n"
      "v_mfma_f32_16x16x32_bf16 v[156:159], a[112:115], v[72:75], v[156:159]\n"
      "s_waitcnt vmcnt(7)\n"
      "ds_write_b32 v16, v14\n"
      "v_mfma_f32_16x16x32_bf16 v[156:159], a[116:119], v[76:79], v[156:159]\n"
      "v_mfma_f32_16x16x32_bf16 v[156:159], a[120:123], v[80:83], v[156:159]\n"
      "v_mfma_f32_16x16x32_bf16 v[156:159], a[124:127], v[84:87], v[156:159]\n"
      "buffer_load_dwordx4 v0, s[8:11], s51 offen offset:3072 sc1 lds\n"
      "v_mfma_f32_16x16x32_bf16 v[156:159], a[128:131], v[88:91], v[156:159]\n"
      "v_mfma_f32_16x16x32_bf16 v[156:159], a[132:135], v[92:95], v[156:159]\n"
      "v_cvt_pk_bf16_f32 v224, v152, v153\n"
      "v_mfma_f32_16x16x32_bf16 v[156:159], a[136:139], v[96:99], v[156:159]\n"
      "v_mfma_f32_16x16x32_bf16 v[156:159], a[140:143], v[100:103], v[156:159]\n"
      "s_min_u32 s36, s36, s6\n"
      "s_add_u32 m0, s43, 126976\n"
      "v_mfma_f32_16x16x32_bf16 v[156:159], a[144:147], v[104:107], v[156:159]\n"
      "buffer_load_dwordx4 v1, s[8:11], s51 offen offset:1024 sc1 lds\n"
      "v_mfma_f32_16x16x32_bf16 v[156:159], a[148:151], v[108:111], v[156:159]\n"
      "ds_read_b32 v15, v17\n"
      "v_mfma_f32_16x16x32_bf16 v[156:159], a[152:155], v[112:115], v[156:159]\n"
      "v_mfma_f32_16x16x32_bf16 v[156:159], a[156:159], v[116:119], v[156:159]\n"
      "v_mfma_f32_16x16x32_bf16 v[156:159], a[160:163], v[120:123], v[156:159]\n"
      "buffer_load_dword v20, v2, s[8:11], s36 offen sc1\n"
      "v_mfma_f32_16x16x32_bf16 v[156:159], a[164:167], v[124:127], v[156:159]\n"
      "v_mfma_f32_16x16x32_bf16 v[156:159], a[168:171], v[128:131], v[156:159]\n"
      "s_waitcnt lgkmcnt(0)\n"
      "v_cmp_gt_i32 vcc, v14, v15\n"
      "v_mfma_f32_16x16x32_bf16 v[156:159], a[172:175], v[132:135], v[156:159]\n"
      "buffer_load_dwordx4 v1, s[8:11], s51 offen offset:0 sc1 lds\n"
      "v_mfma_f32_16x16x32_bf16 v[156:159], a[176:179], v[136:139], v[156:159]\n"
      "s_add_u32 s55, s53, 6656\n"
      "v_mfma_f32_16x16x32_bf16 v[156:159], a[180:183], v[140:143], v[156:159]\n"
      "s_add_u32 s54, s53, 13312\n"
      "v_mfma_f32_16x16x32_bf16 v[156:159], a[184:187], v[144:147], v[156:159]\n"
      "s_waitcnt vmcnt(9)\n"
      "s_cbranch_vccnz .Lsp_dy4_11\n"
      ".Lbk_dy4_11:\n"
      "v_cmp_ne_u32 vcc, s21, v21\n"
      "v_mfma_f32_16x16x32_bf16 v[156:159], a[188:191], v[148:151], v[156:159]\n"
      ".Ld_p1:\n"
      "s_cbranch_execz .Ld_pr\n"
      "v_mfma_f32_16x16x32_bf16 v[160:163], a[192:195], v[56:59], 0\n"
      "v_mfma_f32_16x16x32_bf16 v[160:163], a[196:199], v[60:63], v[160:163]\n"
      "s_cbranch_vccnz .Lsp_df_12\n"
      ".Lbk_df_12:\n"
      "buffer_load_dwordx4 v[176:179], v0, s[8:11], s53 offen sc1\n"
      "v_mfma_f32_16x16x32_bf16 v[160:163], a[200:203], v[64:67], v[160:163]\n"
      "ds_read_b128 v[60:63], v23 offset:1024\n"
      "v_mfma_f32_16x16x32_bf16 v[160:163], a[204:207], v[68:71], v[160:163]\n"
      "ds_read_b128 v[64:67], v23 offset:2048\n"
      "v_mfma_f32_16x16x32_bf16 v[160:163], a[208:211], v[72:75], v[160:163]\n"
      "buffer_load_dwordx2 v[180:181], v13, s[8:11], s53 offen sc1\n"
      "v_mfma_f32_16x16x32_bf16 v[160:163], a[212:215], v[76:79], v[160:163]\n"
      "ds_read_b128 v[72:75], v23 offset:4096\n"
      "v_mfma_f32_16x16x32_bf16 v[160:163], a[216:219], v[80:83], v[160:163]\n"
      "ds_read_b128 v[68:71], v23 offset:3072\n"
      "s_add_u32 s58, s41, 4294967264\n"
      "v_mfma_f32_16x16x32_bf16 v[160:163], a[220:223], v[84:87], v[160:163]\n"
      "ds_read_b128 v[80:83], v23 offset:6144\n"
      "v_mfma_f32_16x16x32_bf16 v[160:163], a[224:227], v[88:91], v[160:163]\n"
      "v_cvt_pk_bf16_f32 v225, v154, v155\n"
      "v_mfma_f32_16x16x32_bf16 v[160:163], a[228:231], v[92:95], v[160:163]\n"
      "buffer_load_dwordx4 v[184:187], v0, s[8:11], s55 offen sc1\n"
      "v_mfma_f32_16x16x32_bf16 v[160:163], a[232:235], v[96:99], v[160:163]\n"
      "ds_read_b128 v[84:87], v23 offset:7168\n"
      "v_cvt_pk_bf16_f32 v226, v156, v157\n"
      "v_mfma_f32_16x16x32_bf16 v[160:163], a[236:239], v[100:103], v[160:163]\n"
      "ds_read_b128 v[76:79], v23 offset:5120\n"
      "v_mfma_f32_16x16x32_bf16 v[160:163], a[240:243], v[104:107], v[160:163]\n"
      "s_add_u32 s37, s37, 106496\n"
      "ds_read_b128 v[56:59], v23 offset:0\n"
      "v_mfma_f32_16x16x32_bf16 v[160:163], a[244:247], v[108:111], v[160:163]\n"
      "s_waitcnt vmcnt(10) lgkmcnt(4)\n"
      "buffer_store_dword v18, v4, s[8:11], s52 offen sc1\n"
      "v_mfma_f32_16x16x32_bf16 v[160:163], a[248:251], v[112:115], v[160:163]\n"
      "ds_read_b128 v[88:91], v23 offset:8192\n"
      "v_mfma_f32_16x16x32_bf16 v[160:163], a[252:255], v[116:119], v[160:163]\n"
      "ds_read_b128 v[96:99], v23 offset:10240\n"
      "v_mfma_f32_16x16x32_bf16 v[160:163], v[24:27], v[120:123], v[160:163]\n"
      "buffer_load_dwordx4 v[192:195], v0, s[8:11], s54 offen sc1\n"
      "v_mfma_f32_16x16x32_bf16 v[160:163], v[28:31], v[124:127], v[160:163]\n"
      "v_mfma_f32_16x16x32_bf16 v[160:163], v[32:35], v[128:131], v[160:163]\n"
      "buffer_load_dwordx2 v[188:189], v13, s[8:11], s55 offen sc1\n"
      "v_mfma_f32_16x16x32_bf16 v[160:163], v[36:39], v[132:135], v[160:163]\n"
      "ds_read_b128 v[100:103], v23 offset:11264\n"
      "v_mfma_f32_16x16x32_bf16 v[160:163], v[40:43], v[136:139], v[160:163]\n"
      "s_add_u32 s55, s53, 19968\n"
      "ds_read_b128 v[132:135], v23 offset:19456\n"
      "v_mfma_f32_16x16x32_bf16 v[160:163], v[44:47], v[140:143], v[160:163]\n"
      "ds_read_b128 v[104:107], v23 offset:12288\n"
      "v_mfma_f32_16x16x32_bf16 v[160:163], v[48:51], v[144:147], v[160:163]\n"
      "s_waitcnt lgkmcnt(3)\n"
      "buffer_load_dwordx2 v[196:197], v13, s[8:11], s54 offen sc1\n"
      "v_mfma_f32_16x16x32_bf16 v[160:163], v[52:55], v[148:151], v[160:163]\n"
      "s_sub_u32 s59, s59, 1\n"
      "s_cbranch_scc1 .Ld_out3\n"
      ".Ld_t4:\n"
      "v_mfma_f32_16x16x32_bf16 v[152:155], a[0:3], v[56:59], 0\n"
      "ds_read_b128 v[92:95], v23 offset:9216\n"
      "v_mfma_f32_16x16x32_bf16 v[152:155], a[4:7], v[60:63], v[152:155]\n"
      "ds_read_b128 v[108:111], v23 offset:13312\n"
      "s_add_u32 m0, s43, 0\n"
      "v_mfma_f32_16x16x32_bf16 v[152:155], a[8:11], v[64:67], v[152:155]\n"
      "ds_read_b128 v[128:131], v23 offset:18432\n"
      "v_cvt_pk_bf16_f32 v228, v160, v161\n"
      "v_mfma_f32_16x16x32_bf16 v[152:155], a[12:15], v[68:71], v[152:155]\n"
      "buffer_load_dwordx4 v[200:203], v0, s[8:11], s55 offen sc1\n"
      "v_mfma_f32_16x16x32_bf16 v[152:155], a[16:19], v[72:75], v[152:155]\n"
      "ds_read_b128 v[116:119], v23 offset:15360\n"
      "v_cvt_pk_bf16_f32 v227, v158, v159\n"
      "v_mfma_f32_16x16x32_bf16 v[152:155], a[20:23], v[76:79], v[152:155]\n"
      "ds_read_b128 v[112:115], v23 offset:14336\n"
      "v_mfma_f32_16x16x32_bf16 v[152:155], a[24:27], v[80:83], v[152:155]\n"
      "s_mov_b32 s51, s36\n"
      "ds_read_b128 v[120:123], v23 offset:16384\n"
      "v_mfma_f32_16x16x32_bf16 v[152:155], a[28:31], v[84:87], v[152:155]\n"
      "buffer_store_dwordx4 v[224:227], v0, s[8:11], s37 offen sc1\n"
      "v_mfma_f32_16x16x32_bf16 v[152:155], a[32:35], v[88:91], v[152:155]\n"
      "s_waitcnt lgkmcnt(4)\n"
      "ds_read_b128 v[140:143], v23 offset:21504\n"
      "v_mfma_f32_16x16x32_bf16 v[152:155], a[36:39], v[92:95], v[152:155]\n"
      "ds_read_b128 v[124:127], v23 offset:17408\n"
      "v_mfma_f32_16x16x32_bf16 v[152:155], a[40:43], v[96:99], v[152:155]\n"
      "ds_read_b128 v[136:139], v23 offset:20480\n"
      "v_mfma_f32_16x16x32_bf16 v[152:155], a[44:47], v[100:103], v[152:155]\n"
      "v_cvt_pk_bf16_f32 v229, v162, v163\n"
      "v_mfma_f32_16x16x32_bf16 v[152:155], a[48:51], v[104:107], v[152:155]\n"
      "buffer_store_dwordx2 v[228:229], v13, s[8:11], s37 offen sc1\n"
      "v_mfma_f32_16x16x32_bf16 v[152:155], a[52:55], v[108:111], v[152:155]\n"
      "s_waitcnt lgkmcnt(3)\n"
      "ds_read_b128 v[148:151], v23 offset:23552\n"
      "v_add_u32_e64 v14, 1, v14\n"
      "v_mfma_f32_16x16x32_bf16 v[152:155], a[56:59], v[112:115], v[152:155]\n"
      "s_add_u32 s36, s36, 165888\n"
      "s_add_u32 s37, s37, 106496\n"
      "ds_read_b128 v[144:147], v23 offset:22528\n"
      "v_mfma_f32_16x16x32_bf16 v[152:155], a[60:63], v[116:119], v[152:155]\n"
      "v_mfma_f32_16x16x32_bf16 v[152:155], a[64:67], v[120:123], v[152:155]\n"
      "s_waitcnt lgkmcnt(3)\n"
      "s_min_u32 s36, s36, s6\n"
      "v_mfma_f32_16x16x32_bf16 v[152:155], a[68:71], v[124:127], v[152:155]\n"
      "buffer_load_dwordx2 v[204:205], v13, s[8:11], s55 offen sc1\n"
      "v_mfma_f32_16x16x32_bf16 v[152:155], a[72:75], v[128:131], v[152:155]\n"
      "s_cmp_lt_u32 s41, 32\n"
      "s_cselect_b32 s56, s39, 0x40000000\n"
      "v_mfma_f32_16x16x32_bf16 v[152:155], a[76:79], v[132:135], v[152:155]\n"
      "s_waitcnt lgkmcnt(2)\n"
      "s_add_u32 s54, s53, 26624\n"
      "v_mfma_f32_16x16x32_bf16 v[152:155], a[80:83], v[136:139], v[152:155]\n"
      "s_waitcnt vmcnt(12) lgkmcnt(1)\n"
      "s_add_u32 s53, s53, 599040\n"
      "v_cmp_ne_u32 vcc, s21, v20\n"
      "v_mfma_f32_16x16x32_bf16 v[152:155], a[84:87], v[140:143], v[152:155]\n"
      "s_waitcnt lgkmcnt(0)\n"
      "buffer_load_dwordx2 v[212:213], v13, s[8:11], s54 offen sc1\n"
      "v_mfma_f32_16x16x32_bf16 v[152:155], a[88:91], v[144:147], v[152:155]\n"
      "v_mfma_f32_16x16x32_bf16 v[152:155], a[92:95], v[148:151], v[152:155]\n"
      "s_add_u32 s39, s39, 147456\n"
      "v_mfma_f32_16x16x32_bf16 v[156:159], a[96:99], v[56:59], 0\n"
      "s_waitcnt vmcnt(12)\n"
      "ds_write_b32 v16, v14\n"
      "v_mfma_f32_16x16x32_bf16 v[156:159], a[100:103], v[60:63], v[156:159]\n"
      "buffer_load_dwordx4 v[208:211], v0, s[8:11], s54 offen sc1\n"
      "v_mfma_f32_16x16x32_bf16 v[156:159], a[104:107], v[64:67], v[156:159]\n"
      "s_cmp_lt_u32 s58, 32\n"
      "s_cselect_b32 s57, s40, 0x40000000\n"
      "v_mfma_f32_16x16x32_bf16 v[156:159], a[108:111], v[68:71], v[156:159]\n"
      "s_cbranch_vccnz .Lsp_d0_13\n"
      ".Lbk_d0_13:\n"
      "v_cvt_pk_bf16_f32 v224, v152, v153\n"
      "v_mfma_f32_16x16x32_bf16 v[156:159], a[112:115], v[72:75], v[156:159]\n"
      "v_cvt_pk_bf16_f32 v225, v154, v155\n"
      "v_mfma_f32_16x16x32_bf16 v[156:159], a[116:119], v[76:79], v[156:159]\n"
      "buffer_load_dwordx4 v0, s[8:11], s51 offen offset:0 sc1 lds\n"
      "v_mfma_f32_16x16x32_bf16 v[156:159], a[120:123], v[80:83], v[156:159]\n"
      "s_waitcnt vmcnt(13)\n"
      "v_lshlrev_b32 v234, 16, v178\n"
      "v_lshlrev_b32 v233, 16, v177\n"
      "v_mfma_f32_16x16x32_bf16 v[156:159], a[124:127], v[84:87], v[156:159]\n"
      "v_lshlrev_b32 v232, 16, v176\n"
      "v_add_f32 v166, v166, v233\n"
      "v_mfma_f32_16x16x32_bf16 v[156:159], a[128:131], v[88:91], v[156:159]\n"
      "s_add_u32 s52, s52, 106496\n"
      "v_add_f32 v164, v164, v232\n"
      "v_mfma_f32_16x16x32_bf16 v[156:159], a[132:135], v[92:95], v[156:159]\n"
      "buffer_load_dwordx4 v0, s[8:11], s51 offen offset:1024 sc1 lds\n"
      "v_mfma_f32_16x16x32_bf16 v[156:159], a[136:139], v[96:99], v[156:159]\n"
      "s_waitcnt vmcnt(13) lgkmcnt(0)\n"
      "v_lshlrev_b32 v233, 16, v181\n"
      "v_lshlrev_b32 v235, 16, v179\n"
      "v_mfma_f32_16x16x32_bf16 v[156:159], a[140:143], v[100:103], v[156:159]\n"
      "v_and_b32 v176, 0xffff0000, v176\n"
      "v_lshlrev_b32 v232, 16, v180\n"
      "v_mfma_f32_16x16x32_bf16 v[156:159], a[144:147], v[104:107], v[156:159]\n"
      "v_add_f32 v172, v172, v232\n"
      "v_and_b32 v181, 0xffff0000, v181\n"
      "v_mfma_f32_16x16x32_bf16 v[156:159], a[148:151], v[108:111], v[156:159]\n"
      "v_add_f32 v168, v168, v234\n"
      "v_add_f32 v174, v174, v233\n"
      "v_mfma_f32_16x16x32_bf16 v[156:159], a[152:155], v[112:115], v[156:159]\n"
      "buffer_load_dword v20, v2, s[8:11], s36 offen sc1\n"
      "v_mfma_f32_16x16x32_bf16 v[156:159], a[156:159], v[116:119], v[156:159]\n"
      "ds_read_b32 v15, v17\n"
      "v_add_f32 v165, v165, v176\n"
      "v_mfma_f32_16x16x32_bf16 v[156:159], a[160:163], v[120:123], v[156:159]\n"
      "buffer_load_dwordx4 v0, s[8:11], s51 offen offset:2048 sc1 lds\n"
      "v_mfma_f32_16x16x32_bf16 v[156:159], a[164:167], v[124:127], v[156:159]\n"
      "s_waitcnt vmcnt(14)\n"
      "v_lshlrev_b32 v232, 16, v184\n"
      "v_and_b32 v177, 0xffff0000, v177\n"
      "v_mfma_f32_16x16x32_bf16 v[156:159], a[168:171], v[128:131], v[156:159]\n"
      "s_add_u32 s40, s40, 4294856704\n"
      "v_add_f32 v167, v167, v177\n"
      "v_and_b32 v180, 0xffff0000, v180\n"
      "v_mfma_f32_16x16x32_bf16 v[156:159], a[172:175], v[132:135], v[156:159]\n"
      "v_add_f32 v164, v164, v232\n"
      "v_lshlrev_b32 v233, 16, v185\n"
      "v_mfma_f32_16x16x32_bf16 v[156:159], a[176:179], v[136:139], v[156:159]\n"
      "buffer_load_dwordx4 v0, s[8:11], s51 offen offset:3072 sc1 lds\n"
      "v_mfma_f32_16x16x32_bf16 v[156:159], a[180:183], v[140:143], v[156:159]\n"
      "s_waitcnt lgkmcnt(0)\n"
      "v_add_f32 v166, v166, v233\n"
      "v_cmp_gt_i32 vcc, v14, v15\n"
      "v_mfma_f32_16x16x32_bf16 v[156:159], a[184:187], v[144:147], v[156:159]\n"
      "s_waitcnt vmcnt(12)\n"
      "v_lshlrev_b32 v232, 16, v188\n"
      "v_add_f32 v172, v172, v232\n"
      "v_mfma_f32_16x16x32_bf16 v[156:159], a[188:191], v[148:151], v[156:159]\n"
      "v_and_b32 v185, 0xffff0000, v185\n"
      "v_lshlrev_b32 v232, 16, v192\n"
      "v_mfma_f32_16x16x32_bf16 v[160:163], a[192:195], v[56:59], 0\n"
      "s_add_u32 m0, s43, 4096\n"
      "v_lshlrev_b32 v234, 16, v186\n"
      "v_add_f32 v164, v164, v232\n"
      "v_mfma_f32_16x16x32_bf16 v[160:163], a[196:199], v[60:63], v[160:163]\n"
      "buffer_load_dwordx4 v1, s[8:11], s51 offen offset:1024 sc1 lds\n"
      "v_mfma_f32_16x16x32_bf16 v[160:163], a[200:203], v[64:67], v[160:163]\n"
      "s_cbranch_vccnz .Lsp_dy5_14\n"
      ".Lbk_dy5_14:\n"
      "v_cvt_pk_bf16_f32 v227, v158, v159\n"
      "v_mfma_f32_16x16x32_bf16 v[160:163], a[204:207], v[68:71], v[160:163]\n"
      "ds_read_b128 v[56:59], v23 offset:24576\n"
      "v_lshlrev_b32 v233, 16, v189\n"
      "v_mfma_f32_16x16x32_bf16 v[160:163], a[208:211], v[72:75], v[160:163]\n"
      "s_waitcnt vmcnt(12)\n"
      "ds_read_b128 v[64:67], v23 offset:26624\n"
      "v_lshlrev_b32 v232, 16, v196\n"
      "v_mfma_f32_16x16x32_bf16 v[160:163], a[212:215], v[76:79], v[160:163]\n"
      "buffer_load_dwordx4 v1, s[8:11], s51 offen offset:0 sc1 lds\n"
      "v_mfma_f32_16x16x32_bf16 v[160:163], a[216:219], v[80:83], v[160:163]\n"
      "ds_read_b128 v[76:79], v23 offset:29696\n"
      "v_add_f32 v174, v174, v233\n"
      "v_mfma_f32_16x16x32_bf16 v[160:163], a[220:223], v[84:87], v[160:163]\n"
      "v_and_b32 v184, 0xffff0000, v184\n"
      "v_add_f32 v172, v172, v232\n"
      "v_mfma_f32_16x16x32_bf16 v[160:163], a[224:227], v[88:91], v[160:163]\n"
      "v_add_f32 v170, v170, v235\n"
      "v_cvt_pk_bf16_f32 v226, v156, v157\n"
      "v_mfma_f32_16x16x32_bf16 v[160:163], a[228:231], v[92:95], v[160:163]\n"
      "s_add_u32 s41, s41, 6\n"
      "ds_read_b128 v[88:91], v23 offset:32768\n"
      "v_add_f32 v165, v165, v184\n"
      "v_mfma_f32_16x16x32_bf16 v[160:163], a[232:235], v[96:99], v[160:163]\n"
      "ds_read_b128 v[68:71], v23 offset:27648\n"
      "v_mfma_f32_16x16x32_bf16 v[160:163], a[236:239], v[100:103], v[160:163]\n"
      "ds_read_b128 v[96:99], v23 offset:34816\n"
      "v_add_f32 v168, v168, v234\n"
      "v_mfma_f32_16x16x32_bf16 v[160:163], a[240:243], v[104:107], v[160:163]\n"
      "ds_read_b128 v[80:83], v23 offset:30720\n"
      "v_lshlrev_b32 v233, 16, v193\n"
      "v_mfma_f32_16x16x32_bf16 v[160:163], a[244:247], v[108:111], v[160:163]\n"
      "ds_read_b128 v[72:75], v23 offset:28672\n"
      "v_and_b32 v178, 0xffff0000, v178\n"
      "v_mfma_f32_16x16x32_bf16 v[160:163], a[248:251], v[112:115], v[160:163]\n"
      "s_waitcnt vmcnt(12) lgkmcnt(5)\n"
      "s_mov_b32 s51, s36\n"
      "ds_read_b128 v[112:115], v23 offset:38912\n"
      "v_lshlrev_b32 v232, 16, v200\n"
      "v_mfma_f32_16x16x32_bf16 v[160:163], a[252:255], v[116:119], v[160:163]\n"
      "v_add_f32 v164, v164, v232\n"
      "v_and_b32 v186, 0xffff0000, v186\n"
      "v_mfma_f32_16x16x32_bf16 v[160:163], v[24:27], v[120:123], v[160:163]\n"
      "s_waitcnt vmcnt(10) lgkmcnt(5)\n"
      "buffer_store_dword v18, v4, s[8:11], s52 offen sc1\n"
      "v_mfma_f32_16x16x32_bf16 v[160:163], v[28:31], v[124:127], v[160:163]\n"
      "ds_read_b128 v[60:63], v23 offset:25600\n"
      "v_add_f32 v169, v169, v178\n"
      "v_mfma_f32_16x16x32_bf16 v[160:163], v[32:35], v[128:131], v[160:163]\n"
      "buffer_store_dwordx4 v[224:227], v0, s[8:11], s37 offen sc1\n"
      "v_mfma_f32_16x16x32_bf16 v[160:163], v[36:39], v[132:135], v[160:163]\n"
      "ds_read_b128 v[104:107], v23 offset:36864\n"
      "v_and_b32 v200, 0xffff0000, v200\n"
      "v_mfma_f32_16x16x32_bf16 v[160:163], v[40:43], v[136:139], v[160:163]\n"
      "ds_read_b128 v[84:87], v23 offset:31744\n"
      "v_add_f32 v173, v173, v180\n"
      "v_mfma_f32_16x16x32_bf16 v[160:163], v[44:47], v[140:143], v[160:163]\n"
      "ds_read_b128 v[140:143], v23 offset:46080\n"
      "v_add_f32 v166, v166, v233\n"
      "v_mfma_f32_16x16x32_bf16 v[160:163], v[48:51], v[144:147], v[160:163]\n"
      "ds_read_b128 v[100:103], v23 offset:35840\n"
      "v_lshlrev_b32_e64 v234, 16, v194\n"
      "v_mfma_f32_16x16x32_bf16 v[160:163], v[52:55], v[148:151], v[160:163]\n"
      "s_sub_u32 s59, s59, 1\n"
      "s_cbranch_scc1 .Ld_out4\n"
      ".Ld_t5:\n"
      "v_mfma_f32_16x16x32_bf16 v[152:155], a[0:3], v[56:59], 0\n"
      "s_waitcnt lgkmcnt(4)\n"
      "ds_read_b128 v[92:95], v23 offset:33792\n"
      "ds_read_b128 v[116:119], v23 offset:39936\n"
      "v_mfma_f32_16x16x32_bf16 v[152:155], a[4:7], v[60:63], v[152:155]\n"
      "ds_read_b128 v[108:111], v23 offset:37888\n"
      "v_and_b32 v192, 0xffff0000, v192\n"
      "v_mfma_f32_16x16x32_bf16 v[152:155], a[8:11], v[64:67], v[152:155]\n"
      "ds_read_b128 v[128:131], v23 offset:43008\n"
      "v_cvt_pk_bf16_f32 v229, v162, v163\n"
      "v_mfma_f32_16x16x32_bf16 v[152:155], a[12:15], v[68:71], v[152:155]\n"
      "ds_read_b128 v[136:139], v23 offset:45056\n"
      "v_add_f32 v175, v175, v181\n"
      "v_mfma_f32_16x16x32_bf16 v[152:155], a[16:19], v[72:75], v[152:155]\n"
      "v_cvt_pk_bf16_f32 v228, v160, v161\n"
      "v_and_b32 v189, 0xffff0000, v189\n"
      "v_mfma_f32_16x16x32_bf16 v[152:155], a[20:23], v[76:79], v[152:155]\n"
      "s_waitcnt vmcnt(11) lgkmcnt(5)\n"
      "ds_read_b128 v[144:147], v23 offset:47104\n"
      "v_lshlrev_b32 v232, 16, v204\n"
      "v_mfma_f32_16x16x32_bf16 v[152:155], a[24:27], v[80:83], v[152:155]\n"
      "buffer_store_dwordx2 v[228:229], v13, s[8:11], s37 offen sc1\n"
      "v_mfma_f32_16x16x32_bf16 v[152:155], a[28:31], v[84:87], v[152:155]\n"
      "v_lshlrev_b32 v235, 16, v187\n"
      "v_add_f32 v172, v172, v232\n"
      "v_mfma_f32_16x16x32_bf16 v[152:155], a[32:35], v[88:91], v[152:155]\n"
      "s_waitcnt vmcnt(10) lgkmcnt(3)\n"
      "ds_read_b128 v[120:123], v23 offset:40960\n"
      "v_lshlrev_b32 v232, 16, v208\n"
      "v_mfma_f32_16x16x32_bf16 v[152:155], a[36:39], v[92:95], v[152:155]\n"
      "ds_read_b128 v[124:127], v23 offset:41984\n"
      "v_add_f32 v170, v170, v235\n"
      "v_mfma_f32_16x16x32_bf16 v[152:155], a[40:43], v[96:99], v[152:155]\n"
      "s_add_u32 m0, s43, 24576\n"
      "v_and_b32 v194, 0xffff0000, v194\n"
      "v_and_b32 v208, 0xffff0000, v208\n"
      "v_mfma_f32_16x16x32_bf16 v[152:155], a[44:47], v[100:103], v[152:155]\n"
      "ds_read_b128 v[132:135], v23 offset:44032\n"
      "v_add_f32 v167, v167, v185\n"
      "v_mfma_f32_16x16x32_bf16 v[152:155], a[48:51], v[104:107], v[152:155]\n"
      "v_lshlrev_b32 v233, 16, v197\n"
      "v_add_u32 v14, 1, v14\n"
      "v_mfma_f32_16x16x32_bf16 v[152:155], a[52:55], v[108:111], v[152:155]\n"
      "s_add_u32 s36, s36, 165888\n"
      "v_lshlrev_b32 v235, 16, v195\n"
      "v_add_f32 v168, v168, v234\n"
      "v_mfma_f32_16x16x32_bf16 v[152:155], a[56:59], v[112:115], v[152:155]\n"
      "buffer_load_dwordx4 v[216:219], v6, s[8:11], s38 offen sc1\n"
      "v_mfma_f32_16x16x32_bf16 v[152:155], a[60:63], v[116:119], v[152:155]\n"
      "s_waitcnt lgkmcnt(2)\n"
      "ds_read_b128 v[148:151], v23 offset:48128\n"
      "v_add_f32 v164, v164, v232\n"
      "v_mfma_f32_16x16x32_bf16 v[152:155], a[64:67], v[120:123], v[152:155]\n"
      "s_waitcnt lgkmcnt(2)\n"
      "v_add_f32 v165, v165, v192\n"
      "v_lshlrev_b32 v234, 16, v202\n"
      "v_mfma_f32_16x16x32_bf16 v[152:155], a[68:71], v[124:127], v[152:155]\n"
      "v_add_f32 v174, v174, v233\n"
      "v_lshlrev_b32 v232, 16, v212\n"
      "v_mfma_f32_16x16x32_bf16 v[152:155], a[72:75], v[128:131], v[152:155]\n"
      "s_waitcnt lgkmcnt(1)\n"
      "v_and_b32 v179, 0xffff0000, v179\n"
      "v_add_f32 v169, v169, v186\n"
      "v_mfma_f32_16x16x32_bf16 v[152:155], a[76:79], v[132:135], v[152:155]\n"
      "s_waitcnt vmcnt(8)\n"
      "v_lshlrev_b32 v233, 16, v201\n"
      "v_cmp_ne_u32 vcc, s21, v20\n"
      "v_mfma_f32_16x16x32_bf16 v[152:155], a[80:83], v[136:139], v[152:155]\n"
      "buffer_load_dwordx2 v[220:221], v7, s[8:11], s38 offen sc1\n"
      "v_mfma_f32_16x16x32_bf16 v[152:155], a[84:87], v[140:143], v[152:155]\n"
      "v_add_f32 v166, v166, v233\n"
      "v_and_b32 v193, 0xffff0000, v193\n"
      "v_mfma_f32_16x16x32_bf16 v[152:155], a[88:91], v[144:147], v[152:155]\n"
      "s_waitcnt lgkmcnt(0)\n"
      "v_and_b32 v197, 0xffff0000, v197\n"
      "v_lshlrev_b32 v233, 16, v205\n"
      "v_mfma_f32_16x16x32_bf16 v[152:155], a[92:95], v[148:151], v[152:155]\n"
      "v_add_f32 v170, v170, v235\n"
      "v_add_f32 v174, v174, v233\n"
      "v_mfma_f32_16x16x32_bf16 v[156:159], a[96:99], v[56:59], 0\n"
      "s_cbranch_vccnz .Lsp_d1_15\n"
      ".Lbk_d1_15:\n"
      "v_lshlrev_b32 v235, 16, v203\n"
      "v_mfma_f32_16x16x32_bf16 v[156:159], a[100:103], v[60:63], v[156:159]\n"
      "buffer_load_dwordx4 v0, s[8:11], s51 offen offset:0 sc1 lds\n"
      "v_mfma_f32_16x16x32_bf16 v[156:159], a[104:107], v[64:67], v[156:159]\n"
      "v_and_b32 v187, 0xffff0000, v187\n"
      "v_lshlrev_b32 v233, 16, v209\n"
      "v_mfma_f32_16x16x32_bf16 v[156:159], a[108:111], v[68:71], v[156:159]\n"
      "v_add_f32 v170, v170, v235\n"
      "v_add_f32 v166, v166, v233\n"
      "v_mfma_f32_16x16x32_bf16 v[156:159], a[112:115], v[72:75], v[156:159]\n"
      "v_and_b32 v201, 0xffff0000, v201\n"
      "v_add_f32 v165, v165, v200\n"
      "v_mfma_f32_16x16x32_bf16 v[156:159], a[116:119], v[76:79], v[156:159]\n"
      "v_and_b32 v188, 0xffff0000, v188\n"
      "v_add_f32 v165, v165, v208\n"
      "v_mfma_f32_16x16x32_bf16 v[156:159], a[120:123], v[80:83], v[156:159]\n"
      "buffer_load_dwordx4 v0, s[8:11], s51 offen offset:1024 sc1 lds\n"
      "v_mfma_f32_16x16x32_bf16 v[156:159], a[124:127], v[84:87], v[156:159]\n"
      "v_add_f32 v168, v168, v234\n"
      "v_mul_f32 v237, v164, v164\n"
      "v_mfma_f32_16x16x32_bf16 v[156:159], a[128:131], v[88:91], v[156:159]\n"
      "v_add_f32 v167, v167, v193\n"
      "v_lshlrev_b32 v233, 16, v213\n"
      "v_mfma_f32_16x16x32_bf16 v[156:159], a[132:135], v[92:95], v[156:159]\n"
      "s_waitcnt vmcnt(7)\n"
      "ds_write_b32 v16, v14\n"
      "v_mfma_f32_16x16x32_bf16 v[156:159], a[136:139], v[96:99], v[156:159]\n"
      "buffer_load_dwordx4 v0, s[8:11], s51 offen offset:2048 sc1 lds\n"
      "v_mfma_f32_16x16x32_bf16 v[156:159], a[140:143], v[100:103], v[156:159]\n"
      "v_add_f32 v171, v171, v179\n"
      "v_add_f32 v167, v167, v201\n"
      "v_mfma_f32_16x16x32_bf16 v[156:159], a[144:147], v[104:107], v[156:159]\n"
      "v_lshlrev_b32 v234, 16, v210\n"
      "v_and_b32 v213, 0xffff0000, v213\n"
      "v_mfma_f32_16x16x32_bf16 v[156:159], a[148:151], v[108:111], v[156:159]\n"
      "s_min_u32 s36, s36, s6\n"
      "v_add_f32 v173, v173, v188\n"
      "v_and_b32 v209, 0xffff0000, v209\n"
      "v_mfma_f32_16x16x32_bf16 v[156:159], a[152:155], v[112:115], v[156:159]\n"
      "buffer_load_dword v20, v2, s[8:11], s36 offen sc1\n"
      "v_mfma_f32_16x16x32_bf16 v[156:159], a[156:159], v[116:119], v[156:159]\n"
      "v_add_f32 v236, v164, v165\n"
      "v_add_f32 v175, v175, v189\n"
      "v_mfma_f32_16x16x32_bf16 v[156:159], a[160:163], v[120:123], v[156:159]\n"
      "buffer_load_dwordx4 v0, s[8:11], s51 offen offset:3072 sc1 lds\n"
      "v_mfma_f32_16x16x32_bf16 v[156:159], a[164:167], v[124:127], v[156:159]\n"
      "v_add_f32 v167, v167, v209\n"
      "v_and_b32 v203, 0xffff0000, v203\n"
      "v_mfma_f32_16x16x32_bf16 v[156:159], a[168:171], v[128:131], v[156:159]\n"
      "ds_read_b32 v15, v17\n"
      "v_fmac_f32 v237, v165, v165\n"
      "v_mfma_f32_16x16x32_bf16 v[156:159], a[172:175], v[132:135], v[156:159]\n"
      "s_add_u32 m0, s43, 28672\n"
      "v_add_f32 v175, v175, v197\n"
      "v_and_b32 v204, 0xffff0000, v204\n"
      "v_mfma_f32_16x16x32_bf16 v[156:159], a[176:179], v[136:139], v[156:159]\n"
      "v_and_b32 v195, 0xffff0000, v195\n"
      "v_add_f32 v172, v172, v232\n"
      "v_mfma_f32_16x16x32_bf16 v[156:159], a[180:183], v[140:143], v[156:159]\n"
      "buffer_load_dwordx4 v1, s[8:11], s51 offen offset:0 sc1 lds\n"
      "v_mfma_f32_16x16x32_bf16 v[156:159], a[184:187], v[144:147], v[156:159]\n"
      "v_fmac_f32 v237, v166, v166\n"
      "v_cvt_pk_bf16_f32 v225, v154, v155\n"
      "v_mfma_f32_16x16x32_bf16 v[156:159], a[188:191], v[148:151], v[156:159]\n"
      "s_waitcnt lgkmcnt(0)\n"
      "v_add_f32 v169, v169, v194\n"
      "v_cmp_gt_i32 vcc, v14, v15\n"
      "v_mfma_f32_16x16x32_bf16 v[160:163], a[192:195], v[56:59], 0\n"
      "v_and_b32 v196, 0xffff0000, v196\n"
      "v_and_b32 v212, 0xffff0000, v212\n"
      "v_mfma_f32_16x16x32_bf16 v[160:163], a[196:199], v[60:63], v[160:163]\n"
      "buffer_load_dwordx4 v1, s[8:11], s51 offen offset:1024 sc1 lds\n"
      "v_mfma_f32_16x16x32_bf16 v[160:163], a[200:203], v[64:67], v[160:163]\n"
      "s_cbranch_vccnz .Lsp_dy0_16\n"
      ".Lbk_dy0_16:\n"
      "v_mfma_f32_16x16x32_bf16 v[160:163], a[204:207], v[68:71], v[160:163]\n"
      "ds_read_b128 v[56:59], v0 offset:0\n"
      "v_cvt_pk_bf16_f32 v177, v166, v167\n"
      "v_mfma_f32_16x16x32_bf16 v[160:163], a[208:211], v[72:75], v[160:163]\n"
      "v_add_f32 v171, v171, v187\n"
      "v_and_b32 v205, 0xffff0000, v205\n"
      "v_mfma_f32_16x16x32_bf16 v[160:163], a[212:215], v[76:79], v[160:163]\n"
      "ds_read_b128 v[60:63], v0 offset:1024\n"
      "v_cvt_pk_bf16_f32 v224, v152, v153\n"
      "v_mfma_f32_16x16x32_bf16 v[160:163], a[216:219], v[80:83], v[160:163]\n"
      "s_add_u32 s52, s52, 106496\n"
      "v_and_b32 v202, 0xffff0000, v202\n"
      "v_add_f32 v169, v169, v202\n"
      "v_mfma_f32_16x16x32_bf16 v[160:163], a[220:223], v[84:87], v[160:163]\n"
      "ds_read_b128 v[84:87], v0 offset:7168\n"
      "v_add_f32 v236, v236, v166\n"
      "v_mfma_f32_16x16x32_bf16 v[160:163], a[224:227], v[88:91], v[160:163]\n"
      "ds_read_b128 v[72:75], v0 offset:4096\n"
      "v_and_b32 v210, 0xffff0000, v210\n"
      "v_mfma_f32_16x16x32_bf16 v[160:163], a[228:231], v[92:95], v[160:163]\n"
      "v_add_f32 v173, v173, v196\n"
      "v_lshlrev_b32 v235, 16, v211\n"
      "v_mfma_f32_16x16x32_bf16 v[160:163], a[232:235], v[96:99], v[160:163]\n"
      "v_add_f32 v170, v170, v235\n"
      "v_add_f32 v171, v171, v195\n"
      "v_mfma_f32_16x16x32_bf16 v[160:163], a[236:239], v[100:103], v[160:163]\n"
      "s_add_u32 s37, s37, 106496\n"
      "ds_read_b128 v[88:91], v0 offset:8192\n"
      "v_cvt_pk_bf16_f32 v227, v158, v159\n"
      "v_mfma_f32_16x16x32_bf16 v[160:163], a[240:243], v[104:107], v[160:163]\n"
      "s_waitcnt vmcnt(9) lgkmcnt(3)\n"
      "buffer_store_dword v18, v4, s[8:11], s52 offen sc1\n"
      "v_mfma_f32_16x16x32_bf16 v[160:163], a[244:247], v[108:111], v[160:163]\n"
      "ds_read_b128 v[68:71], v0 offset:3072\n"
      "v_add_f32 v174, v174, v233\n"
      "v_mfma_f32_16x16x32_bf16 v[160:163], a[248:251], v[112:115], v[160:163]\n"
      "ds_read_b128 v[104:107], v0 offset:12288\n"
      "v_add_f32 v169, v169, v210\n"
      "v_mfma_f32_16x16x32_bf16 v[160:163], a[252:255], v[116:119], v[160:163]\n"
      "ds_read_b128 v[96:99], v0 offset:10240\n"
      "v_fmac_f32 v237, v167, v167\n"
      "v_mfma_f32_16x16x32_bf16 v[160:163], v[24:27], v[120:123], v[160:163]\n"
      "ds_read_b128 v[80:83], v0 offset:6144\n"
      "v_add_f32 v175, v175, v205\n"
      "v_mfma_f32_16x16x32_bf16 v[160:163], v[28:31], v[124:127], v[160:163]\n"
      "ds_read_b128 v[64:67], v0 offset:2048\n"
      "v_add_f32 v168, v168, v234\n"
      "v_mfma_f32_16x16x32_bf16 v[160:163], v[32:35], v[128:131], v[160:163]\n"
      "v_add_f32 v236, v236, v167\n"
      "v_cvt_pk_bf16_f32 v226, v156, v157\n"
      "v_mfma_f32_16x16x32_bf16 v[160:163], v[36:39], v[132:135], v[160:163]\n"
      "ds_read_b128 v[92:95], v0 offset:9216\n"
      "v_cvt_pk_bf16_f32 v176, v164, v165\n"
      "v_mfma_f32_16x16x32_bf16 v[160:163], v[40:43], v[136:139], v[160:163]\n"
      "buffer_store_dwordx4 v[224:227], v0, s[8:11], s37 offen sc1\n"
      "ds_read_b128 v[76:79], v0 offset:5120\n"
      "v_mfma_f32_16x16x32_bf16 v[160:163], v[44:47], v[140:143], v[160:163]\n"
      "ds_read_b128 v[132:135], v0 offset:19456\n"
      "v_and_b32 v211, 0xffff0000, v211\n"
      "v_mfma_f32_16x16x32_bf16 v[160:163], v[48:51], v[144:147], v[160:163]\n"
      "ds_read_b128 v[100:103], v0 offset:11264\n"
      "v_add_f32 v171, v171, v203\n"
      "v_mfma_f32_16x16x32_bf16 v[160:163], v[52:55], v[148:151], v[160:163]\n"
      "s_sub_u32 s59, s59, 1\n"
      "s_cbranch_scc0 .Ld_t0\n"
      ".Ld_out5:\n"
      "s_nop 8\n"
      "v_cvt_pk_bf16_f32 v228, v160, v161\n"
      "v_cvt_pk_bf16_f32 v229, v162, v163\n"
      "buffer_store_dwordx2 v[228:229], v13, s[8:11], s37 offen sc1\n"
      "v_cvt_pk_bf16_f32 v178, v168, v169\n"
      "v_add_f32 v175, v175, v213\n"
      "v_add_f32 v171, v171, v211\n"
      "v_add_f32 v173, v173, v204\n"
      "v_cvt_pk_bf16_f32 v179, v170, v171\n"
      "v_add_f32 v173, v173, v212\n"
      "v_cvt_pk_bf16_f32 v180, v172, v173\n"
      "v_cvt_pk_bf16_f32 v181, v174, v175\n"
      "buffer_store_dwordx2 v[180:181], v9, s[16:19], s57 offen\n"
      "buffer_store_dwordx4 v[176:179], v8, s[16:19], s57 offen\n"
      "s_add_u32 s52, s52, 106496\n"
      "s_add_u32 s37, s37, 173056\n"
      "s_waitcnt vmcnt(2)\n"
      "buffer_store_dword v18, v4, s[8:11], s52 offen sc1\n"
      "s_add_u32 s52, s52, 173056\n"
      "s_branch .Ld_end\n"
      ".Ld_out0:\n"
      "s_nop 8\n"
      "v_add_f32 v174, v174, v162\n"
      "v_add_f32 v170, v170, v158\n"
      "v_lshlrev_b32 v172, 16, v220\n"
      "s_add_u32 s38, s38, 147456\n"
      "v_add_f32 v171, v171, v159\n"
      "v_add_f32 v169, v169, v157\n"
      "v_add_f32 v172, v172, v160\n"
      "v_add_f32 v175, v175, v163\n"
      "v_and_b32 v173, 0xffff0000, v220\n"
      "v_add_f32 v173, v173, v161\n"
      "s_add_u32 s52, s52, 173056\n"
      "buffer_load_dword v21, v3, s[8:11], s53 offen sc1\n"
      "s_add_u32 s55, s53, 6656\n"
      "s_add_u32 s54, s53, 13312\n"
      "s_waitcnt vmcnt(0)\n"
      "v_cmp_ne_u32 vcc, s21, v21\n"
      "s_cbranch_vccnz .Lsp_df_17\n"
      ".Lbk_df_17:\n"
      "buffer_load_dwordx4 v[176:179], v0, s[8:11], s53 offen sc1\n"
      "buffer_load_dwordx2 v[180:181], v13, s[8:11], s53 offen sc1\n"
      "s_add_u32 s58, s41, 4294967264\n"
      "buffer_load_dwordx4 v[184:187], v0, s[8:11], s55 offen sc1\n"
      "buffer_load_dwordx4 v[192:195], v0, s[8:11], s54 offen sc1\n"
      "buffer_load_dwordx2 v[188:189], v13, s[8:11], s55 offen sc1\n"
      "s_add_u32 s55, s53, 19968\n"
      "buffer_load_dwordx2 v[196:197], v13, s[8:11], s54 offen sc1\n"
      "buffer_load_dwordx4 v[200:203], v0, s[8:11], s55 offen sc1\n"
      "buffer_load_dwordx2 v[204:205], v13, s[8:11], s55 offen sc1\n"
      "s_cmp_lt_u32 s41, 32\n"
      "s_cselect_b32 s56, s39, 0x40000000\n"
      "s_add_u32 s54, s53, 26624\n"
      "s_add_u32 s53, s53, 599040\n"
      "buffer_load_dwordx2 v[212:213], v13, s[8:11], s54 offen sc1\n"
      "s_add_u32 s39, s39, 147456\n"
      "buffer_load_dwordx4 v[208:211], v0, s[8:11], s54 offen sc1\n"
      "s_cmp_lt_u32 s58, 32\n"
      "s_cselect_b32 s57, s40, 0x40000000\n"
      "s_waitcnt vmcnt(9)\n"
      "v_lshlrev_b32 v234, 16, v178\n"
      "v_lshlrev_b32 v233, 16, v177\n"
      "v_lshlrev_b32 v232, 16, v176\n"
      "v_add_f32 v166, v166, v233\n"
      "v_add_f32 v164, v164, v232\n"
      "s_waitcnt vmcnt(8)\n"
      "v_lshlrev_b32 v233, 16, v181\n"
      "v_lshlrev_b32 v235, 16, v179\n"
      "v_and_b32 v176, 0xffff0000, v176\n"
      "v_lshlrev_b32 v232, 16, v180\n"
      "v_add_f32 v172, v172, v232\n"
      "v_and_b32 v181, 0xffff0000, v181\n"
      "v_add_f32 v168, v168, v234\n"
      "v_add_f32 v174, v174, v233\n"
      "v_add_f32 v165, v165, v176\n"
      "s_waitcnt vmcnt(7)\n"
      "v_lshlrev_b32 v232, 16, v184\n"
      "v_and_b32 v177, 0xffff0000, v177\n"
      "s_add_u32 s40, s40, 4294856704\n"
      "v_add_f32 v167, v167, v177\n"
      "v_and_b32 v180, 0xffff0000, v180\n"
      "v_add_f32 v164, v164, v232\n"
      "v_lshlrev_b32 v233, 16, v185\n"
      "v_add_f32 v166, v166, v233\n"
      "s_waitcnt vmcnt(5)\n"
      "v_lshlrev_b32 v232, 16, v188\n"
      "v_add_f32 v172, v172, v232\n"
      "v_and_b32 v185, 0xffff0000, v185\n"
      "v_lshlrev_b32 v232, 16, v192\n"
      "v_lshlrev_b32 v234, 16, v186\n"
      "v_add_f32 v164, v164, v232\n"
      "v_lshlrev_b32 v233, 16, v189\n"
      "s_waitcnt vmcnt(4)\n"
      "v_lshlrev_b32 v232, 16, v196\n"
      "v_add_f32 v174, v174, v233\n"
      "v_and_b32 v184, 0xffff0000, v184\n"
      "v_add_f32 v172, v172, v232\n"
      "v_add_f32 v170, v170, v235\n"
      "s_add_u32 s41, s41, 6\n"
      "v_add_f32 v165, v165, v184\n"
      "v_add_f32 v168, v168, v234\n"
      "v_lshlrev_b32 v233, 16, v193\n"
      "v_and_b32 v178, 0xffff0000, v178\n"
      "s_waitcnt vmcnt(3)\n"
      "v_lshlrev_b32 v232, 16, v200\n"
      "v_add_f32 v164, v164, v232\n"
      "v_and_b32 v186, 0xffff0000, v186\n"
      "v_add_f32 v169, v169, v178\n"
      "v_and_b32 v200, 0xffff0000, v200\n"
      "v_add_f32 v173, v173, v180\n"
      "v_add_f32 v166, v166, v233\n"
      "v_lshlrev_b32 v234, 16, v194\n"
      "v_and_b32 v192, 0xffff0000, v192\n"
      "v_add_f32 v175, v175, v181\n"
      "v_and_b32 v189, 0xffff0000, v189\n"
      "s_waitcnt vmcnt(2)\n"
      "v_lshlrev_b32 v232, 16, v204\n"
      "v_lshlrev_b32 v235, 16, v187\n"
      "v_add_f32 v172, v172, v232\n"
      "s_waitcnt vmcnt(0)\n"
      "v_lshlrev_b32 v232, 16, v208\n"
      "v_add_f32 v170, v170, v235\n"
      "v_and_b32 v194, 0xffff0000, v194\n"
      "v_and_b32 v208, 0xffff0000, v208\n"
      "v_add_f32 v167, v167, v185\n"
      "v_lshlrev_b32 v233, 16, v197\n"
      "v_lshlrev_b32 v235, 16, v195\n"
      "v_add_f32 v168, v168, v234\n"
      "v_add_f32 v164, v164, v232\n"
      "v_add_f32 v165, v165, v192\n"
      "v_lshlrev_b32 v234, 16, v202\n"
      "v_add_f32 v174, v174, v233\n"
      "v_lshlrev_b32 v232, 16, v212\n"
      "v_and_b32 v179, 0xffff0000, v179\n"
      "v_add_f32 v169, v169, v186\n"
      "v_lshlrev_b32 v233, 16, v201\n"
      "v_add_f32 v166, v166, v233\n"
      "v_and_b32 v193, 0xffff0000, v193\n"
      "v_and_b32 v197, 0xffff0000, v197\n"
      "v_lshlrev_b32 v233, 16, v205\n"
      "v_add_f32 v170, v170, v235\n"
      "v_add_f32 v174, v174, v233\n"
      "v_lshlrev_b32 v235, 16, v203\n"
      "v_and_b32 v187, 0xffff0000, v187\n"
      "v_lshlrev_b32 v233, 16, v209\n"
      "v_add_f32 v170, v170, v235\n"
      "v_add_f32 v166, v166, v233\n"
      "v_and_b32 v201, 0xffff0000, v201\n"
      "v_add_f32 v165, v165, v200\n"
      "v_and_b32 v188, 0xffff0000, v188\n"
      "v_add_f32 v165, v165, v208\n"
      "v_add_f32 v168, v168, v234\n"
      "v_add_f32 v167, v167, v193\n"
      "v_lshlrev_b32 v233, 16, v213\n"
      "v_add_f32 v171, v171, v179\n"
      "v_add_f32 v167, v167, v201\n"
      "v_lshlrev_b32 v234, 16, v210\n"
      "v_and_b32 v213, 0xffff0000, v213\n"
      "v_add_f32 v173, v173, v188\n"
      "v_and_b32 v209, 0xffff0000, v209\n"
      "v_add_f32 v175, v175, v189\n"
      "v_add_f32 v167, v167, v209\n"
      "v_and_b32 v203, 0xffff0000, v203\n"
      "v_add_f32 v175, v175, v197\n"
      "v_and_b32 v204, 0xffff0000, v204\n"
      "v_and_b32 v195, 0xffff0000, v195\n"
      "v_add_f32 v172, v172, v232\n"
      "v_add_f32 v169, v169, v194\n"
      "v_and_b32 v196, 0xffff0000, v196\n"
      "v_and_b32 v212, 0xffff0000, v212\n"
      "v_cvt_pk_bf16_f32 v177, v166, v167\n"
      "v_add_f32 v171, v171, v187\n"
      "v_and_b32 v205, 0xffff0000, v205\n"
      "v_and_b32 v202, 0xffff0000, v202\n"
      "v_add_f32 v169, v169, v202\n"
      "v_and_b32 v210, 0xffff0000, v210\n"
      "v_add_f32 v173, v173, v196\n"
      "v_lshlrev_b32 v235, 16, v211\n"
      "v_add_f32 v170, v170, v235\n"
      "v_add_f32 v171, v171, v195\n"
      "v_add_f32 v174, v174, v233\n"
      "v_add_f32 v169, v169, v210\n"
      "v_add_f32 v175, v175, v205\n"
      "v_add_f32 v168, v168, v234\n"
      "v_cvt_pk_bf16_f32 v176, v164, v165\n"
      "v_and_b32 v211, 0xffff0000, v211\n"
      "v_add_f32 v171, v171, v203\n"
      "v_cvt_pk_bf16_f32 v178, v168, v169\n"
      "v_add_f32 v175, v175, v213\n"
      "v_add_f32 v171, v171, v211\n"
      "v_add_f32 v173, v173, v204\n"
      "v_cvt_pk_bf16_f32 v179, v170, v171\n"
      "v_add_f32 v173, v173, v212\n"
      "v_cvt_pk_bf16_f32 v180, v172, v173\n"
      "v_cvt_pk_bf16_f32 v181, v174, v175\n"
      "buffer_store_dwordx2 v[180:181], v9, s[16:19], s57 offen\n"
      "buffer_store_dwordx4 v[176:179], v8, s[16:19], s57 offen\n"
      "s_branch .Ld_end\n"
      ".Ld_out1:\n"
      "s_nop 8\n"
      "v_cvt_pk_bf16_f32 v229, v162, v163\n"
      "v_cvt_pk_bf16_f32 v228, v160, v161\n"
      "buffer_store_dwordx2 v[228:229], v13, s[8:11], s37 offen sc1\n"
      "s_waitcnt vmcnt(0)\n"
      "buffer_store_dword v18, v4, s[8:11], s52 offen sc1\n"
      "buffer_load_dword v21, v3, s[8:11], s53 offen sc1\n"
      "s_add_u32 s37, s37, 106496\n"
      "s_add_u32 s52, s52, 106496\n"
      "s_add_u32 s55, s53, 6656\n"
      "s_add_u32 s54, s53, 13312\n"
      "s_waitcnt vmcnt(0)\n"
      "v_cmp_ne_u32 vcc, s21, v21\n"
      "s_cbranch_vccnz .Lsp_df_18\n"
      ".Lbk_df_18:\n"
      "buffer_load_dwordx4 v[176:179], v0, s[8:11], s53 offen sc1\n"
      "buffer_load_dwordx2 v[180:181], v13, s[8:11], s53 offen sc1\n"
      "s_add_u32 s58, s41, 4294967264\n"
      "buffer_load_dwordx4 v[184:187], v0, s[8:11], s55 offen sc1\n"
      "buffer_load_dwordx4 v[192:195], v0, s[8:11], s54 offen sc1\n"
      "buffer_load_dwordx2 v[188:189], v13, s[8:11], s55 offen sc1\n"
      "s_add_u32 s55, s53, 19968\n"
      "buffer_load_dwordx2 v[196:197], v13, s[8:11], s54 offen sc1\n"
      "buffer_load_dwordx4 v[200:203], v0, s[8:11], s55 offen sc1\n"
      "buffer_load_dwordx2 v[204:205], v13, s[8:11], s55 offen sc1\n"
      "s_cmp_lt_u32 s41, 32\n"
      "s_cselect_b32 s56, s39, 0x40000000\n"
      "s_add_u32 s54, s53, 26624\n"
      "s_add_u32 s53, s53, 599040\n"
      "buffer_load_dwordx2 v[212:213], v13, s[8:11], s54 offen sc1\n"
      "s_add_u32 s39, s39, 147456\n"
      "buffer_load_dwordx4 v[208:211], v0, s[8:11], s54 offen sc1\n"
      "s_cmp_lt_u32 s58, 32\n"
      "s_cselect_b32 s57, s40, 0x40000000\n"
      "s_waitcnt vmcnt(9)\n"
      "v_lshlrev_b32 v234, 16, v178\n"
      "v_lshlrev_b32 v233, 16, v177\n"
      "v_lshlrev_b32 v232, 16, v176\n"
      "v_add_f32 v166, v166, v233\n"
      "v_add_f32 v164, v164, v232\n"
      "s_waitcnt vmcnt(8)\n"
      "v_lshlrev_b32 v233, 16, v181\n"
      "v_lshlrev_b32 v235, 16, v179\n"
      "v_and_b32 v176, 0xffff0000, v176\n"
      "v_lshlrev_b32 v232, 16, v180\n"
      "v_add_f32 v172, v172, v232\n"
      "v_and_b32 v181, 0xffff0000, v181\n"
      "v_add_f32 v168, v168, v234\n"
      "v_add_f32 v174, v174, v233\n"
      "v_add_f32 v165, v165, v176\n"
      "s_waitcnt vmcnt(7)\n"
      "v_lshlrev_b32 v232, 16, v184\n"
      "v_and_b32 v177, 0xffff0000, v177\n"
      "s_add_u32 s40, s40, 4294856704\n"
      "v_add_f32 v167, v167, v177\n"
      "v_and_b32 v180, 0xffff0000, v180\n"
      "v_add_f32 v164, v164, v232\n"
      "v_lshlrev_b32 v233, 16, v185\n"
      "v_add_f32 v166, v166, v233\n"
      "s_waitcnt vmcnt(5)\n"
      "v_lshlrev_b32 v232, 16, v188\n"
      "v_add_f32 v172, v172, v232\n"
      "v_and_b32 v185, 0xffff0000, v185\n"
      "v_lshlrev_b32 v232, 16, v192\n"
      "v_lshlrev_b32 v234, 16, v186\n"
      "v_add_f32 v164, v164, v232\n"
      "v_lshlrev_b32 v233, 16, v189\n"
      "s_waitcnt vmcnt(4)\n"
      "v_lshlrev_b32 v232, 16, v196\n"
      "v_add_f32 v174, v174, v233\n"
      "v_and_b32 v184, 0xffff0000, v184\n"
      "v_add_f32 v172, v172, v232\n"
      "v_add_f32 v170, v170, v235\n"
      "s_add_u32 s41, s41, 6\n"
      "v_add_f32 v165, v165, v184\n"
      "v_add_f32 v168, v168, v234\n"
      "v_lshlrev_b32 v233, 16, v193\n"
      "v_and_b32 v178, 0xffff0000, v178\n"
      "s_waitcnt vmcnt(3)\n"
      "v_lshlrev_b32 v232, 16, v200\n"
      "v_add_f32 v164, v164, v232\n"
      "v_and_b32 v186, 0xffff0000, v186\n"
      "v_add_f32 v169, v169, v178\n"
      "v_and_b32 v200, 0xffff0000, v200\n"
      "v_add_f32 v173, v173, v180\n"
      "v_add_f32 v166, v166, v233\n"
      "v_lshlrev_b32 v234, 16, v194\n"
      "v_and_b32 v192, 0xffff0000, v192\n"
      "v_add_f32 v175, v175, v181\n"
      "v_and_b32 v189, 0xffff0000, v189\n"
      "s_waitcnt vmcnt(2)\n"
      "v_lshlrev_b32 v232, 16, v204\n"
      "v_lshlrev_b32 v235, 16, v187\n"
      "v_add_f32 v172, v172, v232\n"
      "s_waitcnt vmcnt(0)\n"
      "v_lshlrev_b32 v232, 16, v208\n"
      "v_add_f32 v170, v170, v235\n"
      "v_and_b32 v194, 0xffff0000, v194\n"
      "v_and_b32 v208, 0xffff0000, v208\n"
      "v_add_f32 v167, v167, v185\n"
      "v_lshlrev_b32 v233, 16, v197\n"
      "v_lshlrev_b32 v235, 16, v195\n"
      "v_add_f32 v168, v168, v234\n"
      "v_add_f32 v164, v164, v232\n"
      "v_add_f32 v165, v165, v192\n"
      "v_lshlrev_b32 v234, 16, v202\n"
      "v_add_f32 v174, v174, v233\n"
      "v_lshlrev_b32 v232, 16, v212\n"
      "v_and_b32 v179, 0xffff0000, v179\n"
      "v_add_f32 v169, v169, v186\n"
      "v_lshlrev_b32 v233, 16, v201\n"
      "v_add_f32 v166, v166, v233\n"
      "v_and_b32 v193, 0xffff0000, v193\n"
      "v_and_b32 v197, 0xffff0000, v197\n"
      "v_lshlrev_b32 v233, 16, v205\n"
      "v_add_f32 v170, v170, v235\n"
      "v_add_f32 v174, v174, v233\n"
      "v_lshlrev_b32 v235, 16, v203\n"
      "v_and_b32 v187, 0xffff0000, v187\n"
      "v_lshlrev_b32 v233, 16, v209\n"
      "v_add_f32 v170, v170, v235\n"
      "v_add_f32 v166, v166, v233\n"
      "v_and_b32 v201, 0xffff0000, v201\n"
      "v_add_f32 v165, v165, v200\n"
      "v_and_b32 v188, 0xffff0000, v188\n"
      "v_add_f32 v165, v165, v208\n"
      "v_add_f32 v168, v168, v234\n"
      "v_add_f32 v167, v167, v193\n"
      "v_lshlrev_b32 v233, 16, v213\n"
      "v_add_f32 v171, v171, v179\n"
      "v_add_f32 v167, v167, v201\n"
      "v_lshlrev_b32 v234, 16, v210\n"
      "v_and_b32 v213, 0xffff0000, v213\n"
      "v_add_f32 v173, v173, v188\n"
      "v_and_b32 v209, 0xffff0000, v209\n"
      "v_add_f32 v175, v175, v189\n"
      "v_add_f32 v167, v167, v209\n"
      "v_and_b32 v203, 0xffff0000, v203\n"
      "v_add_f32 v175, v175, v197\n"
      "v_and_b32 v204, 0xffff0000, v204\n"
      "v_and_b32 v195, 0xffff0000, v195\n"
      "v_add_f32 v172, v172, v232\n"
      "v_add_f32 v169, v169, v194\n"
      "v_and_b32 v196, 0xffff0000, v196\n"
      "v_and_b32 v212, 0xffff0000, v212\n"
      "v_cvt_pk_bf16_f32 v177, v166, v167\n"
      "v_add_f32 v171, v171, v187\n"
      "v_and_b32 v205, 0xffff0000, v205\n"
      "v_and_b32 v202, 0xffff0000, v202\n"
      "v_add_f32 v169, v169, v202\n"
      "v_and_b32 v210, 0xffff0000, v210\n"
      "v_add_f32 v173, v173, v196\n"
      "v_lshlrev_b32 v235, 16, v211\n"
      "v_add_f32 v170, v170, v235\n"
      "v_add_f32 v171, v171, v195\n"
      "v_add_f32 v174, v174, v233\n"
      "v_add_f32 v169, v169, v210\n"
      "v_add_f32 v175, v175, v205\n"
      "v_add_f32 v168, v168, v234\n"
      "v_cvt_pk_bf16_f32 v176, v164, v165\n"
      "v_and_b32 v211, 0xffff0000, v211\n"
      "v_add_f32 v171, v171, v203\n"
      "v_cvt_pk_bf16_f32 v178, v168, v169\n"
      "v_add_f32 v175, v175, v213\n"
      "v_add_f32 v171, v171, v211\n"
      "v_add_f32 v173, v173, v204\n"
      "v_cvt_pk_bf16_f32 v179, v170, v171\n"
      "v_add_f32 v173, v173, v212\n"
      "v_cvt_pk_bf16_f32 v180, v172, v173\n"
      "v_cvt_pk_bf16_f32 v181, v174, v175\n"
      "buffer_store_dwordx2 v[180:181], v9, s[16:19], s57 offen\n"
      "buffer_store_dwordx4 v[176:179], v8, s[16:19], s57 offen\n"
      "s_branch .Ld_end\n"
      ".Ld_out2:\n"
      "s_nop 8\n"
      "v_cvt_pk_bf16_f32 v229, v162, v163\n"
      "v_cvt_pk_bf16_f32 v228, v160, v161\n"
      "buffer_store_dwordx2 v[228:229], v13, s[8:11], s37 offen sc1\n"
      "s_add_u32 s52, s52, 106496\n"
      "s_add_u32 s55, s53, 6656\n"
      "s_add_u32 s54, s53, 13312\n"
      "s_waitcnt vmcnt(2)\n"
      "v_cmp_ne_u32 vcc, s21, v21\n"
      "s_cbranch_vccnz .Lsp_df_19\n"
      ".Lbk_df_19:\n"
      "buffer_load_dwordx4 v[176:179], v0, s[8:11], s53 offen sc1\n"
      "buffer_load_dwordx2 v[180:181], v13, s[8:11], s53 offen sc1\n"
      "s_add_u32 s58, s41, 4294967264\n"
      "buffer_load_dwordx4 v[184:187], v0, s[8:11], s55 offen sc1\n"
      "s_add_u32 s37, s37, 106496\n"
      "s_waitcnt vmcnt(3)\n"
      "buffer_store_dword v18, v4, s[8:11], s52 offen sc1\n"
      "buffer_load_dwordx4 v[192:195], v0, s[8:11], s54 offen sc1\n"
      "buffer_load_dwordx2 v[188:189], v13, s[8:11], s55 offen sc1\n"
      "s_add_u32 s55, s53, 19968\n"
      "buffer_load_dwordx2 v[196:197], v13, s[8:11], s54 offen sc1\n"
      "buffer_load_dwordx4 v[200:203], v0, s[8:11], s55 offen sc1\n"
      "buffer_load_dwordx2 v[204:205], v13, s[8:11], s55 offen sc1\n"
      "s_cmp_lt_u32 s41, 32\n"
      "s_cselect_b32 s56, s39, 0x40000000\n"
      "s_add_u32 s54, s53, 26624\n"
      "s_add_u32 s53, s53, 599040\n"
      "buffer_load_dwordx2 v[212:213], v13, s[8:11], s54 offen sc1\n"
      "s_add_u32 s39, s39, 147456\n"
      "buffer_load_dwordx4 v[208:211], v0, s[8:11], s54 offen sc1\n"
      "s_cmp_lt_u32 s58, 32\n"
      "s_cselect_b32 s57, s40, 0x40000000\n"
      "s_waitcnt vmcnt(10)\n"
      "v_lshlrev_b32 v234, 16, v178\n"
      "v_lshlrev_b32 v233, 16, v177\n"
      "v_lshlrev_b32 v232, 16, v176\n"
      "v_add_f32 v166, v166, v233\n"
      "s_add_u32 s52, s52, 106496\n"
      "v_add_f32 v164, v164, v232\n"
      "s_waitcnt vmcnt(9)\n"
      "v_lshlrev_b32 v233, 16, v181\n"
      "v_lshlrev_b32 v235, 16, v179\n"
      "v_and_b32 v176, 0xffff0000, v176\n"
      "v_lshlrev_b32 v232, 16, v180\n"
      "v_add_f32 v172, v172, v232\n"
      "v_and_b32 v181, 0xffff0000, v181\n"
      "v_add_f32 v168, v168, v234\n"
      "v_add_f32 v174, v174, v233\n"
      "v_add_f32 v165, v165, v176\n"
      "s_waitcnt vmcnt(8)\n"
      "v_lshlrev_b32 v232, 16, v184\n"
      "v_and_b32 v177, 0xffff0000, v177\n"
      "s_add_u32 s40, s40, 4294856704\n"
      "v_add_f32 v167, v167, v177\n"
      "v_and_b32 v180, 0xffff0000, v180\n"
      "v_add_f32 v164, v164, v232\n"
      "v_lshlrev_b32 v233, 16, v185\n"
      "v_add_f32 v166, v166, v233\n"
      "s_waitcnt vmcnt(5)\n"
      "v_lshlrev_b32 v232, 16, v188\n"
      "v_add_f32 v172, v172, v232\n"
      "v_and_b32 v185, 0xffff0000, v185\n"
      "v_lshlrev_b32 v232, 16, v192\n"
      "v_lshlrev_b32 v234, 16, v186\n"
      "v_add_f32 v164, v164, v232\n"
      "v_lshlrev_b32 v233, 16, v189\n"
      "s_waitcnt vmcnt(4)\n"
      "v_lshlrev_b32 v232, 16, v196\n"
      "v_add_f32 v174, v174, v233\n"
      "v_and_b32 v184, 0xffff0000, v184\n"
      "v_add_f32 v172, v172, v232\n"
      "v_add_f32 v170, v170, v235\n"
      "s_add_u32 s41, s41, 6\n"
      "v_add_f32 v165, v165, v184\n"
      "v_add_f32 v168, v168, v234\n"
      "v_lshlrev_b32 v233, 16, v193\n"
      "v_and_b32 v178, 0xffff0000, v178\n"
      "s_waitcnt vmcnt(3)\n"
      "v_lshlrev_b32 v232, 16, v200\n"
      "v_add_f32 v164, v164, v232\n"
      "v_and_b32 v186, 0xffff0000, v186\n"
      "v_add_f32 v169, v169, v178\n"
      "v_and_b32 v200, 0xffff0000, v200\n"
      "v_add_f32 v173, v173, v180\n"
      "v_add_f32 v166, v166, v233\n"
      "v_lshlrev_b32 v234, 16, v194\n"
      "v_and_b32 v192, 0xffff0000, v192\n"
      "v_add_f32 v175, v175, v181\n"
      "v_and_b32 v189, 0xffff0000, v189\n"
      "s_waitcnt vmcnt(2)\n"
      "v_lshlrev_b32 v232, 16, v204\n"
      "v_lshlrev_b32 v235, 16, v187\n"
      "v_add_f32 v172, v172, v232\n"
      "s_waitcnt vmcnt(0)\n"
      "v_lshlrev_b32 v232, 16, v208\n"
      "v_add_f32 v170, v170, v235\n"
      "v_and_b32 v194, 0xffff0000, v194\n"
      "v_and_b32 v208, 0xffff0000, v208\n"
      "v_add_f32 v167, v167, v185\n"
      "v_lshlrev_b32 v233, 16, v197\n"
      "v_lshlrev_b32 v235, 16, v195\n"
      "v_add_f32 v168, v168, v234\n"
      "v_add_f32 v164, v164, v232\n"
      "v_add_f32 v165, v165, v192\n"
      "v_lshlrev_b32 v234, 16, v202\n"
      "v_add_f32 v174, v174, v233\n"
      "v_lshlrev_b32 v232, 16, v212\n"
      "v_and_b32 v179, 0xffff0000, v179\n"
      "v_add_f32 v169, v169, v186\n"
      "v_lshlrev_b32 v233, 16, v201\n"
      "v_add_f32 v166, v166, v233\n"
      "v_and_b32 v193, 0xffff0000, v193\n"
      "v_and_b32 v197, 0xffff0000, v197\n"
      "v_lshlrev_b32 v233, 16, v205\n"
      "v_add_f32 v170, v170, v235\n"
      "v_add_f32 v174, v174, v233\n"
      "v_lshlrev_b32 v235, 16, v203\n"
      "v_and_b32 v187, 0xffff0000, v187\n"
      "v_lshlrev_b32 v233, 16, v209\n"
      "v_add_f32 v170, v170, v235\n"
      "v_add_f32 v166, v166, v233\n"
      "v_and_b32 v201, 0xffff0000, v201\n"
      "v_add_f32 v165, v165, v200\n"
      "v_and_b32 v188, 0xffff0000, v188\n"
      "v_add_f32 v165, v165, v208\n"
      "v_add_f32 v168, v168, v234\n"
      "v_add_f32 v167, v167, v193\n"
      "v_lshlrev_b32 v233, 16, v213\n"
      "v_add_f32 v171, v171, v179\n"
      "v_add_f32 v167, v167, v201\n"
      "v_lshlrev_b32 v234, 16, v210\n"
      "v_and_b32 v213, 0xffff0000, v213\n"
      "v_add_f32 v173, v173, v188\n"
      "v_and_b32 v209, 0xffff0000, v209\n"
      "v_add_f32 v175, v175, v189\n"
      "v_add_f32 v167, v167, v209\n"
      "v_and_b32 v203, 0xffff0000, v203\n"
      "v_add_f32 v175, v175, v197\n"
      "v_and_b32 v204, 0xffff0000, v204\n"
      "v_and_b32 v195, 0xffff0000, v195\n"
      "v_add_f32 v172, v172, v232\n"
      "v_add_f32 v169, v169, v194\n"
      "v_and_b32 v196, 0xffff0000, v196\n"
      "v_and_b32 v212, 0xffff0000, v212\n"
      "v_cvt_pk_bf16_f32 v177, v166, v167\n"
      "v_add_f32 v171, v171, v187\n"
      "v_and_b32 v205, 0xffff0000, v205\n"
      "v_and_b32 v202, 0xffff0000, v202\n"
      "v_add_f32 v169, v169, v202\n"
      "v_and_b32 v210, 0xffff0000, v210\n"
      "v_add_f32 v173, v173, v196\n"
      "v_lshlrev_b32 v235, 16, v211\n"
      "v_add_f32 v170, v170, v235\n"
      "v_add_f32 v171, v171, v195\n"
      "v_add_f32 v174, v174, v233\n"
      "v_add_f32 v169, v169, v210\n"
      "v_add_f32 v175, v175, v205\n"
      "v_add_f32 v168, v168, v234\n"
      "v_cvt_pk_bf16_f32 v176, v164, v165\n"
      "v_and_b32 v211, 0xffff0000, v211\n"
      "v_add_f32 v171, v171, v203\n"
      "v_cvt_pk_bf16_f32 v178, v168, v169\n"
      "v_add_f32 v175, v175, v213\n"
      "v_add_f32 v171, v171, v211\n"
      "v_add_f32 v173, v173, v204\n"
      "v_cvt_pk_bf16_f32 v179, v170, v171\n"
      "v_add_f32 v173, v173, v212\n"
      "v_cvt_pk_bf16_f32 v180, v172, v173\n"
      "v_cvt_pk_bf16_f32 v181, v174, v175\n"
      "buffer_store_dwordx2 v[180:181], v9, s[16:19], s57 offen\n"
      "buffer_store_dwordx4 v[176:179], v8, s[16:19], s57 offen\n"
      "s_branch .Ld_end\n"
      ".Ld_out3:\n"
      "s_nop 8\n"
      "v_cvt_pk_bf16_f32 v228, v160, v161\n"
      "buffer_load_dwordx4 v[200:203], v0, s[8:11], s55 offen sc1\n"
      "v_cvt_pk_bf16_f32 v227, v158, v159\n"
      "buffer_store_dwordx4 v[224:227], v0, s[8:11], s37 offen sc1\n"
      "v_cvt_pk_bf16_f32 v229, v162, v163\n"
      "buffer_store_dwordx2 v[228:229], v13, s[8:11], s37 offen sc1\n"
      "s_add_u32 s37, s37, 106496\n"
      "buffer_load_dwordx2 v[204:205], v13, s[8:11], s55 offen sc1\n"
      "s_cmp_lt_u32 s41, 32\n"
      "s_cselect_b32 s56, s39, 0x40000000\n"
      "s_add_u32 s54, s53, 26624\n"
      "s_add_u32 s53, s53, 599040\n"
      "buffer_load_dwordx2 v[212:213], v13, s[8:11], s54 offen sc1\n"
      "s_add_u32 s39, s39, 147456\n"
      "buffer_load_dwordx4 v[208:211], v0, s[8:11], s54 offen sc1\n"
      "s_cmp_lt_u32 s58, 32\n"
      "s_cselect_b32 s57, s40, 0x40000000\n"
      "s_waitcnt vmcnt(12)\n"
      "v_lshlrev_b32 v234, 16, v178\n"
      "v_lshlrev_b32 v233, 16, v177\n"
      "v_lshlrev_b32 v232, 16, v176\n"
      "v_add_f32 v166, v166, v233\n"
      "s_add_u32 s52, s52, 106496\n"
      "v_add_f32 v164, v164, v232\n"
      "s_waitcnt vmcnt(11)\n"
      "v_lshlrev_b32 v233, 16, v181\n"
      "v_lshlrev_b32 v235, 16, v179\n"
      "v_and_b32 v176, 0xffff0000, v176\n"
      "v_lshlrev_b32 v232, 16, v180\n"
      "v_add_f32 v172, v172, v232\n"
      "v_and_b32 v181, 0xffff0000, v181\n"
      "v_add_f32 v168, v168, v234\n"
      "v_add_f32 v174, v174, v233\n"
      "v_add_f32 v165, v165, v176\n"
      "s_waitcnt vmcnt(10)\n"
      "v_lshlrev_b32 v232, 16, v184\n"
      "v_and_b32 v177, 0xffff0000, v177\n"
      "s_add_u32 s40, s40, 4294856704\n"
      "v_add_f32 v167, v167, v177\n"
      "v_and_b32 v180, 0xffff0000, v180\n"
      "v_add_f32 v164, v164, v232\n"
      "v_lshlrev_b32 v233, 16, v185\n"
      "v_add_f32 v166, v166, v233\n"
      "s_waitcnt vmcnt(7)\n"
      "v_lshlrev_b32 v232, 16, v188\n"
      "v_add_f32 v172, v172, v232\n"
      "v_and_b32 v185, 0xffff0000, v185\n"
      "v_lshlrev_b32 v232, 16, v192\n"
      "v_lshlrev_b32 v234, 16, v186\n"
      "v_add_f32 v164, v164, v232\n"
      "v_lshlrev_b32 v233, 16, v189\n"
      "s_waitcnt vmcnt(6)\n"
      "v_lshlrev_b32 v232, 16, v196\n"
      "v_add_f32 v174, v174, v233\n"
      "v_and_b32 v184, 0xffff0000, v184\n"
      "v_add_f32 v172, v172, v232\n"
      "v_add_f32 v170, v170, v235\n"
      "s_add_u32 s41, s41, 6\n"
      "v_add_f32 v165, v165, v184\n"
      "v_add_f32 v168, v168, v234\n"
      "v_lshlrev_b32 v233, 16, v193\n"
      "v_and_b32 v178, 0xffff0000, v178\n"
      "s_waitcnt vmcnt(5)\n"
      "v_lshlrev_b32 v232, 16, v200\n"
      "v_add_f32 v164, v164, v232\n"
      "v_and_b32 v186, 0xffff0000, v186\n"
      "s_waitcnt vmcnt(3)\n"
      "buffer_store_dword v18, v4, s[8:11], s52 offen sc1\n"
      "v_add_f32 v169, v169, v178\n"
      "v_and_b32 v200, 0xffff0000, v200\n"
      "v_add_f32 v173, v173, v180\n"
      "v_add_f32 v166, v166, v233\n"
      "v_lshlrev_b32 v234, 16, v194\n"
      "v_and_b32 v192, 0xffff0000, v192\n"
      "v_add_f32 v175, v175, v181\n"
      "v_and_b32 v189, 0xffff0000, v189\n"
      "s_waitcnt vmcnt(3)\n"
      "v_lshlrev_b32 v232, 16, v204\n"
      "v_lshlrev_b32 v235, 16, v187\n"
      "v_add_f32 v172, v172, v232\n"
      "s_waitcnt vmcnt(1)\n"
      "v_lshlrev_b32 v232, 16, v208\n"
      "v_add_f32 v170, v170, v235\n"
      "v_and_b32 v194, 0xffff0000, v194\n"
      "v_and_b32 v208, 0xffff0000, v208\n"
      "v_add_f32 v167, v167, v185\n"
      "v_lshlrev_b32 v233, 16, v197\n"
      "v_lshlrev_b32 v235, 16, v195\n"
      "v_add_f32 v168, v168, v234\n"
      "v_add_f32 v164, v164, v232\n"
      "v_add_f32 v165, v165, v192\n"
      "v_lshlrev_b32 v234, 16, v202\n"
      "v_add_f32 v174, v174, v233\n"
      "v_lshlrev_b32 v232, 16, v212\n"
      "v_and_b32 v179, 0xffff0000, v179\n"
      "v_add_f32 v169, v169, v186\n"
      "v_lshlrev_b32 v233, 16, v201\n"
      "v_add_f32 v166, v166, v233\n"
      "v_and_b32 v193, 0xffff0000, v193\n"
      "v_and_b32 v197, 0xffff0000, v197\n"
      "v_lshlrev_b32 v233, 16, v205\n"
      "v_add_f32 v170, v170, v235\n"
      "v_add_f32 v174, v174, v233\n"
      "v_lshlrev_b32 v235, 16, v203\n"
      "v_and_b32 v187, 0xffff0000, v187\n"
      "v_lshlrev_b32 v233, 16, v209\n"
      "v_add_f32 v170, v170, v235\n"
      "v_add_f32 v166, v166, v233\n"
      "v_and_b32 v201, 0xffff0000, v201\n"
      "v_add_f32 v165, v165, v200\n"
      "v_and_b32 v188, 0xffff0000, v188\n"
      "v_add_f32 v165, v165, v208\n"
      "v_add_f32 v168, v168, v234\n"
      "v_add_f32 v167, v167, v193\n"
      "v_lshlrev_b32 v233, 16, v213\n"
      "v_add_f32 v171, v171, v179\n"
      "v_add_f32 v167, v167, v201\n"
      "v_lshlrev_b32 v234, 16, v210\n"
      "v_and_b32 v213, 0xffff0000, v213\n"
      "v_add_f32 v173, v173, v188\n"
      "v_and_b32 v209, 0xffff0000, v209\n"
      "v_add_f32 v175, v175, v189\n"
      "v_add_f32 v167, v167, v209\n"
      "v_and_b32 v203, 0xffff0000, v203\n"
      "v_add_f32 v175, v175, v197\n"
      "v_and_b32 v204, 0xffff0000, v204\n"
      "v_and_b32 v195, 0xffff0000, v195\n"
      "v_add_f32 v172, v172, v232\n"
      "v_add_f32 v169, v169, v194\n"
      "v_and_b32 v196, 0xffff0000, v196\n"
      "v_and_b32 v212, 0xffff0000, v212\n"
      "v_cvt_pk_bf16_f32 v177, v166, v167\n"
      "v_add_f32 v171, v171, v187\n"
      "v_and_b32 v205, 0xffff0000, v205\n"
      "s_add_u32 s52, s52, 106496\n"
      "v_and_b32 v202, 0xffff0000, v202\n"
      "v_add_f32 v169, v169, v202\n"
      "v_and_b32 v210, 0xffff0000, v210\n"
      "v_add_f32 v173, v173, v196\n"
      "v_lshlrev_b32 v235, 16, v211\n"
      "v_add_f32 v170, v170, v235\n"
      "v_add_f32 v171, v171, v195\n"
      "v_add_f32 v174, v174, v233\n"
      "v_add_f32 v169, v169, v210\n"
      "v_add_f32 v175, v175, v205\n"
      "v_add_f32 v168, v168, v234\n"
      "v_cvt_pk_bf16_f32 v176, v164, v165\n"
      "v_and_b32 v211, 0xffff0000, v211\n"
      "v_add_f32 v171, v171, v203\n"
      "v_cvt_pk_bf16_f32 v178, v168, v169\n"
      "v_add_f32 v175, v175, v213\n"
      "v_add_f32 v171, v171, v211\n"
      "v_add_f32 v173, v173, v204\n"
      "v_cvt_pk_bf16_f32 v179, v170, v171\n"
      "v_add_f32 v173, v173, v212\n"
      "v_cvt_pk_bf16_f32 v180, v172, v173\n"
      "v_cvt_pk_bf16_f32 v181, v174, v175\n"
      "buffer_store_dwordx2 v[180:181], v9, s[16:19], s57 offen\n"
      "buffer_store_dwordx4 v[176:179], v8, s[16:19], s57 offen\n"
      "s_branch .Ld_end\n"
      ".Ld_out4:\n"
      "v_and_b32 v192, 0xffff0000, v192\n"
      "s_nop 7\n"
      "v_cvt_pk_bf16_f32 v229, v162, v163\n"
      "v_add_f32 v175, v175, v181\n"
      "v_cvt_pk_bf16_f32 v228, v160, v161\n"
      "v_and_b32 v189, 0xffff0000, v189\n"
      "s_waitcnt vmcnt(11)\n"
      "v_lshlrev_b32 v232, 16, v204\n"
      "buffer_store_dwordx2 v[228:229], v13, s[8:11], s37 offen sc1\n"
      "v_lshlrev_b32 v235, 16, v187\n"
      "v_add_f32 v172, v172, v232\n"
      "s_waitcnt vmcnt(10)\n"
      "v_lshlrev_b32 v232, 16, v208\n"
      "v_add_f32 v170, v170, v235\n"
      "v_and_b32 v194, 0xffff0000, v194\n"
      "v_and_b32 v208, 0xffff0000, v208\n"
      "v_add_f32 v167, v167, v185\n"
      "v_lshlrev_b32 v233, 16, v197\n"
      "v_lshlrev_b32 v235, 16, v195\n"
      "v_add_f32 v168, v168, v234\n"
      "v_add_f32 v164, v164, v232\n"
      "v_add_f32 v165, v165, v192\n"
      "v_lshlrev_b32 v234, 16, v202\n"
      "v_add_f32 v174, v174, v233\n"
      "v_lshlrev_b32 v232, 16, v212\n"
      "v_and_b32 v179, 0xffff0000, v179\n"
      "v_add_f32 v169, v169, v186\n"
      "v_lshlrev_b32 v233, 16, v201\n"
      "v_add_f32 v166, v166, v233\n"
      "v_and_b32 v193, 0xffff0000, v193\n"
      "v_and_b32 v197, 0xffff0000, v197\n"
      "v_lshlrev_b32 v233, 16, v205\n"
      "v_add_f32 v170, v170, v235\n"
      "v_add_f32 v174, v174, v233\n"
      "v_lshlrev_b32 v235, 16, v203\n"
      "v_and_b32 v187, 0xffff0000, v187\n"
      "v_lshlrev_b32 v233, 16, v209\n"
      "v_add_f32 v170, v170, v235\n"
      "v_add_f32 v166, v166, v233\n"
      "v_and_b32 v201, 0xffff0000, v201\n"
      "v_add_f32 v165, v165, v200\n"
      "v_and_b32 v188, 0xffff0000, v188\n"
      "v_add_f32 v165, v165, v208\n"
      "v_add_f32 v168, v168, v234\n"
      "v_add_f32 v167, v167, v193\n"
      "v_lshlrev_b32 v233, 16, v213\n"
      "v_add_f32 v171, v171, v179\n"
      "v_add_f32 v167, v167, v201\n"
      "v_lshlrev_b32 v234, 16, v210\n"
      "v_and_b32 v213, 0xffff0000, v213\n"
      "v_add_f32 v173, v173, v188\n"
      "v_and_b32 v209, 0xffff0000, v209\n"
      "v_add_f32 v175, v175, v189\n"
      "v_add_f32 v167, v167, v209\n"
      "v_and_b32 v203, 0xffff0000, v203\n"
      "v_add_f32 v175, v175, v197\n"
      "v_and_b32 v204, 0xffff0000, v204\n"
      "v_and_b32 v195, 0xffff0000, v195\n"
      "v_add_f32 v172, v172, v232\n"
      "v_add_f32 v169, v169, v194\n"
      "v_and_b32 v196, 0xffff0000, v196\n"
      "v_and_b32 v212, 0xffff0000, v212\n"
      "v_cvt_pk_bf16_f32 v177, v166, v167\n"
      "v_add_f32 v171, v171, v187\n"
      "v_and_b32 v205, 0xffff0000, v205\n"
      "s_add_u32 s52, s52, 106496\n"
      "v_and_b32 v202, 0xffff0000, v202\n"
      "v_add_f32 v169, v169, v202\n"
      "v_and_b32 v210, 0xffff0000, v210\n"
      "v_add_f32 v173, v173, v196\n"
      "v_lshlrev_b32 v235, 16, v211\n"
      "v_add_f32 v170, v170, v235\n"
      "v_add_f32 v171, v171, v195\n"
      "s_add_u32 s37, s37, 106496\n"
      "s_waitcnt vmcnt(0)\n"
      "buffer_store_dword v18, v4, s[8:11], s52 offen sc1\n"
      "v_add_f32 v174, v174, v233\n"
      "v_add_f32 v169, v169, v210\n"
      "v_add_f32 v175, v175, v205\n"
      "v_add_f32 v168, v168, v234\n"
      "v_cvt_pk_bf16_f32 v176, v164, v165\n"
      "v_and_b32 v211, 0xffff0000, v211\n"
      "v_add_f32 v171, v171, v203\n"
      "v_cvt_pk_bf16_f32 v178, v168, v169\n"
      "v_add_f32 v175, v175, v213\n"
      "v_add_f32 v171, v171, v211\n"
      "v_add_f32 v173, v173, v204\n"
      "v_cvt_pk_bf16_f32 v179, v170, v171\n"
      "v_add_f32 v173, v173, v212\n"
      "v_cvt_pk_bf16_f32 v180, v172, v173\n"
      "v_cvt_pk_bf16_f32 v181, v174, v175\n"
      "buffer_store_dwordx2 v[180:181], v9, s[16:19], s57 offen\n"
      "buffer_store_dwordx4 v[176:179], v8, s[16:19], s57 offen\n"
      "s_add_u32 s52, s52, 106496\n"
      "s_branch .Ld_end\n"
      ".Ld_in1:\n"
      "s_add_u32 s36, s36, 165888\n"
      "s_min_u32 s36, s36, s6\n"
      "s_mov_b32 s51, s36\n"
      "s_add_u32 s36, s36, 165888\n"
      "s_min_u32 s36, s36, s6\n"
      "buffer_load_dword v20, v2, s[8:11], s36 offen sc1\n"
      "s_mov_b32 s51, s36\n"
      "s_add_u32 m0, s43, 24576\n"
      "s_add_u32 s36, s36, 165888\n"
      "s_waitcnt vmcnt(0)\n"
      "v_cmp_ne_u32 vcc, s21, v20\n"
      "s_cbranch_vccnz .Lsp_d1_20\n"
      ".Lbk_d1_20:\n"
      "buffer_load_dwordx4 v0, s[8:11], s51 offen offset:0 sc1 lds\n"
      "buffer_load_dwordx4 v0, s[8:11], s51 offen offset:1024 sc1 lds\n"
      "buffer_load_dwordx4 v0, s[8:11], s51 offen offset:2048 sc1 lds\n"
      "s_min_u32 s36, s36, s6\n"
      "buffer_load_dword v20, v2, s[8:11], s36 offen sc1\n"
      "buffer_load_dwordx4 v0, s[8:11], s51 offen offset:3072 sc1 lds\n"
      "s_add_u32 m0, s43, 28672\n"
      "s_nop 0\n"
      "buffer_load_dwordx4 v1, s[8:11], s51 offen offset:0 sc1 lds\n"
      "buffer_load_dwordx4 v1, s[8:11], s51 offen offset:1024 sc1 lds\n"
      "s_add_u32 m0, s43, 49152\n"
      "s_mov_b32 s51, s36\n"
      "v_add_u32 v14, 1, v14\n"
      "s_waitcnt vmcnt(3)\n"
      "v_cmp_ne_u32 vcc, s21, v20\n"
      "s_add_u32 s36, s36, 165888\n"
      "s_min_u32 s36, s36, s6\n"
      "s_cbranch_vccnz .Lsp_d2_21\n"
      ".Lbk_d2_21:\n"
      "s_waitcnt vmcnt(0)\n"
      "ds_write_b32 v16, v14\n"
      "buffer_load_dword v20, v2, s[8:11], s36 offen sc1\n"
      "buffer_load_dwordx4 v0, s[8:11], s51 offen offset:3072 sc1 lds\n"
      "s_add_u32 s52, s52, 106496\n"
      "s_add_u32 s37, s37, 173056\n"
      "buffer_load_dwordx4 v0, s[8:11], s51 offen offset:1024 sc1 lds\n"
      "ds_read_b32 v15, v17\n"
      "buffer_load_dwordx4 v0, s[8:11], s51 offen offset:2048 sc1 lds\n"
      "s_waitcnt lgkmcnt(0)\n"
      "v_cmp_gt_i32 vcc, v14, v15\n"
      "buffer_load_dwordx4 v0, s[8:11], s51 offen offset:0 sc1 lds\n"
      "s_add_u32 m0, s43, 53248\n"
      "s_cbranch_vccnz .Lsp_dy1_22\n"
      ".Lbk_dy1_22:\n"
      "buffer_load_dwordx4 v1, s[8:11], s51 offen offset:0 sc1 lds\n"
      "ds_read_b128 v[56:59], v0 offset:24576\n"
      "ds_read_b128 v[64:67], v0 offset:26624\n"
      "buffer_load_dwordx4 v1, s[8:11], s51 offen offset:1024 sc1 lds\n"
      "ds_read_b128 v[68:71], v0 offset:27648\n"
      "ds_read_b128 v[80:83], v0 offset:30720\n"
      "ds_read_b128 v[60:63], v0 offset:25600\n"
      "ds_read_b128 v[92:95], v0 offset:33792\n"
      "ds_read_b128 v[84:87], v0 offset:31744\n"
      "ds_read_b128 v[72:75], v0 offset:28672\n"
      "ds_read_b128 v[76:79], v0 offset:29696\n"
      "ds_read_b128 v[100:103], v0 offset:35840\n"
      "ds_read_b128 v[96:99], v0 offset:34816\n"
      "ds_read_b128 v[116:119], v0 offset:39936\n"
      "ds_read_b128 v[120:123], v0 offset:40960\n"
      "ds_read_b128 v[108:111], v0 offset:37888\n"
      "ds_read_b128 v[88:91], v0 offset:32768\n"
      "s_waitcnt vmcnt(0) lgkmcnt(0)\n"
      "s_nop 4\n"
      "s_branch .Ld_t1\n"
      ".Ld_in2:\n"
      "s_add_u32 s36, s36, 165888\n"
      "s_min_u32 s36, s36, s6\n"
      "s_mov_b32 s51, s36\n"
      "s_add_u32 s36, s36, 165888\n"
      "s_min_u32 s36, s36, s6\n"
      "s_mov_b32 s51, s36\n"
      "s_add_u32 s36, s36, 165888\n"
      "s_min_u32 s36, s36, s6\n"
      "buffer_load_dword v20, v2, s[8:11], s36 offen sc1\n"
      "s_add_u32 m0, s43, 49152\n"
      "s_mov_b32 s51, s36\n"
      "s_waitcnt vmcnt(0)\n"
      "v_cmp_ne_u32 vcc, s21, v20\n"
      "s_add_u32 s36, s36, 165888\n"
      "s_min_u32 s36, s36, s6\n"
      "s_cbranch_vccnz .Lsp_d2_23\n"
      ".Lbk_d2_23:\n"
      "buffer_load_dword v20, v2, s[8:11], s36 offen sc1\n"
      "buffer_load_dwordx4 v0, s[8:11], s51 offen offset:3072 sc1 lds\n"
      "s_add_u32 s52, s52, 106496\n"
      "s_add_u32 s37, s37, 173056\n"
      "buffer_load_dwordx4 v0, s[8:11], s51 offen offset:1024 sc1 lds\n"
      "buffer_load_dwordx4 v0, s[8:11], s51 offen offset:2048 sc1 lds\n"
      "buffer_load_dwordx4 v0, s[8:11], s51 offen offset:0 sc1 lds\n"
      "s_add_u32 m0, s43, 53248\n"
      "s_nop 0\n"
      "buffer_load_dwordx4 v1, s[8:11], s51 offen offset:0 sc1 lds\n"
      "buffer_load_dwordx4 v1, s[8:11], s51 offen offset:1024 sc1 lds\n"
      "s_mov_b32 s51, s36\n"
      "s_add_u32 s36, s36, 165888\n"
      "s_add_u32 m0, s43, 73728\n"
      "s_waitcnt vmcnt(6)\n"
      "v_cmp_ne_u32 vcc, s21, v20\n"
      "s_add_u32 s38, s38, 147456\n"
      "s_min_u32 s36, s36, s6\n"
      "s_cbranch_vccnz .Lsp_d3_24\n"
      ".Lbk_d3_24:\n"
      "buffer_load_dwordx4 v0, s[8:11], s51 offen offset:2048 sc1 lds\n"
      "v_add_u32 v14, 1, v14\n"
      "buffer_load_dwordx4 v0, s[8:11], s51 offen offset:1024 sc1 lds\n"
      "buffer_load_dword v20, v2, s[8:11], s36 offen sc1\n"
      "s_waitcnt vmcnt(3)\n"
      "ds_write_b32 v16, v14\n"
      "buffer_load_dwordx4 v0, s[8:11], s51 offen offset:0 sc1 lds\n"
      "buffer_load_dwordx4 v0, s[8:11], s51 offen offset:3072 sc1 lds\n"
      "s_add_u32 m0, s43, 77824\n"
      "ds_read_b32 v15, v17\n"
      "s_add_u32 s52, s52, 173056\n"
      "buffer_load_dwordx4 v1, s[8:11], s51 offen offset:0 sc1 lds\n"
      "s_waitcnt lgkmcnt(0)\n"
      "v_cmp_gt_i32 vcc, v14, v15\n"
      "buffer_load_dwordx4 v1, s[8:11], s51 offen offset:1024 sc1 lds\n"
      "s_cbranch_vccnz .Lsp_dy2_25\n"
      ".Lbk_dy2_25:\n"
      "ds_read_b128 v[68:71], v22 offset:3072\n"
      "ds_read_b128 v[60:63], v22 offset:1024\n"
      "ds_read_b128 v[80:83], v22 offset:6144\n"
      "ds_read_b128 v[72:75], v22 offset:4096\n"
      "ds_read_b128 v[76:79], v22 offset:5120\n"
      "ds_read_b128 v[96:99], v22 offset:10240\n"
      "ds_read_b128 v[84:87], v22 offset:7168\n"
      "ds_read_b128 v[56:59], v22 offset:0\n"
      "ds_read_b128 v[92:95], v22 offset:9216\n"
      "s_mov_b32 s51, s36\n"
      "ds_read_b128 v[64:67], v22 offset:2048\n"
      "ds_read_b128 v[100:103], v22 offset:11264\n"
      "ds_read_b128 v[132:135], v22 offset:19456\n"
      "ds_read_b128 v[112:115], v22 offset:14336\n"
      "s_add_u32 m0, s43, 98304\n"
      "s_waitcnt vmcnt(0) lgkmcnt(0)\n"
      "s_nop 4\n"
      "s_branch .Ld_t2\n"
      ".Ld_in3:\n"
      "s_add_u32 s36, s36, 165888\n"
      "s_min_u32 s36, s36, s6\n"
      "s_mov_b32 s51, s36\n"
      "s_add_u32 s36, s36, 165888\n"
      "s_min_u32 s36, s36, s6\n"
      "s_mov_b32 s51, s36\n"
      "s_add_u32 s36, s36, 165888\n"
      "s_min_u32 s36, s36, s6\n"
      "s_mov_b32 s51, s36\n"
      "s_add_u32 s36, s36, 165888\n"
      "s_min_u32 s36, s36, s6\n"
      "buffer_load_dword v20, v2, s[8:11], s36 offen sc1\n"
      "s_add_u32 s52, s52, 106496\n"
      "s_add_u32 s37, s37, 173056\n"
      "s_mov_b32 s51, s36\n"
      "s_add_u32 s36, s36, 165888\n"
      "s_add_u32 m0, s43, 73728\n"
      "s_waitcnt vmcnt(0)\n"
      "v_cmp_ne_u32 vcc, s21, v20\n"
      "s_add_u32 s38, s38, 147456\n"
      "s_min_u32 s36, s36, s6\n"
      "s_cbranch_vccnz .Lsp_d3_26\n"
      ".Lbk_d3_26:\n"
      "buffer_load_dwordx4 v0, s[8:11], s51 offen offset:2048 sc1 lds\n"
      "buffer_load_dwordx4 v0, s[8:11], s51 offen offset:1024 sc1 lds\n"
      "buffer_load_dword v20, v2, s[8:11], s36 offen sc1\n"
      "buffer_load_dwordx4 v0, s[8:11], s51 offen offset:0 sc1 lds\n"
      "buffer_load_dwordx4 v0, s[8:11], s51 offen offset:3072 sc1 lds\n"
      "s_add_u32 m0, s43, 77824\n"
      "s_add_u32 s52, s52, 173056\n"
      "buffer_load_dwordx4 v1, s[8:11], s51 offen offset:0 sc1 lds\n"
      "buffer_load_dwordx4 v1, s[8:11], s51 offen offset:1024 sc1 lds\n"
      "s_mov_b32 s51, s36\n"
      "s_add_u32 m0, s43, 98304\n"
      "s_add_u32 s36, s36, 165888\n"
      "v_add_u32 v14, 1, v14\n"
      "s_waitcnt vmcnt(4)\n"
      "v_cmp_ne_u32 vcc, s21, v20\n"
      "s_cbranch_vccnz .Lsp_d4_27\n"
      ".Lbk_d4_27:\n"
      "s_min_u32 s36, s36, s6\n"
      "buffer_load_dwordx4 v0, s[8:11], s51 offen offset:0 sc1 lds\n"
      "buffer_load_dwordx4 v0, s[8:11], s51 offen offset:2048 sc1 lds\n"
      "s_waitcnt vmcnt(2)\n"
      "ds_write_b32 v16, v14\n"
      "buffer_load_dword v20, v2, s[8:11], s36 offen sc1\n"
      "buffer_load_dwordx4 v0, s[8:11], s51 offen offset:3072 sc1 lds\n"
      "ds_read_b32 v15, v17\n"
      "buffer_load_dwordx4 v0, s[8:11], s51 offen offset:1024 sc1 lds\n"
      "s_add_u32 m0, s43, 102400\n"
      "s_nop 0\n"
      "buffer_load_dwordx4 v1, s[8:11], s51 offen offset:1024 sc1 lds\n"
      "s_waitcnt lgkmcnt(0)\n"
      "v_cmp_gt_i32 vcc, v14, v15\n"
      "buffer_load_dwordx4 v1, s[8:11], s51 offen offset:0 sc1 lds\n"
      "s_cbranch_vccnz .Lsp_dy3_28\n"
      ".Lbk_dy3_28:\n"
      "ds_read_b128 v[60:63], v22 offset:25600\n"
      "ds_read_b128 v[64:67], v22 offset:26624\n"
      "s_mov_b32 s51, s36\n"
      "ds_read_b128 v[72:75], v22 offset:28672\n"
      "ds_read_b128 v[68:71], v22 offset:27648\n"
      "ds_read_b128 v[92:95], v22 offset:33792\n"
      "ds_read_b128 v[56:59], v22 offset:24576\n"
      "ds_read_b128 v[100:103], v22 offset:35840\n"
      "ds_read_b128 v[80:83], v22 offset:30720\n"
      "ds_read_b128 v[112:115], v22 offset:38912\n"
      "s_add_u32 s37, s37, 106496\n"
      "ds_read_b128 v[88:91], v22 offset:32768\n"
      "ds_read_b128 v[96:99], v22 offset:34816\n"
      "ds_read_b128 v[76:79], v22 offset:29696\n"
      "ds_read_b128 v[116:119], v22 offset:39936\n"
      "ds_read_b128 v[84:87], v22 offset:31744\n"
      "ds_read_b128 v[104:107], v22 offset:36864\n"
      "s_waitcnt vmcnt(0) lgkmcnt(0)\n"
      "s_nop 4\n"
      "s_branch .Ld_t3\n"
      ".Ld_in4:\n"
      "s_add_u32 s36, s36, 165888\n"
      "s_min_u32 s36, s36, s6\n"
      "s_mov_b32 s51, s36\n"
      "s_add_u32 s36, s36, 165888\n"
      "s_min_u32 s36, s36, s6\n"
      "s_mov_b32 s51, s36\n"
      "s_add_u32 s36, s36, 165888\n"
      "s_min_u32 s36, s36, s6\n"
      "s_mov_b32 s51, s36\n"
      "s_add_u32 s36, s36, 165888\n"
      "s_min_u32 s36, s36, s6\n"
      "s_add_u32 s52, s52, 106496\n"
      "s_add_u32 s37, s37, 173056\n"
      "s_mov_b32 s51, s36\n"
      "s_add_u32 s36, s36, 165888\n"
      "s_add_u32 s38, s38, 147456\n"
      "s_min_u32 s36, s36, s6\n"
      "buffer_load_dword v20, v2, s[8:11], s36 offen sc1\n"
      "s_add_u32 s52, s52, 173056\n"
      "s_mov_b32 s51, s36\n"
      "s_add_u32 m0, s43, 98304\n"
      "s_add_u32 s36, s36, 165888\n"
      "s_waitcnt vmcnt(0)\n"
      "v_cmp_ne_u32 vcc, s21, v20\n"
      "s_cbranch_vccnz .Lsp_d4_29\n"
      ".Lbk_d4_29:\n"
      "s_min_u32 s36, s36, s6\n"
      "buffer_load_dwordx4 v0, s[8:11], s51 offen offset:0 sc1 lds\n"
      "buffer_load_dwordx4 v0, s[8:11], s51 offen offset:2048 sc1 lds\n"
      "buffer_load_dword v20, v2, s[8:11], s36 offen sc1\n"
      "buffer_load_dwordx4 v0, s[8:11], s51 offen offset:3072 sc1 lds\n"
      "buffer_load_dwordx4 v0, s[8:11], s51 offen offset:1024 sc1 lds\n"
      "s_add_u32 m0, s43, 102400\n"
      "s_nop 0\n"
      "buffer_load_dwordx4 v1, s[8:11], s51 offen offset:1024 sc1 lds\n"
      "buffer_load_dwordx4 v1, s[8:11], s51 offen offset:0 sc1 lds\n"
      "s_mov_b32 s51, s36\n"
      "s_add_u32 s37, s37, 106496\n"
      "s_add_u32 m0, s43, 122880\n"
      "s_waitcnt vmcnt(4)\n"
      "v_cmp_ne_u32 vcc, s21, v20\n"
      "s_add_u32 s36, s36, 165888\n"
      "s_add_u32 s52, s52, 106496\n"
      "s_cbranch_vccnz .Lsp_d5_30\n"
      ".Lbk_d5_30:\n"
      "buffer_load_dwordx4 v0, s[8:11], s51 offen offset:1024 sc1 lds\n"
      "v_add_u32 v14, 1, v14\n"
      "buffer_load_dwordx4 v0, s[8:11], s51 offen offset:0 sc1 lds\n"
      "buffer_load_dwordx4 v0, s[8:11], s51 offen offset:2048 sc1 lds\n"
      "s_waitcnt vmcnt(3)\n"
      "ds_write_b32 v16, v14\n"
      "buffer_load_dwordx4 v0, s[8:11], s51 offen offset:3072 sc1 lds\n"
      "s_min_u32 s36, s36, s6\n"
      "s_add_u32 m0, s43, 126976\n"
      "s_nop 0\n"
      "buffer_load_dwordx4 v1, s[8:11], s51 offen offset:1024 sc1 lds\n"
      "ds_read_b32 v15, v17\n"
      "buffer_load_dword v20, v2, s[8:11], s36 offen sc1\n"
      "s_waitcnt lgkmcnt(0)\n"
      "v_cmp_gt_i32 vcc, v14, v15\n"
      "buffer_load_dwordx4 v1, s[8:11], s51 offen offset:0 sc1 lds\n"
      "s_cbranch_vccnz .Lsp_dy4_31\n"
      ".Lbk_dy4_31:\n"
      "ds_read_b128 v[60:63], v23 offset:1024\n"
      "ds_read_b128 v[64:67], v23 offset:2048\n"
      "ds_read_b128 v[72:75], v23 offset:4096\n"
      "ds_read_b128 v[68:71], v23 offset:3072\n"
      "s_add_u32 s58, s41, 4294967264\n"
      "ds_read_b128 v[80:83], v23 offset:6144\n"
      "ds_read_b128 v[84:87], v23 offset:7168\n"
      "ds_read_b128 v[76:79], v23 offset:5120\n"
      "s_add_u32 s37, s37, 106496\n"
      "ds_read_b128 v[56:59], v23 offset:0\n"
      "ds_read_b128 v[88:91], v23 offset:8192\n"
      "ds_read_b128 v[96:99], v23 offset:10240\n"
      "ds_read_b128 v[100:103], v23 offset:11264\n"
      "ds_read_b128 v[132:135], v23 offset:19456\n"
      "ds_read_b128 v[104:107], v23 offset:12288\n"
      "s_waitcnt vmcnt(0) lgkmcnt(0)\n"
      "s_nop 4\n"
      "s_branch .Ld_t4\n"
      ".Ld_in5:\n"
      "s_add_u32 s36, s36, 165888\n"
      "s_min_u32 s36, s36, s6\n"
      "s_mov_b32 s51, s36\n"
      "s_add_u32 s36, s36, 165888\n"
      "s_min_u32 s36, s36, s6\n"
      "s_mov_b32 s51, s36\n"
      "s_add_u32 s36, s36, 165888\n"
      "s_min_u32 s36, s36, s6\n"
      "s_mov_b32 s51, s36\n"
      "s_add_u32 s36, s36, 165888\n"
      "s_min_u32 s36, s36, s6\n"
      "s_add_u32 s52, s52, 106496\n"
      "s_add_u32 s37, s37, 173056\n"
      "s_mov_b32 s51, s36\n"
      "s_add_u32 s36, s36, 165888\n"
      "s_add_u32 s38, s38, 147456\n"
      "s_min_u32 s36, s36, s6\n"
      "s_add_u32 s52, s52, 173056\n"
      "s_mov_b32 s51, s36\n"
      "s_add_u32 s36, s36, 165888\n"
      "s_min_u32 s36, s36, s6\n"
      "buffer_load_dword v20, v2, s[8:11], s36 offen sc1\n"
      "s_mov_b32 s51, s36\n"
      "s_add_u32 s37, s37, 106496\n"
      "s_add_u32 m0, s43, 122880\n"
      "s_waitcnt vmcnt(0)\n"
      "v_cmp_ne_u32 vcc, s21, v20\n"
      "s_add_u32 s36, s36, 165888\n"
      "s_add_u32 s52, s52, 106496\n"
      "s_cbranch_vccnz .Lsp_d5_32\n"
      ".Lbk_d5_32:\n"
      "buffer_load_dwordx4 v0, s[8:11], s51 offen offset:1024 sc1 lds\n"
      "buffer_load_dwordx4 v0, s[8:11], s51 offen offset:0 sc1 lds\n"
      "buffer_load_dwordx4 v0, s[8:11], s51 offen offset:2048 sc1 lds\n"
      "buffer_load_dwordx4 v0, s[8:11], s51 offen offset:3072 sc1 lds\n"
      "s_min_u32 s36, s36, s6\n"
      "s_add_u32 m0, s43, 126976\n"
      "s_nop 0\n"
      "buffer_load_dwordx4 v1, s[8:11], s51 offen offset:1024 sc1 lds\n"
      "buffer_load_dword v20, v2, s[8:11], s36 offen sc1\n"
      "buffer_load_dwordx4 v1, s[8:11], s51 offen offset:0 sc1 lds\n"
      "s_add_u32 s58, s41, 4294967264\n"
      "s_add_u32 s37, s37, 106496\n"
      "s_add_u32 m0, s43, 0\n"
      "s_mov_b32 s51, s36\n"
      "v_add_u32 v14, 1, v14\n"
      "s_add_u32 s36, s36, 165888\n"
      "s_add_u32 s37, s37, 106496\n"
      "s_min_u32 s36, s36, s6\n"
      "s_cmp_lt_u32 s41, 32\n"
      "s_cselect_b32 s56, s39, 0x40000000\n"
      "s_add_u32 s53, s53, 599040\n"
      "s_waitcnt vmcnt(1)\n"
      "v_cmp_ne_u32 vcc, s21, v20\n"
      "s_add_u32 s39, s39, 147456\n"
      "s_waitcnt vmcnt(0)\n"
      "ds_write_b32 v16, v14\n"
      "s_cmp_lt_u32 s58, 32\n"
      "s_cselect_b32 s57, s40, 0x40000000\n"
      "s_cbranch_vccnz .Lsp_d0_33\n"
      ".Lbk_d0_33:\n"
      "buffer_load_dwordx4 v0, s[8:11], s51 offen offset:0 sc1 lds\n"
      "s_add_u32 s52, s52, 106496\n"
      "buffer_load_dwordx4 v0, s[8:11], s51 offen offset:1024 sc1 lds\n"
      "buffer_load_dword v20, v2, s[8:11], s36 offen sc1\n"
      "ds_read_b32 v15, v17\n"
      "buffer_load_dwordx4 v0, s[8:11], s51 offen offset:2048 sc1 lds\n"
      "s_add_u32 s40, s40, 4294856704\n"
      "buffer_load_dwordx4 v0, s[8:11], s51 offen offset:3072 sc1 lds\n"
      "s_waitcnt lgkmcnt(0)\n"
      "v_cmp_gt_i32 vcc, v14, v15\n"
      "s_add_u32 m0, s43, 4096\n"
      "s_nop 0\n"
      "buffer_load_dwordx4 v1, s[8:11], s51 offen offset:1024 sc1 lds\n"
      "s_cbranch_vccnz .Lsp_dy5_34\n"
      ".Lbk_dy5_34:\n"
      "ds_read_b128 v[56:59], v23 offset:24576\n"
      "ds_read_b128 v[64:67], v23 offset:26624\n"
      "buffer_load_dwordx4 v1, s[8:11], s51 offen offset:0 sc1 lds\n"
      "ds_read_b128 v[76:79], v23 offset:29696\n"
      "s_add_u32 s41, s41, 6\n"
      "ds_read_b128 v[88:91], v23 offset:32768\n"
      "ds_read_b128 v[68:71], v23 offset:27648\n"
      "ds_read_b128 v[96:99], v23 offset:34816\n"
      "ds_read_b128 v[80:83], v23 offset:30720\n"
      "ds_read_b128 v[72:75], v23 offset:28672\n"
      "s_mov_b32 s51, s36\n"
      "ds_read_b128 v[112:115], v23 offset:38912\n"
      "ds_read_b128 v[60:63], v23 offset:25600\n"
      "ds_read_b128 v[104:107], v23 offset:36864\n"
      "ds_read_b128 v[84:87], v23 offset:31744\n"
      "ds_read_b128 v[140:143], v23 offset:46080\n"
      "ds_read_b128 v[100:103], v23 offset:35840\n"
      "s_waitcnt vmcnt(0) lgkmcnt(0)\n"
      "s_nop 4\n"
      "s_branch .Ld_t5\n"
      ".Ld_end:\n"
      "s_endpgm\n"
      ".Lsp_d0_1:\n"
      "s_mov_b32 s22, s23\n"
      ".Lsl_d0_1:\n"
      "buffer_load_dword v20, v2, s[8:11], s51 offen sc1\n"
      "s_waitcnt vmcnt(0)\n"
      "v_cmp_ne_u32 vcc, s21, v20\n"
      "s_cbranch_vccz .Lbk_d0_1\n"
      "s_sleep 4\n"
      "s_sub_u32 s22, s22, 1\n"
      "s_cbranch_scc0 .Lsl_d0_1\n"
      "s_mov_b32 s23, 0\n"
      "s_branch .Lbk_d0_1\n"
      ".Lsp_d1_2:\n"
      "s_mov_b32 s22, s23\n"
      ".Lsl_d1_2:\n"
      "buffer_load_dword v20, v2, s[8:11], s51 offen sc1\n"
      "s_waitcnt vmcnt(0)\n"
      "v_cmp_ne_u32 vcc, s21, v20\n"
      "s_cbranch_vccz .Lbk_d1_2\n"
      "s_sleep 4\n"
      "s_sub_u32 s22, s22, 1\n"
      "s_cbranch_scc0 .Lsl_d1_2\n"
      "s_mov_b32 s23, 0\n"
      "s_branch .Lbk_d1_2\n"
      ".Lsp_dy0_3:\n"
      "s_lshl_b32 s22, s23, 4\n"
      ".Lsl_dy0_3:\n"
      "ds_read_b32 v15, v17\n"
      "s_waitcnt lgkmcnt(0)\n"
      "v_cmp_gt_i32 vcc, v14, v15\n"
      "s_cbranch_vccz .Lbk_dy0_3\n"
      "s_sub_u32 s22, s22, 1\n"
      "s_cbranch_scc0 .Lsl_dy0_3\n"
      "s_mov_b32 s23, 0\n"
      "s_branch .Lbk_dy0_3\n"
      ".Lsp_d2_4:\n"
      "s_mov_b32 s22, s23\n"
      "buffer_load_dword v20, v2, s[8:11], s51 offen sc1\n"
      "s_waitcnt vmcnt(0)\n"
      "v_cmp_ne_u32 vcc, s21, v20\n"
      "s_add_u32 s30, s52, 106496\n"
      "buffer_store_dword v18, v4, s[8:11], s30 offen sc1\n"
      "s_cbranch_vccz .Lbk_d2_4\n"
      ".Lsl_d2_4:\n"
      "buffer_load_dword v20, v2, s[8:11], s51 offen sc1\n"
      "s_waitcnt vmcnt(0)\n"
      "v_cmp_ne_u32 vcc, s21, v20\n"
      "s_cbranch_vccz .Lbk_d2_4\n"
      "s_sleep 4\n"
      "s_sub_u32 s22, s22, 1\n"
      "s_cbranch_scc0 .Lsl_d2_4\n"
      "s_mov_b32 s23, 0\n"
      "s_branch .Lbk_d2_4\n"
      ".Lsp_dy1_5:\n"
      "s_lshl_b32 s22, s23, 4\n"
      ".Lsl_dy1_5:\n"
      "ds_read_b32 v15, v17\n"
      "s_waitcnt lgkmcnt(0)\n"
      "v_cmp_gt_i32 vcc, v14, v15\n"
      "s_cbranch_vccz .Lbk_dy1_5\n"
      "s_sub_u32 s22, s22, 1\n"
      "s_cbranch_scc0 .Lsl_dy1_5\n"
      "s_mov_b32 s23, 0\n"
      "s_branch .Lbk_dy1_5\n"
      ".Lsp_d3_6:\n"
      "s_mov_b32 s22, s23\n"
      "buffer_load_dword v20, v2, s[8:11], s51 offen sc1\n"
      "s_waitcnt vmcnt(0)\n"
      "v_cmp_ne_u32 vcc, s21, v20\n"
      "buffer_store_dword v18, v5, s[8:11], s56 offen sc1\n"
      "s_cbranch_vccz .Lbk_d3_6\n"
      ".Lsl_d3_6:\n"
      "buffer_load_dword v20, v2, s[8:11], s51 offen sc1\n"
      "s_waitcnt vmcnt(0)\n"
      "v_cmp_ne_u32 vcc, s21, v20\n"
      "s_cbranch_vccz .Lbk_d3_6\n"
      "s_sleep 4\n"
      "s_sub_u32 s22, s22, 1\n"
      "s_cbranch_scc0 .Lsl_d3_6\n"
      "s_mov_b32 s23, 0\n"
      "s_branch .Lbk_d3_6\n"
      ".Lsp_dy2_7:\n"
      "s_lshl_b32 s22, s23, 4\n"
      ".Lsl_dy2_7:\n"
      "ds_read_b32 v15, v17\n"
      "s_waitcnt lgkmcnt(0)\n"
      "v_cmp_gt_i32 vcc, v14, v15\n"
      "s_cbranch_vccz .Lbk_dy2_7\n"
      "s_sub_u32 s22, s22, 1\n"
      "s_cbranch_scc0 .Lsl_dy2_7\n"
      "s_mov_b32 s23, 0\n"
      "s_branch .Lbk_dy2_7\n"
      ".Lsp_d4_8:\n"
      "s_mov_b32 s22, s23\n"
      "buffer_load_dword v20, v2, s[8:11], s51 offen sc1\n"
      "s_waitcnt vmcnt(0)\n"
      "v_cmp_ne_u32 vcc, s21, v20\n"
      "s_add_u32 s30, s52, 0\n"
      "buffer_store_dword v18, v4, s[8:11], s30 offen sc1\n"
      "s_cbranch_vccz .Lbk_d4_8\n"
      ".Lsl_d4_8:\n"
      "buffer_load_dword v20, v2, s[8:11], s51 offen sc1\n"
      "s_waitcnt vmcnt(0)\n"
      "v_cmp_ne_u32 vcc, s21, v20\n"
      "s_cbranch_vccz .Lbk_d4_8\n"
      "s_sleep 4\n"
      "s_sub_u32 s22, s22, 1\n"
      "s_cbranch_scc0 .Lsl_d4_8\n"
      "s_mov_b32 s23, 0\n"
      "s_branch .Lbk_d4_8\n"
      ".Lsp_dy3_9:\n"
      "s_lshl_b32 s22, s23, 4\n"
      ".Lsl_dy3_9:\n"
      "ds_read_b32 v15, v17\n"
      "s_waitcnt lgkmcnt(0)\n"
      "v_cmp_gt_i32 vcc, v14, v15\n"
      "s_cbranch_vccz .Lbk_dy3_9\n"
      "s_sub_u32 s22, s22, 1\n"
      "s_cbranch_scc0 .Lsl_dy3_9\n"
      "s_mov_b32 s23, 0\n"
      "s_branch .Lbk_dy3_9\n"
      ".Lsp_d5_10:\n"
      "s_mov_b32 s22, s23\n"
      "buffer_load_dword v20, v2, s[8:11], s51 offen sc1\n"
      "s_waitcnt vmcnt(0)\n"
      "v_cmp_ne_u32 vcc, s21, v20\n"
      "s_add_u32 s30, s52, 0\n"
      "buffer_store_dword v18, v4, s[8:11], s30 offen sc1\n"
      "s_cbranch_vccz .Lbk_d5_10\n"
      ".Lsl_d5_10:\n"
      "buffer_load_dword v20, v2, s[8:11], s51 offen sc1\n"
      "s_waitcnt vmcnt(0)\n"
      "v_cmp_ne_u32 vcc, s21, v20\n"
      "s_cbranch_vccz .Lbk_d5_10\n"
      "s_sleep 4\n"
      "s_sub_u32 s22, s22, 1\n"
      "s_cbranch_scc0 .Lsl_d5_10\n"
      "s_mov_b32 s23, 0\n"
      "s_branch .Lbk_d5_10\n"
      ".Lsp_dy4_11:\n"
      "s_lshl_b32 s22, s23, 4\n"
      ".Lsl_dy4_11:\n"
      "ds_read_b32 v15, v17\n"
      "s_waitcnt lgkmcnt(0)\n"
      "v_cmp_gt_i32 vcc, v14, v15\n"
      "s_cbranch_vccz .Lbk_dy4_11\n"
      "s_sub_u32 s22, s22, 1\n"
      "s_cbranch_scc0 .Lsl_dy4_11\n"
      "s_mov_b32 s23, 0\n"
      "s_branch .Lbk_dy4_11\n"
      ".Lsp_df_12:\n"
      "s_mov_b32 s22, s23\n"
      "buffer_load_dword v21, v3, s[8:11], s53 offen sc1\n"
      "s_waitcnt vmcnt(0)\n"
      "v_cmp_ne_u32 vcc, s21, v21\n"
      "s_add_u32 s30, s52, 0\n"
      "buffer_store_dword v18, v4, s[8:11], s30 offen sc1\n"
      "s_cbranch_vccz .Lbk_df_12\n"
      ".Lsl_df_12:\n"
      "buffer_load_dword v21, v3, s[8:11], s53 offen sc1\n"
      "s_waitcnt vmcnt(0)\n"
      "v_cmp_ne_u32 vcc, s21, v21\n"
      "s_cbranch_vccz .Lbk_df_12\n"
      "s_sleep 4\n"
      "s_sub_u32 s22, s22, 1\n"
      "s_cbranch_scc0 .Lsl_df_12\n"
      "s_mov_b32 s23, 0\n"
      "s_branch .Lbk_df_12\n"
      ".Lsp_d0_13:\n"
      "s_mov_b32 s22, s23\n"
      "buffer_load_dword v20, v2, s[8:11], s51 offen sc1\n"
      "s_waitcnt vmcnt(0)\n"
      "v_cmp_ne_u32 vcc, s21, v20\n"
      "s_add_u32 s30, s52, 106496\n"
      "buffer_store_dword v18, v4, s[8:11], s30 offen sc1\n"
      "s_cbranch_vccz .Lbk_d0_13\n"
      ".Lsl_d0_13:\n"
      "buffer_load_dword v20, v2, s[8:11], s51 offen sc1\n"
      "s_waitcnt vmcnt(0)\n"
      "v_cmp_ne_u32 vcc, s21, v20\n"
      "s_cbranch_vccz .Lbk_d0_13\n"
      "s_sleep 4\n"
      "s_sub_u32 s22, s22, 1\n"
      "s_cbranch_scc0 .Lsl_d0_13\n"
      "s_mov_b32 s23, 0\n"
      "s_branch .Lbk_d0_13\n"
      ".Lsp_dy5_14:\n"
      "s_lshl_b32 s22, s23, 4\n"
      ".Lsl_dy5_14:\n"
      "ds_read_b32 v15, v17\n"
      "s_waitcnt lgkmcnt(0)\n"
      "v_cmp_gt_i32 vcc, v14, v15\n"
      "s_cbranch_vccz .Lbk_dy5_14\n"
      "s_sub_u32 s22, s22, 1\n"
      "s_cbranch_scc0 .Lsl_dy5_14\n"
      "s_mov_b32 s23, 0\n"
      "s_branch .Lbk_dy5_14\n"
      ".Lsp_d1_15:\n"
      "s_mov_b32 s22, s23\n"
      "buffer_load_dword v20, v2, s[8:11], s51 offen sc1\n"
      "s_waitcnt vmcnt(0)\n"
      "v_cmp_ne_u32 vcc, s21, v20\n"
      "s_add_u32 s30, s52, 106496\n"
      "buffer_store_dword v18, v4, s[8:11], s30 offen sc1\n"
      "s_cbranch_vccz .Lbk_d1_15\n"
      ".Lsl_d1_15:\n"
      "buffer_load_dword v20, v2, s[8:11], s51 offen sc1\n"
      "s_waitcnt vmcnt(0)\n"
      "v_cmp_ne_u32 vcc, s21, v20\n"
      "s_cbranch_vccz .Lbk_d1_15\n"
      "s_sleep 4\n"
      "s_sub_u32 s22, s22, 1\n"
      "s_cbranch_scc0 .Lsl_d1_15\n"
      "s_mov_b32 s23, 0\n"
      "s_branch .Lbk_d1_15\n"
      ".Lsp_dy0_16:\n"
      "s_lshl_b32 s22, s23, 4\n"
      ".Lsl_dy0_16:\n"
      "ds_read_b32 v15, v17\n"
      "s_waitcnt lgkmcnt(0)\n"
      "v_cmp_gt_i32 vcc, v14, v15\n"
      "s_cbranch_vccz .Lbk_dy0_16\n"
      "s_sub_u32 s22, s22, 1\n"
      "s_cbranch_scc0 .Lsl_dy0_16\n"
      "s_mov_b32 s23, 0\n"
      "s_branch .Lbk_dy0_16\n"
      ".Lsp_df_17:\n"
      "s_mov_b32 s22, s23\n"
      "buffer_load_dword v21, v3, s[8:11], s53 offen sc1\n"
      "s_waitcnt vmcnt(0)\n"
      "v_cmp_ne_u32 vcc, s21, v21\n"
      "s_add_u32 s30, s52, 0\n"
      "buffer_store_dword v18, v4, s[8:11], s30 offen sc1\n"
      "s_cbranch_vccz .Lbk_df_17\n"
      ".Lsl_df_17:\n"
      "buffer_load_dword v21, v3, s[8:11], s53 offen sc1\n"
      "s_waitcnt vmcnt(0)\n"
      "v_cmp_ne_u32 vcc, s21, v21\n"
      "s_cbranch_vccz .Lbk_df_17\n"
      "s_sleep 4\n"
      "s_sub_u32 s22, s22, 1\n"
      "s_cbranch_scc0 .Lsl_df_17\n"
      "s_mov_b32 s23, 0\n"
      "s_branch .Lbk_df_17\n"
      ".Lsp_df_18:\n"
      "s_mov_b32 s22, s23\n"
      "buffer_load_dword v21, v3, s[8:11], s53 offen sc1\n"
      "s_waitcnt vmcnt(0)\n"
      "v_cmp_ne_u32 vcc, s21, v21\n"
      "s_add_u32 s30, s52, 0\n"
      "buffer_store_dword v18, v4, s[8:11], s30 offen sc1\n"
      "s_cbranch_vccz .Lbk_df_18\n"
      ".Lsl_df_18:\n"
      "buffer_load_dword v21, v3, s[8:11], s53 offen sc1\n"
      "s_waitcnt vmcnt(0)\n"
      "v_cmp_ne_u32 vcc, s21, v21\n"
      "s_cbranch_vccz .Lbk_df_18\n"
      "s_sleep 4\n"
      "s_sub_u32 s22, s22, 1\n"
      "s_cbranch_scc0 .Lsl_df_18\n"
      "s_mov_b32 s23, 0\n"
      "s_branch .Lbk_df_18\n"
      ".Lsp_df_19:\n"
      "s_mov_b32 s22, s23\n"
      "buffer_load_dword v21, v3, s[8:11], s53 offen sc1\n"
      "s_waitcnt vmcnt(0)\n"
      "v_cmp_ne_u32 vcc, s21, v21\n"
      "s_add_u32 s30, s52, 0\n"
      "buffer_store_dword v18, v4, s[8:11], s30 offen sc1\n"
      "s_cbranch_vccz .Lbk_df_19\n"
      ".Lsl_df_19:\n"
      "buffer_load_dword v21, v3, s[8:11], s53 offen sc1\n"
      "s_waitcnt vmcnt(0)\n"
      "v_cmp_ne_u32 vcc, s21, v21\n"
      "s_cbranch_vccz .Lbk_df_19\n"
      "s_sleep 4\n"
      "s_sub_u32 s22, s22, 1\n"
      "s_cbranch_scc0 .Lsl_df_19\n"
      "s_mov_b32 s23, 0\n"
      "s_branch .Lbk_df_19\n"
      ".Lsp_d1_20:\n"
      "s_mov_b32 s22, s23\n"
      ".Lsl_d1_20:\n"
      "buffer_load_dword v20, v2, s[8:11], s51 offen sc1\n"
      "s_waitcnt vmcnt(0)\n"
      "v_cmp_ne_u32 vcc, s21, v20\n"
      "s_cbranch_vccz .Lbk_d1_20\n"
      "s_sleep 4\n"
      "s_sub_u32 s22, s22, 1\n"
      "s_cbranch_scc0 .Lsl_d1_20\n"
      "s_mov_b32 s23, 0\n"
      "s_branch .Lbk_d1_20\n"
      ".Lsp_d2_21:\n"
      "s_mov_b32 s22, s23\n"
      ".Lsl_d2_21:\n"
      "buffer_load_dword v20, v2, s[8:11], s51 offen sc1\n"
      "s_waitcnt vmcnt(0)\n"
      "v_cmp_ne_u32 vcc, s21, v20\n"
      "s_cbranch_vccz .Lbk_d2_21\n"
      "s_sleep 4\n"
      "s_sub_u32 s22, s22, 1\n"
      "s_cbranch_scc0 .Lsl_d2_21\n"
      "s_mov_b32 s23, 0\n"
      "s_branch .Lbk_d2_21\n"
      ".Lsp_dy1_22:\n"
      "s_lshl_b32 s22, s23, 4\n"
      ".Lsl_dy1_22:\n"
      "ds_read_b32 v15, v17\n"
      "s_waitcnt lgkmcnt(0)\n"
      "v_cmp_gt_i32 vcc, v14, v15\n"
      "s_cbranch_vccz .Lbk_dy1_22\n"
      "s_sub_u32 s22, s22, 1\n"
      "s_cbranch_scc0 .Lsl_dy1_22\n"
      "s_mov_b32 s23, 0\n"
      "s_branch .Lbk_dy1_22\n"
      ".Lsp_d2_23:\n"
      "s_mov_b32 s22, s23\n"
      ".Lsl_d2_23:\n"
      "buffer_load_dword v20, v2, s[8:11], s51 offen sc1\n"
      "s_waitcnt vmcnt(0)\n"
      "v_cmp_ne_u32 vcc, s21, v20\n"
      "s_cbranch_vccz .Lbk_d2_23\n"
      "s_sleep 4\n"
      "s_sub_u32 s22, s22, 1\n"
      "s_cbranch_scc0 .Lsl_d2_23\n"
      "s_mov_b32 s23, 0\n"
      "s_branch .Lbk_d2_23\n"
      ".Lsp_d3_24:\n"
      "s_mov_b32 s22, s23\n"
      ".Lsl_d3_24:\n"
      "buffer_load_dword v20, v2, s[8:11], s51 offen sc1\n"
      "s_waitcnt vmcnt(0)\n"
      "v_cmp_ne_u32 vcc, s21, v20\n"
      "s_cbranch_vccz .Lbk_d3_24\n"
      "s_sleep 4\n"
      "s_sub_u32 s22, s22, 1\n"
      "s_cbranch_scc0 .Lsl_d3_24\n"
      "s_mov_b32 s23, 0\n"
      "s_branch .Lbk_d3_24\n"
      ".Lsp_dy2_25:\n"
      "s_lshl_b32 s22, s23, 4\n"
      ".Lsl_dy2_25:\n"
      "ds_read_b32 v15, v17\n"
      "s_waitcnt lgkmcnt(0)\n"
      "v_cmp_gt_i32 vcc, v14, v15\n"
      "s_cbranch_vccz .Lbk_dy2_25\n"
      "s_sub_u32 s22, s22, 1\n"
      "s_cbranch_scc0 .Lsl_dy2_25\n"
      "s_mov_b32 s23, 0\n"
      "s_branch .Lbk_dy2_25\n"
      ".Lsp_d3_26:\n"
      "s_mov_b32 s22, s23\n"
      ".Lsl_d3_26:\n"
      "buffer_load_dword v20, v2, s[8:11], s51 offen sc1\n"
      "s_waitcnt vmcnt(0)\n"
      "v_cmp_ne_u32 vcc, s21, v20\n"
      "s_cbranch_vccz .Lbk_d3_26\n"
      "s_sleep 4\n"
      "s_sub_u32 s22, s22, 1\n"
      "s_cbranch_scc0 .Lsl_d3_26\n"
      "s_mov_b32 s23, 0\n"
      "s_branch .Lbk_d3_26\n"
      ".Lsp_d4_27:\n"
      "s_mov_b32 s22, s23\n"
      ".Lsl_d4_27:\n"
      "buffer_load_dword v20, v2, s[8:11], s51 offen sc1\n"
      "s_waitcnt vmcnt(0)\n"
      "v_cmp_ne_u32 vcc, s21, v20\n"
      "s_cbranch_vccz .Lbk_d4_27\n"
      "s_sleep 4\n"
      "s_sub_u32 s22, s22, 1\n"
      "s_cbranch_scc0 .Lsl_d4_27\n"
      "s_mov_b32 s23, 0\n"
      "s_branch .Lbk_d4_27\n"
      ".Lsp_dy3_28:\n"
      "s_lshl_b32 s22, s23, 4\n"
      ".Lsl_dy3_28:\n"
      "ds_read_b32 v15, v17\n"
      "s_waitcnt lgkmcnt(0)\n"
      "v_cmp_gt_i32 vcc, v14, v15\n"
      "s_cbranch_vccz .Lbk_dy3_28\n"
      "s_sub_u32 s22, s22, 1\n"
      "s_cbranch_scc0 .Lsl_dy3_28\n"
      "s_mov_b32 s23, 0\n"
      "s_branch .Lbk_dy3_28\n"
      ".Lsp_d4_29:\n"
      "s_mov_b32 s22, s23\n"
      ".Lsl_d4_29:\n"
      "buffer_load_dword v20, v2, s[8:11], s51 offen sc1\n"
      "s_waitcnt vmcnt(0)\n"
      "v_cmp_ne_u32 vcc, s21, v20\n"
      "s_cbranch_vccz .Lbk_d4_29\n"
      "s_sleep 4\n"
      "s_sub_u32 s22, s22, 1\n"
      "s_cbranch_scc0 .Lsl_d4_29\n"
      "s_mov_b32 s23, 0\n"
      "s_branch .Lbk_d4_29\n"
      ".Lsp_d5_30:\n"
      "s_mov_b32 s22, s23\n"
      ".Lsl_d5_30:\n"
      "buffer_load_dword v20, v2, s[8:11], s51 offen sc1\n"
      "s_waitcnt vmcnt(0)\n"
      "v_cmp_ne_u32 vcc, s21, v20\n"
      "s_cbranch_vccz .Lbk_d5_30\n"
      "s_sleep 4\n"
      "s_sub_u32 s22, s22, 1\n"
      "s_cbranch_scc0 .Lsl_d5_30\n"
      "s_mov_b32 s23, 0\n"
      "s_branch .Lbk_d5_30\n"
      ".Lsp_dy4_31:\n"
      "s_lshl_b32 s22, s23, 4\n"
      ".Lsl_dy4_31:\n"
      "ds_read_b32 v15, v17\n"
      "s_waitcnt lgkmcnt(0)\n"
      "v_cmp_gt_i32 vcc, v14, v15\n"
      "s_cbranch_vccz .Lbk_dy4_31\n"
      "s_sub_u32 s22, s22, 1\n"
      "s_cbranch_scc0 .Lsl_dy4_31\n"
      "s_mov_b32 s23, 0\n"
      "s_branch .Lbk_dy4_31\n"
      ".Lsp_d5_32:\n"
      "s_mov_b32 s22, s23\n"
      ".Lsl_d5_32:\n"
      "buffer_load_dword v20, v2, s[8:11], s51 offen sc1\n"
      "s_waitcnt vmcnt(0)\n"
      "v_cmp_ne_u32 vcc, s21, v20\n"
      "s_cbranch_vccz .Lbk_d5_32\n"
      "s_sleep 4\n"
      "s_sub_u32 s22, s22, 1\n"
      "s_cbranch_scc0 .Lsl_d5_32\n"
      "s_mov_b32 s23, 0\n"
      "s_branch .Lbk_d5_32\n"
      ".Lsp_d0_33:\n"
      "s_mov_b32 s22, s23\n"
      ".Lsl_d0_33:\n"
      "buffer_load_dword v20, v2, s[8:11], s51 offen sc1\n"
      "s_waitcnt vmcnt(0)\n"
      "v_cmp_ne_u32 vcc, s21, v20\n"
      "s_cbranch_vccz .Lbk_d0_33\n"
      "s_sleep 4\n"
      "s_sub_u32 s22, s22, 1\n"
      "s_cbranch_scc0 .Lsl_d0_33\n"
      "s_mov_b32 s23, 0\n"
      "s_branch .Lbk_d0_33\n"
      ".Lsp_dy5_34:\n"
      "s_lshl_b32 s22, s23, 4\n"
      ".Lsl_dy5_34:\n"
      "ds_read_b32 v15, v17\n"
      "s_waitcnt lgkmcnt(0)\n"
      "v_cmp_gt_i32 vcc, v14, v15\n"
      "s_cbranch_vccz .Lbk_dy5_34\n"
      "s_sub_u32 s22, s22, 1\n"
      "s_cbranch_scc0 .Lsl_dy5_34\n"
      "s_mov_b32 s23, 0\n"
      "s_branch .Lbk_dy5_34\n"
      ".Ld_last:\n"
      :
      : "{s[8:11]}"(rsWS), "{s[12:15]}"(rsA), "{s[16:19]}"(rsB), "{s[92:95]}"(rsC), "{v0}"(v_l16), "{v1}"(v_l16b), "{v2}"(v_fa), "{v3}"(v_ga), "{v4}"(v_ra), "{v5}"(v_rf), "{v6}"(v_xp), "{v7}"(v_xs), "{v8}"(v_op), "{v9}"(v_os), "{v10}"(v_sw), "{v11}"(v_sr), "{v12}"(v_ss), "{v13}"(v_l8), "{v15}"(v_al), "{v16}"(v_yw), "{v17}"(v_yr), "{v19}"(v_hi), "{v176}"(v_at), "{v177}"(v_as), "{v178}"(v_vo0), "{v179}"(v_vo1), "{v180}"(v_vo2), "{v181}"(v_pw), "{v183}"(v_pu), "{s36}"(s_sh), "{s37}"(s_ps), "{s38}"(s_x1), "{s39}"(s_xr), "{s40}"(s_or), "{s41}"(s_p0), "{s42}"(s_m3), "{s43}"(s_m6), "{s44}"(s_so), "{s45}"(s_ph), "{s46}"(s_i0), "{s47}"(s_i5), "{s48}"(s_j0), "{s49}"(s_j5), "{s50}"(s_he), "{s60}"(s_k0)
      : "v255", "a255", "s99", "vcc", "scc", "memory");

  } else {
    const unsigned j = part, f = 4 * j + w;
    const u32x4 rsA = srd(w >> 1 ? Wu : Wg, 576 * 4608 * 2), rsC = srd(Wo, 576 * 576 * 2);
    const unsigned wv = (slot - (MAP ? 16 : 14)) * 4 + w, tw0 = (wv * 81 + lane) * 128;
    const unsigned v_tw[2] = {MAP ? (fast ? tw0 : OOB) : (lane & 7) < 5 ? (8 * wv + l8) * 1152 + (xcd >> 2) * 512 + (lane & 7) * 128 : OOB,
                              MAP && fast && lane < 17 ? tw0 + 8192 : OOB};
    const unsigned v_l5 = w < 3 ? v_l16 + 16384 - w * 3072 : OOB;
    const unsigned v_fa = 19456 - w * 4096 + (lane >> 3) * 128 + (lane & 7) * 4, v_ra = up * 8 * HREC + l5 + 147456 - f * 1024 + f * 128;
    const unsigned v_st = 18432 + i16 * 32 + q * 8, v_pw = 147456 - f * 1024 + f * 128 + l8 * HREC + l7;
    const unsigned v_at = 65536 + (8 * g + q) * 256 + ((4 * w + p) ^ (4 * q)) * 16;
    const unsigned v_vo0 = (16 * (w & 1) + g) * 9216 + 256 * j + (i16 ^ (4 * g)) * 16;
    const unsigned v_vo1 = v_vo0 + 4 * 9216 - 1024, v_vo2 = v_vo1 + 4 * 9216 - 1024, v_vo3 = v_vo2 + 4 * 9216 - 1024;
    const unsigned s_sx = WS_X1 + gr * NREC * REC + w * 4096, s_sh = WS_H + gr * NREC * HREC + f * 1024;
    const unsigned s_m4 = w * 4096, s_m5 = 16384 + w * 1024;
    asm volatile(
      "s_buffer_load_dword s21, s[8:11], 0\n"
      "s_mov_b32 s23, 65536\n"
      "s_mov_b32 s31, 0x40000000\n"
      "s_mov_b32 s41, 0\n"
      "s_sleep 16\n"
      "buffer_load_dword v58, v54, s[92:95], s41 offen\n"
      "buffer_load_dword v59, v55, s[92:95], s41 offen\n"
      "s_waitcnt vmcnt(0)\n"
      "s_add_u32 m0, s38, 65536\n"
      "s_nop 0\n"
      "buffer_load_dwordx4 v6, s[12:15], s41 offen offset:0 lds\n"
      "buffer_load_dwordx4 v7, s[12:15], s41 offen offset:1024 lds\n"
      "buffer_load_dwordx4 v8, s[12:15], s41 offen offset:2048 lds\n"
      "buffer_load_dwordx4 v9, s[12:15], s41 offen offset:3072 lds\n"
      "s_add_u32 s41, s41, 294912\n"
      "s_add_u32 m0, s38, 81920\n"
      "s_nop 0\n"
      "buffer_load_dwordx4 v6, s[12:15], s41 offen offset:0 lds\n"
      "buffer_load_dwordx4 v7, s[12:15], s41 offen offset:1024 lds\n"
      "buffer_load_dwordx4 v8, s[12:15], s41 offen offset:2048 lds\n"
      "buffer_load_dwordx4 v9, s[12:15], s41 offen offset:3072 lds\n"
      "s_add_u32 s41, s41, 294912\n"
      "s_add_u32 m0, s38, 98304\n"
      "s_nop 0\n"
      "buffer_load_dwordx4 v6, s[12:15], s41 offen offset:0 lds\n"
      "buffer_load_dwordx4 v7, s[12:15], s41 offen offset:1024 lds\n"
      "buffer_load_dwordx4 v8, s[12:15], s41 offen offset:2048 lds\n"
      "buffer_load_dwordx4 v9, s[12:15], s41 offen offset:3072 lds\n"
      "s_add_u32 s41, s41, 294912\n"
      "s_add_u32 m0, s38, 114688\n"
      "s_nop 0\n"
      "buffer_load_dwordx4 v6, s[12:15], s41 offen offset:0 lds\n"
      "buffer_load_dwordx4 v7, s[12:15], s41 offen offset:1024 lds\n"
      "buffer_load_dwordx4 v8, s[12:15], s41 offen offset:2048 lds\n"
      "buffer_load_dwordx4 v9, s[12:15], s41 offen offset:3072 lds\n"
      "s_add_u32 s41, s41, 294912\n"
      "s_add_u32 m0, s38, 131072\n"
      "s_nop 0\n"
      "buffer_load_dwordx4 v6, s[12:15], s41 offen offset:0 lds\n"
      "buffer_load_dwordx4 v7, s[12:15], s41 offen offset:1024 lds\n"
      "buffer_load_dwordx4 v8, s[12:15], s41 offen offset:2048 lds\n"
      "buffer_load_dwordx4 v9, s[12:15], s41 offen offset:3072 lds\n"
      "s_add_u32 s41, s41, 294912\n"
      "v_mov_b32 v172, 0x3f803f80\n"
      "v_mov_b32 v173, 0x3f803f80\n"
      "v_mov_b32 v174, 0x3f803f80\n"
      "v_mov_b32 v175, 0x3f803f80\n"
      "v_add_u32 v19, 49152, v5\n"
      "s_mov_b64 exec, 0\n"
      "s_lshr_b32 s22, s38, 12\n"
      ".Lg_pl:\n"
      "s_cmp_eq_u32 s22, 0\n"
      "s_cbranch_scc1 .Lg_p0\n"
      "s_cmp_eq_u32 s22, 1\n"
      "s_cbranch_scc1 .Lg_p1\n"
      "s_branch .Lg_pe\n"
      ".Lg_pr:\n"
      "s_add_u32 s22, s22, 4\n"
      "s_branch .Lg_pl\n"
      ".Lg_pe:\n"
      "s_mov_b64 exec, -1\n"
      "s_waitcnt vmcnt(16)\n"
      "s_barrier\n"
      "ds_read_b64_tr_b16 a[0:1], v5 offset:0\n"
      "ds_read_b64_tr_b16 a[2:3], v5 offset:1024\n"
      "ds_read_b64_tr_b16 a[72:73], v5 offset:8192\n"
      "ds_read_b64_tr_b16 a[74:75], v5 offset:9216\n"
      "ds_read_b64_tr_b16 a[144:145], v5 offset:8\n"
      "ds_read_b64_tr_b16 a[146:147], v5 offset:1032\n"
      "ds_read_b64_tr_b16 a[216:217], v5 offset:8200\n"
      "ds_read_b64_tr_b16 a[218:219], v5 offset:9224\n"
      "s_add_u32 m0, s38, 147456\n"
      "s_nop 0\n"
      "buffer_load_dwordx4 v6, s[12:15], s41 offen offset:0 lds\n"
      "buffer_load_dwordx4 v7, s[12:15], s41 offen offset:1024 lds\n"
      "buffer_load_dwordx4 v8, s[12:15], s41 offen offset:2048 lds\n"
      "buffer_load_dwordx4 v9, s[12:15], s41 offen offset:3072 lds\n"
      "s_add_u32 s41, s41, 294912\n"
      "s_waitcnt vmcnt(16) lgkmcnt(0)\n"
      "s_barrier\n"
      "ds_read_b64_tr_b16 a[4:5], v5 offset:16384\n"
      "ds_read_b64_tr_b16 a[6:7], v5 offset:17408\n"
      "ds_read_b64_tr_b16 a[76:77], v5 offset:24576\n"
      "ds_read_b64_tr_b16 a[78:79], v5 offset:25600\n"
      "ds_read_b64_tr_b16 a[148:149], v5 offset:16392\n"
      "ds_read_b64_tr_b16 a[150:151], v5 offset:17416\n"
      "ds_read_b64_tr_b16 a[220:221], v5 offset:24584\n"
      "ds_read_b64_tr_b16 a[222:223], v5 offset:25608\n"
      "v_mfma_f32_16x16x32_bf16 v[140:143], a[0:3], v[172:175], 0\n"
      "v_mfma_f32_16x16x32_bf16 v[144:147], a[72:75], v[172:175], 0\n"
      "v_mfma_f32_16x16x32_bf16 v[148:151], a[144:147], v[172:175], 0\n"
      "v_mfma_f32_16x16x32_bf16 v[152:155], a[216:219], v[172:175], 0\n"
      "s_add_u32 m0, s38, 65536\n"
      "s_nop 0\n"
      "buffer_load_dwordx4 v6, s[12:15], s41 offen offset:0 lds\n"
      "buffer_load_dwordx4 v7, s[12:15], s41 offen offset:1024 lds\n"
      "buffer_load_dwordx4 v8, s[12:15], s41 offen offset:2048 lds\n"
      "buffer_load_dwordx4 v9, s[12:15], s41 offen offset:3072 lds\n"
      "s_add_u32 s41, s41, 294912\n"
      "s_waitcnt vmcnt(16) lgkmcnt(0)\n"
      "s_barrier\n"
      "ds_read_b64_tr_b16 a[8:9], v5 offset:32768\n"
      "ds_read_b64_tr_b16 a[10:11], v5 offset:33792\n"
      "ds_read_b64_tr_b16 a[80:81], v5 offset:40960\n"
      "ds_read_b64_tr_b16 a[82:83], v5 offset:41984\n"
      "ds_read_b64_tr_b16 a[152:153], v5 offset:32776\n"
      "ds_read_b64_tr_b16 a[154:155], v5 offset:33800\n"
      "ds_read_b64_tr_b16 a[224:225], v5 offset:40968\n"
      "ds_read_b64_tr_b16 a[226:227], v5 offset:41992\n"
      "v_mfma_f32_16x16x32_bf16 v[140:143], a[4:7], v[172:175], v[140:143]\n"
      "v_mfma_f32_16x16x32_bf16 v[144:147], a[76:79], v[172:175], v[144:147]\n"
      "v_mfma_f32_16x16x32_bf16 v[148:151], a[148:151], v[172:175], v[148:151]\n"
      "v_mfma_f32_16x16x32_bf16 v[152:155], a[220:223], v[172:175], v[152:155]\n"
      "s_add_u32 m0, s38, 81920\n"
      "s_nop 0\n"
      "buffer_load_dwordx4 v6, s[12:15], s41 offen offset:0 lds\n"
      "buffer_load_dwordx4 v7, s[12:15], s41 offen offset:1024 lds\n"
      "buffer_load_dwordx4 v8, s[12:15], s41 offen offset:2048 lds\n"
      "buffer_load_dwordx4 v9, s[12:15], s41 offen offset:3072 lds\n"
      "s_add_u32 s41, s41, 294912\n"
      "s_waitcnt vmcnt(16) lgkmcnt(0)\n"
      "s_barrier\n"
      "ds_read_b64_tr_b16 a[12:13], v19 offset:0\n"
      "ds_read_b64_tr_b16 a[14:15], v19 offset:1024\n"
      "ds_read_b64_tr_b16 a[84:85], v19 offset:8192\n"
      "ds_read_b64_tr_b16 a[86:87], v19 offset:9216\n"
      "ds_read_b64_tr_b16 a[156:157], v19 offset:8\n"
      "ds_read_b64_tr_b16 a[158:159], v19 offset:1032\n"
      "ds_read_b64_tr_b16 a[228:229], v19 offset:8200\n"
      "ds_read_b64_tr_b16 a[230:231], v19 offset:9224\n"
      "v_mfma_f32_16x16x32_bf16 v[140:143], a[8:11], v[172:175], v[140:143]\n"
      "v_mfma_f32_16x16x32_bf16 v[144:147], a[80:83], v[172:175], v[144:147]\n"
      "v_mfma_f32_16x16x32_bf16 v[148:151], a[152:155], v[172:175], v[148:151]\n"
      "v_mfma_f32_16x16x32_bf16 v[152:155], a[224:227], v[172:175], v[152:155]\n"
      "s_add_u32 m0, s38, 98304\n"
      "s_nop 0\n"
      "buffer_load_dwordx4 v6, s[12:15], s41 offen offset:0 lds\n"
      "buffer_load_dwordx4 v7, s[12:15], s41 offen offset:1024 lds\n"
      "buffer_load_dwordx4 v8, s[12:15], s41 offen offset:2048 lds\n"
      "buffer_load_dwordx4 v9, s[12:15], s41 offen offset:3072 lds\n"
      "s_add_u32 s41, s41, 294912\n"
      "s_waitcnt vmcnt(16) lgkmcnt(0)\n"
      "s_barrier\n"
      "ds_read_b64_tr_b16 a[16:17], v19 offset:16384\n"
      "ds_read_b64_tr_b16 a[18:19], v19 offset:17408\n"
      "ds_read_b64_tr_b16 a[88:89], v19 offset:24576\n"
      "ds_read_b64_tr_b16 a[90:91], v19 offset:25600\n"
      "ds_read_b64_tr_b16 a[160:161], v19 offset:16392\n"
      "ds_read_b64_tr_b16 a[162:163], v19 offset:17416\n"
      "ds_read_b64_tr_b16 a[232:233], v19 offset:24584\n"
      "ds_read_b64_tr_b16 a[234:235], v19 offset:25608\n"
      "v_mfma_f32_16x16x32_bf16 v[140:143], a[12:15], v[172:175], v[140:143]\n"
      "v_mfma_f32_16x16x32_bf16 v[144:147], a[84:87], v[172:175], v[144:147]\n"
      "v_mfma_f32_16x16x32_bf16 v[148:151], a[156:159], v[172:175], v[148:151]\n"
      "v_mfma_f32_16x16x32_bf16 v[152:155], a[228:231], v[172:175], v[152:155]\n"
      "s_add_u32 m0, s38, 114688\n"
      "s_nop 0\n"
      "buffer_load_dwordx4 v6, s[12:15], s41 offen offset:0 lds\n"
      "buffer_load_dwordx4 v7, s[12:15], s41 offen offset:1024 lds\n"
      "buffer_load_dwordx4 v8, s[12:15], s41 offen offset:2048 lds\n"
      "buffer_load_dwordx4 v9, s[12:15], s41 offen offset:3072 lds\n"
      "s_add_u32 s41, s41, 294912\n"
      "s_waitcnt vmcnt(16) lgkmcnt(0)\n"
      "s_barrier\n"
      "ds_read_b64_tr_b16 a[20:21], v19 offset:32768\n"
      "ds_read_b64_tr_b16 a[22:23], v19 offset:33792\n"
      "ds_read_b64_tr_b16 a[92:93], v19 offset:40960\n"
      "ds_read_b64_tr_b16 a[94:95], v19 offset:41984\n"
      "ds_read_b64_tr_b16 a[164:165], v19 offset:32776\n"
      "ds_read_b64_tr_b16 a[166:167], v19 offset:33800\n"
      "ds_read_b64_tr_b16 a[236:237], v19 offset:40968\n"
      "ds_read_b64_tr_b16 a[238:239], v19 offset:41992\n"
      "v_mfma_f32_16x16x32_bf16 v[140:143], a[16:19], v[172:175], v[140:143]\n"
      "v_mfma_f32_16x16x32_bf16 v[144:147], a[88:91], v[172:175], v[144:147]\n"
      "v_mfma_f32_16x16x32_bf16 v[148:151], a[160:163], v[172:175], v[148:151]\n"
      "v_mfma_f32_16x16x32_bf16 v[152:155], a[232:235], v[172:175], v[152:155]\n"
      "s_add_u32 m0, s38, 131072\n"
      "s_nop 0\n"
      "buffer_load_dwordx4 v6, s[12:15], s41 offen offset:0 lds\n"
      "buffer_load_dwordx4 v7, s[12:15], s41 offen offset:1024 lds\n"
      "buffer_load_dwordx4 v8, s[12:15], s41 offen offset:2048 lds\n"
      "buffer_load_dwordx4 v9, s[12:15], s41 offen offset:3072 lds\n"
      "s_add_u32 s41, s41, 294912\n"
      "s_waitcnt vmcnt(16) lgkmcnt(0)\n"
      "s_barrier\n"
      "ds_read_b64_tr_b16 a[24:25], v5 offset:0\n"
      "ds_read_b64_tr_b16 a[26:27], v5 offset:1024\n"
      "ds_read_b64_tr_b16 a[96:97], v5 offset:8192\n"
      "ds_read_b64_tr_b16 a[98:99], v5 offset:9216\n"
      "ds_read_b64_tr_b16 a[168:169], v5 offset:8\n"
      "ds_read_b64_tr_b16 a[170:171], v5 offset:1032\n"
      "ds_read_b64_tr_b16 a[240:241], v5 offset:8200\n"
      "ds_read_b64_tr_b16 a[242:243], v5 offset:9224\n"
      "v_mfma_f32_16x16x32_bf16 v[140:143], a[20:23], v[172:175], v[140:143]\n"
      "v_mfma_f32_16x16x32_bf16 v[144:147], a[92:95], v[172:175], v[144:147]\n"
      "v_mfma_f32_16x16x32_bf16 v[148:151], a[164:167], v[172:175], v[148:151]\n"
      "v_mfma_f32_16x16x32_bf16 v[152:155], a[236:239], v[172:175], v[152:155]\n"
      "s_add_u32 m0, s38, 147456\n"
      "s_nop 0\n"
      "buffer_load_dwordx4 v6, s[12:15], s41 offen offset:0 lds\n"
      "buffer_load_dwordx4 v7, s[12:15], s41 offen offset:1024 lds\n"
      "buffer_load_dwordx4 v8, s[12:15], s41 offen offset:2048 lds\n"
      "buffer_load_dwordx4 v9, s[12:15], s41 offen offset:3072 lds\n"
      "s_add_u32 s41, s41, 294912\n"
      "s_waitcnt vmcnt(16) lgkmcnt(0)\n"
      "s_barrier\n"
      "ds_read_b64_tr_b16 a[28:29], v5 offset:16384\n"
      "ds_read_b64_tr_b16 a[30:31], v5 offset:17408\n"
      "ds_read_b64_tr_b16 a[100:101], v5 offset:24576\n"
      "ds_read_b64_tr_b16 a[102:103], v5 offset:25600\n"
      "ds_read_b64_tr_b16 a[172:173], v5 offset:16392\n"
      "ds_read_b64_tr_b16 a[174:175], v5 offset:17416\n"
      "ds_read_b64_tr_b16 a[244:245], v5 offset:24584\n"
      "ds_read_b64_tr_b16 a[246:247], v5 offset:25608\n"
      "v_mfma_f32_16x16x32_bf16 v[140:143], a[24:27], v[172:175], v[140:143]\n"
      "v_mfma_f32_16x16x32_bf16 v[144:147], a[96:99], v[172:175], v[144:147]\n"
      "v_mfma_f32_16x16x32_bf16 v[148:151], a[168:171], v[172:175], v[148:151]\n"
      "v_mfma_f32_16x16x32_bf16 v[152:155], a[240:243], v[172:175], v[152:155]\n"
      "s_add_u32 m0, s38, 65536\n"
      "s_nop 0\n"
      "buffer_load_dwordx4 v6, s[12:15], s41 offen offset:0 lds\n"
      "buffer_load_dwordx4 v7, s[12:15], s41 offen offset:1024 lds\n"
      "buffer_load_dwordx4 v8, s[12:15], s41 offen offset:2048 lds\n"
      "buffer_load_dwordx4 v9, s[12:15], s41 offen offset:3072 lds\n"
      "s_add_u32 s41, s41, 294912\n"
      "s_waitcnt vmcnt(16) lgkmcnt(0)\n"
      "s_barrier\n"
      "ds_read_b64_tr_b16 a[32:33], v5 offset:32768\n"
      "ds_read_b64_tr_b16 a[34:35], v5 offset:33792\n"
      "ds_read_b64_tr_b16 a[104:105], v5 offset:40960\n"
      "ds_read_b64_tr_b16 a[106:107], v5 offset:41984\n"
      "ds_read_b64_tr_b16 a[176:177], v5 offset:32776\n"
      "ds_read_b64_tr_b16 a[178:179], v5 offset:33800\n"
      "ds_read_b64_tr_b16 a[248:249], v5 offset:40968\n"
      "ds_read_b64_tr_b16 a[250:251], v5 offset:41992\n"
      "v_mfma_f32_16x16x32_bf16 v[140:143], a[28:31], v[172:175], v[140:143]\n"
      "v_mfma_f32_16x16x32_bf16 v[144:147], a[100:103], v[172:175], v[144:147]\n"
      "v_mfma_f32_16x16x32_bf16 v[148:151], a[172:175], v[172:175], v[148:151]\n"
      "v_mfma_f32_16x16x32_bf16 v[152:155], a[244:247], v[172:175], v[152:155]\n"
      "s_add_u32 m0, s38, 81920\n"
      "s_nop 0\n"
      "buffer_load_dwordx4 v6, s[12:15], s41 offen offset:0 lds\n"
      "buffer_load_dwordx4 v7, s[12:15], s41 offen offset:1024 lds\n"
      "buffer_load_dwordx4 v8, s[12:15], s41 offen offset:2048 lds\n"
      "buffer_load_dwordx4 v9, s[12:15], s41 offen offset:3072 lds\n"
      "s_add_u32 s41, s41, 294912\n"
      "s_waitcnt vmcnt(16) lgkmcnt(0)\n"
      "s_barrier\n"
      "ds_read_b64_tr_b16 a[36:37], v19 offset:0\n"
      "ds_read_b64_tr_b16 a[38:39], v19 offset:1024\n"
      "ds_read_b64_tr_b16 a[108:109], v19 offset:8192\n"
      "ds_read_b64_tr_b16 a[110:111], v19 offset:9216\n"
      "ds_read_b64_tr_b16 a[180:181], v19 offset:8\n"
      "ds_read_b64_tr_b16 a[182:183], v19 offset:1032\n"
      "ds_read_b64_tr_b16 a[252:253], v19 offset:8200\n"
      "ds_read_b64_tr_b16 a[254:255], v19 offset:9224\n"
      "v_mfma_f32_16x16x32_bf16 v[140:143], a[32:35], v[172:175], v[140:143]\n"
      "v_mfma_f32_16x16x32_bf16 v[144:147], a[104:107], v[172:175], v[144:147]\n"
      "v_mfma_f32_16x16x32_bf16 v[148:151], a[176:179], v[172:175], v[148:151]\n"
      "v_mfma_f32_16x16x32_bf16 v[152:155], a[248:251], v[172:175], v[152:155]\n"
      "s_add_u32 m0, s38, 98304\n"
      "s_nop 0\n"
      "buffer_load_dwordx4 v6, s[12:15], s41 offen offset:0 lds\n"
      "buffer_load_dwordx4 v7, s[12:15], s41 offen offset:1024 lds\n"
      "buffer_load_dwordx4 v8, s[12:15], s41 offen offset:2048 lds\n"
      "buffer_load_dwordx4 v9, s[12:15], s41 offen offset:3072 lds\n"
      "s_add_u32 s41, s41, 294912\n"
      "s_waitcnt vmcnt(16) lgkmcnt(0)\n"
      "s_barrier\n"
      "ds_read_b64_tr_b16 a[40:41], v19 offset:16384\n"
      "ds_read_b64_tr_b16 a[42:43], v19 offset:17408\n"
      "ds_read_b64_tr_b16 a[112:113], v19 offset:24576\n"
      "ds_read_b64_tr_b16 a[114:115], v19 offset:25600\n"
      "ds_read_b64_tr_b16 a[184:185], v19 offset:16392\n"
      "ds_read_b64_tr_b16 a[186:187], v19 offset:17416\n"
      "ds_read_b64_tr_b16 v[20:21], v19 offset:24584\n"
      "ds_read_b64_tr_b16 v[22:23], v19 offset:25608\n"
      "v_mfma_f32_16x16x32_bf16 v[140:143], a[36:39], v[172:175], v[140:143]\n"
      "v_mfma_f32_16x16x32_bf16 v[144:147], a[108:111], v[172:175], v[144:147]\n"
      "v_mfma_f32_16x16x32_bf16 v[148:151], a[180:183], v[172:175], v[148:151]\n"
      "v_mfma_f32_16x16x32_bf16 v[152:155], a[252:255], v[172:175], v[152:155]\n"
      "s_add_u32 m0, s38, 114688\n"
      "s_nop 0\n"
      "buffer_load_dwordx4 v6, s[12:15], s41 offen offset:0 lds\n"
      "buffer_load_dwordx4 v7, s[12:15], s41 offen offset:1024 lds\n"
      "buffer_load_dwordx4 v8, s[12:15], s41 offen offset:2048 lds\n"
      "buffer_load_dwordx4 v9, s[12:15], s41 offen offset:3072 lds\n"
      "s_add_u32 s41, s41, 294912\n"
      "s_waitcnt vmcnt(16) lgkmcnt(0)\n"
      "s_barrier\n"
      "ds_read_b64_tr_b16 a[44:45], v19 offset:32768\n"
      "ds_read_b64_tr_b16 a[46:47], v19 offset:33792\n"
      "ds_read_b64_tr_b16 a[116:117], v19 offset:40960\n"
      "ds_read_b64_tr_b16 a[118:119], v19 offset:41984\n"
      "ds_read_b64_tr_b16 a[188:189], v19 offset:32776\n"
      "ds_read_b64_tr_b16 a[190:191], v19 offset:33800\n"
      "ds_read_b64_tr_b16 v[24:25], v19 offset:40968\n"
      "ds_read_b64_tr_b16 v[26:27], v19 offset:41992\n"
      "v_mfma_f32_16x16x32_bf16 v[140:143], a[40:43], v[172:175], v[140:143]\n"
      "v_mfma_f32_16x16x32_bf16 v[144:147], a[112:115], v[172:175], v[144:147]\n"
      "v_mfma_f32_16x16x32_bf16 v[148:151], a[184:187], v[172:175], v[148:151]\n"
      "v_mfma_f32_16x16x32_bf16 v[152:155], v[20:23], v[172:175], v[152:155]\n"
      "s_add_u32 m0, s38, 131072\n"
      "s_nop 0\n"
      "buffer_load_dwordx4 v6, s[12:15], s41 offen offset:0 lds\n"
      "buffer_load_dwordx4 v7, s[12:15], s41 offen offset:1024 lds\n"
      "buffer_load_dwordx4 v8, s[12:15], s41 offen offset:2048 lds\n"
      "buffer_load_dwordx4 v9, s[12:15], s41 offen offset:3072 lds\n"
      "s_add_u32 s41, s41, 294912\n"
      "s_waitcnt vmcnt(16) lgkmcnt(0)\n"
      "s_barrier\n"
      "ds_read_b64_tr_b16 a[48:49], v5 offset:0\n"
      "ds_read_b64_tr_b16 a[50:51], v5 offset:1024\n"
      "ds_read_b64_tr_b16 a[120:121], v5 offset:8192\n"
      "ds_read_b64_tr_b16 a[122:123], v5 offset:9216\n"
      "ds_read_b64_tr_b16 a[192:193], v5 offset:8\n"
      "ds_read_b64_tr_b16 a[194:195], v5 offset:1032\n"
      "ds_read_b64_tr_b16 v[28:29], v5 offset:8200\n"
      "ds_read_b64_tr_b16 v[30:31], v5 offset:9224\n"
      "v_mfma_f32_16x16x32_bf16 v[140:143], a[44:47], v[172:175], v[140:143]\n"
      "v_mfma_f32_16x16x32_bf16 v[144:147], a[116:119], v[172:175], v[144:147]\n"
      "v_mfma_f32_16x16x32_bf16 v[148:151], a[188:191], v[172:175], v[148:151]\n"
      "v_mfma_f32_16x16x32_bf16 v[152:155], v[24:27], v[172:175], v[152:155]\n"
      "s_add_u32 m0, s38, 147456\n"
      "s_nop 0\n"
      "buffer_load_dwordx4 v6, s[12:15], s41 offen offset:0 lds\n"
      "buffer_load_dwordx4 v7, s[12:15], s41 offen offset:1024 lds\n"
      "buffer_load_dwordx4 v8, s[12:15], s41 offen offset:2048 lds\n"
      "buffer_load_dwordx4 v9, s[12:15], s41 offen offset:3072 lds\n"
      "s_add_u32 s41, s41, 294912\n"
      "s_waitcnt vmcnt(16) lgkmcnt(0)\n"
      "s_barrier\n"
      "ds_read_b64_tr_b16 a[52:53], v5 offset:16384\n"
      "ds_read_b64_tr_b16 a[54:55], v5 offset:17408\n"
      "ds_read_b64_tr_b16 a[124:125], v5 offset:24576\n"
      "ds_read_b64_tr_b16 a[126:127], v5 offset:25600\n"
      "ds_read_b64_tr_b16 a[196:197], v5 offset:16392\n"
      "ds_read_b64_tr_b16 a[198:199], v5 offset:17416\n"
      "ds_read_b64_tr_b16 v[32:33], v5 offset:24584\n"
      "ds_read_b64_tr_b16 v[34:35], v5 offset:25608\n"
      "v_mfma_f32_16x16x32_bf16 v[140:143], a[48:51], v[172:175], v[140:143]\n"
      "v_mfma_f32_16x16x32_bf16 v[144:147], a[120:123], v[172:175], v[144:147]\n"
      "v_mfma_f32_16x16x32_bf16 v[148:151], a[192:195], v[172:175], v[148:151]\n"
      "v_mfma_f32_16x16x32_bf16 v[152:155], v[28:31], v[172:175], v[152:155]\n"
      "s_waitcnt vmcnt(12) lgkmcnt(0)\n"
      "s_barrier\n"
      "ds_read_b64_tr_b16 a[56:57], v5 offset:32768\n"
      "ds_read_b64_tr_b16 a[58:59], v5 offset:33792\n"
      "ds_read_b64_tr_b16 a[128:129], v5 offset:40960\n"
      "ds_read_b64_tr_b16 a[130:131], v5 offset:41984\n"
      "ds_read_b64_tr_b16 a[200:201], v5 offset:32776\n"
      "ds_read_b64_tr_b16 a[202:203], v5 offset:33800\n"
      "ds_read_b64_tr_b16 v[36:37], v5 offset:40968\n"
      "ds_read_b64_tr_b16 v[38:39], v5 offset:41992\n"
      "v_mfma_f32_16x16x32_bf16 v[140:143], a[52:55], v[172:175], v[140:143]\n"
      "v_mfma_f32_16x16x32_bf16 v[144:147], a[124:127], v[172:175], v[144:147]\n"
      "v_mfma_f32_16x16x32_bf16 v[148:151], a[196:199], v[172:175], v[148:151]\n"
      "v_mfma_f32_16x16x32_bf16 v[152:155], v[32:35], v[172:175], v[152:155]\n"
      "s_waitcnt vmcnt(8) lgkmcnt(0)\n"
      "s_barrier\n"
      "ds_read_b64_tr_b16 a[60:61], v19 offset:0\n"
      "ds_read_b64_tr_b16 a[62:63], v19 offset:1024\n"
      "ds_read_b64_tr_b16 a[132:133], v19 offset:8192\n"
      "ds_read_b64_tr_b16 a[134:135], v19 offset:9216\n"
      "ds_read_b64_tr_b16 a[204:205], v19 offset:8\n"
      "ds_read_b64_tr_b16 a[206:207], v19 offset:1032\n"
      "ds_read_b64_tr_b16 v[40:41], v19 offset:8200\n"
      "ds_read_b64_tr_b16 v[42:43], v19 offset:9224\n"
      "v_mfma_f32_16x16x32_bf16 v[140:143], a[56:59], v[172:175], v[140:143]\n"
      "v_mfma_f32_16x16x32_bf16 v[144:147], a[128:131], v[172:175], v[144:147]\n"
      "v_mfma_f32_16x16x32_bf16 v[148:151], a[200:203], v[172:175], v[148:151]\n"
      "v_mfma_f32_16x16x32_bf16 v[152:155], v[36:39], v[172:175], v[152:155]\n"
      "s_waitcnt vmcnt(4) lgkmcnt(0)\n"
      "s_barrier\n"
      "ds_read_b64_tr_b16 a[64:65], v19 offset:16384\n"
      "ds_read_b64_tr_b16 a[66:67], v19 offset:17408\n"
      "ds_read_b64_tr_b16 a[136:137], v19 offset:24576\n"
      "ds_read_b64_tr_b16 a[138:139], v19 offset:25600\n"
      "ds_read_b64_tr_b16 a[208:209], v19 offset:16392\n"
      "ds_read_b64_tr_b16 a[210:211], v19 offset:17416\n"
      "ds_read_b64_tr_b16 v[44:45], v19 offset:24584\n"
      "ds_read_b64_tr_b16 v[46:47], v19 offset:25608\n"
      "v_mfma_f32_16x16x32_bf16 v[140:143], a[60:63], v[172:175], v[140:143]\n"
      "v_mfma_f32_16x16x32_bf16 v[144:147], a[132:135], v[172:175], v[144:147]\n"
      "v_mfma_f32_16x16x32_bf16 v[148:151], a[204:207], v[172:175], v[148:151]\n"
      "v_mfma_f32_16x16x32_bf16 v[152:155], v[40:43], v[172:175], v[152:155]\n"
      "s_waitcnt vmcnt(0) lgkmcnt(0)\n"
      "s_barrier\n"
      "ds_read_b64_tr_b16 a[68:69], v19 offset:32768\n"
      "ds_read_b64_tr_b16 a[70:71], v19 offset:33792\n"
      "ds_read_b64_tr_b16 a[140:141], v19 offset:40960\n"
      "ds_read_b64_tr_b16 a[142:143], v19 offset:41984\n"
      "ds_read_b64_tr_b16 a[212:213], v19 offset:32776\n"
      "ds_read_b64_tr_b16 a[214:215], v19 offset:33800\n"
      "ds_read_b64_tr_b16 v[48:49], v19 offset:40968\n"
      "ds_read_b64_tr_b16 v[50:51], v19 offset:41992\n"
      "v_mfma_f32_16x16x32_bf16 v[140:143], a[64:67], v[172:175], v[140:143]\n"
      "v_mfma_f32_16x16x32_bf16 v[144:147], a[136:139], v[172:175], v[144:147]\n"
      "v_mfma_f32_16x16x32_bf16 v[148:151], a[208:211], v[172:175], v[148:151]\n"
      "v_mfma_f32_16x16x32_bf16 v[152:155], v[44:47], v[172:175], v[152:155]\n"
      "s_waitcnt lgkmcnt(0)\n"
      "v_mfma_f32_16x16x32_bf16 v[140:143], a[68:71], v[172:175], v[140:143]\n"
      "v_mfma_f32_16x16x32_bf16 v[144:147], a[140:143], v[172:175], v[144:147]\n"
      "v_mfma_f32_16x16x32_bf16 v[148:151], a[212:215], v[172:175], v[148:151]\n"
      "v_mfma_f32_16x16x32_bf16 v[152:155], v[48:51], v[172:175], v[152:155]\n"
      "s_getpc_b64 vcc\n"
      ".Lg_ct:\n"
      "s_add_u32 vcc_lo, vcc_lo, .Lg_p0-.Lg_ct\n"
      "s_addc_u32 vcc_hi, vcc_hi, 0\n"
      "s_lshl_b32 s30, s38, 1\n"
      "v_lshl_add_u32 v60, v0, 3, s30\n"
      "v_min_u32 v60, .Lg_last-.Lg_p0-4, v60\n"
      "global_load_dword v60, v60, vcc\n"
      "s_mul_i32 s21, s21, 1664525\n"
      "s_add_u32 s21, s21, 1013904223\n"
      "v_mov_b32 v15, s21\n"
      "buffer_store_dword v15, v12, s[8:11], 0 offen sc1\n"
      "v_not_b32 v52, v15\n"
      "v_not_b32 v53, v15\n"
      "v_not_b32 v54, v15\n"
      "v_not_b32 v55, v15\n"
      "s_add_u32 s30, s37, 0\n"
      "buffer_store_dwordx4 v[52:55], v10, s[8:11], s30 offen sc1\n"
      "v_xor_b32 v15, v15, v16\n"
      "v_add_u32 v17, 40960, v0\n"
      "s_barrier\n"
      "v_mov_b32 v11, 0\n"
      "ds_write_b32 v13, v11\n"
      "s_waitcnt lgkmcnt(0)\n"
      "s_barrier\n"
      "s_add_u32 s6, s36, 1548288\n"
      "s_add_u32 s36, s36, 4294942720\n"
      "s_add_u32 s42, s37, 4294635520\n"
      "s_add_u32 s37, s37, 4294801408\n"
      "s_add_u32 s36, s36, 24576\n"
      "s_min_u32 s36, s36, s6\n"
      "buffer_load_dword v18, v2, s[8:11], s36 offen sc1\n"
      "s_mov_b32 s40, s36\n"
      "s_add_u32 s36, s36, 24576\n"
      "s_add_u32 m0, s38, 0\n"
      "s_waitcnt vmcnt(0)\n"
      "v_cmp_ne_u32 vcc, s21, v18\n"
      "s_cbranch_vccnz .Lsp_g0_1\n"
      ".Lbk_g0_1:\n"
      "buffer_load_dwordx4 v0, s[8:11], s40 offen offset:2048 sc1 lds\n"
      "buffer_load_dwordx4 v0, s[8:11], s40 offen offset:1024 sc1 lds\n"
      "buffer_load_dwordx4 v0, s[8:11], s40 offen offset:0 sc1 lds\n"
      "buffer_load_dwordx4 v0, s[8:11], s40 offen offset:3072 sc1 lds\n"
      "s_min_u32 s36, s36, s6\n"
      "s_add_u32 m0, s39, 0\n"
      "s_nop 0\n"
      "buffer_load_dwordx4 v1, s[8:11], s40 offen offset:0 sc1 lds\n"
      "buffer_load_dword v18, v2, s[8:11], s36 offen sc1\n"
      "s_mov_b32 s40, s36\n"
      "v_add_u32 v11, 1, v11\n"
      "s_add_u32 s36, s36, 24576\n"
      "s_add_u32 m0, s38, 20480\n"
      "s_waitcnt vmcnt(1)\n"
      "ds_write_b32 v13, v11\n"
      "s_waitcnt vmcnt(0)\n"
      "v_cmp_ne_u32 vcc, s21, v18\n"
      "ds_read_b32 v12, v14\n"
      "s_cbranch_vccnz .Lsp_g0_2\n"
      ".Lbk_g0_2:\n"
      "buffer_load_dwordx4 v0, s[8:11], s40 offen offset:2048 sc1 lds\n"
      "s_waitcnt lgkmcnt(0)\n"
      "v_cmp_gt_i32 vcc, v11, v12\n"
      "buffer_load_dwordx4 v0, s[8:11], s40 offen offset:1024 sc1 lds\n"
      "buffer_load_dwordx4 v0, s[8:11], s40 offen offset:0 sc1 lds\n"
      "s_cbranch_vccnz .Lsp_gy0_3\n"
      ".Lbk_gy0_3:\n"
      "ds_read_b128 v[52:55], v0 offset:0\n"
      "ds_read_b128 v[56:59], v0 offset:1024\n"
      "buffer_load_dwordx4 v0, s[8:11], s40 offen offset:3072 sc1 lds\n"
      "s_min_u32 s36, s36, s6\n"
      "ds_read_b128 v[60:63], v0 offset:2048\n"
      "ds_read_b128 v[76:79], v0 offset:6144\n"
      "s_add_u32 m0, s39, 20480\n"
      "ds_read_b128 v[68:71], v0 offset:4096\n"
      "buffer_load_dwordx4 v1, s[8:11], s40 offen offset:0 sc1 lds\n"
      "ds_read_b128 v[72:75], v0 offset:5120\n"
      "ds_read_b128 v[96:99], v0 offset:11264\n"
      "buffer_load_dword v16, v13, s[8:11], s31 offen\n"
      "ds_read_b128 v[64:67], v0 offset:3072\n"
      "ds_read_b128 v[84:87], v0 offset:8192\n"
      "ds_read_b128 v[88:91], v0 offset:9216\n"
      "buffer_load_dword v18, v2, s[8:11], s36 offen sc1\n"
      "ds_read_b128 v[80:83], v0 offset:7168\n"
      "s_mov_b32 s20, 15\n"
      "s_waitcnt lgkmcnt(0)\n"
      "s_nop 4\n"
      "s_branch .Lg_loop\n"
      ".p2align 12\n"
      ".Lg_p0:\n"
      "s_branch .Lg_pr\n"
      ".p2align 5\n"
      ".Lg_loop:\n"
      "v_mfma_f32_16x16x32_bf16 v[124:127], a[0:3], v[52:55], 0\n"
      "s_mov_b32 s40, s36\n"
      "ds_read_b128 v[120:123], v0 offset:17408\n"
      "v_fmac_f32 v130, v176, v146\n"
      "v_mfma_f32_16x16x32_bf16 v[124:127], a[4:7], v[56:59], v[124:127]\n"
      "s_waitcnt lgkmcnt(5)\n"
      "ds_read_b128 v[108:111], v0 offset:14336\n"
      "v_fma_f32 v160, v160, v177, v177\n"
      "v_mfma_f32_16x16x32_bf16 v[124:127], a[8:11], v[60:63], v[124:127]\n"
      "s_waitcnt lgkmcnt(5)\n"
      "ds_read_b128 v[92:95], v0 offset:10240\n"
      "v_fma_f32 v163, v163, v177, v177\n"
      "v_mfma_f32_16x16x32_bf16 v[124:127], a[12:15], v[64:67], v[124:127]\n"
      "ds_read_b128 v[112:115], v0 offset:15360\n"
      "v_fmac_f32 v131, v176, v147\n"
      "v_mfma_f32_16x16x32_bf16 v[124:127], a[16:19], v[68:71], v[124:127]\n"
      "ds_read_b128 v[100:103], v0 offset:12288\n"
      "v_exp_f32 v162, v162\n"
      "v_mfma_f32_16x16x32_bf16 v[124:127], a[20:23], v[72:75], v[124:127]\n"
      "ds_read_b128 v[104:107], v0 offset:13312\n"
      "v_mul_f32 v129, v129, v157\n"
      "v_fma_f32 v162, v162, v177, v177\n"
      "v_fmac_f32 v136, v176, v152\n"
      "v_mfma_f32_16x16x32_bf16 v[124:127], a[24:27], v[76:79], v[124:127]\n"
      "s_waitcnt lgkmcnt(6)\n"
      "v_mul_f32 v131, v131, v159\n"
      "v_fmac_f32 v138, v176, v154\n"
      "v_mfma_f32_16x16x32_bf16 v[124:127], a[28:31], v[80:83], v[124:127]\n"
      "ds_read_b128 v[116:119], v0 offset:16384\n"
      "v_rcp_f32 v163, v163\n"
      "v_mfma_f32_16x16x32_bf16 v[124:127], a[32:35], v[84:87], v[124:127]\n"
      "ds_read2st64_b64 v[168:171], v4 offset0:0 offset1:1\n"
      "v_rcp_f32 v162, v162\n"
      "v_mfma_f32_16x16x32_bf16 v[124:127], a[36:39], v[88:91], v[124:127]\n"
      "s_waitcnt lgkmcnt(5)\n"
      "v_rcp_f32 v161, v161\n"
      "v_mfma_f32_16x16x32_bf16 v[124:127], a[40:43], v[92:95], v[124:127]\n"
      "v_mul_f32 v162, v134, v162\n"
      "v_mul_f32 v163, v135, v163\n"
      "v_mfma_f32_16x16x32_bf16 v[124:127], a[44:47], v[96:99], v[124:127]\n"
      "s_waitcnt lgkmcnt(3)\n"
      "v_mul_f32 v130, v130, v158\n"
      "v_mul_f32 v138, v138, v162\n"
      "v_mfma_f32_16x16x32_bf16 v[124:127], a[48:51], v[100:103], v[124:127]\n"
      "s_waitcnt lgkmcnt(2)\n"
      "v_mul_f32 v161, v133, v161\n"
      "v_fmac_f32 v137, v176, v153\n"
      "v_mfma_f32_16x16x32_bf16 v[124:127], a[52:55], v[104:107], v[124:127]\n"
      "v_cvt_pk_bf16_f32 v165, v130, v131\n"
      "v_fmac_f32 v139, v176, v155\n"
      "v_mfma_f32_16x16x32_bf16 v[124:127], a[56:59], v[108:111], v[124:127]\n"
      "v_rcp_f32 v160, v160\n"
      "v_mfma_f32_16x16x32_bf16 v[124:127], a[60:63], v[112:115], v[124:127]\n"
      "s_waitcnt lgkmcnt(0)\n"
      "v_mul_f32 v160, v132, v160\n"
      "v_cvt_pk_bf16_f32 v164, v128, v129\n"
      "v_mul_f32 v136, v136, v160\n"
      "v_mul_f32 v137, v137, v161\n"
      "v_mfma_f32_16x16x32_bf16 v[124:127], a[64:67], v[116:119], v[124:127]\n"
      "v_mul_f32 v139, v139, v163\n"
      "v_sub_f32 v176, -v168, v170\n"
      "v_mfma_f32_16x16x32_bf16 v[124:127], a[68:71], v[120:123], v[124:127]\n"
      "v_cvt_pk_bf16_f32 v167, v138, v139\n"
      "v_cvt_pk_bf16_f32 v166, v136, v137\n"
      "v_mfma_f32_16x16x32_bf16 v[132:135], a[144:147], v[52:55], 0\n"
      "buffer_store_dwordx4 v[164:167], v0, s[8:11], s37 offen sc1\n"
      "v_mfma_f32_16x16x32_bf16 v[132:135], a[148:151], v[56:59], v[132:135]\n"
      "v_add_f32 v169, v169, v171\n"
      "v_fma_f32 v169, -v176, v176, v169\n"
      "v_mfma_f32_16x16x32_bf16 v[132:135], a[152:155], v[60:63], v[132:135]\n"
      "v_max_f32 v177, 0x3727c5ac, v169\n"
      "v_fmac_f32 v126, v176, v142\n"
      "v_mfma_f32_16x16x32_bf16 v[132:135], a[156:159], v[64:67], v[132:135]\n"
      "v_rsq_f32 v170, v177\n"
      "v_mfma_f32_16x16x32_bf16 v[132:135], a[160:163], v[68:71], v[132:135]\n"
      "v_add_u32 v11, 1, v11\n"
      "v_mul_f32 v158, v126, v126\n"
      "v_mfma_f32_16x16x32_bf16 v[132:135], a[164:167], v[72:75], v[132:135]\n"
      "v_mul_f32 v178, 0xc0135761, v170\n"
      "v_mul_f32 v171, 0x3d372713, v178\n"
      "v_mfma_f32_16x16x32_bf16 v[132:135], a[168:171], v[76:79], v[132:135]\n"
      "v_fmac_f32 v125, v176, v141\n"
      "v_fmac_f32 v127, v176, v143\n"
      "v_mfma_f32_16x16x32_bf16 v[132:135], a[172:175], v[80:83], v[132:135]\n"
      "s_add_u32 s37, s37, 165888\n"
      "v_mul_f32 v170, v170, v170\n"
      "v_fmac_f32 v124, v176, v140\n"
      "v_mfma_f32_16x16x32_bf16 v[132:135], a[176:179], v[84:87], v[132:135]\n"
      "v_mul_f32 v179, v171, v170\n"
      "v_mul_f32 v157, v125, v125\n"
      "v_mfma_f32_16x16x32_bf16 v[132:135], a[180:183], v[88:91], v[132:135]\n"
      "v_mul_f32 v159, v127, v127\n"
      "v_fma_f32 v158, v158, v179, v178\n"
      "v_mfma_f32_16x16x32_bf16 v[132:135], a[184:187], v[92:95], v[132:135]\n"
      "v_fma_f32 v159, v159, v179, v178\n"
      "v_mul_f32 v159, v127, v159\n"
      "v_mfma_f32_16x16x32_bf16 v[132:135], a[188:191], v[96:99], v[132:135]\n"
      "v_mul_f32 v156, v124, v124\n"
      "v_mul_f32 v158, v126, v158\n"
      "v_mfma_f32_16x16x32_bf16 v[132:135], a[192:195], v[100:103], v[132:135]\n"
      "v_fma_f32 v157, v157, v179, v178\n"
      "v_fma_f32 v156, v156, v179, v178\n"
      "v_mfma_f32_16x16x32_bf16 v[132:135], a[196:199], v[104:107], v[132:135]\n"
      "s_add_u32 s36, s36, 24576\n"
      "v_mul_f32 v156, v124, v156\n"
      "v_mul_f32 v157, v125, v157\n"
      "v_mfma_f32_16x16x32_bf16 v[132:135], a[200:203], v[108:111], v[132:135]\n"
      "s_add_u32 m0, s38, 40960\n"
      "v_exp_f32 v156, v156\n"
      "v_mfma_f32_16x16x32_bf16 v[132:135], a[204:207], v[112:115], v[132:135]\n"
      "v_exp_f32 v158, v158\n"
      "v_mfma_f32_16x16x32_bf16 v[132:135], a[208:211], v[116:119], v[132:135]\n"
      "v_fma_f32 v156, v156, v177, v177\n"
      "v_fma_f32 v158, v158, v177, v177\n"
      "v_mfma_f32_16x16x32_bf16 v[132:135], a[212:215], v[120:123], v[132:135]\n"
      "s_waitcnt vmcnt(3)\n"
      "ds_write_b32 v13, v11\n"
      "v_mfma_f32_16x16x32_bf16 v[128:131], a[72:75], v[52:55], 0\n"
      "v_exp_f32 v157, v157\n"
      "v_mfma_f32_16x16x32_bf16 v[128:131], a[76:79], v[56:59], v[128:131]\n"
      "s_add_u32 s42, s42, 165888\n"
      "v_rcp_f32 v156, v156\n"
      "v_mfma_f32_16x16x32_bf16 v[128:131], a[80:83], v[60:63], v[128:131]\n"
      "v_fmac_f32 v133, v176, v149\n"
      "v_mul_f32 v161, v133, v133\n"
      "v_mfma_f32_16x16x32_bf16 v[128:131], a[84:87], v[64:67], v[128:131]\n"
      "v_fmac_f32 v135, v176, v151\n"
      "v_fmac_f32 v132, v176, v148\n"
      "v_mfma_f32_16x16x32_bf16 v[128:131], a[88:91], v[68:71], v[128:131]\n"
      "v_rcp_f32 v158, v158\n"
      "v_mfma_f32_16x16x32_bf16 v[128:131], a[92:95], v[72:75], v[128:131]\n"
      "v_mul_f32 v163, v135, v135\n"
      "v_fmac_f32 v134, v176, v150\n"
      "v_mfma_f32_16x16x32_bf16 v[128:131], a[96:99], v[76:79], v[128:131]\n"
      "s_waitcnt vmcnt(1) lgkmcnt(0)\n"
      "v_cmp_ne_u32 vcc, s21, v18\n"
      "v_fma_f32 v161, v161, v179, v178\n"
      "v_mfma_f32_16x16x32_bf16 v[128:131], a[100:103], v[80:83], v[128:131]\n"
      "v_mul_f32 v162, v134, v134\n"
      "v_mul_f32 v161, v133, v161\n"
      "v_mfma_f32_16x16x32_bf16 v[128:131], a[104:107], v[84:87], v[128:131]\n"
      "ds_read_b32 v12, v14\n"
      "v_fma_f32 v157, v157, v177, v177\n"
      "v_mfma_f32_16x16x32_bf16 v[128:131], a[108:111], v[88:91], v[128:131]\n"
      "s_cbranch_vccnz .Lsp_g0_4\n"
      ".Lbk_g0_4:\n"
      "v_fma_f32 v163, v163, v179, v178\n"
      "v_mfma_f32_16x16x32_bf16 v[128:131], a[112:115], v[92:95], v[128:131]\n"
      "buffer_load_dwordx4 v0, s[8:11], s40 offen offset:2048 sc1 lds\n"
      "v_mfma_f32_16x16x32_bf16 v[128:131], a[116:119], v[96:99], v[128:131]\n"
      "v_exp_f32 v161, v161\n"
      "v_mfma_f32_16x16x32_bf16 v[128:131], a[120:123], v[100:103], v[128:131]\n"
      "v_exp_f32 v159, v159\n"
      "v_mfma_f32_16x16x32_bf16 v[128:131], a[124:127], v[104:107], v[128:131]\n"
      "s_waitcnt lgkmcnt(0)\n"
      "v_fma_f32 v159, v159, v177, v177\n"
      "v_cmp_gt_i32 vcc, v11, v12\n"
      "v_mfma_f32_16x16x32_bf16 v[128:131], a[128:131], v[108:111], v[128:131]\n"
      "buffer_load_dwordx4 v0, s[8:11], s40 offen offset:1024 sc1 lds\n"
      "v_mfma_f32_16x16x32_bf16 v[128:131], a[132:135], v[112:115], v[128:131]\n"
      "v_rcp_f32 v159, v159\n"
      "v_mfma_f32_16x16x32_bf16 v[128:131], a[136:139], v[116:119], v[128:131]\n"
      "v_mul_f32 v163, v135, v163\n"
      "v_fma_f32 v161, v161, v177, v177\n"
      "v_mfma_f32_16x16x32_bf16 v[128:131], a[140:143], v[120:123], v[128:131]\n"
      "v_rcp_f32 v157, v157\n"
      "v_mfma_f32_16x16x32_bf16 v[136:139], a[216:219], v[52:55], 0\n"
      "buffer_load_dwordx4 v0, s[8:11], s40 offen offset:0 sc1 lds\n"
      "v_mfma_f32_16x16x32_bf16 v[136:139], a[220:223], v[56:59], v[136:139]\n"
      "s_cbranch_vccnz .Lsp_gy0_5\n"
      ".Lbk_gy0_5:\n"
      "v_mul_f32 v160, v132, v132\n"
      "v_mfma_f32_16x16x32_bf16 v[136:139], a[224:227], v[60:63], v[136:139]\n"
      "ds_read_b128 v[52:55], v0 offset:20480\n"
      "v_mul_f32 v159, v127, v159\n"
      "v_mfma_f32_16x16x32_bf16 v[136:139], a[228:231], v[64:67], v[136:139]\n"
      "ds_read_b128 v[56:59], v0 offset:21504\n"
      "v_fma_f32 v160, v160, v179, v178\n"
      "v_mfma_f32_16x16x32_bf16 v[136:139], a[232:235], v[68:71], v[136:139]\n"
      "buffer_load_dwordx4 v0, s[8:11], s40 offen offset:3072 sc1 lds\n"
      "v_mfma_f32_16x16x32_bf16 v[136:139], a[236:239], v[72:75], v[136:139]\n"
      "s_min_u32 s36, s36, s6\n"
      "v_mul_f32 v156, v124, v156\n"
      "v_mul_f32 v158, v126, v158\n"
      "v_mfma_f32_16x16x32_bf16 v[136:139], a[240:243], v[76:79], v[136:139]\n"
      "ds_read_b128 v[60:63], v0 offset:22528\n"
      "v_mul_f32 v157, v125, v157\n"
      "v_mfma_f32_16x16x32_bf16 v[136:139], a[244:247], v[80:83], v[136:139]\n"
      "ds_read_b128 v[76:79], v0 offset:26624\n"
      "v_mul_f32 v160, v132, v160\n"
      "v_mfma_f32_16x16x32_bf16 v[136:139], a[248:251], v[84:87], v[136:139]\n"
      "s_add_u32 m0, s39, 40960\n"
      "ds_read_b128 v[68:71], v0 offset:24576\n"
      "buffer_load_dwordx4 v1, s[8:11], s40 offen offset:0 sc1 lds\n"
      "v_mfma_f32_16x16x32_bf16 v[136:139], a[252:255], v[88:91], v[136:139]\n"
      "ds_read_b128 v[72:75], v0 offset:25600\n"
      "v_fmac_f32 v128, v176, v144\n"
      "v_mfma_f32_16x16x32_bf16 v[136:139], v[20:23], v[92:95], v[136:139]\n"
      "v_exp_f32 v160, v160\n"
      "v_mfma_f32_16x16x32_bf16 v[136:139], v[24:27], v[96:99], v[136:139]\n"
      "ds_read_b128 v[96:99], v0 offset:31744\n"
      "v_exp_f32 v163, v163\n"
      "v_mfma_f32_16x16x32_bf16 v[136:139], v[28:31], v[100:103], v[136:139]\n"
      "s_waitcnt vmcnt(5) lgkmcnt(5)\n"
      "buffer_store_dword v15, v3, s[8:11], s42 offen sc1\n"
      "v_mfma_f32_16x16x32_bf16 v[136:139], v[32:35], v[104:107], v[136:139]\n"
      "ds_read_b128 v[64:67], v0 offset:23552\n"
      "v_fma_f32 v162, v162, v179, v178\n"
      "v_mfma_f32_16x16x32_bf16 v[136:139], v[36:39], v[108:111], v[136:139]\n"
      "ds_read_b128 v[84:87], v0 offset:28672\n"
      "v_mul_f32 v162, v134, v162\n"
      "v_mfma_f32_16x16x32_bf16 v[136:139], v[40:43], v[112:115], v[136:139]\n"
      "ds_read_b128 v[88:91], v0 offset:29696\n"
      "v_fmac_f32 v129, v176, v145\n"
      "v_mfma_f32_16x16x32_bf16 v[136:139], v[44:47], v[116:119], v[136:139]\n"
      "buffer_load_dword v18, v2, s[8:11], s36 offen sc1\n"
      "v_mfma_f32_16x16x32_bf16 v[136:139], v[48:51], v[120:123], v[136:139]\n"
      "ds_read_b128 v[80:83], v0 offset:27648\n"
      "v_mul_f32 v128, v128, v156\n"
      "v_mfma_f32_16x16x32_bf16 v[124:127], a[0:3], v[52:55], 0\n"
      "s_mov_b32 s40, s36\n"
      "ds_read_b128 v[120:123], v0 offset:37888\n"
      "v_fmac_f32 v130, v176, v146\n"
      "v_mfma_f32_16x16x32_bf16 v[124:127], a[4:7], v[56:59], v[124:127]\n"
      "s_waitcnt lgkmcnt(5)\n"
      "ds_read_b128 v[108:111], v0 offset:34816\n"
      "v_fma_f32 v160, v160, v177, v177\n"
      "v_mfma_f32_16x16x32_bf16 v[124:127], a[8:11], v[60:63], v[124:127]\n"
      "s_waitcnt lgkmcnt(5)\n"
      "ds_read_b128 v[92:95], v0 offset:30720\n"
      "v_fma_f32 v163, v163, v177, v177\n"
      "v_mfma_f32_16x16x32_bf16 v[124:127], a[12:15], v[64:67], v[124:127]\n"
      "ds_read_b128 v[112:115], v0 offset:35840\n"
      "v_fmac_f32 v131, v176, v147\n"
      "v_mfma_f32_16x16x32_bf16 v[124:127], a[16:19], v[68:71], v[124:127]\n"
      "ds_read_b128 v[100:103], v0 offset:32768\n"
      "v_exp_f32 v162, v162\n"
      "v_mfma_f32_16x16x32_bf16 v[124:127], a[20:23], v[72:75], v[124:127]\n"
      "ds_read_b128 v[104:107], v0 offset:33792\n"
      "v_mul_f32 v129, v129, v157\n"
      "v_fma_f32 v162, v162, v177, v177\n"
      "v_fmac_f32 v136, v176, v152\n"
      "v_mfma_f32_16x16x32_bf16 v[124:127], a[24:27], v[76:79], v[124:127]\n"
      "s_waitcnt lgkmcnt(6)\n"
      "v_mul_f32 v131, v131, v159\n"
      "v_fmac_f32 v138, v176, v154\n"
      "v_mfma_f32_16x16x32_bf16 v[124:127], a[28:31], v[80:83], v[124:127]\n"
      "ds_read_b128 v[116:119], v0 offset:36864\n"
      "v_rcp_f32 v163, v163\n"
      "v_mfma_f32_16x16x32_bf16 v[124:127], a[32:35], v[84:87], v[124:127]\n"
      "ds_read2st64_b64 v[168:171], v4 offset0:40 offset1:41\n"
      "v_rcp_f32 v162, v162\n"
      "v_mfma_f32_16x16x32_bf16 v[124:127], a[36:39], v[88:91], v[124:127]\n"
      "s_waitcnt lgkmcnt(5)\n"
      "v_rcp_f32 v161, v161\n"
      "v_mfma_f32_16x16x32_bf16 v[124:127], a[40:43], v[92:95], v[124:127]\n"
      "v_mul_f32 v162, v134, v162\n"
      "v_mul_f32 v163, v135, v163\n"
      "v_mfma_f32_16x16x32_bf16 v[124:127], a[44:47], v[96:99], v[124:127]\n"
      "s_waitcnt lgkmcnt(3)\n"
      "v_mul_f32 v130, v130, v158\n"
      "v_mul_f32 v138, v138, v162\n"
      "v_mfma_f32_16x16x32_bf16 v[124:127], a[48:51], v[100:103], v[124:127]\n"
      "s_waitcnt lgkmcnt(2)\n"
      "v_mul_f32 v161, v133, v161\n"
      "v_fmac_f32 v137, v176, v153\n"
      "v_mfma_f32_16x16x32_bf16 v[124:127], a[52:55], v[104:107], v[124:127]\n"
      "v_cvt_pk_bf16_f32 v165, v130, v131\n"
      "v_fmac_f32 v139, v176, v155\n"
      "v_mfma_f32_16x16x32_bf16 v[124:127], a[56:59], v[108:111], v[124:127]\n"
      "v_rcp_f32 v160, v160\n"
      "v_mfma_f32_16x16x32_bf16 v[124:127], a[60:63], v[112:115], v[124:127]\n"
      "s_waitcnt lgkmcnt(0)\n"
      "v_mul_f32 v160, v132, v160\n"
      "v_cvt_pk_bf16_f32 v164, v128, v129\n"
      "v_mul_f32 v136, v136, v160\n"
      "v_mul_f32 v137, v137, v161\n"
      "v_mfma_f32_16x16x32_bf16 v[124:127], a[64:67], v[116:119], v[124:127]\n"
      "v_mul_f32 v139, v139, v163\n"
      "v_sub_f32 v176, -v168, v170\n"
      "v_mfma_f32_16x16x32_bf16 v[124:127], a[68:71], v[120:123], v[124:127]\n"
      "v_cvt_pk_bf16_f32 v167, v138, v139\n"
      "v_cvt_pk_bf16_f32 v166, v136, v137\n"
      "v_mfma_f32_16x16x32_bf16 v[132:135], a[144:147], v[52:55], 0\n"
      "buffer_store_dwordx4 v[164:167], v0, s[8:11], s37 offen sc1\n"
      "v_mfma_f32_16x16x32_bf16 v[132:135], a[148:151], v[56:59], v[132:135]\n"
      "v_add_f32 v169, v169, v171\n"
      "v_fma_f32 v169, -v176, v176, v169\n"
      "v_mfma_f32_16x16x32_bf16 v[132:135], a[152:155], v[60:63], v[132:135]\n"
      "v_max_f32 v177, 0x3727c5ac, v169\n"
      "v_fmac_f32 v126, v176, v142\n"
      "v_mfma_f32_16x16x32_bf16 v[132:135], a[156:159], v[64:67], v[132:135]\n"
      "v_rsq_f32 v170, v177\n"
      "v_mfma_f32_16x16x32_bf16 v[132:135], a[160:163], v[68:71], v[132:135]\n"
      "v_add_u32 v11, 1, v11\n"
      "v_mul_f32 v158, v126, v126\n"
      "v_mfma_f32_16x16x32_bf16 v[132:135], a[164:167], v[72:75], v[132:135]\n"
      "v_mul_f32 v178, 0xc0135761, v170\n"
      "v_mul_f32 v171, 0x3d372713, v178\n"
      "v_mfma_f32_16x16x32_bf16 v[132:135], a[168:171], v[76:79], v[132:135]\n"
      "v_fmac_f32 v125, v176, v141\n"
      "v_fmac_f32 v127, v176, v143\n"
      "v_mfma_f32_16x16x32_bf16 v[132:135], a[172:175], v[80:83], v[132:135]\n"
      "s_add_u32 s37, s37, 165888\n"
      "v_mul_f32 v170, v170, v170\n"
      "v_fmac_f32 v124, v176, v140\n"
      "v_mfma_f32_16x16x32_bf16 v[132:135], a[176:179], v[84:87], v[132:135]\n"
      "v_mul_f32 v179, v171, v170\n"
      "v_mul_f32 v157, v125, v125\n"
      "v_mfma_f32_16x16x32_bf16 v[132:135], a[180:183], v[88:91], v[132:135]\n"
      "v_mul_f32 v159, v127, v127\n"
      "v_fma_f32 v158, v158, v179, v178\n"
      "v_mfma_f32_16x16x32_bf16 v[132:135], a[184:187], v[92:95], v[132:135]\n"
      "v_fma_f32 v159, v159, v179, v178\n"
      "v_mul_f32 v159, v127, v159\n"
      "v_mfma_f32_16x16x32_bf16 v[132:135], a[188:191], v[96:99], v[132:135]\n"
      "v_mul_f32 v156, v124, v124\n"
      "v_mul_f32 v158, v126, v158\n"
      "v_mfma_f32_16x16x32_bf16 v[132:135], a[192:195], v[100:103], v[132:135]\n"
      "v_fma_f32 v157, v157, v179, v178\n"
      "v_fma_f32 v156, v156, v179, v178\n"
      "v_mfma_f32_16x16x32_bf16 v[132:135], a[196:199], v[104:107], v[132:135]\n"
      "s_add_u32 s36, s36, 24576\n"
      "v_mul_f32 v156, v124, v156\n"
      "v_mul_f32 v157, v125, v157\n"
      "v_mfma_f32_16x16x32_bf16 v[132:135], a[200:203], v[108:111], v[132:135]\n"
      "s_add_u32 m0, s38, 61440\n"
      "v_exp_f32 v156, v156\n"
      "v_mfma_f32_16x16x32_bf16 v[132:135], a[204:207], v[112:115], v[132:135]\n"
      "v_exp_f32 v158, v158\n"
      "v_mfma_f32_16x16x32_bf16 v[132:135], a[208:211], v[116:119], v[132:135]\n"
      "v_fma_f32 v156, v156, v177, v177\n"
      "v_fma_f32 v158, v158, v177, v177\n"
      "v_mfma_f32_16x16x32_bf16 v[132:135], a[212:215], v[120:123], v[132:135]\n"
      "s_waitcnt vmcnt(3)\n"
      "ds_write_b32 v13, v11\n"
      "v_mfma_f32_16x16x32_bf16 v[128:131], a[72:75], v[52:55], 0\n"
      "v_exp_f32 v157, v157\n"
      "v_mfma_f32_16x16x32_bf16 v[128:131], a[76:79], v[56:59], v[128:131]\n"
      "s_add_u32 s42, s42, 165888\n"
      "v_rcp_f32 v156, v156\n"
      "v_mfma_f32_16x16x32_bf16 v[128:131], a[80:83], v[60:63], v[128:131]\n"
      "v_fmac_f32 v133, v176, v149\n"
      "v_mul_f32 v161, v133, v133\n"
      "v_mfma_f32_16x16x32_bf16 v[128:131], a[84:87], v[64:67], v[128:131]\n"
      "v_fmac_f32 v135, v176, v151\n"
      "v_fmac_f32 v132, v176, v148\n"
      "v_mfma_f32_16x16x32_bf16 v[128:131], a[88:91], v[68:71], v[128:131]\n"
      "v_rcp_f32 v158, v158\n"
      "v_mfma_f32_16x16x32_bf16 v[128:131], a[92:95], v[72:75], v[128:131]\n"
      "v_mul_f32 v163, v135, v135\n"
      "v_fmac_f32_e64 v134, v176, v150\n"
      "v_mfma_f32_16x16x32_bf16 v[128:131], a[96:99], v[76:79], v[128:131]\n"
      "s_waitcnt vmcnt(1) lgkmcnt(0)\n"
      "v_cmp_ne_u32 vcc, s21, v18\n"
      "v_fma_f32 v161, v161, v179, v178\n"
      "v_mfma_f32_16x16x32_bf16 v[128:131], a[100:103], v[80:83], v[128:131]\n"
      "v_mul_f32_e64 v162, v134, v134\n"
      "v_mul_f32_e64 v161, v133, v161\n"
      "v_mfma_f32_16x16x32_bf16 v[128:131], a[104:107], v[84:87], v[128:131]\n"
      "ds_read_b32 v12, v14\n"
      "v_fma_f32 v157, v157, v177, v177\n"
      "v_mfma_f32_16x16x32_bf16 v[128:131], a[108:111], v[88:91], v[128:131]\n"
      "s_cbranch_vccnz .Lsp_g0_6\n"
      ".Lbk_g0_6:\n"
      "v_fma_f32 v163, v163, v179, v178\n"
      "v_mfma_f32_16x16x32_bf16 v[128:131], a[112:115], v[92:95], v[128:131]\n"
      "buffer_load_dwordx4 v0, s[8:11], s40 offen offset:2048 sc1 lds\n"
      "v_mfma_f32_16x16x32_bf16 v[128:131], a[116:119], v[96:99], v[128:131]\n"
      "v_exp_f32 v161, v161\n"
      "v_mfma_f32_16x16x32_bf16 v[128:131], a[120:123], v[100:103], v[128:131]\n"
      "v_exp_f32 v159, v159\n"
      "v_mfma_f32_16x16x32_bf16 v[128:131], a[124:127], v[104:107], v[128:131]\n"
      "s_waitcnt lgkmcnt(0)\n"
      "v_fma_f32 v159, v159, v177, v177\n"
      "v_cmp_gt_i32 vcc, v11, v12\n"
      "v_mfma_f32_16x16x32_bf16 v[128:131], a[128:131], v[108:111], v[128:131]\n"
      "buffer_load_dwordx4 v0, s[8:11], s40 offen offset:1024 sc1 lds\n"
      "v_mfma_f32_16x16x32_bf16 v[128:131], a[132:135], v[112:115], v[128:131]\n"
      "v_rcp_f32 v159, v159\n"
      "v_mfma_f32_16x16x32_bf16 v[128:131], a[136:139], v[116:119], v[128:131]\n"
      "v_mul_f32 v163, v135, v163\n"
      "v_fma_f32 v161, v161, v177, v177\n"
      "v_mfma_f32_16x16x32_bf16 v[128:131], a[140:143], v[120:123], v[128:131]\n"
      "v_rcp_f32 v157, v157\n"
      "v_mfma_f32_16x16x32_bf16 v[136:139], a[216:219], v[52:55], 0\n"
      "buffer_load_dwordx4 v0, s[8:11], s40 offen offset:0 sc1 lds\n"
      "v_mfma_f32_16x16x32_bf16 v[136:139], a[220:223], v[56:59], v[136:139]\n"
      "s_cbranch_vccnz .Lsp_gy0_7\n"
      ".Lbk_gy0_7:\n"
      "v_mul_f32 v160, v132, v132\n"
      "v_mfma_f32_16x16x32_bf16 v[136:139], a[224:227], v[60:63], v[136:139]\n"
      "ds_read_b128 v[52:55], v17 offset:0\n"
      "v_mul_f32 v159, v127, v159\n"
      "v_mfma_f32_16x16x32_bf16 v[136:139], a[228:231], v[64:67], v[136:139]\n"
      "ds_read_b128 v[56:59], v17 offset:1024\n"
      "v_fma_f32 v160, v160, v179, v178\n"
      "v_mfma_f32_16x16x32_bf16 v[136:139], a[232:235], v[68:71], v[136:139]\n"
      "buffer_load_dwordx4 v0, s[8:11], s40 offen offset:3072 sc1 lds\n"
      "v_mfma_f32_16x16x32_bf16 v[136:139], a[236:239], v[72:75], v[136:139]\n"
      "s_min_u32 s36, s36, s6\n"
      "v_mul_f32 v156, v124, v156\n"
      "v_mul_f32 v158, v126, v158\n"
      "v_mfma_f32_16x16x32_bf16 v[136:139], a[240:243], v[76:79], v[136:139]\n"
      "ds_read_b128 v[60:63], v17 offset:2048\n"
      "v_mul_f32 v157, v125, v157\n"
      "v_mfma_f32_16x16x32_bf16 v[136:139], a[244:247], v[80:83], v[136:139]\n"
      "ds_read_b128 v[76:79], v17 offset:6144\n"
      "v_mul_f32 v160, v132, v160\n"
      "v_mfma_f32_16x16x32_bf16 v[136:139], a[248:251], v[84:87], v[136:139]\n"
      "s_add_u32 m0, s39, 61440\n"
      "ds_read_b128 v[68:71], v17 offset:4096\n"
      "buffer_load_dwordx4 v1, s[8:11], s40 offen offset:0 sc1 lds\n"
      "v_mfma_f32_16x16x32_bf16 v[136:139], a[252:255], v[88:91], v[136:139]\n"
      "ds_read_b128 v[72:75], v17 offset:5120\n"
      "v_fmac_f32 v128, v176, v144\n"
      "v_mfma_f32_16x16x32_bf16 v[136:139], v[20:23], v[92:95], v[136:139]\n"
      "v_exp_f32 v160, v160\n"
      "v_mfma_f32_16x16x32_bf16 v[136:139], v[24:27], v[96:99], v[136:139]\n"
      "ds_read_b128 v[96:99], v17 offset:11264\n"
      "v_exp_f32 v163, v163\n"
      "v_mfma_f32_16x16x32_bf16 v[136:139], v[28:31], v[100:103], v[136:139]\n"
      "s_waitcnt vmcnt(5) lgkmcnt(5)\n"
      "buffer_store_dword v15, v3, s[8:11], s42 offen sc1\n"
      "v_mfma_f32_16x16x32_bf16 v[136:139], v[32:35], v[104:107], v[136:139]\n"
      "ds_read_b128 v[64:67], v17 offset:3072\n"
      "v_fma_f32 v162, v162, v179, v178\n"
      "v_mfma_f32_16x16x32_bf16 v[136:139], v[36:39], v[108:111], v[136:139]\n"
      "ds_read_b128 v[84:87], v17 offset:8192\n"
      "v_mul_f32 v162, v134, v162\n"
      "v_mfma_f32_16x16x32_bf16 v[136:139], v[40:43], v[112:115], v[136:139]\n"
      "ds_read_b128 v[88:91], v17 offset:9216\n"
      "v_fmac_f32 v129, v176, v145\n"
      "v_mfma_f32_16x16x32_bf16 v[136:139], v[44:47], v[116:119], v[136:139]\n"
      "buffer_load_dword v18, v2, s[8:11], s36 offen sc1\n"
      "v_mfma_f32_16x16x32_bf16 v[136:139], v[48:51], v[120:123], v[136:139]\n"
      "ds_read_b128 v[80:83], v17 offset:7168\n"
      "v_mul_f32 v128, v128, v156\n"
      "v_mfma_f32_16x16x32_bf16 v[124:127], a[0:3], v[52:55], 0\n"
      "s_mov_b32 s40, s36\n"
      "ds_read_b128 v[120:123], v17 offset:17408\n"
      "v_fmac_f32 v130, v176, v146\n"
      "v_mfma_f32_16x16x32_bf16 v[124:127], a[4:7], v[56:59], v[124:127]\n"
      "s_waitcnt lgkmcnt(5)\n"
      "ds_read_b128 v[108:111], v17 offset:14336\n"
      "v_fma_f32 v160, v160, v177, v177\n"
      "v_mfma_f32_16x16x32_bf16 v[124:127], a[8:11], v[60:63], v[124:127]\n"
      "s_waitcnt lgkmcnt(5)\n"
      "ds_read_b128 v[92:95], v17 offset:10240\n"
      "v_fma_f32 v163, v163, v177, v177\n"
      "v_mfma_f32_16x16x32_bf16 v[124:127], a[12:15], v[64:67], v[124:127]\n"
      "ds_read_b128 v[112:115], v17 offset:15360\n"
      "v_fmac_f32 v131, v176, v147\n"
      "v_mfma_f32_16x16x32_bf16 v[124:127], a[16:19], v[68:71], v[124:127]\n"
      "ds_read_b128 v[100:103], v17 offset:12288\n"
      "v_exp_f32 v162, v162\n"
      "v_mfma_f32_16x16x32_bf16 v[124:127], a[20:23], v[72:75], v[124:127]\n"
      "ds_read_b128 v[104:107], v17 offset:13312\n"
      "v_mul_f32 v129, v129, v157\n"
      "v_fma_f32 v162, v162, v177, v177\n"
      "v_fmac_f32 v136, v176, v152\n"
      "v_mfma_f32_16x16x32_bf16 v[124:127], a[24:27], v[76:79], v[124:127]\n"
      "s_waitcnt lgkmcnt(6)\n"
      "v_mul_f32 v131, v131, v159\n"
      "v_fmac_f32 v138, v176, v154\n"
      "v_mfma_f32_16x16x32_bf16 v[124:127], a[28:31], v[80:83], v[124:127]\n"
      "ds_read_b128 v[116:119], v17 offset:16384\n"
      "v_rcp_f32 v163, v163\n"
      "v_mfma_f32_16x16x32_bf16 v[124:127], a[32:35], v[84:87], v[124:127]\n"
      "ds_read2st64_b64 v[168:171], v4 offset0:80 offset1:81\n"
      "v_rcp_f32 v162, v162\n"
      "v_mfma_f32_16x16x32_bf16 v[124:127], a[36:39], v[88:91], v[124:127]\n"
      "s_waitcnt lgkmcnt(5)\n"
      "v_rcp_f32 v161, v161\n"
      "v_mfma_f32_16x16x32_bf16 v[124:127], a[40:43], v[92:95], v[124:127]\n"
      "v_mul_f32 v162, v134, v162\n"
      "v_mul_f32 v163, v135, v163\n"
      "v_mfma_f32_16x16x32_bf16 v[124:127], a[44:47], v[96:99], v[124:127]\n"
      "s_waitcnt lgkmcnt(3)\n"
      "v_mul_f32 v130, v130, v158\n"
      "v_mul_f32 v138, v138, v162\n"
      "v_mfma_f32_16x16x32_bf16 v[124:127], a[48:51], v[100:103], v[124:127]\n"
      "s_waitcnt lgkmcnt(2)\n"
      "v_mul_f32 v161, v133, v161\n"
      "v_fmac_f32 v137, v176, v153\n"
      "v_mfma_f32_16x16x32_bf16 v[124:127], a[52:55], v[104:107], v[124:127]\n"
      "v_cvt_pk_bf16_f32 v165, v130, v131\n"
      "v_fmac_f32 v139, v176, v155\n"
      "v_mfma_f32_16x16x32_bf16 v[124:127], a[56:59], v[108:111], v[124:127]\n"
      "v_rcp_f32 v160, v160\n"
      "v_mfma_f32_16x16x32_bf16 v[124:127], a[60:63], v[112:115], v[124:127]\n"
      "s_waitcnt lgkmcnt(0)\n"
      "v_mul_f32 v160, v132, v160\n"
      "v_cvt_pk_bf16_f32 v164, v128, v129\n"
      "v_mul_f32 v136, v136, v160\n"
      "v_mul_f32 v137, v137, v161\n"
      "v_mfma_f32_16x16x32_bf16 v[124:127], a[64:67], v[116:119], v[124:127]\n"
      "v_mul_f32 v139, v139, v163\n"
      "v_sub_f32 v176, -v168, v170\n"
      "v_mfma_f32_16x16x32_bf16 v[124:127], a[68:71], v[120:123], v[124:127]\n"
      "v_cvt_pk_bf16_f32 v167, v138, v139\n"
      "v_cvt_pk_bf16_f32 v166, v136, v137\n"
      "v_mfma_f32_16x16x32_bf16 v[132:135], a[144:147], v[52:55], 0\n"
      "buffer_store_dwordx4 v[164:167], v0, s[8:11], s37 offen sc1\n"
      "v_mfma_f32_16x16x32_bf16 v[132:135], a[148:151], v[56:59], v[132:135]\n"
      "v_add_f32 v169, v169, v171\n"
      "v_fma_f32 v169, -v176, v176, v169\n"
      "v_mfma_f32_16x16x32_bf16 v[132:135], a[152:155], v[60:63], v[132:135]\n"
      "v_max_f32 v177, 0x3727c5ac, v169\n"
      "v_fmac_f32 v126, v176, v142\n"
      "v_mfma_f32_16x16x32_bf16 v[132:135], a[156:159], v[64:67], v[132:135]\n"
      "v_rsq_f32 v170, v177\n"
      "v_mfma_f32_16x16x32_bf16 v[132:135], a[160:163], v[68:71], v[132:135]\n"
      "v_add_u32 v11, 1, v11\n"
      "v_mul_f32 v158, v126, v126\n"
      "v_mfma_f32_16x16x32_bf16 v[132:135], a[164:167], v[72:75], v[132:135]\n"
      "v_mul_f32 v178, 0xc0135761, v170\n"
      "v_mul_f32 v171, 0x3d372713, v178\n"
      "v_mfma_f32_16x16x32_bf16 v[132:135], a[168:171], v[76:79], v[132:135]\n"
      "v_fmac_f32 v125, v176, v141\n"
      "v_fmac_f32 v127, v176, v143\n"
      "v_mfma_f32_16x16x32_bf16 v[132:135], a[172:175], v[80:83], v[132:135]\n"
      "s_add_u32 s37, s37, 165888\n"
      "v_mul_f32 v170, v170, v170\n"
      "v_fmac_f32 v124, v176, v140\n"
      "v_mfma_f32_16x16x32_bf16 v[132:135], a[176:179], v[84:87], v[132:135]\n"
      "v_mul_f32 v179, v171, v170\n"
      "v_mul_f32 v157, v125, v125\n"
      "v_mfma_f32_16x16x32_bf16 v[132:135], a[180:183], v[88:91], v[132:135]\n"
      "v_mul_f32 v159, v127, v127\n"
      "v_fma_f32 v158, v158, v179, v178\n"
      "v_mfma_f32_16x16x32_bf16 v[132:135], a[184:187], v[92:95], v[132:135]\n"
      "v_fma_f32 v159, v159, v179, v178\n"
      "v_mul_f32 v159, v127, v159\n"
      "v_mfma_f32_16x16x32_bf16 v[132:135], a[188:191], v[96:99], v[132:135]\n"
      "v_mul_f32 v156, v124, v124\n"
      "v_mul_f32 v158, v126, v158\n"
      "v_mfma_f32_16x16x32_bf16 v[132:135], a[192:195], v[100:103], v[132:135]\n"
      "v_fma_f32 v157, v157, v179, v178\n"
      "v_fma_f32 v156, v156, v179, v178\n"
      "v_mfma_f32_16x16x32_bf16 v[132:135], a[196:199], v[104:107], v[132:135]\n"
      "s_add_u32 s36, s36, 24576\n"
      "v_mul_f32 v156, v124, v156\n"
      "v_mul_f32 v157, v125, v157\n"
      "v_mfma_f32_16x16x32_bf16 v[132:135], a[200:203], v[108:111], v[132:135]\n"
      "s_add_u32 m0, s38, 0\n"
      "v_exp_f32 v156, v156\n"
      "v_mfma_f32_16x16x32_bf16 v[132:135], a[204:207], v[112:115], v[132:135]\n"
      "v_exp_f32 v158, v158\n"
      "v_mfma_f32_16x16x32_bf16 v[132:135], a[208:211], v[116:119], v[132:135]\n"
      "v_fma_f32 v156, v156, v177, v177\n"
      "v_fma_f32 v158, v158, v177, v177\n"
      "v_mfma_f32_16x16x32_bf16 v[132:135], a[212:215], v[120:123], v[132:135]\n"
      "s_waitcnt vmcnt(3)\n"
      "ds_write_b32 v13, v11\n"
      "v_mfma_f32_16x16x32_bf16 v[128:131], a[72:75], v[52:55], 0\n"
      "v_exp_f32 v157, v157\n"
      "v_mfma_f32_16x16x32_bf16 v[128:131], a[76:79], v[56:59], v[128:131]\n"
      "s_add_u32 s42, s42, 165888\n"
      "v_rcp_f32 v156, v156\n"
      "v_mfma_f32_16x16x32_bf16 v[128:131], a[80:83], v[60:63], v[128:131]\n"
      "v_fmac_f32 v133, v176, v149\n"
      "v_mul_f32 v161, v133, v133\n"
      "v_mfma_f32_16x16x32_bf16 v[128:131], a[84:87], v[64:67], v[128:131]\n"
      "v_fmac_f32 v135, v176, v151\n"
      "v_fmac_f32 v132, v176, v148\n"
      "v_mfma_f32_16x16x32_bf16 v[128:131], a[88:91], v[68:71], v[128:131]\n"
      "v_rcp_f32 v158, v158\n"
      "v_mfma_f32_16x16x32_bf16 v[128:131], a[92:95], v[72:75], v[128:131]\n"
      "v_mul_f32 v163, v135, v135\n"
      "v_fmac_f32 v134, v176, v150\n"
      "v_mfma_f32_16x16x32_bf16 v[128:131], a[96:99], v[76:79], v[128:131]\n"
      "s_waitcnt vmcnt(1) lgkmcnt(0)\n"
      "v_cmp_ne_u32 vcc, s21, v18\n"
      "v_fma_f32 v161, v161, v179, v178\n"
      "v_mfma_f32_16x16x32_bf16 v[128:131], a[100:103], v[80:83], v[128:131]\n"
      "v_mul_f32 v162, v134, v134\n"
      "v_mul_f32 v161, v133, v161\n"
      "v_mfma_f32_16x16x32_bf16 v[128:131], a[104:107], v[84:87], v[128:131]\n"
      "ds_read_b32 v12, v14\n"
      "v_fma_f32 v157, v157, v177, v177\n"
      "v_mfma_f32_16x16x32_bf16 v[128:131], a[108:111], v[88:91], v[128:131]\n"
      "s_cbranch_vccnz .Lsp_g0_8\n"
      ".Lbk_g0_8:\n"
      "v_fma_f32 v163, v163, v179, v178\n"
      "v_mfma_f32_16x16x32_bf16 v[128:131], a[112:115], v[92:95], v[128:131]\n"
      "buffer_load_dwordx4 v0, s[8:11], s40 offen offset:2048 sc1 lds\n"
      "v_mfma_f32_16x16x32_bf16 v[128:131], a[116:119], v[96:99], v[128:131]\n"
      "v_exp_f32 v161, v161\n"
      "v_mfma_f32_16x16x32_bf16 v[128:131], a[120:123], v[100:103], v[128:131]\n"
      "v_exp_f32 v159, v159\n"
      "v_mfma_f32_16x16x32_bf16 v[128:131], a[124:127], v[104:107], v[128:131]\n"
      "s_waitcnt lgkmcnt(0)\n"
      "v_fma_f32 v159, v159, v177, v177\n"
      "v_cmp_gt_i32 vcc, v11, v12\n"
      "v_mfma_f32_16x16x32_bf16 v[128:131], a[128:131], v[108:111], v[128:131]\n"
      "buffer_load_dwordx4 v0, s[8:11], s40 offen offset:1024 sc1 lds\n"
      "v_mfma_f32_16x16x32_bf16 v[128:131], a[132:135], v[112:115], v[128:131]\n"
      "v_rcp_f32 v159, v159\n"
      "v_mfma_f32_16x16x32_bf16 v[128:131], a[136:139], v[116:119], v[128:131]\n"
      "v_mul_f32 v163, v135, v163\n"
      "v_fma_f32 v161, v161, v177, v177\n"
      "v_mfma_f32_16x16x32_bf16 v[128:131], a[140:143], v[120:123], v[128:131]\n"
      "v_rcp_f32 v157, v157\n"
      "v_mfma_f32_16x16x32_bf16 v[136:139], a[216:219], v[52:55], 0\n"
      "buffer_load_dwordx4 v0, s[8:11], s40 offen offset:0 sc1 lds\n"
      "v_mfma_f32_16x16x32_bf16 v[136:139], a[220:223], v[56:59], v[136:139]\n"
      "s_cbranch_vccnz .Lsp_gy0_9\n"
      ".Lbk_gy0_9:\n"
      "v_mul_f32 v160, v132, v132\n"
      "v_mfma_f32_16x16x32_bf16 v[136:139], a[224:227], v[60:63], v[136:139]\n"
      "ds_read_b128 v[52:55], v17 offset:20480\n"
      "v_mul_f32 v159, v127, v159\n"
      "v_mfma_f32_16x16x32_bf16 v[136:139], a[228:231], v[64:67], v[136:139]\n"
      "ds_read_b128 v[56:59], v17 offset:21504\n"
      "v_fma_f32 v160, v160, v179, v178\n"
      "v_mfma_f32_16x16x32_bf16 v[136:139], a[232:235], v[68:71], v[136:139]\n"
      "buffer_load_dwordx4 v0, s[8:11], s40 offen offset:3072 sc1 lds\n"
      "v_mfma_f32_16x16x32_bf16 v[136:139], a[236:239], v[72:75], v[136:139]\n"
      "s_min_u32 s36, s36, s6\n"
      "v_mul_f32 v156, v124, v156\n"
      "v_mul_f32 v158, v126, v158\n"
      "v_mfma_f32_16x16x32_bf16 v[136:139], a[240:243], v[76:79], v[136:139]\n"
      "ds_read_b128 v[60:63], v17 offset:22528\n"
      "v_mul_f32 v157, v125, v157\n"
      "v_mfma_f32_16x16x32_bf16 v[136:139], a[244:247], v[80:83], v[136:139]\n"
      "ds_read_b128 v[76:79], v17 offset:26624\n"
      "v_mul_f32 v160, v132, v160\n"
      "v_mfma_f32_16x16x32_bf16 v[136:139], a[248:251], v[84:87], v[136:139]\n"
      "s_add_u32 m0, s39, 0\n"
      "ds_read_b128 v[68:71], v17 offset:24576\n"
      "buffer_load_dwordx4 v1, s[8:11], s40 offen offset:0 sc1 lds\n"
      "v_mfma_f32_16x16x32_bf16 v[136:139], a[252:255], v[88:91], v[136:139]\n"
      "ds_read_b128 v[72:75], v17 offset:25600\n"
      "v_fmac_f32 v128, v176, v144\n"
      "v_mfma_f32_16x16x32_bf16 v[136:139], v[20:23], v[92:95], v[136:139]\n"
      "v_exp_f32 v160, v160\n"
      "v_mfma_f32_16x16x32_bf16 v[136:139], v[24:27], v[96:99], v[136:139]\n"
      "ds_read_b128 v[96:99], v17 offset:31744\n"
      "v_exp_f32 v163, v163\n"
      "v_mfma_f32_16x16x32_bf16 v[136:139], v[28:31], v[100:103], v[136:139]\n"
      "s_waitcnt vmcnt(5) lgkmcnt(5)\n"
      "buffer_store_dword v15, v3, s[8:11], s42 offen sc1\n"
      "v_mfma_f32_16x16x32_bf16 v[136:139], v[32:35], v[104:107], v[136:139]\n"
      ".Lg_p1:\n"
      "s_cbranch_execz .Lg_pr\n"
      "ds_read_b128 v[64:67], v17 offset:23552\n"
      "v_fma_f32 v162, v162, v179, v178\n"
      "v_mfma_f32_16x16x32_bf16 v[136:139], v[36:39], v[108:111], v[136:139]\n"
      "ds_read_b128 v[84:87], v17 offset:28672\n"
      "v_mul_f32 v162, v134, v162\n"
      "v_mfma_f32_16x16x32_bf16 v[136:139], v[40:43], v[112:115], v[136:139]\n"
      "ds_read_b128 v[88:91], v17 offset:29696\n"
      "v_fmac_f32 v129, v176, v145\n"
      "v_mfma_f32_16x16x32_bf16 v[136:139], v[44:47], v[116:119], v[136:139]\n"
      "buffer_load_dword v18, v2, s[8:11], s36 offen sc1\n"
      "v_mfma_f32_16x16x32_bf16 v[136:139], v[48:51], v[120:123], v[136:139]\n"
      "ds_read_b128 v[80:83], v17 offset:27648\n"
      "v_mul_f32 v128, v128, v156\n"
      "v_mfma_f32_16x16x32_bf16 v[124:127], a[0:3], v[52:55], 0\n"
      "s_mov_b32 s40, s36\n"
      "ds_read_b128 v[120:123], v17 offset:37888\n"
      "v_fmac_f32 v130, v176, v146\n"
      "v_mfma_f32_16x16x32_bf16 v[124:127], a[4:7], v[56:59], v[124:127]\n"
      "s_waitcnt lgkmcnt(5)\n"
      "ds_read_b128 v[108:111], v17 offset:34816\n"
      "v_fma_f32 v160, v160, v177, v177\n"
      "v_mfma_f32_16x16x32_bf16 v[124:127], a[8:11], v[60:63], v[124:127]\n"
      "s_waitcnt lgkmcnt(5)\n"
      "ds_read_b128 v[92:95], v17 offset:30720\n"
      "v_fma_f32 v163, v163, v177, v177\n"
      "v_mfma_f32_16x16x32_bf16 v[124:127], a[12:15], v[64:67], v[124:127]\n"
      "ds_read_b128 v[112:115], v17 offset:35840\n"
      "v_fmac_f32 v131, v176, v147\n"
      "v_mfma_f32_16x16x32_bf16 v[124:127], a[16:19], v[68:71], v[124:127]\n"
      "ds_read_b128 v[100:103], v17 offset:32768\n"
      "v_exp_f32 v162, v162\n"
      "v_mfma_f32_16x16x32_bf16 v[124:127], a[20:23], v[72:75], v[124:127]\n"
      "ds_read_b128 v[104:107], v17 offset:33792\n"
      "v_mul_f32 v129, v129, v157\n"
      "v_fma_f32 v162, v162, v177, v177\n"
      "v_fmac_f32 v136, v176, v152\n"
      "v_mfma_f32_16x16x32_bf16 v[124:127], a[24:27], v[76:79], v[124:127]\n"
      "s_waitcnt lgkmcnt(6)\n"
      "v_mul_f32 v131, v131, v159\n"
      "v_fmac_f32 v138, v176, v154\n"
      "v_mfma_f32_16x16x32_bf16 v[124:127], a[28:31], v[80:83], v[124:127]\n"
      "ds_read_b128 v[116:119], v17 offset:36864\n"
      "v_rcp_f32 v163, v163\n"
      "v_mfma_f32_16x16x32_bf16 v[124:127], a[32:35], v[84:87], v[124:127]\n"
      "ds_read2st64_b64 v[168:171], v4 offset0:120 offset1:121\n"
      "v_rcp_f32 v162, v162\n"
      "v_mfma_f32_16x16x32_bf16 v[124:127], a[36:39], v[88:91], v[124:127]\n"
      "s_waitcnt lgkmcnt(5)\n"
      "v_rcp_f32 v161, v161\n"
      "v_mfma_f32_16x16x32_bf16 v[124:127], a[40:43], v[92:95], v[124:127]\n"
      "v_mul_f32 v162, v134, v162\n"
      "v_mul_f32 v163, v135, v163\n"
      "v_mfma_f32_16x16x32_bf16 v[124:127], a[44:47], v[96:99], v[124:127]\n"
      "s_waitcnt lgkmcnt(3)\n"
      "v_mul_f32 v130, v130, v158\n"
      "v_mul_f32 v138, v138, v162\n"
      "v_mfma_f32_16x16x32_bf16 v[124:127], a[48:51], v[100:103], v[124:127]\n"
      "s_waitcnt lgkmcnt(2)\n"
      "v_mul_f32 v161, v133, v161\n"
      "v_fmac_f32 v137, v176, v153\n"
      "v_mfma_f32_16x16x32_bf16 v[124:127], a[52:55], v[104:107], v[124:127]\n"
      "v_cvt_pk_bf16_f32 v165, v130, v131\n"
      "v_fmac_f32 v139, v176, v155\n"
      "v_mfma_f32_16x16x32_bf16 v[124:127], a[56:59], v[108:111], v[124:127]\n"
      "v_rcp_f32 v160, v160\n"
      "v_mfma_f32_16x16x32_bf16 v[124:127], a[60:63], v[112:115], v[124:127]\n"
      "s_waitcnt lgkmcnt(0)\n"
      "v_mul_f32 v160, v132, v160\n"
      "v_cvt_pk_bf16_f32 v164, v128, v129\n"
      "v_mul_f32 v136, v136, v160\n"
      "v_mul_f32 v137, v137, v161\n"
      "v_mfma_f32_16x16x32_bf16 v[124:127], a[64:67], v[116:119], v[124:127]\n"
      "v_mul_f32 v139, v139, v163\n"
      "v_sub_f32 v176, -v168, v170\n"
      "v_mfma_f32_16x16x32_bf16 v[124:127], a[68:71], v[120:123], v[124:127]\n"
      "v_cvt_pk_bf16_f32 v167, v138, v139\n"
      "v_cvt_pk_bf16_f32 v166, v136, v137\n"
      "v_mfma_f32_16x16x32_bf16 v[132:135], a[144:147], v[52:55], 0\n"
      "buffer_store_dwordx4 v[164:167], v0, s[8:11], s37 offen sc1\n"
      "v_mfma_f32_16x16x32_bf16 v[132:135], a[148:151], v[56:59], v[132:135]\n"
      "v_add_f32 v169, v169, v171\n"
      "v_fma_f32 v169, -v176, v176, v169\n"
      "v_mfma_f32_16x16x32_bf16 v[132:135], a[152:155], v[60:63], v[132:135]\n"
      "v_max_f32 v177, 0x3727c5ac, v169\n"
      "v_fmac_f32 v126, v176, v142\n"
      "v_mfma_f32_16x16x32_bf16 v[132:135], a[156:159], v[64:67], v[132:135]\n"
      "v_rsq_f32 v170, v177\n"
      "v_mfma_f32_16x16x32_bf16 v[132:135], a[160:163], v[68:71], v[132:135]\n"
      "v_add_u32 v11, 1, v11\n"
      "v_mul_f32 v158, v126, v126\n"
      "v_mfma_f32_16x16x32_bf16 v[132:135], a[164:167], v[72:75], v[132:135]\n"
      "v_mul_f32 v178, 0xc0135761, v170\n"
      "v_mul_f32 v171, 0x3d372713, v178\n"
      "v_mfma_f32_16x16x32_bf16 v[132:135], a[168:171], v[76:79], v[132:135]\n"
      "v_fmac_f32 v125, v176, v141\n"
      "v_fmac_f32 v127, v176, v143\n"
      "v_mfma_f32_16x16x32_bf16 v[132:135], a[172:175], v[80:83], v[132:135]\n"
      "s_add_u32 s37, s37, 165888\n"
      "v_mul_f32 v170, v170, v170\n"
      "v_fmac_f32 v124, v176, v140\n"
      "v_mfma_f32_16x16x32_bf16 v[132:135], a[176:179], v[84:87], v[132:135]\n"
      "v_mul_f32 v179, v171, v170\n"
      "v_mul_f32 v157, v125, v125\n"
      "v_mfma_f32_16x16x32_bf16 v[132:135], a[180:183], v[88:91], v[132:135]\n"
      "v_mul_f32 v159, v127, v127\n"
      "v_fma_f32 v158, v158, v179, v178\n"
      "v_mfma_f32_16x16x32_bf16 v[132:135], a[184:187], v[92:95], v[132:135]\n"
      "v_fma_f32 v159, v159, v179, v178\n"
      "v_mul_f32 v159, v127, v159\n"
      "v_mfma_f32_16x16x32_bf16 v[132:135], a[188:191], v[96:99], v[132:135]\n"
      "v_mul_f32 v156, v124, v124\n"
      "v_mul_f32 v158, v126, v158\n"
      "v_mfma_f32_16x16x32_bf16 v[132:135], a[192:195], v[100:103], v[132:135]\n"
      "v_fma_f32 v157, v157, v179, v178\n"
      "v_fma_f32 v156, v156, v179, v178\n"
      "v_mfma_f32_16x16x32_bf16 v[132:135], a[196:199], v[104:107], v[132:135]\n"
      "s_add_u32 s36, s36, 24576\n"
      "v_mul_f32 v156, v124, v156\n"
      "v_mul_f32 v157, v125, v157\n"
      "v_mfma_f32_16x16x32_bf16 v[132:135], a[200:203], v[108:111], v[132:135]\n"
      "s_add_u32 m0, s38, 20480\n"
      "v_exp_f32 v156, v156\n"
      "v_mfma_f32_16x16x32_bf16 v[132:135], a[204:207], v[112:115], v[132:135]\n"
      "v_exp_f32 v158, v158\n"
      "v_mfma_f32_16x16x32_bf16 v[132:135], a[208:211], v[116:119], v[132:135]\n"
      "v_fma_f32 v156, v156, v177, v177\n"
      "v_fma_f32 v158, v158, v177, v177\n"
      "v_mfma_f32_16x16x32_bf16 v[132:135], a[212:215], v[120:123], v[132:135]\n"
      "s_waitcnt vmcnt(3)\n"
      "ds_write_b32 v13, v11\n"
      "v_mfma_f32_16x16x32_bf16 v[128:131], a[72:75], v[52:55], 0\n"
      "v_exp_f32 v157, v157\n"
      "v_mfma_f32_16x16x32_bf16 v[128:131], a[76:79], v[56:59], v[128:131]\n"
      "s_add_u32 s42, s42, 165888\n"
      "v_rcp_f32 v156, v156\n"
      "v_mfma_f32_16x16x32_bf16 v[128:131], a[80:83], v[60:63], v[128:131]\n"
      "v_fmac_f32 v133, v176, v149\n"
      "v_mul_f32 v161, v133, v133\n"
      "v_mfma_f32_16x16x32_bf16 v[128:131], a[84:87], v[64:67], v[128:131]\n"
      "v_fmac_f32 v135, v176, v151\n"
      "v_fmac_f32 v132, v176, v148\n"
      "v_mfma_f32_16x16x32_bf16 v[128:131], a[88:91], v[68:71], v[128:131]\n"
      "v_rcp_f32 v158, v158\n"
      "v_mfma_f32_16x16x32_bf16 v[128:131], a[92:95], v[72:75], v[128:131]\n"
      "v_mul_f32 v163, v135, v135\n"
      "v_fmac_f32 v134, v176, v150\n"
      "v_mfma_f32_16x16x32_bf16 v[128:131], a[96:99], v[76:79], v[128:131]\n"
      "s_waitcnt vmcnt(1) lgkmcnt(0)\n"
      "v_cmp_ne_u32 vcc, s21, v18\n"
      "v_fma_f32 v161, v161, v179, v178\n"
      "v_mfma_f32_16x16x32_bf16 v[128:131], a[100:103], v[80:83], v[128:131]\n"
      "v_mul_f32 v162, v134, v134\n"
      "v_mul_f32_e64 v161, v133, v161\n"
      "v_mfma_f32_16x16x32_bf16 v[128:131], a[104:107], v[84:87], v[128:131]\n"
      "ds_read_b32 v12, v14\n"
      "v_fma_f32 v157, v157, v177, v177\n"
      "v_mfma_f32_16x16x32_bf16 v[128:131], a[108:111], v[88:91], v[128:131]\n"
      "s_cbranch_vccnz .Lsp_g0_10\n"
      ".Lbk_g0_10:\n"
      "v_fma_f32 v163, v163, v179, v178\n"
      "v_mfma_f32_16x16x32_bf16 v[128:131], a[112:115], v[92:95], v[128:131]\n"
      "buffer_load_dwordx4 v0, s[8:11], s40 offen offset:2048 sc1 lds\n"
      "v_mfma_f32_16x16x32_bf16 v[128:131], a[116:119], v[96:99], v[128:131]\n"
      "v_exp_f32 v161, v161\n"
      "v_mfma_f32_16x16x32_bf16 v[128:131], a[120:123], v[100:103], v[128:131]\n"
      "v_exp_f32 v159, v159\n"
      "v_mfma_f32_16x16x32_bf16 v[128:131], a[124:127], v[104:107], v[128:131]\n"
      "s_waitcnt lgkmcnt(0)\n"
      "v_fma_f32 v159, v159, v177, v177\n"
      "v_cmp_gt_i32 vcc, v11, v12\n"
      "v_mfma_f32_16x16x32_bf16 v[128:131], a[128:131], v[108:111], v[128:131]\n"
      "buffer_load_dwordx4 v0, s[8:11], s40 offen offset:1024 sc1 lds\n"
      "v_mfma_f32_16x16x32_bf16 v[128:131], a[132:135], v[112:115], v[128:131]\n"
      "v_rcp_f32 v159, v159\n"
      "v_mfma_f32_16x16x32_bf16 v[128:131], a[136:139], v[116:119], v[128:131]\n"
      "v_mul_f32 v163, v135, v163\n"
      "v_fma_f32 v161, v161, v177, v177\n"
      "v_mfma_f32_16x16x32_bf16 v[128:131], a[140:143], v[120:123], v[128:131]\n"
      "v_rcp_f32 v157, v157\n"
      "v_mfma_f32_16x16x32_bf16 v[136:139], a[216:219], v[52:55], 0\n"
      "buffer_load_dwordx4 v0, s[8:11], s40 offen offset:0 sc1 lds\n"
      "v_mfma_f32_16x16x32_bf16 v[136:139], a[220:223], v[56:59], v[136:139]\n"
      "s_cbranch_vccnz .Lsp_gy0_11\n"
      ".Lbk_gy0_11:\n"
      "v_mul_f32 v160, v132, v132\n"
      "v_mfma_f32_16x16x32_bf16 v[136:139], a[224:227], v[60:63], v[136:139]\n"
      "ds_read_b128 v[52:55], v0 offset:0\n"
      "v_mul_f32 v159, v127, v159\n"
      "v_mfma_f32_16x16x32_bf16 v[136:139], a[228:231], v[64:67], v[136:139]\n"
      "ds_read_b128 v[56:59], v0 offset:1024\n"
      "v_fma_f32 v160, v160, v179, v178\n"
      "v_mfma_f32_16x16x32_bf16 v[136:139], a[232:235], v[68:71], v[136:139]\n"
      "buffer_load_dwordx4 v0, s[8:11], s40 offen offset:3072 sc1 lds\n"
      "v_mfma_f32_16x16x32_bf16 v[136:139], a[236:239], v[72:75], v[136:139]\n"
      "s_min_u32 s36, s36, s6\n"
      "v_mul_f32 v156, v124, v156\n"
      "v_mul_f32 v158, v126, v158\n"
      "v_mfma_f32_16x16x32_bf16 v[136:139], a[240:243], v[76:79], v[136:139]\n"
      "ds_read_b128 v[60:63], v0 offset:2048\n"
      "v_mul_f32 v157, v125, v157\n"
      "v_mfma_f32_16x16x32_bf16 v[136:139], a[244:247], v[80:83], v[136:139]\n"
      "ds_read_b128 v[76:79], v0 offset:6144\n"
      "v_mul_f32 v160, v132, v160\n"
      "v_mfma_f32_16x16x32_bf16 v[136:139], a[248:251], v[84:87], v[136:139]\n"
      "s_add_u32 m0, s39, 20480\n"
      "ds_read_b128 v[68:71], v0 offset:4096\n"
      "buffer_load_dwordx4 v1, s[8:11], s40 offen offset:0 sc1 lds\n"
      "v_mfma_f32_16x16x32_bf16 v[136:139], a[252:255], v[88:91], v[136:139]\n"
      "ds_read_b128 v[72:75], v0 offset:5120\n"
      "v_fmac_f32 v128, v176, v144\n"
      "v_mfma_f32_16x16x32_bf16 v[136:139], v[20:23], v[92:95], v[136:139]\n"
      "v_exp_f32 v160, v160\n"
      "v_mfma_f32_16x16x32_bf16 v[136:139], v[24:27], v[96:99], v[136:139]\n"
      "ds_read_b128 v[96:99], v0 offset:11264\n"
      "v_exp_f32 v163, v163\n"
      "v_mfma_f32_16x16x32_bf16 v[136:139], v[28:31], v[100:103], v[136:139]\n"
      "s_waitcnt vmcnt(5) lgkmcnt(5)\n"
      "buffer_store_dword v15, v3, s[8:11], s42 offen sc1\n"
      "v_mfma_f32_16x16x32_bf16 v[136:139], v[32:35], v[104:107], v[136:139]\n"
      "ds_read_b128 v[64:67], v0 offset:3072\n"
      "v_fma_f32 v162, v162, v179, v178\n"
      "v_mfma_f32_16x16x32_bf16 v[136:139], v[36:39], v[108:111], v[136:139]\n"
      "ds_read_b128 v[84:87], v0 offset:8192\n"
      "v_mul_f32 v162, v134, v162\n"
      "v_mfma_f32_16x16x32_bf16 v[136:139], v[40:43], v[112:115], v[136:139]\n"
      "ds_read_b128 v[88:91], v0 offset:9216\n"
      "v_fmac_f32 v129, v176, v145\n"
      "v_mfma_f32_16x16x32_bf16 v[136:139], v[44:47], v[116:119], v[136:139]\n"
      "buffer_load_dword v18, v2, s[8:11], s36 offen sc1\n"
      "v_mfma_f32_16x16x32_bf16 v[136:139], v[48:51], v[120:123], v[136:139]\n"
      "ds_read_b128 v[80:83], v0 offset:7168\n"
      "v_mul_f32 v128, v128, v156\n"
      "s_sub_u32 s20, s20, 1\n"
      "s_cbranch_scc0 .Lg_loop\n"
      ".Lg_tail:\n"
      "v_fmac_f32 v130, v176, v146\n"
      "v_fma_f32 v160, v160, v177, v177\n"
      "v_fma_f32 v163, v163, v177, v177\n"
      "v_fmac_f32 v131, v176, v147\n"
      "v_exp_f32 v162, v162\n"
      "v_mul_f32 v129, v129, v157\n"
      "v_fma_f32 v162, v162, v177, v177\n"
      "v_fmac_f32 v136, v176, v152\n"
      "v_mul_f32 v131, v131, v159\n"
      "v_fmac_f32 v138, v176, v154\n"
      "v_rcp_f32 v163, v163\n"
      "v_rcp_f32 v162, v162\n"
      "v_rcp_f32 v161, v161\n"
      "v_mul_f32 v162, v134, v162\n"
      "v_mul_f32 v163, v135, v163\n"
      "v_mul_f32 v130, v130, v158\n"
      "v_mul_f32 v138, v138, v162\n"
      "v_mul_f32 v161, v133, v161\n"
      "v_fmac_f32 v137, v176, v153\n"
      "v_cvt_pk_bf16_f32 v165, v130, v131\n"
      "v_fmac_f32 v139, v176, v155\n"
      "v_rcp_f32 v160, v160\n"
      "s_nop 0\n"
      "v_mul_f32 v160, v132, v160\n"
      "v_cvt_pk_bf16_f32 v164, v128, v129\n"
      "v_mul_f32 v136, v136, v160\n"
      "v_mul_f32 v137, v137, v161\n"
      "v_mul_f32 v139, v139, v163\n"
      "v_cvt_pk_bf16_f32 v167, v138, v139\n"
      "v_cvt_pk_bf16_f32 v166, v136, v137\n"
      "buffer_store_dwordx4 v[164:167], v0, s[8:11], s37 offen sc1\n"
      "s_add_u32 s37, s37, 165888\n"
      "s_add_u32 s42, s42, 165888\n"
      "s_waitcnt vmcnt(0)\n"
      "buffer_store_dword v15, v3, s[8:11], s42 offen sc1\n"
      "s_add_u32 s42, s42, 165888\n"
      "s_endpgm\n"
      ".Lsp_g0_1:\n"
      "s_mov_b32 s22, s23\n"
      ".Lsl_g0_1:\n"
      "buffer_load_dword v18, v2, s[8:11], s40 offen sc1\n"
      "s_waitcnt vmcnt(0)\n"
      "v_cmp_ne_u32 vcc, s21, v18\n"
      "s_cbranch_vccz .Lbk_g0_1\n"
      "s_sleep 4\n"
      "s_sub_u32 s22, s22, 1\n"
      "s_cbranch_scc0 .Lsl_g0_1\n"
      "s_mov_b32 s23, 0\n"
      "s_branch .Lbk_g0_1\n"
      ".Lsp_g0_2:\n"
      "s_mov_b32 s22, s23\n"
      ".Lsl_g0_2:\n"
      "buffer_load_dword v18, v2, s[8:11], s40 offen sc1\n"
      "s_waitcnt vmcnt(0)\n"
      "v_cmp_ne_u32 vcc, s21, v18\n"
      "s_cbranch_vccz .Lbk_g0_2\n"
      "s_sleep 4\n"
      "s_sub_u32 s22, s22, 1\n"
      "s_cbranch_scc0 .Lsl_g0_2\n"
      "s_mov_b32 s23, 0\n"
      "s_branch .Lbk_g0_2\n"
      ".Lsp_gy0_3:\n"
      "s_lshl_b32 s22, s23, 4\n"
      ".Lsl_gy0_3:\n"
      "ds_read_b32 v12, v14\n"
      "s_waitcnt lgkmcnt(0)\n"
      "v_cmp_gt_i32 vcc, v11, v12\n"
      "s_cbranch_vccz .Lbk_gy0_3\n"
      "s_sub_u32 s22, s22, 1\n"
      "s_cbranch_scc0 .Lsl_gy0_3\n"
      "s_mov_b32 s23, 0\n"
      "s_branch .Lbk_gy0_3\n"
      ".Lsp_g0_4:\n"
      "s_mov_b32 s22, s23\n"
      "buffer_load_dword v18, v2, s[8:11], s40 offen sc1\n"
      "s_waitcnt vmcnt(0)\n"
      "v_cmp_ne_u32 vcc, s21, v18\n"
      "s_add_u32 s30, s42, 0\n"
      "buffer_store_dword v15, v3, s[8:11], s30 offen sc1\n"
      "s_cbranch_vccz .Lbk_g0_4\n"
      ".Lsl_g0_4:\n"
      "buffer_load_dword v18, v2, s[8:11], s40 offen sc1\n"
      "s_waitcnt vmcnt(0)\n"
      "v_cmp_ne_u32 vcc, s21, v18\n"
      "s_cbranch_vccz .Lbk_g0_4\n"
      "s_sleep 4\n"
      "s_sub_u32 s22, s22, 1\n"
      "s_cbranch_scc0 .Lsl_g0_4\n"
      "s_mov_b32 s23, 0\n"
      "s_branch .Lbk_g0_4\n"
      ".Lsp_gy0_5:\n"
      "s_lshl_b32 s22, s23, 4\n"
      ".Lsl_gy0_5:\n"
      "ds_read_b32 v12, v14\n"
      "s_waitcnt lgkmcnt(0)\n"
      "v_cmp_gt_i32 vcc, v11, v12\n"
      "s_cbranch_vccz .Lbk_gy0_5\n"
      "s_sub_u32 s22, s22, 1\n"
      "s_cbranch_scc0 .Lsl_gy0_5\n"
      "s_mov_b32 s23, 0\n"
      "s_branch .Lbk_gy0_5\n"
      ".Lsp_g0_6:\n"
      "s_mov_b32 s22, s23\n"
      "buffer_load_dword v18, v2, s[8:11], s40 offen sc1\n"
      "s_waitcnt vmcnt(0)\n"
      "v_cmp_ne_u32 vcc, s21, v18\n"
      "s_add_u32 s30, s42, 0\n"
      "buffer_store_dword v15, v3, s[8:11], s30 offen sc1\n"
      "s_cbranch_vccz .Lbk_g0_6\n"
      ".Lsl_g0_6:\n"
      "buffer_load_dword v18, v2, s[8:11], s40 offen sc1\n"
      "s_waitcnt vmcnt(0)\n"
      "v_cmp_ne_u32 vcc, s21, v18\n"
      "s_cbranch_vccz .Lbk_g0_6\n"
      "s_sleep 4\n"
      "s_sub_u32 s22, s22, 1\n"
      "s_cbranch_scc0 .Lsl_g0_6\n"
      "s_mov_b32 s23, 0\n"
      "s_branch .Lbk_g0_6\n"
      ".Lsp_gy0_7:\n"
      "s_lshl_b32 s22, s23, 4\n"
      ".Lsl_gy0_7:\n"
      "ds_read_b32 v12, v14\n"
      "s_waitcnt lgkmcnt(0)\n"
      "v_cmp_gt_i32 vcc, v11, v12\n"
      "s_cbranch_vccz .Lbk_gy0_7\n"
      "s_sub_u32 s22, s22, 1\n"
      "s_cbranch_scc0 .Lsl_gy0_7\n"
      "s_mov_b32 s23, 0\n"
      "s_branch .Lbk_gy0_7\n"
      ".Lsp_g0_8:\n"
      "s_mov_b32 s22, s23\n"
      "buffer_load_dword v18, v2, s[8:11], s40 offen sc1\n"
      "s_waitcnt vmcnt(0)\n"
      "v_cmp_ne_u32 vcc, s21, v18\n"
      "s_add_u32 s30, s42, 0\n"
      "buffer_store_dword v15, v3, s[8:11], s30 offen sc1\n"
      "s_cbranch_vccz .Lbk_g0_8\n"
      ".Lsl_g0_8:\n"
      "buffer_load_dword v18, v2, s[8:11], s40 offen sc1\n"
      "s_waitcnt vmcnt(0)\n"
      "v_cmp_ne_u32 vcc, s21, v18\n"
      "s_cbranch_vccz .Lbk_g0_8\n"
      "s_sleep 4\n"
      "s_sub_u32 s22, s22, 1\n"
      "s_cbranch_scc0 .Lsl_g0_8\n"
      "s_mov_b32 s23, 0\n"
      "s_branch .Lbk_g0_8\n"
      ".Lsp_gy0_9:\n"
      "s_lshl_b32 s22, s23, 4\n"
      ".Lsl_gy0_9:\n"
      "ds_read_b32 v12, v14\n"
      "s_waitcnt lgkmcnt(0)\n"
      "v_cmp_gt_i32 vcc, v11, v12\n"
      "s_cbranch_vccz .Lbk_gy0_9\n"
      "s_sub_u32 s22, s22, 1\n"
      "s_cbranch_scc0 .Lsl_gy0_9\n"
      "s_mov_b32 s23, 0\n"
      "s_branch .Lbk_gy0_9\n"
      ".Lsp_g0_10:\n"
      "s_mov_b32 s22, s23\n"
      "buffer_load_dword v18, v2, s[8:11], s40 offen sc1\n"
      "s_waitcnt vmcnt(0)\n"
      "v_cmp_ne_u32 vcc, s21, v18\n"
      "s_add_u32 s30, s42, 0\n"
      "buffer_store_dword v15, v3, s[8:11], s30 offen sc1\n"
      "s_cbranch_vccz .Lbk_g0_10\n"
      ".Lsl_g0_10:\n"
      "buffer_load_dword v18, v2, s[8:11], s40 offen sc1\n"
      "s_waitcnt vmcnt(0)\n"
      "v_cmp_ne_u32 vcc, s21, v18\n"
      "s_cbranch_vccz .Lbk_g0_10\n"
      "s_sleep 4\n"
      "s_sub_u32 s22, s22, 1\n"
      "s_cbranch_scc0 .Lsl_g0_10\n"
      "s_mov_b32 s23, 0\n"
      "s_branch .Lbk_g0_10\n"
      ".Lsp_gy0_11:\n"
      "s_lshl_b32 s22, s23, 4\n"
      ".Lsl_gy0_11:\n"
      "ds_read_b32 v12, v14\n"
      "s_waitcnt lgkmcnt(0)\n"
      "v_cmp_gt_i32 vcc, v11, v12\n"
      "s_cbranch_vccz .Lbk_gy0_11\n"
      "s_sub_u32 s22, s22, 1\n"
      "s_cbranch_scc0 .Lsl_gy0_11\n"
      "s_mov_b32 s23, 0\n"
      "s_branch .Lbk_gy0_11\n"
      ".Lg_last:\n"
      :
      : "{s[8:11]}"(rsWS), "{s[12:15]}"(rsA), "{s[92:95]}"(rsC), "{v0}"(v_l16), "{v1}"(v_l5), "{v2}"(v_fa), "{v3}"(v_ra), "{v4}"(v_st), "{v5}"(v_at), "{v6}"(v_vo0), "{v7}"(v_vo1), "{v8}"(v_vo2), "{v9}"(v_vo3), "{v10}"(v_pw), "{v12}"(v_al), "{v13}"(v_yw), "{v14}"(v_yr), "{v16}"(v_hi), "{v54}"(v_tw[0]), "{v55}"(v_tw[1]), "{s36}"(s_sx), "{s37}"(s_sh), "{s38}"(s_m4), "{s39}"(s_m5)
      : "v255", "a255", "s99", "vcc", "scc", "memory");

  }
}

extern "C" void kernel_launch(void* const* d_in, const int* in_sizes, int n_in, void* d_out, int out_size,
                              void* d_ws, size_t ws_size, hipStream_t stream) {
  char* ws = (char*)d_ws + ((256 - ((size_t)d_ws & 255)) & 255);
  (void)hipFuncSetAttribute((const void*)k_all, hipFuncAttributeMaxDynamicSharedMemorySize, LDS_BYTES);
  k_all<<<256, 256, LDS_BYTES, stream>>>((const u16*)d_in[0], (const u16*)d_in[1], (const u16*)d_in[2], (const u16*)d_in[4],
                                         (const u16*)d_in[5], (const u16*)d_in[6], ws, (u16*)d_out);
}
```

```cpp
#include <hip/hip_runtime.h>
#include <stdint.h>

typedef unsigned short u16;
typedef __attribute__((ext_vector_type(4))) unsigned u32x4;
constexpr unsigned WS_DBG = 65536u;
constexpr unsigned WS_XIN = 1310720u;
constexpr unsigned WS_OB = 9175040u;
constexpr unsigned WS_X1 = 17039360u;
constexpr unsigned WS_H = 26034176u;
constexpr unsigned WS_PS = 78589952u;
constexpr unsigned WS_END = 109740032u;
constexpr unsigned KSK = 0u;
constexpr unsigned ENT = 1u;
constexpr unsigned ROT = 0u;
constexpr unsigned MAP = 2u;
constexpr unsigned FLIP = 0u;
constexpr unsigned PW = 1664u;
constexpr unsigned REC = 24576u;
constexpr unsigned HREC = 165888u;
constexpr unsigned PREC = 99840u;
constexpr unsigned NREC = 80u;
constexpr unsigned OOB = 0x40000000u, NEG = 0xff61b1e6u;
constexpr int LDS_BYTES = 160 * 1024;

__device__ __forceinline__ u32x4 srd(const void* p, unsigned bytes) {
  const unsigned long long a = (unsigned long long)p;
  return u32x4{(unsigned)a, (unsigned)(a >> 32) & 0xffffu, bytes, 0x00020000u};
}

extern "C" __global__ __launch_bounds__(256, 1) __attribute__((amdgpu_waves_per_eu(1, 1))) void k_all(
    const u16* X, const u16* Wqkv, const u16* Wo, const u16* Wg, const u16* Wu, const u16* Wd, char* ws, u16* out) {
  const unsigned b = blockIdx.x, xcd = b & 7, slot = b >> 3;
  const unsigned w = __builtin_amdgcn_readfirstlane(threadIdx.x >> 6), lane = threadIdx.x & 63;
  const bool fast = ((xcd ^ FLIP) & 1) == 0;
  if (ENT) {
    constexpr unsigned NL = MAP ? 41 : 27, LR = MAP ? 9 : 6;
    const unsigned wi = slot * 4 + w, n0 = wi * NL + lane, n = n0 < 576 * LR ? n0 : 0, r = n / LR, c = n - LR * r;
    const unsigned m = (xcd * 128 + wi) * 4 + lane - NL, t = m / 144;
    unsigned oc = (wi * 8 + (lane & 7)) * 128;
    asm volatile("s_getpc_b64 vcc\n.Lent_pc:\n\ts_sub_u32 vcc_lo, vcc_lo, .Lent_pc-k_all\n\ts_subb_u32 vcc_hi, vcc_hi, 0\n\ts_mov_b32 m0, %1\n\t"
                 "v_min_u32 %0, .Lq_last-k_all, %0\n\tglobal_load_lds_dword %0, vcc" : "+v"(oc) : "s"(162816 + w * 256) : "vcc", "scc", "memory");
    unsigned oq = r * 3456 + c / (LR / 3) * 1152 + ((MAP ? xcd & 6 : xcd) + c % (LR / 3)) * 128, ox = (32 * (t & 3) + 31 - (t >> 2)) * 18432 + (m - 144 * t) * 128;
    asm volatile("" : "+v"(oq), "+v"(ox));
    const bool isq = lane < NL && (fast || !MAP);
    oq = isq ? oq : lane - NL < 4 ? ox : 0;
    asm volatile("" : "+v"(oq));
    __builtin_amdgcn_sched_barrier(0);
    const char* pa = (const char*)(isq ? Wqkv : X) + oq;
    asm volatile("global_load_lds_dword %0, off" ::"v"(pa) : "memory");
    __builtin_amdgcn_sched_barrier(0);
  }
  unsigned role, gr, part;
  if (MAP == 2) {
    const unsigned e = xcd >> 1, td = (slot - 10) / 3, e1 = (e - 1) & 3;
    role = fast ? (slot >= 8) + (slot >= 10) + (slot >= 28) : 3;
    gr = role == 0 ? slot & 3 : role == 1 ? e : role == 2 ? (td < 4 ? td : td == 4 ? (e + 1) & 3 : e) : !fast ? slot & 3 : slot < 31 ? (e + slot - 27) & 3 : e1;
    part = role == 0 ? 2 * e + (slot >> 2) : role == 1 ? slot - 8 : role == 2 ? (td < 4 ? e : td) * 3 + (slot - 10) % 3
         : !fast ? 8 * e + (slot >> 2) : slot < 31 ? 32 + e : 32 + e1;
  } else if (MAP) {
    const unsigned e = xcd >> 1, sd = slot - (fast ? 10 : 0), sg = slot - (fast ? 16 : 12), i3 = sg / 3, e1 = (e - 1) & 3;
    role = fast ? (slot >= 8) + (slot >= 10) + (slot >= 16) : 2 + (slot >= 12);
    gr = role == 0 ? slot & 3 : role == 1 ? e : role == 2 ? sd / 3 + (fast ? 2 * (e & 1) : 0) : !fast ? sg & 3 : sg < 12 ? (e + 1 + sg - 3 * i3) & 3 : e1;
    part = role == 0 ? 2 * e + (slot >> 2) : role == 1 ? slot - 8 : role == 2 ? (fast ? e >> 1 : e + 2) * 3 + sd % 3
         : !fast ? 5 * e + (sg >> 2) : sg < 12 ? 20 + 4 * e + i3 : 8 + 4 * e1 + sg;
  } else {
    const unsigned td = ((xcd + 4) & 7) * 3 + (slot - 5) / 3, tg = xcd * 3 + (slot - 14) / 6;
    role = (slot >= 4) + (slot >= 5) + (slot >= 14);
    gr = role == 0 ? slot : role == 1 ? 3 - (xcd & 3) : role == 2 ? td & 3 : tg & 3;
    part = role == 0 ? xcd : role == 1 ? xcd >> 2 : role == 2 ? (td >> 2) * 3 + (slot - 5) % 3 : 6 * (tg >> 2) + (slot - 14) % 6;
  }
  const unsigned i16 = lane & 15, g = lane >> 4, q = i16 >> 2, p = lane & 3;
  const u32x4 rsWS = srd(ws, WS_END);
  const unsigned v_l16 = lane * 16, v_dbg = WS_DBG + (((role == 0 ? 0 : role == 1 ? 32 : role == 2 ? 40 : 112) + part * 4 + gr) * 4 + w) * 1024 + lane * 4;
  const unsigned yb = role & 1 ? 81920 : role ? 149760 : 61440, v_yw = yb + w * 4, v_yr = yb + p * 4;
  const unsigned half = (i16 + 16 * (g >> 1)) * 16 + (g & 1) * 8;
  const unsigned line = lane < 32 ? lane * 4 : OOB, v_al = w ? OOB : line + 128 * (1 + b);
  const unsigned up = lane >> 5, l5 = (lane & 31) * 4, v_hi = 0u - up;
  const unsigned l8 = lane >> 3, l7 = (lane & 7) * 16;
  (void)v_dbg;
  if (role == 0) {
    const unsigned hg = part, h = 4 * hg + w, par = i16 & 1;
    const u32x4 rsA = srd(Wqkv, 576 * 1728 * 2);
    const unsigned v_l5 = w < 3 || lane < 32 ? v_l16 + 16384 - w * 3072 : OOB;
    const unsigned v_fa = 20480 - w * 4096 + (lane < 48 ? lane >> 2 : 11) * 128 + p * 4, v_ra = up * 8 * REC + l5 + 20480 + h * 128;
    const unsigned v_st = 18432 + i16 * 32 + q * 8, v_tw = 61568 + w * 128 + i16 * 8, v_tr = 61568 + w * 128 + g * 32;
    const unsigned v_om = h * 512 + half, v_oe = g ? OOB : 16384 + h * 128 + i16 * 8;
    const unsigned v_pw = 20480 + h * 128 + l8 * REC + l7;
    unsigned v_vo[4], v_bc[4], v_bp[4], v_mk[4], v_sel[4] = {0, 0, 0, 0}, v_ci[4];
#pragma unroll
    for (unsigned i = 0; i < 4; ++i) {
      const unsigned kk = 4 * g + i, ka = 4 * g + 2 * (i & 1) + par;
      v_bc[i] = (i & 1) == par && kk <= i16 ? 0u : NEG;
      v_bp[i] = (i & 1) == par && kk >= i16 + 2 ? 0u : NEG;
      v_mk[i] = (i < 2 ? ka > i16 : ka <= i16) ? 0xffffu << (16 * par) : 0u;
      v_ci[i] = p == 2 ? 0x3f800000u : 0u;
    }
    v_sel[3] = lane == 16 ? 0x3f80u : lane == 17 ? 0x3f800000u : 0u;
#pragma unroll
    for (unsigned i = 0; i < 4; ++i) {
      const unsigned c = 64 * i + lane, cc = c % 30;
      v_vo[i] = c < 240 && cc < 27 ? (8 * w + c / 30) * 3456 + cc / 9 * 1152 + 144 * hg + cc % 9 * 16 - 1024 * i : OOB;
    }
    const unsigned rb = 65536 + g * 4224 + q * 480;
    const unsigned v_at = rb + 36 * w + 4 * (w & 1) + 8 * p, v_ax = rb + (p < 2 ? p : 2) * 144 + 72 * (w >> 1) + 32;
    const unsigned v_pm = (16 * g + 4 * (i16 >> 1 & 3) + 2 * (w & 1) + par) * 4, v_ms = i16 < 6 ? ~0u : 0u;
    const unsigned s_sx = WS_XIN + (gr * NREC + KSK) * REC + w * 4096, s_ev = s_sx + (32 - KSK) * REC, s_del = 0u - (gr ? NREC + 32 : 32) * REC;
    const unsigned s_kil = gr ? 0x7f7fffffu : 0xff7fffffu, s_so = WS_OB + (gr * NREC + KSK) * REC;
    const unsigned s_m4 = w * 4096, s_m5 = 16384 + w * 1024, s_ms = w * 4224, s_up = part + gr + w;
    asm volatile(
      "s_buffer_load_dword s21, s[8:11], 0\n"
      "s_mov_b32 s23, 65536\n"
      "s_mov_b32 s31, 0x40000000\n"
      "s_mov_b32 s51, 0\n"
      "s_add_u32 m0, s43, 65536\n"
      "s_nop 0\n"
      "buffer_load_dwordx4 v48, s[12:15], s51 offen offset:0 lds\n"
      "buffer_load_dwordx4 v49, s[12:15], s51 offen offset:1024 lds\n"
      "buffer_load_dwordx4 v50, s[12:15], s51 offen offset:2048 lds\n"
      "buffer_load_dwordx4 v51, s[12:15], s51 offen offset:3072 lds\n"
      "s_add_u32 s51, s51, 110592\n"
      "s_add_u32 m0, s43, 82432\n"
      "s_nop 0\n"
      "buffer_load_dwordx4 v48, s[12:15], s51 offen offset:0 lds\n"
      "buffer_load_dwordx4 v49, s[12:15], s51 offen offset:1024 lds\n"
      "buffer_load_dwordx4 v50, s[12:15], s51 offen offset:2048 lds\n"
      "buffer_load_dwordx4 v51, s[12:15], s51 offen offset:3072 lds\n"
      "s_add_u32 s51, s51, 110592\n"
      "s_add_u32 m0, s43, 99328\n"
      "s_nop 0\n"
      "buffer_load_dwordx4 v48, s[12:15], s51 offen offset:0 lds\n"
      "buffer_load_dwordx4 v49, s[12:15], s51 offen offset:1024 lds\n"
      "buffer_load_dwordx4 v50, s[12:15], s51 offen offset:2048 lds\n"
      "buffer_load_dwordx4 v51, s[12:15], s51 offen offset:3072 lds\n"
      "s_add_u32 s51, s51, 110592\n"
      "s_add_u32 s6, s36, 1572864\n"
      "s_add_u32 s36, s36, 4294942720\n"
      "s_add_u32 s48, s37, 811008\n"
      "s_mov_b32 s53, 0x7f7fffff\n"
      "v_mov_b32 v179, 0\n"
      "s_mov_b32 s54, 0xffff\n"
      "s_mov_b32 s55, 0\n"
      "v_mov_b32 v9, 0\n"
      "ds_write_b32 v11, v9\n"
      "v_mov_b32 v56, 0x3f803f80\n"
      "v_mov_b32 v57, 0x3f803f80\n"
      "v_mov_b32 v58, 0x3f803f80\n"
      "v_mov_b32 v59, 0x3f803f80\n"
      "s_mov_b64 exec, 0\n"
      "s_lshr_b32 s22, s41, 12\n"
      ".Lq_pl:\n"
      "s_cmp_eq_u32 s22, 0\n"
      "s_cbranch_scc1 .Lq_p0\n"
      "s_cmp_eq_u32 s22, 1\n"
      "s_cbranch_scc1 .Lq_p1\n"
      "s_branch .Lq_pe\n"
      ".Lq_pr:\n"
      "s_add_u32 s22, s22, 4\n"
      "s_branch .Lq_pl\n"
      ".Lq_pe:\n"
      "s_mov_b64 exec, -1\n"
      "s_waitcnt vmcnt(8) lgkmcnt(0)\n"
      "s_barrier\n"
      "ds_read_b64_tr_b16 v[60:61], v53 offset:0\n"
      "ds_read_b64_tr_b16 v[62:63], v53 offset:1920\n"
      "ds_read_b64_tr_b16 a[0:1], v52 offset:144\n"
      "ds_read_b64_tr_b16 a[2:3], v52 offset:2064\n"
      "ds_read_b64_tr_b16 a[72:73], v52 offset:288\n"
      "ds_read_b64_tr_b16 a[74:75], v52 offset:2208\n"
      "ds_read_b64_tr_b16 a[144:145], v52 offset:0\n"
      "ds_read_b64_tr_b16 a[146:147], v52 offset:1920\n"
      "v_add_u32 v52, 16896, v52\n"
      "v_add_u32 v53, 16896, v53\n"
      "s_add_u32 m0, s43, 116224\n"
      "s_nop 0\n"
      "buffer_load_dwordx4 v48, s[12:15], s51 offen offset:0 lds\n"
      "buffer_load_dwordx4 v49, s[12:15], s51 offen offset:1024 lds\n"
      "buffer_load_dwordx4 v50, s[12:15], s51 offen offset:2048 lds\n"
      "buffer_load_dwordx4 v51, s[12:15], s51 offen offset:3072 lds\n"
      "s_add_u32 s51, s51, 110592\n"
      "s_waitcnt vmcnt(8) lgkmcnt(0)\n"
      "s_barrier\n"
      "ds_read_b64_tr_b16 v[68:69], v53 offset:0\n"
      "ds_read_b64_tr_b16 v[70:71], v53 offset:1920\n"
      "ds_read_b64_tr_b16 a[4:5], v52 offset:144\n"
      "ds_read_b64_tr_b16 a[6:7], v52 offset:2064\n"
      "ds_read_b64_tr_b16 a[76:77], v52 offset:288\n"
      "ds_read_b64_tr_b16 a[78:79], v52 offset:2208\n"
      "ds_read_b64_tr_b16 a[148:149], v52 offset:0\n"
      "ds_read_b64_tr_b16 a[150:151], v52 offset:1920\n"
      "v_add_u32 v52, 16896, v52\n"
      "v_add_u32 v53, 16896, v53\n"
      "ds_bpermute_b32 v64, v54, v60\n"
      "ds_bpermute_b32 v65, v54, v61\n"
      "ds_bpermute_b32 v66, v54, v62\n"
      "ds_bpermute_b32 v67, v54, v63\n"
      "v_mfma_f32_16x16x32_bf16 v[136:139], a[0:3], v[56:59], 0\n"
      "v_mfma_f32_16x16x32_bf16 v[144:147], v[56:59], a[72:75], 0\n"
      "v_mfma_f32_16x16x32_bf16 v[148:151], a[144:147], v[56:59], 0\n"
      "s_add_u32 m0, s43, 65536\n"
      "s_nop 0\n"
      "buffer_load_dwordx4 v48, s[12:15], s51 offen offset:0 lds\n"
      "buffer_load_dwordx4 v49, s[12:15], s51 offen offset:1024 lds\n"
      "buffer_load_dwordx4 v50, s[12:15], s51 offen offset:2048 lds\n"
      "buffer_load_dwordx4 v51, s[12:15], s51 offen offset:3072 lds\n"
      "s_add_u32 s51, s51, 110592\n"
      "s_waitcnt vmcnt(8) lgkmcnt(0)\n"
      "s_barrier\n"
      "ds_read_b64_tr_b16 v[60:61], v53 offset:0\n"
      "ds_read_b64_tr_b16 v[62:63], v53 offset:1920\n"
      "ds_read_b64_tr_b16 a[8:9], v52 offset:144\n"
      "ds_read_b64_tr_b16 a[10:11], v52 offset:2064\n"
      "ds_read_b64_tr_b16 a[80:81], v52 offset:288\n"
      "ds_read_b64_tr_b16 a[82:83], v52 offset:2208\n"
      "ds_read_b64_tr_b16 a[152:153], v52 offset:0\n"
      "ds_read_b64_tr_b16 a[154:155], v52 offset:1920\n"
      "v_add_u32 v52, 16896, v52\n"
      "v_add_u32 v53, 16896, v53\n"
      "ds_bpermute_b32 v72, v54, v68\n"
      "ds_bpermute_b32 v73, v54, v69\n"
      "ds_bpermute_b32 v74, v54, v70\n"
      "ds_bpermute_b32 v75, v54, v71\n"
      "v_mfma_f32_16x16x32_bf16 v[136:139], a[4:7], v[56:59], v[136:139]\n"
      "v_mfma_f32_16x16x32_bf16 v[144:147], v[56:59], a[76:79], v[144:147]\n"
      "v_mfma_f32_16x16x32_bf16 v[148:151], a[148:151], v[56:59], v[148:151]\n"
      "s_add_u32 m0, s43, 82432\n"
      "s_nop 0\n"
      "buffer_load_dwordx4 v48, s[12:15], s51 offen offset:0 lds\n"
      "buffer_load_dwordx4 v49, s[12:15], s51 offen offset:1024 lds\n"
      "buffer_load_dwordx4 v50, s[12:15], s51 offen offset:2048 lds\n"
      "buffer_load_dwordx4 v51, s[12:15], s51 offen offset:3072 lds\n"
      "s_add_u32 s51, s51, 110592\n"
      "v_and_b32 v64, v55, v64\n"
      "v_and_b32 v65, v55, v65\n"
      "v_and_b32 v66, v55, v66\n"
      "v_and_b32 v67, v55, v67\n"
      "v_accvgpr_write_b32 a216, v64\n"
      "v_accvgpr_write_b32 a217, v65\n"
      "v_accvgpr_write_b32 a218, v66\n"
      "v_accvgpr_write_b32 a219, v67\n"
      "v_mfma_f32_16x16x32_bf16 v[140:143], v[64:67], v[56:59], 0\n"
      "s_waitcnt vmcnt(8) lgkmcnt(0)\n"
      "s_barrier\n"
      "ds_read_b64_tr_b16 v[68:69], v53 offset:0\n"
      "ds_read_b64_tr_b16 v[70:71], v53 offset:1920\n"
      "ds_read_b64_tr_b16 a[12:13], v52 offset:144\n"
      "ds_read_b64_tr_b16 a[14:15], v52 offset:2064\n"
      "ds_read_b64_tr_b16 a[84:85], v52 offset:288\n"
      "ds_read_b64_tr_b16 a[86:87], v52 offset:2208\n"
      "ds_read_b64_tr_b16 a[156:157], v52 offset:0\n"
      "ds_read_b64_tr_b16 a[158:159], v52 offset:1920\n"
      "v_add_u32 v52, 4294916608, v52\n"
      "v_add_u32 v53, 4294916608, v53\n"
      "ds_bpermute_b32 v64, v54, v60\n"
      "ds_bpermute_b32 v65, v54, v61\n"
      "ds_bpermute_b32 v66, v54, v62\n"
      "ds_bpermute_b32 v67, v54, v63\n"
      "v_mfma_f32_16x16x32_bf16 v[136:139], a[8:11], v[56:59], v[136:139]\n"
      "v_mfma_f32_16x16x32_bf16 v[144:147], v[56:59], a[80:83], v[144:147]\n"
      "v_mfma_f32_16x16x32_bf16 v[148:151], a[152:155], v[56:59], v[148:151]\n"
      "s_add_u32 m0, s43, 99328\n"
      "s_nop 0\n"
      "buffer_load_dwordx4 v48, s[12:15], s51 offen offset:0 lds\n"
      "buffer_load_dwordx4 v49, s[12:15], s51 offen offset:1024 lds\n"
      "buffer_load_dwordx4 v50, s[12:15], s51 offen offset:2048 lds\n"
      "buffer_load_dwordx4 v51, s[12:15], s51 offen offset:3072 lds\n"
      "s_add_u32 s51, s51, 110592\n"
      "v_and_b32 v72, v55, v72\n"
      "v_and_b32 v73, v55, v73\n"
      "v_and_b32 v74, v55, v74\n"
      "v_and_b32 v75, v55, v75\n"
      "v_accvgpr_write_b32 a220, v72\n"
      "v_accvgpr_write_b32 a221, v73\n"
      "v_accvgpr_write_b32 a222, v74\n"
      "v_accvgpr_write_b32 a223, v75\n"
      "v_mfma_f32_16x16x32_bf16 v[140:143], v[72:75], v[56:59], v[140:143]\n"
      "s_waitcnt vmcnt(8) lgkmcnt(0)\n"
      "s_barrier\n"
      "ds_read_b64_tr_b16 v[60:61], v53 offset:0\n"
      "ds_read_b64_tr_b16 v[62:63], v53 offset:1920\n"
      "ds_read_b64_tr_b16 a[16:17], v52 offset:144\n"
      "ds_read_b64_tr_b16 a[18:19], v52 offset:2064\n"
      "ds_read_b64_tr_b16 a[88:89], v52 offset:288\n"
      "ds_read_b64_tr_b16 a[90:91], v52 offset:2208\n"
      "ds_read_b64_tr_b16 a[160:161], v52 offset:0\n"
      "ds_read_b64_tr_b16 a[162:163], v52 offset:1920\n"
      "v_add_u32 v52, 16896, v52\n"
      "v_add_u32 v53, 16896, v53\n"
      "ds_bpermute_b32 v72, v54, v68\n"
      "ds_bpermute_b32 v73, v54, v69\n"
      "ds_bpermute_b32 v74, v54, v70\n"
      "ds_bpermute_b32 v75, v54, v71\n"
      "v_mfma_f32_16x16x32_bf16 v[136:139], a[12:15], v[56:59], v[136:139]\n"
      "v_mfma_f32_16x16x32_bf16 v[144:147], v[56:59], a[84:87], v[144:147]\n"
      "v_mfma_f32_16x16x32_bf16 v[148:151], a[156:159], v[56:59], v[148:151]\n"
      "s_add_u32 m0, s43, 116224\n"
      "s_nop 0\n"
      "buffer_load_dwordx4 v48, s[12:15], s51 offen offset:0 lds\n"
      "buffer_load_dwordx4 v49, s[12:15], s51 offen offset:1024 lds\n"
      "buffer_load_dwordx4 v50, s[12:15], s51 offen offset:2048 lds\n"
      "buffer_load_dwordx4 v51, s[12:15], s51 offen offset:3072 lds\n"
      "s_add_u32 s51, s51, 110592\n"
      "v_and_b32 v64, v55, v64\n"
      "v_and_b32 v65, v55, v65\n"
      "v_and_b32 v66, v55, v66\n"
      "v_and_b32 v67, v55, v67\n"
      "v_accvgpr_write_b32 a224, v64\n"
      "v_accvgpr_write_b32 a225, v65\n"
      "v_accvgpr_write_b32 a226, v66\n"
      "v_accvgpr_write_b32 a227, v67\n"
      "v_mfma_f32_16x16x32_bf16 v[140:143], v[64:67], v[56:59], v[140:143]\n"
      "s_waitcnt vmcnt(8) lgkmcnt(0)\n"
      "s_barrier\n"
      "ds_read_b64_tr_b16 v[68:69], v53 offset:0\n"
      "ds_read_b64_tr_b16 v[70:71], v53 offset:1920\n"
      "ds_read_b64_tr_b16 a[20:21], v52 offset:144\n"
      "ds_read_b64_tr_b16 a[22:23], v52 offset:2064\n"
      "ds_read_b64_tr_b16 a[92:93], v52 offset:288\n"
      "ds_read_b64_tr_b16 a[94:95], v52 offset:2208\n"
      "ds_read_b64_tr_b16 a[164:165], v52 offset:0\n"
      "ds_read_b64_tr_b16 a[166:167], v52 offset:1920\n"
      "v_add_u32 v52, 16896, v52\n"
      "v_add_u32 v53, 16896, v53\n"
      "ds_bpermute_b32 v64, v54, v60\n"
      "ds_bpermute_b32 v65, v54, v61\n"
      "ds_bpermute_b32 v66, v54, v62\n"
      "ds_bpermute_b32 v67, v54, v63\n"
      "v_mfma_f32_16x16x32_bf16 v[136:139], a[16:19], v[56:59], v[136:139]\n"
      "v_mfma_f32_16x16x32_bf16 v[144:147], v[56:59], a[88:91], v[144:147]\n"
      "v_mfma_f32_16x16x32_bf16 v[148:151], a[160:163], v[56:59], v[148:151]\n"
      "s_add_u32 m0, s43, 65536\n"
      "s_nop 0\n"
      "buffer_load_dwordx4 v48, s[12:15], s51 offen offset:0 lds\n"
      "buffer_load_dwordx4 v49, s[12:15], s51 offen offset:1024 lds\n"
      "buffer_load_dwordx4 v50, s[12:15], s51 offen offset:2048 lds\n"
      "buffer_load_dwordx4 v51, s[12:15], s51 offen offset:3072 lds\n"
      "s_add_u32 s51, s51, 110592\n"
      "v_and_b32 v72, v55, v72\n"
      "v_and_b32 v73, v55, v73\n"
      "v_and_b32 v74, v55, v74\n"
      "v_and_b32 v75, v55, v75\n"
      "v_accvgpr_write_b32 a228, v72\n"
      "v_accvgpr_write_b32 a229, v73\n"
      "v_accvgpr_write_b32 a230, v74\n"
      "v_accvgpr_write_b32 a231, v75\n"
      "v_mfma_f32_16x16x32_bf16 v[140:143], v[72:75], v[56:59], v[140:143]\n"
      "s_waitcnt vmcnt(8) lgkmcnt(0)\n"
      "s_barrier\n"
      "ds_read_b64_tr_b16 v[60:61], v53 offset:0\n"
      "ds_read_b64_tr_b16 v[62:63], v53 offset:1920\n"
      "ds_read_b64_tr_b16 a[24:25], v52 offset:144\n"
      "ds_read_b64_tr_b16 a[26:27], v52 offset:2064\n"
      "ds_read_b64_tr_b16 a[96:97], v52 offset:288\n"
      "ds_read_b64_tr_b16 a[98:99], v52 offset:2208\n"
      "ds_read_b64_tr_b16 a[168:169], v52 offset:0\n"
      "ds_read_b64_tr_b16 a[170:171], v52 offset:1920\n"
      "v_add_u32 v52, 16896, v52\n"
      "v_add_u32 v53, 16896, v53\n"
      "ds_bpermute_b32 v72, v54, v68\n"
      "ds_bpermute_b32 v73, v54, v69\n"
      "ds_bpermute_b32 v74, v54, v70\n"
      "ds_bpermute_b32 v75, v54, v71\n"
      "v_mfma_f32_16x16x32_bf16 v[136:139], a[20:23], v[56:59], v[136:139]\n"
      "v_mfma_f32_16x16x32_bf16 v[144:147], v[56:59], a[92:95], v[144:147]\n"
      "v_mfma_f32_16x16x32_bf16 v[148:151], a[164:167], v[56:59], v[148:151]\n"
      "s_add_u32 m0, s43, 82432\n"
      "s_nop 0\n"
      "buffer_load_dwordx4 v48, s[12:15], s51 offen offset:0 lds\n"
      "buffer_load_dwordx4 v49, s[12:15], s51 offen offset:1024 lds\n"
      "buffer_load_dwordx4 v50, s[12:15], s51 offen offset:2048 lds\n"
      "buffer_load_dwordx4 v51, s[12:15], s51 offen offset:3072 lds\n"
      "s_add_u32 s51, s51, 110592\n"
      "v_and_b32 v64, v55, v64\n"
      "v_and_b32 v65, v55, v65\n"
      "v_and_b32 v66, v55, v66\n"
      "v_and_b32 v67, v55, v67\n"
      "v_accvgpr_write_b32 a232, v64\n"
      "v_accvgpr_write_b32 a233, v65\n"
      "v_accvgpr_write_b32 a234, v66\n"
      "v_accvgpr_write_b32 a235, v67\n"
      "v_mfma_f32_16x16x32_bf16 v[140:143], v[64:67], v[56:59], v[140:143]\n"
      "s_waitcnt vmcnt(8) lgkmcnt(0)\n"
      "s_barrier\n"
      "ds_read_b64_tr_b16 v[68:69], v53 offset:0\n"
      "ds_read_b64_tr_b16 v[70:71], v53 offset:1920\n"
      "ds_read_b64_tr_b16 a[28:29], v52 offset:144\n"
      "ds_read_b64_tr_b16 a[30:31], v52 offset:2064\n"
      "ds_read_b64_tr_b16 a[100:101], v52 offset:288\n"
      "ds_read_b64_tr_b16 a[102:103], v52 offset:2208\n"
      "ds_read_b64_tr_b16 a[172:173], v52 offset:0\n"
      "ds_read_b64_tr_b16 a[174:175], v52 offset:1920\n"
      "v_add_u32 v52, 4294916608, v52\n"
      "v_add_u32 v53, 4294916608, v53\n"
      "ds_bpermute_b32 v64, v54, v60\n"
      "ds_bpermute_b32 v65, v54, v61\n"
      "ds_bpermute_b32 v66, v54, v62\n"
      "ds_bpermute_b32 v67, v54, v63\n"
      "v_mfma_f32_16x16x32_bf16 v[136:139], a[24:27], v[56:59], v[136:139]\n"
      "v_mfma_f32_16x16x32_bf16 v[144:147], v[56:59], a[96:99], v[144:147]\n"
      "v_mfma_f32_16x16x32_bf16 v[148:151], a[168:171], v[56:59], v[148:151]\n"
      "s_add_u32 m0, s43, 99328\n"
      "s_nop 0\n"
      "buffer_load_dwordx4 v48, s[12:15], s51 offen offset:0 lds\n"
      "buffer_load_dwordx4 v49, s[12:15], s51 offen offset:1024 lds\n"
      "buffer_load_dwordx4 v50, s[12:15], s51 offen offset:2048 lds\n"
      "buffer_load_dwordx4 v51, s[12:15], s51 offen offset:3072 lds\n"
      "s_add_u32 s51, s51, 110592\n"
      "v_and_b32 v72, v55, v72\n"
      "v_and_b32 v73, v55, v73\n"
      "v_and_b32 v74, v55, v74\n"
      "v_and_b32 v75, v55, v75\n"
      "v_accvgpr_write_b32 a236, v72\n"
      "v_accvgpr_write_b32 a237, v73\n"
      "v_accvgpr_write_b32 a238, v74\n"
      "v_accvgpr_write_b32 a239, v75\n"
      "v_mfma_f32_16x16x32_bf16 v[140:143], v[72:75], v[56:59], v[140:143]\n"
      "s_waitcnt vmcnt(8) lgkmcnt(0)\n"
      "s_barrier\n"
      "ds_read_b64_tr_b16 v[60:61], v53 offset:0\n"
      "ds_read_b64_tr_b16 v[62:63], v53 offset:1920\n"
      "ds_read_b64_tr_b16 a[32:33], v52 offset:144\n"
      "ds_read_b64_tr_b16 a[34:35], v52 offset:2064\n"
      "ds_read_b64_tr_b16 a[104:105], v52 offset:288\n"
      "ds_read_b64_tr_b16 a[106:107], v52 offset:2208\n"
      "ds_read_b64_tr_b16 a[176:177], v52 offset:0\n"
      "ds_read_b64_tr_b16 a[178:179], v52 offset:1920\n"
      "v_add_u32 v52, 16896, v52\n"
      "v_add_u32 v53, 16896, v53\n"
      "ds_bpermute_b32 v72, v54, v68\n"
      "ds_bpermute_b32 v73, v54, v69\n"
      "ds_bpermute_b32 v74, v54, v70\n"
      "ds_bpermute_b32 v75, v54, v71\n"
      "v_mfma_f32_16x16x32_bf16 v[136:139], a[28:31], v[56:59], v[136:139]\n"
      "v_mfma_f32_16x16x32_bf16 v[144:147], v[56:59], a[100:103], v[144:147]\n"
      "v_mfma_f32_16x16x32_bf16 v[148:151], a[172:175], v[56:59], v[148:151]\n"
      "s_add_u32 m0, s43, 116224\n"
      "s_nop 0\n"
      "buffer_load_dwordx4 v48, s[12:15], s51 offen offset:0 lds\n"
      "buffer_load_dwordx4 v49, s[12:15], s51 offen offset:1024 lds\n"
      "buffer_load_dwordx4 v50, s[12:15], s51 offen offset:2048 lds\n"
      "buffer_load_dwordx4 v51, s[12:15], s51 offen offset:3072 lds\n"
      "s_add_u32 s51, s51, 110592\n"
      "v_and_b32 v64, v55, v64\n"
      "v_and_b32 v65, v55, v65\n"
      "v_and_b32 v66, v55, v66\n"
      "v_and_b32 v67, v55, v67\n"
      "v_accvgpr_write_b32 a240, v64\n"
      "v_accvgpr_write_b32 a241, v65\n"
      "v_accvgpr_write_b32 a242, v66\n"
      "v_accvgpr_write_b32 a243, v67\n"
      "v_mfma_f32_16x16x32_bf16 v[140:143], v[64:67], v[56:59], v[140:143]\n"
      "s_waitcnt vmcnt(8) lgkmcnt(0)\n"
      "s_barrier\n"
      "ds_read_b64_tr_b16 v[68:69], v53 offset:0\n"
      "ds_read_b64_tr_b16 v[70:71], v53 offset:1920\n"
      "ds_read_b64_tr_b16 a[36:37], v52 offset:144\n"
      "ds_read_b64_tr_b16 a[38:39], v52 offset:2064\n"
      "ds_read_b64_tr_b16 a[108:109], v52 offset:288\n"
      "ds_read_b64_tr_b16 a[110:111], v52 offset:2208\n"
      "ds_read_b64_tr_b16 a[180:181], v52 offset:0\n"
      "ds_read_b64_tr_b16 a[182:183], v52 offset:1920\n"
      "v_add_u32 v52, 16896, v52\n"
      "v_add_u32 v53, 16896, v53\n"
      "ds_bpermute_b32 v64, v54, v60\n"
      "ds_bpermute_b32 v65, v54, v61\n"
      "ds_bpermute_b32 v66, v54, v62\n"
      "ds_bpermute_b32 v67, v54, v63\n"
      "v_mfma_f32_16x16x32_bf16 v[136:139], a[32:35], v[56:59], v[136:139]\n"
      "v_mfma_f32_16x16x32_bf16 v[144:147], v[56:59], a[104:107], v[144:147]\n"
      "v_mfma_f32_16x16x32_bf16 v[148:151], a[176:179], v[56:59], v[148:151]\n"
      "s_add_u32 m0, s43, 65536\n"
      "s_nop 0\n"
      "buffer_load_dwordx4 v48, s[12:15], s51 offen offset:0 lds\n"
      "buffer_load_dwordx4 v49, s[12:15], s51 offen offset:1024 lds\n"
      "buffer_load_dwordx4 v50, s[12:15], s51 offen offset:2048 lds\n"
      "buffer_load_dwordx4 v51, s[12:15], s51 offen offset:3072 lds\n"
      "s_add_u32 s51, s51, 110592\n"
      "v_and_b32 v72, v55, v72\n"
      "v_and_b32 v73, v55, v73\n"
      "v_and_b32 v74, v55, v74\n"
      "v_and_b32 v75, v55, v75\n"
      "v_accvgpr_write_b32 a244, v72\n"
      "v_accvgpr_write_b32 a245, v73\n"
      "v_accvgpr_write_b32 a246, v74\n"
      "v_accvgpr_write_b32 a247, v75\n"
      "v_mfma_f32_16x16x32_bf16 v[140:143], v[72:75], v[56:59], v[140:143]\n"
      "s_waitcnt vmcnt(8) lgkmcnt(0)\n"
      "s_barrier\n"
      "ds_read_b64_tr_b16 v[60:61], v53 offset:0\n"
      "ds_read_b64_tr_b16 v[62:63], v53 offset:1920\n"
      "ds_read_b64_tr_b16 a[40:41], v52 offset:144\n"
      "ds_read_b64_tr_b16 a[42:43], v52 offset:2064\n"
      "ds_read_b64_tr_b16 a[112:113], v52 offset:288\n"
      "ds_read_b64_tr_b16 a[114:115], v52 offset:2208\n"
      "ds_read_b64_tr_b16 a[184:185], v52 offset:0\n"
      "ds_read_b64_tr_b16 a[186:187], v52 offset:1920\n"
      "v_add_u32 v52, 16896, v52\n"
      "v_add_u32 v53, 16896, v53\n"
      "ds_bpermute_b32 v72, v54, v68\n"
      "ds_bpermute_b32 v73, v54, v69\n"
      "ds_bpermute_b32 v74, v54, v70\n"
      "ds_bpermute_b32 v75, v54, v71\n"
      "v_mfma_f32_16x16x32_bf16 v[136:139], a[36:39], v[56:59], v[136:139]\n"
      "v_mfma_f32_16x16x32_bf16 v[144:147], v[56:59], a[108:111], v[144:147]\n"
      "v_mfma_f32_16x16x32_bf16 v[148:151], a[180:183], v[56:59], v[148:151]\n"
      "s_add_u32 m0, s43, 82432\n"
      "s_nop 0\n"
      "buffer_load_dwordx4 v48, s[12:15], s51 offen offset:0 lds\n"
      "buffer_load_dwordx4 v49, s[12:15], s51 offen offset:1024 lds\n"
      "buffer_load_dwordx4 v50, s[12:15], s51 offen offset:2048 lds\n"
      "buffer_load_dwordx4 v51, s[12:15], s51 offen offset:3072 lds\n"
      "s_add_u32 s51, s51, 110592\n"
      "v_and_b32 v64, v55, v64\n"
      "v_and_b32 v65, v55, v65\n"
      "v_and_b32 v66, v55, v66\n"
      "v_and_b32 v67, v55, v67\n"
      "v_accvgpr_write_b32 a248, v64\n"
      "v_accvgpr_write_b32 a249, v65\n"
      "v_accvgpr_write_b32 a250, v66\n"
      "v_accvgpr_write_b32 a251, v67\n"
      "v_mfma_f32_16x16x32_bf16 v[140:143], v[64:67], v[56:59], v[140:143]\n"
      "s_waitcnt vmcnt(8) lgkmcnt(0)\n"
      "s_barrier\n"
      "ds_read_b64_tr_b16 v[68:69], v53 offset:0\n"
      "ds_read_b64_tr_b16 v[70:71], v53 offset:1920\n"
      "ds_read_b64_tr_b16 a[44:45], v52 offset:144\n"
      "ds_read_b64_tr_b16 a[46:47], v52 offset:2064\n"
      "ds_read_b64_tr_b16 a[116:117], v52 offset:288\n"
      "ds_read_b64_tr_b16 a[118:119], v52 offset:2208\n"
      "ds_read_b64_tr_b16 a[188:189], v52 offset:0\n"
      "ds_read_b64_tr_b16 a[190:191], v52 offset:1920\n"
      "v_add_u32 v52, 4294916608, v52\n"
      "v_add_u32 v53, 4294916608, v53\n"
      "ds_bpermute_b32 v64, v54, v60\n"
      "ds_bpermute_b32 v65, v54, v61\n"
      "ds_bpermute_b32 v66, v54, v62\n"
      "ds_bpermute_b32 v67, v54, v63\n"
      "v_mfma_f32_16x16x32_bf16 v[136:139], a[40:43], v[56:59], v[136:139]\n"
      "v_mfma_f32_16x16x32_bf16 v[144:147], v[56:59], a[112:115], v[144:147]\n"
      "v_mfma_f32_16x16x32_bf16 v[148:151], a[184:187], v[56:59], v[148:151]\n"
      "s_add_u32 m0, s43, 99328\n"
      "s_nop 0\n"
      "buffer_load_dwordx4 v48, s[12:15], s51 offen offset:0 lds\n"
      "buffer_load_dwordx4 v49, s[12:15], s51 offen offset:1024 lds\n"
      "buffer_load_dwordx4 v50, s[12:15], s51 offen offset:2048 lds\n"
      "buffer_load_dwordx4 v51, s[12:15], s51 offen offset:3072 lds\n"
      "s_add_u32 s51, s51, 110592\n"
      "v_and_b32 v72, v55, v72\n"
      "v_and_b32 v73, v55, v73\n"
      "v_and_b32 v74, v55, v74\n"
      "v_and_b32 v75, v55, v75\n"
      "v_accvgpr_write_b32 a252, v72\n"
      "v_accvgpr_write_b32 a253, v73\n"
      "v_accvgpr_write_b32 a254, v74\n"
      "v_accvgpr_write_b32 a255, v75\n"
      "v_mfma_f32_16x16x32_bf16 v[140:143], v[72:75], v[56:59], v[140:143]\n"
      "s_waitcnt vmcnt(8) lgkmcnt(0)\n"
      "s_barrier\n"
      "ds_read_b64_tr_b16 v[60:61], v53 offset:0\n"
      "ds_read_b64_tr_b16 v[62:63], v53 offset:1920\n"
      "ds_read_b64_tr_b16 a[48:49], v52 offset:144\n"
      "ds_read_b64_tr_b16 a[50:51], v52 offset:2064\n"
      "ds_read_b64_tr_b16 a[120:121], v52 offset:288\n"
      "ds_read_b64_tr_b16 a[122:123], v52 offset:2208\n"
      "ds_read_b64_tr_b16 a[192:193], v52 offset:0\n"
      "ds_read_b64_tr_b16 a[194:195], v52 offset:1920\n"
      "v_add_u32 v52, 16896, v52\n"
      "v_add_u32 v53, 16896, v53\n"
      "ds_bpermute_b32 v72, v54, v68\n"
      "ds_bpermute_b32 v73, v54, v69\n"
      "ds_bpermute_b32 v74, v54, v70\n"
      "ds_bpermute_b32 v75, v54, v71\n"
      "v_mfma_f32_16x16x32_bf16 v[136:139], a[44:47], v[56:59], v[136:139]\n"
      "v_mfma_f32_16x16x32_bf16 v[144:147], v[56:59], a[116:119], v[144:147]\n"
      "v_mfma_f32_16x16x32_bf16 v[148:151], a[188:191], v[56:59], v[148:151]\n"
      "s_add_u32 m0, s43, 116224\n"
      "s_nop 0\n"
      "buffer_load_dwordx4 v48, s[12:15], s51 offen offset:0 lds\n"
      "buffer_load_dwordx4 v49, s[12:15], s51 offen offset:1024 lds\n"
      "buffer_load_dwordx4 v50, s[12:15], s51 offen offset:2048 lds\n"
      "buffer_load_dwordx4 v51, s[12:15], s51 offen offset:3072 lds\n"
      "s_add_u32 s51, s51, 110592\n"
      "v_and_b32 v64, v55, v64\n"
      "v_and_b32 v65, v55, v65\n"
      "v_and_b32 v66, v55, v66\n"
      "v_and_b32 v67, v55, v67\n"
      "v_mov_b32 v16, v64\n"
      "v_mov_b32 v17, v65\n"
      "v_mov_b32 v18, v66\n"
      "v_mov_b32 v19, v67\n"
      "v_mfma_f32_16x16x32_bf16 v[140:143], v[64:67], v[56:59], v[140:143]\n"
      "s_add_u32 s30, s36, 24576\n"
      "buffer_load_dword v78, v2, s[8:11], s30 offen sc1\n"
      "s_add_u32 s30, s36, 49152\n"
      "buffer_load_dword v79, v2, s[8:11], s30 offen sc1\n"
      "s_waitcnt vmcnt(10) lgkmcnt(0)\n"
      "s_barrier\n"
      "ds_read_b64_tr_b16 v[68:69], v53 offset:0\n"
      "ds_read_b64_tr_b16 v[70:71], v53 offset:1920\n"
      "ds_read_b64_tr_b16 a[52:53], v52 offset:144\n"
      "ds_read_b64_tr_b16 a[54:55], v52 offset:2064\n"
      "ds_read_b64_tr_b16 a[124:125], v52 offset:288\n"
      "ds_read_b64_tr_b16 a[126:127], v52 offset:2208\n"
      "ds_read_b64_tr_b16 a[196:197], v52 offset:0\n"
      "ds_read_b64_tr_b16 a[198:199], v52 offset:1920\n"
      "v_add_u32 v52, 16896, v52\n"
      "v_add_u32 v53, 16896, v53\n"
      "ds_bpermute_b32 v64, v54, v60\n"
      "ds_bpermute_b32 v65, v54, v61\n"
      "ds_bpermute_b32 v66, v54, v62\n"
      "ds_bpermute_b32 v67, v54, v63\n"
      "v_mfma_f32_16x16x32_bf16 v[136:139], a[48:51], v[56:59], v[136:139]\n"
      "v_mfma_f32_16x16x32_bf16 v[144:147], v[56:59], a[120:123], v[144:147]\n"
      "v_mfma_f32_16x16x32_bf16 v[148:151], a[192:195], v[56:59], v[148:151]\n"
      "s_add_u32 m0, s43, 65536\n"
      "s_nop 0\n"
      "buffer_load_dwordx4 v48, s[12:15], s51 offen offset:0 lds\n"
      "buffer_load_dwordx4 v49, s[12:15], s51 offen offset:1024 lds\n"
      "buffer_load_dwordx4 v50, s[12:15], s51 offen offset:2048 lds\n"
      "buffer_load_dwordx4 v51, s[12:15], s51 offen offset:3072 lds\n"
      "s_add_u32 s51, s51, 110592\n"
      "v_and_b32 v72, v55, v72\n"
      "v_and_b32 v73, v55, v73\n"
      "v_and_b32 v74, v55, v74\n"
      "v_and_b32 v75, v55, v75\n"
      "v_mov_b32 v20, v72\n"
      "v_mov_b32 v21, v73\n"
      "v_mov_b32 v22, v74\n"
      "v_mov_b32 v23, v75\n"
      "v_mfma_f32_16x16x32_bf16 v[140:143], v[72:75], v[56:59], v[140:143]\n"
      "s_waitcnt vmcnt(10) lgkmcnt(0)\n"
      "s_barrier\n"
      "ds_read_b64_tr_b16 v[60:61], v53 offset:0\n"
      "ds_read_b64_tr_b16 v[62:63], v53 offset:1920\n"
      "ds_read_b64_tr_b16 a[56:57], v52 offset:144\n"
      "ds_read_b64_tr_b16 a[58:59], v52 offset:2064\n"
      "ds_read_b64_tr_b16 a[128:129], v52 offset:288\n"
      "ds_read_b64_tr_b16 a[130:131], v52 offset:2208\n"
      "ds_read_b64_tr_b16 a[200:201], v52 offset:0\n"
      "ds_read_b64_tr_b16 a[202:203], v52 offset:1920\n"
      "v_add_u32 v52, 16896, v52\n"
      "v_add_u32 v53, 16896, v53\n"
      "ds_bpermute_b32 v72, v54, v68\n"
      "ds_bpermute_b32 v73, v54, v69\n"
      "ds_bpermute_b32 v74, v54, v70\n"
      "ds_bpermute_b32 v75, v54, v71\n"
      "v_mfma_f32_16x16x32_bf16 v[136:139], a[52:55], v[56:59], v[136:139]\n"
      "v_mfma_f32_16x16x32_bf16 v[144:147], v[56:59], a[124:127], v[144:147]\n"
      "v_mfma_f32_16x16x32_bf16 v[148:151], a[196:199], v[56:59], v[148:151]\n"
      "s_add_u32 m0, s43, 82432\n"
      "s_nop 0\n"
      "buffer_load_dwordx4 v48, s[12:15], s51 offen offset:0 lds\n"
      "buffer_load_dwordx4 v49, s[12:15], s51 offen offset:1024 lds\n"
      "buffer_load_dwordx4 v50, s[12:15], s51 offen offset:2048 lds\n"
      "buffer_load_dwordx4 v51, s[12:15], s51 offen offset:3072 lds\n"
      "s_add_u32 s51, s51, 110592\n"
      "v_and_b32 v64, v55, v64\n"
      "v_and_b32 v65, v55, v65\n"
      "v_and_b32 v66, v55, v66\n"
      "v_and_b32 v67, v55, v67\n"
      "v_mov_b32 v24, v64\n"
      "v_mov_b32 v25, v65\n"
      "v_mov_b32 v26, v66\n"
      "v_mov_b32 v27, v67\n"
      "v_mfma_f32_16x16x32_bf16 v[140:143], v[64:67], v[56:59], v[140:143]\n"
      "s_mul_i32 s21, s21, 1664525\n"
      "s_add_u32 s21, s21, 1013904223\n"
      "v_mov_b32 v13, s21\n"
      "buffer_store_dword v13, v10, s[8:11], 0 offen sc1\n"
      "v_not_b32 v80, v13\n"
      "v_not_b32 v81, v13\n"
      "v_not_b32 v82, v13\n"
      "v_not_b32 v83, v13\n"
      "s_add_u32 s30, s40, 0\n"
      "buffer_store_dwordx4 v[80:83], v76, s[8:11], s30 offen sc1\n"
      "v_xor_b32 v13, v13, v14\n"
      "s_add_u32 s52, s40, 4294893568\n"
      "s_add_u32 s40, s40, 4294918144\n"
      "s_add_u32 s36, s36, 24576\n"
      "s_mov_b32 s45, s36\n"
      "s_min_u32 s45, s45, s6\n"
      "s_waitcnt vmcnt(11)\n"
      "v_cmp_ne_u32 vcc, s21, v78\n"
      "s_add_u32 m0, s41, 0\n"
      "s_add_u32 s36, s36, 24576\n"
      "s_mov_b32 s46, s45\n"
      "s_cbranch_vccnz .Lsp_q0_1\n"
      ".Lbk_q0_1:\n"
      "buffer_load_dwordx4 v0, s[8:11], s46 offen offset:1024 sc1 lds\n"
      "buffer_load_dwordx4 v0, s[8:11], s46 offen offset:0 sc1 lds\n"
      "buffer_load_dwordx4 v0, s[8:11], s46 offen offset:2048 sc1 lds\n"
      "s_mov_b32 s45, s36\n"
      "s_min_u32 s45, s45, s6\n"
      "buffer_load_dwordx4 v0, s[8:11], s46 offen offset:3072 sc1 lds\n"
      "s_add_u32 m0, s42, 0\n"
      "s_nop 0\n"
      "buffer_load_dwordx4 v1, s[8:11], s46 offen offset:0 sc1 lds\n"
      "s_waitcnt lgkmcnt(0)\n"
      "s_barrier\n"
      "ds_read_b64_tr_b16 v[68:69], v53 offset:0\n"
      "ds_read_b64_tr_b16 v[70:71], v53 offset:1920\n"
      "ds_read_b64_tr_b16 a[60:61], v52 offset:144\n"
      "ds_read_b64_tr_b16 a[62:63], v52 offset:2064\n"
      "ds_read_b64_tr_b16 a[132:133], v52 offset:288\n"
      "ds_read_b64_tr_b16 a[134:135], v52 offset:2208\n"
      "ds_read_b64_tr_b16 a[204:205], v52 offset:0\n"
      "ds_read_b64_tr_b16 a[206:207], v52 offset:1920\n"
      "v_add_u32 v52, 4294916608, v52\n"
      "v_add_u32 v53, 4294916608, v53\n"
      "ds_bpermute_b32 v64, v54, v60\n"
      "ds_bpermute_b32 v65, v54, v61\n"
      "ds_bpermute_b32 v66, v54, v62\n"
      "ds_bpermute_b32 v67, v54, v63\n"
      "v_mfma_f32_16x16x32_bf16 v[136:139], a[56:59], v[56:59], v[136:139]\n"
      "v_mfma_f32_16x16x32_bf16 v[144:147], v[56:59], a[128:131], v[144:147]\n"
      "v_mfma_f32_16x16x32_bf16 v[148:151], a[200:203], v[56:59], v[148:151]\n"
      "v_and_b32 v72, v55, v72\n"
      "v_and_b32 v73, v55, v73\n"
      "v_and_b32 v74, v55, v74\n"
      "v_and_b32 v75, v55, v75\n"
      "v_mov_b32 v28, v72\n"
      "v_mov_b32 v29, v73\n"
      "v_mov_b32 v30, v74\n"
      "v_mov_b32 v31, v75\n"
      "v_mfma_f32_16x16x32_bf16 v[140:143], v[72:75], v[56:59], v[140:143]\n"
      "s_waitcnt vmcnt(11) lgkmcnt(0)\n"
      "s_barrier\n"
      "ds_read_b64_tr_b16 v[60:61], v53 offset:0\n"
      "ds_read_b64_tr_b16 v[62:63], v53 offset:1920\n"
      "ds_read_b64_tr_b16 a[64:65], v52 offset:144\n"
      "ds_read_b64_tr_b16 a[66:67], v52 offset:2064\n"
      "ds_read_b64_tr_b16 a[136:137], v52 offset:288\n"
      "ds_read_b64_tr_b16 a[138:139], v52 offset:2208\n"
      "ds_read_b64_tr_b16 a[208:209], v52 offset:0\n"
      "ds_read_b64_tr_b16 a[210:211], v52 offset:1920\n"
      "v_add_u32 v52, 16896, v52\n"
      "v_add_u32 v53, 16896, v53\n"
      "ds_bpermute_b32 v72, v54, v68\n"
      "ds_bpermute_b32 v73, v54, v69\n"
      "ds_bpermute_b32 v74, v54, v70\n"
      "ds_bpermute_b32 v75, v54, v71\n"
      "v_mfma_f32_16x16x32_bf16 v[136:139], a[60:63], v[56:59], v[136:139]\n"
      "v_mfma_f32_16x16x32_bf16 v[144:147], v[56:59], a[132:135], v[144:147]\n"
      "v_mfma_f32_16x16x32_bf16 v[148:151], a[204:207], v[56:59], v[148:151]\n"
      "v_and_b32 v64, v55, v64\n"
      "v_and_b32 v65, v55, v65\n"
      "v_and_b32 v66, v55, v66\n"
      "v_and_b32 v67, v55, v67\n"
      "v_mov_b32 v32, v64\n"
      "v_mov_b32 v33, v65\n"
      "v_mov_b32 v34, v66\n"
      "v_mov_b32 v35, v67\n"
      "v_mfma_f32_16x16x32_bf16 v[140:143], v[64:67], v[56:59], v[140:143]\n"
      "s_waitcnt vmcnt(7) lgkmcnt(0)\n"
      "s_barrier\n"
      "ds_read_b64_tr_b16 v[68:69], v53 offset:0\n"
      "ds_read_b64_tr_b16 v[70:71], v53 offset:1920\n"
      "ds_read_b64_tr_b16 a[68:69], v52 offset:144\n"
      "ds_read_b64_tr_b16 a[70:71], v52 offset:2064\n"
      "ds_read_b64_tr_b16 a[140:141], v52 offset:288\n"
      "ds_read_b64_tr_b16 a[142:143], v52 offset:2208\n"
      "ds_read_b64_tr_b16 a[212:213], v52 offset:0\n"
      "ds_read_b64_tr_b16 a[214:215], v52 offset:1920\n"
      "v_add_u32 v52, 16896, v52\n"
      "v_add_u32 v53, 16896, v53\n"
      "ds_bpermute_b32 v64, v54, v60\n"
      "ds_bpermute_b32 v65, v54, v61\n"
      "ds_bpermute_b32 v66, v54, v62\n"
      "ds_bpermute_b32 v67, v54, v63\n"
      "v_mfma_f32_16x16x32_bf16 v[136:139], a[64:67], v[56:59], v[136:139]\n"
      "v_mfma_f32_16x16x32_bf16 v[144:147], v[56:59], a[136:139], v[144:147]\n"
      "v_mfma_f32_16x16x32_bf16 v[148:151], a[208:211], v[56:59], v[148:151]\n"
      "v_and_b32 v72, v55, v72\n"
      "v_and_b32 v73, v55, v73\n"
      "v_and_b32 v74, v55, v74\n"
      "v_and_b32 v75, v55, v75\n"
      "v_mov_b32 v36, v72\n"
      "v_mov_b32 v37, v73\n"
      "v_mov_b32 v38, v74\n"
      "v_mov_b32 v39, v75\n"
      "v_mfma_f32_16x16x32_bf16 v[140:143], v[72:75], v[56:59], v[140:143]\n"
      "s_waitcnt lgkmcnt(0)\n"
      "ds_bpermute_b32 v72, v54, v68\n"
      "ds_bpermute_b32 v73, v54, v69\n"
      "ds_bpermute_b32 v74, v54, v70\n"
      "ds_bpermute_b32 v75, v54, v71\n"
      "v_mfma_f32_16x16x32_bf16 v[136:139], a[68:71], v[56:59], v[136:139]\n"
      "v_mfma_f32_16x16x32_bf16 v[144:147], v[56:59], a[140:143], v[144:147]\n"
      "v_mfma_f32_16x16x32_bf16 v[148:151], a[212:215], v[56:59], v[148:151]\n"
      "v_and_b32 v64, v55, v64\n"
      "v_and_b32 v65, v55, v65\n"
      "v_and_b32 v66, v55, v66\n"
      "v_and_b32 v67, v55, v67\n"
      "v_mov_b32 v40, v64\n"
      "v_mov_b32 v41, v65\n"
      "v_mov_b32 v42, v66\n"
      "v_mov_b32 v43, v67\n"
      "v_mfma_f32_16x16x32_bf16 v[140:143], v[64:67], v[56:59], v[140:143]\n"
      "s_waitcnt lgkmcnt(0)\n"
      "v_and_b32 v72, v55, v72\n"
      "v_and_b32 v73, v55, v73\n"
      "v_and_b32 v74, v55, v74\n"
      "v_and_b32 v75, v55, v75\n"
      "v_mov_b32 v44, v72\n"
      "v_mov_b32 v45, v73\n"
      "v_mov_b32 v46, v74\n"
      "v_mov_b32 v47, v75\n"
      "v_mfma_f32_16x16x32_bf16 v[140:143], v[72:75], v[56:59], v[140:143]\n"
      "s_add_u32 s36, s36, 24576\n"
      "s_cmp_eq_u32 s36, s37\n"
      "s_cselect_b32 s47, s38, 0\n"
      "s_cselect_b32 s37, s48, s37\n"
      "s_cselect_b32 s49, s39, s53\n"
      "s_add_u32 m0, s41, 20480\n"
      "s_mov_b32 s46, s45\n"
      "s_add_u32 s45, s36, s47\n"
      "v_add_u32 v9, 1, v9\n"
      "v_cmp_ne_u32 vcc, s21, v79\n"
      "s_min_u32 s45, s45, s6\n"
      "s_cbranch_vccnz .Lsp_q1_2\n"
      ".Lbk_q1_2:\n"
      "buffer_load_dwordx4 v0, s[8:11], s46 offen offset:1024 sc1 lds\n"
      "s_waitcnt vmcnt(1)\n"
      "ds_write_b32 v11, v9\n"
      "buffer_load_dwordx4 v0, s[8:11], s46 offen offset:0 sc1 lds\n"
      "buffer_load_dwordx4 v0, s[8:11], s46 offen offset:3072 sc1 lds\n"
      "buffer_load_dword v15, v2, s[8:11], s45 offen sc1\n"
      "ds_read_b32 v10, v12\n"
      "buffer_load_dwordx4 v0, s[8:11], s46 offen offset:2048 sc1 lds\n"
      "s_waitcnt lgkmcnt(0)\n"
      "v_cmp_gt_i32 vcc, v9, v10\n"
      "s_add_u32 m0, s42, 20480\n"
      "s_nop 0\n"
      "buffer_load_dwordx4 v1, s[8:11], s46 offen offset:0 sc1 lds\n"
      "s_cbranch_vccnz .Lsp_qy0_3\n"
      ".Lbk_qy0_3:\n"
      "ds_read_b128 v[52:55], v0 offset:1024\n"
      "ds_read_b128 v[48:51], v0 offset:0\n"
      "ds_read_b128 v[56:59], v0 offset:2048\n"
      "ds_read_b128 v[68:71], v0 offset:5120\n"
      "ds_read_b128 v[64:67], v0 offset:4096\n"
      "ds_read_b128 v[60:63], v0 offset:3072\n"
      "buffer_load_dword v14, v11, s[8:11], s31 offen\n"
      "ds_read2st64_b64 v[212:215], v4 offset0:0 offset1:1\n"
      "ds_read_b128 v[72:75], v0 offset:6144\n"
      "ds_read_b128 v[76:79], v0 offset:7168\n"
      "ds_read_b128 v[88:91], v0 offset:10240\n"
      "ds_read_b128 v[108:111], v0 offset:15360\n"
      "s_mov_b32 s20, 21\n"
      "s_waitcnt lgkmcnt(0)\n"
      "s_nop 4\n"
      "s_branch .Lq_loop\n"
      ".p2align 12\n"
      ".Lq_p0:\n"
      "s_branch .Lq_pr\n"
      ".p2align 5\n"
      ".Lq_loop:\n"
      "v_mfma_f32_16x16x32_bf16 v[120:123], a[0:3], v[48:51], 0\n"
      "ds_read_b128 v[80:83], v0 offset:8192\n"
      "ds_read_b128 v[84:87], v0 offset:9216\n"
      "v_mfma_f32_16x16x32_bf16 v[120:123], a[4:7], v[52:55], v[120:123]\n"
      "ds_read_b128 v[96:99], v0 offset:12288\n"
      "v_mul_f32 v232, 0x3eae1a93, v219\n"
      "v_mfma_f32_16x16x32_bf16 v[120:123], a[8:11], v[56:59], v[120:123]\n"
      "s_waitcnt lgkmcnt(7)\n"
      "ds_read_b128 v[104:107], v0 offset:14336\n"
      "v_fma_f32 v135, -v218, v151, v135\n"
      "v_mfma_f32_16x16x32_bf16 v[120:123], a[12:15], v[60:63], v[120:123]\n"
      "v_fma_f32 v133, -v218, v149, v133\n"
      "v_fma_f32 v134, -v218, v150, v134\n"
      "v_mfma_f32_16x16x32_bf16 v[120:123], a[16:19], v[64:67], v[120:123]\n"
      "ds_read_b128 v[92:95], v0 offset:11264\n"
      "v_fma_f32 v132, -v218, v148, v132\n"
      "v_mfma_f32_16x16x32_bf16 v[120:123], a[20:23], v[68:71], v[120:123]\n"
      "s_waitcnt lgkmcnt(5)\n"
      "ds_read_b64 v[216:217], v4 offset:1024\n"
      "v_cvt_pk_bf16_f32 v176, v132, v133\n"
      "v_mfma_f32_16x16x32_bf16 v[120:123], a[24:27], v[72:75], v[120:123]\n"
      "ds_read_b128 v[100:103], v0 offset:13312\n"
      "v_cvt_pk_bf16_f32 v177, v134, v135\n"
      "v_mfma_f32_16x16x32_bf16 v[120:123], a[28:31], v[76:79], v[120:123]\n"
      "s_waitcnt lgkmcnt(5)\n"
      "v_rcp_f32 v210, v210\n"
      "v_mfma_f32_16x16x32_bf16 v[120:123], a[32:35], v[80:83], v[120:123]\n"
      "v_mul_f32 v204, v204, v210\n"
      "v_add_f32 v212, v212, v214\n"
      "v_mfma_f32_16x16x32_bf16 v[188:191], v[172:175], v[176:179], v[152:155]\n"
      "ds_read_b128 v[112:115], v0 offset:16384\n"
      "v_mfma_f32_16x16x32_bf16 v[120:123], a[36:39], v[84:87], v[120:123]\n"
      "ds_read_b128 v[116:119], v0 offset:17408\n"
      "v_mul_f32 v208, v208, v210\n"
      "v_mfma_f32_16x16x32_bf16 v[120:123], a[40:43], v[88:91], v[120:123]\n"
      "s_waitcnt lgkmcnt(4)\n"
      "s_add_u32 s36, s36, 24576\n"
      "v_mul_f32 v207, v207, v210\n"
      "v_mfma_f32_16x16x32_bf16 v[120:123], a[44:47], v[92:95], v[120:123]\n"
      "v_mul_f32 v209, v209, v210\n"
      "v_cvt_pk_bf16_f32 v230, v208, v209\n"
      "v_mfma_f32_16x16x32_bf16 v[120:123], a[48:51], v[96:99], v[120:123]\n"
      "s_waitcnt lgkmcnt(2)\n"
      "buffer_store_dwordx2 v[230:231], v8, s[8:11], s40 offen sc1\n"
      "v_mfma_f32_16x16x32_bf16 v[120:123], a[52:55], v[100:103], v[120:123]\n"
      "v_mul_f32 v206, v206, v210\n"
      "v_add_f32 v218, v212, v216\n"
      "v_mfma_f32_16x16x32_bf16 v[120:123], a[56:59], v[104:107], v[120:123]\n"
      "s_mov_b32 s46, s45\n"
      "v_add_f32 v213, v213, v215\n"
      "v_mfma_f32_16x16x32_bf16 v[120:123], a[60:63], v[108:111], v[120:123]\n"
      "s_waitcnt lgkmcnt(1)\n"
      "v_cvt_pk_bf16_f32 v229, v206, v207\n"
      "v_mfma_f32_16x16x32_bf16 v[120:123], a[64:67], v[112:115], v[120:123]\n"
      "s_waitcnt lgkmcnt(0)\n"
      "v_mul_f32 v205, v205, v210\n"
      "v_cvt_pk_bf16_f32 v228, v204, v205\n"
      "v_mfma_f32_16x16x32_bf16 v[120:123], a[68:71], v[116:119], v[120:123]\n"
      "buffer_store_dwordx2 v[228:229], v7, s[8:11], s40 offen sc1\n"
      "v_mfma_f32_16x16x32_bf16 v[124:127], a[216:219], v[48:51], 0\n"
      "v_add_f32 v213, v213, v217\n"
      "v_fma_f32 v213, -v218, v218, v213\n"
      "v_mfma_f32_16x16x32_bf16 v[124:127], a[220:223], v[52:55], v[124:127]\n"
      "v_max_f32 v213, 0x3727c5ac, v213\n"
      "s_mov_b32 s45, s36\n"
      "v_mfma_f32_16x16x32_bf16 v[124:127], a[224:227], v[56:59], v[124:127]\n"
      "v_mov_b32 v187, v185\n"
      "v_mfma_f32_16x16x32_bf16 v[124:127], a[228:231], v[60:63], v[124:127]\n"
      "v_rsq_f32 v219, v213\n"
      "v_mfma_f32_16x16x32_bf16 v[124:127], a[232:235], v[64:67], v[124:127]\n"
      "s_add_u32 m0, s41, 40960\n"
      "ds_write_b64 v5, v[218:219] offset:0\n"
      "v_mfma_f32_16x16x32_bf16 v[124:127], a[236:239], v[68:71], v[124:127]\n"
      "s_waitcnt vmcnt(5)\n"
      "v_mov_b32 v186, v184\n"
      "v_cmp_ne_u32 vcc, s21, v15\n"
      "v_mfma_f32_16x16x32_bf16 v[124:127], a[240:243], v[72:75], v[124:127]\n"
      "s_min_u32 s45, s45, s6\n"
      "v_mfma_f32_16x16x32_bf16 v[124:127], a[244:247], v[76:79], v[124:127]\n"
      "v_cndmask_b32 v233, v219, 1.0, s[54:55]\n"
      "v_add_u32 v9, 1, v9\n"
      "v_mfma_f32_16x16x32_bf16 v[124:127], a[248:251], v[80:83], v[124:127]\n"
      "v_fma_f32 v123, -v218, v139, v123\n"
      "v_fma_f32 v122, -v218, v138, v122\n"
      "v_mfma_f32_16x16x32_bf16 v[124:127], a[252:255], v[84:87], v[124:127]\n"
      "v_fma_f32 v120, -v218, v136, v120\n"
      "v_fma_f32 v121, -v218, v137, v121\n"
      "v_mfma_f32_16x16x32_bf16 v[124:127], v[16:19], v[88:91], v[124:127]\n"
      "ds_read_b128 v[224:227], v6 offset:16\n"
      "v_mul_f32 v121, v219, v121\n"
      "v_mfma_f32_16x16x32_bf16 v[124:127], v[20:23], v[92:95], v[124:127]\n"
      "v_mul_f32 v120, v219, v120\n"
      "v_mfma_f32_16x16x32_bf16 v[124:127], v[24:27], v[96:99], v[124:127]\n"
      "s_cbranch_vccnz .Lsp_q2_4\n"
      ".Lbk_q2_4:\n"
      "v_mfma_f32_16x16x32_bf16 v[124:127], v[28:31], v[100:103], v[124:127]\n"
      "buffer_load_dword v15, v2, s[8:11], s45 offen sc1\n"
      "v_mfma_f32_16x16x32_bf16 v[124:127], v[32:35], v[104:107], v[124:127]\n"
      "v_mul_f32 v123, v219, v123\n"
      "v_mul_f32 v122, v219, v122\n"
      "v_mfma_f32_16x16x32_bf16 v[124:127], v[36:39], v[108:111], v[124:127]\n"
      "s_waitcnt vmcnt(4) lgkmcnt(1)\n"
      "ds_write_b32 v11, v9\n"
      "v_mfma_f32_16x16x32_bf16 v[124:127], v[40:43], v[112:115], v[124:127]\n"
      "v_mov_b32 v182, v180\n"
      "v_mfma_f32_16x16x32_bf16 v[124:127], v[44:47], v[116:119], v[124:127]\n"
      "v_cvt_pk_bf16_f32 v173, v122, v123\n"
      "v_mfma_f32_16x16x32_bf16 v[128:131], v[48:51], a[72:75], 0\n"
      "buffer_load_dwordx4 v0, s[8:11], s46 offen offset:1024 sc1 lds\n"
      "v_mfma_f32_16x16x32_bf16 v[128:131], v[52:55], a[76:79], v[128:131]\n"
      "ds_read_b128 v[220:223], v6 offset:0\n"
      "v_cvt_pk_bf16_f32 v172, v120, v121\n"
      "v_mfma_f32_16x16x32_bf16 v[128:131], v[56:59], a[80:83], v[128:131]\n"
      "v_fma_f32 v126, -v218, v142, v126\n"
      "v_fma_f32 v124, -v218, v140, v124\n"
      "v_mfma_f32_16x16x32_bf16 v[128:131], v[60:63], a[84:87], v[128:131]\n"
      "s_add_u32 s52, s52, 24576\n"
      "v_fma_f32 v125, -v218, v141, v125\n"
      "v_mul_f32 v124, v233, v124\n"
      "v_mfma_f32_16x16x32_bf16 v[128:131], v[64:67], a[88:91], v[128:131]\n"
      "v_mul_f32 v126, v219, v126\n"
      "v_mul_f32 v125, v233, v125\n"
      "v_mfma_f32_16x16x32_bf16 v[128:131], v[68:71], a[92:95], v[128:131]\n"
      "v_fma_f32 v127, -v218, v143, v127\n"
      "v_cvt_pk_bf16_f32 v175, v124, v125\n"
      "v_mfma_f32_16x16x32_bf16 v[128:131], v[72:75], a[96:99], v[128:131]\n"
      "v_mul_f32 v127, v219, v127\n"
      "v_cvt_pk_bf16_f32 v174, v126, v127\n"
      "v_mfma_f32_16x16x32_bf16 v[128:131], v[76:79], a[100:103], v[128:131]\n"
      "buffer_load_dwordx4 v0, s[8:11], s46 offen offset:2048 sc1 lds\n"
      "v_mfma_f32_16x16x32_bf16 v[192:195], v[172:175], v[176:179], v[156:159]\n"
      "ds_read_b32 v10, v12\n"
      "v_mfma_f32_16x16x32_bf16 v[196:199], v[172:175], v[164:167], v[168:171]\n"
      "v_mov_b32 v183, v181\n"
      "v_mov_b32 v178, v175\n"
      "v_mfma_f32_16x16x32_bf16 v[128:131], v[80:83], a[104:107], v[128:131]\n"
      "buffer_load_dwordx4 v0, s[8:11], s46 offen offset:0 sc1 lds\n"
      "v_mfma_f32_16x16x32_bf16 v[128:131], v[84:87], a[108:111], v[128:131]\n"
      "v_max_f32 v235, v194, v195\n"
      "v_max_f32 v234, v192, v193\n"
      "v_mfma_f32_16x16x32_bf16 v[128:131], v[88:91], a[112:115], v[128:131]\n"
      "v_max3_f32 v234, v188, v189, v234\n"
      "v_max3_f32 v235, v190, v191, v235\n"
      "v_mfma_f32_16x16x32_bf16 v[128:131], v[92:95], a[116:119], v[128:131]\n"
      "v_max_f32 v236, v234, v235\n"
      "v_mov_b32 v237, v236\n"
      "v_mfma_f32_16x16x32_bf16 v[128:131], v[96:99], a[120:123], v[128:131]\n"
      "v_permlane16_swap_b32 v236, v237\n"
      "v_mfma_f32_16x16x32_bf16 v[128:131], v[100:103], a[124:127], v[128:131]\n"
      "v_max_f32 v236, v236, v237\n"
      "v_mov_b32 v237, v236\n"
      "v_mfma_f32_16x16x32_bf16 v[128:131], v[104:107], a[128:131], v[128:131]\n"
      "s_waitcnt lgkmcnt(0)\n"
      "v_permlane32_swap_b32 v236, v237\n"
      "v_cmp_gt_i32 vcc, v9, v10\n"
      "v_mfma_f32_16x16x32_bf16 v[128:131], v[108:111], a[132:135], v[128:131]\n"
      "buffer_load_dwordx4 v0, s[8:11], s46 offen offset:3072 sc1 lds\n"
      "v_mfma_f32_16x16x32_bf16 v[128:131], v[112:115], a[136:139], v[128:131]\n"
      "s_add_u32 m0, s42, 40960\n"
      "v_max_f32 v236, v236, v237\n"
      "v_mul_f32 v236, v236, v232\n"
      "v_mfma_f32_16x16x32_bf16 v[128:131], v[116:119], a[140:143], v[128:131]\n"
      "s_add_u32 s40, s40, 24576\n"
      "v_fma_f32 v234, v234, v232, -v236\n"
      "v_fma_f32 v235, v235, v232, -v236\n"
      "v_mfma_f32_16x16x32_bf16 v[132:135], a[144:147], v[48:51], 0\n"
      "v_exp_f32 v234, v234\n"
      "v_mfma_f32_16x16x32_bf16 v[132:135], a[148:151], v[52:55], v[132:135]\n"
      "s_cbranch_vccnz .Lsp_qy1_5\n"
      ".Lbk_qy1_5:\n"
      "ds_read_b128 v[48:51], v0 offset:20480\n"
      "v_mfma_f32_16x16x32_bf16 v[132:135], a[152:155], v[56:59], v[132:135]\n"
      "v_exp_f32 v235, v235\n"
      "v_mfma_f32_16x16x32_bf16 v[132:135], a[156:159], v[60:63], v[132:135]\n"
      "ds_read_b128 v[56:59], v0 offset:22528\n"
      "v_fma_f32 v130, -v224, v147, v130\n"
      "v_mfma_f32_16x16x32_bf16 v[132:135], a[160:163], v[64:67], v[132:135]\n"
      "ds_read_b64 v[216:217], v4 offset:21504\n"
      "v_fma_f32 v129, -v222, v146, v129\n"
      "v_mfma_f32_16x16x32_bf16 v[132:135], a[164:167], v[68:71], v[132:135]\n"
      "buffer_load_dwordx4 v1, s[8:11], s46 offen offset:0 sc1 lds\n"
      "v_mfma_f32_16x16x32_bf16 v[132:135], a[168:171], v[72:75], v[132:135]\n"
      "v_cvt_pk_bf16_f32 v234, v234, v234\n"
      "v_fma_f32 v131, -v226, v144, v131\n"
      "v_mfma_f32_16x16x32_bf16 v[132:135], a[172:175], v[76:79], v[132:135]\n"
      "ds_read_b128 v[60:63], v0 offset:23552\n"
      "v_and_b32 v200, v160, v234\n"
      "v_mfma_f32_16x16x32_bf16 v[132:135], a[176:179], v[80:83], v[132:135]\n"
      "v_cvt_pk_bf16_f32 v235, v235, v235\n"
      "v_mul_f32 v129, v223, v129\n"
      "v_mfma_f32_16x16x32_bf16 v[132:135], a[180:183], v[84:87], v[132:135]\n"
      "ds_read_b128 v[52:55], v0 offset:21504\n"
      "v_mul_f32 v131, v227, v131\n"
      "v_mfma_f32_16x16x32_bf16 v[132:135], a[184:187], v[88:91], v[132:135]\n"
      "ds_read_b128 v[88:91], v0 offset:30720\n"
      "v_mul_f32 v130, v225, v130\n"
      "v_cvt_pk_bf16_f32 v185, v198, v199\n"
      "v_mfma_f32_16x16x32_bf16 v[132:135], a[188:191], v[92:95], v[132:135]\n"
      "v_cvt_pk_bf16_f32 v184, v196, v197\n"
      "v_and_b32 v201, v161, v235\n"
      "v_fma_f32 v128, -v220, v145, v128\n"
      "v_mfma_f32_16x16x32_bf16 v[132:135], a[192:195], v[96:99], v[132:135]\n"
      "ds_read_b128 v[68:71], v0 offset:25600\n"
      "v_and_b32 v203, v163, v235\n"
      "v_mfma_f32_16x16x32_bf16 v[132:135], a[196:199], v[100:103], v[132:135]\n"
      "v_mul_f32 v128, v221, v128\n"
      "v_and_b32 v202, v162, v234\n"
      "v_mfma_f32_16x16x32_bf16 v[132:135], a[200:203], v[104:107], v[132:135]\n"
      "v_cvt_pk_bf16_f32 v180, v128, v129\n"
      "v_cvt_pk_bf16_f32 v181, v130, v131\n"
      "v_mfma_f32_16x16x32_bf16 v[208:211], v[184:187], v[200:203], 0\n"
      "ds_read_b128 v[64:67], v0 offset:24576\n"
      "ds_read_b128 v[72:75], v0 offset:26624\n"
      "v_mfma_f32_16x16x32_bf16 v[204:207], v[180:183], v[200:203], 0\n"
      "s_waitcnt vmcnt(6) lgkmcnt(4)\n"
      "buffer_store_dword v13, v3, s[8:11], s52 offen sc1\n"
      "v_mfma_f32_16x16x32_bf16 v[132:135], a[204:207], v[108:111], v[132:135]\n"
      "ds_read2st64_b64 v[212:215], v4 offset0:40 offset1:41\n"
      "v_mfma_f32_16x16x32_bf16 v[132:135], a[208:211], v[112:115], v[132:135]\n"
      "v_rcp_f32 v210, v210\n"
      "v_mfma_f32_16x16x32_bf16 v[132:135], a[212:215], v[116:119], v[132:135]\n"
      "ds_read_b128 v[76:79], v0 offset:27648\n"
      "v_mul_f32 v209, v209, v210\n"
      "v_mfma_f32_16x16x32_bf16 v[120:123], a[0:3], v[48:51], 0\n"
      "ds_read_b128 v[80:83], v0 offset:28672\n"
      "ds_read_b128 v[100:103], v0 offset:33792\n"
      "v_mfma_f32_16x16x32_bf16 v[120:123], a[4:7], v[52:55], v[120:123]\n"
      "ds_read_b128 v[92:95], v0 offset:31744\n"
      "v_mul_f32 v208, v208, v210\n"
      "v_mfma_f32_16x16x32_bf16 v[120:123], a[8:11], v[56:59], v[120:123]\n"
      "ds_read_b128 v[84:87], v0 offset:29696\n"
      "v_fma_f32 v134, -v218, v150, v134\n"
      "v_mfma_f32_16x16x32_bf16 v[120:123], a[12:15], v[60:63], v[120:123]\n"
      "s_waitcnt lgkmcnt(6)\n"
      "ds_read_b128 v[104:107], v0 offset:34816\n"
      "v_fma_f32 v132, -v218, v148, v132\n"
      "v_mfma_f32_16x16x32_bf16 v[120:123], a[16:19], v[64:67], v[120:123]\n"
      "s_mov_b32 s50, s49\n"
      "ds_read_b128 v[96:99], v0 offset:32768\n"
      "v_mfma_f32_16x16x32_bf16 v[120:123], a[20:23], v[68:71], v[120:123]\n"
      "ds_read_b128 v[108:111], v0 offset:35840\n"
      "v_fma_f32 v133, -v218, v149, v133\n"
      "v_mfma_f32_16x16x32_bf16 v[120:123], a[24:27], v[72:75], v[120:123]\n"
      "s_waitcnt lgkmcnt(7)\n"
      "v_fma_f32 v135, -v218, v151, v135\n"
      "v_cvt_pk_bf16_f32 v177, v134, v135\n"
      "v_mfma_f32_16x16x32_bf16 v[120:123], a[28:31], v[76:79], v[120:123]\n"
      "s_waitcnt lgkmcnt(5)\n"
      "ds_read_b128 v[112:115], v0 offset:36864\n"
      "v_cvt_pk_bf16_f32 v176, v132, v133\n"
      "v_mfma_f32_16x16x32_bf16 v[120:123], a[32:35], v[80:83], v[120:123]\n"
      "v_mov_b32 v187, v185\n"
      "v_mul_f32 v232, 0x3eae1a93, v219\n"
      "v_mfma_f32_16x16x32_bf16 v[188:191], v[172:175], v[176:179], v[152:155]\n"
      "s_waitcnt lgkmcnt(4)\n"
      "ds_read_b128 v[116:119], v0 offset:37888\n"
      "v_mul_f32 v207, v207, v210\n"
      "v_mfma_f32_16x16x32_bf16 v[120:123], a[36:39], v[84:87], v[120:123]\n"
      "v_mul_f32 v204, v204, v210\n"
      "v_mfma_f32_16x16x32_bf16 v[120:123], a[40:43], v[88:91], v[120:123]\n"
      "v_cvt_pk_bf16_f32 v230, v208, v209\n"
      "v_add_f32 v213, v213, v215\n"
      "v_mfma_f32_16x16x32_bf16 v[120:123], a[44:47], v[92:95], v[120:123]\n"
      "s_waitcnt lgkmcnt(2)\n"
      "v_mul_f32 v205, v205, v210\n"
      "v_mfma_f32_16x16x32_bf16 v[120:123], a[48:51], v[96:99], v[120:123]\n"
      "buffer_store_dwordx2 v[230:231], v8, s[8:11], s40 offen sc1\n"
      "v_mfma_f32_16x16x32_bf16 v[120:123], a[52:55], v[100:103], v[120:123]\n"
      "v_add_f32 v212, v212, v214\n"
      "v_mfma_f32_16x16x32_bf16 v[120:123], a[56:59], v[104:107], v[120:123]\n"
      "v_cvt_pk_bf16_f32 v228, v204, v205\n"
      "v_mul_f32 v206, v206, v210\n"
      "v_mfma_f32_16x16x32_bf16 v[120:123], a[60:63], v[108:111], v[120:123]\n"
      "s_waitcnt vmcnt(7) lgkmcnt(0)\n"
      "v_cvt_pk_bf16_f32 v229, v206, v207\n"
      "v_cmp_ne_u32 vcc, s21, v15\n"
      "v_mfma_f32_16x16x32_bf16 v[120:123], a[64:67], v[112:115], v[120:123]\n"
      "s_add_u32 m0, s41, 0\n"
      "v_mov_b32 v182, v180\n"
      "v_mfma_f32_16x16x32_bf16 v[120:123], a[68:71], v[116:119], v[120:123]\n"
      "buffer_store_dwordx2 v[228:229], v7, s[8:11], s40 offen sc1\n"
      "v_mfma_f32_16x16x32_bf16 v[124:127], a[216:219], v[48:51], 0\n"
      "v_add_f32 v218, v212, v216\n"
      "v_add_f32_e64 v213, v213, v217\n"
      "v_mfma_f32_16x16x32_bf16 v[124:127], a[220:223], v[52:55], v[124:127]\n"
      "s_add_u32 s36, s36, 24576\n"
      "v_fma_f32 v213, -v218, v218, v213\n"
      "v_mfma_f32_16x16x32_bf16 v[124:127], a[224:227], v[56:59], v[124:127]\n"
      "s_mov_b32 s46, s45\n"
      "s_cbranch_vccnz .Lsp_q0_6\n"
      ".Lbk_q0_6:\n"
      "v_mfma_f32_16x16x32_bf16 v[124:127], a[228:231], v[60:63], v[124:127]\n"
      "buffer_load_dwordx4 v0, s[8:11], s46 offen offset:1024 sc1 lds\n"
      "v_mfma_f32_16x16x32_bf16 v[124:127], a[232:235], v[64:67], v[124:127]\n"
      "v_max_f32 v213, 0x3727c5ac, v213\n"
      "v_fma_f32 v120, -v218, v136, v120\n"
      "v_mfma_f32_16x16x32_bf16 v[124:127], a[236:239], v[68:71], v[124:127]\n"
      "v_mov_b32 v186, v184\n"
      "v_mfma_f32_16x16x32_bf16 v[124:127], a[240:243], v[72:75], v[124:127]\n"
      "v_add_u32 v9, 1, v9\n"
      "v_mfma_f32_16x16x32_bf16 v[124:127], a[244:247], v[76:79], v[124:127]\n"
      "s_add_u32 s40, s40, 24576\n"
      "v_fma_f32 v123, -v218, v139, v123\n"
      "v_mfma_f32_16x16x32_bf16 v[124:127], a[248:251], v[80:83], v[124:127]\n"
      "v_rsq_f32 v219, v213\n"
      "v_mfma_f32_16x16x32_bf16 v[124:127], a[252:255], v[84:87], v[124:127]\n"
      "v_mfma_f32_16x16x32_bf16 v[124:127], v[16:19], v[88:91], v[124:127]\n"
      "v_fma_f32 v121, -v218, v137, v121\n"
      "v_fma_f32 v122, -v218, v138, v122\n"
      "v_mfma_f32_16x16x32_bf16 v[124:127], v[20:23], v[92:95], v[124:127]\n"
      "buffer_load_dwordx4 v0, s[8:11], s46 offen offset:0 sc1 lds\n"
      "v_mfma_f32_16x16x32_bf16 v[124:127], v[24:27], v[96:99], v[124:127]\n"
      "v_mul_f32 v120, v219, v120\n"
      "v_mul_f32 v122, v219, v122\n"
      "v_mfma_f32_16x16x32_bf16 v[124:127], v[28:31], v[100:103], v[124:127]\n"
      "ds_write_b64 v5, v[218:219] offset:512\n"
      "v_mfma_f32_16x16x32_bf16 v[124:127], v[32:35], v[104:107], v[124:127]\n"
      "v_mul_f32 v121, v219, v121\n"
      "v_mul_f32 v123, v219, v123\n"
      "v_mfma_f32_16x16x32_bf16 v[124:127], v[36:39], v[108:111], v[124:127]\n"
      "buffer_load_dwordx4 v0, s[8:11], s46 offen offset:2048 sc1 lds\n"
      "v_mfma_f32_16x16x32_bf16 v[124:127], v[40:43], v[112:115], v[124:127]\n"
      "s_mov_b32 s45, s36\n"
      "ds_read_b128 v[224:227], v6 offset:528\n"
      "v_mfma_f32_16x16x32_bf16 v[124:127], v[44:47], v[116:119], v[124:127]\n"
      "s_min_u32 s45, s45, s6\n"
      "v_cndmask_b32 v233, v219, 1.0, s[54:55]\n"
      "v_mfma_f32_16x16x32_bf16 v[128:131], v[48:51], a[72:75], 0\n"
      "s_add_u32 s52, s52, 24576\n"
      "v_cvt_pk_bf16_f32 v172, v120, v121\n"
      "v_cvt_pk_bf16_f32 v173, v122, v123\n"
      "v_mfma_f32_16x16x32_bf16 v[128:131], v[52:55], a[76:79], v[128:131]\n"
      "buffer_load_dwordx4 v0, s[8:11], s46 offen offset:3072 sc1 lds\n"
      "v_mfma_f32_16x16x32_bf16 v[128:131], v[56:59], a[80:83], v[128:131]\n"
      "v_fma_f32 v126, -v218, v142, v126\n"
      "v_fma_f32 v124, -v218, v140, v124\n"
      "v_mul_f32 v126, v219, v126\n"
      "v_mfma_f32_16x16x32_bf16 v[128:131], v[60:63], a[84:87], v[128:131]\n"
      "v_fma_f32 v125, -v218, v141, v125\n"
      "v_mul_f32 v125, v233, v125\n"
      "v_mfma_f32_16x16x32_bf16 v[128:131], v[64:67], a[88:91], v[128:131]\n"
      "s_waitcnt vmcnt(7) lgkmcnt(1)\n"
      "ds_write_b32 v11, v9\n"
      "v_mfma_f32_16x16x32_bf16 v[128:131], v[68:71], a[92:95], v[128:131]\n"
      "v_fma_f32 v127, -v218, v143, v127\n"
      "v_mul_f32 v124, v233, v124\n"
      "v_mul_f32 v127, v219, v127\n"
      "v_mfma_f32_16x16x32_bf16 v[128:131], v[72:75], a[96:99], v[128:131]\n"
      "v_cvt_pk_bf16_f32 v174, v126, v127\n"
      "v_cvt_pk_bf16_f32 v175, v124, v125\n"
      "v_mfma_f32_16x16x32_bf16 v[128:131], v[76:79], a[100:103], v[128:131]\n"
      "buffer_load_dword v15, v2, s[8:11], s45 offen sc1\n"
      "v_mfma_f32_16x16x32_bf16 v[192:195], v[172:175], v[176:179], v[156:159]\n"
      "v_mov_b32 v183, v181\n"
      "v_mfma_f32_16x16x32_bf16 v[196:199], v[172:175], v[164:167], v[168:171]\n"
      "ds_read_b128 v[220:223], v6 offset:512\n"
      "s_add_u32 m0, s42, 0\n"
      "v_mfma_f32_16x16x32_bf16 v[128:131], v[80:83], a[104:107], v[128:131]\n"
      "buffer_load_dwordx4 v1, s[8:11], s46 offen offset:0 sc1 lds\n"
      "v_mfma_f32_16x16x32_bf16 v[128:131], v[84:87], a[108:111], v[128:131]\n"
      "v_max_f32 v235, v194, v195\n"
      "v_max3_f32 v235, v190, v191, v235\n"
      "v_mfma_f32_16x16x32_bf16 v[128:131], v[88:91], a[112:115], v[128:131]\n"
      "v_max_f32 v234, v192, v193\n"
      "v_max3_f32 v234, v188, v189, v234\n"
      "v_mfma_f32_16x16x32_bf16 v[128:131], v[92:95], a[116:119], v[128:131]\n"
      "ds_read_b32 v10, v12\n"
      "v_max_f32 v236, v234, v235\n"
      "v_mfma_f32_16x16x32_bf16 v[128:131], v[96:99], a[120:123], v[128:131]\n"
      "v_mov_b32 v237, v236\n"
      "v_cvt_pk_bf16_f32 v185, v198, v199\n"
      "v_mfma_f32_16x16x32_bf16 v[128:131], v[100:103], a[124:127], v[128:131]\n"
      "v_permlane16_swap_b32 v236, v237\n"
      "v_mfma_f32_16x16x32_bf16 v[128:131], v[104:107], a[128:131], v[128:131]\n"
      "v_max_f32 v236, v236, v237\n"
      "v_mov_b32 v237, v236\n"
      "v_mfma_f32_16x16x32_bf16 v[128:131], v[108:111], a[132:135], v[128:131]\n"
      "v_permlane32_swap_b32 v236, v237\n"
      "v_mfma_f32_16x16x32_bf16 v[128:131], v[112:115], a[136:139], v[128:131]\n"
      "s_waitcnt lgkmcnt(0)\n"
      "v_cmp_gt_i32 vcc, v9, v10\n"
      "v_max_f32 v236, v236, v237\n"
      "v_mfma_f32_16x16x32_bf16 v[128:131], v[116:119], a[140:143], v[128:131]\n"
      "v_mul_f32_e64 v236, v236, v232\n"
      "v_fma_f32 v235, v235, v232, -v236\n"
      "v_mfma_f32_16x16x32_bf16 v[132:135], a[144:147], v[48:51], 0\n"
      "v_exp_f32_e64 v235, v235\n"
      "v_mfma_f32_16x16x32_bf16 v[132:135], a[148:151], v[52:55], v[132:135]\n"
      "s_cbranch_vccnz .Lsp_qy2_7\n"
      ".Lbk_qy2_7:\n"
      "v_cvt_pk_bf16_f32 v235, v235, v235\n"
      "v_mfma_f32_16x16x32_bf16 v[132:135], a[152:155], v[56:59], v[132:135]\n"
      "v_fma_f32 v130, -v224, v147, v130\n"
      "v_mul_f32 v130, v225, v130\n"
      "v_mfma_f32_16x16x32_bf16 v[132:135], a[156:159], v[60:63], v[132:135]\n"
      "v_fma_f32 v129, -v222, v146, v129\n"
      "v_cvt_pk_bf16_f32 v184, v196, v197\n"
      "v_mfma_f32_16x16x32_bf16 v[132:135], a[160:163], v[64:67], v[132:135]\n"
      "v_fma_f32 v234, v234, v232, -v236\n"
      "v_fma_f32 v131, -v226, v144, v131\n"
      "v_mfma_f32_16x16x32_bf16 v[132:135], a[164:167], v[68:71], v[132:135]\n"
      "ds_read_b128 v[52:55], v0 offset:41984\n"
      "ds_read_b128 v[64:67], v0 offset:45056\n"
      "v_mfma_f32_16x16x32_bf16 v[132:135], a[168:171], v[72:75], v[132:135]\n"
      "ds_read_b128 v[56:59], v0 offset:43008\n"
      "v_mul_f32 v129, v223, v129\n"
      "v_mfma_f32_16x16x32_bf16 v[132:135], a[172:175], v[76:79], v[132:135]\n"
      "v_exp_f32 v234, v234\n"
      "v_mfma_f32_16x16x32_bf16 v[132:135], a[176:179], v[80:83], v[132:135]\n"
      "v_fma_f32 v128, -v220, v145, v128\n"
      "v_and_b32 v203, v163, v235\n"
      "v_mfma_f32_16x16x32_bf16 v[132:135], a[180:183], v[84:87], v[132:135]\n"
      "v_cvt_pk_bf16_f32 v234, v234, v234\n"
      "v_and_b32 v201, v161, v235\n"
      "v_mfma_f32_16x16x32_bf16 v[132:135], a[184:187], v[88:91], v[132:135]\n"
      "ds_read_b128 v[48:51], v0 offset:40960\n"
      "v_and_b32 v202, v162, v234\n"
      "v_mfma_f32_16x16x32_bf16 v[132:135], a[188:191], v[92:95], v[132:135]\n"
      "ds_read_b128 v[60:63], v0 offset:44032\n"
      "v_mul_f32 v128, v221, v128\n"
      "v_mfma_f32_16x16x32_bf16 v[132:135], a[192:195], v[96:99], v[132:135]\n"
      "ds_read_b128 v[76:79], v0 offset:48128\n"
      "v_cvt_pk_bf16_f32 v180, v128, v129\n"
      "v_mfma_f32_16x16x32_bf16 v[132:135], a[196:199], v[100:103], v[132:135]\n"
      "ds_read_b128 v[100:103], v0 offset:54272\n"
      "v_and_b32 v200, v160, v234\n"
      "v_mfma_f32_16x16x32_bf16 v[132:135], a[200:203], v[104:107], v[132:135]\n"
      "v_mul_f32 v131, v227, v131\n"
      "v_cvt_pk_bf16_f32 v181, v130, v131\n"
      "v_mfma_f32_16x16x32_bf16 v[208:211], v[184:187], v[200:203], 0\n"
      "s_waitcnt vmcnt(6) lgkmcnt(4)\n"
      "buffer_store_dword v13, v3, s[8:11], s52 offen sc1\n"
      "v_mfma_f32_16x16x32_bf16 v[204:207], v[180:183], v[200:203], 0\n"
      "ds_read_b128 v[84:87], v0 offset:50176\n"
      "ds_read_b128 v[72:75], v0 offset:47104\n"
      "v_mfma_f32_16x16x32_bf16 v[132:135], a[204:207], v[108:111], v[132:135]\n"
      "ds_read_b128 v[68:71], v0 offset:46080\n"
      "v_mov_b32 v178, v175\n"
      "v_mfma_f32_16x16x32_bf16 v[132:135], a[208:211], v[112:115], v[132:135]\n"
      "v_rcp_f32 v210, v210\n"
      "v_mfma_f32_16x16x32_bf16 v[132:135], a[212:215], v[116:119], v[132:135]\n"
      "s_waitcnt lgkmcnt(4)\n"
      "ds_read_b128 v[92:95], v0 offset:52224\n"
      "v_mul_f32 v205, v205, v210\n"
      "v_mfma_f32_16x16x32_bf16 v[120:123], a[0:3], v[48:51], 0\n"
      "s_add_u32 s36, s36, 24576\n"
      "ds_read_b128 v[80:83], v0 offset:49152\n"
      "v_mov_b32 v182, v180\n"
      "v_mfma_f32_16x16x32_bf16 v[120:123], a[4:7], v[52:55], v[120:123]\n"
      "ds_read_b128 v[96:99], v0 offset:53248\n"
      "v_mfma_f32_16x16x32_bf16 v[120:123], a[8:11], v[56:59], v[120:123]\n"
      "ds_read_b128 v[88:91], v0 offset:51200\n"
      "v_fma_f32 v132, -v218, v148, v132\n"
      "v_mfma_f32_16x16x32_bf16 v[120:123], a[12:15], v[60:63], v[120:123]\n"
      "s_cmp_eq_u32 s36, s37\n"
      "v_fma_f32 v133, -v218, v149, v133\n"
      "v_cvt_pk_bf16_f32 v176, v132, v133\n"
      "v_mfma_f32_16x16x32_bf16 v[120:123], a[16:19], v[64:67], v[120:123]\n"
      "s_waitcnt lgkmcnt(4)\n"
      "ds_read2st64_b64 v[212:215], v4 offset0:80 offset1:81\n"
      "s_cselect_b32 s47, s38, 0\n"
      "v_mfma_f32_16x16x32_bf16 v[120:123], a[20:23], v[68:71], v[120:123]\n"
      "s_cselect_b32 s37, s48, s37\n"
      "ds_read_b128 v[112:115], v0 offset:57344\n"
      "v_fma_f32 v135, -v218, v151, v135\n"
      "v_mfma_f32_16x16x32_bf16 v[120:123], a[24:27], v[72:75], v[120:123]\n"
      "s_cselect_b32 s49, s39, s53\n"
      "v_fma_f32 v134, -v218, v150, v134\n"
      "v_mov_b32 v183, v181\n"
      "v_mfma_f32_16x16x32_bf16 v[120:123], a[28:31], v[76:79], v[120:123]\n"
      "s_waitcnt lgkmcnt(4)\n"
      "ds_read_b128 v[104:107], v0 offset:55296\n"
      "v_cvt_pk_bf16_f32 v177, v134, v135\n"
      "v_mfma_f32_16x16x32_bf16 v[120:123], a[32:35], v[80:83], v[120:123]\n"
      "ds_read_b128 v[108:111], v0 offset:56320\n"
      "v_mul_f32 v206, v206, v210\n"
      "v_mfma_f32_16x16x32_bf16 v[188:191], v[172:175], v[176:179], v[152:155]\n"
      "ds_read_b64 v[216:217], v4 offset:41984\n"
      "v_mul_f32 v207, v207, v210\n"
      "v_mfma_f32_16x16x32_bf16 v[120:123], a[36:39], v[84:87], v[120:123]\n"
      "s_waitcnt lgkmcnt(5)\n"
      "s_add_u32 m0, s41, 20480\n"
      "ds_read_b128 v[116:119], v0 offset:58368\n"
      "v_mfma_f32_16x16x32_bf16 v[120:123], a[40:43], v[88:91], v[120:123]\n"
      "s_mov_b32 s46, s45\n"
      "s_add_u32 s45, s36, s47\n"
      "v_mul_f32 v209, v209, v210\n"
      "v_mfma_f32_16x16x32_bf16 v[120:123], a[44:47], v[92:95], v[120:123]\n"
      "v_cvt_pk_bf16_f32 v229, v206, v207\n"
      "v_mfma_f32_16x16x32_bf16 v[120:123], a[48:51], v[96:99], v[120:123]\n"
      "s_waitcnt lgkmcnt(4)\n"
      "v_add_f32 v213, v213, v215\n"
      "v_mfma_f32_16x16x32_bf16 v[120:123], a[52:55], v[100:103], v[120:123]\n"
      "s_waitcnt lgkmcnt(3)\n"
      "v_mul_f32 v232, 0x3eae1a93, v219\n"
      "v_mul_f32 v204, v204, v210\n"
      "v_mfma_f32_16x16x32_bf16 v[120:123], a[56:59], v[104:107], v[120:123]\n"
      "s_waitcnt lgkmcnt(2)\n"
      "v_mul_f32 v208, v208, v210\n"
      "v_cvt_pk_bf16_f32 v230, v208, v209\n"
      "v_mfma_f32_16x16x32_bf16 v[120:123], a[60:63], v[108:111], v[120:123]\n"
      "v_mfma_f32_16x16x32_bf16 v[120:123], a[64:67], v[112:115], v[120:123]\n"
      "s_waitcnt lgkmcnt(0)\n"
      "buffer_store_dwordx2 v[230:231], v8, s[8:11], s40 offen sc1\n"
      "v_mfma_f32_16x16x32_bf16 v[120:123], a[68:71], v[116:119], v[120:123]\n"
      "v_cvt_pk_bf16_f32 v228, v204, v205\n"
      "v_mfma_f32_16x16x32_bf16 v[124:127], a[216:219], v[48:51], 0\n"
      "v_add_f32 v212, v212, v214\n"
      "v_add_f32 v213, v213, v217\n"
      "v_mfma_f32_16x16x32_bf16 v[124:127], a[220:223], v[52:55], v[124:127]\n"
      "v_add_f32 v218, v212, v216\n"
      "v_fma_f32 v213, -v218, v218, v213\n"
      "v_mfma_f32_16x16x32_bf16 v[124:127], a[224:227], v[56:59], v[124:127]\n"
      "buffer_store_dwordx2 v[228:229], v7, s[8:11], s40 offen sc1\n"
      "v_mfma_f32_16x16x32_bf16 v[124:127], a[228:231], v[60:63], v[124:127]\n"
      "s_add_u32 s52, s52, 24576\n"
      "v_fma_f32 v123, -v218, v139, v123\n"
      "v_add_u32 v9, 1, v9\n"
      "v_mfma_f32_16x16x32_bf16 v[124:127], a[232:235], v[64:67], v[124:127]\n"
      "v_fma_f32 v122, -v218, v138, v122\n"
      "v_mfma_f32_16x16x32_bf16 v[124:127], a[236:239], v[68:71], v[124:127]\n"
      "s_waitcnt vmcnt(4)\n"
      "v_max_f32 v213, 0x3727c5ac, v213\n"
      "v_cmp_ne_u32 vcc, s21, v15\n"
      "v_mfma_f32_16x16x32_bf16 v[124:127], a[240:243], v[72:75], v[124:127]\n"
      "v_rsq_f32 v219, v213\n"
      "v_mfma_f32_16x16x32_bf16 v[124:127], a[244:247], v[76:79], v[124:127]\n"
      "v_mov_b32 v186, v184\n"
      "v_fma_f32 v120, -v218, v136, v120\n"
      "v_mfma_f32_16x16x32_bf16 v[124:127], a[248:251], v[80:83], v[124:127]\n"
      "s_min_u32 s45, s45, s6\n"
      "ds_write_b64 v5, v[218:219] offset:1024\n"
      "v_mfma_f32_16x16x32_bf16 v[124:127], a[252:255], v[84:87], v[124:127]\n"
      "s_cbranch_vccnz .Lsp_q1_8\n"
      ".Lbk_q1_8:\n"
      "v_mfma_f32_16x16x32_bf16 v[124:127], v[16:19], v[88:91], v[124:127]\n"
      "buffer_load_dwordx4 v0, s[8:11], s46 offen offset:1024 sc1 lds\n"
      "v_mfma_f32_16x16x32_bf16 v[124:127], v[20:23], v[92:95], v[124:127]\n"
      "v_cndmask_b32 v233, v219, 1.0, s[54:55]\n"
      "v_mul_f32 v123, v219, v123\n"
      "v_mfma_f32_16x16x32_bf16 v[124:127], v[24:27], v[96:99], v[124:127]\n"
      "ds_read_b128 v[220:223], v6 offset:1024\n"
      "v_mul_f32 v120, v219, v120\n"
      "v_mfma_f32_16x16x32_bf16 v[124:127], v[28:31], v[100:103], v[124:127]\n"
      "v_mul_f32 v122, v219, v122\n"
      "v_fma_f32 v121, -v218, v137, v121\n"
      "v_mfma_f32_16x16x32_bf16 v[124:127], v[32:35], v[104:107], v[124:127]\n"
      "s_waitcnt vmcnt(4)\n"
      "ds_write_b32 v11, v9\n"
      "v_mfma_f32_16x16x32_bf16 v[124:127], v[36:39], v[108:111], v[124:127]\n"
      "buffer_load_dwordx4 v0, s[8:11], s46 offen offset:0 sc1 lds\n"
      "v_mfma_f32_16x16x32_bf16 v[124:127], v[40:43], v[112:115], v[124:127]\n"
      "v_mfma_f32_16x16x32_bf16 v[124:127], v[44:47], v[116:119], v[124:127]\n"
      "v_cvt_pk_bf16_f32 v173, v122, v123\n"
      "v_mfma_f32_16x16x32_bf16 v[128:131], v[48:51], a[72:75], 0\n"
      "v_mul_f32 v121, v219, v121\n"
      "v_cvt_pk_bf16_f32 v172, v120, v121\n"
      "v_mfma_f32_16x16x32_bf16 v[128:131], v[52:55], a[76:79], v[128:131]\n"
      "buffer_load_dwordx4 v0, s[8:11], s46 offen offset:3072 sc1 lds\n"
      "v_mfma_f32_16x16x32_bf16 v[128:131], v[56:59], a[80:83], v[128:131]\n"
      "v_fma_f32 v125, -v218, v141, v125\n"
      "v_fma_f32 v124, -v218, v140, v124\n"
      "v_mfma_f32_16x16x32_bf16 v[128:131], v[60:63], a[84:87], v[128:131]\n"
      "v_fma_f32 v127, -v218, v143, v127\n"
      "v_mul_f32 v125, v233, v125\n"
      "v_mfma_f32_16x16x32_bf16 v[128:131], v[64:67], a[88:91], v[128:131]\n"
      "v_mul_f32 v124, v233, v124\n"
      "v_mul_f32 v127, v219, v127\n"
      "v_mfma_f32_16x16x32_bf16 v[128:131], v[68:71], a[92:95], v[128:131]\n"
      "v_fma_f32 v126, -v218, v142, v126\n"
      "v_mul_f32 v126, v219, v126\n"
      "v_mfma_f32_16x16x32_bf16 v[128:131], v[72:75], a[96:99], v[128:131]\n"
      "v_cvt_pk_bf16_f32 v175, v124, v125\n"
      "v_cvt_pk_bf16_f32 v174, v126, v127\n"
      "v_mfma_f32_16x16x32_bf16 v[128:131], v[76:79], a[100:103], v[128:131]\n"
      "buffer_load_dword v15, v2, s[8:11], s45 offen sc1\n"
      "v_mfma_f32_16x16x32_bf16 v[192:195], v[172:175], v[176:179], v[156:159]\n"
      "ds_read_b128 v[224:227], v6 offset:1040\n"
      "v_mov_b32 v187, v185\n"
      "v_mfma_f32_16x16x32_bf16 v[196:199], v[172:175], v[164:167], v[168:171]\n"
      "ds_read_b32 v10, v12\n"
      "v_mfma_f32_16x16x32_bf16 v[128:131], v[80:83], a[104:107], v[128:131]\n"
      "buffer_load_dwordx4 v0, s[8:11], s46 offen offset:2048 sc1 lds\n"
      "v_mfma_f32_16x16x32_bf16 v[128:131], v[84:87], a[108:111], v[128:131]\n"
      "v_med3_f32 v234, v192, v193, s50\n"
      "v_med3_f32 v235, v194, v195, s50\n"
      "v_mfma_f32_16x16x32_bf16 v[128:131], v[88:91], a[112:115], v[128:131]\n"
      "v_cvt_pk_bf16_f32 v185, v198, v199\n"
      "v_max3_f32 v234, v188, v189, v234\n"
      "v_mfma_f32_16x16x32_bf16 v[128:131], v[92:95], a[116:119], v[128:131]\n"
      "v_max3_f32 v235, v190, v191, v235\n"
      "v_max_f32 v236, v234, v235\n"
      "v_mfma_f32_16x16x32_bf16 v[128:131], v[96:99], a[120:123], v[128:131]\n"
      "s_waitcnt lgkmcnt(0)\n"
      "v_mov_b32 v237, v236\n"
      "v_cmp_gt_i32 vcc, v9, v10\n"
      "v_mfma_f32_16x16x32_bf16 v[128:131], v[100:103], a[124:127], v[128:131]\n"
      "s_add_u32 m0, s42, 20480\n"
      "v_permlane16_swap_b32 v236, v237\n"
      "v_mfma_f32_16x16x32_bf16 v[128:131], v[104:107], a[128:131], v[128:131]\n"
      "v_max_f32 v236, v236, v237\n"
      "v_mov_b32 v237, v236\n"
      "v_mfma_f32_16x16x32_bf16 v[128:131], v[108:111], a[132:135], v[128:131]\n"
      "buffer_load_dwordx4 v1, s[8:11], s46 offen offset:0 sc1 lds\n"
      "v_mfma_f32_16x16x32_bf16 v[128:131], v[112:115], a[136:139], v[128:131]\n"
      ".Lq_p1:\n"
      "s_cbranch_execz .Lq_pr\n"
      "v_permlane32_swap_b32 v236, v237\n"
      "v_mfma_f32_16x16x32_bf16 v[128:131], v[116:119], a[140:143], v[128:131]\n"
      "v_max_f32 v236, v236, v237\n"
      "v_mul_f32_e64 v236, v236, v232\n"
      "v_mfma_f32_16x16x32_bf16 v[132:135], a[144:147], v[48:51], 0\n"
      "s_cbranch_vccnz .Lsp_qy0_9\n"
      ".Lbk_qy0_9:\n"
      "v_fma_f32 v234, v234, v232, -v236\n"
      "v_mfma_f32_16x16x32_bf16 v[132:135], a[148:151], v[52:55], v[132:135]\n"
      "ds_read_b128 v[52:55], v0 offset:1024\n"
      "ds_read_b128 v[48:51], v0 offset:0\n"
      "v_mfma_f32_16x16x32_bf16 v[132:135], a[152:155], v[56:59], v[132:135]\n"
      "v_fma_f32 v131, -v226, v144, v131\n"
      "v_fma_f32 v235, v235, v232, -v236\n"
      "v_mfma_f32_16x16x32_bf16 v[132:135], a[156:159], v[60:63], v[132:135]\n"
      "ds_read_b128 v[56:59], v0 offset:2048\n"
      "v_fma_f32 v128, -v220, v145, v128\n"
      "v_mfma_f32_16x16x32_bf16 v[132:135], a[160:163], v[64:67], v[132:135]\n"
      "v_exp_f32 v234, v234\n"
      "v_mfma_f32_16x16x32_bf16 v[132:135], a[164:167], v[68:71], v[132:135]\n"
      "v_exp_f32 v235, v235\n"
      "v_mfma_f32_16x16x32_bf16 v[132:135], a[168:171], v[72:75], v[132:135]\n"
      "v_cvt_pk_bf16_f32 v234, v234, v234\n"
      "v_fma_f32 v129, -v222, v146, v129\n"
      "v_mfma_f32_16x16x32_bf16 v[132:135], a[172:175], v[76:79], v[132:135]\n"
      "v_mul_f32 v129, v223, v129\n"
      "v_fma_f32 v130, -v224, v147, v130\n"
      "v_mfma_f32_16x16x32_bf16 v[132:135], a[176:179], v[80:83], v[132:135]\n"
      "v_cvt_pk_bf16_f32 v235, v235, v235\n"
      "v_and_b32 v200, v160, v234\n"
      "v_mfma_f32_16x16x32_bf16 v[132:135], a[180:183], v[84:87], v[132:135]\n"
      "v_cvt_pk_bf16_f32 v184, v196, v197\n"
      "v_and_b32 v201, v161, v235\n"
      "v_mfma_f32_16x16x32_bf16 v[132:135], a[184:187], v[88:91], v[132:135]\n"
      "ds_read_b128 v[68:71], v0 offset:5120\n"
      "v_mul_f32 v130, v225, v130\n"
      "v_mfma_f32_16x16x32_bf16 v[132:135], a[188:191], v[92:95], v[132:135]\n"
      "v_and_b32 v203, v163, v235\n"
      "v_mul_f32_e64 v131, v227, v131\n"
      "v_mfma_f32_16x16x32_bf16 v[132:135], a[192:195], v[96:99], v[132:135]\n"
      "ds_read_b128 v[64:67], v0 offset:4096\n"
      "v_mul_f32_e64 v128, v221, v128\n"
      "v_mfma_f32_16x16x32_bf16 v[132:135], a[196:199], v[100:103], v[132:135]\n"
      "v_and_b32_e64 v202, v162, v234\n"
      "v_cvt_pk_bf16_f32 v181, v130, v131\n"
      "v_mfma_f32_16x16x32_bf16 v[132:135], a[200:203], v[104:107], v[132:135]\n"
      "s_add_u32 s40, s40, 24576\n"
      "ds_read_b128 v[60:63], v0 offset:3072\n"
      "v_cvt_pk_bf16_f32 v180, v128, v129\n"
      "v_mfma_f32_16x16x32_bf16 v[208:211], v[184:187], v[200:203], 0\n"
      "s_waitcnt vmcnt(6) lgkmcnt(3)\n"
      "buffer_store_dword v13, v3, s[8:11], s52 offen sc1\n"
      "ds_read2st64_b64 v[212:215], v4 offset0:0 offset1:1\n"
      "v_mfma_f32_16x16x32_bf16 v[204:207], v[180:183], v[200:203], 0\n"
      "ds_read_b128 v[72:75], v0 offset:6144\n"
      "v_mfma_f32_16x16x32_bf16 v[132:135], a[204:207], v[108:111], v[132:135]\n"
      "ds_read_b128 v[76:79], v0 offset:7168\n"
      "v_mov_b32_e64 v178, v175\n"
      "v_mfma_f32_16x16x32_bf16 v[132:135], a[208:211], v[112:115], v[132:135]\n"
      "ds_read_b128 v[88:91], v0 offset:10240\n"
      "ds_read_b128 v[108:111], v0 offset:15360\n"
      "v_mfma_f32_16x16x32_bf16 v[132:135], a[212:215], v[116:119], v[132:135]\n"
      "s_sub_u32 s20, s20, 1\n"
      "s_cbranch_scc0 .Lq_loop\n"
      ".Lq_tail:\n"
      "v_mul_f32 v232, 0x3eae1a93, v219\n"
      "s_nop 7\n"
      "v_fma_f32 v135, -v218, v151, v135\n"
      "v_fma_f32 v133, -v218, v149, v133\n"
      "v_fma_f32 v134, -v218, v150, v134\n"
      "v_fma_f32 v132, -v218, v148, v132\n"
      "v_cvt_pk_bf16_f32 v176, v132, v133\n"
      "v_cvt_pk_bf16_f32 v177, v134, v135\n"
      "v_rcp_f32 v210, v210\n"
      "s_nop 0\n"
      "v_mul_f32 v204, v204, v210\n"
      "v_mfma_f32_16x16x32_bf16 v[188:191], v[172:175], v[176:179], v[152:155]\n"
      "v_mul_f32 v208, v208, v210\n"
      "v_mul_f32 v207, v207, v210\n"
      "v_mul_f32 v209, v209, v210\n"
      "v_cvt_pk_bf16_f32 v230, v208, v209\n"
      "buffer_store_dwordx2 v[230:231], v8, s[8:11], s40 offen sc1\n"
      "v_mul_f32 v206, v206, v210\n"
      "v_cvt_pk_bf16_f32 v229, v206, v207\n"
      "v_mul_f32 v205, v205, v210\n"
      "v_cvt_pk_bf16_f32 v228, v204, v205\n"
      "buffer_store_dwordx2 v[228:229], v7, s[8:11], s40 offen sc1\n"
      "v_mov_b32 v187, v185\n"
      "v_mov_b32 v186, v184\n"
      "v_mov_b32 v182, v180\n"
      "s_add_u32 s52, s52, 24576\n"
      "v_mov_b32 v183, v181\n"
      "s_add_u32 s40, s40, 24576\n"
      "s_waitcnt vmcnt(0)\n"
      "buffer_store_dword v13, v3, s[8:11], s52 offen sc1\n"
      "s_add_u32 s52, s52, 24576\n"
      "s_cmp_lg_u32 s44, 0\n"
      "s_cbranch_scc1 .Lq_done\n"
      "v_lshlrev_b32 v48, 3, v0\n"
      "s_mov_b32 s22, s23\n"
      ".Lq_up:\n"
      "s_mov_b32 s30, 128\n"
      "buffer_load_dword v49, v48, s[8:11], s30 offen sc1\n"
      "s_mov_b32 s30, 8320\n"
      "buffer_load_dword v50, v48, s[8:11], s30 offen sc1\n"
      "s_mov_b32 s30, 16512\n"
      "buffer_load_dword v51, v48, s[8:11], s30 offen sc1\n"
      "s_mov_b32 s30, 24704\n"
      "buffer_load_dword v52, v48, s[8:11], s30 offen sc1\n"
      "s_waitcnt vmcnt(0)\n"
      "v_xor_b32 v49, s21, v49\n"
      "v_xor_b32 v50, s21, v50\n"
      "v_xor_b32 v51, s21, v51\n"
      "v_xor_b32 v52, s21, v52\n"
      "v_or3_b32 v49, v49, v50, v51\n"
      "v_or_b32 v49, v49, v52\n"
      "v_cmp_ne_u32 vcc, 0, v49\n"
      "s_cbranch_vccz .Lq_ok\n"
      "s_sleep 4\n"
      "s_sub_u32 s22, s22, 1\n"
      "s_cbranch_scc0 .Lq_up\n"
      ".Lq_ok:\n"
      "v_lshrrev_b32 v48, 2, v0\n"
      "v_and_b32 v48, 0x7c, v48\n"
      "v_mov_b32 v49, s21\n"
      "buffer_store_dword v49, v48, s[8:11], 0 offen sc1\n"
      ".Lq_done:\n"
      "s_endpgm\n"
      ".Lsp_q0_1:\n"
      "s_mov_b32 s22, s23\n"
      ".Lsl_q0_1:\n"
      "buffer_load_dword v15, v2, s[8:11], s46 offen sc1\n"
      "s_waitcnt vmcnt(0)\n"
      "v_cmp_ne_u32 vcc, s21, v15\n"
      "s_cbranch_vccz .Lbk_q0_1\n"
      "s_sleep 4\n"
      "s_sub_u32 s22, s22, 1\n"
      "s_cbranch_scc0 .Lsl_q0_1\n"
      "s_mov_b32 s23, 0\n"
      "s_branch .Lbk_q0_1\n"
      ".Lsp_q1_2:\n"
      "s_mov_b32 s22, s23\n"
      ".Lsl_q1_2:\n"
      "buffer_load_dword v15, v2, s[8:11], s46 offen sc1\n"
      "s_waitcnt vmcnt(0)\n"
      "v_cmp_ne_u32 vcc, s21, v15\n"
      "s_cbranch_vccz .Lbk_q1_2\n"
      "s_sleep 4\n"
      "s_sub_u32 s22, s22, 1\n"
      "s_cbranch_scc0 .Lsl_q1_2\n"
      "s_mov_b32 s23, 0\n"
      "s_branch .Lbk_q1_2\n"
      ".Lsp_qy0_3:\n"
      "s_lshl_b32 s22, s23, 4\n"
      ".Lsl_qy0_3:\n"
      "ds_read_b32 v10, v12\n"
      "s_waitcnt lgkmcnt(0)\n"
      "v_cmp_gt_i32 vcc, v9, v10\n"
      "s_cbranch_vccz .Lbk_qy0_3\n"
      "s_sub_u32 s22, s22, 1\n"
      "s_cbranch_scc0 .Lsl_qy0_3\n"
      "s_mov_b32 s23, 0\n"
      "s_branch .Lbk_qy0_3\n"
      ".Lsp_q2_4:\n"
      "s_mov_b32 s22, s23\n"
      "buffer_load_dword v15, v2, s[8:11], s46 offen sc1\n"
      "s_waitcnt vmcnt(0)\n"
      "v_cmp_ne_u32 vcc, s21, v15\n"
      "s_add_u32 s30, s52, 24576\n"
      "buffer_store_dword v13, v3, s[8:11], s30 offen sc1\n"
      "s_cbranch_vccz .Lbk_q2_4\n"
      ".Lsl_q2_4:\n"
      "buffer_load_dword v15, v2, s[8:11], s46 offen sc1\n"
      "s_waitcnt vmcnt(0)\n"
      "v_cmp_ne_u32 vcc, s21, v15\n"
      "s_cbranch_vccz .Lbk_q2_4\n"
      "s_sleep 4\n"
      "s_sub_u32 s22, s22, 1\n"
      "s_cbranch_scc0 .Lsl_q2_4\n"
      "s_mov_b32 s23, 0\n"
      "s_branch .Lbk_q2_4\n"
      ".Lsp_qy1_5:\n"
      "s_lshl_b32 s22, s23, 4\n"
      ".Lsl_qy1_5:\n"
      "ds_read_b32 v10, v12\n"
      "s_waitcnt lgkmcnt(0)\n"
      "v_cmp_gt_i32 vcc, v9, v10\n"
      "s_cbranch_vccz .Lbk_qy1_5\n"
      "s_sub_u32 s22, s22, 1\n"
      "s_cbranch_scc0 .Lsl_qy1_5\n"
      "s_mov_b32 s23, 0\n"
      "s_branch .Lbk_qy1_5\n"
      ".Lsp_q0_6:\n"
      "s_mov_b32 s22, s23\n"
      "buffer_load_dword v15, v2, s[8:11], s46 offen sc1\n"
      "s_waitcnt vmcnt(0)\n"
      "v_cmp_ne_u32 vcc, s21, v15\n"
      "s_add_u32 s30, s52, 24576\n"
      "buffer_store_dword v13, v3, s[8:11], s30 offen sc1\n"
      "s_cbranch_vccz .Lbk_q0_6\n"
      ".Lsl_q0_6:\n"
      "buffer_load_dword v15, v2, s[8:11], s46 offen sc1\n"
      "s_waitcnt vmcnt(0)\n"
      "v_cmp_ne_u32 vcc, s21, v15\n"
      "s_cbranch_vccz .Lbk_q0_6\n"
      "s_sleep 4\n"
      "s_sub_u32 s22, s22, 1\n"
      "s_cbranch_scc0 .Lsl_q0_6\n"
      "s_mov_b32 s23, 0\n"
      "s_branch .Lbk_q0_6\n"
      ".Lsp_qy2_7:\n"
      "s_lshl_b32 s22, s23, 4\n"
      ".Lsl_qy2_7:\n"
      "ds_read_b32 v10, v12\n"
      "s_waitcnt lgkmcnt(0)\n"
      "v_cmp_gt_i32 vcc, v9, v10\n"
      "s_cbranch_vccz .Lbk_qy2_7\n"
      "s_sub_u32 s22, s22, 1\n"
      "s_cbranch_scc0 .Lsl_qy2_7\n"
      "s_mov_b32 s23, 0\n"
      "s_branch .Lbk_qy2_7\n"
      ".Lsp_q1_8:\n"
      "s_mov_b32 s22, s23\n"
      "buffer_load_dword v15, v2, s[8:11], s46 offen sc1\n"
      "s_waitcnt vmcnt(0)\n"
      "v_cmp_ne_u32 vcc, s21, v15\n"
      "s_add_u32 s30, s52, 0\n"
      "buffer_store_dword v13, v3, s[8:11], s30 offen sc1\n"
      "s_cbranch_vccz .Lbk_q1_8\n"
      ".Lsl_q1_8:\n"
      "buffer_load_dword v15, v2, s[8:11], s46 offen sc1\n"
      "s_waitcnt vmcnt(0)\n"
      "v_cmp_ne_u32 vcc, s21, v15\n"
      "s_cbranch_vccz .Lbk_q1_8\n"
      "s_sleep 4\n"
      "s_sub_u32 s22, s22, 1\n"
      "s_cbranch_scc0 .Lsl_q1_8\n"
      "s_mov_b32 s23, 0\n"
      "s_branch .Lbk_q1_8\n"
      ".Lsp_qy0_9:\n"
      "s_lshl_b32 s22, s23, 4\n"
      ".Lsl_qy0_9:\n"
      "ds_read_b32 v10, v12\n"
      "s_waitcnt lgkmcnt(0)\n"
      "v_cmp_gt_i32 vcc, v9, v10\n"
      "s_cbranch_vccz .Lbk_qy0_9\n"
      "s_sub_u32 s22, s22, 1\n"
      "s_cbranch_scc0 .Lsl_qy0_9\n"
      "s_mov_b32 s23, 0\n"
      "s_branch .Lbk_qy0_9\n"
      ".Lq_last:\n"
      :
      : "{s[8:11]}"(rsWS), "{s[12:15]}"(rsA), "{v0}"(v_l16), "{v1}"(v_l5), "{v2}"(v_fa), "{v3}"(v_ra), "{v4}"(v_st), "{v5}"(v_tw), "{v6}"(v_tr), "{v7}"(v_om), "{v8}"(v_oe), "{v10}"(v_al), "{v11}"(v_yw), "{v12}"(v_yr), "{v14}"(v_hi), "{v152}"(v_bc[0]), "{v153}"(v_bc[1]), "{v154}"(v_bc[2]), "{v155}"(v_bc[3]), "{v156}"(v_bp[0]), "{v157}"(v_bp[1]), "{v158}"(v_bp[2]), "{v159}"(v_bp[3]), "{v160}"(v_mk[0]), "{v161}"(v_mk[1]), "{v162}"(v_mk[2]), "{v163}"(v_mk[3]), "{v164}"(v_sel[0]), "{v165}"(v_sel[1]), "{v166}"(v_sel[2]), "{v167}"(v_sel[3]), "{v168}"(v_ci[0]), "{v169}"(v_ci[1]), "{v170}"(v_ci[2]), "{v171}"(v_ci[3]), "{v48}"(v_vo[0]), "{v49}"(v_vo[1]), "{v50}"(v_vo[2]), "{v51}"(v_vo[3]), "{v52}"(v_at), "{v53}"(v_ax), "{v54}"(v_pm), "{v55}"(v_ms), "{v76}"(v_pw), "{s36}"(s_sx), "{s37}"(s_ev), "{s38}"(s_del), "{s39}"(s_kil), "{s40}"(s_so), "{s41}"(s_m4), "{s42}"(s_m5), "{s43}"(s_ms), "{s44}"(s_up)
      : "v255", "a255", "s99", "vcc", "scc", "memory");

  } else if (role == 1) {
    const unsigned c = part, o = w & 1;
    const u32x4 rsA = srd(Wo, 576 * 576 * 2);
    const unsigned v_l5 = v_l16 + 16384 - w * 3072, e0 = 16384 + o * 2048 + g * 512 + i16 * 8, e1 = e0 + 2048 - o * 4096;
    const unsigned v_ex[8] = {e0, e1, e0 + 20480, e1 + 20480, e0 + 40960, e1 + 40960, e0 + 61440, e1 + 61440}, v_l2 = v_l16 + 40960;
    const unsigned v_fa = 20480 - w * 4096 + (8 * w + (lane >> 3)) * 128 + (lane & 7) * 4, v_ra = up * 8 * REC + l5 + 19456 + (4 * c + w) * 128;
    const unsigned v_xp = (9 * c + 2 * w) * 1024 + v_l16, v_xs = (9 * c + 8) * 1024 + (w >> 1) * 512 + half;
    const unsigned v_rp = v_xp + WS_XIN, v_rs = v_xs + WS_XIN;
    const unsigned v_ew = 82944 + (w >> 1) * 1024 + lane * 4;
    const unsigned v_sw = 86016 + i16 * 144 + (4 * w + g) * 8, v_sr = 86016 + (8 * (w >> 1) + l8) * 144 + l7;
    const unsigned v_ss = 18432 + c * 512 + (w >> 1) * 256 + l8 * 32 + p * 8;
    const unsigned v_pw = 19456 + (4 * c + w) * 128 + l8 * REC + l7;
    unsigned v_vo[9], v_vp[9];
#pragma unroll
    for (unsigned j = 0; j < 9; ++j) {
      const unsigned ch = 64 * j + lane, r = ch / 36, x = 576 * c + ch % 36 * 16 - 1024 * (j & 3);
      v_vo[j] = (20 * o + r) * 1152 + x;
      v_vp[j] = (36 * (4 * o + (r >> 2)) + 16 + (r & 3)) * 1152 + x;
    }
    const unsigned ro = (8 * g + q) * 576;
    const unsigned v_t0 = ro + o * 18432 + 128 * w + 16 * p, v_t1 = v_t0 + 18432 - 2 * o * 18432, v_ts = ro + o * 18432 + 512 + 32 * (w >> 1) + 8 * p;
    const unsigned s_sx = WS_OB + (gr * NREC + KSK) * REC + w * 4096, s_ev = s_sx + (31 - KSK) * REC, s_sy = WS_X1 + (gr * NREC + KSK) * REC;
    const unsigned s_m4 = w * 4096, s_m5 = 16384 + w * 1024, s_m9 = w * 9216, s_so = (w >> 1) * 8 * 41472;
    const unsigned s_s8 = s_so, s_wg = o, s_rb = WS_OB + w * 4096;
    asm volatile(
      "s_buffer_load_dword s21, s[8:11], 0\n"
      "s_mov_b32 s23, 65536\n"
      "s_mov_b32 s31, 0x40000000\n"
      "s_add_u32 m0, s41, 0\n"
      "s_nop 0\n"
      "buffer_load_dwordx4 v100, s[12:15], s42 offen offset:0 lds\n"
      "buffer_load_dwordx4 v101, s[12:15], s42 offen offset:1024 lds\n"
      "buffer_load_dwordx4 v102, s[12:15], s42 offen offset:2048 lds\n"
      "buffer_load_dwordx4 v103, s[12:15], s42 offen offset:3072 lds\n"
      "s_add_u32 m0, s41, 4096\n"
      "s_nop 0\n"
      "buffer_load_dwordx4 v104, s[12:15], s42 offen offset:0 lds\n"
      "buffer_load_dwordx4 v105, s[12:15], s42 offen offset:1024 lds\n"
      "buffer_load_dwordx4 v106, s[12:15], s42 offen offset:2048 lds\n"
      "buffer_load_dwordx4 v107, s[12:15], s42 offen offset:3072 lds\n"
      "s_add_u32 m0, s41, 8192\n"
      "s_nop 0\n"
      "buffer_load_dwordx4 v108, s[12:15], s42 offen offset:0 lds\n"
      "s_add_u32 s42, s42, 41472\n"
      "s_add_u32 m0, s41, 36864\n"
      "s_nop 0\n"
      "buffer_load_dwordx4 v100, s[12:15], s42 offen offset:0 lds\n"
      "buffer_load_dwordx4 v101, s[12:15], s42 offen offset:1024 lds\n"
      "buffer_load_dwordx4 v102, s[12:15], s42 offen offset:2048 lds\n"
      "buffer_load_dwordx4 v103, s[12:15], s42 offen offset:3072 lds\n"
      "s_add_u32 m0, s41, 40960\n"
      "s_nop 0\n"
      "buffer_load_dwordx4 v104, s[12:15], s42 offen offset:0 lds\n"
      "buffer_load_dwordx4 v105, s[12:15], s42 offen offset:1024 lds\n"
      "buffer_load_dwordx4 v106, s[12:15], s42 offen offset:2048 lds\n"
      "buffer_load_dwordx4 v107, s[12:15], s42 offen offset:3072 lds\n"
      "s_add_u32 m0, s41, 45056\n"
      "s_nop 0\n"
      "buffer_load_dwordx4 v108, s[12:15], s42 offen offset:0 lds\n"
      "s_add_u32 s42, s42, 41472\n"
      "s_add_u32 m0, s41, 73728\n"
      "s_nop 0\n"
      "buffer_load_dwordx4 v100, s[12:15], s42 offen offset:0 lds\n"
      "buffer_load_dwordx4 v101, s[12:15], s42 offen offset:1024 lds\n"
      "buffer_load_dwordx4 v102, s[12:15], s42 offen offset:2048 lds\n"
      "buffer_load_dwordx4 v103, s[12:15], s42 offen offset:3072 lds\n"
      "s_add_u32 m0, s41, 77824\n"
      "s_nop 0\n"
      "buffer_load_dwordx4 v104, s[12:15], s42 offen offset:0 lds\n"
      "buffer_load_dwordx4 v105, s[12:15], s42 offen offset:1024 lds\n"
      "buffer_load_dwordx4 v106, s[12:15], s42 offen offset:2048 lds\n"
      "buffer_load_dwordx4 v107, s[12:15], s42 offen offset:3072 lds\n"
      "s_add_u32 m0, s41, 81920\n"
      "s_nop 0\n"
      "buffer_load_dwordx4 v108, s[12:15], s42 offen offset:0 lds\n"
      "s_add_u32 s42, s42, 41472\n"
      "s_waitcnt vmcnt(18)\n"
      "s_barrier\n"
      "ds_read_b64_tr_b16 v[64:65], v120 offset:0\n"
      "ds_read_b64_tr_b16 v[66:67], v120 offset:2304\n"
      "ds_read_b64_tr_b16 a[0:1], v118 offset:0\n"
      "ds_read_b64_tr_b16 a[2:3], v118 offset:2304\n"
      "ds_read_b64_tr_b16 a[72:73], v118 offset:8\n"
      "ds_read_b64_tr_b16 a[74:75], v118 offset:2312\n"
      "ds_read_b64_tr_b16 a[144:145], v118 offset:64\n"
      "ds_read_b64_tr_b16 a[146:147], v118 offset:2368\n"
      "ds_read_b64_tr_b16 a[216:217], v118 offset:72\n"
      "ds_read_b64_tr_b16 a[218:219], v118 offset:2376\n"
      "ds_read_b64_tr_b16 a[36:37], v119 offset:0\n"
      "ds_read_b64_tr_b16 a[38:39], v119 offset:2304\n"
      "ds_read_b64_tr_b16 a[108:109], v119 offset:8\n"
      "ds_read_b64_tr_b16 a[110:111], v119 offset:2312\n"
      "ds_read_b64_tr_b16 a[180:181], v119 offset:64\n"
      "ds_read_b64_tr_b16 a[182:183], v119 offset:2368\n"
      "ds_read_b64_tr_b16 a[252:253], v119 offset:72\n"
      "ds_read_b64_tr_b16 a[254:255], v119 offset:2376\n"
      "v_add_u32 v118, 36864, v118\n"
      "v_add_u32 v119, 36864, v119\n"
      "v_add_u32 v120, 36864, v120\n"
      "s_add_u32 m0, s41, 110592\n"
      "s_nop 0\n"
      "buffer_load_dwordx4 v100, s[12:15], s42 offen offset:0 lds\n"
      "buffer_load_dwordx4 v101, s[12:15], s42 offen offset:1024 lds\n"
      "buffer_load_dwordx4 v102, s[12:15], s42 offen offset:2048 lds\n"
      "buffer_load_dwordx4 v103, s[12:15], s42 offen offset:3072 lds\n"
      "s_add_u32 m0, s41, 114688\n"
      "s_nop 0\n"
      "buffer_load_dwordx4 v104, s[12:15], s42 offen offset:0 lds\n"
      "buffer_load_dwordx4 v105, s[12:15], s42 offen offset:1024 lds\n"
      "buffer_load_dwordx4 v106, s[12:15], s42 offen offset:2048 lds\n"
      "buffer_load_dwordx4 v107, s[12:15], s42 offen offset:3072 lds\n"
      "s_add_u32 m0, s41, 118784\n"
      "s_nop 0\n"
      "buffer_load_dwordx4 v108, s[12:15], s42 offen offset:0 lds\n"
      "s_add_u32 s42, s42, 41472\n"
      "s_waitcnt vmcnt(18) lgkmcnt(0)\n"
      "s_barrier\n"
      "ds_read_b64_tr_b16 v[68:69], v120 offset:0\n"
      "ds_read_b64_tr_b16 v[70:71], v120 offset:2304\n"
      "ds_read_b64_tr_b16 a[4:5], v118 offset:0\n"
      "ds_read_b64_tr_b16 a[6:7], v118 offset:2304\n"
      "ds_read_b64_tr_b16 a[76:77], v118 offset:8\n"
      "ds_read_b64_tr_b16 a[78:79], v118 offset:2312\n"
      "ds_read_b64_tr_b16 a[148:149], v118 offset:64\n"
      "ds_read_b64_tr_b16 a[150:151], v118 offset:2368\n"
      "ds_read_b64_tr_b16 a[220:221], v118 offset:72\n"
      "ds_read_b64_tr_b16 a[222:223], v118 offset:2376\n"
      "ds_read_b64_tr_b16 a[40:41], v119 offset:0\n"
      "ds_read_b64_tr_b16 a[42:43], v119 offset:2304\n"
      "ds_read_b64_tr_b16 a[112:113], v119 offset:8\n"
      "ds_read_b64_tr_b16 a[114:115], v119 offset:2312\n"
      "ds_read_b64_tr_b16 a[184:185], v119 offset:64\n"
      "ds_read_b64_tr_b16 a[186:187], v119 offset:2368\n"
      "ds_read_b64_tr_b16 v[32:33], v119 offset:72\n"
      "ds_read_b64_tr_b16 v[34:35], v119 offset:2376\n"
      "v_add_u32 v118, 36864, v118\n"
      "v_add_u32 v119, 36864, v119\n"
      "v_add_u32 v120, 36864, v120\n"
      "s_add_u32 m0, s41, 0\n"
      "s_nop 0\n"
      "buffer_load_dwordx4 v100, s[12:15], s42 offen offset:0 lds\n"
      "buffer_load_dwordx4 v101, s[12:15], s42 offen offset:1024 lds\n"
      "buffer_load_dwordx4 v102, s[12:15], s42 offen offset:2048 lds\n"
      "buffer_load_dwordx4 v103, s[12:15], s42 offen offset:3072 lds\n"
      "s_add_u32 m0, s41, 4096\n"
      "s_nop 0\n"
      "buffer_load_dwordx4 v104, s[12:15], s42 offen offset:0 lds\n"
      "buffer_load_dwordx4 v105, s[12:15], s42 offen offset:1024 lds\n"
      "buffer_load_dwordx4 v106, s[12:15], s42 offen offset:2048 lds\n"
      "buffer_load_dwordx4 v107, s[12:15], s42 offen offset:3072 lds\n"
      "s_add_u32 m0, s41, 8192\n"
      "s_nop 0\n"
      "buffer_load_dwordx4 v108, s[12:15], s42 offen offset:0 lds\n"
      "s_add_u32 s42, s42, 41472\n"
      "s_waitcnt vmcnt(18) lgkmcnt(0)\n"
      "s_barrier\n"
      "ds_read_b64_tr_b16 v[72:73], v120 offset:0\n"
      "ds_read_b64_tr_b16 v[74:75], v120 offset:2304\n"
      "ds_read_b64_tr_b16 a[8:9], v118 offset:0\n"
      "ds_read_b64_tr_b16 a[10:11], v118 offset:2304\n"
      "ds_read_b64_tr_b16 a[80:81], v118 offset:8\n"
      "ds_read_b64_tr_b16 a[82:83], v118 offset:2312\n"
      "ds_read_b64_tr_b16 a[152:153], v118 offset:64\n"
      "ds_read_b64_tr_b16 a[154:155], v118 offset:2368\n"
      "ds_read_b64_tr_b16 a[224:225], v118 offset:72\n"
      "ds_read_b64_tr_b16 a[226:227], v118 offset:2376\n"
      "ds_read_b64_tr_b16 a[44:45], v119 offset:0\n"
      "ds_read_b64_tr_b16 a[46:47], v119 offset:2304\n"
      "ds_read_b64_tr_b16 a[116:117], v119 offset:8\n"
      "ds_read_b64_tr_b16 a[118:119], v119 offset:2312\n"
      "ds_read_b64_tr_b16 a[188:189], v119 offset:64\n"
      "ds_read_b64_tr_b16 a[190:191], v119 offset:2368\n"
      "ds_read_b64_tr_b16 v[36:37], v119 offset:72\n"
      "ds_read_b64_tr_b16 v[38:39], v119 offset:2376\n"
      "v_add_u32 v118, 36864, v118\n"
      "v_add_u32 v119, 36864, v119\n"
      "v_add_u32 v120, 36864, v120\n"
      "s_add_u32 m0, s41, 36864\n"
      "s_nop 0\n"
      "buffer_load_dwordx4 v100, s[12:15], s42 offen offset:0 lds\n"
      "buffer_load_dwordx4 v101, s[12:15], s42 offen offset:1024 lds\n"
      "buffer_load_dwordx4 v102, s[12:15], s42 offen offset:2048 lds\n"
      "buffer_load_dwordx4 v103, s[12:15], s42 offen offset:3072 lds\n"
      "s_add_u32 m0, s41, 40960\n"
      "s_nop 0\n"
      "buffer_load_dwordx4 v104, s[12:15], s42 offen offset:0 lds\n"
      "buffer_load_dwordx4 v105, s[12:15], s42 offen offset:1024 lds\n"
      "buffer_load_dwordx4 v106, s[12:15], s42 offen offset:2048 lds\n"
      "buffer_load_dwordx4 v107, s[12:15], s42 offen offset:3072 lds\n"
      "s_add_u32 m0, s41, 45056\n"
      "s_nop 0\n"
      "buffer_load_dwordx4 v108, s[12:15], s42 offen offset:0 lds\n"
      "s_add_u32 s42, s42, 41472\n"
      "s_waitcnt vmcnt(18) lgkmcnt(0)\n"
      "s_barrier\n"
      "ds_read_b64_tr_b16 v[76:77], v120 offset:0\n"
      "ds_read_b64_tr_b16 v[78:79], v120 offset:2304\n"
      "ds_read_b64_tr_b16 a[12:13], v118 offset:0\n"
      "ds_read_b64_tr_b16 a[14:15], v118 offset:2304\n"
      "ds_read_b64_tr_b16 a[84:85], v118 offset:8\n"
      "ds_read_b64_tr_b16 a[86:87], v118 offset:2312\n"
      "ds_read_b64_tr_b16 a[156:157], v118 offset:64\n"
      "ds_read_b64_tr_b16 a[158:159], v118 offset:2368\n"
      "ds_read_b64_tr_b16 a[228:229], v118 offset:72\n"
      "ds_read_b64_tr_b16 a[230:231], v118 offset:2376\n"
      "ds_read_b64_tr_b16 a[48:49], v119 offset:0\n"
      "ds_read_b64_tr_b16 a[50:51], v119 offset:2304\n"
      "ds_read_b64_tr_b16 a[120:121], v119 offset:8\n"
      "ds_read_b64_tr_b16 a[122:123], v119 offset:2312\n"
      "ds_read_b64_tr_b16 a[192:193], v119 offset:64\n"
      "ds_read_b64_tr_b16 a[194:195], v119 offset:2368\n"
      "ds_read_b64_tr_b16 v[40:41], v119 offset:72\n"
      "ds_read_b64_tr_b16 v[42:43], v119 offset:2376\n"
      "v_add_u32 v118, 4294856704, v118\n"
      "v_add_u32 v119, 4294856704, v119\n"
      "v_add_u32 v120, 4294856704, v120\n"
      "s_add_u32 m0, s41, 73728\n"
      "s_nop 0\n"
      "buffer_load_dwordx4 v100, s[12:15], s42 offen offset:0 lds\n"
      "buffer_load_dwordx4 v101, s[12:15], s42 offen offset:1024 lds\n"
      "buffer_load_dwordx4 v102, s[12:15], s42 offen offset:2048 lds\n"
      "buffer_load_dwordx4 v103, s[12:15], s42 offen offset:3072 lds\n"
      "s_add_u32 m0, s41, 77824\n"
      "s_nop 0\n"
      "buffer_load_dwordx4 v104, s[12:15], s42 offen offset:0 lds\n"
      "buffer_load_dwordx4 v105, s[12:15], s42 offen offset:1024 lds\n"
      "buffer_load_dwordx4 v106, s[12:15], s42 offen offset:2048 lds\n"
      "buffer_load_dwordx4 v107, s[12:15], s42 offen offset:3072 lds\n"
      "s_add_u32 m0, s41, 81920\n"
      "s_nop 0\n"
      "buffer_load_dwordx4 v108, s[12:15], s42 offen offset:0 lds\n"
      "s_add_u32 s42, s42, 41472\n"
      "s_waitcnt vmcnt(18) lgkmcnt(0)\n"
      "s_barrier\n"
      "ds_read_b64_tr_b16 v[80:81], v120 offset:0\n"
      "ds_read_b64_tr_b16 v[82:83], v120 offset:2304\n"
      "ds_read_b64_tr_b16 a[16:17], v118 offset:0\n"
      "ds_read_b64_tr_b16 a[18:19], v118 offset:2304\n"
      "ds_read_b64_tr_b16 a[88:89], v118 offset:8\n"
      "ds_read_b64_tr_b16 a[90:91], v118 offset:2312\n"
      "ds_read_b64_tr_b16 a[160:161], v118 offset:64\n"
      "ds_read_b64_tr_b16 a[162:163], v118 offset:2368\n"
      "ds_read_b64_tr_b16 a[232:233], v118 offset:72\n"
      "ds_read_b64_tr_b16 a[234:235], v118 offset:2376\n"
      "ds_read_b64_tr_b16 a[52:53], v119 offset:0\n"
      "ds_read_b64_tr_b16 a[54:55], v119 offset:2304\n"
      "ds_read_b64_tr_b16 a[124:125], v119 offset:8\n"
      "ds_read_b64_tr_b16 a[126:127], v119 offset:2312\n"
      "ds_read_b64_tr_b16 a[196:197], v119 offset:64\n"
      "ds_read_b64_tr_b16 a[198:199], v119 offset:2368\n"
      "ds_read_b64_tr_b16 v[44:45], v119 offset:72\n"
      "ds_read_b64_tr_b16 v[46:47], v119 offset:2376\n"
      "v_add_u32 v118, 36864, v118\n"
      "v_add_u32 v119, 36864, v119\n"
      "v_add_u32 v120, 36864, v120\n"
      "s_add_u32 m0, s41, 110592\n"
      "s_nop 0\n"
      "buffer_load_dwordx4 v100, s[12:15], s42 offen offset:0 lds\n"
      "buffer_load_dwordx4 v101, s[12:15], s42 offen offset:1024 lds\n"
      "buffer_load_dwordx4 v102, s[12:15], s42 offen offset:2048 lds\n"
      "buffer_load_dwordx4 v103, s[12:15], s42 offen offset:3072 lds\n"
      "s_add_u32 m0, s41, 114688\n"
      "s_nop 0\n"
      "buffer_load_dwordx4 v104, s[12:15], s42 offen offset:0 lds\n"
      "buffer_load_dwordx4 v105, s[12:15], s42 offen offset:1024 lds\n"
      "buffer_load_dwordx4 v106, s[12:15], s42 offen offset:2048 lds\n"
      "buffer_load_dwordx4 v107, s[12:15], s42 offen offset:3072 lds\n"
      "s_add_u32 m0, s41, 118784\n"
      "s_nop 0\n"
      "buffer_load_dwordx4 v108, s[12:15], s42 offen offset:0 lds\n"
      "s_add_u32 s42, s42, 41472\n"
      "s_waitcnt vmcnt(18) lgkmcnt(0)\n"
      "s_barrier\n"
      "ds_read_b64_tr_b16 v[84:85], v120 offset:0\n"
      "ds_read_b64_tr_b16 v[86:87], v120 offset:2304\n"
      "ds_read_b64_tr_b16 a[20:21], v118 offset:0\n"
      "ds_read_b64_tr_b16 a[22:23], v118 offset:2304\n"
      "ds_read_b64_tr_b16 a[92:93], v118 offset:8\n"
      "ds_read_b64_tr_b16 a[94:95], v118 offset:2312\n"
      "ds_read_b64_tr_b16 a[164:165], v118 offset:64\n"
      "ds_read_b64_tr_b16 a[166:167], v118 offset:2368\n"
      "ds_read_b64_tr_b16 a[236:237], v118 offset:72\n"
      "ds_read_b64_tr_b16 a[238:239], v118 offset:2376\n"
      "ds_read_b64_tr_b16 a[56:57], v119 offset:0\n"
      "ds_read_b64_tr_b16 a[58:59], v119 offset:2304\n"
      "ds_read_b64_tr_b16 a[128:129], v119 offset:8\n"
      "ds_read_b64_tr_b16 a[130:131], v119 offset:2312\n"
      "ds_read_b64_tr_b16 a[200:201], v119 offset:64\n"
      "ds_read_b64_tr_b16 a[202:203], v119 offset:2368\n"
      "ds_read_b64_tr_b16 v[48:49], v119 offset:72\n"
      "ds_read_b64_tr_b16 v[50:51], v119 offset:2376\n"
      "v_add_u32 v118, 36864, v118\n"
      "v_add_u32 v119, 36864, v119\n"
      "v_add_u32 v120, 36864, v120\n"
      "s_add_u32 m0, s41, 0\n"
      "s_nop 0\n"
      "buffer_load_dwordx4 v109, s[12:15], s43 offen offset:0 lds\n"
      "buffer_load_dwordx4 v110, s[12:15], s43 offen offset:1024 lds\n"
      "buffer_load_dwordx4 v111, s[12:15], s43 offen offset:2048 lds\n"
      "buffer_load_dwordx4 v112, s[12:15], s43 offen offset:3072 lds\n"
      "s_add_u32 m0, s41, 4096\n"
      "s_nop 0\n"
      "buffer_load_dwordx4 v113, s[12:15], s43 offen offset:0 lds\n"
      "buffer_load_dwordx4 v114, s[12:15], s43 offen offset:1024 lds\n"
      "buffer_load_dwordx4 v115, s[12:15], s43 offen offset:2048 lds\n"
      "buffer_load_dwordx4 v116, s[12:15], s43 offen offset:3072 lds\n"
      "s_add_u32 m0, s41, 8192\n"
      "s_nop 0\n"
      "buffer_load_dwordx4 v117, s[12:15], s43 offen offset:0 lds\n"
      "s_add_u32 s42, s42, 41472\n"
      "s_waitcnt vmcnt(18) lgkmcnt(0)\n"
      "s_barrier\n"
      "ds_read_b64_tr_b16 v[88:89], v120 offset:0\n"
      "ds_read_b64_tr_b16 v[90:91], v120 offset:2304\n"
      "ds_read_b64_tr_b16 a[24:25], v118 offset:0\n"
      "ds_read_b64_tr_b16 a[26:27], v118 offset:2304\n"
      "ds_read_b64_tr_b16 a[96:97], v118 offset:8\n"
      "ds_read_b64_tr_b16 a[98:99], v118 offset:2312\n"
      "ds_read_b64_tr_b16 a[168:169], v118 offset:64\n"
      "ds_read_b64_tr_b16 a[170:171], v118 offset:2368\n"
      "ds_read_b64_tr_b16 a[240:241], v118 offset:72\n"
      "ds_read_b64_tr_b16 a[242:243], v118 offset:2376\n"
      "ds_read_b64_tr_b16 a[60:61], v119 offset:0\n"
      "ds_read_b64_tr_b16 a[62:63], v119 offset:2304\n"
      "ds_read_b64_tr_b16 a[132:133], v119 offset:8\n"
      "ds_read_b64_tr_b16 a[134:135], v119 offset:2312\n"
      "ds_read_b64_tr_b16 a[204:205], v119 offset:64\n"
      "ds_read_b64_tr_b16 a[206:207], v119 offset:2368\n"
      "ds_read_b64_tr_b16 v[52:53], v119 offset:72\n"
      "ds_read_b64_tr_b16 v[54:55], v119 offset:2376\n"
      "v_add_u32 v118, 36864, v118\n"
      "v_add_u32 v119, 36864, v119\n"
      "v_add_u32 v120, 36864, v120\n"
      "s_waitcnt vmcnt(9) lgkmcnt(0)\n"
      "s_barrier\n"
      "ds_read_b64_tr_b16 v[92:93], v120 offset:0\n"
      "ds_read_b64_tr_b16 v[94:95], v120 offset:2304\n"
      "ds_read_b64_tr_b16 a[28:29], v118 offset:0\n"
      "ds_read_b64_tr_b16 a[30:31], v118 offset:2304\n"
      "ds_read_b64_tr_b16 a[100:101], v118 offset:8\n"
      "ds_read_b64_tr_b16 a[102:103], v118 offset:2312\n"
      "ds_read_b64_tr_b16 a[172:173], v118 offset:64\n"
      "ds_read_b64_tr_b16 a[174:175], v118 offset:2368\n"
      "ds_read_b64_tr_b16 a[244:245], v118 offset:72\n"
      "ds_read_b64_tr_b16 a[246:247], v118 offset:2376\n"
      "ds_read_b64_tr_b16 a[64:65], v119 offset:0\n"
      "ds_read_b64_tr_b16 a[66:67], v119 offset:2304\n"
      "ds_read_b64_tr_b16 a[136:137], v119 offset:8\n"
      "ds_read_b64_tr_b16 a[138:139], v119 offset:2312\n"
      "ds_read_b64_tr_b16 a[208:209], v119 offset:64\n"
      "ds_read_b64_tr_b16 a[210:211], v119 offset:2368\n"
      "ds_read_b64_tr_b16 v[56:57], v119 offset:72\n"
      "ds_read_b64_tr_b16 v[58:59], v119 offset:2376\n"
      "v_add_u32 v118, 4294856704, v118\n"
      "v_add_u32 v119, 4294856704, v119\n"
      "v_add_u32 v120, 4294856704, v120\n"
      "s_waitcnt vmcnt(0) lgkmcnt(0)\n"
      "s_barrier\n"
      "ds_read_b64_tr_b16 v[96:97], v120 offset:0\n"
      "ds_read_b64_tr_b16 v[98:99], v120 offset:2304\n"
      "ds_read_b64_tr_b16 a[32:33], v118 offset:0\n"
      "ds_read_b64_tr_b16 a[34:35], v118 offset:2304\n"
      "ds_read_b64_tr_b16 a[104:105], v118 offset:8\n"
      "ds_read_b64_tr_b16 a[106:107], v118 offset:2312\n"
      "ds_read_b64_tr_b16 a[176:177], v118 offset:64\n"
      "ds_read_b64_tr_b16 a[178:179], v118 offset:2368\n"
      "ds_read_b64_tr_b16 a[248:249], v118 offset:72\n"
      "ds_read_b64_tr_b16 a[250:251], v118 offset:2376\n"
      "ds_read_b64_tr_b16 a[68:69], v119 offset:0\n"
      "ds_read_b64_tr_b16 a[70:71], v119 offset:2304\n"
      "ds_read_b64_tr_b16 a[140:141], v119 offset:8\n"
      "ds_read_b64_tr_b16 a[142:143], v119 offset:2312\n"
      "ds_read_b64_tr_b16 a[212:213], v119 offset:64\n"
      "ds_read_b64_tr_b16 a[214:215], v119 offset:2368\n"
      "ds_read_b64_tr_b16 v[60:61], v119 offset:72\n"
      "ds_read_b64_tr_b16 v[62:63], v119 offset:2376\n"
      "v_add_u32 v118, 36864, v118\n"
      "v_add_u32 v119, 36864, v119\n"
      "v_add_u32 v120, 36864, v120\n"
      "s_waitcnt lgkmcnt(0)\n"
      "s_getpc_b64 vcc\n"
      ".Lw_ct:\n"
      "s_add_u32 vcc_lo, vcc_lo, .Lw_p0-.Lw_ct\n"
      "s_addc_u32 vcc_hi, vcc_hi, 0\n"
      "s_lshl_b32 s30, s39, 1\n"
      "v_lshl_add_u32 v130, v0, 3, s30\n"
      "v_min_u32 v130, .Lw_last-.Lw_p0-4, v130\n"
      "global_load_dword v130, v130, vcc\n"
      "s_mul_i32 s21, s21, 1664525\n"
      "s_add_u32 s21, s21, 1013904223\n"
      "v_mov_b32 v25, s21\n"
      "buffer_store_dword v25, v22, s[8:11], 0 offen sc1\n"
      "v_not_b32 v124, v25\n"
      "v_not_b32 v125, v25\n"
      "v_not_b32 v126, v25\n"
      "v_not_b32 v127, v25\n"
      "s_add_u32 s30, s38, 0\n"
      "buffer_store_dwordx4 v[124:127], v121, s[8:11], s30 offen sc1\n"
      "v_xor_b32 v25, v25, v26\n"
      "v_mov_b32 v27, 0x3ae38e39\n"
      "s_barrier\n"
      "v_mov_b32 v21, 0\n"
      "ds_write_b32 v23, v21\n"
      "s_waitcnt lgkmcnt(0)\n"
      "s_barrier\n"
      "s_add_u32 s6, s36, 1572864\n"
      "s_add_u32 s36, s36, 4294942720\n"
      "s_mov_b32 s51, 24576\n"
      "s_waitcnt vmcnt(0) lgkmcnt(0)\n"
      "s_cmp_lg_u32 s44, 0\n"
      "s_cbranch_scc1 .Lv_start\n"
      "s_mov_b64 exec, 0\n"
      "s_lshr_b32 s22, s39, 13\n"
      ".Lw_pl:\n"
      "s_cmp_eq_u32 s22, 0\n"
      "s_cbranch_scc1 .Lw_p0\n"
      "s_cmp_eq_u32 s22, 1\n"
      "s_cbranch_scc1 .Lw_p1\n"
      "s_branch .Lw_pe\n"
      ".Lw_pr:\n"
      "s_add_u32 s22, s22, 2\n"
      "s_branch .Lw_pl\n"
      ".Lw_pe:\n"
      "s_mov_b64 exec, -1\n"
      "s_add_u32 s50, s38, 4294893568\n"
      "s_add_u32 s49, s38, 4294893568\n"
      "s_add_u32 s38, s38, 4294942720\n"
      "s_cmp_eq_u32 s36, s37\n"
      "s_cselect_b32 s48, 49152, s51\n"
      "s_add_u32 s36, s36, s48\n"
      "s_min_u32 s36, s36, s6\n"
      "buffer_load_dword v28, v11, s[8:11], s36 offen sc1\n"
      "s_mov_b32 s46, s36\n"
      "s_add_u32 s36, s36, 24576\n"
      "s_min_u32 s36, s36, s6\n"
      "s_waitcnt vmcnt(0)\n"
      "v_cmp_ne_u32 vcc, s21, v28\n"
      "s_add_u32 m0, s39, 0\n"
      "s_cbranch_vccnz .Lsp_w0_1\n"
      ".Lbk_w0_1:\n"
      "buffer_load_dword v28, v11, s[8:11], s36 offen sc1\n"
      "buffer_load_dwordx4 v0, s[8:11], s46 offen offset:0 sc1 lds\n"
      "buffer_load_dwordx4 v0, s[8:11], s46 offen offset:1024 sc1 lds\n"
      "buffer_load_dwordx4 v0, s[8:11], s46 offen offset:3072 sc1 lds\n"
      "buffer_load_dwordx4 v0, s[8:11], s46 offen offset:2048 sc1 lds\n"
      "s_add_u32 m0, s40, 0\n"
      "s_nop 0\n"
      "buffer_load_dwordx4 v1, s[8:11], s46 offen offset:0 sc1 lds\n"
      "s_sub_u32 s47, s46, s45\n"
      "buffer_load_dwordx2 v[200:201], v14, s[8:11], s47 offen sc1\n"
      "buffer_load_dwordx4 v[192:195], v13, s[8:11], s47 offen sc1\n"
      "buffer_load_dword v26, v23, s[8:11], s31 offen\n"
      "s_add_u32 m0, s39, 20480\n"
      "s_mov_b32 s46, s36\n"
      "s_waitcnt vmcnt(8)\n"
      "v_cmp_ne_u32 vcc, s21, v28\n"
      "buffer_load_dword v26, v23, s[8:11], s31 offen\n"
      "s_cmp_eq_u32 s36, s37\n"
      "s_cselect_b32 s48, 49152, s51\n"
      "s_add_u32 s36, s36, s48\n"
      "s_cbranch_vccnz .Lsp_w1_2\n"
      ".Lbk_w1_2:\n"
      "s_min_u32 s36, s36, s6\n"
      "buffer_load_dword v28, v11, s[8:11], s36 offen sc1\n"
      "buffer_load_dwordx4 v0, s[8:11], s46 offen offset:0 sc1 lds\n"
      "buffer_load_dwordx4 v0, s[8:11], s46 offen offset:1024 sc1 lds\n"
      "buffer_load_dwordx4 v0, s[8:11], s46 offen offset:3072 sc1 lds\n"
      "v_add_u32 v21, 1, v21\n"
      "s_waitcnt vmcnt(8)\n"
      "ds_write_b32 v23, v21\n"
      "buffer_load_dwordx4 v0, s[8:11], s46 offen offset:2048 sc1 lds\n"
      "s_add_u32 m0, s40, 20480\n"
      "buffer_load_dwordx4 v[196:199], v13, s[8:11], s47 offen offset:1024 sc1\n"
      "buffer_load_dwordx4 v1, s[8:11], s46 offen offset:0 sc1 lds\n"
      "ds_read_b32 v22, v24\n"
      "s_waitcnt lgkmcnt(0)\n"
      "v_cmp_gt_i32 vcc, v21, v22\n"
      "s_waitcnt vmcnt(10)\n"
      "v_and_b32 v189, 0xffff0000, v200\n"
      "v_lshlrev_b32 v188, 16, v200\n"
      "s_cbranch_vccnz .Lsp_wy0_3\n"
      ".Lbk_wy0_3:\n"
      "ds_read_b128 v[104:107], v0 offset:1024\n"
      "v_lshlrev_b32 v190, 16, v201\n"
      "ds_read_b128 v[100:103], v0 offset:0\n"
      "ds_read_b128 v[120:123], v0 offset:5120\n"
      "s_waitcnt vmcnt(9)\n"
      "v_lshlrev_b32 v178, 16, v195\n"
      "ds_read_b128 v[112:115], v0 offset:3072\n"
      "ds_read_b128 v[116:119], v0 offset:4096\n"
      "v_lshlrev_b32 v174, 16, v193\n"
      "ds_read_b128 v[124:127], v0 offset:6144\n"
      "s_sub_u32 s47, s46, s45\n"
      "ds_read_b128 v[108:111], v0 offset:2048\n"
      "v_and_b32 v191, 0xffff0000, v201\n"
      "v_and_b32 v177, 0xffff0000, v194\n"
      "ds_read_b128 v[136:139], v0 offset:8192\n"
      "v_lshlrev_b32 v176, 16, v194\n"
      "ds_read_b128 v[128:131], v0 offset:7168\n"
      "v_lshlrev_b32 v172, 16, v192\n"
      "v_and_b32 v175, 0xffff0000, v193\n"
      "buffer_load_dwordx2 v[200:201], v14, s[8:11], s47 offen sc1\n"
      "ds_read2_b32 v[132:133], v2 offset0:0 offset1:32\n"
      "v_and_b32 v173, 0xffff0000, v192\n"
      "ds_read2_b32 v[134:135], v2 offset0:64 offset1:96\n"
      "s_mov_b32 s20, 31\n"
      "s_waitcnt lgkmcnt(0)\n"
      "s_nop 4\n"
      "s_branch .Lw_loop\n"
      ".p2align 12\n"
      ".Lw_p0:\n"
      "s_branch .Lw_pr\n"
      ".p2align 5\n"
      ".Lw_loop:\n"
      "v_mfma_f32_16x16x32_bf16 v[188:191], v[64:67], v[100:103], v[188:191]\n"
      "ds_read_b128 v[140:143], v0 offset:9216\n"
      "v_and_b32 v179, 0xffff0000, v195\n"
      "v_mfma_f32_16x16x32_bf16 v[188:191], v[68:71], v[104:107], v[188:191]\n"
      "buffer_load_dwordx4 v[192:195], v13, s[8:11], s47 offen sc1\n"
      "v_mfma_f32_16x16x32_bf16 v[188:191], v[72:75], v[108:111], v[188:191]\n"
      "v_add_f32 v218, v218, v182\n"
      "v_mfma_f32_16x16x32_bf16 v[188:191], v[76:79], v[112:115], v[188:191]\n"
      "ds_read_b128 v[156:159], v0 offset:13312\n"
      "v_fmac_f32 v219, v184, v184\n"
      "v_mfma_f32_16x16x32_bf16 v[188:191], v[80:83], v[116:119], v[188:191]\n"
      "ds_read_b128 v[164:167], v0 offset:15360\n"
      "v_cvt_pk_bf16_f32 v213, v182, v183\n"
      "v_mfma_f32_16x16x32_bf16 v[188:191], v[84:87], v[120:123], v[188:191]\n"
      "ds_read_b128 v[144:147], v0 offset:10240\n"
      "v_mfma_f32_16x16x32_bf16 v[188:191], v[88:91], v[124:127], v[188:191]\n"
      "s_waitcnt lgkmcnt(4)\n"
      "v_cvt_pk_bf16_f32 v215, v186, v187\n"
      "v_add_f32 v218, v218, v183\n"
      "v_mfma_f32_16x16x32_bf16 v[188:191], v[92:95], v[128:131], v[188:191]\n"
      "buffer_store_dwordx4 v[208:211], v15, s[8:11], s38 offen sc1\n"
      "v_mfma_f32_16x16x32_bf16 v[188:191], v[96:99], v[132:135], v[188:191]\n"
      "v_cvt_pk_bf16_f32 v212, v180, v181\n"
      "v_add_f32 v218, v218, v184\n"
      "v_mfma_f32_16x16x32_bf16 v[172:175], a[0:3], v[100:103], v[172:175]\n"
      "s_add_u32 s50, s50, 24576\n"
      "v_cvt_pk_bf16_f32 v214, v184, v185\n"
      "v_add_f32 v218, v218, v185\n"
      "v_mfma_f32_16x16x32_bf16 v[172:175], a[4:7], v[104:107], v[172:175]\n"
      "s_mov_b32 s46, s36\n"
      "v_add_f32 v218, v218, v186\n"
      "v_mfma_f32_16x16x32_bf16 v[172:175], a[8:11], v[108:111], v[172:175]\n"
      "ds_read_b128 v[148:151], v0 offset:11264\n"
      "v_add_f32 v218, v218, v187\n"
      "v_mfma_f32_16x16x32_bf16 v[172:175], a[12:15], v[112:115], v[172:175]\n"
      "v_fmac_f32 v219, v185, v185\n"
      "v_add_u32 v21, 1, v21\n"
      "v_mfma_f32_16x16x32_bf16 v[172:175], a[16:19], v[116:119], v[172:175]\n"
      "s_add_u32 s36, s36, 24576\n"
      "ds_read_b128 v[152:155], v0 offset:12288\n"
      "v_fmac_f32 v219, v186, v186\n"
      "v_mfma_f32_16x16x32_bf16 v[172:175], a[20:23], v[120:123], v[172:175]\n"
      "buffer_store_dwordx2 v[216:217], v16, s[8:11], s38 offen sc1\n"
      "v_mfma_f32_16x16x32_bf16 v[172:175], a[24:27], v[124:127], v[172:175]\n"
      "ds_read_b128 v[160:163], v0 offset:14336\n"
      "v_mfma_f32_16x16x32_bf16 v[172:175], a[28:31], v[128:131], v[172:175]\n"
      "ds_read2_b32 v[170:171], v3 offset0:64 offset1:96\n"
      "v_mfma_f32_16x16x32_bf16 v[172:175], a[32:35], v[132:135], v[172:175]\n"
      "ds_read2_b32 v[168:169], v3 offset0:0 offset1:32\n"
      "s_min_u32 s36, s36, s6\n"
      "v_mfma_f32_16x16x32_bf16 v[172:175], a[36:39], v[136:139], v[172:175]\n"
      "s_waitcnt lgkmcnt(4)\n"
      "v_mfma_f32_16x16x32_bf16 v[172:175], a[40:43], v[140:143], v[172:175]\n"
      "buffer_store_dwordx4 v[212:215], v15, s[8:11], s38 offen offset:1024 sc1\n"
      "v_mfma_f32_16x16x32_bf16 v[172:175], a[44:47], v[144:147], v[172:175]\n"
      "ds_write_b32 v18, v218\n"
      "v_mfma_f32_16x16x32_bf16 v[172:175], a[48:51], v[148:151], v[172:175]\n"
      "s_waitcnt vmcnt(11) lgkmcnt(4)\n"
      "v_fmac_f32_e64 v219, v187, v187\n"
      "v_cmp_ne_u32 vcc, s21, v28\n"
      "v_mfma_f32_16x16x32_bf16 v[172:175], a[52:55], v[152:155], v[172:175]\n"
      "ds_write_b32 v18, v219 offset:4\n"
      "v_mfma_f32_16x16x32_bf16 v[172:175], a[56:59], v[156:159], v[172:175]\n"
      "s_waitcnt lgkmcnt(3)\n"
      "s_add_u32 m0, s39, 40960\n"
      "v_mfma_f32_16x16x32_bf16 v[172:175], a[60:63], v[160:163], v[172:175]\n"
      "ds_write_b32 v23, v21\n"
      "v_mfma_f32_16x16x32_bf16 v[172:175], a[64:67], v[164:167], v[172:175]\n"
      "s_waitcnt lgkmcnt(3)\n"
      "s_cbranch_vccnz .Lsp_w0_4\n"
      ".Lbk_w0_4:\n"
      "v_mfma_f32_16x16x32_bf16 v[172:175], a[68:71], v[168:171], v[172:175]\n"
      "v_mfma_f32_16x16x32_bf16 v[176:179], a[72:75], v[100:103], v[176:179]\n"
      "buffer_load_dword v28, v11, s[8:11], s36 offen sc1\n"
      "v_mfma_f32_16x16x32_bf16 v[176:179], a[76:79], v[104:107], v[176:179]\n"
      "s_waitcnt vmcnt(7) lgkmcnt(2)\n"
      "v_lshlrev_b32 v186, 16, v199\n"
      "v_lshlrev_b32 v184, 16, v198\n"
      "v_mfma_f32_16x16x32_bf16 v[176:179], a[80:83], v[108:111], v[176:179]\n"
      "v_lshlrev_b32 v180, 16, v196\n"
      "v_and_b32 v183, 0xffff0000, v197\n"
      "v_mfma_f32_16x16x32_bf16 v[176:179], a[84:87], v[112:115], v[176:179]\n"
      "buffer_store_dword v25, v12, s[8:11], s50 offen sc1\n"
      "v_mfma_f32_16x16x32_bf16 v[176:179], a[88:91], v[116:119], v[176:179]\n"
      "v_lshlrev_b32 v182, 16, v197\n"
      "v_and_b32 v181, 0xffff0000, v196\n"
      "v_mfma_f32_16x16x32_bf16 v[176:179], a[92:95], v[120:123], v[176:179]\n"
      "v_and_b32 v187, 0xffff0000, v199\n"
      "v_and_b32 v185, 0xffff0000, v198\n"
      "v_mfma_f32_16x16x32_bf16 v[176:179], a[96:99], v[124:127], v[176:179]\n"
      "buffer_load_dwordx4 v[196:199], v13, s[8:11], s47 offen offset:1024 sc1\n"
      "v_mfma_f32_16x16x32_bf16 v[176:179], a[100:103], v[128:131], v[176:179]\n"
      "ds_read_b32 v22, v24\n"
      "v_mul_f32 v219, v172, v172\n"
      "v_mfma_f32_16x16x32_bf16 v[176:179], a[104:107], v[132:135], v[176:179]\n"
      "v_mfma_f32_16x16x32_bf16 v[176:179], a[108:111], v[136:139], v[176:179]\n"
      "v_mfma_f32_16x16x32_bf16 v[176:179], a[112:115], v[140:143], v[176:179]\n"
      "buffer_load_dwordx4 v0, s[8:11], s46 offen offset:0 sc1 lds\n"
      "v_mfma_f32_16x16x32_bf16 v[176:179], a[116:119], v[144:147], v[176:179]\n"
      "v_add_f32 v218, v172, v173\n"
      "v_fmac_f32 v219, v173, v173\n"
      "v_mfma_f32_16x16x32_bf16 v[176:179], a[120:123], v[148:151], v[176:179]\n"
      "s_waitcnt lgkmcnt(0)\n"
      "v_cmp_gt_i32 vcc, v21, v22\n"
      "v_mfma_f32_16x16x32_bf16 v[176:179], a[124:127], v[152:155], v[176:179]\n"
      "v_mfma_f32_16x16x32_bf16 v[176:179], a[128:131], v[156:159], v[176:179]\n"
      "buffer_load_dwordx4 v0, s[8:11], s46 offen offset:1024 sc1 lds\n"
      "v_mfma_f32_16x16x32_bf16 v[176:179], a[132:135], v[160:163], v[176:179]\n"
      "s_cbranch_vccnz .Lsp_wz0_5\n"
      ".Lbk_wz0_5:\n"
      "ds_read2st64_b32 v[204:205], v17 offset1:1\n"
      "v_mfma_f32_16x16x32_bf16 v[176:179], a[136:139], v[164:167], v[176:179]\n"
      "ds_read2st64_b32 v[206:207], v17 offset0:2 offset1:3\n"
      "v_add_u32 v21, 1, v21\n"
      "v_mfma_f32_16x16x32_bf16 v[176:179], a[140:143], v[168:171], v[176:179]\n"
      "s_waitcnt vmcnt(10)\n"
      "ds_write_b32 v23, v21\n"
      "v_mfma_f32_16x16x32_bf16 v[180:183], a[144:147], v[100:103], v[180:183]\n"
      "buffer_load_dwordx4 v0, s[8:11], s46 offen offset:3072 sc1 lds\n"
      "v_mfma_f32_16x16x32_bf16 v[180:183], a[148:151], v[104:107], v[180:183]\n"
      "v_add_f32 v218, v218, v174\n"
      "v_fmac_f32 v219, v174, v174\n"
      "v_mfma_f32_16x16x32_bf16 v[180:183], a[152:155], v[108:111], v[180:183]\n"
      "v_fmac_f32 v219, v175, v175\n"
      "v_fmac_f32 v219, v176, v176\n"
      "v_mfma_f32_16x16x32_bf16 v[180:183], a[156:159], v[112:115], v[180:183]\n"
      "v_add_f32 v218, v218, v175\n"
      "v_add_f32 v218, v218, v176\n"
      "v_mfma_f32_16x16x32_bf16 v[180:183], a[160:163], v[116:119], v[180:183]\n"
      "buffer_load_dwordx4 v0, s[8:11], s46 offen offset:2048 sc1 lds\n"
      "v_mfma_f32_16x16x32_bf16 v[180:183], a[164:167], v[120:123], v[180:183]\n"
      "s_add_u32 m0, s40, 40960\n"
      "v_add_f32 v218, v218, v177\n"
      "v_cvt_pk_bf16_f32 v209, v174, v175\n"
      "v_mfma_f32_16x16x32_bf16 v[180:183], a[168:171], v[124:127], v[180:183]\n"
      "s_waitcnt lgkmcnt(0)\n"
      "v_add_f32 v188, v188, v204\n"
      "v_add_f32 v190, v190, v206\n"
      "v_mfma_f32_16x16x32_bf16 v[180:183], a[172:175], v[128:131], v[180:183]\n"
      "v_cvt_pk_bf16_f32 v208, v172, v173\n"
      "v_fmac_f32 v219, v177, v177\n"
      "v_mfma_f32_16x16x32_bf16 v[180:183], a[176:179], v[132:135], v[180:183]\n"
      "buffer_load_dwordx4 v1, s[8:11], s46 offen offset:0 sc1 lds\n"
      "v_mfma_f32_16x16x32_bf16 v[180:183], a[180:183], v[136:139], v[180:183]\n"
      "s_add_u32 s38, s38, 24576\n"
      "v_cvt_pk_bf16_f32 v210, v176, v177\n"
      "v_mfma_f32_16x16x32_bf16 v[180:183], a[184:187], v[140:143], v[180:183]\n"
      "ds_read_b32 v22, v24\n"
      "v_add_f32 v218, v218, v178\n"
      "v_mfma_f32_16x16x32_bf16 v[180:183], a[188:191], v[144:147], v[180:183]\n"
      "v_add_f32 v218, v218, v179\n"
      "v_add_f32 v218, v218, v188\n"
      "v_mfma_f32_16x16x32_bf16 v[180:183], a[192:195], v[148:151], v[180:183]\n"
      "s_add_u32 s50, s50, 24576\n"
      "v_fmac_f32 v219, v178, v178\n"
      "v_fmac_f32 v219, v179, v179\n"
      "v_mfma_f32_16x16x32_bf16 v[180:183], a[196:199], v[152:155], v[180:183]\n"
      "v_add_f32 v191, v191, v207\n"
      "v_cvt_pk_bf16_f32 v211, v178, v179\n"
      "v_mfma_f32_16x16x32_bf16 v[180:183], a[200:203], v[156:159], v[180:183]\n"
      "v_fmac_f32 v219, v188, v188\n"
      "v_add_f32 v189, v189, v205\n"
      "v_mfma_f32_16x16x32_bf16 v[180:183], a[204:207], v[160:163], v[180:183]\n"
      "s_waitcnt lgkmcnt(0)\n"
      "v_fmac_f32 v219, v189, v189\n"
      "v_cmp_gt_i32 vcc, v21, v22\n"
      "v_mfma_f32_16x16x32_bf16 v[180:183], a[208:211], v[164:167], v[180:183]\n"
      "v_fmac_f32 v219, v190, v190\n"
      "v_add_f32 v218, v218, v189\n"
      "v_mfma_f32_16x16x32_bf16 v[180:183], a[212:215], v[168:171], v[180:183]\n"
      "s_waitcnt vmcnt(11)\n"
      "v_lshlrev_b32 v172, 16, v192\n"
      "v_mfma_f32_16x16x32_bf16 v[184:187], a[216:219], v[100:103], v[184:187]\n"
      "v_and_b32 v175, 0xffff0000, v193\n"
      "v_and_b32 v177, 0xffff0000, v194\n"
      "v_mfma_f32_16x16x32_bf16 v[184:187], a[220:223], v[104:107], v[184:187]\n"
      "s_cbranch_vccnz .Lsp_wy1_6\n"
      ".Lbk_wy1_6:\n"
      "v_add_f32 v218, v218, v190\n"
      "v_mfma_f32_16x16x32_bf16 v[184:187], a[224:227], v[108:111], v[184:187]\n"
      "ds_read_b128 v[104:107], v0 offset:21504\n"
      "v_cvt_pk_bf16_f32 v216, v188, v189\n"
      "v_mfma_f32_16x16x32_bf16 v[184:187], a[228:231], v[112:115], v[184:187]\n"
      "v_add_f32 v218, v218, v191\n"
      "v_add_f32 v218, v218, v180\n"
      "v_mfma_f32_16x16x32_bf16 v[184:187], a[232:235], v[116:119], v[184:187]\n"
      "ds_read_b128 v[116:119], v0 offset:24576\n"
      "v_cvt_pk_bf16_f32 v217, v190, v191\n"
      "v_mfma_f32_16x16x32_bf16 v[184:187], a[236:239], v[120:123], v[184:187]\n"
      "ds_read_b128 v[108:111], v0 offset:22528\n"
      "v_lshlrev_b32 v188, 16, v200\n"
      "v_mfma_f32_16x16x32_bf16 v[184:187], a[240:243], v[124:127], v[184:187]\n"
      "ds_read_b128 v[100:103], v0 offset:20480\n"
      "v_fmac_f32 v219, v191, v191\n"
      "v_mfma_f32_16x16x32_bf16 v[184:187], a[244:247], v[128:131], v[184:187]\n"
      "v_lshlrev_b32 v190, 16, v201\n"
      "v_and_b32 v191, 0xffff0000, v201\n"
      "v_mfma_f32_16x16x32_bf16 v[184:187], a[248:251], v[132:135], v[184:187]\n"
      "ds_read2_b32 v[134:135], v4 offset0:64 offset1:96\n"
      "v_lshlrev_b32 v174, 16, v193\n"
      "v_mfma_f32_16x16x32_bf16 v[184:187], a[252:255], v[136:139], v[184:187]\n"
      "s_sub_u32 s47, s46, s45\n"
      "v_and_b32 v189, 0xffff0000, v200\n"
      "v_fmac_f32 v219, v180, v180\n"
      "v_mfma_f32_16x16x32_bf16 v[184:187], v[32:35], v[140:143], v[184:187]\n"
      "buffer_store_dwordx4 v[208:211], v15, s[8:11], s38 offen sc1\n"
      "v_mfma_f32_16x16x32_bf16 v[184:187], v[36:39], v[144:147], v[184:187]\n"
      "ds_read_b128 v[136:139], v0 offset:28672\n"
      "v_fmac_f32 v219, v181, v181\n"
      "v_mfma_f32_16x16x32_bf16 v[184:187], v[40:43], v[148:151], v[184:187]\n"
      "ds_read2_b32 v[132:133], v4 offset0:0 offset1:32\n"
      "v_lshlrev_b32 v178, 16, v195\n"
      "v_mfma_f32_16x16x32_bf16 v[184:187], v[44:47], v[152:155], v[184:187]\n"
      "ds_read_b128 v[112:115], v0 offset:23552\n"
      "v_add_f32 v218, v218, v181\n"
      "v_mfma_f32_16x16x32_bf16 v[184:187], v[48:51], v[156:159], v[184:187]\n"
      "ds_read_b128 v[120:123], v0 offset:25600\n"
      "v_and_b32 v173, 0xffff0000, v192\n"
      "v_mfma_f32_16x16x32_bf16 v[184:187], v[52:55], v[160:163], v[184:187]\n"
      "ds_read_b128 v[124:127], v0 offset:26624\n"
      "v_lshlrev_b32 v176, 16, v194\n"
      "v_mfma_f32_16x16x32_bf16 v[184:187], v[56:59], v[164:167], v[184:187]\n"
      "buffer_load_dwordx2 v[200:201], v14, s[8:11], s47 offen sc1\n"
      "v_mfma_f32_16x16x32_bf16 v[184:187], v[60:63], v[168:171], v[184:187]\n"
      "s_waitcnt lgkmcnt(5)\n"
      "ds_read_b128 v[128:131], v0 offset:27648\n"
      "v_add_f32 v218, v218, v182\n"
      "v_mfma_f32_16x16x32_bf16 v[188:191], v[64:67], v[100:103], v[188:191]\n"
      "ds_read_b128 v[140:143], v0 offset:29696\n"
      "v_cvt_pk_bf16_f32 v213, v182, v183\n"
      "v_mfma_f32_16x16x32_bf16 v[188:191], v[68:71], v[104:107], v[188:191]\n"
      "ds_read_b128 v[152:155], v0 offset:32768\n"
      "v_mfma_f32_16x16x32_bf16 v[188:191], v[72:75], v[108:111], v[188:191]\n"
      "s_waitcnt lgkmcnt(5)\n"
      "buffer_store_dwordx2 v[216:217], v16, s[8:11], s38 offen sc1\n"
      "v_mfma_f32_16x16x32_bf16 v[188:191], v[76:79], v[112:115], v[188:191]\n"
      "ds_read_b128 v[144:147], v0 offset:30720\n"
      "v_cvt_pk_bf16_f32 v212, v180, v181\n"
      "v_mfma_f32_16x16x32_bf16 v[188:191], v[80:83], v[116:119], v[188:191]\n"
      "s_waitcnt lgkmcnt(4)\n"
      "ds_read_b128 v[164:167], v0 offset:35840\n"
      "v_add_f32 v218, v218, v183\n"
      "v_mfma_f32_16x16x32_bf16 v[188:191], v[84:87], v[120:123], v[188:191]\n"
      "v_fmac_f32 v219, v182, v182\n"
      "v_add_f32 v218, v218, v184\n"
      "v_mfma_f32_16x16x32_bf16 v[188:191], v[88:91], v[124:127], v[188:191]\n"
      "s_waitcnt lgkmcnt(4)\n"
      "ds_read_b128 v[148:151], v0 offset:31744\n"
      "v_add_f32 v218, v218, v185\n"
      "v_mfma_f32_16x16x32_bf16 v[188:191], v[92:95], v[128:131], v[188:191]\n"
      "ds_read_b128 v[160:163], v0 offset:34816\n"
      "v_and_b32 v179, 0xffff0000, v195\n"
      "v_mfma_f32_16x16x32_bf16 v[188:191], v[96:99], v[132:135], v[188:191]\n"
      "buffer_load_dwordx4 v[192:195], v13, s[8:11], s47 offen sc1\n"
      "v_mfma_f32_16x16x32_bf16 v[172:175], a[0:3], v[100:103], v[172:175]\n"
      "v_cvt_pk_bf16_f32 v215, v186, v187\n"
      "v_fmac_f32 v219, v183, v183\n"
      "v_mfma_f32_16x16x32_bf16 v[172:175], a[4:7], v[104:107], v[172:175]\n"
      "v_cvt_pk_bf16_f32 v214, v184, v185\n"
      "v_mfma_f32_16x16x32_bf16 v[172:175], a[8:11], v[108:111], v[172:175]\n"
      "ds_read2_b32 v[168:169], v5 offset0:0 offset1:32\n"
      "v_mfma_f32_16x16x32_bf16 v[172:175], a[12:15], v[112:115], v[172:175]\n"
      "ds_read_b128 v[156:159], v0 offset:33792\n"
      "v_mfma_f32_16x16x32_bf16 v[172:175], a[16:19], v[116:119], v[172:175]\n"
      "buffer_store_dwordx4 v[212:215], v15, s[8:11], s38 offen offset:1024 sc1\n"
      "v_mfma_f32_16x16x32_bf16 v[172:175], a[20:23], v[120:123], v[172:175]\n"
      "v_fmac_f32 v219, v184, v184\n"
      "v_fmac_f32 v219, v185, v185\n"
      "v_mfma_f32_16x16x32_bf16 v[172:175], a[24:27], v[124:127], v[172:175]\n"
      "ds_read2_b32 v[170:171], v5 offset0:64 offset1:96\n"
      "v_add_f32 v218, v218, v186\n"
      "v_mfma_f32_16x16x32_bf16 v[172:175], a[28:31], v[128:131], v[172:175]\n"
      "v_fmac_f32 v219, v186, v186\n"
      "v_add_f32 v218, v218, v187\n"
      "v_mfma_f32_16x16x32_bf16 v[172:175], a[32:35], v[132:135], v[172:175]\n"
      "ds_write_b32 v18, v218\n"
      "v_mfma_f32_16x16x32_bf16 v[172:175], a[36:39], v[136:139], v[172:175]\n"
      "s_waitcnt lgkmcnt(3)\n"
      "s_add_u32 m0, s39, 61440\n"
      "v_add_u32 v21, 1, v21\n"
      "v_mfma_f32_16x16x32_bf16 v[172:175], a[40:43], v[140:143], v[172:175]\n"
      "s_mov_b32 s46, s36\n"
      "v_mfma_f32_16x16x32_bf16 v[172:175], a[44:47], v[144:147], v[172:175]\n"
      "v_fmac_f32 v219, v187, v187\n"
      "v_mfma_f32_16x16x32_bf16 v[172:175], a[48:51], v[148:151], v[172:175]\n"
      "ds_write_b32 v18, v219 offset:4\n"
      "v_mfma_f32_16x16x32_bf16 v[172:175], a[52:55], v[152:155], v[172:175]\n"
      "s_waitcnt vmcnt(10) lgkmcnt(2)\n"
      "v_and_b32 v181, 0xffff0000, v196\n"
      "v_mfma_f32_16x16x32_bf16 v[172:175], a[56:59], v[156:159], v[172:175]\n"
      "ds_write_b32 v23, v21\n"
      "v_mfma_f32_16x16x32_bf16 v[172:175], a[60:63], v[160:163], v[172:175]\n"
      "v_lshlrev_b32 v186, 16, v199\n"
      "v_cmp_ne_u32 vcc, s21, v28\n"
      "v_mfma_f32_16x16x32_bf16 v[172:175], a[64:67], v[164:167], v[172:175]\n"
      "buffer_store_dword v25, v12, s[8:11], s50 offen sc1\n"
      "v_mfma_f32_16x16x32_bf16 v[172:175], a[68:71], v[168:171], v[172:175]\n"
      "s_cmp_eq_u32 s36, s37\n"
      "s_cselect_b32 s48, 49152, s51\n"
      "s_add_u32 s36, s36, s48\n"
      "v_mfma_f32_16x16x32_bf16 v[176:179], a[72:75], v[100:103], v[176:179]\n"
      "s_cbranch_vccnz .Lsp_w1_7\n"
      ".Lbk_w1_7:\n"
      "s_min_u32 s36, s36, s6\n"
      "v_mfma_f32_16x16x32_bf16 v[176:179], a[76:79], v[104:107], v[176:179]\n"
      "buffer_load_dword v28, v11, s[8:11], s36 offen sc1\n"
      "v_mfma_f32_16x16x32_bf16 v[176:179], a[80:83], v[108:111], v[176:179]\n"
      "v_and_b32 v183, 0xffff0000, v197\n"
      "v_mul_f32 v219, v172, v172\n"
      "v_mfma_f32_16x16x32_bf16 v[176:179], a[84:87], v[112:115], v[176:179]\n"
      "buffer_load_dwordx4 v0, s[8:11], s46 offen offset:0 sc1 lds\n"
      "v_mfma_f32_16x16x32_bf16 v[176:179], a[88:91], v[116:119], v[176:179]\n"
      "v_mfma_f32_16x16x32_bf16 v[176:179], a[92:95], v[120:123], v[176:179]\n"
      "v_fmac_f32 v219, v173, v173\n"
      "v_mfma_f32_16x16x32_bf16 v[176:179], a[96:99], v[124:127], v[176:179]\n"
      "v_lshlrev_b32 v184, 16, v198\n"
      "v_and_b32 v185, 0xffff0000, v198\n"
      "v_mfma_f32_16x16x32_bf16 v[176:179], a[100:103], v[128:131], v[176:179]\n"
      "ds_read_b32 v22, v24\n"
      "v_fmac_f32 v219, v174, v174\n"
      "v_mfma_f32_16x16x32_bf16 v[176:179], a[104:107], v[132:135], v[176:179]\n"
      "buffer_load_dwordx4 v0, s[8:11], s46 offen offset:1024 sc1 lds\n"
      "v_mfma_f32_16x16x32_bf16 v[176:179], a[108:111], v[136:139], v[176:179]\n"
      "v_lshlrev_b32 v182, 16, v197\n"
      "v_add_f32 v218, v172, v173\n"
      "v_mfma_f32_16x16x32_bf16 v[176:179], a[112:115], v[140:143], v[176:179]\n"
      "v_add_f32 v218, v218, v174\n"
      "v_fmac_f32 v219, v175, v175\n"
      "v_mfma_f32_16x16x32_bf16 v[176:179], a[116:119], v[144:147], v[176:179]\n"
      "v_cvt_pk_bf16_f32 v208, v172, v173\n"
      "v_mfma_f32_16x16x32_bf16 v[176:179], a[120:123], v[148:151], v[176:179]\n"
      "s_waitcnt lgkmcnt(0)\n"
      "v_and_b32 v187, 0xffff0000, v199\n"
      "v_cmp_gt_i32 vcc, v21, v22\n"
      "v_mfma_f32_16x16x32_bf16 v[176:179], a[124:127], v[152:155], v[176:179]\n"
      "buffer_load_dwordx4 v0, s[8:11], s46 offen offset:3072 sc1 lds\n"
      "v_mfma_f32_16x16x32_bf16 v[176:179], a[128:131], v[156:159], v[176:179]\n"
      "v_lshlrev_b32 v180, 16, v196\n"
      "v_add_f32 v218, v218, v175\n"
      "v_mfma_f32_16x16x32_bf16 v[176:179], a[132:135], v[160:163], v[176:179]\n"
      "s_cbranch_vccnz .Lsp_wz1_8\n"
      ".Lbk_wz1_8:\n"
      "ds_read2st64_b32 v[204:205], v17 offset1:1\n"
      "v_mfma_f32_16x16x32_bf16 v[176:179], a[136:139], v[164:167], v[176:179]\n"
      "ds_read2st64_b32 v[206:207], v17 offset0:2 offset1:3\n"
      "v_add_u32 v21, 1, v21\n"
      "v_mfma_f32_16x16x32_bf16 v[176:179], a[140:143], v[168:171], v[176:179]\n"
      "s_waitcnt vmcnt(10)\n"
      "ds_write_b32 v23, v21\n"
      "v_mfma_f32_16x16x32_bf16 v[180:183], a[144:147], v[100:103], v[180:183]\n"
      "buffer_load_dwordx4 v0, s[8:11], s46 offen offset:2048 sc1 lds\n"
      "v_mfma_f32_16x16x32_bf16 v[180:183], a[148:151], v[104:107], v[180:183]\n"
      "v_mfma_f32_16x16x32_bf16 v[180:183], a[152:155], v[108:111], v[180:183]\n"
      "s_add_u32 m0, s40, 61440\n"
      "v_add_f32 v218, v218, v176\n"
      "v_mfma_f32_16x16x32_bf16 v[180:183], a[156:159], v[112:115], v[180:183]\n"
      "v_fmac_f32 v219, v176, v176\n"
      "v_cvt_pk_bf16_f32 v209, v174, v175\n"
      "v_mfma_f32_16x16x32_bf16 v[180:183], a[160:163], v[116:119], v[180:183]\n"
      "buffer_load_dwordx4 v[196:199], v13, s[8:11], s47 offen offset:1024 sc1\n"
      "v_mfma_f32_16x16x32_bf16 v[180:183], a[164:167], v[120:123], v[180:183]\n"
      "s_waitcnt lgkmcnt(1)\n"
      "v_cvt_pk_bf16_f32 v211, v178, v179\n"
      "v_add_f32 v190, v190, v206\n"
      "v_mfma_f32_16x16x32_bf16 v[180:183], a[168:171], v[124:127], v[180:183]\n"
      "v_add_f32 v218, v218, v177\n"
      "v_add_f32 v218, v218, v178\n"
      "v_mfma_f32_16x16x32_bf16 v[180:183], a[172:175], v[128:131], v[180:183]\n"
      "v_add_f32 v218, v218, v179\n"
      "v_add_f32 v189, v189, v205\n"
      "v_mfma_f32_16x16x32_bf16 v[180:183], a[176:179], v[132:135], v[180:183]\n"
      "buffer_load_dwordx4 v1, s[8:11], s46 offen offset:0 sc1 lds\n"
      "v_mfma_f32_16x16x32_bf16 v[180:183], a[180:183], v[136:139], v[180:183]\n"
      "v_add_f32 v188, v188, v204\n"
      "v_fmac_f32 v219, v177, v177\n"
      "v_mfma_f32_16x16x32_bf16 v[180:183], a[184:187], v[140:143], v[180:183]\n"
      "ds_read_b32 v22, v24\n"
      "v_fmac_f32 v219, v178, v178\n"
      "v_mfma_f32_16x16x32_bf16 v[180:183], a[188:191], v[144:147], v[180:183]\n"
      "v_cvt_pk_bf16_f32 v210, v176, v177\n"
      "v_fmac_f32 v219, v179, v179\n"
      "v_mfma_f32_16x16x32_bf16 v[180:183], a[192:195], v[148:151], v[180:183]\n"
      "v_add_f32 v191, v191, v207\n"
      "v_fmac_f32 v219, v188, v188\n"
      "v_mfma_f32_16x16x32_bf16 v[180:183], a[196:199], v[152:155], v[180:183]\n"
      "v_add_f32 v218, v218, v188\n"
      "v_mfma_f32_16x16x32_bf16 v[180:183], a[200:203], v[156:159], v[180:183]\n"
      "v_add_f32 v218, v218, v189\n"
      "v_add_f32 v218, v218, v190\n"
      "v_mfma_f32_16x16x32_bf16 v[180:183], a[204:207], v[160:163], v[180:183]\n"
      "s_waitcnt lgkmcnt(0)\n"
      "v_fmac_f32 v219, v189, v189\n"
      "v_cmp_gt_i32 vcc, v21, v22\n"
      "v_mfma_f32_16x16x32_bf16 v[180:183], a[208:211], v[164:167], v[180:183]\n"
      "v_cvt_pk_bf16_f32 v216, v188, v189\n"
      "v_fmac_f32 v219, v190, v190\n"
      "v_mfma_f32_16x16x32_bf16 v[180:183], a[212:215], v[168:171], v[180:183]\n"
      "s_waitcnt vmcnt(11)\n"
      "v_and_b32 v189, 0xffff0000, v200\n"
      "v_fmac_f32 v219, v191, v191\n"
      "v_mfma_f32_16x16x32_bf16 v[184:187], a[216:219], v[100:103], v[184:187]\n"
      "s_add_u32 s38, s38, 24576\n"
      "v_cvt_pk_bf16_f32 v217, v190, v191\n"
      "v_lshlrev_b32_e64 v188, 16, v200\n"
      "v_mfma_f32_16x16x32_bf16 v[184:187], a[220:223], v[104:107], v[184:187]\n"
      "s_cbranch_vccnz .Lsp_wy0_9\n"
      ".Lbk_wy0_9:\n"
      "v_mfma_f32_16x16x32_bf16 v[184:187], a[224:227], v[108:111], v[184:187]\n"
      "ds_read_b128 v[104:107], v10 offset:1024\n"
      "v_add_f32 v218, v218, v191\n"
      "v_mfma_f32_16x16x32_bf16 v[184:187], a[228:231], v[112:115], v[184:187]\n"
      "v_fmac_f32 v219, v180, v180\n"
      "v_lshlrev_b32 v190, 16, v201\n"
      "v_mfma_f32_16x16x32_bf16 v[184:187], a[232:235], v[116:119], v[184:187]\n"
      "ds_read_b128 v[100:103], v10 offset:0\n"
      "v_add_f32 v218, v218, v180\n"
      "v_mfma_f32_16x16x32_bf16 v[184:187], a[236:239], v[120:123], v[184:187]\n"
      "s_waitcnt vmcnt(9)\n"
      "ds_read_b128 v[120:123], v10 offset:5120\n"
      "v_lshlrev_b32 v178, 16, v195\n"
      "v_mfma_f32_16x16x32_bf16 v[184:187], a[240:243], v[124:127], v[184:187]\n"
      "ds_read_b128 v[112:115], v10 offset:3072\n"
      "v_fmac_f32 v219, v181, v181\n"
      "v_mfma_f32_16x16x32_bf16 v[184:187], a[244:247], v[128:131], v[184:187]\n"
      "ds_read_b128 v[116:119], v10 offset:4096\n"
      "v_lshlrev_b32 v174, 16, v193\n"
      "v_mfma_f32_16x16x32_bf16 v[184:187], a[248:251], v[132:135], v[184:187]\n"
      "ds_read_b128 v[124:127], v10 offset:6144\n"
      "v_fmac_f32 v219, v182, v182\n"
      "v_mfma_f32_16x16x32_bf16 v[184:187], a[252:255], v[136:139], v[184:187]\n"
      "s_sub_u32 s47, s46, s45\n"
      "ds_read_b128 v[108:111], v10 offset:2048\n"
      "v_mfma_f32_16x16x32_bf16 v[184:187], v[32:35], v[140:143], v[184:187]\n"
      "v_and_b32 v191, 0xffff0000, v201\n"
      "v_and_b32 v177, 0xffff0000, v194\n"
      "v_mfma_f32_16x16x32_bf16 v[184:187], v[36:39], v[144:147], v[184:187]\n"
      "ds_read_b128 v[136:139], v10 offset:8192\n"
      "v_lshlrev_b32 v176, 16, v194\n"
      "v_mfma_f32_16x16x32_bf16 v[184:187], v[40:43], v[148:151], v[184:187]\n"
      "ds_read_b128 v[128:131], v10 offset:7168\n"
      "v_lshlrev_b32 v172, 16, v192\n"
      "v_mfma_f32_16x16x32_bf16 v[184:187], v[44:47], v[152:155], v[184:187]\n"
      "v_fmac_f32 v219, v183, v183\n"
      "v_and_b32 v175, 0xffff0000, v193\n"
      "v_mfma_f32_16x16x32_bf16 v[184:187], v[48:51], v[156:159], v[184:187]\n"
      "buffer_load_dwordx2 v[200:201], v14, s[8:11], s47 offen sc1\n"
      "v_mfma_f32_16x16x32_bf16 v[184:187], v[52:55], v[160:163], v[184:187]\n"
      "ds_read2_b32 v[132:133], v6 offset0:0 offset1:32\n"
      "v_and_b32 v173, 0xffff0000, v192\n"
      "v_mfma_f32_16x16x32_bf16 v[184:187], v[56:59], v[164:167], v[184:187]\n"
      "s_waitcnt lgkmcnt(3)\n"
      "ds_read2_b32 v[134:135], v6 offset0:64 offset1:96\n"
      "v_add_f32 v218, v218, v181\n"
      "v_mfma_f32_16x16x32_bf16 v[184:187], v[60:63], v[168:171], v[184:187]\n"
      "s_sub_u32 s20, s20, 1\n"
      "s_cbranch_scc1 .Lw_tail\n"
      "v_mfma_f32_16x16x32_bf16 v[188:191], v[64:67], v[100:103], v[188:191]\n"
      "ds_read_b128 v[140:143], v10 offset:9216\n"
      "v_and_b32 v179, 0xffff0000, v195\n"
      "v_mfma_f32_16x16x32_bf16 v[188:191], v[68:71], v[104:107], v[188:191]\n"
      "buffer_load_dwordx4 v[192:195], v13, s[8:11], s47 offen sc1\n"
      "v_mfma_f32_16x16x32_bf16 v[188:191], v[72:75], v[108:111], v[188:191]\n"
      "v_add_f32 v218, v218, v182\n"
      "v_mfma_f32_16x16x32_bf16 v[188:191], v[76:79], v[112:115], v[188:191]\n"
      "ds_read_b128 v[156:159], v10 offset:13312\n"
      "v_fmac_f32 v219, v184, v184\n"
      "v_mfma_f32_16x16x32_bf16 v[188:191], v[80:83], v[116:119], v[188:191]\n"
      "ds_read_b128 v[164:167], v10 offset:15360\n"
      "v_cvt_pk_bf16_f32 v213, v182, v183\n"
      "v_mfma_f32_16x16x32_bf16 v[188:191], v[84:87], v[120:123], v[188:191]\n"
      "ds_read_b128 v[144:147], v10 offset:10240\n"
      "v_mfma_f32_16x16x32_bf16 v[188:191], v[88:91], v[124:127], v[188:191]\n"
      "s_waitcnt lgkmcnt(4)\n"
      "v_cvt_pk_bf16_f32 v215, v186, v187\n"
      "v_add_f32 v218, v218, v183\n"
      "v_mfma_f32_16x16x32_bf16 v[188:191], v[92:95], v[128:131], v[188:191]\n"
      "buffer_store_dwordx4 v[208:211], v15, s[8:11], s38 offen sc1\n"
      "v_mfma_f32_16x16x32_bf16 v[188:191], v[96:99], v[132:135], v[188:191]\n"
      "v_cvt_pk_bf16_f32 v212, v180, v181\n"
      "v_add_f32 v218, v218, v184\n"
      "v_mfma_f32_16x16x32_bf16 v[172:175], a[0:3], v[100:103], v[172:175]\n"
      "s_add_u32 s50, s50, 24576\n"
      "v_cvt_pk_bf16_f32 v214, v184, v185\n"
      "v_add_f32 v218, v218, v185\n"
      "v_mfma_f32_16x16x32_bf16 v[172:175], a[4:7], v[104:107], v[172:175]\n"
      "s_mov_b32 s46, s36\n"
      "v_add_f32 v218, v218, v186\n"
      "v_mfma_f32_16x16x32_bf16 v[172:175], a[8:11], v[108:111], v[172:175]\n"
      "ds_read_b128 v[148:151], v10 offset:11264\n"
      "v_add_f32 v218, v218, v187\n"
      "v_mfma_f32_16x16x32_bf16 v[172:175], a[12:15], v[112:115], v[172:175]\n"
      "v_fmac_f32 v219, v185, v185\n"
      "v_add_u32 v21, 1, v21\n"
      "v_mfma_f32_16x16x32_bf16 v[172:175], a[16:19], v[116:119], v[172:175]\n"
      "s_add_u32 s36, s36, 24576\n"
      "ds_read_b128 v[152:155], v10 offset:12288\n"
      "v_fmac_f32 v219, v186, v186\n"
      "v_mfma_f32_16x16x32_bf16 v[172:175], a[20:23], v[120:123], v[172:175]\n"
      "buffer_store_dwordx2 v[216:217], v16, s[8:11], s38 offen sc1\n"
      "v_mfma_f32_16x16x32_bf16 v[172:175], a[24:27], v[124:127], v[172:175]\n"
      "ds_read_b128 v[160:163], v10 offset:14336\n"
      "v_mfma_f32_16x16x32_bf16 v[172:175], a[28:31], v[128:131], v[172:175]\n"
      "ds_read2_b32 v[170:171], v7 offset0:64 offset1:96\n"
      "v_mfma_f32_16x16x32_bf16 v[172:175], a[32:35], v[132:135], v[172:175]\n"
      "ds_read2_b32 v[168:169], v7 offset0:0 offset1:32\n"
      "s_min_u32 s36, s36, s6\n"
      "v_mfma_f32_16x16x32_bf16 v[172:175], a[36:39], v[136:139], v[172:175]\n"
      "s_waitcnt lgkmcnt(4)\n"
      "v_mfma_f32_16x16x32_bf16 v[172:175], a[40:43], v[140:143], v[172:175]\n"
      "buffer_store_dwordx4 v[212:215], v15, s[8:11], s38 offen offset:1024 sc1\n"
      "v_mfma_f32_16x16x32_bf16 v[172:175], a[44:47], v[144:147], v[172:175]\n"
      "ds_write_b32 v18, v218\n"
      "v_mfma_f32_16x16x32_bf16 v[172:175], a[48:51], v[148:151], v[172:175]\n"
      "s_waitcnt vmcnt(11) lgkmcnt(4)\n"
      "v_fmac_f32 v219, v187, v187\n"
      "v_cmp_ne_u32 vcc, s21, v28\n"
      "v_mfma_f32_16x16x32_bf16 v[172:175], a[52:55], v[152:155], v[172:175]\n"
      "ds_write_b32 v18, v219 offset:4\n"
      "v_mfma_f32_16x16x32_bf16 v[172:175], a[56:59], v[156:159], v[172:175]\n"
      "s_waitcnt lgkmcnt(3)\n"
      "s_add_u32 m0, s39, 0\n"
      "v_mfma_f32_16x16x32_bf16 v[172:175], a[60:63], v[160:163], v[172:175]\n"
      "ds_write_b32 v23, v21\n"
      "v_mfma_f32_16x16x32_bf16 v[172:175], a[64:67], v[164:167], v[172:175]\n"
      "s_waitcnt lgkmcnt(3)\n"
      "s_cbranch_vccnz .Lsp_w0_10\n"
      ".Lbk_w0_10:\n"
      "v_mfma_f32_16x16x32_bf16 v[172:175], a[68:71], v[168:171], v[172:175]\n"
      "v_mfma_f32_16x16x32_bf16 v[176:179], a[72:75], v[100:103], v[176:179]\n"
      "buffer_load_dword v28, v11, s[8:11], s36 offen sc1\n"
      "v_mfma_f32_16x16x32_bf16 v[176:179], a[76:79], v[104:107], v[176:179]\n"
      "s_waitcnt vmcnt(7) lgkmcnt(2)\n"
      "v_lshlrev_b32 v186, 16, v199\n"
      "v_lshlrev_b32 v184, 16, v198\n"
      "v_mfma_f32_16x16x32_bf16 v[176:179], a[80:83], v[108:111], v[176:179]\n"
      "v_lshlrev_b32 v180, 16, v196\n"
      "v_and_b32 v183, 0xffff0000, v197\n"
      "v_mfma_f32_16x16x32_bf16 v[176:179], a[84:87], v[112:115], v[176:179]\n"
      "buffer_store_dword v25, v12, s[8:11], s50 offen sc1\n"
      "v_mfma_f32_16x16x32_bf16 v[176:179], a[88:91], v[116:119], v[176:179]\n"
      "v_lshlrev_b32 v182, 16, v197\n"
      "v_and_b32 v181, 0xffff0000, v196\n"
      "v_mfma_f32_16x16x32_bf16 v[176:179], a[92:95], v[120:123], v[176:179]\n"
      "v_and_b32 v187, 0xffff0000, v199\n"
      "v_and_b32 v185, 0xffff0000, v198\n"
      "v_mfma_f32_16x16x32_bf16 v[176:179], a[96:99], v[124:127], v[176:179]\n"
      "buffer_load_dwordx4 v[196:199], v13, s[8:11], s47 offen offset:1024 sc1\n"
      "v_mfma_f32_16x16x32_bf16 v[176:179], a[100:103], v[128:131], v[176:179]\n"
      "ds_read_b32 v22, v24\n"
      "v_mul_f32 v219, v172, v172\n"
      "v_mfma_f32_16x16x32_bf16 v[176:179], a[104:107], v[132:135], v[176:179]\n"
      "v_mfma_f32_16x16x32_bf16 v[176:179], a[108:111], v[136:139], v[176:179]\n"
      "v_mfma_f32_16x16x32_bf16 v[176:179], a[112:115], v[140:143], v[176:179]\n"
      "buffer_load_dwordx4 v0, s[8:11], s46 offen offset:0 sc1 lds\n"
      "v_mfma_f32_16x16x32_bf16 v[176:179], a[116:119], v[144:147], v[176:179]\n"
      "v_add_f32 v218, v172, v173\n"
      "v_fmac_f32 v219, v173, v173\n"
      "v_mfma_f32_16x16x32_bf16 v[176:179], a[120:123], v[148:151], v[176:179]\n"
      "s_waitcnt lgkmcnt(0)\n"
      "v_cmp_gt_i32 vcc, v21, v22\n"
      "v_mfma_f32_16x16x32_bf16 v[176:179], a[124:127], v[152:155], v[176:179]\n"
      "v_mfma_f32_16x16x32_bf16 v[176:179], a[128:131], v[156:159], v[176:179]\n"
      "buffer_load_dwordx4 v0, s[8:11], s46 offen offset:1024 sc1 lds\n"
      "v_mfma_f32_16x16x32_bf16 v[176:179], a[132:135], v[160:163], v[176:179]\n"
      "s_cbranch_vccnz .Lsp_wz0_11\n"
      ".Lbk_wz0_11:\n"
      "ds_read2st64_b32 v[204:205], v17 offset1:1\n"
      "v_mfma_f32_16x16x32_bf16 v[176:179], a[136:139], v[164:167], v[176:179]\n"
      "ds_read2st64_b32 v[206:207], v17 offset0:2 offset1:3\n"
      "v_add_u32 v21, 1, v21\n"
      "v_mfma_f32_16x16x32_bf16 v[176:179], a[140:143], v[168:171], v[176:179]\n"
      "s_waitcnt vmcnt(10)\n"
      "ds_write_b32 v23, v21\n"
      "v_mfma_f32_16x16x32_bf16 v[180:183], a[144:147], v[100:103], v[180:183]\n"
      "buffer_load_dwordx4 v0, s[8:11], s46 offen offset:3072 sc1 lds\n"
      "v_mfma_f32_16x16x32_bf16 v[180:183], a[148:151], v[104:107], v[180:183]\n"
      "v_add_f32 v218, v218, v174\n"
      "v_fmac_f32 v219, v174, v174\n"
      "v_mfma_f32_16x16x32_bf16 v[180:183], a[152:155], v[108:111], v[180:183]\n"
      "v_fmac_f32 v219, v175, v175\n"
      "v_fmac_f32 v219, v176, v176\n"
      "v_mfma_f32_16x16x32_bf16 v[180:183], a[156:159], v[112:115], v[180:183]\n"
      "v_add_f32 v218, v218, v175\n"
      "v_add_f32 v218, v218, v176\n"
      "v_mfma_f32_16x16x32_bf16 v[180:183], a[160:163], v[116:119], v[180:183]\n"
      "buffer_load_dwordx4 v0, s[8:11], s46 offen offset:2048 sc1 lds\n"
      "v_mfma_f32_16x16x32_bf16 v[180:183], a[164:167], v[120:123], v[180:183]\n"
      "s_add_u32 m0, s40, 0\n"
      "v_add_f32 v218, v218, v177\n"
      "v_cvt_pk_bf16_f32 v209, v174, v175\n"
      "v_mfma_f32_16x16x32_bf16 v[180:183], a[168:171], v[124:127], v[180:183]\n"
      "s_waitcnt lgkmcnt(0)\n"
      "v_add_f32 v188, v188, v204\n"
      "v_add_f32 v190, v190, v206\n"
      "v_mfma_f32_16x16x32_bf16 v[180:183], a[172:175], v[128:131], v[180:183]\n"
      "v_cvt_pk_bf16_f32 v208, v172, v173\n"
      "v_fmac_f32 v219, v177, v177\n"
      "v_mfma_f32_16x16x32_bf16 v[180:183], a[176:179], v[132:135], v[180:183]\n"
      "buffer_load_dwordx4 v1, s[8:11], s46 offen offset:0 sc1 lds\n"
      "v_mfma_f32_16x16x32_bf16 v[180:183], a[180:183], v[136:139], v[180:183]\n"
      "s_add_u32 s38, s38, 24576\n"
      "v_cvt_pk_bf16_f32 v210, v176, v177\n"
      "v_mfma_f32_16x16x32_bf16 v[180:183], a[184:187], v[140:143], v[180:183]\n"
      "ds_read_b32 v22, v24\n"
      "v_add_f32 v218, v218, v178\n"
      "v_mfma_f32_16x16x32_bf16 v[180:183], a[188:191], v[144:147], v[180:183]\n"
      "v_add_f32 v218, v218, v179\n"
      "v_add_f32 v218, v218, v188\n"
      "v_mfma_f32_16x16x32_bf16 v[180:183], a[192:195], v[148:151], v[180:183]\n"
      "s_add_u32 s50, s50, 24576\n"
      "v_fmac_f32 v219, v178, v178\n"
      "v_fmac_f32 v219, v179, v179\n"
      "v_mfma_f32_16x16x32_bf16 v[180:183], a[196:199], v[152:155], v[180:183]\n"
      "v_add_f32 v191, v191, v207\n"
      "v_cvt_pk_bf16_f32 v211, v178, v179\n"
      "v_mfma_f32_16x16x32_bf16 v[180:183], a[200:203], v[156:159], v[180:183]\n"
      "v_fmac_f32 v219, v188, v188\n"
      "v_add_f32 v189, v189, v205\n"
      "v_mfma_f32_16x16x32_bf16 v[180:183], a[204:207], v[160:163], v[180:183]\n"
      "s_waitcnt lgkmcnt(0)\n"
      "v_fmac_f32 v219, v189, v189\n"
      "v_cmp_gt_i32 vcc, v21, v22\n"
      "v_mfma_f32_16x16x32_bf16 v[180:183], a[208:211], v[164:167], v[180:183]\n"
      ".Lw_p1:\n"
      "s_cbranch_execz .Lw_pr\n"
      "v_fmac_f32 v219, v190, v190\n"
      "v_add_f32 v218, v218, v189\n"
      "v_mfma_f32_16x16x32_bf16 v[180:183], a[212:215], v[168:171], v[180:183]\n"
      "s_waitcnt vmcnt(11)\n"
      "v_lshlrev_b32 v172, 16, v192\n"
      "v_mfma_f32_16x16x32_bf16 v[184:187], a[216:219], v[100:103], v[184:187]\n"
      "v_and_b32 v175, 0xffff0000, v193\n"
      "v_and_b32 v177, 0xffff0000, v194\n"
      "v_mfma_f32_16x16x32_bf16 v[184:187], a[220:223], v[104:107], v[184:187]\n"
      "s_cbranch_vccnz .Lsp_wy1_12\n"
      ".Lbk_wy1_12:\n"
      "v_add_f32 v218, v218, v190\n"
      "v_mfma_f32_16x16x32_bf16 v[184:187], a[224:227], v[108:111], v[184:187]\n"
      "ds_read_b128 v[104:107], v10 offset:21504\n"
      "v_cvt_pk_bf16_f32 v216, v188, v189\n"
      "v_mfma_f32_16x16x32_bf16 v[184:187], a[228:231], v[112:115], v[184:187]\n"
      "v_add_f32 v218, v218, v191\n"
      "v_add_f32 v218, v218, v180\n"
      "v_mfma_f32_16x16x32_bf16 v[184:187], a[232:235], v[116:119], v[184:187]\n"
      "ds_read_b128 v[116:119], v10 offset:24576\n"
      "v_cvt_pk_bf16_f32 v217, v190, v191\n"
      "v_mfma_f32_16x16x32_bf16 v[184:187], a[236:239], v[120:123], v[184:187]\n"
      "ds_read_b128 v[108:111], v10 offset:22528\n"
      "v_lshlrev_b32 v188, 16, v200\n"
      "v_mfma_f32_16x16x32_bf16 v[184:187], a[240:243], v[124:127], v[184:187]\n"
      "ds_read_b128 v[100:103], v10 offset:20480\n"
      "v_fmac_f32 v219, v191, v191\n"
      "v_mfma_f32_16x16x32_bf16 v[184:187], a[244:247], v[128:131], v[184:187]\n"
      "v_lshlrev_b32 v190, 16, v201\n"
      "v_and_b32 v191, 0xffff0000, v201\n"
      "v_mfma_f32_16x16x32_bf16 v[184:187], a[248:251], v[132:135], v[184:187]\n"
      "ds_read2_b32 v[134:135], v8 offset0:64 offset1:96\n"
      "v_lshlrev_b32 v174, 16, v193\n"
      "v_mfma_f32_16x16x32_bf16 v[184:187], a[252:255], v[136:139], v[184:187]\n"
      "s_sub_u32 s47, s46, s45\n"
      "v_and_b32 v189, 0xffff0000, v200\n"
      "v_fmac_f32 v219, v180, v180\n"
      "v_mfma_f32_16x16x32_bf16 v[184:187], v[32:35], v[140:143], v[184:187]\n"
      "buffer_store_dwordx4 v[208:211], v15, s[8:11], s38 offen sc1\n"
      "v_mfma_f32_16x16x32_bf16 v[184:187], v[36:39], v[144:147], v[184:187]\n"
      "ds_read_b128 v[136:139], v10 offset:28672\n"
      "v_fmac_f32 v219, v181, v181\n"
      "v_mfma_f32_16x16x32_bf16 v[184:187], v[40:43], v[148:151], v[184:187]\n"
      "ds_read2_b32 v[132:133], v8 offset0:0 offset1:32\n"
      "v_lshlrev_b32 v178, 16, v195\n"
      "v_mfma_f32_16x16x32_bf16 v[184:187], v[44:47], v[152:155], v[184:187]\n"
      "ds_read_b128 v[112:115], v10 offset:23552\n"
      "v_add_f32 v218, v218, v181\n"
      "v_mfma_f32_16x16x32_bf16 v[184:187], v[48:51], v[156:159], v[184:187]\n"
      "ds_read_b128 v[120:123], v10 offset:25600\n"
      "v_and_b32 v173, 0xffff0000, v192\n"
      "v_mfma_f32_16x16x32_bf16 v[184:187], v[52:55], v[160:163], v[184:187]\n"
      "ds_read_b128 v[124:127], v10 offset:26624\n"
      "v_lshlrev_b32 v176, 16, v194\n"
      "v_mfma_f32_16x16x32_bf16 v[184:187], v[56:59], v[164:167], v[184:187]\n"
      "buffer_load_dwordx2 v[200:201], v14, s[8:11], s47 offen sc1\n"
      "v_mfma_f32_16x16x32_bf16 v[184:187], v[60:63], v[168:171], v[184:187]\n"
      "s_waitcnt lgkmcnt(5)\n"
      "ds_read_b128 v[128:131], v10 offset:27648\n"
      "v_add_f32 v218, v218, v182\n"
      "v_mfma_f32_16x16x32_bf16 v[188:191], v[64:67], v[100:103], v[188:191]\n"
      "ds_read_b128 v[140:143], v10 offset:29696\n"
      "v_cvt_pk_bf16_f32 v213, v182, v183\n"
      "v_mfma_f32_16x16x32_bf16 v[188:191], v[68:71], v[104:107], v[188:191]\n"
      "ds_read_b128 v[152:155], v10 offset:32768\n"
      "v_mfma_f32_16x16x32_bf16 v[188:191], v[72:75], v[108:111], v[188:191]\n"
      "s_waitcnt lgkmcnt(5)\n"
      "buffer_store_dwordx2 v[216:217], v16, s[8:11], s38 offen sc1\n"
      "v_mfma_f32_16x16x32_bf16 v[188:191], v[76:79], v[112:115], v[188:191]\n"
      "ds_read_b128 v[144:147], v10 offset:30720\n"
      "v_cvt_pk_bf16_f32 v212, v180, v181\n"
      "v_mfma_f32_16x16x32_bf16 v[188:191], v[80:83], v[116:119], v[188:191]\n"
      "s_waitcnt lgkmcnt(4)\n"
      "ds_read_b128 v[164:167], v10 offset:35840\n"
      "v_add_f32 v218, v218, v183\n"
      "v_mfma_f32_16x16x32_bf16 v[188:191], v[84:87], v[120:123], v[188:191]\n"
      "v_fmac_f32 v219, v182, v182\n"
      "v_add_f32 v218, v218, v184\n"
      "v_mfma_f32_16x16x32_bf16 v[188:191], v[88:91], v[124:127], v[188:191]\n"
      "s_waitcnt lgkmcnt(4)\n"
      "ds_read_b128 v[148:151], v10 offset:31744\n"
      "v_add_f32 v218, v218, v185\n"
      "v_mfma_f32_16x16x32_bf16 v[188:191], v[92:95], v[128:131], v[188:191]\n"
      "ds_read_b128 v[160:163], v10 offset:34816\n"
      "v_and_b32 v179, 0xffff0000, v195\n"
      "v_mfma_f32_16x16x32_bf16 v[188:191], v[96:99], v[132:135], v[188:191]\n"
      "buffer_load_dwordx4 v[192:195], v13, s[8:11], s47 offen sc1\n"
      "v_mfma_f32_16x16x32_bf16 v[172:175], a[0:3], v[100:103], v[172:175]\n"
      "v_cvt_pk_bf16_f32 v215, v186, v187\n"
      "v_fmac_f32 v219, v183, v183\n"
      "v_mfma_f32_16x16x32_bf16 v[172:175], a[4:7], v[104:107], v[172:175]\n"
      "v_cvt_pk_bf16_f32 v214, v184, v185\n"
      "v_mfma_f32_16x16x32_bf16 v[172:175], a[8:11], v[108:111], v[172:175]\n"
      "ds_read2_b32 v[168:169], v9 offset0:0 offset1:32\n"
      "v_mfma_f32_16x16x32_bf16 v[172:175], a[12:15], v[112:115], v[172:175]\n"
      "ds_read_b128 v[156:159], v10 offset:33792\n"
      "v_mfma_f32_16x16x32_bf16 v[172:175], a[16:19], v[116:119], v[172:175]\n"
      "buffer_store_dwordx4 v[212:215], v15, s[8:11], s38 offen offset:1024 sc1\n"
      "v_mfma_f32_16x16x32_bf16 v[172:175], a[20:23], v[120:123], v[172:175]\n"
      "v_fmac_f32 v219, v184, v184\n"
      "v_fmac_f32 v219, v185, v185\n"
      "v_mfma_f32_16x16x32_bf16 v[172:175], a[24:27], v[124:127], v[172:175]\n"
      "ds_read2_b32 v[170:171], v9 offset0:64 offset1:96\n"
      "v_add_f32 v218, v218, v186\n"
      "v_mfma_f32_16x16x32_bf16 v[172:175], a[28:31], v[128:131], v[172:175]\n"
      "v_fmac_f32 v219, v186, v186\n"
      "v_add_f32 v218, v218, v187\n"
      "v_mfma_f32_16x16x32_bf16 v[172:175], a[32:35], v[132:135], v[172:175]\n"
      "ds_write_b32 v18, v218\n"
      "v_mfma_f32_16x16x32_bf16 v[172:175], a[36:39], v[136:139], v[172:175]\n"
      "s_waitcnt lgkmcnt(3)\n"
      "s_add_u32 m0, s39, 20480\n"
      "v_add_u32 v21, 1, v21\n"
      "v_mfma_f32_16x16x32_bf16 v[172:175], a[40:43], v[140:143], v[172:175]\n"
      "s_mov_b32 s46, s36\n"
      "v_mfma_f32_16x16x32_bf16 v[172:175], a[44:47], v[144:147], v[172:175]\n"
      "v_fmac_f32 v219, v187, v187\n"
      "v_mfma_f32_16x16x32_bf16 v[172:175], a[48:51], v[148:151], v[172:175]\n"
      "ds_write_b32 v18, v219 offset:4\n"
      "v_mfma_f32_16x16x32_bf16 v[172:175], a[52:55], v[152:155], v[172:175]\n"
      "s_waitcnt vmcnt(10) lgkmcnt(2)\n"
      "v_and_b32 v181, 0xffff0000, v196\n"
      "v_mfma_f32_16x16x32_bf16 v[172:175], a[56:59], v[156:159], v[172:175]\n"
      "ds_write_b32 v23, v21\n"
      "v_mfma_f32_16x16x32_bf16 v[172:175], a[60:63], v[160:163], v[172:175]\n"
      "v_lshlrev_b32 v186, 16, v199\n"
      "v_cmp_ne_u32 vcc, s21, v28\n"
      "v_mfma_f32_16x16x32_bf16 v[172:175], a[64:67], v[164:167], v[172:175]\n"
      "buffer_store_dword v25, v12, s[8:11], s50 offen sc1\n"
      "v_mfma_f32_16x16x32_bf16 v[172:175], a[68:71], v[168:171], v[172:175]\n"
      "s_cmp_eq_u32 s36, s37\n"
      "s_cselect_b32 s48, 49152, s51\n"
      "s_add_u32 s36, s36, s48\n"
      "v_mfma_f32_16x16x32_bf16 v[176:179], a[72:75], v[100:103], v[176:179]\n"
      "s_cbranch_vccnz .Lsp_w1_13\n"
      ".Lbk_w1_13:\n"
      "s_min_u32 s36, s36, s6\n"
      "v_mfma_f32_16x16x32_bf16 v[176:179], a[76:79], v[104:107], v[176:179]\n"
      "buffer_load_dword v28, v11, s[8:11], s36 offen sc1\n"
      "v_mfma_f32_16x16x32_bf16 v[176:179], a[80:83], v[108:111], v[176:179]\n"
      "v_and_b32 v183, 0xffff0000, v197\n"
      "v_mul_f32 v219, v172, v172\n"
      "v_mfma_f32_16x16x32_bf16 v[176:179], a[84:87], v[112:115], v[176:179]\n"
      "buffer_load_dwordx4 v0, s[8:11], s46 offen offset:0 sc1 lds\n"
      "v_mfma_f32_16x16x32_bf16 v[176:179], a[88:91], v[116:119], v[176:179]\n"
      "v_mfma_f32_16x16x32_bf16 v[176:179], a[92:95], v[120:123], v[176:179]\n"
      "v_fmac_f32 v219, v173, v173\n"
      "v_mfma_f32_16x16x32_bf16 v[176:179], a[96:99], v[124:127], v[176:179]\n"
      "v_lshlrev_b32 v184, 16, v198\n"
      "v_and_b32 v185, 0xffff0000, v198\n"
      "v_mfma_f32_16x16x32_bf16 v[176:179], a[100:103], v[128:131], v[176:179]\n"
      "ds_read_b32 v22, v24\n"
      "v_fmac_f32 v219, v174, v174\n"
      "v_mfma_f32_16x16x32_bf16 v[176:179], a[104:107], v[132:135], v[176:179]\n"
      "buffer_load_dwordx4 v0, s[8:11], s46 offen offset:1024 sc1 lds\n"
      "v_mfma_f32_16x16x32_bf16 v[176:179], a[108:111], v[136:139], v[176:179]\n"
      "v_lshlrev_b32 v182, 16, v197\n"
      "v_add_f32 v218, v172, v173\n"
      "v_mfma_f32_16x16x32_bf16 v[176:179], a[112:115], v[140:143], v[176:179]\n"
      "v_add_f32 v218, v218, v174\n"
      "v_fmac_f32 v219, v175, v175\n"
      "v_mfma_f32_16x16x32_bf16 v[176:179], a[116:119], v[144:147], v[176:179]\n"
      "v_cvt_pk_bf16_f32 v208, v172, v173\n"
      "v_mfma_f32_16x16x32_bf16 v[176:179], a[120:123], v[148:151], v[176:179]\n"
      "s_waitcnt lgkmcnt(0)\n"
      "v_and_b32 v187, 0xffff0000, v199\n"
      "v_cmp_gt_i32 vcc, v21, v22\n"
      "v_mfma_f32_16x16x32_bf16 v[176:179], a[124:127], v[152:155], v[176:179]\n"
      "buffer_load_dwordx4 v0, s[8:11], s46 offen offset:3072 sc1 lds\n"
      "v_mfma_f32_16x16x32_bf16 v[176:179], a[128:131], v[156:159], v[176:179]\n"
      "v_lshlrev_b32 v180, 16, v196\n"
      "v_add_f32 v218, v218, v175\n"
      "v_mfma_f32_16x16x32_bf16 v[176:179], a[132:135], v[160:163], v[176:179]\n"
      "s_cbranch_vccnz .Lsp_wz1_14\n"
      ".Lbk_wz1_14:\n"
      "ds_read2st64_b32 v[204:205], v17 offset1:1\n"
      "v_mfma_f32_16x16x32_bf16 v[176:179], a[136:139], v[164:167], v[176:179]\n"
      "ds_read2st64_b32 v[206:207], v17 offset0:2 offset1:3\n"
      "v_add_u32 v21, 1, v21\n"
      "v_mfma_f32_16x16x32_bf16 v[176:179], a[140:143], v[168:171], v[176:179]\n"
      "s_waitcnt vmcnt(10)\n"
      "ds_write_b32 v23, v21\n"
      "v_mfma_f32_16x16x32_bf16 v[180:183], a[144:147], v[100:103], v[180:183]\n"
      "buffer_load_dwordx4 v0, s[8:11], s46 offen offset:2048 sc1 lds\n"
      "v_mfma_f32_16x16x32_bf16 v[180:183], a[148:151], v[104:107], v[180:183]\n"
      "v_mfma_f32_16x16x32_bf16 v[180:183], a[152:155], v[108:111], v[180:183]\n"
      "s_add_u32 m0, s40, 20480\n"
      "v_add_f32 v218, v218, v176\n"
      "v_mfma_f32_16x16x32_bf16 v[180:183], a[156:159], v[112:115], v[180:183]\n"
      "v_fmac_f32 v219, v176, v176\n"
      "v_cvt_pk_bf16_f32 v209, v174, v175\n"
      "v_mfma_f32_16x16x32_bf16 v[180:183], a[160:163], v[116:119], v[180:183]\n"
      "buffer_load_dwordx4 v[196:199], v13, s[8:11], s47 offen offset:1024 sc1\n"
      "v_mfma_f32_16x16x32_bf16 v[180:183], a[164:167], v[120:123], v[180:183]\n"
      "s_waitcnt lgkmcnt(1)\n"
      "v_cvt_pk_bf16_f32 v211, v178, v179\n"
      "v_add_f32 v190, v190, v206\n"
      "v_mfma_f32_16x16x32_bf16 v[180:183], a[168:171], v[124:127], v[180:183]\n"
      "v_add_f32 v218, v218, v177\n"
      "v_add_f32 v218, v218, v178\n"
      "v_mfma_f32_16x16x32_bf16 v[180:183], a[172:175], v[128:131], v[180:183]\n"
      "v_add_f32 v218, v218, v179\n"
      "v_add_f32 v189, v189, v205\n"
      "v_mfma_f32_16x16x32_bf16 v[180:183], a[176:179], v[132:135], v[180:183]\n"
      "buffer_load_dwordx4 v1, s[8:11], s46 offen offset:0 sc1 lds\n"
      "v_mfma_f32_16x16x32_bf16 v[180:183], a[180:183], v[136:139], v[180:183]\n"
      "v_add_f32 v188, v188, v204\n"
      "v_fmac_f32 v219, v177, v177\n"
      "v_mfma_f32_16x16x32_bf16 v[180:183], a[184:187], v[140:143], v[180:183]\n"
      "ds_read_b32 v22, v24\n"
      "v_fmac_f32 v219, v178, v178\n"
      "v_mfma_f32_16x16x32_bf16 v[180:183], a[188:191], v[144:147], v[180:183]\n"
      "v_cvt_pk_bf16_f32 v210, v176, v177\n"
      "v_fmac_f32 v219, v179, v179\n"
      "v_mfma_f32_16x16x32_bf16 v[180:183], a[192:195], v[148:151], v[180:183]\n"
      "v_add_f32 v191, v191, v207\n"
      "v_fmac_f32 v219, v188, v188\n"
      "v_mfma_f32_16x16x32_bf16 v[180:183], a[196:199], v[152:155], v[180:183]\n"
      "v_add_f32 v218, v218, v188\n"
      "v_mfma_f32_16x16x32_bf16 v[180:183], a[200:203], v[156:159], v[180:183]\n"
      "v_add_f32 v218, v218, v189\n"
      "v_add_f32 v218, v218, v190\n"
      "v_mfma_f32_16x16x32_bf16 v[180:183], a[204:207], v[160:163], v[180:183]\n"
      "s_waitcnt lgkmcnt(0)\n"
      "v_fmac_f32 v219, v189, v189\n"
      "v_cmp_gt_i32 vcc, v21, v22\n"
      "v_mfma_f32_16x16x32_bf16 v[180:183], a[208:211], v[164:167], v[180:183]\n"
      "v_cvt_pk_bf16_f32 v216, v188, v189\n"
      "v_fmac_f32 v219, v190, v190\n"
      "v_mfma_f32_16x16x32_bf16 v[180:183], a[212:215], v[168:171], v[180:183]\n"
      "s_waitcnt vmcnt(11)\n"
      "v_and_b32 v189, 0xffff0000, v200\n"
      "v_fmac_f32 v219, v191, v191\n"
      "v_mfma_f32_16x16x32_bf16 v[184:187], a[216:219], v[100:103], v[184:187]\n"
      "s_add_u32 s38, s38, 24576\n"
      "v_cvt_pk_bf16_f32 v217, v190, v191\n"
      "v_lshlrev_b32_e64 v188, 16, v200\n"
      "v_mfma_f32_16x16x32_bf16 v[184:187], a[220:223], v[104:107], v[184:187]\n"
      "s_cbranch_vccnz .Lsp_wy0_15\n"
      ".Lbk_wy0_15:\n"
      "v_mfma_f32_16x16x32_bf16 v[184:187], a[224:227], v[108:111], v[184:187]\n"
      "ds_read_b128 v[104:107], v0 offset:1024\n"
      "v_add_f32 v218, v218, v191\n"
      "v_mfma_f32_16x16x32_bf16 v[184:187], a[228:231], v[112:115], v[184:187]\n"
      "v_fmac_f32 v219, v180, v180\n"
      "v_lshlrev_b32 v190, 16, v201\n"
      "v_mfma_f32_16x16x32_bf16 v[184:187], a[232:235], v[116:119], v[184:187]\n"
      "ds_read_b128 v[100:103], v0 offset:0\n"
      "v_add_f32 v218, v218, v180\n"
      "v_mfma_f32_16x16x32_bf16 v[184:187], a[236:239], v[120:123], v[184:187]\n"
      "s_waitcnt vmcnt(9)\n"
      "ds_read_b128 v[120:123], v0 offset:5120\n"
      "v_lshlrev_b32 v178, 16, v195\n"
      "v_mfma_f32_16x16x32_bf16 v[184:187], a[240:243], v[124:127], v[184:187]\n"
      "ds_read_b128 v[112:115], v0 offset:3072\n"
      "v_fmac_f32 v219, v181, v181\n"
      "v_mfma_f32_16x16x32_bf16 v[184:187], a[244:247], v[128:131], v[184:187]\n"
      "ds_read_b128 v[116:119], v0 offset:4096\n"
      "v_lshlrev_b32 v174, 16, v193\n"
      "v_mfma_f32_16x16x32_bf16 v[184:187], a[248:251], v[132:135], v[184:187]\n"
      "ds_read_b128 v[124:127], v0 offset:6144\n"
      "v_fmac_f32 v219, v182, v182\n"
      "v_mfma_f32_16x16x32_bf16 v[184:187], a[252:255], v[136:139], v[184:187]\n"
      "s_sub_u32 s47, s46, s45\n"
      "ds_read_b128 v[108:111], v0 offset:2048\n"
      "v_mfma_f32_16x16x32_bf16 v[184:187], v[32:35], v[140:143], v[184:187]\n"
      "v_and_b32 v191, 0xffff0000, v201\n"
      "v_and_b32 v177, 0xffff0000, v194\n"
      "v_mfma_f32_16x16x32_bf16 v[184:187], v[36:39], v[144:147], v[184:187]\n"
      "ds_read_b128 v[136:139], v0 offset:8192\n"
      "v_lshlrev_b32 v176, 16, v194\n"
      "v_mfma_f32_16x16x32_bf16 v[184:187], v[40:43], v[148:151], v[184:187]\n"
      "ds_read_b128 v[128:131], v0 offset:7168\n"
      "v_lshlrev_b32 v172, 16, v192\n"
      "v_mfma_f32_16x16x32_bf16 v[184:187], v[44:47], v[152:155], v[184:187]\n"
      "v_fmac_f32 v219, v183, v183\n"
      "v_and_b32 v175, 0xffff0000, v193\n"
      "v_mfma_f32_16x16x32_bf16 v[184:187], v[48:51], v[156:159], v[184:187]\n"
      "buffer_load_dwordx2 v[200:201], v14, s[8:11], s47 offen sc1\n"
      "v_mfma_f32_16x16x32_bf16 v[184:187], v[52:55], v[160:163], v[184:187]\n"
      "ds_read2_b32 v[132:133], v2 offset0:0 offset1:32\n"
      "v_and_b32 v173, 0xffff0000, v192\n"
      "v_mfma_f32_16x16x32_bf16 v[184:187], v[56:59], v[164:167], v[184:187]\n"
      "s_waitcnt lgkmcnt(3)\n"
      "ds_read2_b32 v[134:135], v2 offset0:64 offset1:96\n"
      "v_add_f32 v218, v218, v181\n"
      "v_mfma_f32_16x16x32_bf16 v[184:187], v[60:63], v[168:171], v[184:187]\n"
      "s_sub_u32 s20, s20, 1\n"
      "s_cbranch_scc0 .Lw_loop\n"
      ".Lw_tail:\n"
      "v_add_f32 v218, v218, v182\n"
      "s_nop 7\n"
      "v_fmac_f32 v219, v184, v184\n"
      "v_cvt_pk_bf16_f32 v213, v182, v183\n"
      "v_cvt_pk_bf16_f32 v215, v186, v187\n"
      "v_add_f32 v218, v218, v183\n"
      "buffer_store_dwordx4 v[208:211], v15, s[8:11], s38 offen sc1\n"
      "v_cvt_pk_bf16_f32 v212, v180, v181\n"
      "v_add_f32 v218, v218, v184\n"
      "s_add_u32 s50, s50, 24576\n"
      "v_cvt_pk_bf16_f32 v214, v184, v185\n"
      "v_add_f32 v218, v218, v185\n"
      "v_add_f32 v218, v218, v186\n"
      "v_add_f32 v218, v218, v187\n"
      "v_fmac_f32 v219, v185, v185\n"
      "v_fmac_f32 v219, v186, v186\n"
      "buffer_store_dwordx2 v[216:217], v16, s[8:11], s38 offen sc1\n"
      "buffer_store_dwordx4 v[212:215], v15, s[8:11], s38 offen offset:1024 sc1\n"
      "ds_write_b32 v18, v218\n"
      "v_fmac_f32 v219, v187, v187\n"
      "ds_write_b32 v18, v219 offset:4\n"
      "s_waitcnt lgkmcnt(0)\n"
      "s_barrier\n"
      "s_waitcnt vmcnt(12)\n"
      "buffer_store_dword v25, v12, s[8:11], s50 offen sc1\n"
      "s_add_u32 s38, s38, 24576\n"
      "s_add_u32 s50, s50, 24576\n"
      "s_waitcnt vmcnt(1)\n"
      "buffer_store_dword v25, v12, s[8:11], s50 offen sc1\n"
      "s_add_u32 s50, s50, 24576\n"
      "s_endpgm\n"
      ".Lv_start:\n"
      "s_mov_b64 exec, 0\n"
      "s_lshr_b32 s22, s39, 13\n"
      ".Lv_pl:\n"
      "s_cmp_eq_u32 s22, 0\n"
      "s_cbranch_scc1 .Lv_p0\n"
      "s_cmp_eq_u32 s22, 1\n"
      "s_cbranch_scc1 .Lv_p1\n"
      "s_branch .Lv_pe\n"
      ".Lv_pr:\n"
      "s_add_u32 s22, s22, 2\n"
      "s_branch .Lv_pl\n"
      ".Lv_pe:\n"
      "s_mov_b64 exec, -1\n"
      "s_add_u32 s50, s38, 4294893568\n"
      "s_add_u32 s49, s38, 4294918144\n"
      "s_add_u32 s38, s38, 4294942720\n"
      "s_cmp_eq_u32 s36, s37\n"
      "s_cselect_b32 s48, 49152, s51\n"
      "s_add_u32 s36, s36, s48\n"
      "s_min_u32 s36, s36, s6\n"
      "buffer_load_dword v28, v11, s[8:11], s36 offen sc1\n"
      "s_mov_b32 s46, s36\n"
      "s_add_u32 s36, s36, 24576\n"
      "s_min_u32 s36, s36, s6\n"
      "s_add_u32 m0, s39, 0\n"
      "s_waitcnt vmcnt(0)\n"
      "v_cmp_ne_u32 vcc, s21, v28\n"
      "s_cbranch_vccnz .Lsp_v0_16\n"
      ".Lbk_v0_16:\n"
      "buffer_load_dword v28, v11, s[8:11], s36 offen sc1\n"
      "buffer_load_dwordx4 v0, s[8:11], s46 offen offset:3072 sc1 lds\n"
      "buffer_load_dwordx4 v0, s[8:11], s46 offen offset:0 sc1 lds\n"
      "buffer_load_dwordx4 v0, s[8:11], s46 offen offset:1024 sc1 lds\n"
      "buffer_load_dwordx4 v0, s[8:11], s46 offen offset:2048 sc1 lds\n"
      "s_add_u32 m0, s40, 0\n"
      "s_nop 0\n"
      "buffer_load_dwordx4 v1, s[8:11], s46 offen offset:0 sc1 lds\n"
      "s_sub_u32 s47, s46, s45\n"
      "buffer_load_dwordx4 v[192:195], v13, s[8:11], s47 offen sc1\n"
      "s_mov_b32 s46, s36\n"
      "s_cmp_eq_u32 s36, s37\n"
      "s_cselect_b32 s48, 49152, s51\n"
      "buffer_load_dword v26, v23, s[8:11], s31 offen\n"
      "s_add_u32 s36, s36, s48\n"
      "s_waitcnt vmcnt(7)\n"
      "v_cmp_ne_u32 vcc, s21, v28\n"
      "s_min_u32 s36, s36, s6\n"
      "s_cbranch_vccnz .Lsp_v1_17\n"
      ".Lbk_v1_17:\n"
      "buffer_load_dword v28, v11, s[8:11], s36 offen sc1\n"
      "s_add_u32 m0, s39, 20480\n"
      "s_nop 0\n"
      "buffer_load_dwordx4 v0, s[8:11], s46 offen offset:0 sc1 lds\n"
      "buffer_load_dwordx4 v0, s[8:11], s46 offen offset:1024 sc1 lds\n"
      "buffer_load_dwordx4 v0, s[8:11], s46 offen offset:2048 sc1 lds\n"
      "buffer_load_dwordx4 v[196:199], v13, s[8:11], s47 offen offset:1024 sc1\n"
      "v_add_u32 v21, 1, v21\n"
      "s_waitcnt vmcnt(7)\n"
      "ds_write_b32 v23, v21\n"
      "buffer_load_dwordx4 v0, s[8:11], s46 offen offset:3072 sc1 lds\n"
      "s_add_u32 m0, s40, 20480\n"
      "s_nop 0\n"
      "buffer_load_dwordx4 v1, s[8:11], s46 offen offset:0 sc1 lds\n"
      "ds_read_b32 v22, v24\n"
      "buffer_load_dword v26, v23, s[8:11], s31 offen\n"
      "s_waitcnt lgkmcnt(0)\n"
      "v_cmp_gt_i32 vcc, v21, v22\n"
      "buffer_load_dword v26, v23, s[8:11], s31 offen\n"
      "s_cbranch_vccnz .Lsp_vy0_18\n"
      ".Lbk_vy0_18:\n"
      "s_sub_u32 s47, s46, s45\n"
      "s_waitcnt vmcnt(10)\n"
      "v_lshlrev_b32 v174, 16, v193\n"
      "v_and_b32 v179, 0xffff0000, v195\n"
      "ds_read_b128 v[100:103], v0 offset:8192\n"
      "ds_read_b128 v[108:111], v0 offset:10240\n"
      "v_and_b32 v173, 0xffff0000, v192\n"
      "ds_read_b128 v[120:123], v0 offset:13312\n"
      "ds_read_b128 v[128:131], v0 offset:15360\n"
      "v_lshlrev_b32 v178, 16, v195\n"
      "ds_read_b128 v[104:107], v0 offset:9216\n"
      "v_lshlrev_b32 v172, 16, v192\n"
      "buffer_load_dword v26, v23, s[8:11], s31 offen\n"
      "s_mov_b32 s46, s36\n"
      "ds_read_b128 v[112:115], v0 offset:11264\n"
      "ds_read2_b32 v[134:135], v2 offset0:64 offset1:96\n"
      "ds_read2_b32 v[132:133], v2 offset0:0 offset1:32\n"
      "s_add_u32 s36, s36, 24576\n"
      "ds_read_b128 v[116:119], v0 offset:12288\n"
      "ds_read_b128 v[124:127], v0 offset:14336\n"
      "ds_read_b128 v[160:163], v0 offset:6144\n"
      "v_and_b32 v177, 0xffff0000, v194\n"
      "s_mov_b32 s20, 31\n"
      "s_waitcnt lgkmcnt(0)\n"
      "s_nop 4\n"
      "s_branch .Lv_loop\n"
      ".p2align 12\n"
      ".Lv_p0:\n"
      "s_branch .Lv_pr\n"
      ".p2align 5\n"
      ".Lv_loop:\n"
      "v_mfma_f32_16x16x32_bf16 v[188:191], v[64:67], v[100:103], 0\n"
      "ds_read_b128 v[140:143], v0 offset:1024\n"
      "v_and_b32 v175, 0xffff0000, v193\n"
      "v_mfma_f32_16x16x32_bf16 v[188:191], v[68:71], v[104:107], v[188:191]\n"
      "ds_read2_b32 v[168:169], v3 offset0:0 offset1:32\n"
      "v_lshlrev_b32 v176, 16, v194\n"
      "v_mfma_f32_16x16x32_bf16 v[188:191], v[72:75], v[108:111], v[188:191]\n"
      "s_waitcnt lgkmcnt(5)\n"
      "ds_read_b128 v[164:167], v0 offset:7168\n"
      "v_add_f32 v218, v218, v184\n"
      "v_mfma_f32_16x16x32_bf16 v[188:191], v[76:79], v[112:115], v[188:191]\n"
      "s_waitcnt lgkmcnt(5)\n"
      "ds_read_b128 v[144:147], v0 offset:2048\n"
      "v_add_f32 v218, v218, v185\n"
      "v_mfma_f32_16x16x32_bf16 v[188:191], v[80:83], v[116:119], v[188:191]\n"
      "buffer_load_dwordx4 v[192:195], v13, s[8:11], s47 offen sc1\n"
      "v_mfma_f32_16x16x32_bf16 v[188:191], v[84:87], v[120:123], v[188:191]\n"
      "s_waitcnt lgkmcnt(4)\n"
      "ds_read2_b32 v[170:171], v3 offset0:64 offset1:96\n"
      "v_fmac_f32 v219, v183, v183\n"
      "v_mfma_f32_16x16x32_bf16 v[188:191], v[88:91], v[124:127], v[188:191]\n"
      "s_min_u32 s36, s36, s6\n"
      "ds_read_b128 v[136:139], v0 offset:0\n"
      "v_fmac_f32 v219, v184, v184\n"
      "v_mfma_f32_16x16x32_bf16 v[188:191], v[92:95], v[128:131], v[188:191]\n"
      "ds_read_b128 v[148:151], v0 offset:3072\n"
      "v_mfma_f32_16x16x32_bf16 v[188:191], v[96:99], v[132:135], v[188:191]\n"
      "v_cvt_pk_bf16_f32 v215, v186, v187\n"
      "v_add_f32 v218, v218, v186\n"
      "v_mfma_f32_16x16x32_bf16 v[172:175], a[0:3], v[100:103], v[172:175]\n"
      "ds_read_b128 v[152:155], v0 offset:4096\n"
      "v_add_f32 v218, v218, v187\n"
      "v_mfma_f32_16x16x32_bf16 v[172:175], a[4:7], v[104:107], v[172:175]\n"
      "ds_read_b128 v[156:159], v0 offset:5120\n"
      "v_fmac_f32 v219, v185, v185\n"
      "v_mfma_f32_16x16x32_bf16 v[172:175], a[8:11], v[108:111], v[172:175]\n"
      "s_add_u32 m0, s39, 40960\n"
      "v_cvt_pk_bf16_f32 v214, v184, v185\n"
      "v_mfma_f32_16x16x32_bf16 v[172:175], a[12:15], v[112:115], v[172:175]\n"
      "ds_write_b32 v18, v218\n"
      "v_mfma_f32_16x16x32_bf16 v[172:175], a[16:19], v[116:119], v[172:175]\n"
      "s_waitcnt vmcnt(10) lgkmcnt(4)\n"
      "v_fmac_f32_e64 v219, v186, v186\n"
      "v_cmp_ne_u32 vcc, s21, v28\n"
      "v_mfma_f32_16x16x32_bf16 v[172:175], a[20:23], v[120:123], v[172:175]\n"
      "ds_write_b32 v17, v190 offset:512\n"
      "v_mfma_f32_16x16x32_bf16 v[172:175], a[24:27], v[124:127], v[172:175]\n"
      "buffer_store_dwordx4 v[212:215], v15, s[8:11], s38 offen offset:1024 sc1\n"
      "v_mfma_f32_16x16x32_bf16 v[172:175], a[28:31], v[128:131], v[172:175]\n"
      "ds_write_b32 v17, v188 offset:0\n"
      "v_mfma_f32_16x16x32_bf16 v[172:175], a[32:35], v[132:135], v[172:175]\n"
      "v_fmac_f32_e64 v219, v187, v187\n"
      "v_mfma_f32_16x16x32_bf16 v[172:175], a[36:39], v[136:139], v[172:175]\n"
      "ds_write_b32 v17, v191 offset:768\n"
      "v_mfma_f32_16x16x32_bf16 v[172:175], a[40:43], v[140:143], v[172:175]\n"
      "s_cbranch_vccnz .Lsp_v0_19\n"
      ".Lbk_v0_19:\n"
      "v_add_u32 v21, 1, v21\n"
      "v_mfma_f32_16x16x32_bf16 v[172:175], a[44:47], v[144:147], v[172:175]\n"
      "s_waitcnt lgkmcnt(3)\n"
      "ds_write_b32 v18, v219 offset:4\n"
      "v_mfma_f32_16x16x32_bf16 v[172:175], a[48:51], v[148:151], v[172:175]\n"
      "buffer_load_dword v28, v11, s[8:11], s36 offen sc1\n"
      "v_mfma_f32_16x16x32_bf16 v[172:175], a[52:55], v[152:155], v[172:175]\n"
      "ds_write_b32 v17, v189 offset:256\n"
      "v_mfma_f32_16x16x32_bf16 v[172:175], a[56:59], v[156:159], v[172:175]\n"
      "s_waitcnt vmcnt(8) lgkmcnt(3)\n"
      "v_lshlrev_b32 v186, 16, v199\n"
      "v_and_b32 v185, 0xffff0000, v198\n"
      "v_mfma_f32_16x16x32_bf16 v[172:175], a[60:63], v[160:163], v[172:175]\n"
      "ds_write_b32 v23, v21\n"
      "v_mfma_f32_16x16x32_bf16 v[172:175], a[64:67], v[164:167], v[172:175]\n"
      "v_and_b32 v181, 0xffff0000, v196\n"
      "v_and_b32 v187, 0xffff0000, v199\n"
      "v_mfma_f32_16x16x32_bf16 v[172:175], a[68:71], v[168:171], v[172:175]\n"
      "buffer_load_dwordx4 v0, s[8:11], s46 offen offset:3072 sc1 lds\n"
      "v_mfma_f32_16x16x32_bf16 v[176:179], a[72:75], v[100:103], v[176:179]\n"
      "v_lshlrev_b32 v182, 16, v197\n"
      "s_add_u32 s38, s38, 24576\n"
      "v_mfma_f32_16x16x32_bf16 v[176:179], a[76:79], v[104:107], v[176:179]\n"
      "v_lshlrev_b32 v184, 16, v198\n"
      "v_mfma_f32_16x16x32_bf16 v[176:179], a[80:83], v[108:111], v[176:179]\n"
      "v_cvt_pk_bf16_f32 v208, v172, v173\n"
      "v_mfma_f32_16x16x32_bf16 v[176:179], a[84:87], v[112:115], v[176:179]\n"
      "v_mul_f32 v219, v172, v172\n"
      "v_mfma_f32_16x16x32_bf16 v[176:179], a[88:91], v[116:119], v[176:179]\n"
      "v_lshlrev_b32 v180, 16, v196\n"
      "v_and_b32 v183, 0xffff0000, v197\n"
      "v_mfma_f32_16x16x32_bf16 v[176:179], a[92:95], v[120:123], v[176:179]\n"
      "v_fmac_f32 v219, v173, v173\n"
      "v_mfma_f32_16x16x32_bf16 v[176:179], a[96:99], v[124:127], v[176:179]\n"
      "buffer_load_dwordx4 v[196:199], v13, s[8:11], s47 offen offset:1024 sc1\n"
      "v_mfma_f32_16x16x32_bf16 v[176:179], a[100:103], v[128:131], v[176:179]\n"
      "ds_read_b32 v22, v24\n"
      "v_mfma_f32_16x16x32_bf16 v[176:179], a[104:107], v[132:135], v[176:179]\n"
      "v_fmac_f32 v219, v174, v174\n"
      "v_mfma_f32_16x16x32_bf16 v[176:179], a[108:111], v[136:139], v[176:179]\n"
      "v_fmac_f32 v219, v175, v175\n"
      "v_mfma_f32_16x16x32_bf16 v[176:179], a[112:115], v[140:143], v[176:179]\n"
      "buffer_load_dwordx4 v0, s[8:11], s46 offen offset:0 sc1 lds\n"
      "v_mfma_f32_16x16x32_bf16 v[176:179], a[116:119], v[144:147], v[176:179]\n"
      "v_mfma_f32_16x16x32_bf16 v[176:179], a[120:123], v[148:151], v[176:179]\n"
      "s_waitcnt lgkmcnt(0)\n"
      "v_cmp_gt_i32 vcc, v21, v22\n"
      "v_add_f32_e64 v218, v172, v173\n"
      "v_mfma_f32_16x16x32_bf16 v[176:179], a[124:127], v[152:155], v[176:179]\n"
      "v_add_f32_e64 v218, v218, v174\n"
      "v_add_f32_e64 v218, v218, v175\n"
      "v_mfma_f32_16x16x32_bf16 v[176:179], a[128:131], v[156:159], v[176:179]\n"
      "buffer_load_dwordx4 v0, s[8:11], s46 offen offset:1024 sc1 lds\n"
      "v_mfma_f32_16x16x32_bf16 v[176:179], a[132:135], v[160:163], v[176:179]\n"
      "s_cbranch_vccnz .Lsp_vz0_20\n"
      ".Lbk_vz0_20:\n"
      "ds_read_b128 v[204:207], v19\n"
      "v_mfma_f32_16x16x32_bf16 v[176:179], a[136:139], v[164:167], v[176:179]\n"
      "v_cvt_pk_bf16_f32 v209, v174, v175\n"
      "v_add_u32 v21, 1, v21\n"
      "v_mfma_f32_16x16x32_bf16 v[176:179], a[140:143], v[168:171], v[176:179]\n"
      "s_waitcnt vmcnt(10)\n"
      "ds_write_b32 v23, v21\n"
      "v_mfma_f32_16x16x32_bf16 v[180:183], a[144:147], v[100:103], v[180:183]\n"
      "v_mfma_f32_16x16x32_bf16 v[180:183], a[148:151], v[104:107], v[180:183]\n"
      "buffer_load_dwordx4 v0, s[8:11], s46 offen offset:2048 sc1 lds\n"
      "v_mfma_f32_16x16x32_bf16 v[180:183], a[152:155], v[108:111], v[180:183]\n"
      "s_add_u32 s49, s49, 24576\n"
      "s_add_u32 m0, s40, 40960\n"
      "v_mfma_f32_16x16x32_bf16 v[180:183], a[156:159], v[112:115], v[180:183]\n"
      "v_mfma_f32_16x16x32_bf16 v[180:183], a[160:163], v[116:119], v[180:183]\n"
      "s_waitcnt lgkmcnt(1)\n"
      "v_add_f32 v204, v204, v206\n"
      "v_cvt_pk_bf16_f32 v210, v176, v177\n"
      "v_mfma_f32_16x16x32_bf16 v[180:183], a[164:167], v[120:123], v[180:183]\n"
      "buffer_load_dwordx4 v1, s[8:11], s46 offen offset:0 sc1 lds\n"
      "v_mfma_f32_16x16x32_bf16 v[180:183], a[168:171], v[124:127], v[180:183]\n"
      "v_add_f32 v205, v205, v207\n"
      "v_add_f32_dpp v204, v204, v204 quad_perm:[1,0,3,2] row_mask:0xf bank_mask:0xf\n"
      "v_mfma_f32_16x16x32_bf16 v[180:183], a[172:175], v[128:131], v[180:183]\n"
      "v_fmac_f32 v219, v176, v176\n"
      "v_mfma_f32_16x16x32_bf16 v[180:183], a[176:179], v[132:135], v[180:183]\n"
      "v_add_f32_dpp v204, v204, v204 quad_perm:[2,3,0,1] row_mask:0xf bank_mask:0xf\n"
      "v_cvt_pk_bf16_f32 v211, v178, v179\n"
      "v_mfma_f32_16x16x32_bf16 v[180:183], a[180:183], v[136:139], v[180:183]\n"
      "buffer_store_dwordx4 v[208:211], v15, s[8:11], s38 offen sc1\n"
      "v_mfma_f32_16x16x32_bf16 v[180:183], a[184:187], v[140:143], v[180:183]\n"
      "ds_read_b32 v22, v24\n"
      "v_add_f32_dpp v205, v205, v205 quad_perm:[1,0,3,2] row_mask:0xf bank_mask:0xf\n"
      "v_mfma_f32_16x16x32_bf16 v[180:183], a[188:191], v[144:147], v[180:183]\n"
      "s_add_u32 s50, s50, 24576\n"
      "v_add_f32_dpp v205, v205, v205 quad_perm:[2,3,0,1] row_mask:0xf bank_mask:0xf\n"
      "v_fmac_f32 v219, v177, v177\n"
      "v_mfma_f32_16x16x32_bf16 v[180:183], a[192:195], v[148:151], v[180:183]\n"
      "v_add_f32_dpp v205, v205, v205 row_half_mirror row_mask:0xf bank_mask:0xf\n"
      "v_fmac_f32 v219, v178, v178\n"
      "v_mfma_f32_16x16x32_bf16 v[180:183], a[196:199], v[152:155], v[180:183]\n"
      "v_add_f32_dpp v204, v204, v204 row_half_mirror row_mask:0xf bank_mask:0xf\n"
      "v_mfma_f32_16x16x32_bf16 v[180:183], a[200:203], v[156:159], v[180:183]\n"
      "v_mul_f32 v204, v27, v204\n"
      "v_fmaak_f32 v205, v27, v205, 0x36a7c5ac\n"
      "v_mfma_f32_16x16x32_bf16 v[180:183], a[204:207], v[160:163], v[180:183]\n"
      "s_waitcnt vmcnt(12) lgkmcnt(1)\n"
      "buffer_store_dword v25, v12, s[8:11], s50 offen sc1\n"
      "v_mfma_f32_16x16x32_bf16 v[180:183], a[208:211], v[164:167], v[180:183]\n"
      "s_waitcnt lgkmcnt(0)\n"
      "v_cmp_gt_i32 vcc, v21, v22\n"
      "v_add_f32 v218, v218, v176\n"
      "v_mfma_f32_16x16x32_bf16 v[180:183], a[212:215], v[168:171], v[180:183]\n"
      "v_add_f32 v218, v218, v177\n"
      "v_add_f32_e64 v218, v218, v178\n"
      "v_mfma_f32_16x16x32_bf16 v[184:187], a[216:219], v[100:103], v[184:187]\n"
      "buffer_store_dwordx2 v[204:205], v20, s[8:11], s49 offen sc1\n"
      "v_mfma_f32_16x16x32_bf16 v[184:187], a[220:223], v[104:107], v[184:187]\n"
      "s_sub_u32 s47, s46, s45\n"
      "s_cbranch_vccnz .Lsp_vy1_21\n"
      ".Lbk_vy1_21:\n"
      "v_mfma_f32_16x16x32_bf16 v[184:187], a[224:227], v[108:111], v[184:187]\n"
      "s_waitcnt vmcnt(11)\n"
      "ds_read_b128 v[104:107], v0 offset:29696\n"
      "v_and_b32 v177, 0xffff0000, v194\n"
      "v_mfma_f32_16x16x32_bf16 v[184:187], a[228:231], v[112:115], v[184:187]\n"
      "v_fmac_f32 v219, v179, v179\n"
      "v_mfma_f32_16x16x32_bf16 v[184:187], a[232:235], v[116:119], v[184:187]\n"
      "v_fmac_f32 v219, v180, v180\n"
      "v_add_f32 v218, v218, v179\n"
      "v_mfma_f32_16x16x32_bf16 v[184:187], a[236:239], v[120:123], v[184:187]\n"
      "v_cvt_pk_bf16_f32 v212, v180, v181\n"
      "v_and_b32 v179, 0xffff0000, v195\n"
      "v_mfma_f32_16x16x32_bf16 v[184:187], a[240:243], v[124:127], v[184:187]\n"
      "ds_read_b128 v[120:123], v0 offset:33792\n"
      "v_lshlrev_b32 v174, 16, v193\n"
      "v_mfma_f32_16x16x32_bf16 v[184:187], a[244:247], v[128:131], v[184:187]\n"
      "ds_read_b128 v[112:115], v0 offset:31744\n"
      "v_add_f32 v218, v218, v180\n"
      "v_mfma_f32_16x16x32_bf16 v[184:187], a[248:251], v[132:135], v[184:187]\n"
      "ds_read_b128 v[100:103], v0 offset:28672\n"
      "v_add_f32 v218, v218, v181\n"
      "v_mfma_f32_16x16x32_bf16 v[184:187], a[252:255], v[136:139], v[184:187]\n"
      "ds_read_b128 v[124:127], v0 offset:34816\n"
      "v_mfma_f32_16x16x32_bf16 v[184:187], v[32:35], v[140:143], v[184:187]\n"
      "ds_read_b128 v[108:111], v0 offset:30720\n"
      "v_and_b32 v175, 0xffff0000, v193\n"
      "v_mfma_f32_16x16x32_bf16 v[184:187], v[36:39], v[144:147], v[184:187]\n"
      "ds_read_b128 v[116:119], v0 offset:32768\n"
      "v_fmac_f32 v219, v181, v181\n"
      "v_mfma_f32_16x16x32_bf16 v[184:187], v[40:43], v[148:151], v[184:187]\n"
      "v_add_f32 v218, v218, v182\n"
      "v_mfma_f32_16x16x32_bf16 v[184:187], v[44:47], v[152:155], v[184:187]\n"
      "ds_read2_b32 v[132:133], v4 offset0:0 offset1:32\n"
      "v_and_b32 v173, 0xffff0000, v192\n"
      "v_mfma_f32_16x16x32_bf16 v[184:187], v[48:51], v[156:159], v[184:187]\n"
      "ds_read_b128 v[128:131], v0 offset:35840\n"
      "v_lshlrev_b32 v178, 16, v195\n"
      "v_mfma_f32_16x16x32_bf16 v[184:187], v[52:55], v[160:163], v[184:187]\n"
      "ds_read_b128 v[156:159], v0 offset:25600\n"
      "v_lshlrev_b32 v172, 16, v192\n"
      "v_mfma_f32_16x16x32_bf16 v[184:187], v[56:59], v[164:167], v[184:187]\n"
      "ds_read_b128 v[144:147], v0 offset:22528\n"
      "v_lshlrev_b32 v176, 16, v194\n"
      "v_mfma_f32_16x16x32_bf16 v[184:187], v[60:63], v[168:171], v[184:187]\n"
      "s_waitcnt lgkmcnt(5)\n"
      "buffer_load_dwordx4 v[192:195], v13, s[8:11], s47 offen sc1\n"
      "v_mfma_f32_16x16x32_bf16 v[188:191], v[64:67], v[100:103], 0\n"
      "ds_read2_b32 v[134:135], v4 offset0:64 offset1:96\n"
      "v_fmac_f32 v219, v182, v182\n"
      "v_mfma_f32_16x16x32_bf16 v[188:191], v[68:71], v[104:107], v[188:191]\n"
      "ds_read_b128 v[148:151], v0 offset:23552\n"
      "v_add_f32 v218, v218, v183\n"
      "v_mfma_f32_16x16x32_bf16 v[188:191], v[72:75], v[108:111], v[188:191]\n"
      "s_mov_b32 s46, s36\n"
      "v_cvt_pk_bf16_f32 v213, v182, v183\n"
      "v_cvt_pk_bf16_f32 v215, v186, v187\n"
      "v_mfma_f32_16x16x32_bf16 v[188:191], v[76:79], v[112:115], v[188:191]\n"
      "s_waitcnt lgkmcnt(4)\n"
      "ds_read_b128 v[160:163], v0 offset:26624\n"
      "v_fmac_f32 v219, v183, v183\n"
      "v_mfma_f32_16x16x32_bf16 v[188:191], v[80:83], v[116:119], v[188:191]\n"
      "ds_read_b128 v[140:143], v0 offset:21504\n"
      "v_fmac_f32 v219, v184, v184\n"
      "v_mfma_f32_16x16x32_bf16 v[188:191], v[84:87], v[120:123], v[188:191]\n"
      "ds_read2_b32 v[168:169], v5 offset0:0 offset1:32\n"
      "v_add_f32 v218, v218, v184\n"
      "v_mfma_f32_16x16x32_bf16 v[188:191], v[88:91], v[124:127], v[188:191]\n"
      "ds_read_b128 v[152:155], v0 offset:24576\n"
      "v_fmac_f32 v219, v185, v185\n"
      "v_mfma_f32_16x16x32_bf16 v[188:191], v[92:95], v[128:131], v[188:191]\n"
      "s_waitcnt lgkmcnt(5)\n"
      "ds_read_b128 v[136:139], v0 offset:20480\n"
      "v_add_f32 v218, v218, v185\n"
      "v_mfma_f32_16x16x32_bf16 v[188:191], v[96:99], v[132:135], v[188:191]\n"
      "s_cmp_eq_u32 s36, s37\n"
      "s_cselect_b32 s48, 49152, s51\n"
      "ds_read_b128 v[164:167], v0 offset:27648\n"
      "v_mfma_f32_16x16x32_bf16 v[172:175], a[0:3], v[100:103], v[172:175]\n"
      "ds_read2_b32 v[170:171], v5 offset0:64 offset1:96\n"
      "v_cvt_pk_bf16_f32 v214, v184, v185\n"
      "v_mfma_f32_16x16x32_bf16 v[172:175], a[4:7], v[104:107], v[172:175]\n"
      "v_fmac_f32 v219, v186, v186\n"
      "v_add_f32 v218, v218, v186\n"
      "v_mfma_f32_16x16x32_bf16 v[172:175], a[8:11], v[108:111], v[172:175]\n"
      "ds_write_b32 v17, v189 offset:256\n"
      "v_mfma_f32_16x16x32_bf16 v[172:175], a[12:15], v[112:115], v[172:175]\n"
      "v_add_f32 v218, v218, v187\n"
      "v_add_u32 v21, 1, v21\n"
      "v_mfma_f32_16x16x32_bf16 v[172:175], a[16:19], v[116:119], v[172:175]\n"
      "buffer_store_dwordx4 v[212:215], v15, s[8:11], s38 offen offset:1024 sc1\n"
      "v_mfma_f32_16x16x32_bf16 v[172:175], a[20:23], v[120:123], v[172:175]\n"
      "ds_write_b32 v17, v190 offset:512\n"
      "v_mfma_f32_16x16x32_bf16 v[172:175], a[24:27], v[124:127], v[172:175]\n"
      "v_fmac_f32 v219, v187, v187\n"
      "v_mfma_f32_16x16x32_bf16 v[172:175], a[28:31], v[128:131], v[172:175]\n"
      "s_add_u32 s36, s36, s48\n"
      "ds_write_b32 v18, v218\n"
      "v_mfma_f32_16x16x32_bf16 v[172:175], a[32:35], v[132:135], v[172:175]\n"
      "s_waitcnt vmcnt(11) lgkmcnt(3)\n"
      "v_cmp_ne_u32 vcc, s21, v28\n"
      "v_mfma_f32_16x16x32_bf16 v[172:175], a[36:39], v[136:139], v[172:175]\n"
      "ds_write_b32 v17, v188 offset:0\n"
      "v_mfma_f32_16x16x32_bf16 v[172:175], a[40:43], v[140:143], v[172:175]\n"
      "s_waitcnt vmcnt(9) lgkmcnt(3)\n"
      "s_min_u32 s36, s36, s6\n"
      "v_lshlrev_b32 v182, 16, v197\n"
      "v_mfma_f32_16x16x32_bf16 v[172:175], a[44:47], v[144:147], v[172:175]\n"
      "ds_write_b32 v17, v191 offset:768\n"
      "v_mfma_f32_16x16x32_bf16 v[172:175], a[48:51], v[148:151], v[172:175]\n"
      "s_cbranch_vccnz .Lsp_v1_22\n"
      ".Lbk_v1_22:\n"
      "v_and_b32 v181, 0xffff0000, v196\n"
      "v_mfma_f32_16x16x32_bf16 v[172:175], a[52:55], v[152:155], v[172:175]\n"
      "ds_write_b32 v18, v219 offset:4\n"
      "v_mfma_f32_16x16x32_bf16 v[172:175], a[56:59], v[156:159], v[172:175]\n"
      "buffer_load_dword v28, v11, s[8:11], s36 offen sc1\n"
      "v_mfma_f32_16x16x32_bf16 v[172:175], a[60:63], v[160:163], v[172:175]\n"
      "s_add_u32 m0, s39, 61440\n"
      "ds_write_b32 v23, v21\n"
      "v_mfma_f32_16x16x32_bf16 v[172:175], a[64:67], v[164:167], v[172:175]\n"
      "v_and_b32 v187, 0xffff0000, v199\n"
      "v_and_b32 v183, 0xffff0000, v197\n"
      "v_mfma_f32_16x16x32_bf16 v[172:175], a[68:71], v[168:171], v[172:175]\n"
      "buffer_load_dwordx4 v0, s[8:11], s46 offen offset:0 sc1 lds\n"
      "v_mfma_f32_16x16x32_bf16 v[176:179], a[72:75], v[100:103], v[176:179]\n"
      "s_add_u32 s49, s49, 24576\n"
      "v_mfma_f32_16x16x32_bf16 v[176:179], a[76:79], v[104:107], v[176:179]\n"
      "v_mfma_f32_16x16x32_bf16 v[176:179], a[80:83], v[108:111], v[176:179]\n"
      "v_cvt_pk_bf16_f32 v209, v174, v175\n"
      "v_mfma_f32_16x16x32_bf16 v[176:179], a[84:87], v[112:115], v[176:179]\n"
      "buffer_load_dwordx4 v0, s[8:11], s46 offen offset:1024 sc1 lds\n"
      "v_mfma_f32_16x16x32_bf16 v[176:179], a[88:91], v[116:119], v[176:179]\n"
      "v_add_f32 v218, v172, v173\n"
      "v_mfma_f32_16x16x32_bf16 v[176:179], a[92:95], v[120:123], v[176:179]\n"
      "v_add_f32 v218, v218, v174\n"
      "v_lshlrev_b32 v180, 16, v196\n"
      "v_mfma_f32_16x16x32_bf16 v[176:179], a[96:99], v[124:127], v[176:179]\n"
      "ds_read_b32 v22, v24\n"
      "v_lshlrev_b32 v186, 16, v199\n"
      "v_mfma_f32_16x16x32_bf16 v[176:179], a[100:103], v[128:131], v[176:179]\n"
      "buffer_load_dwordx4 v0, s[8:11], s46 offen offset:2048 sc1 lds\n"
      "v_mfma_f32_16x16x32_bf16 v[176:179], a[104:107], v[132:135], v[176:179]\n"
      "v_mul_f32 v219, v172, v172\n"
      "v_fmac_f32 v219, v173, v173\n"
      "v_mfma_f32_16x16x32_bf16 v[176:179], a[108:111], v[136:139], v[176:179]\n"
      "v_lshlrev_b32 v184, 16, v198\n"
      "v_mfma_f32_16x16x32_bf16 v[176:179], a[112:115], v[140:143], v[176:179]\n"
      "v_fmac_f32_e64 v219, v174, v174\n"
      "v_mfma_f32_16x16x32_bf16 v[176:179], a[116:119], v[144:147], v[176:179]\n"
      "s_waitcnt lgkmcnt(0)\n"
      "v_cmp_gt_i32 vcc, v21, v22\n"
      "v_add_f32_e64 v218, v218, v175\n"
      "v_mfma_f32_16x16x32_bf16 v[176:179], a[120:123], v[148:151], v[176:179]\n"
      "v_mfma_f32_16x16x32_bf16 v[176:179], a[124:127], v[152:155], v[176:179]\n"
      "v_and_b32 v185, 0xffff0000, v198\n"
      "v_cvt_pk_bf16_f32 v208, v172, v173\n"
      "v_mfma_f32_16x16x32_bf16 v[176:179], a[128:131], v[156:159], v[176:179]\n"
      "buffer_load_dwordx4 v[196:199], v13, s[8:11], s47 offen offset:1024 sc1\n"
      "v_mfma_f32_16x16x32_bf16 v[176:179], a[132:135], v[160:163], v[176:179]\n"
      "s_cbranch_vccnz .Lsp_vz1_23\n"
      ".Lbk_vz1_23:\n"
      "ds_read_b128 v[204:207], v19\n"
      "v_mfma_f32_16x16x32_bf16 v[176:179], a[136:139], v[164:167], v[176:179]\n"
      "v_add_u32 v21, 1, v21\n"
      "v_mfma_f32_16x16x32_bf16 v[176:179], a[140:143], v[168:171], v[176:179]\n"
      "s_waitcnt vmcnt(10)\n"
      "ds_write_b32 v23, v21\n"
      "v_mfma_f32_16x16x32_bf16 v[180:183], a[144:147], v[100:103], v[180:183]\n"
      "v_fmac_f32 v219, v175, v175\n"
      "v_mfma_f32_16x16x32_bf16 v[180:183], a[148:151], v[104:107], v[180:183]\n"
      "buffer_load_dwordx4 v0, s[8:11], s46 offen offset:3072 sc1 lds\n"
      "v_mfma_f32_16x16x32_bf16 v[180:183], a[152:155], v[108:111], v[180:183]\n"
      "v_cvt_pk_bf16_f32 v210, v176, v177\n"
      "v_fmac_f32 v219, v176, v176\n"
      "v_mfma_f32_16x16x32_bf16 v[180:183], a[156:159], v[112:115], v[180:183]\n"
      "s_add_u32 m0, s40, 61440\n"
      "v_fmac_f32 v219, v177, v177\n"
      "v_mfma_f32_16x16x32_bf16 v[180:183], a[160:163], v[116:119], v[180:183]\n"
      "s_waitcnt lgkmcnt(1)\n"
      "v_add_f32 v204, v204, v206\n"
      "v_add_f32 v205, v205, v207\n"
      "v_mfma_f32_16x16x32_bf16 v[180:183], a[164:167], v[120:123], v[180:183]\n"
      "v_add_f32_dpp v204, v204, v204 quad_perm:[1,0,3,2] row_mask:0xf bank_mask:0xf\n"
      "v_fmac_f32 v219, v178, v178\n"
      "v_mfma_f32_16x16x32_bf16 v[180:183], a[168:171], v[124:127], v[180:183]\n"
      "buffer_load_dwordx4 v1, s[8:11], s46 offen offset:0 sc1 lds\n"
      "v_mfma_f32_16x16x32_bf16 v[180:183], a[172:175], v[128:131], v[180:183]\n"
      "v_add_f32_dpp v205, v205, v205 quad_perm:[1,0,3,2] row_mask:0xf bank_mask:0xf\n"
      "v_add_f32_dpp v204, v204, v204 quad_perm:[2,3,0,1] row_mask:0xf bank_mask:0xf\n"
      "v_mfma_f32_16x16x32_bf16 v[180:183], a[176:179], v[132:135], v[180:183]\n"
      "v_add_f32_dpp v204, v204, v204 row_half_mirror row_mask:0xf bank_mask:0xf\n"
      "v_cvt_pk_bf16_f32 v211, v178, v179\n"
      "v_mfma_f32_16x16x32_bf16 v[180:183], a[180:183], v[136:139], v[180:183]\n"
      "s_add_u32 s38, s38, 24576\n"
      "v_mul_f32 v204, v27, v204\n"
      "v_add_f32_dpp v205, v205, v205 quad_perm:[2,3,0,1] row_mask:0xf bank_mask:0xf\n"
      "v_mfma_f32_16x16x32_bf16 v[180:183], a[184:187], v[140:143], v[180:183]\n"
      "ds_read_b32 v22, v24\n"
      "v_add_f32 v218, v218, v176\n"
      "v_mfma_f32_16x16x32_bf16 v[180:183], a[188:191], v[144:147], v[180:183]\n"
      "v_add_f32_dpp v205, v205, v205 row_half_mirror row_mask:0xf bank_mask:0xf\n"
      "v_fmaak_f32 v205, v27, v205, 0x36a7c5ac\n"
      "v_mfma_f32_16x16x32_bf16 v[180:183], a[192:195], v[148:151], v[180:183]\n"
      "buffer_store_dwordx2 v[204:205], v20, s[8:11], s49 offen sc1\n"
      "v_mfma_f32_16x16x32_bf16 v[180:183], a[196:199], v[152:155], v[180:183]\n"
      "s_add_u32 s50, s50, 24576\n"
      "v_add_f32 v218, v218, v177\n"
      "v_mfma_f32_16x16x32_bf16 v[180:183], a[200:203], v[156:159], v[180:183]\n"
      "v_add_f32 v218, v218, v178\n"
      "v_mfma_f32_16x16x32_bf16 v[180:183], a[204:207], v[160:163], v[180:183]\n"
      "s_waitcnt lgkmcnt(0)\n"
      "v_add_f32 v218, v218, v179\n"
      "v_cmp_gt_i32 vcc, v21, v22\n"
      "v_mfma_f32_16x16x32_bf16 v[180:183], a[208:211], v[164:167], v[180:183]\n"
      "s_waitcnt vmcnt(10)\n"
      "buffer_store_dword v25, v12, s[8:11], s50 offen sc1\n"
      "v_mfma_f32_16x16x32_bf16 v[180:183], a[212:215], v[168:171], v[180:183]\n"
      "v_fmac_f32 v219, v179, v179\n"
      "v_mfma_f32_16x16x32_bf16 v[184:187], a[216:219], v[100:103], v[184:187]\n"
      "s_cbranch_vccnz .Lsp_vy0_24\n"
      ".Lbk_vy0_24:\n"
      "s_sub_u32 s47, s46, s45\n"
      "v_mfma_f32_16x16x32_bf16 v[184:187], a[220:223], v[104:107], v[184:187]\n"
      "s_waitcnt vmcnt(10)\n"
      "v_lshlrev_b32 v174, 16, v193\n"
      "v_and_b32 v179, 0xffff0000, v195\n"
      "v_mfma_f32_16x16x32_bf16 v[184:187], a[224:227], v[108:111], v[184:187]\n"
      "ds_read_b128 v[100:103], v10 offset:8192\n"
      "v_cvt_pk_bf16_f32 v213, v182, v183\n"
      "v_mfma_f32_16x16x32_bf16 v[184:187], a[228:231], v[112:115], v[184:187]\n"
      "ds_read_b128 v[108:111], v10 offset:10240\n"
      "v_and_b32 v173, 0xffff0000, v192\n"
      "v_mfma_f32_16x16x32_bf16 v[184:187], a[232:235], v[116:119], v[184:187]\n"
      "v_fmac_f32 v219, v180, v180\n"
      "v_mfma_f32_16x16x32_bf16 v[184:187], a[236:239], v[120:123], v[184:187]\n"
      "ds_read_b128 v[120:123], v10 offset:13312\n"
      "v_add_f32 v218, v218, v180\n"
      "v_mfma_f32_16x16x32_bf16 v[184:187], a[240:243], v[124:127], v[184:187]\n"
      "v_add_f32 v218, v218, v181\n"
      "v_add_f32 v218, v218, v182\n"
      "v_mfma_f32_16x16x32_bf16 v[184:187], a[244:247], v[128:131], v[184:187]\n"
      "ds_read_b128 v[128:131], v10 offset:15360\n"
      "v_lshlrev_b32 v178, 16, v195\n"
      "v_mfma_f32_16x16x32_bf16 v[184:187], a[248:251], v[132:135], v[184:187]\n"
      "v_mfma_f32_16x16x32_bf16 v[184:187], a[252:255], v[136:139], v[184:187]\n"
      "ds_read_b128 v[104:107], v10 offset:9216\n"
      "v_lshlrev_b32 v172, 16, v192\n"
      "v_mfma_f32_16x16x32_bf16 v[184:187], v[32:35], v[140:143], v[184:187]\n"
      "buffer_store_dwordx4 v[208:211], v15, s[8:11], s38 offen sc1\n"
      "v_mfma_f32_16x16x32_bf16 v[184:187], v[36:39], v[144:147], v[184:187]\n"
      "s_mov_b32 s46, s36\n"
      "ds_read_b128 v[112:115], v10 offset:11264\n"
      "v_fmac_f32 v219, v181, v181\n"
      "v_mfma_f32_16x16x32_bf16 v[184:187], v[40:43], v[148:151], v[184:187]\n"
      "ds_read2_b32 v[134:135], v6 offset0:64 offset1:96\n"
      "v_cvt_pk_bf16_f32 v212, v180, v181\n"
      "v_mfma_f32_16x16x32_bf16 v[184:187], v[44:47], v[152:155], v[184:187]\n"
      "ds_read2_b32 v[132:133], v6 offset0:0 offset1:32\n"
      "s_add_u32 s36, s36, 24576\n"
      "v_mfma_f32_16x16x32_bf16 v[184:187], v[48:51], v[156:159], v[184:187]\n"
      "ds_read_b128 v[116:119], v10 offset:12288\n"
      "v_fmac_f32 v219, v182, v182\n"
      "v_mfma_f32_16x16x32_bf16 v[184:187], v[52:55], v[160:163], v[184:187]\n"
      "ds_read_b128 v[124:127], v10 offset:14336\n"
      "v_add_f32 v218, v218, v183\n"
      "v_mfma_f32_16x16x32_bf16 v[184:187], v[56:59], v[164:167], v[184:187]\n"
      "s_waitcnt lgkmcnt(5)\n"
      "ds_read_b128 v[160:163], v10 offset:6144\n"
      "v_and_b32 v177, 0xffff0000, v194\n"
      "v_mfma_f32_16x16x32_bf16 v[184:187], v[60:63], v[168:171], v[184:187]\n"
      "s_sub_u32 s20, s20, 1\n"
      "s_cbranch_scc1 .Lv_tail\n"
      "v_mfma_f32_16x16x32_bf16 v[188:191], v[64:67], v[100:103], 0\n"
      "ds_read_b128 v[140:143], v10 offset:1024\n"
      "v_and_b32 v175, 0xffff0000, v193\n"
      "v_mfma_f32_16x16x32_bf16 v[188:191], v[68:71], v[104:107], v[188:191]\n"
      "ds_read2_b32 v[168:169], v7 offset0:0 offset1:32\n"
      "v_lshlrev_b32 v176, 16, v194\n"
      "v_mfma_f32_16x16x32_bf16 v[188:191], v[72:75], v[108:111], v[188:191]\n"
      "s_waitcnt lgkmcnt(5)\n"
      "ds_read_b128 v[164:167], v10 offset:7168\n"
      "v_add_f32 v218, v218, v184\n"
      "v_mfma_f32_16x16x32_bf16 v[188:191], v[76:79], v[112:115], v[188:191]\n"
      "s_waitcnt lgkmcnt(5)\n"
      "ds_read_b128 v[144:147], v10 offset:2048\n"
      "v_add_f32 v218, v218, v185\n"
      "v_mfma_f32_16x16x32_bf16 v[188:191], v[80:83], v[116:119], v[188:191]\n"
      "buffer_load_dwordx4 v[192:195], v13, s[8:11], s47 offen sc1\n"
      "v_mfma_f32_16x16x32_bf16 v[188:191], v[84:87], v[120:123], v[188:191]\n"
      "s_waitcnt lgkmcnt(4)\n"
      "ds_read2_b32 v[170:171], v7 offset0:64 offset1:96\n"
      "v_fmac_f32 v219, v183, v183\n"
      "v_mfma_f32_16x16x32_bf16 v[188:191], v[88:91], v[124:127], v[188:191]\n"
      "s_min_u32 s36, s36, s6\n"
      "ds_read_b128 v[136:139], v10 offset:0\n"
      "v_fmac_f32 v219, v184, v184\n"
      "v_mfma_f32_16x16x32_bf16 v[188:191], v[92:95], v[128:131], v[188:191]\n"
      "ds_read_b128 v[148:151], v10 offset:3072\n"
      "v_mfma_f32_16x16x32_bf16 v[188:191], v[96:99], v[132:135], v[188:191]\n"
      "v_cvt_pk_bf16_f32 v215, v186, v187\n"
      "v_add_f32 v218, v218, v186\n"
      "v_mfma_f32_16x16x32_bf16 v[172:175], a[0:3], v[100:103], v[172:175]\n"
      "ds_read_b128 v[152:155], v10 offset:4096\n"
      "v_add_f32 v218, v218, v187\n"
      "v_mfma_f32_16x16x32_bf16 v[172:175], a[4:7], v[104:107], v[172:175]\n"
      "ds_read_b128 v[156:159], v10 offset:5120\n"
      "v_fmac_f32 v219, v185, v185\n"
      "v_mfma_f32_16x16x32_bf16 v[172:175], a[8:11], v[108:111], v[172:175]\n"
      "s_add_u32 m0, s39, 0\n"
      "v_cvt_pk_bf16_f32 v214, v184, v185\n"
      "v_mfma_f32_16x16x32_bf16 v[172:175], a[12:15], v[112:115], v[172:175]\n"
      "ds_write_b32 v18, v218\n"
      "v_mfma_f32_16x16x32_bf16 v[172:175], a[16:19], v[116:119], v[172:175]\n"
      "s_waitcnt vmcnt(10) lgkmcnt(4)\n"
      "v_fmac_f32 v219, v186, v186\n"
      "v_cmp_ne_u32 vcc, s21, v28\n"
      "v_mfma_f32_16x16x32_bf16 v[172:175], a[20:23], v[120:123], v[172:175]\n"
      "ds_write_b32 v17, v190 offset:512\n"
      "v_mfma_f32_16x16x32_bf16 v[172:175], a[24:27], v[124:127], v[172:175]\n"
      "buffer_store_dwordx4 v[212:215], v15, s[8:11], s38 offen offset:1024 sc1\n"
      "v_mfma_f32_16x16x32_bf16 v[172:175], a[28:31], v[128:131], v[172:175]\n"
      "ds_write_b32 v17, v188 offset:0\n"
      "v_mfma_f32_16x16x32_bf16 v[172:175], a[32:35], v[132:135], v[172:175]\n"
      "v_fmac_f32_e64 v219, v187, v187\n"
      "v_mfma_f32_16x16x32_bf16 v[172:175], a[36:39], v[136:139], v[172:175]\n"
      "ds_write_b32 v17, v191 offset:768\n"
      "v_mfma_f32_16x16x32_bf16 v[172:175], a[40:43], v[140:143], v[172:175]\n"
      "s_cbranch_vccnz .Lsp_v0_25\n"
      ".Lbk_v0_25:\n"
      "v_add_u32 v21, 1, v21\n"
      "v_mfma_f32_16x16x32_bf16 v[172:175], a[44:47], v[144:147], v[172:175]\n"
      "s_waitcnt lgkmcnt(3)\n"
      "ds_write_b32 v18, v219 offset:4\n"
      "v_mfma_f32_16x16x32_bf16 v[172:175], a[48:51], v[148:151], v[172:175]\n"
      "buffer_load_dword v28, v11, s[8:11], s36 offen sc1\n"
      "v_mfma_f32_16x16x32_bf16 v[172:175], a[52:55], v[152:155], v[172:175]\n"
      "ds_write_b32 v17, v189 offset:256\n"
      "v_mfma_f32_16x16x32_bf16 v[172:175], a[56:59], v[156:159], v[172:175]\n"
      "s_waitcnt vmcnt(8) lgkmcnt(3)\n"
      "v_lshlrev_b32 v186, 16, v199\n"
      "v_and_b32 v185, 0xffff0000, v198\n"
      "v_mfma_f32_16x16x32_bf16 v[172:175], a[60:63], v[160:163], v[172:175]\n"
      "ds_write_b32 v23, v21\n"
      "v_mfma_f32_16x16x32_bf16 v[172:175], a[64:67], v[164:167], v[172:175]\n"
      "v_and_b32 v181, 0xffff0000, v196\n"
      "v_and_b32 v187, 0xffff0000, v199\n"
      "v_mfma_f32_16x16x32_bf16 v[172:175], a[68:71], v[168:171], v[172:175]\n"
      "buffer_load_dwordx4 v0, s[8:11], s46 offen offset:3072 sc1 lds\n"
      "v_mfma_f32_16x16x32_bf16 v[176:179], a[72:75], v[100:103], v[176:179]\n"
      "v_lshlrev_b32 v182, 16, v197\n"
      "s_add_u32 s38, s38, 24576\n"
      "v_mfma_f32_16x16x32_bf16 v[176:179], a[76:79], v[104:107], v[176:179]\n"
      "v_lshlrev_b32 v184, 16, v198\n"
      "v_mfma_f32_16x16x32_bf16 v[176:179], a[80:83], v[108:111], v[176:179]\n"
      "v_cvt_pk_bf16_f32 v208, v172, v173\n"
      "v_mfma_f32_16x16x32_bf16 v[176:179], a[84:87], v[112:115], v[176:179]\n"
      "v_mul_f32 v219, v172, v172\n"
      "v_mfma_f32_16x16x32_bf16 v[176:179], a[88:91], v[116:119], v[176:179]\n"
      "v_lshlrev_b32 v180, 16, v196\n"
      "v_and_b32 v183, 0xffff0000, v197\n"
      "v_mfma_f32_16x16x32_bf16 v[176:179], a[92:95], v[120:123], v[176:179]\n"
      "v_fmac_f32 v219, v173, v173\n"
      "v_mfma_f32_16x16x32_bf16 v[176:179], a[96:99], v[124:127], v[176:179]\n"
      "buffer_load_dwordx4 v[196:199], v13, s[8:11], s47 offen offset:1024 sc1\n"
      "v_mfma_f32_16x16x32_bf16 v[176:179], a[100:103], v[128:131], v[176:179]\n"
      "ds_read_b32 v22, v24\n"
      "v_mfma_f32_16x16x32_bf16 v[176:179], a[104:107], v[132:135], v[176:179]\n"
      "v_fmac_f32 v219, v174, v174\n"
      "v_mfma_f32_16x16x32_bf16 v[176:179], a[108:111], v[136:139], v[176:179]\n"
      "v_fmac_f32 v219, v175, v175\n"
      "v_mfma_f32_16x16x32_bf16 v[176:179], a[112:115], v[140:143], v[176:179]\n"
      "buffer_load_dwordx4 v0, s[8:11], s46 offen offset:0 sc1 lds\n"
      "v_mfma_f32_16x16x32_bf16 v[176:179], a[116:119], v[144:147], v[176:179]\n"
      "v_mfma_f32_16x16x32_bf16 v[176:179], a[120:123], v[148:151], v[176:179]\n"
      "s_waitcnt lgkmcnt(0)\n"
      "v_cmp_gt_i32 vcc, v21, v22\n"
      "v_add_f32_e64 v218, v172, v173\n"
      "v_mfma_f32_16x16x32_bf16 v[176:179], a[124:127], v[152:155], v[176:179]\n"
      "v_add_f32_e64 v218, v218, v174\n"
      "v_add_f32_e64 v218, v218, v175\n"
      "v_mfma_f32_16x16x32_bf16 v[176:179], a[128:131], v[156:159], v[176:179]\n"
      "buffer_load_dwordx4 v0, s[8:11], s46 offen offset:1024 sc1 lds\n"
      "v_mfma_f32_16x16x32_bf16 v[176:179], a[132:135], v[160:163], v[176:179]\n"
      "s_cbranch_vccnz .Lsp_vz0_26\n"
      ".Lbk_vz0_26:\n"
      "ds_read_b128 v[204:207], v19\n"
      "v_mfma_f32_16x16x32_bf16 v[176:179], a[136:139], v[164:167], v[176:179]\n"
      "v_cvt_pk_bf16_f32 v209, v174, v175\n"
      "v_add_u32 v21, 1, v21\n"
      "v_mfma_f32_16x16x32_bf16 v[176:179], a[140:143], v[168:171], v[176:179]\n"
      "s_waitcnt vmcnt(10)\n"
      "ds_write_b32 v23, v21\n"
      "v_mfma_f32_16x16x32_bf16 v[180:183], a[144:147], v[100:103], v[180:183]\n"
      "v_mfma_f32_16x16x32_bf16 v[180:183], a[148:151], v[104:107], v[180:183]\n"
      "buffer_load_dwordx4 v0, s[8:11], s46 offen offset:2048 sc1 lds\n"
      "v_mfma_f32_16x16x32_bf16 v[180:183], a[152:155], v[108:111], v[180:183]\n"
      "s_add_u32 s49, s49, 24576\n"
      "s_add_u32 m0, s40, 0\n"
      "v_mfma_f32_16x16x32_bf16 v[180:183], a[156:159], v[112:115], v[180:183]\n"
      "v_mfma_f32_16x16x32_bf16 v[180:183], a[160:163], v[116:119], v[180:183]\n"
      "s_waitcnt lgkmcnt(1)\n"
      "v_add_f32 v204, v204, v206\n"
      "v_cvt_pk_bf16_f32 v210, v176, v177\n"
      "v_mfma_f32_16x16x32_bf16 v[180:183], a[164:167], v[120:123], v[180:183]\n"
      "buffer_load_dwordx4 v1, s[8:11], s46 offen offset:0 sc1 lds\n"
      "v_mfma_f32_16x16x32_bf16 v[180:183], a[168:171], v[124:127], v[180:183]\n"
      "v_add_f32 v205, v205, v207\n"
      "v_add_f32_dpp v204, v204, v204 quad_perm:[1,0,3,2] row_mask:0xf bank_mask:0xf\n"
      "v_mfma_f32_16x16x32_bf16 v[180:183], a[172:175], v[128:131], v[180:183]\n"
      "v_fmac_f32 v219, v176, v176\n"
      "v_mfma_f32_16x16x32_bf16 v[180:183], a[176:179], v[132:135], v[180:183]\n"
      "v_add_f32_dpp v204, v204, v204 quad_perm:[2,3,0,1] row_mask:0xf bank_mask:0xf\n"
      "v_cvt_pk_bf16_f32 v211, v178, v179\n"
      "v_mfma_f32_16x16x32_bf16 v[180:183], a[180:183], v[136:139], v[180:183]\n"
      "buffer_store_dwordx4 v[208:211], v15, s[8:11], s38 offen sc1\n"
      "v_mfma_f32_16x16x32_bf16 v[180:183], a[184:187], v[140:143], v[180:183]\n"
      "ds_read_b32 v22, v24\n"
      "v_add_f32_dpp v205, v205, v205 quad_perm:[1,0,3,2] row_mask:0xf bank_mask:0xf\n"
      "v_mfma_f32_16x16x32_bf16 v[180:183], a[188:191], v[144:147], v[180:183]\n"
      "s_add_u32 s50, s50, 24576\n"
      "v_add_f32_dpp v205, v205, v205 quad_perm:[2,3,0,1] row_mask:0xf bank_mask:0xf\n"
      "v_fmac_f32 v219, v177, v177\n"
      "v_mfma_f32_16x16x32_bf16 v[180:183], a[192:195], v[148:151], v[180:183]\n"
      "v_add_f32_dpp v205, v205, v205 row_half_mirror row_mask:0xf bank_mask:0xf\n"
      "v_fmac_f32 v219, v178, v178\n"
      "v_mfma_f32_16x16x32_bf16 v[180:183], a[196:199], v[152:155], v[180:183]\n"
      ".Lv_p1:\n"
      "s_cbranch_execz .Lv_pr\n"
      "v_add_f32_dpp v204, v204, v204 row_half_mirror row_mask:0xf bank_mask:0xf\n"
      "v_mfma_f32_16x16x32_bf16 v[180:183], a[200:203], v[156:159], v[180:183]\n"
      "v_mul_f32 v204, v27, v204\n"
      "v_fmaak_f32 v205, v27, v205, 0x36a7c5ac\n"
      "v_mfma_f32_16x16x32_bf16 v[180:183], a[204:207], v[160:163], v[180:183]\n"
      "s_waitcnt vmcnt(12) lgkmcnt(1)\n"
      "buffer_store_dword v25, v12, s[8:11], s50 offen sc1\n"
      "v_mfma_f32_16x16x32_bf16 v[180:183], a[208:211], v[164:167], v[180:183]\n"
      "s_waitcnt lgkmcnt(0)\n"
      "v_cmp_gt_i32 vcc, v21, v22\n"
      "v_add_f32 v218, v218, v176\n"
      "v_mfma_f32_16x16x32_bf16 v[180:183], a[212:215], v[168:171], v[180:183]\n"
      "v_add_f32 v218, v218, v177\n"
      "v_add_f32_e64 v218, v218, v178\n"
      "v_mfma_f32_16x16x32_bf16 v[184:187], a[216:219], v[100:103], v[184:187]\n"
      "buffer_store_dwordx2 v[204:205], v20, s[8:11], s49 offen sc1\n"
      "v_mfma_f32_16x16x32_bf16 v[184:187], a[220:223], v[104:107], v[184:187]\n"
      "s_sub_u32 s47, s46, s45\n"
      "s_cbranch_vccnz .Lsp_vy1_27\n"
      ".Lbk_vy1_27:\n"
      "v_mfma_f32_16x16x32_bf16 v[184:187], a[224:227], v[108:111], v[184:187]\n"
      "s_waitcnt vmcnt(11)\n"
      "ds_read_b128 v[104:107], v10 offset:29696\n"
      "v_and_b32 v177, 0xffff0000, v194\n"
      "v_mfma_f32_16x16x32_bf16 v[184:187], a[228:231], v[112:115], v[184:187]\n"
      "v_fmac_f32 v219, v179, v179\n"
      "v_mfma_f32_16x16x32_bf16 v[184:187], a[232:235], v[116:119], v[184:187]\n"
      "v_fmac_f32 v219, v180, v180\n"
      "v_add_f32 v218, v218, v179\n"
      "v_mfma_f32_16x16x32_bf16 v[184:187], a[236:239], v[120:123], v[184:187]\n"
      "v_cvt_pk_bf16_f32 v212, v180, v181\n"
      "v_and_b32 v179, 0xffff0000, v195\n"
      "v_mfma_f32_16x16x32_bf16 v[184:187], a[240:243], v[124:127], v[184:187]\n"
      "ds_read_b128 v[120:123], v10 offset:33792\n"
      "v_lshlrev_b32 v174, 16, v193\n"
      "v_mfma_f32_16x16x32_bf16 v[184:187], a[244:247], v[128:131], v[184:187]\n"
      "ds_read_b128 v[112:115], v10 offset:31744\n"
      "v_add_f32 v218, v218, v180\n"
      "v_mfma_f32_16x16x32_bf16 v[184:187], a[248:251], v[132:135], v[184:187]\n"
      "ds_read_b128 v[100:103], v10 offset:28672\n"
      "v_add_f32 v218, v218, v181\n"
      "v_mfma_f32_16x16x32_bf16 v[184:187], a[252:255], v[136:139], v[184:187]\n"
      "ds_read_b128 v[124:127], v10 offset:34816\n"
      "v_mfma_f32_16x16x32_bf16 v[184:187], v[32:35], v[140:143], v[184:187]\n"
      "ds_read_b128 v[108:111], v10 offset:30720\n"
      "v_and_b32 v175, 0xffff0000, v193\n"
      "v_mfma_f32_16x16x32_bf16 v[184:187], v[36:39], v[144:147], v[184:187]\n"
      "ds_read_b128 v[116:119], v10 offset:32768\n"
      "v_fmac_f32 v219, v181, v181\n"
      "v_mfma_f32_16x16x32_bf16 v[184:187], v[40:43], v[148:151], v[184:187]\n"
      "v_add_f32 v218, v218, v182\n"
      "v_mfma_f32_16x16x32_bf16 v[184:187], v[44:47], v[152:155], v[184:187]\n"
      "ds_read2_b32 v[132:133], v8 offset0:0 offset1:32\n"
      "v_and_b32 v173, 0xffff0000, v192\n"
      "v_mfma_f32_16x16x32_bf16 v[184:187], v[48:51], v[156:159], v[184:187]\n"
      "ds_read_b128 v[128:131], v10 offset:35840\n"
      "v_lshlrev_b32 v178, 16, v195\n"
      "v_mfma_f32_16x16x32_bf16 v[184:187], v[52:55], v[160:163], v[184:187]\n"
      "ds_read_b128 v[156:159], v10 offset:25600\n"
      "v_lshlrev_b32 v172, 16, v192\n"
      "v_mfma_f32_16x16x32_bf16 v[184:187], v[56:59], v[164:167], v[184:187]\n"
      "ds_read_b128 v[144:147], v10 offset:22528\n"
      "v_lshlrev_b32 v176, 16, v194\n"
      "v_mfma_f32_16x16x32_bf16 v[184:187], v[60:63], v[168:171], v[184:187]\n"
      "s_waitcnt lgkmcnt(5)\n"
      "buffer_load_dwordx4 v[192:195], v13, s[8:11], s47 offen sc1\n"
      "v_mfma_f32_16x16x32_bf16 v[188:191], v[64:67], v[100:103], 0\n"
      "ds_read2_b32 v[134:135], v8 offset0:64 offset1:96\n"
      "v_fmac_f32 v219, v182, v182\n"
      "v_mfma_f32_16x16x32_bf16 v[188:191], v[68:71], v[104:107], v[188:191]\n"
      "ds_read_b128 v[148:151], v10 offset:23552\n"
      "v_add_f32 v218, v218, v183\n"
      "v_mfma_f32_16x16x32_bf16 v[188:191], v[72:75], v[108:111], v[188:191]\n"
      "s_mov_b32 s46, s36\n"
      "v_cvt_pk_bf16_f32 v213, v182, v183\n"
      "v_cvt_pk_bf16_f32 v215, v186, v187\n"
      "v_mfma_f32_16x16x32_bf16 v[188:191], v[76:79], v[112:115], v[188:191]\n"
      "s_waitcnt lgkmcnt(4)\n"
      "ds_read_b128 v[160:163], v10 offset:26624\n"
      "v_fmac_f32 v219, v183, v183\n"
      "v_mfma_f32_16x16x32_bf16 v[188:191], v[80:83], v[116:119], v[188:191]\n"
      "ds_read_b128 v[140:143], v10 offset:21504\n"
      "v_fmac_f32 v219, v184, v184\n"
      "v_mfma_f32_16x16x32_bf16 v[188:191], v[84:87], v[120:123], v[188:191]\n"
      "ds_read2_b32 v[168:169], v9 offset0:0 offset1:32\n"
      "v_add_f32 v218, v218, v184\n"
      "v_mfma_f32_16x16x32_bf16 v[188:191], v[88:91], v[124:127], v[188:191]\n"
      "ds_read_b128 v[152:155], v10 offset:24576\n"
      "v_fmac_f32 v219, v185, v185\n"
      "v_mfma_f32_16x16x32_bf16 v[188:191], v[92:95], v[128:131], v[188:191]\n"
      "s_waitcnt lgkmcnt(5)\n"
      "ds_read_b128 v[136:139], v10 offset:20480\n"
      "v_add_f32 v218, v218, v185\n"
      "v_mfma_f32_16x16x32_bf16 v[188:191], v[96:99], v[132:135], v[188:191]\n"
      "s_cmp_eq_u32 s36, s37\n"
      "s_cselect_b32 s48, 49152, s51\n"
      "ds_read_b128 v[164:167], v10 offset:27648\n"
      "v_mfma_f32_16x16x32_bf16 v[172:175], a[0:3], v[100:103], v[172:175]\n"
      "ds_read2_b32 v[170:171], v9 offset0:64 offset1:96\n"
      "v_cvt_pk_bf16_f32 v214, v184, v185\n"
      "v_mfma_f32_16x16x32_bf16 v[172:175], a[4:7], v[104:107], v[172:175]\n"
      "v_fmac_f32 v219, v186, v186\n"
      "v_add_f32 v218, v218, v186\n"
      "v_mfma_f32_16x16x32_bf16 v[172:175], a[8:11], v[108:111], v[172:175]\n"
      "ds_write_b32 v17, v189 offset:256\n"
      "v_mfma_f32_16x16x32_bf16 v[172:175], a[12:15], v[112:115], v[172:175]\n"
      "v_add_f32 v218, v218, v187\n"
      "v_add_u32 v21, 1, v21\n"
      "v_mfma_f32_16x16x32_bf16 v[172:175], a[16:19], v[116:119], v[172:175]\n"
      "buffer_store_dwordx4 v[212:215], v15, s[8:11], s38 offen offset:1024 sc1\n"
      "v_mfma_f32_16x16x32_bf16 v[172:175], a[20:23], v[120:123], v[172:175]\n"
      "ds_write_b32 v17, v190 offset:512\n"
      "v_mfma_f32_16x16x32_bf16 v[172:175], a[24:27], v[124:127], v[172:175]\n"
      "v_fmac_f32 v219, v187, v187\n"
      "v_mfma_f32_16x16x32_bf16 v[172:175], a[28:31], v[128:131], v[172:175]\n"
      "s_add_u32 s36, s36, s48\n"
      "ds_write_b32 v18, v218\n"
      "v_mfma_f32_16x16x32_bf16 v[172:175], a[32:35], v[132:135], v[172:175]\n"
      "s_waitcnt vmcnt(11) lgkmcnt(3)\n"
      "v_cmp_ne_u32 vcc, s21, v28\n"
      "v_mfma_f32_16x16x32_bf16 v[172:175], a[36:39], v[136:139], v[172:175]\n"
      "ds_write_b32 v17, v188 offset:0\n"
      "v_mfma_f32_16x16x32_bf16 v[172:175], a[40:43], v[140:143], v[172:175]\n"
      "s_waitcnt vmcnt(9) lgkmcnt(3)\n"
      "s_min_u32 s36, s36, s6\n"
      "v_lshlrev_b32 v182, 16, v197\n"
      "v_mfma_f32_16x16x32_bf16 v[172:175], a[44:47], v[144:147], v[172:175]\n"
      "ds_write_b32 v17, v191 offset:768\n"
      "v_mfma_f32_16x16x32_bf16 v[172:175], a[48:51], v[148:151], v[172:175]\n"
      "s_cbranch_vccnz .Lsp_v1_28\n"
      ".Lbk_v1_28:\n"
      "v_and_b32 v181, 0xffff0000, v196\n"
      "v_mfma_f32_16x16x32_bf16 v[172:175], a[52:55], v[152:155], v[172:175]\n"
      "ds_write_b32 v18, v219 offset:4\n"
      "v_mfma_f32_16x16x32_bf16 v[172:175], a[56:59], v[156:159], v[172:175]\n"
      "buffer_load_dword v28, v11, s[8:11], s36 offen sc1\n"
      "v_mfma_f32_16x16x32_bf16 v[172:175], a[60:63], v[160:163], v[172:175]\n"
      "s_add_u32 m0, s39, 20480\n"
      "ds_write_b32 v23, v21\n"
      "v_mfma_f32_16x16x32_bf16 v[172:175], a[64:67], v[164:167], v[172:175]\n"
      "v_and_b32 v187, 0xffff0000, v199\n"
      "v_and_b32 v183, 0xffff0000, v197\n"
      "v_mfma_f32_16x16x32_bf16 v[172:175], a[68:71], v[168:171], v[172:175]\n"
      "buffer_load_dwordx4 v0, s[8:11], s46 offen offset:0 sc1 lds\n"
      "v_mfma_f32_16x16x32_bf16 v[176:179], a[72:75], v[100:103], v[176:179]\n"
      "s_add_u32 s49, s49, 24576\n"
      "v_mfma_f32_16x16x32_bf16 v[176:179], a[76:79], v[104:107], v[176:179]\n"
      "v_mfma_f32_16x16x32_bf16 v[176:179], a[80:83], v[108:111], v[176:179]\n"
      "v_cvt_pk_bf16_f32 v209, v174, v175\n"
      "v_mfma_f32_16x16x32_bf16 v[176:179], a[84:87], v[112:115], v[176:179]\n"
      "buffer_load_dwordx4 v0, s[8:11], s46 offen offset:1024 sc1 lds\n"
      "v_mfma_f32_16x16x32_bf16 v[176:179], a[88:91], v[116:119], v[176:179]\n"
      "v_add_f32 v218, v172, v173\n"
      "v_mfma_f32_16x16x32_bf16 v[176:179], a[92:95], v[120:123], v[176:179]\n"
      "v_add_f32 v218, v218, v174\n"
      "v_lshlrev_b32 v180, 16, v196\n"
      "v_mfma_f32_16x16x32_bf16 v[176:179], a[96:99], v[124:127], v[176:179]\n"
      "ds_read_b32 v22, v24\n"
      "v_lshlrev_b32 v186, 16, v199\n"
      "v_mfma_f32_16x16x32_bf16 v[176:179], a[100:103], v[128:131], v[176:179]\n"
      "buffer_load_dwordx4 v0, s[8:11], s46 offen offset:2048 sc1 lds\n"
      "v_mfma_f32_16x16x32_bf16 v[176:179], a[104:107], v[132:135], v[176:179]\n"
      "v_mul_f32 v219, v172, v172\n"
      "v_fmac_f32 v219, v173, v173\n"
      "v_mfma_f32_16x16x32_bf16 v[176:179], a[108:111], v[136:139], v[176:179]\n"
      "v_lshlrev_b32 v184, 16, v198\n"
      "v_mfma_f32_16x16x32_bf16 v[176:179], a[112:115], v[140:143], v[176:179]\n"
      "v_fmac_f32_e64 v219, v174, v174\n"
      "v_mfma_f32_16x16x32_bf16 v[176:179], a[116:119], v[144:147], v[176:179]\n"
      "s_waitcnt lgkmcnt(0)\n"
      "v_cmp_gt_i32 vcc, v21, v22\n"
      "v_add_f32_e64 v218, v218, v175\n"
      "v_mfma_f32_16x16x32_bf16 v[176:179], a[120:123], v[148:151], v[176:179]\n"
      "v_mfma_f32_16x16x32_bf16 v[176:179], a[124:127], v[152:155], v[176:179]\n"
      "v_and_b32 v185, 0xffff0000, v198\n"
      "v_cvt_pk_bf16_f32 v208, v172, v173\n"
      "v_mfma_f32_16x16x32_bf16 v[176:179], a[128:131], v[156:159], v[176:179]\n"
      "buffer_load_dwordx4 v[196:199], v13, s[8:11], s47 offen offset:1024 sc1\n"
      "v_mfma_f32_16x16x32_bf16 v[176:179], a[132:135], v[160:163], v[176:179]\n"
      "s_cbranch_vccnz .Lsp_vz1_29\n"
      ".Lbk_vz1_29:\n"
      "ds_read_b128 v[204:207], v19\n"
      "v_mfma_f32_16x16x32_bf16 v[176:179], a[136:139], v[164:167], v[176:179]\n"
      "v_add_u32 v21, 1, v21\n"
      "v_mfma_f32_16x16x32_bf16 v[176:179], a[140:143], v[168:171], v[176:179]\n"
      "s_waitcnt vmcnt(10)\n"
      "ds_write_b32 v23, v21\n"
      "v_mfma_f32_16x16x32_bf16 v[180:183], a[144:147], v[100:103], v[180:183]\n"
      "v_fmac_f32 v219, v175, v175\n"
      "v_mfma_f32_16x16x32_bf16 v[180:183], a[148:151], v[104:107], v[180:183]\n"
      "buffer_load_dwordx4 v0, s[8:11], s46 offen offset:3072 sc1 lds\n"
      "v_mfma_f32_16x16x32_bf16 v[180:183], a[152:155], v[108:111], v[180:183]\n"
      "v_cvt_pk_bf16_f32 v210, v176, v177\n"
      "v_fmac_f32 v219, v176, v176\n"
      "v_mfma_f32_16x16x32_bf16 v[180:183], a[156:159], v[112:115], v[180:183]\n"
      "s_add_u32 m0, s40, 20480\n"
      "v_fmac_f32 v219, v177, v177\n"
      "v_mfma_f32_16x16x32_bf16 v[180:183], a[160:163], v[116:119], v[180:183]\n"
      "s_waitcnt lgkmcnt(1)\n"
      "v_add_f32 v204, v204, v206\n"
      "v_add_f32 v205, v205, v207\n"
      "v_mfma_f32_16x16x32_bf16 v[180:183], a[164:167], v[120:123], v[180:183]\n"
      "v_add_f32_dpp v204, v204, v204 quad_perm:[1,0,3,2] row_mask:0xf bank_mask:0xf\n"
      "v_fmac_f32 v219, v178, v178\n"
      "v_mfma_f32_16x16x32_bf16 v[180:183], a[168:171], v[124:127], v[180:183]\n"
      "buffer_load_dwordx4 v1, s[8:11], s46 offen offset:0 sc1 lds\n"
      "v_mfma_f32_16x16x32_bf16 v[180:183], a[172:175], v[128:131], v[180:183]\n"
      "v_add_f32_dpp v205, v205, v205 quad_perm:[1,0,3,2] row_mask:0xf bank_mask:0xf\n"
      "v_add_f32_dpp v204, v204, v204 quad_perm:[2,3,0,1] row_mask:0xf bank_mask:0xf\n"
      "v_mfma_f32_16x16x32_bf16 v[180:183], a[176:179], v[132:135], v[180:183]\n"
      "v_add_f32_dpp v204, v204, v204 row_half_mirror row_mask:0xf bank_mask:0xf\n"
      "v_cvt_pk_bf16_f32 v211, v178, v179\n"
      "v_mfma_f32_16x16x32_bf16 v[180:183], a[180:183], v[136:139], v[180:183]\n"
      "s_add_u32 s38, s38, 24576\n"
      "v_mul_f32 v204, v27, v204\n"
      "v_add_f32_dpp v205, v205, v205 quad_perm:[2,3,0,1] row_mask:0xf bank_mask:0xf\n"
      "v_mfma_f32_16x16x32_bf16 v[180:183], a[184:187], v[140:143], v[180:183]\n"
      "ds_read_b32 v22, v24\n"
      "v_add_f32 v218, v218, v176\n"
      "v_mfma_f32_16x16x32_bf16 v[180:183], a[188:191], v[144:147], v[180:183]\n"
      "v_add_f32_dpp v205, v205, v205 row_half_mirror row_mask:0xf bank_mask:0xf\n"
      "v_fmaak_f32 v205, v27, v205, 0x36a7c5ac\n"
      "v_mfma_f32_16x16x32_bf16 v[180:183], a[192:195], v[148:151], v[180:183]\n"
      "buffer_store_dwordx2 v[204:205], v20, s[8:11], s49 offen sc1\n"
      "v_mfma_f32_16x16x32_bf16 v[180:183], a[196:199], v[152:155], v[180:183]\n"
      "s_add_u32 s50, s50, 24576\n"
      "v_add_f32 v218, v218, v177\n"
      "v_mfma_f32_16x16x32_bf16 v[180:183], a[200:203], v[156:159], v[180:183]\n"
      "v_add_f32 v218, v218, v178\n"
      "v_mfma_f32_16x16x32_bf16 v[180:183], a[204:207], v[160:163], v[180:183]\n"
      "s_waitcnt lgkmcnt(0)\n"
      "v_add_f32 v218, v218, v179\n"
      "v_cmp_gt_i32 vcc, v21, v22\n"
      "v_mfma_f32_16x16x32_bf16 v[180:183], a[208:211], v[164:167], v[180:183]\n"
      "s_waitcnt vmcnt(10)\n"
      "buffer_store_dword v25, v12, s[8:11], s50 offen sc1\n"
      "v_mfma_f32_16x16x32_bf16 v[180:183], a[212:215], v[168:171], v[180:183]\n"
      "v_fmac_f32 v219, v179, v179\n"
      "v_mfma_f32_16x16x32_bf16 v[184:187], a[216:219], v[100:103], v[184:187]\n"
      "s_cbranch_vccnz .Lsp_vy0_30\n"
      ".Lbk_vy0_30:\n"
      "s_sub_u32 s47, s46, s45\n"
      "v_mfma_f32_16x16x32_bf16 v[184:187], a[220:223], v[104:107], v[184:187]\n"
      "s_waitcnt vmcnt(10)\n"
      "v_lshlrev_b32 v174, 16, v193\n"
      "v_and_b32 v179, 0xffff0000, v195\n"
      "v_mfma_f32_16x16x32_bf16 v[184:187], a[224:227], v[108:111], v[184:187]\n"
      "ds_read_b128 v[100:103], v0 offset:8192\n"
      "v_cvt_pk_bf16_f32 v213, v182, v183\n"
      "v_mfma_f32_16x16x32_bf16 v[184:187], a[228:231], v[112:115], v[184:187]\n"
      "ds_read_b128 v[108:111], v0 offset:10240\n"
      "v_and_b32 v173, 0xffff0000, v192\n"
      "v_mfma_f32_16x16x32_bf16 v[184:187], a[232:235], v[116:119], v[184:187]\n"
      "v_fmac_f32 v219, v180, v180\n"
      "v_mfma_f32_16x16x32_bf16 v[184:187], a[236:239], v[120:123], v[184:187]\n"
      "ds_read_b128 v[120:123], v0 offset:13312\n"
      "v_add_f32 v218, v218, v180\n"
      "v_mfma_f32_16x16x32_bf16 v[184:187], a[240:243], v[124:127], v[184:187]\n"
      "v_add_f32 v218, v218, v181\n"
      "v_add_f32 v218, v218, v182\n"
      "v_mfma_f32_16x16x32_bf16 v[184:187], a[244:247], v[128:131], v[184:187]\n"
      "ds_read_b128 v[128:131], v0 offset:15360\n"
      "v_lshlrev_b32 v178, 16, v195\n"
      "v_mfma_f32_16x16x32_bf16 v[184:187], a[248:251], v[132:135], v[184:187]\n"
      "v_mfma_f32_16x16x32_bf16 v[184:187], a[252:255], v[136:139], v[184:187]\n"
      "ds_read_b128 v[104:107], v0 offset:9216\n"
      "v_lshlrev_b32 v172, 16, v192\n"
      "v_mfma_f32_16x16x32_bf16 v[184:187], v[32:35], v[140:143], v[184:187]\n"
      "buffer_store_dwordx4 v[208:211], v15, s[8:11], s38 offen sc1\n"
      "v_mfma_f32_16x16x32_bf16 v[184:187], v[36:39], v[144:147], v[184:187]\n"
      "s_mov_b32 s46, s36\n"
      "ds_read_b128 v[112:115], v0 offset:11264\n"
      "v_fmac_f32 v219, v181, v181\n"
      "v_mfma_f32_16x16x32_bf16 v[184:187], v[40:43], v[148:151], v[184:187]\n"
      "ds_read2_b32 v[134:135], v2 offset0:64 offset1:96\n"
      "v_cvt_pk_bf16_f32 v212, v180, v181\n"
      "v_mfma_f32_16x16x32_bf16 v[184:187], v[44:47], v[152:155], v[184:187]\n"
      "ds_read2_b32 v[132:133], v2 offset0:0 offset1:32\n"
      "s_add_u32 s36, s36, 24576\n"
      "v_mfma_f32_16x16x32_bf16 v[184:187], v[48:51], v[156:159], v[184:187]\n"
      "ds_read_b128 v[116:119], v0 offset:12288\n"
      "v_fmac_f32 v219, v182, v182\n"
      "v_mfma_f32_16x16x32_bf16 v[184:187], v[52:55], v[160:163], v[184:187]\n"
      "ds_read_b128 v[124:127], v0 offset:14336\n"
      "v_add_f32 v218, v218, v183\n"
      "v_mfma_f32_16x16x32_bf16 v[184:187], v[56:59], v[164:167], v[184:187]\n"
      "s_waitcnt lgkmcnt(5)\n"
      "ds_read_b128 v[160:163], v0 offset:6144\n"
      "v_and_b32 v177, 0xffff0000, v194\n"
      "v_mfma_f32_16x16x32_bf16 v[184:187], v[60:63], v[168:171], v[184:187]\n"
      "s_sub_u32 s20, s20, 1\n"
      "s_cbranch_scc0 .Lv_loop\n"
      ".Lv_tail:\n"
      "s_nop 8\n"
      "v_add_f32 v218, v218, v184\n"
      "v_add_f32 v218, v218, v185\n"
      "v_fmac_f32 v219, v183, v183\n"
      "v_fmac_f32 v219, v184, v184\n"
      "v_cvt_pk_bf16_f32 v215, v186, v187\n"
      "v_add_f32 v218, v218, v186\n"
      "v_add_f32 v218, v218, v187\n"
      "v_fmac_f32 v219, v185, v185\n"
      "v_cvt_pk_bf16_f32 v214, v184, v185\n"
      "ds_write_b32 v18, v218\n"
      "v_fmac_f32 v219, v186, v186\n"
      "buffer_store_dwordx4 v[212:215], v15, s[8:11], s38 offen offset:1024 sc1\n"
      "v_fmac_f32 v219, v187, v187\n"
      "ds_write_b32 v18, v219 offset:4\n"
      "s_waitcnt lgkmcnt(0)\n"
      "s_barrier\n"
      "s_add_u32 s38, s38, 24576\n"
      "ds_read_b128 v[204:207], v19\n"
      "s_add_u32 s49, s49, 24576\n"
      "s_waitcnt lgkmcnt(0)\n"
      "v_add_f32 v204, v204, v206\n"
      "v_add_f32 v205, v205, v207\n"
      "s_nop 0\n"
      "v_add_f32_dpp v204, v204, v204 quad_perm:[1,0,3,2] row_mask:0xf bank_mask:0xf\n"
      "s_nop 1\n"
      "v_add_f32_dpp v204, v204, v204 quad_perm:[2,3,0,1] row_mask:0xf bank_mask:0xf\n"
      "v_add_f32_dpp v205, v205, v205 quad_perm:[1,0,3,2] row_mask:0xf bank_mask:0xf\n"
      "s_add_u32 s50, s50, 24576\n"
      "s_nop 0\n"
      "v_add_f32_dpp v205, v205, v205 quad_perm:[2,3,0,1] row_mask:0xf bank_mask:0xf\n"
      "s_nop 1\n"
      "v_add_f32_dpp v205, v205, v205 row_half_mirror row_mask:0xf bank_mask:0xf\n"
      "v_add_f32_dpp v204, v204, v204 row_half_mirror row_mask:0xf bank_mask:0xf\n"
      "v_mul_f32 v204, v27, v204\n"
      "v_fmaak_f32 v205, v27, v205, 0x36a7c5ac\n"
      "s_waitcnt vmcnt(3)\n"
      "buffer_store_dword v25, v12, s[8:11], s50 offen sc1\n"
      "buffer_store_dwordx2 v[204:205], v20, s[8:11], s49 offen sc1\n"
      "s_add_u32 s49, s49, 24576\n"
      "s_add_u32 s50, s50, 24576\n"
      "s_waitcnt vmcnt(0)\n"
      "buffer_store_dword v25, v12, s[8:11], s50 offen sc1\n"
      "s_add_u32 s50, s50, 24576\n"
      "s_endpgm\n"
      ".Lsp_w0_1:\n"
      "s_mov_b32 s22, s23\n"
      ".Lsl_w0_1:\n"
      "buffer_load_dword v28, v11, s[8:11], s46 offen sc1\n"
      "s_waitcnt vmcnt(0)\n"
      "v_cmp_ne_u32 vcc, s21, v28\n"
      "s_cbranch_vccz .Lbk_w0_1\n"
      "s_sleep 4\n"
      "s_sub_u32 s22, s22, 1\n"
      "s_cbranch_scc0 .Lsl_w0_1\n"
      "s_mov_b32 s23, 0\n"
      "s_branch .Lbk_w0_1\n"
      ".Lsp_w1_2:\n"
      "s_mov_b32 s22, s23\n"
      ".Lsl_w1_2:\n"
      "buffer_load_dword v28, v11, s[8:11], s46 offen sc1\n"
      "s_waitcnt vmcnt(0)\n"
      "v_cmp_ne_u32 vcc, s21, v28\n"
      "s_cbranch_vccz .Lbk_w1_2\n"
      "s_sleep 4\n"
      "s_sub_u32 s22, s22, 1\n"
      "s_cbranch_scc0 .Lsl_w1_2\n"
      "s_mov_b32 s23, 0\n"
      "s_branch .Lbk_w1_2\n"
      ".Lsp_wy0_3:\n"
      "s_lshl_b32 s22, s23, 4\n"
      ".Lsl_wy0_3:\n"
      "ds_read_b32 v22, v24\n"
      "s_waitcnt lgkmcnt(0)\n"
      "v_cmp_gt_i32 vcc, v21, v22\n"
      "s_cbranch_vccz .Lbk_wy0_3\n"
      "s_sub_u32 s22, s22, 1\n"
      "s_cbranch_scc0 .Lsl_wy0_3\n"
      "s_mov_b32 s23, 0\n"
      "s_branch .Lbk_wy0_3\n"
      ".Lsp_w0_4:\n"
      "s_mov_b32 s22, s23\n"
      "buffer_load_dword v28, v11, s[8:11], s46 offen sc1\n"
      "s_waitcnt vmcnt(0)\n"
      "v_cmp_ne_u32 vcc, s21, v28\n"
      "s_add_u32 s30, s50, 0\n"
      "buffer_store_dword v25, v12, s[8:11], s30 offen sc1\n"
      "s_add_u32 s30, s50, 24576\n"
      "buffer_store_dword v25, v12, s[8:11], s30 offen sc1\n"
      "s_cbranch_vccz .Lbk_w0_4\n"
      ".Lsl_w0_4:\n"
      "buffer_load_dword v28, v11, s[8:11], s46 offen sc1\n"
      "s_waitcnt vmcnt(0)\n"
      "v_cmp_ne_u32 vcc, s21, v28\n"
      "s_cbranch_vccz .Lbk_w0_4\n"
      "s_sleep 4\n"
      "s_sub_u32 s22, s22, 1\n"
      "s_cbranch_scc0 .Lsl_w0_4\n"
      "s_mov_b32 s23, 0\n"
      "s_branch .Lbk_w0_4\n"
      ".Lsp_wz0_5:\n"
      "s_lshl_b32 s22, s23, 4\n"
      ".Lsl_wz0_5:\n"
      "ds_read_b32 v22, v24\n"
      "s_waitcnt lgkmcnt(0)\n"
      "v_cmp_gt_i32 vcc, v21, v22\n"
      "s_cbranch_vccz .Lbk_wz0_5\n"
      "s_sub_u32 s22, s22, 1\n"
      "s_cbranch_scc0 .Lsl_wz0_5\n"
      "s_mov_b32 s23, 0\n"
      "s_branch .Lbk_wz0_5\n"
      ".Lsp_wy1_6:\n"
      "s_lshl_b32 s22, s23, 4\n"
      ".Lsl_wy1_6:\n"
      "ds_read_b32 v22, v24\n"
      "s_waitcnt lgkmcnt(0)\n"
      "v_cmp_gt_i32 vcc, v21, v22\n"
      "s_cbranch_vccz .Lbk_wy1_6\n"
      "s_sub_u32 s22, s22, 1\n"
      "s_cbranch_scc0 .Lsl_wy1_6\n"
      "s_mov_b32 s23, 0\n"
      "s_branch .Lbk_wy1_6\n"
      ".Lsp_w1_7:\n"
      "s_mov_b32 s22, s23\n"
      "buffer_load_dword v28, v11, s[8:11], s46 offen sc1\n"
      "s_waitcnt vmcnt(0)\n"
      "v_cmp_ne_u32 vcc, s21, v28\n"
      "s_add_u32 s30, s50, 24576\n"
      "buffer_store_dword v25, v12, s[8:11], s30 offen sc1\n"
      "s_cbranch_vccz .Lbk_w1_7\n"
      ".Lsl_w1_7:\n"
      "buffer_load_dword v28, v11, s[8:11], s46 offen sc1\n"
      "s_waitcnt vmcnt(0)\n"
      "v_cmp_ne_u32 vcc, s21, v28\n"
      "s_cbranch_vccz .Lbk_w1_7\n"
      "s_sleep 4\n"
      "s_sub_u32 s22, s22, 1\n"
      "s_cbranch_scc0 .Lsl_w1_7\n"
      "s_mov_b32 s23, 0\n"
      "s_branch .Lbk_w1_7\n"
      ".Lsp_wz1_8:\n"
      "s_lshl_b32 s22, s23, 4\n"
      ".Lsl_wz1_8:\n"
      "ds_read_b32 v22, v24\n"
      "s_waitcnt lgkmcnt(0)\n"
      "v_cmp_gt_i32 vcc, v21, v22\n"
      "s_cbranch_vccz .Lbk_wz1_8\n"
      "s_sub_u32 s22, s22, 1\n"
      "s_cbranch_scc0 .Lsl_wz1_8\n"
      "s_mov_b32 s23, 0\n"
      "s_branch .Lbk_wz1_8\n"
      ".Lsp_wy0_9:\n"
      "s_lshl_b32 s22, s23, 4\n"
      ".Lsl_wy0_9:\n"
      "ds_read_b32 v22, v24\n"
      "s_waitcnt lgkmcnt(0)\n"
      "v_cmp_gt_i32 vcc, v21, v22\n"
      "s_cbranch_vccz .Lbk_wy0_9\n"
      "s_sub_u32 s22, s22, 1\n"
      "s_cbranch_scc0 .Lsl_wy0_9\n"
      "s_mov_b32 s23, 0\n"
      "s_branch .Lbk_wy0_9\n"
      ".Lsp_w0_10:\n"
      "s_mov_b32 s22, s23\n"
      "buffer_load_dword v28, v11, s[8:11], s46 offen sc1\n"
      "s_waitcnt vmcnt(0)\n"
      "v_cmp_ne_u32 vcc, s21, v28\n"
      "s_add_u32 s30, s50, 0\n"
      "buffer_store_dword v25, v12, s[8:11], s30 offen sc1\n"
      "s_add_u32 s30, s50, 24576\n"
      "buffer_store_dword v25, v12, s[8:11], s30 offen sc1\n"
      "s_cbranch_vccz .Lbk_w0_10\n"
      ".Lsl_w0_10:\n"
      "buffer_load_dword v28, v11, s[8:11], s46 offen sc1\n"
      "s_waitcnt vmcnt(0)\n"
      "v_cmp_ne_u32 vcc, s21, v28\n"
      "s_cbranch_vccz .Lbk_w0_10\n"
      "s_sleep 4\n"
      "s_sub_u32 s22, s22, 1\n"
      "s_cbranch_scc0 .Lsl_w0_10\n"
      "s_mov_b32 s23, 0\n"
      "s_branch .Lbk_w0_10\n"
      ".Lsp_wz0_11:\n"
      "s_lshl_b32 s22, s23, 4\n"
      ".Lsl_wz0_11:\n"
      "ds_read_b32 v22, v24\n"
      "s_waitcnt lgkmcnt(0)\n"
      "v_cmp_gt_i32 vcc, v21, v22\n"
      "s_cbranch_vccz .Lbk_wz0_11\n"
      "s_sub_u32 s22, s22, 1\n"
      "s_cbranch_scc0 .Lsl_wz0_11\n"
      "s_mov_b32 s23, 0\n"
      "s_branch .Lbk_wz0_11\n"
      ".Lsp_wy1_12:\n"
      "s_lshl_b32 s22, s23, 4\n"
      ".Lsl_wy1_12:\n"
      "ds_read_b32 v22, v24\n"
      "s_waitcnt lgkmcnt(0)\n"
      "v_cmp_gt_i32 vcc, v21, v22\n"
      "s_cbranch_vccz .Lbk_wy1_12\n"
      "s_sub_u32 s22, s22, 1\n"
      "s_cbranch_scc0 .Lsl_wy1_12\n"
      "s_mov_b32 s23, 0\n"
      "s_branch .Lbk_wy1_12\n"
      ".Lsp_w1_13:\n"
      "s_mov_b32 s22, s23\n"
      "buffer_load_dword v28, v11, s[8:11], s46 offen sc1\n"
      "s_waitcnt vmcnt(0)\n"
      "v_cmp_ne_u32 vcc, s21, v28\n"
      "s_add_u32 s30, s50, 24576\n"
      "buffer_store_dword v25, v12, s[8:11], s30 offen sc1\n"
      "s_cbranch_vccz .Lbk_w1_13\n"
      ".Lsl_w1_13:\n"
      "buffer_load_dword v28, v11, s[8:11], s46 offen sc1\n"
      "s_waitcnt vmcnt(0)\n"
      "v_cmp_ne_u32 vcc, s21, v28\n"
      "s_cbranch_vccz .Lbk_w1_13\n"
      "s_sleep 4\n"
      "s_sub_u32 s22, s22, 1\n"
      "s_cbranch_scc0 .Lsl_w1_13\n"
      "s_mov_b32 s23, 0\n"
      "s_branch .Lbk_w1_13\n"
      ".Lsp_wz1_14:\n"
      "s_lshl_b32 s22, s23, 4\n"
      ".Lsl_wz1_14:\n"
      "ds_read_b32 v22, v24\n"
      "s_waitcnt lgkmcnt(0)\n"
      "v_cmp_gt_i32 vcc, v21, v22\n"
      "s_cbranch_vccz .Lbk_wz1_14\n"
      "s_sub_u32 s22, s22, 1\n"
      "s_cbranch_scc0 .Lsl_wz1_14\n"
      "s_mov_b32 s23, 0\n"
      "s_branch .Lbk_wz1_14\n"
      ".Lsp_wy0_15:\n"
      "s_lshl_b32 s22, s23, 4\n"
      ".Lsl_wy0_15:\n"
      "ds_read_b32 v22, v24\n"
      "s_waitcnt lgkmcnt(0)\n"
      "v_cmp_gt_i32 vcc, v21, v22\n"
      "s_cbranch_vccz .Lbk_wy0_15\n"
      "s_sub_u32 s22, s22, 1\n"
      "s_cbranch_scc0 .Lsl_wy0_15\n"
      "s_mov_b32 s23, 0\n"
      "s_branch .Lbk_wy0_15\n"
      ".Lsp_v0_16:\n"
      "s_mov_b32 s22, s23\n"
      ".Lsl_v0_16:\n"
      "buffer_load_dword v28, v11, s[8:11], s46 offen sc1\n"
      "s_waitcnt vmcnt(0)\n"
      "v_cmp_ne_u32 vcc, s21, v28\n"
      "s_cbranch_vccz .Lbk_v0_16\n"
      "s_sleep 4\n"
      "s_sub_u32 s22, s22, 1\n"
      "s_cbranch_scc0 .Lsl_v0_16\n"
      "s_mov_b32 s23, 0\n"
      "s_branch .Lbk_v0_16\n"
      ".Lsp_v1_17:\n"
      "s_mov_b32 s22, s23\n"
      ".Lsl_v1_17:\n"
      "buffer_load_dword v28, v11, s[8:11], s46 offen sc1\n"
      "s_waitcnt vmcnt(0)\n"
      "v_cmp_ne_u32 vcc, s21, v28\n"
      "s_cbranch_vccz .Lbk_v1_17\n"
      "s_sleep 4\n"
      "s_sub_u32 s22, s22, 1\n"
      "s_cbranch_scc0 .Lsl_v1_17\n"
      "s_mov_b32 s23, 0\n"
      "s_branch .Lbk_v1_17\n"
      ".Lsp_vy0_18:\n"
      "s_lshl_b32 s22, s23, 4\n"
      ".Lsl_vy0_18:\n"
      "ds_read_b32 v22, v24\n"
      "s_waitcnt lgkmcnt(0)\n"
      "v_cmp_gt_i32 vcc, v21, v22\n"
      "s_cbranch_vccz .Lbk_vy0_18\n"
      "s_sub_u32 s22, s22, 1\n"
      "s_cbranch_scc0 .Lsl_vy0_18\n"
      "s_mov_b32 s23, 0\n"
      "s_branch .Lbk_vy0_18\n"
      ".Lsp_v0_19:\n"
      "s_mov_b32 s22, s23\n"
      "buffer_load_dword v28, v11, s[8:11], s46 offen sc1\n"
      "s_waitcnt vmcnt(0)\n"
      "v_cmp_ne_u32 vcc, s21, v28\n"
      "s_add_u32 s30, s50, 24576\n"
      "buffer_store_dword v25, v12, s[8:11], s30 offen sc1\n"
      "s_cbranch_vccz .Lbk_v0_19\n"
      ".Lsl_v0_19:\n"
      "buffer_load_dword v28, v11, s[8:11], s46 offen sc1\n"
      "s_waitcnt vmcnt(0)\n"
      "v_cmp_ne_u32 vcc, s21, v28\n"
      "s_cbranch_vccz .Lbk_v0_19\n"
      "s_sleep 4\n"
      "s_sub_u32 s22, s22, 1\n"
      "s_cbranch_scc0 .Lsl_v0_19\n"
      "s_mov_b32 s23, 0\n"
      "s_branch .Lbk_v0_19\n"
      ".Lsp_vz0_20:\n"
      "s_lshl_b32 s22, s23, 4\n"
      ".Lsl_vz0_20:\n"
      "ds_read_b32 v22, v24\n"
      "s_waitcnt lgkmcnt(0)\n"
      "v_cmp_gt_i32 vcc, v21, v22\n"
      "s_cbranch_vccz .Lbk_vz0_20\n"
      "s_sub_u32 s22, s22, 1\n"
      "s_cbranch_scc0 .Lsl_vz0_20\n"
      "s_mov_b32 s23, 0\n"
      "s_branch .Lbk_vz0_20\n"
      ".Lsp_vy1_21:\n"
      "s_lshl_b32 s22, s23, 4\n"
      ".Lsl_vy1_21:\n"
      "ds_read_b32 v22, v24\n"
      "s_waitcnt lgkmcnt(0)\n"
      "v_cmp_gt_i32 vcc, v21, v22\n"
      "s_cbranch_vccz .Lbk_vy1_21\n"
      "s_sub_u32 s22, s22, 1\n"
      "s_cbranch_scc0 .Lsl_vy1_21\n"
      "s_mov_b32 s23, 0\n"
      "s_branch .Lbk_vy1_21\n"
      ".Lsp_v1_22:\n"
      "s_mov_b32 s22, s23\n"
      "buffer_load_dword v28, v11, s[8:11], s46 offen sc1\n"
      "s_waitcnt vmcnt(0)\n"
      "v_cmp_ne_u32 vcc, s21, v28\n"
      "s_add_u32 s30, s50, 24576\n"
      "buffer_store_dword v25, v12, s[8:11], s30 offen sc1\n"
      "s_cbranch_vccz .Lbk_v1_22\n"
      ".Lsl_v1_22:\n"
      "buffer_load_dword v28, v11, s[8:11], s46 offen sc1\n"
      "s_waitcnt vmcnt(0)\n"
      "v_cmp_ne_u32 vcc, s21, v28\n"
      "s_cbranch_vccz .Lbk_v1_22\n"
      "s_sleep 4\n"
      "s_sub_u32 s22, s22, 1\n"
      "s_cbranch_scc0 .Lsl_v1_22\n"
      "s_mov_b32 s23, 0\n"
      "s_branch .Lbk_v1_22\n"
      ".Lsp_vz1_23:\n"
      "s_lshl_b32 s22, s23, 4\n"
      ".Lsl_vz1_23:\n"
      "ds_read_b32 v22, v24\n"
      "s_waitcnt lgkmcnt(0)\n"
      "v_cmp_gt_i32 vcc, v21, v22\n"
      "s_cbranch_vccz .Lbk_vz1_23\n"
      "s_sub_u32 s22, s22, 1\n"
      "s_cbranch_scc0 .Lsl_vz1_23\n"
      "s_mov_b32 s23, 0\n"
      "s_branch .Lbk_vz1_23\n"
      ".Lsp_vy0_24:\n"
      "s_lshl_b32 s22, s23, 4\n"
      ".Lsl_vy0_24:\n"
      "ds_read_b32 v22, v24\n"
      "s_waitcnt lgkmcnt(0)\n"
      "v_cmp_gt_i32 vcc, v21, v22\n"
      "s_cbranch_vccz .Lbk_vy0_24\n"
      "s_sub_u32 s22, s22, 1\n"
      "s_cbranch_scc0 .Lsl_vy0_24\n"
      "s_mov_b32 s23, 0\n"
      "s_branch .Lbk_vy0_24\n"
      ".Lsp_v0_25:\n"
      "s_mov_b32 s22, s23\n"
      "buffer_load_dword v28, v11, s[8:11], s46 offen sc1\n"
      "s_waitcnt vmcnt(0)\n"
      "v_cmp_ne_u32 vcc, s21, v28\n"
      "s_add_u32 s30, s50, 24576\n"
      "buffer_store_dword v25, v12, s[8:11], s30 offen sc1\n"
      "s_cbranch_vccz .Lbk_v0_25\n"
      ".Lsl_v0_25:\n"
      "buffer_load_dword v28, v11, s[8:11], s46 offen sc1\n"
      "s_waitcnt vmcnt(0)\n"
      "v_cmp_ne_u32 vcc, s21, v28\n"
      "s_cbranch_vccz .Lbk_v0_25\n"
      "s_sleep 4\n"
      "s_sub_u32 s22, s22, 1\n"
      "s_cbranch_scc0 .Lsl_v0_25\n"
      "s_mov_b32 s23, 0\n"
      "s_branch .Lbk_v0_25\n"
      ".Lsp_vz0_26:\n"
      "s_lshl_b32 s22, s23, 4\n"
      ".Lsl_vz0_26:\n"
      "ds_read_b32 v22, v24\n"
      "s_waitcnt lgkmcnt(0)\n"
      "v_cmp_gt_i32 vcc, v21, v22\n"
      "s_cbranch_vccz .Lbk_vz0_26\n"
      "s_sub_u32 s22, s22, 1\n"
      "s_cbranch_scc0 .Lsl_vz0_26\n"
      "s_mov_b32 s23, 0\n"
      "s_branch .Lbk_vz0_26\n"
      ".Lsp_vy1_27:\n"
      "s_lshl_b32 s22, s23, 4\n"
      ".Lsl_vy1_27:\n"
      "ds_read_b32 v22, v24\n"
      "s_waitcnt lgkmcnt(0)\n"
      "v_cmp_gt_i32 vcc, v21, v22\n"
      "s_cbranch_vccz .Lbk_vy1_27\n"
      "s_sub_u32 s22, s22, 1\n"
      "s_cbranch_scc0 .Lsl_vy1_27\n"
      "s_mov_b32 s23, 0\n"
      "s_branch .Lbk_vy1_27\n"
      ".Lsp_v1_28:\n"
      "s_mov_b32 s22, s23\n"
      "buffer_load_dword v28, v11, s[8:11], s46 offen sc1\n"
      "s_waitcnt vmcnt(0)\n"
      "v_cmp_ne_u32 vcc, s21, v28\n"
      "s_add_u32 s30, s50, 24576\n"
      "buffer_store_dword v25, v12, s[8:11], s30 offen sc1\n"
      "s_cbranch_vccz .Lbk_v1_28\n"
      ".Lsl_v1_28:\n"
      "buffer_load_dword v28, v11, s[8:11], s46 offen sc1\n"
      "s_waitcnt vmcnt(0)\n"
      "v_cmp_ne_u32 vcc, s21, v28\n"
      "s_cbranch_vccz .Lbk_v1_28\n"
      "s_sleep 4\n"
      "s_sub_u32 s22, s22, 1\n"
      "s_cbranch_scc0 .Lsl_v1_28\n"
      "s_mov_b32 s23, 0\n"
      "s_branch .Lbk_v1_28\n"
      ".Lsp_vz1_29:\n"
      "s_lshl_b32 s22, s23, 4\n"
      ".Lsl_vz1_29:\n"
      "ds_read_b32 v22, v24\n"
      "s_waitcnt lgkmcnt(0)\n"
      "v_cmp_gt_i32 vcc, v21, v22\n"
      "s_cbranch_vccz .Lbk_vz1_29\n"
      "s_sub_u32 s22, s22, 1\n"
      "s_cbranch_scc0 .Lsl_vz1_29\n"
      "s_mov_b32 s23, 0\n"
      "s_branch .Lbk_vz1_29\n"
      ".Lsp_vy0_30:\n"
      "s_lshl_b32 s22, s23, 4\n"
      ".Lsl_vy0_30:\n"
      "ds_read_b32 v22, v24\n"
      "s_waitcnt lgkmcnt(0)\n"
      "v_cmp_gt_i32 vcc, v21, v22\n"
      "s_cbranch_vccz .Lbk_vy0_30\n"
      "s_sub_u32 s22, s22, 1\n"
      "s_cbranch_scc0 .Lsl_vy0_30\n"
      "s_mov_b32 s23, 0\n"
      "s_branch .Lbk_vy0_30\n"
      ".Lw_last:\n"
      :
      : "{s[8:11]}"(rsWS), "{s[12:15]}"(rsA), "{v0}"(v_l16), "{v1}"(v_l5), "{v2}"(v_ex[0]), "{v3}"(v_ex[1]), "{v4}"(v_ex[2]), "{v5}"(v_ex[3]), "{v6}"(v_ex[4]), "{v7}"(v_ex[5]), "{v8}"(v_ex[6]), "{v9}"(v_ex[7]), "{v10}"(v_l2), "{v11}"(v_fa), "{v12}"(v_ra), "{v13}"(v_rp), "{v14}"(v_rs), "{v15}"(v_xp), "{v16}"(v_xs), "{v17}"(v_ew), "{v18}"(v_sw), "{v19}"(v_sr), "{v20}"(v_ss), "{v22}"(v_al), "{v23}"(v_yw), "{v24}"(v_yr), "{v26}"(v_hi), "{v100}"(v_vo[0]), "{v101}"(v_vo[1]), "{v102}"(v_vo[2]), "{v103}"(v_vo[3]), "{v104}"(v_vo[4]), "{v105}"(v_vo[5]), "{v106}"(v_vo[6]), "{v107}"(v_vo[7]), "{v108}"(v_vo[8]), "{v109}"(v_vp[0]), "{v110}"(v_vp[1]), "{v111}"(v_vp[2]), "{v112}"(v_vp[3]), "{v113}"(v_vp[4]), "{v114}"(v_vp[5]), "{v115}"(v_vp[6]), "{v116}"(v_vp[7]), "{v117}"(v_vp[8]), "{v118}"(v_t0), "{v119}"(v_t1), "{v120}"(v_ts), "{v121}"(v_pw), "{s36}"(s_sx), "{s37}"(s_ev), "{s38}"(s_sy), "{s39}"(s_m4), "{s40}"(s_m5), "{s41}"(s_m9), "{s42}"(s_so), "{s43}"(s_s8), "{s44}"(s_wg), "{s45}"(s_rb)
      : "v255", "a255", "s99", "vcc", "scc", "memory");

  } else if (role == 2) {
    const unsigned ks = part / 3, n3 = part % 3;
    const u32x4 rsA = srd(Wd, 4608 * 576 * 2), rsB = srd(out, 2048 * 576 * 2), rsC = srd(X, 2048 * 576 * 2);
    const unsigned v_l16b = v_l16 + 4096, v_l8 = 1024 + lane * 8, f0 = 24 * ks + 6 * w;
    const unsigned v_fa = 147456 - f0 * 1024 + (f0 + (l8 < 5 ? l8 : 5)) * 128 + l7 / 4, v_ga = (l8 < 4 ? l8 : 4) * 4 * PW + 1536 + l7 / 4;
    const unsigned v_ra = up * 6 * PREC + l5 + 1536, v_rf = up * 33 * REC + l5 + 20480 + (4 * n3 + w) * 128;
    const unsigned v_pw = l8 < 5 ? (l8 + 7) * PREC + l8 * 4 * PW + 1536 + l7 : OOB, v_pu = ks && l8 + ks < 5 ? OOB : v_pw - 6 * PREC;
    const unsigned pf = 3 * (w >> 1) + 2 * (w & 1), sf = 3 * (w >> 1) + 1;
    const unsigned v_at = (8 * g + q) * 384 + 64 * pf + 16 * p, v_as = (8 * g + q) * 384 + 64 * sf + 32 * (w & 1) + 8 * p;
    auto vo = [&](unsigned i) { const unsigned c = 64 * i + lane; return (8 * w + c / 24) * 1152 + 384 * n3 + c % 24 * 16 - 1024 * i; };
    const unsigned v_vo0 = vo(0), v_vo1 = vo(1), v_vo2 = vo(2);
    const unsigned v_xp = (6 * n3 + pf) * 1024 + v_l16, v_xs = (6 * n3 + sf) * 1024 + (w & 1) * 512 + half;
    const unsigned v_op = i16 * 1152 + 384 * n3 + 64 * pf + 16 * g, v_os = i16 * 1152 + 384 * n3 + 64 * sf + 32 * (w & 1) + 8 * g;
    const unsigned v_sw = 147456 + i16 * 136 + (4 * w + g) * 8, v_sr = 147456 + (4 * w + g) * 136 + i16 * 8;
    const unsigned v_ss = 18432 + n3 * 512 + w * 128 + g * 32 + p * 8;
    const unsigned s_ph = ks ? 6 - ks : 0, s_p0 = 0u - s_ph, r0 = gr * NREC + s_p0;
    const unsigned s_sh = WS_H + r0 * HREC + f0 * 1024, s_ps = WS_PS + r0 * PREC + (n3 * 20 + w) * PW;
    const unsigned s_x1 = WS_X1 + r0 * REC, s_xr = WS_XIN + (r0 + 33) * REC, s_or = (32 * gr + 63 - s_p0) * 18432;
    const unsigned s_k0 = ROT * 6 * gr;
    const unsigned s_m3 = w * 3072, s_m6 = w * 6144, s_so = ks * 768 * 1152;
    const unsigned s_i0 = (32 * gr + 31 - ks) * 18432, s_i5 = ks < 2 ? s_i0 - 30 * 18432 : OOB;
    const unsigned s_j0 = WS_XIN + (gr * NREC + ks) * REC, s_j5 = ks < 2 ? s_j0 + 30 * REC : OOB, s_he = s_sh + (63 + s_ph) * HREC;
    asm volatile(
      "s_buffer_load_dword s21, s[8:11], 0\n"
      "s_mov_b32 s23, 65536\n"
      "s_mov_b32 s31, 0x40000000\n"
      "buffer_load_dwordx4 v[56:59], v8, s[92:95], s46 offen\n"
      "buffer_load_dwordx2 v[60:61], v9, s[92:95], s46 offen\n"
      "s_waitcnt lgkmcnt(0)\n"
      "s_mul_i32 s21, s21, 1664525\n"
      "s_add_u32 s21, s21, 1013904223\n"
      "v_mov_b32 v18, s21\n"
      "v_mov_b32 v21, v18\n"
      "v_not_b32 v224, v18\n"
      "v_not_b32 v225, v18\n"
      "v_not_b32 v226, v18\n"
      "v_not_b32 v227, v18\n"
      "v_xor_b32 v18, v18, v19\n"
      "s_waitcnt vmcnt(0)\n"
      "buffer_store_dwordx4 v[56:59], v6, s[8:11], s48 offen sc1\n"
      "buffer_store_dwordx2 v[60:61], v7, s[8:11], s48 offen sc1\n"
      "v_lshlrev_b32 v164, 16, v56\n"
      "v_and_b32 v165, 0xffff0000, v56\n"
      "v_lshlrev_b32 v166, 16, v57\n"
      "v_and_b32 v167, 0xffff0000, v57\n"
      "v_lshlrev_b32 v168, 16, v58\n"
      "v_and_b32 v169, 0xffff0000, v58\n"
      "v_lshlrev_b32 v170, 16, v59\n"
      "v_and_b32 v171, 0xffff0000, v59\n"
      "v_lshlrev_b32 v172, 16, v60\n"
      "v_and_b32 v173, 0xffff0000, v60\n"
      "v_lshlrev_b32 v174, 16, v61\n"
      "v_and_b32 v175, 0xffff0000, v61\n"
      "v_add_f32 v62, v164, v165\n"
      "v_mul_f32 v63, v164, v164\n"
      "v_fmac_f32 v63, v165, v165\n"
      "v_add_f32 v62, v62, v166\n"
      "v_fmac_f32 v63, v166, v166\n"
      "v_add_f32 v62, v62, v167\n"
      "v_fmac_f32 v63, v167, v167\n"
      "v_add_f32 v62, v62, v168\n"
      "v_fmac_f32 v63, v168, v168\n"
      "v_add_f32 v62, v62, v169\n"
      "v_fmac_f32 v63, v169, v169\n"
      "v_add_f32 v62, v62, v170\n"
      "v_fmac_f32 v63, v170, v170\n"
      "v_add_f32 v62, v62, v171\n"
      "v_fmac_f32 v63, v171, v171\n"
      "v_add_f32 v62, v62, v172\n"
      "v_fmac_f32 v63, v172, v172\n"
      "v_add_f32 v62, v62, v173\n"
      "v_fmac_f32 v63, v173, v173\n"
      "v_add_f32 v62, v62, v174\n"
      "v_fmac_f32 v63, v174, v174\n"
      "v_add_f32 v62, v62, v175\n"
      "v_fmac_f32 v63, v175, v175\n"
      "ds_write_b64 v10, v[62:63] offset:0\n"
      "s_waitcnt lgkmcnt(0)\n"
      "s_barrier\n"
      "ds_read_b64 v[152:153], v11 offset:0\n"
      "s_waitcnt lgkmcnt(0)\n"
      "v_add_f32_dpp v152, v152, v152 row_ror:8 row_mask:0xf bank_mask:0xf\n"
      "v_add_f32_dpp v153, v153, v153 row_ror:8 row_mask:0xf bank_mask:0xf\n"
      "s_nop 0\n"
      "v_add_f32_dpp v152, v152, v152 row_ror:4 row_mask:0xf bank_mask:0xf\n"
      "v_add_f32_dpp v153, v153, v153 row_ror:4 row_mask:0xf bank_mask:0xf\n"
      "s_nop 0\n"
      "v_add_f32_dpp v152, v152, v152 row_ror:2 row_mask:0xf bank_mask:0xf\n"
      "v_add_f32_dpp v153, v153, v153 row_ror:2 row_mask:0xf bank_mask:0xf\n"
      "s_nop 0\n"
      "v_add_f32_dpp v152, v152, v152 row_ror:1 row_mask:0xf bank_mask:0xf\n"
      "v_add_f32_dpp v153, v153, v153 row_ror:1 row_mask:0xf bank_mask:0xf\n"
      "v_mul_f32 v152, 0x3ae38e39, v152\n"
      "v_add_f32 v153, 0x3afba882, v153\n"
      "v_mul_f32 v153, 0x3ae38e39, v153\n"
      "buffer_store_dwordx2 v[152:153], v12, s[8:11], s48 offen sc1\n"
      "s_add_u32 s30, s46, 4294856704\n"
      "buffer_load_dwordx4 v[64:67], v8, s[92:95], s30 offen\n"
      "buffer_load_dwordx2 v[68:69], v9, s[92:95], s30 offen\n"
      "s_add_u32 s30, s46, 4294746112\n"
      "buffer_load_dwordx4 v[72:75], v8, s[92:95], s30 offen\n"
      "buffer_load_dwordx2 v[76:77], v9, s[92:95], s30 offen\n"
      "s_waitcnt vmcnt(4)\n"
      "buffer_store_dword v18, v5, s[8:11], s48 offen sc1\n"
      "s_add_u32 s30, s46, 4294635520\n"
      "buffer_load_dwordx4 v[80:83], v8, s[92:95], s30 offen\n"
      "buffer_load_dwordx2 v[84:85], v9, s[92:95], s30 offen\n"
      "s_add_u32 s30, s46, 4294524928\n"
      "buffer_load_dwordx4 v[88:91], v8, s[92:95], s30 offen\n"
      "buffer_load_dwordx2 v[92:93], v9, s[92:95], s30 offen\n"
      "buffer_load_dwordx4 v[96:99], v8, s[92:95], s47 offen\n"
      "buffer_load_dwordx2 v[100:101], v9, s[92:95], s47 offen\n"
      "buffer_store_dword v18, v15, s[8:11], 0 offen sc1\n"
      "s_cmp_eq_u32 s45, 0\n"
      "s_cselect_b32 s30, 0x40000000, s37\n"
      "buffer_store_dword v21, v3, s[8:11], s30 offen sc1\n"
      "s_waitcnt vmcnt(11)\n"
      "s_add_u32 s30, s48, 147456\n"
      "buffer_store_dwordx4 v[64:67], v6, s[8:11], s30 offen sc1\n"
      "buffer_store_dwordx2 v[68:69], v7, s[8:11], s30 offen sc1\n"
      "v_lshlrev_b32 v164, 16, v64\n"
      "v_and_b32 v165, 0xffff0000, v64\n"
      "v_lshlrev_b32 v166, 16, v65\n"
      "v_and_b32 v167, 0xffff0000, v65\n"
      "v_lshlrev_b32 v168, 16, v66\n"
      "v_and_b32 v169, 0xffff0000, v66\n"
      "v_lshlrev_b32 v170, 16, v67\n"
      "v_and_b32 v171, 0xffff0000, v67\n"
      "v_lshlrev_b32 v172, 16, v68\n"
      "v_and_b32 v173, 0xffff0000, v68\n"
      "v_lshlrev_b32 v174, 16, v69\n"
      "v_and_b32 v175, 0xffff0000, v69\n"
      "v_add_f32 v70, v164, v165\n"
      "v_mul_f32 v71, v164, v164\n"
      "v_fmac_f32 v71, v165, v165\n"
      "v_add_f32 v70, v70, v166\n"
      "v_fmac_f32 v71, v166, v166\n"
      "v_add_f32 v70, v70, v167\n"
      "v_fmac_f32 v71, v167, v167\n"
      "v_add_f32 v70, v70, v168\n"
      "v_fmac_f32 v71, v168, v168\n"
      "v_add_f32 v70, v70, v169\n"
      "v_fmac_f32 v71, v169, v169\n"
      "v_add_f32 v70, v70, v170\n"
      "v_fmac_f32 v71, v170, v170\n"
      "v_add_f32 v70, v70, v171\n"
      "v_fmac_f32 v71, v171, v171\n"
      "v_add_f32 v70, v70, v172\n"
      "v_fmac_f32 v71, v172, v172\n"
      "v_add_f32 v70, v70, v173\n"
      "v_fmac_f32 v71, v173, v173\n"
      "v_add_f32 v70, v70, v174\n"
      "v_fmac_f32 v71, v174, v174\n"
      "v_add_f32 v70, v70, v175\n"
      "v_fmac_f32 v71, v175, v175\n"
      "ds_write_b64 v10, v[70:71] offset:2176\n"
      "s_waitcnt vmcnt(11)\n"
      "s_add_u32 s30, s48, 294912\n"
      "buffer_store_dwordx4 v[72:75], v6, s[8:11], s30 offen sc1\n"
      "buffer_store_dwordx2 v[76:77], v7, s[8:11], s30 offen sc1\n"
      "v_lshlrev_b32 v164, 16, v72\n"
      "v_and_b32 v165, 0xffff0000, v72\n"
      "v_lshlrev_b32 v166, 16, v73\n"
      "v_and_b32 v167, 0xffff0000, v73\n"
      "v_lshlrev_b32 v168, 16, v74\n"
      "v_and_b32 v169, 0xffff0000, v74\n"
      "v_lshlrev_b32 v170, 16, v75\n"
      "v_and_b32 v171, 0xffff0000, v75\n"
      "v_lshlrev_b32 v172, 16, v76\n"
      "v_and_b32 v173, 0xffff0000, v76\n"
      "v_lshlrev_b32 v174, 16, v77\n"
      "v_and_b32 v175, 0xffff0000, v77\n"
      "v_add_f32 v78, v164, v165\n"
      "v_mul_f32 v79, v164, v164\n"
      "v_fmac_f32 v79, v165, v165\n"
      "v_add_f32 v78, v78, v166\n"
      "v_fmac_f32 v79, v166, v166\n"
      "v_add_f32 v78, v78, v167\n"
      "v_fmac_f32 v79, v167, v167\n"
      "v_add_f32 v78, v78, v168\n"
      "v_fmac_f32 v79, v168, v168\n"
      "v_add_f32 v78, v78, v169\n"
      "v_fmac_f32 v79, v169, v169\n"
      "v_add_f32 v78, v78, v170\n"
      "v_fmac_f32 v79, v170, v170\n"
      "v_add_f32 v78, v78, v171\n"
      "v_fmac_f32 v79, v171, v171\n"
      "v_add_f32 v78, v78, v172\n"
      "v_fmac_f32 v79, v172, v172\n"
      "v_add_f32 v78, v78, v173\n"
      "v_fmac_f32 v79, v173, v173\n"
      "v_add_f32 v78, v78, v174\n"
      "v_fmac_f32 v79, v174, v174\n"
      "v_add_f32 v78, v78, v175\n"
      "v_fmac_f32 v79, v175, v175\n"
      "ds_write_b64 v10, v[78:79] offset:4352\n"
      "s_waitcnt vmcnt(10)\n"
      "s_add_u32 s30, s48, 442368\n"
      "buffer_store_dwordx4 v[80:83], v6, s[8:11], s30 offen sc1\n"
      "buffer_store_dwordx2 v[84:85], v7, s[8:11], s30 offen sc1\n"
      "v_lshlrev_b32 v164, 16, v80\n"
      "v_and_b32 v165, 0xffff0000, v80\n"
      "v_lshlrev_b32 v166, 16, v81\n"
      "v_and_b32 v167, 0xffff0000, v81\n"
      "v_lshlrev_b32 v168, 16, v82\n"
      "v_and_b32 v169, 0xffff0000, v82\n"
      "v_lshlrev_b32 v170, 16, v83\n"
      "v_and_b32 v171, 0xffff0000, v83\n"
      "v_lshlrev_b32 v172, 16, v84\n"
      "v_and_b32 v173, 0xffff0000, v84\n"
      "v_lshlrev_b32 v174, 16, v85\n"
      "v_and_b32 v175, 0xffff0000, v85\n"
      "v_add_f32 v86, v164, v165\n"
      "v_mul_f32 v87, v164, v164\n"
      "v_fmac_f32 v87, v165, v165\n"
      "v_add_f32 v86, v86, v166\n"
      "v_fmac_f32 v87, v166, v166\n"
      "v_add_f32 v86, v86, v167\n"
      "v_fmac_f32 v87, v167, v167\n"
      "v_add_f32 v86, v86, v168\n"
      "v_fmac_f32 v87, v168, v168\n"
      "v_add_f32 v86, v86, v169\n"
      "v_fmac_f32 v87, v169, v169\n"
      "v_add_f32 v86, v86, v170\n"
      "v_fmac_f32 v87, v170, v170\n"
      "v_add_f32 v86, v86, v171\n"
      "v_fmac_f32 v87, v171, v171\n"
      "v_add_f32 v86, v86, v172\n"
      "v_fmac_f32 v87, v172, v172\n"
      "v_add_f32 v86, v86, v173\n"
      "v_fmac_f32 v87, v173, v173\n"
      "v_add_f32 v86, v86, v174\n"
      "v_fmac_f32 v87, v174, v174\n"
      "v_add_f32 v86, v86, v175\n"
      "v_fmac_f32 v87, v175, v175\n"
      "ds_write_b64 v10, v[86:87] offset:6528\n"
      "s_waitcnt vmcnt(10)\n"
      "s_add_u32 s30, s48, 589824\n"
      "buffer_store_dwordx4 v[88:91], v6, s[8:11], s30 offen sc1\n"
      "buffer_store_dwordx2 v[92:93], v7, s[8:11], s30 offen sc1\n"
      "v_lshlrev_b32 v164, 16, v88\n"
      "v_and_b32 v165, 0xffff0000, v88\n"
      "v_lshlrev_b32 v166, 16, v89\n"
      "v_and_b32 v167, 0xffff0000, v89\n"
      "v_lshlrev_b32 v168, 16, v90\n"
      "v_and_b32 v169, 0xffff0000, v90\n"
      "v_lshlrev_b32 v170, 16, v91\n"
      "v_and_b32 v171, 0xffff0000, v91\n"
      "v_lshlrev_b32 v172, 16, v92\n"
      "v_and_b32 v173, 0xffff0000, v92\n"
      "v_lshlrev_b32 v174, 16, v93\n"
      "v_and_b32 v175, 0xffff0000, v93\n"
      "v_add_f32 v94, v164, v165\n"
      "v_mul_f32 v95, v164, v164\n"
      "v_fmac_f32 v95, v165, v165\n"
      "v_add_f32 v94, v94, v166\n"
      "v_fmac_f32 v95, v166, v166\n"
      "v_add_f32 v94, v94, v167\n"
      "v_fmac_f32 v95, v167, v167\n"
      "v_add_f32 v94, v94, v168\n"
      "v_fmac_f32 v95, v168, v168\n"
      "v_add_f32 v94, v94, v169\n"
      "v_fmac_f32 v95, v169, v169\n"
      "v_add_f32 v94, v94, v170\n"
      "v_fmac_f32 v95, v170, v170\n"
      "v_add_f32 v94, v94, v171\n"
      "v_fmac_f32 v95, v171, v171\n"
      "v_add_f32 v94, v94, v172\n"
      "v_fmac_f32 v95, v172, v172\n"
      "v_add_f32 v94, v94, v173\n"
      "v_fmac_f32 v95, v173, v173\n"
      "v_add_f32 v94, v94, v174\n"
      "v_fmac_f32 v95, v174, v174\n"
      "v_add_f32 v94, v94, v175\n"
      "v_fmac_f32 v95, v175, v175\n"
      "ds_write_b64 v10, v[94:95] offset:8704\n"
      "s_waitcnt vmcnt(10)\n"
      "buffer_store_dwordx4 v[96:99], v6, s[8:11], s49 offen sc1\n"
      "buffer_store_dwordx2 v[100:101], v7, s[8:11], s49 offen sc1\n"
      "v_lshlrev_b32 v164, 16, v96\n"
      "v_and_b32 v165, 0xffff0000, v96\n"
      "v_lshlrev_b32 v166, 16, v97\n"
      "v_and_b32 v167, 0xffff0000, v97\n"
      "v_lshlrev_b32 v168, 16, v98\n"
      "v_and_b32 v169, 0xffff0000, v98\n"
      "v_lshlrev_b32 v170, 16, v99\n"
      "v_and_b32 v171, 0xffff0000, v99\n"
      "v_lshlrev_b32 v172, 16, v100\n"
      "v_and_b32 v173, 0xffff0000, v100\n"
      "v_lshlrev_b32 v174, 16, v101\n"
      "v_and_b32 v175, 0xffff0000, v101\n"
      "v_add_f32 v102, v164, v165\n"
      "v_mul_f32 v103, v164, v164\n"
      "v_fmac_f32 v103, v165, v165\n"
      "v_add_f32 v102, v102, v166\n"
      "v_fmac_f32 v103, v166, v166\n"
      "v_add_f32 v102, v102, v167\n"
      "v_fmac_f32 v103, v167, v167\n"
      "v_add_f32 v102, v102, v168\n"
      "v_fmac_f32 v103, v168, v168\n"
      "v_add_f32 v102, v102, v169\n"
      "v_fmac_f32 v103, v169, v169\n"
      "v_add_f32 v102, v102, v170\n"
      "v_fmac_f32 v103, v170, v170\n"
      "v_add_f32 v102, v102, v171\n"
      "v_fmac_f32 v103, v171, v171\n"
      "v_add_f32 v102, v102, v172\n"
      "v_fmac_f32 v103, v172, v172\n"
      "v_add_f32 v102, v102, v173\n"
      "v_fmac_f32 v103, v173, v173\n"
      "v_add_f32 v102, v102, v174\n"
      "v_fmac_f32 v103, v174, v174\n"
      "v_add_f32 v102, v102, v175\n"
      "v_fmac_f32 v103, v175, v175\n"
      "ds_write_b64 v10, v[102:103] offset:10880\n"
      "s_waitcnt lgkmcnt(0)\n"
      "s_barrier\n"
      "ds_read_b64 v[154:155], v11 offset:2176\n"
      "ds_read_b64 v[156:157], v11 offset:4352\n"
      "ds_read_b64 v[158:159], v11 offset:6528\n"
      "ds_read_b64 v[160:161], v11 offset:8704\n"
      "ds_read_b64 v[162:163], v11 offset:10880\n"
      "s_waitcnt lgkmcnt(0)\n"
      "v_add_f32_dpp v154, v154, v154 row_ror:8 row_mask:0xf bank_mask:0xf\n"
      "v_add_f32_dpp v155, v155, v155 row_ror:8 row_mask:0xf bank_mask:0xf\n"
      "v_add_f32_dpp v156, v156, v156 row_ror:8 row_mask:0xf bank_mask:0xf\n"
      "v_add_f32_dpp v157, v157, v157 row_ror:8 row_mask:0xf bank_mask:0xf\n"
      "v_add_f32_dpp v158, v158, v158 row_ror:8 row_mask:0xf bank_mask:0xf\n"
      "v_add_f32_dpp v159, v159, v159 row_ror:8 row_mask:0xf bank_mask:0xf\n"
      "v_add_f32_dpp v160, v160, v160 row_ror:8 row_mask:0xf bank_mask:0xf\n"
      "v_add_f32_dpp v161, v161, v161 row_ror:8 row_mask:0xf bank_mask:0xf\n"
      "v_add_f32_dpp v162, v162, v162 row_ror:8 row_mask:0xf bank_mask:0xf\n"
      "v_add_f32_dpp v163, v163, v163 row_ror:8 row_mask:0xf bank_mask:0xf\n"
      "v_add_f32_dpp v154, v154, v154 row_ror:4 row_mask:0xf bank_mask:0xf\n"
      "v_add_f32_dpp v155, v155, v155 row_ror:4 row_mask:0xf bank_mask:0xf\n"
      "v_add_f32_dpp v156, v156, v156 row_ror:4 row_mask:0xf bank_mask:0xf\n"
      "v_add_f32_dpp v157, v157, v157 row_ror:4 row_mask:0xf bank_mask:0xf\n"
      "v_add_f32_dpp v158, v158, v158 row_ror:4 row_mask:0xf bank_mask:0xf\n"
      "v_add_f32_dpp v159, v159, v159 row_ror:4 row_mask:0xf bank_mask:0xf\n"
      "v_add_f32_dpp v160, v160, v160 row_ror:4 row_mask:0xf bank_mask:0xf\n"
      "v_add_f32_dpp v161, v161, v161 row_ror:4 row_mask:0xf bank_mask:0xf\n"
      "v_add_f32_dpp v162, v162, v162 row_ror:4 row_mask:0xf bank_mask:0xf\n"
      "v_add_f32_dpp v163, v163, v163 row_ror:4 row_mask:0xf bank_mask:0xf\n"
      "v_add_f32_dpp v154, v154, v154 row_ror:2 row_mask:0xf bank_mask:0xf\n"
      "v_add_f32_dpp v155, v155, v155 row_ror:2 row_mask:0xf bank_mask:0xf\n"
      "v_add_f32_dpp v156, v156, v156 row_ror:2 row_mask:0xf bank_mask:0xf\n"
      "v_add_f32_dpp v157, v157, v157 row_ror:2 row_mask:0xf bank_mask:0xf\n"
      "v_add_f32_dpp v158, v158, v158 row_ror:2 row_mask:0xf bank_mask:0xf\n"
      "v_add_f32_dpp v159, v159, v159 row_ror:2 row_mask:0xf bank_mask:0xf\n"
      "v_add_f32_dpp v160, v160, v160 row_ror:2 row_mask:0xf bank_mask:0xf\n"
      "v_add_f32_dpp v161, v161, v161 row_ror:2 row_mask:0xf bank_mask:0xf\n"
      "v_add_f32_dpp v162, v162, v162 row_ror:2 row_mask:0xf bank_mask:0xf\n"
      "v_add_f32_dpp v163, v163, v163 row_ror:2 row_mask:0xf bank_mask:0xf\n"
      "v_add_f32_dpp v154, v154, v154 row_ror:1 row_mask:0xf bank_mask:0xf\n"
      "v_add_f32_dpp v155, v155, v155 row_ror:1 row_mask:0xf bank_mask:0xf\n"
      "v_add_f32_dpp v156, v156, v156 row_ror:1 row_mask:0xf bank_mask:0xf\n"
      "v_add_f32_dpp v157, v157, v157 row_ror:1 row_mask:0xf bank_mask:0xf\n"
      "v_add_f32_dpp v158, v158, v158 row_ror:1 row_mask:0xf bank_mask:0xf\n"
      "v_add_f32_dpp v159, v159, v159 row_ror:1 row_mask:0xf bank_mask:0xf\n"
      "v_add_f32_dpp v160, v160, v160 row_ror:1 row_mask:0xf bank_mask:0xf\n"
      "v_add_f32_dpp v161, v161, v161 row_ror:1 row_mask:0xf bank_mask:0xf\n"
      "v_add_f32_dpp v162, v162, v162 row_ror:1 row_mask:0xf bank_mask:0xf\n"
      "v_add_f32_dpp v163, v163, v163 row_ror:1 row_mask:0xf bank_mask:0xf\n"
      "v_mul_f32 v154, 0x3ae38e39, v154\n"
      "v_add_f32 v155, 0x3afba882, v155\n"
      "v_mul_f32 v155, 0x3ae38e39, v155\n"
      "v_mul_f32 v156, 0x3ae38e39, v156\n"
      "v_add_f32 v157, 0x3afba882, v157\n"
      "v_mul_f32 v157, 0x3ae38e39, v157\n"
      "v_mul_f32 v158, 0x3ae38e39, v158\n"
      "v_add_f32 v159, 0x3afba882, v159\n"
      "v_mul_f32 v159, 0x3ae38e39, v159\n"
      "v_mul_f32 v160, 0x3ae38e39, v160\n"
      "v_add_f32 v161, 0x3afba882, v161\n"
      "v_mul_f32 v161, 0x3ae38e39, v161\n"
      "v_mul_f32 v162, 0x3ae38e39, v162\n"
      "v_add_f32 v163, 0x3afba882, v163\n"
      "v_mul_f32 v163, 0x3ae38e39, v163\n"
      "s_add_u32 s30, s48, 147456\n"
      "buffer_store_dwordx2 v[154:155], v12, s[8:11], s30 offen sc1\n"
      "s_add_u32 s30, s48, 294912\n"
      "buffer_store_dwordx2 v[156:157], v12, s[8:11], s30 offen sc1\n"
      "s_add_u32 s30, s48, 442368\n"
      "buffer_store_dwordx2 v[158:159], v12, s[8:11], s30 offen sc1\n"
      "s_add_u32 s30, s48, 589824\n"
      "buffer_store_dwordx2 v[160:161], v12, s[8:11], s30 offen sc1\n"
      "buffer_store_dwordx2 v[162:163], v12, s[8:11], s49 offen sc1\n"
      "s_waitcnt vmcnt(0)\n"
      "s_add_u32 s30, s48, 147456\n"
      "buffer_store_dword v18, v5, s[8:11], s30 offen sc1\n"
      "s_add_u32 s30, s48, 294912\n"
      "buffer_store_dword v18, v5, s[8:11], s30 offen sc1\n"
      "s_add_u32 s30, s48, 442368\n"
      "buffer_store_dword v18, v5, s[8:11], s30 offen sc1\n"
      "s_add_u32 s30, s48, 589824\n"
      "buffer_store_dword v18, v5, s[8:11], s30 offen sc1\n"
      "buffer_store_dword v18, v5, s[8:11], s49 offen sc1\n"
      "v_and_b32 v20, 0x40000000, v19\n"
      "v_or_b32 v5, v20, v5\n"
      "s_add_u32 s30, s37, 0\n"
      "buffer_store_dwordx4 v[224:227], v183, s[8:11], s30 offen sc1\n"
      "s_add_u32 s30, s37, 0\n"
      "buffer_store_dwordx4 v[224:227], v181, s[8:11], s30 offen sc1\n"
      "s_sleep 32\n"
      "s_waitcnt vmcnt(0) lgkmcnt(0)\n"
      "s_mov_b32 s61, s60\n"
      "s_mov_b32 s62, 6\n"
      ".Ld_r0:\n"
      "s_mul_i32 s63, s61, 36864\n"
      "s_add_u32 s63, s63, s44\n"
      "s_mul_i32 s30, s61, 8192\n"
      "s_lshr_b32 s30, s30, 16\n"
      "s_mul_i32 s30, s30, 8\n"
      "s_sub_u32 s30, s61, s30\n"
      "s_mul_i32 s30, s30, 12288\n"
      "s_add_u32 s30, s30, 0\n"
      "s_add_u32 m0, s42, s30\n"
      "s_add_u32 s61, s61, 1\n"
      "buffer_load_dwordx4 v178, s[12:15], s63 offen offset:0 lds\n"
      "buffer_load_dwordx4 v179, s[12:15], s63 offen offset:1024 lds\n"
      "buffer_load_dwordx4 v180, s[12:15], s63 offen offset:2048 lds\n"
      "s_cmp_eq_u32 s61, 24\n"
      "s_cselect_b32 s61, 0, s61\n"
      "s_sub_u32 s62, s62, 1\n"
      "s_cbranch_scc0 .Ld_r0\n"
      "v_add_u32 v20, 49152, v176\n"
      "v_add_u32 v21, 49152, v177\n"
      "v_add_u32 v22, 98304, v176\n"
      "v_add_u32 v23, 98304, v177\n"
      "s_mov_b32 s62, 23\n"
      "s_cmp_eq_u32 s60, 6\n"
      "s_cbranch_scc1 .Ld_rs6\n"
      "s_cmp_eq_u32 s60, 12\n"
      "s_cbranch_scc1 .Ld_rs12\n"
      "s_cmp_eq_u32 s60, 18\n"
      "s_cbranch_scc1 .Ld_rs18\n"
      ".Ld_rs0:\n"
      "s_add_u32 s63, s44, 258048\n"
      "s_cmp_ge_u32 s62, 7\n"
      "s_cselect_b32 s63, s63, s31\n"
      "s_add_u32 m0, s42, 86016\n"
      "s_waitcnt vmcnt(18) lgkmcnt(0)\n"
      "s_barrier\n"
      "ds_read_b64_tr_b16 a[0:1], v176 offset:0\n"
      "ds_read_b64_tr_b16 a[2:3], v176 offset:1536\n"
      "ds_read_b64_tr_b16 a[96:97], v176 offset:8\n"
      "ds_read_b64_tr_b16 a[98:99], v176 offset:1544\n"
      "ds_read_b64_tr_b16 a[192:193], v177 offset:0\n"
      "ds_read_b64_tr_b16 a[194:195], v177 offset:1536\n"
      "buffer_load_dwordx4 v178, s[12:15], s63 offen offset:0 lds\n"
      "buffer_load_dwordx4 v179, s[12:15], s63 offen offset:1024 lds\n"
      "buffer_load_dwordx4 v180, s[12:15], s63 offen offset:2048 lds\n"
      "s_sub_u32 s62, s62, 1\n"
      ".Ld_rs1:\n"
      "s_add_u32 s63, s44, 294912\n"
      "s_cmp_ge_u32 s62, 7\n"
      "s_cselect_b32 s63, s63, s31\n"
      "s_add_u32 m0, s42, 0\n"
      "s_waitcnt vmcnt(18) lgkmcnt(0)\n"
      "s_barrier\n"
      "ds_read_b64_tr_b16 a[4:5], v176 offset:12288\n"
      "ds_read_b64_tr_b16 a[6:7], v176 offset:13824\n"
      "ds_read_b64_tr_b16 a[100:101], v176 offset:12296\n"
      "ds_read_b64_tr_b16 a[102:103], v176 offset:13832\n"
      "ds_read_b64_tr_b16 a[196:197], v177 offset:12288\n"
      "ds_read_b64_tr_b16 a[198:199], v177 offset:13824\n"
      "buffer_load_dwordx4 v178, s[12:15], s63 offen offset:0 lds\n"
      "buffer_load_dwordx4 v179, s[12:15], s63 offen offset:1024 lds\n"
      "buffer_load_dwordx4 v180, s[12:15], s63 offen offset:2048 lds\n"
      "s_sub_u32 s62, s62, 1\n"
      ".Ld_rs2:\n"
      "s_add_u32 s63, s44, 331776\n"
      "s_cmp_ge_u32 s62, 7\n"
      "s_cselect_b32 s63, s63, s31\n"
      "s_add_u32 m0, s42, 12288\n"
      "s_waitcnt vmcnt(18) lgkmcnt(0)\n"
      "s_barrier\n"
      "ds_read_b64_tr_b16 a[8:9], v176 offset:24576\n"
      "ds_read_b64_tr_b16 a[10:11], v176 offset:26112\n"
      "ds_read_b64_tr_b16 a[104:105], v176 offset:24584\n"
      "ds_read_b64_tr_b16 a[106:107], v176 offset:26120\n"
      "ds_read_b64_tr_b16 a[200:201], v177 offset:24576\n"
      "ds_read_b64_tr_b16 a[202:203], v177 offset:26112\n"
      "buffer_load_dwordx4 v178, s[12:15], s63 offen offset:0 lds\n"
      "buffer_load_dwordx4 v179, s[12:15], s63 offen offset:1024 lds\n"
      "buffer_load_dwordx4 v180, s[12:15], s63 offen offset:2048 lds\n"
      "s_sub_u32 s62, s62, 1\n"
      ".Ld_rs3:\n"
      "s_add_u32 s63, s44, 368640\n"
      "s_cmp_ge_u32 s62, 7\n"
      "s_cselect_b32 s63, s63, s31\n"
      "s_add_u32 m0, s42, 24576\n"
      "s_waitcnt vmcnt(18) lgkmcnt(0)\n"
      "s_barrier\n"
      "ds_read_b64_tr_b16 a[12:13], v176 offset:36864\n"
      "ds_read_b64_tr_b16 a[14:15], v176 offset:38400\n"
      "ds_read_b64_tr_b16 a[108:109], v176 offset:36872\n"
      "ds_read_b64_tr_b16 a[110:111], v176 offset:38408\n"
      "ds_read_b64_tr_b16 a[204:205], v177 offset:36864\n"
      "ds_read_b64_tr_b16 a[206:207], v177 offset:38400\n"
      "buffer_load_dwordx4 v178, s[12:15], s63 offen offset:0 lds\n"
      "buffer_load_dwordx4 v179, s[12:15], s63 offen offset:1024 lds\n"
      "buffer_load_dwordx4 v180, s[12:15], s63 offen offset:2048 lds\n"
      "s_sub_u32 s62, s62, 1\n"
      ".Ld_rs4:\n"
      "s_add_u32 s63, s44, 405504\n"
      "s_cmp_ge_u32 s62, 7\n"
      "s_cselect_b32 s63, s63, s31\n"
      "s_add_u32 m0, s42, 36864\n"
      "s_waitcnt vmcnt(18) lgkmcnt(0)\n"
      "s_barrier\n"
      "ds_read_b64_tr_b16 a[16:17], v20 offset:0\n"
      "ds_read_b64_tr_b16 a[18:19], v20 offset:1536\n"
      "ds_read_b64_tr_b16 a[112:113], v20 offset:8\n"
      "ds_read_b64_tr_b16 a[114:115], v20 offset:1544\n"
      "ds_read_b64_tr_b16 a[208:209], v21 offset:0\n"
      "ds_read_b64_tr_b16 a[210:211], v21 offset:1536\n"
      "buffer_load_dwordx4 v178, s[12:15], s63 offen offset:0 lds\n"
      "buffer_load_dwordx4 v179, s[12:15], s63 offen offset:1024 lds\n"
      "buffer_load_dwordx4 v180, s[12:15], s63 offen offset:2048 lds\n"
      "s_sub_u32 s62, s62, 1\n"
      ".Ld_rs5:\n"
      "s_add_u32 s63, s44, 442368\n"
      "s_cmp_ge_u32 s62, 7\n"
      "s_cselect_b32 s63, s63, s31\n"
      "s_add_u32 m0, s42, 49152\n"
      "s_waitcnt vmcnt(18) lgkmcnt(0)\n"
      "s_barrier\n"
      "ds_read_b64_tr_b16 a[20:21], v20 offset:12288\n"
      "ds_read_b64_tr_b16 a[22:23], v20 offset:13824\n"
      "ds_read_b64_tr_b16 a[116:117], v20 offset:12296\n"
      "ds_read_b64_tr_b16 a[118:119], v20 offset:13832\n"
      "ds_read_b64_tr_b16 a[212:213], v21 offset:12288\n"
      "ds_read_b64_tr_b16 a[214:215], v21 offset:13824\n"
      "buffer_load_dwordx4 v178, s[12:15], s63 offen offset:0 lds\n"
      "buffer_load_dwordx4 v179, s[12:15], s63 offen offset:1024 lds\n"
      "buffer_load_dwordx4 v180, s[12:15], s63 offen offset:2048 lds\n"
      "s_sub_u32 s62, s62, 1\n"
      "s_cbranch_scc1 .Ld_re\n"
      ".Ld_rs6:\n"
      "s_add_u32 s63, s44, 479232\n"
      "s_cmp_ge_u32 s62, 7\n"
      "s_cselect_b32 s63, s63, s31\n"
      "s_add_u32 m0, s42, 61440\n"
      "s_waitcnt vmcnt(18) lgkmcnt(0)\n"
      "s_barrier\n"
      "ds_read_b64_tr_b16 a[24:25], v20 offset:24576\n"
      "ds_read_b64_tr_b16 a[26:27], v20 offset:26112\n"
      "ds_read_b64_tr_b16 a[120:121], v20 offset:24584\n"
      "ds_read_b64_tr_b16 a[122:123], v20 offset:26120\n"
      "ds_read_b64_tr_b16 a[216:217], v21 offset:24576\n"
      "ds_read_b64_tr_b16 a[218:219], v21 offset:26112\n"
      "buffer_load_dwordx4 v178, s[12:15], s63 offen offset:0 lds\n"
      "buffer_load_dwordx4 v179, s[12:15], s63 offen offset:1024 lds\n"
      "buffer_load_dwordx4 v180, s[12:15], s63 offen offset:2048 lds\n"
      "s_sub_u32 s62, s62, 1\n"
      ".Ld_rs7:\n"
      "s_add_u32 s63, s44, 516096\n"
      "s_cmp_ge_u32 s62, 7\n"
      "s_cselect_b32 s63, s63, s31\n"
      "s_add_u32 m0, s42, 73728\n"
      "s_waitcnt vmcnt(18) lgkmcnt(0)\n"
      "s_barrier\n"
      "ds_read_b64_tr_b16 a[28:29], v20 offset:36864\n"
      "ds_read_b64_tr_b16 a[30:31], v20 offset:38400\n"
      "ds_read_b64_tr_b16 a[124:125], v20 offset:36872\n"
      "ds_read_b64_tr_b16 a[126:127], v20 offset:38408\n"
      "ds_read_b64_tr_b16 a[220:221], v21 offset:36864\n"
      "ds_read_b64_tr_b16 a[222:223], v21 offset:38400\n"
      "buffer_load_dwordx4 v178, s[12:15], s63 offen offset:0 lds\n"
      "buffer_load_dwordx4 v179, s[12:15], s63 offen offset:1024 lds\n"
      "buffer_load_dwordx4 v180, s[12:15], s63 offen offset:2048 lds\n"
      "s_sub_u32 s62, s62, 1\n"
      ".Ld_rs8:\n"
      "s_add_u32 s63, s44, 552960\n"
      "s_cmp_ge_u32 s62, 7\n"
      "s_cselect_b32 s63, s63, s31\n"
      "s_add_u32 m0, s42, 86016\n"
      "s_waitcnt vmcnt(18) lgkmcnt(0)\n"
      "s_barrier\n"
      "ds_read_b64_tr_b16 a[32:33], v176 offset:0\n"
      "ds_read_b64_tr_b16 a[34:35], v176 offset:1536\n"
      "ds_read_b64_tr_b16 a[128:129], v176 offset:8\n"
      "ds_read_b64_tr_b16 a[130:131], v176 offset:1544\n"
      "ds_read_b64_tr_b16 a[224:225], v177 offset:0\n"
      "ds_read_b64_tr_b16 a[226:227], v177 offset:1536\n"
      "buffer_load_dwordx4 v178, s[12:15], s63 offen offset:0 lds\n"
      "buffer_load_dwordx4 v179, s[12:15], s63 offen offset:1024 lds\n"
      "buffer_load_dwordx4 v180, s[12:15], s63 offen offset:2048 lds\n"
      "s_sub_u32 s62, s62, 1\n"
      ".Ld_rs9:\n"
      "s_add_u32 s63, s44, 589824\n"
      "s_cmp_ge_u32 s62, 7\n"
      "s_cselect_b32 s63, s63, s31\n"
      "s_add_u32 m0, s42, 0\n"
      "s_waitcnt vmcnt(18) lgkmcnt(0)\n"
      "s_barrier\n"
      "ds_read_b64_tr_b16 a[36:37], v176 offset:12288\n"
      "ds_read_b64_tr_b16 a[38:39], v176 offset:13824\n"
      "ds_read_b64_tr_b16 a[132:133], v176 offset:12296\n"
      "ds_read_b64_tr_b16 a[134:135], v176 offset:13832\n"
      "ds_read_b64_tr_b16 a[228:229], v177 offset:12288\n"
      "ds_read_b64_tr_b16 a[230:231], v177 offset:13824\n"
      "buffer_load_dwordx4 v178, s[12:15], s63 offen offset:0 lds\n"
      "buffer_load_dwordx4 v179, s[12:15], s63 offen offset:1024 lds\n"
      "buffer_load_dwordx4 v180, s[12:15], s63 offen offset:2048 lds\n"
      "s_sub_u32 s62, s62, 1\n"
      ".Ld_rs10:\n"
      "s_add_u32 s63, s44, 626688\n"
      "s_cmp_ge_u32 s62, 7\n"
      "s_cselect_b32 s63, s63, s31\n"
      "s_add_u32 m0, s42, 12288\n"
      "s_waitcnt vmcnt(18) lgkmcnt(0)\n"
      "s_barrier\n"
      "ds_read_b64_tr_b16 a[40:41], v176 offset:24576\n"
      "ds_read_b64_tr_b16 a[42:43], v176 offset:26112\n"
      "ds_read_b64_tr_b16 a[136:137], v176 offset:24584\n"
      "ds_read_b64_tr_b16 a[138:139], v176 offset:26120\n"
      "ds_read_b64_tr_b16 a[232:233], v177 offset:24576\n"
      "ds_read_b64_tr_b16 a[234:235], v177 offset:26112\n"
      "buffer_load_dwordx4 v178, s[12:15], s63 offen offset:0 lds\n"
      "buffer_load_dwordx4 v179, s[12:15], s63 offen offset:1024 lds\n"
      "buffer_load_dwordx4 v180, s[12:15], s63 offen offset:2048 lds\n"
      "s_sub_u32 s62, s62, 1\n"
      ".Ld_rs11:\n"
      "s_add_u32 s63, s44, 663552\n"
      "s_cmp_ge_u32 s62, 7\n"
      "s_cselect_b32 s63, s63, s31\n"
      "s_add_u32 m0, s42, 24576\n"
      "s_waitcnt vmcnt(18) lgkmcnt(0)\n"
      "s_barrier\n"
      "ds_read_b64_tr_b16 a[44:45], v176 offset:36864\n"
      "ds_read_b64_tr_b16 a[46:47], v176 offset:38400\n"
      "ds_read_b64_tr_b16 a[140:141], v176 offset:36872\n"
      "ds_read_b64_tr_b16 a[142:143], v176 offset:38408\n"
      "ds_read_b64_tr_b16 a[236:237], v177 offset:36864\n"
      "ds_read_b64_tr_b16 a[238:239], v177 offset:38400\n"
      "buffer_load_dwordx4 v178, s[12:15], s63 offen offset:0 lds\n"
      "buffer_load_dwordx4 v179, s[12:15], s63 offen offset:1024 lds\n"
      "buffer_load_dwordx4 v180, s[12:15], s63 offen offset:2048 lds\n"
      "s_sub_u32 s62, s62, 1\n"
      "s_cbranch_scc1 .Ld_re\n"
      ".Ld_rs12:\n"
      "s_add_u32 s63, s44, 700416\n"
      "s_cmp_ge_u32 s62, 7\n"
      "s_cselect_b32 s63, s63, s31\n"
      "s_add_u32 m0, s42, 36864\n"
      "s_waitcnt vmcnt(18) lgkmcnt(0)\n"
      "s_barrier\n"
      "ds_read_b64_tr_b16 a[48:49], v20 offset:0\n"
      "ds_read_b64_tr_b16 a[50:51], v20 offset:1536\n"
      "ds_read_b64_tr_b16 a[144:145], v20 offset:8\n"
      "ds_read_b64_tr_b16 a[146:147], v20 offset:1544\n"
      "ds_read_b64_tr_b16 a[240:241], v21 offset:0\n"
      "ds_read_b64_tr_b16 a[242:243], v21 offset:1536\n"
      "buffer_load_dwordx4 v178, s[12:15], s63 offen offset:0 lds\n"
      "buffer_load_dwordx4 v179, s[12:15], s63 offen offset:1024 lds\n"
      "buffer_load_dwordx4 v180, s[12:15], s63 offen offset:2048 lds\n"
      "s_sub_u32 s62, s62, 1\n"
      ".Ld_rs13:\n"
      "s_add_u32 s63, s44, 737280\n"
      "s_cmp_ge_u32 s62, 7\n"
      "s_cselect_b32 s63, s63, s31\n"
      "s_add_u32 m0, s42, 49152\n"
      "s_waitcnt vmcnt(18) lgkmcnt(0)\n"
      "s_barrier\n"
      "ds_read_b64_tr_b16 a[52:53], v20 offset:12288\n"
      "ds_read_b64_tr_b16 a[54:55], v20 offset:13824\n"
      "ds_read_b64_tr_b16 a[148:149], v20 offset:12296\n"
      "ds_read_b64_tr_b16 a[150:151], v20 offset:13832\n"
      "ds_read_b64_tr_b16 a[244:245], v21 offset:12288\n"
      "ds_read_b64_tr_b16 a[246:247], v21 offset:13824\n"
      "buffer_load_dwordx4 v178, s[12:15], s63 offen offset:0 lds\n"
      "buffer_load_dwordx4 v179, s[12:15], s63 offen offset:1024 lds\n"
      "buffer_load_dwordx4 v180, s[12:15], s63 offen offset:2048 lds\n"
      "s_sub_u32 s62, s62, 1\n"
      ".Ld_rs14:\n"
      "s_add_u32 s63, s44, 774144\n"
      "s_cmp_ge_u32 s62, 7\n"
      "s_cselect_b32 s63, s63, s31\n"
      "s_add_u32 m0, s42, 61440\n"
      "s_waitcnt vmcnt(18) lgkmcnt(0)\n"
      "s_barrier\n"
      "ds_read_b64_tr_b16 a[56:57], v20 offset:24576\n"
      "ds_read_b64_tr_b16 a[58:59], v20 offset:26112\n"
      "ds_read_b64_tr_b16 a[152:153], v20 offset:24584\n"
      "ds_read_b64_tr_b16 a[154:155], v20 offset:26120\n"
      "ds_read_b64_tr_b16 a[248:249], v21 offset:24576\n"
      "ds_read_b64_tr_b16 a[250:251], v21 offset:26112\n"
      "buffer_load_dwordx4 v178, s[12:15], s63 offen offset:0 lds\n"
      "buffer_load_dwordx4 v179, s[12:15], s63 offen offset:1024 lds\n"
      "buffer_load_dwordx4 v180, s[12:15], s63 offen offset:2048 lds\n"
      "s_sub_u32 s62, s62, 1\n"
      ".Ld_rs15:\n"
      "s_add_u32 s63, s44, 811008\n"
      "s_cmp_ge_u32 s62, 7\n"
      "s_cselect_b32 s63, s63, s31\n"
      "s_add_u32 m0, s42, 73728\n"
      "s_waitcnt vmcnt(18) lgkmcnt(0)\n"
      "s_barrier\n"
      "ds_read_b64_tr_b16 a[60:61], v20 offset:36864\n"
      "ds_read_b64_tr_b16 a[62:63], v20 offset:38400\n"
      "ds_read_b64_tr_b16 a[156:157], v20 offset:36872\n"
      "ds_read_b64_tr_b16 a[158:159], v20 offset:38408\n"
      "ds_read_b64_tr_b16 a[252:253], v21 offset:36864\n"
      "ds_read_b64_tr_b16 a[254:255], v21 offset:38400\n"
      "buffer_load_dwordx4 v178, s[12:15], s63 offen offset:0 lds\n"
      "buffer_load_dwordx4 v179, s[12:15], s63 offen offset:1024 lds\n"
      "buffer_load_dwordx4 v180, s[12:15], s63 offen offset:2048 lds\n"
      "s_sub_u32 s62, s62, 1\n"
      ".Ld_rs16:\n"
      "s_add_u32 s63, s44, 847872\n"
      "s_cmp_ge_u32 s62, 7\n"
      "s_cselect_b32 s63, s63, s31\n"
      "s_add_u32 m0, s42, 86016\n"
      "s_waitcnt vmcnt(18) lgkmcnt(0)\n"
      "s_barrier\n"
      "ds_read_b64_tr_b16 a[64:65], v176 offset:0\n"
      "ds_read_b64_tr_b16 a[66:67], v176 offset:1536\n"
      "ds_read_b64_tr_b16 a[160:161], v176 offset:8\n"
      "ds_read_b64_tr_b16 a[162:163], v176 offset:1544\n"
      "ds_read_b64_tr_b16 v[24:25], v177 offset:0\n"
      "ds_read_b64_tr_b16 v[26:27], v177 offset:1536\n"
      "buffer_load_dwordx4 v178, s[12:15], s63 offen offset:0 lds\n"
      "buffer_load_dwordx4 v179, s[12:15], s63 offen offset:1024 lds\n"
      "buffer_load_dwordx4 v180, s[12:15], s63 offen offset:2048 lds\n"
      "s_sub_u32 s62, s62, 1\n"
      ".Ld_rs17:\n"
      "s_add_u32 s63, s44, 0\n"
      "s_cmp_ge_u32 s62, 7\n"
      "s_cselect_b32 s63, s63, s31\n"
      "s_add_u32 m0, s42, 0\n"
      "s_waitcnt vmcnt(18) lgkmcnt(0)\n"
      "s_barrier\n"
      "ds_read_b64_tr_b16 a[68:69], v176 offset:12288\n"
      "ds_read_b64_tr_b16 a[70:71], v176 offset:13824\n"
      "ds_read_b64_tr_b16 a[164:165], v176 offset:12296\n"
      "ds_read_b64_tr_b16 a[166:167], v176 offset:13832\n"
      "ds_read_b64_tr_b16 v[28:29], v177 offset:12288\n"
      "ds_read_b64_tr_b16 v[30:31], v177 offset:13824\n"
      "buffer_load_dwordx4 v178, s[12:15], s63 offen offset:0 lds\n"
      "buffer_load_dwordx4 v179, s[12:15], s63 offen offset:1024 lds\n"
      "buffer_load_dwordx4 v180, s[12:15], s63 offen offset:2048 lds\n"
      "s_sub_u32 s62, s62, 1\n"
      "s_cbranch_scc1 .Ld_re\n"
      ".Ld_rs18:\n"
      "s_add_u32 s63, s44, 36864\n"
      "s_cmp_ge_u32 s62, 7\n"
      "s_cselect_b32 s63, s63, s31\n"
      "s_add_u32 m0, s42, 12288\n"
      "s_waitcnt vmcnt(18) lgkmcnt(0)\n"
      "s_barrier\n"
      "ds_read_b64_tr_b16 a[72:73], v176 offset:24576\n"
      "ds_read_b64_tr_b16 a[74:75], v176 offset:26112\n"
      "ds_read_b64_tr_b16 a[168:169], v176 offset:24584\n"
      "ds_read_b64_tr_b16 a[170:171], v176 offset:26120\n"
      "ds_read_b64_tr_b16 v[32:33], v177 offset:24576\n"
      "ds_read_b64_tr_b16 v[34:35], v177 offset:26112\n"
      "buffer_load_dwordx4 v178, s[12:15], s63 offen offset:0 lds\n"
      "buffer_load_dwordx4 v179, s[12:15], s63 offen offset:1024 lds\n"
      "buffer_load_dwordx4 v180, s[12:15], s63 offen offset:2048 lds\n"
      "s_sub_u32 s62, s62, 1\n"
      ".Ld_rs19:\n"
      "s_add_u32 s63, s44, 73728\n"
      "s_cmp_ge_u32 s62, 7\n"
      "s_cselect_b32 s63, s63, s31\n"
      "s_add_u32 m0, s42, 24576\n"
      "s_waitcnt vmcnt(18) lgkmcnt(0)\n"
      "s_barrier\n"
      "ds_read_b64_tr_b16 a[76:77], v176 offset:36864\n"
      "ds_read_b64_tr_b16 a[78:79], v176 offset:38400\n"
      "ds_read_b64_tr_b16 a[172:173], v176 offset:36872\n"
      "ds_read_b64_tr_b16 a[174:175], v176 offset:38408\n"
      "ds_read_b64_tr_b16 v[36:37], v177 offset:36864\n"
      "ds_read_b64_tr_b16 v[38:39], v177 offset:38400\n"
      "buffer_load_dwordx4 v178, s[12:15], s63 offen offset:0 lds\n"
      "buffer_load_dwordx4 v179, s[12:15], s63 offen offset:1024 lds\n"
      "buffer_load_dwordx4 v180, s[12:15], s63 offen offset:2048 lds\n"
      "s_sub_u32 s62, s62, 1\n"
      ".Ld_rs20:\n"
      "s_add_u32 s63, s44, 110592\n"
      "s_cmp_ge_u32 s62, 7\n"
      "s_cselect_b32 s63, s63, s31\n"
      "s_add_u32 m0, s42, 36864\n"
      "s_waitcnt vmcnt(18) lgkmcnt(0)\n"
      "s_barrier\n"
      "ds_read_b64_tr_b16 a[80:81], v20 offset:0\n"
      "ds_read_b64_tr_b16 a[82:83], v20 offset:1536\n"
      "ds_read_b64_tr_b16 a[176:177], v20 offset:8\n"
      "ds_read_b64_tr_b16 a[178:179], v20 offset:1544\n"
      "ds_read_b64_tr_b16 v[40:41], v21 offset:0\n"
      "ds_read_b64_tr_b16 v[42:43], v21 offset:1536\n"
      "buffer_load_dwordx4 v178, s[12:15], s63 offen offset:0 lds\n"
      "buffer_load_dwordx4 v179, s[12:15], s63 offen offset:1024 lds\n"
      "buffer_load_dwordx4 v180, s[12:15], s63 offen offset:2048 lds\n"
      "s_sub_u32 s62, s62, 1\n"
      ".Ld_rs21:\n"
      "s_add_u32 s63, s44, 147456\n"
      "s_cmp_ge_u32 s62, 7\n"
      "s_cselect_b32 s63, s63, s31\n"
      "s_add_u32 m0, s42, 49152\n"
      "s_waitcnt vmcnt(18) lgkmcnt(0)\n"
      "s_barrier\n"
      "ds_read_b64_tr_b16 a[84:85], v20 offset:12288\n"
      "ds_read_b64_tr_b16 a[86:87], v20 offset:13824\n"
      "ds_read_b64_tr_b16 a[180:181], v20 offset:12296\n"
      "ds_read_b64_tr_b16 a[182:183], v20 offset:13832\n"
      "ds_read_b64_tr_b16 v[44:45], v21 offset:12288\n"
      "ds_read_b64_tr_b16 v[46:47], v21 offset:13824\n"
      "buffer_load_dwordx4 v178, s[12:15], s63 offen offset:0 lds\n"
      "buffer_load_dwordx4 v179, s[12:15], s63 offen offset:1024 lds\n"
      "buffer_load_dwordx4 v180, s[12:15], s63 offen offset:2048 lds\n"
      "s_sub_u32 s62, s62, 1\n"
      ".Ld_rs22:\n"
      "s_add_u32 s63, s44, 184320\n"
      "s_cmp_ge_u32 s62, 7\n"
      "s_cselect_b32 s63, s63, s31\n"
      "s_add_u32 m0, s42, 61440\n"
      "s_waitcnt vmcnt(18) lgkmcnt(0)\n"
      "s_barrier\n"
      "ds_read_b64_tr_b16 a[88:89], v20 offset:24576\n"
      "ds_read_b64_tr_b16 a[90:91], v20 offset:26112\n"
      "ds_read_b64_tr_b16 a[184:185], v20 offset:24584\n"
      "ds_read_b64_tr_b16 a[186:187], v20 offset:26120\n"
      "ds_read_b64_tr_b16 v[48:49], v21 offset:24576\n"
      "ds_read_b64_tr_b16 v[50:51], v21 offset:26112\n"
      "buffer_load_dwordx4 v178, s[12:15], s63 offen offset:0 lds\n"
      "buffer_load_dwordx4 v179, s[12:15], s63 offen offset:1024 lds\n"
      "buffer_load_dwordx4 v180, s[12:15], s63 offen offset:2048 lds\n"
      "s_sub_u32 s62, s62, 1\n"
      ".Ld_rs23:\n"
      "s_add_u32 s63, s44, 221184\n"
      "s_cmp_ge_u32 s62, 7\n"
      "s_cselect_b32 s63, s63, s31\n"
      "s_add_u32 m0, s42, 73728\n"
      "s_waitcnt vmcnt(18) lgkmcnt(0)\n"
      "s_barrier\n"
      "ds_read_b64_tr_b16 a[92:93], v20 offset:36864\n"
      "ds_read_b64_tr_b16 a[94:95], v20 offset:38400\n"
      "ds_read_b64_tr_b16 a[188:189], v20 offset:36872\n"
      "ds_read_b64_tr_b16 a[190:191], v20 offset:38408\n"
      "ds_read_b64_tr_b16 v[52:53], v21 offset:36864\n"
      "ds_read_b64_tr_b16 v[54:55], v21 offset:38400\n"
      "buffer_load_dwordx4 v178, s[12:15], s63 offen offset:0 lds\n"
      "buffer_load_dwordx4 v179, s[12:15], s63 offen offset:1024 lds\n"
      "buffer_load_dwordx4 v180, s[12:15], s63 offen offset:2048 lds\n"
      "s_sub_u32 s62, s62, 1\n"
      "s_cbranch_scc0 .Ld_rs0\n"
      ".Ld_re:\n"
      "s_waitcnt vmcnt(0) lgkmcnt(0)\n"
      "v_add_u32 v22, 49152, v0\n"
      "v_add_u32 v23, 98304, v0\n"
      "s_lshr_b32 s58, s42, 10\n"
      "s_mul_i32 s58, s58, 1366\n"
      "s_and_b32 s58, s58, 0x7000\n"
      "s_getpc_b64 vcc\n"
      ".Ld_ct:\n"
      "s_add_u32 vcc_lo, vcc_lo, .Ld_p0-.Ld_ct\n"
      "s_addc_u32 vcc_hi, vcc_hi, 0\n"
      "s_lshl_b32 s30, s58, 1\n"
      "v_lshl_add_u32 v56, v0, 3, s30\n"
      "v_min_u32 v56, .Ld_last-.Ld_p0-4, v56\n"
      "global_load_dword v56, v56, vcc\n"
      "s_waitcnt vmcnt(0) lgkmcnt(0)\n"
      "s_mov_b64 exec, 0\n"
      "s_lshr_b32 s22, s58, 12\n"
      ".Ld_pl:\n"
      "s_cmp_eq_u32 s22, 0\n"
      "s_cbranch_scc1 .Ld_p0\n"
      "s_cmp_eq_u32 s22, 1\n"
      "s_cbranch_scc1 .Ld_p1\n"
      "s_branch .Ld_pe\n"
      ".Ld_pr:\n"
      "s_add_u32 s22, s22, 4\n"
      "s_branch .Ld_pl\n"
      ".Ld_pe:\n"
      "s_mov_b64 exec, -1\n"
      "s_barrier\n"
      "v_mov_b32 v14, 0\n"
      "ds_write_b32 v16, v14\n"
      "s_waitcnt lgkmcnt(0)\n"
      "s_barrier\n"
      "s_mov_b32 s6, s50\n"
      "s_add_u32 s36, s36, 4294801408\n"
      "s_add_u32 s53, s37, 0\n"
      "s_add_u32 s52, s37, 4294787584\n"
      "s_add_u32 s37, s37, 4294894080\n"
      "s_mov_b32 s56, 0x40000000\n"
      "s_mov_b32 s57, 0x40000000\n"
      "s_mov_b32 s59, 63\n"
      "s_cmp_eq_u32 s45, 1\n"
      "s_cbranch_scc1 .Ld_in1\n"
      "s_cmp_eq_u32 s45, 2\n"
      "s_cbranch_scc1 .Ld_in2\n"
      "s_cmp_eq_u32 s45, 3\n"
      "s_cbranch_scc1 .Ld_in3\n"
      "s_cmp_eq_u32 s45, 4\n"
      "s_cbranch_scc1 .Ld_in4\n"
      "s_cmp_eq_u32 s45, 5\n"
      "s_cbranch_scc1 .Ld_in5\n"
      "s_add_u32 s36, s36, 165888\n"
      "s_min_u32 s36, s36, s6\n"
      "buffer_load_dword v20, v2, s[8:11], s36 offen sc1\n"
      "s_add_u32 m0, s43, 0\n"
      "s_mov_b32 s51, s36\n"
      "s_add_u32 s36, s36, 165888\n"
      "s_min_u32 s36, s36, s6\n"
      "s_waitcnt vmcnt(0)\n"
      "v_cmp_ne_u32 vcc, s21, v20\n"
      "s_cbranch_vccnz .Lsp_d0_1\n"
      ".Lbk_d0_1:\n"
      "buffer_load_dwordx4 v0, s[8:11], s51 offen offset:0 sc1 lds\n"
      "buffer_load_dwordx4 v0, s[8:11], s51 offen offset:1024 sc1 lds\n"
      "buffer_load_dword v20, v2, s[8:11], s36 offen sc1\n"
      "buffer_load_dwordx4 v0, s[8:11], s51 offen offset:2048 sc1 lds\n"
      "buffer_load_dwordx4 v0, s[8:11], s51 offen offset:3072 sc1 lds\n"
      "s_add_u32 m0, s43, 4096\n"
      "s_nop 0\n"
      "buffer_load_dwordx4 v1, s[8:11], s51 offen offset:1024 sc1 lds\n"
      "buffer_load_dwordx4 v1, s[8:11], s51 offen offset:0 sc1 lds\n"
      "s_mov_b32 s51, s36\n"
      "s_add_u32 m0, s43, 24576\n"
      "v_add_u32 v14, 1, v14\n"
      "s_add_u32 s36, s36, 165888\n"
      "buffer_load_dwordx4 v[216:219], v6, s[8:11], s38 offen sc1\n"
      "s_waitcnt vmcnt(5)\n"
      "v_cmp_ne_u32 vcc, s21, v20\n"
      "buffer_load_dwordx2 v[220:221], v7, s[8:11], s38 offen sc1\n"
      "s_cbranch_vccnz .Lsp_d1_2\n"
      ".Lbk_d1_2:\n"
      "buffer_load_dwordx4 v0, s[8:11], s51 offen offset:0 sc1 lds\n"
      "buffer_load_dwordx4 v0, s[8:11], s51 offen offset:1024 sc1 lds\n"
      "s_waitcnt vmcnt(4)\n"
      "ds_write_b32 v16, v14\n"
      "buffer_load_dwordx4 v0, s[8:11], s51 offen offset:2048 sc1 lds\n"
      "s_min_u32 s36, s36, s6\n"
      "buffer_load_dword v20, v2, s[8:11], s36 offen sc1\n"
      "buffer_load_dwordx4 v0, s[8:11], s51 offen offset:3072 sc1 lds\n"
      "ds_read_b32 v15, v17\n"
      "s_add_u32 m0, s43, 28672\n"
      "s_nop 0\n"
      "buffer_load_dwordx4 v1, s[8:11], s51 offen offset:0 sc1 lds\n"
      "s_waitcnt lgkmcnt(0)\n"
      "v_cmp_gt_i32 vcc, v14, v15\n"
      "buffer_load_dwordx4 v1, s[8:11], s51 offen offset:1024 sc1 lds\n"
      "s_cbranch_vccnz .Lsp_dy0_3\n"
      ".Lbk_dy0_3:\n"
      "ds_read_b128 v[56:59], v0 offset:0\n"
      "ds_read_b128 v[60:63], v0 offset:1024\n"
      "ds_read_b128 v[84:87], v0 offset:7168\n"
      "ds_read_b128 v[72:75], v0 offset:4096\n"
      "ds_read_b128 v[88:91], v0 offset:8192\n"
      "ds_read_b128 v[68:71], v0 offset:3072\n"
      "ds_read_b128 v[104:107], v0 offset:12288\n"
      "ds_read_b128 v[96:99], v0 offset:10240\n"
      "ds_read_b128 v[80:83], v0 offset:6144\n"
      "ds_read_b128 v[64:67], v0 offset:2048\n"
      "ds_read_b128 v[92:95], v0 offset:9216\n"
      "ds_read_b128 v[76:79], v0 offset:5120\n"
      "ds_read_b128 v[132:135], v0 offset:19456\n"
      "ds_read_b128 v[100:103], v0 offset:11264\n"
      "s_waitcnt vmcnt(0) lgkmcnt(0)\n"
      "s_nop 4\n"
      "s_branch .Ld_t0\n"
      ".p2align 12\n"
      ".Ld_p0:\n"
      "s_branch .Ld_pr\n"
      ".p2align 5\n"
      ".Ld_t0:\n"
      "v_mfma_f32_16x16x32_bf16 v[152:155], a[0:3], v[56:59], 0\n"
      "ds_read_b128 v[108:111], v0 offset:13312\n"
      "ds_read_b128 v[120:123], v0 offset:16384\n"
      "v_mfma_f32_16x16x32_bf16 v[152:155], a[4:7], v[60:63], v[152:155]\n"
      "s_waitcnt lgkmcnt(6)\n"
      "ds_read_b128 v[116:119], v0 offset:15360\n"
      "v_add_f32 v236, v236, v168\n"
      "v_mfma_f32_16x16x32_bf16 v[152:155], a[8:11], v[64:67], v[152:155]\n"
      "v_cvt_pk_bf16_f32 v228, v160, v161\n"
      "v_cvt_pk_bf16_f32 v229, v162, v163\n"
      "v_mfma_f32_16x16x32_bf16 v[152:155], a[12:15], v[68:71], v[152:155]\n"
      "buffer_store_dwordx2 v[228:229], v13, s[8:11], s37 offen sc1\n"
      "v_mfma_f32_16x16x32_bf16 v[152:155], a[16:19], v[72:75], v[152:155]\n"
      "s_waitcnt lgkmcnt(4)\n"
      "v_cvt_pk_bf16_f32 v178, v168, v169\n"
      "v_add_f32 v175, v175, v213\n"
      "v_mfma_f32_16x16x32_bf16 v[152:155], a[20:23], v[76:79], v[152:155]\n"
      "ds_read_b128 v[112:115], v0 offset:14336\n"
      "v_add_f32 v171, v171, v211\n"
      "v_mfma_f32_16x16x32_bf16 v[152:155], a[24:27], v[80:83], v[152:155]\n"
      "ds_read_b128 v[124:127], v0 offset:17408\n"
      "v_add_f32 v236, v236, v169\n"
      "v_mfma_f32_16x16x32_bf16 v[152:155], a[28:31], v[84:87], v[152:155]\n"
      "v_add_f32 v173, v173, v204\n"
      "v_cvt_pk_bf16_f32 v179, v170, v171\n"
      "v_mfma_f32_16x16x32_bf16 v[152:155], a[32:35], v[88:91], v[152:155]\n"
      "ds_read_b128 v[128:131], v0 offset:18432\n"
      "v_add_f32 v173, v173, v212\n"
      "v_mfma_f32_16x16x32_bf16 v[152:155], a[36:39], v[92:95], v[152:155]\n"
      "buffer_store_dwordx4 v[176:179], v6, s[8:11], s56 offen sc1\n"
      "v_mfma_f32_16x16x32_bf16 v[152:155], a[40:43], v[96:99], v[152:155]\n"
      "s_waitcnt lgkmcnt(3)\n"
      "ds_read_b128 v[148:151], v0 offset:23552\n"
      "v_cvt_pk_bf16_f32 v180, v172, v173\n"
      "v_mfma_f32_16x16x32_bf16 v[152:155], a[44:47], v[100:103], v[152:155]\n"
      "ds_read_b128 v[144:147], v0 offset:22528\n"
      "v_fmac_f32 v237, v168, v168\n"
      "v_mfma_f32_16x16x32_bf16 v[152:155], a[48:51], v[104:107], v[152:155]\n"
      "s_add_u32 m0, s43, 49152\n"
      "ds_read_b128 v[136:139], v0 offset:20480\n"
      "v_fmac_f32 v237, v169, v169\n"
      "v_mfma_f32_16x16x32_bf16 v[152:155], a[52:55], v[108:111], v[152:155]\n"
      "s_waitcnt lgkmcnt(4)\n"
      "s_mov_b32 s51, s36\n"
      "ds_read_b128 v[140:143], v0 offset:21504\n"
      "v_mfma_f32_16x16x32_bf16 v[152:155], a[56:59], v[112:115], v[152:155]\n"
      "v_cvt_pk_bf16_f32 v181, v174, v175\n"
      "v_fmac_f32 v237, v170, v170\n"
      "v_mfma_f32_16x16x32_bf16 v[152:155], a[60:63], v[116:119], v[152:155]\n"
      "buffer_store_dwordx2 v[180:181], v7, s[8:11], s56 offen sc1\n"
      "v_mfma_f32_16x16x32_bf16 v[152:155], a[64:67], v[120:123], v[152:155]\n"
      "v_add_u32 v14, 1, v14\n"
      "v_mfma_f32_16x16x32_bf16 v[152:155], a[68:71], v[124:127], v[152:155]\n"
      "s_waitcnt lgkmcnt(3)\n"
      "v_add_f32 v236, v236, v170\n"
      "v_fmac_f32 v237, v171, v171\n"
      "v_mfma_f32_16x16x32_bf16 v[152:155], a[72:75], v[128:131], v[152:155]\n"
      "v_fmac_f32 v237, v172, v172\n"
      "v_fmac_f32 v237, v173, v173\n"
      "v_mfma_f32_16x16x32_bf16 v[152:155], a[76:79], v[132:135], v[152:155]\n"
      "s_waitcnt lgkmcnt(1)\n"
      "buffer_store_dwordx2 v[180:181], v9, s[16:19], s57 offen\n"
      "v_mfma_f32_16x16x32_bf16 v[152:155], a[80:83], v[136:139], v[152:155]\n"
      "s_waitcnt lgkmcnt(0)\n"
      "v_add_f32 v236, v236, v171\n"
      "v_fmac_f32 v237, v174, v174\n"
      "v_mfma_f32_16x16x32_bf16 v[152:155], a[84:87], v[140:143], v[152:155]\n"
      "v_add_f32 v236, v236, v172\n"
      "v_fmac_f32 v237, v175, v175\n"
      "v_mfma_f32_16x16x32_bf16 v[152:155], a[88:91], v[144:147], v[152:155]\n"
      "v_add_f32 v236, v236, v173\n"
      "v_add_f32 v236, v236, v174\n"
      "v_mfma_f32_16x16x32_bf16 v[152:155], a[92:95], v[148:151], v[152:155]\n"
      "buffer_store_dwordx4 v[176:179], v8, s[16:19], s57 offen\n"
      "v_mfma_f32_16x16x32_bf16 v[156:159], a[96:99], v[56:59], 0\n"
      "s_waitcnt vmcnt(15)\n"
      "v_add_f32 v236, v236, v175\n"
      "v_lshlrev_b32 v166, 16, v217\n"
      "v_mfma_f32_16x16x32_bf16 v[156:159], a[100:103], v[60:63], v[156:159]\n"
      "s_waitcnt vmcnt(10)\n"
      "v_cmp_ne_u32 vcc, s21, v20\n"
      "v_mfma_f32_16x16x32_bf16 v[156:159], a[104:107], v[64:67], v[156:159]\n"
      "ds_write_b64 v10, v[236:237]\n"
      "v_mfma_f32_16x16x32_bf16 v[156:159], a[108:111], v[68:71], v[156:159]\n"
      "s_add_u32 s36, s36, 165888\n"
      "v_mfma_f32_16x16x32_bf16 v[156:159], a[112:115], v[72:75], v[156:159]\n"
      "s_min_u32 s36, s36, s6\n"
      "s_cbranch_vccnz .Lsp_d2_4\n"
      ".Lbk_d2_4:\n"
      "v_mfma_f32_16x16x32_bf16 v[156:159], a[116:119], v[76:79], v[156:159]\n"
      "s_waitcnt vmcnt(7)\n"
      "ds_write_b32 v16, v14\n"
      "v_mfma_f32_16x16x32_bf16 v[156:159], a[120:123], v[80:83], v[156:159]\n"
      "buffer_load_dword v20, v2, s[8:11], s36 offen sc1\n"
      "v_mfma_f32_16x16x32_bf16 v[156:159], a[124:127], v[84:87], v[156:159]\n"
      "v_and_b32 v167, 0xffff0000, v217\n"
      "v_lshlrev_b32 v164, 16, v216\n"
      "v_mfma_f32_16x16x32_bf16 v[156:159], a[128:131], v[88:91], v[156:159]\n"
      "v_mfma_f32_16x16x32_bf16 v[156:159], a[132:135], v[92:95], v[156:159]\n"
      "buffer_load_dwordx4 v0, s[8:11], s51 offen offset:3072 sc1 lds\n"
      "v_mfma_f32_16x16x32_bf16 v[156:159], a[136:139], v[96:99], v[156:159]\n"
      "s_add_u32 s52, s52, 106496\n"
      "v_mfma_f32_16x16x32_bf16 v[156:159], a[140:143], v[100:103], v[156:159]\n"
      "v_mfma_f32_16x16x32_bf16 v[156:159], a[144:147], v[104:107], v[156:159]\n"
      "s_add_u32 s37, s37, 173056\n"
      "v_and_b32 v165, 0xffff0000, v216\n"
      "v_mfma_f32_16x16x32_bf16 v[156:159], a[148:151], v[108:111], v[156:159]\n"
      "buffer_load_dwordx4 v0, s[8:11], s51 offen offset:1024 sc1 lds\n"
      "v_mfma_f32_16x16x32_bf16 v[156:159], a[152:155], v[112:115], v[156:159]\n"
      "ds_read_b32 v15, v17\n"
      "v_mfma_f32_16x16x32_bf16 v[156:159], a[156:159], v[116:119], v[156:159]\n"
      "v_mfma_f32_16x16x32_bf16 v[156:159], a[160:163], v[120:123], v[156:159]\n"
      "v_lshlrev_b32 v170, 16, v219\n"
      "v_mfma_f32_16x16x32_bf16 v[156:159], a[164:167], v[124:127], v[156:159]\n"
      "buffer_load_dwordx4 v0, s[8:11], s51 offen offset:2048 sc1 lds\n"
      "v_mfma_f32_16x16x32_bf16 v[156:159], a[168:171], v[128:131], v[156:159]\n"
      "v_mfma_f32_16x16x32_bf16 v[156:159], a[172:175], v[132:135], v[156:159]\n"
      "s_waitcnt lgkmcnt(0)\n"
      "v_cmp_gt_i32 vcc, v14, v15\n"
      "v_mfma_f32_16x16x32_bf16 v[156:159], a[176:179], v[136:139], v[156:159]\n"
      "v_add_f32 v165, v165, v153\n"
      "v_mfma_f32_16x16x32_bf16 v[156:159], a[180:183], v[140:143], v[156:159]\n"
      "buffer_load_dwordx4 v0, s[8:11], s51 offen offset:0 sc1 lds\n"
      "v_mfma_f32_16x16x32_bf16 v[156:159], a[184:187], v[144:147], v[156:159]\n"
      "v_add_f32 v164, v164, v152\n"
      "s_add_u32 m0, s43, 53248\n"
      "v_mfma_f32_16x16x32_bf16 v[156:159], a[188:191], v[148:151], v[156:159]\n"
      "s_cbranch_vccnz .Lsp_dy1_5\n"
      ".Lbk_dy1_5:\n"
      "ds_read_b64 v[238:239], v11\n"
      "v_mfma_f32_16x16x32_bf16 v[160:163], a[192:195], v[56:59], 0\n"
      "v_lshlrev_b32 v168, 16, v218\n"
      "v_mfma_f32_16x16x32_bf16 v[160:163], a[196:199], v[60:63], v[160:163]\n"
      "buffer_load_dwordx4 v1, s[8:11], s51 offen offset:0 sc1 lds\n"
      "v_mfma_f32_16x16x32_bf16 v[160:163], a[200:203], v[64:67], v[160:163]\n"
      "v_mfma_f32_16x16x32_bf16 v[160:163], a[204:207], v[68:71], v[160:163]\n"
      "ds_read_b128 v[56:59], v0 offset:24576\n"
      "v_mfma_f32_16x16x32_bf16 v[160:163], a[208:211], v[72:75], v[160:163]\n"
      "ds_read_b128 v[64:67], v0 offset:26624\n"
      "v_mfma_f32_16x16x32_bf16 v[160:163], a[212:215], v[76:79], v[160:163]\n"
      "buffer_load_dwordx4 v1, s[8:11], s51 offen offset:1024 sc1 lds\n"
      "v_mfma_f32_16x16x32_bf16 v[160:163], a[216:219], v[80:83], v[160:163]\n"
      "s_waitcnt lgkmcnt(2)\n"
      "ds_read_b128 v[68:71], v0 offset:27648\n"
      "v_add_f32_dpp v238, v238, v238 row_ror:8 row_mask:0xf bank_mask:0xf\n"
      "v_mfma_f32_16x16x32_bf16 v[160:163], a[220:223], v[84:87], v[160:163]\n"
      "ds_read_b128 v[80:83], v0 offset:30720\n"
      "v_add_f32_dpp v238, v238, v238 row_ror:4 row_mask:0xf bank_mask:0xf\n"
      "v_mfma_f32_16x16x32_bf16 v[160:163], a[224:227], v[88:91], v[160:163]\n"
      "ds_read_b128 v[60:63], v0 offset:25600\n"
      "v_add_f32_dpp v238, v238, v238 row_ror:2 row_mask:0xf bank_mask:0xf\n"
      "v_mfma_f32_16x16x32_bf16 v[160:163], a[228:231], v[92:95], v[160:163]\n"
      "ds_read_b128 v[92:95], v0 offset:33792\n"
      "v_add_f32_dpp v239, v239, v239 row_ror:8 row_mask:0xf bank_mask:0xf\n"
      "v_mfma_f32_16x16x32_bf16 v[160:163], a[232:235], v[96:99], v[160:163]\n"
      "ds_read_b128 v[84:87], v0 offset:31744\n"
      "v_add_f32_dpp v239, v239, v239 row_ror:4 row_mask:0xf bank_mask:0xf\n"
      "v_mfma_f32_16x16x32_bf16 v[160:163], a[236:239], v[100:103], v[160:163]\n"
      "s_waitcnt vmcnt(11) lgkmcnt(5)\n"
      "buffer_store_dword v18, v4, s[8:11], s52 offen sc1\n"
      "v_mfma_f32_16x16x32_bf16 v[160:163], a[240:243], v[104:107], v[160:163]\n"
      "ds_read_b128 v[72:75], v0 offset:28672\n"
      "v_add_f32_dpp v239, v239, v239 row_ror:2 row_mask:0xf bank_mask:0xf\n"
      "v_mfma_f32_16x16x32_bf16 v[160:163], a[244:247], v[108:111], v[160:163]\n"
      "ds_read_b128 v[76:79], v0 offset:29696\n"
      "v_add_f32_dpp v239, v239, v239 row_ror:1 row_mask:0xf bank_mask:0xf\n"
      "v_mfma_f32_16x16x32_bf16 v[160:163], a[248:251], v[112:115], v[160:163]\n"
      "v_add_f32_dpp v238, v238, v238 row_ror:1 row_mask:0xf bank_mask:0xf\n"
      "v_add_f32 v168, v168, v156\n"
      "v_mfma_f32_16x16x32_bf16 v[160:163], a[252:255], v[116:119], v[160:163]\n"
      "ds_read_b128 v[100:103], v0 offset:35840\n"
      "v_mul_f32 v238, 0x3ae38e39, v238\n"
      "v_mfma_f32_16x16x32_bf16 v[160:163], v[24:27], v[120:123], v[160:163]\n"
      "v_and_b32 v169, 0xffff0000, v218\n"
      "v_mfma_f32_16x16x32_bf16 v[160:163], v[28:31], v[124:127], v[160:163]\n"
      "ds_read_b128 v[96:99], v0 offset:34816\n"
      "v_and_b32 v175, 0xffff0000, v221\n"
      "v_mfma_f32_16x16x32_bf16 v[160:163], v[32:35], v[128:131], v[160:163]\n"
      "ds_read_b128 v[116:119], v0 offset:39936\n"
      "v_lshlrev_b32 v174, 16, v221\n"
      "v_mfma_f32_16x16x32_bf16 v[160:163], v[36:39], v[132:135], v[160:163]\n"
      "ds_read_b128 v[120:123], v0 offset:40960\n"
      "v_add_f32 v167, v167, v155\n"
      "v_mfma_f32_16x16x32_bf16 v[160:163], v[40:43], v[136:139], v[160:163]\n"
      "ds_read_b128 v[108:111], v0 offset:37888\n"
      "v_and_b32 v171, 0xffff0000, v219\n"
      "v_mfma_f32_16x16x32_bf16 v[160:163], v[44:47], v[140:143], v[160:163]\n"
      "ds_read_b128 v[88:91], v0 offset:32768\n"
      "v_add_f32 v166, v166, v154\n"
      "v_mfma_f32_16x16x32_bf16 v[160:163], v[48:51], v[144:147], v[160:163]\n"
      "v_add_f32 v239, 0x3afba882, v239\n"
      "v_mul_f32 v239, 0x3ae38e39, v239\n"
      "v_mfma_f32_16x16x32_bf16 v[160:163], v[52:55], v[148:151], v[160:163]\n"
      "s_sub_u32 s59, s59, 1\n"
      "s_cbranch_scc1 .Ld_out0\n"
      ".Ld_t1:\n"
      "v_mfma_f32_16x16x32_bf16 v[152:155], a[0:3], v[56:59], 0\n"
      "s_waitcnt lgkmcnt(4)\n"
      "buffer_store_dwordx2 v[238:239], v12, s[8:11], s56 offen sc1\n"
      "v_mfma_f32_16x16x32_bf16 v[152:155], a[4:7], v[60:63], v[152:155]\n"
      "ds_read_b128 v[104:107], v0 offset:36864\n"
      "v_mfma_f32_16x16x32_bf16 v[152:155], a[8:11], v[64:67], v[152:155]\n"
      "ds_read_b128 v[140:143], v0 offset:46080\n"
      "v_mfma_f32_16x16x32_bf16 v[152:155], a[12:15], v[68:71], v[152:155]\n"
      "ds_read_b128 v[112:115], v0 offset:38912\n"
      "v_mfma_f32_16x16x32_bf16 v[152:155], a[16:19], v[72:75], v[152:155]\n"
      "ds_read_b128 v[124:127], v0 offset:41984\n"
      "v_mfma_f32_16x16x32_bf16 v[152:155], a[20:23], v[76:79], v[152:155]\n"
      "ds_read_b128 v[132:135], v0 offset:44032\n"
      "v_mfma_f32_16x16x32_bf16 v[152:155], a[24:27], v[80:83], v[152:155]\n"
      "s_mov_b32 s51, s36\n"
      "v_add_f32 v174, v174, v162\n"
      "s_add_u32 s36, s36, 165888\n"
      "v_mfma_f32_16x16x32_bf16 v[152:155], a[28:31], v[84:87], v[152:155]\n"
      "s_waitcnt lgkmcnt(5)\n"
      "ds_read_b128 v[148:151], v0 offset:48128\n"
      "v_mfma_f32_16x16x32_bf16 v[152:155], a[32:35], v[88:91], v[152:155]\n"
      "ds_read_b128 v[128:131], v0 offset:43008\n"
      "s_add_u32 m0, s43, 73728\n"
      "v_mfma_f32_16x16x32_bf16 v[152:155], a[36:39], v[92:95], v[152:155]\n"
      "ds_read_b128 v[136:139], v0 offset:45056\n"
      "v_mfma_f32_16x16x32_bf16 v[152:155], a[40:43], v[96:99], v[152:155]\n"
      "v_mfma_f32_16x16x32_bf16 v[152:155], a[44:47], v[100:103], v[152:155]\n"
      "s_waitcnt lgkmcnt(4)\n"
      "v_add_f32 v170, v170, v158\n"
      "v_mfma_f32_16x16x32_bf16 v[152:155], a[48:51], v[104:107], v[152:155]\n"
      "v_mfma_f32_16x16x32_bf16 v[152:155], a[52:55], v[108:111], v[152:155]\n"
      "s_waitcnt vmcnt(8) lgkmcnt(3)\n"
      "v_lshlrev_b32 v172, 16, v220\n"
      "v_cmp_ne_u32 vcc, s21, v20\n"
      "v_mfma_f32_16x16x32_bf16 v[152:155], a[56:59], v[112:115], v[152:155]\n"
      "ds_read_b128 v[144:147], v0 offset:47104\n"
      "s_add_u32 s38, s38, 147456\n"
      "v_mfma_f32_16x16x32_bf16 v[152:155], a[60:63], v[116:119], v[152:155]\n"
      "s_min_u32 s36, s36, s6\n"
      "v_add_f32 v171, v171, v159\n"
      "v_mfma_f32_16x16x32_bf16 v[152:155], a[64:67], v[120:123], v[152:155]\n"
      "v_mfma_f32_16x16x32_bf16 v[152:155], a[68:71], v[124:127], v[152:155]\n"
      "s_waitcnt lgkmcnt(1)\n"
      "v_mfma_f32_16x16x32_bf16 v[152:155], a[72:75], v[128:131], v[152:155]\n"
      "v_add_f32 v169, v169, v157\n"
      "v_add_f32 v172, v172, v160\n"
      "v_mfma_f32_16x16x32_bf16 v[152:155], a[76:79], v[132:135], v[152:155]\n"
      "v_mfma_f32_16x16x32_bf16 v[152:155], a[80:83], v[136:139], v[152:155]\n"
      "s_cbranch_vccnz .Lsp_d3_6\n"
      ".Lbk_d3_6:\n"
      "v_mfma_f32_16x16x32_bf16 v[152:155], a[84:87], v[140:143], v[152:155]\n"
      "s_waitcnt lgkmcnt(0)\n"
      "v_mfma_f32_16x16x32_bf16 v[152:155], a[88:91], v[144:147], v[152:155]\n"
      "v_mfma_f32_16x16x32_bf16 v[152:155], a[92:95], v[148:151], v[152:155]\n"
      "buffer_load_dwordx4 v0, s[8:11], s51 offen offset:2048 sc1 lds\n"
      "v_mfma_f32_16x16x32_bf16 v[156:159], a[96:99], v[56:59], 0\n"
      "v_mfma_f32_16x16x32_bf16 v[156:159], a[100:103], v[60:63], v[156:159]\n"
      "v_add_u32 v14, 1, v14\n"
      "v_mfma_f32_16x16x32_bf16 v[156:159], a[104:107], v[64:67], v[156:159]\n"
      "v_cvt_pk_bf16_f32 v225, v154, v155\n"
      "v_mfma_f32_16x16x32_bf16 v[156:159], a[108:111], v[68:71], v[156:159]\n"
      "v_add_f32 v175, v175, v163\n"
      "v_mfma_f32_16x16x32_bf16 v[156:159], a[112:115], v[72:75], v[156:159]\n"
      "buffer_load_dwordx4 v0, s[8:11], s51 offen offset:1024 sc1 lds\n"
      "v_mfma_f32_16x16x32_bf16 v[156:159], a[116:119], v[76:79], v[156:159]\n"
      "v_mfma_f32_16x16x32_bf16 v[156:159], a[120:123], v[80:83], v[156:159]\n"
      "v_mfma_f32_16x16x32_bf16 v[156:159], a[124:127], v[84:87], v[156:159]\n"
      "v_and_b32 v173, 0xffff0000, v220\n"
      "v_mfma_f32_16x16x32_bf16 v[156:159], a[128:131], v[88:91], v[156:159]\n"
      "buffer_load_dword v20, v2, s[8:11], s36 offen sc1\n"
      "v_mfma_f32_16x16x32_bf16 v[156:159], a[132:135], v[92:95], v[156:159]\n"
      "s_waitcnt vmcnt(5)\n"
      "ds_write_b32 v16, v14\n"
      "v_mfma_f32_16x16x32_bf16 v[156:159], a[136:139], v[96:99], v[156:159]\n"
      "buffer_load_dwordx4 v0, s[8:11], s51 offen offset:0 sc1 lds\n"
      "v_mfma_f32_16x16x32_bf16 v[156:159], a[140:143], v[100:103], v[156:159]\n"
      "v_add_f32 v173, v173, v161\n"
      "v_mfma_f32_16x16x32_bf16 v[156:159], a[144:147], v[104:107], v[156:159]\n"
      "v_mfma_f32_16x16x32_bf16 v[156:159], a[148:151], v[108:111], v[156:159]\n"
      "v_mfma_f32_16x16x32_bf16 v[156:159], a[152:155], v[112:115], v[156:159]\n"
      "v_mfma_f32_16x16x32_bf16 v[156:159], a[156:159], v[116:119], v[156:159]\n"
      "buffer_load_dwordx4 v0, s[8:11], s51 offen offset:3072 sc1 lds\n"
      "v_mfma_f32_16x16x32_bf16 v[156:159], a[160:163], v[120:123], v[156:159]\n"
      "s_add_u32 m0, s43, 77824\n"
      "v_mfma_f32_16x16x32_bf16 v[156:159], a[164:167], v[124:127], v[156:159]\n"
      "v_mfma_f32_16x16x32_bf16 v[156:159], a[168:171], v[128:131], v[156:159]\n"
      "ds_read_b32 v15, v17\n"
      "v_mfma_f32_16x16x32_bf16 v[156:159], a[172:175], v[132:135], v[156:159]\n"
      "s_add_u32 s52, s52, 173056\n"
      "v_mfma_f32_16x16x32_bf16 v[156:159], a[176:179], v[136:139], v[156:159]\n"
      "buffer_load_dwordx4 v1, s[8:11], s51 offen offset:0 sc1 lds\n"
      "v_mfma_f32_16x16x32_bf16 v[156:159], a[180:183], v[140:143], v[156:159]\n"
      "v_mfma_f32_16x16x32_bf16 v[156:159], a[184:187], v[144:147], v[156:159]\n"
      "v_cvt_pk_bf16_f32 v224, v152, v153\n"
      "v_mfma_f32_16x16x32_bf16 v[156:159], a[188:191], v[148:151], v[156:159]\n"
      "s_waitcnt lgkmcnt(0)\n"
      "v_cmp_gt_i32 vcc, v14, v15\n"
      "v_mfma_f32_16x16x32_bf16 v[160:163], a[192:195], v[56:59], 0\n"
      "buffer_load_dwordx4 v1, s[8:11], s51 offen offset:1024 sc1 lds\n"
      "v_mfma_f32_16x16x32_bf16 v[160:163], a[196:199], v[60:63], v[160:163]\n"
      "v_mfma_f32_16x16x32_bf16 v[160:163], a[200:203], v[64:67], v[160:163]\n"
      "s_cbranch_vccnz .Lsp_dy2_7\n"
      ".Lbk_dy2_7:\n"
      "v_mfma_f32_16x16x32_bf16 v[160:163], a[204:207], v[68:71], v[160:163]\n"
      "ds_read_b128 v[68:71], v22 offset:3072\n"
      "v_mfma_f32_16x16x32_bf16 v[160:163], a[208:211], v[72:75], v[160:163]\n"
      "ds_read_b128 v[60:63], v22 offset:1024\n"
      "v_mfma_f32_16x16x32_bf16 v[160:163], a[212:215], v[76:79], v[160:163]\n"
      "s_waitcnt vmcnt(7)\n"
      "buffer_store_dword v18, v5, s[8:11], s56 offen sc1\n"
      "v_mfma_f32_16x16x32_bf16 v[160:163], a[216:219], v[80:83], v[160:163]\n"
      "v_cvt_pk_bf16_f32 v227, v158, v159\n"
      "v_mfma_f32_16x16x32_bf16 v[160:163], a[220:223], v[84:87], v[160:163]\n"
      "ds_read_b128 v[80:83], v22 offset:6144\n"
      "v_cvt_pk_bf16_f32 v226, v156, v157\n"
      "v_mfma_f32_16x16x32_bf16 v[160:163], a[224:227], v[88:91], v[160:163]\n"
      "ds_read_b128 v[72:75], v22 offset:4096\n"
      "v_mfma_f32_16x16x32_bf16 v[160:163], a[228:231], v[92:95], v[160:163]\n"
      "ds_read_b128 v[76:79], v22 offset:5120\n"
      "v_mfma_f32_16x16x32_bf16 v[160:163], a[232:235], v[96:99], v[160:163]\n"
      "ds_read_b128 v[96:99], v22 offset:10240\n"
      "v_mfma_f32_16x16x32_bf16 v[160:163], a[236:239], v[100:103], v[160:163]\n"
      "v_mfma_f32_16x16x32_bf16 v[160:163], a[240:243], v[104:107], v[160:163]\n"
      "buffer_store_dwordx4 v[224:227], v0, s[8:11], s37 offen sc1\n"
      "v_mfma_f32_16x16x32_bf16 v[160:163], a[244:247], v[108:111], v[160:163]\n"
      "v_mfma_f32_16x16x32_bf16 v[160:163], a[248:251], v[112:115], v[160:163]\n"
      "ds_read_b128 v[84:87], v22 offset:7168\n"
      "v_mfma_f32_16x16x32_bf16 v[160:163], a[252:255], v[116:119], v[160:163]\n"
      "v_mfma_f32_16x16x32_bf16 v[160:163], v[24:27], v[120:123], v[160:163]\n"
      "ds_read_b128 v[56:59], v22 offset:0\n"
      "v_mfma_f32_16x16x32_bf16 v[160:163], v[28:31], v[124:127], v[160:163]\n"
      "ds_read_b128 v[92:95], v22 offset:9216\n"
      "s_mov_b32 s51, s36\n"
      "v_mfma_f32_16x16x32_bf16 v[160:163], v[32:35], v[128:131], v[160:163]\n"
      "ds_read_b128 v[64:67], v22 offset:2048\n"
      "v_mfma_f32_16x16x32_bf16 v[160:163], v[36:39], v[132:135], v[160:163]\n"
      "ds_read_b128 v[100:103], v22 offset:11264\n"
      "v_mfma_f32_16x16x32_bf16 v[160:163], v[40:43], v[136:139], v[160:163]\n"
      "ds_read_b128 v[132:135], v22 offset:19456\n"
      "v_mfma_f32_16x16x32_bf16 v[160:163], v[44:47], v[140:143], v[160:163]\n"
      "v_mfma_f32_16x16x32_bf16 v[160:163], v[48:51], v[144:147], v[160:163]\n"
      "s_waitcnt lgkmcnt(4)\n"
      "ds_read_b128 v[112:115], v22 offset:14336\n"
      "s_add_u32 m0, s43, 98304\n"
      "v_mfma_f32_16x16x32_bf16 v[160:163], v[52:55], v[148:151], v[160:163]\n"
      "s_sub_u32 s59, s59, 1\n"
      "s_cbranch_scc1 .Ld_out1\n"
      ".Ld_t2:\n"
      "v_mfma_f32_16x16x32_bf16 v[152:155], a[0:3], v[56:59], 0\n"
      "ds_read_b128 v[88:91], v22 offset:8192\n"
      "v_mfma_f32_16x16x32_bf16 v[152:155], a[4:7], v[60:63], v[152:155]\n"
      "s_waitcnt lgkmcnt(4)\n"
      "ds_read_b128 v[120:123], v22 offset:16384\n"
      "v_mfma_f32_16x16x32_bf16 v[152:155], a[8:11], v[64:67], v[152:155]\n"
      "s_add_u32 s36, s36, 165888\n"
      "ds_read_b128 v[104:107], v22 offset:12288\n"
      "v_cvt_pk_bf16_f32 v229, v162, v163\n"
      "v_mfma_f32_16x16x32_bf16 v[152:155], a[12:15], v[68:71], v[152:155]\n"
      "ds_read_b128 v[148:151], v22 offset:23552\n"
      "v_cvt_pk_bf16_f32 v228, v160, v161\n"
      "v_mfma_f32_16x16x32_bf16 v[152:155], a[16:19], v[72:75], v[152:155]\n"
      "buffer_store_dwordx2 v[228:229], v13, s[8:11], s37 offen sc1\n"
      "v_mfma_f32_16x16x32_bf16 v[152:155], a[20:23], v[76:79], v[152:155]\n"
      "ds_read_b128 v[108:111], v22 offset:13312\n"
      "v_mfma_f32_16x16x32_bf16 v[152:155], a[24:27], v[80:83], v[152:155]\n"
      "ds_read_b128 v[116:119], v22 offset:15360\n"
      "v_mfma_f32_16x16x32_bf16 v[152:155], a[28:31], v[84:87], v[152:155]\n"
      "s_waitcnt lgkmcnt(5)\n"
      "ds_read_b128 v[124:127], v22 offset:17408\n"
      "v_mfma_f32_16x16x32_bf16 v[152:155], a[32:35], v[88:91], v[152:155]\n"
      "ds_read_b128 v[140:143], v22 offset:21504\n"
      "v_mfma_f32_16x16x32_bf16 v[152:155], a[36:39], v[92:95], v[152:155]\n"
      "ds_read_b128 v[128:131], v22 offset:18432\n"
      "v_mfma_f32_16x16x32_bf16 v[152:155], a[40:43], v[96:99], v[152:155]\n"
      "v_mfma_f32_16x16x32_bf16 v[152:155], a[44:47], v[100:103], v[152:155]\n"
      "s_waitcnt lgkmcnt(5)\n"
      "ds_read_b128 v[136:139], v22 offset:20480\n"
      "v_mfma_f32_16x16x32_bf16 v[152:155], a[48:51], v[104:107], v[152:155]\n"
      "s_waitcnt lgkmcnt(5)\n"
      "v_mfma_f32_16x16x32_bf16 v[152:155], a[52:55], v[108:111], v[152:155]\n"
      "v_mfma_f32_16x16x32_bf16 v[152:155], a[56:59], v[112:115], v[152:155]\n"
      "s_waitcnt lgkmcnt(3)\n"
      "ds_read_b128 v[144:147], v22 offset:22528\n"
      "v_mfma_f32_16x16x32_bf16 v[152:155], a[60:63], v[116:119], v[152:155]\n"
      "v_add_u32 v14, 1, v14\n"
      "v_mfma_f32_16x16x32_bf16 v[152:155], a[64:67], v[120:123], v[152:155]\n"
      "s_waitcnt vmcnt(7) lgkmcnt(2)\n"
      "v_cmp_ne_u32 vcc, s21, v20\n"
      "v_mfma_f32_16x16x32_bf16 v[152:155], a[68:71], v[124:127], v[152:155]\n"
      "v_mfma_f32_16x16x32_bf16 v[152:155], a[72:75], v[128:131], v[152:155]\n"
      "v_mfma_f32_16x16x32_bf16 v[152:155], a[76:79], v[132:135], v[152:155]\n"
      "s_waitcnt lgkmcnt(1)\n"
      "v_mfma_f32_16x16x32_bf16 v[152:155], a[80:83], v[136:139], v[152:155]\n"
      "v_mfma_f32_16x16x32_bf16 v[152:155], a[84:87], v[140:143], v[152:155]\n"
      "s_waitcnt lgkmcnt(0)\n"
      "s_cbranch_vccnz .Lsp_d4_8\n"
      ".Lbk_d4_8:\n"
      "v_mfma_f32_16x16x32_bf16 v[152:155], a[88:91], v[144:147], v[152:155]\n"
      "v_mfma_f32_16x16x32_bf16 v[152:155], a[92:95], v[148:151], v[152:155]\n"
      "s_min_u32 s36, s36, s6\n"
      "v_mfma_f32_16x16x32_bf16 v[156:159], a[96:99], v[56:59], 0\n"
      "buffer_load_dwordx4 v0, s[8:11], s51 offen offset:0 sc1 lds\n"
      "v_mfma_f32_16x16x32_bf16 v[156:159], a[100:103], v[60:63], v[156:159]\n"
      "v_mfma_f32_16x16x32_bf16 v[156:159], a[104:107], v[64:67], v[156:159]\n"
      "v_mfma_f32_16x16x32_bf16 v[156:159], a[108:111], v[68:71], v[156:159]\n"
      "v_mfma_f32_16x16x32_bf16 v[156:159], a[112:115], v[72:75], v[156:159]\n"
      "buffer_load_dwordx4 v0, s[8:11], s51 offen offset:2048 sc1 lds\n"
      "v_mfma_f32_16x16x32_bf16 v[156:159], a[116:119], v[76:79], v[156:159]\n"
      "s_waitcnt vmcnt(5)\n"
      "ds_write_b32 v16, v14\n"
      "v_mfma_f32_16x16x32_bf16 v[156:159], a[120:123], v[80:83], v[156:159]\n"
      "v_mfma_f32_16x16x32_bf16 v[156:159], a[124:127], v[84:87], v[156:159]\n"
      "v_mfma_f32_16x16x32_bf16 v[156:159], a[128:131], v[88:91], v[156:159]\n"
      "v_mfma_f32_16x16x32_bf16 v[156:159], a[132:135], v[92:95], v[156:159]\n"
      "buffer_load_dword v20, v2, s[8:11], s36 offen sc1\n"
      "v_mfma_f32_16x16x32_bf16 v[156:159], a[136:139], v[96:99], v[156:159]\n"
      "v_mfma_f32_16x16x32_bf16 v[156:159], a[140:143], v[100:103], v[156:159]\n"
      "v_cvt_pk_bf16_f32 v225, v154, v155\n"
      "v_mfma_f32_16x16x32_bf16 v[156:159], a[144:147], v[104:107], v[156:159]\n"
      "buffer_load_dwordx4 v0, s[8:11], s51 offen offset:3072 sc1 lds\n"
      "v_mfma_f32_16x16x32_bf16 v[156:159], a[148:151], v[108:111], v[156:159]\n"
      "v_mfma_f32_16x16x32_bf16 v[156:159], a[152:155], v[112:115], v[156:159]\n"
      "v_mfma_f32_16x16x32_bf16 v[156:159], a[156:159], v[116:119], v[156:159]\n"
      "ds_read_b32 v15, v17\n"
      "v_mfma_f32_16x16x32_bf16 v[156:159], a[160:163], v[120:123], v[156:159]\n"
      "buffer_load_dwordx4 v0, s[8:11], s51 offen offset:1024 sc1 lds\n"
      "v_mfma_f32_16x16x32_bf16 v[156:159], a[164:167], v[124:127], v[156:159]\n"
      "s_add_u32 m0, s43, 102400\n"
      "v_mfma_f32_16x16x32_bf16 v[156:159], a[168:171], v[128:131], v[156:159]\n"
      "v_mfma_f32_16x16x32_bf16 v[156:159], a[172:175], v[132:135], v[156:159]\n"
      "v_mfma_f32_16x16x32_bf16 v[156:159], a[176:179], v[136:139], v[156:159]\n"
      "buffer_load_dwordx4 v1, s[8:11], s51 offen offset:1024 sc1 lds\n"
      "v_mfma_f32_16x16x32_bf16 v[156:159], a[180:183], v[140:143], v[156:159]\n"
      "v_mfma_f32_16x16x32_bf16 v[156:159], a[184:187], v[144:147], v[156:159]\n"
      "s_waitcnt lgkmcnt(0)\n"
      "v_cmp_gt_i32 vcc, v14, v15\n"
      "v_mfma_f32_16x16x32_bf16 v[156:159], a[188:191], v[148:151], v[156:159]\n"
      "v_mfma_f32_16x16x32_bf16 v[160:163], a[192:195], v[56:59], 0\n"
      "buffer_load_dwordx4 v1, s[8:11], s51 offen offset:0 sc1 lds\n"
      "v_mfma_f32_16x16x32_bf16 v[160:163], a[196:199], v[60:63], v[160:163]\n"
      "s_cbranch_vccnz .Lsp_dy3_9\n"
      ".Lbk_dy3_9:\n"
      "ds_read_b128 v[60:63], v22 offset:25600\n"
      "v_mfma_f32_16x16x32_bf16 v[160:163], a[200:203], v[64:67], v[160:163]\n"
      "v_mfma_f32_16x16x32_bf16 v[160:163], a[204:207], v[68:71], v[160:163]\n"
      "v_cvt_pk_bf16_f32 v227, v158, v159\n"
      "v_mfma_f32_16x16x32_bf16 v[160:163], a[208:211], v[72:75], v[160:163]\n"
      "ds_read_b128 v[64:67], v22 offset:26624\n"
      "v_mfma_f32_16x16x32_bf16 v[160:163], a[212:215], v[76:79], v[160:163]\n"
      "s_mov_b32 s51, s36\n"
      "ds_read_b128 v[72:75], v22 offset:28672\n"
      "v_mfma_f32_16x16x32_bf16 v[160:163], a[216:219], v[80:83], v[160:163]\n"
      "ds_read_b128 v[68:71], v22 offset:27648\n"
      "v_mfma_f32_16x16x32_bf16 v[160:163], a[220:223], v[84:87], v[160:163]\n"
      "v_cvt_pk_bf16_f32 v224, v152, v153\n"
      "v_mfma_f32_16x16x32_bf16 v[160:163], a[224:227], v[88:91], v[160:163]\n"
      "s_waitcnt vmcnt(7) lgkmcnt(3)\n"
      "buffer_store_dword v18, v4, s[8:11], s52 offen sc1\n"
      "v_mfma_f32_16x16x32_bf16 v[160:163], a[228:231], v[92:95], v[160:163]\n"
      "ds_read_b128 v[92:95], v22 offset:33792\n"
      "v_mfma_f32_16x16x32_bf16 v[160:163], a[232:235], v[96:99], v[160:163]\n"
      "ds_read_b128 v[56:59], v22 offset:24576\n"
      "v_mfma_f32_16x16x32_bf16 v[160:163], a[236:239], v[100:103], v[160:163]\n"
      "buffer_load_dword v21, v3, s[8:11], s53 offen sc1\n"
      "v_mfma_f32_16x16x32_bf16 v[160:163], a[240:243], v[104:107], v[160:163]\n"
      "ds_read_b128 v[100:103], v22 offset:35840\n"
      "v_mfma_f32_16x16x32_bf16 v[160:163], a[244:247], v[108:111], v[160:163]\n"
      "ds_read_b128 v[80:83], v22 offset:30720\n"
      "v_mfma_f32_16x16x32_bf16 v[160:163], a[248:251], v[112:115], v[160:163]\n"
      "ds_read_b128 v[112:115], v22 offset:38912\n"
      "v_mfma_f32_16x16x32_bf16 v[160:163], a[252:255], v[116:119], v[160:163]\n"
      "s_add_u32 s37, s37, 106496\n"
      "ds_read_b128 v[88:91], v22 offset:32768\n"
      "v_mfma_f32_16x16x32_bf16 v[160:163], v[24:27], v[120:123], v[160:163]\n"
      "ds_read_b128 v[96:99], v22 offset:34816\n"
      "v_cvt_pk_bf16_f32 v226, v156, v157\n"
      "v_mfma_f32_16x16x32_bf16 v[160:163], v[28:31], v[124:127], v[160:163]\n"
      "ds_read_b128 v[76:79], v22 offset:29696\n"
      "v_mfma_f32_16x16x32_bf16 v[160:163], v[32:35], v[128:131], v[160:163]\n"
      "v_mfma_f32_16x16x32_bf16 v[160:163], v[36:39], v[132:135], v[160:163]\n"
      "buffer_store_dwordx4 v[224:227], v0, s[8:11], s37 offen sc1\n"
      "v_mfma_f32_16x16x32_bf16 v[160:163], v[40:43], v[136:139], v[160:163]\n"
      "ds_read_b128 v[116:119], v22 offset:39936\n"
      "v_mfma_f32_16x16x32_bf16 v[160:163], v[44:47], v[140:143], v[160:163]\n"
      "ds_read_b128 v[84:87], v22 offset:31744\n"
      "v_mfma_f32_16x16x32_bf16 v[160:163], v[48:51], v[144:147], v[160:163]\n"
      "s_waitcnt lgkmcnt(4)\n"
      "ds_read_b128 v[104:107], v22 offset:36864\n"
      "v_mfma_f32_16x16x32_bf16 v[160:163], v[52:55], v[148:151], v[160:163]\n"
      "s_sub_u32 s59, s59, 1\n"
      "s_cbranch_scc1 .Ld_out2\n"
      ".Ld_t3:\n"
      "v_mfma_f32_16x16x32_bf16 v[152:155], a[0:3], v[56:59], 0\n"
      "ds_read_b128 v[120:123], v22 offset:40960\n"
      "v_mfma_f32_16x16x32_bf16 v[152:155], a[4:7], v[60:63], v[152:155]\n"
      "ds_read_b128 v[108:111], v22 offset:37888\n"
      "v_mfma_f32_16x16x32_bf16 v[152:155], a[8:11], v[64:67], v[152:155]\n"
      "ds_read_b128 v[132:135], v22 offset:44032\n"
      "v_mfma_f32_16x16x32_bf16 v[152:155], a[12:15], v[68:71], v[152:155]\n"
      "ds_read_b128 v[136:139], v22 offset:45056\n"
      "v_cvt_pk_bf16_f32 v229, v162, v163\n"
      "v_mfma_f32_16x16x32_bf16 v[152:155], a[16:19], v[72:75], v[152:155]\n"
      "s_waitcnt lgkmcnt(5)\n"
      "v_mfma_f32_16x16x32_bf16 v[152:155], a[20:23], v[76:79], v[152:155]\n"
      "ds_read_b128 v[124:127], v22 offset:41984\n"
      "v_mfma_f32_16x16x32_bf16 v[152:155], a[24:27], v[80:83], v[152:155]\n"
      "s_add_u32 m0, s43, 122880\n"
      "v_cvt_pk_bf16_f32 v228, v160, v161\n"
      "v_mfma_f32_16x16x32_bf16 v[152:155], a[28:31], v[84:87], v[152:155]\n"
      "buffer_store_dwordx2 v[228:229], v13, s[8:11], s37 offen sc1\n"
      "v_mfma_f32_16x16x32_bf16 v[152:155], a[32:35], v[88:91], v[152:155]\n"
      "v_mfma_f32_16x16x32_bf16 v[152:155], a[36:39], v[92:95], v[152:155]\n"
      "ds_read_b128 v[128:131], v22 offset:43008\n"
      "v_mfma_f32_16x16x32_bf16 v[152:155], a[40:43], v[96:99], v[152:155]\n"
      "ds_read_b128 v[144:147], v22 offset:47104\n"
      "v_mfma_f32_16x16x32_bf16 v[152:155], a[44:47], v[100:103], v[152:155]\n"
      "s_waitcnt lgkmcnt(3)\n"
      "ds_read_b128 v[148:151], v22 offset:48128\n"
      "v_mfma_f32_16x16x32_bf16 v[152:155], a[48:51], v[104:107], v[152:155]\n"
      "ds_read_b128 v[140:143], v22 offset:46080\n"
      "v_mfma_f32_16x16x32_bf16 v[152:155], a[52:55], v[108:111], v[152:155]\n"
      "v_mfma_f32_16x16x32_bf16 v[152:155], a[56:59], v[112:115], v[152:155]\n"
      "s_waitcnt vmcnt(8) lgkmcnt(4)\n"
      "v_cmp_ne_u32 vcc, s21, v20\n"
      "v_mfma_f32_16x16x32_bf16 v[152:155], a[60:63], v[116:119], v[152:155]\n"
      "v_mfma_f32_16x16x32_bf16 v[152:155], a[64:67], v[120:123], v[152:155]\n"
      "s_add_u32 s36, s36, 165888\n"
      "s_add_u32 s52, s52, 106496\n"
      "v_mfma_f32_16x16x32_bf16 v[152:155], a[68:71], v[124:127], v[152:155]\n"
      "s_waitcnt lgkmcnt(2)\n"
      "v_mfma_f32_16x16x32_bf16 v[152:155], a[72:75], v[128:131], v[152:155]\n"
      "s_cbranch_vccnz .Lsp_d5_10\n"
      ".Lbk_d5_10:\n"
      "v_mfma_f32_16x16x32_bf16 v[152:155], a[76:79], v[132:135], v[152:155]\n"
      "buffer_load_dwordx4 v0, s[8:11], s51 offen offset:1024 sc1 lds\n"
      "v_mfma_f32_16x16x32_bf16 v[152:155], a[80:83], v[136:139], v[152:155]\n"
      "s_waitcnt lgkmcnt(0)\n"
      "v_mfma_f32_16x16x32_bf16 v[152:155], a[84:87], v[140:143], v[152:155]\n"
      "v_add_u32 v14, 1, v14\n"
      "v_mfma_f32_16x16x32_bf16 v[152:155], a[88:91], v[144:147], v[152:155]\n"
      "v_mfma_f32_16x16x32_bf16 v[152:155], a[92:95], v[148:151], v[152:155]\n"
      "buffer_load_dwordx4 v0, s[8:11], s51 offen offset:0 sc1 lds\n"
      "v_mfma_f32_16x16x32_bf16 v[156:159], a[96:99], v[56:59], 0\n"
      "v_mfma_f32_16x16x32_bf16 v[156:159], a[100:103], v[60:63], v[156:159]\n"
      "v_mfma_f32_16x16x32_bf16 v[156:159], a[104:107], v[64:67], v[156:159]\n"
      "v_mfma_f32_16x16x32_bf16 v[156:159], a[108:111], v[68:71], v[156:159]\n"
      "buffer_load_dwordx4 v0, s[8:11], s51 offen offset:2048 sc1 lds\n"
      "v_mfma_f32_16x16x32_bf16 v[156:159], a[112:115], v[72:75], v[156:159]\n"
      "s_waitcnt vmcnt(7)\n"
      "ds_write_b32 v16, v14\n"
      "v_mfma_f32_16x16x32_bf16 v[156:159], a[116:119], v[76:79], v[156:159]\n"
      "v_mfma_f32_16x16x32_bf16 v[156:159], a[120:123], v[80:83], v[156:159]\n"
      "v_mfma_f32_16x16x32_bf16 v[156:159], a[124:127], v[84:87], v[156:159]\n"
      "buffer_load_dwordx4 v0, s[8:11], s51 offen offset:3072 sc1 lds\n"
      "v_mfma_f32_16x16x32_bf16 v[156:159], a[128:131], v[88:91], v[156:159]\n"
      "v_mfma_f32_16x16x32_bf16 v[156:159], a[132:135], v[92:95], v[156:159]\n"
      "v_cvt_pk_bf16_f32 v224, v152, v153\n"
      "v_mfma_f32_16x16x32_bf16 v[156:159], a[136:139], v[96:99], v[156:159]\n"
      "v_mfma_f32_16x16x32_bf16 v[156:159], a[140:143], v[100:103], v[156:159]\n"
      "s_min_u32 s36, s36, s6\n"
      "s_add_u32 m0, s43, 126976\n"
      "v_mfma_f32_16x16x32_bf16 v[156:159], a[144:147], v[104:107], v[156:159]\n"
      "buffer_load_dwordx4 v1, s[8:11], s51 offen offset:1024 sc1 lds\n"
      "v_mfma_f32_16x16x32_bf16 v[156:159], a[148:151], v[108:111], v[156:159]\n"
      "ds_read_b32 v15, v17\n"
      "v_mfma_f32_16x16x32_bf16 v[156:159], a[152:155], v[112:115], v[156:159]\n"
      "v_mfma_f32_16x16x32_bf16 v[156:159], a[156:159], v[116:119], v[156:159]\n"
      "v_mfma_f32_16x16x32_bf16 v[156:159], a[160:163], v[120:123], v[156:159]\n"
      "buffer_load_dword v20, v2, s[8:11], s36 offen sc1\n"
      "v_mfma_f32_16x16x32_bf16 v[156:159], a[164:167], v[124:127], v[156:159]\n"
      "v_mfma_f32_16x16x32_bf16 v[156:159], a[168:171], v[128:131], v[156:159]\n"
      "s_waitcnt lgkmcnt(0)\n"
      "v_cmp_gt_i32 vcc, v14, v15\n"
      "v_mfma_f32_16x16x32_bf16 v[156:159], a[172:175], v[132:135], v[156:159]\n"
      "buffer_load_dwordx4 v1, s[8:11], s51 offen offset:0 sc1 lds\n"
      "v_mfma_f32_16x16x32_bf16 v[156:159], a[176:179], v[136:139], v[156:159]\n"
      "s_add_u32 s55, s53, 6656\n"
      "v_mfma_f32_16x16x32_bf16 v[156:159], a[180:183], v[140:143], v[156:159]\n"
      "s_add_u32 s54, s53, 13312\n"
      "v_mfma_f32_16x16x32_bf16 v[156:159], a[184:187], v[144:147], v[156:159]\n"
      "s_waitcnt vmcnt(9)\n"
      "s_cbranch_vccnz .Lsp_dy4_11\n"
      ".Lbk_dy4_11:\n"
      "v_cmp_ne_u32 vcc, s21, v21\n"
      "v_mfma_f32_16x16x32_bf16 v[156:159], a[188:191], v[148:151], v[156:159]\n"
      ".Ld_p1:\n"
      "s_cbranch_execz .Ld_pr\n"
      "v_mfma_f32_16x16x32_bf16 v[160:163], a[192:195], v[56:59], 0\n"
      "v_mfma_f32_16x16x32_bf16 v[160:163], a[196:199], v[60:63], v[160:163]\n"
      "s_cbranch_vccnz .Lsp_df_12\n"
      ".Lbk_df_12:\n"
      "buffer_load_dwordx4 v[176:179], v0, s[8:11], s53 offen sc1\n"
      "v_mfma_f32_16x16x32_bf16 v[160:163], a[200:203], v[64:67], v[160:163]\n"
      "ds_read_b128 v[60:63], v23 offset:1024\n"
      "v_mfma_f32_16x16x32_bf16 v[160:163], a[204:207], v[68:71], v[160:163]\n"
      "ds_read_b128 v[64:67], v23 offset:2048\n"
      "v_mfma_f32_16x16x32_bf16 v[160:163], a[208:211], v[72:75], v[160:163]\n"
      "buffer_load_dwordx2 v[180:181], v13, s[8:11], s53 offen sc1\n"
      "v_mfma_f32_16x16x32_bf16 v[160:163], a[212:215], v[76:79], v[160:163]\n"
      "ds_read_b128 v[72:75], v23 offset:4096\n"
      "v_mfma_f32_16x16x32_bf16 v[160:163], a[216:219], v[80:83], v[160:163]\n"
      "ds_read_b128 v[68:71], v23 offset:3072\n"
      "s_add_u32 s58, s41, 4294967264\n"
      "v_mfma_f32_16x16x32_bf16 v[160:163], a[220:223], v[84:87], v[160:163]\n"
      "ds_read_b128 v[80:83], v23 offset:6144\n"
      "v_mfma_f32_16x16x32_bf16 v[160:163], a[224:227], v[88:91], v[160:163]\n"
      "v_cvt_pk_bf16_f32 v225, v154, v155\n"
      "v_mfma_f32_16x16x32_bf16 v[160:163], a[228:231], v[92:95], v[160:163]\n"
      "buffer_load_dwordx4 v[184:187], v0, s[8:11], s55 offen sc1\n"
      "v_mfma_f32_16x16x32_bf16 v[160:163], a[232:235], v[96:99], v[160:163]\n"
      "ds_read_b128 v[84:87], v23 offset:7168\n"
      "v_cvt_pk_bf16_f32 v226, v156, v157\n"
      "v_mfma_f32_16x16x32_bf16 v[160:163], a[236:239], v[100:103], v[160:163]\n"
      "ds_read_b128 v[76:79], v23 offset:5120\n"
      "v_mfma_f32_16x16x32_bf16 v[160:163], a[240:243], v[104:107], v[160:163]\n"
      "s_add_u32 s37, s37, 106496\n"
      "ds_read_b128 v[56:59], v23 offset:0\n"
      "v_mfma_f32_16x16x32_bf16 v[160:163], a[244:247], v[108:111], v[160:163]\n"
      "s_waitcnt vmcnt(10) lgkmcnt(4)\n"
      "buffer_store_dword v18, v4, s[8:11], s52 offen sc1\n"
      "v_mfma_f32_16x16x32_bf16 v[160:163], a[248:251], v[112:115], v[160:163]\n"
      "ds_read_b128 v[88:91], v23 offset:8192\n"
      "v_mfma_f32_16x16x32_bf16 v[160:163], a[252:255], v[116:119], v[160:163]\n"
      "ds_read_b128 v[96:99], v23 offset:10240\n"
      "v_mfma_f32_16x16x32_bf16 v[160:163], v[24:27], v[120:123], v[160:163]\n"
      "buffer_load_dwordx4 v[192:195], v0, s[8:11], s54 offen sc1\n"
      "v_mfma_f32_16x16x32_bf16 v[160:163], v[28:31], v[124:127], v[160:163]\n"
      "v_mfma_f32_16x16x32_bf16 v[160:163], v[32:35], v[128:131], v[160:163]\n"
      "buffer_load_dwordx2 v[188:189], v13, s[8:11], s55 offen sc1\n"
      "v_mfma_f32_16x16x32_bf16 v[160:163], v[36:39], v[132:135], v[160:163]\n"
      "ds_read_b128 v[100:103], v23 offset:11264\n"
      "v_mfma_f32_16x16x32_bf16 v[160:163], v[40:43], v[136:139], v[160:163]\n"
      "s_add_u32 s55, s53, 19968\n"
      "ds_read_b128 v[132:135], v23 offset:19456\n"
      "v_mfma_f32_16x16x32_bf16 v[160:163], v[44:47], v[140:143], v[160:163]\n"
      "ds_read_b128 v[104:107], v23 offset:12288\n"
      "v_mfma_f32_16x16x32_bf16 v[160:163], v[48:51], v[144:147], v[160:163]\n"
      "s_waitcnt lgkmcnt(3)\n"
      "buffer_load_dwordx2 v[196:197], v13, s[8:11], s54 offen sc1\n"
      "v_mfma_f32_16x16x32_bf16 v[160:163], v[52:55], v[148:151], v[160:163]\n"
      "s_sub_u32 s59, s59, 1\n"
      "s_cbranch_scc1 .Ld_out3\n"
      ".Ld_t4:\n"
      "v_mfma_f32_16x16x32_bf16 v[152:155], a[0:3], v[56:59], 0\n"
      "ds_read_b128 v[92:95], v23 offset:9216\n"
      "v_mfma_f32_16x16x32_bf16 v[152:155], a[4:7], v[60:63], v[152:155]\n"
      "ds_read_b128 v[108:111], v23 offset:13312\n"
      "s_add_u32 m0, s43, 0\n"
      "v_mfma_f32_16x16x32_bf16 v[152:155], a[8:11], v[64:67], v[152:155]\n"
      "ds_read_b128 v[128:131], v23 offset:18432\n"
      "v_cvt_pk_bf16_f32 v228, v160, v161\n"
      "v_mfma_f32_16x16x32_bf16 v[152:155], a[12:15], v[68:71], v[152:155]\n"
      "buffer_load_dwordx4 v[200:203], v0, s[8:11], s55 offen sc1\n"
      "v_mfma_f32_16x16x32_bf16 v[152:155], a[16:19], v[72:75], v[152:155]\n"
      "ds_read_b128 v[116:119], v23 offset:15360\n"
      "v_cvt_pk_bf16_f32 v227, v158, v159\n"
      "v_mfma_f32_16x16x32_bf16 v[152:155], a[20:23], v[76:79], v[152:155]\n"
      "ds_read_b128 v[112:115], v23 offset:14336\n"
      "v_mfma_f32_16x16x32_bf16 v[152:155], a[24:27], v[80:83], v[152:155]\n"
      "s_mov_b32 s51, s36\n"
      "ds_read_b128 v[120:123], v23 offset:16384\n"
      "v_mfma_f32_16x16x32_bf16 v[152:155], a[28:31], v[84:87], v[152:155]\n"
      "buffer_store_dwordx4 v[224:227], v0, s[8:11], s37 offen sc1\n"
      "v_mfma_f32_16x16x32_bf16 v[152:155], a[32:35], v[88:91], v[152:155]\n"
      "s_waitcnt lgkmcnt(4)\n"
      "ds_read_b128 v[140:143], v23 offset:21504\n"
      "v_mfma_f32_16x16x32_bf16 v[152:155], a[36:39], v[92:95], v[152:155]\n"
      "ds_read_b128 v[124:127], v23 offset:17408\n"
      "v_mfma_f32_16x16x32_bf16 v[152:155], a[40:43], v[96:99], v[152:155]\n"
      "ds_read_b128 v[136:139], v23 offset:20480\n"
      "v_mfma_f32_16x16x32_bf16 v[152:155], a[44:47], v[100:103], v[152:155]\n"
      "v_cvt_pk_bf16_f32 v229, v162, v163\n"
      "v_mfma_f32_16x16x32_bf16 v[152:155], a[48:51], v[104:107], v[152:155]\n"
      "buffer_store_dwordx2 v[228:229], v13, s[8:11], s37 offen sc1\n"
      "v_mfma_f32_16x16x32_bf16 v[152:155], a[52:55], v[108:111], v[152:155]\n"
      "s_waitcnt lgkmcnt(3)\n"
      "ds_read_b128 v[148:151], v23 offset:23552\n"
      "v_add_u32_e64 v14, 1, v14\n"
      "v_mfma_f32_16x16x32_bf16 v[152:155], a[56:59], v[112:115], v[152:155]\n"
      "s_add_u32 s36, s36, 165888\n"
      "s_add_u32 s37, s37, 106496\n"
      "ds_read_b128 v[144:147], v23 offset:22528\n"
      "v_mfma_f32_16x16x32_bf16 v[152:155], a[60:63], v[116:119], v[152:155]\n"
      "v_mfma_f32_16x16x32_bf16 v[152:155], a[64:67], v[120:123], v[152:155]\n"
      "s_waitcnt lgkmcnt(3)\n"
      "s_min_u32 s36, s36, s6\n"
      "v_mfma_f32_16x16x32_bf16 v[152:155], a[68:71], v[124:127], v[152:155]\n"
      "buffer_load_dwordx2 v[204:205], v13, s[8:11], s55 offen sc1\n"
      "v_mfma_f32_16x16x32_bf16 v[152:155], a[72:75], v[128:131], v[152:155]\n"
      "s_cmp_lt_u32 s41, 32\n"
      "s_cselect_b32 s56, s39, 0x40000000\n"
      "v_mfma_f32_16x16x32_bf16 v[152:155], a[76:79], v[132:135], v[152:155]\n"
      "s_waitcnt lgkmcnt(2)\n"
      "s_add_u32 s54, s53, 26624\n"
      "v_mfma_f32_16x16x32_bf16 v[152:155], a[80:83], v[136:139], v[152:155]\n"
      "s_waitcnt vmcnt(12) lgkmcnt(1)\n"
      "s_add_u32 s53, s53, 599040\n"
      "v_cmp_ne_u32 vcc, s21, v20\n"
      "v_mfma_f32_16x16x32_bf16 v[152:155], a[84:87], v[140:143], v[152:155]\n"
      "s_waitcnt lgkmcnt(0)\n"
      "buffer_load_dwordx2 v[212:213], v13, s[8:11], s54 offen sc1\n"
      "v_mfma_f32_16x16x32_bf16 v[152:155], a[88:91], v[144:147], v[152:155]\n"
      "v_mfma_f32_16x16x32_bf16 v[152:155], a[92:95], v[148:151], v[152:155]\n"
      "s_add_u32 s39, s39, 147456\n"
      "v_mfma_f32_16x16x32_bf16 v[156:159], a[96:99], v[56:59], 0\n"
      "s_waitcnt vmcnt(12)\n"
      "ds_write_b32 v16, v14\n"
      "v_mfma_f32_16x16x32_bf16 v[156:159], a[100:103], v[60:63], v[156:159]\n"
      "buffer_load_dwordx4 v[208:211], v0, s[8:11], s54 offen sc1\n"
      "v_mfma_f32_16x16x32_bf16 v[156:159], a[104:107], v[64:67], v[156:159]\n"
      "s_cmp_lt_u32 s58, 32\n"
      "s_cselect_b32 s57, s40, 0x40000000\n"
      "v_mfma_f32_16x16x32_bf16 v[156:159], a[108:111], v[68:71], v[156:159]\n"
      "s_cbranch_vccnz .Lsp_d0_13\n"
      ".Lbk_d0_13:\n"
      "v_cvt_pk_bf16_f32 v224, v152, v153\n"
      "v_mfma_f32_16x16x32_bf16 v[156:159], a[112:115], v[72:75], v[156:159]\n"
      "v_cvt_pk_bf16_f32 v225, v154, v155\n"
      "v_mfma_f32_16x16x32_bf16 v[156:159], a[116:119], v[76:79], v[156:159]\n"
      "buffer_load_dwordx4 v0, s[8:11], s51 offen offset:0 sc1 lds\n"
      "v_mfma_f32_16x16x32_bf16 v[156:159], a[120:123], v[80:83], v[156:159]\n"
      "s_waitcnt vmcnt(13)\n"
      "v_lshlrev_b32 v234, 16, v178\n"
      "v_lshlrev_b32 v233, 16, v177\n"
      "v_mfma_f32_16x16x32_bf16 v[156:159], a[124:127], v[84:87], v[156:159]\n"
      "v_lshlrev_b32 v232, 16, v176\n"
      "v_add_f32 v166, v166, v233\n"
      "v_mfma_f32_16x16x32_bf16 v[156:159], a[128:131], v[88:91], v[156:159]\n"
      "s_add_u32 s52, s52, 106496\n"
      "v_add_f32 v164, v164, v232\n"
      "v_mfma_f32_16x16x32_bf16 v[156:159], a[132:135], v[92:95], v[156:159]\n"
      "buffer_load_dwordx4 v0, s[8:11], s51 offen offset:1024 sc1 lds\n"
      "v_mfma_f32_16x16x32_bf16 v[156:159], a[136:139], v[96:99], v[156:159]\n"
      "s_waitcnt vmcnt(13) lgkmcnt(0)\n"
      "v_lshlrev_b32 v233, 16, v181\n"
      "v_lshlrev_b32 v235, 16, v179\n"
      "v_mfma_f32_16x16x32_bf16 v[156:159], a[140:143], v[100:103], v[156:159]\n"
      "v_and_b32 v176, 0xffff0000, v176\n"
      "v_lshlrev_b32 v232, 16, v180\n"
      "v_mfma_f32_16x16x32_bf16 v[156:159], a[144:147], v[104:107], v[156:159]\n"
      "v_add_f32 v172, v172, v232\n"
      "v_and_b32 v181, 0xffff0000, v181\n"
      "v_mfma_f32_16x16x32_bf16 v[156:159], a[148:151], v[108:111], v[156:159]\n"
      "v_add_f32 v168, v168, v234\n"
      "v_add_f32 v174, v174, v233\n"
      "v_mfma_f32_16x16x32_bf16 v[156:159], a[152:155], v[112:115], v[156:159]\n"
      "buffer_load_dword v20, v2, s[8:11], s36 offen sc1\n"
      "v_mfma_f32_16x16x32_bf16 v[156:159], a[156:159], v[116:119], v[156:159]\n"
      "ds_read_b32 v15, v17\n"
      "v_add_f32 v165, v165, v176\n"
      "v_mfma_f32_16x16x32_bf16 v[156:159], a[160:163], v[120:123], v[156:159]\n"
      "buffer_load_dwordx4 v0, s[8:11], s51 offen offset:2048 sc1 lds\n"
      "v_mfma_f32_16x16x32_bf16 v[156:159], a[164:167], v[124:127], v[156:159]\n"
      "s_waitcnt vmcnt(14)\n"
      "v_lshlrev_b32 v232, 16, v184\n"
      "v_and_b32 v177, 0xffff0000, v177\n"
      "v_mfma_f32_16x16x32_bf16 v[156:159], a[168:171], v[128:131], v[156:159]\n"
      "s_add_u32 s40, s40, 4294856704\n"
      "v_add_f32 v167, v167, v177\n"
      "v_and_b32 v180, 0xffff0000, v180\n"
      "v_mfma_f32_16x16x32_bf16 v[156:159], a[172:175], v[132:135], v[156:159]\n"
      "v_add_f32 v164, v164, v232\n"
      "v_lshlrev_b32 v233, 16, v185\n"
      "v_mfma_f32_16x16x32_bf16 v[156:159], a[176:179], v[136:139], v[156:159]\n"
      "buffer_load_dwordx4 v0, s[8:11], s51 offen offset:3072 sc1 lds\n"
      "v_mfma_f32_16x16x32_bf16 v[156:159], a[180:183], v[140:143], v[156:159]\n"
      "s_waitcnt lgkmcnt(0)\n"
      "v_add_f32 v166, v166, v233\n"
      "v_cmp_gt_i32 vcc, v14, v15\n"
      "v_mfma_f32_16x16x32_bf16 v[156:159], a[184:187], v[144:147], v[156:159]\n"
      "s_waitcnt vmcnt(12)\n"
      "v_lshlrev_b32 v232, 16, v188\n"
      "v_add_f32 v172, v172, v232\n"
      "v_mfma_f32_16x16x32_bf16 v[156:159], a[188:191], v[148:151], v[156:159]\n"
      "v_and_b32 v185, 0xffff0000, v185\n"
      "v_lshlrev_b32 v232, 16, v192\n"
      "v_mfma_f32_16x16x32_bf16 v[160:163], a[192:195], v[56:59], 0\n"
      "s_add_u32 m0, s43, 4096\n"
      "v_lshlrev_b32 v234, 16, v186\n"
      "v_add_f32 v164, v164, v232\n"
      "v_mfma_f32_16x16x32_bf16 v[160:163], a[196:199], v[60:63], v[160:163]\n"
      "buffer_load_dwordx4 v1, s[8:11], s51 offen offset:1024 sc1 lds\n"
      "v_mfma_f32_16x16x32_bf16 v[160:163], a[200:203], v[64:67], v[160:163]\n"
      "s_cbranch_vccnz .Lsp_dy5_14\n"
      ".Lbk_dy5_14:\n"
      "v_cvt_pk_bf16_f32 v227, v158, v159\n"
      "v_mfma_f32_16x16x32_bf16 v[160:163], a[204:207], v[68:71], v[160:163]\n"
      "ds_read_b128 v[56:59], v23 offset:24576\n"
      "v_lshlrev_b32 v233, 16, v189\n"
      "v_mfma_f32_16x16x32_bf16 v[160:163], a[208:211], v[72:75], v[160:163]\n"
      "s_waitcnt vmcnt(12)\n"
      "ds_read_b128 v[64:67], v23 offset:26624\n"
      "v_lshlrev_b32 v232, 16, v196\n"
      "v_mfma_f32_16x16x32_bf16 v[160:163], a[212:215], v[76:79], v[160:163]\n"
      "buffer_load_dwordx4 v1, s[8:11], s51 offen offset:0 sc1 lds\n"
      "v_mfma_f32_16x16x32_bf16 v[160:163], a[216:219], v[80:83], v[160:163]\n"
      "ds_read_b128 v[76:79], v23 offset:29696\n"
      "v_add_f32 v174, v174, v233\n"
      "v_mfma_f32_16x16x32_bf16 v[160:163], a[220:223], v[84:87], v[160:163]\n"
      "v_and_b32 v184, 0xffff0000, v184\n"
      "v_add_f32 v172, v172, v232\n"
      "v_mfma_f32_16x16x32_bf16 v[160:163], a[224:227], v[88:91], v[160:163]\n"
      "v_add_f32 v170, v170, v235\n"
      "v_cvt_pk_bf16_f32 v226, v156, v157\n"
      "v_mfma_f32_16x16x32_bf16 v[160:163], a[228:231], v[92:95], v[160:163]\n"
      "s_add_u32 s41, s41, 6\n"
      "ds_read_b128 v[88:91], v23 offset:32768\n"
      "v_add_f32 v165, v165, v184\n"
      "v_mfma_f32_16x16x32_bf16 v[160:163], a[232:235], v[96:99], v[160:163]\n"
      "ds_read_b128 v[68:71], v23 offset:27648\n"
      "v_mfma_f32_16x16x32_bf16 v[160:163], a[236:239], v[100:103], v[160:163]\n"
      "ds_read_b128 v[96:99], v23 offset:34816\n"
      "v_add_f32 v168, v168, v234\n"
      "v_mfma_f32_16x16x32_bf16 v[160:163], a[240:243], v[104:107], v[160:163]\n"
      "ds_read_b128 v[80:83], v23 offset:30720\n"
      "v_lshlrev_b32 v233, 16, v193\n"
      "v_mfma_f32_16x16x32_bf16 v[160:163], a[244:247], v[108:111], v[160:163]\n"
      "ds_read_b128 v[72:75], v23 offset:28672\n"
      "v_and_b32 v178, 0xffff0000, v178\n"
      "v_mfma_f32_16x16x32_bf16 v[160:163], a[248:251], v[112:115], v[160:163]\n"
      "s_waitcnt vmcnt(12) lgkmcnt(5)\n"
      "s_mov_b32 s51, s36\n"
      "ds_read_b128 v[112:115], v23 offset:38912\n"
      "v_lshlrev_b32 v232, 16, v200\n"
      "v_mfma_f32_16x16x32_bf16 v[160:163], a[252:255], v[116:119], v[160:163]\n"
      "v_add_f32 v164, v164, v232\n"
      "v_and_b32 v186, 0xffff0000, v186\n"
      "v_mfma_f32_16x16x32_bf16 v[160:163], v[24:27], v[120:123], v[160:163]\n"
      "s_waitcnt vmcnt(10) lgkmcnt(5)\n"
      "buffer_store_dword v18, v4, s[8:11], s52 offen sc1\n"
      "v_mfma_f32_16x16x32_bf16 v[160:163], v[28:31], v[124:127], v[160:163]\n"
      "ds_read_b128 v[60:63], v23 offset:25600\n"
      "v_add_f32 v169, v169, v178\n"
      "v_mfma_f32_16x16x32_bf16 v[160:163], v[32:35], v[128:131], v[160:163]\n"
      "buffer_store_dwordx4 v[224:227], v0, s[8:11], s37 offen sc1\n"
      "v_mfma_f32_16x16x32_bf16 v[160:163], v[36:39], v[132:135], v[160:163]\n"
      "ds_read_b128 v[104:107], v23 offset:36864\n"
      "v_and_b32 v200, 0xffff0000, v200\n"
      "v_mfma_f32_16x16x32_bf16 v[160:163], v[40:43], v[136:139], v[160:163]\n"
      "ds_read_b128 v[84:87], v23 offset:31744\n"
      "v_add_f32 v173, v173, v180\n"
      "v_mfma_f32_16x16x32_bf16 v[160:163], v[44:47], v[140:143], v[160:163]\n"
      "ds_read_b128 v[140:143], v23 offset:46080\n"
      "v_add_f32 v166, v166, v233\n"
      "v_mfma_f32_16x16x32_bf16 v[160:163], v[48:51], v[144:147], v[160:163]\n"
      "ds_read_b128 v[100:103], v23 offset:35840\n"
      "v_lshlrev_b32_e64 v234, 16, v194\n"
      "v_mfma_f32_16x16x32_bf16 v[160:163], v[52:55], v[148:151], v[160:163]\n"
      "s_sub_u32 s59, s59, 1\n"
      "s_cbranch_scc1 .Ld_out4\n"
      ".Ld_t5:\n"
      "v_mfma_f32_16x16x32_bf16 v[152:155], a[0:3], v[56:59], 0\n"
      "s_waitcnt lgkmcnt(4)\n"
      "ds_read_b128 v[92:95], v23 offset:33792\n"
      "ds_read_b128 v[116:119], v23 offset:39936\n"
      "v_mfma_f32_16x16x32_bf16 v[152:155], a[4:7], v[60:63], v[152:155]\n"
      "ds_read_b128 v[108:111], v23 offset:37888\n"
      "v_and_b32 v192, 0xffff0000, v192\n"
      "v_mfma_f32_16x16x32_bf16 v[152:155], a[8:11], v[64:67], v[152:155]\n"
      "ds_read_b128 v[128:131], v23 offset:43008\n"
      "v_cvt_pk_bf16_f32 v229, v162, v163\n"
      "v_mfma_f32_16x16x32_bf16 v[152:155], a[12:15], v[68:71], v[152:155]\n"
      "ds_read_b128 v[136:139], v23 offset:45056\n"
      "v_add_f32 v175, v175, v181\n"
      "v_mfma_f32_16x16x32_bf16 v[152:155], a[16:19], v[72:75], v[152:155]\n"
      "v_cvt_pk_bf16_f32 v228, v160, v161\n"
      "v_and_b32 v189, 0xffff0000, v189\n"
      "v_mfma_f32_16x16x32_bf16 v[152:155], a[20:23], v[76:79], v[152:155]\n"
      "s_waitcnt vmcnt(11) lgkmcnt(5)\n"
      "ds_read_b128 v[144:147], v23 offset:47104\n"
      "v_lshlrev_b32 v232, 16, v204\n"
      "v_mfma_f32_16x16x32_bf16 v[152:155], a[24:27], v[80:83], v[152:155]\n"
      "buffer_store_dwordx2 v[228:229], v13, s[8:11], s37 offen sc1\n"
      "v_mfma_f32_16x16x32_bf16 v[152:155], a[28:31], v[84:87], v[152:155]\n"
      "v_lshlrev_b32 v235, 16, v187\n"
      "v_add_f32 v172, v172, v232\n"
      "v_mfma_f32_16x16x32_bf16 v[152:155], a[32:35], v[88:91], v[152:155]\n"
      "s_waitcnt vmcnt(10) lgkmcnt(3)\n"
      "ds_read_b128 v[120:123], v23 offset:40960\n"
      "v_lshlrev_b32 v232, 16, v208\n"
      "v_mfma_f32_16x16x32_bf16 v[152:155], a[36:39], v[92:95], v[152:155]\n"
      "ds_read_b128 v[124:127], v23 offset:41984\n"
      "v_add_f32 v170, v170, v235\n"
      "v_mfma_f32_16x16x32_bf16 v[152:155], a[40:43], v[96:99], v[152:155]\n"
      "s_add_u32 m0, s43, 24576\n"
      "v_and_b32 v194, 0xffff0000, v194\n"
      "v_and_b32 v208, 0xffff0000, v208\n"
      "v_mfma_f32_16x16x32_bf16 v[152:155], a[44:47], v[100:103], v[152:155]\n"
      "ds_read_b128 v[132:135], v23 offset:44032\n"
      "v_add_f32 v167, v167, v185\n"
      "v_mfma_f32_16x16x32_bf16 v[152:155], a[48:51], v[104:107], v[152:155]\n"
      "v_lshlrev_b32 v233, 16, v197\n"
      "v_add_u32 v14, 1, v14\n"
      "v_mfma_f32_16x16x32_bf16 v[152:155], a[52:55], v[108:111], v[152:155]\n"
      "s_add_u32 s36, s36, 165888\n"
      "v_lshlrev_b32 v235, 16, v195\n"
      "v_add_f32 v168, v168, v234\n"
      "v_mfma_f32_16x16x32_bf16 v[152:155], a[56:59], v[112:115], v[152:155]\n"
      "buffer_load_dwordx4 v[216:219], v6, s[8:11], s38 offen sc1\n"
      "v_mfma_f32_16x16x32_bf16 v[152:155], a[60:63], v[116:119], v[152:155]\n"
      "s_waitcnt lgkmcnt(2)\n"
      "ds_read_b128 v[148:151], v23 offset:48128\n"
      "v_add_f32 v164, v164, v232\n"
      "v_mfma_f32_16x16x32_bf16 v[152:155], a[64:67], v[120:123], v[152:155]\n"
      "s_waitcnt lgkmcnt(2)\n"
      "v_add_f32 v165, v165, v192\n"
      "v_lshlrev_b32 v234, 16, v202\n"
      "v_mfma_f32_16x16x32_bf16 v[152:155], a[68:71], v[124:127], v[152:155]\n"
      "v_add_f32 v174, v174, v233\n"
      "v_lshlrev_b32 v232, 16, v212\n"
      "v_mfma_f32_16x16x32_bf16 v[152:155], a[72:75], v[128:131], v[152:155]\n"
      "s_waitcnt lgkmcnt(1)\n"
      "v_and_b32 v179, 0xffff0000, v179\n"
      "v_add_f32 v169, v169, v186\n"
      "v_mfma_f32_16x16x32_bf16 v[152:155], a[76:79], v[132:135], v[152:155]\n"
      "s_waitcnt vmcnt(8)\n"
      "v_lshlrev_b32 v233, 16, v201\n"
      "v_cmp_ne_u32 vcc, s21, v20\n"
      "v_mfma_f32_16x16x32_bf16 v[152:155], a[80:83], v[136:139], v[152:155]\n"
      "buffer_load_dwordx2 v[220:221], v7, s[8:11], s38 offen sc1\n"
      "v_mfma_f32_16x16x32_bf16 v[152:155], a[84:87], v[140:143], v[152:155]\n"
      "v_add_f32 v166, v166, v233\n"
      "v_and_b32 v193, 0xffff0000, v193\n"
      "v_mfma_f32_16x16x32_bf16 v[152:155], a[88:91], v[144:147], v[152:155]\n"
      "s_waitcnt lgkmcnt(0)\n"
      "v_and_b32 v197, 0xffff0000, v197\n"
      "v_lshlrev_b32 v233, 16, v205\n"
      "v_mfma_f32_16x16x32_bf16 v[152:155], a[92:95], v[148:151], v[152:155]\n"
      "v_add_f32 v170, v170, v235\n"
      "v_add_f32 v174, v174, v233\n"
      "v_mfma_f32_16x16x32_bf16 v[156:159], a[96:99], v[56:59], 0\n"
      "s_cbranch_vccnz .Lsp_d1_15\n"
      ".Lbk_d1_15:\n"
      "v_lshlrev_b32 v235, 16, v203\n"
      "v_mfma_f32_16x16x32_bf16 v[156:159], a[100:103], v[60:63], v[156:159]\n"
      "buffer_load_dwordx4 v0, s[8:11], s51 offen offset:0 sc1 lds\n"
      "v_mfma_f32_16x16x32_bf16 v[156:159], a[104:107], v[64:67], v[156:159]\n"
      "v_and_b32 v187, 0xffff0000, v187\n"
      "v_lshlrev_b32 v233, 16, v209\n"
      "v_mfma_f32_16x16x32_bf16 v[156:159], a[108:111], v[68:71], v[156:159]\n"
      "v_add_f32 v170, v170, v235\n"
      "v_add_f32 v166, v166, v233\n"
      "v_mfma_f32_16x16x32_bf16 v[156:159], a[112:115], v[72:75], v[156:159]\n"
      "v_and_b32 v201, 0xffff0000, v201\n"
      "v_add_f32 v165, v165, v200\n"
      "v_mfma_f32_16x16x32_bf16 v[156:159], a[116:119], v[76:79], v[156:159]\n"
      "v_and_b32 v188, 0xffff0000, v188\n"
      "v_add_f32 v165, v165, v208\n"
      "v_mfma_f32_16x16x32_bf16 v[156:159], a[120:123], v[80:83], v[156:159]\n"
      "buffer_load_dwordx4 v0, s[8:11], s51 offen offset:1024 sc1 lds\n"
      "v_mfma_f32_16x16x32_bf16 v[156:159], a[124:127], v[84:87], v[156:159]\n"
      "v_add_f32 v168, v168, v234\n"
      "v_mul_f32 v237, v164, v164\n"
      "v_mfma_f32_16x16x32_bf16 v[156:159], a[128:131], v[88:91], v[156:159]\n"
      "v_add_f32 v167, v167, v193\n"
      "v_lshlrev_b32 v233, 16, v213\n"
      "v_mfma_f32_16x16x32_bf16 v[156:159], a[132:135], v[92:95], v[156:159]\n"
      "s_waitcnt vmcnt(7)\n"
      "ds_write_b32 v16, v14\n"
      "v_mfma_f32_16x16x32_bf16 v[156:159], a[136:139], v[96:99], v[156:159]\n"
      "buffer_load_dwordx4 v0, s[8:11], s51 offen offset:2048 sc1 lds\n"
      "v_mfma_f32_16x16x32_bf16 v[156:159], a[140:143], v[100:103], v[156:159]\n"
      "v_add_f32 v171, v171, v179\n"
      "v_add_f32 v167, v167, v201\n"
      "v_mfma_f32_16x16x32_bf16 v[156:159], a[144:147], v[104:107], v[156:159]\n"
      "v_lshlrev_b32 v234, 16, v210\n"
      "v_and_b32 v213, 0xffff0000, v213\n"
      "v_mfma_f32_16x16x32_bf16 v[156:159], a[148:151], v[108:111], v[156:159]\n"
      "s_min_u32 s36, s36, s6\n"
      "v_add_f32 v173, v173, v188\n"
      "v_and_b32 v209, 0xffff0000, v209\n"
      "v_mfma_f32_16x16x32_bf16 v[156:159], a[152:155], v[112:115], v[156:159]\n"
      "buffer_load_dword v20, v2, s[8:11], s36 offen sc1\n"
      "v_mfma_f32_16x16x32_bf16 v[156:159], a[156:159], v[116:119], v[156:159]\n"
      "v_add_f32 v236, v164, v165\n"
      "v_add_f32 v175, v175, v189\n"
      "v_mfma_f32_16x16x32_bf16 v[156:159], a[160:163], v[120:123], v[156:159]\n"
      "buffer_load_dwordx4 v0, s[8:11], s51 offen offset:3072 sc1 lds\n"
      "v_mfma_f32_16x16x32_bf16 v[156:159], a[164:167], v[124:127], v[156:159]\n"
      "v_add_f32 v167, v167, v209\n"
      "v_and_b32 v203, 0xffff0000, v203\n"
      "v_mfma_f32_16x16x32_bf16 v[156:159], a[168:171], v[128:131], v[156:159]\n"
      "ds_read_b32 v15, v17\n"
      "v_fmac_f32 v237, v165, v165\n"
      "v_mfma_f32_16x16x32_bf16 v[156:159], a[172:175], v[132:135], v[156:159]\n"
      "s_add_u32 m0, s43, 28672\n"
      "v_add_f32 v175, v175, v197\n"
      "v_and_b32 v204, 0xffff0000, v204\n"
      "v_mfma_f32_16x16x32_bf16 v[156:159], a[176:179], v[136:139], v[156:159]\n"
      "v_and_b32 v195, 0xffff0000, v195\n"
      "v_add_f32 v172, v172, v232\n"
      "v_mfma_f32_16x16x32_bf16 v[156:159], a[180:183], v[140:143], v[156:159]\n"
      "buffer_load_dwordx4 v1, s[8:11], s51 offen offset:0 sc1 lds\n"
      "v_mfma_f32_16x16x32_bf16 v[156:159], a[184:187], v[144:147], v[156:159]\n"
      "v_fmac_f32 v237, v166, v166\n"
      "v_cvt_pk_bf16_f32 v225, v154, v155\n"
      "v_mfma_f32_16x16x32_bf16 v[156:159], a[188:191], v[148:151], v[156:159]\n"
      "s_waitcnt lgkmcnt(0)\n"
      "v_add_f32 v169, v169, v194\n"
      "v_cmp_gt_i32 vcc, v14, v15\n"
      "v_mfma_f32_16x16x32_bf16 v[160:163], a[192:195], v[56:59], 0\n"
      "v_and_b32 v196, 0xffff0000, v196\n"
      "v_and_b32 v212, 0xffff0000, v212\n"
      "v_mfma_f32_16x16x32_bf16 v[160:163], a[196:199], v[60:63], v[160:163]\n"
      "buffer_load_dwordx4 v1, s[8:11], s51 offen offset:1024 sc1 lds\n"
      "v_mfma_f32_16x16x32_bf16 v[160:163], a[200:203], v[64:67], v[160:163]\n"
      "s_cbranch_vccnz .Lsp_dy0_16\n"
      ".Lbk_dy0_16:\n"
      "v_mfma_f32_16x16x32_bf16 v[160:163], a[204:207], v[68:71], v[160:163]\n"
      "ds_read_b128 v[56:59], v0 offset:0\n"
      "v_cvt_pk_bf16_f32 v177, v166, v167\n"
      "v_mfma_f32_16x16x32_bf16 v[160:163], a[208:211], v[72:75], v[160:163]\n"
      "v_add_f32 v171, v171, v187\n"
      "v_and_b32 v205, 0xffff0000, v205\n"
      "v_mfma_f32_16x16x32_bf16 v[160:163], a[212:215], v[76:79], v[160:163]\n"
      "ds_read_b128 v[60:63], v0 offset:1024\n"
      "v_cvt_pk_bf16_f32 v224, v152, v153\n"
      "v_mfma_f32_16x16x32_bf16 v[160:163], a[216:219], v[80:83], v[160:163]\n"
      "s_add_u32 s52, s52, 106496\n"
      "v_and_b32 v202, 0xffff0000, v202\n"
      "v_add_f32 v169, v169, v202\n"
      "v_mfma_f32_16x16x32_bf16 v[160:163], a[220:223], v[84:87], v[160:163]\n"
      "ds_read_b128 v[84:87], v0 offset:7168\n"
      "v_add_f32 v236, v236, v166\n"
      "v_mfma_f32_16x16x32_bf16 v[160:163], a[224:227], v[88:91], v[160:163]\n"
      "ds_read_b128 v[72:75], v0 offset:4096\n"
      "v_and_b32 v210, 0xffff0000, v210\n"
      "v_mfma_f32_16x16x32_bf16 v[160:163], a[228:231], v[92:95], v[160:163]\n"
      "v_add_f32 v173, v173, v196\n"
      "v_lshlrev_b32 v235, 16, v211\n"
      "v_mfma_f32_16x16x32_bf16 v[160:163], a[232:235], v[96:99], v[160:163]\n"
      "v_add_f32 v170, v170, v235\n"
      "v_add_f32 v171, v171, v195\n"
      "v_mfma_f32_16x16x32_bf16 v[160:163], a[236:239], v[100:103], v[160:163]\n"
      "s_add_u32 s37, s37, 106496\n"
      "ds_read_b128 v[88:91], v0 offset:8192\n"
      "v_cvt_pk_bf16_f32 v227, v158, v159\n"
      "v_mfma_f32_16x16x32_bf16 v[160:163], a[240:243], v[104:107], v[160:163]\n"
      "s_waitcnt vmcnt(9) lgkmcnt(3)\n"
      "buffer_store_dword v18, v4, s[8:11], s52 offen sc1\n"
      "v_mfma_f32_16x16x32_bf16 v[160:163], a[244:247], v[108:111], v[160:163]\n"
      "ds_read_b128 v[68:71], v0 offset:3072\n"
      "v_add_f32 v174, v174, v233\n"
      "v_mfma_f32_16x16x32_bf16 v[160:163], a[248:251], v[112:115], v[160:163]\n"
      "ds_read_b128 v[104:107], v0 offset:12288\n"
      "v_add_f32 v169, v169, v210\n"
      "v_mfma_f32_16x16x32_bf16 v[160:163], a[252:255], v[116:119], v[160:163]\n"
      "ds_read_b128 v[96:99], v0 offset:10240\n"
      "v_fmac_f32 v237, v167, v167\n"
      "v_mfma_f32_16x16x32_bf16 v[160:163], v[24:27], v[120:123], v[160:163]\n"
      "ds_read_b128 v[80:83], v0 offset:6144\n"
      "v_add_f32 v175, v175, v205\n"
      "v_mfma_f32_16x16x32_bf16 v[160:163], v[28:31], v[124:127], v[160:163]\n"
      "ds_read_b128 v[64:67], v0 offset:2048\n"
      "v_add_f32 v168, v168, v234\n"
      "v_mfma_f32_16x16x32_bf16 v[160:163], v[32:35], v[128:131], v[160:163]\n"
      "v_add_f32 v236, v236, v167\n"
      "v_cvt_pk_bf16_f32 v226, v156, v157\n"
      "v_mfma_f32_16x16x32_bf16 v[160:163], v[36:39], v[132:135], v[160:163]\n"
      "ds_read_b128 v[92:95], v0 offset:9216\n"
      "v_cvt_pk_bf16_f32 v176, v164, v165\n"
      "v_mfma_f32_16x16x32_bf16 v[160:163], v[40:43], v[136:139], v[160:163]\n"
      "buffer_store_dwordx4 v[224:227], v0, s[8:11], s37 offen sc1\n"
      "ds_read_b128 v[76:79], v0 offset:5120\n"
      "v_mfma_f32_16x16x32_bf16 v[160:163], v[44:47], v[140:143], v[160:163]\n"
      "ds_read_b128 v[132:135], v0 offset:19456\n"
      "v_and_b32 v211, 0xffff0000, v211\n"
      "v_mfma_f32_16x16x32_bf16 v[160:163], v[48:51], v[144:147], v[160:163]\n"
      "ds_read_b128 v[100:103], v0 offset:11264\n"
      "v_add_f32 v171, v171, v203\n"
      "v_mfma_f32_16x16x32_bf16 v[160:163], v[52:55], v[148:151], v[160:163]\n"
      "s_sub_u32 s59, s59, 1\n"
      "s_cbranch_scc0 .Ld_t0\n"
      ".Ld_out5:\n"
      "s_nop 8\n"
      "v_cvt_pk_bf16_f32 v228, v160, v161\n"
      "v_cvt_pk_bf16_f32 v229, v162, v163\n"
      "buffer_store_dwordx2 v[228:229], v13, s[8:11], s37 offen sc1\n"
      "v_cvt_pk_bf16_f32 v178, v168, v169\n"
      "v_add_f32 v175, v175, v213\n"
      "v_add_f32 v171, v171, v211\n"
      "v_add_f32 v173, v173, v204\n"
      "v_cvt_pk_bf16_f32 v179, v170, v171\n"
      "v_add_f32 v173, v173, v212\n"
      "v_cvt_pk_bf16_f32 v180, v172, v173\n"
      "v_cvt_pk_bf16_f32 v181, v174, v175\n"
      "buffer_store_dwordx2 v[180:181], v9, s[16:19], s57 offen\n"
      "buffer_store_dwordx4 v[176:179], v8, s[16:19], s57 offen\n"
      "s_add_u32 s52, s52, 106496\n"
      "s_add_u32 s37, s37, 173056\n"
      "s_waitcnt vmcnt(2)\n"
      "buffer_store_dword v18, v4, s[8:11], s52 offen sc1\n"
      "s_add_u32 s52, s52, 173056\n"
      "s_branch .Ld_end\n"
      ".Ld_out0:\n"
      "s_nop 8\n"
      "v_add_f32 v174, v174, v162\n"
      "v_add_f32 v170, v170, v158\n"
      "v_lshlrev_b32 v172, 16, v220\n"
      "s_add_u32 s38, s38, 147456\n"
      "v_add_f32 v171, v171, v159\n"
      "v_add_f32 v169, v169, v157\n"
      "v_add_f32 v172, v172, v160\n"
      "v_add_f32 v175, v175, v163\n"
      "v_and_b32 v173, 0xffff0000, v220\n"
      "v_add_f32 v173, v173, v161\n"
      "s_add_u32 s52, s52, 173056\n"
      "buffer_load_dword v21, v3, s[8:11], s53 offen sc1\n"
      "s_add_u32 s55, s53, 6656\n"
      "s_add_u32 s54, s53, 13312\n"
      "s_waitcnt vmcnt(0)\n"
      "v_cmp_ne_u32 vcc, s21, v21\n"
      "s_cbranch_vccnz .Lsp_df_17\n"
      ".Lbk_df_17:\n"
      "buffer_load_dwordx4 v[176:179], v0, s[8:11], s53 offen sc1\n"
      "buffer_load_dwordx2 v[180:181], v13, s[8:11], s53 offen sc1\n"
      "s_add_u32 s58, s41, 4294967264\n"
      "buffer_load_dwordx4 v[184:187], v0, s[8:11], s55 offen sc1\n"
      "buffer_load_dwordx4 v[192:195], v0, s[8:11], s54 offen sc1\n"
      "buffer_load_dwordx2 v[188:189], v13, s[8:11], s55 offen sc1\n"
      "s_add_u32 s55, s53, 19968\n"
      "buffer_load_dwordx2 v[196:197], v13, s[8:11], s54 offen sc1\n"
      "buffer_load_dwordx4 v[200:203], v0, s[8:11], s55 offen sc1\n"
      "buffer_load_dwordx2 v[204:205], v13, s[8:11], s55 offen sc1\n"
      "s_cmp_lt_u32 s41, 32\n"
      "s_cselect_b32 s56, s39, 0x40000000\n"
      "s_add_u32 s54, s53, 26624\n"
      "s_add_u32 s53, s53, 599040\n"
      "buffer_load_dwordx2 v[212:213], v13, s[8:11], s54 offen sc1\n"
      "s_add_u32 s39, s39, 147456\n"
      "buffer_load_dwordx4 v[208:211], v0, s[8:11], s54 offen sc1\n"
      "s_cmp_lt_u32 s58, 32\n"
      "s_cselect_b32 s57, s40, 0x40000000\n"
      "s_waitcnt vmcnt(9)\n"
      "v_lshlrev_b32 v234, 16, v178\n"
      "v_lshlrev_b32 v233, 16, v177\n"
      "v_lshlrev_b32 v232, 16, v176\n"
      "v_add_f32 v166, v166, v233\n"
      "v_add_f32 v164, v164, v232\n"
      "s_waitcnt vmcnt(8)\n"
      "v_lshlrev_b32 v233, 16, v181\n"
      "v_lshlrev_b32 v235, 16, v179\n"
      "v_and_b32 v176, 0xffff0000, v176\n"
      "v_lshlrev_b32 v232, 16, v180\n"
      "v_add_f32 v172, v172, v232\n"
      "v_and_b32 v181, 0xffff0000, v181\n"
      "v_add_f32 v168, v168, v234\n"
      "v_add_f32 v174, v174, v233\n"
      "v_add_f32 v165, v165, v176\n"
      "s_waitcnt vmcnt(7)\n"
      "v_lshlrev_b32 v232, 16, v184\n"
      "v_and_b32 v177, 0xffff0000, v177\n"
      "s_add_u32 s40, s40, 4294856704\n"
      "v_add_f32 v167, v167, v177\n"
      "v_and_b32 v180, 0xffff0000, v180\n"
      "v_add_f32 v164, v164, v232\n"
      "v_lshlrev_b32 v233, 16, v185\n"
      "v_add_f32 v166, v166, v233\n"
      "s_waitcnt vmcnt(5)\n"
      "v_lshlrev_b32 v232, 16, v188\n"
      "v_add_f32 v172, v172, v232\n"
      "v_and_b32 v185, 0xffff0000, v185\n"
      "v_lshlrev_b32 v232, 16, v192\n"
      "v_lshlrev_b32 v234, 16, v186\n"
      "v_add_f32 v164, v164, v232\n"
      "v_lshlrev_b32 v233, 16, v189\n"
      "s_waitcnt vmcnt(4)\n"
      "v_lshlrev_b32 v232, 16, v196\n"
      "v_add_f32 v174, v174, v233\n"
      "v_and_b32 v184, 0xffff0000, v184\n"
      "v_add_f32 v172, v172, v232\n"
      "v_add_f32 v170, v170, v235\n"
      "s_add_u32 s41, s41, 6\n"
      "v_add_f32 v165, v165, v184\n"
      "v_add_f32 v168, v168, v234\n"
      "v_lshlrev_b32 v233, 16, v193\n"
      "v_and_b32 v178, 0xffff0000, v178\n"
      "s_waitcnt vmcnt(3)\n"
      "v_lshlrev_b32 v232, 16, v200\n"
      "v_add_f32 v164, v164, v232\n"
      "v_and_b32 v186, 0xffff0000, v186\n"
      "v_add_f32 v169, v169, v178\n"
      "v_and_b32 v200, 0xffff0000, v200\n"
      "v_add_f32 v173, v173, v180\n"
      "v_add_f32 v166, v166, v233\n"
      "v_lshlrev_b32 v234, 16, v194\n"
      "v_and_b32 v192, 0xffff0000, v192\n"
      "v_add_f32 v175, v175, v181\n"
      "v_and_b32 v189, 0xffff0000, v189\n"
      "s_waitcnt vmcnt(2)\n"
      "v_lshlrev_b32 v232, 16, v204\n"
      "v_lshlrev_b32 v235, 16, v187\n"
      "v_add_f32 v172, v172, v232\n"
      "s_waitcnt vmcnt(0)\n"
      "v_lshlrev_b32 v232, 16, v208\n"
      "v_add_f32 v170, v170, v235\n"
      "v_and_b32 v194, 0xffff0000, v194\n"
      "v_and_b32 v208, 0xffff0000, v208\n"
      "v_add_f32 v167, v167, v185\n"
      "v_lshlrev_b32 v233, 16, v197\n"
      "v_lshlrev_b32 v235, 16, v195\n"
      "v_add_f32 v168, v168, v234\n"
      "v_add_f32 v164, v164, v232\n"
      "v_add_f32 v165, v165, v192\n"
      "v_lshlrev_b32 v234, 16, v202\n"
      "v_add_f32 v174, v174, v233\n"
      "v_lshlrev_b32 v232, 16, v212\n"
      "v_and_b32 v179, 0xffff0000, v179\n"
      "v_add_f32 v169, v169, v186\n"
      "v_lshlrev_b32 v233, 16, v201\n"
      "v_add_f32 v166, v166, v233\n"
      "v_and_b32 v193, 0xffff0000, v193\n"
      "v_and_b32 v197, 0xffff0000, v197\n"
      "v_lshlrev_b32 v233, 16, v205\n"
      "v_add_f32 v170, v170, v235\n"
      "v_add_f32 v174, v174, v233\n"
      "v_lshlrev_b32 v235, 16, v203\n"
      "v_and_b32 v187, 0xffff0000, v187\n"
      "v_lshlrev_b32 v233, 16, v209\n"
      "v_add_f32 v170, v170, v235\n"
      "v_add_f32 v166, v166, v233\n"
      "v_and_b32 v201, 0xffff0000, v201\n"
      "v_add_f32 v165, v165, v200\n"
      "v_and_b32 v188, 0xffff0000, v188\n"
      "v_add_f32 v165, v165, v208\n"
      "v_add_f32 v168, v168, v234\n"
      "v_add_f32 v167, v167, v193\n"
      "v_lshlrev_b32 v233, 16, v213\n"
      "v_add_f32 v171, v171, v179\n"
      "v_add_f32 v167, v167, v201\n"
      "v_lshlrev_b32 v234, 16, v210\n"
      "v_and_b32 v213, 0xffff0000, v213\n"
      "v_add_f32 v173, v173, v188\n"
      "v_and_b32 v209, 0xffff0000, v209\n"
      "v_add_f32 v175, v175, v189\n"
      "v_add_f32 v167, v167, v209\n"
      "v_and_b32 v203, 0xffff0000, v203\n"
      "v_add_f32 v175, v175, v197\n"
      "v_and_b32 v204, 0xffff0000, v204\n"
      "v_and_b32 v195, 0xffff0000, v195\n"
      "v_add_f32 v172, v172, v232\n"
      "v_add_f32 v169, v169, v194\n"
      "v_and_b32 v196, 0xffff0000, v196\n"
      "v_and_b32 v212, 0xffff0000, v212\n"
      "v_cvt_pk_bf16_f32 v177, v166, v167\n"
      "v_add_f32 v171, v171, v187\n"
      "v_and_b32 v205, 0xffff0000, v205\n"
      "v_and_b32 v202, 0xffff0000, v202\n"
      "v_add_f32 v169, v169, v202\n"
      "v_and_b32 v210, 0xffff0000, v210\n"
      "v_add_f32 v173, v173, v196\n"
      "v_lshlrev_b32 v235, 16, v211\n"
      "v_add_f32 v170, v170, v235\n"
      "v_add_f32 v171, v171, v195\n"
      "v_add_f32 v174, v174, v233\n"
      "v_add_f32 v169, v169, v210\n"
      "v_add_f32 v175, v175, v205\n"
      "v_add_f32 v168, v168, v234\n"
      "v_cvt_pk_bf16_f32 v176, v164, v165\n"
      "v_and_b32 v211, 0xffff0000, v211\n"
      "v_add_f32 v171, v171, v203\n"
      "v_cvt_pk_bf16_f32 v178, v168, v169\n"
      "v_add_f32 v175, v175, v213\n"
      "v_add_f32 v171, v171, v211\n"
      "v_add_f32 v173, v173, v204\n"
      "v_cvt_pk_bf16_f32 v179, v170, v171\n"
      "v_add_f32 v173, v173, v212\n"
      "v_cvt_pk_bf16_f32 v180, v172, v173\n"
      "v_cvt_pk_bf16_f32 v181, v174, v175\n"
      "buffer_store_dwordx2 v[180:181], v9, s[16:19], s57 offen\n"
      "buffer_store_dwordx4 v[176:179], v8, s[16:19], s57 offen\n"
      "s_branch .Ld_end\n"
      ".Ld_out1:\n"
      "s_nop 8\n"
      "v_cvt_pk_bf16_f32 v229, v162, v163\n"
      "v_cvt_pk_bf16_f32 v228, v160, v161\n"
      "buffer_store_dwordx2 v[228:229], v13, s[8:11], s37 offen sc1\n"
      "s_waitcnt vmcnt(0)\n"
      "buffer_store_dword v18, v4, s[8:11], s52 offen sc1\n"
      "buffer_load_dword v21, v3, s[8:11], s53 offen sc1\n"
      "s_add_u32 s37, s37, 106496\n"
      "s_add_u32 s52, s52, 106496\n"
      "s_add_u32 s55, s53, 6656\n"
      "s_add_u32 s54, s53, 13312\n"
      "s_waitcnt vmcnt(0)\n"
      "v_cmp_ne_u32 vcc, s21, v21\n"
      "s_cbranch_vccnz .Lsp_df_18\n"
      ".Lbk_df_18:\n"
      "buffer_load_dwordx4 v[176:179], v0, s[8:11], s53 offen sc1\n"
      "buffer_load_dwordx2 v[180:181], v13, s[8:11], s53 offen sc1\n"
      "s_add_u32 s58, s41, 4294967264\n"
      "buffer_load_dwordx4 v[184:187], v0, s[8:11], s55 offen sc1\n"
      "buffer_load_dwordx4 v[192:195], v0, s[8:11], s54 offen sc1\n"
      "buffer_load_dwordx2 v[188:189], v13, s[8:11], s55 offen sc1\n"
      "s_add_u32 s55, s53, 19968\n"
      "buffer_load_dwordx2 v[196:197], v13, s[8:11], s54 offen sc1\n"
      "buffer_load_dwordx4 v[200:203], v0, s[8:11], s55 offen sc1\n"
      "buffer_load_dwordx2 v[204:205], v13, s[8:11], s55 offen sc1\n"
      "s_cmp_lt_u32 s41, 32\n"
      "s_cselect_b32 s56, s39, 0x40000000\n"
      "s_add_u32 s54, s53, 26624\n"
      "s_add_u32 s53, s53, 599040\n"
      "buffer_load_dwordx2 v[212:213], v13, s[8:11], s54 offen sc1\n"
      "s_add_u32 s39, s39, 147456\n"
      "buffer_load_dwordx4 v[208:211], v0, s[8:11], s54 offen sc1\n"
      "s_cmp_lt_u32 s58, 32\n"
      "s_cselect_b32 s57, s40, 0x40000000\n"
      "s_waitcnt vmcnt(9)\n"
      "v_lshlrev_b32 v234, 16, v178\n"
      "v_lshlrev_b32 v233, 16, v177\n"
      "v_lshlrev_b32 v232, 16, v176\n"
      "v_add_f32 v166, v166, v233\n"
      "v_add_f32 v164, v164, v232\n"
      "s_waitcnt vmcnt(8)\n"
      "v_lshlrev_b32 v233, 16, v181\n"
      "v_lshlrev_b32 v235, 16, v179\n"
      "v_and_b32 v176, 0xffff0000, v176\n"
      "v_lshlrev_b32 v232, 16, v180\n"
      "v_add_f32 v172, v172, v232\n"
      "v_and_b32 v181, 0xffff0000, v181\n"
      "v_add_f32 v168, v168, v234\n"
      "v_add_f32 v174, v174, v233\n"
      "v_add_f32 v165, v165, v176\n"
      "s_waitcnt vmcnt(7)\n"
      "v_lshlrev_b32 v232, 16, v184\n"
      "v_and_b32 v177, 0xffff0000, v177\n"
      "s_add_u32 s40, s40, 4294856704\n"
      "v_add_f32 v167, v167, v177\n"
      "v_and_b32 v180, 0xffff0000, v180\n"
      "v_add_f32 v164, v164, v232\n"
      "v_lshlrev_b32 v233, 16, v185\n"
      "v_add_f32 v166, v166, v233\n"
      "s_waitcnt vmcnt(5)\n"
      "v_lshlrev_b32 v232, 16, v188\n"
      "v_add_f32 v172, v172, v232\n"
      "v_and_b32 v185, 0xffff0000, v185\n"
      "v_lshlrev_b32 v232, 16, v192\n"
      "v_lshlrev_b32 v234, 16, v186\n"
      "v_add_f32 v164, v164, v232\n"
      "v_lshlrev_b32 v233, 16, v189\n"
      "s_waitcnt vmcnt(4)\n"
      "v_lshlrev_b32 v232, 16, v196\n"
      "v_add_f32 v174, v174, v233\n"
      "v_and_b32 v184, 0xffff0000, v184\n"
      "v_add_f32 v172, v172, v232\n"
      "v_add_f32 v170, v170, v235\n"
      "s_add_u32 s41, s41, 6\n"
      "v_add_f32 v165, v165, v184\n"
      "v_add_f32 v168, v168, v234\n"
      "v_lshlrev_b32 v233, 16, v193\n"
      "v_and_b32 v178, 0xffff0000, v178\n"
      "s_waitcnt vmcnt(3)\n"
      "v_lshlrev_b32 v232, 16, v200\n"
      "v_add_f32 v164, v164, v232\n"
      "v_and_b32 v186, 0xffff0000, v186\n"
      "v_add_f32 v169, v169, v178\n"
      "v_and_b32 v200, 0xffff0000, v200\n"
      "v_add_f32 v173, v173, v180\n"
      "v_add_f32 v166, v166, v233\n"
      "v_lshlrev_b32 v234, 16, v194\n"
      "v_and_b32 v192, 0xffff0000, v192\n"
      "v_add_f32 v175, v175, v181\n"
      "v_and_b32 v189, 0xffff0000, v189\n"
      "s_waitcnt vmcnt(2)\n"
      "v_lshlrev_b32 v232, 16, v204\n"
      "v_lshlrev_b32 v235, 16, v187\n"
      "v_add_f32 v172, v172, v232\n"
      "s_waitcnt vmcnt(0)\n"
      "v_lshlrev_b32 v232, 16, v208\n"
      "v_add_f32 v170, v170, v235\n"
      "v_and_b32 v194, 0xffff0000, v194\n"
      "v_and_b32 v208, 0xffff0000, v208\n"
      "v_add_f32 v167, v167, v185\n"
      "v_lshlrev_b32 v233, 16, v197\n"
      "v_lshlrev_b32 v235, 16, v195\n"
      "v_add_f32 v168, v168, v234\n"
      "v_add_f32 v164, v164, v232\n"
      "v_add_f32 v165, v165, v192\n"
      "v_lshlrev_b32 v234, 16, v202\n"
      "v_add_f32 v174, v174, v233\n"
      "v_lshlrev_b32 v232, 16, v212\n"
      "v_and_b32 v179, 0xffff0000, v179\n"
      "v_add_f32 v169, v169, v186\n"
      "v_lshlrev_b32 v233, 16, v201\n"
      "v_add_f32 v166, v166, v233\n"
      "v_and_b32 v193, 0xffff0000, v193\n"
      "v_and_b32 v197, 0xffff0000, v197\n"
      "v_lshlrev_b32 v233, 16, v205\n"
      "v_add_f32 v170, v170, v235\n"
      "v_add_f32 v174, v174, v233\n"
      "v_lshlrev_b32 v235, 16, v203\n"
      "v_and_b32 v187, 0xffff0000, v187\n"
      "v_lshlrev_b32 v233, 16, v209\n"
      "v_add_f32 v170, v170, v235\n"
      "v_add_f32 v166, v166, v233\n"
      "v_and_b32 v201, 0xffff0000, v201\n"
      "v_add_f32 v165, v165, v200\n"
      "v_and_b32 v188, 0xffff0000, v188\n"
      "v_add_f32 v165, v165, v208\n"
      "v_add_f32 v168, v168, v234\n"
      "v_add_f32 v167, v167, v193\n"
      "v_lshlrev_b32 v233, 16, v213\n"
      "v_add_f32 v171, v171, v179\n"
      "v_add_f32 v167, v167, v201\n"
      "v_lshlrev_b32 v234, 16, v210\n"
      "v_and_b32 v213, 0xffff0000, v213\n"
      "v_add_f32 v173, v173, v188\n"
      "v_and_b32 v209, 0xffff0000, v209\n"
      "v_add_f32 v175, v175, v189\n"
      "v_add_f32 v167, v167, v209\n"
      "v_and_b32 v203, 0xffff0000, v203\n"
      "v_add_f32 v175, v175, v197\n"
      "v_and_b32 v204, 0xffff0000, v204\n"
      "v_and_b32 v195, 0xffff0000, v195\n"
      "v_add_f32 v172, v172, v232\n"
      "v_add_f32 v169, v169, v194\n"
      "v_and_b32 v196, 0xffff0000, v196\n"
      "v_and_b32 v212, 0xffff0000, v212\n"
      "v_cvt_pk_bf16_f32 v177, v166, v167\n"
      "v_add_f32 v171, v171, v187\n"
      "v_and_b32 v205, 0xffff0000, v205\n"
      "v_and_b32 v202, 0xffff0000, v202\n"
      "v_add_f32 v169, v169, v202\n"
      "v_and_b32 v210, 0xffff0000, v210\n"
      "v_add_f32 v173, v173, v196\n"
      "v_lshlrev_b32 v235, 16, v211\n"
      "v_add_f32 v170, v170, v235\n"
      "v_add_f32 v171, v171, v195\n"
      "v_add_f32 v174, v174, v233\n"
      "v_add_f32 v169, v169, v210\n"
      "v_add_f32 v175, v175, v205\n"
      "v_add_f32 v168, v168, v234\n"
      "v_cvt_pk_bf16_f32 v176, v164, v165\n"
      "v_and_b32 v211, 0xffff0000, v211\n"
      "v_add_f32 v171, v171, v203\n"
      "v_cvt_pk_bf16_f32 v178, v168, v169\n"
      "v_add_f32 v175, v175, v213\n"
      "v_add_f32 v171, v171, v211\n"
      "v_add_f32 v173, v173, v204\n"
      "v_cvt_pk_bf16_f32 v179, v170, v171\n"
      "v_add_f32 v173, v173, v212\n"
      "v_cvt_pk_bf16_f32 v180, v172, v173\n"
      "v_cvt_pk_bf16_f32 v181, v174, v175\n"
      "buffer_store_dwordx2 v[180:181], v9, s[16:19], s57 offen\n"
      "buffer_store_dwordx4 v[176:179], v8, s[16:19], s57 offen\n"
      "s_branch .Ld_end\n"
      ".Ld_out2:\n"
      "s_nop 8\n"
      "v_cvt_pk_bf16_f32 v229, v162, v163\n"
      "v_cvt_pk_bf16_f32 v228, v160, v161\n"
      "buffer_store_dwordx2 v[228:229], v13, s[8:11], s37 offen sc1\n"
      "s_add_u32 s52, s52, 106496\n"
      "s_add_u32 s55, s53, 6656\n"
      "s_add_u32 s54, s53, 13312\n"
      "s_waitcnt vmcnt(2)\n"
      "v_cmp_ne_u32 vcc, s21, v21\n"
      "s_cbranch_vccnz .Lsp_df_19\n"
      ".Lbk_df_19:\n"
      "buffer_load_dwordx4 v[176:179], v0, s[8:11], s53 offen sc1\n"
      "buffer_load_dwordx2 v[180:181], v13, s[8:11], s53 offen sc1\n"
      "s_add_u32 s58, s41, 4294967264\n"
      "buffer_load_dwordx4 v[184:187], v0, s[8:11], s55 offen sc1\n"
      "s_add_u32 s37, s37, 106496\n"
      "s_waitcnt vmcnt(3)\n"
      "buffer_store_dword v18, v4, s[8:11], s52 offen sc1\n"
      "buffer_load_dwordx4 v[192:195], v0, s[8:11], s54 offen sc1\n"
      "buffer_load_dwordx2 v[188:189], v13, s[8:11], s55 offen sc1\n"
      "s_add_u32 s55, s53, 19968\n"
      "buffer_load_dwordx2 v[196:197], v13, s[8:11], s54 offen sc1\n"
      "buffer_load_dwordx4 v[200:203], v0, s[8:11], s55 offen sc1\n"
      "buffer_load_dwordx2 v[204:205], v13, s[8:11], s55 offen sc1\n"
      "s_cmp_lt_u32 s41, 32\n"
      "s_cselect_b32 s56, s39, 0x40000000\n"
      "s_add_u32 s54, s53, 26624\n"
      "s_add_u32 s53, s53, 599040\n"
      "buffer_load_dwordx2 v[212:213], v13, s[8:11], s54 offen sc1\n"
      "s_add_u32 s39, s39, 147456\n"
      "buffer_load_dwordx4 v[208:211], v0, s[8:11], s54 offen sc1\n"
      "s_cmp_lt_u32 s58, 32\n"
      "s_cselect_b32 s57, s40, 0x40000000\n"
      "s_waitcnt vmcnt(10)\n"
      "v_lshlrev_b32 v234, 16, v178\n"
      "v_lshlrev_b32 v233, 16, v177\n"
      "v_lshlrev_b32 v232, 16, v176\n"
      "v_add_f32 v166, v166, v233\n"
      "s_add_u32 s52, s52, 106496\n"
      "v_add_f32 v164, v164, v232\n"
      "s_waitcnt vmcnt(9)\n"
      "v_lshlrev_b32 v233, 16, v181\n"
      "v_lshlrev_b32 v235, 16, v179\n"
      "v_and_b32 v176, 0xffff0000, v176\n"
      "v_lshlrev_b32 v232, 16, v180\n"
      "v_add_f32 v172, v172, v232\n"
      "v_and_b32 v181, 0xffff0000, v181\n"
      "v_add_f32 v168, v168, v234\n"
      "v_add_f32 v174, v174, v233\n"
      "v_add_f32 v165, v165, v176\n"
      "s_waitcnt vmcnt(8)\n"
      "v_lshlrev_b32 v232, 16, v184\n"
      "v_and_b32 v177, 0xffff0000, v177\n"
      "s_add_u32 s40, s40, 4294856704\n"
      "v_add_f32 v167, v167, v177\n"
      "v_and_b32 v180, 0xffff0000, v180\n"
      "v_add_f32 v164, v164, v232\n"
      "v_lshlrev_b32 v233, 16, v185\n"
      "v_add_f32 v166, v166, v233\n"
      "s_waitcnt vmcnt(5)\n"
      "v_lshlrev_b32 v232, 16, v188\n"
      "v_add_f32 v172, v172, v232\n"
      "v_and_b32 v185, 0xffff0000, v185\n"
      "v_lshlrev_b32 v232, 16, v192\n"
      "v_lshlrev_b32 v234, 16, v186\n"
      "v_add_f32 v164, v164, v232\n"
      "v_lshlrev_b32 v233, 16, v189\n"
      "s_waitcnt vmcnt(4)\n"
      "v_lshlrev_b32 v232, 16, v196\n"
      "v_add_f32 v174, v174, v233\n"
      "v_and_b32 v184, 0xffff0000, v184\n"
      "v_add_f32 v172, v172, v232\n"
      "v_add_f32 v170, v170, v235\n"
      "s_add_u32 s41, s41, 6\n"
      "v_add_f32 v165, v165, v184\n"
      "v_add_f32 v168, v168, v234\n"
      "v_lshlrev_b32 v233, 16, v193\n"
      "v_and_b32 v178, 0xffff0000, v178\n"
      "s_waitcnt vmcnt(3)\n"
      "v_lshlrev_b32 v232, 16, v200\n"
      "v_add_f32 v164, v164, v232\n"
      "v_and_b32 v186, 0xffff0000, v186\n"
      "v_add_f32 v169, v169, v178\n"
      "v_and_b32 v200, 0xffff0000, v200\n"
      "v_add_f32 v173, v173, v180\n"
      "v_add_f32 v166, v166, v233\n"
      "v_lshlrev_b32 v234, 16, v194\n"
      "v_and_b32 v192, 0xffff0000, v192\n"
      "v_add_f32 v175, v175, v181\n"
      "v_and_b32 v189, 0xffff0000, v189\n"
      "s_waitcnt vmcnt(2)\n"
      "v_lshlrev_b32 v232, 16, v204\n"
      "v_lshlrev_b32 v235, 16, v187\n"
      "v_add_f32 v172, v172, v232\n"
      "s_waitcnt vmcnt(0)\n"
      "v_lshlrev_b32 v232, 16, v208\n"
      "v_add_f32 v170, v170, v235\n"
      "v_and_b32 v194, 0xffff0000, v194\n"
      "v_and_b32 v208, 0xffff0000, v208\n"
      "v_add_f32 v167, v167, v185\n"
      "v_lshlrev_b32 v233, 16, v197\n"
      "v_lshlrev_b32 v235, 16, v195\n"
      "v_add_f32 v168, v168, v234\n"
      "v_add_f32 v164, v164, v232\n"
      "v_add_f32 v165, v165, v192\n"
      "v_lshlrev_b32 v234, 16, v202\n"
      "v_add_f32 v174, v174, v233\n"
      "v_lshlrev_b32 v232, 16, v212\n"
      "v_and_b32 v179, 0xffff0000, v179\n"
      "v_add_f32 v169, v169, v186\n"
      "v_lshlrev_b32 v233, 16, v201\n"
      "v_add_f32 v166, v166, v233\n"
      "v_and_b32 v193, 0xffff0000, v193\n"
      "v_and_b32 v197, 0xffff0000, v197\n"
      "v_lshlrev_b32 v233, 16, v205\n"
      "v_add_f32 v170, v170, v235\n"
      "v_add_f32 v174, v174, v233\n"
      "v_lshlrev_b32 v235, 16, v203\n"
      "v_and_b32 v187, 0xffff0000, v187\n"
      "v_lshlrev_b32 v233, 16, v209\n"
      "v_add_f32 v170, v170, v235\n"
      "v_add_f32 v166, v166, v233\n"
      "v_and_b32 v201, 0xffff0000, v201\n"
      "v_add_f32 v165, v165, v200\n"
      "v_and_b32 v188, 0xffff0000, v188\n"
      "v_add_f32 v165, v165, v208\n"
      "v_add_f32 v168, v168, v234\n"
      "v_add_f32 v167, v167, v193\n"
      "v_lshlrev_b32 v233, 16, v213\n"
      "v_add_f32 v171, v171, v179\n"
      "v_add_f32 v167, v167, v201\n"
      "v_lshlrev_b32 v234, 16, v210\n"
      "v_and_b32 v213, 0xffff0000, v213\n"
      "v_add_f32 v173, v173, v188\n"
      "v_and_b32 v209, 0xffff0000, v209\n"
      "v_add_f32 v175, v175, v189\n"
      "v_add_f32 v167, v167, v209\n"
      "v_and_b32 v203, 0xffff0000, v203\n"
      "v_add_f32 v175, v175, v197\n"
      "v_and_b32 v204, 0xffff0000, v204\n"
      "v_and_b32 v195, 0xffff0000, v195\n"
      "v_add_f32 v172, v172, v232\n"
      "v_add_f32 v169, v169, v194\n"
      "v_and_b32 v196, 0xffff0000, v196\n"
      "v_and_b32 v212, 0xffff0000, v212\n"
      "v_cvt_pk_bf16_f32 v177, v166, v167\n"
      "v_add_f32 v171, v171, v187\n"
      "v_and_b32 v205, 0xffff0000, v205\n"
      "v_and_b32 v202, 0xffff0000, v202\n"
      "v_add_f32 v169, v169, v202\n"
      "v_and_b32 v210, 0xffff0000, v210\n"
      "v_add_f32 v173, v173, v196\n"
      "v_lshlrev_b32 v235, 16, v211\n"
      "v_add_f32 v170, v170, v235\n"
      "v_add_f32 v171, v171, v195\n"
      "v_add_f32 v174, v174, v233\n"
      "v_add_f32 v169, v169, v210\n"
      "v_add_f32 v175, v175, v205\n"
      "v_add_f32 v168, v168, v234\n"
      "v_cvt_pk_bf16_f32 v176, v164, v165\n"
      "v_and_b32 v211, 0xffff0000, v211\n"
      "v_add_f32 v171, v171, v203\n"
      "v_cvt_pk_bf16_f32 v178, v168, v169\n"
      "v_add_f32 v175, v175, v213\n"
      "v_add_f32 v171, v171, v211\n"
      "v_add_f32 v173, v173, v204\n"
      "v_cvt_pk_bf16_f32 v179, v170, v171\n"
      "v_add_f32 v173, v173, v212\n"
      "v_cvt_pk_bf16_f32 v180, v172, v173\n"
      "v_cvt_pk_bf16_f32 v181, v174, v175\n"
      "buffer_store_dwordx2 v[180:181], v9, s[16:19], s57 offen\n"
      "buffer_store_dwordx4 v[176:179], v8, s[16:19], s57 offen\n"
      "s_branch .Ld_end\n"
      ".Ld_out3:\n"
      "s_nop 8\n"
      "v_cvt_pk_bf16_f32 v228, v160, v161\n"
      "buffer_load_dwordx4 v[200:203], v0, s[8:11], s55 offen sc1\n"
      "v_cvt_pk_bf16_f32 v227, v158, v159\n"
      "buffer_store_dwordx4 v[224:227], v0, s[8:11], s37 offen sc1\n"
      "v_cvt_pk_bf16_f32 v229, v162, v163\n"
      "buffer_store_dwordx2 v[228:229], v13, s[8:11], s37 offen sc1\n"
      "s_add_u32 s37, s37, 106496\n"
      "buffer_load_dwordx2 v[204:205], v13, s[8:11], s55 offen sc1\n"
      "s_cmp_lt_u32 s41, 32\n"
      "s_cselect_b32 s56, s39, 0x40000000\n"
      "s_add_u32 s54, s53, 26624\n"
      "s_add_u32 s53, s53, 599040\n"
      "buffer_load_dwordx2 v[212:213], v13, s[8:11], s54 offen sc1\n"
      "s_add_u32 s39, s39, 147456\n"
      "buffer_load_dwordx4 v[208:211], v0, s[8:11], s54 offen sc1\n"
      "s_cmp_lt_u32 s58, 32\n"
      "s_cselect_b32 s57, s40, 0x40000000\n"
      "s_waitcnt vmcnt(12)\n"
      "v_lshlrev_b32 v234, 16, v178\n"
      "v_lshlrev_b32 v233, 16, v177\n"
      "v_lshlrev_b32 v232, 16, v176\n"
      "v_add_f32 v166, v166, v233\n"
      "s_add_u32 s52, s52, 106496\n"
      "v_add_f32 v164, v164, v232\n"
      "s_waitcnt vmcnt(11)\n"
      "v_lshlrev_b32 v233, 16, v181\n"
      "v_lshlrev_b32 v235, 16, v179\n"
      "v_and_b32 v176, 0xffff0000, v176\n"
      "v_lshlrev_b32 v232, 16, v180\n"
      "v_add_f32 v172, v172, v232\n"
      "v_and_b32 v181, 0xffff0000, v181\n"
      "v_add_f32 v168, v168, v234\n"
      "v_add_f32 v174, v174, v233\n"
      "v_add_f32 v165, v165, v176\n"
      "s_waitcnt vmcnt(10)\n"
      "v_lshlrev_b32 v232, 16, v184\n"
      "v_and_b32 v177, 0xffff0000, v177\n"
      "s_add_u32 s40, s40, 4294856704\n"
      "v_add_f32 v167, v167, v177\n"
      "v_and_b32 v180, 0xffff0000, v180\n"
      "v_add_f32 v164, v164, v232\n"
      "v_lshlrev_b32 v233, 16, v185\n"
      "v_add_f32 v166, v166, v233\n"
      "s_waitcnt vmcnt(7)\n"
      "v_lshlrev_b32 v232, 16, v188\n"
      "v_add_f32 v172, v172, v232\n"
      "v_and_b32 v185, 0xffff0000, v185\n"
      "v_lshlrev_b32 v232, 16, v192\n"
      "v_lshlrev_b32 v234, 16, v186\n"
      "v_add_f32 v164, v164, v232\n"
      "v_lshlrev_b32 v233, 16, v189\n"
      "s_waitcnt vmcnt(6)\n"
      "v_lshlrev_b32 v232, 16, v196\n"
      "v_add_f32 v174, v174, v233\n"
      "v_and_b32 v184, 0xffff0000, v184\n"
      "v_add_f32 v172, v172, v232\n"
      "v_add_f32 v170, v170, v235\n"
      "s_add_u32 s41, s41, 6\n"
      "v_add_f32 v165, v165, v184\n"
      "v_add_f32 v168, v168, v234\n"
      "v_lshlrev_b32 v233, 16, v193\n"
      "v_and_b32 v178, 0xffff0000, v178\n"
      "s_waitcnt vmcnt(5)\n"
      "v_lshlrev_b32 v232, 16, v200\n"
      "v_add_f32 v164, v164, v232\n"
      "v_and_b32 v186, 0xffff0000, v186\n"
      "s_waitcnt vmcnt(3)\n"
      "buffer_store_dword v18, v4, s[8:11], s52 offen sc1\n"
      "v_add_f32 v169, v169, v178\n"
      "v_and_b32 v200, 0xffff0000, v200\n"
      "v_add_f32 v173, v173, v180\n"
      "v_add_f32 v166, v166, v233\n"
      "v_lshlrev_b32 v234, 16, v194\n"
      "v_and_b32 v192, 0xffff0000, v192\n"
      "v_add_f32 v175, v175, v181\n"
      "v_and_b32 v189, 0xffff0000, v189\n"
      "s_waitcnt vmcnt(3)\n"
      "v_lshlrev_b32 v232, 16, v204\n"
      "v_lshlrev_b32 v235, 16, v187\n"
      "v_add_f32 v172, v172, v232\n"
      "s_waitcnt vmcnt(1)\n"
      "v_lshlrev_b32 v232, 16, v208\n"
      "v_add_f32 v170, v170, v235\n"
      "v_and_b32 v194, 0xffff0000, v194\n"
      "v_and_b32 v208, 0xffff0000, v208\n"
      "v_add_f32 v167, v167, v185\n"
      "v_lshlrev_b32 v233, 16, v197\n"
      "v_lshlrev_b32 v235, 16, v195\n"
      "v_add_f32 v168, v168, v234\n"
      "v_add_f32 v164, v164, v232\n"
      "v_add_f32 v165, v165, v192\n"
      "v_lshlrev_b32 v234, 16, v202\n"
      "v_add_f32 v174, v174, v233\n"
      "v_lshlrev_b32 v232, 16, v212\n"
      "v_and_b32 v179, 0xffff0000, v179\n"
      "v_add_f32 v169, v169, v186\n"
      "v_lshlrev_b32 v233, 16, v201\n"
      "v_add_f32 v166, v166, v233\n"
      "v_and_b32 v193, 0xffff0000, v193\n"
      "v_and_b32 v197, 0xffff0000, v197\n"
      "v_lshlrev_b32 v233, 16, v205\n"
      "v_add_f32 v170, v170, v235\n"
      "v_add_f32 v174, v174, v233\n"
      "v_lshlrev_b32 v235, 16, v203\n"
      "v_and_b32 v187, 0xffff0000, v187\n"
      "v_lshlrev_b32 v233, 16, v209\n"
      "v_add_f32 v170, v170, v235\n"
      "v_add_f32 v166, v166, v233\n"
      "v_and_b32 v201, 0xffff0000, v201\n"
      "v_add_f32 v165, v165, v200\n"
      "v_and_b32 v188, 0xffff0000, v188\n"
      "v_add_f32 v165, v165, v208\n"
      "v_add_f32 v168, v168, v234\n"
      "v_add_f32 v167, v167, v193\n"
      "v_lshlrev_b32 v233, 16, v213\n"
      "v_add_f32 v171, v171, v179\n"
      "v_add_f32 v167, v167, v201\n"
      "v_lshlrev_b32 v234, 16, v210\n"
      "v_and_b32 v213, 0xffff0000, v213\n"
      "v_add_f32 v173, v173, v188\n"
      "v_and_b32 v209, 0xffff0000, v209\n"
      "v_add_f32 v175, v175, v189\n"
      "v_add_f32 v167, v167, v209\n"
      "v_and_b32 v203, 0xffff0000, v203\n"
      "v_add_f32 v175, v175, v197\n"
      "v_and_b32 v204, 0xffff0000, v204\n"
      "v_and_b32 v195, 0xffff0000, v195\n"
      "v_add_f32 v172, v172, v232\n"
      "v_add_f32 v169, v169, v194\n"
      "v_and_b32 v196, 0xffff0000, v196\n"
      "v_and_b32 v212, 0xffff0000, v212\n"
      "v_cvt_pk_bf16_f32 v177, v166, v167\n"
      "v_add_f32 v171, v171, v187\n"
      "v_and_b32 v205, 0xffff0000, v205\n"
      "s_add_u32 s52, s52, 106496\n"
      "v_and_b32 v202, 0xffff0000, v202\n"
      "v_add_f32 v169, v169, v202\n"
      "v_and_b32 v210, 0xffff0000, v210\n"
      "v_add_f32 v173, v173, v196\n"
      "v_lshlrev_b32 v235, 16, v211\n"
      "v_add_f32 v170, v170, v235\n"
      "v_add_f32 v171, v171, v195\n"
      "v_add_f32 v174, v174, v233\n"
      "v_add_f32 v169, v169, v210\n"
      "v_add_f32 v175, v175, v205\n"
      "v_add_f32 v168, v168, v234\n"
      "v_cvt_pk_bf16_f32 v176, v164, v165\n"
      "v_and_b32 v211, 0xffff0000, v211\n"
      "v_add_f32 v171, v171, v203\n"
      "v_cvt_pk_bf16_f32 v178, v168, v169\n"
      "v_add_f32 v175, v175, v213\n"
      "v_add_f32 v171, v171, v211\n"
      "v_add_f32 v173, v173, v204\n"
      "v_cvt_pk_bf16_f32 v179, v170, v171\n"
      "v_add_f32 v173, v173, v212\n"
      "v_cvt_pk_bf16_f32 v180, v172, v173\n"
      "v_cvt_pk_bf16_f32 v181, v174, v175\n"
      "buffer_store_dwordx2 v[180:181], v9, s[16:19], s57 offen\n"
      "buffer_store_dwordx4 v[176:179], v8, s[16:19], s57 offen\n"
      "s_branch .Ld_end\n"
      ".Ld_out4:\n"
      "v_and_b32 v192, 0xffff0000, v192\n"
      "s_nop 7\n"
      "v_cvt_pk_bf16_f32 v229, v162, v163\n"
      "v_add_f32 v175, v175, v181\n"
      "v_cvt_pk_bf16_f32 v228, v160, v161\n"
      "v_and_b32 v189, 0xffff0000, v189\n"
      "s_waitcnt vmcnt(11)\n"
      "v_lshlrev_b32 v232, 16, v204\n"
      "buffer_store_dwordx2 v[228:229], v13, s[8:11], s37 offen sc1\n"
      "v_lshlrev_b32 v235, 16, v187\n"
      "v_add_f32 v172, v172, v232\n"
      "s_waitcnt vmcnt(10)\n"
      "v_lshlrev_b32 v232, 16, v208\n"
      "v_add_f32 v170, v170, v235\n"
      "v_and_b32 v194, 0xffff0000, v194\n"
      "v_and_b32 v208, 0xffff0000, v208\n"
      "v_add_f32 v167, v167, v185\n"
      "v_lshlrev_b32 v233, 16, v197\n"
      "v_lshlrev_b32 v235, 16, v195\n"
      "v_add_f32 v168, v168, v234\n"
      "v_add_f32 v164, v164, v232\n"
      "v_add_f32 v165, v165, v192\n"
      "v_lshlrev_b32 v234, 16, v202\n"
      "v_add_f32 v174, v174, v233\n"
      "v_lshlrev_b32 v232, 16, v212\n"
      "v_and_b32 v179, 0xffff0000, v179\n"
      "v_add_f32 v169, v169, v186\n"
      "v_lshlrev_b32 v233, 16, v201\n"
      "v_add_f32 v166, v166, v233\n"
      "v_and_b32 v193, 0xffff0000, v193\n"
      "v_and_b32 v197, 0xffff0000, v197\n"
      "v_lshlrev_b32 v233, 16, v205\n"
      "v_add_f32 v170, v170, v235\n"
      "v_add_f32 v174, v174, v233\n"
      "v_lshlrev_b32 v235, 16, v203\n"
      "v_and_b32 v187, 0xffff0000, v187\n"
      "v_lshlrev_b32 v233, 16, v209\n"
      "v_add_f32 v170, v170, v235\n"
      "v_add_f32 v166, v166, v233\n"
      "v_and_b32 v201, 0xffff0000, v201\n"
      "v_add_f32 v165, v165, v200\n"
      "v_and_b32 v188, 0xffff0000, v188\n"
      "v_add_f32 v165, v165, v208\n"
      "v_add_f32 v168, v168, v234\n"
      "v_add_f32 v167, v167, v193\n"
      "v_lshlrev_b32 v233, 16, v213\n"
      "v_add_f32 v171, v171, v179\n"
      "v_add_f32 v167, v167, v201\n"
      "v_lshlrev_b32 v234, 16, v210\n"
      "v_and_b32 v213, 0xffff0000, v213\n"
      "v_add_f32 v173, v173, v188\n"
      "v_and_b32 v209, 0xffff0000, v209\n"
      "v_add_f32 v175, v175, v189\n"
      "v_add_f32 v167, v167, v209\n"
      "v_and_b32 v203, 0xffff0000, v203\n"
      "v_add_f32 v175, v175, v197\n"
      "v_and_b32 v204, 0xffff0000, v204\n"
      "v_and_b32 v195, 0xffff0000, v195\n"
      "v_add_f32 v172, v172, v232\n"
      "v_add_f32 v169, v169, v194\n"
      "v_and_b32 v196, 0xffff0000, v196\n"
      "v_and_b32 v212, 0xffff0000, v212\n"
      "v_cvt_pk_bf16_f32 v177, v166, v167\n"
      "v_add_f32 v171, v171, v187\n"
      "v_and_b32 v205, 0xffff0000, v205\n"
      "s_add_u32 s52, s52, 106496\n"
      "v_and_b32 v202, 0xffff0000, v202\n"
      "v_add_f32 v169, v169, v202\n"
      "v_and_b32 v210, 0xffff0000, v210\n"
      "v_add_f32 v173, v173, v196\n"
      "v_lshlrev_b32 v235, 16, v211\n"
      "v_add_f32 v170, v170, v235\n"
      "v_add_f32 v171, v171, v195\n"
      "s_add_u32 s37, s37, 106496\n"
      "s_waitcnt vmcnt(0)\n"
      "buffer_store_dword v18, v4, s[8:11], s52 offen sc1\n"
      "v_add_f32 v174, v174, v233\n"
      "v_add_f32 v169, v169, v210\n"
      "v_add_f32 v175, v175, v205\n"
      "v_add_f32 v168, v168, v234\n"
      "v_cvt_pk_bf16_f32 v176, v164, v165\n"
      "v_and_b32 v211, 0xffff0000, v211\n"
      "v_add_f32 v171, v171, v203\n"
      "v_cvt_pk_bf16_f32 v178, v168, v169\n"
      "v_add_f32 v175, v175, v213\n"
      "v_add_f32 v171, v171, v211\n"
      "v_add_f32 v173, v173, v204\n"
      "v_cvt_pk_bf16_f32 v179, v170, v171\n"
      "v_add_f32 v173, v173, v212\n"
      "v_cvt_pk_bf16_f32 v180, v172, v173\n"
      "v_cvt_pk_bf16_f32 v181, v174, v175\n"
      "buffer_store_dwordx2 v[180:181], v9, s[16:19], s57 offen\n"
      "buffer_store_dwordx4 v[176:179], v8, s[16:19], s57 offen\n"
      "s_add_u32 s52, s52, 106496\n"
      "s_branch .Ld_end\n"
      ".Ld_in1:\n"
      "s_add_u32 s36, s36, 165888\n"
      "s_min_u32 s36, s36, s6\n"
      "s_mov_b32 s51, s36\n"
      "s_add_u32 s36, s36, 165888\n"
      "s_min_u32 s36, s36, s6\n"
      "buffer_load_dword v20, v2, s[8:11], s36 offen sc1\n"
      "s_mov_b32 s51, s36\n"
      "s_add_u32 m0, s43, 24576\n"
      "s_add_u32 s36, s36, 165888\n"
      "s_waitcnt vmcnt(0)\n"
      "v_cmp_ne_u32 vcc, s21, v20\n"
      "s_cbranch_vccnz .Lsp_d1_20\n"
      ".Lbk_d1_20:\n"
      "buffer_load_dwordx4 v0, s[8:11], s51 offen offset:0 sc1 lds\n"
      "buffer_load_dwordx4 v0, s[8:11], s51 offen offset:1024 sc1 lds\n"
      "buffer_load_dwordx4 v0, s[8:11], s51 offen offset:2048 sc1 lds\n"
      "s_min_u32 s36, s36, s6\n"
      "buffer_load_dword v20, v2, s[8:11], s36 offen sc1\n"
      "buffer_load_dwordx4 v0, s[8:11], s51 offen offset:3072 sc1 lds\n"
      "s_add_u32 m0, s43, 28672\n"
      "s_nop 0\n"
      "buffer_load_dwordx4 v1, s[8:11], s51 offen offset:0 sc1 lds\n"
      "buffer_load_dwordx4 v1, s[8:11], s51 offen offset:1024 sc1 lds\n"
      "s_add_u32 m0, s43, 49152\n"
      "s_mov_b32 s51, s36\n"
      "v_add_u32 v14, 1, v14\n"
      "s_waitcnt vmcnt(3)\n"
      "v_cmp_ne_u32 vcc, s21, v20\n"
      "s_add_u32 s36, s36, 165888\n"
      "s_min_u32 s36, s36, s6\n"
      "s_cbranch_vccnz .Lsp_d2_21\n"
      ".Lbk_d2_21:\n"
      "s_waitcnt vmcnt(0)\n"
      "ds_write_b32 v16, v14\n"
      "buffer_load_dword v20, v2, s[8:11], s36 offen sc1\n"
      "buffer_load_dwordx4 v0, s[8:11], s51 offen offset:3072 sc1 lds\n"
      "s_add_u32 s52, s52, 106496\n"
      "s_add_u32 s37, s37, 173056\n"
      "buffer_load_dwordx4 v0, s[8:11], s51 offen offset:1024 sc1 lds\n"
      "ds_read_b32 v15, v17\n"
      "buffer_load_dwordx4 v0, s[8:11], s51 offen offset:2048 sc1 lds\n"
      "s_waitcnt lgkmcnt(0)\n"
      "v_cmp_gt_i32 vcc, v14, v15\n"
      "buffer_load_dwordx4 v0, s[8:11], s51 offen offset:0 sc1 lds\n"
      "s_add_u32 m0, s43, 53248\n"
      "s_cbranch_vccnz .Lsp_dy1_22\n"
      ".Lbk_dy1_22:\n"
      "buffer_load_dwordx4 v1, s[8:11], s51 offen offset:0 sc1 lds\n"
      "ds_read_b128 v[56:59], v0 offset:24576\n"
      "ds_read_b128 v[64:67], v0 offset:26624\n"
      "buffer_load_dwordx4 v1, s[8:11], s51 offen offset:1024 sc1 lds\n"
      "ds_read_b128 v[68:71], v0 offset:27648\n"
      "ds_read_b128 v[80:83], v0 offset:30720\n"
      "ds_read_b128 v[60:63], v0 offset:25600\n"
      "ds_read_b128 v[92:95], v0 offset:33792\n"
      "ds_read_b128 v[84:87], v0 offset:31744\n"
      "ds_read_b128 v[72:75], v0 offset:28672\n"
      "ds_read_b128 v[76:79], v0 offset:29696\n"
      "ds_read_b128 v[100:103], v0 offset:35840\n"
      "ds_read_b128 v[96:99], v0 offset:34816\n"
      "ds_read_b128 v[116:119], v0 offset:39936\n"
      "ds_read_b128 v[120:123], v0 offset:40960\n"
      "ds_read_b128 v[108:111], v0 offset:37888\n"
      "ds_read_b128 v[88:91], v0 offset:32768\n"
      "s_waitcnt vmcnt(0) lgkmcnt(0)\n"
      "s_nop 4\n"
      "s_branch .Ld_t1\n"
      ".Ld_in2:\n"
      "s_add_u32 s36, s36, 165888\n"
      "s_min_u32 s36, s36, s6\n"
      "s_mov_b32 s51, s36\n"
      "s_add_u32 s36, s36, 165888\n"
      "s_min_u32 s36, s36, s6\n"
      "s_mov_b32 s51, s36\n"
      "s_add_u32 s36, s36, 165888\n"
      "s_min_u32 s36, s36, s6\n"
      "buffer_load_dword v20, v2, s[8:11], s36 offen sc1\n"
      "s_add_u32 m0, s43, 49152\n"
      "s_mov_b32 s51, s36\n"
      "s_waitcnt vmcnt(0)\n"
      "v_cmp_ne_u32 vcc, s21, v20\n"
      "s_add_u32 s36, s36, 165888\n"
      "s_min_u32 s36, s36, s6\n"
      "s_cbranch_vccnz .Lsp_d2_23\n"
      ".Lbk_d2_23:\n"
      "buffer_load_dword v20, v2, s[8:11], s36 offen sc1\n"
      "buffer_load_dwordx4 v0, s[8:11], s51 offen offset:3072 sc1 lds\n"
      "s_add_u32 s52, s52, 106496\n"
      "s_add_u32 s37, s37, 173056\n"
      "buffer_load_dwordx4 v0, s[8:11], s51 offen offset:1024 sc1 lds\n"
      "buffer_load_dwordx4 v0, s[8:11], s51 offen offset:2048 sc1 lds\n"
      "buffer_load_dwordx4 v0, s[8:11], s51 offen offset:0 sc1 lds\n"
      "s_add_u32 m0, s43, 53248\n"
      "s_nop 0\n"
      "buffer_load_dwordx4 v1, s[8:11], s51 offen offset:0 sc1 lds\n"
      "buffer_load_dwordx4 v1, s[8:11], s51 offen offset:1024 sc1 lds\n"
      "s_mov_b32 s51, s36\n"
      "s_add_u32 s36, s36, 165888\n"
      "s_add_u32 m0, s43, 73728\n"
      "s_waitcnt vmcnt(6)\n"
      "v_cmp_ne_u32 vcc, s21, v20\n"
      "s_add_u32 s38, s38, 147456\n"
      "s_min_u32 s36, s36, s6\n"
      "s_cbranch_vccnz .Lsp_d3_24\n"
      ".Lbk_d3_24:\n"
      "buffer_load_dwordx4 v0, s[8:11], s51 offen offset:2048 sc1 lds\n"
      "v_add_u32 v14, 1, v14\n"
      "buffer_load_dwordx4 v0, s[8:11], s51 offen offset:1024 sc1 lds\n"
      "buffer_load_dword v20, v2, s[8:11], s36 offen sc1\n"
      "s_waitcnt vmcnt(3)\n"
      "ds_write_b32 v16, v14\n"
      "buffer_load_dwordx4 v0, s[8:11], s51 offen offset:0 sc1 lds\n"
      "buffer_load_dwordx4 v0, s[8:11], s51 offen offset:3072 sc1 lds\n"
      "s_add_u32 m0, s43, 77824\n"
      "ds_read_b32 v15, v17\n"
      "s_add_u32 s52, s52, 173056\n"
      "buffer_load_dwordx4 v1, s[8:11], s51 offen offset:0 sc1 lds\n"
      "s_waitcnt lgkmcnt(0)\n"
      "v_cmp_gt_i32 vcc, v14, v15\n"
      "buffer_load_dwordx4 v1, s[8:11], s51 offen offset:1024 sc1 lds\n"
      "s_cbranch_vccnz .Lsp_dy2_25\n"
      ".Lbk_dy2_25:\n"
      "ds_read_b128 v[68:71], v22 offset:3072\n"
      "ds_read_b128 v[60:63], v22 offset:1024\n"
      "ds_read_b128 v[80:83], v22 offset:6144\n"
      "ds_read_b128 v[72:75], v22 offset:4096\n"
      "ds_read_b128 v[76:79], v22 offset:5120\n"
      "ds_read_b128 v[96:99], v22 offset:10240\n"
      "ds_read_b128 v[84:87], v22 offset:7168\n"
      "ds_read_b128 v[56:59], v22 offset:0\n"
      "ds_read_b128 v[92:95], v22 offset:9216\n"
      "s_mov_b32 s51, s36\n"
      "ds_read_b128 v[64:67], v22 offset:2048\n"
      "ds_read_b128 v[100:103], v22 offset:11264\n"
      "ds_read_b128 v[132:135], v22 offset:19456\n"
      "ds_read_b128 v[112:115], v22 offset:14336\n"
      "s_add_u32 m0, s43, 98304\n"
      "s_waitcnt vmcnt(0) lgkmcnt(0)\n"
      "s_nop 4\n"
      "s_branch .Ld_t2\n"
      ".Ld_in3:\n"
      "s_add_u32 s36, s36, 165888\n"
      "s_min_u32 s36, s36, s6\n"
      "s_mov_b32 s51, s36\n"
      "s_add_u32 s36, s36, 165888\n"
      "s_min_u32 s36, s36, s6\n"
      "s_mov_b32 s51, s36\n"
      "s_add_u32 s36, s36, 165888\n"
      "s_min_u32 s36, s36, s6\n"
      "s_mov_b32 s51, s36\n"
      "s_add_u32 s36, s36, 165888\n"
      "s_min_u32 s36, s36, s6\n"
      "buffer_load_dword v20, v2, s[8:11], s36 offen sc1\n"
      "s_add_u32 s52, s52, 106496\n"
      "s_add_u32 s37, s37, 173056\n"
      "s_mov_b32 s51, s36\n"
      "s_add_u32 s36, s36, 165888\n"
      "s_add_u32 m0, s43, 73728\n"
      "s_waitcnt vmcnt(0)\n"
      "v_cmp_ne_u32 vcc, s21, v20\n"
      "s_add_u32 s38, s38, 147456\n"
      "s_min_u32 s36, s36, s6\n"
      "s_cbranch_vccnz .Lsp_d3_26\n"
      ".Lbk_d3_26:\n"
      "buffer_load_dwordx4 v0, s[8:11], s51 offen offset:2048 sc1 lds\n"
      "buffer_load_dwordx4 v0, s[8:11], s51 offen offset:1024 sc1 lds\n"
      "buffer_load_dword v20, v2, s[8:11], s36 offen sc1\n"
      "buffer_load_dwordx4 v0, s[8:11], s51 offen offset:0 sc1 lds\n"
      "buffer_load_dwordx4 v0, s[8:11], s51 offen offset:3072 sc1 lds\n"
      "s_add_u32 m0, s43, 77824\n"
      "s_add_u32 s52, s52, 173056\n"
      "buffer_load_dwordx4 v1, s[8:11], s51 offen offset:0 sc1 lds\n"
      "buffer_load_dwordx4 v1, s[8:11], s51 offen offset:1024 sc1 lds\n"
      "s_mov_b32 s51, s36\n"
      "s_add_u32 m0, s43, 98304\n"
      "s_add_u32 s36, s36, 165888\n"
      "v_add_u32 v14, 1, v14\n"
      "s_waitcnt vmcnt(4)\n"
      "v_cmp_ne_u32 vcc, s21, v20\n"
      "s_cbranch_vccnz .Lsp_d4_27\n"
      ".Lbk_d4_27:\n"
      "s_min_u32 s36, s36, s6\n"
      "buffer_load_dwordx4 v0, s[8:11], s51 offen offset:0 sc1 lds\n"
      "buffer_load_dwordx4 v0, s[8:11], s51 offen offset:2048 sc1 lds\n"
      "s_waitcnt vmcnt(2)\n"
      "ds_write_b32 v16, v14\n"
      "buffer_load_dword v20, v2, s[8:11], s36 offen sc1\n"
      "buffer_load_dwordx4 v0, s[8:11], s51 offen offset:3072 sc1 lds\n"
      "ds_read_b32 v15, v17\n"
      "buffer_load_dwordx4 v0, s[8:11], s51 offen offset:1024 sc1 lds\n"
      "s_add_u32 m0, s43, 102400\n"
      "s_nop 0\n"
      "buffer_load_dwordx4 v1, s[8:11], s51 offen offset:1024 sc1 lds\n"
      "s_waitcnt lgkmcnt(0)\n"
      "v_cmp_gt_i32 vcc, v14, v15\n"
      "buffer_load_dwordx4 v1, s[8:11], s51 offen offset:0 sc1 lds\n"
      "s_cbranch_vccnz .Lsp_dy3_28\n"
      ".Lbk_dy3_28:\n"
      "ds_read_b128 v[60:63], v22 offset:25600\n"
      "ds_read_b128 v[64:67], v22 offset:26624\n"
      "s_mov_b32 s51, s36\n"
      "ds_read_b128 v[72:75], v22 offset:28672\n"
      "ds_read_b128 v[68:71], v22 offset:27648\n"
      "ds_read_b128 v[92:95], v22 offset:33792\n"
      "ds_read_b128 v[56:59], v22 offset:24576\n"
      "ds_read_b128 v[100:103], v22 offset:35840\n"
      "ds_read_b128 v[80:83], v22 offset:30720\n"
      "ds_read_b128 v[112:115], v22 offset:38912\n"
      "s_add_u32 s37, s37, 106496\n"
      "ds_read_b128 v[88:91], v22 offset:32768\n"
      "ds_read_b128 v[96:99], v22 offset:34816\n"
      "ds_read_b128 v[76:79], v22 offset:29696\n"
      "ds_read_b128 v[116:119], v22 offset:39936\n"
      "ds_read_b128 v[84:87], v22 offset:31744\n"
      "ds_read_b128 v[104:107], v22 offset:36864\n"
      "s_waitcnt vmcnt(0) lgkmcnt(0)\n"
      "s_nop 4\n"
      "s_branch .Ld_t3\n"
      ".Ld_in4:\n"
      "s_add_u32 s36, s36, 165888\n"
      "s_min_u32 s36, s36, s6\n"
      "s_mov_b32 s51, s36\n"
      "s_add_u32 s36, s36, 165888\n"
      "s_min_u32 s36, s36, s6\n"
      "s_mov_b32 s51, s36\n"
      "s_add_u32 s36, s36, 165888\n"
      "s_min_u32 s36, s36, s6\n"
      "s_mov_b32 s51, s36\n"
      "s_add_u32 s36, s36, 165888\n"
      "s_min_u32 s36, s36, s6\n"
      "s_add_u32 s52, s52, 106496\n"
      "s_add_u32 s37, s37, 173056\n"
      "s_mov_b32 s51, s36\n"
      "s_add_u32 s36, s36, 165888\n"
      "s_add_u32 s38, s38, 147456\n"
      "s_min_u32 s36, s36, s6\n"
      "buffer_load_dword v20, v2, s[8:11], s36 offen sc1\n"
      "s_add_u32 s52, s52, 173056\n"
      "s_mov_b32 s51, s36\n"
      "s_add_u32 m0, s43, 98304\n"
      "s_add_u32 s36, s36, 165888\n"
      "s_waitcnt vmcnt(0)\n"
      "v_cmp_ne_u32 vcc, s21, v20\n"
      "s_cbranch_vccnz .Lsp_d4_29\n"
      ".Lbk_d4_29:\n"
      "s_min_u32 s36, s36, s6\n"
      "buffer_load_dwordx4 v0, s[8:11], s51 offen offset:0 sc1 lds\n"
      "buffer_load_dwordx4 v0, s[8:11], s51 offen offset:2048 sc1 lds\n"
      "buffer_load_dword v20, v2, s[8:11], s36 offen sc1\n"
      "buffer_load_dwordx4 v0, s[8:11], s51 offen offset:3072 sc1 lds\n"
      "buffer_load_dwordx4 v0, s[8:11], s51 offen offset:1024 sc1 lds\n"
      "s_add_u32 m0, s43, 102400\n"
      "s_nop 0\n"
      "buffer_load_dwordx4 v1, s[8:11], s51 offen offset:1024 sc1 lds\n"
      "buffer_load_dwordx4 v1, s[8:11], s51 offen offset:0 sc1 lds\n"
      "s_mov_b32 s51, s36\n"
      "s_add_u32 s37, s37, 106496\n"
      "s_add_u32 m0, s43, 122880\n"
      "s_waitcnt vmcnt(4)\n"
      "v_cmp_ne_u32 vcc, s21, v20\n"
      "s_add_u32 s36, s36, 165888\n"
      "s_add_u32 s52, s52, 106496\n"
      "s_cbranch_vccnz .Lsp_d5_30\n"
      ".Lbk_d5_30:\n"
      "buffer_load_dwordx4 v0, s[8:11], s51 offen offset:1024 sc1 lds\n"
      "v_add_u32 v14, 1, v14\n"
      "buffer_load_dwordx4 v0, s[8:11], s51 offen offset:0 sc1 lds\n"
      "buffer_load_dwordx4 v0, s[8:11], s51 offen offset:2048 sc1 lds\n"
      "s_waitcnt vmcnt(3)\n"
      "ds_write_b32 v16, v14\n"
      "buffer_load_dwordx4 v0, s[8:11], s51 offen offset:3072 sc1 lds\n"
      "s_min_u32 s36, s36, s6\n"
      "s_add_u32 m0, s43, 126976\n"
      "s_nop 0\n"
      "buffer_load_dwordx4 v1, s[8:11], s51 offen offset:1024 sc1 lds\n"
      "ds_read_b32 v15, v17\n"
      "buffer_load_dword v20, v2, s[8:11], s36 offen sc1\n"
      "s_waitcnt lgkmcnt(0)\n"
      "v_cmp_gt_i32 vcc, v14, v15\n"
      "buffer_load_dwordx4 v1, s[8:11], s51 offen offset:0 sc1 lds\n"
      "s_cbranch_vccnz .Lsp_dy4_31\n"
      ".Lbk_dy4_31:\n"
      "ds_read_b128 v[60:63], v23 offset:1024\n"
      "ds_read_b128 v[64:67], v23 offset:2048\n"
      "ds_read_b128 v[72:75], v23 offset:4096\n"
      "ds_read_b128 v[68:71], v23 offset:3072\n"
      "s_add_u32 s58, s41, 4294967264\n"
      "ds_read_b128 v[80:83], v23 offset:6144\n"
      "ds_read_b128 v[84:87], v23 offset:7168\n"
      "ds_read_b128 v[76:79], v23 offset:5120\n"
      "s_add_u32 s37, s37, 106496\n"
      "ds_read_b128 v[56:59], v23 offset:0\n"
      "ds_read_b128 v[88:91], v23 offset:8192\n"
      "ds_read_b128 v[96:99], v23 offset:10240\n"
      "ds_read_b128 v[100:103], v23 offset:11264\n"
      "ds_read_b128 v[132:135], v23 offset:19456\n"
      "ds_read_b128 v[104:107], v23 offset:12288\n"
      "s_waitcnt vmcnt(0) lgkmcnt(0)\n"
      "s_nop 4\n"
      "s_branch .Ld_t4\n"
      ".Ld_in5:\n"
      "s_add_u32 s36, s36, 165888\n"
      "s_min_u32 s36, s36, s6\n"
      "s_mov_b32 s51, s36\n"
      "s_add_u32 s36, s36, 165888\n"
      "s_min_u32 s36, s36, s6\n"
      "s_mov_b32 s51, s36\n"
      "s_add_u32 s36, s36, 165888\n"
      "s_min_u32 s36, s36, s6\n"
      "s_mov_b32 s51, s36\n"
      "s_add_u32 s36, s36, 165888\n"
      "s_min_u32 s36, s36, s6\n"
      "s_add_u32 s52, s52, 106496\n"
      "s_add_u32 s37, s37, 173056\n"
      "s_mov_b32 s51, s36\n"
      "s_add_u32 s36, s36, 165888\n"
      "s_add_u32 s38, s38, 147456\n"
      "s_min_u32 s36, s36, s6\n"
      "s_add_u32 s52, s52, 173056\n"
      "s_mov_b32 s51, s36\n"
      "s_add_u32 s36, s36, 165888\n"
      "s_min_u32 s36, s36, s6\n"
      "buffer_load_dword v20, v2, s[8:11], s36 offen sc1\n"
      "s_mov_b32 s51, s36\n"
      "s_add_u32 s37, s37, 106496\n"
      "s_add_u32 m0, s43, 122880\n"
      "s_waitcnt vmcnt(0)\n"
      "v_cmp_ne_u32 vcc, s21, v20\n"
      "s_add_u32 s36, s36, 165888\n"
      "s_add_u32 s52, s52, 106496\n"
      "s_cbranch_vccnz .Lsp_d5_32\n"
      ".Lbk_d5_32:\n"
      "buffer_load_dwordx4 v0, s[8:11], s51 offen offset:1024 sc1 lds\n"
      "buffer_load_dwordx4 v0, s[8:11], s51 offen offset:0 sc1 lds\n"
      "buffer_load_dwordx4 v0, s[8:11], s51 offen offset:2048 sc1 lds\n"
      "buffer_load_dwordx4 v0, s[8:11], s51 offen offset:3072 sc1 lds\n"
      "s_min_u32 s36, s36, s6\n"
      "s_add_u32 m0, s43, 126976\n"
      "s_nop 0\n"
      "buffer_load_dwordx4 v1, s[8:11], s51 offen offset:1024 sc1 lds\n"
      "buffer_load_dword v20, v2, s[8:11], s36 offen sc1\n"
      "buffer_load_dwordx4 v1, s[8:11], s51 offen offset:0 sc1 lds\n"
      "s_add_u32 s58, s41, 4294967264\n"
      "s_add_u32 s37, s37, 106496\n"
      "s_add_u32 m0, s43, 0\n"
      "s_mov_b32 s51, s36\n"
      "v_add_u32 v14, 1, v14\n"
      "s_add_u32 s36, s36, 165888\n"
      "s_add_u32 s37, s37, 106496\n"
      "s_min_u32 s36, s36, s6\n"
      "s_cmp_lt_u32 s41, 32\n"
      "s_cselect_b32 s56, s39, 0x40000000\n"
      "s_add_u32 s53, s53, 599040\n"
      "s_waitcnt vmcnt(1)\n"
      "v_cmp_ne_u32 vcc, s21, v20\n"
      "s_add_u32 s39, s39, 147456\n"
      "s_waitcnt vmcnt(0)\n"
      "ds_write_b32 v16, v14\n"
      "s_cmp_lt_u32 s58, 32\n"
      "s_cselect_b32 s57, s40, 0x40000000\n"
      "s_cbranch_vccnz .Lsp_d0_33\n"
      ".Lbk_d0_33:\n"
      "buffer_load_dwordx4 v0, s[8:11], s51 offen offset:0 sc1 lds\n"
      "s_add_u32 s52, s52, 106496\n"
      "buffer_load_dwordx4 v0, s[8:11], s51 offen offset:1024 sc1 lds\n"
      "buffer_load_dword v20, v2, s[8:11], s36 offen sc1\n"
      "ds_read_b32 v15, v17\n"
      "buffer_load_dwordx4 v0, s[8:11], s51 offen offset:2048 sc1 lds\n"
      "s_add_u32 s40, s40, 4294856704\n"
      "buffer_load_dwordx4 v0, s[8:11], s51 offen offset:3072 sc1 lds\n"
      "s_waitcnt lgkmcnt(0)\n"
      "v_cmp_gt_i32 vcc, v14, v15\n"
      "s_add_u32 m0, s43, 4096\n"
      "s_nop 0\n"
      "buffer_load_dwordx4 v1, s[8:11], s51 offen offset:1024 sc1 lds\n"
      "s_cbranch_vccnz .Lsp_dy5_34\n"
      ".Lbk_dy5_34:\n"
      "ds_read_b128 v[56:59], v23 offset:24576\n"
      "ds_read_b128 v[64:67], v23 offset:26624\n"
      "buffer_load_dwordx4 v1, s[8:11], s51 offen offset:0 sc1 lds\n"
      "ds_read_b128 v[76:79], v23 offset:29696\n"
      "s_add_u32 s41, s41, 6\n"
      "ds_read_b128 v[88:91], v23 offset:32768\n"
      "ds_read_b128 v[68:71], v23 offset:27648\n"
      "ds_read_b128 v[96:99], v23 offset:34816\n"
      "ds_read_b128 v[80:83], v23 offset:30720\n"
      "ds_read_b128 v[72:75], v23 offset:28672\n"
      "s_mov_b32 s51, s36\n"
      "ds_read_b128 v[112:115], v23 offset:38912\n"
      "ds_read_b128 v[60:63], v23 offset:25600\n"
      "ds_read_b128 v[104:107], v23 offset:36864\n"
      "ds_read_b128 v[84:87], v23 offset:31744\n"
      "ds_read_b128 v[140:143], v23 offset:46080\n"
      "ds_read_b128 v[100:103], v23 offset:35840\n"
      "s_waitcnt vmcnt(0) lgkmcnt(0)\n"
      "s_nop 4\n"
      "s_branch .Ld_t5\n"
      ".Ld_end:\n"
      "s_endpgm\n"
      ".Lsp_d0_1:\n"
      "s_mov_b32 s22, s23\n"
      ".Lsl_d0_1:\n"
      "buffer_load_dword v20, v2, s[8:11], s51 offen sc1\n"
      "s_waitcnt vmcnt(0)\n"
      "v_cmp_ne_u32 vcc, s21, v20\n"
      "s_cbranch_vccz .Lbk_d0_1\n"
      "s_sleep 4\n"
      "s_sub_u32 s22, s22, 1\n"
      "s_cbranch_scc0 .Lsl_d0_1\n"
      "s_mov_b32 s23, 0\n"
      "s_branch .Lbk_d0_1\n"
      ".Lsp_d1_2:\n"
      "s_mov_b32 s22, s23\n"
      ".Lsl_d1_2:\n"
      "buffer_load_dword v20, v2, s[8:11], s51 offen sc1\n"
      "s_waitcnt vmcnt(0)\n"
      "v_cmp_ne_u32 vcc, s21, v20\n"
      "s_cbranch_vccz .Lbk_d1_2\n"
      "s_sleep 4\n"
      "s_sub_u32 s22, s22, 1\n"
      "s_cbranch_scc0 .Lsl_d1_2\n"
      "s_mov_b32 s23, 0\n"
      "s_branch .Lbk_d1_2\n"
      ".Lsp_dy0_3:\n"
      "s_lshl_b32 s22, s23, 4\n"
      ".Lsl_dy0_3:\n"
      "ds_read_b32 v15, v17\n"
      "s_waitcnt lgkmcnt(0)\n"
      "v_cmp_gt_i32 vcc, v14, v15\n"
      "s_cbranch_vccz .Lbk_dy0_3\n"
      "s_sub_u32 s22, s22, 1\n"
      "s_cbranch_scc0 .Lsl_dy0_3\n"
      "s_mov_b32 s23, 0\n"
      "s_branch .Lbk_dy0_3\n"
      ".Lsp_d2_4:\n"
      "s_mov_b32 s22, s23\n"
      "buffer_load_dword v20, v2, s[8:11], s51 offen sc1\n"
      "s_waitcnt vmcnt(0)\n"
      "v_cmp_ne_u32 vcc, s21, v20\n"
      "s_add_u32 s30, s52, 106496\n"
      "buffer_store_dword v18, v4, s[8:11], s30 offen sc1\n"
      "s_cbranch_vccz .Lbk_d2_4\n"
      ".Lsl_d2_4:\n"
      "buffer_load_dword v20, v2, s[8:11], s51 offen sc1\n"
      "s_waitcnt vmcnt(0)\n"
      "v_cmp_ne_u32 vcc, s21, v20\n"
      "s_cbranch_vccz .Lbk_d2_4\n"
      "s_sleep 4\n"
      "s_sub_u32 s22, s22, 1\n"
      "s_cbranch_scc0 .Lsl_d2_4\n"
      "s_mov_b32 s23, 0\n"
      "s_branch .Lbk_d2_4\n"
      ".Lsp_dy1_5:\n"
      "s_lshl_b32 s22, s23, 4\n"
      ".Lsl_dy1_5:\n"
      "ds_read_b32 v15, v17\n"
      "s_waitcnt lgkmcnt(0)\n"
      "v_cmp_gt_i32 vcc, v14, v15\n"
      "s_cbranch_vccz .Lbk_dy1_5\n"
      "s_sub_u32 s22, s22, 1\n"
      "s_cbranch_scc0 .Lsl_dy1_5\n"
      "s_mov_b32 s23, 0\n"
      "s_branch .Lbk_dy1_5\n"
      ".Lsp_d3_6:\n"
      "s_mov_b32 s22, s23\n"
      "buffer_load_dword v20, v2, s[8:11], s51 offen sc1\n"
      "s_waitcnt vmcnt(0)\n"
      "v_cmp_ne_u32 vcc, s21, v20\n"
      "buffer_store_dword v18, v5, s[8:11], s56 offen sc1\n"
      "s_cbranch_vccz .Lbk_d3_6\n"
      ".Lsl_d3_6:\n"
      "buffer_load_dword v20, v2, s[8:11], s51 offen sc1\n"
      "s_waitcnt vmcnt(0)\n"
      "v_cmp_ne_u32 vcc, s21, v20\n"
      "s_cbranch_vccz .Lbk_d3_6\n"
      "s_sleep 4\n"
      "s_sub_u32 s22, s22, 1\n"
      "s_cbranch_scc0 .Lsl_d3_6\n"
      "s_mov_b32 s23, 0\n"
      "s_branch .Lbk_d3_6\n"
      ".Lsp_dy2_7:\n"
      "s_lshl_b32 s22, s23, 4\n"
      ".Lsl_dy2_7:\n"
      "ds_read_b32 v15, v17\n"
      "s_waitcnt lgkmcnt(0)\n"
      "v_cmp_gt_i32 vcc, v14, v15\n"
      "s_cbranch_vccz .Lbk_dy2_7\n"
      "s_sub_u32 s22, s22, 1\n"
      "s_cbranch_scc0 .Lsl_dy2_7\n"
      "s_mov_b32 s23, 0\n"
      "s_branch .Lbk_dy2_7\n"
      ".Lsp_d4_8:\n"
      "s_mov_b32 s22, s23\n"
      "buffer_load_dword v20, v2, s[8:11], s51 offen sc1\n"
      "s_waitcnt vmcnt(0)\n"
      "v_cmp_ne_u32 vcc, s21, v20\n"
      "s_add_u32 s30, s52, 0\n"
      "buffer_store_dword v18, v4, s[8:11], s30 offen sc1\n"
      "s_cbranch_vccz .Lbk_d4_8\n"
      ".Lsl_d4_8:\n"
      "buffer_load_dword v20, v2, s[8:11], s51 offen sc1\n"
      "s_waitcnt vmcnt(0)\n"
      "v_cmp_ne_u32 vcc, s21, v20\n"
      "s_cbranch_vccz .Lbk_d4_8\n"
      "s_sleep 4\n"
      "s_sub_u32 s22, s22, 1\n"
      "s_cbranch_scc0 .Lsl_d4_8\n"
      "s_mov_b32 s23, 0\n"
      "s_branch .Lbk_d4_8\n"
      ".Lsp_dy3_9:\n"
      "s_lshl_b32 s22, s23, 4\n"
      ".Lsl_dy3_9:\n"
      "ds_read_b32 v15, v17\n"
      "s_waitcnt lgkmcnt(0)\n"
      "v_cmp_gt_i32 vcc, v14, v15\n"
      "s_cbranch_vccz .Lbk_dy3_9\n"
      "s_sub_u32 s22, s22, 1\n"
      "s_cbranch_scc0 .Lsl_dy3_9\n"
      "s_mov_b32 s23, 0\n"
      "s_branch .Lbk_dy3_9\n"
      ".Lsp_d5_10:\n"
      "s_mov_b32 s22, s23\n"
      "buffer_load_dword v20, v2, s[8:11], s51 offen sc1\n"
      "s_waitcnt vmcnt(0)\n"
      "v_cmp_ne_u32 vcc, s21, v20\n"
      "s_add_u32 s30, s52, 0\n"
      "buffer_store_dword v18, v4, s[8:11], s30 offen sc1\n"
      "s_cbranch_vccz .Lbk_d5_10\n"
      ".Lsl_d5_10:\n"
      "buffer_load_dword v20, v2, s[8:11], s51 offen sc1\n"
      "s_waitcnt vmcnt(0)\n"
      "v_cmp_ne_u32 vcc, s21, v20\n"
      "s_cbranch_vccz .Lbk_d5_10\n"
      "s_sleep 4\n"
      "s_sub_u32 s22, s22, 1\n"
      "s_cbranch_scc0 .Lsl_d5_10\n"
      "s_mov_b32 s23, 0\n"
      "s_branch .Lbk_d5_10\n"
      ".Lsp_dy4_11:\n"
      "s_lshl_b32 s22, s23, 4\n"
      ".Lsl_dy4_11:\n"
      "ds_read_b32 v15, v17\n"
      "s_waitcnt lgkmcnt(0)\n"
      "v_cmp_gt_i32 vcc, v14, v15\n"
      "s_cbranch_vccz .Lbk_dy4_11\n"
      "s_sub_u32 s22, s22, 1\n"
      "s_cbranch_scc0 .Lsl_dy4_11\n"
      "s_mov_b32 s23, 0\n"
      "s_branch .Lbk_dy4_11\n"
      ".Lsp_df_12:\n"
      "s_mov_b32 s22, s23\n"
      "buffer_load_dword v21, v3, s[8:11], s53 offen sc1\n"
      "s_waitcnt vmcnt(0)\n"
      "v_cmp_ne_u32 vcc, s21, v21\n"
      "s_add_u32 s30, s52, 0\n"
      "buffer_store_dword v18, v4, s[8:11], s30 offen sc1\n"
      "s_cbranch_vccz .Lbk_df_12\n"
      ".Lsl_df_12:\n"
      "buffer_load_dword v21, v3, s[8:11], s53 offen sc1\n"
      "s_waitcnt vmcnt(0)\n"
      "v_cmp_ne_u32 vcc, s21, v21\n"
      "s_cbranch_vccz .Lbk_df_12\n"
      "s_sleep 4\n"
      "s_sub_u32 s22, s22, 1\n"
      "s_cbranch_scc0 .Lsl_df_12\n"
      "s_mov_b32 s23, 0\n"
      "s_branch .Lbk_df_12\n"
      ".Lsp_d0_13:\n"
      "s_mov_b32 s22, s23\n"
      "buffer_load_dword v20, v2, s[8:11], s51 offen sc1\n"
      "s_waitcnt vmcnt(0)\n"
      "v_cmp_ne_u32 vcc, s21, v20\n"
      "s_add_u32 s30, s52, 106496\n"
      "buffer_store_dword v18, v4, s[8:11], s30 offen sc1\n"
      "s_cbranch_vccz .Lbk_d0_13\n"
      ".Lsl_d0_13:\n"
      "buffer_load_dword v20, v2, s[8:11], s51 offen sc1\n"
      "s_waitcnt vmcnt(0)\n"
      "v_cmp_ne_u32 vcc, s21, v20\n"
      "s_cbranch_vccz .Lbk_d0_13\n"
      "s_sleep 4\n"
      "s_sub_u32 s22, s22, 1\n"
      "s_cbranch_scc0 .Lsl_d0_13\n"
      "s_mov_b32 s23, 0\n"
      "s_branch .Lbk_d0_13\n"
      ".Lsp_dy5_14:\n"
      "s_lshl_b32 s22, s23, 4\n"
      ".Lsl_dy5_14:\n"
      "ds_read_b32 v15, v17\n"
      "s_waitcnt lgkmcnt(0)\n"
      "v_cmp_gt_i32 vcc, v14, v15\n"
      "s_cbranch_vccz .Lbk_dy5_14\n"
      "s_sub_u32 s22, s22, 1\n"
      "s_cbranch_scc0 .Lsl_dy5_14\n"
      "s_mov_b32 s23, 0\n"
      "s_branch .Lbk_dy5_14\n"
      ".Lsp_d1_15:\n"
      "s_mov_b32 s22, s23\n"
      "buffer_load_dword v20, v2, s[8:11], s51 offen sc1\n"
      "s_waitcnt vmcnt(0)\n"
      "v_cmp_ne_u32 vcc, s21, v20\n"
      "s_add_u32 s30, s52, 106496\n"
      "buffer_store_dword v18, v4, s[8:11], s30 offen sc1\n"
      "s_cbranch_vccz .Lbk_d1_15\n"
      ".Lsl_d1_15:\n"
      "buffer_load_dword v20, v2, s[8:11], s51 offen sc1\n"
      "s_waitcnt vmcnt(0)\n"
      "v_cmp_ne_u32 vcc, s21, v20\n"
      "s_cbranch_vccz .Lbk_d1_15\n"
      "s_sleep 4\n"
      "s_sub_u32 s22, s22, 1\n"
      "s_cbranch_scc0 .Lsl_d1_15\n"
      "s_mov_b32 s23, 0\n"
      "s_branch .Lbk_d1_15\n"
      ".Lsp_dy0_16:\n"
      "s_lshl_b32 s22, s23, 4\n"
      ".Lsl_dy0_16:\n"
      "ds_read_b32 v15, v17\n"
      "s_waitcnt lgkmcnt(0)\n"
      "v_cmp_gt_i32 vcc, v14, v15\n"
      "s_cbranch_vccz .Lbk_dy0_16\n"
      "s_sub_u32 s22, s22, 1\n"
      "s_cbranch_scc0 .Lsl_dy0_16\n"
      "s_mov_b32 s23, 0\n"
      "s_branch .Lbk_dy0_16\n"
      ".Lsp_df_17:\n"
      "s_mov_b32 s22, s23\n"
      "buffer_load_dword v21, v3, s[8:11], s53 offen sc1\n"
      "s_waitcnt vmcnt(0)\n"
      "v_cmp_ne_u32 vcc, s21, v21\n"
      "s_add_u32 s30, s52, 0\n"
      "buffer_store_dword v18, v4, s[8:11], s30 offen sc1\n"
      "s_cbranch_vccz .Lbk_df_17\n"
      ".Lsl_df_17:\n"
      "buffer_load_dword v21, v3, s[8:11], s53 offen sc1\n"
      "s_waitcnt vmcnt(0)\n"
      "v_cmp_ne_u32 vcc, s21, v21\n"
      "s_cbranch_vccz .Lbk_df_17\n"
      "s_sleep 4\n"
      "s_sub_u32 s22, s22, 1\n"
      "s_cbranch_scc0 .Lsl_df_17\n"
      "s_mov_b32 s23, 0\n"
      "s_branch .Lbk_df_17\n"
      ".Lsp_df_18:\n"
      "s_mov_b32 s22, s23\n"
      "buffer_load_dword v21, v3, s[8:11], s53 offen sc1\n"
      "s_waitcnt vmcnt(0)\n"
      "v_cmp_ne_u32 vcc, s21, v21\n"
      "s_add_u32 s30, s52, 0\n"
      "buffer_store_dword v18, v4, s[8:11], s30 offen sc1\n"
      "s_cbranch_vccz .Lbk_df_18\n"
      ".Lsl_df_18:\n"
      "buffer_load_dword v21, v3, s[8:11], s53 offen sc1\n"
      "s_waitcnt vmcnt(0)\n"
      "v_cmp_ne_u32 vcc, s21, v21\n"
      "s_cbranch_vccz .Lbk_df_18\n"
      "s_sleep 4\n"
      "s_sub_u32 s22, s22, 1\n"
      "s_cbranch_scc0 .Lsl_df_18\n"
      "s_mov_b32 s23, 0\n"
      "s_branch .Lbk_df_18\n"
      ".Lsp_df_19:\n"
      "s_mov_b32 s22, s23\n"
      "buffer_load_dword v21, v3, s[8:11], s53 offen sc1\n"
      "s_waitcnt vmcnt(0)\n"
      "v_cmp_ne_u32 vcc, s21, v21\n"
      "s_add_u32 s30, s52, 0\n"
      "buffer_store_dword v18, v4, s[8:11], s30 offen sc1\n"
      "s_cbranch_vccz .Lbk_df_19\n"
      ".Lsl_df_19:\n"
      "buffer_load_dword v21, v3, s[8:11], s53 offen sc1\n"
      "s_waitcnt vmcnt(0)\n"
      "v_cmp_ne_u32 vcc, s21, v21\n"
      "s_cbranch_vccz .Lbk_df_19\n"
      "s_sleep 4\n"
      "s_sub_u32 s22, s22, 1\n"
      "s_cbranch_scc0 .Lsl_df_19\n"
      "s_mov_b32 s23, 0\n"
      "s_branch .Lbk_df_19\n"
      ".Lsp_d1_20:\n"
      "s_mov_b32 s22, s23\n"
      ".Lsl_d1_20:\n"
      "buffer_load_dword v20, v2, s[8:11], s51 offen sc1\n"
      "s_waitcnt vmcnt(0)\n"
      "v_cmp_ne_u32 vcc, s21, v20\n"
      "s_cbranch_vccz .Lbk_d1_20\n"
      "s_sleep 4\n"
      "s_sub_u32 s22, s22, 1\n"
      "s_cbranch_scc0 .Lsl_d1_20\n"
      "s_mov_b32 s23, 0\n"
      "s_branch .Lbk_d1_20\n"
      ".Lsp_d2_21:\n"
      "s_mov_b32 s22, s23\n"
      ".Lsl_d2_21:\n"
      "buffer_load_dword v20, v2, s[8:11], s51 offen sc1\n"
      "s_waitcnt vmcnt(0)\n"
      "v_cmp_ne_u32 vcc, s21, v20\n"
      "s_cbranch_vccz .Lbk_d2_21\n"
      "s_sleep 4\n"
      "s_sub_u32 s22, s22, 1\n"
      "s_cbranch_scc0 .Lsl_d2_21\n"
      "s_mov_b32 s23, 0\n"
      "s_branch .Lbk_d2_21\n"
      ".Lsp_dy1_22:\n"
      "s_lshl_b32 s22, s23, 4\n"
      ".Lsl_dy1_22:\n"
      "ds_read_b32 v15, v17\n"
      "s_waitcnt lgkmcnt(0)\n"
      "v_cmp_gt_i32 vcc, v14, v15\n"
      "s_cbranch_vccz .Lbk_dy1_22\n"
      "s_sub_u32 s22, s22, 1\n"
      "s_cbranch_scc0 .Lsl_dy1_22\n"
      "s_mov_b32 s23, 0\n"
      "s_branch .Lbk_dy1_22\n"
      ".Lsp_d2_23:\n"
      "s_mov_b32 s22, s23\n"
      ".Lsl_d2_23:\n"
      "buffer_load_dword v20, v2, s[8:11], s51 offen sc1\n"
      "s_waitcnt vmcnt(0)\n"
      "v_cmp_ne_u32 vcc, s21, v20\n"
      "s_cbranch_vccz .Lbk_d2_23\n"
      "s_sleep 4\n"
      "s_sub_u32 s22, s22, 1\n"
      "s_cbranch_scc0 .Lsl_d2_23\n"
      "s_mov_b32 s23, 0\n"
      "s_branch .Lbk_d2_23\n"
      ".Lsp_d3_24:\n"
      "s_mov_b32 s22, s23\n"
      ".Lsl_d3_24:\n"
      "buffer_load_dword v20, v2, s[8:11], s51 offen sc1\n"
      "s_waitcnt vmcnt(0)\n"
      "v_cmp_ne_u32 vcc, s21, v20\n"
      "s_cbranch_vccz .Lbk_d3_24\n"
      "s_sleep 4\n"
      "s_sub_u32 s22, s22, 1\n"
      "s_cbranch_scc0 .Lsl_d3_24\n"
      "s_mov_b32 s23, 0\n"
      "s_branch .Lbk_d3_24\n"
      ".Lsp_dy2_25:\n"
      "s_lshl_b32 s22, s23, 4\n"
      ".Lsl_dy2_25:\n"
      "ds_read_b32 v15, v17\n"
      "s_waitcnt lgkmcnt(0)\n"
      "v_cmp_gt_i32 vcc, v14, v15\n"
      "s_cbranch_vccz .Lbk_dy2_25\n"
      "s_sub_u32 s22, s22, 1\n"
      "s_cbranch_scc0 .Lsl_dy2_25\n"
      "s_mov_b32 s23, 0\n"
      "s_branch .Lbk_dy2_25\n"
      ".Lsp_d3_26:\n"
      "s_mov_b32 s22, s23\n"
      ".Lsl_d3_26:\n"
      "buffer_load_dword v20, v2, s[8:11], s51 offen sc1\n"
      "s_waitcnt vmcnt(0)\n"
      "v_cmp_ne_u32 vcc, s21, v20\n"
      "s_cbranch_vccz .Lbk_d3_26\n"
      "s_sleep 4\n"
      "s_sub_u32 s22, s22, 1\n"
      "s_cbranch_scc0 .Lsl_d3_26\n"
      "s_mov_b32 s23, 0\n"
      "s_branch .Lbk_d3_26\n"
      ".Lsp_d4_27:\n"
      "s_mov_b32 s22, s23\n"
      ".Lsl_d4_27:\n"
      "buffer_load_dword v20, v2, s[8:11], s51 offen sc1\n"
      "s_waitcnt vmcnt(0)\n"
      "v_cmp_ne_u32 vcc, s21, v20\n"
      "s_cbranch_vccz .Lbk_d4_27\n"
      "s_sleep 4\n"
      "s_sub_u32 s22, s22, 1\n"
      "s_cbranch_scc0 .Lsl_d4_27\n"
      "s_mov_b32 s23, 0\n"
      "s_branch .Lbk_d4_27\n"
      ".Lsp_dy3_28:\n"
      "s_lshl_b32 s22, s23, 4\n"
      ".Lsl_dy3_28:\n"
      "ds_read_b32 v15, v17\n"
      "s_waitcnt lgkmcnt(0)\n"
      "v_cmp_gt_i32 vcc, v14, v15\n"
      "s_cbranch_vccz .Lbk_dy3_28\n"
      "s_sub_u32 s22, s22, 1\n"
      "s_cbranch_scc0 .Lsl_dy3_28\n"
      "s_mov_b32 s23, 0\n"
      "s_branch .Lbk_dy3_28\n"
      ".Lsp_d4_29:\n"
      "s_mov_b32 s22, s23\n"
      ".Lsl_d4_29:\n"
      "buffer_load_dword v20, v2, s[8:11], s51 offen sc1\n"
      "s_waitcnt vmcnt(0)\n"
      "v_cmp_ne_u32 vcc, s21, v20\n"
      "s_cbranch_vccz .Lbk_d4_29\n"
      "s_sleep 4\n"
      "s_sub_u32 s22, s22, 1\n"
      "s_cbranch_scc0 .Lsl_d4_29\n"
      "s_mov_b32 s23, 0\n"
      "s_branch .Lbk_d4_29\n"
      ".Lsp_d5_30:\n"
      "s_mov_b32 s22, s23\n"
      ".Lsl_d5_30:\n"
      "buffer_load_dword v20, v2, s[8:11], s51 offen sc1\n"
      "s_waitcnt vmcnt(0)\n"
      "v_cmp_ne_u32 vcc, s21, v20\n"
      "s_cbranch_vccz .Lbk_d5_30\n"
      "s_sleep 4\n"
      "s_sub_u32 s22, s22, 1\n"
      "s_cbranch_scc0 .Lsl_d5_30\n"
      "s_mov_b32 s23, 0\n"
      "s_branch .Lbk_d5_30\n"
      ".Lsp_dy4_31:\n"
      "s_lshl_b32 s22, s23, 4\n"
      ".Lsl_dy4_31:\n"
      "ds_read_b32 v15, v17\n"
      "s_waitcnt lgkmcnt(0)\n"
      "v_cmp_gt_i32 vcc, v14, v15\n"
      "s_cbranch_vccz .Lbk_dy4_31\n"
      "s_sub_u32 s22, s22, 1\n"
      "s_cbranch_scc0 .Lsl_dy4_31\n"
      "s_mov_b32 s23, 0\n"
      "s_branch .Lbk_dy4_31\n"
      ".Lsp_d5_32:\n"
      "s_mov_b32 s22, s23\n"
      ".Lsl_d5_32:\n"
      "buffer_load_dword v20, v2, s[8:11], s51 offen sc1\n"
      "s_waitcnt vmcnt(0)\n"
      "v_cmp_ne_u32 vcc, s21, v20\n"
      "s_cbranch_vccz .Lbk_d5_32\n"
      "s_sleep 4\n"
      "s_sub_u32 s22, s22, 1\n"
      "s_cbranch_scc0 .Lsl_d5_32\n"
      "s_mov_b32 s23, 0\n"
      "s_branch .Lbk_d5_32\n"
      ".Lsp_d0_33:\n"
      "s_mov_b32 s22, s23\n"
      ".Lsl_d0_33:\n"
      "buffer_load_dword v20, v2, s[8:11], s51 offen sc1\n"
      "s_waitcnt vmcnt(0)\n"
      "v_cmp_ne_u32 vcc, s21, v20\n"
      "s_cbranch_vccz .Lbk_d0_33\n"
      "s_sleep 4\n"
      "s_sub_u32 s22, s22, 1\n"
      "s_cbranch_scc0 .Lsl_d0_33\n"
      "s_mov_b32 s23, 0\n"
      "s_branch .Lbk_d0_33\n"
      ".Lsp_dy5_34:\n"
      "s_lshl_b32 s22, s23, 4\n"
      ".Lsl_dy5_34:\n"
      "ds_read_b32 v15, v17\n"
      "s_waitcnt lgkmcnt(0)\n"
      "v_cmp_gt_i32 vcc, v14, v15\n"
      "s_cbranch_vccz .Lbk_dy5_34\n"
      "s_sub_u32 s22, s22, 1\n"
      "s_cbranch_scc0 .Lsl_dy5_34\n"
      "s_mov_b32 s23, 0\n"
      "s_branch .Lbk_dy5_34\n"
      ".Ld_last:\n"
      :
      : "{s[8:11]}"(rsWS), "{s[12:15]}"(rsA), "{s[16:19]}"(rsB), "{s[92:95]}"(rsC), "{v0}"(v_l16), "{v1}"(v_l16b), "{v2}"(v_fa), "{v3}"(v_ga), "{v4}"(v_ra), "{v5}"(v_rf), "{v6}"(v_xp), "{v7}"(v_xs), "{v8}"(v_op), "{v9}"(v_os), "{v10}"(v_sw), "{v11}"(v_sr), "{v12}"(v_ss), "{v13}"(v_l8), "{v15}"(v_al), "{v16}"(v_yw), "{v17}"(v_yr), "{v19}"(v_hi), "{v176}"(v_at), "{v177}"(v_as), "{v178}"(v_vo0), "{v179}"(v_vo1), "{v180}"(v_vo2), "{v181}"(v_pw), "{v183}"(v_pu), "{s36}"(s_sh), "{s37}"(s_ps), "{s38}"(s_x1), "{s39}"(s_xr), "{s40}"(s_or), "{s41}"(s_p0), "{s42}"(s_m3), "{s43}"(s_m6), "{s44}"(s_so), "{s45}"(s_ph), "{s46}"(s_i0), "{s47}"(s_i5), "{s48}"(s_j0), "{s49}"(s_j5), "{s50}"(s_he), "{s60}"(s_k0)
      : "v255", "a255", "s99", "vcc", "scc", "memory");

  } else {
    const unsigned j = part, f = 4 * j + w;
    const u32x4 rsA = srd(w >> 1 ? Wu : Wg, 576 * 4608 * 2), rsC = srd(Wo, 576 * 576 * 2);
    const unsigned wv = MAP == 2 ? ((xcd >> 1) * 16 + slot * 4 + w) : (slot - (MAP ? 16 : 14)) * 4 + w, tw0 = (wv * 81 + lane) * 128;
    const bool twx = MAP == 2 ? !fast && slot < 4 : fast;
    const unsigned v_tw[2] = {MAP ? (twx ? tw0 : OOB) : (lane & 7) < 5 ? (8 * wv + l8) * 1152 + (xcd >> 2) * 512 + (lane & 7) * 128 : OOB,
                              MAP && twx && lane < 17 ? tw0 + 8192 : OOB};
    const unsigned v_l5 = w < 3 ? v_l16 + 16384 - w * 3072 : OOB;
    const unsigned v_fa = 19456 - w * 4096 + (lane >> 3) * 128 + (lane & 7) * 4, v_ra = up * 8 * HREC + l5 + 147456 - f * 1024 + f * 128;
    const unsigned one = q == g ? 0x3f80u << (16 * (p & 1)) : 0u, v_id[2] = {p < 2 ? one : 0u, p < 2 ? 0u : one};
    const unsigned v_st = 18432 + i16 * 32 + q * 8, v_pw = 147456 - f * 1024 + f * 128 + l8 * HREC + l7;
    const unsigned v_at = 65536 + (8 * g + q) * 256 + ((4 * w + p) ^ (4 * q)) * 16;
    const unsigned v_vo0 = (16 * (w & 1) + g) * 9216 + 256 * j + (i16 ^ (4 * g)) * 16;
    const unsigned v_vo1 = v_vo0 + 4 * 9216 - 1024, v_vo2 = v_vo1 + 4 * 9216 - 1024, v_vo3 = v_vo2 + 4 * 9216 - 1024;
    const unsigned s_sx = WS_X1 + gr * NREC * REC + w * 4096, s_sh = WS_H + gr * NREC * HREC + f * 1024;
    const unsigned s_m4 = w * 4096, s_m5 = 16384 + w * 1024;
    asm volatile(
      "s_buffer_load_dword s21, s[8:11], 0\n"
      "s_mov_b32 s23, 65536\n"
      "s_mov_b32 s31, 0x40000000\n"
      "s_mov_b32 s41, 0\n"
      "s_sleep 16\n"
      "buffer_load_dword v58, v54, s[92:95], s41 offen\n"
      "buffer_load_dword v59, v55, s[92:95], s41 offen\n"
      "s_waitcnt vmcnt(0)\n"
      "s_add_u32 m0, s38, 65536\n"
      "s_nop 0\n"
      "buffer_load_dwordx4 v6, s[12:15], s41 offen offset:0 lds\n"
      "buffer_load_dwordx4 v7, s[12:15], s41 offen offset:1024 lds\n"
      "buffer_load_dwordx4 v8, s[12:15], s41 offen offset:2048 lds\n"
      "buffer_load_dwordx4 v9, s[12:15], s41 offen offset:3072 lds\n"
      "s_add_u32 s41, s41, 294912\n"
      "s_add_u32 m0, s38, 81920\n"
      "s_nop 0\n"
      "buffer_load_dwordx4 v6, s[12:15], s41 offen offset:0 lds\n"
      "buffer_load_dwordx4 v7, s[12:15], s41 offen offset:1024 lds\n"
      "buffer_load_dwordx4 v8, s[12:15], s41 offen offset:2048 lds\n"
      "buffer_load_dwordx4 v9, s[12:15], s41 offen offset:3072 lds\n"
      "s_add_u32 s41, s41, 294912\n"
      "s_add_u32 m0, s38, 98304\n"
      "s_nop 0\n"
      "buffer_load_dwordx4 v6, s[12:15], s41 offen offset:0 lds\n"
      "buffer_load_dwordx4 v7, s[12:15], s41 offen offset:1024 lds\n"
      "buffer_load_dwordx4 v8, s[12:15], s41 offen offset:2048 lds\n"
      "buffer_load_dwordx4 v9, s[12:15], s41 offen offset:3072 lds\n"
      "s_add_u32 s41, s41, 294912\n"
      "s_add_u32 m0, s38, 114688\n"
      "s_nop 0\n"
      "buffer_load_dwordx4 v6, s[12:15], s41 offen offset:0 lds\n"
      "buffer_load_dwordx4 v7, s[12:15], s41 offen offset:1024 lds\n"
      "buffer_load_dwordx4 v8, s[12:15], s41 offen offset:2048 lds\n"
      "buffer_load_dwordx4 v9, s[12:15], s41 offen offset:3072 lds\n"
      "s_add_u32 s41, s41, 294912\n"
      "s_add_u32 m0, s38, 131072\n"
      "s_nop 0\n"
      "buffer_load_dwordx4 v6, s[12:15], s41 offen offset:0 lds\n"
      "buffer_load_dwordx4 v7, s[12:15], s41 offen offset:1024 lds\n"
      "buffer_load_dwordx4 v8, s[12:15], s41 offen offset:2048 lds\n"
      "buffer_load_dwordx4 v9, s[12:15], s41 offen offset:3072 lds\n"
      "s_add_u32 s41, s41, 294912\n"
      "v_mov_b32 v172, 0x3f803f80\n"
      "v_mov_b32 v173, 0x3f803f80\n"
      "v_mov_b32 v174, 0x3f803f80\n"
      "v_mov_b32 v175, 0x3f803f80\n"
      "v_add_u32 v19, 49152, v5\n"
      "s_mov_b64 exec, 0\n"
      "s_lshr_b32 s22, s38, 12\n"
      ".Lg_pl:\n"
      "s_cmp_eq_u32 s22, 0\n"
      "s_cbranch_scc1 .Lg_p0\n"
      "s_cmp_eq_u32 s22, 1\n"
      "s_cbranch_scc1 .Lg_p1\n"
      "s_branch .Lg_pe\n"
      ".Lg_pr:\n"
      "s_add_u32 s22, s22, 4\n"
      "s_branch .Lg_pl\n"
      ".Lg_pe:\n"
      "s_mov_b64 exec, -1\n"
      "s_waitcnt vmcnt(16)\n"
      "s_barrier\n"
      "ds_read_b64_tr_b16 a[0:1], v5 offset:0\n"
      "ds_read_b64_tr_b16 a[2:3], v5 offset:1024\n"
      "ds_read_b64_tr_b16 a[72:73], v5 offset:8192\n"
      "ds_read_b64_tr_b16 a[74:75], v5 offset:9216\n"
      "ds_read_b64_tr_b16 a[144:145], v5 offset:8\n"
      "ds_read_b64_tr_b16 a[146:147], v5 offset:1032\n"
      "ds_read_b64_tr_b16 a[216:217], v5 offset:8200\n"
      "ds_read_b64_tr_b16 a[218:219], v5 offset:9224\n"
      "s_add_u32 m0, s38, 147456\n"
      "s_nop 0\n"
      "buffer_load_dwordx4 v6, s[12:15], s41 offen offset:0 lds\n"
      "buffer_load_dwordx4 v7, s[12:15], s41 offen offset:1024 lds\n"
      "buffer_load_dwordx4 v8, s[12:15], s41 offen offset:2048 lds\n"
      "buffer_load_dwordx4 v9, s[12:15], s41 offen offset:3072 lds\n"
      "s_add_u32 s41, s41, 294912\n"
      "s_waitcnt vmcnt(16) lgkmcnt(0)\n"
      "s_barrier\n"
      "ds_read_b64_tr_b16 a[4:5], v5 offset:16384\n"
      "ds_read_b64_tr_b16 a[6:7], v5 offset:17408\n"
      "ds_read_b64_tr_b16 a[76:77], v5 offset:24576\n"
      "ds_read_b64_tr_b16 a[78:79], v5 offset:25600\n"
      "ds_read_b64_tr_b16 a[148:149], v5 offset:16392\n"
      "ds_read_b64_tr_b16 a[150:151], v5 offset:17416\n"
      "ds_read_b64_tr_b16 a[220:221], v5 offset:24584\n"
      "ds_read_b64_tr_b16 a[222:223], v5 offset:25608\n"
      "v_mfma_f32_16x16x32_bf16 v[140:143], v[172:175], a[0:3], 0\n"
      "v_mfma_f32_16x16x32_bf16 v[144:147], v[172:175], a[72:75], 0\n"
      "v_mfma_f32_16x16x32_bf16 v[148:151], v[172:175], a[144:147], 0\n"
      "v_mfma_f32_16x16x32_bf16 v[152:155], v[172:175], a[216:219], 0\n"
      "s_add_u32 m0, s38, 65536\n"
      "s_nop 0\n"
      "buffer_load_dwordx4 v6, s[12:15], s41 offen offset:0 lds\n"
      "buffer_load_dwordx4 v7, s[12:15], s41 offen offset:1024 lds\n"
      "buffer_load_dwordx4 v8, s[12:15], s41 offen offset:2048 lds\n"
      "buffer_load_dwordx4 v9, s[12:15], s41 offen offset:3072 lds\n"
      "s_add_u32 s41, s41, 294912\n"
      "s_waitcnt vmcnt(16) lgkmcnt(0)\n"
      "s_barrier\n"
      "ds_read_b64_tr_b16 a[8:9], v5 offset:32768\n"
      "ds_read_b64_tr_b16 a[10:11], v5 offset:33792\n"
      "ds_read_b64_tr_b16 a[80:81], v5 offset:40960\n"
      "ds_read_b64_tr_b16 a[82:83], v5 offset:41984\n"
      "ds_read_b64_tr_b16 a[152:153], v5 offset:32776\n"
      "ds_read_b64_tr_b16 a[154:155], v5 offset:33800\n"
      "ds_read_b64_tr_b16 a[224:225], v5 offset:40968\n"
      "ds_read_b64_tr_b16 a[226:227], v5 offset:41992\n"
      "v_mfma_f32_16x16x32_bf16 v[140:143], v[172:175], a[4:7], v[140:143]\n"
      "v_mfma_f32_16x16x32_bf16 v[144:147], v[172:175], a[76:79], v[144:147]\n"
      "v_mfma_f32_16x16x32_bf16 v[148:151], v[172:175], a[148:151], v[148:151]\n"
      "v_mfma_f32_16x16x32_bf16 v[152:155], v[172:175], a[220:223], v[152:155]\n"
      "s_add_u32 m0, s38, 81920\n"
      "s_nop 0\n"
      "buffer_load_dwordx4 v6, s[12:15], s41 offen offset:0 lds\n"
      "buffer_load_dwordx4 v7, s[12:15], s41 offen offset:1024 lds\n"
      "buffer_load_dwordx4 v8, s[12:15], s41 offen offset:2048 lds\n"
      "buffer_load_dwordx4 v9, s[12:15], s41 offen offset:3072 lds\n"
      "s_add_u32 s41, s41, 294912\n"
      "s_waitcnt vmcnt(16) lgkmcnt(0)\n"
      "s_barrier\n"
      "ds_read_b64_tr_b16 a[12:13], v19 offset:0\n"
      "ds_read_b64_tr_b16 a[14:15], v19 offset:1024\n"
      "ds_read_b64_tr_b16 a[84:85], v19 offset:8192\n"
      "ds_read_b64_tr_b16 a[86:87], v19 offset:9216\n"
      "ds_read_b64_tr_b16 a[156:157], v19 offset:8\n"
      "ds_read_b64_tr_b16 a[158:159], v19 offset:1032\n"
      "ds_read_b64_tr_b16 a[228:229], v19 offset:8200\n"
      "ds_read_b64_tr_b16 a[230:231], v19 offset:9224\n"
      "v_mfma_f32_16x16x32_bf16 v[140:143], v[172:175], a[8:11], v[140:143]\n"
      "v_mfma_f32_16x16x32_bf16 v[144:147], v[172:175], a[80:83], v[144:147]\n"
      "v_mfma_f32_16x16x32_bf16 v[148:151], v[172:175], a[152:155], v[148:151]\n"
      "v_mfma_f32_16x16x32_bf16 v[152:155], v[172:175], a[224:227], v[152:155]\n"
      "s_add_u32 m0, s38, 98304\n"
      "s_nop 0\n"
      "buffer_load_dwordx4 v6, s[12:15], s41 offen offset:0 lds\n"
      "buffer_load_dwordx4 v7, s[12:15], s41 offen offset:1024 lds\n"
      "buffer_load_dwordx4 v8, s[12:15], s41 offen offset:2048 lds\n"
      "buffer_load_dwordx4 v9, s[12:15], s41 offen offset:3072 lds\n"
      "s_add_u32 s41, s41, 294912\n"
      "s_waitcnt vmcnt(16) lgkmcnt(0)\n"
      "s_barrier\n"
      "ds_read_b64_tr_b16 a[16:17], v19 offset:16384\n"
      "ds_read_b64_tr_b16 a[18:19], v19 offset:17408\n"
      "ds_read_b64_tr_b16 a[88:89], v19 offset:24576\n"
      "ds_read_b64_tr_b16 a[90:91], v19 offset:25600\n"
      "ds_read_b64_tr_b16 a[160:161], v19 offset:16392\n"
      "ds_read_b64_tr_b16 a[162:163], v19 offset:17416\n"
      "ds_read_b64_tr_b16 a[232:233], v19 offset:24584\n"
      "ds_read_b64_tr_b16 a[234:235], v19 offset:25608\n"
      "v_mfma_f32_16x16x32_bf16 v[140:143], v[172:175], a[12:15], v[140:143]\n"
      "v_mfma_f32_16x16x32_bf16 v[144:147], v[172:175], a[84:87], v[144:147]\n"
      "v_mfma_f32_16x16x32_bf16 v[148:151], v[172:175], a[156:159], v[148:151]\n"
      "v_mfma_f32_16x16x32_bf16 v[152:155], v[172:175], a[228:231], v[152:155]\n"
      "s_add_u32 m0, s38, 114688\n"
      "s_nop 0\n"
      "buffer_load_dwordx4 v6, s[12:15], s41 offen offset:0 lds\n"
      "buffer_load_dwordx4 v7, s[12:15], s41 offen offset:1024 lds\n"
      "buffer_load_dwordx4 v8, s[12:15], s41 offen offset:2048 lds\n"
      "buffer_load_dwordx4 v9, s[12:15], s41 offen offset:3072 lds\n"
      "s_add_u32 s41, s41, 294912\n"
      "s_waitcnt vmcnt(16) lgkmcnt(0)\n"
      "s_barrier\n"
      "ds_read_b64_tr_b16 a[20:21], v19 offset:32768\n"
      "ds_read_b64_tr_b16 a[22:23], v19 offset:33792\n"
      "ds_read_b64_tr_b16 a[92:93], v19 offset:40960\n"
      "ds_read_b64_tr_b16 a[94:95], v19 offset:41984\n"
      "ds_read_b64_tr_b16 a[164:165], v19 offset:32776\n"
      "ds_read_b64_tr_b16 a[166:167], v19 offset:33800\n"
      "ds_read_b64_tr_b16 a[236:237], v19 offset:40968\n"
      "ds_read_b64_tr_b16 a[238:239], v19 offset:41992\n"
      "v_mfma_f32_16x16x32_bf16 v[140:143], v[172:175], a[16:19], v[140:143]\n"
      "v_mfma_f32_16x16x32_bf16 v[144:147], v[172:175], a[88:91], v[144:147]\n"
      "v_mfma_f32_16x16x32_bf16 v[148:151], v[172:175], a[160:163], v[148:151]\n"
      "v_mfma_f32_16x16x32_bf16 v[152:155], v[172:175], a[232:235], v[152:155]\n"
      "s_add_u32 m0, s38, 131072\n"
      "s_nop 0\n"
      "buffer_load_dwordx4 v6, s[12:15], s41 offen offset:0 lds\n"
      "buffer_load_dwordx4 v7, s[12:15], s41 offen offset:1024 lds\n"
      "buffer_load_dwordx4 v8, s[12:15], s41 offen offset:2048 lds\n"
      "buffer_load_dwordx4 v9, s[12:15], s41 offen offset:3072 lds\n"
      "s_add_u32 s41, s41, 294912\n"
      "s_waitcnt vmcnt(16) lgkmcnt(0)\n"
      "s_barrier\n"
      "ds_read_b64_tr_b16 a[24:25], v5 offset:0\n"
      "ds_read_b64_tr_b16 a[26:27], v5 offset:1024\n"
      "ds_read_b64_tr_b16 a[96:97], v5 offset:8192\n"
      "ds_read_b64_tr_b16 a[98:99], v5 offset:9216\n"
      "ds_read_b64_tr_b16 a[168:169], v5 offset:8\n"
      "ds_read_b64_tr_b16 a[170:171], v5 offset:1032\n"
      "ds_read_b64_tr_b16 a[240:241], v5 offset:8200\n"
      "ds_read_b64_tr_b16 a[242:243], v5 offset:9224\n"
      "v_mfma_f32_16x16x32_bf16 v[140:143], v[172:175], a[20:23], v[140:143]\n"
      "v_mfma_f32_16x16x32_bf16 v[144:147], v[172:175], a[92:95], v[144:147]\n"
      "v_mfma_f32_16x16x32_bf16 v[148:151], v[172:175], a[164:167], v[148:151]\n"
      "v_mfma_f32_16x16x32_bf16 v[152:155], v[172:175], a[236:239], v[152:155]\n"
      "s_add_u32 m0, s38, 147456\n"
      "s_nop 0\n"
      "buffer_load_dwordx4 v6, s[12:15], s41 offen offset:0 lds\n"
      "buffer_load_dwordx4 v7, s[12:15], s41 offen offset:1024 lds\n"
      "buffer_load_dwordx4 v8, s[12:15], s41 offen offset:2048 lds\n"
      "buffer_load_dwordx4 v9, s[12:15], s41 offen offset:3072 lds\n"
      "s_add_u32 s41, s41, 294912\n"
      "s_waitcnt vmcnt(16) lgkmcnt(0)\n"
      "s_barrier\n"
      "ds_read_b64_tr_b16 a[28:29], v5 offset:16384\n"
      "ds_read_b64_tr_b16 a[30:31], v5 offset:17408\n"
      "ds_read_b64_tr_b16 a[100:101], v5 offset:24576\n"
      "ds_read_b64_tr_b16 a[102:103], v5 offset:25600\n"
      "ds_read_b64_tr_b16 a[172:173], v5 offset:16392\n"
      "ds_read_b64_tr_b16 a[174:175], v5 offset:17416\n"
      "ds_read_b64_tr_b16 a[244:245], v5 offset:24584\n"
      "ds_read_b64_tr_b16 a[246:247], v5 offset:25608\n"
      "v_mfma_f32_16x16x32_bf16 v[140:143], v[172:175], a[24:27], v[140:143]\n"
      "v_mfma_f32_16x16x32_bf16 v[144:147], v[172:175], a[96:99], v[144:147]\n"
      "v_mfma_f32_16x16x32_bf16 v[148:151], v[172:175], a[168:171], v[148:151]\n"
      "v_mfma_f32_16x16x32_bf16 v[152:155], v[172:175], a[240:243], v[152:155]\n"
      "s_add_u32 m0, s38, 65536\n"
      "s_nop 0\n"
      "buffer_load_dwordx4 v6, s[12:15], s41 offen offset:0 lds\n"
      "buffer_load_dwordx4 v7, s[12:15], s41 offen offset:1024 lds\n"
      "buffer_load_dwordx4 v8, s[12:15], s41 offen offset:2048 lds\n"
      "buffer_load_dwordx4 v9, s[12:15], s41 offen offset:3072 lds\n"
      "s_add_u32 s41, s41, 294912\n"
      "s_waitcnt vmcnt(16) lgkmcnt(0)\n"
      "s_barrier\n"
      "ds_read_b64_tr_b16 a[32:33], v5 offset:32768\n"
      "ds_read_b64_tr_b16 a[34:35], v5 offset:33792\n"
      "ds_read_b64_tr_b16 a[104:105], v5 offset:40960\n"
      "ds_read_b64_tr_b16 a[106:107], v5 offset:41984\n"
      "ds_read_b64_tr_b16 a[176:177], v5 offset:32776\n"
      "ds_read_b64_tr_b16 a[178:179], v5 offset:33800\n"
      "ds_read_b64_tr_b16 a[248:249], v5 offset:40968\n"
      "ds_read_b64_tr_b16 a[250:251], v5 offset:41992\n"
      "v_mfma_f32_16x16x32_bf16 v[140:143], v[172:175], a[28:31], v[140:143]\n"
      "v_mfma_f32_16x16x32_bf16 v[144:147], v[172:175], a[100:103], v[144:147]\n"
      "v_mfma_f32_16x16x32_bf16 v[148:151], v[172:175], a[172:175], v[148:151]\n"
      "v_mfma_f32_16x16x32_bf16 v[152:155], v[172:175], a[244:247], v[152:155]\n"
      "s_add_u32 m0, s38, 81920\n"
      "s_nop 0\n"
      "buffer_load_dwordx4 v6, s[12:15], s41 offen offset:0 lds\n"
      "buffer_load_dwordx4 v7, s[12:15], s41 offen offset:1024 lds\n"
      "buffer_load_dwordx4 v8, s[12:15], s41 offen offset:2048 lds\n"
      "buffer_load_dwordx4 v9, s[12:15], s41 offen offset:3072 lds\n"
      "s_add_u32 s41, s41, 294912\n"
      "s_waitcnt vmcnt(16) lgkmcnt(0)\n"
      "s_barrier\n"
      "ds_read_b64_tr_b16 a[36:37], v19 offset:0\n"
      "ds_read_b64_tr_b16 a[38:39], v19 offset:1024\n"
      "ds_read_b64_tr_b16 a[108:109], v19 offset:8192\n"
      "ds_read_b64_tr_b16 a[110:111], v19 offset:9216\n"
      "ds_read_b64_tr_b16 a[180:181], v19 offset:8\n"
      "ds_read_b64_tr_b16 a[182:183], v19 offset:1032\n"
      "ds_read_b64_tr_b16 a[252:253], v19 offset:8200\n"
      "ds_read_b64_tr_b16 a[254:255], v19 offset:9224\n"
      "v_mfma_f32_16x16x32_bf16 v[140:143], v[172:175], a[32:35], v[140:143]\n"
      "v_mfma_f32_16x16x32_bf16 v[144:147], v[172:175], a[104:107], v[144:147]\n"
      "v_mfma_f32_16x16x32_bf16 v[148:151], v[172:175], a[176:179], v[148:151]\n"
      "v_mfma_f32_16x16x32_bf16 v[152:155], v[172:175], a[248:251], v[152:155]\n"
      "s_add_u32 m0, s38, 98304\n"
      "s_nop 0\n"
      "buffer_load_dwordx4 v6, s[12:15], s41 offen offset:0 lds\n"
      "buffer_load_dwordx4 v7, s[12:15], s41 offen offset:1024 lds\n"
      "buffer_load_dwordx4 v8, s[12:15], s41 offen offset:2048 lds\n"
      "buffer_load_dwordx4 v9, s[12:15], s41 offen offset:3072 lds\n"
      "s_add_u32 s41, s41, 294912\n"
      "s_waitcnt vmcnt(16) lgkmcnt(0)\n"
      "s_barrier\n"
      "ds_read_b64_tr_b16 a[40:41], v19 offset:16384\n"
      "ds_read_b64_tr_b16 a[42:43], v19 offset:17408\n"
      "ds_read_b64_tr_b16 a[112:113], v19 offset:24576\n"
      "ds_read_b64_tr_b16 a[114:115], v19 offset:25600\n"
      "ds_read_b64_tr_b16 a[184:185], v19 offset:16392\n"
      "ds_read_b64_tr_b16 a[186:187], v19 offset:17416\n"
      "ds_read_b64_tr_b16 v[20:21], v19 offset:24584\n"
      "ds_read_b64_tr_b16 v[22:23], v19 offset:25608\n"
      "v_mfma_f32_16x16x32_bf16 v[140:143], v[172:175], a[36:39], v[140:143]\n"
      "v_mfma_f32_16x16x32_bf16 v[144:147], v[172:175], a[108:111], v[144:147]\n"
      "v_mfma_f32_16x16x32_bf16 v[148:151], v[172:175], a[180:183], v[148:151]\n"
      "v_mfma_f32_16x16x32_bf16 v[152:155], v[172:175], a[252:255], v[152:155]\n"
      "s_add_u32 m0, s38, 114688\n"
      "s_nop 0\n"
      "buffer_load_dwordx4 v6, s[12:15], s41 offen offset:0 lds\n"
      "buffer_load_dwordx4 v7, s[12:15], s41 offen offset:1024 lds\n"
      "buffer_load_dwordx4 v8, s[12:15], s41 offen offset:2048 lds\n"
      "buffer_load_dwordx4 v9, s[12:15], s41 offen offset:3072 lds\n"
      "s_add_u32 s41, s41, 294912\n"
      "s_waitcnt vmcnt(16) lgkmcnt(0)\n"
      "s_barrier\n"
      "ds_read_b64_tr_b16 a[44:45], v19 offset:32768\n"
      "ds_read_b64_tr_b16 a[46:47], v19 offset:33792\n"
      "ds_read_b64_tr_b16 a[116:117], v19 offset:40960\n"
      "ds_read_b64_tr_b16 a[118:119], v19 offset:41984\n"
      "ds_read_b64_tr_b16 a[188:189], v19 offset:32776\n"
      "ds_read_b64_tr_b16 a[190:191], v19 offset:33800\n"
      "ds_read_b64_tr_b16 v[24:25], v19 offset:40968\n"
      "ds_read_b64_tr_b16 v[26:27], v19 offset:41992\n"
      "v_mfma_f32_16x16x32_bf16 v[140:143], v[172:175], a[40:43], v[140:143]\n"
      "v_mfma_f32_16x16x32_bf16 v[144:147], v[172:175], a[112:115], v[144:147]\n"
      "v_mfma_f32_16x16x32_bf16 v[148:151], v[172:175], a[184:187], v[148:151]\n"
      "v_mfma_f32_16x16x32_bf16 v[152:155], v[172:175], v[20:23], v[152:155]\n"
      "s_add_u32 m0, s38, 131072\n"
      "s_nop 0\n"
      "buffer_load_dwordx4 v6, s[12:15], s41 offen offset:0 lds\n"
      "buffer_load_dwordx4 v7, s[12:15], s41 offen offset:1024 lds\n"
      "buffer_load_dwordx4 v8, s[12:15], s41 offen offset:2048 lds\n"
      "buffer_load_dwordx4 v9, s[12:15], s41 offen offset:3072 lds\n"
      "s_add_u32 s41, s41, 294912\n"
      "s_waitcnt vmcnt(16) lgkmcnt(0)\n"
      "s_barrier\n"
      "ds_read_b64_tr_b16 a[48:49], v5 offset:0\n"
      "ds_read_b64_tr_b16 a[50:51], v5 offset:1024\n"
      "ds_read_b64_tr_b16 a[120:121], v5 offset:8192\n"
      "ds_read_b64_tr_b16 a[122:123], v5 offset:9216\n"
      "ds_read_b64_tr_b16 a[192:193], v5 offset:8\n"
      "ds_read_b64_tr_b16 a[194:195], v5 offset:1032\n"
      "ds_read_b64_tr_b16 v[28:29], v5 offset:8200\n"
      "ds_read_b64_tr_b16 v[30:31], v5 offset:9224\n"
      "v_mfma_f32_16x16x32_bf16 v[140:143], v[172:175], a[44:47], v[140:143]\n"
      "v_mfma_f32_16x16x32_bf16 v[144:147], v[172:175], a[116:119], v[144:147]\n"
      "v_mfma_f32_16x16x32_bf16 v[148:151], v[172:175], a[188:191], v[148:151]\n"
      "v_mfma_f32_16x16x32_bf16 v[152:155], v[172:175], v[24:27], v[152:155]\n"
      "s_add_u32 m0, s38, 147456\n"
      "s_nop 0\n"
      "buffer_load_dwordx4 v6, s[12:15], s41 offen offset:0 lds\n"
      "buffer_load_dwordx4 v7, s[12:15], s41 offen offset:1024 lds\n"
      "buffer_load_dwordx4 v8, s[12:15], s41 offen offset:2048 lds\n"
      "buffer_load_dwordx4 v9, s[12:15], s41 offen offset:3072 lds\n"
      "s_add_u32 s41, s41, 294912\n"
      "s_waitcnt vmcnt(16) lgkmcnt(0)\n"
      "s_barrier\n"
      "ds_read_b64_tr_b16 a[52:53], v5 offset:16384\n"
      "ds_read_b64_tr_b16 a[54:55], v5 offset:17408\n"
      "ds_read_b64_tr_b16 a[124:125], v5 offset:24576\n"
      "ds_read_b64_tr_b16 a[126:127], v5 offset:25600\n"
      "ds_read_b64_tr_b16 a[196:197], v5 offset:16392\n"
      "ds_read_b64_tr_b16 a[198:199], v5 offset:17416\n"
      "ds_read_b64_tr_b16 v[32:33], v5 offset:24584\n"
      "ds_read_b64_tr_b16 v[34:35], v5 offset:25608\n"
      "v_mfma_f32_16x16x32_bf16 v[140:143], v[172:175], a[48:51], v[140:143]\n"
      "v_mfma_f32_16x16x32_bf16 v[144:147], v[172:175], a[120:123], v[144:147]\n"
      "v_mfma_f32_16x16x32_bf16 v[148:151], v[172:175], a[192:195], v[148:151]\n"
      "v_mfma_f32_16x16x32_bf16 v[152:155], v[172:175], v[28:31], v[152:155]\n"
      "s_waitcnt vmcnt(12) lgkmcnt(0)\n"
      "s_barrier\n"
      "ds_read_b64_tr_b16 a[56:57], v5 offset:32768\n"
      "ds_read_b64_tr_b16 a[58:59], v5 offset:33792\n"
      "ds_read_b64_tr_b16 a[128:129], v5 offset:40960\n"
      "ds_read_b64_tr_b16 a[130:131], v5 offset:41984\n"
      "ds_read_b64_tr_b16 a[200:201], v5 offset:32776\n"
      "ds_read_b64_tr_b16 a[202:203], v5 offset:33800\n"
      "ds_read_b64_tr_b16 v[36:37], v5 offset:40968\n"
      "ds_read_b64_tr_b16 v[38:39], v5 offset:41992\n"
      "v_mfma_f32_16x16x32_bf16 v[140:143], v[172:175], a[52:55], v[140:143]\n"
      "v_mfma_f32_16x16x32_bf16 v[144:147], v[172:175], a[124:127], v[144:147]\n"
      "v_mfma_f32_16x16x32_bf16 v[148:151], v[172:175], a[196:199], v[148:151]\n"
      "v_mfma_f32_16x16x32_bf16 v[152:155], v[172:175], v[32:35], v[152:155]\n"
      "s_waitcnt vmcnt(8) lgkmcnt(0)\n"
      "s_barrier\n"
      "ds_read_b64_tr_b16 a[60:61], v19 offset:0\n"
      "ds_read_b64_tr_b16 a[62:63], v19 offset:1024\n"
      "ds_read_b64_tr_b16 a[132:133], v19 offset:8192\n"
      "ds_read_b64_tr_b16 a[134:135], v19 offset:9216\n"
      "ds_read_b64_tr_b16 a[204:205], v19 offset:8\n"
      "ds_read_b64_tr_b16 a[206:207], v19 offset:1032\n"
      "ds_read_b64_tr_b16 v[40:41], v19 offset:8200\n"
      "ds_read_b64_tr_b16 v[42:43], v19 offset:9224\n"
      "v_mfma_f32_16x16x32_bf16 v[140:143], v[172:175], a[56:59], v[140:143]\n"
      "v_mfma_f32_16x16x32_bf16 v[144:147], v[172:175], a[128:131], v[144:147]\n"
      "v_mfma_f32_16x16x32_bf16 v[148:151], v[172:175], a[200:203], v[148:151]\n"
      "v_mfma_f32_16x16x32_bf16 v[152:155], v[172:175], v[36:39], v[152:155]\n"
      "s_waitcnt vmcnt(4) lgkmcnt(0)\n"
      "s_barrier\n"
      "ds_read_b64_tr_b16 a[64:65], v19 offset:16384\n"
      "ds_read_b64_tr_b16 a[66:67], v19 offset:17408\n"
      "ds_read_b64_tr_b16 a[136:137], v19 offset:24576\n"
      "ds_read_b64_tr_b16 a[138:139], v19 offset:25600\n"
      "ds_read_b64_tr_b16 a[208:209], v19 offset:16392\n"
      "ds_read_b64_tr_b16 a[210:211], v19 offset:17416\n"
      "ds_read_b64_tr_b16 v[44:45], v19 offset:24584\n"
      "ds_read_b64_tr_b16 v[46:47], v19 offset:25608\n"
      "v_mfma_f32_16x16x32_bf16 v[140:143], v[172:175], a[60:63], v[140:143]\n"
      "v_mfma_f32_16x16x32_bf16 v[144:147], v[172:175], a[132:135], v[144:147]\n"
      "v_mfma_f32_16x16x32_bf16 v[148:151], v[172:175], a[204:207], v[148:151]\n"
      "v_mfma_f32_16x16x32_bf16 v[152:155], v[172:175], v[40:43], v[152:155]\n"
      "s_waitcnt vmcnt(0) lgkmcnt(0)\n"
      "s_barrier\n"
      "ds_read_b64_tr_b16 a[68:69], v19 offset:32768\n"
      "ds_read_b64_tr_b16 a[70:71], v19 offset:33792\n"
      "ds_read_b64_tr_b16 a[140:141], v19 offset:40960\n"
      "ds_read_b64_tr_b16 a[142:143], v19 offset:41984\n"
      "ds_read_b64_tr_b16 a[212:213], v19 offset:32776\n"
      "ds_read_b64_tr_b16 a[214:215], v19 offset:33800\n"
      "ds_read_b64_tr_b16 v[48:49], v19 offset:40968\n"
      "ds_read_b64_tr_b16 v[50:51], v19 offset:41992\n"
      "v_mfma_f32_16x16x32_bf16 v[140:143], v[172:175], a[64:67], v[140:143]\n"
      "v_mfma_f32_16x16x32_bf16 v[144:147], v[172:175], a[136:139], v[144:147]\n"
      "v_mfma_f32_16x16x32_bf16 v[148:151], v[172:175], a[208:211], v[148:151]\n"
      "v_mfma_f32_16x16x32_bf16 v[152:155], v[172:175], v[44:47], v[152:155]\n"
      "s_waitcnt lgkmcnt(0)\n"
      "v_mfma_f32_16x16x32_bf16 v[140:143], v[172:175], a[68:71], v[140:143]\n"
      "v_mfma_f32_16x16x32_bf16 v[144:147], v[172:175], a[140:143], v[144:147]\n"
      "v_mfma_f32_16x16x32_bf16 v[148:151], v[172:175], a[212:215], v[148:151]\n"
      "v_mfma_f32_16x16x32_bf16 v[152:155], v[172:175], v[48:51], v[152:155]\n"
      "s_getpc_b64 vcc\n"
      ".Lg_ct:\n"
      "s_add_u32 vcc_lo, vcc_lo, .Lg_p0-.Lg_ct\n"
      "s_addc_u32 vcc_hi, vcc_hi, 0\n"
      "s_lshl_b32 s30, s38, 1\n"
      "v_lshl_add_u32 v60, v0, 3, s30\n"
      "v_min_u32 v60, .Lg_last-.Lg_p0-4, v60\n"
      "global_load_dword v60, v60, vcc\n"
      "s_mul_i32 s21, s21, 1664525\n"
      "s_add_u32 s21, s21, 1013904223\n"
      "v_mov_b32 v15, s21\n"
      "buffer_store_dword v15, v12, s[8:11], 0 offen sc1\n"
      "v_not_b32 v52, v15\n"
      "v_not_b32 v53, v15\n"
      "v_not_b32 v54, v15\n"
      "v_not_b32 v55, v15\n"
      "s_add_u32 s30, s37, 0\n"
      "buffer_store_dwordx4 v[52:55], v10, s[8:11], s30 offen sc1\n"
      "v_xor_b32 v15, v15, v16\n"
      "v_mov_b32 v66, 0\n"
      "v_mov_b32 v67, 0\n"
      "v_mov_b32 v68, 0\n"
      "v_mov_b32 v69, 0\n"
      "v_mov_b32 v70, v64\n"
      "v_mov_b32 v71, v65\n"
      "v_mul_f32 v140, 0xbae38e39, v140\n"
      "v_mul_f32 v141, 0xbae38e39, v141\n"
      "v_mul_f32 v142, 0xbae38e39, v142\n"
      "v_mul_f32 v143, 0xbae38e39, v143\n"
      "v_mul_f32 v144, 0xbae38e39, v144\n"
      "v_mul_f32 v145, 0xbae38e39, v145\n"
      "v_mul_f32 v146, 0xbae38e39, v146\n"
      "v_mul_f32 v147, 0xbae38e39, v147\n"
      "v_mul_f32 v148, 0xbae38e39, v148\n"
      "v_mul_f32 v149, 0xbae38e39, v149\n"
      "v_mul_f32 v150, 0xbae38e39, v150\n"
      "v_mul_f32 v151, 0xbae38e39, v151\n"
      "v_mul_f32 v152, 0xbae38e39, v152\n"
      "v_mul_f32 v153, 0xbae38e39, v153\n"
      "v_mul_f32 v154, 0xbae38e39, v154\n"
      "v_mul_f32 v155, 0xbae38e39, v155\n"
      "v_mfma_f32_16x16x32_bf16 v[72:75], v[64:67], a[0:3], v[140:143]\n"
      "v_mfma_f32_16x16x32_bf16 v[76:79], v[68:71], a[0:3], v[140:143]\n"
      "v_mfma_f32_16x16x32_bf16 v[80:83], v[64:67], a[4:7], v[140:143]\n"
      "v_mfma_f32_16x16x32_bf16 v[84:87], v[68:71], a[4:7], v[140:143]\n"
      "v_mfma_f32_16x16x32_bf16 v[88:91], v[64:67], a[8:11], v[140:143]\n"
      "v_mfma_f32_16x16x32_bf16 v[92:95], v[68:71], a[8:11], v[140:143]\n"
      "v_cvt_pk_bf16_f32 v72, v72, v73\n"
      "v_cvt_pk_bf16_f32 v73, v74, v75\n"
      "v_cvt_pk_bf16_f32 v74, v76, v77\n"
      "v_cvt_pk_bf16_f32 v75, v78, v79\n"
      "v_accvgpr_write_b32 a0, v72\n"
      "v_accvgpr_write_b32 a1, v73\n"
      "v_accvgpr_write_b32 a2, v74\n"
      "v_accvgpr_write_b32 a3, v75\n"
      "v_mfma_f32_16x16x32_bf16 v[72:75], v[64:67], a[12:15], v[140:143]\n"
      "v_mfma_f32_16x16x32_bf16 v[76:79], v[68:71], a[12:15], v[140:143]\n"
      "v_cvt_pk_bf16_f32 v80, v80, v81\n"
      "v_cvt_pk_bf16_f32 v81, v82, v83\n"
      "v_cvt_pk_bf16_f32 v82, v84, v85\n"
      "v_cvt_pk_bf16_f32 v83, v86, v87\n"
      "v_accvgpr_write_b32 a4, v80\n"
      "v_accvgpr_write_b32 a5, v81\n"
      "v_accvgpr_write_b32 a6, v82\n"
      "v_accvgpr_write_b32 a7, v83\n"
      "v_mfma_f32_16x16x32_bf16 v[80:83], v[64:67], a[16:19], v[140:143]\n"
      "v_mfma_f32_16x16x32_bf16 v[84:87], v[68:71], a[16:19], v[140:143]\n"
      "v_cvt_pk_bf16_f32 v88, v88, v89\n"
      "v_cvt_pk_bf16_f32 v89, v90, v91\n"
      "v_cvt_pk_bf16_f32 v90, v92, v93\n"
      "v_cvt_pk_bf16_f32 v91, v94, v95\n"
      "v_accvgpr_write_b32 a8, v88\n"
      "v_accvgpr_write_b32 a9, v89\n"
      "v_accvgpr_write_b32 a10, v90\n"
      "v_accvgpr_write_b32 a11, v91\n"
      "v_mfma_f32_16x16x32_bf16 v[88:91], v[64:67], a[20:23], v[140:143]\n"
      "v_mfma_f32_16x16x32_bf16 v[92:95], v[68:71], a[20:23], v[140:143]\n"
      "v_cvt_pk_bf16_f32 v72, v72, v73\n"
      "v_cvt_pk_bf16_f32 v73, v74, v75\n"
      "v_cvt_pk_bf16_f32 v74, v76, v77\n"
      "v_cvt_pk_bf16_f32 v75, v78, v79\n"
      "v_accvgpr_write_b32 a12, v72\n"
      "v_accvgpr_write_b32 a13, v73\n"
      "v_accvgpr_write_b32 a14, v74\n"
      "v_accvgpr_write_b32 a15, v75\n"
      "v_mfma_f32_16x16x32_bf16 v[72:75], v[64:67], a[24:27], v[140:143]\n"
      "v_mfma_f32_16x16x32_bf16 v[76:79], v[68:71], a[24:27], v[140:143]\n"
      "v_cvt_pk_bf16_f32 v80, v80, v81\n"
      "v_cvt_pk_bf16_f32 v81, v82, v83\n"
      "v_cvt_pk_bf16_f32 v82, v84, v85\n"
      "v_cvt_pk_bf16_f32 v83, v86, v87\n"
      "v_accvgpr_write_b32 a16, v80\n"
      "v_accvgpr_write_b32 a17, v81\n"
      "v_accvgpr_write_b32 a18, v82\n"
      "v_accvgpr_write_b32 a19, v83\n"
      "v_mfma_f32_16x16x32_bf16 v[80:83], v[64:67], a[28:31], v[140:143]\n"
      "v_mfma_f32_16x16x32_bf16 v[84:87], v[68:71], a[28:31], v[140:143]\n"
      "v_cvt_pk_bf16_f32 v88, v88, v89\n"
      "v_cvt_pk_bf16_f32 v89, v90, v91\n"
      "v_cvt_pk_bf16_f32 v90, v92, v93\n"
      "v_cvt_pk_bf16_f32 v91, v94, v95\n"
      "v_accvgpr_write_b32 a20, v88\n"
      "v_accvgpr_write_b32 a21, v89\n"
      "v_accvgpr_write_b32 a22, v90\n"
      "v_accvgpr_write_b32 a23, v91\n"
      "v_mfma_f32_16x16x32_bf16 v[88:91], v[64:67], a[32:35], v[140:143]\n"
      "v_mfma_f32_16x16x32_bf16 v[92:95], v[68:71], a[32:35], v[140:143]\n"
      "v_cvt_pk_bf16_f32 v72, v72, v73\n"
      "v_cvt_pk_bf16_f32 v73, v74, v75\n"
      "v_cvt_pk_bf16_f32 v74, v76, v77\n"
      "v_cvt_pk_bf16_f32 v75, v78, v79\n"
      "v_accvgpr_write_b32 a24, v72\n"
      "v_accvgpr_write_b32 a25, v73\n"
      "v_accvgpr_write_b32 a26, v74\n"
      "v_accvgpr_write_b32 a27, v75\n"
      "v_mfma_f32_16x16x32_bf16 v[72:75], v[64:67], a[36:39], v[140:143]\n"
      "v_mfma_f32_16x16x32_bf16 v[76:79], v[68:71], a[36:39], v[140:143]\n"
      "v_cvt_pk_bf16_f32 v80, v80, v81\n"
      "v_cvt_pk_bf16_f32 v81, v82, v83\n"
      "v_cvt_pk_bf16_f32 v82, v84, v85\n"
      "v_cvt_pk_bf16_f32 v83, v86, v87\n"
      "v_accvgpr_write_b32 a28, v80\n"
      "v_accvgpr_write_b32 a29, v81\n"
      "v_accvgpr_write_b32 a30, v82\n"
      "v_accvgpr_write_b32 a31, v83\n"
      "v_mfma_f32_16x16x32_bf16 v[80:83], v[64:67], a[40:43], v[140:143]\n"
      "v_mfma_f32_16x16x32_bf16 v[84:87], v[68:71], a[40:43], v[140:143]\n"
      "v_cvt_pk_bf16_f32 v88, v88, v89\n"
      "v_cvt_pk_bf16_f32 v89, v90, v91\n"
      "v_cvt_pk_bf16_f32 v90, v92, v93\n"
      "v_cvt_pk_bf16_f32 v91, v94, v95\n"
      "v_accvgpr_write_b32 a32, v88\n"
      "v_accvgpr_write_b32 a33, v89\n"
      "v_accvgpr_write_b32 a34, v90\n"
      "v_accvgpr_write_b32 a35, v91\n"
      "v_mfma_f32_16x16x32_bf16 v[88:91], v[64:67], a[44:47], v[140:143]\n"
      "v_mfma_f32_16x16x32_bf16 v[92:95], v[68:71], a[44:47], v[140:143]\n"
      "v_cvt_pk_bf16_f32 v72, v72, v73\n"
      "v_cvt_pk_bf16_f32 v73, v74, v75\n"
      "v_cvt_pk_bf16_f32 v74, v76, v77\n"
      "v_cvt_pk_bf16_f32 v75, v78, v79\n"
      "v_accvgpr_write_b32 a36, v72\n"
      "v_accvgpr_write_b32 a37, v73\n"
      "v_accvgpr_write_b32 a38, v74\n"
      "v_accvgpr_write_b32 a39, v75\n"
      "v_mfma_f32_16x16x32_bf16 v[72:75], v[64:67], a[48:51], v[140:143]\n"
      "v_mfma_f32_16x16x32_bf16 v[76:79], v[68:71], a[48:51], v[140:143]\n"
      "v_cvt_pk_bf16_f32 v80, v80, v81\n"
      "v_cvt_pk_bf16_f32 v81, v82, v83\n"
      "v_cvt_pk_bf16_f32 v82, v84, v85\n"
      "v_cvt_pk_bf16_f32 v83, v86, v87\n"
      "v_accvgpr_write_b32 a40, v80\n"
      "v_accvgpr_write_b32 a41, v81\n"
      "v_accvgpr_write_b32 a42, v82\n"
      "v_accvgpr_write_b32 a43, v83\n"
      "v_mfma_f32_16x16x32_bf16 v[80:83], v[64:67], a[52:55], v[140:143]\n"
      "v_mfma_f32_16x16x32_bf16 v[84:87], v[68:71], a[52:55], v[140:143]\n"
      "v_cvt_pk_bf16_f32 v88, v88, v89\n"
      "v_cvt_pk_bf16_f32 v89, v90, v91\n"
      "v_cvt_pk_bf16_f32 v90, v92, v93\n"
      "v_cvt_pk_bf16_f32 v91, v94, v95\n"
      "v_accvgpr_write_b32 a44, v88\n"
      "v_accvgpr_write_b32 a45, v89\n"
      "v_accvgpr_write_b32 a46, v90\n"
      "v_accvgpr_write_b32 a47, v91\n"
      "v_mfma_f32_16x16x32_bf16 v[88:91], v[64:67], a[56:59], v[140:143]\n"
      "v_mfma_f32_16x16x32_bf16 v[92:95], v[68:71], a[56:59], v[140:143]\n"
      "v_cvt_pk_bf16_f32 v72, v72, v73\n"
      "v_cvt_pk_bf16_f32 v73, v74, v75\n"
      "v_cvt_pk_bf16_f32 v74, v76, v77\n"
      "v_cvt_pk_bf16_f32 v75, v78, v79\n"
      "v_accvgpr_write_b32 a48, v72\n"
      "v_accvgpr_write_b32 a49, v73\n"
      "v_accvgpr_write_b32 a50, v74\n"
      "v_accvgpr_write_b32 a51, v75\n"
      "v_mfma_f32_16x16x32_bf16 v[72:75], v[64:67], a[60:63], v[140:143]\n"
      "v_mfma_f32_16x16x32_bf16 v[76:79], v[68:71], a[60:63], v[140:143]\n"
      "v_cvt_pk_bf16_f32 v80, v80, v81\n"
      "v_cvt_pk_bf16_f32 v81, v82, v83\n"
      "v_cvt_pk_bf16_f32 v82, v84, v85\n"
      "v_cvt_pk_bf16_f32 v83, v86, v87\n"
      "v_accvgpr_write_b32 a52, v80\n"
      "v_accvgpr_write_b32 a53, v81\n"
      "v_accvgpr_write_b32 a54, v82\n"
      "v_accvgpr_write_b32 a55, v83\n"
      "v_mfma_f32_16x16x32_bf16 v[80:83], v[64:67], a[64:67], v[140:143]\n"
      "v_mfma_f32_16x16x32_bf16 v[84:87], v[68:71], a[64:67], v[140:143]\n"
      "v_cvt_pk_bf16_f32 v88, v88, v89\n"
      "v_cvt_pk_bf16_f32 v89, v90, v91\n"
      "v_cvt_pk_bf16_f32 v90, v92, v93\n"
      "v_cvt_pk_bf16_f32 v91, v94, v95\n"
      "v_accvgpr_write_b32 a56, v88\n"
      "v_accvgpr_write_b32 a57, v89\n"
      "v_accvgpr_write_b32 a58, v90\n"
      "v_accvgpr_write_b32 a59, v91\n"
      "v_mfma_f32_16x16x32_bf16 v[88:91], v[64:67], a[68:71], v[140:143]\n"
      "v_mfma_f32_16x16x32_bf16 v[92:95], v[68:71], a[68:71], v[140:143]\n"
      "v_cvt_pk_bf16_f32 v72, v72, v73\n"
      "v_cvt_pk_bf16_f32 v73, v74, v75\n"
      "v_cvt_pk_bf16_f32 v74, v76, v77\n"
      "v_cvt_pk_bf16_f32 v75, v78, v79\n"
      "v_accvgpr_write_b32 a60, v72\n"
      "v_accvgpr_write_b32 a61, v73\n"
      "v_accvgpr_write_b32 a62, v74\n"
      "v_accvgpr_write_b32 a63, v75\n"
      "v_mfma_f32_16x16x32_bf16 v[72:75], v[64:67], a[72:75], v[144:147]\n"
      "v_mfma_f32_16x16x32_bf16 v[76:79], v[68:71], a[72:75], v[144:147]\n"
      "v_cvt_pk_bf16_f32 v80, v80, v81\n"
      "v_cvt_pk_bf16_f32 v81, v82, v83\n"
      "v_cvt_pk_bf16_f32 v82, v84, v85\n"
      "v_cvt_pk_bf16_f32 v83, v86, v87\n"
      "v_accvgpr_write_b32 a64, v80\n"
      "v_accvgpr_write_b32 a65, v81\n"
      "v_accvgpr_write_b32 a66, v82\n"
      "v_accvgpr_write_b32 a67, v83\n"
      "v_mfma_f32_16x16x32_bf16 v[80:83], v[64:67], a[76:79], v[144:147]\n"
      "v_mfma_f32_16x16x32_bf16 v[84:87], v[68:71], a[76:79], v[144:147]\n"
      "v_cvt_pk_bf16_f32 v88, v88, v89\n"
      "v_cvt_pk_bf16_f32 v89, v90, v91\n"
      "v_cvt_pk_bf16_f32 v90, v92, v93\n"
      "v_cvt_pk_bf16_f32 v91, v94, v95\n"
      "v_accvgpr_write_b32 a68, v88\n"
      "v_accvgpr_write_b32 a69, v89\n"
      "v_accvgpr_write_b32 a70, v90\n"
      "v_accvgpr_write_b32 a71, v91\n"
      "v_mfma_f32_16x16x32_bf16 v[88:91], v[64:67], a[80:83], v[144:147]\n"
      "v_mfma_f32_16x16x32_bf16 v[92:95], v[68:71], a[80:83], v[144:147]\n"
      "v_cvt_pk_bf16_f32 v72, v72, v73\n"
      "v_cvt_pk_bf16_f32 v73, v74, v75\n"
      "v_cvt_pk_bf16_f32 v74, v76, v77\n"
      "v_cvt_pk_bf16_f32 v75, v78, v79\n"
      "v_accvgpr_write_b32 a72, v72\n"
      "v_accvgpr_write_b32 a73, v73\n"
      "v_accvgpr_write_b32 a74, v74\n"
      "v_accvgpr_write_b32 a75, v75\n"
      "v_mfma_f32_16x16x32_bf16 v[72:75], v[64:67], a[84:87], v[144:147]\n"
      "v_mfma_f32_16x16x32_bf16 v[76:79], v[68:71], a[84:87], v[144:147]\n"
      "v_cvt_pk_bf16_f32 v80, v80, v81\n"
      "v_cvt_pk_bf16_f32 v81, v82, v83\n"
      "v_cvt_pk_bf16_f32 v82, v84, v85\n"
      "v_cvt_pk_bf16_f32 v83, v86, v87\n"
      "v_accvgpr_write_b32 a76, v80\n"
      "v_accvgpr_write_b32 a77, v81\n"
      "v_accvgpr_write_b32 a78, v82\n"
      "v_accvgpr_write_b32 a79, v83\n"
      "v_mfma_f32_16x16x32_bf16 v[80:83], v[64:67], a[88:91], v[144:147]\n"
      "v_mfma_f32_16x16x32_bf16 v[84:87], v[68:71], a[88:91], v[144:147]\n"
      "v_cvt_pk_bf16_f32 v88, v88, v89\n"
      "v_cvt_pk_bf16_f32 v89, v90, v91\n"
      "v_cvt_pk_bf16_f32 v90, v92, v93\n"
      "v_cvt_pk_bf16_f32 v91, v94, v95\n"
      "v_accvgpr_write_b32 a80, v88\n"
      "v_accvgpr_write_b32 a81, v89\n"
      "v_accvgpr_write_b32 a82, v90\n"
      "v_accvgpr_write_b32 a83, v91\n"
      "v_mfma_f32_16x16x32_bf16 v[88:91], v[64:67], a[92:95], v[144:147]\n"
      "v_mfma_f32_16x16x32_bf16 v[92:95], v[68:71], a[92:95], v[144:147]\n"
      "v_cvt_pk_bf16_f32 v72, v72, v73\n"
      "v_cvt_pk_bf16_f32 v73, v74, v75\n"
      "v_cvt_pk_bf16_f32 v74, v76, v77\n"
      "v_cvt_pk_bf16_f32 v75, v78, v79\n"
      "v_accvgpr_write_b32 a84, v72\n"
      "v_accvgpr_write_b32 a85, v73\n"
      "v_accvgpr_write_b32 a86, v74\n"
      "v_accvgpr_write_b32 a87, v75\n"
      "v_mfma_f32_16x16x32_bf16 v[72:75], v[64:67], a[96:99], v[144:147]\n"
      "v_mfma_f32_16x16x32_bf16 v[76:79], v[68:71], a[96:99], v[144:147]\n"
      "v_cvt_pk_bf16_f32 v80, v80, v81\n"
      "v_cvt_pk_bf16_f32 v81, v82, v83\n"
      "v_cvt_pk_bf16_f32 v82, v84, v85\n"
      "v_cvt_pk_bf16_f32 v83, v86, v87\n"
      "v_accvgpr_write_b32 a88, v80\n"
      "v_accvgpr_write_b32 a89, v81\n"
      "v_accvgpr_write_b32 a90, v82\n"
      "v_accvgpr_write_b32 a91, v83\n"
      "v_mfma_f32_16x16x32_bf16 v[80:83], v[64:67], a[100:103], v[144:147]\n"
      "v_mfma_f32_16x16x32_bf16 v[84:87], v[68:71], a[100:103], v[144:147]\n"
      "v_cvt_pk_bf16_f32 v88, v88, v89\n"
      "v_cvt_pk_bf16_f32 v89, v90, v91\n"
      "v_cvt_pk_bf16_f32 v90, v92, v93\n"
      "v_cvt_pk_bf16_f32 v91, v94, v95\n"
      "v_accvgpr_write_b32 a92, v88\n"
      "v_accvgpr_write_b32 a93, v89\n"
      "v_accvgpr_write_b32 a94, v90\n"
      "v_accvgpr_write_b32 a95, v91\n"
      "v_mfma_f32_16x16x32_bf16 v[88:91], v[64:67], a[104:107], v[144:147]\n"
      "v_mfma_f32_16x16x32_bf16 v[92:95], v[68:71], a[104:107], v[144:147]\n"
      "v_cvt_pk_bf16_f32 v72, v72, v73\n"
      "v_cvt_pk_bf16_f32 v73, v74, v75\n"
      "v_cvt_pk_bf16_f32 v74, v76, v77\n"
      "v_cvt_pk_bf16_f32 v75, v78, v79\n"
      "v_accvgpr_write_b32 a96, v72\n"
      "v_accvgpr_write_b32 a97, v73\n"
      "v_accvgpr_write_b32 a98, v74\n"
      "v_accvgpr_write_b32 a99, v75\n"
      "v_mfma_f32_16x16x32_bf16 v[72:75], v[64:67], a[108:111], v[144:147]\n"
      "v_mfma_f32_16x16x32_bf16 v[76:79], v[68:71], a[108:111], v[144:147]\n"
      "v_cvt_pk_bf16_f32 v80, v80, v81\n"
      "v_cvt_pk_bf16_f32 v81, v82, v83\n"
      "v_cvt_pk_bf16_f32 v82, v84, v85\n"
      "v_cvt_pk_bf16_f32 v83, v86, v87\n"
      "v_accvgpr_write_b32 a100, v80\n"
      "v_accvgpr_write_b32 a101, v81\n"
      "v_accvgpr_write_b32 a102, v82\n"
      "v_accvgpr_write_b32 a103, v83\n"
      "v_mfma_f32_16x16x32_bf16 v[80:83], v[64:67], a[112:115], v[144:147]\n"
      "v_mfma_f32_16x16x32_bf16 v[84:87], v[68:71], a[112:115], v[144:147]\n"
      "v_cvt_pk_bf16_f32 v88, v88, v89\n"
      "v_cvt_pk_bf16_f32 v89, v90, v91\n"
      "v_cvt_pk_bf16_f32 v90, v92, v93\n"
      "v_cvt_pk_bf16_f32 v91, v94, v95\n"
      "v_accvgpr_write_b32 a104, v88\n"
      "v_accvgpr_write_b32 a105, v89\n"
      "v_accvgpr_write_b32 a106, v90\n"
      "v_accvgpr_write_b32 a107, v91\n"
      "v_mfma_f32_16x16x32_bf16 v[88:91], v[64:67], a[116:119], v[144:147]\n"
      "v_mfma_f32_16x16x32_bf16 v[92:95], v[68:71], a[116:119], v[144:147]\n"
      "v_cvt_pk_bf16_f32 v72, v72, v73\n"
      "v_cvt_pk_bf16_f32 v73, v74, v75\n"
      "v_cvt_pk_bf16_f32 v74, v76, v77\n"
      "v_cvt_pk_bf16_f32 v75, v78, v79\n"
      "v_accvgpr_write_b32 a108, v72\n"
      "v_accvgpr_write_b32 a109, v73\n"
      "v_accvgpr_write_b32 a110, v74\n"
      "v_accvgpr_write_b32 a111, v75\n"
      "v_mfma_f32_16x16x32_bf16 v[72:75], v[64:67], a[120:123], v[144:147]\n"
      "v_mfma_f32_16x16x32_bf16 v[76:79], v[68:71], a[120:123], v[144:147]\n"
      "v_cvt_pk_bf16_f32 v80, v80, v81\n"
      "v_cvt_pk_bf16_f32 v81, v82, v83\n"
      "v_cvt_pk_bf16_f32 v82, v84, v85\n"
      "v_cvt_pk_bf16_f32 v83, v86, v87\n"
      "v_accvgpr_write_b32 a112, v80\n"
      "v_accvgpr_write_b32 a113, v81\n"
      "v_accvgpr_write_b32 a114, v82\n"
      "v_accvgpr_write_b32 a115, v83\n"
      "v_mfma_f32_16x16x32_bf16 v[80:83], v[64:67], a[124:127], v[144:147]\n"
      "v_mfma_f32_16x16x32_bf16 v[84:87], v[68:71], a[124:127], v[144:147]\n"
      "v_cvt_pk_bf16_f32 v88, v88, v89\n"
      "v_cvt_pk_bf16_f32 v89, v90, v91\n"
      "v_cvt_pk_bf16_f32 v90, v92, v93\n"
      "v_cvt_pk_bf16_f32 v91, v94, v95\n"
      "v_accvgpr_write_b32 a116, v88\n"
      "v_accvgpr_write_b32 a117, v89\n"
      "v_accvgpr_write_b32 a118, v90\n"
      "v_accvgpr_write_b32 a119, v91\n"
      "v_mfma_f32_16x16x32_bf16 v[88:91], v[64:67], a[128:131], v[144:147]\n"
      "v_mfma_f32_16x16x32_bf16 v[92:95], v[68:71], a[128:131], v[144:147]\n"
      "v_cvt_pk_bf16_f32 v72, v72, v73\n"
      "v_cvt_pk_bf16_f32 v73, v74, v75\n"
      "v_cvt_pk_bf16_f32 v74, v76, v77\n"
      "v_cvt_pk_bf16_f32 v75, v78, v79\n"
      "v_accvgpr_write_b32 a120, v72\n"
      "v_accvgpr_write_b32 a121, v73\n"
      "v_accvgpr_write_b32 a122, v74\n"
      "v_accvgpr_write_b32 a123, v75\n"
      "v_mfma_f32_16x16x32_bf16 v[72:75], v[64:67], a[132:135], v[144:147]\n"
      "v_mfma_f32_16x16x32_bf16 v[76:79], v[68:71], a[132:135], v[144:147]\n"
      "v_cvt_pk_bf16_f32 v80, v80, v81\n"
      "v_cvt_pk_bf16_f32 v81, v82, v83\n"
      "v_cvt_pk_bf16_f32 v82, v84, v85\n"
      "v_cvt_pk_bf16_f32 v83, v86, v87\n"
      "v_accvgpr_write_b32 a124, v80\n"
      "v_accvgpr_write_b32 a125, v81\n"
      "v_accvgpr_write_b32 a126, v82\n"
      "v_accvgpr_write_b32 a127, v83\n"
      "v_mfma_f32_16x16x32_bf16 v[80:83], v[64:67], a[136:139], v[144:147]\n"
      "v_mfma_f32_16x16x32_bf16 v[84:87], v[68:71], a[136:139], v[144:147]\n"
      "v_cvt_pk_bf16_f32 v88, v88, v89\n"
      "v_cvt_pk_bf16_f32 v89, v90, v91\n"
      "v_cvt_pk_bf16_f32 v90, v92, v93\n"
      "v_cvt_pk_bf16_f32 v91, v94, v95\n"
      "v_accvgpr_write_b32 a128, v88\n"
      "v_accvgpr_write_b32 a129, v89\n"
      "v_accvgpr_write_b32 a130, v90\n"
      "v_accvgpr_write_b32 a131, v91\n"
      "v_mfma_f32_16x16x32_bf16 v[88:91], v[64:67], a[140:143], v[144:147]\n"
      "v_mfma_f32_16x16x32_bf16 v[92:95], v[68:71], a[140:143], v[144:147]\n"
      "v_cvt_pk_bf16_f32 v72, v72, v73\n"
      "v_cvt_pk_bf16_f32 v73, v74, v75\n"
      "v_cvt_pk_bf16_f32 v74, v76, v77\n"
      "v_cvt_pk_bf16_f32 v75, v78, v79\n"
      "v_accvgpr_write_b32 a132, v72\n"
      "v_accvgpr_write_b32 a133, v73\n"
      "v_accvgpr_write_b32 a134, v74\n"
      "v_accvgpr_write_b32 a135, v75\n"
      "v_mfma_f32_16x16x32_bf16 v[72:75], v[64:67], a[144:147], v[148:151]\n"
      "v_mfma_f32_16x16x32_bf16 v[76:79], v[68:71], a[144:147], v[148:151]\n"
      "v_cvt_pk_bf16_f32 v80, v80, v81\n"
      "v_cvt_pk_bf16_f32 v81, v82, v83\n"
      "v_cvt_pk_bf16_f32 v82, v84, v85\n"
      "v_cvt_pk_bf16_f32 v83, v86, v87\n"
      "v_accvgpr_write_b32 a136, v80\n"
      "v_accvgpr_write_b32 a137, v81\n"
      "v_accvgpr_write_b32 a138, v82\n"
      "v_accvgpr_write_b32 a139, v83\n"
      "v_mfma_f32_16x16x32_bf16 v[80:83], v[64:67], a[148:151], v[148:151]\n"
      "v_mfma_f32_16x16x32_bf16 v[84:87], v[68:71], a[148:151], v[148:151]\n"
      "v_cvt_pk_bf16_f32 v88, v88, v89\n"
      "v_cvt_pk_bf16_f32 v89, v90, v91\n"
      "v_cvt_pk_bf16_f32 v90, v92, v93\n"
      "v_cvt_pk_bf16_f32 v91, v94, v95\n"
      "v_accvgpr_write_b32 a140, v88\n"
      "v_accvgpr_write_b32 a141, v89\n"
      "v_accvgpr_write_b32 a142, v90\n"
      "v_accvgpr_write_b32 a143, v91\n"
      "v_mfma_f32_16x16x32_bf16 v[88:91], v[64:67], a[152:155], v[148:151]\n"
      "v_mfma_f32_16x16x32_bf16 v[92:95], v[68:71], a[152:155], v[148:151]\n"
      "v_cvt_pk_bf16_f32 v72, v72, v73\n"
      "v_cvt_pk_bf16_f32 v73, v74, v75\n"
      "v_cvt_pk_bf16_f32 v74, v76, v77\n"
      "v_cvt_pk_bf16_f32 v75, v78, v79\n"
      "v_accvgpr_write_b32 a144, v72\n"
      "v_accvgpr_write_b32 a145, v73\n"
      "v_accvgpr_write_b32 a146, v74\n"
      "v_accvgpr_write_b32 a147, v75\n"
      "v_mfma_f32_16x16x32_bf16 v[72:75], v[64:67], a[156:159], v[148:151]\n"
      "v_mfma_f32_16x16x32_bf16 v[76:79], v[68:71], a[156:159], v[148:151]\n"
      "v_cvt_pk_bf16_f32 v80, v80, v81\n"
      "v_cvt_pk_bf16_f32 v81, v82, v83\n"
      "v_cvt_pk_bf16_f32 v82, v84, v85\n"
      "v_cvt_pk_bf16_f32 v83, v86, v87\n"
      "v_accvgpr_write_b32 a148, v80\n"
      "v_accvgpr_write_b32 a149, v81\n"
      "v_accvgpr_write_b32 a150, v82\n"
      "v_accvgpr_write_b32 a151, v83\n"
      "v_mfma_f32_16x16x32_bf16 v[80:83], v[64:67], a[160:163], v[148:151]\n"
      "v_mfma_f32_16x16x32_bf16 v[84:87], v[68:71], a[160:163], v[148:151]\n"
      "v_cvt_pk_bf16_f32 v88, v88, v89\n"
      "v_cvt_pk_bf16_f32 v89, v90, v91\n"
      "v_cvt_pk_bf16_f32 v90, v92, v93\n"
      "v_cvt_pk_bf16_f32 v91, v94, v95\n"
      "v_accvgpr_write_b32 a152, v88\n"
      "v_accvgpr_write_b32 a153, v89\n"
      "v_accvgpr_write_b32 a154, v90\n"
      "v_accvgpr_write_b32 a155, v91\n"
      "v_mfma_f32_16x16x32_bf16 v[88:91], v[64:67], a[164:167], v[148:151]\n"
      "v_mfma_f32_16x16x32_bf16 v[92:95], v[68:71], a[164:167], v[148:151]\n"
      "v_cvt_pk_bf16_f32 v72, v72, v73\n"
      "v_cvt_pk_bf16_f32 v73, v74, v75\n"
      "v_cvt_pk_bf16_f32 v74, v76, v77\n"
      "v_cvt_pk_bf16_f32 v75, v78, v79\n"
      "v_accvgpr_write_b32 a156, v72\n"
      "v_accvgpr_write_b32 a157, v73\n"
      "v_accvgpr_write_b32 a158, v74\n"
      "v_accvgpr_write_b32 a159, v75\n"
      "v_mfma_f32_16x16x32_bf16 v[72:75], v[64:67], a[168:171], v[148:151]\n"
      "v_mfma_f32_16x16x32_bf16 v[76:79], v[68:71], a[168:171], v[148:151]\n"
      "v_cvt_pk_bf16_f32 v80, v80, v81\n"
      "v_cvt_pk_bf16_f32 v81, v82, v83\n"
      "v_cvt_pk_bf16_f32 v82, v84, v85\n"
      "v_cvt_pk_bf16_f32 v83, v86, v87\n"
      "v_accvgpr_write_b32 a160, v80\n"
      "v_accvgpr_write_b32 a161, v81\n"
      "v_accvgpr_write_b32 a162, v82\n"
      "v_accvgpr_write_b32 a163, v83\n"
      "v_mfma_f32_16x16x32_bf16 v[80:83], v[64:67], a[172:175], v[148:151]\n"
      "v_mfma_f32_16x16x32_bf16 v[84:87], v[68:71], a[172:175], v[148:151]\n"
      "v_cvt_pk_bf16_f32 v88, v88, v89\n"
      "v_cvt_pk_bf16_f32 v89, v90, v91\n"
      "v_cvt_pk_bf16_f32 v90, v92, v93\n"
      "v_cvt_pk_bf16_f32 v91, v94, v95\n"
      "v_accvgpr_write_b32 a164, v88\n"
      "v_accvgpr_write_b32 a165, v89\n"
      "v_accvgpr_write_b32 a166, v90\n"
      "v_accvgpr_write_b32 a167, v91\n"
      "v_mfma_f32_16x16x32_bf16 v[88:91], v[64:67], a[176:179], v[148:151]\n"
      "v_mfma_f32_16x16x32_bf16 v[92:95], v[68:71], a[176:179], v[148:151]\n"
      "v_cvt_pk_bf16_f32 v72, v72, v73\n"
      "v_cvt_pk_bf16_f32 v73, v74, v75\n"
      "v_cvt_pk_bf16_f32 v74, v76, v77\n"
      "v_cvt_pk_bf16_f32 v75, v78, v79\n"
      "v_accvgpr_write_b32 a168, v72\n"
      "v_accvgpr_write_b32 a169, v73\n"
      "v_accvgpr_write_b32 a170, v74\n"
      "v_accvgpr_write_b32 a171, v75\n"
      "v_mfma_f32_16x16x32_bf16 v[72:75], v[64:67], a[180:183], v[148:151]\n"
      "v_mfma_f32_16x16x32_bf16 v[76:79], v[68:71], a[180:183], v[148:151]\n"
      "v_cvt_pk_bf16_f32 v80, v80, v81\n"
      "v_cvt_pk_bf16_f32 v81, v82, v83\n"
      "v_cvt_pk_bf16_f32 v82, v84, v85\n"
      "v_cvt_pk_bf16_f32 v83, v86, v87\n"
      "v_accvgpr_write_b32 a172, v80\n"
      "v_accvgpr_write_b32 a173, v81\n"
      "v_accvgpr_write_b32 a174, v82\n"
      "v_accvgpr_write_b32 a175, v83\n"
      "v_mfma_f32_16x16x32_bf16 v[80:83], v[64:67], a[184:187], v[148:151]\n"
      "v_mfma_f32_16x16x32_bf16 v[84:87], v[68:71], a[184:187], v[148:151]\n"
      "v_cvt_pk_bf16_f32 v88, v88, v89\n"
      "v_cvt_pk_bf16_f32 v89, v90, v91\n"
      "v_cvt_pk_bf16_f32 v90, v92, v93\n"
      "v_cvt_pk_bf16_f32 v91, v94, v95\n"
      "v_accvgpr_write_b32 a176, v88\n"
      "v_accvgpr_write_b32 a177, v89\n"
      "v_accvgpr_write_b32 a178, v90\n"
      "v_accvgpr_write_b32 a179, v91\n"
      "v_mfma_f32_16x16x32_bf16 v[88:91], v[64:67], a[188:191], v[148:151]\n"
      "v_mfma_f32_16x16x32_bf16 v[92:95], v[68:71], a[188:191], v[148:151]\n"
      "v_cvt_pk_bf16_f32 v72, v72, v73\n"
      "v_cvt_pk_bf16_f32 v73, v74, v75\n"
      "v_cvt_pk_bf16_f32 v74, v76, v77\n"
      "v_cvt_pk_bf16_f32 v75, v78, v79\n"
      "v_accvgpr_write_b32 a180, v72\n"
      "v_accvgpr_write_b32 a181, v73\n"
      "v_accvgpr_write_b32 a182, v74\n"
      "v_accvgpr_write_b32 a183, v75\n"
      "v_mfma_f32_16x16x32_bf16 v[72:75], v[64:67], a[192:195], v[148:151]\n"
      "v_mfma_f32_16x16x32_bf16 v[76:79], v[68:71], a[192:195], v[148:151]\n"
      "v_cvt_pk_bf16_f32 v80, v80, v81\n"
      "v_cvt_pk_bf16_f32 v81, v82, v83\n"
      "v_cvt_pk_bf16_f32 v82, v84, v85\n"
      "v_cvt_pk_bf16_f32 v83, v86, v87\n"
      "v_accvgpr_write_b32 a184, v80\n"
      "v_accvgpr_write_b32 a185, v81\n"
      "v_accvgpr_write_b32 a186, v82\n"
      "v_accvgpr_write_b32 a187, v83\n"
      "v_mfma_f32_16x16x32_bf16 v[80:83], v[64:67], a[196:199], v[148:151]\n"
      "v_mfma_f32_16x16x32_bf16 v[84:87], v[68:71], a[196:199], v[148:151]\n"
      "v_cvt_pk_bf16_f32 v88, v88, v89\n"
      "v_cvt_pk_bf16_f32 v89, v90, v91\n"
      "v_cvt_pk_bf16_f32 v90, v92, v93\n"
      "v_cvt_pk_bf16_f32 v91, v94, v95\n"
      "v_accvgpr_write_b32 a188, v88\n"
      "v_accvgpr_write_b32 a189, v89\n"
      "v_accvgpr_write_b32 a190, v90\n"
      "v_accvgpr_write_b32 a191, v91\n"
      "v_mfma_f32_16x16x32_bf16 v[88:91], v[64:67], a[200:203], v[148:151]\n"
      "v_mfma_f32_16x16x32_bf16 v[92:95], v[68:71], a[200:203], v[148:151]\n"
      "v_cvt_pk_bf16_f32 v72, v72, v73\n"
      "v_cvt_pk_bf16_f32 v73, v74, v75\n"
      "v_cvt_pk_bf16_f32 v74, v76, v77\n"
      "v_cvt_pk_bf16_f32 v75, v78, v79\n"
      "v_accvgpr_write_b32 a192, v72\n"
      "v_accvgpr_write_b32 a193, v73\n"
      "v_accvgpr_write_b32 a194, v74\n"
      "v_accvgpr_write_b32 a195, v75\n"
      "v_mfma_f32_16x16x32_bf16 v[72:75], v[64:67], a[204:207], v[148:151]\n"
      "v_mfma_f32_16x16x32_bf16 v[76:79], v[68:71], a[204:207], v[148:151]\n"
      "v_cvt_pk_bf16_f32 v80, v80, v81\n"
      "v_cvt_pk_bf16_f32 v81, v82, v83\n"
      "v_cvt_pk_bf16_f32 v82, v84, v85\n"
      "v_cvt_pk_bf16_f32 v83, v86, v87\n"
      "v_accvgpr_write_b32 a196, v80\n"
      "v_accvgpr_write_b32 a197, v81\n"
      "v_accvgpr_write_b32 a198, v82\n"
      "v_accvgpr_write_b32 a199, v83\n"
      "v_mfma_f32_16x16x32_bf16 v[80:83], v[64:67], a[208:211], v[148:151]\n"
      "v_mfma_f32_16x16x32_bf16 v[84:87], v[68:71], a[208:211], v[148:151]\n"
      "v_cvt_pk_bf16_f32 v88, v88, v89\n"
      "v_cvt_pk_bf16_f32 v89, v90, v91\n"
      "v_cvt_pk_bf16_f32 v90, v92, v93\n"
      "v_cvt_pk_bf16_f32 v91, v94, v95\n"
      "v_accvgpr_write_b32 a200, v88\n"
      "v_accvgpr_write_b32 a201, v89\n"
      "v_accvgpr_write_b32 a202, v90\n"
      "v_accvgpr_write_b32 a203, v91\n"
      "v_mfma_f32_16x16x32_bf16 v[88:91], v[64:67], a[212:215], v[148:151]\n"
      "v_mfma_f32_16x16x32_bf16 v[92:95], v[68:71], a[212:215], v[148:151]\n"
      "v_cvt_pk_bf16_f32 v72, v72, v73\n"
      "v_cvt_pk_bf16_f32 v73, v74, v75\n"
      "v_cvt_pk_bf16_f32 v74, v76, v77\n"
      "v_cvt_pk_bf16_f32 v75, v78, v79\n"
      "v_accvgpr_write_b32 a204, v72\n"
      "v_accvgpr_write_b32 a205, v73\n"
      "v_accvgpr_write_b32 a206, v74\n"
      "v_accvgpr_write_b32 a207, v75\n"
      "v_mfma_f32_16x16x32_bf16 v[72:75], v[64:67], a[216:219], v[152:155]\n"
      "v_mfma_f32_16x16x32_bf16 v[76:79], v[68:71], a[216:219], v[152:155]\n"
      "v_cvt_pk_bf16_f32 v80, v80, v81\n"
      "v_cvt_pk_bf16_f32 v81, v82, v83\n"
      "v_cvt_pk_bf16_f32 v82, v84, v85\n"
      "v_cvt_pk_bf16_f32 v83, v86, v87\n"
      "v_accvgpr_write_b32 a208, v80\n"
      "v_accvgpr_write_b32 a209, v81\n"
      "v_accvgpr_write_b32 a210, v82\n"
      "v_accvgpr_write_b32 a211, v83\n"
      "v_mfma_f32_16x16x32_bf16 v[80:83], v[64:67], a[220:223], v[152:155]\n"
      "v_mfma_f32_16x16x32_bf16 v[84:87], v[68:71], a[220:223], v[152:155]\n"
      "v_cvt_pk_bf16_f32 v88, v88, v89\n"
      "v_cvt_pk_bf16_f32 v89, v90, v91\n"
      "v_cvt_pk_bf16_f32 v90, v92, v93\n"
      "v_cvt_pk_bf16_f32 v91, v94, v95\n"
      "v_accvgpr_write_b32 a212, v88\n"
      "v_accvgpr_write_b32 a213, v89\n"
      "v_accvgpr_write_b32 a214, v90\n"
      "v_accvgpr_write_b32 a215, v91\n"
      "v_mfma_f32_16x16x32_bf16 v[88:91], v[64:67], a[224:227], v[152:155]\n"
      "v_mfma_f32_16x16x32_bf16 v[92:95], v[68:71], a[224:227], v[152:155]\n"
      "v_cvt_pk_bf16_f32 v72, v72, v73\n"
      "v_cvt_pk_bf16_f32 v73, v74, v75\n"
      "v_cvt_pk_bf16_f32 v74, v76, v77\n"
      "v_cvt_pk_bf16_f32 v75, v78, v79\n"
      "v_accvgpr_write_b32 a216, v72\n"
      "v_accvgpr_write_b32 a217, v73\n"
      "v_accvgpr_write_b32 a218, v74\n"
      "v_accvgpr_write_b32 a219, v75\n"
      "v_mfma_f32_16x16x32_bf16 v[72:75], v[64:67], a[228:231], v[152:155]\n"
      "v_mfma_f32_16x16x32_bf16 v[76:79], v[68:71], a[228:231], v[152:155]\n"
      "v_cvt_pk_bf16_f32 v80, v80, v81\n"
      "v_cvt_pk_bf16_f32 v81, v82, v83\n"
      "v_cvt_pk_bf16_f32 v82, v84, v85\n"
      "v_cvt_pk_bf16_f32 v83, v86, v87\n"
      "v_accvgpr_write_b32 a220, v80\n"
      "v_accvgpr_write_b32 a221, v81\n"
      "v_accvgpr_write_b32 a222, v82\n"
      "v_accvgpr_write_b32 a223, v83\n"
      "v_mfma_f32_16x16x32_bf16 v[80:83], v[64:67], a[232:235], v[152:155]\n"
      "v_mfma_f32_16x16x32_bf16 v[84:87], v[68:71], a[232:235], v[152:155]\n"
      "v_cvt_pk_bf16_f32 v88, v88, v89\n"
      "v_cvt_pk_bf16_f32 v89, v90, v91\n"
      "v_cvt_pk_bf16_f32 v90, v92, v93\n"
      "v_cvt_pk_bf16_f32 v91, v94, v95\n"
      "v_accvgpr_write_b32 a224, v88\n"
      "v_accvgpr_write_b32 a225, v89\n"
      "v_accvgpr_write_b32 a226, v90\n"
      "v_accvgpr_write_b32 a227, v91\n"
      "v_mfma_f32_16x16x32_bf16 v[88:91], v[64:67], a[236:239], v[152:155]\n"
      "v_mfma_f32_16x16x32_bf16 v[92:95], v[68:71], a[236:239], v[152:155]\n"
      "v_cvt_pk_bf16_f32 v72, v72, v73\n"
      "v_cvt_pk_bf16_f32 v73, v74, v75\n"
      "v_cvt_pk_bf16_f32 v74, v76, v77\n"
      "v_cvt_pk_bf16_f32 v75, v78, v79\n"
      "v_accvgpr_write_b32 a228, v72\n"
      "v_accvgpr_write_b32 a229, v73\n"
      "v_accvgpr_write_b32 a230, v74\n"
      "v_accvgpr_write_b32 a231, v75\n"
      "v_mfma_f32_16x16x32_bf16 v[72:75], v[64:67], a[240:243], v[152:155]\n"
      "v_mfma_f32_16x16x32_bf16 v[76:79], v[68:71], a[240:243], v[152:155]\n"
      "v_cvt_pk_bf16_f32 v80, v80, v81\n"
      "v_cvt_pk_bf16_f32 v81, v82, v83\n"
      "v_cvt_pk_bf16_f32 v82, v84, v85\n"
      "v_cvt_pk_bf16_f32 v83, v86, v87\n"
      "v_accvgpr_write_b32 a232, v80\n"
      "v_accvgpr_write_b32 a233, v81\n"
      "v_accvgpr_write_b32 a234, v82\n"
      "v_accvgpr_write_b32 a235, v83\n"
      "v_mfma_f32_16x16x32_bf16 v[80:83], v[64:67], a[244:247], v[152:155]\n"
      "v_mfma_f32_16x16x32_bf16 v[84:87], v[68:71], a[244:247], v[152:155]\n"
      "v_cvt_pk_bf16_f32 v88, v88, v89\n"
      "v_cvt_pk_bf16_f32 v89, v90, v91\n"
      "v_cvt_pk_bf16_f32 v90, v92, v93\n"
      "v_cvt_pk_bf16_f32 v91, v94, v95\n"
      "v_accvgpr_write_b32 a236, v88\n"
      "v_accvgpr_write_b32 a237, v89\n"
      "v_accvgpr_write_b32 a238, v90\n"
      "v_accvgpr_write_b32 a239, v91\n"
      "v_mfma_f32_16x16x32_bf16 v[88:91], v[64:67], a[248:251], v[152:155]\n"
      "v_mfma_f32_16x16x32_bf16 v[92:95], v[68:71], a[248:251], v[152:155]\n"
      "v_cvt_pk_bf16_f32 v72, v72, v73\n"
      "v_cvt_pk_bf16_f32 v73, v74, v75\n"
      "v_cvt_pk_bf16_f32 v74, v76, v77\n"
      "v_cvt_pk_bf16_f32 v75, v78, v79\n"
      "v_accvgpr_write_b32 a240, v72\n"
      "v_accvgpr_write_b32 a241, v73\n"
      "v_accvgpr_write_b32 a242, v74\n"
      "v_accvgpr_write_b32 a243, v75\n"
      "v_mfma_f32_16x16x32_bf16 v[72:75], v[64:67], a[252:255], v[152:155]\n"
      "v_mfma_f32_16x16x32_bf16 v[76:79], v[68:71], a[252:255], v[152:155]\n"
      "v_cvt_pk_bf16_f32 v80, v80, v81\n"
      "v_cvt_pk_bf16_f32 v81, v82, v83\n"
      "v_cvt_pk_bf16_f32 v82, v84, v85\n"
      "v_cvt_pk_bf16_f32 v83, v86, v87\n"
      "v_accvgpr_write_b32 a244, v80\n"
      "v_accvgpr_write_b32 a245, v81\n"
      "v_accvgpr_write_b32 a246, v82\n"
      "v_accvgpr_write_b32 a247, v83\n"
      "v_mfma_f32_16x16x32_bf16 v[80:83], v[64:67], v[20:23], v[152:155]\n"
      "v_mfma_f32_16x16x32_bf16 v[84:87], v[68:71], v[20:23], v[152:155]\n"
      "v_cvt_pk_bf16_f32 v88, v88, v89\n"
      "v_cvt_pk_bf16_f32 v89, v90, v91\n"
      "v_cvt_pk_bf16_f32 v90, v92, v93\n"
      "v_cvt_pk_bf16_f32 v91, v94, v95\n"
      "v_accvgpr_write_b32 a248, v88\n"
      "v_accvgpr_write_b32 a249, v89\n"
      "v_accvgpr_write_b32 a250, v90\n"
      "v_accvgpr_write_b32 a251, v91\n"
      "v_mfma_f32_16x16x32_bf16 v[88:91], v[64:67], v[24:27], v[152:155]\n"
      "v_mfma_f32_16x16x32_bf16 v[92:95], v[68:71], v[24:27], v[152:155]\n"
      "v_cvt_pk_bf16_f32 v72, v72, v73\n"
      "v_cvt_pk_bf16_f32 v73, v74, v75\n"
      "v_cvt_pk_bf16_f32 v74, v76, v77\n"
      "v_cvt_pk_bf16_f32 v75, v78, v79\n"
      "v_accvgpr_write_b32 a252, v72\n"
      "v_accvgpr_write_b32 a253, v73\n"
      "v_accvgpr_write_b32 a254, v74\n"
      "v_accvgpr_write_b32 a255, v75\n"
      "v_mfma_f32_16x16x32_bf16 v[72:75], v[64:67], v[28:31], v[152:155]\n"
      "v_mfma_f32_16x16x32_bf16 v[76:79], v[68:71], v[28:31], v[152:155]\n"
      "v_cvt_pk_bf16_f32 v20, v80, v81\n"
      "v_cvt_pk_bf16_f32 v21, v82, v83\n"
      "v_cvt_pk_bf16_f32 v22, v84, v85\n"
      "v_cvt_pk_bf16_f32 v23, v86, v87\n"
      "v_mfma_f32_16x16x32_bf16 v[80:83], v[64:67], v[32:35], v[152:155]\n"
      "v_mfma_f32_16x16x32_bf16 v[84:87], v[68:71], v[32:35], v[152:155]\n"
      "v_cvt_pk_bf16_f32 v24, v88, v89\n"
      "v_cvt_pk_bf16_f32 v25, v90, v91\n"
      "v_cvt_pk_bf16_f32 v26, v92, v93\n"
      "v_cvt_pk_bf16_f32 v27, v94, v95\n"
      "v_mfma_f32_16x16x32_bf16 v[88:91], v[64:67], v[36:39], v[152:155]\n"
      "v_mfma_f32_16x16x32_bf16 v[92:95], v[68:71], v[36:39], v[152:155]\n"
      "v_cvt_pk_bf16_f32 v28, v72, v73\n"
      "v_cvt_pk_bf16_f32 v29, v74, v75\n"
      "v_cvt_pk_bf16_f32 v30, v76, v77\n"
      "v_cvt_pk_bf16_f32 v31, v78, v79\n"
      "v_mfma_f32_16x16x32_bf16 v[72:75], v[64:67], v[40:43], v[152:155]\n"
      "v_mfma_f32_16x16x32_bf16 v[76:79], v[68:71], v[40:43], v[152:155]\n"
      "v_cvt_pk_bf16_f32 v32, v80, v81\n"
      "v_cvt_pk_bf16_f32 v33, v82, v83\n"
      "v_cvt_pk_bf16_f32 v34, v84, v85\n"
      "v_cvt_pk_bf16_f32 v35, v86, v87\n"
      "v_mfma_f32_16x16x32_bf16 v[80:83], v[64:67], v[44:47], v[152:155]\n"
      "v_mfma_f32_16x16x32_bf16 v[84:87], v[68:71], v[44:47], v[152:155]\n"
      "v_cvt_pk_bf16_f32 v36, v88, v89\n"
      "v_cvt_pk_bf16_f32 v37, v90, v91\n"
      "v_cvt_pk_bf16_f32 v38, v92, v93\n"
      "v_cvt_pk_bf16_f32 v39, v94, v95\n"
      "v_mfma_f32_16x16x32_bf16 v[88:91], v[64:67], v[48:51], v[152:155]\n"
      "v_mfma_f32_16x16x32_bf16 v[92:95], v[68:71], v[48:51], v[152:155]\n"
      "v_cvt_pk_bf16_f32 v40, v72, v73\n"
      "v_cvt_pk_bf16_f32 v41, v74, v75\n"
      "v_cvt_pk_bf16_f32 v42, v76, v77\n"
      "v_cvt_pk_bf16_f32 v43, v78, v79\n"
      "v_cvt_pk_bf16_f32 v44, v80, v81\n"
      "v_cvt_pk_bf16_f32 v45, v82, v83\n"
      "v_cvt_pk_bf16_f32 v46, v84, v85\n"
      "v_cvt_pk_bf16_f32 v47, v86, v87\n"
      "v_cvt_pk_bf16_f32 v48, v88, v89\n"
      "v_cvt_pk_bf16_f32 v49, v90, v91\n"
      "v_cvt_pk_bf16_f32 v50, v92, v93\n"
      "v_cvt_pk_bf16_f32 v51, v94, v95\n"
      "v_add_u32 v17, 40960, v0\n"
      "s_barrier\n"
      "v_mov_b32 v11, 0\n"
      "ds_write_b32 v13, v11\n"
      "s_waitcnt lgkmcnt(0)\n"
      "s_barrier\n"
      "s_add_u32 s6, s36, 1548288\n"
      "s_add_u32 s36, s36, 4294942720\n"
      "s_add_u32 s42, s37, 4294635520\n"
      "s_add_u32 s37, s37, 4294801408\n"
      "s_add_u32 s36, s36, 24576\n"
      "s_min_u32 s36, s36, s6\n"
      "buffer_load_dword v18, v2, s[8:11], s36 offen sc1\n"
      "s_add_u32 m0, s38, 0\n"
      "s_mov_b32 s40, s36\n"
      "s_waitcnt vmcnt(0)\n"
      "v_cmp_ne_u32 vcc, s21, v18\n"
      "s_add_u32 s36, s36, 24576\n"
      "s_cbranch_vccnz .Lsp_g0_1\n"
      ".Lbk_g0_1:\n"
      "buffer_load_dwordx4 v0, s[8:11], s40 offen offset:0 sc1 lds\n"
      "buffer_load_dwordx4 v0, s[8:11], s40 offen offset:1024 sc1 lds\n"
      "buffer_load_dwordx4 v0, s[8:11], s40 offen offset:2048 sc1 lds\n"
      "buffer_load_dwordx4 v0, s[8:11], s40 offen offset:3072 sc1 lds\n"
      "s_add_u32 m0, s39, 0\n"
      "s_nop 0\n"
      "buffer_load_dwordx4 v1, s[8:11], s40 offen offset:0 sc1 lds\n"
      "s_min_u32 s36, s36, s6\n"
      "buffer_load_dword v18, v2, s[8:11], s36 offen sc1\n"
      "v_add_u32 v11, 1, v11\n"
      "s_add_u32 m0, s38, 20480\n"
      "s_mov_b32 s40, s36\n"
      "s_waitcnt vmcnt(1)\n"
      "ds_write_b32 v13, v11\n"
      "s_waitcnt vmcnt(0)\n"
      "v_cmp_ne_u32 vcc, s21, v18\n"
      "s_add_u32 s36, s36, 24576\n"
      "s_cbranch_vccnz .Lsp_g0_2\n"
      ".Lbk_g0_2:\n"
      "buffer_load_dwordx4 v0, s[8:11], s40 offen offset:0 sc1 lds\n"
      "ds_read_b32 v12, v14\n"
      "buffer_load_dwordx4 v0, s[8:11], s40 offen offset:1024 sc1 lds\n"
      "s_waitcnt lgkmcnt(0)\n"
      "v_cmp_gt_i32 vcc, v11, v12\n"
      "buffer_load_dwordx4 v0, s[8:11], s40 offen offset:2048 sc1 lds\n"
      "s_cbranch_vccnz .Lsp_gy0_3\n"
      ".Lbk_gy0_3:\n"
      "ds_read_b128 v[52:55], v0 offset:0\n"
      "ds_read_b128 v[60:63], v0 offset:2048\n"
      "buffer_load_dwordx4 v0, s[8:11], s40 offen offset:3072 sc1 lds\n"
      "s_add_u32 m0, s39, 20480\n"
      "ds_read_b128 v[68:71], v0 offset:4096\n"
      "ds_read2st64_b64 v[168:171], v4 offset0:0 offset1:1\n"
      "ds_read_b128 v[56:59], v0 offset:1024\n"
      "buffer_load_dwordx4 v1, s[8:11], s40 offen offset:0 sc1 lds\n"
      "ds_read_b128 v[64:67], v0 offset:3072\n"
      "s_min_u32 s36, s36, s6\n"
      "buffer_load_dword v18, v2, s[8:11], s36 offen sc1\n"
      "ds_read_b128 v[72:75], v0 offset:5120\n"
      "ds_read_b128 v[76:79], v0 offset:6144\n"
      "buffer_load_dword v16, v13, s[8:11], s31 offen\n"
      "ds_read_b128 v[80:83], v0 offset:7168\n"
      "ds_read_b128 v[92:95], v0 offset:10240\n"
      "s_mov_b32 s20, 15\n"
      "s_waitcnt lgkmcnt(0)\n"
      "s_nop 4\n"
      "s_branch .Lg_loop\n"
      ".p2align 12\n"
      ".Lg_p0:\n"
      "s_branch .Lg_pr\n"
      ".p2align 5\n"
      ".Lg_loop:\n"
      "v_mfma_f32_16x16x32_bf16 v[124:127], a[0:3], v[52:55], 0\n"
      "ds_read_b128 v[84:87], v0 offset:8192\n"
      "ds_read_b128 v[104:107], v0 offset:13312\n"
      "v_mfma_f32_16x16x32_bf16 v[124:127], a[4:7], v[56:59], v[124:127]\n"
      "ds_read_b128 v[88:91], v0 offset:9216\n"
      "v_mul_f32 v130, v130, v158\n"
      "v_mfma_f32_16x16x32_bf16 v[124:127], a[8:11], v[60:63], v[124:127]\n"
      "s_waitcnt lgkmcnt(7)\n"
      "ds_read_b128 v[108:111], v0 offset:14336\n"
      "v_add_f32 v169, v169, v171\n"
      "v_mfma_f32_16x16x32_bf16 v[124:127], a[12:15], v[64:67], v[124:127]\n"
      "ds_read_b128 v[96:99], v0 offset:11264\n"
      "v_fma_f32 v161, v161, v177, v177\n"
      "v_mfma_f32_16x16x32_bf16 v[124:127], a[16:19], v[68:71], v[124:127]\n"
      "s_waitcnt lgkmcnt(7)\n"
      "ds_read_b128 v[100:103], v0 offset:12288\n"
      "v_mul_f32 v128, v128, v156\n"
      "v_mfma_f32_16x16x32_bf16 v[124:127], a[20:23], v[72:75], v[124:127]\n"
      "v_rcp_f32 v161, v161\n"
      "v_mfma_f32_16x16x32_bf16 v[124:127], a[24:27], v[76:79], v[124:127]\n"
      "s_waitcnt lgkmcnt(6)\n"
      "ds_read_b128 v[112:115], v0 offset:15360\n"
      "v_mul_f32 v129, v129, v157\n"
      "v_mfma_f32_16x16x32_bf16 v[124:127], a[28:31], v[80:83], v[124:127]\n"
      "s_waitcnt lgkmcnt(5)\n"
      "ds_read_b128 v[120:123], v0 offset:17408\n"
      "v_mul_f32 v131, v131, v159\n"
      "v_mfma_f32_16x16x32_bf16 v[124:127], a[32:35], v[84:87], v[124:127]\n"
      "s_waitcnt lgkmcnt(5)\n"
      "ds_read_b128 v[116:119], v0 offset:16384\n"
      "v_cvt_pk_bf16_f32 v165, v130, v131\n"
      "v_mfma_f32_16x16x32_bf16 v[124:127], a[36:39], v[88:91], v[124:127]\n"
      "v_cvt_pk_bf16_f32 v164, v128, v129\n"
      "v_mfma_f32_16x16x32_bf16 v[124:127], a[40:43], v[92:95], v[124:127]\n"
      "s_waitcnt lgkmcnt(4)\n"
      "v_fma_f32 v160, v160, v177, v177\n"
      "v_sub_f32 v176, -v168, v170\n"
      "v_mfma_f32_16x16x32_bf16 v[124:127], a[44:47], v[96:99], v[124:127]\n"
      "s_waitcnt lgkmcnt(3)\n"
      "v_rcp_f32 v160, v160\n"
      "v_mfma_f32_16x16x32_bf16 v[124:127], a[48:51], v[100:103], v[124:127]\n"
      "v_mul_f32 v161, v133, v161\n"
      "v_fma_f32 v169, -v176, v176, v169\n"
      "v_mfma_f32_16x16x32_bf16 v[124:127], a[52:55], v[104:107], v[124:127]\n"
      "v_max_f32 v177, 0x3727c5ac, v169\n"
      "v_mfma_f32_16x16x32_bf16 v[124:127], a[56:59], v[108:111], v[124:127]\n"
      "s_waitcnt lgkmcnt(1)\n"
      "v_mul_f32 v162, v134, v162\n"
      "v_mul_f32 v137, v137, v161\n"
      "v_mfma_f32_16x16x32_bf16 v[124:127], a[60:63], v[112:115], v[124:127]\n"
      "s_waitcnt lgkmcnt(0)\n"
      "v_mul_f32 v138, v138, v162\n"
      "v_mul_f32 v139, v139, v163\n"
      "v_mfma_f32_16x16x32_bf16 v[124:127], a[64:67], v[116:119], v[124:127]\n"
      "v_mul_f32 v160, v132, v160\n"
      "v_cvt_pk_bf16_f32 v167, v138, v139\n"
      "v_mfma_f32_16x16x32_bf16 v[124:127], a[68:71], v[120:123], v[124:127]\n"
      "v_mul_f32 v136, v136, v160\n"
      "v_cvt_pk_bf16_f32 v166, v136, v137\n"
      "v_mfma_f32_16x16x32_bf16 v[132:135], a[144:147], v[52:55], 0\n"
      "buffer_store_dwordx4 v[164:167], v0, s[8:11], s37 offen sc1\n"
      "v_mfma_f32_16x16x32_bf16 v[132:135], a[148:151], v[56:59], v[132:135]\n"
      "v_rsq_f32 v170, v177\n"
      "v_mfma_f32_16x16x32_bf16 v[132:135], a[152:155], v[60:63], v[132:135]\n"
      "v_mul_f32 v158, v126, v126\n"
      "v_mfma_f32_16x16x32_bf16 v[132:135], a[156:159], v[64:67], v[132:135]\n"
      "v_mul_f32 v178, 0xc0135761, v170\n"
      "v_mul_f32 v171, 0x3d372713, v178\n"
      "v_mfma_f32_16x16x32_bf16 v[132:135], a[160:163], v[68:71], v[132:135]\n"
      "v_mul_f32 v170, v170, v170\n"
      "v_mul_f32 v179, v171, v170\n"
      "v_mfma_f32_16x16x32_bf16 v[132:135], a[164:167], v[72:75], v[132:135]\n"
      "v_mul_f32 v159, v127, v127\n"
      "v_mfma_f32_16x16x32_bf16 v[132:135], a[168:171], v[76:79], v[132:135]\n"
      "v_mul_f32 v156, v124, v124\n"
      "v_fma_f32 v159, v159, v179, v178\n"
      "v_mfma_f32_16x16x32_bf16 v[132:135], a[172:175], v[80:83], v[132:135]\n"
      "v_mul_f32 v157, v125, v125\n"
      "v_fma_f32 v158, v158, v179, v178\n"
      "v_mfma_f32_16x16x32_bf16 v[132:135], a[176:179], v[84:87], v[132:135]\n"
      "s_add_u32 s42, s42, 165888\n"
      "v_add_u32 v11, 1, v11\n"
      "v_mul_f32 v158, v126, v158\n"
      "v_mfma_f32_16x16x32_bf16 v[132:135], a[180:183], v[88:91], v[132:135]\n"
      "v_fma_f32 v157, v157, v179, v178\n"
      "v_fma_f32 v156, v156, v179, v178\n"
      "v_mfma_f32_16x16x32_bf16 v[132:135], a[184:187], v[92:95], v[132:135]\n"
      "v_exp_f32 v158, v158\n"
      "v_mfma_f32_16x16x32_bf16 v[132:135], a[188:191], v[96:99], v[132:135]\n"
      "s_add_u32 m0, s38, 40960\n"
      "v_mfma_f32_16x16x32_bf16 v[132:135], a[192:195], v[100:103], v[132:135]\n"
      "s_mov_b32 s40, s36\n"
      "v_mul_f32 v156, v124, v156\n"
      "v_fma_f32 v158, v158, v177, v177\n"
      "v_mfma_f32_16x16x32_bf16 v[132:135], a[196:199], v[104:107], v[132:135]\n"
      "v_exp_f32 v156, v156\n"
      "v_mfma_f32_16x16x32_bf16 v[132:135], a[200:203], v[108:111], v[132:135]\n"
      "v_fma_f32 v156, v156, v177, v177\n"
      "v_mul_f32 v159, v127, v159\n"
      "v_mfma_f32_16x16x32_bf16 v[132:135], a[204:207], v[112:115], v[132:135]\n"
      "v_rcp_f32 v156, v156\n"
      "v_mfma_f32_16x16x32_bf16 v[132:135], a[208:211], v[116:119], v[132:135]\n"
      "v_exp_f32 v159, v159\n"
      "v_mfma_f32_16x16x32_bf16 v[132:135], a[212:215], v[120:123], v[132:135]\n"
      "v_fma_f32 v159, v159, v177, v177\n"
      "v_mfma_f32_16x16x32_bf16 v[128:131], a[72:75], v[52:55], 0\n"
      "s_waitcnt vmcnt(3)\n"
      "ds_write_b32 v13, v11\n"
      "v_mfma_f32_16x16x32_bf16 v[128:131], a[76:79], v[56:59], v[128:131]\n"
      "s_add_u32 s37, s37, 165888\n"
      "v_rcp_f32 v158, v158\n"
      "v_mfma_f32_16x16x32_bf16 v[128:131], a[80:83], v[60:63], v[128:131]\n"
      "v_mul_f32 v162, v134, v134\n"
      "v_mul_f32 v163, v135, v135\n"
      "v_mfma_f32_16x16x32_bf16 v[128:131], a[84:87], v[64:67], v[128:131]\n"
      "v_rcp_f32 v159, v159\n"
      "v_mfma_f32_16x16x32_bf16 v[128:131], a[88:91], v[68:71], v[128:131]\n"
      "s_waitcnt vmcnt(2)\n"
      "v_cmp_ne_u32 vcc, s21, v18\n"
      "v_fma_f32 v163, v163, v179, v178\n"
      "v_mfma_f32_16x16x32_bf16 v[128:131], a[92:95], v[72:75], v[128:131]\n"
      "v_fma_f32 v162, v162, v179, v178\n"
      "v_mul_f32 v162, v134, v162\n"
      "v_mfma_f32_16x16x32_bf16 v[128:131], a[96:99], v[76:79], v[128:131]\n"
      "s_add_u32 s36, s36, 24576\n"
      "v_mul_f32 v161, v133, v133\n"
      "v_mul_f32 v159, v127, v159\n"
      "v_mfma_f32_16x16x32_bf16 v[128:131], a[100:103], v[80:83], v[128:131]\n"
      "v_exp_f32 v162, v162\n"
      "v_mfma_f32_16x16x32_bf16 v[128:131], a[104:107], v[84:87], v[128:131]\n"
      "s_cbranch_vccnz .Lsp_g0_4\n"
      ".Lbk_g0_4:\n"
      "v_mul_f32 v160, v132, v132\n"
      "v_mfma_f32_16x16x32_bf16 v[128:131], a[108:111], v[88:91], v[128:131]\n"
      "buffer_load_dwordx4 v0, s[8:11], s40 offen offset:0 sc1 lds\n"
      "v_mfma_f32_16x16x32_bf16 v[128:131], a[112:115], v[92:95], v[128:131]\n"
      "ds_read_b32 v12, v14\n"
      "v_fma_f32 v161, v161, v179, v178\n"
      "v_mfma_f32_16x16x32_bf16 v[128:131], a[116:119], v[96:99], v[128:131]\n"
      "v_mul_f32 v156, v124, v156\n"
      "v_mul_f32 v161, v133, v161\n"
      "v_mfma_f32_16x16x32_bf16 v[128:131], a[120:123], v[100:103], v[128:131]\n"
      "v_fma_f32 v160, v160, v179, v178\n"
      "v_mul_f32 v163, v135, v163\n"
      "v_mfma_f32_16x16x32_bf16 v[128:131], a[124:127], v[104:107], v[128:131]\n"
      "buffer_load_dwordx4 v0, s[8:11], s40 offen offset:1024 sc1 lds\n"
      "v_mfma_f32_16x16x32_bf16 v[128:131], a[128:131], v[108:111], v[128:131]\n"
      "v_exp_f32 v163, v163\n"
      "v_mfma_f32_16x16x32_bf16 v[128:131], a[132:135], v[112:115], v[128:131]\n"
      "v_mul_f32 v157, v125, v157\n"
      "v_fma_f32 v163, v163, v177, v177\n"
      "v_mfma_f32_16x16x32_bf16 v[128:131], a[136:139], v[116:119], v[128:131]\n"
      "s_waitcnt lgkmcnt(0)\n"
      "v_cmp_gt_i32 vcc, v11, v12\n"
      "v_mul_f32 v160, v132, v160\n"
      "v_mfma_f32_16x16x32_bf16 v[128:131], a[140:143], v[120:123], v[128:131]\n"
      "buffer_load_dwordx4 v0, s[8:11], s40 offen offset:2048 sc1 lds\n"
      "v_mfma_f32_16x16x32_bf16 v[136:139], a[216:219], v[52:55], 0\n"
      "v_rcp_f32 v163, v163\n"
      "v_mfma_f32_16x16x32_bf16 v[136:139], a[220:223], v[56:59], v[136:139]\n"
      "s_cbranch_vccnz .Lsp_gy0_5\n"
      ".Lbk_gy0_5:\n"
      "ds_read_b128 v[52:55], v0 offset:20480\n"
      "v_mfma_f32_16x16x32_bf16 v[136:139], a[224:227], v[60:63], v[136:139]\n"
      "v_exp_f32 v157, v157\n"
      "v_mfma_f32_16x16x32_bf16 v[136:139], a[228:231], v[64:67], v[136:139]\n"
      "ds_read_b128 v[60:63], v0 offset:22528\n"
      "v_mul_f32 v163, v135, v163\n"
      "v_mfma_f32_16x16x32_bf16 v[136:139], a[232:235], v[68:71], v[136:139]\n"
      "buffer_load_dwordx4 v0, s[8:11], s40 offen offset:3072 sc1 lds\n"
      "v_mfma_f32_16x16x32_bf16 v[136:139], a[236:239], v[72:75], v[136:139]\n"
      "v_exp_f32 v161, v161\n"
      "v_mfma_f32_16x16x32_bf16 v[136:139], a[240:243], v[76:79], v[136:139]\n"
      "s_add_u32 m0, s39, 40960\n"
      "ds_read_b128 v[68:71], v0 offset:24576\n"
      "ds_read2st64_b64 v[168:171], v4 offset0:40 offset1:41\n"
      "v_mfma_f32_16x16x32_bf16 v[136:139], a[244:247], v[80:83], v[136:139]\n"
      "ds_read_b128 v[56:59], v0 offset:21504\n"
      "v_fma_f32 v162, v162, v177, v177\n"
      "v_mfma_f32_16x16x32_bf16 v[136:139], a[248:251], v[84:87], v[136:139]\n"
      "buffer_load_dwordx4 v1, s[8:11], s40 offen offset:0 sc1 lds\n"
      "v_mfma_f32_16x16x32_bf16 v[136:139], a[252:255], v[88:91], v[136:139]\n"
      "ds_read_b128 v[64:67], v0 offset:23552\n"
      "v_fma_f32 v157, v157, v177, v177\n"
      "v_mfma_f32_16x16x32_bf16 v[136:139], v[20:23], v[92:95], v[136:139]\n"
      "s_min_u32 s36, s36, s6\n"
      "v_rcp_f32 v162, v162\n"
      "v_mfma_f32_16x16x32_bf16 v[136:139], v[24:27], v[96:99], v[136:139]\n"
      "v_exp_f32 v160, v160\n"
      "v_mfma_f32_16x16x32_bf16 v[136:139], v[28:31], v[100:103], v[136:139]\n"
      "v_rcp_f32 v157, v157\n"
      "v_mfma_f32_16x16x32_bf16 v[136:139], v[32:35], v[104:107], v[136:139]\n"
      "buffer_load_dword v18, v2, s[8:11], s36 offen sc1\n"
      "v_mfma_f32_16x16x32_bf16 v[136:139], v[36:39], v[108:111], v[136:139]\n"
      "ds_read_b128 v[72:75], v0 offset:25600\n"
      "ds_read_b128 v[76:79], v0 offset:26624\n"
      "v_mfma_f32_16x16x32_bf16 v[136:139], v[40:43], v[112:115], v[136:139]\n"
      "s_waitcnt vmcnt(6) lgkmcnt(3)\n"
      "buffer_store_dword v15, v3, s[8:11], s42 offen sc1\n"
      "v_mfma_f32_16x16x32_bf16 v[136:139], v[44:47], v[116:119], v[136:139]\n"
      "ds_read_b128 v[80:83], v0 offset:27648\n"
      "v_mul_f32 v158, v126, v158\n"
      "v_mfma_f32_16x16x32_bf16 v[136:139], v[48:51], v[120:123], v[136:139]\n"
      "ds_read_b128 v[92:95], v0 offset:30720\n"
      "v_mul_f32 v157, v125, v157\n"
      "v_mfma_f32_16x16x32_bf16 v[124:127], a[0:3], v[52:55], 0\n"
      "ds_read_b128 v[84:87], v0 offset:28672\n"
      "ds_read_b128 v[104:107], v0 offset:33792\n"
      "v_mfma_f32_16x16x32_bf16 v[124:127], a[4:7], v[56:59], v[124:127]\n"
      "ds_read_b128 v[88:91], v0 offset:29696\n"
      "v_mul_f32 v130, v130, v158\n"
      "v_mfma_f32_16x16x32_bf16 v[124:127], a[8:11], v[60:63], v[124:127]\n"
      "s_waitcnt lgkmcnt(7)\n"
      "ds_read_b128 v[108:111], v0 offset:34816\n"
      "v_add_f32 v169, v169, v171\n"
      "v_mfma_f32_16x16x32_bf16 v[124:127], a[12:15], v[64:67], v[124:127]\n"
      "ds_read_b128 v[96:99], v0 offset:31744\n"
      "v_fma_f32 v161, v161, v177, v177\n"
      "v_mfma_f32_16x16x32_bf16 v[124:127], a[16:19], v[68:71], v[124:127]\n"
      "s_waitcnt lgkmcnt(7)\n"
      "ds_read_b128 v[100:103], v0 offset:32768\n"
      "v_mul_f32 v128, v128, v156\n"
      "v_mfma_f32_16x16x32_bf16 v[124:127], a[20:23], v[72:75], v[124:127]\n"
      "v_rcp_f32 v161, v161\n"
      "v_mfma_f32_16x16x32_bf16 v[124:127], a[24:27], v[76:79], v[124:127]\n"
      "s_waitcnt lgkmcnt(6)\n"
      "ds_read_b128 v[112:115], v0 offset:35840\n"
      "v_mul_f32 v129, v129, v157\n"
      "v_mfma_f32_16x16x32_bf16 v[124:127], a[28:31], v[80:83], v[124:127]\n"
      "s_waitcnt lgkmcnt(5)\n"
      "ds_read_b128 v[120:123], v0 offset:37888\n"
      "v_mul_f32 v131, v131, v159\n"
      "v_mfma_f32_16x16x32_bf16 v[124:127], a[32:35], v[84:87], v[124:127]\n"
      "s_waitcnt lgkmcnt(5)\n"
      "ds_read_b128 v[116:119], v0 offset:36864\n"
      "v_cvt_pk_bf16_f32 v165, v130, v131\n"
      "v_mfma_f32_16x16x32_bf16 v[124:127], a[36:39], v[88:91], v[124:127]\n"
      "v_cvt_pk_bf16_f32 v164, v128, v129\n"
      "v_mfma_f32_16x16x32_bf16 v[124:127], a[40:43], v[92:95], v[124:127]\n"
      "s_waitcnt lgkmcnt(4)\n"
      "v_fma_f32 v160, v160, v177, v177\n"
      "v_sub_f32 v176, -v168, v170\n"
      "v_mfma_f32_16x16x32_bf16 v[124:127], a[44:47], v[96:99], v[124:127]\n"
      "s_waitcnt lgkmcnt(3)\n"
      "v_rcp_f32 v160, v160\n"
      "v_mfma_f32_16x16x32_bf16 v[124:127], a[48:51], v[100:103], v[124:127]\n"
      "v_mul_f32 v161, v133, v161\n"
      "v_fma_f32 v169, -v176, v176, v169\n"
      "v_mfma_f32_16x16x32_bf16 v[124:127], a[52:55], v[104:107], v[124:127]\n"
      "v_max_f32 v177, 0x3727c5ac, v169\n"
      "v_mfma_f32_16x16x32_bf16 v[124:127], a[56:59], v[108:111], v[124:127]\n"
      "s_waitcnt lgkmcnt(1)\n"
      "v_mul_f32 v162, v134, v162\n"
      "v_mul_f32 v137, v137, v161\n"
      "v_mfma_f32_16x16x32_bf16 v[124:127], a[60:63], v[112:115], v[124:127]\n"
      "s_waitcnt lgkmcnt(0)\n"
      "v_mul_f32 v138, v138, v162\n"
      "v_mul_f32 v139, v139, v163\n"
      "v_mfma_f32_16x16x32_bf16 v[124:127], a[64:67], v[116:119], v[124:127]\n"
      "v_mul_f32 v160, v132, v160\n"
      "v_cvt_pk_bf16_f32 v167, v138, v139\n"
      "v_mfma_f32_16x16x32_bf16 v[124:127], a[68:71], v[120:123], v[124:127]\n"
      "v_mul_f32 v136, v136, v160\n"
      "v_cvt_pk_bf16_f32 v166, v136, v137\n"
      "v_mfma_f32_16x16x32_bf16 v[132:135], a[144:147], v[52:55], 0\n"
      "buffer_store_dwordx4 v[164:167], v0, s[8:11], s37 offen sc1\n"
      "v_mfma_f32_16x16x32_bf16 v[132:135], a[148:151], v[56:59], v[132:135]\n"
      "v_rsq_f32 v170, v177\n"
      "v_mfma_f32_16x16x32_bf16 v[132:135], a[152:155], v[60:63], v[132:135]\n"
      "v_mul_f32 v158, v126, v126\n"
      "v_mfma_f32_16x16x32_bf16 v[132:135], a[156:159], v[64:67], v[132:135]\n"
      "v_mul_f32 v178, 0xc0135761, v170\n"
      "v_mul_f32 v171, 0x3d372713, v178\n"
      "v_mfma_f32_16x16x32_bf16 v[132:135], a[160:163], v[68:71], v[132:135]\n"
      "v_mul_f32 v170, v170, v170\n"
      "v_mul_f32 v179, v171, v170\n"
      "v_mfma_f32_16x16x32_bf16 v[132:135], a[164:167], v[72:75], v[132:135]\n"
      "v_mul_f32 v159, v127, v127\n"
      "v_mfma_f32_16x16x32_bf16 v[132:135], a[168:171], v[76:79], v[132:135]\n"
      "v_mul_f32 v156, v124, v124\n"
      "v_fma_f32 v159, v159, v179, v178\n"
      "v_mfma_f32_16x16x32_bf16 v[132:135], a[172:175], v[80:83], v[132:135]\n"
      "v_mul_f32 v157, v125, v125\n"
      "v_fma_f32 v158, v158, v179, v178\n"
      "v_mfma_f32_16x16x32_bf16 v[132:135], a[176:179], v[84:87], v[132:135]\n"
      "s_add_u32 s42, s42, 165888\n"
      "v_add_u32 v11, 1, v11\n"
      "v_mul_f32 v158, v126, v158\n"
      "v_mfma_f32_16x16x32_bf16 v[132:135], a[180:183], v[88:91], v[132:135]\n"
      "v_fma_f32 v157, v157, v179, v178\n"
      "v_fma_f32 v156, v156, v179, v178\n"
      "v_mfma_f32_16x16x32_bf16 v[132:135], a[184:187], v[92:95], v[132:135]\n"
      "v_exp_f32 v158, v158\n"
      "v_mfma_f32_16x16x32_bf16 v[132:135], a[188:191], v[96:99], v[132:135]\n"
      "s_add_u32 m0, s38, 61440\n"
      "v_mfma_f32_16x16x32_bf16 v[132:135], a[192:195], v[100:103], v[132:135]\n"
      "s_mov_b32 s40, s36\n"
      "v_mul_f32 v156, v124, v156\n"
      "v_fma_f32 v158, v158, v177, v177\n"
      "v_mfma_f32_16x16x32_bf16 v[132:135], a[196:199], v[104:107], v[132:135]\n"
      "v_exp_f32 v156, v156\n"
      "v_mfma_f32_16x16x32_bf16 v[132:135], a[200:203], v[108:111], v[132:135]\n"
      "v_fma_f32 v156, v156, v177, v177\n"
      "v_mul_f32 v159, v127, v159\n"
      "v_mfma_f32_16x16x32_bf16 v[132:135], a[204:207], v[112:115], v[132:135]\n"
      "v_rcp_f32 v156, v156\n"
      "v_mfma_f32_16x16x32_bf16 v[132:135], a[208:211], v[116:119], v[132:135]\n"
      "v_exp_f32 v159, v159\n"
      "v_mfma_f32_16x16x32_bf16 v[132:135], a[212:215], v[120:123], v[132:135]\n"
      "v_fma_f32 v159, v159, v177, v177\n"
      "v_mfma_f32_16x16x32_bf16 v[128:131], a[72:75], v[52:55], 0\n"
      "s_waitcnt vmcnt(3)\n"
      "ds_write_b32 v13, v11\n"
      "v_mfma_f32_16x16x32_bf16 v[128:131], a[76:79], v[56:59], v[128:131]\n"
      "s_add_u32 s37, s37, 165888\n"
      "v_rcp_f32 v158, v158\n"
      "v_mfma_f32_16x16x32_bf16 v[128:131], a[80:83], v[60:63], v[128:131]\n"
      "v_mul_f32 v162, v134, v134\n"
      "v_mul_f32 v163, v135, v135\n"
      "v_mfma_f32_16x16x32_bf16 v[128:131], a[84:87], v[64:67], v[128:131]\n"
      "v_rcp_f32 v159, v159\n"
      "v_mfma_f32_16x16x32_bf16 v[128:131], a[88:91], v[68:71], v[128:131]\n"
      "s_waitcnt vmcnt(2)\n"
      "v_cmp_ne_u32 vcc, s21, v18\n"
      "v_fma_f32 v163, v163, v179, v178\n"
      "v_mfma_f32_16x16x32_bf16 v[128:131], a[92:95], v[72:75], v[128:131]\n"
      "v_fma_f32 v162, v162, v179, v178\n"
      "v_mul_f32 v162, v134, v162\n"
      "v_mfma_f32_16x16x32_bf16 v[128:131], a[96:99], v[76:79], v[128:131]\n"
      "s_add_u32 s36, s36, 24576\n"
      "v_mul_f32 v161, v133, v133\n"
      "v_mul_f32 v159, v127, v159\n"
      "v_mfma_f32_16x16x32_bf16 v[128:131], a[100:103], v[80:83], v[128:131]\n"
      "v_exp_f32 v162, v162\n"
      "v_mfma_f32_16x16x32_bf16 v[128:131], a[104:107], v[84:87], v[128:131]\n"
      "s_cbranch_vccnz .Lsp_g0_6\n"
      ".Lbk_g0_6:\n"
      "v_mul_f32 v160, v132, v132\n"
      "v_mfma_f32_16x16x32_bf16 v[128:131], a[108:111], v[88:91], v[128:131]\n"
      "buffer_load_dwordx4 v0, s[8:11], s40 offen offset:0 sc1 lds\n"
      "v_mfma_f32_16x16x32_bf16 v[128:131], a[112:115], v[92:95], v[128:131]\n"
      "ds_read_b32 v12, v14\n"
      "v_fma_f32 v161, v161, v179, v178\n"
      "v_mfma_f32_16x16x32_bf16 v[128:131], a[116:119], v[96:99], v[128:131]\n"
      "v_mul_f32 v156, v124, v156\n"
      "v_mul_f32 v161, v133, v161\n"
      "v_mfma_f32_16x16x32_bf16 v[128:131], a[120:123], v[100:103], v[128:131]\n"
      "v_fma_f32 v160, v160, v179, v178\n"
      "v_mul_f32 v163, v135, v163\n"
      "v_mfma_f32_16x16x32_bf16 v[128:131], a[124:127], v[104:107], v[128:131]\n"
      "buffer_load_dwordx4 v0, s[8:11], s40 offen offset:1024 sc1 lds\n"
      "v_mfma_f32_16x16x32_bf16 v[128:131], a[128:131], v[108:111], v[128:131]\n"
      "v_exp_f32 v163, v163\n"
      "v_mfma_f32_16x16x32_bf16 v[128:131], a[132:135], v[112:115], v[128:131]\n"
      "v_mul_f32 v157, v125, v157\n"
      "v_fma_f32 v163, v163, v177, v177\n"
      "v_mfma_f32_16x16x32_bf16 v[128:131], a[136:139], v[116:119], v[128:131]\n"
      "s_waitcnt lgkmcnt(0)\n"
      "v_cmp_gt_i32 vcc, v11, v12\n"
      "v_mul_f32 v160, v132, v160\n"
      "v_mfma_f32_16x16x32_bf16 v[128:131], a[140:143], v[120:123], v[128:131]\n"
      "buffer_load_dwordx4 v0, s[8:11], s40 offen offset:2048 sc1 lds\n"
      "v_mfma_f32_16x16x32_bf16 v[136:139], a[216:219], v[52:55], 0\n"
      "v_rcp_f32_e64 v163, v163\n"
      "v_mfma_f32_16x16x32_bf16 v[136:139], a[220:223], v[56:59], v[136:139]\n"
      "s_cbranch_vccnz .Lsp_gy0_7\n"
      ".Lbk_gy0_7:\n"
      "ds_read_b128 v[52:55], v17 offset:0\n"
      "v_mfma_f32_16x16x32_bf16 v[136:139], a[224:227], v[60:63], v[136:139]\n"
      "v_exp_f32 v157, v157\n"
      "v_mfma_f32_16x16x32_bf16 v[136:139], a[228:231], v[64:67], v[136:139]\n"
      "ds_read_b128 v[60:63], v17 offset:2048\n"
      "v_mul_f32 v163, v135, v163\n"
      "v_mfma_f32_16x16x32_bf16 v[136:139], a[232:235], v[68:71], v[136:139]\n"
      "buffer_load_dwordx4 v0, s[8:11], s40 offen offset:3072 sc1 lds\n"
      "v_mfma_f32_16x16x32_bf16 v[136:139], a[236:239], v[72:75], v[136:139]\n"
      "v_exp_f32 v161, v161\n"
      "v_mfma_f32_16x16x32_bf16 v[136:139], a[240:243], v[76:79], v[136:139]\n"
      "s_add_u32 m0, s39, 61440\n"
      "ds_read_b128 v[68:71], v17 offset:4096\n"
      "ds_read2st64_b64 v[168:171], v4 offset0:80 offset1:81\n"
      "v_mfma_f32_16x16x32_bf16 v[136:139], a[244:247], v[80:83], v[136:139]\n"
      "ds_read_b128 v[56:59], v17 offset:1024\n"
      "v_fma_f32 v162, v162, v177, v177\n"
      "v_mfma_f32_16x16x32_bf16 v[136:139], a[248:251], v[84:87], v[136:139]\n"
      "buffer_load_dwordx4 v1, s[8:11], s40 offen offset:0 sc1 lds\n"
      "v_mfma_f32_16x16x32_bf16 v[136:139], a[252:255], v[88:91], v[136:139]\n"
      "ds_read_b128 v[64:67], v17 offset:3072\n"
      "v_fma_f32 v157, v157, v177, v177\n"
      "v_mfma_f32_16x16x32_bf16 v[136:139], v[20:23], v[92:95], v[136:139]\n"
      "s_min_u32 s36, s36, s6\n"
      "v_rcp_f32 v162, v162\n"
      "v_mfma_f32_16x16x32_bf16 v[136:139], v[24:27], v[96:99], v[136:139]\n"
      "v_exp_f32 v160, v160\n"
      "v_mfma_f32_16x16x32_bf16 v[136:139], v[28:31], v[100:103], v[136:139]\n"
      "v_rcp_f32 v157, v157\n"
      "v_mfma_f32_16x16x32_bf16 v[136:139], v[32:35], v[104:107], v[136:139]\n"
      "buffer_load_dword v18, v2, s[8:11], s36 offen sc1\n"
      "v_mfma_f32_16x16x32_bf16 v[136:139], v[36:39], v[108:111], v[136:139]\n"
      "ds_read_b128 v[72:75], v17 offset:5120\n"
      "ds_read_b128 v[76:79], v17 offset:6144\n"
      "v_mfma_f32_16x16x32_bf16 v[136:139], v[40:43], v[112:115], v[136:139]\n"
      "s_waitcnt vmcnt(6) lgkmcnt(3)\n"
      "buffer_store_dword v15, v3, s[8:11], s42 offen sc1\n"
      "v_mfma_f32_16x16x32_bf16 v[136:139], v[44:47], v[116:119], v[136:139]\n"
      "ds_read_b128 v[80:83], v17 offset:7168\n"
      "v_mul_f32 v158, v126, v158\n"
      "v_mfma_f32_16x16x32_bf16 v[136:139], v[48:51], v[120:123], v[136:139]\n"
      "ds_read_b128 v[92:95], v17 offset:10240\n"
      "v_mul_f32 v157, v125, v157\n"
      "v_mfma_f32_16x16x32_bf16 v[124:127], a[0:3], v[52:55], 0\n"
      "ds_read_b128 v[84:87], v17 offset:8192\n"
      "ds_read_b128 v[104:107], v17 offset:13312\n"
      "v_mfma_f32_16x16x32_bf16 v[124:127], a[4:7], v[56:59], v[124:127]\n"
      "ds_read_b128 v[88:91], v17 offset:9216\n"
      "v_mul_f32 v130, v130, v158\n"
      "v_mfma_f32_16x16x32_bf16 v[124:127], a[8:11], v[60:63], v[124:127]\n"
      "s_waitcnt lgkmcnt(7)\n"
      "ds_read_b128 v[108:111], v17 offset:14336\n"
      "v_add_f32 v169, v169, v171\n"
      "v_mfma_f32_16x16x32_bf16 v[124:127], a[12:15], v[64:67], v[124:127]\n"
      "ds_read_b128 v[96:99], v17 offset:11264\n"
      "v_fma_f32 v161, v161, v177, v177\n"
      "v_mfma_f32_16x16x32_bf16 v[124:127], a[16:19], v[68:71], v[124:127]\n"
      "s_waitcnt lgkmcnt(7)\n"
      "ds_read_b128 v[100:103], v17 offset:12288\n"
      "v_mul_f32 v128, v128, v156\n"
      "v_mfma_f32_16x16x32_bf16 v[124:127], a[20:23], v[72:75], v[124:127]\n"
      "v_rcp_f32 v161, v161\n"
      "v_mfma_f32_16x16x32_bf16 v[124:127], a[24:27], v[76:79], v[124:127]\n"
      "s_waitcnt lgkmcnt(6)\n"
      "ds_read_b128 v[112:115], v17 offset:15360\n"
      "v_mul_f32 v129, v129, v157\n"
      "v_mfma_f32_16x16x32_bf16 v[124:127], a[28:31], v[80:83], v[124:127]\n"
      "s_waitcnt lgkmcnt(5)\n"
      "ds_read_b128 v[120:123], v17 offset:17408\n"
      "v_mul_f32 v131, v131, v159\n"
      "v_mfma_f32_16x16x32_bf16 v[124:127], a[32:35], v[84:87], v[124:127]\n"
      "s_waitcnt lgkmcnt(5)\n"
      "ds_read_b128 v[116:119], v17 offset:16384\n"
      "v_cvt_pk_bf16_f32 v165, v130, v131\n"
      "v_mfma_f32_16x16x32_bf16 v[124:127], a[36:39], v[88:91], v[124:127]\n"
      "v_cvt_pk_bf16_f32 v164, v128, v129\n"
      "v_mfma_f32_16x16x32_bf16 v[124:127], a[40:43], v[92:95], v[124:127]\n"
      "s_waitcnt lgkmcnt(4)\n"
      "v_fma_f32 v160, v160, v177, v177\n"
      "v_sub_f32 v176, -v168, v170\n"
      "v_mfma_f32_16x16x32_bf16 v[124:127], a[44:47], v[96:99], v[124:127]\n"
      "s_waitcnt lgkmcnt(3)\n"
      "v_rcp_f32 v160, v160\n"
      "v_mfma_f32_16x16x32_bf16 v[124:127], a[48:51], v[100:103], v[124:127]\n"
      "v_mul_f32 v161, v133, v161\n"
      "v_fma_f32 v169, -v176, v176, v169\n"
      "v_mfma_f32_16x16x32_bf16 v[124:127], a[52:55], v[104:107], v[124:127]\n"
      "v_max_f32 v177, 0x3727c5ac, v169\n"
      "v_mfma_f32_16x16x32_bf16 v[124:127], a[56:59], v[108:111], v[124:127]\n"
      "s_waitcnt lgkmcnt(1)\n"
      "v_mul_f32 v162, v134, v162\n"
      "v_mul_f32 v137, v137, v161\n"
      "v_mfma_f32_16x16x32_bf16 v[124:127], a[60:63], v[112:115], v[124:127]\n"
      "s_waitcnt lgkmcnt(0)\n"
      "v_mul_f32 v138, v138, v162\n"
      "v_mul_f32 v139, v139, v163\n"
      "v_mfma_f32_16x16x32_bf16 v[124:127], a[64:67], v[116:119], v[124:127]\n"
      "v_mul_f32 v160, v132, v160\n"
      "v_cvt_pk_bf16_f32 v167, v138, v139\n"
      "v_mfma_f32_16x16x32_bf16 v[124:127], a[68:71], v[120:123], v[124:127]\n"
      "v_mul_f32 v136, v136, v160\n"
      "v_cvt_pk_bf16_f32 v166, v136, v137\n"
      "v_mfma_f32_16x16x32_bf16 v[132:135], a[144:147], v[52:55], 0\n"
      "buffer_store_dwordx4 v[164:167], v0, s[8:11], s37 offen sc1\n"
      "v_mfma_f32_16x16x32_bf16 v[132:135], a[148:151], v[56:59], v[132:135]\n"
      "v_rsq_f32 v170, v177\n"
      "v_mfma_f32_16x16x32_bf16 v[132:135], a[152:155], v[60:63], v[132:135]\n"
      "v_mul_f32 v158, v126, v126\n"
      "v_mfma_f32_16x16x32_bf16 v[132:135], a[156:159], v[64:67], v[132:135]\n"
      "v_mul_f32 v178, 0xc0135761, v170\n"
      "v_mul_f32 v171, 0x3d372713, v178\n"
      "v_mfma_f32_16x16x32_bf16 v[132:135], a[160:163], v[68:71], v[132:135]\n"
      "v_mul_f32 v170, v170, v170\n"
      "v_mul_f32 v179, v171, v170\n"
      "v_mfma_f32_16x16x32_bf16 v[132:135], a[164:167], v[72:75], v[132:135]\n"
      "v_mul_f32 v159, v127, v127\n"
      "v_mfma_f32_16x16x32_bf16 v[132:135], a[168:171], v[76:79], v[132:135]\n"
      "v_mul_f32 v156, v124, v124\n"
      "v_fma_f32 v159, v159, v179, v178\n"
      "v_mfma_f32_16x16x32_bf16 v[132:135], a[172:175], v[80:83], v[132:135]\n"
      "v_mul_f32 v157, v125, v125\n"
      "v_fma_f32 v158, v158, v179, v178\n"
      "v_mfma_f32_16x16x32_bf16 v[132:135], a[176:179], v[84:87], v[132:135]\n"
      "s_add_u32 s42, s42, 165888\n"
      "v_add_u32 v11, 1, v11\n"
      "v_mul_f32 v158, v126, v158\n"
      "v_mfma_f32_16x16x32_bf16 v[132:135], a[180:183], v[88:91], v[132:135]\n"
      "v_fma_f32 v157, v157, v179, v178\n"
      "v_fma_f32 v156, v156, v179, v178\n"
      "v_mfma_f32_16x16x32_bf16 v[132:135], a[184:187], v[92:95], v[132:135]\n"
      "v_exp_f32 v158, v158\n"
      "v_mfma_f32_16x16x32_bf16 v[132:135], a[188:191], v[96:99], v[132:135]\n"
      "s_add_u32 m0, s38, 0\n"
      "v_mfma_f32_16x16x32_bf16 v[132:135], a[192:195], v[100:103], v[132:135]\n"
      "s_mov_b32 s40, s36\n"
      "v_mul_f32 v156, v124, v156\n"
      "v_fma_f32 v158, v158, v177, v177\n"
      "v_mfma_f32_16x16x32_bf16 v[132:135], a[196:199], v[104:107], v[132:135]\n"
      "v_exp_f32 v156, v156\n"
      "v_mfma_f32_16x16x32_bf16 v[132:135], a[200:203], v[108:111], v[132:135]\n"
      "v_fma_f32 v156, v156, v177, v177\n"
      "v_mul_f32 v159, v127, v159\n"
      "v_mfma_f32_16x16x32_bf16 v[132:135], a[204:207], v[112:115], v[132:135]\n"
      "v_rcp_f32 v156, v156\n"
      "v_mfma_f32_16x16x32_bf16 v[132:135], a[208:211], v[116:119], v[132:135]\n"
      "v_exp_f32 v159, v159\n"
      "v_mfma_f32_16x16x32_bf16 v[132:135], a[212:215], v[120:123], v[132:135]\n"
      "v_fma_f32 v159, v159, v177, v177\n"
      "v_mfma_f32_16x16x32_bf16 v[128:131], a[72:75], v[52:55], 0\n"
      "s_waitcnt vmcnt(3)\n"
      "ds_write_b32 v13, v11\n"
      "v_mfma_f32_16x16x32_bf16 v[128:131], a[76:79], v[56:59], v[128:131]\n"
      "s_add_u32 s37, s37, 165888\n"
      "v_rcp_f32 v158, v158\n"
      "v_mfma_f32_16x16x32_bf16 v[128:131], a[80:83], v[60:63], v[128:131]\n"
      "v_mul_f32 v162, v134, v134\n"
      "v_mul_f32 v163, v135, v135\n"
      "v_mfma_f32_16x16x32_bf16 v[128:131], a[84:87], v[64:67], v[128:131]\n"
      "v_rcp_f32 v159, v159\n"
      "v_mfma_f32_16x16x32_bf16 v[128:131], a[88:91], v[68:71], v[128:131]\n"
      "s_waitcnt vmcnt(2)\n"
      "v_cmp_ne_u32 vcc, s21, v18\n"
      "v_fma_f32 v163, v163, v179, v178\n"
      "v_mfma_f32_16x16x32_bf16 v[128:131], a[92:95], v[72:75], v[128:131]\n"
      "v_fma_f32 v162, v162, v179, v178\n"
      "v_mul_f32 v162, v134, v162\n"
      "v_mfma_f32_16x16x32_bf16 v[128:131], a[96:99], v[76:79], v[128:131]\n"
      "s_add_u32 s36, s36, 24576\n"
      "v_mul_f32 v161, v133, v133\n"
      "v_mul_f32_e64 v159, v127, v159\n"
      "v_mfma_f32_16x16x32_bf16 v[128:131], a[100:103], v[80:83], v[128:131]\n"
      "v_exp_f32_e64 v162, v162\n"
      "v_mfma_f32_16x16x32_bf16 v[128:131], a[104:107], v[84:87], v[128:131]\n"
      "s_cbranch_vccnz .Lsp_g0_8\n"
      ".Lbk_g0_8:\n"
      "v_mul_f32 v160, v132, v132\n"
      "v_mfma_f32_16x16x32_bf16 v[128:131], a[108:111], v[88:91], v[128:131]\n"
      "buffer_load_dwordx4 v0, s[8:11], s40 offen offset:0 sc1 lds\n"
      "v_mfma_f32_16x16x32_bf16 v[128:131], a[112:115], v[92:95], v[128:131]\n"
      "ds_read_b32 v12, v14\n"
      "v_fma_f32 v161, v161, v179, v178\n"
      "v_mfma_f32_16x16x32_bf16 v[128:131], a[116:119], v[96:99], v[128:131]\n"
      "v_mul_f32 v156, v124, v156\n"
      "v_mul_f32 v161, v133, v161\n"
      "v_mfma_f32_16x16x32_bf16 v[128:131], a[120:123], v[100:103], v[128:131]\n"
      "v_fma_f32 v160, v160, v179, v178\n"
      "v_mul_f32 v163, v135, v163\n"
      "v_mfma_f32_16x16x32_bf16 v[128:131], a[124:127], v[104:107], v[128:131]\n"
      "buffer_load_dwordx4 v0, s[8:11], s40 offen offset:1024 sc1 lds\n"
      "v_mfma_f32_16x16x32_bf16 v[128:131], a[128:131], v[108:111], v[128:131]\n"
      "v_exp_f32 v163, v163\n"
      "v_mfma_f32_16x16x32_bf16 v[128:131], a[132:135], v[112:115], v[128:131]\n"
      "v_mul_f32 v157, v125, v157\n"
      "v_fma_f32 v163, v163, v177, v177\n"
      "v_mfma_f32_16x16x32_bf16 v[128:131], a[136:139], v[116:119], v[128:131]\n"
      "s_waitcnt lgkmcnt(0)\n"
      "v_cmp_gt_i32 vcc, v11, v12\n"
      "v_mul_f32 v160, v132, v160\n"
      "v_mfma_f32_16x16x32_bf16 v[128:131], a[140:143], v[120:123], v[128:131]\n"
      "buffer_load_dwordx4 v0, s[8:11], s40 offen offset:2048 sc1 lds\n"
      "v_mfma_f32_16x16x32_bf16 v[136:139], a[216:219], v[52:55], 0\n"
      "v_rcp_f32_e64 v163, v163\n"
      "v_mfma_f32_16x16x32_bf16 v[136:139], a[220:223], v[56:59], v[136:139]\n"
      "s_cbranch_vccnz .Lsp_gy0_9\n"
      ".Lbk_gy0_9:\n"
      "ds_read_b128 v[52:55], v17 offset:20480\n"
      "v_mfma_f32_16x16x32_bf16 v[136:139], a[224:227], v[60:63], v[136:139]\n"
      "v_exp_f32 v157, v157\n"
      "v_mfma_f32_16x16x32_bf16 v[136:139], a[228:231], v[64:67], v[136:139]\n"
      "ds_read_b128 v[60:63], v17 offset:22528\n"
      "v_mul_f32 v163, v135, v163\n"
      "v_mfma_f32_16x16x32_bf16 v[136:139], a[232:235], v[68:71], v[136:139]\n"
      "buffer_load_dwordx4 v0, s[8:11], s40 offen offset:3072 sc1 lds\n"
      "v_mfma_f32_16x16x32_bf16 v[136:139], a[236:239], v[72:75], v[136:139]\n"
      "v_exp_f32 v161, v161\n"
      "v_mfma_f32_16x16x32_bf16 v[136:139], a[240:243], v[76:79], v[136:139]\n"
      "s_add_u32 m0, s39, 0\n"
      "ds_read_b128 v[68:71], v17 offset:24576\n"
      "ds_read2st64_b64 v[168:171], v4 offset0:120 offset1:121\n"
      "v_mfma_f32_16x16x32_bf16 v[136:139], a[244:247], v[80:83], v[136:139]\n"
      "ds_read_b128 v[56:59], v17 offset:21504\n"
      "v_fma_f32 v162, v162, v177, v177\n"
      "v_mfma_f32_16x16x32_bf16 v[136:139], a[248:251], v[84:87], v[136:139]\n"
      "buffer_load_dwordx4 v1, s[8:11], s40 offen offset:0 sc1 lds\n"
      "v_mfma_f32_16x16x32_bf16 v[136:139], a[252:255], v[88:91], v[136:139]\n"
      "ds_read_b128 v[64:67], v17 offset:23552\n"
      "v_fma_f32 v157, v157, v177, v177\n"
      "v_mfma_f32_16x16x32_bf16 v[136:139], v[20:23], v[92:95], v[136:139]\n"
      "s_min_u32 s36, s36, s6\n"
      "v_rcp_f32 v162, v162\n"
      "v_mfma_f32_16x16x32_bf16 v[136:139], v[24:27], v[96:99], v[136:139]\n"
      "v_exp_f32 v160, v160\n"
      "v_mfma_f32_16x16x32_bf16 v[136:139], v[28:31], v[100:103], v[136:139]\n"
      "v_rcp_f32 v157, v157\n"
      "v_mfma_f32_16x16x32_bf16 v[136:139], v[32:35], v[104:107], v[136:139]\n"
      "buffer_load_dword v18, v2, s[8:11], s36 offen sc1\n"
      "v_mfma_f32_16x16x32_bf16 v[136:139], v[36:39], v[108:111], v[136:139]\n"
      "ds_read_b128 v[72:75], v17 offset:25600\n"
      "ds_read_b128 v[76:79], v17 offset:26624\n"
      "v_mfma_f32_16x16x32_bf16 v[136:139], v[40:43], v[112:115], v[136:139]\n"
      "s_waitcnt vmcnt(6) lgkmcnt(3)\n"
      "buffer_store_dword v15, v3, s[8:11], s42 offen sc1\n"
      "v_mfma_f32_16x16x32_bf16 v[136:139], v[44:47], v[116:119], v[136:139]\n"
      "ds_read_b128 v[80:83], v17 offset:27648\n"
      "v_mul_f32 v158, v126, v158\n"
      "v_mfma_f32_16x16x32_bf16 v[136:139], v[48:51], v[120:123], v[136:139]\n"
      "ds_read_b128 v[92:95], v17 offset:30720\n"
      "v_mul_f32 v157, v125, v157\n"
      "v_mfma_f32_16x16x32_bf16 v[124:127], a[0:3], v[52:55], 0\n"
      "ds_read_b128 v[84:87], v17 offset:28672\n"
      "ds_read_b128 v[104:107], v17 offset:33792\n"
      "v_mfma_f32_16x16x32_bf16 v[124:127], a[4:7], v[56:59], v[124:127]\n"
      "ds_read_b128 v[88:91], v17 offset:29696\n"
      "v_mul_f32 v130, v130, v158\n"
      "v_mfma_f32_16x16x32_bf16 v[124:127], a[8:11], v[60:63], v[124:127]\n"
      "s_waitcnt lgkmcnt(7)\n"
      "ds_read_b128 v[108:111], v17 offset:34816\n"
      "v_add_f32 v169, v169, v171\n"
      "v_mfma_f32_16x16x32_bf16 v[124:127], a[12:15], v[64:67], v[124:127]\n"
      ".Lg_p1:\n"
      "s_cbranch_execz .Lg_pr\n"
      "ds_read_b128 v[96:99], v17 offset:31744\n"
      "v_fma_f32 v161, v161, v177, v177\n"
      "v_mfma_f32_16x16x32_bf16 v[124:127], a[16:19], v[68:71], v[124:127]\n"
      "s_waitcnt lgkmcnt(7)\n"
      "ds_read_b128 v[100:103], v17 offset:32768\n"
      "v_mul_f32 v128, v128, v156\n"
      "v_mfma_f32_16x16x32_bf16 v[124:127], a[20:23], v[72:75], v[124:127]\n"
      "v_rcp_f32 v161, v161\n"
      "v_mfma_f32_16x16x32_bf16 v[124:127], a[24:27], v[76:79], v[124:127]\n"
      "s_waitcnt lgkmcnt(6)\n"
      "ds_read_b128 v[112:115], v17 offset:35840\n"
      "v_mul_f32 v129, v129, v157\n"
      "v_mfma_f32_16x16x32_bf16 v[124:127], a[28:31], v[80:83], v[124:127]\n"
      "s_waitcnt lgkmcnt(5)\n"
      "ds_read_b128 v[120:123], v17 offset:37888\n"
      "v_mul_f32 v131, v131, v159\n"
      "v_mfma_f32_16x16x32_bf16 v[124:127], a[32:35], v[84:87], v[124:127]\n"
      "s_waitcnt lgkmcnt(5)\n"
      "ds_read_b128 v[116:119], v17 offset:36864\n"
      "v_cvt_pk_bf16_f32 v165, v130, v131\n"
      "v_mfma_f32_16x16x32_bf16 v[124:127], a[36:39], v[88:91], v[124:127]\n"
      "v_cvt_pk_bf16_f32 v164, v128, v129\n"
      "v_mfma_f32_16x16x32_bf16 v[124:127], a[40:43], v[92:95], v[124:127]\n"
      "s_waitcnt lgkmcnt(4)\n"
      "v_fma_f32 v160, v160, v177, v177\n"
      "v_sub_f32 v176, -v168, v170\n"
      "v_mfma_f32_16x16x32_bf16 v[124:127], a[44:47], v[96:99], v[124:127]\n"
      "s_waitcnt lgkmcnt(3)\n"
      "v_rcp_f32 v160, v160\n"
      "v_mfma_f32_16x16x32_bf16 v[124:127], a[48:51], v[100:103], v[124:127]\n"
      "v_mul_f32 v161, v133, v161\n"
      "v_fma_f32 v169, -v176, v176, v169\n"
      "v_mfma_f32_16x16x32_bf16 v[124:127], a[52:55], v[104:107], v[124:127]\n"
      "v_max_f32 v177, 0x3727c5ac, v169\n"
      "v_mfma_f32_16x16x32_bf16 v[124:127], a[56:59], v[108:111], v[124:127]\n"
      "s_waitcnt lgkmcnt(1)\n"
      "v_mul_f32 v162, v134, v162\n"
      "v_mul_f32 v137, v137, v161\n"
      "v_mfma_f32_16x16x32_bf16 v[124:127], a[60:63], v[112:115], v[124:127]\n"
      "s_waitcnt lgkmcnt(0)\n"
      "v_mul_f32 v138, v138, v162\n"
      "v_mul_f32 v139, v139, v163\n"
      "v_mfma_f32_16x16x32_bf16 v[124:127], a[64:67], v[116:119], v[124:127]\n"
      "v_mul_f32 v160, v132, v160\n"
      "v_cvt_pk_bf16_f32 v167, v138, v139\n"
      "v_mfma_f32_16x16x32_bf16 v[124:127], a[68:71], v[120:123], v[124:127]\n"
      "v_mul_f32 v136, v136, v160\n"
      "v_cvt_pk_bf16_f32 v166, v136, v137\n"
      "v_mfma_f32_16x16x32_bf16 v[132:135], a[144:147], v[52:55], 0\n"
      "buffer_store_dwordx4 v[164:167], v0, s[8:11], s37 offen sc1\n"
      "v_mfma_f32_16x16x32_bf16 v[132:135], a[148:151], v[56:59], v[132:135]\n"
      "v_rsq_f32 v170, v177\n"
      "v_mfma_f32_16x16x32_bf16 v[132:135], a[152:155], v[60:63], v[132:135]\n"
      "v_mul_f32 v158, v126, v126\n"
      "v_mfma_f32_16x16x32_bf16 v[132:135], a[156:159], v[64:67], v[132:135]\n"
      "v_mul_f32 v178, 0xc0135761, v170\n"
      "v_mul_f32 v171, 0x3d372713, v178\n"
      "v_mfma_f32_16x16x32_bf16 v[132:135], a[160:163], v[68:71], v[132:135]\n"
      "v_mul_f32 v170, v170, v170\n"
      "v_mul_f32 v179, v171, v170\n"
      "v_mfma_f32_16x16x32_bf16 v[132:135], a[164:167], v[72:75], v[132:135]\n"
      "v_mul_f32 v159, v127, v127\n"
      "v_mfma_f32_16x16x32_bf16 v[132:135], a[168:171], v[76:79], v[132:135]\n"
      "v_mul_f32 v156, v124, v124\n"
      "v_fma_f32 v159, v159, v179, v178\n"
      "v_mfma_f32_16x16x32_bf16 v[132:135], a[172:175], v[80:83], v[132:135]\n"
      "v_mul_f32 v157, v125, v125\n"
      "v_fma_f32 v158, v158, v179, v178\n"
      "v_mfma_f32_16x16x32_bf16 v[132:135], a[176:179], v[84:87], v[132:135]\n"
      "s_add_u32 s42, s42, 165888\n"
      "v_add_u32 v11, 1, v11\n"
      "v_mul_f32 v158, v126, v158\n"
      "v_mfma_f32_16x16x32_bf16 v[132:135], a[180:183], v[88:91], v[132:135]\n"
      "v_fma_f32 v157, v157, v179, v178\n"
      "v_fma_f32 v156, v156, v179, v178\n"
      "v_mfma_f32_16x16x32_bf16 v[132:135], a[184:187], v[92:95], v[132:135]\n"
      "v_exp_f32 v158, v158\n"
      "v_mfma_f32_16x16x32_bf16 v[132:135], a[188:191], v[96:99], v[132:135]\n"
      "s_add_u32 m0, s38, 20480\n"
      "v_mfma_f32_16x16x32_bf16 v[132:135], a[192:195], v[100:103], v[132:135]\n"
      "s_mov_b32 s40, s36\n"
      "v_mul_f32 v156, v124, v156\n"
      "v_fma_f32 v158, v158, v177, v177\n"
      "v_mfma_f32_16x16x32_bf16 v[132:135], a[196:199], v[104:107], v[132:135]\n"
      "v_exp_f32 v156, v156\n"
      "v_mfma_f32_16x16x32_bf16 v[132:135], a[200:203], v[108:111], v[132:135]\n"
      "v_fma_f32 v156, v156, v177, v177\n"
      "v_mul_f32 v159, v127, v159\n"
      "v_mfma_f32_16x16x32_bf16 v[132:135], a[204:207], v[112:115], v[132:135]\n"
      "v_rcp_f32 v156, v156\n"
      "v_mfma_f32_16x16x32_bf16 v[132:135], a[208:211], v[116:119], v[132:135]\n"
      "v_exp_f32 v159, v159\n"
      "v_mfma_f32_16x16x32_bf16 v[132:135], a[212:215], v[120:123], v[132:135]\n"
      "v_fma_f32 v159, v159, v177, v177\n"
      "v_mfma_f32_16x16x32_bf16 v[128:131], a[72:75], v[52:55], 0\n"
      "s_waitcnt vmcnt(3)\n"
      "ds_write_b32 v13, v11\n"
      "v_mfma_f32_16x16x32_bf16 v[128:131], a[76:79], v[56:59], v[128:131]\n"
      "s_add_u32 s37, s37, 165888\n"
      "v_rcp_f32 v158, v158\n"
      "v_mfma_f32_16x16x32_bf16 v[128:131], a[80:83], v[60:63], v[128:131]\n"
      "v_mul_f32 v162, v134, v134\n"
      "v_mul_f32 v163, v135, v135\n"
      "v_mfma_f32_16x16x32_bf16 v[128:131], a[84:87], v[64:67], v[128:131]\n"
      "v_rcp_f32 v159, v159\n"
      "v_mfma_f32_16x16x32_bf16 v[128:131], a[88:91], v[68:71], v[128:131]\n"
      "s_waitcnt vmcnt(2)\n"
      "v_cmp_ne_u32 vcc, s21, v18\n"
      "v_fma_f32 v163, v163, v179, v178\n"
      "v_mfma_f32_16x16x32_bf16 v[128:131], a[92:95], v[72:75], v[128:131]\n"
      "v_fma_f32 v162, v162, v179, v178\n"
      "v_mul_f32 v162, v134, v162\n"
      "v_mfma_f32_16x16x32_bf16 v[128:131], a[96:99], v[76:79], v[128:131]\n"
      "s_add_u32 s36, s36, 24576\n"
      "v_mul_f32 v161, v133, v133\n"
      "v_mul_f32 v159, v127, v159\n"
      "v_mfma_f32_16x16x32_bf16 v[128:131], a[100:103], v[80:83], v[128:131]\n"
      "v_exp_f32_e64 v162, v162\n"
      "v_mfma_f32_16x16x32_bf16 v[128:131], a[104:107], v[84:87], v[128:131]\n"
      "s_cbranch_vccnz .Lsp_g0_10\n"
      ".Lbk_g0_10:\n"
      "v_mul_f32 v160, v132, v132\n"
      "v_mfma_f32_16x16x32_bf16 v[128:131], a[108:111], v[88:91], v[128:131]\n"
      "buffer_load_dwordx4 v0, s[8:11], s40 offen offset:0 sc1 lds\n"
      "v_mfma_f32_16x16x32_bf16 v[128:131], a[112:115], v[92:95], v[128:131]\n"
      "ds_read_b32 v12, v14\n"
      "v_fma_f32 v161, v161, v179, v178\n"
      "v_mfma_f32_16x16x32_bf16 v[128:131], a[116:119], v[96:99], v[128:131]\n"
      "v_mul_f32 v156, v124, v156\n"
      "v_mul_f32 v161, v133, v161\n"
      "v_mfma_f32_16x16x32_bf16 v[128:131], a[120:123], v[100:103], v[128:131]\n"
      "v_fma_f32 v160, v160, v179, v178\n"
      "v_mul_f32 v163, v135, v163\n"
      "v_mfma_f32_16x16x32_bf16 v[128:131], a[124:127], v[104:107], v[128:131]\n"
      "buffer_load_dwordx4 v0, s[8:11], s40 offen offset:1024 sc1 lds\n"
      "v_mfma_f32_16x16x32_bf16 v[128:131], a[128:131], v[108:111], v[128:131]\n"
      "v_exp_f32 v163, v163\n"
      "v_mfma_f32_16x16x32_bf16 v[128:131], a[132:135], v[112:115], v[128:131]\n"
      "v_mul_f32 v157, v125, v157\n"
      "v_fma_f32 v163, v163, v177, v177\n"
      "v_mfma_f32_16x16x32_bf16 v[128:131], a[136:139], v[116:119], v[128:131]\n"
      "s_waitcnt lgkmcnt(0)\n"
      "v_cmp_gt_i32 vcc, v11, v12\n"
      "v_mul_f32 v160, v132, v160\n"
      "v_mfma_f32_16x16x32_bf16 v[128:131], a[140:143], v[120:123], v[128:131]\n"
      "buffer_load_dwordx4 v0, s[8:11], s40 offen offset:2048 sc1 lds\n"
      "v_mfma_f32_16x16x32_bf16 v[136:139], a[216:219], v[52:55], 0\n"
      "v_rcp_f32_e64 v163, v163\n"
      "v_mfma_f32_16x16x32_bf16 v[136:139], a[220:223], v[56:59], v[136:139]\n"
      "s_cbranch_vccnz .Lsp_gy0_11\n"
      ".Lbk_gy0_11:\n"
      "ds_read_b128 v[52:55], v0 offset:0\n"
      "v_mfma_f32_16x16x32_bf16 v[136:139], a[224:227], v[60:63], v[136:139]\n"
      "v_exp_f32 v157, v157\n"
      "v_mfma_f32_16x16x32_bf16 v[136:139], a[228:231], v[64:67], v[136:139]\n"
      "ds_read_b128 v[60:63], v0 offset:2048\n"
      "v_mul_f32 v163, v135, v163\n"
      "v_mfma_f32_16x16x32_bf16 v[136:139], a[232:235], v[68:71], v[136:139]\n"
      "buffer_load_dwordx4 v0, s[8:11], s40 offen offset:3072 sc1 lds\n"
      "v_mfma_f32_16x16x32_bf16 v[136:139], a[236:239], v[72:75], v[136:139]\n"
      "v_exp_f32 v161, v161\n"
      "v_mfma_f32_16x16x32_bf16 v[136:139], a[240:243], v[76:79], v[136:139]\n"
      "s_add_u32 m0, s39, 20480\n"
      "ds_read_b128 v[68:71], v0 offset:4096\n"
      "ds_read2st64_b64 v[168:171], v4 offset0:0 offset1:1\n"
      "v_mfma_f32_16x16x32_bf16 v[136:139], a[244:247], v[80:83], v[136:139]\n"
      "ds_read_b128 v[56:59], v0 offset:1024\n"
      "v_fma_f32 v162, v162, v177, v177\n"
      "v_mfma_f32_16x16x32_bf16 v[136:139], a[248:251], v[84:87], v[136:139]\n"
      "buffer_load_dwordx4 v1, s[8:11], s40 offen offset:0 sc1 lds\n"
      "v_mfma_f32_16x16x32_bf16 v[136:139], a[252:255], v[88:91], v[136:139]\n"
      "ds_read_b128 v[64:67], v0 offset:3072\n"
      "v_fma_f32 v157, v157, v177, v177\n"
      "v_mfma_f32_16x16x32_bf16 v[136:139], v[20:23], v[92:95], v[136:139]\n"
      "s_min_u32 s36, s36, s6\n"
      "v_rcp_f32 v162, v162\n"
      "v_mfma_f32_16x16x32_bf16 v[136:139], v[24:27], v[96:99], v[136:139]\n"
      "v_exp_f32 v160, v160\n"
      "v_mfma_f32_16x16x32_bf16 v[136:139], v[28:31], v[100:103], v[136:139]\n"
      "v_rcp_f32 v157, v157\n"
      "v_mfma_f32_16x16x32_bf16 v[136:139], v[32:35], v[104:107], v[136:139]\n"
      "buffer_load_dword v18, v2, s[8:11], s36 offen sc1\n"
      "v_mfma_f32_16x16x32_bf16 v[136:139], v[36:39], v[108:111], v[136:139]\n"
      "ds_read_b128 v[72:75], v0 offset:5120\n"
      "ds_read_b128 v[76:79], v0 offset:6144\n"
      "v_mfma_f32_16x16x32_bf16 v[136:139], v[40:43], v[112:115], v[136:139]\n"
      "s_waitcnt vmcnt(6) lgkmcnt(3)\n"
      "buffer_store_dword v15, v3, s[8:11], s42 offen sc1\n"
      "v_mfma_f32_16x16x32_bf16 v[136:139], v[44:47], v[116:119], v[136:139]\n"
      "ds_read_b128 v[80:83], v0 offset:7168\n"
      "v_mul_f32 v158, v126, v158\n"
      "v_mfma_f32_16x16x32_bf16 v[136:139], v[48:51], v[120:123], v[136:139]\n"
      "ds_read_b128 v[92:95], v0 offset:10240\n"
      "v_mul_f32 v157, v125, v157\n"
      "s_sub_u32 s20, s20, 1\n"
      "s_cbranch_scc0 .Lg_loop\n"
      ".Lg_tail:\n"
      "v_mul_f32 v130, v130, v158\n"
      "v_fma_f32 v161, v161, v177, v177\n"
      "v_mul_f32 v128, v128, v156\n"
      "v_rcp_f32 v161, v161\n"
      "v_mul_f32 v129, v129, v157\n"
      "v_mul_f32 v131, v131, v159\n"
      "v_cvt_pk_bf16_f32 v165, v130, v131\n"
      "v_cvt_pk_bf16_f32 v164, v128, v129\n"
      "v_fma_f32 v160, v160, v177, v177\n"
      "v_rcp_f32 v160, v160\n"
      "v_mul_f32 v161, v133, v161\n"
      "v_mul_f32 v162, v134, v162\n"
      "v_mul_f32 v137, v137, v161\n"
      "v_mul_f32 v138, v138, v162\n"
      "v_mul_f32 v139, v139, v163\n"
      "v_mul_f32 v160, v132, v160\n"
      "v_cvt_pk_bf16_f32 v167, v138, v139\n"
      "v_mul_f32 v136, v136, v160\n"
      "v_cvt_pk_bf16_f32 v166, v136, v137\n"
      "buffer_store_dwordx4 v[164:167], v0, s[8:11], s37 offen sc1\n"
      "s_add_u32 s42, s42, 165888\n"
      "s_add_u32 s37, s37, 165888\n"
      "s_waitcnt vmcnt(0)\n"
      "buffer_store_dword v15, v3, s[8:11], s42 offen sc1\n"
      "s_add_u32 s42, s42, 165888\n"
      "s_endpgm\n"
      ".Lsp_g0_1:\n"
      "s_mov_b32 s22, s23\n"
      ".Lsl_g0_1:\n"
      "buffer_load_dword v18, v2, s[8:11], s40 offen sc1\n"
      "s_waitcnt vmcnt(0)\n"
      "v_cmp_ne_u32 vcc, s21, v18\n"
      "s_cbranch_vccz .Lbk_g0_1\n"
      "s_sleep 4\n"
      "s_sub_u32 s22, s22, 1\n"
      "s_cbranch_scc0 .Lsl_g0_1\n"
      "s_mov_b32 s23, 0\n"
      "s_branch .Lbk_g0_1\n"
      ".Lsp_g0_2:\n"
      "s_mov_b32 s22, s23\n"
      ".Lsl_g0_2:\n"
      "buffer_load_dword v18, v2, s[8:11], s40 offen sc1\n"
      "s_waitcnt vmcnt(0)\n"
      "v_cmp_ne_u32 vcc, s21, v18\n"
      "s_cbranch_vccz .Lbk_g0_2\n"
      "s_sleep 4\n"
      "s_sub_u32 s22, s22, 1\n"
      "s_cbranch_scc0 .Lsl_g0_2\n"
      "s_mov_b32 s23, 0\n"
      "s_branch .Lbk_g0_2\n"
      ".Lsp_gy0_3:\n"
      "s_lshl_b32 s22, s23, 4\n"
      ".Lsl_gy0_3:\n"
      "ds_read_b32 v12, v14\n"
      "s_waitcnt lgkmcnt(0)\n"
      "v_cmp_gt_i32 vcc, v11, v12\n"
      "s_cbranch_vccz .Lbk_gy0_3\n"
      "s_sub_u32 s22, s22, 1\n"
      "s_cbranch_scc0 .Lsl_gy0_3\n"
      "s_mov_b32 s23, 0\n"
      "s_branch .Lbk_gy0_3\n"
      ".Lsp_g0_4:\n"
      "s_mov_b32 s22, s23\n"
      "buffer_load_dword v18, v2, s[8:11], s40 offen sc1\n"
      "s_waitcnt vmcnt(0)\n"
      "v_cmp_ne_u32 vcc, s21, v18\n"
      "s_add_u32 s30, s42, 0\n"
      "buffer_store_dword v15, v3, s[8:11], s30 offen sc1\n"
      "s_cbranch_vccz .Lbk_g0_4\n"
      ".Lsl_g0_4:\n"
      "buffer_load_dword v18, v2, s[8:11], s40 offen sc1\n"
      "s_waitcnt vmcnt(0)\n"
      "v_cmp_ne_u32 vcc, s21, v18\n"
      "s_cbranch_vccz .Lbk_g0_4\n"
      "s_sleep 4\n"
      "s_sub_u32 s22, s22, 1\n"
      "s_cbranch_scc0 .Lsl_g0_4\n"
      "s_mov_b32 s23, 0\n"
      "s_branch .Lbk_g0_4\n"
      ".Lsp_gy0_5:\n"
      "s_lshl_b32 s22, s23, 4\n"
      ".Lsl_gy0_5:\n"
      "ds_read_b32 v12, v14\n"
      "s_waitcnt lgkmcnt(0)\n"
      "v_cmp_gt_i32 vcc, v11, v12\n"
      "s_cbranch_vccz .Lbk_gy0_5\n"
      "s_sub_u32 s22, s22, 1\n"
      "s_cbranch_scc0 .Lsl_gy0_5\n"
      "s_mov_b32 s23, 0\n"
      "s_branch .Lbk_gy0_5\n"
      ".Lsp_g0_6:\n"
      "s_mov_b32 s22, s23\n"
      "buffer_load_dword v18, v2, s[8:11], s40 offen sc1\n"
      "s_waitcnt vmcnt(0)\n"
      "v_cmp_ne_u32 vcc, s21, v18\n"
      "s_add_u32 s30, s42, 0\n"
      "buffer_store_dword v15, v3, s[8:11], s30 offen sc1\n"
      "s_cbranch_vccz .Lbk_g0_6\n"
      ".Lsl_g0_6:\n"
      "buffer_load_dword v18, v2, s[8:11], s40 offen sc1\n"
      "s_waitcnt vmcnt(0)\n"
      "v_cmp_ne_u32 vcc, s21, v18\n"
      "s_cbranch_vccz .Lbk_g0_6\n"
      "s_sleep 4\n"
      "s_sub_u32 s22, s22, 1\n"
      "s_cbranch_scc0 .Lsl_g0_6\n"
      "s_mov_b32 s23, 0\n"
      "s_branch .Lbk_g0_6\n"
      ".Lsp_gy0_7:\n"
      "s_lshl_b32 s22, s23, 4\n"
      ".Lsl_gy0_7:\n"
      "ds_read_b32 v12, v14\n"
      "s_waitcnt lgkmcnt(0)\n"
      "v_cmp_gt_i32 vcc, v11, v12\n"
      "s_cbranch_vccz .Lbk_gy0_7\n"
      "s_sub_u32 s22, s22, 1\n"
      "s_cbranch_scc0 .Lsl_gy0_7\n"
      "s_mov_b32 s23, 0\n"
      "s_branch .Lbk_gy0_7\n"
      ".Lsp_g0_8:\n"
      "s_mov_b32 s22, s23\n"
      "buffer_load_dword v18, v2, s[8:11], s40 offen sc1\n"
      "s_waitcnt vmcnt(0)\n"
      "v_cmp_ne_u32 vcc, s21, v18\n"
      "s_add_u32 s30, s42, 0\n"
      "buffer_store_dword v15, v3, s[8:11], s30 offen sc1\n"
      "s_cbranch_vccz .Lbk_g0_8\n"
      ".Lsl_g0_8:\n"
      "buffer_load_dword v18, v2, s[8:11], s40 offen sc1\n"
      "s_waitcnt vmcnt(0)\n"
      "v_cmp_ne_u32 vcc, s21, v18\n"
      "s_cbranch_vccz .Lbk_g0_8\n"
      "s_sleep 4\n"
      "s_sub_u32 s22, s22, 1\n"
      "s_cbranch_scc0 .Lsl_g0_8\n"
      "s_mov_b32 s23, 0\n"
      "s_branch .Lbk_g0_8\n"
      ".Lsp_gy0_9:\n"
      "s_lshl_b32 s22, s23, 4\n"
      ".Lsl_gy0_9:\n"
      "ds_read_b32 v12, v14\n"
      "s_waitcnt lgkmcnt(0)\n"
      "v_cmp_gt_i32 vcc, v11, v12\n"
      "s_cbranch_vccz .Lbk_gy0_9\n"
      "s_sub_u32 s22, s22, 1\n"
      "s_cbranch_scc0 .Lsl_gy0_9\n"
      "s_mov_b32 s23, 0\n"
      "s_branch .Lbk_gy0_9\n"
      ".Lsp_g0_10:\n"
      "s_mov_b32 s22, s23\n"
      "buffer_load_dword v18, v2, s[8:11], s40 offen sc1\n"
      "s_waitcnt vmcnt(0)\n"
      "v_cmp_ne_u32 vcc, s21, v18\n"
      "s_add_u32 s30, s42, 0\n"
      "buffer_store_dword v15, v3, s[8:11], s30 offen sc1\n"
      "s_cbranch_vccz .Lbk_g0_10\n"
      ".Lsl_g0_10:\n"
      "buffer_load_dword v18, v2, s[8:11], s40 offen sc1\n"
      "s_waitcnt vmcnt(0)\n"
      "v_cmp_ne_u32 vcc, s21, v18\n"
      "s_cbranch_vccz .Lbk_g0_10\n"
      "s_sleep 4\n"
      "s_sub_u32 s22, s22, 1\n"
      "s_cbranch_scc0 .Lsl_g0_10\n"
      "s_mov_b32 s23, 0\n"
      "s_branch .Lbk_g0_10\n"
      ".Lsp_gy0_11:\n"
      "s_lshl_b32 s22, s23, 4\n"
      ".Lsl_gy0_11:\n"
      "ds_read_b32 v12, v14\n"
      "s_waitcnt lgkmcnt(0)\n"
      "v_cmp_gt_i32 vcc, v11, v12\n"
      "s_cbranch_vccz .Lbk_gy0_11\n"
      "s_sub_u32 s22, s22, 1\n"
      "s_cbranch_scc0 .Lsl_gy0_11\n"
      "s_mov_b32 s23, 0\n"
      "s_branch .Lbk_gy0_11\n"
      ".Lg_last:\n"
      :
      : "{s[8:11]}"(rsWS), "{s[12:15]}"(rsA), "{s[92:95]}"(rsC), "{v0}"(v_l16), "{v1}"(v_l5), "{v2}"(v_fa), "{v3}"(v_ra), "{v4}"(v_st), "{v5}"(v_at), "{v6}"(v_vo0), "{v7}"(v_vo1), "{v8}"(v_vo2), "{v9}"(v_vo3), "{v10}"(v_pw), "{v12}"(v_al), "{v13}"(v_yw), "{v14}"(v_yr), "{v16}"(v_hi), "{v54}"(v_tw[0]), "{v55}"(v_tw[1]), "{v64}"(v_id[0]), "{v65}"(v_id[1]), "{s36}"(s_sx), "{s37}"(s_sh), "{s38}"(s_m4), "{s39}"(s_m5)
      : "v255", "a255", "s99", "vcc", "scc", "memory");

  }
}

extern "C" void kernel_launch(void* const* d_in, const int* in_sizes, int n_in, void* d_out, int out_size,
                              void* d_ws, size_t ws_size, hipStream_t stream) {
  char* ws = (char*)d_ws + ((256 - ((size_t)d_ws & 255)) & 255);
  (void)hipFuncSetAttribute((const void*)k_all, hipFuncAttributeMaxDynamicSharedMemorySize, LDS_BYTES);
  k_all<<<256, 256, LDS_BYTES, stream>>>((const u16*)d_in[0], (const u16*)d_in[1], (const u16*)d_in[2], (const u16*)d_in[4],
                                         (const u16*)d_in[5], (const u16*)d_in[6], ws, (u16*)d_out);
}
```

```cpp
#include <hip/hip_runtime.h>
#include <stdint.h>

typedef unsigned short u16;
typedef __attribute__((ext_vector_type(4))) unsigned u32x4;
constexpr unsigned WS_DBG = 65536u;
constexpr unsigned WS_XIN = 1310720u;
constexpr unsigned WS_OB = 9175040u;
constexpr unsigned WS_X1 = 17039360u;
constexpr unsigned WS_H = 26034176u;
constexpr unsigned WS_PS = 78589952u;
constexpr unsigned WS_END = 109740032u;
constexpr unsigned KSK = 0u;
constexpr unsigned ENT = 1u;
constexpr unsigned ROT = 0u;
constexpr unsigned FLIP = 0u;
constexpr unsigned PW = 1664u;
constexpr unsigned REC = 24576u;
constexpr unsigned HREC = 165888u;
constexpr unsigned PREC = 99840u;
constexpr unsigned NREC = 80u;
constexpr unsigned OOB = 0x40000000u, NEG = 0xff61b1e6u;
constexpr int LDS_BYTES = 160 * 1024;

__device__ __forceinline__ u32x4 srd(const void* p, unsigned bytes) {
  const unsigned long long a = (unsigned long long)p;
  return u32x4{(unsigned)a, (unsigned)(a >> 32) & 0xffffu, bytes, 0x00020000u};
}

extern "C" __global__ __launch_bounds__(256, 1) __attribute__((amdgpu_waves_per_eu(1, 1))) void k_all(
    const u16* X, const u16* Wqkv, const u16* Wo, const u16* Wg, const u16* Wu, const u16* Wd, char* ws, u16* out) {
  const unsigned b = blockIdx.x, xcd = b & 7, slot = b >> 3;
  const unsigned w = __builtin_amdgcn_readfirstlane(threadIdx.x >> 6), lane = threadIdx.x & 63;
  const bool fast = ((xcd ^ FLIP) & 1) == 0;
  auto qvo = [&](unsigned i, unsigned hg) {
    const unsigned c = 64 * i + lane, r = c * 2185 >> 16, cc = c - 30 * r, sc = cc * 57 >> 9;
    const unsigned o = (8 * w + r) * 3456 + sc * (1152 - 144) + 144 * hg + cc * 16 - 1024 * i;
    return o | (c < 240 && cc < 27 ? 0u : OOB);
  };
  if (ENT) {
    constexpr unsigned NL = 41, LR = 9;
    const unsigned wi = slot * 4 + w, n0 = wi * NL + lane, n = n0 < 576 * LR ? n0 : 0, r = n / LR, c = n - LR * r;
    const unsigned lx = lane - NL, m = (xcd * 128 + wi) * 4 + lx, t = m / 144;
    unsigned oc = (wi * 8 + (lane & 7)) * 128;
    asm volatile("s_getpc_b64 vcc\n.Lent_pc:\n\ts_sub_u32 vcc_lo, vcc_lo, .Lent_pc-k_all\n\ts_subb_u32 vcc_hi, vcc_hi, 0\n\ts_mov_b32 m0, %1\n\t"
                 "v_min_u32 %0, .Lw_last-k_all, %0\n\tglobal_load_lds_dword %0, vcc" : "+v"(oc) : "s"(162816 + w * 256) : "vcc", "scc", "memory");
    unsigned oq = r * 3456 + c / (LR / 3) * 1152 + ((xcd & 6) + c % (LR / 3)) * 128, ox = (32 * (t & 3) + 31 - (t >> 2)) * 18432 + (m - 144 * t) * 128;
    asm volatile("" : "+v"(oq), "+v"(ox));
    const bool isq = lane < NL && fast;
    oq = isq ? oq : lx < 4 ? ox : 0;
    asm volatile("" : "+v"(oq));
    __builtin_amdgcn_sched_barrier(0);
    const char* pa = (const char*)(isq ? Wqkv : X) + oq;
    asm volatile("s_mov_b32 m0, %1\n\ts_nop 0\n\tglobal_load_lds_dword %0, off" ::"v"(pa), "s"(162816 + w * 256) : "memory");
    __builtin_amdgcn_sched_barrier(0);
  }
  const unsigned i16 = lane & 15, g = lane >> 4, q = i16 >> 2, p = lane & 3;
  const u32x4 rsWS = srd(ws, WS_END);
  const unsigned v_l16 = lane * 16, e = xcd >> 1;
  const unsigned half = (i16 + 16 * (g >> 1)) * 16 + (g & 1) * 8;
  const unsigned line = lane < 32 ? lane * 4 : OOB, v_al = w ? OOB : line + 128 * (1 + b);
  const unsigned up = lane >> 5, l5 = (lane & 31) * 4, v_hi = 0u - up;
  const unsigned l8 = lane >> 3, l7 = (lane & 7) * 16;
#define COMMON(ID, YB) const unsigned v_dbg = WS_DBG + (((ID) + part * 4 + gr) * 4 + w) * 1024 + lane * 4, v_yw = (YB) + w * 4, v_yr = (YB) + p * 4; (void)v_dbg
  if (__builtin_expect(fast && slot < 8, 1)) {
    const unsigned gr = slot & 3, part = 2 * e + (slot >> 2);
    COMMON(0, 61440);
    const unsigned hg = part, h = 4 * hg + w, par = i16 & 1;
    const u32x4 rsA = srd(Wqkv, 576 * 1728 * 2);
    const unsigned v_l5 = w < 3 || lane < 32 ? v_l16 + 16384 - w * 3072 : OOB;
    const unsigned v_fa = 20480 - w * 4096 + (lane < 48 ? lane >> 2 : 11) * 128 + p * 4, v_ra = up * 8 * REC + l5 + 20480 + h * 128;
    const unsigned v_st = 18432 + i16 * 32 + q * 8, v_tw = 61568 + w * 128 + i16 * 8, v_tr = 61568 + w * 128 + g * 32;
    const unsigned v_om = h * 512 + half, v_oe = g ? OOB : 16384 + h * 128 + i16 * 8;
    const unsigned v_pw = 20480 + h * 128 + l8 * REC + l7;
    unsigned v_vo[4], v_bc[4], v_bp[4], v_mk[4], v_sel[4] = {0, 0, 0, 0}, v_ci[4];
#pragma unroll
    for (unsigned i = 0; i < 4; ++i) {
      const unsigned kk = 4 * g + i, ka = 4 * g + 2 * (i & 1) + par;
      v_bc[i] = (i & 1) == par && kk <= i16 ? 0u : NEG;
      v_bp[i] = (i & 1) == par && kk >= i16 + 2 ? 0u : NEG;
      v_mk[i] = (i < 2 ? ka > i16 : ka <= i16) ? 0xffffu << (16 * par) : 0u;
      v_ci[i] = p == 2 ? 0x3f800000u : 0u;
    }
    v_sel[3] = lane == 16 ? 0x3f80u : lane == 17 ? 0x3f800000u : 0u;
#pragma unroll
    for (unsigned i = 0; i < 4; ++i) v_vo[i] = qvo(i, hg);
    const unsigned rb = 65536 + g * 4224 + q * 480;
    const unsigned v_at = rb + 36 * w + 4 * (w & 1) + 8 * p, v_ax = rb + (p < 2 ? p : 2) * 144 + 72 * (w >> 1) + 32;
    const unsigned v_pm = (16 * g + 4 * (i16 >> 1 & 3) + 2 * (w & 1) + par) * 4, v_ms = i16 < 6 ? ~0u : 0u;
    const unsigned s_sx = WS_XIN + (gr * NREC + KSK) * REC + w * 4096, s_ev = s_sx + (32 - KSK) * REC, s_del = 0u - (gr ? NREC + 32 : 32) * REC;
    const unsigned s_kil = gr ? 0x7f7fffffu : 0xff7fffffu, s_so = WS_OB + (gr * NREC + KSK) * REC;
    const unsigned s_m4 = w * 4096, s_m5 = 16384 + w * 1024, s_ms = w * 4224, s_up = part + gr + w;
    asm volatile(
      "s_buffer_load_dword s21, s[8:11], 0\n"
      "s_mov_b32 s23, 65536\n"
      "s_mov_b32 s31, 0x40000000\n"
      "s_mov_b32 s51, 0\n"
      "s_add_u32 m0, s43, 65536\n"
      "s_nop 0\n"
      "buffer_load_dwordx4 v48, s[12:15], s51 offen offset:0 lds\n"
      "buffer_load_dwordx4 v49, s[12:15], s51 offen offset:1024 lds\n"
      "buffer_load_dwordx4 v50, s[12:15], s51 offen offset:2048 lds\n"
      "buffer_load_dwordx4 v51, s[12:15], s51 offen offset:3072 lds\n"
      "s_add_u32 s51, s51, 110592\n"
      "s_add_u32 m0, s43, 82432\n"
      "s_nop 0\n"
      "buffer_load_dwordx4 v48, s[12:15], s51 offen offset:0 lds\n"
      "buffer_load_dwordx4 v49, s[12:15], s51 offen offset:1024 lds\n"
      "buffer_load_dwordx4 v50, s[12:15], s51 offen offset:2048 lds\n"
      "buffer_load_dwordx4 v51, s[12:15], s51 offen offset:3072 lds\n"
      "s_add_u32 s51, s51, 110592\n"
      "s_add_u32 m0, s43, 99328\n"
      "s_nop 0\n"
      "buffer_load_dwordx4 v48, s[12:15], s51 offen offset:0 lds\n"
      "buffer_load_dwordx4 v49, s[12:15], s51 offen offset:1024 lds\n"
      "buffer_load_dwordx4 v50, s[12:15], s51 offen offset:2048 lds\n"
      "buffer_load_dwordx4 v51, s[12:15], s51 offen offset:3072 lds\n"
      "s_add_u32 s51, s51, 110592\n"
      "s_add_u32 s6, s36, 1572864\n"
      "s_add_u32 s36, s36, 4294942720\n"
      "s_add_u32 s48, s37, 811008\n"
      "s_mov_b32 s53, 0x7f7fffff\n"
      "v_mov_b32 v179, 0\n"
      "s_mov_b32 s54, 0xffff\n"
      "s_mov_b32 s55, 0\n"
      "v_mov_b32 v9, 0\n"
      "ds_write_b32 v11, v9\n"
      "v_mov_b32 v56, 0x3f803f80\n"
      "v_mov_b32 v57, 0x3f803f80\n"
      "v_mov_b32 v58, 0x3f803f80\n"
      "v_mov_b32 v59, 0x3f803f80\n"
      "s_mov_b64 exec, 0\n"
      "s_lshr_b32 s22, s41, 12\n"
      ".Lq_pl:\n"
      "s_cmp_eq_u32 s22, 0\n"
      "s_cbranch_scc1 .Lq_p0\n"
      "s_cmp_eq_u32 s22, 1\n"
      "s_cbranch_scc1 .Lq_p1\n"
      "s_cmp_eq_u32 s22, 3\n"
      "s_cbranch_scc1 .Lq_px\n"
      "s_branch .Lq_pe\n"
      ".Lq_pr:\n"
      "s_add_u32 s22, s22, 4\n"
      "s_branch .Lq_pl\n"
      ".Lq_pe:\n"
      "s_mov_b64 exec, -1\n"
      "s_waitcnt vmcnt(8) lgkmcnt(0)\n"
      "s_barrier\n"
      "ds_read_b64_tr_b16 v[60:61], v53 offset:0\n"
      "ds_read_b64_tr_b16 v[62:63], v53 offset:1920\n"
      "ds_read_b64_tr_b16 a[0:1], v52 offset:144\n"
      "ds_read_b64_tr_b16 a[2:3], v52 offset:2064\n"
      "ds_read_b64_tr_b16 a[72:73], v52 offset:288\n"
      "ds_read_b64_tr_b16 a[74:75], v52 offset:2208\n"
      "ds_read_b64_tr_b16 a[144:145], v52 offset:0\n"
      "ds_read_b64_tr_b16 a[146:147], v52 offset:1920\n"
      "s_add_u32 m0, s43, 116224\n"
      "s_nop 0\n"
      "buffer_load_dwordx4 v48, s[12:15], s51 offen offset:0 lds\n"
      "buffer_load_dwordx4 v49, s[12:15], s51 offen offset:1024 lds\n"
      "buffer_load_dwordx4 v50, s[12:15], s51 offen offset:2048 lds\n"
      "buffer_load_dwordx4 v51, s[12:15], s51 offen offset:3072 lds\n"
      "s_add_u32 s51, s51, 110592\n"
      "s_waitcnt vmcnt(8) lgkmcnt(0)\n"
      "s_barrier\n"
      "ds_read_b64_tr_b16 v[68:69], v53 offset:16896\n"
      "ds_read_b64_tr_b16 v[70:71], v53 offset:18816\n"
      "ds_read_b64_tr_b16 a[4:5], v52 offset:17040\n"
      "ds_read_b64_tr_b16 a[6:7], v52 offset:18960\n"
      "ds_read_b64_tr_b16 a[76:77], v52 offset:17184\n"
      "ds_read_b64_tr_b16 a[78:79], v52 offset:19104\n"
      "ds_read_b64_tr_b16 a[148:149], v52 offset:16896\n"
      "ds_read_b64_tr_b16 a[150:151], v52 offset:18816\n"
      "ds_bpermute_b32 v64, v54, v60\n"
      "ds_bpermute_b32 v65, v54, v61\n"
      "ds_bpermute_b32 v66, v54, v62\n"
      "ds_bpermute_b32 v67, v54, v63\n"
      "v_mfma_f32_16x16x32_bf16 v[136:139], a[0:3], v[56:59], 0\n"
      "v_mfma_f32_16x16x32_bf16 v[144:147], v[56:59], a[72:75], 0\n"
      "v_mfma_f32_16x16x32_bf16 v[148:151], a[144:147], v[56:59], 0\n"
      "s_add_u32 m0, s43, 65536\n"
      "s_nop 0\n"
      "buffer_load_dwordx4 v48, s[12:15], s51 offen offset:0 lds\n"
      "buffer_load_dwordx4 v49, s[12:15], s51 offen offset:1024 lds\n"
      "buffer_load_dwordx4 v50, s[12:15], s51 offen offset:2048 lds\n"
      "buffer_load_dwordx4 v51, s[12:15], s51 offen offset:3072 lds\n"
      "s_add_u32 s51, s51, 110592\n"
      "s_waitcnt vmcnt(8) lgkmcnt(0)\n"
      "s_barrier\n"
      "ds_read_b64_tr_b16 v[60:61], v53 offset:33792\n"
      "ds_read_b64_tr_b16 v[62:63], v53 offset:35712\n"
      "ds_read_b64_tr_b16 a[8:9], v52 offset:33936\n"
      "ds_read_b64_tr_b16 a[10:11], v52 offset:35856\n"
      "ds_read_b64_tr_b16 a[80:81], v52 offset:34080\n"
      "ds_read_b64_tr_b16 a[82:83], v52 offset:36000\n"
      "ds_read_b64_tr_b16 a[152:153], v52 offset:33792\n"
      "ds_read_b64_tr_b16 a[154:155], v52 offset:35712\n"
      "ds_bpermute_b32 v72, v54, v68\n"
      "ds_bpermute_b32 v73, v54, v69\n"
      "ds_bpermute_b32 v74, v54, v70\n"
      "ds_bpermute_b32 v75, v54, v71\n"
      "v_mfma_f32_16x16x32_bf16 v[136:139], a[4:7], v[56:59], v[136:139]\n"
      "v_mfma_f32_16x16x32_bf16 v[144:147], v[56:59], a[76:79], v[144:147]\n"
      "v_mfma_f32_16x16x32_bf16 v[148:151], a[148:151], v[56:59], v[148:151]\n"
      "s_add_u32 m0, s43, 82432\n"
      "s_nop 0\n"
      "buffer_load_dwordx4 v48, s[12:15], s51 offen offset:0 lds\n"
      "buffer_load_dwordx4 v49, s[12:15], s51 offen offset:1024 lds\n"
      "buffer_load_dwordx4 v50, s[12:15], s51 offen offset:2048 lds\n"
      "buffer_load_dwordx4 v51, s[12:15], s51 offen offset:3072 lds\n"
      "s_add_u32 s51, s51, 110592\n"
      "v_and_b32 v64, v55, v64\n"
      "v_and_b32 v65, v55, v65\n"
      "v_and_b32 v66, v55, v66\n"
      "v_and_b32 v67, v55, v67\n"
      "v_accvgpr_write_b32 a216, v64\n"
      "v_accvgpr_write_b32 a217, v65\n"
      "v_accvgpr_write_b32 a218, v66\n"
      "v_accvgpr_write_b32 a219, v67\n"
      "v_mfma_f32_16x16x32_bf16 v[140:143], v[64:67], v[56:59], 0\n"
      "s_waitcnt vmcnt(8) lgkmcnt(0)\n"
      "s_barrier\n"
      "ds_read_b64_tr_b16 v[68:69], v53 offset:50688\n"
      "ds_read_b64_tr_b16 v[70:71], v53 offset:52608\n"
      "ds_read_b64_tr_b16 a[12:13], v52 offset:50832\n"
      "ds_read_b64_tr_b16 a[14:15], v52 offset:52752\n"
      "ds_read_b64_tr_b16 a[84:85], v52 offset:50976\n"
      "ds_read_b64_tr_b16 a[86:87], v52 offset:52896\n"
      "ds_read_b64_tr_b16 a[156:157], v52 offset:50688\n"
      "ds_read_b64_tr_b16 a[158:159], v52 offset:52608\n"
      "ds_bpermute_b32 v64, v54, v60\n"
      "ds_bpermute_b32 v65, v54, v61\n"
      "ds_bpermute_b32 v66, v54, v62\n"
      "ds_bpermute_b32 v67, v54, v63\n"
      "v_mfma_f32_16x16x32_bf16 v[136:139], a[8:11], v[56:59], v[136:139]\n"
      "v_mfma_f32_16x16x32_bf16 v[144:147], v[56:59], a[80:83], v[144:147]\n"
      "v_mfma_f32_16x16x32_bf16 v[148:151], a[152:155], v[56:59], v[148:151]\n"
      "s_add_u32 m0, s43, 99328\n"
      "s_nop 0\n"
      "buffer_load_dwordx4 v48, s[12:15], s51 offen offset:0 lds\n"
      "buffer_load_dwordx4 v49, s[12:15], s51 offen offset:1024 lds\n"
      "buffer_load_dwordx4 v50, s[12:15], s51 offen offset:2048 lds\n"
      "buffer_load_dwordx4 v51, s[12:15], s51 offen offset:3072 lds\n"
      "s_add_u32 s51, s51, 110592\n"
      "v_and_b32 v72, v55, v72\n"
      "v_and_b32 v73, v55, v73\n"
      "v_and_b32 v74, v55, v74\n"
      "v_and_b32 v75, v55, v75\n"
      "v_accvgpr_write_b32 a220, v72\n"
      "v_accvgpr_write_b32 a221, v73\n"
      "v_accvgpr_write_b32 a222, v74\n"
      "v_accvgpr_write_b32 a223, v75\n"
      "v_mfma_f32_16x16x32_bf16 v[140:143], v[72:75], v[56:59], v[140:143]\n"
      "s_waitcnt vmcnt(8) lgkmcnt(0)\n"
      "s_barrier\n"
      "ds_read_b64_tr_b16 v[60:61], v53 offset:0\n"
      "ds_read_b64_tr_b16 v[62:63], v53 offset:1920\n"
      "ds_read_b64_tr_b16 a[16:17], v52 offset:144\n"
      "ds_read_b64_tr_b16 a[18:19], v52 offset:2064\n"
      "ds_read_b64_tr_b16 a[88:89], v52 offset:288\n"
      "ds_read_b64_tr_b16 a[90:91], v52 offset:2208\n"
      "ds_read_b64_tr_b16 a[160:161], v52 offset:0\n"
      "ds_read_b64_tr_b16 a[162:163], v52 offset:1920\n"
      "ds_bpermute_b32 v72, v54, v68\n"
      "ds_bpermute_b32 v73, v54, v69\n"
      "ds_bpermute_b32 v74, v54, v70\n"
      "ds_bpermute_b32 v75, v54, v71\n"
      "v_mfma_f32_16x16x32_bf16 v[136:139], a[12:15], v[56:59], v[136:139]\n"
      "v_mfma_f32_16x16x32_bf16 v[144:147], v[56:59], a[84:87], v[144:147]\n"
      "v_mfma_f32_16x16x32_bf16 v[148:151], a[156:159], v[56:59], v[148:151]\n"
      "s_add_u32 m0, s43, 116224\n"
      "s_nop 0\n"
      "buffer_load_dwordx4 v48, s[12:15], s51 offen offset:0 lds\n"
      "buffer_load_dwordx4 v49, s[12:15], s51 offen offset:1024 lds\n"
      "buffer_load_dwordx4 v50, s[12:15], s51 offen offset:2048 lds\n"
      "buffer_load_dwordx4 v51, s[12:15], s51 offen offset:3072 lds\n"
      "s_add_u32 s51, s51, 110592\n"
      "v_and_b32 v64, v55, v64\n"
      "v_and_b32 v65, v55, v65\n"
      "v_and_b32 v66, v55, v66\n"
      "v_and_b32 v67, v55, v67\n"
      "v_accvgpr_write_b32 a224, v64\n"
      "v_accvgpr_write_b32 a225, v65\n"
      "v_accvgpr_write_b32 a226, v66\n"
      "v_accvgpr_write_b32 a227, v67\n"
      "v_mfma_f32_16x16x32_bf16 v[140:143], v[64:67], v[56:59], v[140:143]\n"
      "s_waitcnt vmcnt(8) lgkmcnt(0)\n"
      "s_barrier\n"
      "ds_read_b64_tr_b16 v[68:69], v53 offset:16896\n"
      "ds_read_b64_tr_b16 v[70:71], v53 offset:18816\n"
      "ds_read_b64_tr_b16 a[20:21], v52 offset:17040\n"
      "ds_read_b64_tr_b16 a[22:23], v52 offset:18960\n"
      "ds_read_b64_tr_b16 a[92:93], v52 offset:17184\n"
      "ds_read_b64_tr_b16 a[94:95], v52 offset:19104\n"
      "ds_read_b64_tr_b16 a[164:165], v52 offset:16896\n"
      "ds_read_b64_tr_b16 a[166:167], v52 offset:18816\n"
      "ds_bpermute_b32 v64, v54, v60\n"
      "ds_bpermute_b32 v65, v54, v61\n"
      "ds_bpermute_b32 v66, v54, v62\n"
      "ds_bpermute_b32 v67, v54, v63\n"
      "v_mfma_f32_16x16x32_bf16 v[136:139], a[16:19], v[56:59], v[136:139]\n"
      "v_mfma_f32_16x16x32_bf16 v[144:147], v[56:59], a[88:91], v[144:147]\n"
      "v_mfma_f32_16x16x32_bf16 v[148:151], a[160:163], v[56:59], v[148:151]\n"
      "s_add_u32 m0, s43, 65536\n"
      "s_nop 0\n"
      "buffer_load_dwordx4 v48, s[12:15], s51 offen offset:0 lds\n"
      "buffer_load_dwordx4 v49, s[12:15], s51 offen offset:1024 lds\n"
      "buffer_load_dwordx4 v50, s[12:15], s51 offen offset:2048 lds\n"
      "buffer_load_dwordx4 v51, s[12:15], s51 offen offset:3072 lds\n"
      "s_add_u32 s51, s51, 110592\n"
      "v_and_b32 v72, v55, v72\n"
      "v_and_b32 v73, v55, v73\n"
      "v_and_b32 v74, v55, v74\n"
      "v_and_b32 v75, v55, v75\n"
      "v_accvgpr_write_b32 a228, v72\n"
      "v_accvgpr_write_b32 a229, v73\n"
      "v_accvgpr_write_b32 a230, v74\n"
      "v_accvgpr_write_b32 a231, v75\n"
      "v_mfma_f32_16x16x32_bf16 v[140:143], v[72:75], v[56:59], v[140:143]\n"
      "s_waitcnt vmcnt(8) lgkmcnt(0)\n"
      "s_barrier\n"
      "ds_read_b64_tr_b16 v[60:61], v53 offset:33792\n"
      "ds_read_b64_tr_b16 v[62:63], v53 offset:35712\n"
      "ds_read_b64_tr_b16 a[24:25], v52 offset:33936\n"
      "ds_read_b64_tr_b16 a[26:27], v52 offset:35856\n"
      "ds_read_b64_tr_b16 a[96:97], v52 offset:34080\n"
      "ds_read_b64_tr_b16 a[98:99], v52 offset:36000\n"
      "ds_read_b64_tr_b16 a[168:169], v52 offset:33792\n"
      "ds_read_b64_tr_b16 a[170:171], v52 offset:35712\n"
      "ds_bpermute_b32 v72, v54, v68\n"
      "ds_bpermute_b32 v73, v54, v69\n"
      "ds_bpermute_b32 v74, v54, v70\n"
      "ds_bpermute_b32 v75, v54, v71\n"
      "v_mfma_f32_16x16x32_bf16 v[136:139], a[20:23], v[56:59], v[136:139]\n"
      "v_mfma_f32_16x16x32_bf16 v[144:147], v[56:59], a[92:95], v[144:147]\n"
      "v_mfma_f32_16x16x32_bf16 v[148:151], a[164:167], v[56:59], v[148:151]\n"
      "s_add_u32 m0, s43, 82432\n"
      "s_nop 0\n"
      "buffer_load_dwordx4 v48, s[12:15], s51 offen offset:0 lds\n"
      "buffer_load_dwordx4 v49, s[12:15], s51 offen offset:1024 lds\n"
      "buffer_load_dwordx4 v50, s[12:15], s51 offen offset:2048 lds\n"
      "buffer_load_dwordx4 v51, s[12:15], s51 offen offset:3072 lds\n"
      "s_add_u32 s51, s51, 110592\n"
      "v_and_b32 v64, v55, v64\n"
      "v_and_b32 v65, v55, v65\n"
      "v_and_b32 v66, v55, v66\n"
      "v_and_b32 v67, v55, v67\n"
      "v_accvgpr_write_b32 a232, v64\n"
      "v_accvgpr_write_b32 a233, v65\n"
      "v_accvgpr_write_b32 a234, v66\n"
      "v_accvgpr_write_b32 a235, v67\n"
      "v_mfma_f32_16x16x32_bf16 v[140:143], v[64:67], v[56:59], v[140:143]\n"
      "s_waitcnt vmcnt(8) lgkmcnt(0)\n"
      "s_barrier\n"
      "ds_read_b64_tr_b16 v[68:69], v53 offset:50688\n"
      "ds_read_b64_tr_b16 v[70:71], v53 offset:52608\n"
      "ds_read_b64_tr_b16 a[28:29], v52 offset:50832\n"
      "ds_read_b64_tr_b16 a[30:31], v52 offset:52752\n"
      "ds_read_b64_tr_b16 a[100:101], v52 offset:50976\n"
      "ds_read_b64_tr_b16 a[102:103], v52 offset:52896\n"
      "ds_read_b64_tr_b16 a[172:173], v52 offset:50688\n"
      "ds_read_b64_tr_b16 a[174:175], v52 offset:52608\n"
      "ds_bpermute_b32 v64, v54, v60\n"
      "ds_bpermute_b32 v65, v54, v61\n"
      "ds_bpermute_b32 v66, v54, v62\n"
      "ds_bpermute_b32 v67, v54, v63\n"
      "v_mfma_f32_16x16x32_bf16 v[136:139], a[24:27], v[56:59], v[136:139]\n"
      "v_mfma_f32_16x16x32_bf16 v[144:147], v[56:59], a[96:99], v[144:147]\n"
      "v_mfma_f32_16x16x32_bf16 v[148:151], a[168:171], v[56:59], v[148:151]\n"
      "s_add_u32 m0, s43, 99328\n"
      "s_nop 0\n"
      "buffer_load_dwordx4 v48, s[12:15], s51 offen offset:0 lds\n"
      "buffer_load_dwordx4 v49, s[12:15], s51 offen offset:1024 lds\n"
      "buffer_load_dwordx4 v50, s[12:15], s51 offen offset:2048 lds\n"
      "buffer_load_dwordx4 v51, s[12:15], s51 offen offset:3072 lds\n"
      "s_add_u32 s51, s51, 110592\n"
      "v_and_b32 v72, v55, v72\n"
      "v_and_b32 v73, v55, v73\n"
      "v_and_b32 v74, v55, v74\n"
      "v_and_b32 v75, v55, v75\n"
      "v_accvgpr_write_b32 a236, v72\n"
      "v_accvgpr_write_b32 a237, v73\n"
      "v_accvgpr_write_b32 a238, v74\n"
      "v_accvgpr_write_b32 a239, v75\n"
      "v_mfma_f32_16x16x32_bf16 v[140:143], v[72:75], v[56:59], v[140:143]\n"
      "s_waitcnt vmcnt(8) lgkmcnt(0)\n"
      "s_barrier\n"
      "ds_read_b64_tr_b16 v[60:61], v53 offset:0\n"
      "ds_read_b64_tr_b16 v[62:63], v53 offset:1920\n"
      "ds_read_b64_tr_b16 a[32:33], v52 offset:144\n"
      "ds_read_b64_tr_b16 a[34:35], v52 offset:2064\n"
      "ds_read_b64_tr_b16 a[104:105], v52 offset:288\n"
      "ds_read_b64_tr_b16 a[106:107], v52 offset:2208\n"
      "ds_read_b64_tr_b16 a[176:177], v52 offset:0\n"
      "ds_read_b64_tr_b16 a[178:179], v52 offset:1920\n"
      "ds_bpermute_b32 v72, v54, v68\n"
      "ds_bpermute_b32 v73, v54, v69\n"
      "ds_bpermute_b32 v74, v54, v70\n"
      ".Lq_px:\n"
      "s_cbranch_execz .Lq_pr\n"
      "ds_bpermute_b32 v75, v54, v71\n"
      "v_mfma_f32_16x16x32_bf16 v[136:139], a[28:31], v[56:59], v[136:139]\n"
      "v_mfma_f32_16x16x32_bf16 v[144:147], v[56:59], a[100:103], v[144:147]\n"
      "v_mfma_f32_16x16x32_bf16 v[148:151], a[172:175], v[56:59], v[148:151]\n"
      "s_add_u32 m0, s43, 116224\n"
      "s_nop 0\n"
      "buffer_load_dwordx4 v48, s[12:15], s51 offen offset:0 lds\n"
      "buffer_load_dwordx4 v49, s[12:15], s51 offen offset:1024 lds\n"
      "buffer_load_dwordx4 v50, s[12:15], s51 offen offset:2048 lds\n"
      "buffer_load_dwordx4 v51, s[12:15], s51 offen offset:3072 lds\n"
      "s_add_u32 s51, s51, 110592\n"
      "v_and_b32 v64, v55, v64\n"
      "v_and_b32 v65, v55, v65\n"
      "v_and_b32 v66, v55, v66\n"
      "v_and_b32 v67, v55, v67\n"
      "v_accvgpr_write_b32 a240, v64\n"
      "v_accvgpr_write_b32 a241, v65\n"
      "v_accvgpr_write_b32 a242, v66\n"
      "v_accvgpr_write_b32 a243, v67\n"
      "v_mfma_f32_16x16x32_bf16 v[140:143], v[64:67], v[56:59], v[140:143]\n"
      "s_waitcnt vmcnt(8) lgkmcnt(0)\n"
      "s_barrier\n"
      "ds_read_b64_tr_b16 v[68:69], v53 offset:16896\n"
      "ds_read_b64_tr_b16 v[70:71], v53 offset:18816\n"
      "ds_read_b64_tr_b16 a[36:37], v52 offset:17040\n"
      "ds_read_b64_tr_b16 a[38:39], v52 offset:18960\n"
      "ds_read_b64_tr_b16 a[108:109], v52 offset:17184\n"
      "ds_read_b64_tr_b16 a[110:111], v52 offset:19104\n"
      "ds_read_b64_tr_b16 a[180:181], v52 offset:16896\n"
      "ds_read_b64_tr_b16 a[182:183], v52 offset:18816\n"
      "ds_bpermute_b32 v64, v54, v60\n"
      "ds_bpermute_b32 v65, v54, v61\n"
      "ds_bpermute_b32 v66, v54, v62\n"
      "ds_bpermute_b32 v67, v54, v63\n"
      "v_mfma_f32_16x16x32_bf16 v[136:139], a[32:35], v[56:59], v[136:139]\n"
      "v_mfma_f32_16x16x32_bf16 v[144:147], v[56:59], a[104:107], v[144:147]\n"
      "v_mfma_f32_16x16x32_bf16 v[148:151], a[176:179], v[56:59], v[148:151]\n"
      "s_add_u32 m0, s43, 65536\n"
      "s_nop 0\n"
      "buffer_load_dwordx4 v48, s[12:15], s51 offen offset:0 lds\n"
      "buffer_load_dwordx4 v49, s[12:15], s51 offen offset:1024 lds\n"
      "buffer_load_dwordx4 v50, s[12:15], s51 offen offset:2048 lds\n"
      "buffer_load_dwordx4 v51, s[12:15], s51 offen offset:3072 lds\n"
      "s_add_u32 s51, s51, 110592\n"
      "v_and_b32 v72, v55, v72\n"
      "v_and_b32 v73, v55, v73\n"
      "v_and_b32 v74, v55, v74\n"
      "v_and_b32 v75, v55, v75\n"
      "v_accvgpr_write_b32 a244, v72\n"
      "v_accvgpr_write_b32 a245, v73\n"
      "v_accvgpr_write_b32 a246, v74\n"
      "v_accvgpr_write_b32 a247, v75\n"
      "v_mfma_f32_16x16x32_bf16 v[140:143], v[72:75], v[56:59], v[140:143]\n"
      "s_waitcnt vmcnt(8) lgkmcnt(0)\n"
      "s_barrier\n"
      "ds_read_b64_tr_b16 v[60:61], v53 offset:33792\n"
      "ds_read_b64_tr_b16 v[62:63], v53 offset:35712\n"
      "ds_read_b64_tr_b16 a[40:41], v52 offset:33936\n"
      "ds_read_b64_tr_b16 a[42:43], v52 offset:35856\n"
      "ds_read_b64_tr_b16 a[112:113], v52 offset:34080\n"
      "ds_read_b64_tr_b16 a[114:115], v52 offset:36000\n"
      "ds_read_b64_tr_b16 a[184:185], v52 offset:33792\n"
      "ds_read_b64_tr_b16 a[186:187], v52 offset:35712\n"
      "ds_bpermute_b32 v72, v54, v68\n"
      "ds_bpermute_b32 v73, v54, v69\n"
      "ds_bpermute_b32 v74, v54, v70\n"
      "ds_bpermute_b32 v75, v54, v71\n"
      "v_mfma_f32_16x16x32_bf16 v[136:139], a[36:39], v[56:59], v[136:139]\n"
      "v_mfma_f32_16x16x32_bf16 v[144:147], v[56:59], a[108:111], v[144:147]\n"
      "v_mfma_f32_16x16x32_bf16 v[148:151], a[180:183], v[56:59], v[148:151]\n"
      "s_add_u32 m0, s43, 82432\n"
      "s_nop 0\n"
      "buffer_load_dwordx4 v48, s[12:15], s51 offen offset:0 lds\n"
      "buffer_load_dwordx4 v49, s[12:15], s51 offen offset:1024 lds\n"
      "buffer_load_dwordx4 v50, s[12:15], s51 offen offset:2048 lds\n"
      "buffer_load_dwordx4 v51, s[12:15], s51 offen offset:3072 lds\n"
      "s_add_u32 s51, s51, 110592\n"
      "v_and_b32 v64, v55, v64\n"
      "v_and_b32 v65, v55, v65\n"
      "v_and_b32 v66, v55, v66\n"
      "v_and_b32 v67, v55, v67\n"
      "v_accvgpr_write_b32 a248, v64\n"
      "v_accvgpr_write_b32 a249, v65\n"
      "v_accvgpr_write_b32 a250, v66\n"
      "v_accvgpr_write_b32 a251, v67\n"
      "v_mfma_f32_16x16x32_bf16 v[140:143], v[64:67], v[56:59], v[140:143]\n"
      "s_waitcnt vmcnt(8) lgkmcnt(0)\n"
      "s_barrier\n"
      "ds_read_b64_tr_b16 v[68:69], v53 offset:50688\n"
      "ds_read_b64_tr_b16 v[70:71], v53 offset:52608\n"
      "ds_read_b64_tr_b16 a[44:45], v52 offset:50832\n"
      "ds_read_b64_tr_b16 a[46:47], v52 offset:52752\n"
      "ds_read_b64_tr_b16 a[116:117], v52 offset:50976\n"
      "ds_read_b64_tr_b16 a[118:119], v52 offset:52896\n"
      "ds_read_b64_tr_b16 a[188:189], v52 offset:50688\n"
      "ds_read_b64_tr_b16 a[190:191], v52 offset:52608\n"
      "ds_bpermute_b32 v64, v54, v60\n"
      "ds_bpermute_b32 v65, v54, v61\n"
      "ds_bpermute_b32 v66, v54, v62\n"
      "ds_bpermute_b32 v67, v54, v63\n"
      "v_mfma_f32_16x16x32_bf16 v[136:139], a[40:43], v[56:59], v[136:139]\n"
      "v_mfma_f32_16x16x32_bf16 v[144:147], v[56:59], a[112:115], v[144:147]\n"
      "v_mfma_f32_16x16x32_bf16 v[148:151], a[184:187], v[56:59], v[148:151]\n"
      "s_add_u32 m0, s43, 99328\n"
      "s_nop 0\n"
      "buffer_load_dwordx4 v48, s[12:15], s51 offen offset:0 lds\n"
      "buffer_load_dwordx4 v49, s[12:15], s51 offen offset:1024 lds\n"
      "buffer_load_dwordx4 v50, s[12:15], s51 offen offset:2048 lds\n"
      "buffer_load_dwordx4 v51, s[12:15], s51 offen offset:3072 lds\n"
      "s_add_u32 s51, s51, 110592\n"
      "v_and_b32 v72, v55, v72\n"
      "v_and_b32 v73, v55, v73\n"
      "v_and_b32 v74, v55, v74\n"
      "v_and_b32 v75, v55, v75\n"
      "v_accvgpr_write_b32 a252, v72\n"
      "v_accvgpr_write_b32 a253, v73\n"
      "v_accvgpr_write_b32 a254, v74\n"
      "v_accvgpr_write_b32 a255, v75\n"
      "v_mfma_f32_16x16x32_bf16 v[140:143], v[72:75], v[56:59], v[140:143]\n"
      "s_waitcnt vmcnt(8) lgkmcnt(0)\n"
      "s_barrier\n"
      "ds_read_b64_tr_b16 v[60:61], v53 offset:0\n"
      "ds_read_b64_tr_b16 v[62:63], v53 offset:1920\n"
      "ds_read_b64_tr_b16 a[48:49], v52 offset:144\n"
      "ds_read_b64_tr_b16 a[50:51], v52 offset:2064\n"
      "ds_read_b64_tr_b16 a[120:121], v52 offset:288\n"
      "ds_read_b64_tr_b16 a[122:123], v52 offset:2208\n"
      "ds_read_b64_tr_b16 a[192:193], v52 offset:0\n"
      "ds_read_b64_tr_b16 a[194:195], v52 offset:1920\n"
      "ds_bpermute_b32 v72, v54, v68\n"
      "ds_bpermute_b32 v73, v54, v69\n"
      "ds_bpermute_b32 v74, v54, v70\n"
      "ds_bpermute_b32 v75, v54, v71\n"
      "v_mfma_f32_16x16x32_bf16 v[136:139], a[44:47], v[56:59], v[136:139]\n"
      "v_mfma_f32_16x16x32_bf16 v[144:147], v[56:59], a[116:119], v[144:147]\n"
      "v_mfma_f32_16x16x32_bf16 v[148:151], a[188:191], v[56:59], v[148:151]\n"
      "s_add_u32 m0, s43, 116224\n"
      "s_nop 0\n"
      "buffer_load_dwordx4 v48, s[12:15], s51 offen offset:0 lds\n"
      "buffer_load_dwordx4 v49, s[12:15], s51 offen offset:1024 lds\n"
      "buffer_load_dwordx4 v50, s[12:15], s51 offen offset:2048 lds\n"
      "buffer_load_dwordx4 v51, s[12:15], s51 offen offset:3072 lds\n"
      "s_add_u32 s51, s51, 110592\n"
      "v_and_b32 v64, v55, v64\n"
      "v_and_b32 v65, v55, v65\n"
      "v_and_b32 v66, v55, v66\n"
      "v_and_b32 v67, v55, v67\n"
      "v_mov_b32 v16, v64\n"
      "v_mov_b32 v17, v65\n"
      "v_mov_b32 v18, v66\n"
      "v_mov_b32 v19, v67\n"
      "v_mfma_f32_16x16x32_bf16 v[140:143], v[64:67], v[56:59], v[140:143]\n"
      "s_add_u32 s30, s36, 24576\n"
      "buffer_load_dword v204, v2, s[8:11], s30 offen sc1\n"
      "s_add_u32 s30, s36, 49152\n"
      "buffer_load_dword v205, v2, s[8:11], s30 offen sc1\n"
      "s_waitcnt vmcnt(10) lgkmcnt(0)\n"
      "s_barrier\n"
      "ds_read_b64_tr_b16 v[68:69], v53 offset:16896\n"
      "ds_read_b64_tr_b16 v[70:71], v53 offset:18816\n"
      "ds_read_b64_tr_b16 a[52:53], v52 offset:17040\n"
      "ds_read_b64_tr_b16 a[54:55], v52 offset:18960\n"
      "ds_read_b64_tr_b16 a[124:125], v52 offset:17184\n"
      "ds_read_b64_tr_b16 a[126:127], v52 offset:19104\n"
      "ds_read_b64_tr_b16 a[196:197], v52 offset:16896\n"
      "ds_read_b64_tr_b16 a[198:199], v52 offset:18816\n"
      "ds_bpermute_b32 v64, v54, v60\n"
      "ds_bpermute_b32 v65, v54, v61\n"
      "ds_bpermute_b32 v66, v54, v62\n"
      "ds_bpermute_b32 v67, v54, v63\n"
      "v_mfma_f32_16x16x32_bf16 v[136:139], a[48:51], v[56:59], v[136:139]\n"
      "v_mfma_f32_16x16x32_bf16 v[144:147], v[56:59], a[120:123], v[144:147]\n"
      "v_mfma_f32_16x16x32_bf16 v[148:151], a[192:195], v[56:59], v[148:151]\n"
      "s_add_u32 m0, s43, 65536\n"
      "s_nop 0\n"
      "buffer_load_dwordx4 v48, s[12:15], s51 offen offset:0 lds\n"
      "buffer_load_dwordx4 v49, s[12:15], s51 offen offset:1024 lds\n"
      "buffer_load_dwordx4 v50, s[12:15], s51 offen offset:2048 lds\n"
      "buffer_load_dwordx4 v51, s[12:15], s51 offen offset:3072 lds\n"
      "s_add_u32 s51, s51, 110592\n"
      "v_and_b32 v72, v55, v72\n"
      "v_and_b32 v73, v55, v73\n"
      "v_and_b32 v74, v55, v74\n"
      "v_and_b32 v75, v55, v75\n"
      "v_mov_b32 v20, v72\n"
      "v_mov_b32 v21, v73\n"
      "v_mov_b32 v22, v74\n"
      "v_mov_b32 v23, v75\n"
      "v_mfma_f32_16x16x32_bf16 v[140:143], v[72:75], v[56:59], v[140:143]\n"
      "s_waitcnt vmcnt(10) lgkmcnt(0)\n"
      "s_barrier\n"
      "ds_read_b64_tr_b16 v[60:61], v53 offset:33792\n"
      "ds_read_b64_tr_b16 v[62:63], v53 offset:35712\n"
      "ds_read_b64_tr_b16 a[56:57], v52 offset:33936\n"
      "ds_read_b64_tr_b16 a[58:59], v52 offset:35856\n"
      "ds_read_b64_tr_b16 a[128:129], v52 offset:34080\n"
      "ds_read_b64_tr_b16 a[130:131], v52 offset:36000\n"
      "ds_read_b64_tr_b16 a[200:201], v52 offset:33792\n"
      "ds_read_b64_tr_b16 a[202:203], v52 offset:35712\n"
      "ds_bpermute_b32 v72, v54, v68\n"
      "ds_bpermute_b32 v73, v54, v69\n"
      "ds_bpermute_b32 v74, v54, v70\n"
      "ds_bpermute_b32 v75, v54, v71\n"
      "v_mfma_f32_16x16x32_bf16 v[136:139], a[52:55], v[56:59], v[136:139]\n"
      "v_mfma_f32_16x16x32_bf16 v[144:147], v[56:59], a[124:127], v[144:147]\n"
      "v_mfma_f32_16x16x32_bf16 v[148:151], a[196:199], v[56:59], v[148:151]\n"
      "s_add_u32 m0, s43, 82432\n"
      "s_nop 0\n"
      "buffer_load_dwordx4 v48, s[12:15], s51 offen offset:0 lds\n"
      "buffer_load_dwordx4 v49, s[12:15], s51 offen offset:1024 lds\n"
      "buffer_load_dwordx4 v50, s[12:15], s51 offen offset:2048 lds\n"
      "buffer_load_dwordx4 v51, s[12:15], s51 offen offset:3072 lds\n"
      "s_add_u32 s51, s51, 110592\n"
      "v_and_b32 v64, v55, v64\n"
      "v_and_b32 v65, v55, v65\n"
      "v_and_b32 v66, v55, v66\n"
      "v_and_b32 v67, v55, v67\n"
      "v_mov_b32 v24, v64\n"
      "v_mov_b32 v25, v65\n"
      "v_mov_b32 v26, v66\n"
      "v_mov_b32 v27, v67\n"
      "v_mfma_f32_16x16x32_bf16 v[140:143], v[64:67], v[56:59], v[140:143]\n"
      "s_mul_i32 s21, s21, 1664525\n"
      "s_add_u32 s21, s21, 1013904223\n"
      "v_mov_b32 v13, s21\n"
      "buffer_store_dword v13, v10, s[8:11], 0 offen sc1\n"
      "v_not_b32 v200, v13\n"
      "v_not_b32 v201, v13\n"
      "v_not_b32 v202, v13\n"
      "v_not_b32 v203, v13\n"
      "s_add_u32 s30, s40, 0\n"
      "buffer_store_dwordx4 v[200:203], v76, s[8:11], s30 offen sc1\n"
      "v_xor_b32 v13, v13, v14\n"
      "s_add_u32 s52, s40, 4294893568\n"
      "s_add_u32 s40, s40, 4294918144\n"
      "s_add_u32 s36, s36, 24576\n"
      "s_mov_b32 s45, s36\n"
      "s_min_u32 s45, s45, s6\n"
      "s_waitcnt vmcnt(11)\n"
      "v_cmp_ne_u32 vcc, s21, v204\n"
      "s_add_u32 m0, s41, 0\n"
      "s_add_u32 s36, s36, 24576\n"
      "s_mov_b32 s46, s45\n"
      "s_cbranch_vccnz .Lsp_q0_1\n"
      ".Lbk_q0_1:\n"
      "buffer_load_dwordx4 v0, s[8:11], s46 offen offset:1024 sc1 lds\n"
      "buffer_load_dwordx4 v0, s[8:11], s46 offen offset:0 sc1 lds\n"
      "buffer_load_dwordx4 v0, s[8:11], s46 offen offset:2048 sc1 lds\n"
      "s_mov_b32 s45, s36\n"
      "s_min_u32 s45, s45, s6\n"
      "buffer_load_dwordx4 v0, s[8:11], s46 offen offset:3072 sc1 lds\n"
      "s_add_u32 m0, s42, 0\n"
      "s_nop 0\n"
      "buffer_load_dwordx4 v1, s[8:11], s46 offen offset:0 sc1 lds\n"
      "s_waitcnt lgkmcnt(0)\n"
      "s_barrier\n"
      "ds_read_b64_tr_b16 v[68:69], v53 offset:50688\n"
      "ds_read_b64_tr_b16 v[70:71], v53 offset:52608\n"
      "ds_read_b64_tr_b16 a[60:61], v52 offset:50832\n"
      "ds_read_b64_tr_b16 a[62:63], v52 offset:52752\n"
      "ds_read_b64_tr_b16 a[132:133], v52 offset:50976\n"
      "ds_read_b64_tr_b16 a[134:135], v52 offset:52896\n"
      "ds_read_b64_tr_b16 a[204:205], v52 offset:50688\n"
      "ds_read_b64_tr_b16 a[206:207], v52 offset:52608\n"
      "ds_bpermute_b32 v64, v54, v60\n"
      "ds_bpermute_b32 v65, v54, v61\n"
      "ds_bpermute_b32 v66, v54, v62\n"
      "ds_bpermute_b32 v67, v54, v63\n"
      "v_mfma_f32_16x16x32_bf16 v[136:139], a[56:59], v[56:59], v[136:139]\n"
      "v_mfma_f32_16x16x32_bf16 v[144:147], v[56:59], a[128:131], v[144:147]\n"
      "v_mfma_f32_16x16x32_bf16 v[148:151], a[200:203], v[56:59], v[148:151]\n"
      "v_and_b32 v72, v55, v72\n"
      "v_and_b32 v73, v55, v73\n"
      "v_and_b32 v74, v55, v74\n"
      "v_and_b32 v75, v55, v75\n"
      "v_mov_b32 v28, v72\n"
      "v_mov_b32 v29, v73\n"
      "v_mov_b32 v30, v74\n"
      "v_mov_b32 v31, v75\n"
      "v_mfma_f32_16x16x32_bf16 v[140:143], v[72:75], v[56:59], v[140:143]\n"
      "s_waitcnt vmcnt(11) lgkmcnt(0)\n"
      "s_barrier\n"
      "ds_read_b64_tr_b16 v[60:61], v53 offset:0\n"
      "ds_read_b64_tr_b16 v[62:63], v53 offset:1920\n"
      "ds_read_b64_tr_b16 a[64:65], v52 offset:144\n"
      "ds_read_b64_tr_b16 a[66:67], v52 offset:2064\n"
      "ds_read_b64_tr_b16 a[136:137], v52 offset:288\n"
      "ds_read_b64_tr_b16 a[138:139], v52 offset:2208\n"
      "ds_read_b64_tr_b16 a[208:209], v52 offset:0\n"
      "ds_read_b64_tr_b16 a[210:211], v52 offset:1920\n"
      "ds_bpermute_b32 v72, v54, v68\n"
      "ds_bpermute_b32 v73, v54, v69\n"
      "ds_bpermute_b32 v74, v54, v70\n"
      "ds_bpermute_b32 v75, v54, v71\n"
      "v_mfma_f32_16x16x32_bf16 v[136:139], a[60:63], v[56:59], v[136:139]\n"
      "v_mfma_f32_16x16x32_bf16 v[144:147], v[56:59], a[132:135], v[144:147]\n"
      "v_mfma_f32_16x16x32_bf16 v[148:151], a[204:207], v[56:59], v[148:151]\n"
      "v_and_b32 v64, v55, v64\n"
      "v_and_b32 v65, v55, v65\n"
      "v_and_b32 v66, v55, v66\n"
      "v_and_b32 v67, v55, v67\n"
      "v_mov_b32 v32, v64\n"
      "v_mov_b32 v33, v65\n"
      "v_mov_b32 v34, v66\n"
      "v_mov_b32 v35, v67\n"
      "v_mfma_f32_16x16x32_bf16 v[140:143], v[64:67], v[56:59], v[140:143]\n"
      "s_waitcnt vmcnt(7) lgkmcnt(0)\n"
      "s_barrier\n"
      "ds_read_b64_tr_b16 v[68:69], v53 offset:16896\n"
      "ds_read_b64_tr_b16 v[70:71], v53 offset:18816\n"
      "ds_read_b64_tr_b16 a[68:69], v52 offset:17040\n"
      "ds_read_b64_tr_b16 a[70:71], v52 offset:18960\n"
      "ds_read_b64_tr_b16 a[140:141], v52 offset:17184\n"
      "ds_read_b64_tr_b16 a[142:143], v52 offset:19104\n"
      "ds_read_b64_tr_b16 a[212:213], v52 offset:16896\n"
      "ds_read_b64_tr_b16 a[214:215], v52 offset:18816\n"
      "ds_bpermute_b32 v64, v54, v60\n"
      "ds_bpermute_b32 v65, v54, v61\n"
      "ds_bpermute_b32 v66, v54, v62\n"
      "ds_bpermute_b32 v67, v54, v63\n"
      "v_mfma_f32_16x16x32_bf16 v[136:139], a[64:67], v[56:59], v[136:139]\n"
      "v_mfma_f32_16x16x32_bf16 v[144:147], v[56:59], a[136:139], v[144:147]\n"
      "v_mfma_f32_16x16x32_bf16 v[148:151], a[208:211], v[56:59], v[148:151]\n"
      "v_and_b32 v72, v55, v72\n"
      "v_and_b32 v73, v55, v73\n"
      "v_and_b32 v74, v55, v74\n"
      "v_and_b32 v75, v55, v75\n"
      "v_mov_b32 v36, v72\n"
      "v_mov_b32 v37, v73\n"
      "v_mov_b32 v38, v74\n"
      "v_mov_b32 v39, v75\n"
      "v_mfma_f32_16x16x32_bf16 v[140:143], v[72:75], v[56:59], v[140:143]\n"
      "s_waitcnt lgkmcnt(0)\n"
      "ds_bpermute_b32 v72, v54, v68\n"
      "ds_bpermute_b32 v73, v54, v69\n"
      "ds_bpermute_b32 v74, v54, v70\n"
      "ds_bpermute_b32 v75, v54, v71\n"
      "v_mfma_f32_16x16x32_bf16 v[136:139], a[68:71], v[56:59], v[136:139]\n"
      "v_mfma_f32_16x16x32_bf16 v[144:147], v[56:59], a[140:143], v[144:147]\n"
      "v_mfma_f32_16x16x32_bf16 v[148:151], a[212:215], v[56:59], v[148:151]\n"
      "v_and_b32 v64, v55, v64\n"
      "v_and_b32 v65, v55, v65\n"
      "v_and_b32 v66, v55, v66\n"
      "v_and_b32 v67, v55, v67\n"
      "v_mov_b32 v40, v64\n"
      "v_mov_b32 v41, v65\n"
      "v_mov_b32 v42, v66\n"
      "v_mov_b32 v43, v67\n"
      "v_mfma_f32_16x16x32_bf16 v[140:143], v[64:67], v[56:59], v[140:143]\n"
      "s_waitcnt lgkmcnt(0)\n"
      "v_and_b32 v72, v55, v72\n"
      "v_and_b32 v73, v55, v73\n"
      "v_and_b32 v74, v55, v74\n"
      "v_and_b32 v75, v55, v75\n"
      "v_mov_b32 v44, v72\n"
      "v_mov_b32 v45, v73\n"
      "v_mov_b32 v46, v74\n"
      "v_mov_b32 v47, v75\n"
      "v_mfma_f32_16x16x32_bf16 v[140:143], v[72:75], v[56:59], v[140:143]\n"
      "s_add_u32 s36, s36, 24576\n"
      "s_cmp_eq_u32 s36, s37\n"
      "s_cselect_b32 s47, s38, 0\n"
      "s_cselect_b32 s37, s48, s37\n"
      "s_cselect_b32 s49, s39, s53\n"
      "s_add_u32 m0, s41, 20480\n"
      "s_mov_b32 s46, s45\n"
      "s_add_u32 s45, s36, s47\n"
      "v_add_u32 v9, 1, v9\n"
      "v_cmp_ne_u32 vcc, s21, v205\n"
      "s_min_u32 s45, s45, s6\n"
      "s_cbranch_vccnz .Lsp_q1_2\n"
      ".Lbk_q1_2:\n"
      "buffer_load_dwordx4 v0, s[8:11], s46 offen offset:1024 sc1 lds\n"
      "s_waitcnt vmcnt(1)\n"
      "ds_write_b32 v11, v9\n"
      "buffer_load_dwordx4 v0, s[8:11], s46 offen offset:0 sc1 lds\n"
      "buffer_load_dwordx4 v0, s[8:11], s46 offen offset:3072 sc1 lds\n"
      "buffer_load_dword v15, v2, s[8:11], s45 offen sc1\n"
      "ds_read_b32 v10, v12\n"
      "buffer_load_dwordx4 v0, s[8:11], s46 offen offset:2048 sc1 lds\n"
      "s_waitcnt lgkmcnt(0)\n"
      "v_cmp_gt_i32 vcc, v9, v10\n"
      "s_add_u32 m0, s42, 20480\n"
      "s_nop 0\n"
      "buffer_load_dwordx4 v1, s[8:11], s46 offen offset:0 sc1 lds\n"
      "s_cbranch_vccnz .Lsp_qy0_3\n"
      ".Lbk_qy0_3:\n"
      "ds_read_b128 v[52:55], v0 offset:1024\n"
      "ds_read_b128 v[48:51], v0 offset:0\n"
      "ds_read_b128 v[56:59], v0 offset:2048\n"
      "ds_read_b128 v[68:71], v0 offset:5120\n"
      "ds_read_b128 v[64:67], v0 offset:4096\n"
      "ds_read_b128 v[60:63], v0 offset:3072\n"
      "buffer_load_dword v14, v11, s[8:11], s31 offen\n"
      "ds_read2st64_b64 v[212:215], v4 offset0:0 offset1:1\n"
      "ds_read_b128 v[72:75], v0 offset:6144\n"
      "ds_read_b128 v[76:79], v0 offset:7168\n"
      "ds_read_b128 v[88:91], v0 offset:10240\n"
      "ds_read_b128 v[108:111], v0 offset:15360\n"
      "s_mov_b32 s20, 21\n"
      "s_waitcnt lgkmcnt(0)\n"
      "s_nop 4\n"
      "s_branch .Lq_loop\n"
      ".p2align 12\n"
      ".Lq_p0:\n"
      "s_branch .Lq_pr\n"
      ".p2align 5\n"
      ".Lq_loop:\n"
      "v_mfma_f32_16x16x32_bf16 v[120:123], a[0:3], v[48:51], 0\n"
      "ds_read_b128 v[80:83], v0 offset:8192\n"
      "ds_read_b128 v[84:87], v0 offset:9216\n"
      "v_mfma_f32_16x16x32_bf16 v[120:123], a[4:7], v[52:55], v[120:123]\n"
      "ds_read_b128 v[96:99], v0 offset:12288\n"
      "v_mul_f32 v232, 0x3eae1a93, v219\n"
      "v_mfma_f32_16x16x32_bf16 v[120:123], a[8:11], v[56:59], v[120:123]\n"
      "s_waitcnt lgkmcnt(7)\n"
      "ds_read_b128 v[104:107], v0 offset:14336\n"
      "v_fma_f32 v135, -v218, v151, v135\n"
      "v_mfma_f32_16x16x32_bf16 v[120:123], a[12:15], v[60:63], v[120:123]\n"
      "v_fma_f32 v133, -v218, v149, v133\n"
      "v_fma_f32 v134, -v218, v150, v134\n"
      "v_mfma_f32_16x16x32_bf16 v[120:123], a[16:19], v[64:67], v[120:123]\n"
      "ds_read_b128 v[92:95], v0 offset:11264\n"
      "v_fma_f32 v132, -v218, v148, v132\n"
      "v_mfma_f32_16x16x32_bf16 v[120:123], a[20:23], v[68:71], v[120:123]\n"
      "s_waitcnt lgkmcnt(5)\n"
      "ds_read_b64 v[216:217], v4 offset:1024\n"
      "v_cvt_pk_bf16_f32 v176, v132, v133\n"
      "v_mfma_f32_16x16x32_bf16 v[120:123], a[24:27], v[72:75], v[120:123]\n"
      "ds_read_b128 v[100:103], v0 offset:13312\n"
      "v_cvt_pk_bf16_f32 v177, v134, v135\n"
      "v_mfma_f32_16x16x32_bf16 v[120:123], a[28:31], v[76:79], v[120:123]\n"
      "s_waitcnt lgkmcnt(5)\n"
      "v_rcp_f32 v210, v210\n"
      "v_mfma_f32_16x16x32_bf16 v[120:123], a[32:35], v[80:83], v[120:123]\n"
      "v_mul_f32 v204, v204, v210\n"
      "v_add_f32 v212, v212, v214\n"
      "v_mfma_f32_16x16x32_bf16 v[188:191], v[172:175], v[176:179], v[152:155]\n"
      "ds_read_b128 v[112:115], v0 offset:16384\n"
      "v_mfma_f32_16x16x32_bf16 v[120:123], a[36:39], v[84:87], v[120:123]\n"
      "ds_read_b128 v[116:119], v0 offset:17408\n"
      "v_mul_f32 v208, v208, v210\n"
      "v_mfma_f32_16x16x32_bf16 v[120:123], a[40:43], v[88:91], v[120:123]\n"
      "s_waitcnt lgkmcnt(4)\n"
      "s_add_u32 s36, s36, 24576\n"
      "v_mul_f32 v207, v207, v210\n"
      "v_mfma_f32_16x16x32_bf16 v[120:123], a[44:47], v[92:95], v[120:123]\n"
      "v_mul_f32 v209, v209, v210\n"
      "v_cvt_pk_bf16_f32 v230, v208, v209\n"
      "v_mfma_f32_16x16x32_bf16 v[120:123], a[48:51], v[96:99], v[120:123]\n"
      "s_waitcnt lgkmcnt(2)\n"
      "buffer_store_dwordx2 v[230:231], v8, s[8:11], s40 offen sc1\n"
      "v_mfma_f32_16x16x32_bf16 v[120:123], a[52:55], v[100:103], v[120:123]\n"
      "v_mul_f32 v206, v206, v210\n"
      "v_add_f32 v218, v212, v216\n"
      "v_mfma_f32_16x16x32_bf16 v[120:123], a[56:59], v[104:107], v[120:123]\n"
      "s_mov_b32 s46, s45\n"
      "v_add_f32 v213, v213, v215\n"
      "v_mfma_f32_16x16x32_bf16 v[120:123], a[60:63], v[108:111], v[120:123]\n"
      "s_waitcnt lgkmcnt(1)\n"
      "v_cvt_pk_bf16_f32 v229, v206, v207\n"
      "v_mfma_f32_16x16x32_bf16 v[120:123], a[64:67], v[112:115], v[120:123]\n"
      "s_waitcnt lgkmcnt(0)\n"
      "v_mul_f32 v205, v205, v210\n"
      "v_cvt_pk_bf16_f32 v228, v204, v205\n"
      "v_mfma_f32_16x16x32_bf16 v[120:123], a[68:71], v[116:119], v[120:123]\n"
      "buffer_store_dwordx2 v[228:229], v7, s[8:11], s40 offen sc1\n"
      "v_mfma_f32_16x16x32_bf16 v[124:127], a[216:219], v[48:51], 0\n"
      "v_add_f32 v213, v213, v217\n"
      "v_fma_f32 v213, -v218, v218, v213\n"
      "v_mfma_f32_16x16x32_bf16 v[124:127], a[220:223], v[52:55], v[124:127]\n"
      "v_max_f32 v213, 0x3727c5ac, v213\n"
      "s_mov_b32 s45, s36\n"
      "v_mfma_f32_16x16x32_bf16 v[124:127], a[224:227], v[56:59], v[124:127]\n"
      "v_mov_b32 v187, v185\n"
      "v_mfma_f32_16x16x32_bf16 v[124:127], a[228:231], v[60:63], v[124:127]\n"
      "v_rsq_f32 v219, v213\n"
      "v_mfma_f32_16x16x32_bf16 v[124:127], a[232:235], v[64:67], v[124:127]\n"
      "s_add_u32 m0, s41, 40960\n"
      "ds_write_b64 v5, v[218:219] offset:0\n"
      "v_mfma_f32_16x16x32_bf16 v[124:127], a[236:239], v[68:71], v[124:127]\n"
      "s_waitcnt vmcnt(5)\n"
      "v_mov_b32 v186, v184\n"
      "v_cmp_ne_u32 vcc, s21, v15\n"
      "v_mfma_f32_16x16x32_bf16 v[124:127], a[240:243], v[72:75], v[124:127]\n"
      "s_min_u32 s45, s45, s6\n"
      "v_mfma_f32_16x16x32_bf16 v[124:127], a[244:247], v[76:79], v[124:127]\n"
      "v_cndmask_b32 v233, v219, 1.0, s[54:55]\n"
      "v_add_u32 v9, 1, v9\n"
      "v_mfma_f32_16x16x32_bf16 v[124:127], a[248:251], v[80:83], v[124:127]\n"
      "v_fma_f32 v123, -v218, v139, v123\n"
      "v_fma_f32 v122, -v218, v138, v122\n"
      "v_mfma_f32_16x16x32_bf16 v[124:127], a[252:255], v[84:87], v[124:127]\n"
      "v_fma_f32 v120, -v218, v136, v120\n"
      "v_fma_f32 v121, -v218, v137, v121\n"
      "v_mfma_f32_16x16x32_bf16 v[124:127], v[16:19], v[88:91], v[124:127]\n"
      "ds_read_b128 v[224:227], v6 offset:16\n"
      "v_mul_f32 v121, v219, v121\n"
      "v_mfma_f32_16x16x32_bf16 v[124:127], v[20:23], v[92:95], v[124:127]\n"
      "v_mul_f32 v120, v219, v120\n"
      "v_mfma_f32_16x16x32_bf16 v[124:127], v[24:27], v[96:99], v[124:127]\n"
      "s_cbranch_vccnz .Lsp_q2_4\n"
      ".Lbk_q2_4:\n"
      "v_mfma_f32_16x16x32_bf16 v[124:127], v[28:31], v[100:103], v[124:127]\n"
      "buffer_load_dword v15, v2, s[8:11], s45 offen sc1\n"
      "v_mfma_f32_16x16x32_bf16 v[124:127], v[32:35], v[104:107], v[124:127]\n"
      "v_mul_f32 v123, v219, v123\n"
      "v_mul_f32 v122, v219, v122\n"
      "v_mfma_f32_16x16x32_bf16 v[124:127], v[36:39], v[108:111], v[124:127]\n"
      "s_waitcnt vmcnt(4) lgkmcnt(1)\n"
      "ds_write_b32 v11, v9\n"
      "v_mfma_f32_16x16x32_bf16 v[124:127], v[40:43], v[112:115], v[124:127]\n"
      "v_mov_b32 v182, v180\n"
      "v_mfma_f32_16x16x32_bf16 v[124:127], v[44:47], v[116:119], v[124:127]\n"
      "v_cvt_pk_bf16_f32 v173, v122, v123\n"
      "v_mfma_f32_16x16x32_bf16 v[128:131], v[48:51], a[72:75], 0\n"
      "buffer_load_dwordx4 v0, s[8:11], s46 offen offset:1024 sc1 lds\n"
      "v_mfma_f32_16x16x32_bf16 v[128:131], v[52:55], a[76:79], v[128:131]\n"
      "ds_read_b128 v[220:223], v6 offset:0\n"
      "v_cvt_pk_bf16_f32 v172, v120, v121\n"
      "v_mfma_f32_16x16x32_bf16 v[128:131], v[56:59], a[80:83], v[128:131]\n"
      "v_fma_f32 v126, -v218, v142, v126\n"
      "v_fma_f32 v124, -v218, v140, v124\n"
      "v_mfma_f32_16x16x32_bf16 v[128:131], v[60:63], a[84:87], v[128:131]\n"
      "s_add_u32 s52, s52, 24576\n"
      "v_fma_f32 v125, -v218, v141, v125\n"
      "v_mul_f32 v124, v233, v124\n"
      "v_mfma_f32_16x16x32_bf16 v[128:131], v[64:67], a[88:91], v[128:131]\n"
      "v_mul_f32 v126, v219, v126\n"
      "v_mul_f32 v125, v233, v125\n"
      "v_mfma_f32_16x16x32_bf16 v[128:131], v[68:71], a[92:95], v[128:131]\n"
      "v_fma_f32 v127, -v218, v143, v127\n"
      "v_cvt_pk_bf16_f32 v175, v124, v125\n"
      "v_mfma_f32_16x16x32_bf16 v[128:131], v[72:75], a[96:99], v[128:131]\n"
      "v_mul_f32 v127, v219, v127\n"
      "v_cvt_pk_bf16_f32 v174, v126, v127\n"
      "v_mfma_f32_16x16x32_bf16 v[128:131], v[76:79], a[100:103], v[128:131]\n"
      "buffer_load_dwordx4 v0, s[8:11], s46 offen offset:2048 sc1 lds\n"
      "v_mfma_f32_16x16x32_bf16 v[192:195], v[172:175], v[176:179], v[156:159]\n"
      "ds_read_b32 v10, v12\n"
      "v_mfma_f32_16x16x32_bf16 v[196:199], v[172:175], v[164:167], v[168:171]\n"
      "v_mov_b32 v183, v181\n"
      "v_mov_b32 v178, v175\n"
      "v_mfma_f32_16x16x32_bf16 v[128:131], v[80:83], a[104:107], v[128:131]\n"
      "buffer_load_dwordx4 v0, s[8:11], s46 offen offset:0 sc1 lds\n"
      "v_mfma_f32_16x16x32_bf16 v[128:131], v[84:87], a[108:111], v[128:131]\n"
      "v_max_f32 v235, v194, v195\n"
      "v_max_f32 v234, v192, v193\n"
      "v_mfma_f32_16x16x32_bf16 v[128:131], v[88:91], a[112:115], v[128:131]\n"
      "v_max3_f32 v234, v188, v189, v234\n"
      "v_max3_f32 v235, v190, v191, v235\n"
      "v_mfma_f32_16x16x32_bf16 v[128:131], v[92:95], a[116:119], v[128:131]\n"
      "v_max_f32 v236, v234, v235\n"
      "v_mov_b32 v237, v236\n"
      "v_mfma_f32_16x16x32_bf16 v[128:131], v[96:99], a[120:123], v[128:131]\n"
      "v_permlane16_swap_b32 v236, v237\n"
      "v_mfma_f32_16x16x32_bf16 v[128:131], v[100:103], a[124:127], v[128:131]\n"
      "v_max_f32 v236, v236, v237\n"
      "v_mov_b32 v237, v236\n"
      "v_mfma_f32_16x16x32_bf16 v[128:131], v[104:107], a[128:131], v[128:131]\n"
      "s_waitcnt lgkmcnt(0)\n"
      "v_permlane32_swap_b32 v236, v237\n"
      "v_cmp_gt_i32 vcc, v9, v10\n"
      "v_mfma_f32_16x16x32_bf16 v[128:131], v[108:111], a[132:135], v[128:131]\n"
      "buffer_load_dwordx4 v0, s[8:11], s46 offen offset:3072 sc1 lds\n"
      "v_mfma_f32_16x16x32_bf16 v[128:131], v[112:115], a[136:139], v[128:131]\n"
      "s_add_u32 m0, s42, 40960\n"
      "v_max_f32 v236, v236, v237\n"
      "v_mul_f32 v236, v236, v232\n"
      "v_mfma_f32_16x16x32_bf16 v[128:131], v[116:119], a[140:143], v[128:131]\n"
      "s_add_u32 s40, s40, 24576\n"
      "v_fma_f32 v234, v234, v232, -v236\n"
      "v_fma_f32 v235, v235, v232, -v236\n"
      "v_mfma_f32_16x16x32_bf16 v[132:135], a[144:147], v[48:51], 0\n"
      "v_exp_f32 v234, v234\n"
      "v_mfma_f32_16x16x32_bf16 v[132:135], a[148:151], v[52:55], v[132:135]\n"
      "s_cbranch_vccnz .Lsp_qy1_5\n"
      ".Lbk_qy1_5:\n"
      "ds_read_b128 v[48:51], v0 offset:20480\n"
      "v_mfma_f32_16x16x32_bf16 v[132:135], a[152:155], v[56:59], v[132:135]\n"
      "v_exp_f32 v235, v235\n"
      "v_mfma_f32_16x16x32_bf16 v[132:135], a[156:159], v[60:63], v[132:135]\n"
      "ds_read_b128 v[56:59], v0 offset:22528\n"
      "v_fma_f32 v130, -v224, v147, v130\n"
      "v_mfma_f32_16x16x32_bf16 v[132:135], a[160:163], v[64:67], v[132:135]\n"
      "ds_read_b64 v[216:217], v4 offset:21504\n"
      "v_fma_f32 v129, -v222, v146, v129\n"
      "v_mfma_f32_16x16x32_bf16 v[132:135], a[164:167], v[68:71], v[132:135]\n"
      "buffer_load_dwordx4 v1, s[8:11], s46 offen offset:0 sc1 lds\n"
      "v_mfma_f32_16x16x32_bf16 v[132:135], a[168:171], v[72:75], v[132:135]\n"
      "v_cvt_pk_bf16_f32 v234, v234, v234\n"
      "v_fma_f32 v131, -v226, v144, v131\n"
      "v_mfma_f32_16x16x32_bf16 v[132:135], a[172:175], v[76:79], v[132:135]\n"
      "ds_read_b128 v[60:63], v0 offset:23552\n"
      "v_and_b32 v200, v160, v234\n"
      "v_mfma_f32_16x16x32_bf16 v[132:135], a[176:179], v[80:83], v[132:135]\n"
      "v_cvt_pk_bf16_f32 v235, v235, v235\n"
      "v_mul_f32 v129, v223, v129\n"
      "v_mfma_f32_16x16x32_bf16 v[132:135], a[180:183], v[84:87], v[132:135]\n"
      "ds_read_b128 v[52:55], v0 offset:21504\n"
      "v_mul_f32 v131, v227, v131\n"
      "v_mfma_f32_16x16x32_bf16 v[132:135], a[184:187], v[88:91], v[132:135]\n"
      "ds_read_b128 v[88:91], v0 offset:30720\n"
      "v_mul_f32 v130, v225, v130\n"
      "v_cvt_pk_bf16_f32 v185, v198, v199\n"
      "v_mfma_f32_16x16x32_bf16 v[132:135], a[188:191], v[92:95], v[132:135]\n"
      "v_cvt_pk_bf16_f32 v184, v196, v197\n"
      "v_and_b32 v201, v161, v235\n"
      "v_fma_f32 v128, -v220, v145, v128\n"
      "v_mfma_f32_16x16x32_bf16 v[132:135], a[192:195], v[96:99], v[132:135]\n"
      "ds_read_b128 v[68:71], v0 offset:25600\n"
      "v_and_b32 v203, v163, v235\n"
      "v_mfma_f32_16x16x32_bf16 v[132:135], a[196:199], v[100:103], v[132:135]\n"
      "v_mul_f32 v128, v221, v128\n"
      "v_and_b32 v202, v162, v234\n"
      "v_mfma_f32_16x16x32_bf16 v[132:135], a[200:203], v[104:107], v[132:135]\n"
      "v_cvt_pk_bf16_f32 v180, v128, v129\n"
      "v_cvt_pk_bf16_f32 v181, v130, v131\n"
      "v_mfma_f32_16x16x32_bf16 v[208:211], v[184:187], v[200:203], 0\n"
      "ds_read_b128 v[64:67], v0 offset:24576\n"
      "ds_read_b128 v[72:75], v0 offset:26624\n"
      "v_mfma_f32_16x16x32_bf16 v[204:207], v[180:183], v[200:203], 0\n"
      "s_waitcnt vmcnt(6) lgkmcnt(4)\n"
      "buffer_store_dword v13, v3, s[8:11], s52 offen sc1\n"
      "v_mfma_f32_16x16x32_bf16 v[132:135], a[204:207], v[108:111], v[132:135]\n"
      "ds_read2st64_b64 v[212:215], v4 offset0:40 offset1:41\n"
      "v_mfma_f32_16x16x32_bf16 v[132:135], a[208:211], v[112:115], v[132:135]\n"
      "v_rcp_f32 v210, v210\n"
      "v_mfma_f32_16x16x32_bf16 v[132:135], a[212:215], v[116:119], v[132:135]\n"
      "ds_read_b128 v[76:79], v0 offset:27648\n"
      "v_mul_f32 v209, v209, v210\n"
      "v_mfma_f32_16x16x32_bf16 v[120:123], a[0:3], v[48:51], 0\n"
      "ds_read_b128 v[80:83], v0 offset:28672\n"
      "ds_read_b128 v[100:103], v0 offset:33792\n"
      "v_mfma_f32_16x16x32_bf16 v[120:123], a[4:7], v[52:55], v[120:123]\n"
      "ds_read_b128 v[92:95], v0 offset:31744\n"
      "v_mul_f32 v208, v208, v210\n"
      "v_mfma_f32_16x16x32_bf16 v[120:123], a[8:11], v[56:59], v[120:123]\n"
      "ds_read_b128 v[84:87], v0 offset:29696\n"
      "v_fma_f32 v134, -v218, v150, v134\n"
      "v_mfma_f32_16x16x32_bf16 v[120:123], a[12:15], v[60:63], v[120:123]\n"
      "s_waitcnt lgkmcnt(6)\n"
      "ds_read_b128 v[104:107], v0 offset:34816\n"
      "v_fma_f32 v132, -v218, v148, v132\n"
      "v_mfma_f32_16x16x32_bf16 v[120:123], a[16:19], v[64:67], v[120:123]\n"
      "s_mov_b32 s50, s49\n"
      "ds_read_b128 v[96:99], v0 offset:32768\n"
      "v_mfma_f32_16x16x32_bf16 v[120:123], a[20:23], v[68:71], v[120:123]\n"
      "ds_read_b128 v[108:111], v0 offset:35840\n"
      "v_fma_f32 v133, -v218, v149, v133\n"
      "v_mfma_f32_16x16x32_bf16 v[120:123], a[24:27], v[72:75], v[120:123]\n"
      "s_waitcnt lgkmcnt(7)\n"
      "v_fma_f32 v135, -v218, v151, v135\n"
      "v_cvt_pk_bf16_f32 v177, v134, v135\n"
      "v_mfma_f32_16x16x32_bf16 v[120:123], a[28:31], v[76:79], v[120:123]\n"
      "s_waitcnt lgkmcnt(5)\n"
      "ds_read_b128 v[112:115], v0 offset:36864\n"
      "v_cvt_pk_bf16_f32 v176, v132, v133\n"
      "v_mfma_f32_16x16x32_bf16 v[120:123], a[32:35], v[80:83], v[120:123]\n"
      "v_mov_b32 v187, v185\n"
      "v_mul_f32 v232, 0x3eae1a93, v219\n"
      "v_mfma_f32_16x16x32_bf16 v[188:191], v[172:175], v[176:179], v[152:155]\n"
      "s_waitcnt lgkmcnt(4)\n"
      "ds_read_b128 v[116:119], v0 offset:37888\n"
      "v_mul_f32 v207, v207, v210\n"
      "v_mfma_f32_16x16x32_bf16 v[120:123], a[36:39], v[84:87], v[120:123]\n"
      "v_mul_f32 v204, v204, v210\n"
      "v_mfma_f32_16x16x32_bf16 v[120:123], a[40:43], v[88:91], v[120:123]\n"
      "v_cvt_pk_bf16_f32 v230, v208, v209\n"
      "v_add_f32 v213, v213, v215\n"
      "v_mfma_f32_16x16x32_bf16 v[120:123], a[44:47], v[92:95], v[120:123]\n"
      "s_waitcnt lgkmcnt(2)\n"
      "v_mul_f32 v205, v205, v210\n"
      "v_mfma_f32_16x16x32_bf16 v[120:123], a[48:51], v[96:99], v[120:123]\n"
      "buffer_store_dwordx2 v[230:231], v8, s[8:11], s40 offen sc1\n"
      "v_mfma_f32_16x16x32_bf16 v[120:123], a[52:55], v[100:103], v[120:123]\n"
      "v_add_f32 v212, v212, v214\n"
      "v_mfma_f32_16x16x32_bf16 v[120:123], a[56:59], v[104:107], v[120:123]\n"
      "v_cvt_pk_bf16_f32 v228, v204, v205\n"
      "v_mul_f32 v206, v206, v210\n"
      "v_mfma_f32_16x16x32_bf16 v[120:123], a[60:63], v[108:111], v[120:123]\n"
      "s_waitcnt vmcnt(7) lgkmcnt(0)\n"
      "v_cvt_pk_bf16_f32 v229, v206, v207\n"
      "v_cmp_ne_u32 vcc, s21, v15\n"
      "v_mfma_f32_16x16x32_bf16 v[120:123], a[64:67], v[112:115], v[120:123]\n"
      "s_add_u32 m0, s41, 0\n"
      "v_mov_b32 v182, v180\n"
      "v_mfma_f32_16x16x32_bf16 v[120:123], a[68:71], v[116:119], v[120:123]\n"
      "buffer_store_dwordx2 v[228:229], v7, s[8:11], s40 offen sc1\n"
      "v_mfma_f32_16x16x32_bf16 v[124:127], a[216:219], v[48:51], 0\n"
      "v_add_f32 v218, v212, v216\n"
      "v_add_f32_e64 v213, v213, v217\n"
      "v_mfma_f32_16x16x32_bf16 v[124:127], a[220:223], v[52:55], v[124:127]\n"
      "s_add_u32 s36, s36, 24576\n"
      "v_fma_f32 v213, -v218, v218, v213\n"
      "v_mfma_f32_16x16x32_bf16 v[124:127], a[224:227], v[56:59], v[124:127]\n"
      "s_mov_b32 s46, s45\n"
      "s_cbranch_vccnz .Lsp_q0_6\n"
      ".Lbk_q0_6:\n"
      "v_mfma_f32_16x16x32_bf16 v[124:127], a[228:231], v[60:63], v[124:127]\n"
      "buffer_load_dwordx4 v0, s[8:11], s46 offen offset:1024 sc1 lds\n"
      "v_mfma_f32_16x16x32_bf16 v[124:127], a[232:235], v[64:67], v[124:127]\n"
      "v_max_f32 v213, 0x3727c5ac, v213\n"
      "v_fma_f32 v120, -v218, v136, v120\n"
      "v_mfma_f32_16x16x32_bf16 v[124:127], a[236:239], v[68:71], v[124:127]\n"
      "v_mov_b32 v186, v184\n"
      "v_mfma_f32_16x16x32_bf16 v[124:127], a[240:243], v[72:75], v[124:127]\n"
      "v_add_u32 v9, 1, v9\n"
      "v_mfma_f32_16x16x32_bf16 v[124:127], a[244:247], v[76:79], v[124:127]\n"
      "s_add_u32 s40, s40, 24576\n"
      "v_fma_f32 v123, -v218, v139, v123\n"
      "v_mfma_f32_16x16x32_bf16 v[124:127], a[248:251], v[80:83], v[124:127]\n"
      "v_rsq_f32 v219, v213\n"
      "v_mfma_f32_16x16x32_bf16 v[124:127], a[252:255], v[84:87], v[124:127]\n"
      "v_mfma_f32_16x16x32_bf16 v[124:127], v[16:19], v[88:91], v[124:127]\n"
      "v_fma_f32 v121, -v218, v137, v121\n"
      "v_fma_f32 v122, -v218, v138, v122\n"
      "v_mfma_f32_16x16x32_bf16 v[124:127], v[20:23], v[92:95], v[124:127]\n"
      "buffer_load_dwordx4 v0, s[8:11], s46 offen offset:0 sc1 lds\n"
      "v_mfma_f32_16x16x32_bf16 v[124:127], v[24:27], v[96:99], v[124:127]\n"
      "v_mul_f32 v120, v219, v120\n"
      "v_mul_f32 v122, v219, v122\n"
      "v_mfma_f32_16x16x32_bf16 v[124:127], v[28:31], v[100:103], v[124:127]\n"
      "ds_write_b64 v5, v[218:219] offset:512\n"
      "v_mfma_f32_16x16x32_bf16 v[124:127], v[32:35], v[104:107], v[124:127]\n"
      "v_mul_f32 v121, v219, v121\n"
      "v_mul_f32 v123, v219, v123\n"
      "v_mfma_f32_16x16x32_bf16 v[124:127], v[36:39], v[108:111], v[124:127]\n"
      "buffer_load_dwordx4 v0, s[8:11], s46 offen offset:2048 sc1 lds\n"
      "v_mfma_f32_16x16x32_bf16 v[124:127], v[40:43], v[112:115], v[124:127]\n"
      "s_mov_b32 s45, s36\n"
      "ds_read_b128 v[224:227], v6 offset:528\n"
      "v_mfma_f32_16x16x32_bf16 v[124:127], v[44:47], v[116:119], v[124:127]\n"
      "s_min_u32 s45, s45, s6\n"
      "v_cndmask_b32 v233, v219, 1.0, s[54:55]\n"
      "v_mfma_f32_16x16x32_bf16 v[128:131], v[48:51], a[72:75], 0\n"
      "s_add_u32 s52, s52, 24576\n"
      "v_cvt_pk_bf16_f32 v172, v120, v121\n"
      "v_cvt_pk_bf16_f32 v173, v122, v123\n"
      "v_mfma_f32_16x16x32_bf16 v[128:131], v[52:55], a[76:79], v[128:131]\n"
      "buffer_load_dwordx4 v0, s[8:11], s46 offen offset:3072 sc1 lds\n"
      "v_mfma_f32_16x16x32_bf16 v[128:131], v[56:59], a[80:83], v[128:131]\n"
      "v_fma_f32 v126, -v218, v142, v126\n"
      "v_fma_f32 v124, -v218, v140, v124\n"
      "v_mul_f32 v126, v219, v126\n"
      "v_mfma_f32_16x16x32_bf16 v[128:131], v[60:63], a[84:87], v[128:131]\n"
      "v_fma_f32 v125, -v218, v141, v125\n"
      "v_mul_f32 v125, v233, v125\n"
      "v_mfma_f32_16x16x32_bf16 v[128:131], v[64:67], a[88:91], v[128:131]\n"
      "s_waitcnt vmcnt(7) lgkmcnt(1)\n"
      "ds_write_b32 v11, v9\n"
      "v_mfma_f32_16x16x32_bf16 v[128:131], v[68:71], a[92:95], v[128:131]\n"
      "v_fma_f32 v127, -v218, v143, v127\n"
      "v_mul_f32 v124, v233, v124\n"
      "v_mul_f32 v127, v219, v127\n"
      "v_mfma_f32_16x16x32_bf16 v[128:131], v[72:75], a[96:99], v[128:131]\n"
      "v_cvt_pk_bf16_f32 v174, v126, v127\n"
      "v_cvt_pk_bf16_f32 v175, v124, v125\n"
      "v_mfma_f32_16x16x32_bf16 v[128:131], v[76:79], a[100:103], v[128:131]\n"
      "buffer_load_dword v15, v2, s[8:11], s45 offen sc1\n"
      "v_mfma_f32_16x16x32_bf16 v[192:195], v[172:175], v[176:179], v[156:159]\n"
      "v_mov_b32 v183, v181\n"
      "v_mfma_f32_16x16x32_bf16 v[196:199], v[172:175], v[164:167], v[168:171]\n"
      "ds_read_b128 v[220:223], v6 offset:512\n"
      "s_add_u32 m0, s42, 0\n"
      "v_mfma_f32_16x16x32_bf16 v[128:131], v[80:83], a[104:107], v[128:131]\n"
      "buffer_load_dwordx4 v1, s[8:11], s46 offen offset:0 sc1 lds\n"
      "v_mfma_f32_16x16x32_bf16 v[128:131], v[84:87], a[108:111], v[128:131]\n"
      "v_max_f32 v235, v194, v195\n"
      "v_max3_f32 v235, v190, v191, v235\n"
      "v_mfma_f32_16x16x32_bf16 v[128:131], v[88:91], a[112:115], v[128:131]\n"
      "v_max_f32 v234, v192, v193\n"
      "v_max3_f32 v234, v188, v189, v234\n"
      "v_mfma_f32_16x16x32_bf16 v[128:131], v[92:95], a[116:119], v[128:131]\n"
      "ds_read_b32 v10, v12\n"
      "v_max_f32 v236, v234, v235\n"
      "v_mfma_f32_16x16x32_bf16 v[128:131], v[96:99], a[120:123], v[128:131]\n"
      "v_mov_b32 v237, v236\n"
      "v_cvt_pk_bf16_f32 v185, v198, v199\n"
      "v_mfma_f32_16x16x32_bf16 v[128:131], v[100:103], a[124:127], v[128:131]\n"
      "v_permlane16_swap_b32 v236, v237\n"
      "v_mfma_f32_16x16x32_bf16 v[128:131], v[104:107], a[128:131], v[128:131]\n"
      "v_max_f32 v236, v236, v237\n"
      "v_mov_b32 v237, v236\n"
      "v_mfma_f32_16x16x32_bf16 v[128:131], v[108:111], a[132:135], v[128:131]\n"
      "v_permlane32_swap_b32 v236, v237\n"
      "v_mfma_f32_16x16x32_bf16 v[128:131], v[112:115], a[136:139], v[128:131]\n"
      "s_waitcnt lgkmcnt(0)\n"
      "v_cmp_gt_i32 vcc, v9, v10\n"
      "v_max_f32 v236, v236, v237\n"
      "v_mfma_f32_16x16x32_bf16 v[128:131], v[116:119], a[140:143], v[128:131]\n"
      "v_mul_f32_e64 v236, v236, v232\n"
      "v_fma_f32 v235, v235, v232, -v236\n"
      "v_mfma_f32_16x16x32_bf16 v[132:135], a[144:147], v[48:51], 0\n"
      "v_exp_f32_e64 v235, v235\n"
      "v_mfma_f32_16x16x32_bf16 v[132:135], a[148:151], v[52:55], v[132:135]\n"
      "s_cbranch_vccnz .Lsp_qy2_7\n"
      ".Lbk_qy2_7:\n"
      "v_cvt_pk_bf16_f32 v235, v235, v235\n"
      "v_mfma_f32_16x16x32_bf16 v[132:135], a[152:155], v[56:59], v[132:135]\n"
      "v_fma_f32 v130, -v224, v147, v130\n"
      "v_mul_f32 v130, v225, v130\n"
      "v_mfma_f32_16x16x32_bf16 v[132:135], a[156:159], v[60:63], v[132:135]\n"
      "v_fma_f32 v129, -v222, v146, v129\n"
      "v_cvt_pk_bf16_f32 v184, v196, v197\n"
      "v_mfma_f32_16x16x32_bf16 v[132:135], a[160:163], v[64:67], v[132:135]\n"
      "v_fma_f32 v234, v234, v232, -v236\n"
      "v_fma_f32 v131, -v226, v144, v131\n"
      "v_mfma_f32_16x16x32_bf16 v[132:135], a[164:167], v[68:71], v[132:135]\n"
      "ds_read_b128 v[52:55], v0 offset:41984\n"
      "ds_read_b128 v[64:67], v0 offset:45056\n"
      "v_mfma_f32_16x16x32_bf16 v[132:135], a[168:171], v[72:75], v[132:135]\n"
      "ds_read_b128 v[56:59], v0 offset:43008\n"
      "v_mul_f32 v129, v223, v129\n"
      "v_mfma_f32_16x16x32_bf16 v[132:135], a[172:175], v[76:79], v[132:135]\n"
      "v_exp_f32 v234, v234\n"
      "v_mfma_f32_16x16x32_bf16 v[132:135], a[176:179], v[80:83], v[132:135]\n"
      "v_fma_f32 v128, -v220, v145, v128\n"
      "v_and_b32 v203, v163, v235\n"
      "v_mfma_f32_16x16x32_bf16 v[132:135], a[180:183], v[84:87], v[132:135]\n"
      "v_cvt_pk_bf16_f32 v234, v234, v234\n"
      "v_and_b32 v201, v161, v235\n"
      "v_mfma_f32_16x16x32_bf16 v[132:135], a[184:187], v[88:91], v[132:135]\n"
      "ds_read_b128 v[48:51], v0 offset:40960\n"
      "v_and_b32 v202, v162, v234\n"
      "v_mfma_f32_16x16x32_bf16 v[132:135], a[188:191], v[92:95], v[132:135]\n"
      "ds_read_b128 v[60:63], v0 offset:44032\n"
      "v_mul_f32 v128, v221, v128\n"
      "v_mfma_f32_16x16x32_bf16 v[132:135], a[192:195], v[96:99], v[132:135]\n"
      "ds_read_b128 v[76:79], v0 offset:48128\n"
      "v_cvt_pk_bf16_f32 v180, v128, v129\n"
      "v_mfma_f32_16x16x32_bf16 v[132:135], a[196:199], v[100:103], v[132:135]\n"
      "ds_read_b128 v[100:103], v0 offset:54272\n"
      "v_and_b32 v200, v160, v234\n"
      "v_mfma_f32_16x16x32_bf16 v[132:135], a[200:203], v[104:107], v[132:135]\n"
      "v_mul_f32 v131, v227, v131\n"
      "v_cvt_pk_bf16_f32 v181, v130, v131\n"
      "v_mfma_f32_16x16x32_bf16 v[208:211], v[184:187], v[200:203], 0\n"
      "s_waitcnt vmcnt(6) lgkmcnt(4)\n"
      "buffer_store_dword v13, v3, s[8:11], s52 offen sc1\n"
      "v_mfma_f32_16x16x32_bf16 v[204:207], v[180:183], v[200:203], 0\n"
      "ds_read_b128 v[84:87], v0 offset:50176\n"
      "ds_read_b128 v[72:75], v0 offset:47104\n"
      "v_mfma_f32_16x16x32_bf16 v[132:135], a[204:207], v[108:111], v[132:135]\n"
      "ds_read_b128 v[68:71], v0 offset:46080\n"
      "v_mov_b32 v178, v175\n"
      "v_mfma_f32_16x16x32_bf16 v[132:135], a[208:211], v[112:115], v[132:135]\n"
      "v_rcp_f32 v210, v210\n"
      "v_mfma_f32_16x16x32_bf16 v[132:135], a[212:215], v[116:119], v[132:135]\n"
      "s_waitcnt lgkmcnt(4)\n"
      "ds_read_b128 v[92:95], v0 offset:52224\n"
      "v_mul_f32 v205, v205, v210\n"
      "v_mfma_f32_16x16x32_bf16 v[120:123], a[0:3], v[48:51], 0\n"
      "s_add_u32 s36, s36, 24576\n"
      "ds_read_b128 v[80:83], v0 offset:49152\n"
      "v_mov_b32 v182, v180\n"
      "v_mfma_f32_16x16x32_bf16 v[120:123], a[4:7], v[52:55], v[120:123]\n"
      "ds_read_b128 v[96:99], v0 offset:53248\n"
      "v_mfma_f32_16x16x32_bf16 v[120:123], a[8:11], v[56:59], v[120:123]\n"
      "ds_read_b128 v[88:91], v0 offset:51200\n"
      "v_fma_f32 v132, -v218, v148, v132\n"
      "v_mfma_f32_16x16x32_bf16 v[120:123], a[12:15], v[60:63], v[120:123]\n"
      "s_cmp_eq_u32 s36, s37\n"
      "v_fma_f32 v133, -v218, v149, v133\n"
      "v_cvt_pk_bf16_f32 v176, v132, v133\n"
      "v_mfma_f32_16x16x32_bf16 v[120:123], a[16:19], v[64:67], v[120:123]\n"
      "s_waitcnt lgkmcnt(4)\n"
      "ds_read2st64_b64 v[212:215], v4 offset0:80 offset1:81\n"
      "s_cselect_b32 s47, s38, 0\n"
      "v_mfma_f32_16x16x32_bf16 v[120:123], a[20:23], v[68:71], v[120:123]\n"
      "s_cselect_b32 s37, s48, s37\n"
      "ds_read_b128 v[112:115], v0 offset:57344\n"
      "v_fma_f32 v135, -v218, v151, v135\n"
      "v_mfma_f32_16x16x32_bf16 v[120:123], a[24:27], v[72:75], v[120:123]\n"
      "s_cselect_b32 s49, s39, s53\n"
      "v_fma_f32 v134, -v218, v150, v134\n"
      "v_mov_b32 v183, v181\n"
      "v_mfma_f32_16x16x32_bf16 v[120:123], a[28:31], v[76:79], v[120:123]\n"
      "s_waitcnt lgkmcnt(4)\n"
      "ds_read_b128 v[104:107], v0 offset:55296\n"
      "v_cvt_pk_bf16_f32 v177, v134, v135\n"
      "v_mfma_f32_16x16x32_bf16 v[120:123], a[32:35], v[80:83], v[120:123]\n"
      "ds_read_b128 v[108:111], v0 offset:56320\n"
      "v_mul_f32 v206, v206, v210\n"
      "v_mfma_f32_16x16x32_bf16 v[188:191], v[172:175], v[176:179], v[152:155]\n"
      "ds_read_b64 v[216:217], v4 offset:41984\n"
      "v_mul_f32 v207, v207, v210\n"
      "v_mfma_f32_16x16x32_bf16 v[120:123], a[36:39], v[84:87], v[120:123]\n"
      "s_waitcnt lgkmcnt(5)\n"
      "s_add_u32 m0, s41, 20480\n"
      "ds_read_b128 v[116:119], v0 offset:58368\n"
      "v_mfma_f32_16x16x32_bf16 v[120:123], a[40:43], v[88:91], v[120:123]\n"
      "s_mov_b32 s46, s45\n"
      "s_add_u32 s45, s36, s47\n"
      "v_mul_f32 v209, v209, v210\n"
      "v_mfma_f32_16x16x32_bf16 v[120:123], a[44:47], v[92:95], v[120:123]\n"
      "v_cvt_pk_bf16_f32 v229, v206, v207\n"
      "v_mfma_f32_16x16x32_bf16 v[120:123], a[48:51], v[96:99], v[120:123]\n"
      "s_waitcnt lgkmcnt(4)\n"
      "v_add_f32 v213, v213, v215\n"
      "v_mfma_f32_16x16x32_bf16 v[120:123], a[52:55], v[100:103], v[120:123]\n"
      "s_waitcnt lgkmcnt(3)\n"
      "v_mul_f32 v232, 0x3eae1a93, v219\n"
      "v_mul_f32 v204, v204, v210\n"
      "v_mfma_f32_16x16x32_bf16 v[120:123], a[56:59], v[104:107], v[120:123]\n"
      "s_waitcnt lgkmcnt(2)\n"
      "v_mul_f32 v208, v208, v210\n"
      "v_cvt_pk_bf16_f32 v230, v208, v209\n"
      "v_mfma_f32_16x16x32_bf16 v[120:123], a[60:63], v[108:111], v[120:123]\n"
      "v_mfma_f32_16x16x32_bf16 v[120:123], a[64:67], v[112:115], v[120:123]\n"
      "s_waitcnt lgkmcnt(0)\n"
      "buffer_store_dwordx2 v[230:231], v8, s[8:11], s40 offen sc1\n"
      "v_mfma_f32_16x16x32_bf16 v[120:123], a[68:71], v[116:119], v[120:123]\n"
      "v_cvt_pk_bf16_f32 v228, v204, v205\n"
      "v_mfma_f32_16x16x32_bf16 v[124:127], a[216:219], v[48:51], 0\n"
      "v_add_f32 v212, v212, v214\n"
      "v_add_f32 v213, v213, v217\n"
      "v_mfma_f32_16x16x32_bf16 v[124:127], a[220:223], v[52:55], v[124:127]\n"
      "v_add_f32 v218, v212, v216\n"
      "v_fma_f32 v213, -v218, v218, v213\n"
      "v_mfma_f32_16x16x32_bf16 v[124:127], a[224:227], v[56:59], v[124:127]\n"
      "buffer_store_dwordx2 v[228:229], v7, s[8:11], s40 offen sc1\n"
      "v_mfma_f32_16x16x32_bf16 v[124:127], a[228:231], v[60:63], v[124:127]\n"
      "s_add_u32 s52, s52, 24576\n"
      "v_fma_f32 v123, -v218, v139, v123\n"
      "v_add_u32 v9, 1, v9\n"
      "v_mfma_f32_16x16x32_bf16 v[124:127], a[232:235], v[64:67], v[124:127]\n"
      "v_fma_f32 v122, -v218, v138, v122\n"
      "v_mfma_f32_16x16x32_bf16 v[124:127], a[236:239], v[68:71], v[124:127]\n"
      "s_waitcnt vmcnt(4)\n"
      "v_max_f32 v213, 0x3727c5ac, v213\n"
      "v_cmp_ne_u32 vcc, s21, v15\n"
      "v_mfma_f32_16x16x32_bf16 v[124:127], a[240:243], v[72:75], v[124:127]\n"
      "v_rsq_f32 v219, v213\n"
      "v_mfma_f32_16x16x32_bf16 v[124:127], a[244:247], v[76:79], v[124:127]\n"
      "v_mov_b32 v186, v184\n"
      "v_fma_f32 v120, -v218, v136, v120\n"
      "v_mfma_f32_16x16x32_bf16 v[124:127], a[248:251], v[80:83], v[124:127]\n"
      "s_min_u32 s45, s45, s6\n"
      "ds_write_b64 v5, v[218:219] offset:1024\n"
      "v_mfma_f32_16x16x32_bf16 v[124:127], a[252:255], v[84:87], v[124:127]\n"
      "s_cbranch_vccnz .Lsp_q1_8\n"
      ".Lbk_q1_8:\n"
      "v_mfma_f32_16x16x32_bf16 v[124:127], v[16:19], v[88:91], v[124:127]\n"
      "buffer_load_dwordx4 v0, s[8:11], s46 offen offset:1024 sc1 lds\n"
      "v_mfma_f32_16x16x32_bf16 v[124:127], v[20:23], v[92:95], v[124:127]\n"
      "v_cndmask_b32 v233, v219, 1.0, s[54:55]\n"
      "v_mul_f32 v123, v219, v123\n"
      "v_mfma_f32_16x16x32_bf16 v[124:127], v[24:27], v[96:99], v[124:127]\n"
      "ds_read_b128 v[220:223], v6 offset:1024\n"
      "v_mul_f32 v120, v219, v120\n"
      "v_mfma_f32_16x16x32_bf16 v[124:127], v[28:31], v[100:103], v[124:127]\n"
      "v_mul_f32 v122, v219, v122\n"
      "v_fma_f32 v121, -v218, v137, v121\n"
      "v_mfma_f32_16x16x32_bf16 v[124:127], v[32:35], v[104:107], v[124:127]\n"
      "s_waitcnt vmcnt(4)\n"
      "ds_write_b32 v11, v9\n"
      "v_mfma_f32_16x16x32_bf16 v[124:127], v[36:39], v[108:111], v[124:127]\n"
      "buffer_load_dwordx4 v0, s[8:11], s46 offen offset:0 sc1 lds\n"
      "v_mfma_f32_16x16x32_bf16 v[124:127], v[40:43], v[112:115], v[124:127]\n"
      "v_mfma_f32_16x16x32_bf16 v[124:127], v[44:47], v[116:119], v[124:127]\n"
      "v_cvt_pk_bf16_f32 v173, v122, v123\n"
      "v_mfma_f32_16x16x32_bf16 v[128:131], v[48:51], a[72:75], 0\n"
      "v_mul_f32 v121, v219, v121\n"
      "v_cvt_pk_bf16_f32 v172, v120, v121\n"
      "v_mfma_f32_16x16x32_bf16 v[128:131], v[52:55], a[76:79], v[128:131]\n"
      "buffer_load_dwordx4 v0, s[8:11], s46 offen offset:3072 sc1 lds\n"
      "v_mfma_f32_16x16x32_bf16 v[128:131], v[56:59], a[80:83], v[128:131]\n"
      "v_fma_f32 v125, -v218, v141, v125\n"
      "v_fma_f32 v124, -v218, v140, v124\n"
      "v_mfma_f32_16x16x32_bf16 v[128:131], v[60:63], a[84:87], v[128:131]\n"
      "v_fma_f32 v127, -v218, v143, v127\n"
      "v_mul_f32 v125, v233, v125\n"
      "v_mfma_f32_16x16x32_bf16 v[128:131], v[64:67], a[88:91], v[128:131]\n"
      "v_mul_f32 v124, v233, v124\n"
      "v_mul_f32 v127, v219, v127\n"
      "v_mfma_f32_16x16x32_bf16 v[128:131], v[68:71], a[92:95], v[128:131]\n"
      "v_fma_f32 v126, -v218, v142, v126\n"
      "v_mul_f32 v126, v219, v126\n"
      "v_mfma_f32_16x16x32_bf16 v[128:131], v[72:75], a[96:99], v[128:131]\n"
      "v_cvt_pk_bf16_f32 v175, v124, v125\n"
      "v_cvt_pk_bf16_f32 v174, v126, v127\n"
      "v_mfma_f32_16x16x32_bf16 v[128:131], v[76:79], a[100:103], v[128:131]\n"
      "buffer_load_dword v15, v2, s[8:11], s45 offen sc1\n"
      "v_mfma_f32_16x16x32_bf16 v[192:195], v[172:175], v[176:179], v[156:159]\n"
      "ds_read_b128 v[224:227], v6 offset:1040\n"
      "v_mov_b32 v187, v185\n"
      "v_mfma_f32_16x16x32_bf16 v[196:199], v[172:175], v[164:167], v[168:171]\n"
      "ds_read_b32 v10, v12\n"
      "v_mfma_f32_16x16x32_bf16 v[128:131], v[80:83], a[104:107], v[128:131]\n"
      "buffer_load_dwordx4 v0, s[8:11], s46 offen offset:2048 sc1 lds\n"
      "v_mfma_f32_16x16x32_bf16 v[128:131], v[84:87], a[108:111], v[128:131]\n"
      "v_med3_f32 v234, v192, v193, s50\n"
      "v_med3_f32 v235, v194, v195, s50\n"
      "v_mfma_f32_16x16x32_bf16 v[128:131], v[88:91], a[112:115], v[128:131]\n"
      "v_cvt_pk_bf16_f32 v185, v198, v199\n"
      "v_max3_f32 v234, v188, v189, v234\n"
      "v_mfma_f32_16x16x32_bf16 v[128:131], v[92:95], a[116:119], v[128:131]\n"
      "v_max3_f32 v235, v190, v191, v235\n"
      "v_max_f32 v236, v234, v235\n"
      "v_mfma_f32_16x16x32_bf16 v[128:131], v[96:99], a[120:123], v[128:131]\n"
      "s_waitcnt lgkmcnt(0)\n"
      "v_mov_b32 v237, v236\n"
      "v_cmp_gt_i32 vcc, v9, v10\n"
      "v_mfma_f32_16x16x32_bf16 v[128:131], v[100:103], a[124:127], v[128:131]\n"
      "s_add_u32 m0, s42, 20480\n"
      "v_permlane16_swap_b32 v236, v237\n"
      "v_mfma_f32_16x16x32_bf16 v[128:131], v[104:107], a[128:131], v[128:131]\n"
      "v_max_f32 v236, v236, v237\n"
      "v_mov_b32 v237, v236\n"
      "v_mfma_f32_16x16x32_bf16 v[128:131], v[108:111], a[132:135], v[128:131]\n"
      "buffer_load_dwordx4 v1, s[8:11], s46 offen offset:0 sc1 lds\n"
      "v_mfma_f32_16x16x32_bf16 v[128:131], v[112:115], a[136:139], v[128:131]\n"
      ".Lq_p1:\n"
      "s_cbranch_execz .Lq_pr\n"
      "v_permlane32_swap_b32 v236, v237\n"
      "v_mfma_f32_16x16x32_bf16 v[128:131], v[116:119], a[140:143], v[128:131]\n"
      "v_max_f32 v236, v236, v237\n"
      "v_mul_f32_e64 v236, v236, v232\n"
      "v_mfma_f32_16x16x32_bf16 v[132:135], a[144:147], v[48:51], 0\n"
      "s_cbranch_vccnz .Lsp_qy0_9\n"
      ".Lbk_qy0_9:\n"
      "v_fma_f32 v234, v234, v232, -v236\n"
      "v_mfma_f32_16x16x32_bf16 v[132:135], a[148:151], v[52:55], v[132:135]\n"
      "ds_read_b128 v[52:55], v0 offset:1024\n"
      "ds_read_b128 v[48:51], v0 offset:0\n"
      "v_mfma_f32_16x16x32_bf16 v[132:135], a[152:155], v[56:59], v[132:135]\n"
      "v_fma_f32 v131, -v226, v144, v131\n"
      "v_fma_f32 v235, v235, v232, -v236\n"
      "v_mfma_f32_16x16x32_bf16 v[132:135], a[156:159], v[60:63], v[132:135]\n"
      "ds_read_b128 v[56:59], v0 offset:2048\n"
      "v_fma_f32 v128, -v220, v145, v128\n"
      "v_mfma_f32_16x16x32_bf16 v[132:135], a[160:163], v[64:67], v[132:135]\n"
      "v_exp_f32 v234, v234\n"
      "v_mfma_f32_16x16x32_bf16 v[132:135], a[164:167], v[68:71], v[132:135]\n"
      "v_exp_f32 v235, v235\n"
      "v_mfma_f32_16x16x32_bf16 v[132:135], a[168:171], v[72:75], v[132:135]\n"
      "v_cvt_pk_bf16_f32 v234, v234, v234\n"
      "v_fma_f32 v129, -v222, v146, v129\n"
      "v_mfma_f32_16x16x32_bf16 v[132:135], a[172:175], v[76:79], v[132:135]\n"
      "v_mul_f32 v129, v223, v129\n"
      "v_fma_f32 v130, -v224, v147, v130\n"
      "v_mfma_f32_16x16x32_bf16 v[132:135], a[176:179], v[80:83], v[132:135]\n"
      "v_cvt_pk_bf16_f32 v235, v235, v235\n"
      "v_and_b32 v200, v160, v234\n"
      "v_mfma_f32_16x16x32_bf16 v[132:135], a[180:183], v[84:87], v[132:135]\n"
      "v_cvt_pk_bf16_f32 v184, v196, v197\n"
      "v_and_b32 v201, v161, v235\n"
      "v_mfma_f32_16x16x32_bf16 v[132:135], a[184:187], v[88:91], v[132:135]\n"
      "ds_read_b128 v[68:71], v0 offset:5120\n"
      "v_mul_f32 v130, v225, v130\n"
      "v_mfma_f32_16x16x32_bf16 v[132:135], a[188:191], v[92:95], v[132:135]\n"
      "v_and_b32 v203, v163, v235\n"
      "v_mul_f32_e64 v131, v227, v131\n"
      "v_mfma_f32_16x16x32_bf16 v[132:135], a[192:195], v[96:99], v[132:135]\n"
      "ds_read_b128 v[64:67], v0 offset:4096\n"
      "v_mul_f32_e64 v128, v221, v128\n"
      "v_mfma_f32_16x16x32_bf16 v[132:135], a[196:199], v[100:103], v[132:135]\n"
      "v_and_b32_e64 v202, v162, v234\n"
      "v_cvt_pk_bf16_f32 v181, v130, v131\n"
      "v_mfma_f32_16x16x32_bf16 v[132:135], a[200:203], v[104:107], v[132:135]\n"
      "s_add_u32 s40, s40, 24576\n"
      "ds_read_b128 v[60:63], v0 offset:3072\n"
      "v_cvt_pk_bf16_f32 v180, v128, v129\n"
      "v_mfma_f32_16x16x32_bf16 v[208:211], v[184:187], v[200:203], 0\n"
      "s_waitcnt vmcnt(6) lgkmcnt(3)\n"
      "buffer_store_dword v13, v3, s[8:11], s52 offen sc1\n"
      "ds_read2st64_b64 v[212:215], v4 offset0:0 offset1:1\n"
      "v_mfma_f32_16x16x32_bf16 v[204:207], v[180:183], v[200:203], 0\n"
      "ds_read_b128 v[72:75], v0 offset:6144\n"
      "v_mfma_f32_16x16x32_bf16 v[132:135], a[204:207], v[108:111], v[132:135]\n"
      "ds_read_b128 v[76:79], v0 offset:7168\n"
      "v_mov_b32_e64 v178, v175\n"
      "v_mfma_f32_16x16x32_bf16 v[132:135], a[208:211], v[112:115], v[132:135]\n"
      "ds_read_b128 v[88:91], v0 offset:10240\n"
      "ds_read_b128 v[108:111], v0 offset:15360\n"
      "v_mfma_f32_16x16x32_bf16 v[132:135], a[212:215], v[116:119], v[132:135]\n"
      "s_sub_u32 s20, s20, 1\n"
      "s_cbranch_scc0 .Lq_loop\n"
      ".Lq_tail:\n"
      "v_mul_f32 v232, 0x3eae1a93, v219\n"
      "s_nop 7\n"
      "v_fma_f32 v135, -v218, v151, v135\n"
      "v_fma_f32 v133, -v218, v149, v133\n"
      "v_fma_f32 v134, -v218, v150, v134\n"
      "v_fma_f32 v132, -v218, v148, v132\n"
      "v_cvt_pk_bf16_f32 v176, v132, v133\n"
      "v_cvt_pk_bf16_f32 v177, v134, v135\n"
      "v_rcp_f32 v210, v210\n"
      "s_nop 0\n"
      "v_mul_f32 v204, v204, v210\n"
      "v_mfma_f32_16x16x32_bf16 v[188:191], v[172:175], v[176:179], v[152:155]\n"
      "v_mul_f32 v208, v208, v210\n"
      "v_mul_f32 v207, v207, v210\n"
      "v_mul_f32 v209, v209, v210\n"
      "v_cvt_pk_bf16_f32 v230, v208, v209\n"
      "buffer_store_dwordx2 v[230:231], v8, s[8:11], s40 offen sc1\n"
      "v_mul_f32 v206, v206, v210\n"
      "v_cvt_pk_bf16_f32 v229, v206, v207\n"
      "v_mul_f32 v205, v205, v210\n"
      "v_cvt_pk_bf16_f32 v228, v204, v205\n"
      "buffer_store_dwordx2 v[228:229], v7, s[8:11], s40 offen sc1\n"
      "v_mov_b32 v187, v185\n"
      "v_mov_b32 v186, v184\n"
      "v_mov_b32 v182, v180\n"
      "s_add_u32 s52, s52, 24576\n"
      "v_mov_b32 v183, v181\n"
      "s_add_u32 s40, s40, 24576\n"
      "s_waitcnt vmcnt(0)\n"
      "buffer_store_dword v13, v3, s[8:11], s52 offen sc1\n"
      "s_add_u32 s52, s52, 24576\n"
      "s_cmp_lg_u32 s44, 0\n"
      "s_cbranch_scc1 .Lq_done\n"
      "v_lshlrev_b32 v48, 3, v0\n"
      "s_mov_b32 s22, s23\n"
      ".Lq_up:\n"
      "s_mov_b32 s30, 128\n"
      "buffer_load_dword v49, v48, s[8:11], s30 offen sc1\n"
      "s_mov_b32 s30, 8320\n"
      "buffer_load_dword v50, v48, s[8:11], s30 offen sc1\n"
      "s_mov_b32 s30, 16512\n"
      "buffer_load_dword v51, v48, s[8:11], s30 offen sc1\n"
      "s_mov_b32 s30, 24704\n"
      "buffer_load_dword v52, v48, s[8:11], s30 offen sc1\n"
      "s_waitcnt vmcnt(0)\n"
      "v_xor_b32 v49, s21, v49\n"
      "v_xor_b32 v50, s21, v50\n"
      "v_xor_b32 v51, s21, v51\n"
      "v_xor_b32 v52, s21, v52\n"
      "v_or3_b32 v49, v49, v50, v51\n"
      "v_or_b32 v49, v49, v52\n"
      "v_cmp_ne_u32 vcc, 0, v49\n"
      "s_cbranch_vccz .Lq_ok\n"
      "s_sleep 4\n"
      "s_sub_u32 s22, s22, 1\n"
      "s_cbranch_scc0 .Lq_up\n"
      ".Lq_ok:\n"
      "v_lshrrev_b32 v48, 2, v0\n"
      "v_and_b32 v48, 0x7c, v48\n"
      "v_mov_b32 v49, s21\n"
      "buffer_store_dword v49, v48, s[8:11], 0 offen sc1\n"
      ".Lq_done:\n"
      "s_endpgm\n"
      ".Lsp_q0_1:\n"
      "s_mov_b32 s22, s23\n"
      ".Lsl_q0_1:\n"
      "buffer_load_dword v15, v2, s[8:11], s46 offen sc1\n"
      "s_waitcnt vmcnt(0)\n"
      "v_cmp_ne_u32 vcc, s21, v15\n"
      "s_cbranch_vccz .Lbk_q0_1\n"
      "s_sleep 4\n"
      "s_sub_u32 s22, s22, 1\n"
      "s_cbranch_scc0 .Lsl_q0_1\n"
      "s_mov_b32 s23, 0\n"
      "s_branch .Lbk_q0_1\n"
      ".Lsp_q1_2:\n"
      "s_mov_b32 s22, s23\n"
      ".Lsl_q1_2:\n"
      "buffer_load_dword v15, v2, s[8:11], s46 offen sc1\n"
      "s_waitcnt vmcnt(0)\n"
      "v_cmp_ne_u32 vcc, s21, v15\n"
      "s_cbranch_vccz .Lbk_q1_2\n"
      "s_sleep 4\n"
      "s_sub_u32 s22, s22, 1\n"
      "s_cbranch_scc0 .Lsl_q1_2\n"
      "s_mov_b32 s23, 0\n"
      "s_branch .Lbk_q1_2\n"
      ".Lsp_qy0_3:\n"
      "s_lshl_b32 s22, s23, 4\n"
      ".Lsl_qy0_3:\n"
      "ds_read_b32 v10, v12\n"
      "s_waitcnt lgkmcnt(0)\n"
      "v_cmp_gt_i32 vcc, v9, v10\n"
      "s_cbranch_vccz .Lbk_qy0_3\n"
      "s_sub_u32 s22, s22, 1\n"
      "s_cbranch_scc0 .Lsl_qy0_3\n"
      "s_mov_b32 s23, 0\n"
      "s_branch .Lbk_qy0_3\n"
      ".Lsp_q2_4:\n"
      "s_mov_b32 s22, s23\n"
      "buffer_load_dword v15, v2, s[8:11], s46 offen sc1\n"
      "s_waitcnt vmcnt(0)\n"
      "v_cmp_ne_u32 vcc, s21, v15\n"
      "s_add_u32 s30, s52, 24576\n"
      "buffer_store_dword v13, v3, s[8:11], s30 offen sc1\n"
      "s_cbranch_vccz .Lbk_q2_4\n"
      ".Lsl_q2_4:\n"
      "buffer_load_dword v15, v2, s[8:11], s46 offen sc1\n"
      "s_waitcnt vmcnt(0)\n"
      "v_cmp_ne_u32 vcc, s21, v15\n"
      "s_cbranch_vccz .Lbk_q2_4\n"
      "s_sleep 4\n"
      "s_sub_u32 s22, s22, 1\n"
      "s_cbranch_scc0 .Lsl_q2_4\n"
      "s_mov_b32 s23, 0\n"
      "s_branch .Lbk_q2_4\n"
      ".Lsp_qy1_5:\n"
      "s_lshl_b32 s22, s23, 4\n"
      ".Lsl_qy1_5:\n"
      "ds_read_b32 v10, v12\n"
      "s_waitcnt lgkmcnt(0)\n"
      "v_cmp_gt_i32 vcc, v9, v10\n"
      "s_cbranch_vccz .Lbk_qy1_5\n"
      "s_sub_u32 s22, s22, 1\n"
      "s_cbranch_scc0 .Lsl_qy1_5\n"
      "s_mov_b32 s23, 0\n"
      "s_branch .Lbk_qy1_5\n"
      ".Lsp_q0_6:\n"
      "s_mov_b32 s22, s23\n"
      "buffer_load_dword v15, v2, s[8:11], s46 offen sc1\n"
      "s_waitcnt vmcnt(0)\n"
      "v_cmp_ne_u32 vcc, s21, v15\n"
      "s_add_u32 s30, s52, 24576\n"
      "buffer_store_dword v13, v3, s[8:11], s30 offen sc1\n"
      "s_cbranch_vccz .Lbk_q0_6\n"
      ".Lsl_q0_6:\n"
      "buffer_load_dword v15, v2, s[8:11], s46 offen sc1\n"
      "s_waitcnt vmcnt(0)\n"
      "v_cmp_ne_u32 vcc, s21, v15\n"
      "s_cbranch_vccz .Lbk_q0_6\n"
      "s_sleep 4\n"
      "s_sub_u32 s22, s22, 1\n"
      "s_cbranch_scc0 .Lsl_q0_6\n"
      "s_mov_b32 s23, 0\n"
      "s_branch .Lbk_q0_6\n"
      ".Lsp_qy2_7:\n"
      "s_lshl_b32 s22, s23, 4\n"
      ".Lsl_qy2_7:\n"
      "ds_read_b32 v10, v12\n"
      "s_waitcnt lgkmcnt(0)\n"
      "v_cmp_gt_i32 vcc, v9, v10\n"
      "s_cbranch_vccz .Lbk_qy2_7\n"
      "s_sub_u32 s22, s22, 1\n"
      "s_cbranch_scc0 .Lsl_qy2_7\n"
      "s_mov_b32 s23, 0\n"
      "s_branch .Lbk_qy2_7\n"
      ".Lsp_q1_8:\n"
      "s_mov_b32 s22, s23\n"
      "buffer_load_dword v15, v2, s[8:11], s46 offen sc1\n"
      "s_waitcnt vmcnt(0)\n"
      "v_cmp_ne_u32 vcc, s21, v15\n"
      "s_add_u32 s30, s52, 0\n"
      "buffer_store_dword v13, v3, s[8:11], s30 offen sc1\n"
      "s_cbranch_vccz .Lbk_q1_8\n"
      ".Lsl_q1_8:\n"
      "buffer_load_dword v15, v2, s[8:11], s46 offen sc1\n"
      "s_waitcnt vmcnt(0)\n"
      "v_cmp_ne_u32 vcc, s21, v15\n"
      "s_cbranch_vccz .Lbk_q1_8\n"
      "s_sleep 4\n"
      "s_sub_u32 s22, s22, 1\n"
      "s_cbranch_scc0 .Lsl_q1_8\n"
      "s_mov_b32 s23, 0\n"
      "s_branch .Lbk_q1_8\n"
      ".Lsp_qy0_9:\n"
      "s_lshl_b32 s22, s23, 4\n"
      ".Lsl_qy0_9:\n"
      "ds_read_b32 v10, v12\n"
      "s_waitcnt lgkmcnt(0)\n"
      "v_cmp_gt_i32 vcc, v9, v10\n"
      "s_cbranch_vccz .Lbk_qy0_9\n"
      "s_sub_u32 s22, s22, 1\n"
      "s_cbranch_scc0 .Lsl_qy0_9\n"
      "s_mov_b32 s23, 0\n"
      "s_branch .Lbk_qy0_9\n"
      ".Lq_last:\n"
      :
      : "{s[8:11]}"(rsWS), "{s[12:15]}"(rsA), "{v0}"(v_l16), "{v1}"(v_l5), "{v2}"(v_fa), "{v3}"(v_ra), "{v4}"(v_st), "{v5}"(v_tw), "{v6}"(v_tr), "{v7}"(v_om), "{v8}"(v_oe), "{v10}"(v_al), "{v11}"(v_yw), "{v12}"(v_yr), "{v14}"(v_hi), "{v152}"(v_bc[0]), "{v153}"(v_bc[1]), "{v154}"(v_bc[2]), "{v155}"(v_bc[3]), "{v156}"(v_bp[0]), "{v157}"(v_bp[1]), "{v158}"(v_bp[2]), "{v159}"(v_bp[3]), "{v160}"(v_mk[0]), "{v161}"(v_mk[1]), "{v162}"(v_mk[2]), "{v163}"(v_mk[3]), "{v164}"(v_sel[0]), "{v165}"(v_sel[1]), "{v166}"(v_sel[2]), "{v167}"(v_sel[3]), "{v168}"(v_ci[0]), "{v169}"(v_ci[1]), "{v170}"(v_ci[2]), "{v171}"(v_ci[3]), "{v48}"(v_vo[0]), "{v49}"(v_vo[1]), "{v50}"(v_vo[2]), "{v51}"(v_vo[3]), "{v52}"(v_at), "{v53}"(v_ax), "{v54}"(v_pm), "{v55}"(v_ms), "{v76}"(v_pw), "{s36}"(s_sx), "{s37}"(s_ev), "{s38}"(s_del), "{s39}"(s_kil), "{s40}"(s_so), "{s41}"(s_m4), "{s42}"(s_m5), "{s43}"(s_ms), "{s44}"(s_up)
      : "v255", "a255", "s99", "vcc", "scc", "memory");

  } else if (fast && slot < 10) {
    const unsigned gr = e, part = slot - 8;
    COMMON(32, 81920);
    const unsigned c = part, o = w & 1;
    const u32x4 rsA = srd(Wo, 576 * 576 * 2);
    const unsigned v_l5 = v_l16 + 16384 - w * 3072, e0 = 16384 + o * 2048 + g * 512 + i16 * 8, e1 = e0 + 2048 - o * 4096;
    const unsigned v_ex[8] = {e0, e1, e0 + 20480, e1 + 20480, e0 + 40960, e1 + 40960, e0 + 61440, e1 + 61440}, v_l2 = v_l16 + 40960;
    const unsigned v_fa = 20480 - w * 4096 + (8 * w + (lane >> 3)) * 128 + (lane & 7) * 4, v_ra = up * 8 * REC + l5 + 19456 + (4 * c + w) * 128;
    const unsigned v_xp = (9 * c + 2 * w) * 1024 + v_l16, v_xs = (9 * c + 8) * 1024 + (w >> 1) * 512 + half;
    const unsigned v_rp = v_xp + WS_XIN, v_rs = v_xs + WS_XIN;
    const unsigned v_ew = 82944 + (w >> 1) * 1024 + lane * 4;
    const unsigned v_sw = 86016 + i16 * 144 + (4 * w + g) * 8, v_sr = 86016 + (8 * (w >> 1) + l8) * 144 + l7;
    const unsigned v_ss = 18432 + c * 512 + (w >> 1) * 256 + l8 * 32 + p * 8;
    const unsigned v_pw = 19456 + (4 * c + w) * 128 + l8 * REC + l7;
    unsigned v_vo[9], v_vp[9];
#pragma unroll
    for (unsigned j = 0; j < 9; ++j) {
      const unsigned ch = 64 * j + lane, r = ch / 36, x = 576 * c + ch % 36 * 16 - 1024 * (j & 3);
      v_vo[j] = (20 * o + r) * 1152 + x;
      v_vp[j] = (36 * (4 * o + (r >> 2)) + 16 + (r & 3)) * 1152 + x;
    }
    const unsigned ro = (8 * g + q) * 576;
    const unsigned v_t0 = ro + o * 18432 + 128 * w + 16 * p, v_t1 = v_t0 + 18432 - 2 * o * 18432, v_ts = ro + o * 18432 + 512 + 32 * (w >> 1) + 8 * p;
    const unsigned s_sx = WS_OB + (gr * NREC + KSK) * REC + w * 4096, s_ev = s_sx + (31 - KSK) * REC, s_sy = WS_X1 + (gr * NREC + KSK) * REC;
    const unsigned s_m4 = w * 4096, s_m5 = 16384 + w * 1024, s_m9 = w * 9216, s_so = (w >> 1) * 8 * 41472;
    const unsigned s_s8 = s_so, s_wg = o, s_rb = WS_OB + w * 4096;
    asm volatile(
      "s_buffer_load_dword s21, s[8:11], 0\n"
      "s_mov_b32 s23, 65536\n"
      "s_mov_b32 s31, 0x40000000\n"
      "s_add_u32 m0, s41, 0\n"
      "s_nop 0\n"
      "buffer_load_dwordx4 v100, s[12:15], s42 offen offset:0 lds\n"
      "buffer_load_dwordx4 v101, s[12:15], s42 offen offset:1024 lds\n"
      "buffer_load_dwordx4 v102, s[12:15], s42 offen offset:2048 lds\n"
      "buffer_load_dwordx4 v103, s[12:15], s42 offen offset:3072 lds\n"
      "s_add_u32 m0, s41, 4096\n"
      "s_nop 0\n"
      "buffer_load_dwordx4 v104, s[12:15], s42 offen offset:0 lds\n"
      "buffer_load_dwordx4 v105, s[12:15], s42 offen offset:1024 lds\n"
      "buffer_load_dwordx4 v106, s[12:15], s42 offen offset:2048 lds\n"
      "buffer_load_dwordx4 v107, s[12:15], s42 offen offset:3072 lds\n"
      "s_add_u32 m0, s41, 8192\n"
      "s_nop 0\n"
      "buffer_load_dwordx4 v108, s[12:15], s42 offen offset:0 lds\n"
      "s_add_u32 s42, s42, 41472\n"
      "s_add_u32 m0, s41, 36864\n"
      "s_nop 0\n"
      "buffer_load_dwordx4 v100, s[12:15], s42 offen offset:0 lds\n"
      "buffer_load_dwordx4 v101, s[12:15], s42 offen offset:1024 lds\n"
      "buffer_load_dwordx4 v102, s[12:15], s42 offen offset:2048 lds\n"
      "buffer_load_dwordx4 v103, s[12:15], s42 offen offset:3072 lds\n"
      "s_add_u32 m0, s41, 40960\n"
      "s_nop 0\n"
      "buffer_load_dwordx4 v104, s[12:15], s42 offen offset:0 lds\n"
      "buffer_load_dwordx4 v105, s[12:15], s42 offen offset:1024 lds\n"
      "buffer_load_dwordx4 v106, s[12:15], s42 offen offset:2048 lds\n"
      "buffer_load_dwordx4 v107, s[12:15], s42 offen offset:3072 lds\n"
      "s_add_u32 m0, s41, 45056\n"
      "s_nop 0\n"
      "buffer_load_dwordx4 v108, s[12:15], s42 offen offset:0 lds\n"
      "s_add_u32 s42, s42, 41472\n"
      "s_add_u32 m0, s41, 73728\n"
      "s_nop 0\n"
      "buffer_load_dwordx4 v100, s[12:15], s42 offen offset:0 lds\n"
      "buffer_load_dwordx4 v101, s[12:15], s42 offen offset:1024 lds\n"
      "buffer_load_dwordx4 v102, s[12:15], s42 offen offset:2048 lds\n"
      "buffer_load_dwordx4 v103, s[12:15], s42 offen offset:3072 lds\n"
      "s_add_u32 m0, s41, 77824\n"
      "s_nop 0\n"
      "buffer_load_dwordx4 v104, s[12:15], s42 offen offset:0 lds\n"
      "buffer_load_dwordx4 v105, s[12:15], s42 offen offset:1024 lds\n"
      "buffer_load_dwordx4 v106, s[12:15], s42 offen offset:2048 lds\n"
      "buffer_load_dwordx4 v107, s[12:15], s42 offen offset:3072 lds\n"
      "s_add_u32 m0, s41, 81920\n"
      "s_nop 0\n"
      "buffer_load_dwordx4 v108, s[12:15], s42 offen offset:0 lds\n"
      "s_add_u32 s42, s42, 41472\n"
      "s_waitcnt vmcnt(18)\n"
      "s_barrier\n"
      "ds_read_b64_tr_b16 v[64:65], v120 offset:0\n"
      "ds_read_b64_tr_b16 v[66:67], v120 offset:2304\n"
      "ds_read_b64_tr_b16 a[0:1], v118 offset:0\n"
      "ds_read_b64_tr_b16 a[2:3], v118 offset:2304\n"
      "ds_read_b64_tr_b16 a[72:73], v118 offset:8\n"
      "ds_read_b64_tr_b16 a[74:75], v118 offset:2312\n"
      "ds_read_b64_tr_b16 a[144:145], v118 offset:64\n"
      "ds_read_b64_tr_b16 a[146:147], v118 offset:2368\n"
      "ds_read_b64_tr_b16 a[216:217], v118 offset:72\n"
      "ds_read_b64_tr_b16 a[218:219], v118 offset:2376\n"
      "ds_read_b64_tr_b16 a[36:37], v119 offset:0\n"
      "ds_read_b64_tr_b16 a[38:39], v119 offset:2304\n"
      "ds_read_b64_tr_b16 a[108:109], v119 offset:8\n"
      "ds_read_b64_tr_b16 a[110:111], v119 offset:2312\n"
      "ds_read_b64_tr_b16 a[180:181], v119 offset:64\n"
      "ds_read_b64_tr_b16 a[182:183], v119 offset:2368\n"
      "ds_read_b64_tr_b16 a[252:253], v119 offset:72\n"
      "ds_read_b64_tr_b16 a[254:255], v119 offset:2376\n"
      "v_add_u32 v118, 36864, v118\n"
      "v_add_u32 v119, 36864, v119\n"
      "v_add_u32 v120, 36864, v120\n"
      "s_add_u32 m0, s41, 110592\n"
      "s_nop 0\n"
      "buffer_load_dwordx4 v100, s[12:15], s42 offen offset:0 lds\n"
      "buffer_load_dwordx4 v101, s[12:15], s42 offen offset:1024 lds\n"
      "buffer_load_dwordx4 v102, s[12:15], s42 offen offset:2048 lds\n"
      "buffer_load_dwordx4 v103, s[12:15], s42 offen offset:3072 lds\n"
      "s_add_u32 m0, s41, 114688\n"
      "s_nop 0\n"
      "buffer_load_dwordx4 v104, s[12:15], s42 offen offset:0 lds\n"
      "buffer_load_dwordx4 v105, s[12:15], s42 offen offset:1024 lds\n"
      "buffer_load_dwordx4 v106, s[12:15], s42 offen offset:2048 lds\n"
      "buffer_load_dwordx4 v107, s[12:15], s42 offen offset:3072 lds\n"
      "s_add_u32 m0, s41, 118784\n"
      "s_nop 0\n"
      "buffer_load_dwordx4 v108, s[12:15], s42 offen offset:0 lds\n"
      "s_add_u32 s42, s42, 41472\n"
      "s_waitcnt vmcnt(18) lgkmcnt(0)\n"
      "s_barrier\n"
      "ds_read_b64_tr_b16 v[68:69], v120 offset:0\n"
      "ds_read_b64_tr_b16 v[70:71], v120 offset:2304\n"
      "ds_read_b64_tr_b16 a[4:5], v118 offset:0\n"
      "ds_read_b64_tr_b16 a[6:7], v118 offset:2304\n"
      "ds_read_b64_tr_b16 a[76:77], v118 offset:8\n"
      "ds_read_b64_tr_b16 a[78:79], v118 offset:2312\n"
      "ds_read_b64_tr_b16 a[148:149], v118 offset:64\n"
      "ds_read_b64_tr_b16 a[150:151], v118 offset:2368\n"
      "ds_read_b64_tr_b16 a[220:221], v118 offset:72\n"
      "ds_read_b64_tr_b16 a[222:223], v118 offset:2376\n"
      "ds_read_b64_tr_b16 a[40:41], v119 offset:0\n"
      "ds_read_b64_tr_b16 a[42:43], v119 offset:2304\n"
      "ds_read_b64_tr_b16 a[112:113], v119 offset:8\n"
      "ds_read_b64_tr_b16 a[114:115], v119 offset:2312\n"
      "ds_read_b64_tr_b16 a[184:185], v119 offset:64\n"
      "ds_read_b64_tr_b16 a[186:187], v119 offset:2368\n"
      "ds_read_b64_tr_b16 v[32:33], v119 offset:72\n"
      "ds_read_b64_tr_b16 v[34:35], v119 offset:2376\n"
      "v_add_u32 v118, 36864, v118\n"
      "v_add_u32 v119, 36864, v119\n"
      "v_add_u32 v120, 36864, v120\n"
      "s_add_u32 m0, s41, 0\n"
      "s_nop 0\n"
      "buffer_load_dwordx4 v100, s[12:15], s42 offen offset:0 lds\n"
      "buffer_load_dwordx4 v101, s[12:15], s42 offen offset:1024 lds\n"
      "buffer_load_dwordx4 v102, s[12:15], s42 offen offset:2048 lds\n"
      "buffer_load_dwordx4 v103, s[12:15], s42 offen offset:3072 lds\n"
      "s_add_u32 m0, s41, 4096\n"
      "s_nop 0\n"
      "buffer_load_dwordx4 v104, s[12:15], s42 offen offset:0 lds\n"
      "buffer_load_dwordx4 v105, s[12:15], s42 offen offset:1024 lds\n"
      "buffer_load_dwordx4 v106, s[12:15], s42 offen offset:2048 lds\n"
      "buffer_load_dwordx4 v107, s[12:15], s42 offen offset:3072 lds\n"
      "s_add_u32 m0, s41, 8192\n"
      "s_nop 0\n"
      "buffer_load_dwordx4 v108, s[12:15], s42 offen offset:0 lds\n"
      "s_add_u32 s42, s42, 41472\n"
      "s_waitcnt vmcnt(18) lgkmcnt(0)\n"
      "s_barrier\n"
      "ds_read_b64_tr_b16 v[72:73], v120 offset:0\n"
      "ds_read_b64_tr_b16 v[74:75], v120 offset:2304\n"
      "ds_read_b64_tr_b16 a[8:9], v118 offset:0\n"
      "ds_read_b64_tr_b16 a[10:11], v118 offset:2304\n"
      "ds_read_b64_tr_b16 a[80:81], v118 offset:8\n"
      "ds_read_b64_tr_b16 a[82:83], v118 offset:2312\n"
      "ds_read_b64_tr_b16 a[152:153], v118 offset:64\n"
      "ds_read_b64_tr_b16 a[154:155], v118 offset:2368\n"
      "ds_read_b64_tr_b16 a[224:225], v118 offset:72\n"
      "ds_read_b64_tr_b16 a[226:227], v118 offset:2376\n"
      "ds_read_b64_tr_b16 a[44:45], v119 offset:0\n"
      "ds_read_b64_tr_b16 a[46:47], v119 offset:2304\n"
      "ds_read_b64_tr_b16 a[116:117], v119 offset:8\n"
      "ds_read_b64_tr_b16 a[118:119], v119 offset:2312\n"
      "ds_read_b64_tr_b16 a[188:189], v119 offset:64\n"
      "ds_read_b64_tr_b16 a[190:191], v119 offset:2368\n"
      "ds_read_b64_tr_b16 v[36:37], v119 offset:72\n"
      "ds_read_b64_tr_b16 v[38:39], v119 offset:2376\n"
      "v_add_u32 v118, 36864, v118\n"
      "v_add_u32 v119, 36864, v119\n"
      "v_add_u32 v120, 36864, v120\n"
      "s_add_u32 m0, s41, 36864\n"
      "s_nop 0\n"
      "buffer_load_dwordx4 v100, s[12:15], s42 offen offset:0 lds\n"
      "buffer_load_dwordx4 v101, s[12:15], s42 offen offset:1024 lds\n"
      "buffer_load_dwordx4 v102, s[12:15], s42 offen offset:2048 lds\n"
      "buffer_load_dwordx4 v103, s[12:15], s42 offen offset:3072 lds\n"
      "s_add_u32 m0, s41, 40960\n"
      "s_nop 0\n"
      "buffer_load_dwordx4 v104, s[12:15], s42 offen offset:0 lds\n"
      "buffer_load_dwordx4 v105, s[12:15], s42 offen offset:1024 lds\n"
      "buffer_load_dwordx4 v106, s[12:15], s42 offen offset:2048 lds\n"
      "buffer_load_dwordx4 v107, s[12:15], s42 offen offset:3072 lds\n"
      "s_add_u32 m0, s41, 45056\n"
      "s_nop 0\n"
      "buffer_load_dwordx4 v108, s[12:15], s42 offen offset:0 lds\n"
      "s_add_u32 s42, s42, 41472\n"
      "s_waitcnt vmcnt(18) lgkmcnt(0)\n"
      "s_barrier\n"
      "ds_read_b64_tr_b16 v[76:77], v120 offset:0\n"
      "ds_read_b64_tr_b16 v[78:79], v120 offset:2304\n"
      "ds_read_b64_tr_b16 a[12:13], v118 offset:0\n"
      "ds_read_b64_tr_b16 a[14:15], v118 offset:2304\n"
      "ds_read_b64_tr_b16 a[84:85], v118 offset:8\n"
      "ds_read_b64_tr_b16 a[86:87], v118 offset:2312\n"
      "ds_read_b64_tr_b16 a[156:157], v118 offset:64\n"
      "ds_read_b64_tr_b16 a[158:159], v118 offset:2368\n"
      "ds_read_b64_tr_b16 a[228:229], v118 offset:72\n"
      "ds_read_b64_tr_b16 a[230:231], v118 offset:2376\n"
      "ds_read_b64_tr_b16 a[48:49], v119 offset:0\n"
      "ds_read_b64_tr_b16 a[50:51], v119 offset:2304\n"
      "ds_read_b64_tr_b16 a[120:121], v119 offset:8\n"
      "ds_read_b64_tr_b16 a[122:123], v119 offset:2312\n"
      "ds_read_b64_tr_b16 a[192:193], v119 offset:64\n"
      "ds_read_b64_tr_b16 a[194:195], v119 offset:2368\n"
      "ds_read_b64_tr_b16 v[40:41], v119 offset:72\n"
      "ds_read_b64_tr_b16 v[42:43], v119 offset:2376\n"
      "v_add_u32 v118, 4294856704, v118\n"
      "v_add_u32 v119, 4294856704, v119\n"
      "v_add_u32 v120, 4294856704, v120\n"
      "s_add_u32 m0, s41, 73728\n"
      "s_nop 0\n"
      "buffer_load_dwordx4 v100, s[12:15], s42 offen offset:0 lds\n"
      "buffer_load_dwordx4 v101, s[12:15], s42 offen offset:1024 lds\n"
      "buffer_load_dwordx4 v102, s[12:15], s42 offen offset:2048 lds\n"
      "buffer_load_dwordx4 v103, s[12:15], s42 offen offset:3072 lds\n"
      "s_add_u32 m0, s41, 77824\n"
      "s_nop 0\n"
      "buffer_load_dwordx4 v104, s[12:15], s42 offen offset:0 lds\n"
      "buffer_load_dwordx4 v105, s[12:15], s42 offen offset:1024 lds\n"
      "buffer_load_dwordx4 v106, s[12:15], s42 offen offset:2048 lds\n"
      "buffer_load_dwordx4 v107, s[12:15], s42 offen offset:3072 lds\n"
      "s_add_u32 m0, s41, 81920\n"
      "s_nop 0\n"
      "buffer_load_dwordx4 v108, s[12:15], s42 offen offset:0 lds\n"
      "s_add_u32 s42, s42, 41472\n"
      "s_waitcnt vmcnt(18) lgkmcnt(0)\n"
      "s_barrier\n"
      "ds_read_b64_tr_b16 v[80:81], v120 offset:0\n"
      "ds_read_b64_tr_b16 v[82:83], v120 offset:2304\n"
      "ds_read_b64_tr_b16 a[16:17], v118 offset:0\n"
      "ds_read_b64_tr_b16 a[18:19], v118 offset:2304\n"
      "ds_read_b64_tr_b16 a[88:89], v118 offset:8\n"
      "ds_read_b64_tr_b16 a[90:91], v118 offset:2312\n"
      "ds_read_b64_tr_b16 a[160:161], v118 offset:64\n"
      "ds_read_b64_tr_b16 a[162:163], v118 offset:2368\n"
      "ds_read_b64_tr_b16 a[232:233], v118 offset:72\n"
      "ds_read_b64_tr_b16 a[234:235], v118 offset:2376\n"
      "ds_read_b64_tr_b16 a[52:53], v119 offset:0\n"
      "ds_read_b64_tr_b16 a[54:55], v119 offset:2304\n"
      "ds_read_b64_tr_b16 a[124:125], v119 offset:8\n"
      "ds_read_b64_tr_b16 a[126:127], v119 offset:2312\n"
      "ds_read_b64_tr_b16 a[196:197], v119 offset:64\n"
      "ds_read_b64_tr_b16 a[198:199], v119 offset:2368\n"
      "ds_read_b64_tr_b16 v[44:45], v119 offset:72\n"
      "ds_read_b64_tr_b16 v[46:47], v119 offset:2376\n"
      "v_add_u32 v118, 36864, v118\n"
      "v_add_u32 v119, 36864, v119\n"
      "v_add_u32 v120, 36864, v120\n"
      "s_add_u32 m0, s41, 110592\n"
      "s_nop 0\n"
      "buffer_load_dwordx4 v100, s[12:15], s42 offen offset:0 lds\n"
      "buffer_load_dwordx4 v101, s[12:15], s42 offen offset:1024 lds\n"
      "buffer_load_dwordx4 v102, s[12:15], s42 offen offset:2048 lds\n"
      "buffer_load_dwordx4 v103, s[12:15], s42 offen offset:3072 lds\n"
      "s_add_u32 m0, s41, 114688\n"
      "s_nop 0\n"
      "buffer_load_dwordx4 v104, s[12:15], s42 offen offset:0 lds\n"
      "buffer_load_dwordx4 v105, s[12:15], s42 offen offset:1024 lds\n"
      "buffer_load_dwordx4 v106, s[12:15], s42 offen offset:2048 lds\n"
      "buffer_load_dwordx4 v107, s[12:15], s42 offen offset:3072 lds\n"
      "s_add_u32 m0, s41, 118784\n"
      "s_nop 0\n"
      "buffer_load_dwordx4 v108, s[12:15], s42 offen offset:0 lds\n"
      "s_add_u32 s42, s42, 41472\n"
      "s_waitcnt vmcnt(18) lgkmcnt(0)\n"
      "s_barrier\n"
      "ds_read_b64_tr_b16 v[84:85], v120 offset:0\n"
      "ds_read_b64_tr_b16 v[86:87], v120 offset:2304\n"
      "ds_read_b64_tr_b16 a[20:21], v118 offset:0\n"
      "ds_read_b64_tr_b16 a[22:23], v118 offset:2304\n"
      "ds_read_b64_tr_b16 a[92:93], v118 offset:8\n"
      "ds_read_b64_tr_b16 a[94:95], v118 offset:2312\n"
      "ds_read_b64_tr_b16 a[164:165], v118 offset:64\n"
      "ds_read_b64_tr_b16 a[166:167], v118 offset:2368\n"
      "ds_read_b64_tr_b16 a[236:237], v118 offset:72\n"
      "ds_read_b64_tr_b16 a[238:239], v118 offset:2376\n"
      "ds_read_b64_tr_b16 a[56:57], v119 offset:0\n"
      "ds_read_b64_tr_b16 a[58:59], v119 offset:2304\n"
      "ds_read_b64_tr_b16 a[128:129], v119 offset:8\n"
      "ds_read_b64_tr_b16 a[130:131], v119 offset:2312\n"
      "ds_read_b64_tr_b16 a[200:201], v119 offset:64\n"
      "ds_read_b64_tr_b16 a[202:203], v119 offset:2368\n"
      "ds_read_b64_tr_b16 v[48:49], v119 offset:72\n"
      "ds_read_b64_tr_b16 v[50:51], v119 offset:2376\n"
      "v_add_u32 v118, 36864, v118\n"
      "v_add_u32 v119, 36864, v119\n"
      "v_add_u32 v120, 36864, v120\n"
      "s_add_u32 m0, s41, 0\n"
      "s_nop 0\n"
      "buffer_load_dwordx4 v109, s[12:15], s43 offen offset:0 lds\n"
      "buffer_load_dwordx4 v110, s[12:15], s43 offen offset:1024 lds\n"
      "buffer_load_dwordx4 v111, s[12:15], s43 offen offset:2048 lds\n"
      "buffer_load_dwordx4 v112, s[12:15], s43 offen offset:3072 lds\n"
      "s_add_u32 m0, s41, 4096\n"
      "s_nop 0\n"
      "buffer_load_dwordx4 v113, s[12:15], s43 offen offset:0 lds\n"
      "buffer_load_dwordx4 v114, s[12:15], s43 offen offset:1024 lds\n"
      "buffer_load_dwordx4 v115, s[12:15], s43 offen offset:2048 lds\n"
      "buffer_load_dwordx4 v116, s[12:15], s43 offen offset:3072 lds\n"
      "s_add_u32 m0, s41, 8192\n"
      "s_nop 0\n"
      "buffer_load_dwordx4 v117, s[12:15], s43 offen offset:0 lds\n"
      "s_add_u32 s42, s42, 41472\n"
      "s_waitcnt vmcnt(18) lgkmcnt(0)\n"
      "s_barrier\n"
      "ds_read_b64_tr_b16 v[88:89], v120 offset:0\n"
      "ds_read_b64_tr_b16 v[90:91], v120 offset:2304\n"
      "ds_read_b64_tr_b16 a[24:25], v118 offset:0\n"
      "ds_read_b64_tr_b16 a[26:27], v118 offset:2304\n"
      "ds_read_b64_tr_b16 a[96:97], v118 offset:8\n"
      "ds_read_b64_tr_b16 a[98:99], v118 offset:2312\n"
      "ds_read_b64_tr_b16 a[168:169], v118 offset:64\n"
      "ds_read_b64_tr_b16 a[170:171], v118 offset:2368\n"
      "ds_read_b64_tr_b16 a[240:241], v118 offset:72\n"
      "ds_read_b64_tr_b16 a[242:243], v118 offset:2376\n"
      "ds_read_b64_tr_b16 a[60:61], v119 offset:0\n"
      "ds_read_b64_tr_b16 a[62:63], v119 offset:2304\n"
      "ds_read_b64_tr_b16 a[132:133], v119 offset:8\n"
      "ds_read_b64_tr_b16 a[134:135], v119 offset:2312\n"
      "ds_read_b64_tr_b16 a[204:205], v119 offset:64\n"
      "ds_read_b64_tr_b16 a[206:207], v119 offset:2368\n"
      "ds_read_b64_tr_b16 v[52:53], v119 offset:72\n"
      "ds_read_b64_tr_b16 v[54:55], v119 offset:2376\n"
      "v_add_u32 v118, 36864, v118\n"
      "v_add_u32 v119, 36864, v119\n"
      "v_add_u32 v120, 36864, v120\n"
      "s_waitcnt vmcnt(9) lgkmcnt(0)\n"
      "s_barrier\n"
      "ds_read_b64_tr_b16 v[92:93], v120 offset:0\n"
      "ds_read_b64_tr_b16 v[94:95], v120 offset:2304\n"
      "ds_read_b64_tr_b16 a[28:29], v118 offset:0\n"
      "ds_read_b64_tr_b16 a[30:31], v118 offset:2304\n"
      "ds_read_b64_tr_b16 a[100:101], v118 offset:8\n"
      "ds_read_b64_tr_b16 a[102:103], v118 offset:2312\n"
      "ds_read_b64_tr_b16 a[172:173], v118 offset:64\n"
      "ds_read_b64_tr_b16 a[174:175], v118 offset:2368\n"
      "ds_read_b64_tr_b16 a[244:245], v118 offset:72\n"
      "ds_read_b64_tr_b16 a[246:247], v118 offset:2376\n"
      "ds_read_b64_tr_b16 a[64:65], v119 offset:0\n"
      "ds_read_b64_tr_b16 a[66:67], v119 offset:2304\n"
      "ds_read_b64_tr_b16 a[136:137], v119 offset:8\n"
      "ds_read_b64_tr_b16 a[138:139], v119 offset:2312\n"
      "ds_read_b64_tr_b16 a[208:209], v119 offset:64\n"
      "ds_read_b64_tr_b16 a[210:211], v119 offset:2368\n"
      "ds_read_b64_tr_b16 v[56:57], v119 offset:72\n"
      "ds_read_b64_tr_b16 v[58:59], v119 offset:2376\n"
      "v_add_u32 v118, 4294856704, v118\n"
      "v_add_u32 v119, 4294856704, v119\n"
      "v_add_u32 v120, 4294856704, v120\n"
      "s_waitcnt vmcnt(0) lgkmcnt(0)\n"
      "s_barrier\n"
      "ds_read_b64_tr_b16 v[96:97], v120 offset:0\n"
      "ds_read_b64_tr_b16 v[98:99], v120 offset:2304\n"
      "ds_read_b64_tr_b16 a[32:33], v118 offset:0\n"
      "ds_read_b64_tr_b16 a[34:35], v118 offset:2304\n"
      "ds_read_b64_tr_b16 a[104:105], v118 offset:8\n"
      "ds_read_b64_tr_b16 a[106:107], v118 offset:2312\n"
      "ds_read_b64_tr_b16 a[176:177], v118 offset:64\n"
      "ds_read_b64_tr_b16 a[178:179], v118 offset:2368\n"
      "ds_read_b64_tr_b16 a[248:249], v118 offset:72\n"
      "ds_read_b64_tr_b16 a[250:251], v118 offset:2376\n"
      "ds_read_b64_tr_b16 a[68:69], v119 offset:0\n"
      "ds_read_b64_tr_b16 a[70:71], v119 offset:2304\n"
      "ds_read_b64_tr_b16 a[140:141], v119 offset:8\n"
      "ds_read_b64_tr_b16 a[142:143], v119 offset:2312\n"
      "ds_read_b64_tr_b16 a[212:213], v119 offset:64\n"
      "ds_read_b64_tr_b16 a[214:215], v119 offset:2368\n"
      "ds_read_b64_tr_b16 v[60:61], v119 offset:72\n"
      "ds_read_b64_tr_b16 v[62:63], v119 offset:2376\n"
      "v_add_u32 v118, 36864, v118\n"
      "v_add_u32 v119, 36864, v119\n"
      "v_add_u32 v120, 36864, v120\n"
      "s_waitcnt lgkmcnt(0)\n"
      "s_getpc_b64 vcc\n"
      ".Lw_ct:\n"
      "s_add_u32 vcc_lo, vcc_lo, .Lw_p0-.Lw_ct\n"
      "s_addc_u32 vcc_hi, vcc_hi, 0\n"
      "s_lshl_b32 s30, s39, 1\n"
      "v_lshl_add_u32 v130, v0, 3, s30\n"
      "v_min_u32 v130, .Lw_last-.Lw_p0-4, v130\n"
      "global_load_dword v130, v130, vcc\n"
      "s_mul_i32 s21, s21, 1664525\n"
      "s_add_u32 s21, s21, 1013904223\n"
      "v_mov_b32 v25, s21\n"
      "buffer_store_dword v25, v22, s[8:11], 0 offen sc1\n"
      "v_not_b32 v124, v25\n"
      "v_not_b32 v125, v25\n"
      "v_not_b32 v126, v25\n"
      "v_not_b32 v127, v25\n"
      "s_add_u32 s30, s38, 0\n"
      "buffer_store_dwordx4 v[124:127], v121, s[8:11], s30 offen sc1\n"
      "v_xor_b32 v25, v25, v26\n"
      "v_mov_b32 v27, 0x3ae38e39\n"
      "s_barrier\n"
      "v_mov_b32 v21, 0\n"
      "ds_write_b32 v23, v21\n"
      "s_waitcnt lgkmcnt(0)\n"
      "s_barrier\n"
      "s_add_u32 s6, s36, 1572864\n"
      "s_add_u32 s36, s36, 4294942720\n"
      "s_mov_b32 s51, 24576\n"
      "s_waitcnt vmcnt(0) lgkmcnt(0)\n"
      "s_cmp_lg_u32 s44, 0\n"
      "s_cbranch_scc1 .Lv_start\n"
      "s_mov_b64 exec, 0\n"
      "s_lshr_b32 s22, s39, 13\n"
      ".Lw_pl:\n"
      "s_cmp_eq_u32 s22, 0\n"
      "s_cbranch_scc1 .Lw_p0\n"
      "s_cmp_eq_u32 s22, 1\n"
      "s_cbranch_scc1 .Lw_p1\n"
      "s_branch .Lw_pe\n"
      ".Lw_pr:\n"
      "s_add_u32 s22, s22, 2\n"
      "s_branch .Lw_pl\n"
      ".Lw_pe:\n"
      "s_mov_b64 exec, -1\n"
      "s_add_u32 s50, s38, 4294893568\n"
      "s_add_u32 s49, s38, 4294893568\n"
      "s_add_u32 s38, s38, 4294942720\n"
      "s_cmp_eq_u32 s36, s37\n"
      "s_cselect_b32 s48, 49152, s51\n"
      "s_add_u32 s36, s36, s48\n"
      "s_min_u32 s36, s36, s6\n"
      "buffer_load_dword v28, v11, s[8:11], s36 offen sc1\n"
      "s_mov_b32 s46, s36\n"
      "s_add_u32 s36, s36, 24576\n"
      "s_min_u32 s36, s36, s6\n"
      "s_waitcnt vmcnt(0)\n"
      "v_cmp_ne_u32 vcc, s21, v28\n"
      "s_add_u32 m0, s39, 0\n"
      "s_cbranch_vccnz .Lsp_w0_1\n"
      ".Lbk_w0_1:\n"
      "buffer_load_dword v28, v11, s[8:11], s36 offen sc1\n"
      "buffer_load_dwordx4 v0, s[8:11], s46 offen offset:0 sc1 lds\n"
      "buffer_load_dwordx4 v0, s[8:11], s46 offen offset:1024 sc1 lds\n"
      "buffer_load_dwordx4 v0, s[8:11], s46 offen offset:3072 sc1 lds\n"
      "buffer_load_dwordx4 v0, s[8:11], s46 offen offset:2048 sc1 lds\n"
      "s_add_u32 m0, s40, 0\n"
      "s_nop 0\n"
      "buffer_load_dwordx4 v1, s[8:11], s46 offen offset:0 sc1 lds\n"
      "s_sub_u32 s47, s46, s45\n"
      "buffer_load_dwordx2 v[200:201], v14, s[8:11], s47 offen sc1\n"
      "buffer_load_dwordx4 v[192:195], v13, s[8:11], s47 offen sc1\n"
      "buffer_load_dword v26, v23, s[8:11], s31 offen\n"
      "s_add_u32 m0, s39, 20480\n"
      "s_mov_b32 s46, s36\n"
      "s_waitcnt vmcnt(8)\n"
      "v_cmp_ne_u32 vcc, s21, v28\n"
      "buffer_load_dword v26, v23, s[8:11], s31 offen\n"
      "s_cmp_eq_u32 s36, s37\n"
      "s_cselect_b32 s48, 49152, s51\n"
      "s_add_u32 s36, s36, s48\n"
      "s_cbranch_vccnz .Lsp_w1_2\n"
      ".Lbk_w1_2:\n"
      "s_min_u32 s36, s36, s6\n"
      "buffer_load_dword v28, v11, s[8:11], s36 offen sc1\n"
      "buffer_load_dwordx4 v0, s[8:11], s46 offen offset:0 sc1 lds\n"
      "buffer_load_dwordx4 v0, s[8:11], s46 offen offset:1024 sc1 lds\n"
      "buffer_load_dwordx4 v0, s[8:11], s46 offen offset:3072 sc1 lds\n"
      "v_add_u32 v21, 1, v21\n"
      "s_waitcnt vmcnt(8)\n"
      "ds_write_b32 v23, v21\n"
      "buffer_load_dwordx4 v0, s[8:11], s46 offen offset:2048 sc1 lds\n"
      "s_add_u32 m0, s40, 20480\n"
      "buffer_load_dwordx4 v[196:199], v13, s[8:11], s47 offen offset:1024 sc1\n"
      "buffer_load_dwordx4 v1, s[8:11], s46 offen offset:0 sc1 lds\n"
      "ds_read_b32 v22, v24\n"
      "s_waitcnt lgkmcnt(0)\n"
      "v_cmp_gt_i32 vcc, v21, v22\n"
      "s_waitcnt vmcnt(10)\n"
      "v_and_b32 v189, 0xffff0000, v200\n"
      "v_lshlrev_b32 v188, 16, v200\n"
      "s_cbranch_vccnz .Lsp_wy0_3\n"
      ".Lbk_wy0_3:\n"
      "ds_read_b128 v[104:107], v0 offset:1024\n"
      "v_lshlrev_b32 v190, 16, v201\n"
      "ds_read_b128 v[100:103], v0 offset:0\n"
      "ds_read_b128 v[120:123], v0 offset:5120\n"
      "s_waitcnt vmcnt(9)\n"
      "v_lshlrev_b32 v178, 16, v195\n"
      "ds_read_b128 v[112:115], v0 offset:3072\n"
      "ds_read_b128 v[116:119], v0 offset:4096\n"
      "v_lshlrev_b32 v174, 16, v193\n"
      "ds_read_b128 v[124:127], v0 offset:6144\n"
      "s_sub_u32 s47, s46, s45\n"
      "ds_read_b128 v[108:111], v0 offset:2048\n"
      "v_and_b32 v191, 0xffff0000, v201\n"
      "v_and_b32 v177, 0xffff0000, v194\n"
      "ds_read_b128 v[136:139], v0 offset:8192\n"
      "v_lshlrev_b32 v176, 16, v194\n"
      "ds_read_b128 v[128:131], v0 offset:7168\n"
      "v_lshlrev_b32 v172, 16, v192\n"
      "v_and_b32 v175, 0xffff0000, v193\n"
      "buffer_load_dwordx2 v[200:201], v14, s[8:11], s47 offen sc1\n"
      "ds_read2_b32 v[132:133], v2 offset0:0 offset1:32\n"
      "v_and_b32 v173, 0xffff0000, v192\n"
      "ds_read2_b32 v[134:135], v2 offset0:64 offset1:96\n"
      "s_mov_b32 s20, 31\n"
      "s_waitcnt lgkmcnt(0)\n"
      "s_nop 4\n"
      "s_branch .Lw_loop\n"
      ".p2align 12\n"
      ".Lw_p0:\n"
      "s_branch .Lw_pr\n"
      ".p2align 5\n"
      ".Lw_loop:\n"
      "v_mfma_f32_16x16x32_bf16 v[188:191], v[64:67], v[100:103], v[188:191]\n"
      "ds_read_b128 v[140:143], v0 offset:9216\n"
      "v_and_b32 v179, 0xffff0000, v195\n"
      "v_mfma_f32_16x16x32_bf16 v[188:191], v[68:71], v[104:107], v[188:191]\n"
      "buffer_load_dwordx4 v[192:195], v13, s[8:11], s47 offen sc1\n"
      "v_mfma_f32_16x16x32_bf16 v[188:191], v[72:75], v[108:111], v[188:191]\n"
      "v_add_f32 v218, v218, v182\n"
      "v_mfma_f32_16x16x32_bf16 v[188:191], v[76:79], v[112:115], v[188:191]\n"
      "ds_read_b128 v[156:159], v0 offset:13312\n"
      "v_fmac_f32 v219, v184, v184\n"
      "v_mfma_f32_16x16x32_bf16 v[188:191], v[80:83], v[116:119], v[188:191]\n"
      "ds_read_b128 v[164:167], v0 offset:15360\n"
      "v_cvt_pk_bf16_f32 v213, v182, v183\n"
      "v_mfma_f32_16x16x32_bf16 v[188:191], v[84:87], v[120:123], v[188:191]\n"
      "ds_read_b128 v[144:147], v0 offset:10240\n"
      "v_mfma_f32_16x16x32_bf16 v[188:191], v[88:91], v[124:127], v[188:191]\n"
      "s_waitcnt lgkmcnt(4)\n"
      "v_cvt_pk_bf16_f32 v215, v186, v187\n"
      "v_add_f32 v218, v218, v183\n"
      "v_mfma_f32_16x16x32_bf16 v[188:191], v[92:95], v[128:131], v[188:191]\n"
      "buffer_store_dwordx4 v[208:211], v15, s[8:11], s38 offen sc1\n"
      "v_mfma_f32_16x16x32_bf16 v[188:191], v[96:99], v[132:135], v[188:191]\n"
      "v_cvt_pk_bf16_f32 v212, v180, v181\n"
      "v_add_f32 v218, v218, v184\n"
      "v_mfma_f32_16x16x32_bf16 v[172:175], a[0:3], v[100:103], v[172:175]\n"
      "s_add_u32 s50, s50, 24576\n"
      "v_cvt_pk_bf16_f32 v214, v184, v185\n"
      "v_add_f32 v218, v218, v185\n"
      "v_mfma_f32_16x16x32_bf16 v[172:175], a[4:7], v[104:107], v[172:175]\n"
      "s_mov_b32 s46, s36\n"
      "v_add_f32 v218, v218, v186\n"
      "v_mfma_f32_16x16x32_bf16 v[172:175], a[8:11], v[108:111], v[172:175]\n"
      "ds_read_b128 v[148:151], v0 offset:11264\n"
      "v_add_f32 v218, v218, v187\n"
      "v_mfma_f32_16x16x32_bf16 v[172:175], a[12:15], v[112:115], v[172:175]\n"
      "v_fmac_f32 v219, v185, v185\n"
      "v_add_u32 v21, 1, v21\n"
      "v_mfma_f32_16x16x32_bf16 v[172:175], a[16:19], v[116:119], v[172:175]\n"
      "s_add_u32 s36, s36, 24576\n"
      "ds_read_b128 v[152:155], v0 offset:12288\n"
      "v_fmac_f32 v219, v186, v186\n"
      "v_mfma_f32_16x16x32_bf16 v[172:175], a[20:23], v[120:123], v[172:175]\n"
      "buffer_store_dwordx2 v[216:217], v16, s[8:11], s38 offen sc1\n"
      "v_mfma_f32_16x16x32_bf16 v[172:175], a[24:27], v[124:127], v[172:175]\n"
      "ds_read_b128 v[160:163], v0 offset:14336\n"
      "v_mfma_f32_16x16x32_bf16 v[172:175], a[28:31], v[128:131], v[172:175]\n"
      "ds_read2_b32 v[170:171], v3 offset0:64 offset1:96\n"
      "v_mfma_f32_16x16x32_bf16 v[172:175], a[32:35], v[132:135], v[172:175]\n"
      "ds_read2_b32 v[168:169], v3 offset0:0 offset1:32\n"
      "s_min_u32 s36, s36, s6\n"
      "v_mfma_f32_16x16x32_bf16 v[172:175], a[36:39], v[136:139], v[172:175]\n"
      "s_waitcnt lgkmcnt(4)\n"
      "v_mfma_f32_16x16x32_bf16 v[172:175], a[40:43], v[140:143], v[172:175]\n"
      "buffer_store_dwordx4 v[212:215], v15, s[8:11], s38 offen offset:1024 sc1\n"
      "v_mfma_f32_16x16x32_bf16 v[172:175], a[44:47], v[144:147], v[172:175]\n"
      "ds_write_b32 v18, v218\n"
      "v_mfma_f32_16x16x32_bf16 v[172:175], a[48:51], v[148:151], v[172:175]\n"
      "s_waitcnt vmcnt(11) lgkmcnt(4)\n"
      "v_fmac_f32_e64 v219, v187, v187\n"
      "v_cmp_ne_u32 vcc, s21, v28\n"
      "v_mfma_f32_16x16x32_bf16 v[172:175], a[52:55], v[152:155], v[172:175]\n"
      "ds_write_b32 v18, v219 offset:4\n"
      "v_mfma_f32_16x16x32_bf16 v[172:175], a[56:59], v[156:159], v[172:175]\n"
      "s_waitcnt lgkmcnt(3)\n"
      "s_add_u32 m0, s39, 40960\n"
      "v_mfma_f32_16x16x32_bf16 v[172:175], a[60:63], v[160:163], v[172:175]\n"
      "ds_write_b32 v23, v21\n"
      "v_mfma_f32_16x16x32_bf16 v[172:175], a[64:67], v[164:167], v[172:175]\n"
      "s_waitcnt lgkmcnt(3)\n"
      "s_cbranch_vccnz .Lsp_w0_4\n"
      ".Lbk_w0_4:\n"
      "v_mfma_f32_16x16x32_bf16 v[172:175], a[68:71], v[168:171], v[172:175]\n"
      "v_mfma_f32_16x16x32_bf16 v[176:179], a[72:75], v[100:103], v[176:179]\n"
      "buffer_load_dword v28, v11, s[8:11], s36 offen sc1\n"
      "v_mfma_f32_16x16x32_bf16 v[176:179], a[76:79], v[104:107], v[176:179]\n"
      "s_waitcnt vmcnt(7) lgkmcnt(2)\n"
      "v_lshlrev_b32 v186, 16, v199\n"
      "v_lshlrev_b32 v184, 16, v198\n"
      "v_mfma_f32_16x16x32_bf16 v[176:179], a[80:83], v[108:111], v[176:179]\n"
      "v_lshlrev_b32 v180, 16, v196\n"
      "v_and_b32 v183, 0xffff0000, v197\n"
      "v_mfma_f32_16x16x32_bf16 v[176:179], a[84:87], v[112:115], v[176:179]\n"
      "buffer_store_dword v25, v12, s[8:11], s50 offen sc1\n"
      "v_mfma_f32_16x16x32_bf16 v[176:179], a[88:91], v[116:119], v[176:179]\n"
      "v_lshlrev_b32 v182, 16, v197\n"
      "v_and_b32 v181, 0xffff0000, v196\n"
      "v_mfma_f32_16x16x32_bf16 v[176:179], a[92:95], v[120:123], v[176:179]\n"
      "v_and_b32 v187, 0xffff0000, v199\n"
      "v_and_b32 v185, 0xffff0000, v198\n"
      "v_mfma_f32_16x16x32_bf16 v[176:179], a[96:99], v[124:127], v[176:179]\n"
      "buffer_load_dwordx4 v[196:199], v13, s[8:11], s47 offen offset:1024 sc1\n"
      "v_mfma_f32_16x16x32_bf16 v[176:179], a[100:103], v[128:131], v[176:179]\n"
      "ds_read_b32 v22, v24\n"
      "v_mul_f32 v219, v172, v172\n"
      "v_mfma_f32_16x16x32_bf16 v[176:179], a[104:107], v[132:135], v[176:179]\n"
      "v_mfma_f32_16x16x32_bf16 v[176:179], a[108:111], v[136:139], v[176:179]\n"
      "v_mfma_f32_16x16x32_bf16 v[176:179], a[112:115], v[140:143], v[176:179]\n"
      "buffer_load_dwordx4 v0, s[8:11], s46 offen offset:0 sc1 lds\n"
      "v_mfma_f32_16x16x32_bf16 v[176:179], a[116:119], v[144:147], v[176:179]\n"
      "v_add_f32 v218, v172, v173\n"
      "v_fmac_f32 v219, v173, v173\n"
      "v_mfma_f32_16x16x32_bf16 v[176:179], a[120:123], v[148:151], v[176:179]\n"
      "s_waitcnt lgkmcnt(0)\n"
      "v_cmp_gt_i32 vcc, v21, v22\n"
      "v_mfma_f32_16x16x32_bf16 v[176:179], a[124:127], v[152:155], v[176:179]\n"
      "v_mfma_f32_16x16x32_bf16 v[176:179], a[128:131], v[156:159], v[176:179]\n"
      "buffer_load_dwordx4 v0, s[8:11], s46 offen offset:1024 sc1 lds\n"
      "v_mfma_f32_16x16x32_bf16 v[176:179], a[132:135], v[160:163], v[176:179]\n"
      "s_cbranch_vccnz .Lsp_wz0_5\n"
      ".Lbk_wz0_5:\n"
      "ds_read2st64_b32 v[204:205], v17 offset1:1\n"
      "v_mfma_f32_16x16x32_bf16 v[176:179], a[136:139], v[164:167], v[176:179]\n"
      "ds_read2st64_b32 v[206:207], v17 offset0:2 offset1:3\n"
      "v_add_u32 v21, 1, v21\n"
      "v_mfma_f32_16x16x32_bf16 v[176:179], a[140:143], v[168:171], v[176:179]\n"
      "s_waitcnt vmcnt(10)\n"
      "ds_write_b32 v23, v21\n"
      "v_mfma_f32_16x16x32_bf16 v[180:183], a[144:147], v[100:103], v[180:183]\n"
      "buffer_load_dwordx4 v0, s[8:11], s46 offen offset:3072 sc1 lds\n"
      "v_mfma_f32_16x16x32_bf16 v[180:183], a[148:151], v[104:107], v[180:183]\n"
      "v_add_f32 v218, v218, v174\n"
      "v_fmac_f32 v219, v174, v174\n"
      "v_mfma_f32_16x16x32_bf16 v[180:183], a[152:155], v[108:111], v[180:183]\n"
      "v_fmac_f32 v219, v175, v175\n"
      "v_fmac_f32 v219, v176, v176\n"
      "v_mfma_f32_16x16x32_bf16 v[180:183], a[156:159], v[112:115], v[180:183]\n"
      "v_add_f32 v218, v218, v175\n"
      "v_add_f32 v218, v218, v176\n"
      "v_mfma_f32_16x16x32_bf16 v[180:183], a[160:163], v[116:119], v[180:183]\n"
      "buffer_load_dwordx4 v0, s[8:11], s46 offen offset:2048 sc1 lds\n"
      "v_mfma_f32_16x16x32_bf16 v[180:183], a[164:167], v[120:123], v[180:183]\n"
      "s_add_u32 m0, s40, 40960\n"
      "v_add_f32 v218, v218, v177\n"
      "v_cvt_pk_bf16_f32 v209, v174, v175\n"
      "v_mfma_f32_16x16x32_bf16 v[180:183], a[168:171], v[124:127], v[180:183]\n"
      "s_waitcnt lgkmcnt(0)\n"
      "v_add_f32 v188, v188, v204\n"
      "v_add_f32 v190, v190, v206\n"
      "v_mfma_f32_16x16x32_bf16 v[180:183], a[172:175], v[128:131], v[180:183]\n"
      "v_cvt_pk_bf16_f32 v208, v172, v173\n"
      "v_fmac_f32 v219, v177, v177\n"
      "v_mfma_f32_16x16x32_bf16 v[180:183], a[176:179], v[132:135], v[180:183]\n"
      "buffer_load_dwordx4 v1, s[8:11], s46 offen offset:0 sc1 lds\n"
      "v_mfma_f32_16x16x32_bf16 v[180:183], a[180:183], v[136:139], v[180:183]\n"
      "s_add_u32 s38, s38, 24576\n"
      "v_cvt_pk_bf16_f32 v210, v176, v177\n"
      "v_mfma_f32_16x16x32_bf16 v[180:183], a[184:187], v[140:143], v[180:183]\n"
      "ds_read_b32 v22, v24\n"
      "v_add_f32 v218, v218, v178\n"
      "v_mfma_f32_16x16x32_bf16 v[180:183], a[188:191], v[144:147], v[180:183]\n"
      "v_add_f32 v218, v218, v179\n"
      "v_add_f32 v218, v218, v188\n"
      "v_mfma_f32_16x16x32_bf16 v[180:183], a[192:195], v[148:151], v[180:183]\n"
      "s_add_u32 s50, s50, 24576\n"
      "v_fmac_f32 v219, v178, v178\n"
      "v_fmac_f32 v219, v179, v179\n"
      "v_mfma_f32_16x16x32_bf16 v[180:183], a[196:199], v[152:155], v[180:183]\n"
      "v_add_f32 v191, v191, v207\n"
      "v_cvt_pk_bf16_f32 v211, v178, v179\n"
      "v_mfma_f32_16x16x32_bf16 v[180:183], a[200:203], v[156:159], v[180:183]\n"
      "v_fmac_f32 v219, v188, v188\n"
      "v_add_f32 v189, v189, v205\n"
      "v_mfma_f32_16x16x32_bf16 v[180:183], a[204:207], v[160:163], v[180:183]\n"
      "s_waitcnt lgkmcnt(0)\n"
      "v_fmac_f32 v219, v189, v189\n"
      "v_cmp_gt_i32 vcc, v21, v22\n"
      "v_mfma_f32_16x16x32_bf16 v[180:183], a[208:211], v[164:167], v[180:183]\n"
      "v_fmac_f32 v219, v190, v190\n"
      "v_add_f32 v218, v218, v189\n"
      "v_mfma_f32_16x16x32_bf16 v[180:183], a[212:215], v[168:171], v[180:183]\n"
      "s_waitcnt vmcnt(11)\n"
      "v_lshlrev_b32 v172, 16, v192\n"
      "v_mfma_f32_16x16x32_bf16 v[184:187], a[216:219], v[100:103], v[184:187]\n"
      "v_and_b32 v175, 0xffff0000, v193\n"
      "v_and_b32 v177, 0xffff0000, v194\n"
      "v_mfma_f32_16x16x32_bf16 v[184:187], a[220:223], v[104:107], v[184:187]\n"
      "s_cbranch_vccnz .Lsp_wy1_6\n"
      ".Lbk_wy1_6:\n"
      "v_add_f32 v218, v218, v190\n"
      "v_mfma_f32_16x16x32_bf16 v[184:187], a[224:227], v[108:111], v[184:187]\n"
      "ds_read_b128 v[104:107], v0 offset:21504\n"
      "v_cvt_pk_bf16_f32 v216, v188, v189\n"
      "v_mfma_f32_16x16x32_bf16 v[184:187], a[228:231], v[112:115], v[184:187]\n"
      "v_add_f32 v218, v218, v191\n"
      "v_add_f32 v218, v218, v180\n"
      "v_mfma_f32_16x16x32_bf16 v[184:187], a[232:235], v[116:119], v[184:187]\n"
      "ds_read_b128 v[116:119], v0 offset:24576\n"
      "v_cvt_pk_bf16_f32 v217, v190, v191\n"
      "v_mfma_f32_16x16x32_bf16 v[184:187], a[236:239], v[120:123], v[184:187]\n"
      "ds_read_b128 v[108:111], v0 offset:22528\n"
      "v_lshlrev_b32 v188, 16, v200\n"
      "v_mfma_f32_16x16x32_bf16 v[184:187], a[240:243], v[124:127], v[184:187]\n"
      "ds_read_b128 v[100:103], v0 offset:20480\n"
      "v_fmac_f32 v219, v191, v191\n"
      "v_mfma_f32_16x16x32_bf16 v[184:187], a[244:247], v[128:131], v[184:187]\n"
      "v_lshlrev_b32 v190, 16, v201\n"
      "v_and_b32 v191, 0xffff0000, v201\n"
      "v_mfma_f32_16x16x32_bf16 v[184:187], a[248:251], v[132:135], v[184:187]\n"
      "ds_read2_b32 v[134:135], v4 offset0:64 offset1:96\n"
      "v_lshlrev_b32 v174, 16, v193\n"
      "v_mfma_f32_16x16x32_bf16 v[184:187], a[252:255], v[136:139], v[184:187]\n"
      "s_sub_u32 s47, s46, s45\n"
      "v_and_b32 v189, 0xffff0000, v200\n"
      "v_fmac_f32 v219, v180, v180\n"
      "v_mfma_f32_16x16x32_bf16 v[184:187], v[32:35], v[140:143], v[184:187]\n"
      "buffer_store_dwordx4 v[208:211], v15, s[8:11], s38 offen sc1\n"
      "v_mfma_f32_16x16x32_bf16 v[184:187], v[36:39], v[144:147], v[184:187]\n"
      "ds_read_b128 v[136:139], v0 offset:28672\n"
      "v_fmac_f32 v219, v181, v181\n"
      "v_mfma_f32_16x16x32_bf16 v[184:187], v[40:43], v[148:151], v[184:187]\n"
      "ds_read2_b32 v[132:133], v4 offset0:0 offset1:32\n"
      "v_lshlrev_b32 v178, 16, v195\n"
      "v_mfma_f32_16x16x32_bf16 v[184:187], v[44:47], v[152:155], v[184:187]\n"
      "ds_read_b128 v[112:115], v0 offset:23552\n"
      "v_add_f32 v218, v218, v181\n"
      "v_mfma_f32_16x16x32_bf16 v[184:187], v[48:51], v[156:159], v[184:187]\n"
      "ds_read_b128 v[120:123], v0 offset:25600\n"
      "v_and_b32 v173, 0xffff0000, v192\n"
      "v_mfma_f32_16x16x32_bf16 v[184:187], v[52:55], v[160:163], v[184:187]\n"
      "ds_read_b128 v[124:127], v0 offset:26624\n"
      "v_lshlrev_b32 v176, 16, v194\n"
      "v_mfma_f32_16x16x32_bf16 v[184:187], v[56:59], v[164:167], v[184:187]\n"
      "buffer_load_dwordx2 v[200:201], v14, s[8:11], s47 offen sc1\n"
      "v_mfma_f32_16x16x32_bf16 v[184:187], v[60:63], v[168:171], v[184:187]\n"
      "s_waitcnt lgkmcnt(5)\n"
      "ds_read_b128 v[128:131], v0 offset:27648\n"
      "v_add_f32 v218, v218, v182\n"
      "v_mfma_f32_16x16x32_bf16 v[188:191], v[64:67], v[100:103], v[188:191]\n"
      "ds_read_b128 v[140:143], v0 offset:29696\n"
      "v_cvt_pk_bf16_f32 v213, v182, v183\n"
      "v_mfma_f32_16x16x32_bf16 v[188:191], v[68:71], v[104:107], v[188:191]\n"
      "ds_read_b128 v[152:155], v0 offset:32768\n"
      "v_mfma_f32_16x16x32_bf16 v[188:191], v[72:75], v[108:111], v[188:191]\n"
      "s_waitcnt lgkmcnt(5)\n"
      "buffer_store_dwordx2 v[216:217], v16, s[8:11], s38 offen sc1\n"
      "v_mfma_f32_16x16x32_bf16 v[188:191], v[76:79], v[112:115], v[188:191]\n"
      "ds_read_b128 v[144:147], v0 offset:30720\n"
      "v_cvt_pk_bf16_f32 v212, v180, v181\n"
      "v_mfma_f32_16x16x32_bf16 v[188:191], v[80:83], v[116:119], v[188:191]\n"
      "s_waitcnt lgkmcnt(4)\n"
      "ds_read_b128 v[164:167], v0 offset:35840\n"
      "v_add_f32 v218, v218, v183\n"
      "v_mfma_f32_16x16x32_bf16 v[188:191], v[84:87], v[120:123], v[188:191]\n"
      "v_fmac_f32 v219, v182, v182\n"
      "v_add_f32 v218, v218, v184\n"
      "v_mfma_f32_16x16x32_bf16 v[188:191], v[88:91], v[124:127], v[188:191]\n"
      "s_waitcnt lgkmcnt(4)\n"
      "ds_read_b128 v[148:151], v0 offset:31744\n"
      "v_add_f32 v218, v218, v185\n"
      "v_mfma_f32_16x16x32_bf16 v[188:191], v[92:95], v[128:131], v[188:191]\n"
      "ds_read_b128 v[160:163], v0 offset:34816\n"
      "v_and_b32 v179, 0xffff0000, v195\n"
      "v_mfma_f32_16x16x32_bf16 v[188:191], v[96:99], v[132:135], v[188:191]\n"
      "buffer_load_dwordx4 v[192:195], v13, s[8:11], s47 offen sc1\n"
      "v_mfma_f32_16x16x32_bf16 v[172:175], a[0:3], v[100:103], v[172:175]\n"
      "v_cvt_pk_bf16_f32 v215, v186, v187\n"
      "v_fmac_f32 v219, v183, v183\n"
      "v_mfma_f32_16x16x32_bf16 v[172:175], a[4:7], v[104:107], v[172:175]\n"
      "v_cvt_pk_bf16_f32 v214, v184, v185\n"
      "v_mfma_f32_16x16x32_bf16 v[172:175], a[8:11], v[108:111], v[172:175]\n"
      "ds_read2_b32 v[168:169], v5 offset0:0 offset1:32\n"
      "v_mfma_f32_16x16x32_bf16 v[172:175], a[12:15], v[112:115], v[172:175]\n"
      "ds_read_b128 v[156:159], v0 offset:33792\n"
      "v_mfma_f32_16x16x32_bf16 v[172:175], a[16:19], v[116:119], v[172:175]\n"
      "buffer_store_dwordx4 v[212:215], v15, s[8:11], s38 offen offset:1024 sc1\n"
      "v_mfma_f32_16x16x32_bf16 v[172:175], a[20:23], v[120:123], v[172:175]\n"
      "v_fmac_f32 v219, v184, v184\n"
      "v_fmac_f32 v219, v185, v185\n"
      "v_mfma_f32_16x16x32_bf16 v[172:175], a[24:27], v[124:127], v[172:175]\n"
      "ds_read2_b32 v[170:171], v5 offset0:64 offset1:96\n"
      "v_add_f32 v218, v218, v186\n"
      "v_mfma_f32_16x16x32_bf16 v[172:175], a[28:31], v[128:131], v[172:175]\n"
      "v_fmac_f32 v219, v186, v186\n"
      "v_add_f32 v218, v218, v187\n"
      "v_mfma_f32_16x16x32_bf16 v[172:175], a[32:35], v[132:135], v[172:175]\n"
      "ds_write_b32 v18, v218\n"
      "v_mfma_f32_16x16x32_bf16 v[172:175], a[36:39], v[136:139], v[172:175]\n"
      "s_waitcnt lgkmcnt(3)\n"
      "s_add_u32 m0, s39, 61440\n"
      "v_add_u32 v21, 1, v21\n"
      "v_mfma_f32_16x16x32_bf16 v[172:175], a[40:43], v[140:143], v[172:175]\n"
      "s_mov_b32 s46, s36\n"
      "v_mfma_f32_16x16x32_bf16 v[172:175], a[44:47], v[144:147], v[172:175]\n"
      "v_fmac_f32 v219, v187, v187\n"
      "v_mfma_f32_16x16x32_bf16 v[172:175], a[48:51], v[148:151], v[172:175]\n"
      "ds_write_b32 v18, v219 offset:4\n"
      "v_mfma_f32_16x16x32_bf16 v[172:175], a[52:55], v[152:155], v[172:175]\n"
      "s_waitcnt vmcnt(10) lgkmcnt(2)\n"
      "v_and_b32 v181, 0xffff0000, v196\n"
      "v_mfma_f32_16x16x32_bf16 v[172:175], a[56:59], v[156:159], v[172:175]\n"
      "ds_write_b32 v23, v21\n"
      "v_mfma_f32_16x16x32_bf16 v[172:175], a[60:63], v[160:163], v[172:175]\n"
      "v_lshlrev_b32 v186, 16, v199\n"
      "v_cmp_ne_u32 vcc, s21, v28\n"
      "v_mfma_f32_16x16x32_bf16 v[172:175], a[64:67], v[164:167], v[172:175]\n"
      "buffer_store_dword v25, v12, s[8:11], s50 offen sc1\n"
      "v_mfma_f32_16x16x32_bf16 v[172:175], a[68:71], v[168:171], v[172:175]\n"
      "s_cmp_eq_u32 s36, s37\n"
      "s_cselect_b32 s48, 49152, s51\n"
      "s_add_u32 s36, s36, s48\n"
      "v_mfma_f32_16x16x32_bf16 v[176:179], a[72:75], v[100:103], v[176:179]\n"
      "s_cbranch_vccnz .Lsp_w1_7\n"
      ".Lbk_w1_7:\n"
      "s_min_u32 s36, s36, s6\n"
      "v_mfma_f32_16x16x32_bf16 v[176:179], a[76:79], v[104:107], v[176:179]\n"
      "buffer_load_dword v28, v11, s[8:11], s36 offen sc1\n"
      "v_mfma_f32_16x16x32_bf16 v[176:179], a[80:83], v[108:111], v[176:179]\n"
      "v_and_b32 v183, 0xffff0000, v197\n"
      "v_mul_f32 v219, v172, v172\n"
      "v_mfma_f32_16x16x32_bf16 v[176:179], a[84:87], v[112:115], v[176:179]\n"
      "buffer_load_dwordx4 v0, s[8:11], s46 offen offset:0 sc1 lds\n"
      "v_mfma_f32_16x16x32_bf16 v[176:179], a[88:91], v[116:119], v[176:179]\n"
      "v_mfma_f32_16x16x32_bf16 v[176:179], a[92:95], v[120:123], v[176:179]\n"
      "v_fmac_f32 v219, v173, v173\n"
      "v_mfma_f32_16x16x32_bf16 v[176:179], a[96:99], v[124:127], v[176:179]\n"
      "v_lshlrev_b32 v184, 16, v198\n"
      "v_and_b32 v185, 0xffff0000, v198\n"
      "v_mfma_f32_16x16x32_bf16 v[176:179], a[100:103], v[128:131], v[176:179]\n"
      "ds_read_b32 v22, v24\n"
      "v_fmac_f32 v219, v174, v174\n"
      "v_mfma_f32_16x16x32_bf16 v[176:179], a[104:107], v[132:135], v[176:179]\n"
      "buffer_load_dwordx4 v0, s[8:11], s46 offen offset:1024 sc1 lds\n"
      "v_mfma_f32_16x16x32_bf16 v[176:179], a[108:111], v[136:139], v[176:179]\n"
      "v_lshlrev_b32 v182, 16, v197\n"
      "v_add_f32 v218, v172, v173\n"
      "v_mfma_f32_16x16x32_bf16 v[176:179], a[112:115], v[140:143], v[176:179]\n"
      "v_add_f32 v218, v218, v174\n"
      "v_fmac_f32 v219, v175, v175\n"
      "v_mfma_f32_16x16x32_bf16 v[176:179], a[116:119], v[144:147], v[176:179]\n"
      "v_cvt_pk_bf16_f32 v208, v172, v173\n"
      "v_mfma_f32_16x16x32_bf16 v[176:179], a[120:123], v[148:151], v[176:179]\n"
      "s_waitcnt lgkmcnt(0)\n"
      "v_and_b32 v187, 0xffff0000, v199\n"
      "v_cmp_gt_i32 vcc, v21, v22\n"
      "v_mfma_f32_16x16x32_bf16 v[176:179], a[124:127], v[152:155], v[176:179]\n"
      "buffer_load_dwordx4 v0, s[8:11], s46 offen offset:3072 sc1 lds\n"
      "v_mfma_f32_16x16x32_bf16 v[176:179], a[128:131], v[156:159], v[176:179]\n"
      "v_lshlrev_b32 v180, 16, v196\n"
      "v_add_f32 v218, v218, v175\n"
      "v_mfma_f32_16x16x32_bf16 v[176:179], a[132:135], v[160:163], v[176:179]\n"
      "s_cbranch_vccnz .Lsp_wz1_8\n"
      ".Lbk_wz1_8:\n"
      "ds_read2st64_b32 v[204:205], v17 offset1:1\n"
      "v_mfma_f32_16x16x32_bf16 v[176:179], a[136:139], v[164:167], v[176:179]\n"
      "ds_read2st64_b32 v[206:207], v17 offset0:2 offset1:3\n"
      "v_add_u32 v21, 1, v21\n"
      "v_mfma_f32_16x16x32_bf16 v[176:179], a[140:143], v[168:171], v[176:179]\n"
      "s_waitcnt vmcnt(10)\n"
      "ds_write_b32 v23, v21\n"
      "v_mfma_f32_16x16x32_bf16 v[180:183], a[144:147], v[100:103], v[180:183]\n"
      "buffer_load_dwordx4 v0, s[8:11], s46 offen offset:2048 sc1 lds\n"
      "v_mfma_f32_16x16x32_bf16 v[180:183], a[148:151], v[104:107], v[180:183]\n"
      "v_mfma_f32_16x16x32_bf16 v[180:183], a[152:155], v[108:111], v[180:183]\n"
      "s_add_u32 m0, s40, 61440\n"
      "v_add_f32 v218, v218, v176\n"
      "v_mfma_f32_16x16x32_bf16 v[180:183], a[156:159], v[112:115], v[180:183]\n"
      "v_fmac_f32 v219, v176, v176\n"
      "v_cvt_pk_bf16_f32 v209, v174, v175\n"
      "v_mfma_f32_16x16x32_bf16 v[180:183], a[160:163], v[116:119], v[180:183]\n"
      "buffer_load_dwordx4 v[196:199], v13, s[8:11], s47 offen offset:1024 sc1\n"
      "v_mfma_f32_16x16x32_bf16 v[180:183], a[164:167], v[120:123], v[180:183]\n"
      "s_waitcnt lgkmcnt(1)\n"
      "v_cvt_pk_bf16_f32 v211, v178, v179\n"
      "v_add_f32 v190, v190, v206\n"
      "v_mfma_f32_16x16x32_bf16 v[180:183], a[168:171], v[124:127], v[180:183]\n"
      "v_add_f32 v218, v218, v177\n"
      "v_add_f32 v218, v218, v178\n"
      "v_mfma_f32_16x16x32_bf16 v[180:183], a[172:175], v[128:131], v[180:183]\n"
      "v_add_f32 v218, v218, v179\n"
      "v_add_f32 v189, v189, v205\n"
      "v_mfma_f32_16x16x32_bf16 v[180:183], a[176:179], v[132:135], v[180:183]\n"
      "buffer_load_dwordx4 v1, s[8:11], s46 offen offset:0 sc1 lds\n"
      "v_mfma_f32_16x16x32_bf16 v[180:183], a[180:183], v[136:139], v[180:183]\n"
      "v_add_f32 v188, v188, v204\n"
      "v_fmac_f32 v219, v177, v177\n"
      "v_mfma_f32_16x16x32_bf16 v[180:183], a[184:187], v[140:143], v[180:183]\n"
      "ds_read_b32 v22, v24\n"
      "v_fmac_f32 v219, v178, v178\n"
      "v_mfma_f32_16x16x32_bf16 v[180:183], a[188:191], v[144:147], v[180:183]\n"
      "v_cvt_pk_bf16_f32 v210, v176, v177\n"
      "v_fmac_f32 v219, v179, v179\n"
      "v_mfma_f32_16x16x32_bf16 v[180:183], a[192:195], v[148:151], v[180:183]\n"
      "v_add_f32 v191, v191, v207\n"
      "v_fmac_f32 v219, v188, v188\n"
      "v_mfma_f32_16x16x32_bf16 v[180:183], a[196:199], v[152:155], v[180:183]\n"
      "v_add_f32 v218, v218, v188\n"
      "v_mfma_f32_16x16x32_bf16 v[180:183], a[200:203], v[156:159], v[180:183]\n"
      "v_add_f32 v218, v218, v189\n"
      "v_add_f32 v218, v218, v190\n"
      "v_mfma_f32_16x16x32_bf16 v[180:183], a[204:207], v[160:163], v[180:183]\n"
      "s_waitcnt lgkmcnt(0)\n"
      "v_fmac_f32 v219, v189, v189\n"
      "v_cmp_gt_i32 vcc, v21, v22\n"
      "v_mfma_f32_16x16x32_bf16 v[180:183], a[208:211], v[164:167], v[180:183]\n"
      "v_cvt_pk_bf16_f32 v216, v188, v189\n"
      "v_fmac_f32 v219, v190, v190\n"
      "v_mfma_f32_16x16x32_bf16 v[180:183], a[212:215], v[168:171], v[180:183]\n"
      "s_waitcnt vmcnt(11)\n"
      "v_and_b32 v189, 0xffff0000, v200\n"
      "v_fmac_f32 v219, v191, v191\n"
      "v_mfma_f32_16x16x32_bf16 v[184:187], a[216:219], v[100:103], v[184:187]\n"
      "s_add_u32 s38, s38, 24576\n"
      "v_cvt_pk_bf16_f32 v217, v190, v191\n"
      "v_lshlrev_b32_e64 v188, 16, v200\n"
      "v_mfma_f32_16x16x32_bf16 v[184:187], a[220:223], v[104:107], v[184:187]\n"
      "s_cbranch_vccnz .Lsp_wy0_9\n"
      ".Lbk_wy0_9:\n"
      "v_mfma_f32_16x16x32_bf16 v[184:187], a[224:227], v[108:111], v[184:187]\n"
      "ds_read_b128 v[104:107], v10 offset:1024\n"
      "v_add_f32 v218, v218, v191\n"
      "v_mfma_f32_16x16x32_bf16 v[184:187], a[228:231], v[112:115], v[184:187]\n"
      "v_fmac_f32 v219, v180, v180\n"
      "v_lshlrev_b32 v190, 16, v201\n"
      "v_mfma_f32_16x16x32_bf16 v[184:187], a[232:235], v[116:119], v[184:187]\n"
      "ds_read_b128 v[100:103], v10 offset:0\n"
      "v_add_f32 v218, v218, v180\n"
      "v_mfma_f32_16x16x32_bf16 v[184:187], a[236:239], v[120:123], v[184:187]\n"
      "s_waitcnt vmcnt(9)\n"
      "ds_read_b128 v[120:123], v10 offset:5120\n"
      "v_lshlrev_b32 v178, 16, v195\n"
      "v_mfma_f32_16x16x32_bf16 v[184:187], a[240:243], v[124:127], v[184:187]\n"
      "ds_read_b128 v[112:115], v10 offset:3072\n"
      "v_fmac_f32 v219, v181, v181\n"
      "v_mfma_f32_16x16x32_bf16 v[184:187], a[244:247], v[128:131], v[184:187]\n"
      "ds_read_b128 v[116:119], v10 offset:4096\n"
      "v_lshlrev_b32 v174, 16, v193\n"
      "v_mfma_f32_16x16x32_bf16 v[184:187], a[248:251], v[132:135], v[184:187]\n"
      "ds_read_b128 v[124:127], v10 offset:6144\n"
      "v_fmac_f32 v219, v182, v182\n"
      "v_mfma_f32_16x16x32_bf16 v[184:187], a[252:255], v[136:139], v[184:187]\n"
      "s_sub_u32 s47, s46, s45\n"
      "ds_read_b128 v[108:111], v10 offset:2048\n"
      "v_mfma_f32_16x16x32_bf16 v[184:187], v[32:35], v[140:143], v[184:187]\n"
      "v_and_b32 v191, 0xffff0000, v201\n"
      "v_and_b32 v177, 0xffff0000, v194\n"
      "v_mfma_f32_16x16x32_bf16 v[184:187], v[36:39], v[144:147], v[184:187]\n"
      "ds_read_b128 v[136:139], v10 offset:8192\n"
      "v_lshlrev_b32 v176, 16, v194\n"
      "v_mfma_f32_16x16x32_bf16 v[184:187], v[40:43], v[148:151], v[184:187]\n"
      "ds_read_b128 v[128:131], v10 offset:7168\n"
      "v_lshlrev_b32 v172, 16, v192\n"
      "v_mfma_f32_16x16x32_bf16 v[184:187], v[44:47], v[152:155], v[184:187]\n"
      "v_fmac_f32 v219, v183, v183\n"
      "v_and_b32 v175, 0xffff0000, v193\n"
      "v_mfma_f32_16x16x32_bf16 v[184:187], v[48:51], v[156:159], v[184:187]\n"
      "buffer_load_dwordx2 v[200:201], v14, s[8:11], s47 offen sc1\n"
      "v_mfma_f32_16x16x32_bf16 v[184:187], v[52:55], v[160:163], v[184:187]\n"
      "ds_read2_b32 v[132:133], v6 offset0:0 offset1:32\n"
      "v_and_b32 v173, 0xffff0000, v192\n"
      "v_mfma_f32_16x16x32_bf16 v[184:187], v[56:59], v[164:167], v[184:187]\n"
      "s_waitcnt lgkmcnt(3)\n"
      "ds_read2_b32 v[134:135], v6 offset0:64 offset1:96\n"
      "v_add_f32 v218, v218, v181\n"
      "v_mfma_f32_16x16x32_bf16 v[184:187], v[60:63], v[168:171], v[184:187]\n"
      "s_sub_u32 s20, s20, 1\n"
      "s_cbranch_scc1 .Lw_tail\n"
      "v_mfma_f32_16x16x32_bf16 v[188:191], v[64:67], v[100:103], v[188:191]\n"
      "ds_read_b128 v[140:143], v10 offset:9216\n"
      "v_and_b32 v179, 0xffff0000, v195\n"
      "v_mfma_f32_16x16x32_bf16 v[188:191], v[68:71], v[104:107], v[188:191]\n"
      "buffer_load_dwordx4 v[192:195], v13, s[8:11], s47 offen sc1\n"
      "v_mfma_f32_16x16x32_bf16 v[188:191], v[72:75], v[108:111], v[188:191]\n"
      "v_add_f32 v218, v218, v182\n"
      "v_mfma_f32_16x16x32_bf16 v[188:191], v[76:79], v[112:115], v[188:191]\n"
      "ds_read_b128 v[156:159], v10 offset:13312\n"
      "v_fmac_f32 v219, v184, v184\n"
      "v_mfma_f32_16x16x32_bf16 v[188:191], v[80:83], v[116:119], v[188:191]\n"
      "ds_read_b128 v[164:167], v10 offset:15360\n"
      "v_cvt_pk_bf16_f32 v213, v182, v183\n"
      "v_mfma_f32_16x16x32_bf16 v[188:191], v[84:87], v[120:123], v[188:191]\n"
      "ds_read_b128 v[144:147], v10 offset:10240\n"
      "v_mfma_f32_16x16x32_bf16 v[188:191], v[88:91], v[124:127], v[188:191]\n"
      "s_waitcnt lgkmcnt(4)\n"
      "v_cvt_pk_bf16_f32 v215, v186, v187\n"
      "v_add_f32 v218, v218, v183\n"
      "v_mfma_f32_16x16x32_bf16 v[188:191], v[92:95], v[128:131], v[188:191]\n"
      "buffer_store_dwordx4 v[208:211], v15, s[8:11], s38 offen sc1\n"
      "v_mfma_f32_16x16x32_bf16 v[188:191], v[96:99], v[132:135], v[188:191]\n"
      "v_cvt_pk_bf16_f32 v212, v180, v181\n"
      "v_add_f32 v218, v218, v184\n"
      "v_mfma_f32_16x16x32_bf16 v[172:175], a[0:3], v[100:103], v[172:175]\n"
      "s_add_u32 s50, s50, 24576\n"
      "v_cvt_pk_bf16_f32 v214, v184, v185\n"
      "v_add_f32 v218, v218, v185\n"
      "v_mfma_f32_16x16x32_bf16 v[172:175], a[4:7], v[104:107], v[172:175]\n"
      "s_mov_b32 s46, s36\n"
      "v_add_f32 v218, v218, v186\n"
      "v_mfma_f32_16x16x32_bf16 v[172:175], a[8:11], v[108:111], v[172:175]\n"
      "ds_read_b128 v[148:151], v10 offset:11264\n"
      "v_add_f32 v218, v218, v187\n"
      "v_mfma_f32_16x16x32_bf16 v[172:175], a[12:15], v[112:115], v[172:175]\n"
      "v_fmac_f32 v219, v185, v185\n"
      "v_add_u32 v21, 1, v21\n"
      "v_mfma_f32_16x16x32_bf16 v[172:175], a[16:19], v[116:119], v[172:175]\n"
      "s_add_u32 s36, s36, 24576\n"
      "ds_read_b128 v[152:155], v10 offset:12288\n"
      "v_fmac_f32 v219, v186, v186\n"
      "v_mfma_f32_16x16x32_bf16 v[172:175], a[20:23], v[120:123], v[172:175]\n"
      "buffer_store_dwordx2 v[216:217], v16, s[8:11], s38 offen sc1\n"
      "v_mfma_f32_16x16x32_bf16 v[172:175], a[24:27], v[124:127], v[172:175]\n"
      "ds_read_b128 v[160:163], v10 offset:14336\n"
      "v_mfma_f32_16x16x32_bf16 v[172:175], a[28:31], v[128:131], v[172:175]\n"
      "ds_read2_b32 v[170:171], v7 offset0:64 offset1:96\n"
      "v_mfma_f32_16x16x32_bf16 v[172:175], a[32:35], v[132:135], v[172:175]\n"
      "ds_read2_b32 v[168:169], v7 offset0:0 offset1:32\n"
      "s_min_u32 s36, s36, s6\n"
      "v_mfma_f32_16x16x32_bf16 v[172:175], a[36:39], v[136:139], v[172:175]\n"
      "s_waitcnt lgkmcnt(4)\n"
      "v_mfma_f32_16x16x32_bf16 v[172:175], a[40:43], v[140:143], v[172:175]\n"
      "buffer_store_dwordx4 v[212:215], v15, s[8:11], s38 offen offset:1024 sc1\n"
      "v_mfma_f32_16x16x32_bf16 v[172:175], a[44:47], v[144:147], v[172:175]\n"
      "ds_write_b32 v18, v218\n"
      "v_mfma_f32_16x16x32_bf16 v[172:175], a[48:51], v[148:151], v[172:175]\n"
      "s_waitcnt vmcnt(11) lgkmcnt(4)\n"
      "v_fmac_f32 v219, v187, v187\n"
      "v_cmp_ne_u32 vcc, s21, v28\n"
      "v_mfma_f32_16x16x32_bf16 v[172:175], a[52:55], v[152:155], v[172:175]\n"
      "ds_write_b32 v18, v219 offset:4\n"
      "v_mfma_f32_16x16x32_bf16 v[172:175], a[56:59], v[156:159], v[172:175]\n"
      "s_waitcnt lgkmcnt(3)\n"
      "s_add_u32 m0, s39, 0\n"
      "v_mfma_f32_16x16x32_bf16 v[172:175], a[60:63], v[160:163], v[172:175]\n"
      "ds_write_b32 v23, v21\n"
      "v_mfma_f32_16x16x32_bf16 v[172:175], a[64:67], v[164:167], v[172:175]\n"
      "s_waitcnt lgkmcnt(3)\n"
      "s_cbranch_vccnz .Lsp_w0_10\n"
      ".Lbk_w0_10:\n"
      "v_mfma_f32_16x16x32_bf16 v[172:175], a[68:71], v[168:171], v[172:175]\n"
      "v_mfma_f32_16x16x32_bf16 v[176:179], a[72:75], v[100:103], v[176:179]\n"
      "buffer_load_dword v28, v11, s[8:11], s36 offen sc1\n"
      "v_mfma_f32_16x16x32_bf16 v[176:179], a[76:79], v[104:107], v[176:179]\n"
      "s_waitcnt vmcnt(7) lgkmcnt(2)\n"
      "v_lshlrev_b32 v186, 16, v199\n"
      "v_lshlrev_b32 v184, 16, v198\n"
      "v_mfma_f32_16x16x32_bf16 v[176:179], a[80:83], v[108:111], v[176:179]\n"
      "v_lshlrev_b32 v180, 16, v196\n"
      "v_and_b32 v183, 0xffff0000, v197\n"
      "v_mfma_f32_16x16x32_bf16 v[176:179], a[84:87], v[112:115], v[176:179]\n"
      "buffer_store_dword v25, v12, s[8:11], s50 offen sc1\n"
      "v_mfma_f32_16x16x32_bf16 v[176:179], a[88:91], v[116:119], v[176:179]\n"
      "v_lshlrev_b32 v182, 16, v197\n"
      "v_and_b32 v181, 0xffff0000, v196\n"
      "v_mfma_f32_16x16x32_bf16 v[176:179], a[92:95], v[120:123], v[176:179]\n"
      "v_and_b32 v187, 0xffff0000, v199\n"
      "v_and_b32 v185, 0xffff0000, v198\n"
      "v_mfma_f32_16x16x32_bf16 v[176:179], a[96:99], v[124:127], v[176:179]\n"
      "buffer_load_dwordx4 v[196:199], v13, s[8:11], s47 offen offset:1024 sc1\n"
      "v_mfma_f32_16x16x32_bf16 v[176:179], a[100:103], v[128:131], v[176:179]\n"
      "ds_read_b32 v22, v24\n"
      "v_mul_f32 v219, v172, v172\n"
      "v_mfma_f32_16x16x32_bf16 v[176:179], a[104:107], v[132:135], v[176:179]\n"
      "v_mfma_f32_16x16x32_bf16 v[176:179], a[108:111], v[136:139], v[176:179]\n"
      "v_mfma_f32_16x16x32_bf16 v[176:179], a[112:115], v[140:143], v[176:179]\n"
      "buffer_load_dwordx4 v0, s[8:11], s46 offen offset:0 sc1 lds\n"
      "v_mfma_f32_16x16x32_bf16 v[176:179], a[116:119], v[144:147], v[176:179]\n"
      "v_add_f32 v218, v172, v173\n"
      "v_fmac_f32 v219, v173, v173\n"
      "v_mfma_f32_16x16x32_bf16 v[176:179], a[120:123], v[148:151], v[176:179]\n"
      "s_waitcnt lgkmcnt(0)\n"
      "v_cmp_gt_i32 vcc, v21, v22\n"
      "v_mfma_f32_16x16x32_bf16 v[176:179], a[124:127], v[152:155], v[176:179]\n"
      "v_mfma_f32_16x16x32_bf16 v[176:179], a[128:131], v[156:159], v[176:179]\n"
      "buffer_load_dwordx4 v0, s[8:11], s46 offen offset:1024 sc1 lds\n"
      "v_mfma_f32_16x16x32_bf16 v[176:179], a[132:135], v[160:163], v[176:179]\n"
      "s_cbranch_vccnz .Lsp_wz0_11\n"
      ".Lbk_wz0_11:\n"
      "ds_read2st64_b32 v[204:205], v17 offset1:1\n"
      "v_mfma_f32_16x16x32_bf16 v[176:179], a[136:139], v[164:167], v[176:179]\n"
      "ds_read2st64_b32 v[206:207], v17 offset0:2 offset1:3\n"
      "v_add_u32 v21, 1, v21\n"
      "v_mfma_f32_16x16x32_bf16 v[176:179], a[140:143], v[168:171], v[176:179]\n"
      "s_waitcnt vmcnt(10)\n"
      "ds_write_b32 v23, v21\n"
      "v_mfma_f32_16x16x32_bf16 v[180:183], a[144:147], v[100:103], v[180:183]\n"
      "buffer_load_dwordx4 v0, s[8:11], s46 offen offset:3072 sc1 lds\n"
      "v_mfma_f32_16x16x32_bf16 v[180:183], a[148:151], v[104:107], v[180:183]\n"
      "v_add_f32 v218, v218, v174\n"
      "v_fmac_f32 v219, v174, v174\n"
      "v_mfma_f32_16x16x32_bf16 v[180:183], a[152:155], v[108:111], v[180:183]\n"
      "v_fmac_f32 v219, v175, v175\n"
      "v_fmac_f32 v219, v176, v176\n"
      "v_mfma_f32_16x16x32_bf16 v[180:183], a[156:159], v[112:115], v[180:183]\n"
      "v_add_f32 v218, v218, v175\n"
      "v_add_f32 v218, v218, v176\n"
      "v_mfma_f32_16x16x32_bf16 v[180:183], a[160:163], v[116:119], v[180:183]\n"
      "buffer_load_dwordx4 v0, s[8:11], s46 offen offset:2048 sc1 lds\n"
      "v_mfma_f32_16x16x32_bf16 v[180:183], a[164:167], v[120:123], v[180:183]\n"
      "s_add_u32 m0, s40, 0\n"
      "v_add_f32 v218, v218, v177\n"
      "v_cvt_pk_bf16_f32 v209, v174, v175\n"
      "v_mfma_f32_16x16x32_bf16 v[180:183], a[168:171], v[124:127], v[180:183]\n"
      "s_waitcnt lgkmcnt(0)\n"
      "v_add_f32 v188, v188, v204\n"
      "v_add_f32 v190, v190, v206\n"
      "v_mfma_f32_16x16x32_bf16 v[180:183], a[172:175], v[128:131], v[180:183]\n"
      "v_cvt_pk_bf16_f32 v208, v172, v173\n"
      "v_fmac_f32 v219, v177, v177\n"
      "v_mfma_f32_16x16x32_bf16 v[180:183], a[176:179], v[132:135], v[180:183]\n"
      "buffer_load_dwordx4 v1, s[8:11], s46 offen offset:0 sc1 lds\n"
      "v_mfma_f32_16x16x32_bf16 v[180:183], a[180:183], v[136:139], v[180:183]\n"
      "s_add_u32 s38, s38, 24576\n"
      "v_cvt_pk_bf16_f32 v210, v176, v177\n"
      "v_mfma_f32_16x16x32_bf16 v[180:183], a[184:187], v[140:143], v[180:183]\n"
      "ds_read_b32 v22, v24\n"
      "v_add_f32 v218, v218, v178\n"
      "v_mfma_f32_16x16x32_bf16 v[180:183], a[188:191], v[144:147], v[180:183]\n"
      "v_add_f32 v218, v218, v179\n"
      "v_add_f32 v218, v218, v188\n"
      "v_mfma_f32_16x16x32_bf16 v[180:183], a[192:195], v[148:151], v[180:183]\n"
      "s_add_u32 s50, s50, 24576\n"
      "v_fmac_f32 v219, v178, v178\n"
      "v_fmac_f32 v219, v179, v179\n"
      "v_mfma_f32_16x16x32_bf16 v[180:183], a[196:199], v[152:155], v[180:183]\n"
      "v_add_f32 v191, v191, v207\n"
      "v_cvt_pk_bf16_f32 v211, v178, v179\n"
      "v_mfma_f32_16x16x32_bf16 v[180:183], a[200:203], v[156:159], v[180:183]\n"
      "v_fmac_f32 v219, v188, v188\n"
      "v_add_f32 v189, v189, v205\n"
      "v_mfma_f32_16x16x32_bf16 v[180:183], a[204:207], v[160:163], v[180:183]\n"
      "s_waitcnt lgkmcnt(0)\n"
      "v_fmac_f32 v219, v189, v189\n"
      "v_cmp_gt_i32 vcc, v21, v22\n"
      "v_mfma_f32_16x16x32_bf16 v[180:183], a[208:211], v[164:167], v[180:183]\n"
      ".Lw_p1:\n"
      "s_cbranch_execz .Lw_pr\n"
      "v_fmac_f32 v219, v190, v190\n"
      "v_add_f32 v218, v218, v189\n"
      "v_mfma_f32_16x16x32_bf16 v[180:183], a[212:215], v[168:171], v[180:183]\n"
      "s_waitcnt vmcnt(11)\n"
      "v_lshlrev_b32 v172, 16, v192\n"
      "v_mfma_f32_16x16x32_bf16 v[184:187], a[216:219], v[100:103], v[184:187]\n"
      "v_and_b32 v175, 0xffff0000, v193\n"
      "v_and_b32 v177, 0xffff0000, v194\n"
      "v_mfma_f32_16x16x32_bf16 v[184:187], a[220:223], v[104:107], v[184:187]\n"
      "s_cbranch_vccnz .Lsp_wy1_12\n"
      ".Lbk_wy1_12:\n"
      "v_add_f32 v218, v218, v190\n"
      "v_mfma_f32_16x16x32_bf16 v[184:187], a[224:227], v[108:111], v[184:187]\n"
      "ds_read_b128 v[104:107], v10 offset:21504\n"
      "v_cvt_pk_bf16_f32 v216, v188, v189\n"
      "v_mfma_f32_16x16x32_bf16 v[184:187], a[228:231], v[112:115], v[184:187]\n"
      "v_add_f32 v218, v218, v191\n"
      "v_add_f32 v218, v218, v180\n"
      "v_mfma_f32_16x16x32_bf16 v[184:187], a[232:235], v[116:119], v[184:187]\n"
      "ds_read_b128 v[116:119], v10 offset:24576\n"
      "v_cvt_pk_bf16_f32 v217, v190, v191\n"
      "v_mfma_f32_16x16x32_bf16 v[184:187], a[236:239], v[120:123], v[184:187]\n"
      "ds_read_b128 v[108:111], v10 offset:22528\n"
      "v_lshlrev_b32 v188, 16, v200\n"
      "v_mfma_f32_16x16x32_bf16 v[184:187], a[240:243], v[124:127], v[184:187]\n"
      "ds_read_b128 v[100:103], v10 offset:20480\n"
      "v_fmac_f32 v219, v191, v191\n"
      "v_mfma_f32_16x16x32_bf16 v[184:187], a[244:247], v[128:131], v[184:187]\n"
      "v_lshlrev_b32 v190, 16, v201\n"
      "v_and_b32 v191, 0xffff0000, v201\n"
      "v_mfma_f32_16x16x32_bf16 v[184:187], a[248:251], v[132:135], v[184:187]\n"
      "ds_read2_b32 v[134:135], v8 offset0:64 offset1:96\n"
      "v_lshlrev_b32 v174, 16, v193\n"
      "v_mfma_f32_16x16x32_bf16 v[184:187], a[252:255], v[136:139], v[184:187]\n"
      "s_sub_u32 s47, s46, s45\n"
      "v_and_b32 v189, 0xffff0000, v200\n"
      "v_fmac_f32 v219, v180, v180\n"
      "v_mfma_f32_16x16x32_bf16 v[184:187], v[32:35], v[140:143], v[184:187]\n"
      "buffer_store_dwordx4 v[208:211], v15, s[8:11], s38 offen sc1\n"
      "v_mfma_f32_16x16x32_bf16 v[184:187], v[36:39], v[144:147], v[184:187]\n"
      "ds_read_b128 v[136:139], v10 offset:28672\n"
      "v_fmac_f32 v219, v181, v181\n"
      "v_mfma_f32_16x16x32_bf16 v[184:187], v[40:43], v[148:151], v[184:187]\n"
      "ds_read2_b32 v[132:133], v8 offset0:0 offset1:32\n"
      "v_lshlrev_b32 v178, 16, v195\n"
      "v_mfma_f32_16x16x32_bf16 v[184:187], v[44:47], v[152:155], v[184:187]\n"
      "ds_read_b128 v[112:115], v10 offset:23552\n"
      "v_add_f32 v218, v218, v181\n"
      "v_mfma_f32_16x16x32_bf16 v[184:187], v[48:51], v[156:159], v[184:187]\n"
      "ds_read_b128 v[120:123], v10 offset:25600\n"
      "v_and_b32 v173, 0xffff0000, v192\n"
      "v_mfma_f32_16x16x32_bf16 v[184:187], v[52:55], v[160:163], v[184:187]\n"
      "ds_read_b128 v[124:127], v10 offset:26624\n"
      "v_lshlrev_b32 v176, 16, v194\n"
      "v_mfma_f32_16x16x32_bf16 v[184:187], v[56:59], v[164:167], v[184:187]\n"
      "buffer_load_dwordx2 v[200:201], v14, s[8:11], s47 offen sc1\n"
      "v_mfma_f32_16x16x32_bf16 v[184:187], v[60:63], v[168:171], v[184:187]\n"
      "s_waitcnt lgkmcnt(5)\n"
      "ds_read_b128 v[128:131], v10 offset:27648\n"
      "v_add_f32 v218, v218, v182\n"
      "v_mfma_f32_16x16x32_bf16 v[188:191], v[64:67], v[100:103], v[188:191]\n"
      "ds_read_b128 v[140:143], v10 offset:29696\n"
      "v_cvt_pk_bf16_f32 v213, v182, v183\n"
      "v_mfma_f32_16x16x32_bf16 v[188:191], v[68:71], v[104:107], v[188:191]\n"
      "ds_read_b128 v[152:155], v10 offset:32768\n"
      "v_mfma_f32_16x16x32_bf16 v[188:191], v[72:75], v[108:111], v[188:191]\n"
      "s_waitcnt lgkmcnt(5)\n"
      "buffer_store_dwordx2 v[216:217], v16, s[8:11], s38 offen sc1\n"
      "v_mfma_f32_16x16x32_bf16 v[188:191], v[76:79], v[112:115], v[188:191]\n"
      "ds_read_b128 v[144:147], v10 offset:30720\n"
      "v_cvt_pk_bf16_f32 v212, v180, v181\n"
      "v_mfma_f32_16x16x32_bf16 v[188:191], v[80:83], v[116:119], v[188:191]\n"
      "s_waitcnt lgkmcnt(4)\n"
      "ds_read_b128 v[164:167], v10 offset:35840\n"
      "v_add_f32 v218, v218, v183\n"
      "v_mfma_f32_16x16x32_bf16 v[188:191], v[84:87], v[120:123], v[188:191]\n"
      "v_fmac_f32 v219, v182, v182\n"
      "v_add_f32 v218, v218, v184\n"
      "v_mfma_f32_16x16x32_bf16 v[188:191], v[88:91], v[124:127], v[188:191]\n"
      "s_waitcnt lgkmcnt(4)\n"
      "ds_read_b128 v[148:151], v10 offset:31744\n"
      "v_add_f32 v218, v218, v185\n"
      "v_mfma_f32_16x16x32_bf16 v[188:191], v[92:95], v[128:131], v[188:191]\n"
      "ds_read_b128 v[160:163], v10 offset:34816\n"
      "v_and_b32 v179, 0xffff0000, v195\n"
      "v_mfma_f32_16x16x32_bf16 v[188:191], v[96:99], v[132:135], v[188:191]\n"
      "buffer_load_dwordx4 v[192:195], v13, s[8:11], s47 offen sc1\n"
      "v_mfma_f32_16x16x32_bf16 v[172:175], a[0:3], v[100:103], v[172:175]\n"
      "v_cvt_pk_bf16_f32 v215, v186, v187\n"
      "v_fmac_f32 v219, v183, v183\n"
      "v_mfma_f32_16x16x32_bf16 v[172:175], a[4:7], v[104:107], v[172:175]\n"
      "v_cvt_pk_bf16_f32 v214, v184, v185\n"
      "v_mfma_f32_16x16x32_bf16 v[172:175], a[8:11], v[108:111], v[172:175]\n"
      "ds_read2_b32 v[168:169], v9 offset0:0 offset1:32\n"
      "v_mfma_f32_16x16x32_bf16 v[172:175], a[12:15], v[112:115], v[172:175]\n"
      "ds_read_b128 v[156:159], v10 offset:33792\n"
      "v_mfma_f32_16x16x32_bf16 v[172:175], a[16:19], v[116:119], v[172:175]\n"
      "buffer_store_dwordx4 v[212:215], v15, s[8:11], s38 offen offset:1024 sc1\n"
      "v_mfma_f32_16x16x32_bf16 v[172:175], a[20:23], v[120:123], v[172:175]\n"
      "v_fmac_f32 v219, v184, v184\n"
      "v_fmac_f32 v219, v185, v185\n"
      "v_mfma_f32_16x16x32_bf16 v[172:175], a[24:27], v[124:127], v[172:175]\n"
      "ds_read2_b32 v[170:171], v9 offset0:64 offset1:96\n"
      "v_add_f32 v218, v218, v186\n"
      "v_mfma_f32_16x16x32_bf16 v[172:175], a[28:31], v[128:131], v[172:175]\n"
      "v_fmac_f32 v219, v186, v186\n"
      "v_add_f32 v218, v218, v187\n"
      "v_mfma_f32_16x16x32_bf16 v[172:175], a[32:35], v[132:135], v[172:175]\n"
      "ds_write_b32 v18, v218\n"
      "v_mfma_f32_16x16x32_bf16 v[172:175], a[36:39], v[136:139], v[172:175]\n"
      "s_waitcnt lgkmcnt(3)\n"
      "s_add_u32 m0, s39, 20480\n"
      "v_add_u32 v21, 1, v21\n"
      "v_mfma_f32_16x16x32_bf16 v[172:175], a[40:43], v[140:143], v[172:175]\n"
      "s_mov_b32 s46, s36\n"
      "v_mfma_f32_16x16x32_bf16 v[172:175], a[44:47], v[144:147], v[172:175]\n"
      "v_fmac_f32 v219, v187, v187\n"
      "v_mfma_f32_16x16x32_bf16 v[172:175], a[48:51], v[148:151], v[172:175]\n"
      "ds_write_b32 v18, v219 offset:4\n"
      "v_mfma_f32_16x16x32_bf16 v[172:175], a[52:55], v[152:155], v[172:175]\n"
      "s_waitcnt vmcnt(10) lgkmcnt(2)\n"
      "v_and_b32 v181, 0xffff0000, v196\n"
      "v_mfma_f32_16x16x32_bf16 v[172:175], a[56:59], v[156:159], v[172:175]\n"
      "ds_write_b32 v23, v21\n"
      "v_mfma_f32_16x16x32_bf16 v[172:175], a[60:63], v[160:163], v[172:175]\n"
      "v_lshlrev_b32 v186, 16, v199\n"
      "v_cmp_ne_u32 vcc, s21, v28\n"
      "v_mfma_f32_16x16x32_bf16 v[172:175], a[64:67], v[164:167], v[172:175]\n"
      "buffer_store_dword v25, v12, s[8:11], s50 offen sc1\n"
      "v_mfma_f32_16x16x32_bf16 v[172:175], a[68:71], v[168:171], v[172:175]\n"
      "s_cmp_eq_u32 s36, s37\n"
      "s_cselect_b32 s48, 49152, s51\n"
      "s_add_u32 s36, s36, s48\n"
      "v_mfma_f32_16x16x32_bf16 v[176:179], a[72:75], v[100:103], v[176:179]\n"
      "s_cbranch_vccnz .Lsp_w1_13\n"
      ".Lbk_w1_13:\n"
      "s_min_u32 s36, s36, s6\n"
      "v_mfma_f32_16x16x32_bf16 v[176:179], a[76:79], v[104:107], v[176:179]\n"
      "buffer_load_dword v28, v11, s[8:11], s36 offen sc1\n"
      "v_mfma_f32_16x16x32_bf16 v[176:179], a[80:83], v[108:111], v[176:179]\n"
      "v_and_b32 v183, 0xffff0000, v197\n"
      "v_mul_f32 v219, v172, v172\n"
      "v_mfma_f32_16x16x32_bf16 v[176:179], a[84:87], v[112:115], v[176:179]\n"
      "buffer_load_dwordx4 v0, s[8:11], s46 offen offset:0 sc1 lds\n"
      "v_mfma_f32_16x16x32_bf16 v[176:179], a[88:91], v[116:119], v[176:179]\n"
      "v_mfma_f32_16x16x32_bf16 v[176:179], a[92:95], v[120:123], v[176:179]\n"
      "v_fmac_f32 v219, v173, v173\n"
      "v_mfma_f32_16x16x32_bf16 v[176:179], a[96:99], v[124:127], v[176:179]\n"
      "v_lshlrev_b32 v184, 16, v198\n"
      "v_and_b32 v185, 0xffff0000, v198\n"
      "v_mfma_f32_16x16x32_bf16 v[176:179], a[100:103], v[128:131], v[176:179]\n"
      "ds_read_b32 v22, v24\n"
      "v_fmac_f32 v219, v174, v174\n"
      "v_mfma_f32_16x16x32_bf16 v[176:179], a[104:107], v[132:135], v[176:179]\n"
      "buffer_load_dwordx4 v0, s[8:11], s46 offen offset:1024 sc1 lds\n"
      "v_mfma_f32_16x16x32_bf16 v[176:179], a[108:111], v[136:139], v[176:179]\n"
      "v_lshlrev_b32 v182, 16, v197\n"
      "v_add_f32 v218, v172, v173\n"
      "v_mfma_f32_16x16x32_bf16 v[176:179], a[112:115], v[140:143], v[176:179]\n"
      "v_add_f32 v218, v218, v174\n"
      "v_fmac_f32 v219, v175, v175\n"
      "v_mfma_f32_16x16x32_bf16 v[176:179], a[116:119], v[144:147], v[176:179]\n"
      "v_cvt_pk_bf16_f32 v208, v172, v173\n"
      "v_mfma_f32_16x16x32_bf16 v[176:179], a[120:123], v[148:151], v[176:179]\n"
      "s_waitcnt lgkmcnt(0)\n"
      "v_and_b32 v187, 0xffff0000, v199\n"
      "v_cmp_gt_i32 vcc, v21, v22\n"
      "v_mfma_f32_16x16x32_bf16 v[176:179], a[124:127], v[152:155], v[176:179]\n"
      "buffer_load_dwordx4 v0, s[8:11], s46 offen offset:3072 sc1 lds\n"
      "v_mfma_f32_16x16x32_bf16 v[176:179], a[128:131], v[156:159], v[176:179]\n"
      "v_lshlrev_b32 v180, 16, v196\n"
      "v_add_f32 v218, v218, v175\n"
      "v_mfma_f32_16x16x32_bf16 v[176:179], a[132:135], v[160:163], v[176:179]\n"
      "s_cbranch_vccnz .Lsp_wz1_14\n"
      ".Lbk_wz1_14:\n"
      "ds_read2st64_b32 v[204:205], v17 offset1:1\n"
      "v_mfma_f32_16x16x32_bf16 v[176:179], a[136:139], v[164:167], v[176:179]\n"
      "ds_read2st64_b32 v[206:207], v17 offset0:2 offset1:3\n"
      "v_add_u32 v21, 1, v21\n"
      "v_mfma_f32_16x16x32_bf16 v[176:179], a[140:143], v[168:171], v[176:179]\n"
      "s_waitcnt vmcnt(10)\n"
      "ds_write_b32 v23, v21\n"
      "v_mfma_f32_16x16x32_bf16 v[180:183], a[144:147], v[100:103], v[180:183]\n"
      "buffer_load_dwordx4 v0, s[8:11], s46 offen offset:2048 sc1 lds\n"
      "v_mfma_f32_16x16x32_bf16 v[180:183], a[148:151], v[104:107], v[180:183]\n"
      "v_mfma_f32_16x16x32_bf16 v[180:183], a[152:155], v[108:111], v[180:183]\n"
      "s_add_u32 m0, s40, 20480\n"
      "v_add_f32 v218, v218, v176\n"
      "v_mfma_f32_16x16x32_bf16 v[180:183], a[156:159], v[112:115], v[180:183]\n"
      "v_fmac_f32 v219, v176, v176\n"
      "v_cvt_pk_bf16_f32 v209, v174, v175\n"
      "v_mfma_f32_16x16x32_bf16 v[180:183], a[160:163], v[116:119], v[180:183]\n"
      "buffer_load_dwordx4 v[196:199], v13, s[8:11], s47 offen offset:1024 sc1\n"
      "v_mfma_f32_16x16x32_bf16 v[180:183], a[164:167], v[120:123], v[180:183]\n"
      "s_waitcnt lgkmcnt(1)\n"
      "v_cvt_pk_bf16_f32 v211, v178, v179\n"
      "v_add_f32 v190, v190, v206\n"
      "v_mfma_f32_16x16x32_bf16 v[180:183], a[168:171], v[124:127], v[180:183]\n"
      "v_add_f32 v218, v218, v177\n"
      "v_add_f32 v218, v218, v178\n"
      "v_mfma_f32_16x16x32_bf16 v[180:183], a[172:175], v[128:131], v[180:183]\n"
      "v_add_f32 v218, v218, v179\n"
      "v_add_f32 v189, v189, v205\n"
      "v_mfma_f32_16x16x32_bf16 v[180:183], a[176:179], v[132:135], v[180:183]\n"
      "buffer_load_dwordx4 v1, s[8:11], s46 offen offset:0 sc1 lds\n"
      "v_mfma_f32_16x16x32_bf16 v[180:183], a[180:183], v[136:139], v[180:183]\n"
      "v_add_f32 v188, v188, v204\n"
      "v_fmac_f32 v219, v177, v177\n"
      "v_mfma_f32_16x16x32_bf16 v[180:183], a[184:187], v[140:143], v[180:183]\n"
      "ds_read_b32 v22, v24\n"
      "v_fmac_f32 v219, v178, v178\n"
      "v_mfma_f32_16x16x32_bf16 v[180:183], a[188:191], v[144:147], v[180:183]\n"
      "v_cvt_pk_bf16_f32 v210, v176, v177\n"
      "v_fmac_f32 v219, v179, v179\n"
      "v_mfma_f32_16x16x32_bf16 v[180:183], a[192:195], v[148:151], v[180:183]\n"
      "v_add_f32 v191, v191, v207\n"
      "v_fmac_f32 v219, v188, v188\n"
      "v_mfma_f32_16x16x32_bf16 v[180:183], a[196:199], v[152:155], v[180:183]\n"
      "v_add_f32 v218, v218, v188\n"
      "v_mfma_f32_16x16x32_bf16 v[180:183], a[200:203], v[156:159], v[180:183]\n"
      "v_add_f32 v218, v218, v189\n"
      "v_add_f32 v218, v218, v190\n"
      "v_mfma_f32_16x16x32_bf16 v[180:183], a[204:207], v[160:163], v[180:183]\n"
      "s_waitcnt lgkmcnt(0)\n"
      "v_fmac_f32 v219, v189, v189\n"
      "v_cmp_gt_i32 vcc, v21, v22\n"
      "v_mfma_f32_16x16x32_bf16 v[180:183], a[208:211], v[164:167], v[180:183]\n"
      "v_cvt_pk_bf16_f32 v216, v188, v189\n"
      "v_fmac_f32 v219, v190, v190\n"
      "v_mfma_f32_16x16x32_bf16 v[180:183], a[212:215], v[168:171], v[180:183]\n"
      "s_waitcnt vmcnt(11)\n"
      "v_and_b32 v189, 0xffff0000, v200\n"
      "v_fmac_f32 v219, v191, v191\n"
      "v_mfma_f32_16x16x32_bf16 v[184:187], a[216:219], v[100:103], v[184:187]\n"
      "s_add_u32 s38, s38, 24576\n"
      "v_cvt_pk_bf16_f32 v217, v190, v191\n"
      "v_lshlrev_b32_e64 v188, 16, v200\n"
      "v_mfma_f32_16x16x32_bf16 v[184:187], a[220:223], v[104:107], v[184:187]\n"
      "s_cbranch_vccnz .Lsp_wy0_15\n"
      ".Lbk_wy0_15:\n"
      "v_mfma_f32_16x16x32_bf16 v[184:187], a[224:227], v[108:111], v[184:187]\n"
      "ds_read_b128 v[104:107], v0 offset:1024\n"
      "v_add_f32 v218, v218, v191\n"
      "v_mfma_f32_16x16x32_bf16 v[184:187], a[228:231], v[112:115], v[184:187]\n"
      "v_fmac_f32 v219, v180, v180\n"
      "v_lshlrev_b32 v190, 16, v201\n"
      "v_mfma_f32_16x16x32_bf16 v[184:187], a[232:235], v[116:119], v[184:187]\n"
      "ds_read_b128 v[100:103], v0 offset:0\n"
      "v_add_f32 v218, v218, v180\n"
      "v_mfma_f32_16x16x32_bf16 v[184:187], a[236:239], v[120:123], v[184:187]\n"
      "s_waitcnt vmcnt(9)\n"
      "ds_read_b128 v[120:123], v0 offset:5120\n"
      "v_lshlrev_b32 v178, 16, v195\n"
      "v_mfma_f32_16x16x32_bf16 v[184:187], a[240:243], v[124:127], v[184:187]\n"
      "ds_read_b128 v[112:115], v0 offset:3072\n"
      "v_fmac_f32 v219, v181, v181\n"
      "v_mfma_f32_16x16x32_bf16 v[184:187], a[244:247], v[128:131], v[184:187]\n"
      "ds_read_b128 v[116:119], v0 offset:4096\n"
      "v_lshlrev_b32 v174, 16, v193\n"
      "v_mfma_f32_16x16x32_bf16 v[184:187], a[248:251], v[132:135], v[184:187]\n"
      "ds_read_b128 v[124:127], v0 offset:6144\n"
      "v_fmac_f32 v219, v182, v182\n"
      "v_mfma_f32_16x16x32_bf16 v[184:187], a[252:255], v[136:139], v[184:187]\n"
      "s_sub_u32 s47, s46, s45\n"
      "ds_read_b128 v[108:111], v0 offset:2048\n"
      "v_mfma_f32_16x16x32_bf16 v[184:187], v[32:35], v[140:143], v[184:187]\n"
      "v_and_b32 v191, 0xffff0000, v201\n"
      "v_and_b32 v177, 0xffff0000, v194\n"
      "v_mfma_f32_16x16x32_bf16 v[184:187], v[36:39], v[144:147], v[184:187]\n"
      "ds_read_b128 v[136:139], v0 offset:8192\n"
      "v_lshlrev_b32 v176, 16, v194\n"
      "v_mfma_f32_16x16x32_bf16 v[184:187], v[40:43], v[148:151], v[184:187]\n"
      "ds_read_b128 v[128:131], v0 offset:7168\n"
      "v_lshlrev_b32 v172, 16, v192\n"
      "v_mfma_f32_16x16x32_bf16 v[184:187], v[44:47], v[152:155], v[184:187]\n"
      "v_fmac_f32 v219, v183, v183\n"
      "v_and_b32 v175, 0xffff0000, v193\n"
      "v_mfma_f32_16x16x32_bf16 v[184:187], v[48:51], v[156:159], v[184:187]\n"
      "buffer_load_dwordx2 v[200:201], v14, s[8:11], s47 offen sc1\n"
      "v_mfma_f32_16x16x32_bf16 v[184:187], v[52:55], v[160:163], v[184:187]\n"
      "ds_read2_b32 v[132:133], v2 offset0:0 offset1:32\n"
      "v_and_b32 v173, 0xffff0000, v192\n"
      "v_mfma_f32_16x16x32_bf16 v[184:187], v[56:59], v[164:167], v[184:187]\n"
      "s_waitcnt lgkmcnt(3)\n"
      "ds_read2_b32 v[134:135], v2 offset0:64 offset1:96\n"
      "v_add_f32 v218, v218, v181\n"
      "v_mfma_f32_16x16x32_bf16 v[184:187], v[60:63], v[168:171], v[184:187]\n"
      "s_sub_u32 s20, s20, 1\n"
      "s_cbranch_scc0 .Lw_loop\n"
      ".Lw_tail:\n"
      "v_add_f32 v218, v218, v182\n"
      "s_nop 7\n"
      "v_fmac_f32 v219, v184, v184\n"
      "v_cvt_pk_bf16_f32 v213, v182, v183\n"
      "v_cvt_pk_bf16_f32 v215, v186, v187\n"
      "v_add_f32 v218, v218, v183\n"
      "buffer_store_dwordx4 v[208:211], v15, s[8:11], s38 offen sc1\n"
      "v_cvt_pk_bf16_f32 v212, v180, v181\n"
      "v_add_f32 v218, v218, v184\n"
      "s_add_u32 s50, s50, 24576\n"
      "v_cvt_pk_bf16_f32 v214, v184, v185\n"
      "v_add_f32 v218, v218, v185\n"
      "v_add_f32 v218, v218, v186\n"
      "v_add_f32 v218, v218, v187\n"
      "v_fmac_f32 v219, v185, v185\n"
      "v_fmac_f32 v219, v186, v186\n"
      "buffer_store_dwordx2 v[216:217], v16, s[8:11], s38 offen sc1\n"
      "buffer_store_dwordx4 v[212:215], v15, s[8:11], s38 offen offset:1024 sc1\n"
      "ds_write_b32 v18, v218\n"
      "v_fmac_f32 v219, v187, v187\n"
      "ds_write_b32 v18, v219 offset:4\n"
      "s_waitcnt lgkmcnt(0)\n"
      "s_barrier\n"
      "s_waitcnt vmcnt(12)\n"
      "buffer_store_dword v25, v12, s[8:11], s50 offen sc1\n"
      "s_add_u32 s38, s38, 24576\n"
      "s_add_u32 s50, s50, 24576\n"
      "s_waitcnt vmcnt(1)\n"
      "buffer_store_dword v25, v12, s[8:11], s50 offen sc1\n"
      "s_add_u32 s50, s50, 24576\n"
      "s_endpgm\n"
      ".Lv_start:\n"
      "s_mov_b64 exec, 0\n"
      "s_lshr_b32 s22, s39, 13\n"
      ".Lv_pl:\n"
      "s_cmp_eq_u32 s22, 0\n"
      "s_cbranch_scc1 .Lv_p0\n"
      "s_cmp_eq_u32 s22, 1\n"
      "s_cbranch_scc1 .Lv_p1\n"
      "s_branch .Lv_pe\n"
      ".Lv_pr:\n"
      "s_add_u32 s22, s22, 2\n"
      "s_branch .Lv_pl\n"
      ".Lv_pe:\n"
      "s_mov_b64 exec, -1\n"
      "s_add_u32 s50, s38, 4294893568\n"
      "s_add_u32 s49, s38, 4294918144\n"
      "s_add_u32 s38, s38, 4294942720\n"
      "s_cmp_eq_u32 s36, s37\n"
      "s_cselect_b32 s48, 49152, s51\n"
      "s_add_u32 s36, s36, s48\n"
      "s_min_u32 s36, s36, s6\n"
      "buffer_load_dword v28, v11, s[8:11], s36 offen sc1\n"
      "s_mov_b32 s46, s36\n"
      "s_add_u32 s36, s36, 24576\n"
      "s_min_u32 s36, s36, s6\n"
      "s_add_u32 m0, s39, 0\n"
      "s_waitcnt vmcnt(0)\n"
      "v_cmp_ne_u32 vcc, s21, v28\n"
      "s_cbranch_vccnz .Lsp_v0_16\n"
      ".Lbk_v0_16:\n"
      "buffer_load_dword v28, v11, s[8:11], s36 offen sc1\n"
      "buffer_load_dwordx4 v0, s[8:11], s46 offen offset:3072 sc1 lds\n"
      "buffer_load_dwordx4 v0, s[8:11], s46 offen offset:0 sc1 lds\n"
      "buffer_load_dwordx4 v0, s[8:11], s46 offen offset:1024 sc1 lds\n"
      "buffer_load_dwordx4 v0, s[8:11], s46 offen offset:2048 sc1 lds\n"
      "s_add_u32 m0, s40, 0\n"
      "s_nop 0\n"
      "buffer_load_dwordx4 v1, s[8:11], s46 offen offset:0 sc1 lds\n"
      "s_sub_u32 s47, s46, s45\n"
      "buffer_load_dwordx4 v[192:195], v13, s[8:11], s47 offen sc1\n"
      "s_mov_b32 s46, s36\n"
      "s_cmp_eq_u32 s36, s37\n"
      "s_cselect_b32 s48, 49152, s51\n"
      "buffer_load_dword v26, v23, s[8:11], s31 offen\n"
      "s_add_u32 s36, s36, s48\n"
      "s_waitcnt vmcnt(7)\n"
      "v_cmp_ne_u32 vcc, s21, v28\n"
      "s_min_u32 s36, s36, s6\n"
      "s_cbranch_vccnz .Lsp_v1_17\n"
      ".Lbk_v1_17:\n"
      "buffer_load_dword v28, v11, s[8:11], s36 offen sc1\n"
      "s_add_u32 m0, s39, 20480\n"
      "s_nop 0\n"
      "buffer_load_dwordx4 v0, s[8:11], s46 offen offset:0 sc1 lds\n"
      "buffer_load_dwordx4 v0, s[8:11], s46 offen offset:1024 sc1 lds\n"
      "buffer_load_dwordx4 v0, s[8:11], s46 offen offset:2048 sc1 lds\n"
      "buffer_load_dwordx4 v[196:199], v13, s[8:11], s47 offen offset:1024 sc1\n"
      "v_add_u32 v21, 1, v21\n"
      "s_waitcnt vmcnt(7)\n"
      "ds_write_b32 v23, v21\n"
      "buffer_load_dwordx4 v0, s[8:11], s46 offen offset:3072 sc1 lds\n"
      "s_add_u32 m0, s40, 20480\n"
      "s_nop 0\n"
      "buffer_load_dwordx4 v1, s[8:11], s46 offen offset:0 sc1 lds\n"
      "ds_read_b32 v22, v24\n"
      "buffer_load_dword v26, v23, s[8:11], s31 offen\n"
      "s_waitcnt lgkmcnt(0)\n"
      "v_cmp_gt_i32 vcc, v21, v22\n"
      "buffer_load_dword v26, v23, s[8:11], s31 offen\n"
      "s_cbranch_vccnz .Lsp_vy0_18\n"
      ".Lbk_vy0_18:\n"
      "s_sub_u32 s47, s46, s45\n"
      "s_waitcnt vmcnt(10)\n"
      "v_lshlrev_b32 v174, 16, v193\n"
      "v_and_b32 v179, 0xffff0000, v195\n"
      "ds_read_b128 v[100:103], v0 offset:8192\n"
      "ds_read_b128 v[108:111], v0 offset:10240\n"
      "v_and_b32 v173, 0xffff0000, v192\n"
      "ds_read_b128 v[120:123], v0 offset:13312\n"
      "ds_read_b128 v[128:131], v0 offset:15360\n"
      "v_lshlrev_b32 v178, 16, v195\n"
      "ds_read_b128 v[104:107], v0 offset:9216\n"
      "v_lshlrev_b32 v172, 16, v192\n"
      "buffer_load_dword v26, v23, s[8:11], s31 offen\n"
      "s_mov_b32 s46, s36\n"
      "ds_read_b128 v[112:115], v0 offset:11264\n"
      "ds_read2_b32 v[134:135], v2 offset0:64 offset1:96\n"
      "ds_read2_b32 v[132:133], v2 offset0:0 offset1:32\n"
      "s_add_u32 s36, s36, 24576\n"
      "ds_read_b128 v[116:119], v0 offset:12288\n"
      "ds_read_b128 v[124:127], v0 offset:14336\n"
      "ds_read_b128 v[160:163], v0 offset:6144\n"
      "v_and_b32 v177, 0xffff0000, v194\n"
      "s_mov_b32 s20, 31\n"
      "s_waitcnt lgkmcnt(0)\n"
      "s_nop 4\n"
      "s_branch .Lv_loop\n"
      ".p2align 12\n"
      ".Lv_p0:\n"
      "s_branch .Lv_pr\n"
      ".p2align 5\n"
      ".Lv_loop:\n"
      "v_mfma_f32_16x16x32_bf16 v[188:191], v[64:67], v[100:103], 0\n"
      "ds_read_b128 v[140:143], v0 offset:1024\n"
      "v_and_b32 v175, 0xffff0000, v193\n"
      "v_mfma_f32_16x16x32_bf16 v[188:191], v[68:71], v[104:107], v[188:191]\n"
      "ds_read2_b32 v[168:169], v3 offset0:0 offset1:32\n"
      "v_lshlrev_b32 v176, 16, v194\n"
      "v_mfma_f32_16x16x32_bf16 v[188:191], v[72:75], v[108:111], v[188:191]\n"
      "s_waitcnt lgkmcnt(5)\n"
      "ds_read_b128 v[164:167], v0 offset:7168\n"
      "v_add_f32 v218, v218, v184\n"
      "v_mfma_f32_16x16x32_bf16 v[188:191], v[76:79], v[112:115], v[188:191]\n"
      "s_waitcnt lgkmcnt(5)\n"
      "ds_read_b128 v[144:147], v0 offset:2048\n"
      "v_add_f32 v218, v218, v185\n"
      "v_mfma_f32_16x16x32_bf16 v[188:191], v[80:83], v[116:119], v[188:191]\n"
      "buffer_load_dwordx4 v[192:195], v13, s[8:11], s47 offen sc1\n"
      "v_mfma_f32_16x16x32_bf16 v[188:191], v[84:87], v[120:123], v[188:191]\n"
      "s_waitcnt lgkmcnt(4)\n"
      "ds_read2_b32 v[170:171], v3 offset0:64 offset1:96\n"
      "v_fmac_f32 v219, v183, v183\n"
      "v_mfma_f32_16x16x32_bf16 v[188:191], v[88:91], v[124:127], v[188:191]\n"
      "s_min_u32 s36, s36, s6\n"
      "ds_read_b128 v[136:139], v0 offset:0\n"
      "v_fmac_f32 v219, v184, v184\n"
      "v_mfma_f32_16x16x32_bf16 v[188:191], v[92:95], v[128:131], v[188:191]\n"
      "ds_read_b128 v[148:151], v0 offset:3072\n"
      "v_mfma_f32_16x16x32_bf16 v[188:191], v[96:99], v[132:135], v[188:191]\n"
      "v_cvt_pk_bf16_f32 v215, v186, v187\n"
      "v_add_f32 v218, v218, v186\n"
      "v_mfma_f32_16x16x32_bf16 v[172:175], a[0:3], v[100:103], v[172:175]\n"
      "ds_read_b128 v[152:155], v0 offset:4096\n"
      "v_add_f32 v218, v218, v187\n"
      "v_mfma_f32_16x16x32_bf16 v[172:175], a[4:7], v[104:107], v[172:175]\n"
      "ds_read_b128 v[156:159], v0 offset:5120\n"
      "v_fmac_f32 v219, v185, v185\n"
      "v_mfma_f32_16x16x32_bf16 v[172:175], a[8:11], v[108:111], v[172:175]\n"
      "s_add_u32 m0, s39, 40960\n"
      "v_cvt_pk_bf16_f32 v214, v184, v185\n"
      "v_mfma_f32_16x16x32_bf16 v[172:175], a[12:15], v[112:115], v[172:175]\n"
      "ds_write_b32 v18, v218\n"
      "v_mfma_f32_16x16x32_bf16 v[172:175], a[16:19], v[116:119], v[172:175]\n"
      "s_waitcnt vmcnt(10) lgkmcnt(4)\n"
      "v_fmac_f32_e64 v219, v186, v186\n"
      "v_cmp_ne_u32 vcc, s21, v28\n"
      "v_mfma_f32_16x16x32_bf16 v[172:175], a[20:23], v[120:123], v[172:175]\n"
      "ds_write_b32 v17, v190 offset:512\n"
      "v_mfma_f32_16x16x32_bf16 v[172:175], a[24:27], v[124:127], v[172:175]\n"
      "buffer_store_dwordx4 v[212:215], v15, s[8:11], s38 offen offset:1024 sc1\n"
      "v_mfma_f32_16x16x32_bf16 v[172:175], a[28:31], v[128:131], v[172:175]\n"
      "ds_write_b32 v17, v188 offset:0\n"
      "v_mfma_f32_16x16x32_bf16 v[172:175], a[32:35], v[132:135], v[172:175]\n"
      "v_fmac_f32_e64 v219, v187, v187\n"
      "v_mfma_f32_16x16x32_bf16 v[172:175], a[36:39], v[136:139], v[172:175]\n"
      "ds_write_b32 v17, v191 offset:768\n"
      "v_mfma_f32_16x16x32_bf16 v[172:175], a[40:43], v[140:143], v[172:175]\n"
      "s_cbranch_vccnz .Lsp_v0_19\n"
      ".Lbk_v0_19:\n"
      "v_add_u32 v21, 1, v21\n"
      "v_mfma_f32_16x16x32_bf16 v[172:175], a[44:47], v[144:147], v[172:175]\n"
      "s_waitcnt lgkmcnt(3)\n"
      "ds_write_b32 v18, v219 offset:4\n"
      "v_mfma_f32_16x16x32_bf16 v[172:175], a[48:51], v[148:151], v[172:175]\n"
      "buffer_load_dword v28, v11, s[8:11], s36 offen sc1\n"
      "v_mfma_f32_16x16x32_bf16 v[172:175], a[52:55], v[152:155], v[172:175]\n"
      "ds_write_b32 v17, v189 offset:256\n"
      "v_mfma_f32_16x16x32_bf16 v[172:175], a[56:59], v[156:159], v[172:175]\n"
      "s_waitcnt vmcnt(8) lgkmcnt(3)\n"
      "v_lshlrev_b32 v186, 16, v199\n"
      "v_and_b32 v185, 0xffff0000, v198\n"
      "v_mfma_f32_16x16x32_bf16 v[172:175], a[60:63], v[160:163], v[172:175]\n"
      "ds_write_b32 v23, v21\n"
      "v_mfma_f32_16x16x32_bf16 v[172:175], a[64:67], v[164:167], v[172:175]\n"
      "v_and_b32 v181, 0xffff0000, v196\n"
      "v_and_b32 v187, 0xffff0000, v199\n"
      "v_mfma_f32_16x16x32_bf16 v[172:175], a[68:71], v[168:171], v[172:175]\n"
      "buffer_load_dwordx4 v0, s[8:11], s46 offen offset:3072 sc1 lds\n"
      "v_mfma_f32_16x16x32_bf16 v[176:179], a[72:75], v[100:103], v[176:179]\n"
      "v_lshlrev_b32 v182, 16, v197\n"
      "s_add_u32 s38, s38, 24576\n"
      "v_mfma_f32_16x16x32_bf16 v[176:179], a[76:79], v[104:107], v[176:179]\n"
      "v_lshlrev_b32 v184, 16, v198\n"
      "v_mfma_f32_16x16x32_bf16 v[176:179], a[80:83], v[108:111], v[176:179]\n"
      "v_cvt_pk_bf16_f32 v208, v172, v173\n"
      "v_mfma_f32_16x16x32_bf16 v[176:179], a[84:87], v[112:115], v[176:179]\n"
      "v_mul_f32 v219, v172, v172\n"
      "v_mfma_f32_16x16x32_bf16 v[176:179], a[88:91], v[116:119], v[176:179]\n"
      "v_lshlrev_b32 v180, 16, v196\n"
      "v_and_b32 v183, 0xffff0000, v197\n"
      "v_mfma_f32_16x16x32_bf16 v[176:179], a[92:95], v[120:123], v[176:179]\n"
      "v_fmac_f32 v219, v173, v173\n"
      "v_mfma_f32_16x16x32_bf16 v[176:179], a[96:99], v[124:127], v[176:179]\n"
      "buffer_load_dwordx4 v[196:199], v13, s[8:11], s47 offen offset:1024 sc1\n"
      "v_mfma_f32_16x16x32_bf16 v[176:179], a[100:103], v[128:131], v[176:179]\n"
      "ds_read_b32 v22, v24\n"
      "v_mfma_f32_16x16x32_bf16 v[176:179], a[104:107], v[132:135], v[176:179]\n"
      "v_fmac_f32 v219, v174, v174\n"
      "v_mfma_f32_16x16x32_bf16 v[176:179], a[108:111], v[136:139], v[176:179]\n"
      "v_fmac_f32 v219, v175, v175\n"
      "v_mfma_f32_16x16x32_bf16 v[176:179], a[112:115], v[140:143], v[176:179]\n"
      "buffer_load_dwordx4 v0, s[8:11], s46 offen offset:0 sc1 lds\n"
      "v_mfma_f32_16x16x32_bf16 v[176:179], a[116:119], v[144:147], v[176:179]\n"
      "v_mfma_f32_16x16x32_bf16 v[176:179], a[120:123], v[148:151], v[176:179]\n"
      "s_waitcnt lgkmcnt(0)\n"
      "v_cmp_gt_i32 vcc, v21, v22\n"
      "v_add_f32_e64 v218, v172, v173\n"
      "v_mfma_f32_16x16x32_bf16 v[176:179], a[124:127], v[152:155], v[176:179]\n"
      "v_add_f32_e64 v218, v218, v174\n"
      "v_add_f32_e64 v218, v218, v175\n"
      "v_mfma_f32_16x16x32_bf16 v[176:179], a[128:131], v[156:159], v[176:179]\n"
      "buffer_load_dwordx4 v0, s[8:11], s46 offen offset:1024 sc1 lds\n"
      "v_mfma_f32_16x16x32_bf16 v[176:179], a[132:135], v[160:163], v[176:179]\n"
      "s_cbranch_vccnz .Lsp_vz0_20\n"
      ".Lbk_vz0_20:\n"
      "ds_read_b128 v[204:207], v19\n"
      "v_mfma_f32_16x16x32_bf16 v[176:179], a[136:139], v[164:167], v[176:179]\n"
      "v_cvt_pk_bf16_f32 v209, v174, v175\n"
      "v_add_u32 v21, 1, v21\n"
      "v_mfma_f32_16x16x32_bf16 v[176:179], a[140:143], v[168:171], v[176:179]\n"
      "s_waitcnt vmcnt(10)\n"
      "ds_write_b32 v23, v21\n"
      "v_mfma_f32_16x16x32_bf16 v[180:183], a[144:147], v[100:103], v[180:183]\n"
      "v_mfma_f32_16x16x32_bf16 v[180:183], a[148:151], v[104:107], v[180:183]\n"
      "buffer_load_dwordx4 v0, s[8:11], s46 offen offset:2048 sc1 lds\n"
      "v_mfma_f32_16x16x32_bf16 v[180:183], a[152:155], v[108:111], v[180:183]\n"
      "s_add_u32 s49, s49, 24576\n"
      "s_add_u32 m0, s40, 40960\n"
      "v_mfma_f32_16x16x32_bf16 v[180:183], a[156:159], v[112:115], v[180:183]\n"
      "v_mfma_f32_16x16x32_bf16 v[180:183], a[160:163], v[116:119], v[180:183]\n"
      "s_waitcnt lgkmcnt(1)\n"
      "v_add_f32 v204, v204, v206\n"
      "v_cvt_pk_bf16_f32 v210, v176, v177\n"
      "v_mfma_f32_16x16x32_bf16 v[180:183], a[164:167], v[120:123], v[180:183]\n"
      "buffer_load_dwordx4 v1, s[8:11], s46 offen offset:0 sc1 lds\n"
      "v_mfma_f32_16x16x32_bf16 v[180:183], a[168:171], v[124:127], v[180:183]\n"
      "v_add_f32 v205, v205, v207\n"
      "v_add_f32_dpp v204, v204, v204 quad_perm:[1,0,3,2] row_mask:0xf bank_mask:0xf\n"
      "v_mfma_f32_16x16x32_bf16 v[180:183], a[172:175], v[128:131], v[180:183]\n"
      "v_fmac_f32 v219, v176, v176\n"
      "v_mfma_f32_16x16x32_bf16 v[180:183], a[176:179], v[132:135], v[180:183]\n"
      "v_add_f32_dpp v204, v204, v204 quad_perm:[2,3,0,1] row_mask:0xf bank_mask:0xf\n"
      "v_cvt_pk_bf16_f32 v211, v178, v179\n"
      "v_mfma_f32_16x16x32_bf16 v[180:183], a[180:183], v[136:139], v[180:183]\n"
      "buffer_store_dwordx4 v[208:211], v15, s[8:11], s38 offen sc1\n"
      "v_mfma_f32_16x16x32_bf16 v[180:183], a[184:187], v[140:143], v[180:183]\n"
      "ds_read_b32 v22, v24\n"
      "v_add_f32_dpp v205, v205, v205 quad_perm:[1,0,3,2] row_mask:0xf bank_mask:0xf\n"
      "v_mfma_f32_16x16x32_bf16 v[180:183], a[188:191], v[144:147], v[180:183]\n"
      "s_add_u32 s50, s50, 24576\n"
      "v_add_f32_dpp v205, v205, v205 quad_perm:[2,3,0,1] row_mask:0xf bank_mask:0xf\n"
      "v_fmac_f32 v219, v177, v177\n"
      "v_mfma_f32_16x16x32_bf16 v[180:183], a[192:195], v[148:151], v[180:183]\n"
      "v_add_f32_dpp v205, v205, v205 row_half_mirror row_mask:0xf bank_mask:0xf\n"
      "v_fmac_f32 v219, v178, v178\n"
      "v_mfma_f32_16x16x32_bf16 v[180:183], a[196:199], v[152:155], v[180:183]\n"
      "v_add_f32_dpp v204, v204, v204 row_half_mirror row_mask:0xf bank_mask:0xf\n"
      "v_mfma_f32_16x16x32_bf16 v[180:183], a[200:203], v[156:159], v[180:183]\n"
      "v_mul_f32 v204, v27, v204\n"
      "v_fmaak_f32 v205, v27, v205, 0x36a7c5ac\n"
      "v_mfma_f32_16x16x32_bf16 v[180:183], a[204:207], v[160:163], v[180:183]\n"
      "s_waitcnt vmcnt(12) lgkmcnt(1)\n"
      "buffer_store_dword v25, v12, s[8:11], s50 offen sc1\n"
      "v_mfma_f32_16x16x32_bf16 v[180:183], a[208:211], v[164:167], v[180:183]\n"
      "s_waitcnt lgkmcnt(0)\n"
      "v_cmp_gt_i32 vcc, v21, v22\n"
      "v_add_f32 v218, v218, v176\n"
      "v_mfma_f32_16x16x32_bf16 v[180:183], a[212:215], v[168:171], v[180:183]\n"
      "v_add_f32 v218, v218, v177\n"
      "v_add_f32_e64 v218, v218, v178\n"
      "v_mfma_f32_16x16x32_bf16 v[184:187], a[216:219], v[100:103], v[184:187]\n"
      "buffer_store_dwordx2 v[204:205], v20, s[8:11], s49 offen sc1\n"
      "v_mfma_f32_16x16x32_bf16 v[184:187], a[220:223], v[104:107], v[184:187]\n"
      "s_sub_u32 s47, s46, s45\n"
      "s_cbranch_vccnz .Lsp_vy1_21\n"
      ".Lbk_vy1_21:\n"
      "v_mfma_f32_16x16x32_bf16 v[184:187], a[224:227], v[108:111], v[184:187]\n"
      "s_waitcnt vmcnt(11)\n"
      "ds_read_b128 v[104:107], v0 offset:29696\n"
      "v_and_b32 v177, 0xffff0000, v194\n"
      "v_mfma_f32_16x16x32_bf16 v[184:187], a[228:231], v[112:115], v[184:187]\n"
      "v_fmac_f32 v219, v179, v179\n"
      "v_mfma_f32_16x16x32_bf16 v[184:187], a[232:235], v[116:119], v[184:187]\n"
      "v_fmac_f32 v219, v180, v180\n"
      "v_add_f32 v218, v218, v179\n"
      "v_mfma_f32_16x16x32_bf16 v[184:187], a[236:239], v[120:123], v[184:187]\n"
      "v_cvt_pk_bf16_f32 v212, v180, v181\n"
      "v_and_b32 v179, 0xffff0000, v195\n"
      "v_mfma_f32_16x16x32_bf16 v[184:187], a[240:243], v[124:127], v[184:187]\n"
      "ds_read_b128 v[120:123], v0 offset:33792\n"
      "v_lshlrev_b32 v174, 16, v193\n"
      "v_mfma_f32_16x16x32_bf16 v[184:187], a[244:247], v[128:131], v[184:187]\n"
      "ds_read_b128 v[112:115], v0 offset:31744\n"
      "v_add_f32 v218, v218, v180\n"
      "v_mfma_f32_16x16x32_bf16 v[184:187], a[248:251], v[132:135], v[184:187]\n"
      "ds_read_b128 v[100:103], v0 offset:28672\n"
      "v_add_f32 v218, v218, v181\n"
      "v_mfma_f32_16x16x32_bf16 v[184:187], a[252:255], v[136:139], v[184:187]\n"
      "ds_read_b128 v[124:127], v0 offset:34816\n"
      "v_mfma_f32_16x16x32_bf16 v[184:187], v[32:35], v[140:143], v[184:187]\n"
      "ds_read_b128 v[108:111], v0 offset:30720\n"
      "v_and_b32 v175, 0xffff0000, v193\n"
      "v_mfma_f32_16x16x32_bf16 v[184:187], v[36:39], v[144:147], v[184:187]\n"
      "ds_read_b128 v[116:119], v0 offset:32768\n"
      "v_fmac_f32 v219, v181, v181\n"
      "v_mfma_f32_16x16x32_bf16 v[184:187], v[40:43], v[148:151], v[184:187]\n"
      "v_add_f32 v218, v218, v182\n"
      "v_mfma_f32_16x16x32_bf16 v[184:187], v[44:47], v[152:155], v[184:187]\n"
      "ds_read2_b32 v[132:133], v4 offset0:0 offset1:32\n"
      "v_and_b32 v173, 0xffff0000, v192\n"
      "v_mfma_f32_16x16x32_bf16 v[184:187], v[48:51], v[156:159], v[184:187]\n"
      "ds_read_b128 v[128:131], v0 offset:35840\n"
      "v_lshlrev_b32 v178, 16, v195\n"
      "v_mfma_f32_16x16x32_bf16 v[184:187], v[52:55], v[160:163], v[184:187]\n"
      "ds_read_b128 v[156:159], v0 offset:25600\n"
      "v_lshlrev_b32 v172, 16, v192\n"
      "v_mfma_f32_16x16x32_bf16 v[184:187], v[56:59], v[164:167], v[184:187]\n"
      "ds_read_b128 v[144:147], v0 offset:22528\n"
      "v_lshlrev_b32 v176, 16, v194\n"
      "v_mfma_f32_16x16x32_bf16 v[184:187], v[60:63], v[168:171], v[184:187]\n"
      "s_waitcnt lgkmcnt(5)\n"
      "buffer_load_dwordx4 v[192:195], v13, s[8:11], s47 offen sc1\n"
      "v_mfma_f32_16x16x32_bf16 v[188:191], v[64:67], v[100:103], 0\n"
      "ds_read2_b32 v[134:135], v4 offset0:64 offset1:96\n"
      "v_fmac_f32 v219, v182, v182\n"
      "v_mfma_f32_16x16x32_bf16 v[188:191], v[68:71], v[104:107], v[188:191]\n"
      "ds_read_b128 v[148:151], v0 offset:23552\n"
      "v_add_f32 v218, v218, v183\n"
      "v_mfma_f32_16x16x32_bf16 v[188:191], v[72:75], v[108:111], v[188:191]\n"
      "s_mov_b32 s46, s36\n"
      "v_cvt_pk_bf16_f32 v213, v182, v183\n"
      "v_cvt_pk_bf16_f32 v215, v186, v187\n"
      "v_mfma_f32_16x16x32_bf16 v[188:191], v[76:79], v[112:115], v[188:191]\n"
      "s_waitcnt lgkmcnt(4)\n"
      "ds_read_b128 v[160:163], v0 offset:26624\n"
      "v_fmac_f32 v219, v183, v183\n"
      "v_mfma_f32_16x16x32_bf16 v[188:191], v[80:83], v[116:119], v[188:191]\n"
      "ds_read_b128 v[140:143], v0 offset:21504\n"
      "v_fmac_f32 v219, v184, v184\n"
      "v_mfma_f32_16x16x32_bf16 v[188:191], v[84:87], v[120:123], v[188:191]\n"
      "ds_read2_b32 v[168:169], v5 offset0:0 offset1:32\n"
      "v_add_f32 v218, v218, v184\n"
      "v_mfma_f32_16x16x32_bf16 v[188:191], v[88:91], v[124:127], v[188:191]\n"
      "ds_read_b128 v[152:155], v0 offset:24576\n"
      "v_fmac_f32 v219, v185, v185\n"
      "v_mfma_f32_16x16x32_bf16 v[188:191], v[92:95], v[128:131], v[188:191]\n"
      "s_waitcnt lgkmcnt(5)\n"
      "ds_read_b128 v[136:139], v0 offset:20480\n"
      "v_add_f32 v218, v218, v185\n"
      "v_mfma_f32_16x16x32_bf16 v[188:191], v[96:99], v[132:135], v[188:191]\n"
      "s_cmp_eq_u32 s36, s37\n"
      "s_cselect_b32 s48, 49152, s51\n"
      "ds_read_b128 v[164:167], v0 offset:27648\n"
      "v_mfma_f32_16x16x32_bf16 v[172:175], a[0:3], v[100:103], v[172:175]\n"
      "ds_read2_b32 v[170:171], v5 offset0:64 offset1:96\n"
      "v_cvt_pk_bf16_f32 v214, v184, v185\n"
      "v_mfma_f32_16x16x32_bf16 v[172:175], a[4:7], v[104:107], v[172:175]\n"
      "v_fmac_f32 v219, v186, v186\n"
      "v_add_f32 v218, v218, v186\n"
      "v_mfma_f32_16x16x32_bf16 v[172:175], a[8:11], v[108:111], v[172:175]\n"
      "ds_write_b32 v17, v189 offset:256\n"
      "v_mfma_f32_16x16x32_bf16 v[172:175], a[12:15], v[112:115], v[172:175]\n"
      "v_add_f32 v218, v218, v187\n"
      "v_add_u32 v21, 1, v21\n"
      "v_mfma_f32_16x16x32_bf16 v[172:175], a[16:19], v[116:119], v[172:175]\n"
      "buffer_store_dwordx4 v[212:215], v15, s[8:11], s38 offen offset:1024 sc1\n"
      "v_mfma_f32_16x16x32_bf16 v[172:175], a[20:23], v[120:123], v[172:175]\n"
      "ds_write_b32 v17, v190 offset:512\n"
      "v_mfma_f32_16x16x32_bf16 v[172:175], a[24:27], v[124:127], v[172:175]\n"
      "v_fmac_f32 v219, v187, v187\n"
      "v_mfma_f32_16x16x32_bf16 v[172:175], a[28:31], v[128:131], v[172:175]\n"
      "s_add_u32 s36, s36, s48\n"
      "ds_write_b32 v18, v218\n"
      "v_mfma_f32_16x16x32_bf16 v[172:175], a[32:35], v[132:135], v[172:175]\n"
      "s_waitcnt vmcnt(11) lgkmcnt(3)\n"
      "v_cmp_ne_u32 vcc, s21, v28\n"
      "v_mfma_f32_16x16x32_bf16 v[172:175], a[36:39], v[136:139], v[172:175]\n"
      "ds_write_b32 v17, v188 offset:0\n"
      "v_mfma_f32_16x16x32_bf16 v[172:175], a[40:43], v[140:143], v[172:175]\n"
      "s_waitcnt vmcnt(9) lgkmcnt(3)\n"
      "s_min_u32 s36, s36, s6\n"
      "v_lshlrev_b32 v182, 16, v197\n"
      "v_mfma_f32_16x16x32_bf16 v[172:175], a[44:47], v[144:147], v[172:175]\n"
      "ds_write_b32 v17, v191 offset:768\n"
      "v_mfma_f32_16x16x32_bf16 v[172:175], a[48:51], v[148:151], v[172:175]\n"
      "s_cbranch_vccnz .Lsp_v1_22\n"
      ".Lbk_v1_22:\n"
      "v_and_b32 v181, 0xffff0000, v196\n"
      "v_mfma_f32_16x16x32_bf16 v[172:175], a[52:55], v[152:155], v[172:175]\n"
      "ds_write_b32 v18, v219 offset:4\n"
      "v_mfma_f32_16x16x32_bf16 v[172:175], a[56:59], v[156:159], v[172:175]\n"
      "buffer_load_dword v28, v11, s[8:11], s36 offen sc1\n"
      "v_mfma_f32_16x16x32_bf16 v[172:175], a[60:63], v[160:163], v[172:175]\n"
      "s_add_u32 m0, s39, 61440\n"
      "ds_write_b32 v23, v21\n"
      "v_mfma_f32_16x16x32_bf16 v[172:175], a[64:67], v[164:167], v[172:175]\n"
      "v_and_b32 v187, 0xffff0000, v199\n"
      "v_and_b32 v183, 0xffff0000, v197\n"
      "v_mfma_f32_16x16x32_bf16 v[172:175], a[68:71], v[168:171], v[172:175]\n"
      "buffer_load_dwordx4 v0, s[8:11], s46 offen offset:0 sc1 lds\n"
      "v_mfma_f32_16x16x32_bf16 v[176:179], a[72:75], v[100:103], v[176:179]\n"
      "s_add_u32 s49, s49, 24576\n"
      "v_mfma_f32_16x16x32_bf16 v[176:179], a[76:79], v[104:107], v[176:179]\n"
      "v_mfma_f32_16x16x32_bf16 v[176:179], a[80:83], v[108:111], v[176:179]\n"
      "v_cvt_pk_bf16_f32 v209, v174, v175\n"
      "v_mfma_f32_16x16x32_bf16 v[176:179], a[84:87], v[112:115], v[176:179]\n"
      "buffer_load_dwordx4 v0, s[8:11], s46 offen offset:1024 sc1 lds\n"
      "v_mfma_f32_16x16x32_bf16 v[176:179], a[88:91], v[116:119], v[176:179]\n"
      "v_add_f32 v218, v172, v173\n"
      "v_mfma_f32_16x16x32_bf16 v[176:179], a[92:95], v[120:123], v[176:179]\n"
      "v_add_f32 v218, v218, v174\n"
      "v_lshlrev_b32 v180, 16, v196\n"
      "v_mfma_f32_16x16x32_bf16 v[176:179], a[96:99], v[124:127], v[176:179]\n"
      "ds_read_b32 v22, v24\n"
      "v_lshlrev_b32 v186, 16, v199\n"
      "v_mfma_f32_16x16x32_bf16 v[176:179], a[100:103], v[128:131], v[176:179]\n"
      "buffer_load_dwordx4 v0, s[8:11], s46 offen offset:2048 sc1 lds\n"
      "v_mfma_f32_16x16x32_bf16 v[176:179], a[104:107], v[132:135], v[176:179]\n"
      "v_mul_f32 v219, v172, v172\n"
      "v_fmac_f32 v219, v173, v173\n"
      "v_mfma_f32_16x16x32_bf16 v[176:179], a[108:111], v[136:139], v[176:179]\n"
      "v_lshlrev_b32 v184, 16, v198\n"
      "v_mfma_f32_16x16x32_bf16 v[176:179], a[112:115], v[140:143], v[176:179]\n"
      "v_fmac_f32_e64 v219, v174, v174\n"
      "v_mfma_f32_16x16x32_bf16 v[176:179], a[116:119], v[144:147], v[176:179]\n"
      "s_waitcnt lgkmcnt(0)\n"
      "v_cmp_gt_i32 vcc, v21, v22\n"
      "v_add_f32_e64 v218, v218, v175\n"
      "v_mfma_f32_16x16x32_bf16 v[176:179], a[120:123], v[148:151], v[176:179]\n"
      "v_mfma_f32_16x16x32_bf16 v[176:179], a[124:127], v[152:155], v[176:179]\n"
      "v_and_b32 v185, 0xffff0000, v198\n"
      "v_cvt_pk_bf16_f32 v208, v172, v173\n"
      "v_mfma_f32_16x16x32_bf16 v[176:179], a[128:131], v[156:159], v[176:179]\n"
      "buffer_load_dwordx4 v[196:199], v13, s[8:11], s47 offen offset:1024 sc1\n"
      "v_mfma_f32_16x16x32_bf16 v[176:179], a[132:135], v[160:163], v[176:179]\n"
      "s_cbranch_vccnz .Lsp_vz1_23\n"
      ".Lbk_vz1_23:\n"
      "ds_read_b128 v[204:207], v19\n"
      "v_mfma_f32_16x16x32_bf16 v[176:179], a[136:139], v[164:167], v[176:179]\n"
      "v_add_u32 v21, 1, v21\n"
      "v_mfma_f32_16x16x32_bf16 v[176:179], a[140:143], v[168:171], v[176:179]\n"
      "s_waitcnt vmcnt(10)\n"
      "ds_write_b32 v23, v21\n"
      "v_mfma_f32_16x16x32_bf16 v[180:183], a[144:147], v[100:103], v[180:183]\n"
      "v_fmac_f32 v219, v175, v175\n"
      "v_mfma_f32_16x16x32_bf16 v[180:183], a[148:151], v[104:107], v[180:183]\n"
      "buffer_load_dwordx4 v0, s[8:11], s46 offen offset:3072 sc1 lds\n"
      "v_mfma_f32_16x16x32_bf16 v[180:183], a[152:155], v[108:111], v[180:183]\n"
      "v_cvt_pk_bf16_f32 v210, v176, v177\n"
      "v_fmac_f32 v219, v176, v176\n"
      "v_mfma_f32_16x16x32_bf16 v[180:183], a[156:159], v[112:115], v[180:183]\n"
      "s_add_u32 m0, s40, 61440\n"
      "v_fmac_f32 v219, v177, v177\n"
      "v_mfma_f32_16x16x32_bf16 v[180:183], a[160:163], v[116:119], v[180:183]\n"
      "s_waitcnt lgkmcnt(1)\n"
      "v_add_f32 v204, v204, v206\n"
      "v_add_f32 v205, v205, v207\n"
      "v_mfma_f32_16x16x32_bf16 v[180:183], a[164:167], v[120:123], v[180:183]\n"
      "v_add_f32_dpp v204, v204, v204 quad_perm:[1,0,3,2] row_mask:0xf bank_mask:0xf\n"
      "v_fmac_f32 v219, v178, v178\n"
      "v_mfma_f32_16x16x32_bf16 v[180:183], a[168:171], v[124:127], v[180:183]\n"
      "buffer_load_dwordx4 v1, s[8:11], s46 offen offset:0 sc1 lds\n"
      "v_mfma_f32_16x16x32_bf16 v[180:183], a[172:175], v[128:131], v[180:183]\n"
      "v_add_f32_dpp v205, v205, v205 quad_perm:[1,0,3,2] row_mask:0xf bank_mask:0xf\n"
      "v_add_f32_dpp v204, v204, v204 quad_perm:[2,3,0,1] row_mask:0xf bank_mask:0xf\n"
      "v_mfma_f32_16x16x32_bf16 v[180:183], a[176:179], v[132:135], v[180:183]\n"
      "v_add_f32_dpp v204, v204, v204 row_half_mirror row_mask:0xf bank_mask:0xf\n"
      "v_cvt_pk_bf16_f32 v211, v178, v179\n"
      "v_mfma_f32_16x16x32_bf16 v[180:183], a[180:183], v[136:139], v[180:183]\n"
      "s_add_u32 s38, s38, 24576\n"
      "v_mul_f32 v204, v27, v204\n"
      "v_add_f32_dpp v205, v205, v205 quad_perm:[2,3,0,1] row_mask:0xf bank_mask:0xf\n"
      "v_mfma_f32_16x16x32_bf16 v[180:183], a[184:187], v[140:143], v[180:183]\n"
      "ds_read_b32 v22, v24\n"
      "v_add_f32 v218, v218, v176\n"
      "v_mfma_f32_16x16x32_bf16 v[180:183], a[188:191], v[144:147], v[180:183]\n"
      "v_add_f32_dpp v205, v205, v205 row_half_mirror row_mask:0xf bank_mask:0xf\n"
      "v_fmaak_f32 v205, v27, v205, 0x36a7c5ac\n"
      "v_mfma_f32_16x16x32_bf16 v[180:183], a[192:195], v[148:151], v[180:183]\n"
      "buffer_store_dwordx2 v[204:205], v20, s[8:11], s49 offen sc1\n"
      "v_mfma_f32_16x16x32_bf16 v[180:183], a[196:199], v[152:155], v[180:183]\n"
      "s_add_u32 s50, s50, 24576\n"
      "v_add_f32 v218, v218, v177\n"
      "v_mfma_f32_16x16x32_bf16 v[180:183], a[200:203], v[156:159], v[180:183]\n"
      "v_add_f32 v218, v218, v178\n"
      "v_mfma_f32_16x16x32_bf16 v[180:183], a[204:207], v[160:163], v[180:183]\n"
      "s_waitcnt lgkmcnt(0)\n"
      "v_add_f32 v218, v218, v179\n"
      "v_cmp_gt_i32 vcc, v21, v22\n"
      "v_mfma_f32_16x16x32_bf16 v[180:183], a[208:211], v[164:167], v[180:183]\n"
      "s_waitcnt vmcnt(10)\n"
      "buffer_store_dword v25, v12, s[8:11], s50 offen sc1\n"
      "v_mfma_f32_16x16x32_bf16 v[180:183], a[212:215], v[168:171], v[180:183]\n"
      "v_fmac_f32 v219, v179, v179\n"
      "v_mfma_f32_16x16x32_bf16 v[184:187], a[216:219], v[100:103], v[184:187]\n"
      "s_cbranch_vccnz .Lsp_vy0_24\n"
      ".Lbk_vy0_24:\n"
      "s_sub_u32 s47, s46, s45\n"
      "v_mfma_f32_16x16x32_bf16 v[184:187], a[220:223], v[104:107], v[184:187]\n"
      "s_waitcnt vmcnt(10)\n"
      "v_lshlrev_b32 v174, 16, v193\n"
      "v_and_b32 v179, 0xffff0000, v195\n"
      "v_mfma_f32_16x16x32_bf16 v[184:187], a[224:227], v[108:111], v[184:187]\n"
      "ds_read_b128 v[100:103], v10 offset:8192\n"
      "v_cvt_pk_bf16_f32 v213, v182, v183\n"
      "v_mfma_f32_16x16x32_bf16 v[184:187], a[228:231], v[112:115], v[184:187]\n"
      "ds_read_b128 v[108:111], v10 offset:10240\n"
      "v_and_b32 v173, 0xffff0000, v192\n"
      "v_mfma_f32_16x16x32_bf16 v[184:187], a[232:235], v[116:119], v[184:187]\n"
      "v_fmac_f32 v219, v180, v180\n"
      "v_mfma_f32_16x16x32_bf16 v[184:187], a[236:239], v[120:123], v[184:187]\n"
      "ds_read_b128 v[120:123], v10 offset:13312\n"
      "v_add_f32 v218, v218, v180\n"
      "v_mfma_f32_16x16x32_bf16 v[184:187], a[240:243], v[124:127], v[184:187]\n"
      "v_add_f32 v218, v218, v181\n"
      "v_add_f32 v218, v218, v182\n"
      "v_mfma_f32_16x16x32_bf16 v[184:187], a[244:247], v[128:131], v[184:187]\n"
      "ds_read_b128 v[128:131], v10 offset:15360\n"
      "v_lshlrev_b32 v178, 16, v195\n"
      "v_mfma_f32_16x16x32_bf16 v[184:187], a[248:251], v[132:135], v[184:187]\n"
      "v_mfma_f32_16x16x32_bf16 v[184:187], a[252:255], v[136:139], v[184:187]\n"
      "ds_read_b128 v[104:107], v10 offset:9216\n"
      "v_lshlrev_b32 v172, 16, v192\n"
      "v_mfma_f32_16x16x32_bf16 v[184:187], v[32:35], v[140:143], v[184:187]\n"
      "buffer_store_dwordx4 v[208:211], v15, s[8:11], s38 offen sc1\n"
      "v_mfma_f32_16x16x32_bf16 v[184:187], v[36:39], v[144:147], v[184:187]\n"
      "s_mov_b32 s46, s36\n"
      "ds_read_b128 v[112:115], v10 offset:11264\n"
      "v_fmac_f32 v219, v181, v181\n"
      "v_mfma_f32_16x16x32_bf16 v[184:187], v[40:43], v[148:151], v[184:187]\n"
      "ds_read2_b32 v[134:135], v6 offset0:64 offset1:96\n"
      "v_cvt_pk_bf16_f32 v212, v180, v181\n"
      "v_mfma_f32_16x16x32_bf16 v[184:187], v[44:47], v[152:155], v[184:187]\n"
      "ds_read2_b32 v[132:133], v6 offset0:0 offset1:32\n"
      "s_add_u32 s36, s36, 24576\n"
      "v_mfma_f32_16x16x32_bf16 v[184:187], v[48:51], v[156:159], v[184:187]\n"
      "ds_read_b128 v[116:119], v10 offset:12288\n"
      "v_fmac_f32 v219, v182, v182\n"
      "v_mfma_f32_16x16x32_bf16 v[184:187], v[52:55], v[160:163], v[184:187]\n"
      "ds_read_b128 v[124:127], v10 offset:14336\n"
      "v_add_f32 v218, v218, v183\n"
      "v_mfma_f32_16x16x32_bf16 v[184:187], v[56:59], v[164:167], v[184:187]\n"
      "s_waitcnt lgkmcnt(5)\n"
      "ds_read_b128 v[160:163], v10 offset:6144\n"
      "v_and_b32 v177, 0xffff0000, v194\n"
      "v_mfma_f32_16x16x32_bf16 v[184:187], v[60:63], v[168:171], v[184:187]\n"
      "s_sub_u32 s20, s20, 1\n"
      "s_cbranch_scc1 .Lv_tail\n"
      "v_mfma_f32_16x16x32_bf16 v[188:191], v[64:67], v[100:103], 0\n"
      "ds_read_b128 v[140:143], v10 offset:1024\n"
      "v_and_b32 v175, 0xffff0000, v193\n"
      "v_mfma_f32_16x16x32_bf16 v[188:191], v[68:71], v[104:107], v[188:191]\n"
      "ds_read2_b32 v[168:169], v7 offset0:0 offset1:32\n"
      "v_lshlrev_b32 v176, 16, v194\n"
      "v_mfma_f32_16x16x32_bf16 v[188:191], v[72:75], v[108:111], v[188:191]\n"
      "s_waitcnt lgkmcnt(5)\n"
      "ds_read_b128 v[164:167], v10 offset:7168\n"
      "v_add_f32 v218, v218, v184\n"
      "v_mfma_f32_16x16x32_bf16 v[188:191], v[76:79], v[112:115], v[188:191]\n"
      "s_waitcnt lgkmcnt(5)\n"
      "ds_read_b128 v[144:147], v10 offset:2048\n"
      "v_add_f32 v218, v218, v185\n"
      "v_mfma_f32_16x16x32_bf16 v[188:191], v[80:83], v[116:119], v[188:191]\n"
      "buffer_load_dwordx4 v[192:195], v13, s[8:11], s47 offen sc1\n"
      "v_mfma_f32_16x16x32_bf16 v[188:191], v[84:87], v[120:123], v[188:191]\n"
      "s_waitcnt lgkmcnt(4)\n"
      "ds_read2_b32 v[170:171], v7 offset0:64 offset1:96\n"
      "v_fmac_f32 v219, v183, v183\n"
      "v_mfma_f32_16x16x32_bf16 v[188:191], v[88:91], v[124:127], v[188:191]\n"
      "s_min_u32 s36, s36, s6\n"
      "ds_read_b128 v[136:139], v10 offset:0\n"
      "v_fmac_f32 v219, v184, v184\n"
      "v_mfma_f32_16x16x32_bf16 v[188:191], v[92:95], v[128:131], v[188:191]\n"
      "ds_read_b128 v[148:151], v10 offset:3072\n"
      "v_mfma_f32_16x16x32_bf16 v[188:191], v[96:99], v[132:135], v[188:191]\n"
      "v_cvt_pk_bf16_f32 v215, v186, v187\n"
      "v_add_f32 v218, v218, v186\n"
      "v_mfma_f32_16x16x32_bf16 v[172:175], a[0:3], v[100:103], v[172:175]\n"
      "ds_read_b128 v[152:155], v10 offset:4096\n"
      "v_add_f32 v218, v218, v187\n"
      "v_mfma_f32_16x16x32_bf16 v[172:175], a[4:7], v[104:107], v[172:175]\n"
      "ds_read_b128 v[156:159], v10 offset:5120\n"
      "v_fmac_f32 v219, v185, v185\n"
      "v_mfma_f32_16x16x32_bf16 v[172:175], a[8:11], v[108:111], v[172:175]\n"
      "s_add_u32 m0, s39, 0\n"
      "v_cvt_pk_bf16_f32 v214, v184, v185\n"
      "v_mfma_f32_16x16x32_bf16 v[172:175], a[12:15], v[112:115], v[172:175]\n"
      "ds_write_b32 v18, v218\n"
      "v_mfma_f32_16x16x32_bf16 v[172:175], a[16:19], v[116:119], v[172:175]\n"
      "s_waitcnt vmcnt(10) lgkmcnt(4)\n"
      "v_fmac_f32 v219, v186, v186\n"
      "v_cmp_ne_u32 vcc, s21, v28\n"
      "v_mfma_f32_16x16x32_bf16 v[172:175], a[20:23], v[120:123], v[172:175]\n"
      "ds_write_b32 v17, v190 offset:512\n"
      "v_mfma_f32_16x16x32_bf16 v[172:175], a[24:27], v[124:127], v[172:175]\n"
      "buffer_store_dwordx4 v[212:215], v15, s[8:11], s38 offen offset:1024 sc1\n"
      "v_mfma_f32_16x16x32_bf16 v[172:175], a[28:31], v[128:131], v[172:175]\n"
      "ds_write_b32 v17, v188 offset:0\n"
      "v_mfma_f32_16x16x32_bf16 v[172:175], a[32:35], v[132:135], v[172:175]\n"
      "v_fmac_f32_e64 v219, v187, v187\n"
      "v_mfma_f32_16x16x32_bf16 v[172:175], a[36:39], v[136:139], v[172:175]\n"
      "ds_write_b32 v17, v191 offset:768\n"
      "v_mfma_f32_16x16x32_bf16 v[172:175], a[40:43], v[140:143], v[172:175]\n"
      "s_cbranch_vccnz .Lsp_v0_25\n"
      ".Lbk_v0_25:\n"
      "v_add_u32 v21, 1, v21\n"
      "v_mfma_f32_16x16x32_bf16 v[172:175], a[44:47], v[144:147], v[172:175]\n"
      "s_waitcnt lgkmcnt(3)\n"
      "ds_write_b32 v18, v219 offset:4\n"
      "v_mfma_f32_16x16x32_bf16 v[172:175], a[48:51], v[148:151], v[172:175]\n"
      "buffer_load_dword v28, v11, s[8:11], s36 offen sc1\n"
      "v_mfma_f32_16x16x32_bf16 v[172:175], a[52:55], v[152:155], v[172:175]\n"
      "ds_write_b32 v17, v189 offset:256\n"
      "v_mfma_f32_16x16x32_bf16 v[172:175], a[56:59], v[156:159], v[172:175]\n"
      "s_waitcnt vmcnt(8) lgkmcnt(3)\n"
      "v_lshlrev_b32 v186, 16, v199\n"
      "v_and_b32 v185, 0xffff0000, v198\n"
      "v_mfma_f32_16x16x32_bf16 v[172:175], a[60:63], v[160:163], v[172:175]\n"
      "ds_write_b32 v23, v21\n"
      "v_mfma_f32_16x16x32_bf16 v[172:175], a[64:67], v[164:167], v[172:175]\n"
      "v_and_b32 v181, 0xffff0000, v196\n"
      "v_and_b32 v187, 0xffff0000, v199\n"
      "v_mfma_f32_16x16x32_bf16 v[172:175], a[68:71], v[168:171], v[172:175]\n"
      "buffer_load_dwordx4 v0, s[8:11], s46 offen offset:3072 sc1 lds\n"
      "v_mfma_f32_16x16x32_bf16 v[176:179], a[72:75], v[100:103], v[176:179]\n"
      "v_lshlrev_b32 v182, 16, v197\n"
      "s_add_u32 s38, s38, 24576\n"
      "v_mfma_f32_16x16x32_bf16 v[176:179], a[76:79], v[104:107], v[176:179]\n"
      "v_lshlrev_b32 v184, 16, v198\n"
      "v_mfma_f32_16x16x32_bf16 v[176:179], a[80:83], v[108:111], v[176:179]\n"
      "v_cvt_pk_bf16_f32 v208, v172, v173\n"
      "v_mfma_f32_16x16x32_bf16 v[176:179], a[84:87], v[112:115], v[176:179]\n"
      "v_mul_f32 v219, v172, v172\n"
      "v_mfma_f32_16x16x32_bf16 v[176:179], a[88:91], v[116:119], v[176:179]\n"
      "v_lshlrev_b32 v180, 16, v196\n"
      "v_and_b32 v183, 0xffff0000, v197\n"
      "v_mfma_f32_16x16x32_bf16 v[176:179], a[92:95], v[120:123], v[176:179]\n"
      "v_fmac_f32 v219, v173, v173\n"
      "v_mfma_f32_16x16x32_bf16 v[176:179], a[96:99], v[124:127], v[176:179]\n"
      "buffer_load_dwordx4 v[196:199], v13, s[8:11], s47 offen offset:1024 sc1\n"
      "v_mfma_f32_16x16x32_bf16 v[176:179], a[100:103], v[128:131], v[176:179]\n"
      "ds_read_b32 v22, v24\n"
      "v_mfma_f32_16x16x32_bf16 v[176:179], a[104:107], v[132:135], v[176:179]\n"
      "v_fmac_f32 v219, v174, v174\n"
      "v_mfma_f32_16x16x32_bf16 v[176:179], a[108:111], v[136:139], v[176:179]\n"
      "v_fmac_f32 v219, v175, v175\n"
      "v_mfma_f32_16x16x32_bf16 v[176:179], a[112:115], v[140:143], v[176:179]\n"
      "buffer_load_dwordx4 v0, s[8:11], s46 offen offset:0 sc1 lds\n"
      "v_mfma_f32_16x16x32_bf16 v[176:179], a[116:119], v[144:147], v[176:179]\n"
      "v_mfma_f32_16x16x32_bf16 v[176:179], a[120:123], v[148:151], v[176:179]\n"
      "s_waitcnt lgkmcnt(0)\n"
      "v_cmp_gt_i32 vcc, v21, v22\n"
      "v_add_f32_e64 v218, v172, v173\n"
      "v_mfma_f32_16x16x32_bf16 v[176:179], a[124:127], v[152:155], v[176:179]\n"
      "v_add_f32_e64 v218, v218, v174\n"
      "v_add_f32_e64 v218, v218, v175\n"
      "v_mfma_f32_16x16x32_bf16 v[176:179], a[128:131], v[156:159], v[176:179]\n"
      "buffer_load_dwordx4 v0, s[8:11], s46 offen offset:1024 sc1 lds\n"
      "v_mfma_f32_16x16x32_bf16 v[176:179], a[132:135], v[160:163], v[176:179]\n"
      "s_cbranch_vccnz .Lsp_vz0_26\n"
      ".Lbk_vz0_26:\n"
      "ds_read_b128 v[204:207], v19\n"
      "v_mfma_f32_16x16x32_bf16 v[176:179], a[136:139], v[164:167], v[176:179]\n"
      "v_cvt_pk_bf16_f32 v209, v174, v175\n"
      "v_add_u32 v21, 1, v21\n"
      "v_mfma_f32_16x16x32_bf16 v[176:179], a[140:143], v[168:171], v[176:179]\n"
      "s_waitcnt vmcnt(10)\n"
      "ds_write_b32 v23, v21\n"
      "v_mfma_f32_16x16x32_bf16 v[180:183], a[144:147], v[100:103], v[180:183]\n"
      "v_mfma_f32_16x16x32_bf16 v[180:183], a[148:151], v[104:107], v[180:183]\n"
      "buffer_load_dwordx4 v0, s[8:11], s46 offen offset:2048 sc1 lds\n"
      "v_mfma_f32_16x16x32_bf16 v[180:183], a[152:155], v[108:111], v[180:183]\n"
      "s_add_u32 s49, s49, 24576\n"
      "s_add_u32 m0, s40, 0\n"
      "v_mfma_f32_16x16x32_bf16 v[180:183], a[156:159], v[112:115], v[180:183]\n"
      "v_mfma_f32_16x16x32_bf16 v[180:183], a[160:163], v[116:119], v[180:183]\n"
      "s_waitcnt lgkmcnt(1)\n"
      "v_add_f32 v204, v204, v206\n"
      "v_cvt_pk_bf16_f32 v210, v176, v177\n"
      "v_mfma_f32_16x16x32_bf16 v[180:183], a[164:167], v[120:123], v[180:183]\n"
      "buffer_load_dwordx4 v1, s[8:11], s46 offen offset:0 sc1 lds\n"
      "v_mfma_f32_16x16x32_bf16 v[180:183], a[168:171], v[124:127], v[180:183]\n"
      "v_add_f32 v205, v205, v207\n"
      "v_add_f32_dpp v204, v204, v204 quad_perm:[1,0,3,2] row_mask:0xf bank_mask:0xf\n"
      "v_mfma_f32_16x16x32_bf16 v[180:183], a[172:175], v[128:131], v[180:183]\n"
      "v_fmac_f32 v219, v176, v176\n"
      "v_mfma_f32_16x16x32_bf16 v[180:183], a[176:179], v[132:135], v[180:183]\n"
      "v_add_f32_dpp v204, v204, v204 quad_perm:[2,3,0,1] row_mask:0xf bank_mask:0xf\n"
      "v_cvt_pk_bf16_f32 v211, v178, v179\n"
      "v_mfma_f32_16x16x32_bf16 v[180:183], a[180:183], v[136:139], v[180:183]\n"
      "buffer_store_dwordx4 v[208:211], v15, s[8:11], s38 offen sc1\n"
      "v_mfma_f32_16x16x32_bf16 v[180:183], a[184:187], v[140:143], v[180:183]\n"
      "ds_read_b32 v22, v24\n"
      "v_add_f32_dpp v205, v205, v205 quad_perm:[1,0,3,2] row_mask:0xf bank_mask:0xf\n"
      "v_mfma_f32_16x16x32_bf16 v[180:183], a[188:191], v[144:147], v[180:183]\n"
      "s_add_u32 s50, s50, 24576\n"
      "v_add_f32_dpp v205, v205, v205 quad_perm:[2,3,0,1] row_mask:0xf bank_mask:0xf\n"
      "v_fmac_f32 v219, v177, v177\n"
      "v_mfma_f32_16x16x32_bf16 v[180:183], a[192:195], v[148:151], v[180:183]\n"
      "v_add_f32_dpp v205, v205, v205 row_half_mirror row_mask:0xf bank_mask:0xf\n"
      "v_fmac_f32 v219, v178, v178\n"
      "v_mfma_f32_16x16x32_bf16 v[180:183], a[196:199], v[152:155], v[180:183]\n"
      ".Lv_p1:\n"
      "s_cbranch_execz .Lv_pr\n"
      "v_add_f32_dpp v204, v204, v204 row_half_mirror row_mask:0xf bank_mask:0xf\n"
      "v_mfma_f32_16x16x32_bf16 v[180:183], a[200:203], v[156:159], v[180:183]\n"
      "v_mul_f32 v204, v27, v204\n"
      "v_fmaak_f32 v205, v27, v205, 0x36a7c5ac\n"
      "v_mfma_f32_16x16x32_bf16 v[180:183], a[204:207], v[160:163], v[180:183]\n"
      "s_waitcnt vmcnt(12) lgkmcnt(1)\n"
      "buffer_store_dword v25, v12, s[8:11], s50 offen sc1\n"
      "v_mfma_f32_16x16x32_bf16 v[180:183], a[208:211], v[164:167], v[180:183]\n"
      "s_waitcnt lgkmcnt(0)\n"
      "v_cmp_gt_i32 vcc, v21, v22\n"
      "v_add_f32 v218, v218, v176\n"
      "v_mfma_f32_16x16x32_bf16 v[180:183], a[212:215], v[168:171], v[180:183]\n"
      "v_add_f32 v218, v218, v177\n"
      "v_add_f32_e64 v218, v218, v178\n"
      "v_mfma_f32_16x16x32_bf16 v[184:187], a[216:219], v[100:103], v[184:187]\n"
      "buffer_store_dwordx2 v[204:205], v20, s[8:11], s49 offen sc1\n"
      "v_mfma_f32_16x16x32_bf16 v[184:187], a[220:223], v[104:107], v[184:187]\n"
      "s_sub_u32 s47, s46, s45\n"
      "s_cbranch_vccnz .Lsp_vy1_27\n"
      ".Lbk_vy1_27:\n"
      "v_mfma_f32_16x16x32_bf16 v[184:187], a[224:227], v[108:111], v[184:187]\n"
      "s_waitcnt vmcnt(11)\n"
      "ds_read_b128 v[104:107], v10 offset:29696\n"
      "v_and_b32 v177, 0xffff0000, v194\n"
      "v_mfma_f32_16x16x32_bf16 v[184:187], a[228:231], v[112:115], v[184:187]\n"
      "v_fmac_f32 v219, v179, v179\n"
      "v_mfma_f32_16x16x32_bf16 v[184:187], a[232:235], v[116:119], v[184:187]\n"
      "v_fmac_f32 v219, v180, v180\n"
      "v_add_f32 v218, v218, v179\n"
      "v_mfma_f32_16x16x32_bf16 v[184:187], a[236:239], v[120:123], v[184:187]\n"
      "v_cvt_pk_bf16_f32 v212, v180, v181\n"
      "v_and_b32 v179, 0xffff0000, v195\n"
      "v_mfma_f32_16x16x32_bf16 v[184:187], a[240:243], v[124:127], v[184:187]\n"
      "ds_read_b128 v[120:123], v10 offset:33792\n"
      "v_lshlrev_b32 v174, 16, v193\n"
      "v_mfma_f32_16x16x32_bf16 v[184:187], a[244:247], v[128:131], v[184:187]\n"
      "ds_read_b128 v[112:115], v10 offset:31744\n"
      "v_add_f32 v218, v218, v180\n"
      "v_mfma_f32_16x16x32_bf16 v[184:187], a[248:251], v[132:135], v[184:187]\n"
      "ds_read_b128 v[100:103], v10 offset:28672\n"
      "v_add_f32 v218, v218, v181\n"
      "v_mfma_f32_16x16x32_bf16 v[184:187], a[252:255], v[136:139], v[184:187]\n"
      "ds_read_b128 v[124:127], v10 offset:34816\n"
      "v_mfma_f32_16x16x32_bf16 v[184:187], v[32:35], v[140:143], v[184:187]\n"
      "ds_read_b128 v[108:111], v10 offset:30720\n"
      "v_and_b32 v175, 0xffff0000, v193\n"
      "v_mfma_f32_16x16x32_bf16 v[184:187], v[36:39], v[144:147], v[184:187]\n"
      "ds_read_b128 v[116:119], v10 offset:32768\n"
      "v_fmac_f32 v219, v181, v181\n"
      "v_mfma_f32_16x16x32_bf16 v[184:187], v[40:43], v[148:151], v[184:187]\n"
      "v_add_f32 v218, v218, v182\n"
      "v_mfma_f32_16x16x32_bf16 v[184:187], v[44:47], v[152:155], v[184:187]\n"
      "ds_read2_b32 v[132:133], v8 offset0:0 offset1:32\n"
      "v_and_b32 v173, 0xffff0000, v192\n"
      "v_mfma_f32_16x16x32_bf16 v[184:187], v[48:51], v[156:159], v[184:187]\n"
      "ds_read_b128 v[128:131], v10 offset:35840\n"
      "v_lshlrev_b32 v178, 16, v195\n"
      "v_mfma_f32_16x16x32_bf16 v[184:187], v[52:55], v[160:163], v[184:187]\n"
      "ds_read_b128 v[156:159], v10 offset:25600\n"
      "v_lshlrev_b32 v172, 16, v192\n"
      "v_mfma_f32_16x16x32_bf16 v[184:187], v[56:59], v[164:167], v[184:187]\n"
      "ds_read_b128 v[144:147], v10 offset:22528\n"
      "v_lshlrev_b32 v176, 16, v194\n"
      "v_mfma_f32_16x16x32_bf16 v[184:187], v[60:63], v[168:171], v[184:187]\n"
      "s_waitcnt lgkmcnt(5)\n"
      "buffer_load_dwordx4 v[192:195], v13, s[8:11], s47 offen sc1\n"
      "v_mfma_f32_16x16x32_bf16 v[188:191], v[64:67], v[100:103], 0\n"
      "ds_read2_b32 v[134:135], v8 offset0:64 offset1:96\n"
      "v_fmac_f32 v219, v182, v182\n"
      "v_mfma_f32_16x16x32_bf16 v[188:191], v[68:71], v[104:107], v[188:191]\n"
      "ds_read_b128 v[148:151], v10 offset:23552\n"
      "v_add_f32 v218, v218, v183\n"
      "v_mfma_f32_16x16x32_bf16 v[188:191], v[72:75], v[108:111], v[188:191]\n"
      "s_mov_b32 s46, s36\n"
      "v_cvt_pk_bf16_f32 v213, v182, v183\n"
      "v_cvt_pk_bf16_f32 v215, v186, v187\n"
      "v_mfma_f32_16x16x32_bf16 v[188:191], v[76:79], v[112:115], v[188:191]\n"
      "s_waitcnt lgkmcnt(4)\n"
      "ds_read_b128 v[160:163], v10 offset:26624\n"
      "v_fmac_f32 v219, v183, v183\n"
      "v_mfma_f32_16x16x32_bf16 v[188:191], v[80:83], v[116:119], v[188:191]\n"
      "ds_read_b128 v[140:143], v10 offset:21504\n"
      "v_fmac_f32 v219, v184, v184\n"
      "v_mfma_f32_16x16x32_bf16 v[188:191], v[84:87], v[120:123], v[188:191]\n"
      "ds_read2_b32 v[168:169], v9 offset0:0 offset1:32\n"
      "v_add_f32 v218, v218, v184\n"
      "v_mfma_f32_16x16x32_bf16 v[188:191], v[88:91], v[124:127], v[188:191]\n"
      "ds_read_b128 v[152:155], v10 offset:24576\n"
      "v_fmac_f32 v219, v185, v185\n"
      "v_mfma_f32_16x16x32_bf16 v[188:191], v[92:95], v[128:131], v[188:191]\n"
      "s_waitcnt lgkmcnt(5)\n"
      "ds_read_b128 v[136:139], v10 offset:20480\n"
      "v_add_f32 v218, v218, v185\n"
      "v_mfma_f32_16x16x32_bf16 v[188:191], v[96:99], v[132:135], v[188:191]\n"
      "s_cmp_eq_u32 s36, s37\n"
      "s_cselect_b32 s48, 49152, s51\n"
      "ds_read_b128 v[164:167], v10 offset:27648\n"
      "v_mfma_f32_16x16x32_bf16 v[172:175], a[0:3], v[100:103], v[172:175]\n"
      "ds_read2_b32 v[170:171], v9 offset0:64 offset1:96\n"
      "v_cvt_pk_bf16_f32 v214, v184, v185\n"
      "v_mfma_f32_16x16x32_bf16 v[172:175], a[4:7], v[104:107], v[172:175]\n"
      "v_fmac_f32 v219, v186, v186\n"
      "v_add_f32 v218, v218, v186\n"
      "v_mfma_f32_16x16x32_bf16 v[172:175], a[8:11], v[108:111], v[172:175]\n"
      "ds_write_b32 v17, v189 offset:256\n"
      "v_mfma_f32_16x16x32_bf16 v[172:175], a[12:15], v[112:115], v[172:175]\n"
      "v_add_f32 v218, v218, v187\n"
      "v_add_u32 v21, 1, v21\n"
      "v_mfma_f32_16x16x32_bf16 v[172:175], a[16:19], v[116:119], v[172:175]\n"
      "buffer_store_dwordx4 v[212:215], v15, s[8:11], s38 offen offset:1024 sc1\n"
      "v_mfma_f32_16x16x32_bf16 v[172:175], a[20:23], v[120:123], v[172:175]\n"
      "ds_write_b32 v17, v190 offset:512\n"
      "v_mfma_f32_16x16x32_bf16 v[172:175], a[24:27], v[124:127], v[172:175]\n"
      "v_fmac_f32 v219, v187, v187\n"
      "v_mfma_f32_16x16x32_bf16 v[172:175], a[28:31], v[128:131], v[172:175]\n"
      "s_add_u32 s36, s36, s48\n"
      "ds_write_b32 v18, v218\n"
      "v_mfma_f32_16x16x32_bf16 v[172:175], a[32:35], v[132:135], v[172:175]\n"
      "s_waitcnt vmcnt(11) lgkmcnt(3)\n"
      "v_cmp_ne_u32 vcc, s21, v28\n"
      "v_mfma_f32_16x16x32_bf16 v[172:175], a[36:39], v[136:139], v[172:175]\n"
      "ds_write_b32 v17, v188 offset:0\n"
      "v_mfma_f32_16x16x32_bf16 v[172:175], a[40:43], v[140:143], v[172:175]\n"
      "s_waitcnt vmcnt(9) lgkmcnt(3)\n"
      "s_min_u32 s36, s36, s6\n"
      "v_lshlrev_b32 v182, 16, v197\n"
      "v_mfma_f32_16x16x32_bf16 v[172:175], a[44:47], v[144:147], v[172:175]\n"
      "ds_write_b32 v17, v191 offset:768\n"
      "v_mfma_f32_16x16x32_bf16 v[172:175], a[48:51], v[148:151], v[172:175]\n"
      "s_cbranch_vccnz .Lsp_v1_28\n"
      ".Lbk_v1_28:\n"
      "v_and_b32 v181, 0xffff0000, v196\n"
      "v_mfma_f32_16x16x32_bf16 v[172:175], a[52:55], v[152:155], v[172:175]\n"
      "ds_write_b32 v18, v219 offset:4\n"
      "v_mfma_f32_16x16x32_bf16 v[172:175], a[56:59], v[156:159], v[172:175]\n"
      "buffer_load_dword v28, v11, s[8:11], s36 offen sc1\n"
      "v_mfma_f32_16x16x32_bf16 v[172:175], a[60:63], v[160:163], v[172:175]\n"
      "s_add_u32 m0, s39, 20480\n"
      "ds_write_b32 v23, v21\n"
      "v_mfma_f32_16x16x32_bf16 v[172:175], a[64:67], v[164:167], v[172:175]\n"
      "v_and_b32 v187, 0xffff0000, v199\n"
      "v_and_b32 v183, 0xffff0000, v197\n"
      "v_mfma_f32_16x16x32_bf16 v[172:175], a[68:71], v[168:171], v[172:175]\n"
      "buffer_load_dwordx4 v0, s[8:11], s46 offen offset:0 sc1 lds\n"
      "v_mfma_f32_16x16x32_bf16 v[176:179], a[72:75], v[100:103], v[176:179]\n"
      "s_add_u32 s49, s49, 24576\n"
      "v_mfma_f32_16x16x32_bf16 v[176:179], a[76:79], v[104:107], v[176:179]\n"
      "v_mfma_f32_16x16x32_bf16 v[176:179], a[80:83], v[108:111], v[176:179]\n"
      "v_cvt_pk_bf16_f32 v209, v174, v175\n"
      "v_mfma_f32_16x16x32_bf16 v[176:179], a[84:87], v[112:115], v[176:179]\n"
      "buffer_load_dwordx4 v0, s[8:11], s46 offen offset:1024 sc1 lds\n"
      "v_mfma_f32_16x16x32_bf16 v[176:179], a[88:91], v[116:119], v[176:179]\n"
      "v_add_f32 v218, v172, v173\n"
      "v_mfma_f32_16x16x32_bf16 v[176:179], a[92:95], v[120:123], v[176:179]\n"
      "v_add_f32 v218, v218, v174\n"
      "v_lshlrev_b32 v180, 16, v196\n"
      "v_mfma_f32_16x16x32_bf16 v[176:179], a[96:99], v[124:127], v[176:179]\n"
      "ds_read_b32 v22, v24\n"
      "v_lshlrev_b32 v186, 16, v199\n"
      "v_mfma_f32_16x16x32_bf16 v[176:179], a[100:103], v[128:131], v[176:179]\n"
      "buffer_load_dwordx4 v0, s[8:11], s46 offen offset:2048 sc1 lds\n"
      "v_mfma_f32_16x16x32_bf16 v[176:179], a[104:107], v[132:135], v[176:179]\n"
      "v_mul_f32 v219, v172, v172\n"
      "v_fmac_f32 v219, v173, v173\n"
      "v_mfma_f32_16x16x32_bf16 v[176:179], a[108:111], v[136:139], v[176:179]\n"
      "v_lshlrev_b32 v184, 16, v198\n"
      "v_mfma_f32_16x16x32_bf16 v[176:179], a[112:115], v[140:143], v[176:179]\n"
      "v_fmac_f32_e64 v219, v174, v174\n"
      "v_mfma_f32_16x16x32_bf16 v[176:179], a[116:119], v[144:147], v[176:179]\n"
      "s_waitcnt lgkmcnt(0)\n"
      "v_cmp_gt_i32 vcc, v21, v22\n"
      "v_add_f32_e64 v218, v218, v175\n"
      "v_mfma_f32_16x16x32_bf16 v[176:179], a[120:123], v[148:151], v[176:179]\n"
      "v_mfma_f32_16x16x32_bf16 v[176:179], a[124:127], v[152:155], v[176:179]\n"
      "v_and_b32 v185, 0xffff0000, v198\n"
      "v_cvt_pk_bf16_f32 v208, v172, v173\n"
      "v_mfma_f32_16x16x32_bf16 v[176:179], a[128:131], v[156:159], v[176:179]\n"
      "buffer_load_dwordx4 v[196:199], v13, s[8:11], s47 offen offset:1024 sc1\n"
      "v_mfma_f32_16x16x32_bf16 v[176:179], a[132:135], v[160:163], v[176:179]\n"
      "s_cbranch_vccnz .Lsp_vz1_29\n"
      ".Lbk_vz1_29:\n"
      "ds_read_b128 v[204:207], v19\n"
      "v_mfma_f32_16x16x32_bf16 v[176:179], a[136:139], v[164:167], v[176:179]\n"
      "v_add_u32 v21, 1, v21\n"
      "v_mfma_f32_16x16x32_bf16 v[176:179], a[140:143], v[168:171], v[176:179]\n"
      "s_waitcnt vmcnt(10)\n"
      "ds_write_b32 v23, v21\n"
      "v_mfma_f32_16x16x32_bf16 v[180:183], a[144:147], v[100:103], v[180:183]\n"
      "v_fmac_f32 v219, v175, v175\n"
      "v_mfma_f32_16x16x32_bf16 v[180:183], a[148:151], v[104:107], v[180:183]\n"
      "buffer_load_dwordx4 v0, s[8:11], s46 offen offset:3072 sc1 lds\n"
      "v_mfma_f32_16x16x32_bf16 v[180:183], a[152:155], v[108:111], v[180:183]\n"
      "v_cvt_pk_bf16_f32 v210, v176, v177\n"
      "v_fmac_f32 v219, v176, v176\n"
      "v_mfma_f32_16x16x32_bf16 v[180:183], a[156:159], v[112:115], v[180:183]\n"
      "s_add_u32 m0, s40, 20480\n"
      "v_fmac_f32 v219, v177, v177\n"
      "v_mfma_f32_16x16x32_bf16 v[180:183], a[160:163], v[116:119], v[180:183]\n"
      "s_waitcnt lgkmcnt(1)\n"
      "v_add_f32 v204, v204, v206\n"
      "v_add_f32 v205, v205, v207\n"
      "v_mfma_f32_16x16x32_bf16 v[180:183], a[164:167], v[120:123], v[180:183]\n"
      "v_add_f32_dpp v204, v204, v204 quad_perm:[1,0,3,2] row_mask:0xf bank_mask:0xf\n"
      "v_fmac_f32 v219, v178, v178\n"
      "v_mfma_f32_16x16x32_bf16 v[180:183], a[168:171], v[124:127], v[180:183]\n"
      "buffer_load_dwordx4 v1, s[8:11], s46 offen offset:0 sc1 lds\n"
      "v_mfma_f32_16x16x32_bf16 v[180:183], a[172:175], v[128:131], v[180:183]\n"
      "v_add_f32_dpp v205, v205, v205 quad_perm:[1,0,3,2] row_mask:0xf bank_mask:0xf\n"
      "v_add_f32_dpp v204, v204, v204 quad_perm:[2,3,0,1] row_mask:0xf bank_mask:0xf\n"
      "v_mfma_f32_16x16x32_bf16 v[180:183], a[176:179], v[132:135], v[180:183]\n"
      "v_add_f32_dpp v204, v204, v204 row_half_mirror row_mask:0xf bank_mask:0xf\n"
      "v_cvt_pk_bf16_f32 v211, v178, v179\n"
      "v_mfma_f32_16x16x32_bf16 v[180:183], a[180:183], v[136:139], v[180:183]\n"
      "s_add_u32 s38, s38, 24576\n"
      "v_mul_f32 v204, v27, v204\n"
      "v_add_f32_dpp v205, v205, v205 quad_perm:[2,3,0,1] row_mask:0xf bank_mask:0xf\n"
      "v_mfma_f32_16x16x32_bf16 v[180:183], a[184:187], v[140:143], v[180:183]\n"
      "ds_read_b32 v22, v24\n"
      "v_add_f32 v218, v218, v176\n"
      "v_mfma_f32_16x16x32_bf16 v[180:183], a[188:191], v[144:147], v[180:183]\n"
      "v_add_f32_dpp v205, v205, v205 row_half_mirror row_mask:0xf bank_mask:0xf\n"
      "v_fmaak_f32 v205, v27, v205, 0x36a7c5ac\n"
      "v_mfma_f32_16x16x32_bf16 v[180:183], a[192:195], v[148:151], v[180:183]\n"
      "buffer_store_dwordx2 v[204:205], v20, s[8:11], s49 offen sc1\n"
      "v_mfma_f32_16x16x32_bf16 v[180:183], a[196:199], v[152:155], v[180:183]\n"
      "s_add_u32 s50, s50, 24576\n"
      "v_add_f32 v218, v218, v177\n"
      "v_mfma_f32_16x16x32_bf16 v[180:183], a[200:203], v[156:159], v[180:183]\n"
      "v_add_f32 v218, v218, v178\n"
      "v_mfma_f32_16x16x32_bf16 v[180:183], a[204:207], v[160:163], v[180:183]\n"
      "s_waitcnt lgkmcnt(0)\n"
      "v_add_f32 v218, v218, v179\n"
      "v_cmp_gt_i32 vcc, v21, v22\n"
      "v_mfma_f32_16x16x32_bf16 v[180:183], a[208:211], v[164:167], v[180:183]\n"
      "s_waitcnt vmcnt(10)\n"
      "buffer_store_dword v25, v12, s[8:11], s50 offen sc1\n"
      "v_mfma_f32_16x16x32_bf16 v[180:183], a[212:215], v[168:171], v[180:183]\n"
      "v_fmac_f32 v219, v179, v179\n"
      "v_mfma_f32_16x16x32_bf16 v[184:187], a[216:219], v[100:103], v[184:187]\n"
      "s_cbranch_vccnz .Lsp_vy0_30\n"
      ".Lbk_vy0_30:\n"
      "s_sub_u32 s47, s46, s45\n"
      "v_mfma_f32_16x16x32_bf16 v[184:187], a[220:223], v[104:107], v[184:187]\n"
      "s_waitcnt vmcnt(10)\n"
      "v_lshlrev_b32 v174, 16, v193\n"
      "v_and_b32 v179, 0xffff0000, v195\n"
      "v_mfma_f32_16x16x32_bf16 v[184:187], a[224:227], v[108:111], v[184:187]\n"
      "ds_read_b128 v[100:103], v0 offset:8192\n"
      "v_cvt_pk_bf16_f32 v213, v182, v183\n"
      "v_mfma_f32_16x16x32_bf16 v[184:187], a[228:231], v[112:115], v[184:187]\n"
      "ds_read_b128 v[108:111], v0 offset:10240\n"
      "v_and_b32 v173, 0xffff0000, v192\n"
      "v_mfma_f32_16x16x32_bf16 v[184:187], a[232:235], v[116:119], v[184:187]\n"
      "v_fmac_f32 v219, v180, v180\n"
      "v_mfma_f32_16x16x32_bf16 v[184:187], a[236:239], v[120:123], v[184:187]\n"
      "ds_read_b128 v[120:123], v0 offset:13312\n"
      "v_add_f32 v218, v218, v180\n"
      "v_mfma_f32_16x16x32_bf16 v[184:187], a[240:243], v[124:127], v[184:187]\n"
      "v_add_f32 v218, v218, v181\n"
      "v_add_f32 v218, v218, v182\n"
      "v_mfma_f32_16x16x32_bf16 v[184:187], a[244:247], v[128:131], v[184:187]\n"
      "ds_read_b128 v[128:131], v0 offset:15360\n"
      "v_lshlrev_b32 v178, 16, v195\n"
      "v_mfma_f32_16x16x32_bf16 v[184:187], a[248:251], v[132:135], v[184:187]\n"
      "v_mfma_f32_16x16x32_bf16 v[184:187], a[252:255], v[136:139], v[184:187]\n"
      "ds_read_b128 v[104:107], v0 offset:9216\n"
      "v_lshlrev_b32 v172, 16, v192\n"
      "v_mfma_f32_16x16x32_bf16 v[184:187], v[32:35], v[140:143], v[184:187]\n"
      "buffer_store_dwordx4 v[208:211], v15, s[8:11], s38 offen sc1\n"
      "v_mfma_f32_16x16x32_bf16 v[184:187], v[36:39], v[144:147], v[184:187]\n"
      "s_mov_b32 s46, s36\n"
      "ds_read_b128 v[112:115], v0 offset:11264\n"
      "v_fmac_f32 v219, v181, v181\n"
      "v_mfma_f32_16x16x32_bf16 v[184:187], v[40:43], v[148:151], v[184:187]\n"
      "ds_read2_b32 v[134:135], v2 offset0:64 offset1:96\n"
      "v_cvt_pk_bf16_f32 v212, v180, v181\n"
      "v_mfma_f32_16x16x32_bf16 v[184:187], v[44:47], v[152:155], v[184:187]\n"
      "ds_read2_b32 v[132:133], v2 offset0:0 offset1:32\n"
      "s_add_u32 s36, s36, 24576\n"
      "v_mfma_f32_16x16x32_bf16 v[184:187], v[48:51], v[156:159], v[184:187]\n"
      "ds_read_b128 v[116:119], v0 offset:12288\n"
      "v_fmac_f32 v219, v182, v182\n"
      "v_mfma_f32_16x16x32_bf16 v[184:187], v[52:55], v[160:163], v[184:187]\n"
      "ds_read_b128 v[124:127], v0 offset:14336\n"
      "v_add_f32 v218, v218, v183\n"
      "v_mfma_f32_16x16x32_bf16 v[184:187], v[56:59], v[164:167], v[184:187]\n"
      "s_waitcnt lgkmcnt(5)\n"
      "ds_read_b128 v[160:163], v0 offset:6144\n"
      "v_and_b32 v177, 0xffff0000, v194\n"
      "v_mfma_f32_16x16x32_bf16 v[184:187], v[60:63], v[168:171], v[184:187]\n"
      "s_sub_u32 s20, s20, 1\n"
      "s_cbranch_scc0 .Lv_loop\n"
      ".Lv_tail:\n"
      "s_nop 8\n"
      "v_add_f32 v218, v218, v184\n"
      "v_add_f32 v218, v218, v185\n"
      "v_fmac_f32 v219, v183, v183\n"
      "v_fmac_f32 v219, v184, v184\n"
      "v_cvt_pk_bf16_f32 v215, v186, v187\n"
      "v_add_f32 v218, v218, v186\n"
      "v_add_f32 v218, v218, v187\n"
      "v_fmac_f32 v219, v185, v185\n"
      "v_cvt_pk_bf16_f32 v214, v184, v185\n"
      "ds_write_b32 v18, v218\n"
      "v_fmac_f32 v219, v186, v186\n"
      "buffer_store_dwordx4 v[212:215], v15, s[8:11], s38 offen offset:1024 sc1\n"
      "v_fmac_f32 v219, v187, v187\n"
      "ds_write_b32 v18, v219 offset:4\n"
      "s_waitcnt lgkmcnt(0)\n"
      "s_barrier\n"
      "s_add_u32 s38, s38, 24576\n"
      "ds_read_b128 v[204:207], v19\n"
      "s_add_u32 s49, s49, 24576\n"
      "s_waitcnt lgkmcnt(0)\n"
      "v_add_f32 v204, v204, v206\n"
      "v_add_f32 v205, v205, v207\n"
      "s_nop 0\n"
      "v_add_f32_dpp v204, v204, v204 quad_perm:[1,0,3,2] row_mask:0xf bank_mask:0xf\n"
      "s_nop 1\n"
      "v_add_f32_dpp v204, v204, v204 quad_perm:[2,3,0,1] row_mask:0xf bank_mask:0xf\n"
      "v_add_f32_dpp v205, v205, v205 quad_perm:[1,0,3,2] row_mask:0xf bank_mask:0xf\n"
      "s_add_u32 s50, s50, 24576\n"
      "s_nop 0\n"
      "v_add_f32_dpp v205, v205, v205 quad_perm:[2,3,0,1] row_mask:0xf bank_mask:0xf\n"
      "s_nop 1\n"
      "v_add_f32_dpp v205, v205, v205 row_half_mirror row_mask:0xf bank_mask:0xf\n"
      "v_add_f32_dpp v204, v204, v204 row_half_mirror row_mask:0xf bank_mask:0xf\n"
      "v_mul_f32 v204, v27, v204\n"
      "v_fmaak_f32 v205, v27, v205, 0x36a7c5ac\n"
      "s_waitcnt vmcnt(3)\n"
      "buffer_store_dword v25, v12, s[8:11], s50 offen sc1\n"
      "buffer_store_dwordx2 v[204:205], v20, s[8:11], s49 offen sc1\n"
      "s_add_u32 s49, s49, 24576\n"
      "s_add_u32 s50, s50, 24576\n"
      "s_waitcnt vmcnt(0)\n"
      "buffer_store_dword v25, v12, s[8:11], s50 offen sc1\n"
      "s_add_u32 s50, s50, 24576\n"
      "s_endpgm\n"
      ".Lsp_w0_1:\n"
      "s_mov_b32 s22, s23\n"
      ".Lsl_w0_1:\n"
      "buffer_load_dword v28, v11, s[8:11], s46 offen sc1\n"
      "s_waitcnt vmcnt(0)\n"
      "v_cmp_ne_u32 vcc, s21, v28\n"
      "s_cbranch_vccz .Lbk_w0_1\n"
      "s_sleep 4\n"
      "s_sub_u32 s22, s22, 1\n"
      "s_cbranch_scc0 .Lsl_w0_1\n"
      "s_mov_b32 s23, 0\n"
      "s_branch .Lbk_w0_1\n"
      ".Lsp_w1_2:\n"
      "s_mov_b32 s22, s23\n"
      ".Lsl_w1_2:\n"
      "buffer_load_dword v28, v11, s[8:11], s46 offen sc1\n"
      "s_waitcnt vmcnt(0)\n"
      "v_cmp_ne_u32 vcc, s21, v28\n"
      "s_cbranch_vccz .Lbk_w1_2\n"
      "s_sleep 4\n"
      "s_sub_u32 s22, s22, 1\n"
      "s_cbranch_scc0 .Lsl_w1_2\n"
      "s_mov_b32 s23, 0\n"
      "s_branch .Lbk_w1_2\n"
      ".Lsp_wy0_3:\n"
      "s_lshl_b32 s22, s23, 4\n"
      ".Lsl_wy0_3:\n"
      "ds_read_b32 v22, v24\n"
      "s_waitcnt lgkmcnt(0)\n"
      "v_cmp_gt_i32 vcc, v21, v22\n"
      "s_cbranch_vccz .Lbk_wy0_3\n"
      "s_sub_u32 s22, s22, 1\n"
      "s_cbranch_scc0 .Lsl_wy0_3\n"
      "s_mov_b32 s23, 0\n"
      "s_branch .Lbk_wy0_3\n"
      ".Lsp_w0_4:\n"
      "s_mov_b32 s22, s23\n"
      "buffer_load_dword v28, v11, s[8:11], s46 offen sc1\n"
      "s_waitcnt vmcnt(0)\n"
      "v_cmp_ne_u32 vcc, s21, v28\n"
      "s_add_u32 s30, s50, 0\n"
      "buffer_store_dword v25, v12, s[8:11], s30 offen sc1\n"
      "s_add_u32 s30, s50, 24576\n"
      "buffer_store_dword v25, v12, s[8:11], s30 offen sc1\n"
      "s_cbranch_vccz .Lbk_w0_4\n"
      ".Lsl_w0_4:\n"
      "buffer_load_dword v28, v11, s[8:11], s46 offen sc1\n"
      "s_waitcnt vmcnt(0)\n"
      "v_cmp_ne_u32 vcc, s21, v28\n"
      "s_cbranch_vccz .Lbk_w0_4\n"
      "s_sleep 4\n"
      "s_sub_u32 s22, s22, 1\n"
      "s_cbranch_scc0 .Lsl_w0_4\n"
      "s_mov_b32 s23, 0\n"
      "s_branch .Lbk_w0_4\n"
      ".Lsp_wz0_5:\n"
      "s_lshl_b32 s22, s23, 4\n"
      ".Lsl_wz0_5:\n"
      "ds_read_b32 v22, v24\n"
      "s_waitcnt lgkmcnt(0)\n"
      "v_cmp_gt_i32 vcc, v21, v22\n"
      "s_cbranch_vccz .Lbk_wz0_5\n"
      "s_sub_u32 s22, s22, 1\n"
      "s_cbranch_scc0 .Lsl_wz0_5\n"
      "s_mov_b32 s23, 0\n"
      "s_branch .Lbk_wz0_5\n"
      ".Lsp_wy1_6:\n"
      "s_lshl_b32 s22, s23, 4\n"
      ".Lsl_wy1_6:\n"
      "ds_read_b32 v22, v24\n"
      "s_waitcnt lgkmcnt(0)\n"
      "v_cmp_gt_i32 vcc, v21, v22\n"
      "s_cbranch_vccz .Lbk_wy1_6\n"
      "s_sub_u32 s22, s22, 1\n"
      "s_cbranch_scc0 .Lsl_wy1_6\n"
      "s_mov_b32 s23, 0\n"
      "s_branch .Lbk_wy1_6\n"
      ".Lsp_w1_7:\n"
      "s_mov_b32 s22, s23\n"
      "buffer_load_dword v28, v11, s[8:11], s46 offen sc1\n"
      "s_waitcnt vmcnt(0)\n"
      "v_cmp_ne_u32 vcc, s21, v28\n"
      "s_add_u32 s30, s50, 24576\n"
      "buffer_store_dword v25, v12, s[8:11], s30 offen sc1\n"
      "s_cbranch_vccz .Lbk_w1_7\n"
      ".Lsl_w1_7:\n"
      "buffer_load_dword v28, v11, s[8:11], s46 offen sc1\n"
      "s_waitcnt vmcnt(0)\n"
      "v_cmp_ne_u32 vcc, s21, v28\n"
      "s_cbranch_vccz .Lbk_w1_7\n"
      "s_sleep 4\n"
      "s_sub_u32 s22, s22, 1\n"
      "s_cbranch_scc0 .Lsl_w1_7\n"
      "s_mov_b32 s23, 0\n"
      "s_branch .Lbk_w1_7\n"
      ".Lsp_wz1_8:\n"
      "s_lshl_b32 s22, s23, 4\n"
      ".Lsl_wz1_8:\n"
      "ds_read_b32 v22, v24\n"
      "s_waitcnt lgkmcnt(0)\n"
      "v_cmp_gt_i32 vcc, v21, v22\n"
      "s_cbranch_vccz .Lbk_wz1_8\n"
      "s_sub_u32 s22, s22, 1\n"
      "s_cbranch_scc0 .Lsl_wz1_8\n"
      "s_mov_b32 s23, 0\n"
      "s_branch .Lbk_wz1_8\n"
      ".Lsp_wy0_9:\n"
      "s_lshl_b32 s22, s23, 4\n"
      ".Lsl_wy0_9:\n"
      "ds_read_b32 v22, v24\n"
      "s_waitcnt lgkmcnt(0)\n"
      "v_cmp_gt_i32 vcc, v21, v22\n"
      "s_cbranch_vccz .Lbk_wy0_9\n"
      "s_sub_u32 s22, s22, 1\n"
      "s_cbranch_scc0 .Lsl_wy0_9\n"
      "s_mov_b32 s23, 0\n"
      "s_branch .Lbk_wy0_9\n"
      ".Lsp_w0_10:\n"
      "s_mov_b32 s22, s23\n"
      "buffer_load_dword v28, v11, s[8:11], s46 offen sc1\n"
      "s_waitcnt vmcnt(0)\n"
      "v_cmp_ne_u32 vcc, s21, v28\n"
      "s_add_u32 s30, s50, 0\n"
      "buffer_store_dword v25, v12, s[8:11], s30 offen sc1\n"
      "s_add_u32 s30, s50, 24576\n"
      "buffer_store_dword v25, v12, s[8:11], s30 offen sc1\n"
      "s_cbranch_vccz .Lbk_w0_10\n"
      ".Lsl_w0_10:\n"
      "buffer_load_dword v28, v11, s[8:11], s46 offen sc1\n"
      "s_waitcnt vmcnt(0)\n"
      "v_cmp_ne_u32 vcc, s21, v28\n"
      "s_cbranch_vccz .Lbk_w0_10\n"
      "s_sleep 4\n"
      "s_sub_u32 s22, s22, 1\n"
      "s_cbranch_scc0 .Lsl_w0_10\n"
      "s_mov_b32 s23, 0\n"
      "s_branch .Lbk_w0_10\n"
      ".Lsp_wz0_11:\n"
      "s_lshl_b32 s22, s23, 4\n"
      ".Lsl_wz0_11:\n"
      "ds_read_b32 v22, v24\n"
      "s_waitcnt lgkmcnt(0)\n"
      "v_cmp_gt_i32 vcc, v21, v22\n"
      "s_cbranch_vccz .Lbk_wz0_11\n"
      "s_sub_u32 s22, s22, 1\n"
      "s_cbranch_scc0 .Lsl_wz0_11\n"
      "s_mov_b32 s23, 0\n"
      "s_branch .Lbk_wz0_11\n"
      ".Lsp_wy1_12:\n"
      "s_lshl_b32 s22, s23, 4\n"
      ".Lsl_wy1_12:\n"
      "ds_read_b32 v22, v24\n"
      "s_waitcnt lgkmcnt(0)\n"
      "v_cmp_gt_i32 vcc, v21, v22\n"
      "s_cbranch_vccz .Lbk_wy1_12\n"
      "s_sub_u32 s22, s22, 1\n"
      "s_cbranch_scc0 .Lsl_wy1_12\n"
      "s_mov_b32 s23, 0\n"
      "s_branch .Lbk_wy1_12\n"
      ".Lsp_w1_13:\n"
      "s_mov_b32 s22, s23\n"
      "buffer_load_dword v28, v11, s[8:11], s46 offen sc1\n"
      "s_waitcnt vmcnt(0)\n"
      "v_cmp_ne_u32 vcc, s21, v28\n"
      "s_add_u32 s30, s50, 24576\n"
      "buffer_store_dword v25, v12, s[8:11], s30 offen sc1\n"
      "s_cbranch_vccz .Lbk_w1_13\n"
      ".Lsl_w1_13:\n"
      "buffer_load_dword v28, v11, s[8:11], s46 offen sc1\n"
      "s_waitcnt vmcnt(0)\n"
      "v_cmp_ne_u32 vcc, s21, v28\n"
      "s_cbranch_vccz .Lbk_w1_13\n"
      "s_sleep 4\n"
      "s_sub_u32 s22, s22, 1\n"
      "s_cbranch_scc0 .Lsl_w1_13\n"
      "s_mov_b32 s23, 0\n"
      "s_branch .Lbk_w1_13\n"
      ".Lsp_wz1_14:\n"
      "s_lshl_b32 s22, s23, 4\n"
      ".Lsl_wz1_14:\n"
      "ds_read_b32 v22, v24\n"
      "s_waitcnt lgkmcnt(0)\n"
      "v_cmp_gt_i32 vcc, v21, v22\n"
      "s_cbranch_vccz .Lbk_wz1_14\n"
      "s_sub_u32 s22, s22, 1\n"
      "s_cbranch_scc0 .Lsl_wz1_14\n"
      "s_mov_b32 s23, 0\n"
      "s_branch .Lbk_wz1_14\n"
      ".Lsp_wy0_15:\n"
      "s_lshl_b32 s22, s23, 4\n"
      ".Lsl_wy0_15:\n"
      "ds_read_b32 v22, v24\n"
      "s_waitcnt lgkmcnt(0)\n"
      "v_cmp_gt_i32 vcc, v21, v22\n"
      "s_cbranch_vccz .Lbk_wy0_15\n"
      "s_sub_u32 s22, s22, 1\n"
      "s_cbranch_scc0 .Lsl_wy0_15\n"
      "s_mov_b32 s23, 0\n"
      "s_branch .Lbk_wy0_15\n"
      ".Lsp_v0_16:\n"
      "s_mov_b32 s22, s23\n"
      ".Lsl_v0_16:\n"
      "buffer_load_dword v28, v11, s[8:11], s46 offen sc1\n"
      "s_waitcnt vmcnt(0)\n"
      "v_cmp_ne_u32 vcc, s21, v28\n"
      "s_cbranch_vccz .Lbk_v0_16\n"
      "s_sleep 4\n"
      "s_sub_u32 s22, s22, 1\n"
      "s_cbranch_scc0 .Lsl_v0_16\n"
      "s_mov_b32 s23, 0\n"
      "s_branch .Lbk_v0_16\n"
      ".Lsp_v1_17:\n"
      "s_mov_b32 s22, s23\n"
      ".Lsl_v1_17:\n"
      "buffer_load_dword v28, v11, s[8:11], s46 offen sc1\n"
      "s_waitcnt vmcnt(0)\n"
      "v_cmp_ne_u32 vcc, s21, v28\n"
      "s_cbranch_vccz .Lbk_v1_17\n"
      "s_sleep 4\n"
      "s_sub_u32 s22, s22, 1\n"
      "s_cbranch_scc0 .Lsl_v1_17\n"
      "s_mov_b32 s23, 0\n"
      "s_branch .Lbk_v1_17\n"
      ".Lsp_vy0_18:\n"
      "s_lshl_b32 s22, s23, 4\n"
      ".Lsl_vy0_18:\n"
      "ds_read_b32 v22, v24\n"
      "s_waitcnt lgkmcnt(0)\n"
      "v_cmp_gt_i32 vcc, v21, v22\n"
      "s_cbranch_vccz .Lbk_vy0_18\n"
      "s_sub_u32 s22, s22, 1\n"
      "s_cbranch_scc0 .Lsl_vy0_18\n"
      "s_mov_b32 s23, 0\n"
      "s_branch .Lbk_vy0_18\n"
      ".Lsp_v0_19:\n"
      "s_mov_b32 s22, s23\n"
      "buffer_load_dword v28, v11, s[8:11], s46 offen sc1\n"
      "s_waitcnt vmcnt(0)\n"
      "v_cmp_ne_u32 vcc, s21, v28\n"
      "s_add_u32 s30, s50, 24576\n"
      "buffer_store_dword v25, v12, s[8:11], s30 offen sc1\n"
      "s_cbranch_vccz .Lbk_v0_19\n"
      ".Lsl_v0_19:\n"
      "buffer_load_dword v28, v11, s[8:11], s46 offen sc1\n"
      "s_waitcnt vmcnt(0)\n"
      "v_cmp_ne_u32 vcc, s21, v28\n"
      "s_cbranch_vccz .Lbk_v0_19\n"
      "s_sleep 4\n"
      "s_sub_u32 s22, s22, 1\n"
      "s_cbranch_scc0 .Lsl_v0_19\n"
      "s_mov_b32 s23, 0\n"
      "s_branch .Lbk_v0_19\n"
      ".Lsp_vz0_20:\n"
      "s_lshl_b32 s22, s23, 4\n"
      ".Lsl_vz0_20:\n"
      "ds_read_b32 v22, v24\n"
      "s_waitcnt lgkmcnt(0)\n"
      "v_cmp_gt_i32 vcc, v21, v22\n"
      "s_cbranch_vccz .Lbk_vz0_20\n"
      "s_sub_u32 s22, s22, 1\n"
      "s_cbranch_scc0 .Lsl_vz0_20\n"
      "s_mov_b32 s23, 0\n"
      "s_branch .Lbk_vz0_20\n"
      ".Lsp_vy1_21:\n"
      "s_lshl_b32 s22, s23, 4\n"
      ".Lsl_vy1_21:\n"
      "ds_read_b32 v22, v24\n"
      "s_waitcnt lgkmcnt(0)\n"
      "v_cmp_gt_i32 vcc, v21, v22\n"
      "s_cbranch_vccz .Lbk_vy1_21\n"
      "s_sub_u32 s22, s22, 1\n"
      "s_cbranch_scc0 .Lsl_vy1_21\n"
      "s_mov_b32 s23, 0\n"
      "s_branch .Lbk_vy1_21\n"
      ".Lsp_v1_22:\n"
      "s_mov_b32 s22, s23\n"
      "buffer_load_dword v28, v11, s[8:11], s46 offen sc1\n"
      "s_waitcnt vmcnt(0)\n"
      "v_cmp_ne_u32 vcc, s21, v28\n"
      "s_add_u32 s30, s50, 24576\n"
      "buffer_store_dword v25, v12, s[8:11], s30 offen sc1\n"
      "s_cbranch_vccz .Lbk_v1_22\n"
      ".Lsl_v1_22:\n"
      "buffer_load_dword v28, v11, s[8:11], s46 offen sc1\n"
      "s_waitcnt vmcnt(0)\n"
      "v_cmp_ne_u32 vcc, s21, v28\n"
      "s_cbranch_vccz .Lbk_v1_22\n"
      "s_sleep 4\n"
      "s_sub_u32 s22, s22, 1\n"
      "s_cbranch_scc0 .Lsl_v1_22\n"
      "s_mov_b32 s23, 0\n"
      "s_branch .Lbk_v1_22\n"
      ".Lsp_vz1_23:\n"
      "s_lshl_b32 s22, s23, 4\n"
      ".Lsl_vz1_23:\n"
      "ds_read_b32 v22, v24\n"
      "s_waitcnt lgkmcnt(0)\n"
      "v_cmp_gt_i32 vcc, v21, v22\n"
      "s_cbranch_vccz .Lbk_vz1_23\n"
      "s_sub_u32 s22, s22, 1\n"
      "s_cbranch_scc0 .Lsl_vz1_23\n"
      "s_mov_b32 s23, 0\n"
      "s_branch .Lbk_vz1_23\n"
      ".Lsp_vy0_24:\n"
      "s_lshl_b32 s22, s23, 4\n"
      ".Lsl_vy0_24:\n"
      "ds_read_b32 v22, v24\n"
      "s_waitcnt lgkmcnt(0)\n"
      "v_cmp_gt_i32 vcc, v21, v22\n"
      "s_cbranch_vccz .Lbk_vy0_24\n"
      "s_sub_u32 s22, s22, 1\n"
      "s_cbranch_scc0 .Lsl_vy0_24\n"
      "s_mov_b32 s23, 0\n"
      "s_branch .Lbk_vy0_24\n"
      ".Lsp_v0_25:\n"
      "s_mov_b32 s22, s23\n"
      "buffer_load_dword v28, v11, s[8:11], s46 offen sc1\n"
      "s_waitcnt vmcnt(0)\n"
      "v_cmp_ne_u32 vcc, s21, v28\n"
      "s_add_u32 s30, s50, 24576\n"
      "buffer_store_dword v25, v12, s[8:11], s30 offen sc1\n"
      "s_cbranch_vccz .Lbk_v0_25\n"
      ".Lsl_v0_25:\n"
      "buffer_load_dword v28, v11, s[8:11], s46 offen sc1\n"
      "s_waitcnt vmcnt(0)\n"
      "v_cmp_ne_u32 vcc, s21, v28\n"
      "s_cbranch_vccz .Lbk_v0_25\n"
      "s_sleep 4\n"
      "s_sub_u32 s22, s22, 1\n"
      "s_cbranch_scc0 .Lsl_v0_25\n"
      "s_mov_b32 s23, 0\n"
      "s_branch .Lbk_v0_25\n"
      ".Lsp_vz0_26:\n"
      "s_lshl_b32 s22, s23, 4\n"
      ".Lsl_vz0_26:\n"
      "ds_read_b32 v22, v24\n"
      "s_waitcnt lgkmcnt(0)\n"
      "v_cmp_gt_i32 vcc, v21, v22\n"
      "s_cbranch_vccz .Lbk_vz0_26\n"
      "s_sub_u32 s22, s22, 1\n"
      "s_cbranch_scc0 .Lsl_vz0_26\n"
      "s_mov_b32 s23, 0\n"
      "s_branch .Lbk_vz0_26\n"
      ".Lsp_vy1_27:\n"
      "s_lshl_b32 s22, s23, 4\n"
      ".Lsl_vy1_27:\n"
      "ds_read_b32 v22, v24\n"
      "s_waitcnt lgkmcnt(0)\n"
      "v_cmp_gt_i32 vcc, v21, v22\n"
      "s_cbranch_vccz .Lbk_vy1_27\n"
      "s_sub_u32 s22, s22, 1\n"
      "s_cbranch_scc0 .Lsl_vy1_27\n"
      "s_mov_b32 s23, 0\n"
      "s_branch .Lbk_vy1_27\n"
      ".Lsp_v1_28:\n"
      "s_mov_b32 s22, s23\n"
      "buffer_load_dword v28, v11, s[8:11], s46 offen sc1\n"
      "s_waitcnt vmcnt(0)\n"
      "v_cmp_ne_u32 vcc, s21, v28\n"
      "s_add_u32 s30, s50, 24576\n"
      "buffer_store_dword v25, v12, s[8:11], s30 offen sc1\n"
      "s_cbranch_vccz .Lbk_v1_28\n"
      ".Lsl_v1_28:\n"
      "buffer_load_dword v28, v11, s[8:11], s46 offen sc1\n"
      "s_waitcnt vmcnt(0)\n"
      "v_cmp_ne_u32 vcc, s21, v28\n"
      "s_cbranch_vccz .Lbk_v1_28\n"
      "s_sleep 4\n"
      "s_sub_u32 s22, s22, 1\n"
      "s_cbranch_scc0 .Lsl_v1_28\n"
      "s_mov_b32 s23, 0\n"
      "s_branch .Lbk_v1_28\n"
      ".Lsp_vz1_29:\n"
      "s_lshl_b32 s22, s23, 4\n"
      ".Lsl_vz1_29:\n"
      "ds_read_b32 v22, v24\n"
      "s_waitcnt lgkmcnt(0)\n"
      "v_cmp_gt_i32 vcc, v21, v22\n"
      "s_cbranch_vccz .Lbk_vz1_29\n"
      "s_sub_u32 s22, s22, 1\n"
      "s_cbranch_scc0 .Lsl_vz1_29\n"
      "s_mov_b32 s23, 0\n"
      "s_branch .Lbk_vz1_29\n"
      ".Lsp_vy0_30:\n"
      "s_lshl_b32 s22, s23, 4\n"
      ".Lsl_vy0_30:\n"
      "ds_read_b32 v22, v24\n"
      "s_waitcnt lgkmcnt(0)\n"
      "v_cmp_gt_i32 vcc, v21, v22\n"
      "s_cbranch_vccz .Lbk_vy0_30\n"
      "s_sub_u32 s22, s22, 1\n"
      "s_cbranch_scc0 .Lsl_vy0_30\n"
      "s_mov_b32 s23, 0\n"
      "s_branch .Lbk_vy0_30\n"
      ".Lw_last:\n"
      :
      : "{s[8:11]}"(rsWS), "{s[12:15]}"(rsA), "{v0}"(v_l16), "{v1}"(v_l5), "{v2}"(v_ex[0]), "{v3}"(v_ex[1]), "{v4}"(v_ex[2]), "{v5}"(v_ex[3]), "{v6}"(v_ex[4]), "{v7}"(v_ex[5]), "{v8}"(v_ex[6]), "{v9}"(v_ex[7]), "{v10}"(v_l2), "{v11}"(v_fa), "{v12}"(v_ra), "{v13}"(v_rp), "{v14}"(v_rs), "{v15}"(v_xp), "{v16}"(v_xs), "{v17}"(v_ew), "{v18}"(v_sw), "{v19}"(v_sr), "{v20}"(v_ss), "{v22}"(v_al), "{v23}"(v_yw), "{v24}"(v_yr), "{v26}"(v_hi), "{v100}"(v_vo[0]), "{v101}"(v_vo[1]), "{v102}"(v_vo[2]), "{v103}"(v_vo[3]), "{v104}"(v_vo[4]), "{v105}"(v_vo[5]), "{v106}"(v_vo[6]), "{v107}"(v_vo[7]), "{v108}"(v_vo[8]), "{v109}"(v_vp[0]), "{v110}"(v_vp[1]), "{v111}"(v_vp[2]), "{v112}"(v_vp[3]), "{v113}"(v_vp[4]), "{v114}"(v_vp[5]), "{v115}"(v_vp[6]), "{v116}"(v_vp[7]), "{v117}"(v_vp[8]), "{v118}"(v_t0), "{v119}"(v_t1), "{v120}"(v_ts), "{v121}"(v_pw), "{s36}"(s_sx), "{s37}"(s_ev), "{s38}"(s_sy), "{s39}"(s_m4), "{s40}"(s_m5), "{s41}"(s_m9), "{s42}"(s_so), "{s43}"(s_s8), "{s44}"(s_wg), "{s45}"(s_rb)
      : "v255", "a255", "s99", "vcc", "scc", "memory");

  } else if (fast && slot < 28) {
    const unsigned td = (slot - 10) / 3, gr = td < 4 ? td : (e + 5 - td) & 3, part = (td < 4 ? e : td) * 3 + (slot - 10) % 3;
    COMMON(40, 149760);
    const unsigned ks = part / 3, n3 = part % 3;
    const u32x4 rsA = srd(Wd, 4608 * 576 * 2), rsB = srd(out, 2048 * 576 * 2), rsC = srd(X, 2048 * 576 * 2);
    const unsigned v_l16b = v_l16 + 4096, v_l8 = 1024 + lane * 8, f0 = 24 * ks + 6 * w;
    const unsigned v_fa = 147456 - f0 * 1024 + (f0 + (l8 < 5 ? l8 : 5)) * 128 + l7 / 4, v_ga = (l8 < 4 ? l8 : 4) * 4 * PW + 1536 + l7 / 4;
    const unsigned v_ra = up * 6 * PREC + l5 + 1536, v_rf = up * 33 * REC + l5 + 20480 + (4 * n3 + w) * 128;
    const unsigned v_pw = l8 < 5 ? (l8 + 7) * PREC + l8 * 4 * PW + 1536 + l7 : OOB, v_pu = ks && l8 + ks < 5 ? OOB : v_pw - 6 * PREC;
    const unsigned pf = 3 * (w >> 1) + 2 * (w & 1), sf = 3 * (w >> 1) + 1;
    const unsigned v_at = (8 * g + q) * 384 + 64 * pf + 16 * p, v_as = (8 * g + q) * 384 + 64 * sf + 32 * (w & 1) + 8 * p;
    auto vo = [&](unsigned i) { const unsigned c = 64 * i + lane; return (8 * w + c / 24) * 1152 + 384 * n3 + c % 24 * 16 - 1024 * i; };
    const unsigned v_vo0 = vo(0), v_vo1 = vo(1), v_vo2 = vo(2);
    const unsigned v_xp = (6 * n3 + pf) * 1024 + v_l16, v_xs = (6 * n3 + sf) * 1024 + (w & 1) * 512 + half;
    const unsigned v_op = i16 * 1152 + 384 * n3 + 64 * pf + 16 * g, v_os = i16 * 1152 + 384 * n3 + 64 * sf + 32 * (w & 1) + 8 * g;
    const unsigned v_sw = 147456 + i16 * 136 + (4 * w + g) * 8, v_sr = 147456 + (4 * w + g) * 136 + i16 * 8;
    const unsigned v_ss = 18432 + n3 * 512 + w * 128 + g * 32 + p * 8;
    const unsigned s_ph = ks ? 6 - ks : 0, s_p0 = 0u - s_ph, r0 = gr * NREC + s_p0;
    const unsigned s_sh = WS_H + r0 * HREC + f0 * 1024, s_ps = WS_PS + r0 * PREC + (n3 * 20 + w) * PW;
    const unsigned s_x1 = WS_X1 + r0 * REC, s_xr = WS_XIN + (r0 + 33) * REC, s_or = (32 * gr + 63 - s_p0) * 18432;
    const unsigned s_k0 = ROT * 6 * gr;
    const unsigned s_m3 = w * 3072, s_m6 = w * 6144, s_so = ks * 768 * 1152;
    const unsigned s_i0 = (32 * gr + 31 - ks) * 18432, s_i5 = ks < 2 ? s_i0 - 30 * 18432 : OOB;
    const unsigned s_j0 = WS_XIN + (gr * NREC + ks) * REC, s_j5 = ks < 2 ? s_j0 + 30 * REC : OOB, s_he = s_sh + (63 + s_ph) * HREC;
    asm volatile(
      "s_buffer_load_dword s21, s[8:11], 0\n"
      "s_mov_b32 s23, 65536\n"
      "s_mov_b32 s31, 0x40000000\n"
      "buffer_load_dwordx4 v[56:59], v8, s[92:95], s46 offen\n"
      "buffer_load_dwordx2 v[60:61], v9, s[92:95], s46 offen\n"
      "s_waitcnt lgkmcnt(0)\n"
      "s_mul_i32 s21, s21, 1664525\n"
      "s_add_u32 s21, s21, 1013904223\n"
      "v_mov_b32 v18, s21\n"
      "v_mov_b32 v21, v18\n"
      "v_not_b32 v224, v18\n"
      "v_not_b32 v225, v18\n"
      "v_not_b32 v226, v18\n"
      "v_not_b32 v227, v18\n"
      "v_xor_b32 v18, v18, v19\n"
      "s_waitcnt vmcnt(0)\n"
      "buffer_store_dwordx4 v[56:59], v6, s[8:11], s48 offen sc1\n"
      "buffer_store_dwordx2 v[60:61], v7, s[8:11], s48 offen sc1\n"
      "v_lshlrev_b32 v164, 16, v56\n"
      "v_and_b32 v165, 0xffff0000, v56\n"
      "v_lshlrev_b32 v166, 16, v57\n"
      "v_and_b32 v167, 0xffff0000, v57\n"
      "v_lshlrev_b32 v168, 16, v58\n"
      "v_and_b32 v169, 0xffff0000, v58\n"
      "v_lshlrev_b32 v170, 16, v59\n"
      "v_and_b32 v171, 0xffff0000, v59\n"
      "v_lshlrev_b32 v172, 16, v60\n"
      "v_and_b32 v173, 0xffff0000, v60\n"
      "v_lshlrev_b32 v174, 16, v61\n"
      "v_and_b32 v175, 0xffff0000, v61\n"
      "v_add_f32 v62, v164, v165\n"
      "v_mul_f32 v63, v164, v164\n"
      "v_fmac_f32 v63, v165, v165\n"
      "v_add_f32 v62, v62, v166\n"
      "v_fmac_f32 v63, v166, v166\n"
      "v_add_f32 v62, v62, v167\n"
      "v_fmac_f32 v63, v167, v167\n"
      "v_add_f32 v62, v62, v168\n"
      "v_fmac_f32 v63, v168, v168\n"
      "v_add_f32 v62, v62, v169\n"
      "v_fmac_f32 v63, v169, v169\n"
      "v_add_f32 v62, v62, v170\n"
      "v_fmac_f32 v63, v170, v170\n"
      "v_add_f32 v62, v62, v171\n"
      "v_fmac_f32 v63, v171, v171\n"
      "v_add_f32 v62, v62, v172\n"
      "v_fmac_f32 v63, v172, v172\n"
      "v_add_f32 v62, v62, v173\n"
      "v_fmac_f32 v63, v173, v173\n"
      "v_add_f32 v62, v62, v174\n"
      "v_fmac_f32 v63, v174, v174\n"
      "v_add_f32 v62, v62, v175\n"
      "v_fmac_f32 v63, v175, v175\n"
      "ds_write_b64 v10, v[62:63] offset:0\n"
      "s_waitcnt lgkmcnt(0)\n"
      "s_barrier\n"
      "ds_read_b64 v[152:153], v11 offset:0\n"
      "s_waitcnt lgkmcnt(0)\n"
      "v_add_f32_dpp v152, v152, v152 row_ror:8 row_mask:0xf bank_mask:0xf\n"
      "v_add_f32_dpp v153, v153, v153 row_ror:8 row_mask:0xf bank_mask:0xf\n"
      "s_nop 0\n"
      "v_add_f32_dpp v152, v152, v152 row_ror:4 row_mask:0xf bank_mask:0xf\n"
      "v_add_f32_dpp v153, v153, v153 row_ror:4 row_mask:0xf bank_mask:0xf\n"
      "s_nop 0\n"
      "v_add_f32_dpp v152, v152, v152 row_ror:2 row_mask:0xf bank_mask:0xf\n"
      "v_add_f32_dpp v153, v153, v153 row_ror:2 row_mask:0xf bank_mask:0xf\n"
      "s_nop 0\n"
      "v_add_f32_dpp v152, v152, v152 row_ror:1 row_mask:0xf bank_mask:0xf\n"
      "v_add_f32_dpp v153, v153, v153 row_ror:1 row_mask:0xf bank_mask:0xf\n"
      "v_mul_f32 v152, 0x3ae38e39, v152\n"
      "v_add_f32 v153, 0x3afba882, v153\n"
      "v_mul_f32 v153, 0x3ae38e39, v153\n"
      "buffer_store_dwordx2 v[152:153], v12, s[8:11], s48 offen sc1\n"
      "s_add_u32 s30, s46, 4294856704\n"
      "buffer_load_dwordx4 v[64:67], v8, s[92:95], s30 offen\n"
      "buffer_load_dwordx2 v[68:69], v9, s[92:95], s30 offen\n"
      "s_add_u32 s30, s46, 4294746112\n"
      "buffer_load_dwordx4 v[72:75], v8, s[92:95], s30 offen\n"
      "buffer_load_dwordx2 v[76:77], v9, s[92:95], s30 offen\n"
      "s_waitcnt vmcnt(4)\n"
      "buffer_store_dword v18, v5, s[8:11], s48 offen sc1\n"
      "s_add_u32 s30, s46, 4294635520\n"
      "buffer_load_dwordx4 v[80:83], v8, s[92:95], s30 offen\n"
      "buffer_load_dwordx2 v[84:85], v9, s[92:95], s30 offen\n"
      "s_add_u32 s30, s46, 4294524928\n"
      "buffer_load_dwordx4 v[88:91], v8, s[92:95], s30 offen\n"
      "buffer_load_dwordx2 v[92:93], v9, s[92:95], s30 offen\n"
      "buffer_load_dwordx4 v[96:99], v8, s[92:95], s47 offen\n"
      "buffer_load_dwordx2 v[100:101], v9, s[92:95], s47 offen\n"
      "buffer_store_dword v18, v15, s[8:11], 0 offen sc1\n"
      "s_cmp_eq_u32 s45, 0\n"
      "s_cselect_b32 s30, 0x40000000, s37\n"
      "buffer_store_dword v21, v3, s[8:11], s30 offen sc1\n"
      "s_waitcnt vmcnt(11)\n"
      "s_add_u32 s30, s48, 147456\n"
      "buffer_store_dwordx4 v[64:67], v6, s[8:11], s30 offen sc1\n"
      "buffer_store_dwordx2 v[68:69], v7, s[8:11], s30 offen sc1\n"
      "v_lshlrev_b32 v164, 16, v64\n"
      "v_and_b32 v165, 0xffff0000, v64\n"
      "v_lshlrev_b32 v166, 16, v65\n"
      "v_and_b32 v167, 0xffff0000, v65\n"
      "v_lshlrev_b32 v168, 16, v66\n"
      "v_and_b32 v169, 0xffff0000, v66\n"
      "v_lshlrev_b32 v170, 16, v67\n"
      "v_and_b32 v171, 0xffff0000, v67\n"
      "v_lshlrev_b32 v172, 16, v68\n"
      "v_and_b32 v173, 0xffff0000, v68\n"
      "v_lshlrev_b32 v174, 16, v69\n"
      "v_and_b32 v175, 0xffff0000, v69\n"
      "v_add_f32 v70, v164, v165\n"
      "v_mul_f32 v71, v164, v164\n"
      "v_fmac_f32 v71, v165, v165\n"
      "v_add_f32 v70, v70, v166\n"
      "v_fmac_f32 v71, v166, v166\n"
      "v_add_f32 v70, v70, v167\n"
      "v_fmac_f32 v71, v167, v167\n"
      "v_add_f32 v70, v70, v168\n"
      "v_fmac_f32 v71, v168, v168\n"
      "v_add_f32 v70, v70, v169\n"
      "v_fmac_f32 v71, v169, v169\n"
      "v_add_f32 v70, v70, v170\n"
      "v_fmac_f32 v71, v170, v170\n"
      "v_add_f32 v70, v70, v171\n"
      "v_fmac_f32 v71, v171, v171\n"
      "v_add_f32 v70, v70, v172\n"
      "v_fmac_f32 v71, v172, v172\n"
      "v_add_f32 v70, v70, v173\n"
      "v_fmac_f32 v71, v173, v173\n"
      "v_add_f32 v70, v70, v174\n"
      "v_fmac_f32 v71, v174, v174\n"
      "v_add_f32 v70, v70, v175\n"
      "v_fmac_f32 v71, v175, v175\n"
      "ds_write_b64 v10, v[70:71] offset:2176\n"
      "s_waitcnt vmcnt(11)\n"
      "s_add_u32 s30, s48, 294912\n"
      "buffer_store_dwordx4 v[72:75], v6, s[8:11], s30 offen sc1\n"
      "buffer_store_dwordx2 v[76:77], v7, s[8:11], s30 offen sc1\n"
      "v_lshlrev_b32 v164, 16, v72\n"
      "v_and_b32 v165, 0xffff0000, v72\n"
      "v_lshlrev_b32 v166, 16, v73\n"
      "v_and_b32 v167, 0xffff0000, v73\n"
      "v_lshlrev_b32 v168, 16, v74\n"
      "v_and_b32 v169, 0xffff0000, v74\n"
      "v_lshlrev_b32 v170, 16, v75\n"
      "v_and_b32 v171, 0xffff0000, v75\n"
      "v_lshlrev_b32 v172, 16, v76\n"
      "v_and_b32 v173, 0xffff0000, v76\n"
      "v_lshlrev_b32 v174, 16, v77\n"
      "v_and_b32 v175, 0xffff0000, v77\n"
      "v_add_f32 v78, v164, v165\n"
      "v_mul_f32 v79, v164, v164\n"
      "v_fmac_f32 v79, v165, v165\n"
      "v_add_f32 v78, v78, v166\n"
      "v_fmac_f32 v79, v166, v166\n"
      "v_add_f32 v78, v78, v167\n"
      "v_fmac_f32 v79, v167, v167\n"
      "v_add_f32 v78, v78, v168\n"
      "v_fmac_f32 v79, v168, v168\n"
      "v_add_f32 v78, v78, v169\n"
      "v_fmac_f32 v79, v169, v169\n"
      "v_add_f32 v78, v78, v170\n"
      "v_fmac_f32 v79, v170, v170\n"
      "v_add_f32 v78, v78, v171\n"
      "v_fmac_f32 v79, v171, v171\n"
      "v_add_f32 v78, v78, v172\n"
      "v_fmac_f32 v79, v172, v172\n"
      "v_add_f32 v78, v78, v173\n"
      "v_fmac_f32 v79, v173, v173\n"
      "v_add_f32 v78, v78, v174\n"
      "v_fmac_f32 v79, v174, v174\n"
      "v_add_f32 v78, v78, v175\n"
      "v_fmac_f32 v79, v175, v175\n"
      "ds_write_b64 v10, v[78:79] offset:4352\n"
      "s_waitcnt vmcnt(10)\n"
      "s_add_u32 s30, s48, 442368\n"
      "buffer_store_dwordx4 v[80:83], v6, s[8:11], s30 offen sc1\n"
      "buffer_store_dwordx2 v[84:85], v7, s[8:11], s30 offen sc1\n"
      "v_lshlrev_b32 v164, 16, v80\n"
      "v_and_b32 v165, 0xffff0000, v80\n"
      "v_lshlrev_b32 v166, 16, v81\n"
      "v_and_b32 v167, 0xffff0000, v81\n"
      "v_lshlrev_b32 v168, 16, v82\n"
      "v_and_b32 v169, 0xffff0000, v82\n"
      "v_lshlrev_b32 v170, 16, v83\n"
      "v_and_b32 v171, 0xffff0000, v83\n"
      "v_lshlrev_b32 v172, 16, v84\n"
      "v_and_b32 v173, 0xffff0000, v84\n"
      "v_lshlrev_b32 v174, 16, v85\n"
      "v_and_b32 v175, 0xffff0000, v85\n"
      "v_add_f32 v86, v164, v165\n"
      "v_mul_f32 v87, v164, v164\n"
      "v_fmac_f32 v87, v165, v165\n"
      "v_add_f32 v86, v86, v166\n"
      "v_fmac_f32 v87, v166, v166\n"
      "v_add_f32 v86, v86, v167\n"
      "v_fmac_f32 v87, v167, v167\n"
      "v_add_f32 v86, v86, v168\n"
      "v_fmac_f32 v87, v168, v168\n"
      "v_add_f32 v86, v86, v169\n"
      "v_fmac_f32 v87, v169, v169\n"
      "v_add_f32 v86, v86, v170\n"
      "v_fmac_f32 v87, v170, v170\n"
      "v_add_f32 v86, v86, v171\n"
      "v_fmac_f32 v87, v171, v171\n"
      "v_add_f32 v86, v86, v172\n"
      "v_fmac_f32 v87, v172, v172\n"
      "v_add_f32 v86, v86, v173\n"
      "v_fmac_f32 v87, v173, v173\n"
      "v_add_f32 v86, v86, v174\n"
      "v_fmac_f32 v87, v174, v174\n"
      "v_add_f32 v86, v86, v175\n"
      "v_fmac_f32 v87, v175, v175\n"
      "ds_write_b64 v10, v[86:87] offset:6528\n"
      "s_waitcnt vmcnt(10)\n"
      "s_add_u32 s30, s48, 589824\n"
      "buffer_store_dwordx4 v[88:91], v6, s[8:11], s30 offen sc1\n"
      "buffer_store_dwordx2 v[92:93], v7, s[8:11], s30 offen sc1\n"
      "v_lshlrev_b32 v164, 16, v88\n"
      "v_and_b32 v165, 0xffff0000, v88\n"
      "v_lshlrev_b32 v166, 16, v89\n"
      "v_and_b32 v167, 0xffff0000, v89\n"
      "v_lshlrev_b32 v168, 16, v90\n"
      "v_and_b32 v169, 0xffff0000, v90\n"
      "v_lshlrev_b32 v170, 16, v91\n"
      "v_and_b32 v171, 0xffff0000, v91\n"
      "v_lshlrev_b32 v172, 16, v92\n"
      "v_and_b32 v173, 0xffff0000, v92\n"
      "v_lshlrev_b32 v174, 16, v93\n"
      "v_and_b32 v175, 0xffff0000, v93\n"
      "v_add_f32 v94, v164, v165\n"
      "v_mul_f32 v95, v164, v164\n"
      "v_fmac_f32 v95, v165, v165\n"
      "v_add_f32 v94, v94, v166\n"
      "v_fmac_f32 v95, v166, v166\n"
      "v_add_f32 v94, v94, v167\n"
      "v_fmac_f32 v95, v167, v167\n"
      "v_add_f32 v94, v94, v168\n"
      "v_fmac_f32 v95, v168, v168\n"
      "v_add_f32 v94, v94, v169\n"
      "v_fmac_f32 v95, v169, v169\n"
      "v_add_f32 v94, v94, v170\n"
      "v_fmac_f32 v95, v170, v170\n"
      "v_add_f32 v94, v94, v171\n"
      "v_fmac_f32 v95, v171, v171\n"
      "v_add_f32 v94, v94, v172\n"
      "v_fmac_f32 v95, v172, v172\n"
      "v_add_f32 v94, v94, v173\n"
      "v_fmac_f32 v95, v173, v173\n"
      "v_add_f32 v94, v94, v174\n"
      "v_fmac_f32 v95, v174, v174\n"
      "v_add_f32 v94, v94, v175\n"
      "v_fmac_f32 v95, v175, v175\n"
      "ds_write_b64 v10, v[94:95] offset:8704\n"
      "s_waitcnt vmcnt(10)\n"
      "buffer_store_dwordx4 v[96:99], v6, s[8:11], s49 offen sc1\n"
      "buffer_store_dwordx2 v[100:101], v7, s[8:11], s49 offen sc1\n"
      "v_lshlrev_b32 v164, 16, v96\n"
      "v_and_b32 v165, 0xffff0000, v96\n"
      "v_lshlrev_b32 v166, 16, v97\n"
      "v_and_b32 v167, 0xffff0000, v97\n"
      "v_lshlrev_b32 v168, 16, v98\n"
      "v_and_b32 v169, 0xffff0000, v98\n"
      "v_lshlrev_b32 v170, 16, v99\n"
      "v_and_b32 v171, 0xffff0000, v99\n"
      "v_lshlrev_b32 v172, 16, v100\n"
      "v_and_b32 v173, 0xffff0000, v100\n"
      "v_lshlrev_b32 v174, 16, v101\n"
      "v_and_b32 v175, 0xffff0000, v101\n"
      "v_add_f32 v102, v164, v165\n"
      "v_mul_f32 v103, v164, v164\n"
      "v_fmac_f32 v103, v165, v165\n"
      "v_add_f32 v102, v102, v166\n"
      "v_fmac_f32 v103, v166, v166\n"
      "v_add_f32 v102, v102, v167\n"
      "v_fmac_f32 v103, v167, v167\n"
      "v_add_f32 v102, v102, v168\n"
      "v_fmac_f32 v103, v168, v168\n"
      "v_add_f32 v102, v102, v169\n"
      "v_fmac_f32 v103, v169, v169\n"
      "v_add_f32 v102, v102, v170\n"
      "v_fmac_f32 v103, v170, v170\n"
      "v_add_f32 v102, v102, v171\n"
      "v_fmac_f32 v103, v171, v171\n"
      "v_add_f32 v102, v102, v172\n"
      "v_fmac_f32 v103, v172, v172\n"
      "v_add_f32 v102, v102, v173\n"
      "v_fmac_f32 v103, v173, v173\n"
      "v_add_f32 v102, v102, v174\n"
      "v_fmac_f32 v103, v174, v174\n"
      "v_add_f32 v102, v102, v175\n"
      "v_fmac_f32 v103, v175, v175\n"
      "ds_write_b64 v10, v[102:103] offset:10880\n"
      "s_waitcnt lgkmcnt(0)\n"
      "s_barrier\n"
      "ds_read_b64 v[154:155], v11 offset:2176\n"
      "ds_read_b64 v[156:157], v11 offset:4352\n"
      "ds_read_b64 v[158:159], v11 offset:6528\n"
      "ds_read_b64 v[160:161], v11 offset:8704\n"
      "ds_read_b64 v[162:163], v11 offset:10880\n"
      "s_waitcnt lgkmcnt(0)\n"
      "v_add_f32_dpp v154, v154, v154 row_ror:8 row_mask:0xf bank_mask:0xf\n"
      "v_add_f32_dpp v155, v155, v155 row_ror:8 row_mask:0xf bank_mask:0xf\n"
      "v_add_f32_dpp v156, v156, v156 row_ror:8 row_mask:0xf bank_mask:0xf\n"
      "v_add_f32_dpp v157, v157, v157 row_ror:8 row_mask:0xf bank_mask:0xf\n"
      "v_add_f32_dpp v158, v158, v158 row_ror:8 row_mask:0xf bank_mask:0xf\n"
      "v_add_f32_dpp v159, v159, v159 row_ror:8 row_mask:0xf bank_mask:0xf\n"
      "v_add_f32_dpp v160, v160, v160 row_ror:8 row_mask:0xf bank_mask:0xf\n"
      "v_add_f32_dpp v161, v161, v161 row_ror:8 row_mask:0xf bank_mask:0xf\n"
      "v_add_f32_dpp v162, v162, v162 row_ror:8 row_mask:0xf bank_mask:0xf\n"
      "v_add_f32_dpp v163, v163, v163 row_ror:8 row_mask:0xf bank_mask:0xf\n"
      "v_add_f32_dpp v154, v154, v154 row_ror:4 row_mask:0xf bank_mask:0xf\n"
      "v_add_f32_dpp v155, v155, v155 row_ror:4 row_mask:0xf bank_mask:0xf\n"
      "v_add_f32_dpp v156, v156, v156 row_ror:4 row_mask:0xf bank_mask:0xf\n"
      "v_add_f32_dpp v157, v157, v157 row_ror:4 row_mask:0xf bank_mask:0xf\n"
      "v_add_f32_dpp v158, v158, v158 row_ror:4 row_mask:0xf bank_mask:0xf\n"
      "v_add_f32_dpp v159, v159, v159 row_ror:4 row_mask:0xf bank_mask:0xf\n"
      "v_add_f32_dpp v160, v160, v160 row_ror:4 row_mask:0xf bank_mask:0xf\n"
      "v_add_f32_dpp v161, v161, v161 row_ror:4 row_mask:0xf bank_mask:0xf\n"
      "v_add_f32_dpp v162, v162, v162 row_ror:4 row_mask:0xf bank_mask:0xf\n"
      "v_add_f32_dpp v163, v163, v163 row_ror:4 row_mask:0xf bank_mask:0xf\n"
      "v_add_f32_dpp v154, v154, v154 row_ror:2 row_mask:0xf bank_mask:0xf\n"
      "v_add_f32_dpp v155, v155, v155 row_ror:2 row_mask:0xf bank_mask:0xf\n"
      "v_add_f32_dpp v156, v156, v156 row_ror:2 row_mask:0xf bank_mask:0xf\n"
      "v_add_f32_dpp v157, v157, v157 row_ror:2 row_mask:0xf bank_mask:0xf\n"
      "v_add_f32_dpp v158, v158, v158 row_ror:2 row_mask:0xf bank_mask:0xf\n"
      "v_add_f32_dpp v159, v159, v159 row_ror:2 row_mask:0xf bank_mask:0xf\n"
      "v_add_f32_dpp v160, v160, v160 row_ror:2 row_mask:0xf bank_mask:0xf\n"
      "v_add_f32_dpp v161, v161, v161 row_ror:2 row_mask:0xf bank_mask:0xf\n"
      "v_add_f32_dpp v162, v162, v162 row_ror:2 row_mask:0xf bank_mask:0xf\n"
      "v_add_f32_dpp v163, v163, v163 row_ror:2 row_mask:0xf bank_mask:0xf\n"
      "v_add_f32_dpp v154, v154, v154 row_ror:1 row_mask:0xf bank_mask:0xf\n"
      "v_add_f32_dpp v155, v155, v155 row_ror:1 row_mask:0xf bank_mask:0xf\n"
      "v_add_f32_dpp v156, v156, v156 row_ror:1 row_mask:0xf bank_mask:0xf\n"
      "v_add_f32_dpp v157, v157, v157 row_ror:1 row_mask:0xf bank_mask:0xf\n"
      "v_add_f32_dpp v158, v158, v158 row_ror:1 row_mask:0xf bank_mask:0xf\n"
      "v_add_f32_dpp v159, v159, v159 row_ror:1 row_mask:0xf bank_mask:0xf\n"
      "v_add_f32_dpp v160, v160, v160 row_ror:1 row_mask:0xf bank_mask:0xf\n"
      "v_add_f32_dpp v161, v161, v161 row_ror:1 row_mask:0xf bank_mask:0xf\n"
      "v_add_f32_dpp v162, v162, v162 row_ror:1 row_mask:0xf bank_mask:0xf\n"
      "v_add_f32_dpp v163, v163, v163 row_ror:1 row_mask:0xf bank_mask:0xf\n"
      "v_mul_f32 v154, 0x3ae38e39, v154\n"
      "v_add_f32 v155, 0x3afba882, v155\n"
      "v_mul_f32 v155, 0x3ae38e39, v155\n"
      "v_mul_f32 v156, 0x3ae38e39, v156\n"
      "v_add_f32 v157, 0x3afba882, v157\n"
      "v_mul_f32 v157, 0x3ae38e39, v157\n"
      "v_mul_f32 v158, 0x3ae38e39, v158\n"
      "v_add_f32 v159, 0x3afba882, v159\n"
      "v_mul_f32 v159, 0x3ae38e39, v159\n"
      "v_mul_f32 v160, 0x3ae38e39, v160\n"
      "v_add_f32 v161, 0x3afba882, v161\n"
      "v_mul_f32 v161, 0x3ae38e39, v161\n"
      "v_mul_f32 v162, 0x3ae38e39, v162\n"
      "v_add_f32 v163, 0x3afba882, v163\n"
      "v_mul_f32 v163, 0x3ae38e39, v163\n"
      "s_add_u32 s30, s48, 147456\n"
      "buffer_store_dwordx2 v[154:155], v12, s[8:11], s30 offen sc1\n"
      "s_add_u32 s30, s48, 294912\n"
      "buffer_store_dwordx2 v[156:157], v12, s[8:11], s30 offen sc1\n"
      "s_add_u32 s30, s48, 442368\n"
      "buffer_store_dwordx2 v[158:159], v12, s[8:11], s30 offen sc1\n"
      "s_add_u32 s30, s48, 589824\n"
      "buffer_store_dwordx2 v[160:161], v12, s[8:11], s30 offen sc1\n"
      "buffer_store_dwordx2 v[162:163], v12, s[8:11], s49 offen sc1\n"
      "s_waitcnt vmcnt(0)\n"
      "s_add_u32 s30, s48, 147456\n"
      "buffer_store_dword v18, v5, s[8:11], s30 offen sc1\n"
      "s_add_u32 s30, s48, 294912\n"
      "buffer_store_dword v18, v5, s[8:11], s30 offen sc1\n"
      "s_add_u32 s30, s48, 442368\n"
      "buffer_store_dword v18, v5, s[8:11], s30 offen sc1\n"
      "s_add_u32 s30, s48, 589824\n"
      "buffer_store_dword v18, v5, s[8:11], s30 offen sc1\n"
      "buffer_store_dword v18, v5, s[8:11], s49 offen sc1\n"
      "v_and_b32 v20, 0x40000000, v19\n"
      "v_or_b32 v5, v20, v5\n"
      "s_add_u32 s30, s37, 0\n"
      "buffer_store_dwordx4 v[224:227], v183, s[8:11], s30 offen sc1\n"
      "s_add_u32 s30, s37, 0\n"
      "buffer_store_dwordx4 v[224:227], v181, s[8:11], s30 offen sc1\n"
      "s_sleep 32\n"
      "s_waitcnt vmcnt(0) lgkmcnt(0)\n"
      "s_mov_b32 s61, s60\n"
      "s_mov_b32 s62, 6\n"
      ".Ld_r0:\n"
      "s_mul_i32 s63, s61, 36864\n"
      "s_add_u32 s63, s63, s44\n"
      "s_mul_i32 s30, s61, 8192\n"
      "s_lshr_b32 s30, s30, 16\n"
      "s_mul_i32 s30, s30, 8\n"
      "s_sub_u32 s30, s61, s30\n"
      "s_mul_i32 s30, s30, 12288\n"
      "s_add_u32 s30, s30, 0\n"
      "s_add_u32 m0, s42, s30\n"
      "s_add_u32 s61, s61, 1\n"
      "buffer_load_dwordx4 v178, s[12:15], s63 offen offset:0 lds\n"
      "buffer_load_dwordx4 v179, s[12:15], s63 offen offset:1024 lds\n"
      "buffer_load_dwordx4 v180, s[12:15], s63 offen offset:2048 lds\n"
      "s_cmp_eq_u32 s61, 24\n"
      "s_cselect_b32 s61, 0, s61\n"
      "s_sub_u32 s62, s62, 1\n"
      "s_cbranch_scc0 .Ld_r0\n"
      "v_add_u32 v20, 49152, v176\n"
      "v_add_u32 v21, 49152, v177\n"
      "v_add_u32 v22, 98304, v176\n"
      "v_add_u32 v23, 98304, v177\n"
      "s_mov_b32 s62, 23\n"
      "s_cmp_eq_u32 s60, 6\n"
      "s_cbranch_scc1 .Ld_rs6\n"
      "s_cmp_eq_u32 s60, 12\n"
      "s_cbranch_scc1 .Ld_rs12\n"
      "s_cmp_eq_u32 s60, 18\n"
      "s_cbranch_scc1 .Ld_rs18\n"
      ".Ld_rs0:\n"
      "s_add_u32 s63, s44, 258048\n"
      "s_cmp_ge_u32 s62, 7\n"
      "s_cselect_b32 s63, s63, s31\n"
      "s_add_u32 m0, s42, 86016\n"
      "s_waitcnt vmcnt(18) lgkmcnt(0)\n"
      "s_barrier\n"
      "ds_read_b64_tr_b16 a[0:1], v176 offset:0\n"
      "ds_read_b64_tr_b16 a[2:3], v176 offset:1536\n"
      "ds_read_b64_tr_b16 a[96:97], v176 offset:8\n"
      "ds_read_b64_tr_b16 a[98:99], v176 offset:1544\n"
      "ds_read_b64_tr_b16 a[192:193], v177 offset:0\n"
      "ds_read_b64_tr_b16 a[194:195], v177 offset:1536\n"
      "buffer_load_dwordx4 v178, s[12:15], s63 offen offset:0 lds\n"
      "buffer_load_dwordx4 v179, s[12:15], s63 offen offset:1024 lds\n"
      "buffer_load_dwordx4 v180, s[12:15], s63 offen offset:2048 lds\n"
      "s_sub_u32 s62, s62, 1\n"
      ".Ld_rs1:\n"
      "s_add_u32 s63, s44, 294912\n"
      "s_cmp_ge_u32 s62, 7\n"
      "s_cselect_b32 s63, s63, s31\n"
      "s_add_u32 m0, s42, 0\n"
      "s_waitcnt vmcnt(18) lgkmcnt(0)\n"
      "s_barrier\n"
      "ds_read_b64_tr_b16 a[4:5], v176 offset:12288\n"
      "ds_read_b64_tr_b16 a[6:7], v176 offset:13824\n"
      "ds_read_b64_tr_b16 a[100:101], v176 offset:12296\n"
      "ds_read_b64_tr_b16 a[102:103], v176 offset:13832\n"
      "ds_read_b64_tr_b16 a[196:197], v177 offset:12288\n"
      "ds_read_b64_tr_b16 a[198:199], v177 offset:13824\n"
      "buffer_load_dwordx4 v178, s[12:15], s63 offen offset:0 lds\n"
      "buffer_load_dwordx4 v179, s[12:15], s63 offen offset:1024 lds\n"
      "buffer_load_dwordx4 v180, s[12:15], s63 offen offset:2048 lds\n"
      "s_sub_u32 s62, s62, 1\n"
      ".Ld_rs2:\n"
      "s_add_u32 s63, s44, 331776\n"
      "s_cmp_ge_u32 s62, 7\n"
      "s_cselect_b32 s63, s63, s31\n"
      "s_add_u32 m0, s42, 12288\n"
      "s_waitcnt vmcnt(18) lgkmcnt(0)\n"
      "s_barrier\n"
      "ds_read_b64_tr_b16 a[8:9], v176 offset:24576\n"
      "ds_read_b64_tr_b16 a[10:11], v176 offset:26112\n"
      "ds_read_b64_tr_b16 a[104:105], v176 offset:24584\n"
      "ds_read_b64_tr_b16 a[106:107], v176 offset:26120\n"
      "ds_read_b64_tr_b16 a[200:201], v177 offset:24576\n"
      "ds_read_b64_tr_b16 a[202:203], v177 offset:26112\n"
      "buffer_load_dwordx4 v178, s[12:15], s63 offen offset:0 lds\n"
      "buffer_load_dwordx4 v179, s[12:15], s63 offen offset:1024 lds\n"
      "buffer_load_dwordx4 v180, s[12:15], s63 offen offset:2048 lds\n"
      "s_sub_u32 s62, s62, 1\n"
      ".Ld_rs3:\n"
      "s_add_u32 s63, s44, 368640\n"
      "s_cmp_ge_u32 s62, 7\n"
      "s_cselect_b32 s63, s63, s31\n"
      "s_add_u32 m0, s42, 24576\n"
      "s_waitcnt vmcnt(18) lgkmcnt(0)\n"
      "s_barrier\n"
      "ds_read_b64_tr_b16 a[12:13], v176 offset:36864\n"
      "ds_read_b64_tr_b16 a[14:15], v176 offset:38400\n"
      "ds_read_b64_tr_b16 a[108:109], v176 offset:36872\n"
      "ds_read_b64_tr_b16 a[110:111], v176 offset:38408\n"
      "ds_read_b64_tr_b16 a[204:205], v177 offset:36864\n"
      "ds_read_b64_tr_b16 a[206:207], v177 offset:38400\n"
      "buffer_load_dwordx4 v178, s[12:15], s63 offen offset:0 lds\n"
      "buffer_load_dwordx4 v179, s[12:15], s63 offen offset:1024 lds\n"
      "buffer_load_dwordx4 v180, s[12:15], s63 offen offset:2048 lds\n"
      "s_sub_u32 s62, s62, 1\n"
      ".Ld_rs4:\n"
      "s_add_u32 s63, s44, 405504\n"
      "s_cmp_ge_u32 s62, 7\n"
      "s_cselect_b32 s63, s63, s31\n"
      "s_add_u32 m0, s42, 36864\n"
      "s_waitcnt vmcnt(18) lgkmcnt(0)\n"
      "s_barrier\n"
      "ds_read_b64_tr_b16 a[16:17], v20 offset:0\n"
      "ds_read_b64_tr_b16 a[18:19], v20 offset:1536\n"
      "ds_read_b64_tr_b16 a[112:113], v20 offset:8\n"
      "ds_read_b64_tr_b16 a[114:115], v20 offset:1544\n"
      "ds_read_b64_tr_b16 a[208:209], v21 offset:0\n"
      "ds_read_b64_tr_b16 a[210:211], v21 offset:1536\n"
      "buffer_load_dwordx4 v178, s[12:15], s63 offen offset:0 lds\n"
      "buffer_load_dwordx4 v179, s[12:15], s63 offen offset:1024 lds\n"
      "buffer_load_dwordx4 v180, s[12:15], s63 offen offset:2048 lds\n"
      "s_sub_u32 s62, s62, 1\n"
      ".Ld_rs5:\n"
      "s_add_u32 s63, s44, 442368\n"
      "s_cmp_ge_u32 s62, 7\n"
      "s_cselect_b32 s63, s63, s31\n"
      "s_add_u32 m0, s42, 49152\n"
      "s_waitcnt vmcnt(18) lgkmcnt(0)\n"
      "s_barrier\n"
      "ds_read_b64_tr_b16 a[20:21], v20 offset:12288\n"
      "ds_read_b64_tr_b16 a[22:23], v20 offset:13824\n"
      "ds_read_b64_tr_b16 a[116:117], v20 offset:12296\n"
      "ds_read_b64_tr_b16 a[118:119], v20 offset:13832\n"
      "ds_read_b64_tr_b16 a[212:213], v21 offset:12288\n"
      "ds_read_b64_tr_b16 a[214:215], v21 offset:13824\n"
      "buffer_load_dwordx4 v178, s[12:15], s63 offen offset:0 lds\n"
      "buffer_load_dwordx4 v179, s[12:15], s63 offen offset:1024 lds\n"
      "buffer_load_dwordx4 v180, s[12:15], s63 offen offset:2048 lds\n"
      "s_sub_u32 s62, s62, 1\n"
      "s_cbranch_scc1 .Ld_re\n"
      ".Ld_rs6:\n"
      "s_add_u32 s63, s44, 479232\n"
      "s_cmp_ge_u32 s62, 7\n"
      "s_cselect_b32 s63, s63, s31\n"
      "s_add_u32 m0, s42, 61440\n"
      "s_waitcnt vmcnt(18) lgkmcnt(0)\n"
      "s_barrier\n"
      "ds_read_b64_tr_b16 a[24:25], v20 offset:24576\n"
      "ds_read_b64_tr_b16 a[26:27], v20 offset:26112\n"
      "ds_read_b64_tr_b16 a[120:121], v20 offset:24584\n"
      "ds_read_b64_tr_b16 a[122:123], v20 offset:26120\n"
      "ds_read_b64_tr_b16 a[216:217], v21 offset:24576\n"
      "ds_read_b64_tr_b16 a[218:219], v21 offset:26112\n"
      "buffer_load_dwordx4 v178, s[12:15], s63 offen offset:0 lds\n"
      "buffer_load_dwordx4 v179, s[12:15], s63 offen offset:1024 lds\n"
      "buffer_load_dwordx4 v180, s[12:15], s63 offen offset:2048 lds\n"
      "s_sub_u32 s62, s62, 1\n"
      ".Ld_rs7:\n"
      "s_add_u32 s63, s44, 516096\n"
      "s_cmp_ge_u32 s62, 7\n"
      "s_cselect_b32 s63, s63, s31\n"
      "s_add_u32 m0, s42, 73728\n"
      "s_waitcnt vmcnt(18) lgkmcnt(0)\n"
      "s_barrier\n"
      "ds_read_b64_tr_b16 a[28:29], v20 offset:36864\n"
      "ds_read_b64_tr_b16 a[30:31], v20 offset:38400\n"
      "ds_read_b64_tr_b16 a[124:125], v20 offset:36872\n"
      "ds_read_b64_tr_b16 a[126:127], v20 offset:38408\n"
      "ds_read_b64_tr_b16 a[220:221], v21 offset:36864\n"
      "ds_read_b64_tr_b16 a[222:223], v21 offset:38400\n"
      "buffer_load_dwordx4 v178, s[12:15], s63 offen offset:0 lds\n"
      "buffer_load_dwordx4 v179, s[12:15], s63 offen offset:1024 lds\n"
      "buffer_load_dwordx4 v180, s[12:15], s63 offen offset:2048 lds\n"
      "s_sub_u32 s62, s62, 1\n"
      ".Ld_rs8:\n"
      "s_add_u32 s63, s44, 552960\n"
      "s_cmp_ge_u32 s62, 7\n"
      "s_cselect_b32 s63, s63, s31\n"
      "s_add_u32 m0, s42, 86016\n"
      "s_waitcnt vmcnt(18) lgkmcnt(0)\n"
      "s_barrier\n"
      "ds_read_b64_tr_b16 a[32:33], v176 offset:0\n"
      "ds_read_b64_tr_b16 a[34:35], v176 offset:1536\n"
      "ds_read_b64_tr_b16 a[128:129], v176 offset:8\n"
      "ds_read_b64_tr_b16 a[130:131], v176 offset:1544\n"
      "ds_read_b64_tr_b16 a[224:225], v177 offset:0\n"
      "ds_read_b64_tr_b16 a[226:227], v177 offset:1536\n"
      "buffer_load_dwordx4 v178, s[12:15], s63 offen offset:0 lds\n"
      "buffer_load_dwordx4 v179, s[12:15], s63 offen offset:1024 lds\n"
      "buffer_load_dwordx4 v180, s[12:15], s63 offen offset:2048 lds\n"
      "s_sub_u32 s62, s62, 1\n"
      ".Ld_rs9:\n"
      "s_add_u32 s63, s44, 589824\n"
      "s_cmp_ge_u32 s62, 7\n"
      "s_cselect_b32 s63, s63, s31\n"
      "s_add_u32 m0, s42, 0\n"
      "s_waitcnt vmcnt(18) lgkmcnt(0)\n"
      "s_barrier\n"
      "ds_read_b64_tr_b16 a[36:37], v176 offset:12288\n"
      "ds_read_b64_tr_b16 a[38:39], v176 offset:13824\n"
      "ds_read_b64_tr_b16 a[132:133], v176 offset:12296\n"
      "ds_read_b64_tr_b16 a[134:135], v176 offset:13832\n"
      "ds_read_b64_tr_b16 a[228:229], v177 offset:12288\n"
      "ds_read_b64_tr_b16 a[230:231], v177 offset:13824\n"
      "buffer_load_dwordx4 v178, s[12:15], s63 offen offset:0 lds\n"
      "buffer_load_dwordx4 v179, s[12:15], s63 offen offset:1024 lds\n"
      "buffer_load_dwordx4 v180, s[12:15], s63 offen offset:2048 lds\n"
      "s_sub_u32 s62, s62, 1\n"
      ".Ld_rs10:\n"
      "s_add_u32 s63, s44, 626688\n"
      "s_cmp_ge_u32 s62, 7\n"
      "s_cselect_b32 s63, s63, s31\n"
      "s_add_u32 m0, s42, 12288\n"
      "s_waitcnt vmcnt(18) lgkmcnt(0)\n"
      "s_barrier\n"
      "ds_read_b64_tr_b16 a[40:41], v176 offset:24576\n"
      "ds_read_b64_tr_b16 a[42:43], v176 offset:26112\n"
      "ds_read_b64_tr_b16 a[136:137], v176 offset:24584\n"
      "ds_read_b64_tr_b16 a[138:139], v176 offset:26120\n"
      "ds_read_b64_tr_b16 a[232:233], v177 offset:24576\n"
      "ds_read_b64_tr_b16 a[234:235], v177 offset:26112\n"
      "buffer_load_dwordx4 v178, s[12:15], s63 offen offset:0 lds\n"
      "buffer_load_dwordx4 v179, s[12:15], s63 offen offset:1024 lds\n"
      "buffer_load_dwordx4 v180, s[12:15], s63 offen offset:2048 lds\n"
      "s_sub_u32 s62, s62, 1\n"
      ".Ld_rs11:\n"
      "s_add_u32 s63, s44, 663552\n"
      "s_cmp_ge_u32 s62, 7\n"
      "s_cselect_b32 s63, s63, s31\n"
      "s_add_u32 m0, s42, 24576\n"
      "s_waitcnt vmcnt(18) lgkmcnt(0)\n"
      "s_barrier\n"
      "ds_read_b64_tr_b16 a[44:45], v176 offset:36864\n"
      "ds_read_b64_tr_b16 a[46:47], v176 offset:38400\n"
      "ds_read_b64_tr_b16 a[140:141], v176 offset:36872\n"
      "ds_read_b64_tr_b16 a[142:143], v176 offset:38408\n"
      "ds_read_b64_tr_b16 a[236:237], v177 offset:36864\n"
      "ds_read_b64_tr_b16 a[238:239], v177 offset:38400\n"
      "buffer_load_dwordx4 v178, s[12:15], s63 offen offset:0 lds\n"
      "buffer_load_dwordx4 v179, s[12:15], s63 offen offset:1024 lds\n"
      "buffer_load_dwordx4 v180, s[12:15], s63 offen offset:2048 lds\n"
      "s_sub_u32 s62, s62, 1\n"
      "s_cbranch_scc1 .Ld_re\n"
      ".Ld_rs12:\n"
      "s_add_u32 s63, s44, 700416\n"
      "s_cmp_ge_u32 s62, 7\n"
      "s_cselect_b32 s63, s63, s31\n"
      "s_add_u32 m0, s42, 36864\n"
      "s_waitcnt vmcnt(18) lgkmcnt(0)\n"
      "s_barrier\n"
      "ds_read_b64_tr_b16 a[48:49], v20 offset:0\n"
      "ds_read_b64_tr_b16 a[50:51], v20 offset:1536\n"
      "ds_read_b64_tr_b16 a[144:145], v20 offset:8\n"
      "ds_read_b64_tr_b16 a[146:147], v20 offset:1544\n"
      "ds_read_b64_tr_b16 a[240:241], v21 offset:0\n"
      "ds_read_b64_tr_b16 a[242:243], v21 offset:1536\n"
      "buffer_load_dwordx4 v178, s[12:15], s63 offen offset:0 lds\n"
      "buffer_load_dwordx4 v179, s[12:15], s63 offen offset:1024 lds\n"
      "buffer_load_dwordx4 v180, s[12:15], s63 offen offset:2048 lds\n"
      "s_sub_u32 s62, s62, 1\n"
      ".Ld_rs13:\n"
      "s_add_u32 s63, s44, 737280\n"
      "s_cmp_ge_u32 s62, 7\n"
      "s_cselect_b32 s63, s63, s31\n"
      "s_add_u32 m0, s42, 49152\n"
      "s_waitcnt vmcnt(18) lgkmcnt(0)\n"
      "s_barrier\n"
      "ds_read_b64_tr_b16 a[52:53], v20 offset:12288\n"
      "ds_read_b64_tr_b16 a[54:55], v20 offset:13824\n"
      "ds_read_b64_tr_b16 a[148:149], v20 offset:12296\n"
      "ds_read_b64_tr_b16 a[150:151], v20 offset:13832\n"
      "ds_read_b64_tr_b16 a[244:245], v21 offset:12288\n"
      "ds_read_b64_tr_b16 a[246:247], v21 offset:13824\n"
      "buffer_load_dwordx4 v178, s[12:15], s63 offen offset:0 lds\n"
      "buffer_load_dwordx4 v179, s[12:15], s63 offen offset:1024 lds\n"
      "buffer_load_dwordx4 v180, s[12:15], s63 offen offset:2048 lds\n"
      "s_sub_u32 s62, s62, 1\n"
      ".Ld_rs14:\n"
      "s_add_u32 s63, s44, 774144\n"
      "s_cmp_ge_u32 s62, 7\n"
      "s_cselect_b32 s63, s63, s31\n"
      "s_add_u32 m0, s42, 61440\n"
      "s_waitcnt vmcnt(18) lgkmcnt(0)\n"
      "s_barrier\n"
      "ds_read_b64_tr_b16 a[56:57], v20 offset:24576\n"
      "ds_read_b64_tr_b16 a[58:59], v20 offset:26112\n"
      "ds_read_b64_tr_b16 a[152:153], v20 offset:24584\n"
      "ds_read_b64_tr_b16 a[154:155], v20 offset:26120\n"
      "ds_read_b64_tr_b16 a[248:249], v21 offset:24576\n"
      "ds_read_b64_tr_b16 a[250:251], v21 offset:26112\n"
      "buffer_load_dwordx4 v178, s[12:15], s63 offen offset:0 lds\n"
      "buffer_load_dwordx4 v179, s[12:15], s63 offen offset:1024 lds\n"
      "buffer_load_dwordx4 v180, s[12:15], s63 offen offset:2048 lds\n"
      "s_sub_u32 s62, s62, 1\n"
      ".Ld_rs15:\n"
      "s_add_u32 s63, s44, 811008\n"
      "s_cmp_ge_u32 s62, 7\n"
      "s_cselect_b32 s63, s63, s31\n"
      "s_add_u32 m0, s42, 73728\n"
      "s_waitcnt vmcnt(18) lgkmcnt(0)\n"
      "s_barrier\n"
      "ds_read_b64_tr_b16 a[60:61], v20 offset:36864\n"
      "ds_read_b64_tr_b16 a[62:63], v20 offset:38400\n"
      "ds_read_b64_tr_b16 a[156:157], v20 offset:36872\n"
      "ds_read_b64_tr_b16 a[158:159], v20 offset:38408\n"
      "ds_read_b64_tr_b16 a[252:253], v21 offset:36864\n"
      "ds_read_b64_tr_b16 a[254:255], v21 offset:38400\n"
      "buffer_load_dwordx4 v178, s[12:15], s63 offen offset:0 lds\n"
      "buffer_load_dwordx4 v179, s[12:15], s63 offen offset:1024 lds\n"
      "buffer_load_dwordx4 v180, s[12:15], s63 offen offset:2048 lds\n"
      "s_sub_u32 s62, s62, 1\n"
      ".Ld_rs16:\n"
      "s_add_u32 s63, s44, 847872\n"
      "s_cmp_ge_u32 s62, 7\n"
      "s_cselect_b32 s63, s63, s31\n"
      "s_add_u32 m0, s42, 86016\n"
      "s_waitcnt vmcnt(18) lgkmcnt(0)\n"
      "s_barrier\n"
      "ds_read_b64_tr_b16 a[64:65], v176 offset:0\n"
      "ds_read_b64_tr_b16 a[66:67], v176 offset:1536\n"
      "ds_read_b64_tr_b16 a[160:161], v176 offset:8\n"
      "ds_read_b64_tr_b16 a[162:163], v176 offset:1544\n"
      "ds_read_b64_tr_b16 v[24:25], v177 offset:0\n"
      "ds_read_b64_tr_b16 v[26:27], v177 offset:1536\n"
      "buffer_load_dwordx4 v178, s[12:15], s63 offen offset:0 lds\n"
      "buffer_load_dwordx4 v179, s[12:15], s63 offen offset:1024 lds\n"
      "buffer_load_dwordx4 v180, s[12:15], s63 offen offset:2048 lds\n"
      "s_sub_u32 s62, s62, 1\n"
      ".Ld_rs17:\n"
      "s_add_u32 s63, s44, 0\n"
      "s_cmp_ge_u32 s62, 7\n"
      "s_cselect_b32 s63, s63, s31\n"
      "s_add_u32 m0, s42, 0\n"
      "s_waitcnt vmcnt(18) lgkmcnt(0)\n"
      "s_barrier\n"
      "ds_read_b64_tr_b16 a[68:69], v176 offset:12288\n"
      "ds_read_b64_tr_b16 a[70:71], v176 offset:13824\n"
      "ds_read_b64_tr_b16 a[164:165], v176 offset:12296\n"
      "ds_read_b64_tr_b16 a[166:167], v176 offset:13832\n"
      "ds_read_b64_tr_b16 v[28:29], v177 offset:12288\n"
      "ds_read_b64_tr_b16 v[30:31], v177 offset:13824\n"
      "buffer_load_dwordx4 v178, s[12:15], s63 offen offset:0 lds\n"
      "buffer_load_dwordx4 v179, s[12:15], s63 offen offset:1024 lds\n"
      "buffer_load_dwordx4 v180, s[12:15], s63 offen offset:2048 lds\n"
      "s_sub_u32 s62, s62, 1\n"
      "s_cbranch_scc1 .Ld_re\n"
      ".Ld_rs18:\n"
      "s_add_u32 s63, s44, 36864\n"
      "s_cmp_ge_u32 s62, 7\n"
      "s_cselect_b32 s63, s63, s31\n"
      "s_add_u32 m0, s42, 12288\n"
      "s_waitcnt vmcnt(18) lgkmcnt(0)\n"
      "s_barrier\n"
      "ds_read_b64_tr_b16 a[72:73], v176 offset:24576\n"
      "ds_read_b64_tr_b16 a[74:75], v176 offset:26112\n"
      "ds_read_b64_tr_b16 a[168:169], v176 offset:24584\n"
      "ds_read_b64_tr_b16 a[170:171], v176 offset:26120\n"
      "ds_read_b64_tr_b16 v[32:33], v177 offset:24576\n"
      "ds_read_b64_tr_b16 v[34:35], v177 offset:26112\n"
      "buffer_load_dwordx4 v178, s[12:15], s63 offen offset:0 lds\n"
      "buffer_load_dwordx4 v179, s[12:15], s63 offen offset:1024 lds\n"
      "buffer_load_dwordx4 v180, s[12:15], s63 offen offset:2048 lds\n"
      "s_sub_u32 s62, s62, 1\n"
      ".Ld_rs19:\n"
      "s_add_u32 s63, s44, 73728\n"
      "s_cmp_ge_u32 s62, 7\n"
      "s_cselect_b32 s63, s63, s31\n"
      "s_add_u32 m0, s42, 24576\n"
      "s_waitcnt vmcnt(18) lgkmcnt(0)\n"
      "s_barrier\n"
      "ds_read_b64_tr_b16 a[76:77], v176 offset:36864\n"
      "ds_read_b64_tr_b16 a[78:79], v176 offset:38400\n"
      "ds_read_b64_tr_b16 a[172:173], v176 offset:36872\n"
      "ds_read_b64_tr_b16 a[174:175], v176 offset:38408\n"
      "ds_read_b64_tr_b16 v[36:37], v177 offset:36864\n"
      "ds_read_b64_tr_b16 v[38:39], v177 offset:38400\n"
      "buffer_load_dwordx4 v178, s[12:15], s63 offen offset:0 lds\n"
      "buffer_load_dwordx4 v179, s[12:15], s63 offen offset:1024 lds\n"
      "buffer_load_dwordx4 v180, s[12:15], s63 offen offset:2048 lds\n"
      "s_sub_u32 s62, s62, 1\n"
      ".Ld_rs20:\n"
      "s_add_u32 s63, s44, 110592\n"
      "s_cmp_ge_u32 s62, 7\n"
      "s_cselect_b32 s63, s63, s31\n"
      "s_add_u32 m0, s42, 36864\n"
      "s_waitcnt vmcnt(18) lgkmcnt(0)\n"
      "s_barrier\n"
      "ds_read_b64_tr_b16 a[80:81], v20 offset:0\n"
      "ds_read_b64_tr_b16 a[82:83], v20 offset:1536\n"
      "ds_read_b64_tr_b16 a[176:177], v20 offset:8\n"
      "ds_read_b64_tr_b16 a[178:179], v20 offset:1544\n"
      "ds_read_b64_tr_b16 v[40:41], v21 offset:0\n"
      "ds_read_b64_tr_b16 v[42:43], v21 offset:1536\n"
      "buffer_load_dwordx4 v178, s[12:15], s63 offen offset:0 lds\n"
      "buffer_load_dwordx4 v179, s[12:15], s63 offen offset:1024 lds\n"
      "buffer_load_dwordx4 v180, s[12:15], s63 offen offset:2048 lds\n"
      "s_sub_u32 s62, s62, 1\n"
      ".Ld_rs21:\n"
      "s_add_u32 s63, s44, 147456\n"
      "s_cmp_ge_u32 s62, 7\n"
      "s_cselect_b32 s63, s63, s31\n"
      "s_add_u32 m0, s42, 49152\n"
      "s_waitcnt vmcnt(18) lgkmcnt(0)\n"
      "s_barrier\n"
      "ds_read_b64_tr_b16 a[84:85], v20 offset:12288\n"
      "ds_read_b64_tr_b16 a[86:87], v20 offset:13824\n"
      "ds_read_b64_tr_b16 a[180:181], v20 offset:12296\n"
      "ds_read_b64_tr_b16 a[182:183], v20 offset:13832\n"
      "ds_read_b64_tr_b16 v[44:45], v21 offset:12288\n"
      "ds_read_b64_tr_b16 v[46:47], v21 offset:13824\n"
      "buffer_load_dwordx4 v178, s[12:15], s63 offen offset:0 lds\n"
      "buffer_load_dwordx4 v179, s[12:15], s63 offen offset:1024 lds\n"
      "buffer_load_dwordx4 v180, s[12:15], s63 offen offset:2048 lds\n"
      "s_sub_u32 s62, s62, 1\n"
      ".Ld_rs22:\n"
      "s_add_u32 s63, s44, 184320\n"
      "s_cmp_ge_u32 s62, 7\n"
      "s_cselect_b32 s63, s63, s31\n"
      "s_add_u32 m0, s42, 61440\n"
      "s_waitcnt vmcnt(18) lgkmcnt(0)\n"
      "s_barrier\n"
      "ds_read_b64_tr_b16 a[88:89], v20 offset:24576\n"
      "ds_read_b64_tr_b16 a[90:91], v20 offset:26112\n"
      "ds_read_b64_tr_b16 a[184:185], v20 offset:24584\n"
      "ds_read_b64_tr_b16 a[186:187], v20 offset:26120\n"
      "ds_read_b64_tr_b16 v[48:49], v21 offset:24576\n"
      "ds_read_b64_tr_b16 v[50:51], v21 offset:26112\n"
      "buffer_load_dwordx4 v178, s[12:15], s63 offen offset:0 lds\n"
      "buffer_load_dwordx4 v179, s[12:15], s63 offen offset:1024 lds\n"
      "buffer_load_dwordx4 v180, s[12:15], s63 offen offset:2048 lds\n"
      "s_sub_u32 s62, s62, 1\n"
      ".Ld_rs23:\n"
      "s_add_u32 s63, s44, 221184\n"
      "s_cmp_ge_u32 s62, 7\n"
      "s_cselect_b32 s63, s63, s31\n"
      "s_add_u32 m0, s42, 73728\n"
      "s_waitcnt vmcnt(18) lgkmcnt(0)\n"
      "s_barrier\n"
      "ds_read_b64_tr_b16 a[92:93], v20 offset:36864\n"
      "ds_read_b64_tr_b16 a[94:95], v20 offset:38400\n"
      "ds_read_b64_tr_b16 a[188:189], v20 offset:36872\n"
      "ds_read_b64_tr_b16 a[190:191], v20 offset:38408\n"
      "ds_read_b64_tr_b16 v[52:53], v21 offset:36864\n"
      "ds_read_b64_tr_b16 v[54:55], v21 offset:38400\n"
      "buffer_load_dwordx4 v178, s[12:15], s63 offen offset:0 lds\n"
      "buffer_load_dwordx4 v179, s[12:15], s63 offen offset:1024 lds\n"
      "buffer_load_dwordx4 v180, s[12:15], s63 offen offset:2048 lds\n"
      "s_sub_u32 s62, s62, 1\n"
      "s_cbranch_scc0 .Ld_rs0\n"
      ".Ld_re:\n"
      "s_waitcnt vmcnt(0) lgkmcnt(0)\n"
      "v_add_u32 v22, 49152, v0\n"
      "v_add_u32 v23, 98304, v0\n"
      "s_lshr_b32 s58, s42, 10\n"
      "s_mul_i32 s58, s58, 1366\n"
      "s_and_b32 s58, s58, 0x7000\n"
      "s_getpc_b64 vcc\n"
      ".Ld_ct:\n"
      "s_add_u32 vcc_lo, vcc_lo, .Ld_p0-.Ld_ct\n"
      "s_addc_u32 vcc_hi, vcc_hi, 0\n"
      "s_lshl_b32 s30, s58, 1\n"
      "v_lshl_add_u32 v56, v0, 3, s30\n"
      "v_min_u32 v56, .Ld_last-.Ld_p0-4, v56\n"
      "global_load_dword v56, v56, vcc\n"
      "s_waitcnt vmcnt(0) lgkmcnt(0)\n"
      "s_mov_b64 exec, 0\n"
      "s_lshr_b32 s22, s58, 12\n"
      ".Ld_pl:\n"
      "s_cmp_eq_u32 s22, 0\n"
      "s_cbranch_scc1 .Ld_p0\n"
      "s_cmp_eq_u32 s22, 1\n"
      "s_cbranch_scc1 .Ld_p1\n"
      "s_branch .Ld_pe\n"
      ".Ld_pr:\n"
      "s_add_u32 s22, s22, 4\n"
      "s_branch .Ld_pl\n"
      ".Ld_pe:\n"
      "s_mov_b64 exec, -1\n"
      "s_barrier\n"
      "v_mov_b32 v14, 0\n"
      "ds_write_b32 v16, v14\n"
      "s_waitcnt lgkmcnt(0)\n"
      "s_barrier\n"
      "s_mov_b32 s6, s50\n"
      "s_add_u32 s36, s36, 4294801408\n"
      "s_add_u32 s53, s37, 0\n"
      "s_add_u32 s52, s37, 4294787584\n"
      "s_add_u32 s37, s37, 4294894080\n"
      "s_mov_b32 s56, 0x40000000\n"
      "s_mov_b32 s57, 0x40000000\n"
      "s_mov_b32 s59, 63\n"
      "s_cmp_eq_u32 s45, 1\n"
      "s_cbranch_scc1 .Ld_in1\n"
      "s_cmp_eq_u32 s45, 2\n"
      "s_cbranch_scc1 .Ld_in2\n"
      "s_cmp_eq_u32 s45, 3\n"
      "s_cbranch_scc1 .Ld_in3\n"
      "s_cmp_eq_u32 s45, 4\n"
      "s_cbranch_scc1 .Ld_in4\n"
      "s_cmp_eq_u32 s45, 5\n"
      "s_cbranch_scc1 .Ld_in5\n"
      "s_add_u32 s36, s36, 165888\n"
      "s_min_u32 s36, s36, s6\n"
      "buffer_load_dword v20, v2, s[8:11], s36 offen sc1\n"
      "s_add_u32 m0, s43, 0\n"
      "s_mov_b32 s51, s36\n"
      "s_add_u32 s36, s36, 165888\n"
      "s_min_u32 s36, s36, s6\n"
      "s_waitcnt vmcnt(0)\n"
      "v_cmp_ne_u32 vcc, s21, v20\n"
      "s_cbranch_vccnz .Lsp_d0_1\n"
      ".Lbk_d0_1:\n"
      "buffer_load_dwordx4 v0, s[8:11], s51 offen offset:0 sc1 lds\n"
      "buffer_load_dwordx4 v0, s[8:11], s51 offen offset:1024 sc1 lds\n"
      "buffer_load_dword v20, v2, s[8:11], s36 offen sc1\n"
      "buffer_load_dwordx4 v0, s[8:11], s51 offen offset:2048 sc1 lds\n"
      "buffer_load_dwordx4 v0, s[8:11], s51 offen offset:3072 sc1 lds\n"
      "s_add_u32 m0, s43, 4096\n"
      "s_nop 0\n"
      "buffer_load_dwordx4 v1, s[8:11], s51 offen offset:1024 sc1 lds\n"
      "buffer_load_dwordx4 v1, s[8:11], s51 offen offset:0 sc1 lds\n"
      "s_mov_b32 s51, s36\n"
      "s_add_u32 m0, s43, 24576\n"
      "v_add_u32 v14, 1, v14\n"
      "s_add_u32 s36, s36, 165888\n"
      "buffer_load_dwordx4 v[216:219], v6, s[8:11], s38 offen sc1\n"
      "s_waitcnt vmcnt(5)\n"
      "v_cmp_ne_u32 vcc, s21, v20\n"
      "buffer_load_dwordx2 v[220:221], v7, s[8:11], s38 offen sc1\n"
      "s_cbranch_vccnz .Lsp_d1_2\n"
      ".Lbk_d1_2:\n"
      "buffer_load_dwordx4 v0, s[8:11], s51 offen offset:0 sc1 lds\n"
      "buffer_load_dwordx4 v0, s[8:11], s51 offen offset:1024 sc1 lds\n"
      "s_waitcnt vmcnt(4)\n"
      "ds_write_b32 v16, v14\n"
      "buffer_load_dwordx4 v0, s[8:11], s51 offen offset:2048 sc1 lds\n"
      "s_min_u32 s36, s36, s6\n"
      "buffer_load_dword v20, v2, s[8:11], s36 offen sc1\n"
      "buffer_load_dwordx4 v0, s[8:11], s51 offen offset:3072 sc1 lds\n"
      "ds_read_b32 v15, v17\n"
      "s_add_u32 m0, s43, 28672\n"
      "s_nop 0\n"
      "buffer_load_dwordx4 v1, s[8:11], s51 offen offset:0 sc1 lds\n"
      "s_waitcnt lgkmcnt(0)\n"
      "v_cmp_gt_i32 vcc, v14, v15\n"
      "buffer_load_dwordx4 v1, s[8:11], s51 offen offset:1024 sc1 lds\n"
      "s_cbranch_vccnz .Lsp_dy0_3\n"
      ".Lbk_dy0_3:\n"
      "ds_read_b128 v[56:59], v0 offset:0\n"
      "ds_read_b128 v[60:63], v0 offset:1024\n"
      "ds_read_b128 v[84:87], v0 offset:7168\n"
      "ds_read_b128 v[72:75], v0 offset:4096\n"
      "ds_read_b128 v[88:91], v0 offset:8192\n"
      "ds_read_b128 v[68:71], v0 offset:3072\n"
      "ds_read_b128 v[104:107], v0 offset:12288\n"
      "ds_read_b128 v[96:99], v0 offset:10240\n"
      "ds_read_b128 v[80:83], v0 offset:6144\n"
      "ds_read_b128 v[64:67], v0 offset:2048\n"
      "ds_read_b128 v[92:95], v0 offset:9216\n"
      "ds_read_b128 v[76:79], v0 offset:5120\n"
      "ds_read_b128 v[132:135], v0 offset:19456\n"
      "ds_read_b128 v[100:103], v0 offset:11264\n"
      "s_waitcnt vmcnt(0) lgkmcnt(0)\n"
      "s_nop 4\n"
      "s_branch .Ld_t0\n"
      ".p2align 12\n"
      ".Ld_p0:\n"
      "s_branch .Ld_pr\n"
      ".p2align 5\n"
      ".Ld_t0:\n"
      "v_mfma_f32_16x16x32_bf16 v[152:155], a[0:3], v[56:59], 0\n"
      "ds_read_b128 v[108:111], v0 offset:13312\n"
      "ds_read_b128 v[120:123], v0 offset:16384\n"
      "v_mfma_f32_16x16x32_bf16 v[152:155], a[4:7], v[60:63], v[152:155]\n"
      "s_waitcnt lgkmcnt(6)\n"
      "ds_read_b128 v[116:119], v0 offset:15360\n"
      "v_add_f32 v236, v236, v168\n"
      "v_mfma_f32_16x16x32_bf16 v[152:155], a[8:11], v[64:67], v[152:155]\n"
      "v_cvt_pk_bf16_f32 v228, v160, v161\n"
      "v_cvt_pk_bf16_f32 v229, v162, v163\n"
      "v_mfma_f32_16x16x32_bf16 v[152:155], a[12:15], v[68:71], v[152:155]\n"
      "buffer_store_dwordx2 v[228:229], v13, s[8:11], s37 offen sc1\n"
      "v_mfma_f32_16x16x32_bf16 v[152:155], a[16:19], v[72:75], v[152:155]\n"
      "s_waitcnt lgkmcnt(4)\n"
      "v_cvt_pk_bf16_f32 v178, v168, v169\n"
      "v_add_f32 v175, v175, v213\n"
      "v_mfma_f32_16x16x32_bf16 v[152:155], a[20:23], v[76:79], v[152:155]\n"
      "ds_read_b128 v[112:115], v0 offset:14336\n"
      "v_add_f32 v171, v171, v211\n"
      "v_mfma_f32_16x16x32_bf16 v[152:155], a[24:27], v[80:83], v[152:155]\n"
      "ds_read_b128 v[124:127], v0 offset:17408\n"
      "v_add_f32 v236, v236, v169\n"
      "v_mfma_f32_16x16x32_bf16 v[152:155], a[28:31], v[84:87], v[152:155]\n"
      "v_add_f32 v173, v173, v204\n"
      "v_cvt_pk_bf16_f32 v179, v170, v171\n"
      "v_mfma_f32_16x16x32_bf16 v[152:155], a[32:35], v[88:91], v[152:155]\n"
      "ds_read_b128 v[128:131], v0 offset:18432\n"
      "v_add_f32 v173, v173, v212\n"
      "v_mfma_f32_16x16x32_bf16 v[152:155], a[36:39], v[92:95], v[152:155]\n"
      "buffer_store_dwordx4 v[176:179], v6, s[8:11], s56 offen sc1\n"
      "v_mfma_f32_16x16x32_bf16 v[152:155], a[40:43], v[96:99], v[152:155]\n"
      "s_waitcnt lgkmcnt(3)\n"
      "ds_read_b128 v[148:151], v0 offset:23552\n"
      "v_cvt_pk_bf16_f32 v180, v172, v173\n"
      "v_mfma_f32_16x16x32_bf16 v[152:155], a[44:47], v[100:103], v[152:155]\n"
      "ds_read_b128 v[144:147], v0 offset:22528\n"
      "v_fmac_f32 v237, v168, v168\n"
      "v_mfma_f32_16x16x32_bf16 v[152:155], a[48:51], v[104:107], v[152:155]\n"
      "s_add_u32 m0, s43, 49152\n"
      "ds_read_b128 v[136:139], v0 offset:20480\n"
      "v_fmac_f32 v237, v169, v169\n"
      "v_mfma_f32_16x16x32_bf16 v[152:155], a[52:55], v[108:111], v[152:155]\n"
      "s_waitcnt lgkmcnt(4)\n"
      "s_mov_b32 s51, s36\n"
      "ds_read_b128 v[140:143], v0 offset:21504\n"
      "v_mfma_f32_16x16x32_bf16 v[152:155], a[56:59], v[112:115], v[152:155]\n"
      "v_cvt_pk_bf16_f32 v181, v174, v175\n"
      "v_fmac_f32 v237, v170, v170\n"
      "v_mfma_f32_16x16x32_bf16 v[152:155], a[60:63], v[116:119], v[152:155]\n"
      "buffer_store_dwordx2 v[180:181], v7, s[8:11], s56 offen sc1\n"
      "v_mfma_f32_16x16x32_bf16 v[152:155], a[64:67], v[120:123], v[152:155]\n"
      "v_add_u32 v14, 1, v14\n"
      "v_mfma_f32_16x16x32_bf16 v[152:155], a[68:71], v[124:127], v[152:155]\n"
      "s_waitcnt lgkmcnt(3)\n"
      "v_add_f32 v236, v236, v170\n"
      "v_fmac_f32 v237, v171, v171\n"
      "v_mfma_f32_16x16x32_bf16 v[152:155], a[72:75], v[128:131], v[152:155]\n"
      "v_fmac_f32 v237, v172, v172\n"
      "v_fmac_f32 v237, v173, v173\n"
      "v_mfma_f32_16x16x32_bf16 v[152:155], a[76:79], v[132:135], v[152:155]\n"
      "s_waitcnt lgkmcnt(1)\n"
      "buffer_store_dwordx2 v[180:181], v9, s[16:19], s57 offen\n"
      "v_mfma_f32_16x16x32_bf16 v[152:155], a[80:83], v[136:139], v[152:155]\n"
      "s_waitcnt lgkmcnt(0)\n"
      "v_add_f32 v236, v236, v171\n"
      "v_fmac_f32 v237, v174, v174\n"
      "v_mfma_f32_16x16x32_bf16 v[152:155], a[84:87], v[140:143], v[152:155]\n"
      "v_add_f32 v236, v236, v172\n"
      "v_fmac_f32 v237, v175, v175\n"
      "v_mfma_f32_16x16x32_bf16 v[152:155], a[88:91], v[144:147], v[152:155]\n"
      "v_add_f32 v236, v236, v173\n"
      "v_add_f32 v236, v236, v174\n"
      "v_mfma_f32_16x16x32_bf16 v[152:155], a[92:95], v[148:151], v[152:155]\n"
      "buffer_store_dwordx4 v[176:179], v8, s[16:19], s57 offen\n"
      "v_mfma_f32_16x16x32_bf16 v[156:159], a[96:99], v[56:59], 0\n"
      "s_waitcnt vmcnt(15)\n"
      "v_add_f32 v236, v236, v175\n"
      "v_lshlrev_b32 v166, 16, v217\n"
      "v_mfma_f32_16x16x32_bf16 v[156:159], a[100:103], v[60:63], v[156:159]\n"
      "s_waitcnt vmcnt(10)\n"
      "v_cmp_ne_u32 vcc, s21, v20\n"
      "v_mfma_f32_16x16x32_bf16 v[156:159], a[104:107], v[64:67], v[156:159]\n"
      "ds_write_b64 v10, v[236:237]\n"
      "v_mfma_f32_16x16x32_bf16 v[156:159], a[108:111], v[68:71], v[156:159]\n"
      "s_add_u32 s36, s36, 165888\n"
      "v_mfma_f32_16x16x32_bf16 v[156:159], a[112:115], v[72:75], v[156:159]\n"
      "s_min_u32 s36, s36, s6\n"
      "s_cbranch_vccnz .Lsp_d2_4\n"
      ".Lbk_d2_4:\n"
      "v_mfma_f32_16x16x32_bf16 v[156:159], a[116:119], v[76:79], v[156:159]\n"
      "s_waitcnt vmcnt(7)\n"
      "ds_write_b32 v16, v14\n"
      "v_mfma_f32_16x16x32_bf16 v[156:159], a[120:123], v[80:83], v[156:159]\n"
      "buffer_load_dword v20, v2, s[8:11], s36 offen sc1\n"
      "v_mfma_f32_16x16x32_bf16 v[156:159], a[124:127], v[84:87], v[156:159]\n"
      "v_and_b32 v167, 0xffff0000, v217\n"
      "v_lshlrev_b32 v164, 16, v216\n"
      "v_mfma_f32_16x16x32_bf16 v[156:159], a[128:131], v[88:91], v[156:159]\n"
      "v_mfma_f32_16x16x32_bf16 v[156:159], a[132:135], v[92:95], v[156:159]\n"
      "buffer_load_dwordx4 v0, s[8:11], s51 offen offset:3072 sc1 lds\n"
      "v_mfma_f32_16x16x32_bf16 v[156:159], a[136:139], v[96:99], v[156:159]\n"
      "s_add_u32 s52, s52, 106496\n"
      "v_mfma_f32_16x16x32_bf16 v[156:159], a[140:143], v[100:103], v[156:159]\n"
      "v_mfma_f32_16x16x32_bf16 v[156:159], a[144:147], v[104:107], v[156:159]\n"
      "s_add_u32 s37, s37, 173056\n"
      "v_and_b32 v165, 0xffff0000, v216\n"
      "v_mfma_f32_16x16x32_bf16 v[156:159], a[148:151], v[108:111], v[156:159]\n"
      "buffer_load_dwordx4 v0, s[8:11], s51 offen offset:1024 sc1 lds\n"
      "v_mfma_f32_16x16x32_bf16 v[156:159], a[152:155], v[112:115], v[156:159]\n"
      "ds_read_b32 v15, v17\n"
      "v_mfma_f32_16x16x32_bf16 v[156:159], a[156:159], v[116:119], v[156:159]\n"
      "v_mfma_f32_16x16x32_bf16 v[156:159], a[160:163], v[120:123], v[156:159]\n"
      "v_lshlrev_b32 v170, 16, v219\n"
      "v_mfma_f32_16x16x32_bf16 v[156:159], a[164:167], v[124:127], v[156:159]\n"
      "buffer_load_dwordx4 v0, s[8:11], s51 offen offset:2048 sc1 lds\n"
      "v_mfma_f32_16x16x32_bf16 v[156:159], a[168:171], v[128:131], v[156:159]\n"
      "v_mfma_f32_16x16x32_bf16 v[156:159], a[172:175], v[132:135], v[156:159]\n"
      "s_waitcnt lgkmcnt(0)\n"
      "v_cmp_gt_i32 vcc, v14, v15\n"
      "v_mfma_f32_16x16x32_bf16 v[156:159], a[176:179], v[136:139], v[156:159]\n"
      "v_add_f32 v165, v165, v153\n"
      "v_mfma_f32_16x16x32_bf16 v[156:159], a[180:183], v[140:143], v[156:159]\n"
      "buffer_load_dwordx4 v0, s[8:11], s51 offen offset:0 sc1 lds\n"
      "v_mfma_f32_16x16x32_bf16 v[156:159], a[184:187], v[144:147], v[156:159]\n"
      "v_add_f32 v164, v164, v152\n"
      "s_add_u32 m0, s43, 53248\n"
      "v_mfma_f32_16x16x32_bf16 v[156:159], a[188:191], v[148:151], v[156:159]\n"
      "s_cbranch_vccnz .Lsp_dy1_5\n"
      ".Lbk_dy1_5:\n"
      "ds_read_b64 v[238:239], v11\n"
      "v_mfma_f32_16x16x32_bf16 v[160:163], a[192:195], v[56:59], 0\n"
      "v_lshlrev_b32 v168, 16, v218\n"
      "v_mfma_f32_16x16x32_bf16 v[160:163], a[196:199], v[60:63], v[160:163]\n"
      "buffer_load_dwordx4 v1, s[8:11], s51 offen offset:0 sc1 lds\n"
      "v_mfma_f32_16x16x32_bf16 v[160:163], a[200:203], v[64:67], v[160:163]\n"
      "v_mfma_f32_16x16x32_bf16 v[160:163], a[204:207], v[68:71], v[160:163]\n"
      "ds_read_b128 v[56:59], v0 offset:24576\n"
      "v_mfma_f32_16x16x32_bf16 v[160:163], a[208:211], v[72:75], v[160:163]\n"
      "ds_read_b128 v[64:67], v0 offset:26624\n"
      "v_mfma_f32_16x16x32_bf16 v[160:163], a[212:215], v[76:79], v[160:163]\n"
      "buffer_load_dwordx4 v1, s[8:11], s51 offen offset:1024 sc1 lds\n"
      "v_mfma_f32_16x16x32_bf16 v[160:163], a[216:219], v[80:83], v[160:163]\n"
      "s_waitcnt lgkmcnt(2)\n"
      "ds_read_b128 v[68:71], v0 offset:27648\n"
      "v_add_f32_dpp v238, v238, v238 row_ror:8 row_mask:0xf bank_mask:0xf\n"
      "v_mfma_f32_16x16x32_bf16 v[160:163], a[220:223], v[84:87], v[160:163]\n"
      "ds_read_b128 v[80:83], v0 offset:30720\n"
      "v_add_f32_dpp v238, v238, v238 row_ror:4 row_mask:0xf bank_mask:0xf\n"
      "v_mfma_f32_16x16x32_bf16 v[160:163], a[224:227], v[88:91], v[160:163]\n"
      "ds_read_b128 v[60:63], v0 offset:25600\n"
      "v_add_f32_dpp v238, v238, v238 row_ror:2 row_mask:0xf bank_mask:0xf\n"
      "v_mfma_f32_16x16x32_bf16 v[160:163], a[228:231], v[92:95], v[160:163]\n"
      "ds_read_b128 v[92:95], v0 offset:33792\n"
      "v_add_f32_dpp v239, v239, v239 row_ror:8 row_mask:0xf bank_mask:0xf\n"
      "v_mfma_f32_16x16x32_bf16 v[160:163], a[232:235], v[96:99], v[160:163]\n"
      "ds_read_b128 v[84:87], v0 offset:31744\n"
      "v_add_f32_dpp v239, v239, v239 row_ror:4 row_mask:0xf bank_mask:0xf\n"
      "v_mfma_f32_16x16x32_bf16 v[160:163], a[236:239], v[100:103], v[160:163]\n"
      "s_waitcnt vmcnt(11) lgkmcnt(5)\n"
      "buffer_store_dword v18, v4, s[8:11], s52 offen sc1\n"
      "v_mfma_f32_16x16x32_bf16 v[160:163], a[240:243], v[104:107], v[160:163]\n"
      "ds_read_b128 v[72:75], v0 offset:28672\n"
      "v_add_f32_dpp v239, v239, v239 row_ror:2 row_mask:0xf bank_mask:0xf\n"
      "v_mfma_f32_16x16x32_bf16 v[160:163], a[244:247], v[108:111], v[160:163]\n"
      "ds_read_b128 v[76:79], v0 offset:29696\n"
      "v_add_f32_dpp v239, v239, v239 row_ror:1 row_mask:0xf bank_mask:0xf\n"
      "v_mfma_f32_16x16x32_bf16 v[160:163], a[248:251], v[112:115], v[160:163]\n"
      "v_add_f32_dpp v238, v238, v238 row_ror:1 row_mask:0xf bank_mask:0xf\n"
      "v_add_f32 v168, v168, v156\n"
      "v_mfma_f32_16x16x32_bf16 v[160:163], a[252:255], v[116:119], v[160:163]\n"
      "ds_read_b128 v[100:103], v0 offset:35840\n"
      "v_mul_f32 v238, 0x3ae38e39, v238\n"
      "v_mfma_f32_16x16x32_bf16 v[160:163], v[24:27], v[120:123], v[160:163]\n"
      "v_and_b32 v169, 0xffff0000, v218\n"
      "v_mfma_f32_16x16x32_bf16 v[160:163], v[28:31], v[124:127], v[160:163]\n"
      "ds_read_b128 v[96:99], v0 offset:34816\n"
      "v_and_b32 v175, 0xffff0000, v221\n"
      "v_mfma_f32_16x16x32_bf16 v[160:163], v[32:35], v[128:131], v[160:163]\n"
      "ds_read_b128 v[116:119], v0 offset:39936\n"
      "v_lshlrev_b32 v174, 16, v221\n"
      "v_mfma_f32_16x16x32_bf16 v[160:163], v[36:39], v[132:135], v[160:163]\n"
      "ds_read_b128 v[120:123], v0 offset:40960\n"
      "v_add_f32 v167, v167, v155\n"
      "v_mfma_f32_16x16x32_bf16 v[160:163], v[40:43], v[136:139], v[160:163]\n"
      "ds_read_b128 v[108:111], v0 offset:37888\n"
      "v_and_b32 v171, 0xffff0000, v219\n"
      "v_mfma_f32_16x16x32_bf16 v[160:163], v[44:47], v[140:143], v[160:163]\n"
      "ds_read_b128 v[88:91], v0 offset:32768\n"
      "v_add_f32 v166, v166, v154\n"
      "v_mfma_f32_16x16x32_bf16 v[160:163], v[48:51], v[144:147], v[160:163]\n"
      "v_add_f32 v239, 0x3afba882, v239\n"
      "v_mul_f32 v239, 0x3ae38e39, v239\n"
      "v_mfma_f32_16x16x32_bf16 v[160:163], v[52:55], v[148:151], v[160:163]\n"
      "s_sub_u32 s59, s59, 1\n"
      "s_cbranch_scc1 .Ld_out0\n"
      ".Ld_t1:\n"
      "v_mfma_f32_16x16x32_bf16 v[152:155], a[0:3], v[56:59], 0\n"
      "s_waitcnt lgkmcnt(4)\n"
      "buffer_store_dwordx2 v[238:239], v12, s[8:11], s56 offen sc1\n"
      "v_mfma_f32_16x16x32_bf16 v[152:155], a[4:7], v[60:63], v[152:155]\n"
      "ds_read_b128 v[104:107], v0 offset:36864\n"
      "v_mfma_f32_16x16x32_bf16 v[152:155], a[8:11], v[64:67], v[152:155]\n"
      "ds_read_b128 v[140:143], v0 offset:46080\n"
      "v_mfma_f32_16x16x32_bf16 v[152:155], a[12:15], v[68:71], v[152:155]\n"
      "ds_read_b128 v[112:115], v0 offset:38912\n"
      "v_mfma_f32_16x16x32_bf16 v[152:155], a[16:19], v[72:75], v[152:155]\n"
      "ds_read_b128 v[124:127], v0 offset:41984\n"
      "v_mfma_f32_16x16x32_bf16 v[152:155], a[20:23], v[76:79], v[152:155]\n"
      "ds_read_b128 v[132:135], v0 offset:44032\n"
      "v_mfma_f32_16x16x32_bf16 v[152:155], a[24:27], v[80:83], v[152:155]\n"
      "s_mov_b32 s51, s36\n"
      "v_add_f32 v174, v174, v162\n"
      "s_add_u32 s36, s36, 165888\n"
      "v_mfma_f32_16x16x32_bf16 v[152:155], a[28:31], v[84:87], v[152:155]\n"
      "s_waitcnt lgkmcnt(5)\n"
      "ds_read_b128 v[148:151], v0 offset:48128\n"
      "v_mfma_f32_16x16x32_bf16 v[152:155], a[32:35], v[88:91], v[152:155]\n"
      "ds_read_b128 v[128:131], v0 offset:43008\n"
      "s_add_u32 m0, s43, 73728\n"
      "v_mfma_f32_16x16x32_bf16 v[152:155], a[36:39], v[92:95], v[152:155]\n"
      "ds_read_b128 v[136:139], v0 offset:45056\n"
      "v_mfma_f32_16x16x32_bf16 v[152:155], a[40:43], v[96:99], v[152:155]\n"
      "v_mfma_f32_16x16x32_bf16 v[152:155], a[44:47], v[100:103], v[152:155]\n"
      "s_waitcnt lgkmcnt(4)\n"
      "v_add_f32 v170, v170, v158\n"
      "v_mfma_f32_16x16x32_bf16 v[152:155], a[48:51], v[104:107], v[152:155]\n"
      "v_mfma_f32_16x16x32_bf16 v[152:155], a[52:55], v[108:111], v[152:155]\n"
      "s_waitcnt vmcnt(8) lgkmcnt(3)\n"
      "v_lshlrev_b32 v172, 16, v220\n"
      "v_cmp_ne_u32 vcc, s21, v20\n"
      "v_mfma_f32_16x16x32_bf16 v[152:155], a[56:59], v[112:115], v[152:155]\n"
      "ds_read_b128 v[144:147], v0 offset:47104\n"
      "s_add_u32 s38, s38, 147456\n"
      "v_mfma_f32_16x16x32_bf16 v[152:155], a[60:63], v[116:119], v[152:155]\n"
      "s_min_u32 s36, s36, s6\n"
      "v_add_f32 v171, v171, v159\n"
      "v_mfma_f32_16x16x32_bf16 v[152:155], a[64:67], v[120:123], v[152:155]\n"
      "v_mfma_f32_16x16x32_bf16 v[152:155], a[68:71], v[124:127], v[152:155]\n"
      "s_waitcnt lgkmcnt(1)\n"
      "v_mfma_f32_16x16x32_bf16 v[152:155], a[72:75], v[128:131], v[152:155]\n"
      "v_add_f32 v169, v169, v157\n"
      "v_add_f32 v172, v172, v160\n"
      "v_mfma_f32_16x16x32_bf16 v[152:155], a[76:79], v[132:135], v[152:155]\n"
      "v_mfma_f32_16x16x32_bf16 v[152:155], a[80:83], v[136:139], v[152:155]\n"
      "s_cbranch_vccnz .Lsp_d3_6\n"
      ".Lbk_d3_6:\n"
      "v_mfma_f32_16x16x32_bf16 v[152:155], a[84:87], v[140:143], v[152:155]\n"
      "s_waitcnt lgkmcnt(0)\n"
      "v_mfma_f32_16x16x32_bf16 v[152:155], a[88:91], v[144:147], v[152:155]\n"
      "v_mfma_f32_16x16x32_bf16 v[152:155], a[92:95], v[148:151], v[152:155]\n"
      "buffer_load_dwordx4 v0, s[8:11], s51 offen offset:2048 sc1 lds\n"
      "v_mfma_f32_16x16x32_bf16 v[156:159], a[96:99], v[56:59], 0\n"
      "v_mfma_f32_16x16x32_bf16 v[156:159], a[100:103], v[60:63], v[156:159]\n"
      "v_add_u32 v14, 1, v14\n"
      "v_mfma_f32_16x16x32_bf16 v[156:159], a[104:107], v[64:67], v[156:159]\n"
      "v_cvt_pk_bf16_f32 v225, v154, v155\n"
      "v_mfma_f32_16x16x32_bf16 v[156:159], a[108:111], v[68:71], v[156:159]\n"
      "v_add_f32 v175, v175, v163\n"
      "v_mfma_f32_16x16x32_bf16 v[156:159], a[112:115], v[72:75], v[156:159]\n"
      "buffer_load_dwordx4 v0, s[8:11], s51 offen offset:1024 sc1 lds\n"
      "v_mfma_f32_16x16x32_bf16 v[156:159], a[116:119], v[76:79], v[156:159]\n"
      "v_mfma_f32_16x16x32_bf16 v[156:159], a[120:123], v[80:83], v[156:159]\n"
      "v_mfma_f32_16x16x32_bf16 v[156:159], a[124:127], v[84:87], v[156:159]\n"
      "v_and_b32 v173, 0xffff0000, v220\n"
      "v_mfma_f32_16x16x32_bf16 v[156:159], a[128:131], v[88:91], v[156:159]\n"
      "buffer_load_dword v20, v2, s[8:11], s36 offen sc1\n"
      "v_mfma_f32_16x16x32_bf16 v[156:159], a[132:135], v[92:95], v[156:159]\n"
      "s_waitcnt vmcnt(5)\n"
      "ds_write_b32 v16, v14\n"
      "v_mfma_f32_16x16x32_bf16 v[156:159], a[136:139], v[96:99], v[156:159]\n"
      "buffer_load_dwordx4 v0, s[8:11], s51 offen offset:0 sc1 lds\n"
      "v_mfma_f32_16x16x32_bf16 v[156:159], a[140:143], v[100:103], v[156:159]\n"
      "v_add_f32 v173, v173, v161\n"
      "v_mfma_f32_16x16x32_bf16 v[156:159], a[144:147], v[104:107], v[156:159]\n"
      "v_mfma_f32_16x16x32_bf16 v[156:159], a[148:151], v[108:111], v[156:159]\n"
      "v_mfma_f32_16x16x32_bf16 v[156:159], a[152:155], v[112:115], v[156:159]\n"
      "v_mfma_f32_16x16x32_bf16 v[156:159], a[156:159], v[116:119], v[156:159]\n"
      "buffer_load_dwordx4 v0, s[8:11], s51 offen offset:3072 sc1 lds\n"
      "v_mfma_f32_16x16x32_bf16 v[156:159], a[160:163], v[120:123], v[156:159]\n"
      "s_add_u32 m0, s43, 77824\n"
      "v_mfma_f32_16x16x32_bf16 v[156:159], a[164:167], v[124:127], v[156:159]\n"
      "v_mfma_f32_16x16x32_bf16 v[156:159], a[168:171], v[128:131], v[156:159]\n"
      "ds_read_b32 v15, v17\n"
      "v_mfma_f32_16x16x32_bf16 v[156:159], a[172:175], v[132:135], v[156:159]\n"
      "s_add_u32 s52, s52, 173056\n"
      "v_mfma_f32_16x16x32_bf16 v[156:159], a[176:179], v[136:139], v[156:159]\n"
      "buffer_load_dwordx4 v1, s[8:11], s51 offen offset:0 sc1 lds\n"
      "v_mfma_f32_16x16x32_bf16 v[156:159], a[180:183], v[140:143], v[156:159]\n"
      "v_mfma_f32_16x16x32_bf16 v[156:159], a[184:187], v[144:147], v[156:159]\n"
      "v_cvt_pk_bf16_f32 v224, v152, v153\n"
      "v_mfma_f32_16x16x32_bf16 v[156:159], a[188:191], v[148:151], v[156:159]\n"
      "s_waitcnt lgkmcnt(0)\n"
      "v_cmp_gt_i32 vcc, v14, v15\n"
      "v_mfma_f32_16x16x32_bf16 v[160:163], a[192:195], v[56:59], 0\n"
      "buffer_load_dwordx4 v1, s[8:11], s51 offen offset:1024 sc1 lds\n"
      "v_mfma_f32_16x16x32_bf16 v[160:163], a[196:199], v[60:63], v[160:163]\n"
      "v_mfma_f32_16x16x32_bf16 v[160:163], a[200:203], v[64:67], v[160:163]\n"
      "s_cbranch_vccnz .Lsp_dy2_7\n"
      ".Lbk_dy2_7:\n"
      "v_mfma_f32_16x16x32_bf16 v[160:163], a[204:207], v[68:71], v[160:163]\n"
      "ds_read_b128 v[68:71], v22 offset:3072\n"
      "v_mfma_f32_16x16x32_bf16 v[160:163], a[208:211], v[72:75], v[160:163]\n"
      "ds_read_b128 v[60:63], v22 offset:1024\n"
      "v_mfma_f32_16x16x32_bf16 v[160:163], a[212:215], v[76:79], v[160:163]\n"
      "s_waitcnt vmcnt(7)\n"
      "buffer_store_dword v18, v5, s[8:11], s56 offen sc1\n"
      "v_mfma_f32_16x16x32_bf16 v[160:163], a[216:219], v[80:83], v[160:163]\n"
      "v_cvt_pk_bf16_f32 v227, v158, v159\n"
      "v_mfma_f32_16x16x32_bf16 v[160:163], a[220:223], v[84:87], v[160:163]\n"
      "ds_read_b128 v[80:83], v22 offset:6144\n"
      "v_cvt_pk_bf16_f32 v226, v156, v157\n"
      "v_mfma_f32_16x16x32_bf16 v[160:163], a[224:227], v[88:91], v[160:163]\n"
      "ds_read_b128 v[72:75], v22 offset:4096\n"
      "v_mfma_f32_16x16x32_bf16 v[160:163], a[228:231], v[92:95], v[160:163]\n"
      "ds_read_b128 v[76:79], v22 offset:5120\n"
      "v_mfma_f32_16x16x32_bf16 v[160:163], a[232:235], v[96:99], v[160:163]\n"
      "ds_read_b128 v[96:99], v22 offset:10240\n"
      "v_mfma_f32_16x16x32_bf16 v[160:163], a[236:239], v[100:103], v[160:163]\n"
      "v_mfma_f32_16x16x32_bf16 v[160:163], a[240:243], v[104:107], v[160:163]\n"
      "buffer_store_dwordx4 v[224:227], v0, s[8:11], s37 offen sc1\n"
      "v_mfma_f32_16x16x32_bf16 v[160:163], a[244:247], v[108:111], v[160:163]\n"
      "v_mfma_f32_16x16x32_bf16 v[160:163], a[248:251], v[112:115], v[160:163]\n"
      "ds_read_b128 v[84:87], v22 offset:7168\n"
      "v_mfma_f32_16x16x32_bf16 v[160:163], a[252:255], v[116:119], v[160:163]\n"
      "v_mfma_f32_16x16x32_bf16 v[160:163], v[24:27], v[120:123], v[160:163]\n"
      "ds_read_b128 v[56:59], v22 offset:0\n"
      "v_mfma_f32_16x16x32_bf16 v[160:163], v[28:31], v[124:127], v[160:163]\n"
      "ds_read_b128 v[92:95], v22 offset:9216\n"
      "s_mov_b32 s51, s36\n"
      "v_mfma_f32_16x16x32_bf16 v[160:163], v[32:35], v[128:131], v[160:163]\n"
      "ds_read_b128 v[64:67], v22 offset:2048\n"
      "v_mfma_f32_16x16x32_bf16 v[160:163], v[36:39], v[132:135], v[160:163]\n"
      "ds_read_b128 v[100:103], v22 offset:11264\n"
      "v_mfma_f32_16x16x32_bf16 v[160:163], v[40:43], v[136:139], v[160:163]\n"
      "ds_read_b128 v[132:135], v22 offset:19456\n"
      "v_mfma_f32_16x16x32_bf16 v[160:163], v[44:47], v[140:143], v[160:163]\n"
      "v_mfma_f32_16x16x32_bf16 v[160:163], v[48:51], v[144:147], v[160:163]\n"
      "s_waitcnt lgkmcnt(4)\n"
      "ds_read_b128 v[112:115], v22 offset:14336\n"
      "s_add_u32 m0, s43, 98304\n"
      "v_mfma_f32_16x16x32_bf16 v[160:163], v[52:55], v[148:151], v[160:163]\n"
      "s_sub_u32 s59, s59, 1\n"
      "s_cbranch_scc1 .Ld_out1\n"
      ".Ld_t2:\n"
      "v_mfma_f32_16x16x32_bf16 v[152:155], a[0:3], v[56:59], 0\n"
      "ds_read_b128 v[88:91], v22 offset:8192\n"
      "v_mfma_f32_16x16x32_bf16 v[152:155], a[4:7], v[60:63], v[152:155]\n"
      "s_waitcnt lgkmcnt(4)\n"
      "ds_read_b128 v[120:123], v22 offset:16384\n"
      "v_mfma_f32_16x16x32_bf16 v[152:155], a[8:11], v[64:67], v[152:155]\n"
      "s_add_u32 s36, s36, 165888\n"
      "ds_read_b128 v[104:107], v22 offset:12288\n"
      "v_cvt_pk_bf16_f32 v229, v162, v163\n"
      "v_mfma_f32_16x16x32_bf16 v[152:155], a[12:15], v[68:71], v[152:155]\n"
      "ds_read_b128 v[148:151], v22 offset:23552\n"
      "v_cvt_pk_bf16_f32 v228, v160, v161\n"
      "v_mfma_f32_16x16x32_bf16 v[152:155], a[16:19], v[72:75], v[152:155]\n"
      "buffer_store_dwordx2 v[228:229], v13, s[8:11], s37 offen sc1\n"
      "v_mfma_f32_16x16x32_bf16 v[152:155], a[20:23], v[76:79], v[152:155]\n"
      "ds_read_b128 v[108:111], v22 offset:13312\n"
      "v_mfma_f32_16x16x32_bf16 v[152:155], a[24:27], v[80:83], v[152:155]\n"
      "ds_read_b128 v[116:119], v22 offset:15360\n"
      "v_mfma_f32_16x16x32_bf16 v[152:155], a[28:31], v[84:87], v[152:155]\n"
      "s_waitcnt lgkmcnt(5)\n"
      "ds_read_b128 v[124:127], v22 offset:17408\n"
      "v_mfma_f32_16x16x32_bf16 v[152:155], a[32:35], v[88:91], v[152:155]\n"
      "ds_read_b128 v[140:143], v22 offset:21504\n"
      "v_mfma_f32_16x16x32_bf16 v[152:155], a[36:39], v[92:95], v[152:155]\n"
      "ds_read_b128 v[128:131], v22 offset:18432\n"
      "v_mfma_f32_16x16x32_bf16 v[152:155], a[40:43], v[96:99], v[152:155]\n"
      "v_mfma_f32_16x16x32_bf16 v[152:155], a[44:47], v[100:103], v[152:155]\n"
      "s_waitcnt lgkmcnt(5)\n"
      "ds_read_b128 v[136:139], v22 offset:20480\n"
      "v_mfma_f32_16x16x32_bf16 v[152:155], a[48:51], v[104:107], v[152:155]\n"
      "s_waitcnt lgkmcnt(5)\n"
      "v_mfma_f32_16x16x32_bf16 v[152:155], a[52:55], v[108:111], v[152:155]\n"
      "v_mfma_f32_16x16x32_bf16 v[152:155], a[56:59], v[112:115], v[152:155]\n"
      "s_waitcnt lgkmcnt(3)\n"
      "ds_read_b128 v[144:147], v22 offset:22528\n"
      "v_mfma_f32_16x16x32_bf16 v[152:155], a[60:63], v[116:119], v[152:155]\n"
      "v_add_u32 v14, 1, v14\n"
      "v_mfma_f32_16x16x32_bf16 v[152:155], a[64:67], v[120:123], v[152:155]\n"
      "s_waitcnt vmcnt(7) lgkmcnt(2)\n"
      "v_cmp_ne_u32 vcc, s21, v20\n"
      "v_mfma_f32_16x16x32_bf16 v[152:155], a[68:71], v[124:127], v[152:155]\n"
      "v_mfma_f32_16x16x32_bf16 v[152:155], a[72:75], v[128:131], v[152:155]\n"
      "v_mfma_f32_16x16x32_bf16 v[152:155], a[76:79], v[132:135], v[152:155]\n"
      "s_waitcnt lgkmcnt(1)\n"
      "v_mfma_f32_16x16x32_bf16 v[152:155], a[80:83], v[136:139], v[152:155]\n"
      "v_mfma_f32_16x16x32_bf16 v[152:155], a[84:87], v[140:143], v[152:155]\n"
      "s_waitcnt lgkmcnt(0)\n"
      "s_cbranch_vccnz .Lsp_d4_8\n"
      ".Lbk_d4_8:\n"
      "v_mfma_f32_16x16x32_bf16 v[152:155], a[88:91], v[144:147], v[152:155]\n"
      "v_mfma_f32_16x16x32_bf16 v[152:155], a[92:95], v[148:151], v[152:155]\n"
      "s_min_u32 s36, s36, s6\n"
      "v_mfma_f32_16x16x32_bf16 v[156:159], a[96:99], v[56:59], 0\n"
      "buffer_load_dwordx4 v0, s[8:11], s51 offen offset:0 sc1 lds\n"
      "v_mfma_f32_16x16x32_bf16 v[156:159], a[100:103], v[60:63], v[156:159]\n"
      "v_mfma_f32_16x16x32_bf16 v[156:159], a[104:107], v[64:67], v[156:159]\n"
      "v_mfma_f32_16x16x32_bf16 v[156:159], a[108:111], v[68:71], v[156:159]\n"
      "v_mfma_f32_16x16x32_bf16 v[156:159], a[112:115], v[72:75], v[156:159]\n"
      "buffer_load_dwordx4 v0, s[8:11], s51 offen offset:2048 sc1 lds\n"
      "v_mfma_f32_16x16x32_bf16 v[156:159], a[116:119], v[76:79], v[156:159]\n"
      "s_waitcnt vmcnt(5)\n"
      "ds_write_b32 v16, v14\n"
      "v_mfma_f32_16x16x32_bf16 v[156:159], a[120:123], v[80:83], v[156:159]\n"
      "v_mfma_f32_16x16x32_bf16 v[156:159], a[124:127], v[84:87], v[156:159]\n"
      "v_mfma_f32_16x16x32_bf16 v[156:159], a[128:131], v[88:91], v[156:159]\n"
      "v_mfma_f32_16x16x32_bf16 v[156:159], a[132:135], v[92:95], v[156:159]\n"
      "buffer_load_dword v20, v2, s[8:11], s36 offen sc1\n"
      "v_mfma_f32_16x16x32_bf16 v[156:159], a[136:139], v[96:99], v[156:159]\n"
      "v_mfma_f32_16x16x32_bf16 v[156:159], a[140:143], v[100:103], v[156:159]\n"
      "v_cvt_pk_bf16_f32 v225, v154, v155\n"
      "v_mfma_f32_16x16x32_bf16 v[156:159], a[144:147], v[104:107], v[156:159]\n"
      "buffer_load_dwordx4 v0, s[8:11], s51 offen offset:3072 sc1 lds\n"
      "v_mfma_f32_16x16x32_bf16 v[156:159], a[148:151], v[108:111], v[156:159]\n"
      "v_mfma_f32_16x16x32_bf16 v[156:159], a[152:155], v[112:115], v[156:159]\n"
      "v_mfma_f32_16x16x32_bf16 v[156:159], a[156:159], v[116:119], v[156:159]\n"
      "ds_read_b32 v15, v17\n"
      "v_mfma_f32_16x16x32_bf16 v[156:159], a[160:163], v[120:123], v[156:159]\n"
      "buffer_load_dwordx4 v0, s[8:11], s51 offen offset:1024 sc1 lds\n"
      "v_mfma_f32_16x16x32_bf16 v[156:159], a[164:167], v[124:127], v[156:159]\n"
      "s_add_u32 m0, s43, 102400\n"
      "v_mfma_f32_16x16x32_bf16 v[156:159], a[168:171], v[128:131], v[156:159]\n"
      "v_mfma_f32_16x16x32_bf16 v[156:159], a[172:175], v[132:135], v[156:159]\n"
      "v_mfma_f32_16x16x32_bf16 v[156:159], a[176:179], v[136:139], v[156:159]\n"
      "buffer_load_dwordx4 v1, s[8:11], s51 offen offset:1024 sc1 lds\n"
      "v_mfma_f32_16x16x32_bf16 v[156:159], a[180:183], v[140:143], v[156:159]\n"
      "v_mfma_f32_16x16x32_bf16 v[156:159], a[184:187], v[144:147], v[156:159]\n"
      "s_waitcnt lgkmcnt(0)\n"
      "v_cmp_gt_i32 vcc, v14, v15\n"
      "v_mfma_f32_16x16x32_bf16 v[156:159], a[188:191], v[148:151], v[156:159]\n"
      "v_mfma_f32_16x16x32_bf16 v[160:163], a[192:195], v[56:59], 0\n"
      "buffer_load_dwordx4 v1, s[8:11], s51 offen offset:0 sc1 lds\n"
      "v_mfma_f32_16x16x32_bf16 v[160:163], a[196:199], v[60:63], v[160:163]\n"
      "s_cbranch_vccnz .Lsp_dy3_9\n"
      ".Lbk_dy3_9:\n"
      "ds_read_b128 v[60:63], v22 offset:25600\n"
      "v_mfma_f32_16x16x32_bf16 v[160:163], a[200:203], v[64:67], v[160:163]\n"
      "v_mfma_f32_16x16x32_bf16 v[160:163], a[204:207], v[68:71], v[160:163]\n"
      "v_cvt_pk_bf16_f32 v227, v158, v159\n"
      "v_mfma_f32_16x16x32_bf16 v[160:163], a[208:211], v[72:75], v[160:163]\n"
      "ds_read_b128 v[64:67], v22 offset:26624\n"
      "v_mfma_f32_16x16x32_bf16 v[160:163], a[212:215], v[76:79], v[160:163]\n"
      "s_mov_b32 s51, s36\n"
      "ds_read_b128 v[72:75], v22 offset:28672\n"
      "v_mfma_f32_16x16x32_bf16 v[160:163], a[216:219], v[80:83], v[160:163]\n"
      "ds_read_b128 v[68:71], v22 offset:27648\n"
      "v_mfma_f32_16x16x32_bf16 v[160:163], a[220:223], v[84:87], v[160:163]\n"
      "v_cvt_pk_bf16_f32 v224, v152, v153\n"
      "v_mfma_f32_16x16x32_bf16 v[160:163], a[224:227], v[88:91], v[160:163]\n"
      "s_waitcnt vmcnt(7) lgkmcnt(3)\n"
      "buffer_store_dword v18, v4, s[8:11], s52 offen sc1\n"
      "v_mfma_f32_16x16x32_bf16 v[160:163], a[228:231], v[92:95], v[160:163]\n"
      "ds_read_b128 v[92:95], v22 offset:33792\n"
      "v_mfma_f32_16x16x32_bf16 v[160:163], a[232:235], v[96:99], v[160:163]\n"
      "ds_read_b128 v[56:59], v22 offset:24576\n"
      "v_mfma_f32_16x16x32_bf16 v[160:163], a[236:239], v[100:103], v[160:163]\n"
      "buffer_load_dword v21, v3, s[8:11], s53 offen sc1\n"
      "v_mfma_f32_16x16x32_bf16 v[160:163], a[240:243], v[104:107], v[160:163]\n"
      "ds_read_b128 v[100:103], v22 offset:35840\n"
      "v_mfma_f32_16x16x32_bf16 v[160:163], a[244:247], v[108:111], v[160:163]\n"
      "ds_read_b128 v[80:83], v22 offset:30720\n"
      "v_mfma_f32_16x16x32_bf16 v[160:163], a[248:251], v[112:115], v[160:163]\n"
      "ds_read_b128 v[112:115], v22 offset:38912\n"
      "v_mfma_f32_16x16x32_bf16 v[160:163], a[252:255], v[116:119], v[160:163]\n"
      "s_add_u32 s37, s37, 106496\n"
      "ds_read_b128 v[88:91], v22 offset:32768\n"
      "v_mfma_f32_16x16x32_bf16 v[160:163], v[24:27], v[120:123], v[160:163]\n"
      "ds_read_b128 v[96:99], v22 offset:34816\n"
      "v_cvt_pk_bf16_f32 v226, v156, v157\n"
      "v_mfma_f32_16x16x32_bf16 v[160:163], v[28:31], v[124:127], v[160:163]\n"
      "ds_read_b128 v[76:79], v22 offset:29696\n"
      "v_mfma_f32_16x16x32_bf16 v[160:163], v[32:35], v[128:131], v[160:163]\n"
      "v_mfma_f32_16x16x32_bf16 v[160:163], v[36:39], v[132:135], v[160:163]\n"
      "buffer_store_dwordx4 v[224:227], v0, s[8:11], s37 offen sc1\n"
      "v_mfma_f32_16x16x32_bf16 v[160:163], v[40:43], v[136:139], v[160:163]\n"
      "ds_read_b128 v[116:119], v22 offset:39936\n"
      "v_mfma_f32_16x16x32_bf16 v[160:163], v[44:47], v[140:143], v[160:163]\n"
      "ds_read_b128 v[84:87], v22 offset:31744\n"
      "v_mfma_f32_16x16x32_bf16 v[160:163], v[48:51], v[144:147], v[160:163]\n"
      "s_waitcnt lgkmcnt(4)\n"
      "ds_read_b128 v[104:107], v22 offset:36864\n"
      "v_mfma_f32_16x16x32_bf16 v[160:163], v[52:55], v[148:151], v[160:163]\n"
      "s_sub_u32 s59, s59, 1\n"
      "s_cbranch_scc1 .Ld_out2\n"
      ".Ld_t3:\n"
      "v_mfma_f32_16x16x32_bf16 v[152:155], a[0:3], v[56:59], 0\n"
      "ds_read_b128 v[120:123], v22 offset:40960\n"
      "v_mfma_f32_16x16x32_bf16 v[152:155], a[4:7], v[60:63], v[152:155]\n"
      "ds_read_b128 v[108:111], v22 offset:37888\n"
      "v_mfma_f32_16x16x32_bf16 v[152:155], a[8:11], v[64:67], v[152:155]\n"
      "ds_read_b128 v[132:135], v22 offset:44032\n"
      "v_mfma_f32_16x16x32_bf16 v[152:155], a[12:15], v[68:71], v[152:155]\n"
      "ds_read_b128 v[136:139], v22 offset:45056\n"
      "v_cvt_pk_bf16_f32 v229, v162, v163\n"
      "v_mfma_f32_16x16x32_bf16 v[152:155], a[16:19], v[72:75], v[152:155]\n"
      "s_waitcnt lgkmcnt(5)\n"
      "v_mfma_f32_16x16x32_bf16 v[152:155], a[20:23], v[76:79], v[152:155]\n"
      "ds_read_b128 v[124:127], v22 offset:41984\n"
      "v_mfma_f32_16x16x32_bf16 v[152:155], a[24:27], v[80:83], v[152:155]\n"
      "s_add_u32 m0, s43, 122880\n"
      "v_cvt_pk_bf16_f32 v228, v160, v161\n"
      "v_mfma_f32_16x16x32_bf16 v[152:155], a[28:31], v[84:87], v[152:155]\n"
      "buffer_store_dwordx2 v[228:229], v13, s[8:11], s37 offen sc1\n"
      "v_mfma_f32_16x16x32_bf16 v[152:155], a[32:35], v[88:91], v[152:155]\n"
      "v_mfma_f32_16x16x32_bf16 v[152:155], a[36:39], v[92:95], v[152:155]\n"
      "ds_read_b128 v[128:131], v22 offset:43008\n"
      "v_mfma_f32_16x16x32_bf16 v[152:155], a[40:43], v[96:99], v[152:155]\n"
      "ds_read_b128 v[144:147], v22 offset:47104\n"
      "v_mfma_f32_16x16x32_bf16 v[152:155], a[44:47], v[100:103], v[152:155]\n"
      "s_waitcnt lgkmcnt(3)\n"
      "ds_read_b128 v[148:151], v22 offset:48128\n"
      "v_mfma_f32_16x16x32_bf16 v[152:155], a[48:51], v[104:107], v[152:155]\n"
      "ds_read_b128 v[140:143], v22 offset:46080\n"
      "v_mfma_f32_16x16x32_bf16 v[152:155], a[52:55], v[108:111], v[152:155]\n"
      "v_mfma_f32_16x16x32_bf16 v[152:155], a[56:59], v[112:115], v[152:155]\n"
      "s_waitcnt vmcnt(8) lgkmcnt(4)\n"
      "v_cmp_ne_u32 vcc, s21, v20\n"
      "v_mfma_f32_16x16x32_bf16 v[152:155], a[60:63], v[116:119], v[152:155]\n"
      "v_mfma_f32_16x16x32_bf16 v[152:155], a[64:67], v[120:123], v[152:155]\n"
      "s_add_u32 s36, s36, 165888\n"
      "s_add_u32 s52, s52, 106496\n"
      "v_mfma_f32_16x16x32_bf16 v[152:155], a[68:71], v[124:127], v[152:155]\n"
      "s_waitcnt lgkmcnt(2)\n"
      "v_mfma_f32_16x16x32_bf16 v[152:155], a[72:75], v[128:131], v[152:155]\n"
      "s_cbranch_vccnz .Lsp_d5_10\n"
      ".Lbk_d5_10:\n"
      "v_mfma_f32_16x16x32_bf16 v[152:155], a[76:79], v[132:135], v[152:155]\n"
      "buffer_load_dwordx4 v0, s[8:11], s51 offen offset:1024 sc1 lds\n"
      "v_mfma_f32_16x16x32_bf16 v[152:155], a[80:83], v[136:139], v[152:155]\n"
      "s_waitcnt lgkmcnt(0)\n"
      "v_mfma_f32_16x16x32_bf16 v[152:155], a[84:87], v[140:143], v[152:155]\n"
      "v_add_u32 v14, 1, v14\n"
      "v_mfma_f32_16x16x32_bf16 v[152:155], a[88:91], v[144:147], v[152:155]\n"
      "v_mfma_f32_16x16x32_bf16 v[152:155], a[92:95], v[148:151], v[152:155]\n"
      "buffer_load_dwordx4 v0, s[8:11], s51 offen offset:0 sc1 lds\n"
      "v_mfma_f32_16x16x32_bf16 v[156:159], a[96:99], v[56:59], 0\n"
      "v_mfma_f32_16x16x32_bf16 v[156:159], a[100:103], v[60:63], v[156:159]\n"
      "v_mfma_f32_16x16x32_bf16 v[156:159], a[104:107], v[64:67], v[156:159]\n"
      "v_mfma_f32_16x16x32_bf16 v[156:159], a[108:111], v[68:71], v[156:159]\n"
      "buffer_load_dwordx4 v0, s[8:11], s51 offen offset:2048 sc1 lds\n"
      "v_mfma_f32_16x16x32_bf16 v[156:159], a[112:115], v[72:75], v[156:159]\n"
      "s_waitcnt vmcnt(7)\n"
      "ds_write_b32 v16, v14\n"
      "v_mfma_f32_16x16x32_bf16 v[156:159], a[116:119], v[76:79], v[156:159]\n"
      "v_mfma_f32_16x16x32_bf16 v[156:159], a[120:123], v[80:83], v[156:159]\n"
      "v_mfma_f32_16x16x32_bf16 v[156:159], a[124:127], v[84:87], v[156:159]\n"
      "buffer_load_dwordx4 v0, s[8:11], s51 offen offset:3072 sc1 lds\n"
      "v_mfma_f32_16x16x32_bf16 v[156:159], a[128:131], v[88:91], v[156:159]\n"
      "v_mfma_f32_16x16x32_bf16 v[156:159], a[132:135], v[92:95], v[156:159]\n"
      "v_cvt_pk_bf16_f32 v224, v152, v153\n"
      "v_mfma_f32_16x16x32_bf16 v[156:159], a[136:139], v[96:99], v[156:159]\n"
      "v_mfma_f32_16x16x32_bf16 v[156:159], a[140:143], v[100:103], v[156:159]\n"
      "s_min_u32 s36, s36, s6\n"
      "s_add_u32 m0, s43, 126976\n"
      "v_mfma_f32_16x16x32_bf16 v[156:159], a[144:147], v[104:107], v[156:159]\n"
      "buffer_load_dwordx4 v1, s[8:11], s51 offen offset:1024 sc1 lds\n"
      "v_mfma_f32_16x16x32_bf16 v[156:159], a[148:151], v[108:111], v[156:159]\n"
      "ds_read_b32 v15, v17\n"
      "v_mfma_f32_16x16x32_bf16 v[156:159], a[152:155], v[112:115], v[156:159]\n"
      "v_mfma_f32_16x16x32_bf16 v[156:159], a[156:159], v[116:119], v[156:159]\n"
      "v_mfma_f32_16x16x32_bf16 v[156:159], a[160:163], v[120:123], v[156:159]\n"
      "buffer_load_dword v20, v2, s[8:11], s36 offen sc1\n"
      "v_mfma_f32_16x16x32_bf16 v[156:159], a[164:167], v[124:127], v[156:159]\n"
      "v_mfma_f32_16x16x32_bf16 v[156:159], a[168:171], v[128:131], v[156:159]\n"
      "s_waitcnt lgkmcnt(0)\n"
      "v_cmp_gt_i32 vcc, v14, v15\n"
      "v_mfma_f32_16x16x32_bf16 v[156:159], a[172:175], v[132:135], v[156:159]\n"
      "buffer_load_dwordx4 v1, s[8:11], s51 offen offset:0 sc1 lds\n"
      "v_mfma_f32_16x16x32_bf16 v[156:159], a[176:179], v[136:139], v[156:159]\n"
      "s_add_u32 s55, s53, 6656\n"
      "v_mfma_f32_16x16x32_bf16 v[156:159], a[180:183], v[140:143], v[156:159]\n"
      "s_add_u32 s54, s53, 13312\n"
      "v_mfma_f32_16x16x32_bf16 v[156:159], a[184:187], v[144:147], v[156:159]\n"
      "s_waitcnt vmcnt(9)\n"
      "s_cbranch_vccnz .Lsp_dy4_11\n"
      ".Lbk_dy4_11:\n"
      "v_cmp_ne_u32 vcc, s21, v21\n"
      "v_mfma_f32_16x16x32_bf16 v[156:159], a[188:191], v[148:151], v[156:159]\n"
      ".Ld_p1:\n"
      "s_cbranch_execz .Ld_pr\n"
      "v_mfma_f32_16x16x32_bf16 v[160:163], a[192:195], v[56:59], 0\n"
      "v_mfma_f32_16x16x32_bf16 v[160:163], a[196:199], v[60:63], v[160:163]\n"
      "s_cbranch_vccnz .Lsp_df_12\n"
      ".Lbk_df_12:\n"
      "buffer_load_dwordx4 v[176:179], v0, s[8:11], s53 offen sc1\n"
      "v_mfma_f32_16x16x32_bf16 v[160:163], a[200:203], v[64:67], v[160:163]\n"
      "ds_read_b128 v[60:63], v23 offset:1024\n"
      "v_mfma_f32_16x16x32_bf16 v[160:163], a[204:207], v[68:71], v[160:163]\n"
      "ds_read_b128 v[64:67], v23 offset:2048\n"
      "v_mfma_f32_16x16x32_bf16 v[160:163], a[208:211], v[72:75], v[160:163]\n"
      "buffer_load_dwordx2 v[180:181], v13, s[8:11], s53 offen sc1\n"
      "v_mfma_f32_16x16x32_bf16 v[160:163], a[212:215], v[76:79], v[160:163]\n"
      "ds_read_b128 v[72:75], v23 offset:4096\n"
      "v_mfma_f32_16x16x32_bf16 v[160:163], a[216:219], v[80:83], v[160:163]\n"
      "ds_read_b128 v[68:71], v23 offset:3072\n"
      "s_add_u32 s58, s41, 4294967264\n"
      "v_mfma_f32_16x16x32_bf16 v[160:163], a[220:223], v[84:87], v[160:163]\n"
      "ds_read_b128 v[80:83], v23 offset:6144\n"
      "v_mfma_f32_16x16x32_bf16 v[160:163], a[224:227], v[88:91], v[160:163]\n"
      "v_cvt_pk_bf16_f32 v225, v154, v155\n"
      "v_mfma_f32_16x16x32_bf16 v[160:163], a[228:231], v[92:95], v[160:163]\n"
      "buffer_load_dwordx4 v[184:187], v0, s[8:11], s55 offen sc1\n"
      "v_mfma_f32_16x16x32_bf16 v[160:163], a[232:235], v[96:99], v[160:163]\n"
      "ds_read_b128 v[84:87], v23 offset:7168\n"
      "v_cvt_pk_bf16_f32 v226, v156, v157\n"
      "v_mfma_f32_16x16x32_bf16 v[160:163], a[236:239], v[100:103], v[160:163]\n"
      "ds_read_b128 v[76:79], v23 offset:5120\n"
      "v_mfma_f32_16x16x32_bf16 v[160:163], a[240:243], v[104:107], v[160:163]\n"
      "s_add_u32 s37, s37, 106496\n"
      "ds_read_b128 v[56:59], v23 offset:0\n"
      "v_mfma_f32_16x16x32_bf16 v[160:163], a[244:247], v[108:111], v[160:163]\n"
      "s_waitcnt vmcnt(10) lgkmcnt(4)\n"
      "buffer_store_dword v18, v4, s[8:11], s52 offen sc1\n"
      "v_mfma_f32_16x16x32_bf16 v[160:163], a[248:251], v[112:115], v[160:163]\n"
      "ds_read_b128 v[88:91], v23 offset:8192\n"
      "v_mfma_f32_16x16x32_bf16 v[160:163], a[252:255], v[116:119], v[160:163]\n"
      "ds_read_b128 v[96:99], v23 offset:10240\n"
      "v_mfma_f32_16x16x32_bf16 v[160:163], v[24:27], v[120:123], v[160:163]\n"
      "buffer_load_dwordx4 v[192:195], v0, s[8:11], s54 offen sc1\n"
      "v_mfma_f32_16x16x32_bf16 v[160:163], v[28:31], v[124:127], v[160:163]\n"
      "v_mfma_f32_16x16x32_bf16 v[160:163], v[32:35], v[128:131], v[160:163]\n"
      "buffer_load_dwordx2 v[188:189], v13, s[8:11], s55 offen sc1\n"
      "v_mfma_f32_16x16x32_bf16 v[160:163], v[36:39], v[132:135], v[160:163]\n"
      "ds_read_b128 v[100:103], v23 offset:11264\n"
      "v_mfma_f32_16x16x32_bf16 v[160:163], v[40:43], v[136:139], v[160:163]\n"
      "s_add_u32 s55, s53, 19968\n"
      "ds_read_b128 v[132:135], v23 offset:19456\n"
      "v_mfma_f32_16x16x32_bf16 v[160:163], v[44:47], v[140:143], v[160:163]\n"
      "ds_read_b128 v[104:107], v23 offset:12288\n"
      "v_mfma_f32_16x16x32_bf16 v[160:163], v[48:51], v[144:147], v[160:163]\n"
      "s_waitcnt lgkmcnt(3)\n"
      "buffer_load_dwordx2 v[196:197], v13, s[8:11], s54 offen sc1\n"
      "v_mfma_f32_16x16x32_bf16 v[160:163], v[52:55], v[148:151], v[160:163]\n"
      "s_sub_u32 s59, s59, 1\n"
      "s_cbranch_scc1 .Ld_out3\n"
      ".Ld_t4:\n"
      "v_mfma_f32_16x16x32_bf16 v[152:155], a[0:3], v[56:59], 0\n"
      "ds_read_b128 v[92:95], v23 offset:9216\n"
      "v_mfma_f32_16x16x32_bf16 v[152:155], a[4:7], v[60:63], v[152:155]\n"
      "ds_read_b128 v[108:111], v23 offset:13312\n"
      "s_add_u32 m0, s43, 0\n"
      "v_mfma_f32_16x16x32_bf16 v[152:155], a[8:11], v[64:67], v[152:155]\n"
      "ds_read_b128 v[128:131], v23 offset:18432\n"
      "v_cvt_pk_bf16_f32 v228, v160, v161\n"
      "v_mfma_f32_16x16x32_bf16 v[152:155], a[12:15], v[68:71], v[152:155]\n"
      "buffer_load_dwordx4 v[200:203], v0, s[8:11], s55 offen sc1\n"
      "v_mfma_f32_16x16x32_bf16 v[152:155], a[16:19], v[72:75], v[152:155]\n"
      "ds_read_b128 v[116:119], v23 offset:15360\n"
      "v_cvt_pk_bf16_f32 v227, v158, v159\n"
      "v_mfma_f32_16x16x32_bf16 v[152:155], a[20:23], v[76:79], v[152:155]\n"
      "ds_read_b128 v[112:115], v23 offset:14336\n"
      "v_mfma_f32_16x16x32_bf16 v[152:155], a[24:27], v[80:83], v[152:155]\n"
      "s_mov_b32 s51, s36\n"
      "ds_read_b128 v[120:123], v23 offset:16384\n"
      "v_mfma_f32_16x16x32_bf16 v[152:155], a[28:31], v[84:87], v[152:155]\n"
      "buffer_store_dwordx4 v[224:227], v0, s[8:11], s37 offen sc1\n"
      "v_mfma_f32_16x16x32_bf16 v[152:155], a[32:35], v[88:91], v[152:155]\n"
      "s_waitcnt lgkmcnt(4)\n"
      "ds_read_b128 v[140:143], v23 offset:21504\n"
      "v_mfma_f32_16x16x32_bf16 v[152:155], a[36:39], v[92:95], v[152:155]\n"
      "ds_read_b128 v[124:127], v23 offset:17408\n"
      "v_mfma_f32_16x16x32_bf16 v[152:155], a[40:43], v[96:99], v[152:155]\n"
      "ds_read_b128 v[136:139], v23 offset:20480\n"
      "v_mfma_f32_16x16x32_bf16 v[152:155], a[44:47], v[100:103], v[152:155]\n"
      "v_cvt_pk_bf16_f32 v229, v162, v163\n"
      "v_mfma_f32_16x16x32_bf16 v[152:155], a[48:51], v[104:107], v[152:155]\n"
      "buffer_store_dwordx2 v[228:229], v13, s[8:11], s37 offen sc1\n"
      "v_mfma_f32_16x16x32_bf16 v[152:155], a[52:55], v[108:111], v[152:155]\n"
      "s_waitcnt lgkmcnt(3)\n"
      "ds_read_b128 v[148:151], v23 offset:23552\n"
      "v_add_u32_e64 v14, 1, v14\n"
      "v_mfma_f32_16x16x32_bf16 v[152:155], a[56:59], v[112:115], v[152:155]\n"
      "s_add_u32 s36, s36, 165888\n"
      "s_add_u32 s37, s37, 106496\n"
      "ds_read_b128 v[144:147], v23 offset:22528\n"
      "v_mfma_f32_16x16x32_bf16 v[152:155], a[60:63], v[116:119], v[152:155]\n"
      "v_mfma_f32_16x16x32_bf16 v[152:155], a[64:67], v[120:123], v[152:155]\n"
      "s_waitcnt lgkmcnt(3)\n"
      "s_min_u32 s36, s36, s6\n"
      "v_mfma_f32_16x16x32_bf16 v[152:155], a[68:71], v[124:127], v[152:155]\n"
      "buffer_load_dwordx2 v[204:205], v13, s[8:11], s55 offen sc1\n"
      "v_mfma_f32_16x16x32_bf16 v[152:155], a[72:75], v[128:131], v[152:155]\n"
      "s_cmp_lt_u32 s41, 32\n"
      "s_cselect_b32 s56, s39, 0x40000000\n"
      "v_mfma_f32_16x16x32_bf16 v[152:155], a[76:79], v[132:135], v[152:155]\n"
      "s_waitcnt lgkmcnt(2)\n"
      "s_add_u32 s54, s53, 26624\n"
      "v_mfma_f32_16x16x32_bf16 v[152:155], a[80:83], v[136:139], v[152:155]\n"
      "s_waitcnt vmcnt(12) lgkmcnt(1)\n"
      "s_add_u32 s53, s53, 599040\n"
      "v_cmp_ne_u32 vcc, s21, v20\n"
      "v_mfma_f32_16x16x32_bf16 v[152:155], a[84:87], v[140:143], v[152:155]\n"
      "s_waitcnt lgkmcnt(0)\n"
      "buffer_load_dwordx2 v[212:213], v13, s[8:11], s54 offen sc1\n"
      "v_mfma_f32_16x16x32_bf16 v[152:155], a[88:91], v[144:147], v[152:155]\n"
      "v_mfma_f32_16x16x32_bf16 v[152:155], a[92:95], v[148:151], v[152:155]\n"
      "s_add_u32 s39, s39, 147456\n"
      "v_mfma_f32_16x16x32_bf16 v[156:159], a[96:99], v[56:59], 0\n"
      "s_waitcnt vmcnt(12)\n"
      "ds_write_b32 v16, v14\n"
      "v_mfma_f32_16x16x32_bf16 v[156:159], a[100:103], v[60:63], v[156:159]\n"
      "buffer_load_dwordx4 v[208:211], v0, s[8:11], s54 offen sc1\n"
      "v_mfma_f32_16x16x32_bf16 v[156:159], a[104:107], v[64:67], v[156:159]\n"
      "s_cmp_lt_u32 s58, 32\n"
      "s_cselect_b32 s57, s40, 0x40000000\n"
      "v_mfma_f32_16x16x32_bf16 v[156:159], a[108:111], v[68:71], v[156:159]\n"
      "s_cbranch_vccnz .Lsp_d0_13\n"
      ".Lbk_d0_13:\n"
      "v_cvt_pk_bf16_f32 v224, v152, v153\n"
      "v_mfma_f32_16x16x32_bf16 v[156:159], a[112:115], v[72:75], v[156:159]\n"
      "v_cvt_pk_bf16_f32 v225, v154, v155\n"
      "v_mfma_f32_16x16x32_bf16 v[156:159], a[116:119], v[76:79], v[156:159]\n"
      "buffer_load_dwordx4 v0, s[8:11], s51 offen offset:0 sc1 lds\n"
      "v_mfma_f32_16x16x32_bf16 v[156:159], a[120:123], v[80:83], v[156:159]\n"
      "s_waitcnt vmcnt(13)\n"
      "v_lshlrev_b32 v234, 16, v178\n"
      "v_lshlrev_b32 v233, 16, v177\n"
      "v_mfma_f32_16x16x32_bf16 v[156:159], a[124:127], v[84:87], v[156:159]\n"
      "v_lshlrev_b32 v232, 16, v176\n"
      "v_add_f32 v166, v166, v233\n"
      "v_mfma_f32_16x16x32_bf16 v[156:159], a[128:131], v[88:91], v[156:159]\n"
      "s_add_u32 s52, s52, 106496\n"
      "v_add_f32 v164, v164, v232\n"
      "v_mfma_f32_16x16x32_bf16 v[156:159], a[132:135], v[92:95], v[156:159]\n"
      "buffer_load_dwordx4 v0, s[8:11], s51 offen offset:1024 sc1 lds\n"
      "v_mfma_f32_16x16x32_bf16 v[156:159], a[136:139], v[96:99], v[156:159]\n"
      "s_waitcnt vmcnt(13) lgkmcnt(0)\n"
      "v_lshlrev_b32 v233, 16, v181\n"
      "v_lshlrev_b32 v235, 16, v179\n"
      "v_mfma_f32_16x16x32_bf16 v[156:159], a[140:143], v[100:103], v[156:159]\n"
      "v_and_b32 v176, 0xffff0000, v176\n"
      "v_lshlrev_b32 v232, 16, v180\n"
      "v_mfma_f32_16x16x32_bf16 v[156:159], a[144:147], v[104:107], v[156:159]\n"
      "v_add_f32 v172, v172, v232\n"
      "v_and_b32 v181, 0xffff0000, v181\n"
      "v_mfma_f32_16x16x32_bf16 v[156:159], a[148:151], v[108:111], v[156:159]\n"
      "v_add_f32 v168, v168, v234\n"
      "v_add_f32 v174, v174, v233\n"
      "v_mfma_f32_16x16x32_bf16 v[156:159], a[152:155], v[112:115], v[156:159]\n"
      "buffer_load_dword v20, v2, s[8:11], s36 offen sc1\n"
      "v_mfma_f32_16x16x32_bf16 v[156:159], a[156:159], v[116:119], v[156:159]\n"
      "ds_read_b32 v15, v17\n"
      "v_add_f32 v165, v165, v176\n"
      "v_mfma_f32_16x16x32_bf16 v[156:159], a[160:163], v[120:123], v[156:159]\n"
      "buffer_load_dwordx4 v0, s[8:11], s51 offen offset:2048 sc1 lds\n"
      "v_mfma_f32_16x16x32_bf16 v[156:159], a[164:167], v[124:127], v[156:159]\n"
      "s_waitcnt vmcnt(14)\n"
      "v_lshlrev_b32 v232, 16, v184\n"
      "v_and_b32 v177, 0xffff0000, v177\n"
      "v_mfma_f32_16x16x32_bf16 v[156:159], a[168:171], v[128:131], v[156:159]\n"
      "s_add_u32 s40, s40, 4294856704\n"
      "v_add_f32 v167, v167, v177\n"
      "v_and_b32 v180, 0xffff0000, v180\n"
      "v_mfma_f32_16x16x32_bf16 v[156:159], a[172:175], v[132:135], v[156:159]\n"
      "v_add_f32 v164, v164, v232\n"
      "v_lshlrev_b32 v233, 16, v185\n"
      "v_mfma_f32_16x16x32_bf16 v[156:159], a[176:179], v[136:139], v[156:159]\n"
      "buffer_load_dwordx4 v0, s[8:11], s51 offen offset:3072 sc1 lds\n"
      "v_mfma_f32_16x16x32_bf16 v[156:159], a[180:183], v[140:143], v[156:159]\n"
      "s_waitcnt lgkmcnt(0)\n"
      "v_add_f32 v166, v166, v233\n"
      "v_cmp_gt_i32 vcc, v14, v15\n"
      "v_mfma_f32_16x16x32_bf16 v[156:159], a[184:187], v[144:147], v[156:159]\n"
      "s_waitcnt vmcnt(12)\n"
      "v_lshlrev_b32 v232, 16, v188\n"
      "v_add_f32 v172, v172, v232\n"
      "v_mfma_f32_16x16x32_bf16 v[156:159], a[188:191], v[148:151], v[156:159]\n"
      "v_and_b32 v185, 0xffff0000, v185\n"
      "v_lshlrev_b32 v232, 16, v192\n"
      "v_mfma_f32_16x16x32_bf16 v[160:163], a[192:195], v[56:59], 0\n"
      "s_add_u32 m0, s43, 4096\n"
      "v_lshlrev_b32 v234, 16, v186\n"
      "v_add_f32 v164, v164, v232\n"
      "v_mfma_f32_16x16x32_bf16 v[160:163], a[196:199], v[60:63], v[160:163]\n"
      "buffer_load_dwordx4 v1, s[8:11], s51 offen offset:1024 sc1 lds\n"
      "v_mfma_f32_16x16x32_bf16 v[160:163], a[200:203], v[64:67], v[160:163]\n"
      "s_cbranch_vccnz .Lsp_dy5_14\n"
      ".Lbk_dy5_14:\n"
      "v_cvt_pk_bf16_f32 v227, v158, v159\n"
      "v_mfma_f32_16x16x32_bf16 v[160:163], a[204:207], v[68:71], v[160:163]\n"
      "ds_read_b128 v[56:59], v23 offset:24576\n"
      "v_lshlrev_b32 v233, 16, v189\n"
      "v_mfma_f32_16x16x32_bf16 v[160:163], a[208:211], v[72:75], v[160:163]\n"
      "s_waitcnt vmcnt(12)\n"
      "ds_read_b128 v[64:67], v23 offset:26624\n"
      "v_lshlrev_b32 v232, 16, v196\n"
      "v_mfma_f32_16x16x32_bf16 v[160:163], a[212:215], v[76:79], v[160:163]\n"
      "buffer_load_dwordx4 v1, s[8:11], s51 offen offset:0 sc1 lds\n"
      "v_mfma_f32_16x16x32_bf16 v[160:163], a[216:219], v[80:83], v[160:163]\n"
      "ds_read_b128 v[76:79], v23 offset:29696\n"
      "v_add_f32 v174, v174, v233\n"
      "v_mfma_f32_16x16x32_bf16 v[160:163], a[220:223], v[84:87], v[160:163]\n"
      "v_and_b32 v184, 0xffff0000, v184\n"
      "v_add_f32 v172, v172, v232\n"
      "v_mfma_f32_16x16x32_bf16 v[160:163], a[224:227], v[88:91], v[160:163]\n"
      "v_add_f32 v170, v170, v235\n"
      "v_cvt_pk_bf16_f32 v226, v156, v157\n"
      "v_mfma_f32_16x16x32_bf16 v[160:163], a[228:231], v[92:95], v[160:163]\n"
      "s_add_u32 s41, s41, 6\n"
      "ds_read_b128 v[88:91], v23 offset:32768\n"
      "v_add_f32 v165, v165, v184\n"
      "v_mfma_f32_16x16x32_bf16 v[160:163], a[232:235], v[96:99], v[160:163]\n"
      "ds_read_b128 v[68:71], v23 offset:27648\n"
      "v_mfma_f32_16x16x32_bf16 v[160:163], a[236:239], v[100:103], v[160:163]\n"
      "ds_read_b128 v[96:99], v23 offset:34816\n"
      "v_add_f32 v168, v168, v234\n"
      "v_mfma_f32_16x16x32_bf16 v[160:163], a[240:243], v[104:107], v[160:163]\n"
      "ds_read_b128 v[80:83], v23 offset:30720\n"
      "v_lshlrev_b32 v233, 16, v193\n"
      "v_mfma_f32_16x16x32_bf16 v[160:163], a[244:247], v[108:111], v[160:163]\n"
      "ds_read_b128 v[72:75], v23 offset:28672\n"
      "v_and_b32 v178, 0xffff0000, v178\n"
      "v_mfma_f32_16x16x32_bf16 v[160:163], a[248:251], v[112:115], v[160:163]\n"
      "s_waitcnt vmcnt(12) lgkmcnt(5)\n"
      "s_mov_b32 s51, s36\n"
      "ds_read_b128 v[112:115], v23 offset:38912\n"
      "v_lshlrev_b32 v232, 16, v200\n"
      "v_mfma_f32_16x16x32_bf16 v[160:163], a[252:255], v[116:119], v[160:163]\n"
      "v_add_f32 v164, v164, v232\n"
      "v_and_b32 v186, 0xffff0000, v186\n"
      "v_mfma_f32_16x16x32_bf16 v[160:163], v[24:27], v[120:123], v[160:163]\n"
      "s_waitcnt vmcnt(10) lgkmcnt(5)\n"
      "buffer_store_dword v18, v4, s[8:11], s52 offen sc1\n"
      "v_mfma_f32_16x16x32_bf16 v[160:163], v[28:31], v[124:127], v[160:163]\n"
      "ds_read_b128 v[60:63], v23 offset:25600\n"
      "v_add_f32 v169, v169, v178\n"
      "v_mfma_f32_16x16x32_bf16 v[160:163], v[32:35], v[128:131], v[160:163]\n"
      "buffer_store_dwordx4 v[224:227], v0, s[8:11], s37 offen sc1\n"
      "v_mfma_f32_16x16x32_bf16 v[160:163], v[36:39], v[132:135], v[160:163]\n"
      "ds_read_b128 v[104:107], v23 offset:36864\n"
      "v_and_b32 v200, 0xffff0000, v200\n"
      "v_mfma_f32_16x16x32_bf16 v[160:163], v[40:43], v[136:139], v[160:163]\n"
      "ds_read_b128 v[84:87], v23 offset:31744\n"
      "v_add_f32 v173, v173, v180\n"
      "v_mfma_f32_16x16x32_bf16 v[160:163], v[44:47], v[140:143], v[160:163]\n"
      "ds_read_b128 v[140:143], v23 offset:46080\n"
      "v_add_f32 v166, v166, v233\n"
      "v_mfma_f32_16x16x32_bf16 v[160:163], v[48:51], v[144:147], v[160:163]\n"
      "ds_read_b128 v[100:103], v23 offset:35840\n"
      "v_lshlrev_b32_e64 v234, 16, v194\n"
      "v_mfma_f32_16x16x32_bf16 v[160:163], v[52:55], v[148:151], v[160:163]\n"
      "s_sub_u32 s59, s59, 1\n"
      "s_cbranch_scc1 .Ld_out4\n"
      ".Ld_t5:\n"
      "v_mfma_f32_16x16x32_bf16 v[152:155], a[0:3], v[56:59], 0\n"
      "s_waitcnt lgkmcnt(4)\n"
      "ds_read_b128 v[92:95], v23 offset:33792\n"
      "ds_read_b128 v[116:119], v23 offset:39936\n"
      "v_mfma_f32_16x16x32_bf16 v[152:155], a[4:7], v[60:63], v[152:155]\n"
      "ds_read_b128 v[108:111], v23 offset:37888\n"
      "v_and_b32 v192, 0xffff0000, v192\n"
      "v_mfma_f32_16x16x32_bf16 v[152:155], a[8:11], v[64:67], v[152:155]\n"
      "ds_read_b128 v[128:131], v23 offset:43008\n"
      "v_cvt_pk_bf16_f32 v229, v162, v163\n"
      "v_mfma_f32_16x16x32_bf16 v[152:155], a[12:15], v[68:71], v[152:155]\n"
      "ds_read_b128 v[136:139], v23 offset:45056\n"
      "v_add_f32 v175, v175, v181\n"
      "v_mfma_f32_16x16x32_bf16 v[152:155], a[16:19], v[72:75], v[152:155]\n"
      "v_cvt_pk_bf16_f32 v228, v160, v161\n"
      "v_and_b32 v189, 0xffff0000, v189\n"
      "v_mfma_f32_16x16x32_bf16 v[152:155], a[20:23], v[76:79], v[152:155]\n"
      "s_waitcnt vmcnt(11) lgkmcnt(5)\n"
      "ds_read_b128 v[144:147], v23 offset:47104\n"
      "v_lshlrev_b32 v232, 16, v204\n"
      "v_mfma_f32_16x16x32_bf16 v[152:155], a[24:27], v[80:83], v[152:155]\n"
      "buffer_store_dwordx2 v[228:229], v13, s[8:11], s37 offen sc1\n"
      "v_mfma_f32_16x16x32_bf16 v[152:155], a[28:31], v[84:87], v[152:155]\n"
      "v_lshlrev_b32 v235, 16, v187\n"
      "v_add_f32 v172, v172, v232\n"
      "v_mfma_f32_16x16x32_bf16 v[152:155], a[32:35], v[88:91], v[152:155]\n"
      "s_waitcnt vmcnt(10) lgkmcnt(3)\n"
      "ds_read_b128 v[120:123], v23 offset:40960\n"
      "v_lshlrev_b32 v232, 16, v208\n"
      "v_mfma_f32_16x16x32_bf16 v[152:155], a[36:39], v[92:95], v[152:155]\n"
      "ds_read_b128 v[124:127], v23 offset:41984\n"
      "v_add_f32 v170, v170, v235\n"
      "v_mfma_f32_16x16x32_bf16 v[152:155], a[40:43], v[96:99], v[152:155]\n"
      "s_add_u32 m0, s43, 24576\n"
      "v_and_b32 v194, 0xffff0000, v194\n"
      "v_and_b32 v208, 0xffff0000, v208\n"
      "v_mfma_f32_16x16x32_bf16 v[152:155], a[44:47], v[100:103], v[152:155]\n"
      "ds_read_b128 v[132:135], v23 offset:44032\n"
      "v_add_f32 v167, v167, v185\n"
      "v_mfma_f32_16x16x32_bf16 v[152:155], a[48:51], v[104:107], v[152:155]\n"
      "v_lshlrev_b32 v233, 16, v197\n"
      "v_add_u32 v14, 1, v14\n"
      "v_mfma_f32_16x16x32_bf16 v[152:155], a[52:55], v[108:111], v[152:155]\n"
      "s_add_u32 s36, s36, 165888\n"
      "v_lshlrev_b32 v235, 16, v195\n"
      "v_add_f32 v168, v168, v234\n"
      "v_mfma_f32_16x16x32_bf16 v[152:155], a[56:59], v[112:115], v[152:155]\n"
      "buffer_load_dwordx4 v[216:219], v6, s[8:11], s38 offen sc1\n"
      "v_mfma_f32_16x16x32_bf16 v[152:155], a[60:63], v[116:119], v[152:155]\n"
      "s_waitcnt lgkmcnt(2)\n"
      "ds_read_b128 v[148:151], v23 offset:48128\n"
      "v_add_f32 v164, v164, v232\n"
      "v_mfma_f32_16x16x32_bf16 v[152:155], a[64:67], v[120:123], v[152:155]\n"
      "s_waitcnt lgkmcnt(2)\n"
      "v_add_f32 v165, v165, v192\n"
      "v_lshlrev_b32 v234, 16, v202\n"
      "v_mfma_f32_16x16x32_bf16 v[152:155], a[68:71], v[124:127], v[152:155]\n"
      "v_add_f32 v174, v174, v233\n"
      "v_lshlrev_b32 v232, 16, v212\n"
      "v_mfma_f32_16x16x32_bf16 v[152:155], a[72:75], v[128:131], v[152:155]\n"
      "s_waitcnt lgkmcnt(1)\n"
      "v_and_b32 v179, 0xffff0000, v179\n"
      "v_add_f32 v169, v169, v186\n"
      "v_mfma_f32_16x16x32_bf16 v[152:155], a[76:79], v[132:135], v[152:155]\n"
      "s_waitcnt vmcnt(8)\n"
      "v_lshlrev_b32 v233, 16, v201\n"
      "v_cmp_ne_u32 vcc, s21, v20\n"
      "v_mfma_f32_16x16x32_bf16 v[152:155], a[80:83], v[136:139], v[152:155]\n"
      "buffer_load_dwordx2 v[220:221], v7, s[8:11], s38 offen sc1\n"
      "v_mfma_f32_16x16x32_bf16 v[152:155], a[84:87], v[140:143], v[152:155]\n"
      "v_add_f32 v166, v166, v233\n"
      "v_and_b32 v193, 0xffff0000, v193\n"
      "v_mfma_f32_16x16x32_bf16 v[152:155], a[88:91], v[144:147], v[152:155]\n"
      "s_waitcnt lgkmcnt(0)\n"
      "v_and_b32 v197, 0xffff0000, v197\n"
      "v_lshlrev_b32 v233, 16, v205\n"
      "v_mfma_f32_16x16x32_bf16 v[152:155], a[92:95], v[148:151], v[152:155]\n"
      "v_add_f32 v170, v170, v235\n"
      "v_add_f32 v174, v174, v233\n"
      "v_mfma_f32_16x16x32_bf16 v[156:159], a[96:99], v[56:59], 0\n"
      "s_cbranch_vccnz .Lsp_d1_15\n"
      ".Lbk_d1_15:\n"
      "v_lshlrev_b32 v235, 16, v203\n"
      "v_mfma_f32_16x16x32_bf16 v[156:159], a[100:103], v[60:63], v[156:159]\n"
      "buffer_load_dwordx4 v0, s[8:11], s51 offen offset:0 sc1 lds\n"
      "v_mfma_f32_16x16x32_bf16 v[156:159], a[104:107], v[64:67], v[156:159]\n"
      "v_and_b32 v187, 0xffff0000, v187\n"
      "v_lshlrev_b32 v233, 16, v209\n"
      "v_mfma_f32_16x16x32_bf16 v[156:159], a[108:111], v[68:71], v[156:159]\n"
      "v_add_f32 v170, v170, v235\n"
      "v_add_f32 v166, v166, v233\n"
      "v_mfma_f32_16x16x32_bf16 v[156:159], a[112:115], v[72:75], v[156:159]\n"
      "v_and_b32 v201, 0xffff0000, v201\n"
      "v_add_f32 v165, v165, v200\n"
      "v_mfma_f32_16x16x32_bf16 v[156:159], a[116:119], v[76:79], v[156:159]\n"
      "v_and_b32 v188, 0xffff0000, v188\n"
      "v_add_f32 v165, v165, v208\n"
      "v_mfma_f32_16x16x32_bf16 v[156:159], a[120:123], v[80:83], v[156:159]\n"
      "buffer_load_dwordx4 v0, s[8:11], s51 offen offset:1024 sc1 lds\n"
      "v_mfma_f32_16x16x32_bf16 v[156:159], a[124:127], v[84:87], v[156:159]\n"
      "v_add_f32 v168, v168, v234\n"
      "v_mul_f32 v237, v164, v164\n"
      "v_mfma_f32_16x16x32_bf16 v[156:159], a[128:131], v[88:91], v[156:159]\n"
      "v_add_f32 v167, v167, v193\n"
      "v_lshlrev_b32 v233, 16, v213\n"
      "v_mfma_f32_16x16x32_bf16 v[156:159], a[132:135], v[92:95], v[156:159]\n"
      "s_waitcnt vmcnt(7)\n"
      "ds_write_b32 v16, v14\n"
      "v_mfma_f32_16x16x32_bf16 v[156:159], a[136:139], v[96:99], v[156:159]\n"
      "buffer_load_dwordx4 v0, s[8:11], s51 offen offset:2048 sc1 lds\n"
      "v_mfma_f32_16x16x32_bf16 v[156:159], a[140:143], v[100:103], v[156:159]\n"
      "v_add_f32 v171, v171, v179\n"
      "v_add_f32 v167, v167, v201\n"
      "v_mfma_f32_16x16x32_bf16 v[156:159], a[144:147], v[104:107], v[156:159]\n"
      "v_lshlrev_b32 v234, 16, v210\n"
      "v_and_b32 v213, 0xffff0000, v213\n"
      "v_mfma_f32_16x16x32_bf16 v[156:159], a[148:151], v[108:111], v[156:159]\n"
      "s_min_u32 s36, s36, s6\n"
      "v_add_f32 v173, v173, v188\n"
      "v_and_b32 v209, 0xffff0000, v209\n"
      "v_mfma_f32_16x16x32_bf16 v[156:159], a[152:155], v[112:115], v[156:159]\n"
      "buffer_load_dword v20, v2, s[8:11], s36 offen sc1\n"
      "v_mfma_f32_16x16x32_bf16 v[156:159], a[156:159], v[116:119], v[156:159]\n"
      "v_add_f32 v236, v164, v165\n"
      "v_add_f32 v175, v175, v189\n"
      "v_mfma_f32_16x16x32_bf16 v[156:159], a[160:163], v[120:123], v[156:159]\n"
      "buffer_load_dwordx4 v0, s[8:11], s51 offen offset:3072 sc1 lds\n"
      "v_mfma_f32_16x16x32_bf16 v[156:159], a[164:167], v[124:127], v[156:159]\n"
      "v_add_f32 v167, v167, v209\n"
      "v_and_b32 v203, 0xffff0000, v203\n"
      "v_mfma_f32_16x16x32_bf16 v[156:159], a[168:171], v[128:131], v[156:159]\n"
      "ds_read_b32 v15, v17\n"
      "v_fmac_f32 v237, v165, v165\n"
      "v_mfma_f32_16x16x32_bf16 v[156:159], a[172:175], v[132:135], v[156:159]\n"
      "s_add_u32 m0, s43, 28672\n"
      "v_add_f32 v175, v175, v197\n"
      "v_and_b32 v204, 0xffff0000, v204\n"
      "v_mfma_f32_16x16x32_bf16 v[156:159], a[176:179], v[136:139], v[156:159]\n"
      "v_and_b32 v195, 0xffff0000, v195\n"
      "v_add_f32 v172, v172, v232\n"
      "v_mfma_f32_16x16x32_bf16 v[156:159], a[180:183], v[140:143], v[156:159]\n"
      "buffer_load_dwordx4 v1, s[8:11], s51 offen offset:0 sc1 lds\n"
      "v_mfma_f32_16x16x32_bf16 v[156:159], a[184:187], v[144:147], v[156:159]\n"
      "v_fmac_f32 v237, v166, v166\n"
      "v_cvt_pk_bf16_f32 v225, v154, v155\n"
      "v_mfma_f32_16x16x32_bf16 v[156:159], a[188:191], v[148:151], v[156:159]\n"
      "s_waitcnt lgkmcnt(0)\n"
      "v_add_f32 v169, v169, v194\n"
      "v_cmp_gt_i32 vcc, v14, v15\n"
      "v_mfma_f32_16x16x32_bf16 v[160:163], a[192:195], v[56:59], 0\n"
      "v_and_b32 v196, 0xffff0000, v196\n"
      "v_and_b32 v212, 0xffff0000, v212\n"
      "v_mfma_f32_16x16x32_bf16 v[160:163], a[196:199], v[60:63], v[160:163]\n"
      "buffer_load_dwordx4 v1, s[8:11], s51 offen offset:1024 sc1 lds\n"
      "v_mfma_f32_16x16x32_bf16 v[160:163], a[200:203], v[64:67], v[160:163]\n"
      "s_cbranch_vccnz .Lsp_dy0_16\n"
      ".Lbk_dy0_16:\n"
      "v_mfma_f32_16x16x32_bf16 v[160:163], a[204:207], v[68:71], v[160:163]\n"
      "ds_read_b128 v[56:59], v0 offset:0\n"
      "v_cvt_pk_bf16_f32 v177, v166, v167\n"
      "v_mfma_f32_16x16x32_bf16 v[160:163], a[208:211], v[72:75], v[160:163]\n"
      "v_add_f32 v171, v171, v187\n"
      "v_and_b32 v205, 0xffff0000, v205\n"
      "v_mfma_f32_16x16x32_bf16 v[160:163], a[212:215], v[76:79], v[160:163]\n"
      "ds_read_b128 v[60:63], v0 offset:1024\n"
      "v_cvt_pk_bf16_f32 v224, v152, v153\n"
      "v_mfma_f32_16x16x32_bf16 v[160:163], a[216:219], v[80:83], v[160:163]\n"
      "s_add_u32 s52, s52, 106496\n"
      "v_and_b32 v202, 0xffff0000, v202\n"
      "v_add_f32 v169, v169, v202\n"
      "v_mfma_f32_16x16x32_bf16 v[160:163], a[220:223], v[84:87], v[160:163]\n"
      "ds_read_b128 v[84:87], v0 offset:7168\n"
      "v_add_f32 v236, v236, v166\n"
      "v_mfma_f32_16x16x32_bf16 v[160:163], a[224:227], v[88:91], v[160:163]\n"
      "ds_read_b128 v[72:75], v0 offset:4096\n"
      "v_and_b32 v210, 0xffff0000, v210\n"
      "v_mfma_f32_16x16x32_bf16 v[160:163], a[228:231], v[92:95], v[160:163]\n"
      "v_add_f32 v173, v173, v196\n"
      "v_lshlrev_b32 v235, 16, v211\n"
      "v_mfma_f32_16x16x32_bf16 v[160:163], a[232:235], v[96:99], v[160:163]\n"
      "v_add_f32 v170, v170, v235\n"
      "v_add_f32 v171, v171, v195\n"
      "v_mfma_f32_16x16x32_bf16 v[160:163], a[236:239], v[100:103], v[160:163]\n"
      "s_add_u32 s37, s37, 106496\n"
      "ds_read_b128 v[88:91], v0 offset:8192\n"
      "v_cvt_pk_bf16_f32 v227, v158, v159\n"
      "v_mfma_f32_16x16x32_bf16 v[160:163], a[240:243], v[104:107], v[160:163]\n"
      "s_waitcnt vmcnt(9) lgkmcnt(3)\n"
      "buffer_store_dword v18, v4, s[8:11], s52 offen sc1\n"
      "v_mfma_f32_16x16x32_bf16 v[160:163], a[244:247], v[108:111], v[160:163]\n"
      "ds_read_b128 v[68:71], v0 offset:3072\n"
      "v_add_f32 v174, v174, v233\n"
      "v_mfma_f32_16x16x32_bf16 v[160:163], a[248:251], v[112:115], v[160:163]\n"
      "ds_read_b128 v[104:107], v0 offset:12288\n"
      "v_add_f32 v169, v169, v210\n"
      "v_mfma_f32_16x16x32_bf16 v[160:163], a[252:255], v[116:119], v[160:163]\n"
      "ds_read_b128 v[96:99], v0 offset:10240\n"
      "v_fmac_f32 v237, v167, v167\n"
      "v_mfma_f32_16x16x32_bf16 v[160:163], v[24:27], v[120:123], v[160:163]\n"
      "ds_read_b128 v[80:83], v0 offset:6144\n"
      "v_add_f32 v175, v175, v205\n"
      "v_mfma_f32_16x16x32_bf16 v[160:163], v[28:31], v[124:127], v[160:163]\n"
      "ds_read_b128 v[64:67], v0 offset:2048\n"
      "v_add_f32 v168, v168, v234\n"
      "v_mfma_f32_16x16x32_bf16 v[160:163], v[32:35], v[128:131], v[160:163]\n"
      "v_add_f32 v236, v236, v167\n"
      "v_cvt_pk_bf16_f32 v226, v156, v157\n"
      "v_mfma_f32_16x16x32_bf16 v[160:163], v[36:39], v[132:135], v[160:163]\n"
      "ds_read_b128 v[92:95], v0 offset:9216\n"
      "v_cvt_pk_bf16_f32 v176, v164, v165\n"
      "v_mfma_f32_16x16x32_bf16 v[160:163], v[40:43], v[136:139], v[160:163]\n"
      "buffer_store_dwordx4 v[224:227], v0, s[8:11], s37 offen sc1\n"
      "ds_read_b128 v[76:79], v0 offset:5120\n"
      "v_mfma_f32_16x16x32_bf16 v[160:163], v[44:47], v[140:143], v[160:163]\n"
      "ds_read_b128 v[132:135], v0 offset:19456\n"
      "v_and_b32 v211, 0xffff0000, v211\n"
      "v_mfma_f32_16x16x32_bf16 v[160:163], v[48:51], v[144:147], v[160:163]\n"
      "ds_read_b128 v[100:103], v0 offset:11264\n"
      "v_add_f32 v171, v171, v203\n"
      "v_mfma_f32_16x16x32_bf16 v[160:163], v[52:55], v[148:151], v[160:163]\n"
      "s_sub_u32 s59, s59, 1\n"
      "s_cbranch_scc0 .Ld_t0\n"
      ".Ld_out5:\n"
      "s_nop 8\n"
      "v_cvt_pk_bf16_f32 v228, v160, v161\n"
      "v_cvt_pk_bf16_f32 v229, v162, v163\n"
      "buffer_store_dwordx2 v[228:229], v13, s[8:11], s37 offen sc1\n"
      "v_cvt_pk_bf16_f32 v178, v168, v169\n"
      "v_add_f32 v175, v175, v213\n"
      "v_add_f32 v171, v171, v211\n"
      "v_add_f32 v173, v173, v204\n"
      "v_cvt_pk_bf16_f32 v179, v170, v171\n"
      "v_add_f32 v173, v173, v212\n"
      "v_cvt_pk_bf16_f32 v180, v172, v173\n"
      "v_cvt_pk_bf16_f32 v181, v174, v175\n"
      "buffer_store_dwordx2 v[180:181], v9, s[16:19], s57 offen\n"
      "buffer_store_dwordx4 v[176:179], v8, s[16:19], s57 offen\n"
      "s_add_u32 s52, s52, 106496\n"
      "s_add_u32 s37, s37, 173056\n"
      "s_waitcnt vmcnt(2)\n"
      "buffer_store_dword v18, v4, s[8:11], s52 offen sc1\n"
      "s_add_u32 s52, s52, 173056\n"
      "s_branch .Ld_end\n"
      ".Ld_out0:\n"
      "s_nop 8\n"
      "v_add_f32 v174, v174, v162\n"
      "v_add_f32 v170, v170, v158\n"
      "v_lshlrev_b32 v172, 16, v220\n"
      "s_add_u32 s38, s38, 147456\n"
      "v_add_f32 v171, v171, v159\n"
      "v_add_f32 v169, v169, v157\n"
      "v_add_f32 v172, v172, v160\n"
      "v_add_f32 v175, v175, v163\n"
      "v_and_b32 v173, 0xffff0000, v220\n"
      "v_add_f32 v173, v173, v161\n"
      "s_add_u32 s52, s52, 173056\n"
      "buffer_load_dword v21, v3, s[8:11], s53 offen sc1\n"
      "s_add_u32 s55, s53, 6656\n"
      "s_add_u32 s54, s53, 13312\n"
      "s_waitcnt vmcnt(0)\n"
      "v_cmp_ne_u32 vcc, s21, v21\n"
      "s_cbranch_vccnz .Lsp_df_17\n"
      ".Lbk_df_17:\n"
      "buffer_load_dwordx4 v[176:179], v0, s[8:11], s53 offen sc1\n"
      "buffer_load_dwordx2 v[180:181], v13, s[8:11], s53 offen sc1\n"
      "s_add_u32 s58, s41, 4294967264\n"
      "buffer_load_dwordx4 v[184:187], v0, s[8:11], s55 offen sc1\n"
      "buffer_load_dwordx4 v[192:195], v0, s[8:11], s54 offen sc1\n"
      "buffer_load_dwordx2 v[188:189], v13, s[8:11], s55 offen sc1\n"
      "s_add_u32 s55, s53, 19968\n"
      "buffer_load_dwordx2 v[196:197], v13, s[8:11], s54 offen sc1\n"
      "buffer_load_dwordx4 v[200:203], v0, s[8:11], s55 offen sc1\n"
      "buffer_load_dwordx2 v[204:205], v13, s[8:11], s55 offen sc1\n"
      "s_cmp_lt_u32 s41, 32\n"
      "s_cselect_b32 s56, s39, 0x40000000\n"
      "s_add_u32 s54, s53, 26624\n"
      "s_add_u32 s53, s53, 599040\n"
      "buffer_load_dwordx2 v[212:213], v13, s[8:11], s54 offen sc1\n"
      "s_add_u32 s39, s39, 147456\n"
      "buffer_load_dwordx4 v[208:211], v0, s[8:11], s54 offen sc1\n"
      "s_cmp_lt_u32 s58, 32\n"
      "s_cselect_b32 s57, s40, 0x40000000\n"
      "s_waitcnt vmcnt(9)\n"
      "v_lshlrev_b32 v234, 16, v178\n"
      "v_lshlrev_b32 v233, 16, v177\n"
      "v_lshlrev_b32 v232, 16, v176\n"
      "v_add_f32 v166, v166, v233\n"
      "v_add_f32 v164, v164, v232\n"
      "s_waitcnt vmcnt(8)\n"
      "v_lshlrev_b32 v233, 16, v181\n"
      "v_lshlrev_b32 v235, 16, v179\n"
      "v_and_b32 v176, 0xffff0000, v176\n"
      "v_lshlrev_b32 v232, 16, v180\n"
      "v_add_f32 v172, v172, v232\n"
      "v_and_b32 v181, 0xffff0000, v181\n"
      "v_add_f32 v168, v168, v234\n"
      "v_add_f32 v174, v174, v233\n"
      "v_add_f32 v165, v165, v176\n"
      "s_waitcnt vmcnt(7)\n"
      "v_lshlrev_b32 v232, 16, v184\n"
      "v_and_b32 v177, 0xffff0000, v177\n"
      "s_add_u32 s40, s40, 4294856704\n"
      "v_add_f32 v167, v167, v177\n"
      "v_and_b32 v180, 0xffff0000, v180\n"
      "v_add_f32 v164, v164, v232\n"
      "v_lshlrev_b32 v233, 16, v185\n"
      "v_add_f32 v166, v166, v233\n"
      "s_waitcnt vmcnt(5)\n"
      "v_lshlrev_b32 v232, 16, v188\n"
      "v_add_f32 v172, v172, v232\n"
      "v_and_b32 v185, 0xffff0000, v185\n"
      "v_lshlrev_b32 v232, 16, v192\n"
      "v_lshlrev_b32 v234, 16, v186\n"
      "v_add_f32 v164, v164, v232\n"
      "v_lshlrev_b32 v233, 16, v189\n"
      "s_waitcnt vmcnt(4)\n"
      "v_lshlrev_b32 v232, 16, v196\n"
      "v_add_f32 v174, v174, v233\n"
      "v_and_b32 v184, 0xffff0000, v184\n"
      "v_add_f32 v172, v172, v232\n"
      "v_add_f32 v170, v170, v235\n"
      "s_add_u32 s41, s41, 6\n"
      "v_add_f32 v165, v165, v184\n"
      "v_add_f32 v168, v168, v234\n"
      "v_lshlrev_b32 v233, 16, v193\n"
      "v_and_b32 v178, 0xffff0000, v178\n"
      "s_waitcnt vmcnt(3)\n"
      "v_lshlrev_b32 v232, 16, v200\n"
      "v_add_f32 v164, v164, v232\n"
      "v_and_b32 v186, 0xffff0000, v186\n"
      "v_add_f32 v169, v169, v178\n"
      "v_and_b32 v200, 0xffff0000, v200\n"
      "v_add_f32 v173, v173, v180\n"
      "v_add_f32 v166, v166, v233\n"
      "v_lshlrev_b32 v234, 16, v194\n"
      "v_and_b32 v192, 0xffff0000, v192\n"
      "v_add_f32 v175, v175, v181\n"
      "v_and_b32 v189, 0xffff0000, v189\n"
      "s_waitcnt vmcnt(2)\n"
      "v_lshlrev_b32 v232, 16, v204\n"
      "v_lshlrev_b32 v235, 16, v187\n"
      "v_add_f32 v172, v172, v232\n"
      "s_waitcnt vmcnt(0)\n"
      "v_lshlrev_b32 v232, 16, v208\n"
      "v_add_f32 v170, v170, v235\n"
      "v_and_b32 v194, 0xffff0000, v194\n"
      "v_and_b32 v208, 0xffff0000, v208\n"
      "v_add_f32 v167, v167, v185\n"
      "v_lshlrev_b32 v233, 16, v197\n"
      "v_lshlrev_b32 v235, 16, v195\n"
      "v_add_f32 v168, v168, v234\n"
      "v_add_f32 v164, v164, v232\n"
      "v_add_f32 v165, v165, v192\n"
      "v_lshlrev_b32 v234, 16, v202\n"
      "v_add_f32 v174, v174, v233\n"
      "v_lshlrev_b32 v232, 16, v212\n"
      "v_and_b32 v179, 0xffff0000, v179\n"
      "v_add_f32 v169, v169, v186\n"
      "v_lshlrev_b32 v233, 16, v201\n"
      "v_add_f32 v166, v166, v233\n"
      "v_and_b32 v193, 0xffff0000, v193\n"
      "v_and_b32 v197, 0xffff0000, v197\n"
      "v_lshlrev_b32 v233, 16, v205\n"
      "v_add_f32 v170, v170, v235\n"
      "v_add_f32 v174, v174, v233\n"
      "v_lshlrev_b32 v235, 16, v203\n"
      "v_and_b32 v187, 0xffff0000, v187\n"
      "v_lshlrev_b32 v233, 16, v209\n"
      "v_add_f32 v170, v170, v235\n"
      "v_add_f32 v166, v166, v233\n"
      "v_and_b32 v201, 0xffff0000, v201\n"
      "v_add_f32 v165, v165, v200\n"
      "v_and_b32 v188, 0xffff0000, v188\n"
      "v_add_f32 v165, v165, v208\n"
      "v_add_f32 v168, v168, v234\n"
      "v_add_f32 v167, v167, v193\n"
      "v_lshlrev_b32 v233, 16, v213\n"
      "v_add_f32 v171, v171, v179\n"
      "v_add_f32 v167, v167, v201\n"
      "v_lshlrev_b32 v234, 16, v210\n"
      "v_and_b32 v213, 0xffff0000, v213\n"
      "v_add_f32 v173, v173, v188\n"
      "v_and_b32 v209, 0xffff0000, v209\n"
      "v_add_f32 v175, v175, v189\n"
      "v_add_f32 v167, v167, v209\n"
      "v_and_b32 v203, 0xffff0000, v203\n"
      "v_add_f32 v175, v175, v197\n"
      "v_and_b32 v204, 0xffff0000, v204\n"
      "v_and_b32 v195, 0xffff0000, v195\n"
      "v_add_f32 v172, v172, v232\n"
      "v_add_f32 v169, v169, v194\n"
      "v_and_b32 v196, 0xffff0000, v196\n"
      "v_and_b32 v212, 0xffff0000, v212\n"
      "v_cvt_pk_bf16_f32 v177, v166, v167\n"
      "v_add_f32 v171, v171, v187\n"
      "v_and_b32 v205, 0xffff0000, v205\n"
      "v_and_b32 v202, 0xffff0000, v202\n"
      "v_add_f32 v169, v169, v202\n"
      "v_and_b32 v210, 0xffff0000, v210\n"
      "v_add_f32 v173, v173, v196\n"
      "v_lshlrev_b32 v235, 16, v211\n"
      "v_add_f32 v170, v170, v235\n"
      "v_add_f32 v171, v171, v195\n"
      "v_add_f32 v174, v174, v233\n"
      "v_add_f32 v169, v169, v210\n"
      "v_add_f32 v175, v175, v205\n"
      "v_add_f32 v168, v168, v234\n"
      "v_cvt_pk_bf16_f32 v176, v164, v165\n"
      "v_and_b32 v211, 0xffff0000, v211\n"
      "v_add_f32 v171, v171, v203\n"
      "v_cvt_pk_bf16_f32 v178, v168, v169\n"
      "v_add_f32 v175, v175, v213\n"
      "v_add_f32 v171, v171, v211\n"
      "v_add_f32 v173, v173, v204\n"
      "v_cvt_pk_bf16_f32 v179, v170, v171\n"
      "v_add_f32 v173, v173, v212\n"
      "v_cvt_pk_bf16_f32 v180, v172, v173\n"
      "v_cvt_pk_bf16_f32 v181, v174, v175\n"
      "buffer_store_dwordx2 v[180:181], v9, s[16:19], s57 offen\n"
      "buffer_store_dwordx4 v[176:179], v8, s[16:19], s57 offen\n"
      "s_branch .Ld_end\n"
      ".Ld_out1:\n"
      "s_nop 8\n"
      "v_cvt_pk_bf16_f32 v229, v162, v163\n"
      "v_cvt_pk_bf16_f32 v228, v160, v161\n"
      "buffer_store_dwordx2 v[228:229], v13, s[8:11], s37 offen sc1\n"
      "s_waitcnt vmcnt(0)\n"
      "buffer_store_dword v18, v4, s[8:11], s52 offen sc1\n"
      "buffer_load_dword v21, v3, s[8:11], s53 offen sc1\n"
      "s_add_u32 s37, s37, 106496\n"
      "s_add_u32 s52, s52, 106496\n"
      "s_add_u32 s55, s53, 6656\n"
      "s_add_u32 s54, s53, 13312\n"
      "s_waitcnt vmcnt(0)\n"
      "v_cmp_ne_u32 vcc, s21, v21\n"
      "s_cbranch_vccnz .Lsp_df_18\n"
      ".Lbk_df_18:\n"
      "buffer_load_dwordx4 v[176:179], v0, s[8:11], s53 offen sc1\n"
      "buffer_load_dwordx2 v[180:181], v13, s[8:11], s53 offen sc1\n"
      "s_add_u32 s58, s41, 4294967264\n"
      "buffer_load_dwordx4 v[184:187], v0, s[8:11], s55 offen sc1\n"
      "buffer_load_dwordx4 v[192:195], v0, s[8:11], s54 offen sc1\n"
      "buffer_load_dwordx2 v[188:189], v13, s[8:11], s55 offen sc1\n"
      "s_add_u32 s55, s53, 19968\n"
      "buffer_load_dwordx2 v[196:197], v13, s[8:11], s54 offen sc1\n"
      "buffer_load_dwordx4 v[200:203], v0, s[8:11], s55 offen sc1\n"
      "buffer_load_dwordx2 v[204:205], v13, s[8:11], s55 offen sc1\n"
      "s_cmp_lt_u32 s41, 32\n"
      "s_cselect_b32 s56, s39, 0x40000000\n"
      "s_add_u32 s54, s53, 26624\n"
      "s_add_u32 s53, s53, 599040\n"
      "buffer_load_dwordx2 v[212:213], v13, s[8:11], s54 offen sc1\n"
      "s_add_u32 s39, s39, 147456\n"
      "buffer_load_dwordx4 v[208:211], v0, s[8:11], s54 offen sc1\n"
      "s_cmp_lt_u32 s58, 32\n"
      "s_cselect_b32 s57, s40, 0x40000000\n"
      "s_waitcnt vmcnt(9)\n"
      "v_lshlrev_b32 v234, 16, v178\n"
      "v_lshlrev_b32 v233, 16, v177\n"
      "v_lshlrev_b32 v232, 16, v176\n"
      "v_add_f32 v166, v166, v233\n"
      "v_add_f32 v164, v164, v232\n"
      "s_waitcnt vmcnt(8)\n"
      "v_lshlrev_b32 v233, 16, v181\n"
      "v_lshlrev_b32 v235, 16, v179\n"
      "v_and_b32 v176, 0xffff0000, v176\n"
      "v_lshlrev_b32 v232, 16, v180\n"
      "v_add_f32 v172, v172, v232\n"
      "v_and_b32 v181, 0xffff0000, v181\n"
      "v_add_f32 v168, v168, v234\n"
      "v_add_f32 v174, v174, v233\n"
      "v_add_f32 v165, v165, v176\n"
      "s_waitcnt vmcnt(7)\n"
      "v_lshlrev_b32 v232, 16, v184\n"
      "v_and_b32 v177, 0xffff0000, v177\n"
      "s_add_u32 s40, s40, 4294856704\n"
      "v_add_f32 v167, v167, v177\n"
      "v_and_b32 v180, 0xffff0000, v180\n"
      "v_add_f32 v164, v164, v232\n"
      "v_lshlrev_b32 v233, 16, v185\n"
      "v_add_f32 v166, v166, v233\n"
      "s_waitcnt vmcnt(5)\n"
      "v_lshlrev_b32 v232, 16, v188\n"
      "v_add_f32 v172, v172, v232\n"
      "v_and_b32 v185, 0xffff0000, v185\n"
      "v_lshlrev_b32 v232, 16, v192\n"
      "v_lshlrev_b32 v234, 16, v186\n"
      "v_add_f32 v164, v164, v232\n"
      "v_lshlrev_b32 v233, 16, v189\n"
      "s_waitcnt vmcnt(4)\n"
      "v_lshlrev_b32 v232, 16, v196\n"
      "v_add_f32 v174, v174, v233\n"
      "v_and_b32 v184, 0xffff0000, v184\n"
      "v_add_f32 v172, v172, v232\n"
      "v_add_f32 v170, v170, v235\n"
      "s_add_u32 s41, s41, 6\n"
      "v_add_f32 v165, v165, v184\n"
      "v_add_f32 v168, v168, v234\n"
      "v_lshlrev_b32 v233, 16, v193\n"
      "v_and_b32 v178, 0xffff0000, v178\n"
      "s_waitcnt vmcnt(3)\n"
      "v_lshlrev_b32 v232, 16, v200\n"
      "v_add_f32 v164, v164, v232\n"
      "v_and_b32 v186, 0xffff0000, v186\n"
      "v_add_f32 v169, v169, v178\n"
      "v_and_b32 v200, 0xffff0000, v200\n"
      "v_add_f32 v173, v173, v180\n"
      "v_add_f32 v166, v166, v233\n"
      "v_lshlrev_b32 v234, 16, v194\n"
      "v_and_b32 v192, 0xffff0000, v192\n"
      "v_add_f32 v175, v175, v181\n"
      "v_and_b32 v189, 0xffff0000, v189\n"
      "s_waitcnt vmcnt(2)\n"
      "v_lshlrev_b32 v232, 16, v204\n"
      "v_lshlrev_b32 v235, 16, v187\n"
      "v_add_f32 v172, v172, v232\n"
      "s_waitcnt vmcnt(0)\n"
      "v_lshlrev_b32 v232, 16, v208\n"
      "v_add_f32 v170, v170, v235\n"
      "v_and_b32 v194, 0xffff0000, v194\n"
      "v_and_b32 v208, 0xffff0000, v208\n"
      "v_add_f32 v167, v167, v185\n"
      "v_lshlrev_b32 v233, 16, v197\n"
      "v_lshlrev_b32 v235, 16, v195\n"
      "v_add_f32 v168, v168, v234\n"
      "v_add_f32 v164, v164, v232\n"
      "v_add_f32 v165, v165, v192\n"
      "v_lshlrev_b32 v234, 16, v202\n"
      "v_add_f32 v174, v174, v233\n"
      "v_lshlrev_b32 v232, 16, v212\n"
      "v_and_b32 v179, 0xffff0000, v179\n"
      "v_add_f32 v169, v169, v186\n"
      "v_lshlrev_b32 v233, 16, v201\n"
      "v_add_f32 v166, v166, v233\n"
      "v_and_b32 v193, 0xffff0000, v193\n"
      "v_and_b32 v197, 0xffff0000, v197\n"
      "v_lshlrev_b32 v233, 16, v205\n"
      "v_add_f32 v170, v170, v235\n"
      "v_add_f32 v174, v174, v233\n"
      "v_lshlrev_b32 v235, 16, v203\n"
      "v_and_b32 v187, 0xffff0000, v187\n"
      "v_lshlrev_b32 v233, 16, v209\n"
      "v_add_f32 v170, v170, v235\n"
      "v_add_f32 v166, v166, v233\n"
      "v_and_b32 v201, 0xffff0000, v201\n"
      "v_add_f32 v165, v165, v200\n"
      "v_and_b32 v188, 0xffff0000, v188\n"
      "v_add_f32 v165, v165, v208\n"
      "v_add_f32 v168, v168, v234\n"
      "v_add_f32 v167, v167, v193\n"
      "v_lshlrev_b32 v233, 16, v213\n"
      "v_add_f32 v171, v171, v179\n"
      "v_add_f32 v167, v167, v201\n"
      "v_lshlrev_b32 v234, 16, v210\n"
      "v_and_b32 v213, 0xffff0000, v213\n"
      "v_add_f32 v173, v173, v188\n"
      "v_and_b32 v209, 0xffff0000, v209\n"
      "v_add_f32 v175, v175, v189\n"
      "v_add_f32 v167, v167, v209\n"
      "v_and_b32 v203, 0xffff0000, v203\n"
      "v_add_f32 v175, v175, v197\n"
      "v_and_b32 v204, 0xffff0000, v204\n"
      "v_and_b32 v195, 0xffff0000, v195\n"
      "v_add_f32 v172, v172, v232\n"
      "v_add_f32 v169, v169, v194\n"
      "v_and_b32 v196, 0xffff0000, v196\n"
      "v_and_b32 v212, 0xffff0000, v212\n"
      "v_cvt_pk_bf16_f32 v177, v166, v167\n"
      "v_add_f32 v171, v171, v187\n"
      "v_and_b32 v205, 0xffff0000, v205\n"
      "v_and_b32 v202, 0xffff0000, v202\n"
      "v_add_f32 v169, v169, v202\n"
      "v_and_b32 v210, 0xffff0000, v210\n"
      "v_add_f32 v173, v173, v196\n"
      "v_lshlrev_b32 v235, 16, v211\n"
      "v_add_f32 v170, v170, v235\n"
      "v_add_f32 v171, v171, v195\n"
      "v_add_f32 v174, v174, v233\n"
      "v_add_f32 v169, v169, v210\n"
      "v_add_f32 v175, v175, v205\n"
      "v_add_f32 v168, v168, v234\n"
      "v_cvt_pk_bf16_f32 v176, v164, v165\n"
      "v_and_b32 v211, 0xffff0000, v211\n"
      "v_add_f32 v171, v171, v203\n"
      "v_cvt_pk_bf16_f32 v178, v168, v169\n"
      "v_add_f32 v175, v175, v213\n"
      "v_add_f32 v171, v171, v211\n"
      "v_add_f32 v173, v173, v204\n"
      "v_cvt_pk_bf16_f32 v179, v170, v171\n"
      "v_add_f32 v173, v173, v212\n"
      "v_cvt_pk_bf16_f32 v180, v172, v173\n"
      "v_cvt_pk_bf16_f32 v181, v174, v175\n"
      "buffer_store_dwordx2 v[180:181], v9, s[16:19], s57 offen\n"
      "buffer_store_dwordx4 v[176:179], v8, s[16:19], s57 offen\n"
      "s_branch .Ld_end\n"
      ".Ld_out2:\n"
      "s_nop 8\n"
      "v_cvt_pk_bf16_f32 v229, v162, v163\n"
      "v_cvt_pk_bf16_f32 v228, v160, v161\n"
      "buffer_store_dwordx2 v[228:229], v13, s[8:11], s37 offen sc1\n"
      "s_add_u32 s52, s52, 106496\n"
      "s_add_u32 s55, s53, 6656\n"
      "s_add_u32 s54, s53, 13312\n"
      "s_waitcnt vmcnt(2)\n"
      "v_cmp_ne_u32 vcc, s21, v21\n"
      "s_cbranch_vccnz .Lsp_df_19\n"
      ".Lbk_df_19:\n"
      "buffer_load_dwordx4 v[176:179], v0, s[8:11], s53 offen sc1\n"
      "buffer_load_dwordx2 v[180:181], v13, s[8:11], s53 offen sc1\n"
      "s_add_u32 s58, s41, 4294967264\n"
      "buffer_load_dwordx4 v[184:187], v0, s[8:11], s55 offen sc1\n"
      "s_add_u32 s37, s37, 106496\n"
      "s_waitcnt vmcnt(3)\n"
      "buffer_store_dword v18, v4, s[8:11], s52 offen sc1\n"
      "buffer_load_dwordx4 v[192:195], v0, s[8:11], s54 offen sc1\n"
      "buffer_load_dwordx2 v[188:189], v13, s[8:11], s55 offen sc1\n"
      "s_add_u32 s55, s53, 19968\n"
      "buffer_load_dwordx2 v[196:197], v13, s[8:11], s54 offen sc1\n"
      "buffer_load_dwordx4 v[200:203], v0, s[8:11], s55 offen sc1\n"
      "buffer_load_dwordx2 v[204:205], v13, s[8:11], s55 offen sc1\n"
      "s_cmp_lt_u32 s41, 32\n"
      "s_cselect_b32 s56, s39, 0x40000000\n"
      "s_add_u32 s54, s53, 26624\n"
      "s_add_u32 s53, s53, 599040\n"
      "buffer_load_dwordx2 v[212:213], v13, s[8:11], s54 offen sc1\n"
      "s_add_u32 s39, s39, 147456\n"
      "buffer_load_dwordx4 v[208:211], v0, s[8:11], s54 offen sc1\n"
      "s_cmp_lt_u32 s58, 32\n"
      "s_cselect_b32 s57, s40, 0x40000000\n"
      "s_waitcnt vmcnt(10)\n"
      "v_lshlrev_b32 v234, 16, v178\n"
      "v_lshlrev_b32 v233, 16, v177\n"
      "v_lshlrev_b32 v232, 16, v176\n"
      "v_add_f32 v166, v166, v233\n"
      "s_add_u32 s52, s52, 106496\n"
      "v_add_f32 v164, v164, v232\n"
      "s_waitcnt vmcnt(9)\n"
      "v_lshlrev_b32 v233, 16, v181\n"
      "v_lshlrev_b32 v235, 16, v179\n"
      "v_and_b32 v176, 0xffff0000, v176\n"
      "v_lshlrev_b32 v232, 16, v180\n"
      "v_add_f32 v172, v172, v232\n"
      "v_and_b32 v181, 0xffff0000, v181\n"
      "v_add_f32 v168, v168, v234\n"
      "v_add_f32 v174, v174, v233\n"
      "v_add_f32 v165, v165, v176\n"
      "s_waitcnt vmcnt(8)\n"
      "v_lshlrev_b32 v232, 16, v184\n"
      "v_and_b32 v177, 0xffff0000, v177\n"
      "s_add_u32 s40, s40, 4294856704\n"
      "v_add_f32 v167, v167, v177\n"
      "v_and_b32 v180, 0xffff0000, v180\n"
      "v_add_f32 v164, v164, v232\n"
      "v_lshlrev_b32 v233, 16, v185\n"
      "v_add_f32 v166, v166, v233\n"
      "s_waitcnt vmcnt(5)\n"
      "v_lshlrev_b32 v232, 16, v188\n"
      "v_add_f32 v172, v172, v232\n"
      "v_and_b32 v185, 0xffff0000, v185\n"
      "v_lshlrev_b32 v232, 16, v192\n"
      "v_lshlrev_b32 v234, 16, v186\n"
      "v_add_f32 v164, v164, v232\n"
      "v_lshlrev_b32 v233, 16, v189\n"
      "s_waitcnt vmcnt(4)\n"
      "v_lshlrev_b32 v232, 16, v196\n"
      "v_add_f32 v174, v174, v233\n"
      "v_and_b32 v184, 0xffff0000, v184\n"
      "v_add_f32 v172, v172, v232\n"
      "v_add_f32 v170, v170, v235\n"
      "s_add_u32 s41, s41, 6\n"
      "v_add_f32 v165, v165, v184\n"
      "v_add_f32 v168, v168, v234\n"
      "v_lshlrev_b32 v233, 16, v193\n"
      "v_and_b32 v178, 0xffff0000, v178\n"
      "s_waitcnt vmcnt(3)\n"
      "v_lshlrev_b32 v232, 16, v200\n"
      "v_add_f32 v164, v164, v232\n"
      "v_and_b32 v186, 0xffff0000, v186\n"
      "v_add_f32 v169, v169, v178\n"
      "v_and_b32 v200, 0xffff0000, v200\n"
      "v_add_f32 v173, v173, v180\n"
      "v_add_f32 v166, v166, v233\n"
      "v_lshlrev_b32 v234, 16, v194\n"
      "v_and_b32 v192, 0xffff0000, v192\n"
      "v_add_f32 v175, v175, v181\n"
      "v_and_b32 v189, 0xffff0000, v189\n"
      "s_waitcnt vmcnt(2)\n"
      "v_lshlrev_b32 v232, 16, v204\n"
      "v_lshlrev_b32 v235, 16, v187\n"
      "v_add_f32 v172, v172, v232\n"
      "s_waitcnt vmcnt(0)\n"
      "v_lshlrev_b32 v232, 16, v208\n"
      "v_add_f32 v170, v170, v235\n"
      "v_and_b32 v194, 0xffff0000, v194\n"
      "v_and_b32 v208, 0xffff0000, v208\n"
      "v_add_f32 v167, v167, v185\n"
      "v_lshlrev_b32 v233, 16, v197\n"
      "v_lshlrev_b32 v235, 16, v195\n"
      "v_add_f32 v168, v168, v234\n"
      "v_add_f32 v164, v164, v232\n"
      "v_add_f32 v165, v165, v192\n"
      "v_lshlrev_b32 v234, 16, v202\n"
      "v_add_f32 v174, v174, v233\n"
      "v_lshlrev_b32 v232, 16, v212\n"
      "v_and_b32 v179, 0xffff0000, v179\n"
      "v_add_f32 v169, v169, v186\n"
      "v_lshlrev_b32 v233, 16, v201\n"
      "v_add_f32 v166, v166, v233\n"
      "v_and_b32 v193, 0xffff0000, v193\n"
      "v_and_b32 v197, 0xffff0000, v197\n"
      "v_lshlrev_b32 v233, 16, v205\n"
      "v_add_f32 v170, v170, v235\n"
      "v_add_f32 v174, v174, v233\n"
      "v_lshlrev_b32 v235, 16, v203\n"
      "v_and_b32 v187, 0xffff0000, v187\n"
      "v_lshlrev_b32 v233, 16, v209\n"
      "v_add_f32 v170, v170, v235\n"
      "v_add_f32 v166, v166, v233\n"
      "v_and_b32 v201, 0xffff0000, v201\n"
      "v_add_f32 v165, v165, v200\n"
      "v_and_b32 v188, 0xffff0000, v188\n"
      "v_add_f32 v165, v165, v208\n"
      "v_add_f32 v168, v168, v234\n"
      "v_add_f32 v167, v167, v193\n"
      "v_lshlrev_b32 v233, 16, v213\n"
      "v_add_f32 v171, v171, v179\n"
      "v_add_f32 v167, v167, v201\n"
      "v_lshlrev_b32 v234, 16, v210\n"
      "v_and_b32 v213, 0xffff0000, v213\n"
      "v_add_f32 v173, v173, v188\n"
      "v_and_b32 v209, 0xffff0000, v209\n"
      "v_add_f32 v175, v175, v189\n"
      "v_add_f32 v167, v167, v209\n"
      "v_and_b32 v203, 0xffff0000, v203\n"
      "v_add_f32 v175, v175, v197\n"
      "v_and_b32 v204, 0xffff0000, v204\n"
      "v_and_b32 v195, 0xffff0000, v195\n"
      "v_add_f32 v172, v172, v232\n"
      "v_add_f32 v169, v169, v194\n"
      "v_and_b32 v196, 0xffff0000, v196\n"
      "v_and_b32 v212, 0xffff0000, v212\n"
      "v_cvt_pk_bf16_f32 v177, v166, v167\n"
      "v_add_f32 v171, v171, v187\n"
      "v_and_b32 v205, 0xffff0000, v205\n"
      "v_and_b32 v202, 0xffff0000, v202\n"
      "v_add_f32 v169, v169, v202\n"
      "v_and_b32 v210, 0xffff0000, v210\n"
      "v_add_f32 v173, v173, v196\n"
      "v_lshlrev_b32 v235, 16, v211\n"
      "v_add_f32 v170, v170, v235\n"
      "v_add_f32 v171, v171, v195\n"
      "v_add_f32 v174, v174, v233\n"
      "v_add_f32 v169, v169, v210\n"
      "v_add_f32 v175, v175, v205\n"
      "v_add_f32 v168, v168, v234\n"
      "v_cvt_pk_bf16_f32 v176, v164, v165\n"
      "v_and_b32 v211, 0xffff0000, v211\n"
      "v_add_f32 v171, v171, v203\n"
      "v_cvt_pk_bf16_f32 v178, v168, v169\n"
      "v_add_f32 v175, v175, v213\n"
      "v_add_f32 v171, v171, v211\n"
      "v_add_f32 v173, v173, v204\n"
      "v_cvt_pk_bf16_f32 v179, v170, v171\n"
      "v_add_f32 v173, v173, v212\n"
      "v_cvt_pk_bf16_f32 v180, v172, v173\n"
      "v_cvt_pk_bf16_f32 v181, v174, v175\n"
      "buffer_store_dwordx2 v[180:181], v9, s[16:19], s57 offen\n"
      "buffer_store_dwordx4 v[176:179], v8, s[16:19], s57 offen\n"
      "s_branch .Ld_end\n"
      ".Ld_out3:\n"
      "s_nop 8\n"
      "v_cvt_pk_bf16_f32 v228, v160, v161\n"
      "buffer_load_dwordx4 v[200:203], v0, s[8:11], s55 offen sc1\n"
      "v_cvt_pk_bf16_f32 v227, v158, v159\n"
      "buffer_store_dwordx4 v[224:227], v0, s[8:11], s37 offen sc1\n"
      "v_cvt_pk_bf16_f32 v229, v162, v163\n"
      "buffer_store_dwordx2 v[228:229], v13, s[8:11], s37 offen sc1\n"
      "s_add_u32 s37, s37, 106496\n"
      "buffer_load_dwordx2 v[204:205], v13, s[8:11], s55 offen sc1\n"
      "s_cmp_lt_u32 s41, 32\n"
      "s_cselect_b32 s56, s39, 0x40000000\n"
      "s_add_u32 s54, s53, 26624\n"
      "s_add_u32 s53, s53, 599040\n"
      "buffer_load_dwordx2 v[212:213], v13, s[8:11], s54 offen sc1\n"
      "s_add_u32 s39, s39, 147456\n"
      "buffer_load_dwordx4 v[208:211], v0, s[8:11], s54 offen sc1\n"
      "s_cmp_lt_u32 s58, 32\n"
      "s_cselect_b32 s57, s40, 0x40000000\n"
      "s_waitcnt vmcnt(12)\n"
      "v_lshlrev_b32 v234, 16, v178\n"
      "v_lshlrev_b32 v233, 16, v177\n"
      "v_lshlrev_b32 v232, 16, v176\n"
      "v_add_f32 v166, v166, v233\n"
      "s_add_u32 s52, s52, 106496\n"
      "v_add_f32 v164, v164, v232\n"
      "s_waitcnt vmcnt(11)\n"
      "v_lshlrev_b32 v233, 16, v181\n"
      "v_lshlrev_b32 v235, 16, v179\n"
      "v_and_b32 v176, 0xffff0000, v176\n"
      "v_lshlrev_b32 v232, 16, v180\n"
      "v_add_f32 v172, v172, v232\n"
      "v_and_b32 v181, 0xffff0000, v181\n"
      "v_add_f32 v168, v168, v234\n"
      "v_add_f32 v174, v174, v233\n"
      "v_add_f32 v165, v165, v176\n"
      "s_waitcnt vmcnt(10)\n"
      "v_lshlrev_b32 v232, 16, v184\n"
      "v_and_b32 v177, 0xffff0000, v177\n"
      "s_add_u32 s40, s40, 4294856704\n"
      "v_add_f32 v167, v167, v177\n"
      "v_and_b32 v180, 0xffff0000, v180\n"
      "v_add_f32 v164, v164, v232\n"
      "v_lshlrev_b32 v233, 16, v185\n"
      "v_add_f32 v166, v166, v233\n"
      "s_waitcnt vmcnt(7)\n"
      "v_lshlrev_b32 v232, 16, v188\n"
      "v_add_f32 v172, v172, v232\n"
      "v_and_b32 v185, 0xffff0000, v185\n"
      "v_lshlrev_b32 v232, 16, v192\n"
      "v_lshlrev_b32 v234, 16, v186\n"
      "v_add_f32 v164, v164, v232\n"
      "v_lshlrev_b32 v233, 16, v189\n"
      "s_waitcnt vmcnt(6)\n"
      "v_lshlrev_b32 v232, 16, v196\n"
      "v_add_f32 v174, v174, v233\n"
      "v_and_b32 v184, 0xffff0000, v184\n"
      "v_add_f32 v172, v172, v232\n"
      "v_add_f32 v170, v170, v235\n"
      "s_add_u32 s41, s41, 6\n"
      "v_add_f32 v165, v165, v184\n"
      "v_add_f32 v168, v168, v234\n"
      "v_lshlrev_b32 v233, 16, v193\n"
      "v_and_b32 v178, 0xffff0000, v178\n"
      "s_waitcnt vmcnt(5)\n"
      "v_lshlrev_b32 v232, 16, v200\n"
      "v_add_f32 v164, v164, v232\n"
      "v_and_b32 v186, 0xffff0000, v186\n"
      "s_waitcnt vmcnt(3)\n"
      "buffer_store_dword v18, v4, s[8:11], s52 offen sc1\n"
      "v_add_f32 v169, v169, v178\n"
      "v_and_b32 v200, 0xffff0000, v200\n"
      "v_add_f32 v173, v173, v180\n"
      "v_add_f32 v166, v166, v233\n"
      "v_lshlrev_b32 v234, 16, v194\n"
      "v_and_b32 v192, 0xffff0000, v192\n"
      "v_add_f32 v175, v175, v181\n"
      "v_and_b32 v189, 0xffff0000, v189\n"
      "s_waitcnt vmcnt(3)\n"
      "v_lshlrev_b32 v232, 16, v204\n"
      "v_lshlrev_b32 v235, 16, v187\n"
      "v_add_f32 v172, v172, v232\n"
      "s_waitcnt vmcnt(1)\n"
      "v_lshlrev_b32 v232, 16, v208\n"
      "v_add_f32 v170, v170, v235\n"
      "v_and_b32 v194, 0xffff0000, v194\n"
      "v_and_b32 v208, 0xffff0000, v208\n"
      "v_add_f32 v167, v167, v185\n"
      "v_lshlrev_b32 v233, 16, v197\n"
      "v_lshlrev_b32 v235, 16, v195\n"
      "v_add_f32 v168, v168, v234\n"
      "v_add_f32 v164, v164, v232\n"
      "v_add_f32 v165, v165, v192\n"
      "v_lshlrev_b32 v234, 16, v202\n"
      "v_add_f32 v174, v174, v233\n"
      "v_lshlrev_b32 v232, 16, v212\n"
      "v_and_b32 v179, 0xffff0000, v179\n"
      "v_add_f32 v169, v169, v186\n"
      "v_lshlrev_b32 v233, 16, v201\n"
      "v_add_f32 v166, v166, v233\n"
      "v_and_b32 v193, 0xffff0000, v193\n"
      "v_and_b32 v197, 0xffff0000, v197\n"
      "v_lshlrev_b32 v233, 16, v205\n"
      "v_add_f32 v170, v170, v235\n"
      "v_add_f32 v174, v174, v233\n"
      "v_lshlrev_b32 v235, 16, v203\n"
      "v_and_b32 v187, 0xffff0000, v187\n"
      "v_lshlrev_b32 v233, 16, v209\n"
      "v_add_f32 v170, v170, v235\n"
      "v_add_f32 v166, v166, v233\n"
      "v_and_b32 v201, 0xffff0000, v201\n"
      "v_add_f32 v165, v165, v200\n"
      "v_and_b32 v188, 0xffff0000, v188\n"
      "v_add_f32 v165, v165, v208\n"
      "v_add_f32 v168, v168, v234\n"
      "v_add_f32 v167, v167, v193\n"
      "v_lshlrev_b32 v233, 16, v213\n"
      "v_add_f32 v171, v171, v179\n"
      "v_add_f32 v167, v167, v201\n"
      "v_lshlrev_b32 v234, 16, v210\n"
      "v_and_b32 v213, 0xffff0000, v213\n"
      "v_add_f32 v173, v173, v188\n"
      "v_and_b32 v209, 0xffff0000, v209\n"
      "v_add_f32 v175, v175, v189\n"
      "v_add_f32 v167, v167, v209\n"
      "v_and_b32 v203, 0xffff0000, v203\n"
      "v_add_f32 v175, v175, v197\n"
      "v_and_b32 v204, 0xffff0000, v204\n"
      "v_and_b32 v195, 0xffff0000, v195\n"
      "v_add_f32 v172, v172, v232\n"
      "v_add_f32 v169, v169, v194\n"
      "v_and_b32 v196, 0xffff0000, v196\n"
      "v_and_b32 v212, 0xffff0000, v212\n"
      "v_cvt_pk_bf16_f32 v177, v166, v167\n"
      "v_add_f32 v171, v171, v187\n"
      "v_and_b32 v205, 0xffff0000, v205\n"
      "s_add_u32 s52, s52, 106496\n"
      "v_and_b32 v202, 0xffff0000, v202\n"
      "v_add_f32 v169, v169, v202\n"
      "v_and_b32 v210, 0xffff0000, v210\n"
      "v_add_f32 v173, v173, v196\n"
      "v_lshlrev_b32 v235, 16, v211\n"
      "v_add_f32 v170, v170, v235\n"
      "v_add_f32 v171, v171, v195\n"
      "v_add_f32 v174, v174, v233\n"
      "v_add_f32 v169, v169, v210\n"
      "v_add_f32 v175, v175, v205\n"
      "v_add_f32 v168, v168, v234\n"
      "v_cvt_pk_bf16_f32 v176, v164, v165\n"
      "v_and_b32 v211, 0xffff0000, v211\n"
      "v_add_f32 v171, v171, v203\n"
      "v_cvt_pk_bf16_f32 v178, v168, v169\n"
      "v_add_f32 v175, v175, v213\n"
      "v_add_f32 v171, v171, v211\n"
      "v_add_f32 v173, v173, v204\n"
      "v_cvt_pk_bf16_f32 v179, v170, v171\n"
      "v_add_f32 v173, v173, v212\n"
      "v_cvt_pk_bf16_f32 v180, v172, v173\n"
      "v_cvt_pk_bf16_f32 v181, v174, v175\n"
      "buffer_store_dwordx2 v[180:181], v9, s[16:19], s57 offen\n"
      "buffer_store_dwordx4 v[176:179], v8, s[16:19], s57 offen\n"
      "s_branch .Ld_end\n"
      ".Ld_out4:\n"
      "v_and_b32 v192, 0xffff0000, v192\n"
      "s_nop 7\n"
      "v_cvt_pk_bf16_f32 v229, v162, v163\n"
      "v_add_f32 v175, v175, v181\n"
      "v_cvt_pk_bf16_f32 v228, v160, v161\n"
      "v_and_b32 v189, 0xffff0000, v189\n"
      "s_waitcnt vmcnt(11)\n"
      "v_lshlrev_b32 v232, 16, v204\n"
      "buffer_store_dwordx2 v[228:229], v13, s[8:11], s37 offen sc1\n"
      "v_lshlrev_b32 v235, 16, v187\n"
      "v_add_f32 v172, v172, v232\n"
      "s_waitcnt vmcnt(10)\n"
      "v_lshlrev_b32 v232, 16, v208\n"
      "v_add_f32 v170, v170, v235\n"
      "v_and_b32 v194, 0xffff0000, v194\n"
      "v_and_b32 v208, 0xffff0000, v208\n"
      "v_add_f32 v167, v167, v185\n"
      "v_lshlrev_b32 v233, 16, v197\n"
      "v_lshlrev_b32 v235, 16, v195\n"
      "v_add_f32 v168, v168, v234\n"
      "v_add_f32 v164, v164, v232\n"
      "v_add_f32 v165, v165, v192\n"
      "v_lshlrev_b32 v234, 16, v202\n"
      "v_add_f32 v174, v174, v233\n"
      "v_lshlrev_b32 v232, 16, v212\n"
      "v_and_b32 v179, 0xffff0000, v179\n"
      "v_add_f32 v169, v169, v186\n"
      "v_lshlrev_b32 v233, 16, v201\n"
      "v_add_f32 v166, v166, v233\n"
      "v_and_b32 v193, 0xffff0000, v193\n"
      "v_and_b32 v197, 0xffff0000, v197\n"
      "v_lshlrev_b32 v233, 16, v205\n"
      "v_add_f32 v170, v170, v235\n"
      "v_add_f32 v174, v174, v233\n"
      "v_lshlrev_b32 v235, 16, v203\n"
      "v_and_b32 v187, 0xffff0000, v187\n"
      "v_lshlrev_b32 v233, 16, v209\n"
      "v_add_f32 v170, v170, v235\n"
      "v_add_f32 v166, v166, v233\n"
      "v_and_b32 v201, 0xffff0000, v201\n"
      "v_add_f32 v165, v165, v200\n"
      "v_and_b32 v188, 0xffff0000, v188\n"
      "v_add_f32 v165, v165, v208\n"
      "v_add_f32 v168, v168, v234\n"
      "v_add_f32 v167, v167, v193\n"
      "v_lshlrev_b32 v233, 16, v213\n"
      "v_add_f32 v171, v171, v179\n"
      "v_add_f32 v167, v167, v201\n"
      "v_lshlrev_b32 v234, 16, v210\n"
      "v_and_b32 v213, 0xffff0000, v213\n"
      "v_add_f32 v173, v173, v188\n"
      "v_and_b32 v209, 0xffff0000, v209\n"
      "v_add_f32 v175, v175, v189\n"
      "v_add_f32 v167, v167, v209\n"
      "v_and_b32 v203, 0xffff0000, v203\n"
      "v_add_f32 v175, v175, v197\n"
      "v_and_b32 v204, 0xffff0000, v204\n"
      "v_and_b32 v195, 0xffff0000, v195\n"
      "v_add_f32 v172, v172, v232\n"
      "v_add_f32 v169, v169, v194\n"
      "v_and_b32 v196, 0xffff0000, v196\n"
      "v_and_b32 v212, 0xffff0000, v212\n"
      "v_cvt_pk_bf16_f32 v177, v166, v167\n"
      "v_add_f32 v171, v171, v187\n"
      "v_and_b32 v205, 0xffff0000, v205\n"
      "s_add_u32 s52, s52, 106496\n"
      "v_and_b32 v202, 0xffff0000, v202\n"
      "v_add_f32 v169, v169, v202\n"
      "v_and_b32 v210, 0xffff0000, v210\n"
      "v_add_f32 v173, v173, v196\n"
      "v_lshlrev_b32 v235, 16, v211\n"
      "v_add_f32 v170, v170, v235\n"
      "v_add_f32 v171, v171, v195\n"
      "s_add_u32 s37, s37, 106496\n"
      "s_waitcnt vmcnt(0)\n"
      "buffer_store_dword v18, v4, s[8:11], s52 offen sc1\n"
      "v_add_f32 v174, v174, v233\n"
      "v_add_f32 v169, v169, v210\n"
      "v_add_f32 v175, v175, v205\n"
      "v_add_f32 v168, v168, v234\n"
      "v_cvt_pk_bf16_f32 v176, v164, v165\n"
      "v_and_b32 v211, 0xffff0000, v211\n"
      "v_add_f32 v171, v171, v203\n"
      "v_cvt_pk_bf16_f32 v178, v168, v169\n"
      "v_add_f32 v175, v175, v213\n"
      "v_add_f32 v171, v171, v211\n"
      "v_add_f32 v173, v173, v204\n"
      "v_cvt_pk_bf16_f32 v179, v170, v171\n"
      "v_add_f32 v173, v173, v212\n"
      "v_cvt_pk_bf16_f32 v180, v172, v173\n"
      "v_cvt_pk_bf16_f32 v181, v174, v175\n"
      "buffer_store_dwordx2 v[180:181], v9, s[16:19], s57 offen\n"
      "buffer_store_dwordx4 v[176:179], v8, s[16:19], s57 offen\n"
      "s_add_u32 s52, s52, 106496\n"
      "s_branch .Ld_end\n"
      ".Ld_in1:\n"
      "s_add_u32 s36, s36, 165888\n"
      "s_min_u32 s36, s36, s6\n"
      "s_mov_b32 s51, s36\n"
      "s_add_u32 s36, s36, 165888\n"
      "s_min_u32 s36, s36, s6\n"
      "buffer_load_dword v20, v2, s[8:11], s36 offen sc1\n"
      "s_mov_b32 s51, s36\n"
      "s_add_u32 m0, s43, 24576\n"
      "s_add_u32 s36, s36, 165888\n"
      "s_waitcnt vmcnt(0)\n"
      "v_cmp_ne_u32 vcc, s21, v20\n"
      "s_cbranch_vccnz .Lsp_d1_20\n"
      ".Lbk_d1_20:\n"
      "buffer_load_dwordx4 v0, s[8:11], s51 offen offset:0 sc1 lds\n"
      "buffer_load_dwordx4 v0, s[8:11], s51 offen offset:1024 sc1 lds\n"
      "buffer_load_dwordx4 v0, s[8:11], s51 offen offset:2048 sc1 lds\n"
      "s_min_u32 s36, s36, s6\n"
      "buffer_load_dword v20, v2, s[8:11], s36 offen sc1\n"
      "buffer_load_dwordx4 v0, s[8:11], s51 offen offset:3072 sc1 lds\n"
      "s_add_u32 m0, s43, 28672\n"
      "s_nop 0\n"
      "buffer_load_dwordx4 v1, s[8:11], s51 offen offset:0 sc1 lds\n"
      "buffer_load_dwordx4 v1, s[8:11], s51 offen offset:1024 sc1 lds\n"
      "s_add_u32 m0, s43, 49152\n"
      "s_mov_b32 s51, s36\n"
      "v_add_u32 v14, 1, v14\n"
      "s_waitcnt vmcnt(3)\n"
      "v_cmp_ne_u32 vcc, s21, v20\n"
      "s_add_u32 s36, s36, 165888\n"
      "s_min_u32 s36, s36, s6\n"
      "s_cbranch_vccnz .Lsp_d2_21\n"
      ".Lbk_d2_21:\n"
      "s_waitcnt vmcnt(0)\n"
      "ds_write_b32 v16, v14\n"
      "buffer_load_dword v20, v2, s[8:11], s36 offen sc1\n"
      "buffer_load_dwordx4 v0, s[8:11], s51 offen offset:3072 sc1 lds\n"
      "s_add_u32 s52, s52, 106496\n"
      "s_add_u32 s37, s37, 173056\n"
      "buffer_load_dwordx4 v0, s[8:11], s51 offen offset:1024 sc1 lds\n"
      "ds_read_b32 v15, v17\n"
      "buffer_load_dwordx4 v0, s[8:11], s51 offen offset:2048 sc1 lds\n"
      "s_waitcnt lgkmcnt(0)\n"
      "v_cmp_gt_i32 vcc, v14, v15\n"
      "buffer_load_dwordx4 v0, s[8:11], s51 offen offset:0 sc1 lds\n"
      "s_add_u32 m0, s43, 53248\n"
      "s_cbranch_vccnz .Lsp_dy1_22\n"
      ".Lbk_dy1_22:\n"
      "buffer_load_dwordx4 v1, s[8:11], s51 offen offset:0 sc1 lds\n"
      "ds_read_b128 v[56:59], v0 offset:24576\n"
      "ds_read_b128 v[64:67], v0 offset:26624\n"
      "buffer_load_dwordx4 v1, s[8:11], s51 offen offset:1024 sc1 lds\n"
      "ds_read_b128 v[68:71], v0 offset:27648\n"
      "ds_read_b128 v[80:83], v0 offset:30720\n"
      "ds_read_b128 v[60:63], v0 offset:25600\n"
      "ds_read_b128 v[92:95], v0 offset:33792\n"
      "ds_read_b128 v[84:87], v0 offset:31744\n"
      "ds_read_b128 v[72:75], v0 offset:28672\n"
      "ds_read_b128 v[76:79], v0 offset:29696\n"
      "ds_read_b128 v[100:103], v0 offset:35840\n"
      "ds_read_b128 v[96:99], v0 offset:34816\n"
      "ds_read_b128 v[116:119], v0 offset:39936\n"
      "ds_read_b128 v[120:123], v0 offset:40960\n"
      "ds_read_b128 v[108:111], v0 offset:37888\n"
      "ds_read_b128 v[88:91], v0 offset:32768\n"
      "s_waitcnt vmcnt(0) lgkmcnt(0)\n"
      "s_nop 4\n"
      "s_branch .Ld_t1\n"
      ".Ld_in2:\n"
      "s_add_u32 s36, s36, 165888\n"
      "s_min_u32 s36, s36, s6\n"
      "s_mov_b32 s51, s36\n"
      "s_add_u32 s36, s36, 165888\n"
      "s_min_u32 s36, s36, s6\n"
      "s_mov_b32 s51, s36\n"
      "s_add_u32 s36, s36, 165888\n"
      "s_min_u32 s36, s36, s6\n"
      "buffer_load_dword v20, v2, s[8:11], s36 offen sc1\n"
      "s_add_u32 m0, s43, 49152\n"
      "s_mov_b32 s51, s36\n"
      "s_waitcnt vmcnt(0)\n"
      "v_cmp_ne_u32 vcc, s21, v20\n"
      "s_add_u32 s36, s36, 165888\n"
      "s_min_u32 s36, s36, s6\n"
      "s_cbranch_vccnz .Lsp_d2_23\n"
      ".Lbk_d2_23:\n"
      "buffer_load_dword v20, v2, s[8:11], s36 offen sc1\n"
      "buffer_load_dwordx4 v0, s[8:11], s51 offen offset:3072 sc1 lds\n"
      "s_add_u32 s52, s52, 106496\n"
      "s_add_u32 s37, s37, 173056\n"
      "buffer_load_dwordx4 v0, s[8:11], s51 offen offset:1024 sc1 lds\n"
      "buffer_load_dwordx4 v0, s[8:11], s51 offen offset:2048 sc1 lds\n"
      "buffer_load_dwordx4 v0, s[8:11], s51 offen offset:0 sc1 lds\n"
      "s_add_u32 m0, s43, 53248\n"
      "s_nop 0\n"
      "buffer_load_dwordx4 v1, s[8:11], s51 offen offset:0 sc1 lds\n"
      "buffer_load_dwordx4 v1, s[8:11], s51 offen offset:1024 sc1 lds\n"
      "s_mov_b32 s51, s36\n"
      "s_add_u32 s36, s36, 165888\n"
      "s_add_u32 m0, s43, 73728\n"
      "s_waitcnt vmcnt(6)\n"
      "v_cmp_ne_u32 vcc, s21, v20\n"
      "s_add_u32 s38, s38, 147456\n"
      "s_min_u32 s36, s36, s6\n"
      "s_cbranch_vccnz .Lsp_d3_24\n"
      ".Lbk_d3_24:\n"
      "buffer_load_dwordx4 v0, s[8:11], s51 offen offset:2048 sc1 lds\n"
      "v_add_u32 v14, 1, v14\n"
      "buffer_load_dwordx4 v0, s[8:11], s51 offen offset:1024 sc1 lds\n"
      "buffer_load_dword v20, v2, s[8:11], s36 offen sc1\n"
      "s_waitcnt vmcnt(3)\n"
      "ds_write_b32 v16, v14\n"
      "buffer_load_dwordx4 v0, s[8:11], s51 offen offset:0 sc1 lds\n"
      "buffer_load_dwordx4 v0, s[8:11], s51 offen offset:3072 sc1 lds\n"
      "s_add_u32 m0, s43, 77824\n"
      "ds_read_b32 v15, v17\n"
      "s_add_u32 s52, s52, 173056\n"
      "buffer_load_dwordx4 v1, s[8:11], s51 offen offset:0 sc1 lds\n"
      "s_waitcnt lgkmcnt(0)\n"
      "v_cmp_gt_i32 vcc, v14, v15\n"
      "buffer_load_dwordx4 v1, s[8:11], s51 offen offset:1024 sc1 lds\n"
      "s_cbranch_vccnz .Lsp_dy2_25\n"
      ".Lbk_dy2_25:\n"
      "ds_read_b128 v[68:71], v22 offset:3072\n"
      "ds_read_b128 v[60:63], v22 offset:1024\n"
      "ds_read_b128 v[80:83], v22 offset:6144\n"
      "ds_read_b128 v[72:75], v22 offset:4096\n"
      "ds_read_b128 v[76:79], v22 offset:5120\n"
      "ds_read_b128 v[96:99], v22 offset:10240\n"
      "ds_read_b128 v[84:87], v22 offset:7168\n"
      "ds_read_b128 v[56:59], v22 offset:0\n"
      "ds_read_b128 v[92:95], v22 offset:9216\n"
      "s_mov_b32 s51, s36\n"
      "ds_read_b128 v[64:67], v22 offset:2048\n"
      "ds_read_b128 v[100:103], v22 offset:11264\n"
      "ds_read_b128 v[132:135], v22 offset:19456\n"
      "ds_read_b128 v[112:115], v22 offset:14336\n"
      "s_add_u32 m0, s43, 98304\n"
      "s_waitcnt vmcnt(0) lgkmcnt(0)\n"
      "s_nop 4\n"
      "s_branch .Ld_t2\n"
      ".Ld_in3:\n"
      "s_add_u32 s36, s36, 165888\n"
      "s_min_u32 s36, s36, s6\n"
      "s_mov_b32 s51, s36\n"
      "s_add_u32 s36, s36, 165888\n"
      "s_min_u32 s36, s36, s6\n"
      "s_mov_b32 s51, s36\n"
      "s_add_u32 s36, s36, 165888\n"
      "s_min_u32 s36, s36, s6\n"
      "s_mov_b32 s51, s36\n"
      "s_add_u32 s36, s36, 165888\n"
      "s_min_u32 s36, s36, s6\n"
      "buffer_load_dword v20, v2, s[8:11], s36 offen sc1\n"
      "s_add_u32 s52, s52, 106496\n"
      "s_add_u32 s37, s37, 173056\n"
      "s_mov_b32 s51, s36\n"
      "s_add_u32 s36, s36, 165888\n"
      "s_add_u32 m0, s43, 73728\n"
      "s_waitcnt vmcnt(0)\n"
      "v_cmp_ne_u32 vcc, s21, v20\n"
      "s_add_u32 s38, s38, 147456\n"
      "s_min_u32 s36, s36, s6\n"
      "s_cbranch_vccnz .Lsp_d3_26\n"
      ".Lbk_d3_26:\n"
      "buffer_load_dwordx4 v0, s[8:11], s51 offen offset:2048 sc1 lds\n"
      "buffer_load_dwordx4 v0, s[8:11], s51 offen offset:1024 sc1 lds\n"
      "buffer_load_dword v20, v2, s[8:11], s36 offen sc1\n"
      "buffer_load_dwordx4 v0, s[8:11], s51 offen offset:0 sc1 lds\n"
      "buffer_load_dwordx4 v0, s[8:11], s51 offen offset:3072 sc1 lds\n"
      "s_add_u32 m0, s43, 77824\n"
      "s_add_u32 s52, s52, 173056\n"
      "buffer_load_dwordx4 v1, s[8:11], s51 offen offset:0 sc1 lds\n"
      "buffer_load_dwordx4 v1, s[8:11], s51 offen offset:1024 sc1 lds\n"
      "s_mov_b32 s51, s36\n"
      "s_add_u32 m0, s43, 98304\n"
      "s_add_u32 s36, s36, 165888\n"
      "v_add_u32 v14, 1, v14\n"
      "s_waitcnt vmcnt(4)\n"
      "v_cmp_ne_u32 vcc, s21, v20\n"
      "s_cbranch_vccnz .Lsp_d4_27\n"
      ".Lbk_d4_27:\n"
      "s_min_u32 s36, s36, s6\n"
      "buffer_load_dwordx4 v0, s[8:11], s51 offen offset:0 sc1 lds\n"
      "buffer_load_dwordx4 v0, s[8:11], s51 offen offset:2048 sc1 lds\n"
      "s_waitcnt vmcnt(2)\n"
      "ds_write_b32 v16, v14\n"
      "buffer_load_dword v20, v2, s[8:11], s36 offen sc1\n"
      "buffer_load_dwordx4 v0, s[8:11], s51 offen offset:3072 sc1 lds\n"
      "ds_read_b32 v15, v17\n"
      "buffer_load_dwordx4 v0, s[8:11], s51 offen offset:1024 sc1 lds\n"
      "s_add_u32 m0, s43, 102400\n"
      "s_nop 0\n"
      "buffer_load_dwordx4 v1, s[8:11], s51 offen offset:1024 sc1 lds\n"
      "s_waitcnt lgkmcnt(0)\n"
      "v_cmp_gt_i32 vcc, v14, v15\n"
      "buffer_load_dwordx4 v1, s[8:11], s51 offen offset:0 sc1 lds\n"
      "s_cbranch_vccnz .Lsp_dy3_28\n"
      ".Lbk_dy3_28:\n"
      "ds_read_b128 v[60:63], v22 offset:25600\n"
      "ds_read_b128 v[64:67], v22 offset:26624\n"
      "s_mov_b32 s51, s36\n"
      "ds_read_b128 v[72:75], v22 offset:28672\n"
      "ds_read_b128 v[68:71], v22 offset:27648\n"
      "ds_read_b128 v[92:95], v22 offset:33792\n"
      "ds_read_b128 v[56:59], v22 offset:24576\n"
      "ds_read_b128 v[100:103], v22 offset:35840\n"
      "ds_read_b128 v[80:83], v22 offset:30720\n"
      "ds_read_b128 v[112:115], v22 offset:38912\n"
      "s_add_u32 s37, s37, 106496\n"
      "ds_read_b128 v[88:91], v22 offset:32768\n"
      "ds_read_b128 v[96:99], v22 offset:34816\n"
      "ds_read_b128 v[76:79], v22 offset:29696\n"
      "ds_read_b128 v[116:119], v22 offset:39936\n"
      "ds_read_b128 v[84:87], v22 offset:31744\n"
      "ds_read_b128 v[104:107], v22 offset:36864\n"
      "s_waitcnt vmcnt(0) lgkmcnt(0)\n"
      "s_nop 4\n"
      "s_branch .Ld_t3\n"
      ".Ld_in4:\n"
      "s_add_u32 s36, s36, 165888\n"
      "s_min_u32 s36, s36, s6\n"
      "s_mov_b32 s51, s36\n"
      "s_add_u32 s36, s36, 165888\n"
      "s_min_u32 s36, s36, s6\n"
      "s_mov_b32 s51, s36\n"
      "s_add_u32 s36, s36, 165888\n"
      "s_min_u32 s36, s36, s6\n"
      "s_mov_b32 s51, s36\n"
      "s_add_u32 s36, s36, 165888\n"
      "s_min_u32 s36, s36, s6\n"
      "s_add_u32 s52, s52, 106496\n"
      "s_add_u32 s37, s37, 173056\n"
      "s_mov_b32 s51, s36\n"
      "s_add_u32 s36, s36, 165888\n"
      "s_add_u32 s38, s38, 147456\n"
      "s_min_u32 s36, s36, s6\n"
      "buffer_load_dword v20, v2, s[8:11], s36 offen sc1\n"
      "s_add_u32 s52, s52, 173056\n"
      "s_mov_b32 s51, s36\n"
      "s_add_u32 m0, s43, 98304\n"
      "s_add_u32 s36, s36, 165888\n"
      "s_waitcnt vmcnt(0)\n"
      "v_cmp_ne_u32 vcc, s21, v20\n"
      "s_cbranch_vccnz .Lsp_d4_29\n"
      ".Lbk_d4_29:\n"
      "s_min_u32 s36, s36, s6\n"
      "buffer_load_dwordx4 v0, s[8:11], s51 offen offset:0 sc1 lds\n"
      "buffer_load_dwordx4 v0, s[8:11], s51 offen offset:2048 sc1 lds\n"
      "buffer_load_dword v20, v2, s[8:11], s36 offen sc1\n"
      "buffer_load_dwordx4 v0, s[8:11], s51 offen offset:3072 sc1 lds\n"
      "buffer_load_dwordx4 v0, s[8:11], s51 offen offset:1024 sc1 lds\n"
      "s_add_u32 m0, s43, 102400\n"
      "s_nop 0\n"
      "buffer_load_dwordx4 v1, s[8:11], s51 offen offset:1024 sc1 lds\n"
      "buffer_load_dwordx4 v1, s[8:11], s51 offen offset:0 sc1 lds\n"
      "s_mov_b32 s51, s36\n"
      "s_add_u32 s37, s37, 106496\n"
      "s_add_u32 m0, s43, 122880\n"
      "s_waitcnt vmcnt(4)\n"
      "v_cmp_ne_u32 vcc, s21, v20\n"
      "s_add_u32 s36, s36, 165888\n"
      "s_add_u32 s52, s52, 106496\n"
      "s_cbranch_vccnz .Lsp_d5_30\n"
      ".Lbk_d5_30:\n"
      "buffer_load_dwordx4 v0, s[8:11], s51 offen offset:1024 sc1 lds\n"
      "v_add_u32 v14, 1, v14\n"
      "buffer_load_dwordx4 v0, s[8:11], s51 offen offset:0 sc1 lds\n"
      "buffer_load_dwordx4 v0, s[8:11], s51 offen offset:2048 sc1 lds\n"
      "s_waitcnt vmcnt(3)\n"
      "ds_write_b32 v16, v14\n"
      "buffer_load_dwordx4 v0, s[8:11], s51 offen offset:3072 sc1 lds\n"
      "s_min_u32 s36, s36, s6\n"
      "s_add_u32 m0, s43, 126976\n"
      "s_nop 0\n"
      "buffer_load_dwordx4 v1, s[8:11], s51 offen offset:1024 sc1 lds\n"
      "ds_read_b32 v15, v17\n"
      "buffer_load_dword v20, v2, s[8:11], s36 offen sc1\n"
      "s_waitcnt lgkmcnt(0)\n"
      "v_cmp_gt_i32 vcc, v14, v15\n"
      "buffer_load_dwordx4 v1, s[8:11], s51 offen offset:0 sc1 lds\n"
      "s_cbranch_vccnz .Lsp_dy4_31\n"
      ".Lbk_dy4_31:\n"
      "ds_read_b128 v[60:63], v23 offset:1024\n"
      "ds_read_b128 v[64:67], v23 offset:2048\n"
      "ds_read_b128 v[72:75], v23 offset:4096\n"
      "ds_read_b128 v[68:71], v23 offset:3072\n"
      "s_add_u32 s58, s41, 4294967264\n"
      "ds_read_b128 v[80:83], v23 offset:6144\n"
      "ds_read_b128 v[84:87], v23 offset:7168\n"
      "ds_read_b128 v[76:79], v23 offset:5120\n"
      "s_add_u32 s37, s37, 106496\n"
      "ds_read_b128 v[56:59], v23 offset:0\n"
      "ds_read_b128 v[88:91], v23 offset:8192\n"
      "ds_read_b128 v[96:99], v23 offset:10240\n"
      "ds_read_b128 v[100:103], v23 offset:11264\n"
      "ds_read_b128 v[132:135], v23 offset:19456\n"
      "ds_read_b128 v[104:107], v23 offset:12288\n"
      "s_waitcnt vmcnt(0) lgkmcnt(0)\n"
      "s_nop 4\n"
      "s_branch .Ld_t4\n"
      ".Ld_in5:\n"
      "s_add_u32 s36, s36, 165888\n"
      "s_min_u32 s36, s36, s6\n"
      "s_mov_b32 s51, s36\n"
      "s_add_u32 s36, s36, 165888\n"
      "s_min_u32 s36, s36, s6\n"
      "s_mov_b32 s51, s36\n"
      "s_add_u32 s36, s36, 165888\n"
      "s_min_u32 s36, s36, s6\n"
      "s_mov_b32 s51, s36\n"
      "s_add_u32 s36, s36, 165888\n"
      "s_min_u32 s36, s36, s6\n"
      "s_add_u32 s52, s52, 106496\n"
      "s_add_u32 s37, s37, 173056\n"
      "s_mov_b32 s51, s36\n"
      "s_add_u32 s36, s36, 165888\n"
      "s_add_u32 s38, s38, 147456\n"
      "s_min_u32 s36, s36, s6\n"
      "s_add_u32 s52, s52, 173056\n"
      "s_mov_b32 s51, s36\n"
      "s_add_u32 s36, s36, 165888\n"
      "s_min_u32 s36, s36, s6\n"
      "buffer_load_dword v20, v2, s[8:11], s36 offen sc1\n"
      "s_mov_b32 s51, s36\n"
      "s_add_u32 s37, s37, 106496\n"
      "s_add_u32 m0, s43, 122880\n"
      "s_waitcnt vmcnt(0)\n"
      "v_cmp_ne_u32 vcc, s21, v20\n"
      "s_add_u32 s36, s36, 165888\n"
      "s_add_u32 s52, s52, 106496\n"
      "s_cbranch_vccnz .Lsp_d5_32\n"
      ".Lbk_d5_32:\n"
      "buffer_load_dwordx4 v0, s[8:11], s51 offen offset:1024 sc1 lds\n"
      "buffer_load_dwordx4 v0, s[8:11], s51 offen offset:0 sc1 lds\n"
      "buffer_load_dwordx4 v0, s[8:11], s51 offen offset:2048 sc1 lds\n"
      "buffer_load_dwordx4 v0, s[8:11], s51 offen offset:3072 sc1 lds\n"
      "s_min_u32 s36, s36, s6\n"
      "s_add_u32 m0, s43, 126976\n"
      "s_nop 0\n"
      "buffer_load_dwordx4 v1, s[8:11], s51 offen offset:1024 sc1 lds\n"
      "buffer_load_dword v20, v2, s[8:11], s36 offen sc1\n"
      "buffer_load_dwordx4 v1, s[8:11], s51 offen offset:0 sc1 lds\n"
      "s_add_u32 s58, s41, 4294967264\n"
      "s_add_u32 s37, s37, 106496\n"
      "s_add_u32 m0, s43, 0\n"
      "s_mov_b32 s51, s36\n"
      "v_add_u32 v14, 1, v14\n"
      "s_add_u32 s36, s36, 165888\n"
      "s_add_u32 s37, s37, 106496\n"
      "s_min_u32 s36, s36, s6\n"
      "s_cmp_lt_u32 s41, 32\n"
      "s_cselect_b32 s56, s39, 0x40000000\n"
      "s_add_u32 s53, s53, 599040\n"
      "s_waitcnt vmcnt(1)\n"
      "v_cmp_ne_u32 vcc, s21, v20\n"
      "s_add_u32 s39, s39, 147456\n"
      "s_waitcnt vmcnt(0)\n"
      "ds_write_b32 v16, v14\n"
      "s_cmp_lt_u32 s58, 32\n"
      "s_cselect_b32 s57, s40, 0x40000000\n"
      "s_cbranch_vccnz .Lsp_d0_33\n"
      ".Lbk_d0_33:\n"
      "buffer_load_dwordx4 v0, s[8:11], s51 offen offset:0 sc1 lds\n"
      "s_add_u32 s52, s52, 106496\n"
      "buffer_load_dwordx4 v0, s[8:11], s51 offen offset:1024 sc1 lds\n"
      "buffer_load_dword v20, v2, s[8:11], s36 offen sc1\n"
      "ds_read_b32 v15, v17\n"
      "buffer_load_dwordx4 v0, s[8:11], s51 offen offset:2048 sc1 lds\n"
      "s_add_u32 s40, s40, 4294856704\n"
      "buffer_load_dwordx4 v0, s[8:11], s51 offen offset:3072 sc1 lds\n"
      "s_waitcnt lgkmcnt(0)\n"
      "v_cmp_gt_i32 vcc, v14, v15\n"
      "s_add_u32 m0, s43, 4096\n"
      "s_nop 0\n"
      "buffer_load_dwordx4 v1, s[8:11], s51 offen offset:1024 sc1 lds\n"
      "s_cbranch_vccnz .Lsp_dy5_34\n"
      ".Lbk_dy5_34:\n"
      "ds_read_b128 v[56:59], v23 offset:24576\n"
      "ds_read_b128 v[64:67], v23 offset:26624\n"
      "buffer_load_dwordx4 v1, s[8:11], s51 offen offset:0 sc1 lds\n"
      "ds_read_b128 v[76:79], v23 offset:29696\n"
      "s_add_u32 s41, s41, 6\n"
      "ds_read_b128 v[88:91], v23 offset:32768\n"
      "ds_read_b128 v[68:71], v23 offset:27648\n"
      "ds_read_b128 v[96:99], v23 offset:34816\n"
      "ds_read_b128 v[80:83], v23 offset:30720\n"
      "ds_read_b128 v[72:75], v23 offset:28672\n"
      "s_mov_b32 s51, s36\n"
      "ds_read_b128 v[112:115], v23 offset:38912\n"
      "ds_read_b128 v[60:63], v23 offset:25600\n"
      "ds_read_b128 v[104:107], v23 offset:36864\n"
      "ds_read_b128 v[84:87], v23 offset:31744\n"
      "ds_read_b128 v[140:143], v23 offset:46080\n"
      "ds_read_b128 v[100:103], v23 offset:35840\n"
      "s_waitcnt vmcnt(0) lgkmcnt(0)\n"
      "s_nop 4\n"
      "s_branch .Ld_t5\n"
      ".Ld_end:\n"
      "s_endpgm\n"
      ".Lsp_d0_1:\n"
      "s_mov_b32 s22, s23\n"
      ".Lsl_d0_1:\n"
      "buffer_load_dword v20, v2, s[8:11], s51 offen sc1\n"
      "s_waitcnt vmcnt(0)\n"
      "v_cmp_ne_u32 vcc, s21, v20\n"
      "s_cbranch_vccz .Lbk_d0_1\n"
      "s_sleep 4\n"
      "s_sub_u32 s22, s22, 1\n"
      "s_cbranch_scc0 .Lsl_d0_1\n"
      "s_mov_b32 s23, 0\n"
      "s_branch .Lbk_d0_1\n"
      ".Lsp_d1_2:\n"
      "s_mov_b32 s22, s23\n"
      ".Lsl_d1_2:\n"
      "buffer_load_dword v20, v2, s[8:11], s51 offen sc1\n"
      "s_waitcnt vmcnt(0)\n"
      "v_cmp_ne_u32 vcc, s21, v20\n"
      "s_cbranch_vccz .Lbk_d1_2\n"
      "s_sleep 4\n"
      "s_sub_u32 s22, s22, 1\n"
      "s_cbranch_scc0 .Lsl_d1_2\n"
      "s_mov_b32 s23, 0\n"
      "s_branch .Lbk_d1_2\n"
      ".Lsp_dy0_3:\n"
      "s_lshl_b32 s22, s23, 4\n"
      ".Lsl_dy0_3:\n"
      "ds_read_b32 v15, v17\n"
      "s_waitcnt lgkmcnt(0)\n"
      "v_cmp_gt_i32 vcc, v14, v15\n"
      "s_cbranch_vccz .Lbk_dy0_3\n"
      "s_sub_u32 s22, s22, 1\n"
      "s_cbranch_scc0 .Lsl_dy0_3\n"
      "s_mov_b32 s23, 0\n"
      "s_branch .Lbk_dy0_3\n"
      ".Lsp_d2_4:\n"
      "s_mov_b32 s22, s23\n"
      "buffer_load_dword v20, v2, s[8:11], s51 offen sc1\n"
      "s_waitcnt vmcnt(0)\n"
      "v_cmp_ne_u32 vcc, s21, v20\n"
      "s_add_u32 s30, s52, 106496\n"
      "buffer_store_dword v18, v4, s[8:11], s30 offen sc1\n"
      "s_cbranch_vccz .Lbk_d2_4\n"
      ".Lsl_d2_4:\n"
      "buffer_load_dword v20, v2, s[8:11], s51 offen sc1\n"
      "s_waitcnt vmcnt(0)\n"
      "v_cmp_ne_u32 vcc, s21, v20\n"
      "s_cbranch_vccz .Lbk_d2_4\n"
      "s_sleep 4\n"
      "s_sub_u32 s22, s22, 1\n"
      "s_cbranch_scc0 .Lsl_d2_4\n"
      "s_mov_b32 s23, 0\n"
      "s_branch .Lbk_d2_4\n"
      ".Lsp_dy1_5:\n"
      "s_lshl_b32 s22, s23, 4\n"
      ".Lsl_dy1_5:\n"
      "ds_read_b32 v15, v17\n"
      "s_waitcnt lgkmcnt(0)\n"
      "v_cmp_gt_i32 vcc, v14, v15\n"
      "s_cbranch_vccz .Lbk_dy1_5\n"
      "s_sub_u32 s22, s22, 1\n"
      "s_cbranch_scc0 .Lsl_dy1_5\n"
      "s_mov_b32 s23, 0\n"
      "s_branch .Lbk_dy1_5\n"
      ".Lsp_d3_6:\n"
      "s_mov_b32 s22, s23\n"
      "buffer_load_dword v20, v2, s[8:11], s51 offen sc1\n"
      "s_waitcnt vmcnt(0)\n"
      "v_cmp_ne_u32 vcc, s21, v20\n"
      "buffer_store_dword v18, v5, s[8:11], s56 offen sc1\n"
      "s_cbranch_vccz .Lbk_d3_6\n"
      ".Lsl_d3_6:\n"
      "buffer_load_dword v20, v2, s[8:11], s51 offen sc1\n"
      "s_waitcnt vmcnt(0)\n"
      "v_cmp_ne_u32 vcc, s21, v20\n"
      "s_cbranch_vccz .Lbk_d3_6\n"
      "s_sleep 4\n"
      "s_sub_u32 s22, s22, 1\n"
      "s_cbranch_scc0 .Lsl_d3_6\n"
      "s_mov_b32 s23, 0\n"
      "s_branch .Lbk_d3_6\n"
      ".Lsp_dy2_7:\n"
      "s_lshl_b32 s22, s23, 4\n"
      ".Lsl_dy2_7:\n"
      "ds_read_b32 v15, v17\n"
      "s_waitcnt lgkmcnt(0)\n"
      "v_cmp_gt_i32 vcc, v14, v15\n"
      "s_cbranch_vccz .Lbk_dy2_7\n"
      "s_sub_u32 s22, s22, 1\n"
      "s_cbranch_scc0 .Lsl_dy2_7\n"
      "s_mov_b32 s23, 0\n"
      "s_branch .Lbk_dy2_7\n"
      ".Lsp_d4_8:\n"
      "s_mov_b32 s22, s23\n"
      "buffer_load_dword v20, v2, s[8:11], s51 offen sc1\n"
      "s_waitcnt vmcnt(0)\n"
      "v_cmp_ne_u32 vcc, s21, v20\n"
      "s_add_u32 s30, s52, 0\n"
      "buffer_store_dword v18, v4, s[8:11], s30 offen sc1\n"
      "s_cbranch_vccz .Lbk_d4_8\n"
      ".Lsl_d4_8:\n"
      "buffer_load_dword v20, v2, s[8:11], s51 offen sc1\n"
      "s_waitcnt vmcnt(0)\n"
      "v_cmp_ne_u32 vcc, s21, v20\n"
      "s_cbranch_vccz .Lbk_d4_8\n"
      "s_sleep 4\n"
      "s_sub_u32 s22, s22, 1\n"
      "s_cbranch_scc0 .Lsl_d4_8\n"
      "s_mov_b32 s23, 0\n"
      "s_branch .Lbk_d4_8\n"
      ".Lsp_dy3_9:\n"
      "s_lshl_b32 s22, s23, 4\n"
      ".Lsl_dy3_9:\n"
      "ds_read_b32 v15, v17\n"
      "s_waitcnt lgkmcnt(0)\n"
      "v_cmp_gt_i32 vcc, v14, v15\n"
      "s_cbranch_vccz .Lbk_dy3_9\n"
      "s_sub_u32 s22, s22, 1\n"
      "s_cbranch_scc0 .Lsl_dy3_9\n"
      "s_mov_b32 s23, 0\n"
      "s_branch .Lbk_dy3_9\n"
      ".Lsp_d5_10:\n"
      "s_mov_b32 s22, s23\n"
      "buffer_load_dword v20, v2, s[8:11], s51 offen sc1\n"
      "s_waitcnt vmcnt(0)\n"
      "v_cmp_ne_u32 vcc, s21, v20\n"
      "s_add_u32 s30, s52, 0\n"
      "buffer_store_dword v18, v4, s[8:11], s30 offen sc1\n"
      "s_cbranch_vccz .Lbk_d5_10\n"
      ".Lsl_d5_10:\n"
      "buffer_load_dword v20, v2, s[8:11], s51 offen sc1\n"
      "s_waitcnt vmcnt(0)\n"
      "v_cmp_ne_u32 vcc, s21, v20\n"
      "s_cbranch_vccz .Lbk_d5_10\n"
      "s_sleep 4\n"
      "s_sub_u32 s22, s22, 1\n"
      "s_cbranch_scc0 .Lsl_d5_10\n"
      "s_mov_b32 s23, 0\n"
      "s_branch .Lbk_d5_10\n"
      ".Lsp_dy4_11:\n"
      "s_lshl_b32 s22, s23, 4\n"
      ".Lsl_dy4_11:\n"
      "ds_read_b32 v15, v17\n"
      "s_waitcnt lgkmcnt(0)\n"
      "v_cmp_gt_i32 vcc, v14, v15\n"
      "s_cbranch_vccz .Lbk_dy4_11\n"
      "s_sub_u32 s22, s22, 1\n"
      "s_cbranch_scc0 .Lsl_dy4_11\n"
      "s_mov_b32 s23, 0\n"
      "s_branch .Lbk_dy4_11\n"
      ".Lsp_df_12:\n"
      "s_mov_b32 s22, s23\n"
      "buffer_load_dword v21, v3, s[8:11], s53 offen sc1\n"
      "s_waitcnt vmcnt(0)\n"
      "v_cmp_ne_u32 vcc, s21, v21\n"
      "s_add_u32 s30, s52, 0\n"
      "buffer_store_dword v18, v4, s[8:11], s30 offen sc1\n"
      "s_cbranch_vccz .Lbk_df_12\n"
      ".Lsl_df_12:\n"
      "buffer_load_dword v21, v3, s[8:11], s53 offen sc1\n"
      "s_waitcnt vmcnt(0)\n"
      "v_cmp_ne_u32 vcc, s21, v21\n"
      "s_cbranch_vccz .Lbk_df_12\n"
      "s_sleep 4\n"
      "s_sub_u32 s22, s22, 1\n"
      "s_cbranch_scc0 .Lsl_df_12\n"
      "s_mov_b32 s23, 0\n"
      "s_branch .Lbk_df_12\n"
      ".Lsp_d0_13:\n"
      "s_mov_b32 s22, s23\n"
      "buffer_load_dword v20, v2, s[8:11], s51 offen sc1\n"
      "s_waitcnt vmcnt(0)\n"
      "v_cmp_ne_u32 vcc, s21, v20\n"
      "s_add_u32 s30, s52, 106496\n"
      "buffer_store_dword v18, v4, s[8:11], s30 offen sc1\n"
      "s_cbranch_vccz .Lbk_d0_13\n"
      ".Lsl_d0_13:\n"
      "buffer_load_dword v20, v2, s[8:11], s51 offen sc1\n"
      "s_waitcnt vmcnt(0)\n"
      "v_cmp_ne_u32 vcc, s21, v20\n"
      "s_cbranch_vccz .Lbk_d0_13\n"
      "s_sleep 4\n"
      "s_sub_u32 s22, s22, 1\n"
      "s_cbranch_scc0 .Lsl_d0_13\n"
      "s_mov_b32 s23, 0\n"
      "s_branch .Lbk_d0_13\n"
      ".Lsp_dy5_14:\n"
      "s_lshl_b32 s22, s23, 4\n"
      ".Lsl_dy5_14:\n"
      "ds_read_b32 v15, v17\n"
      "s_waitcnt lgkmcnt(0)\n"
      "v_cmp_gt_i32 vcc, v14, v15\n"
      "s_cbranch_vccz .Lbk_dy5_14\n"
      "s_sub_u32 s22, s22, 1\n"
      "s_cbranch_scc0 .Lsl_dy5_14\n"
      "s_mov_b32 s23, 0\n"
      "s_branch .Lbk_dy5_14\n"
      ".Lsp_d1_15:\n"
      "s_mov_b32 s22, s23\n"
      "buffer_load_dword v20, v2, s[8:11], s51 offen sc1\n"
      "s_waitcnt vmcnt(0)\n"
      "v_cmp_ne_u32 vcc, s21, v20\n"
      "s_add_u32 s30, s52, 106496\n"
      "buffer_store_dword v18, v4, s[8:11], s30 offen sc1\n"
      "s_cbranch_vccz .Lbk_d1_15\n"
      ".Lsl_d1_15:\n"
      "buffer_load_dword v20, v2, s[8:11], s51 offen sc1\n"
      "s_waitcnt vmcnt(0)\n"
      "v_cmp_ne_u32 vcc, s21, v20\n"
      "s_cbranch_vccz .Lbk_d1_15\n"
      "s_sleep 4\n"
      "s_sub_u32 s22, s22, 1\n"
      "s_cbranch_scc0 .Lsl_d1_15\n"
      "s_mov_b32 s23, 0\n"
      "s_branch .Lbk_d1_15\n"
      ".Lsp_dy0_16:\n"
      "s_lshl_b32 s22, s23, 4\n"
      ".Lsl_dy0_16:\n"
      "ds_read_b32 v15, v17\n"
      "s_waitcnt lgkmcnt(0)\n"
      "v_cmp_gt_i32 vcc, v14, v15\n"
      "s_cbranch_vccz .Lbk_dy0_16\n"
      "s_sub_u32 s22, s22, 1\n"
      "s_cbranch_scc0 .Lsl_dy0_16\n"
      "s_mov_b32 s23, 0\n"
      "s_branch .Lbk_dy0_16\n"
      ".Lsp_df_17:\n"
      "s_mov_b32 s22, s23\n"
      "buffer_load_dword v21, v3, s[8:11], s53 offen sc1\n"
      "s_waitcnt vmcnt(0)\n"
      "v_cmp_ne_u32 vcc, s21, v21\n"
      "s_add_u32 s30, s52, 0\n"
      "buffer_store_dword v18, v4, s[8:11], s30 offen sc1\n"
      "s_cbranch_vccz .Lbk_df_17\n"
      ".Lsl_df_17:\n"
      "buffer_load_dword v21, v3, s[8:11], s53 offen sc1\n"
      "s_waitcnt vmcnt(0)\n"
      "v_cmp_ne_u32 vcc, s21, v21\n"
      "s_cbranch_vccz .Lbk_df_17\n"
      "s_sleep 4\n"
      "s_sub_u32 s22, s22, 1\n"
      "s_cbranch_scc0 .Lsl_df_17\n"
      "s_mov_b32 s23, 0\n"
      "s_branch .Lbk_df_17\n"
      ".Lsp_df_18:\n"
      "s_mov_b32 s22, s23\n"
      "buffer_load_dword v21, v3, s[8:11], s53 offen sc1\n"
      "s_waitcnt vmcnt(0)\n"
      "v_cmp_ne_u32 vcc, s21, v21\n"
      "s_add_u32 s30, s52, 0\n"
      "buffer_store_dword v18, v4, s[8:11], s30 offen sc1\n"
      "s_cbranch_vccz .Lbk_df_18\n"
      ".Lsl_df_18:\n"
      "buffer_load_dword v21, v3, s[8:11], s53 offen sc1\n"
      "s_waitcnt vmcnt(0)\n"
      "v_cmp_ne_u32 vcc, s21, v21\n"
      "s_cbranch_vccz .Lbk_df_18\n"
      "s_sleep 4\n"
      "s_sub_u32 s22, s22, 1\n"
      "s_cbranch_scc0 .Lsl_df_18\n"
      "s_mov_b32 s23, 0\n"
      "s_branch .Lbk_df_18\n"
      ".Lsp_df_19:\n"
      "s_mov_b32 s22, s23\n"
      "buffer_load_dword v21, v3, s[8:11], s53 offen sc1\n"
      "s_waitcnt vmcnt(0)\n"
      "v_cmp_ne_u32 vcc, s21, v21\n"
      "s_add_u32 s30, s52, 0\n"
      "buffer_store_dword v18, v4, s[8:11], s30 offen sc1\n"
      "s_cbranch_vccz .Lbk_df_19\n"
      ".Lsl_df_19:\n"
      "buffer_load_dword v21, v3, s[8:11], s53 offen sc1\n"
      "s_waitcnt vmcnt(0)\n"
      "v_cmp_ne_u32 vcc, s21, v21\n"
      "s_cbranch_vccz .Lbk_df_19\n"
      "s_sleep 4\n"
      "s_sub_u32 s22, s22, 1\n"
      "s_cbranch_scc0 .Lsl_df_19\n"
      "s_mov_b32 s23, 0\n"
      "s_branch .Lbk_df_19\n"
      ".Lsp_d1_20:\n"
      "s_mov_b32 s22, s23\n"
      ".Lsl_d1_20:\n"
      "buffer_load_dword v20, v2, s[8:11], s51 offen sc1\n"
      "s_waitcnt vmcnt(0)\n"
      "v_cmp_ne_u32 vcc, s21, v20\n"
      "s_cbranch_vccz .Lbk_d1_20\n"
      "s_sleep 4\n"
      "s_sub_u32 s22, s22, 1\n"
      "s_cbranch_scc0 .Lsl_d1_20\n"
      "s_mov_b32 s23, 0\n"
      "s_branch .Lbk_d1_20\n"
      ".Lsp_d2_21:\n"
      "s_mov_b32 s22, s23\n"
      ".Lsl_d2_21:\n"
      "buffer_load_dword v20, v2, s[8:11], s51 offen sc1\n"
      "s_waitcnt vmcnt(0)\n"
      "v_cmp_ne_u32 vcc, s21, v20\n"
      "s_cbranch_vccz .Lbk_d2_21\n"
      "s_sleep 4\n"
      "s_sub_u32 s22, s22, 1\n"
      "s_cbranch_scc0 .Lsl_d2_21\n"
      "s_mov_b32 s23, 0\n"
      "s_branch .Lbk_d2_21\n"
      ".Lsp_dy1_22:\n"
      "s_lshl_b32 s22, s23, 4\n"
      ".Lsl_dy1_22:\n"
      "ds_read_b32 v15, v17\n"
      "s_waitcnt lgkmcnt(0)\n"
      "v_cmp_gt_i32 vcc, v14, v15\n"
      "s_cbranch_vccz .Lbk_dy1_22\n"
      "s_sub_u32 s22, s22, 1\n"
      "s_cbranch_scc0 .Lsl_dy1_22\n"
      "s_mov_b32 s23, 0\n"
      "s_branch .Lbk_dy1_22\n"
      ".Lsp_d2_23:\n"
      "s_mov_b32 s22, s23\n"
      ".Lsl_d2_23:\n"
      "buffer_load_dword v20, v2, s[8:11], s51 offen sc1\n"
      "s_waitcnt vmcnt(0)\n"
      "v_cmp_ne_u32 vcc, s21, v20\n"
      "s_cbranch_vccz .Lbk_d2_23\n"
      "s_sleep 4\n"
      "s_sub_u32 s22, s22, 1\n"
      "s_cbranch_scc0 .Lsl_d2_23\n"
      "s_mov_b32 s23, 0\n"
      "s_branch .Lbk_d2_23\n"
      ".Lsp_d3_24:\n"
      "s_mov_b32 s22, s23\n"
      ".Lsl_d3_24:\n"
      "buffer_load_dword v20, v2, s[8:11], s51 offen sc1\n"
      "s_waitcnt vmcnt(0)\n"
      "v_cmp_ne_u32 vcc, s21, v20\n"
      "s_cbranch_vccz .Lbk_d3_24\n"
      "s_sleep 4\n"
      "s_sub_u32 s22, s22, 1\n"
      "s_cbranch_scc0 .Lsl_d3_24\n"
      "s_mov_b32 s23, 0\n"
      "s_branch .Lbk_d3_24\n"
      ".Lsp_dy2_25:\n"
      "s_lshl_b32 s22, s23, 4\n"
      ".Lsl_dy2_25:\n"
      "ds_read_b32 v15, v17\n"
      "s_waitcnt lgkmcnt(0)\n"
      "v_cmp_gt_i32 vcc, v14, v15\n"
      "s_cbranch_vccz .Lbk_dy2_25\n"
      "s_sub_u32 s22, s22, 1\n"
      "s_cbranch_scc0 .Lsl_dy2_25\n"
      "s_mov_b32 s23, 0\n"
      "s_branch .Lbk_dy2_25\n"
      ".Lsp_d3_26:\n"
      "s_mov_b32 s22, s23\n"
      ".Lsl_d3_26:\n"
      "buffer_load_dword v20, v2, s[8:11], s51 offen sc1\n"
      "s_waitcnt vmcnt(0)\n"
      "v_cmp_ne_u32 vcc, s21, v20\n"
      "s_cbranch_vccz .Lbk_d3_26\n"
      "s_sleep 4\n"
      "s_sub_u32 s22, s22, 1\n"
      "s_cbranch_scc0 .Lsl_d3_26\n"
      "s_mov_b32 s23, 0\n"
      "s_branch .Lbk_d3_26\n"
      ".Lsp_d4_27:\n"
      "s_mov_b32 s22, s23\n"
      ".Lsl_d4_27:\n"
      "buffer_load_dword v20, v2, s[8:11], s51 offen sc1\n"
      "s_waitcnt vmcnt(0)\n"
      "v_cmp_ne_u32 vcc, s21, v20\n"
      "s_cbranch_vccz .Lbk_d4_27\n"
      "s_sleep 4\n"
      "s_sub_u32 s22, s22, 1\n"
      "s_cbranch_scc0 .Lsl_d4_27\n"
      "s_mov_b32 s23, 0\n"
      "s_branch .Lbk_d4_27\n"
      ".Lsp_dy3_28:\n"
      "s_lshl_b32 s22, s23, 4\n"
      ".Lsl_dy3_28:\n"
      "ds_read_b32 v15, v17\n"
      "s_waitcnt lgkmcnt(0)\n"
      "v_cmp_gt_i32 vcc, v14, v15\n"
      "s_cbranch_vccz .Lbk_dy3_28\n"
      "s_sub_u32 s22, s22, 1\n"
      "s_cbranch_scc0 .Lsl_dy3_28\n"
      "s_mov_b32 s23, 0\n"
      "s_branch .Lbk_dy3_28\n"
      ".Lsp_d4_29:\n"
      "s_mov_b32 s22, s23\n"
      ".Lsl_d4_29:\n"
      "buffer_load_dword v20, v2, s[8:11], s51 offen sc1\n"
      "s_waitcnt vmcnt(0)\n"
      "v_cmp_ne_u32 vcc, s21, v20\n"
      "s_cbranch_vccz .Lbk_d4_29\n"
      "s_sleep 4\n"
      "s_sub_u32 s22, s22, 1\n"
      "s_cbranch_scc0 .Lsl_d4_29\n"
      "s_mov_b32 s23, 0\n"
      "s_branch .Lbk_d4_29\n"
      ".Lsp_d5_30:\n"
      "s_mov_b32 s22, s23\n"
      ".Lsl_d5_30:\n"
      "buffer_load_dword v20, v2, s[8:11], s51 offen sc1\n"
      "s_waitcnt vmcnt(0)\n"
      "v_cmp_ne_u32 vcc, s21, v20\n"
      "s_cbranch_vccz .Lbk_d5_30\n"
      "s_sleep 4\n"
      "s_sub_u32 s22, s22, 1\n"
      "s_cbranch_scc0 .Lsl_d5_30\n"
      "s_mov_b32 s23, 0\n"
      "s_branch .Lbk_d5_30\n"
      ".Lsp_dy4_31:\n"
      "s_lshl_b32 s22, s23, 4\n"
      ".Lsl_dy4_31:\n"
      "ds_read_b32 v15, v17\n"
      "s_waitcnt lgkmcnt(0)\n"
      "v_cmp_gt_i32 vcc, v14, v15\n"
      "s_cbranch_vccz .Lbk_dy4_31\n"
      "s_sub_u32 s22, s22, 1\n"
      "s_cbranch_scc0 .Lsl_dy4_31\n"
      "s_mov_b32 s23, 0\n"
      "s_branch .Lbk_dy4_31\n"
      ".Lsp_d5_32:\n"
      "s_mov_b32 s22, s23\n"
      ".Lsl_d5_32:\n"
      "buffer_load_dword v20, v2, s[8:11], s51 offen sc1\n"
      "s_waitcnt vmcnt(0)\n"
      "v_cmp_ne_u32 vcc, s21, v20\n"
      "s_cbranch_vccz .Lbk_d5_32\n"
      "s_sleep 4\n"
      "s_sub_u32 s22, s22, 1\n"
      "s_cbranch_scc0 .Lsl_d5_32\n"
      "s_mov_b32 s23, 0\n"
      "s_branch .Lbk_d5_32\n"
      ".Lsp_d0_33:\n"
      "s_mov_b32 s22, s23\n"
      ".Lsl_d0_33:\n"
      "buffer_load_dword v20, v2, s[8:11], s51 offen sc1\n"
      "s_waitcnt vmcnt(0)\n"
      "v_cmp_ne_u32 vcc, s21, v20\n"
      "s_cbranch_vccz .Lbk_d0_33\n"
      "s_sleep 4\n"
      "s_sub_u32 s22, s22, 1\n"
      "s_cbranch_scc0 .Lsl_d0_33\n"
      "s_mov_b32 s23, 0\n"
      "s_branch .Lbk_d0_33\n"
      ".Lsp_dy5_34:\n"
      "s_lshl_b32 s22, s23, 4\n"
      ".Lsl_dy5_34:\n"
      "ds_read_b32 v15, v17\n"
      "s_waitcnt lgkmcnt(0)\n"
      "v_cmp_gt_i32 vcc, v14, v15\n"
      "s_cbranch_vccz .Lbk_dy5_34\n"
      "s_sub_u32 s22, s22, 1\n"
      "s_cbranch_scc0 .Lsl_dy5_34\n"
      "s_mov_b32 s23, 0\n"
      "s_branch .Lbk_dy5_34\n"
      ".Ld_last:\n"
      :
      : "{s[8:11]}"(rsWS), "{s[12:15]}"(rsA), "{s[16:19]}"(rsB), "{s[92:95]}"(rsC), "{v0}"(v_l16), "{v1}"(v_l16b), "{v2}"(v_fa), "{v3}"(v_ga), "{v4}"(v_ra), "{v5}"(v_rf), "{v6}"(v_xp), "{v7}"(v_xs), "{v8}"(v_op), "{v9}"(v_os), "{v10}"(v_sw), "{v11}"(v_sr), "{v12}"(v_ss), "{v13}"(v_l8), "{v15}"(v_al), "{v16}"(v_yw), "{v17}"(v_yr), "{v19}"(v_hi), "{v176}"(v_at), "{v177}"(v_as), "{v178}"(v_vo0), "{v179}"(v_vo1), "{v180}"(v_vo2), "{v181}"(v_pw), "{v183}"(v_pu), "{s36}"(s_sh), "{s37}"(s_ps), "{s38}"(s_x1), "{s39}"(s_xr), "{s40}"(s_or), "{s41}"(s_p0), "{s42}"(s_m3), "{s43}"(s_m6), "{s44}"(s_so), "{s45}"(s_ph), "{s46}"(s_i0), "{s47}"(s_i5), "{s48}"(s_j0), "{s49}"(s_j5), "{s50}"(s_he), "{s60}"(s_k0)
      : "v255", "a255", "s99", "vcc", "scc", "memory");

  } else {
    const unsigned e1 = (e - 1) & 3, gr = !fast ? slot & 3 : slot < 31 ? (e + slot - 27) & 3 : e1, part = !fast ? 8 * e + (slot >> 2) : slot < 31 ? 32 + e : 32 + e1;
    COMMON(112, 81920);
    const unsigned j = part, f = 4 * j + w;
    const u32x4 rsA = srd(w >> 1 ? Wu : Wg, 576 * 4608 * 2), rsC = srd(Wo, 576 * 576 * 2);
    const unsigned tw0 = !fast && slot < 4 ? ((e * 16 + slot * 4 + w) * 81 + lane) * 128 : OOB, v_tw[2] = {tw0, lane < 17 ? tw0 + 8192 : OOB};
    const unsigned v_l5 = w < 3 ? v_l16 + 16384 - w * 3072 : OOB;
    const unsigned v_fa = 19456 - w * 4096 + (lane >> 3) * 128 + (lane & 7) * 4, v_ra = up * 8 * HREC + l5 + 147456 - f * 1024 + f * 128;
    const unsigned one = q == g ? 0x3f80u << (16 * (p & 1)) : 0u, v_id[2] = {p < 2 ? one : 0u, p < 2 ? 0u : one};
    const unsigned v_st = 18432 + i16 * 32 + q * 8, v_pw = 147456 - f * 1024 + f * 128 + l8 * HREC + l7;
    const unsigned v_at = 65536 + (8 * g + q) * 256 + ((4 * w + p) ^ (4 * q)) * 16;
    const unsigned v_vo0 = (16 * (w & 1) + g) * 9216 + 256 * j + (i16 ^ (4 * g)) * 16;
    const unsigned v_vo1 = v_vo0 + 4 * 9216 - 1024, v_vo2 = v_vo1 + 4 * 9216 - 1024, v_vo3 = v_vo2 + 4 * 9216 - 1024;
    const unsigned s_sx = WS_X1 + gr * NREC * REC + w * 4096, s_sh = WS_H + gr * NREC * HREC + f * 1024;
    const unsigned s_m4 = w * 4096, s_m5 = 16384 + w * 1024;
    asm volatile(
      "s_buffer_load_dword s21, s[8:11], 0\n"
      "s_mov_b32 s23, 65536\n"
      "s_mov_b32 s31, 0x40000000\n"
      "s_mov_b32 s41, 0\n"
      "s_sleep 16\n"
      "buffer_load_dword v58, v54, s[92:95], s41 offen\n"
      "buffer_load_dword v59, v55, s[92:95], s41 offen\n"
      "s_waitcnt vmcnt(0)\n"
      "s_add_u32 m0, s38, 65536\n"
      "s_nop 0\n"
      "buffer_load_dwordx4 v6, s[12:15], s41 offen offset:0 lds\n"
      "buffer_load_dwordx4 v7, s[12:15], s41 offen offset:1024 lds\n"
      "buffer_load_dwordx4 v8, s[12:15], s41 offen offset:2048 lds\n"
      "buffer_load_dwordx4 v9, s[12:15], s41 offen offset:3072 lds\n"
      "s_add_u32 s41, s41, 294912\n"
      "s_add_u32 m0, s38, 81920\n"
      "s_nop 0\n"
      "buffer_load_dwordx4 v6, s[12:15], s41 offen offset:0 lds\n"
      "buffer_load_dwordx4 v7, s[12:15], s41 offen offset:1024 lds\n"
      "buffer_load_dwordx4 v8, s[12:15], s41 offen offset:2048 lds\n"
      "buffer_load_dwordx4 v9, s[12:15], s41 offen offset:3072 lds\n"
      "s_add_u32 s41, s41, 294912\n"
      "s_add_u32 m0, s38, 98304\n"
      "s_nop 0\n"
      "buffer_load_dwordx4 v6, s[12:15], s41 offen offset:0 lds\n"
      "buffer_load_dwordx4 v7, s[12:15], s41 offen offset:1024 lds\n"
      "buffer_load_dwordx4 v8, s[12:15], s41 offen offset:2048 lds\n"
      "buffer_load_dwordx4 v9, s[12:15], s41 offen offset:3072 lds\n"
      "s_add_u32 s41, s41, 294912\n"
      "s_add_u32 m0, s38, 114688\n"
      "s_nop 0\n"
      "buffer_load_dwordx4 v6, s[12:15], s41 offen offset:0 lds\n"
      "buffer_load_dwordx4 v7, s[12:15], s41 offen offset:1024 lds\n"
      "buffer_load_dwordx4 v8, s[12:15], s41 offen offset:2048 lds\n"
      "buffer_load_dwordx4 v9, s[12:15], s41 offen offset:3072 lds\n"
      "s_add_u32 s41, s41, 294912\n"
      "s_add_u32 m0, s38, 131072\n"
      "s_nop 0\n"
      "buffer_load_dwordx4 v6, s[12:15], s41 offen offset:0 lds\n"
      "buffer_load_dwordx4 v7, s[12:15], s41 offen offset:1024 lds\n"
      "buffer_load_dwordx4 v8, s[12:15], s41 offen offset:2048 lds\n"
      "buffer_load_dwordx4 v9, s[12:15], s41 offen offset:3072 lds\n"
      "s_add_u32 s41, s41, 294912\n"
      "v_mov_b32 v172, 0x3f803f80\n"
      "v_mov_b32 v173, 0x3f803f80\n"
      "v_mov_b32 v174, 0x3f803f80\n"
      "v_mov_b32 v175, 0x3f803f80\n"
      "v_add_u32 v19, 49152, v5\n"
      "s_mov_b64 exec, 0\n"
      "s_lshr_b32 s22, s38, 12\n"
      ".Lg_pl:\n"
      "s_cmp_eq_u32 s22, 0\n"
      "s_cbranch_scc1 .Lg_p0\n"
      "s_cmp_eq_u32 s22, 1\n"
      "s_cbranch_scc1 .Lg_p1\n"
      "s_branch .Lg_pe\n"
      ".Lg_pr:\n"
      "s_add_u32 s22, s22, 4\n"
      "s_branch .Lg_pl\n"
      ".Lg_pe:\n"
      "s_mov_b64 exec, -1\n"
      "s_waitcnt vmcnt(16)\n"
      "s_barrier\n"
      "ds_read_b64_tr_b16 a[0:1], v5 offset:0\n"
      "ds_read_b64_tr_b16 a[2:3], v5 offset:1024\n"
      "ds_read_b64_tr_b16 a[72:73], v5 offset:8192\n"
      "ds_read_b64_tr_b16 a[74:75], v5 offset:9216\n"
      "ds_read_b64_tr_b16 a[144:145], v5 offset:8\n"
      "ds_read_b64_tr_b16 a[146:147], v5 offset:1032\n"
      "ds_read_b64_tr_b16 a[216:217], v5 offset:8200\n"
      "ds_read_b64_tr_b16 a[218:219], v5 offset:9224\n"
      "s_add_u32 m0, s38, 147456\n"
      "s_nop 0\n"
      "buffer_load_dwordx4 v6, s[12:15], s41 offen offset:0 lds\n"
      "buffer_load_dwordx4 v7, s[12:15], s41 offen offset:1024 lds\n"
      "buffer_load_dwordx4 v8, s[12:15], s41 offen offset:2048 lds\n"
      "buffer_load_dwordx4 v9, s[12:15], s41 offen offset:3072 lds\n"
      "s_add_u32 s41, s41, 294912\n"
      "s_waitcnt vmcnt(16) lgkmcnt(0)\n"
      "s_barrier\n"
      "ds_read_b64_tr_b16 a[4:5], v5 offset:16384\n"
      "ds_read_b64_tr_b16 a[6:7], v5 offset:17408\n"
      "ds_read_b64_tr_b16 a[76:77], v5 offset:24576\n"
      "ds_read_b64_tr_b16 a[78:79], v5 offset:25600\n"
      "ds_read_b64_tr_b16 a[148:149], v5 offset:16392\n"
      "ds_read_b64_tr_b16 a[150:151], v5 offset:17416\n"
      "ds_read_b64_tr_b16 a[220:221], v5 offset:24584\n"
      "ds_read_b64_tr_b16 a[222:223], v5 offset:25608\n"
      "v_mfma_f32_16x16x32_bf16 v[140:143], v[172:175], a[0:3], 0\n"
      "v_mfma_f32_16x16x32_bf16 v[144:147], v[172:175], a[72:75], 0\n"
      "v_mfma_f32_16x16x32_bf16 v[148:151], v[172:175], a[144:147], 0\n"
      "v_mfma_f32_16x16x32_bf16 v[152:155], v[172:175], a[216:219], 0\n"
      "s_add_u32 m0, s38, 65536\n"
      "s_nop 0\n"
      "buffer_load_dwordx4 v6, s[12:15], s41 offen offset:0 lds\n"
      "buffer_load_dwordx4 v7, s[12:15], s41 offen offset:1024 lds\n"
      "buffer_load_dwordx4 v8, s[12:15], s41 offen offset:2048 lds\n"
      "buffer_load_dwordx4 v9, s[12:15], s41 offen offset:3072 lds\n"
      "s_add_u32 s41, s41, 294912\n"
      "s_waitcnt vmcnt(16) lgkmcnt(0)\n"
      "s_barrier\n"
      "ds_read_b64_tr_b16 a[8:9], v5 offset:32768\n"
      "ds_read_b64_tr_b16 a[10:11], v5 offset:33792\n"
      "ds_read_b64_tr_b16 a[80:81], v5 offset:40960\n"
      "ds_read_b64_tr_b16 a[82:83], v5 offset:41984\n"
      "ds_read_b64_tr_b16 a[152:153], v5 offset:32776\n"
      "ds_read_b64_tr_b16 a[154:155], v5 offset:33800\n"
      "ds_read_b64_tr_b16 a[224:225], v5 offset:40968\n"
      "ds_read_b64_tr_b16 a[226:227], v5 offset:41992\n"
      "v_mfma_f32_16x16x32_bf16 v[140:143], v[172:175], a[4:7], v[140:143]\n"
      "v_mfma_f32_16x16x32_bf16 v[144:147], v[172:175], a[76:79], v[144:147]\n"
      "v_mfma_f32_16x16x32_bf16 v[148:151], v[172:175], a[148:151], v[148:151]\n"
      "v_mfma_f32_16x16x32_bf16 v[152:155], v[172:175], a[220:223], v[152:155]\n"
      "s_add_u32 m0, s38, 81920\n"
      "s_nop 0\n"
      "buffer_load_dwordx4 v6, s[12:15], s41 offen offset:0 lds\n"
      "buffer_load_dwordx4 v7, s[12:15], s41 offen offset:1024 lds\n"
      "buffer_load_dwordx4 v8, s[12:15], s41 offen offset:2048 lds\n"
      "buffer_load_dwordx4 v9, s[12:15], s41 offen offset:3072 lds\n"
      "s_add_u32 s41, s41, 294912\n"
      "s_waitcnt vmcnt(16) lgkmcnt(0)\n"
      "s_barrier\n"
      "ds_read_b64_tr_b16 a[12:13], v19 offset:0\n"
      "ds_read_b64_tr_b16 a[14:15], v19 offset:1024\n"
      "ds_read_b64_tr_b16 a[84:85], v19 offset:8192\n"
      "ds_read_b64_tr_b16 a[86:87], v19 offset:9216\n"
      "ds_read_b64_tr_b16 a[156:157], v19 offset:8\n"
      "ds_read_b64_tr_b16 a[158:159], v19 offset:1032\n"
      "ds_read_b64_tr_b16 a[228:229], v19 offset:8200\n"
      "ds_read_b64_tr_b16 a[230:231], v19 offset:9224\n"
      "v_mfma_f32_16x16x32_bf16 v[140:143], v[172:175], a[8:11], v[140:143]\n"
      "v_mfma_f32_16x16x32_bf16 v[144:147], v[172:175], a[80:83], v[144:147]\n"
      "v_mfma_f32_16x16x32_bf16 v[148:151], v[172:175], a[152:155], v[148:151]\n"
      "v_mfma_f32_16x16x32_bf16 v[152:155], v[172:175], a[224:227], v[152:155]\n"
      "s_add_u32 m0, s38, 98304\n"
      "s_nop 0\n"
      "buffer_load_dwordx4 v6, s[12:15], s41 offen offset:0 lds\n"
      "buffer_load_dwordx4 v7, s[12:15], s41 offen offset:1024 lds\n"
      "buffer_load_dwordx4 v8, s[12:15], s41 offen offset:2048 lds\n"
      "buffer_load_dwordx4 v9, s[12:15], s41 offen offset:3072 lds\n"
      "s_add_u32 s41, s41, 294912\n"
      "s_waitcnt vmcnt(16) lgkmcnt(0)\n"
      "s_barrier\n"
      "ds_read_b64_tr_b16 a[16:17], v19 offset:16384\n"
      "ds_read_b64_tr_b16 a[18:19], v19 offset:17408\n"
      "ds_read_b64_tr_b16 a[88:89], v19 offset:24576\n"
      "ds_read_b64_tr_b16 a[90:91], v19 offset:25600\n"
      "ds_read_b64_tr_b16 a[160:161], v19 offset:16392\n"
      "ds_read_b64_tr_b16 a[162:163], v19 offset:17416\n"
      "ds_read_b64_tr_b16 a[232:233], v19 offset:24584\n"
      "ds_read_b64_tr_b16 a[234:235], v19 offset:25608\n"
      "v_mfma_f32_16x16x32_bf16 v[140:143], v[172:175], a[12:15], v[140:143]\n"
      "v_mfma_f32_16x16x32_bf16 v[144:147], v[172:175], a[84:87], v[144:147]\n"
      "v_mfma_f32_16x16x32_bf16 v[148:151], v[172:175], a[156:159], v[148:151]\n"
      "v_mfma_f32_16x16x32_bf16 v[152:155], v[172:175], a[228:231], v[152:155]\n"
      "s_add_u32 m0, s38, 114688\n"
      "s_nop 0\n"
      "buffer_load_dwordx4 v6, s[12:15], s41 offen offset:0 lds\n"
      "buffer_load_dwordx4 v7, s[12:15], s41 offen offset:1024 lds\n"
      "buffer_load_dwordx4 v8, s[12:15], s41 offen offset:2048 lds\n"
      "buffer_load_dwordx4 v9, s[12:15], s41 offen offset:3072 lds\n"
      "s_add_u32 s41, s41, 294912\n"
      "s_waitcnt vmcnt(16) lgkmcnt(0)\n"
      "s_barrier\n"
      "ds_read_b64_tr_b16 a[20:21], v19 offset:32768\n"
      "ds_read_b64_tr_b16 a[22:23], v19 offset:33792\n"
      "ds_read_b64_tr_b16 a[92:93], v19 offset:40960\n"
      "ds_read_b64_tr_b16 a[94:95], v19 offset:41984\n"
      "ds_read_b64_tr_b16 a[164:165], v19 offset:32776\n"
      "ds_read_b64_tr_b16 a[166:167], v19 offset:33800\n"
      "ds_read_b64_tr_b16 a[236:237], v19 offset:40968\n"
      "ds_read_b64_tr_b16 a[238:239], v19 offset:41992\n"
      "v_mfma_f32_16x16x32_bf16 v[140:143], v[172:175], a[16:19], v[140:143]\n"
      "v_mfma_f32_16x16x32_bf16 v[144:147], v[172:175], a[88:91], v[144:147]\n"
      "v_mfma_f32_16x16x32_bf16 v[148:151], v[172:175], a[160:163], v[148:151]\n"
      "v_mfma_f32_16x16x32_bf16 v[152:155], v[172:175], a[232:235], v[152:155]\n"
      "s_add_u32 m0, s38, 131072\n"
      "s_nop 0\n"
      "buffer_load_dwordx4 v6, s[12:15], s41 offen offset:0 lds\n"
      "buffer_load_dwordx4 v7, s[12:15], s41 offen offset:1024 lds\n"
      "buffer_load_dwordx4 v8, s[12:15], s41 offen offset:2048 lds\n"
      "buffer_load_dwordx4 v9, s[12:15], s41 offen offset:3072 lds\n"
      "s_add_u32 s41, s41, 294912\n"
      "s_waitcnt vmcnt(16) lgkmcnt(0)\n"
      "s_barrier\n"
      "ds_read_b64_tr_b16 a[24:25], v5 offset:0\n"
      "ds_read_b64_tr_b16 a[26:27], v5 offset:1024\n"
      "ds_read_b64_tr_b16 a[96:97], v5 offset:8192\n"
      "ds_read_b64_tr_b16 a[98:99], v5 offset:9216\n"
      "ds_read_b64_tr_b16 a[168:169], v5 offset:8\n"
      "ds_read_b64_tr_b16 a[170:171], v5 offset:1032\n"
      "ds_read_b64_tr_b16 a[240:241], v5 offset:8200\n"
      "ds_read_b64_tr_b16 a[242:243], v5 offset:9224\n"
      "v_mfma_f32_16x16x32_bf16 v[140:143], v[172:175], a[20:23], v[140:143]\n"
      "v_mfma_f32_16x16x32_bf16 v[144:147], v[172:175], a[92:95], v[144:147]\n"
      "v_mfma_f32_16x16x32_bf16 v[148:151], v[172:175], a[164:167], v[148:151]\n"
      "v_mfma_f32_16x16x32_bf16 v[152:155], v[172:175], a[236:239], v[152:155]\n"
      "s_add_u32 m0, s38, 147456\n"
      "s_nop 0\n"
      "buffer_load_dwordx4 v6, s[12:15], s41 offen offset:0 lds\n"
      "buffer_load_dwordx4 v7, s[12:15], s41 offen offset:1024 lds\n"
      "buffer_load_dwordx4 v8, s[12:15], s41 offen offset:2048 lds\n"
      "buffer_load_dwordx4 v9, s[12:15], s41 offen offset:3072 lds\n"
      "s_add_u32 s41, s41, 294912\n"
      "s_waitcnt vmcnt(16) lgkmcnt(0)\n"
      "s_barrier\n"
      "ds_read_b64_tr_b16 a[28:29], v5 offset:16384\n"
      "ds_read_b64_tr_b16 a[30:31], v5 offset:17408\n"
      "ds_read_b64_tr_b16 a[100:101], v5 offset:24576\n"
      "ds_read_b64_tr_b16 a[102:103], v5 offset:25600\n"
      "ds_read_b64_tr_b16 a[172:173], v5 offset:16392\n"
      "ds_read_b64_tr_b16 a[174:175], v5 offset:17416\n"
      "ds_read_b64_tr_b16 a[244:245], v5 offset:24584\n"
      "ds_read_b64_tr_b16 a[246:247], v5 offset:25608\n"
      "v_mfma_f32_16x16x32_bf16 v[140:143], v[172:175], a[24:27], v[140:143]\n"
      "v_mfma_f32_16x16x32_bf16 v[144:147], v[172:175], a[96:99], v[144:147]\n"
      "v_mfma_f32_16x16x32_bf16 v[148:151], v[172:175], a[168:171], v[148:151]\n"
      "v_mfma_f32_16x16x32_bf16 v[152:155], v[172:175], a[240:243], v[152:155]\n"
      "s_add_u32 m0, s38, 65536\n"
      "s_nop 0\n"
      "buffer_load_dwordx4 v6, s[12:15], s41 offen offset:0 lds\n"
      "buffer_load_dwordx4 v7, s[12:15], s41 offen offset:1024 lds\n"
      "buffer_load_dwordx4 v8, s[12:15], s41 offen offset:2048 lds\n"
      "buffer_load_dwordx4 v9, s[12:15], s41 offen offset:3072 lds\n"
      "s_add_u32 s41, s41, 294912\n"
      "s_waitcnt vmcnt(16) lgkmcnt(0)\n"
      "s_barrier\n"
      "ds_read_b64_tr_b16 a[32:33], v5 offset:32768\n"
      "ds_read_b64_tr_b16 a[34:35], v5 offset:33792\n"
      "ds_read_b64_tr_b16 a[104:105], v5 offset:40960\n"
      "ds_read_b64_tr_b16 a[106:107], v5 offset:41984\n"
      "ds_read_b64_tr_b16 a[176:177], v5 offset:32776\n"
      "ds_read_b64_tr_b16 a[178:179], v5 offset:33800\n"
      "ds_read_b64_tr_b16 a[248:249], v5 offset:40968\n"
      "ds_read_b64_tr_b16 a[250:251], v5 offset:41992\n"
      "v_mfma_f32_16x16x32_bf16 v[140:143], v[172:175], a[28:31], v[140:143]\n"
      "v_mfma_f32_16x16x32_bf16 v[144:147], v[172:175], a[100:103], v[144:147]\n"
      "v_mfma_f32_16x16x32_bf16 v[148:151], v[172:175], a[172:175], v[148:151]\n"
      "v_mfma_f32_16x16x32_bf16 v[152:155], v[172:175], a[244:247], v[152:155]\n"
      "s_add_u32 m0, s38, 81920\n"
      "s_nop 0\n"
      "buffer_load_dwordx4 v6, s[12:15], s41 offen offset:0 lds\n"
      "buffer_load_dwordx4 v7, s[12:15], s41 offen offset:1024 lds\n"
      "buffer_load_dwordx4 v8, s[12:15], s41 offen offset:2048 lds\n"
      "buffer_load_dwordx4 v9, s[12:15], s41 offen offset:3072 lds\n"
      "s_add_u32 s41, s41, 294912\n"
      "s_waitcnt vmcnt(16) lgkmcnt(0)\n"
      "s_barrier\n"
      "ds_read_b64_tr_b16 a[36:37], v19 offset:0\n"
      "ds_read_b64_tr_b16 a[38:39], v19 offset:1024\n"
      "ds_read_b64_tr_b16 a[108:109], v19 offset:8192\n"
      "ds_read_b64_tr_b16 a[110:111], v19 offset:9216\n"
      "ds_read_b64_tr_b16 a[180:181], v19 offset:8\n"
      "ds_read_b64_tr_b16 a[182:183], v19 offset:1032\n"
      "ds_read_b64_tr_b16 a[252:253], v19 offset:8200\n"
      "ds_read_b64_tr_b16 a[254:255], v19 offset:9224\n"
      "v_mfma_f32_16x16x32_bf16 v[140:143], v[172:175], a[32:35], v[140:143]\n"
      "v_mfma_f32_16x16x32_bf16 v[144:147], v[172:175], a[104:107], v[144:147]\n"
      "v_mfma_f32_16x16x32_bf16 v[148:151], v[172:175], a[176:179], v[148:151]\n"
      "v_mfma_f32_16x16x32_bf16 v[152:155], v[172:175], a[248:251], v[152:155]\n"
      "s_add_u32 m0, s38, 98304\n"
      "s_nop 0\n"
      "buffer_load_dwordx4 v6, s[12:15], s41 offen offset:0 lds\n"
      "buffer_load_dwordx4 v7, s[12:15], s41 offen offset:1024 lds\n"
      "buffer_load_dwordx4 v8, s[12:15], s41 offen offset:2048 lds\n"
      "buffer_load_dwordx4 v9, s[12:15], s41 offen offset:3072 lds\n"
      "s_add_u32 s41, s41, 294912\n"
      "s_waitcnt vmcnt(16) lgkmcnt(0)\n"
      "s_barrier\n"
      "ds_read_b64_tr_b16 a[40:41], v19 offset:16384\n"
      "ds_read_b64_tr_b16 a[42:43], v19 offset:17408\n"
      "ds_read_b64_tr_b16 a[112:113], v19 offset:24576\n"
      "ds_read_b64_tr_b16 a[114:115], v19 offset:25600\n"
      "ds_read_b64_tr_b16 a[184:185], v19 offset:16392\n"
      "ds_read_b64_tr_b16 a[186:187], v19 offset:17416\n"
      "ds_read_b64_tr_b16 v[20:21], v19 offset:24584\n"
      "ds_read_b64_tr_b16 v[22:23], v19 offset:25608\n"
      "v_mfma_f32_16x16x32_bf16 v[140:143], v[172:175], a[36:39], v[140:143]\n"
      "v_mfma_f32_16x16x32_bf16 v[144:147], v[172:175], a[108:111], v[144:147]\n"
      "v_mfma_f32_16x16x32_bf16 v[148:151], v[172:175], a[180:183], v[148:151]\n"
      "v_mfma_f32_16x16x32_bf16 v[152:155], v[172:175], a[252:255], v[152:155]\n"
      "s_add_u32 m0, s38, 114688\n"
      "s_nop 0\n"
      "buffer_load_dwordx4 v6, s[12:15], s41 offen offset:0 lds\n"
      "buffer_load_dwordx4 v7, s[12:15], s41 offen offset:1024 lds\n"
      "buffer_load_dwordx4 v8, s[12:15], s41 offen offset:2048 lds\n"
      "buffer_load_dwordx4 v9, s[12:15], s41 offen offset:3072 lds\n"
      "s_add_u32 s41, s41, 294912\n"
      "s_waitcnt vmcnt(16) lgkmcnt(0)\n"
      "s_barrier\n"
      "ds_read_b64_tr_b16 a[44:45], v19 offset:32768\n"
      "ds_read_b64_tr_b16 a[46:47], v19 offset:33792\n"
      "ds_read_b64_tr_b16 a[116:117], v19 offset:40960\n"
      "ds_read_b64_tr_b16 a[118:119], v19 offset:41984\n"
      "ds_read_b64_tr_b16 a[188:189], v19 offset:32776\n"
      "ds_read_b64_tr_b16 a[190:191], v19 offset:33800\n"
      "ds_read_b64_tr_b16 v[24:25], v19 offset:40968\n"
      "ds_read_b64_tr_b16 v[26:27], v19 offset:41992\n"
      "v_mfma_f32_16x16x32_bf16 v[140:143], v[172:175], a[40:43], v[140:143]\n"
      "v_mfma_f32_16x16x32_bf16 v[144:147], v[172:175], a[112:115], v[144:147]\n"
      "v_mfma_f32_16x16x32_bf16 v[148:151], v[172:175], a[184:187], v[148:151]\n"
      "v_mfma_f32_16x16x32_bf16 v[152:155], v[172:175], v[20:23], v[152:155]\n"
      "s_add_u32 m0, s38, 131072\n"
      "s_nop 0\n"
      "buffer_load_dwordx4 v6, s[12:15], s41 offen offset:0 lds\n"
      "buffer_load_dwordx4 v7, s[12:15], s41 offen offset:1024 lds\n"
      "buffer_load_dwordx4 v8, s[12:15], s41 offen offset:2048 lds\n"
      "buffer_load_dwordx4 v9, s[12:15], s41 offen offset:3072 lds\n"
      "s_add_u32 s41, s41, 294912\n"
      "s_waitcnt vmcnt(16) lgkmcnt(0)\n"
      "s_barrier\n"
      "ds_read_b64_tr_b16 a[48:49], v5 offset:0\n"
      "ds_read_b64_tr_b16 a[50:51], v5 offset:1024\n"
      "ds_read_b64_tr_b16 a[120:121], v5 offset:8192\n"
      "ds_read_b64_tr_b16 a[122:123], v5 offset:9216\n"
      "ds_read_b64_tr_b16 a[192:193], v5 offset:8\n"
      "ds_read_b64_tr_b16 a[194:195], v5 offset:1032\n"
      "ds_read_b64_tr_b16 v[28:29], v5 offset:8200\n"
      "ds_read_b64_tr_b16 v[30:31], v5 offset:9224\n"
      "v_mfma_f32_16x16x32_bf16 v[140:143], v[172:175], a[44:47], v[140:143]\n"
      "v_mfma_f32_16x16x32_bf16 v[144:147], v[172:175], a[116:119], v[144:147]\n"
      "v_mfma_f32_16x16x32_bf16 v[148:151], v[172:175], a[188:191], v[148:151]\n"
      "v_mfma_f32_16x16x32_bf16 v[152:155], v[172:175], v[24:27], v[152:155]\n"
      "s_add_u32 m0, s38, 147456\n"
      "s_nop 0\n"
      "buffer_load_dwordx4 v6, s[12:15], s41 offen offset:0 lds\n"
      "buffer_load_dwordx4 v7, s[12:15], s41 offen offset:1024 lds\n"
      "buffer_load_dwordx4 v8, s[12:15], s41 offen offset:2048 lds\n"
      "buffer_load_dwordx4 v9, s[12:15], s41 offen offset:3072 lds\n"
      "s_add_u32 s41, s41, 294912\n"
      "s_waitcnt vmcnt(16) lgkmcnt(0)\n"
      "s_barrier\n"
      "ds_read_b64_tr_b16 a[52:53], v5 offset:16384\n"
      "ds_read_b64_tr_b16 a[54:55], v5 offset:17408\n"
      "ds_read_b64_tr_b16 a[124:125], v5 offset:24576\n"
      "ds_read_b64_tr_b16 a[126:127], v5 offset:25600\n"
      "ds_read_b64_tr_b16 a[196:197], v5 offset:16392\n"
      "ds_read_b64_tr_b16 a[198:199], v5 offset:17416\n"
      "ds_read_b64_tr_b16 v[32:33], v5 offset:24584\n"
      "ds_read_b64_tr_b16 v[34:35], v5 offset:25608\n"
      "v_mfma_f32_16x16x32_bf16 v[140:143], v[172:175], a[48:51], v[140:143]\n"
      "v_mfma_f32_16x16x32_bf16 v[144:147], v[172:175], a[120:123], v[144:147]\n"
      "v_mfma_f32_16x16x32_bf16 v[148:151], v[172:175], a[192:195], v[148:151]\n"
      "v_mfma_f32_16x16x32_bf16 v[152:155], v[172:175], v[28:31], v[152:155]\n"
      "s_waitcnt vmcnt(12) lgkmcnt(0)\n"
      "s_barrier\n"
      "ds_read_b64_tr_b16 a[56:57], v5 offset:32768\n"
      "ds_read_b64_tr_b16 a[58:59], v5 offset:33792\n"
      "ds_read_b64_tr_b16 a[128:129], v5 offset:40960\n"
      "ds_read_b64_tr_b16 a[130:131], v5 offset:41984\n"
      "ds_read_b64_tr_b16 a[200:201], v5 offset:32776\n"
      "ds_read_b64_tr_b16 a[202:203], v5 offset:33800\n"
      "ds_read_b64_tr_b16 v[36:37], v5 offset:40968\n"
      "ds_read_b64_tr_b16 v[38:39], v5 offset:41992\n"
      "v_mfma_f32_16x16x32_bf16 v[140:143], v[172:175], a[52:55], v[140:143]\n"
      "v_mfma_f32_16x16x32_bf16 v[144:147], v[172:175], a[124:127], v[144:147]\n"
      "v_mfma_f32_16x16x32_bf16 v[148:151], v[172:175], a[196:199], v[148:151]\n"
      "v_mfma_f32_16x16x32_bf16 v[152:155], v[172:175], v[32:35], v[152:155]\n"
      "s_waitcnt vmcnt(8) lgkmcnt(0)\n"
      "s_barrier\n"
      "ds_read_b64_tr_b16 a[60:61], v19 offset:0\n"
      "ds_read_b64_tr_b16 a[62:63], v19 offset:1024\n"
      "ds_read_b64_tr_b16 a[132:133], v19 offset:8192\n"
      "ds_read_b64_tr_b16 a[134:135], v19 offset:9216\n"
      "ds_read_b64_tr_b16 a[204:205], v19 offset:8\n"
      "ds_read_b64_tr_b16 a[206:207], v19 offset:1032\n"
      "ds_read_b64_tr_b16 v[40:41], v19 offset:8200\n"
      "ds_read_b64_tr_b16 v[42:43], v19 offset:9224\n"
      "v_mfma_f32_16x16x32_bf16 v[140:143], v[172:175], a[56:59], v[140:143]\n"
      "v_mfma_f32_16x16x32_bf16 v[144:147], v[172:175], a[128:131], v[144:147]\n"
      "v_mfma_f32_16x16x32_bf16 v[148:151], v[172:175], a[200:203], v[148:151]\n"
      "v_mfma_f32_16x16x32_bf16 v[152:155], v[172:175], v[36:39], v[152:155]\n"
      "s_waitcnt vmcnt(4) lgkmcnt(0)\n"
      "s_barrier\n"
      "ds_read_b64_tr_b16 a[64:65], v19 offset:16384\n"
      "ds_read_b64_tr_b16 a[66:67], v19 offset:17408\n"
      "ds_read_b64_tr_b16 a[136:137], v19 offset:24576\n"
      "ds_read_b64_tr_b16 a[138:139], v19 offset:25600\n"
      "ds_read_b64_tr_b16 a[208:209], v19 offset:16392\n"
      "ds_read_b64_tr_b16 a[210:211], v19 offset:17416\n"
      "ds_read_b64_tr_b16 v[44:45], v19 offset:24584\n"
      "ds_read_b64_tr_b16 v[46:47], v19 offset:25608\n"
      "v_mfma_f32_16x16x32_bf16 v[140:143], v[172:175], a[60:63], v[140:143]\n"
      "v_mfma_f32_16x16x32_bf16 v[144:147], v[172:175], a[132:135], v[144:147]\n"
      "v_mfma_f32_16x16x32_bf16 v[148:151], v[172:175], a[204:207], v[148:151]\n"
      "v_mfma_f32_16x16x32_bf16 v[152:155], v[172:175], v[40:43], v[152:155]\n"
      "s_waitcnt vmcnt(0) lgkmcnt(0)\n"
      "s_barrier\n"
      "ds_read_b64_tr_b16 a[68:69], v19 offset:32768\n"
      "ds_read_b64_tr_b16 a[70:71], v19 offset:33792\n"
      "ds_read_b64_tr_b16 a[140:141], v19 offset:40960\n"
      "ds_read_b64_tr_b16 a[142:143], v19 offset:41984\n"
      "ds_read_b64_tr_b16 a[212:213], v19 offset:32776\n"
      "ds_read_b64_tr_b16 a[214:215], v19 offset:33800\n"
      "ds_read_b64_tr_b16 v[48:49], v19 offset:40968\n"
      "ds_read_b64_tr_b16 v[50:51], v19 offset:41992\n"
      "v_mfma_f32_16x16x32_bf16 v[140:143], v[172:175], a[64:67], v[140:143]\n"
      "v_mfma_f32_16x16x32_bf16 v[144:147], v[172:175], a[136:139], v[144:147]\n"
      "v_mfma_f32_16x16x32_bf16 v[148:151], v[172:175], a[208:211], v[148:151]\n"
      "v_mfma_f32_16x16x32_bf16 v[152:155], v[172:175], v[44:47], v[152:155]\n"
      "s_waitcnt lgkmcnt(0)\n"
      "v_mfma_f32_16x16x32_bf16 v[140:143], v[172:175], a[68:71], v[140:143]\n"
      "v_mfma_f32_16x16x32_bf16 v[144:147], v[172:175], a[140:143], v[144:147]\n"
      "v_mfma_f32_16x16x32_bf16 v[148:151], v[172:175], a[212:215], v[148:151]\n"
      "v_mfma_f32_16x16x32_bf16 v[152:155], v[172:175], v[48:51], v[152:155]\n"
      "s_getpc_b64 vcc\n"
      ".Lg_ct:\n"
      "s_add_u32 vcc_lo, vcc_lo, .Lg_p0-.Lg_ct\n"
      "s_addc_u32 vcc_hi, vcc_hi, 0\n"
      "s_lshl_b32 s30, s38, 1\n"
      "v_lshl_add_u32 v60, v0, 3, s30\n"
      "v_min_u32 v60, .Lg_last-.Lg_p0-4, v60\n"
      "global_load_dword v60, v60, vcc\n"
      "s_mul_i32 s21, s21, 1664525\n"
      "s_add_u32 s21, s21, 1013904223\n"
      "v_mov_b32 v15, s21\n"
      "buffer_store_dword v15, v12, s[8:11], 0 offen sc1\n"
      "v_not_b32 v52, v15\n"
      "v_not_b32 v53, v15\n"
      "v_not_b32 v54, v15\n"
      "v_not_b32 v55, v15\n"
      "s_add_u32 s30, s37, 0\n"
      "buffer_store_dwordx4 v[52:55], v10, s[8:11], s30 offen sc1\n"
      "v_xor_b32 v15, v15, v16\n"
      "v_mov_b32 v66, 0\n"
      "v_mov_b32 v67, 0\n"
      "v_mov_b32 v68, 0\n"
      "v_mov_b32 v69, 0\n"
      "v_mov_b32 v70, v64\n"
      "v_mov_b32 v71, v65\n"
      "v_mul_f32 v140, 0xbae38e39, v140\n"
      "v_mul_f32 v141, 0xbae38e39, v141\n"
      "v_mul_f32 v142, 0xbae38e39, v142\n"
      "v_mul_f32 v143, 0xbae38e39, v143\n"
      "v_mul_f32 v144, 0xbae38e39, v144\n"
      "v_mul_f32 v145, 0xbae38e39, v145\n"
      "v_mul_f32 v146, 0xbae38e39, v146\n"
      "v_mul_f32 v147, 0xbae38e39, v147\n"
      "v_mul_f32 v148, 0xbae38e39, v148\n"
      "v_mul_f32 v149, 0xbae38e39, v149\n"
      "v_mul_f32 v150, 0xbae38e39, v150\n"
      "v_mul_f32 v151, 0xbae38e39, v151\n"
      "v_mul_f32 v152, 0xbae38e39, v152\n"
      "v_mul_f32 v153, 0xbae38e39, v153\n"
      "v_mul_f32 v154, 0xbae38e39, v154\n"
      "v_mul_f32 v155, 0xbae38e39, v155\n"
      "v_mfma_f32_16x16x32_bf16 v[72:75], v[64:67], a[0:3], v[140:143]\n"
      "v_mfma_f32_16x16x32_bf16 v[76:79], v[68:71], a[0:3], v[140:143]\n"
      "v_mfma_f32_16x16x32_bf16 v[80:83], v[64:67], a[4:7], v[140:143]\n"
      "v_mfma_f32_16x16x32_bf16 v[84:87], v[68:71], a[4:7], v[140:143]\n"
      "v_mfma_f32_16x16x32_bf16 v[88:91], v[64:67], a[8:11], v[140:143]\n"
      "v_mfma_f32_16x16x32_bf16 v[92:95], v[68:71], a[8:11], v[140:143]\n"
      "s_nop 3\n"
      "v_cvt_pk_bf16_f32 v72, v72, v73\n"
      "v_cvt_pk_bf16_f32 v73, v74, v75\n"
      "v_cvt_pk_bf16_f32 v74, v76, v77\n"
      "v_cvt_pk_bf16_f32 v75, v78, v79\n"
      "v_accvgpr_write_b32 a0, v72\n"
      "v_accvgpr_write_b32 a1, v73\n"
      "v_accvgpr_write_b32 a2, v74\n"
      "v_accvgpr_write_b32 a3, v75\n"
      "v_mfma_f32_16x16x32_bf16 v[72:75], v[64:67], a[12:15], v[140:143]\n"
      "v_mfma_f32_16x16x32_bf16 v[76:79], v[68:71], a[12:15], v[140:143]\n"
      "v_cvt_pk_bf16_f32 v80, v80, v81\n"
      "v_cvt_pk_bf16_f32 v81, v82, v83\n"
      "v_cvt_pk_bf16_f32 v82, v84, v85\n"
      "v_cvt_pk_bf16_f32 v83, v86, v87\n"
      "v_accvgpr_write_b32 a4, v80\n"
      "v_accvgpr_write_b32 a5, v81\n"
      "v_accvgpr_write_b32 a6, v82\n"
      "v_accvgpr_write_b32 a7, v83\n"
      "v_mfma_f32_16x16x32_bf16 v[80:83], v[64:67], a[16:19], v[140:143]\n"
      "v_mfma_f32_16x16x32_bf16 v[84:87], v[68:71], a[16:19], v[140:143]\n"
      "v_cvt_pk_bf16_f32 v88, v88, v89\n"
      "v_cvt_pk_bf16_f32 v89, v90, v91\n"
      "v_cvt_pk_bf16_f32 v90, v92, v93\n"
      "v_cvt_pk_bf16_f32 v91, v94, v95\n"
      "v_accvgpr_write_b32 a8, v88\n"
      "v_accvgpr_write_b32 a9, v89\n"
      "v_accvgpr_write_b32 a10, v90\n"
      "v_accvgpr_write_b32 a11, v91\n"
      "v_mfma_f32_16x16x32_bf16 v[88:91], v[64:67], a[20:23], v[140:143]\n"
      "v_mfma_f32_16x16x32_bf16 v[92:95], v[68:71], a[20:23], v[140:143]\n"
      "v_cvt_pk_bf16_f32 v72, v72, v73\n"
      "v_cvt_pk_bf16_f32 v73, v74, v75\n"
      "v_cvt_pk_bf16_f32 v74, v76, v77\n"
      "v_cvt_pk_bf16_f32 v75, v78, v79\n"
      "v_accvgpr_write_b32 a12, v72\n"
      "v_accvgpr_write_b32 a13, v73\n"
      "v_accvgpr_write_b32 a14, v74\n"
      "v_accvgpr_write_b32 a15, v75\n"
      "v_mfma_f32_16x16x32_bf16 v[72:75], v[64:67], a[24:27], v[140:143]\n"
      "v_mfma_f32_16x16x32_bf16 v[76:79], v[68:71], a[24:27], v[140:143]\n"
      "v_cvt_pk_bf16_f32 v80, v80, v81\n"
      "v_cvt_pk_bf16_f32 v81, v82, v83\n"
      "v_cvt_pk_bf16_f32 v82, v84, v85\n"
      "v_cvt_pk_bf16_f32 v83, v86, v87\n"
      "v_accvgpr_write_b32 a16, v80\n"
      "v_accvgpr_write_b32 a17, v81\n"
      "v_accvgpr_write_b32 a18, v82\n"
      "v_accvgpr_write_b32 a19, v83\n"
      "v_mfma_f32_16x16x32_bf16 v[80:83], v[64:67], a[28:31], v[140:143]\n"
      "v_mfma_f32_16x16x32_bf16 v[84:87], v[68:71], a[28:31], v[140:143]\n"
      "v_cvt_pk_bf16_f32 v88, v88, v89\n"
      "v_cvt_pk_bf16_f32 v89, v90, v91\n"
      "v_cvt_pk_bf16_f32 v90, v92, v93\n"
      "v_cvt_pk_bf16_f32 v91, v94, v95\n"
      "v_accvgpr_write_b32 a20, v88\n"
      "v_accvgpr_write_b32 a21, v89\n"
      "v_accvgpr_write_b32 a22, v90\n"
      "v_accvgpr_write_b32 a23, v91\n"
      "v_mfma_f32_16x16x32_bf16 v[88:91], v[64:67], a[32:35], v[140:143]\n"
      "v_mfma_f32_16x16x32_bf16 v[92:95], v[68:71], a[32:35], v[140:143]\n"
      "v_cvt_pk_bf16_f32 v72, v72, v73\n"
      "v_cvt_pk_bf16_f32 v73, v74, v75\n"
      "v_cvt_pk_bf16_f32 v74, v76, v77\n"
      "v_cvt_pk_bf16_f32 v75, v78, v79\n"
      "v_accvgpr_write_b32 a24, v72\n"
      "v_accvgpr_write_b32 a25, v73\n"
      "v_accvgpr_write_b32 a26, v74\n"
      "v_accvgpr_write_b32 a27, v75\n"
      "v_mfma_f32_16x16x32_bf16 v[72:75], v[64:67], a[36:39], v[140:143]\n"
      "v_mfma_f32_16x16x32_bf16 v[76:79], v[68:71], a[36:39], v[140:143]\n"
      "v_cvt_pk_bf16_f32 v80, v80, v81\n"
      "v_cvt_pk_bf16_f32 v81, v82, v83\n"
      "v_cvt_pk_bf16_f32 v82, v84, v85\n"
      "v_cvt_pk_bf16_f32 v83, v86, v87\n"
      "v_accvgpr_write_b32 a28, v80\n"
      "v_accvgpr_write_b32 a29, v81\n"
      "v_accvgpr_write_b32 a30, v82\n"
      "v_accvgpr_write_b32 a31, v83\n"
      "v_mfma_f32_16x16x32_bf16 v[80:83], v[64:67], a[40:43], v[140:143]\n"
      "v_mfma_f32_16x16x32_bf16 v[84:87], v[68:71], a[40:43], v[140:143]\n"
      "v_cvt_pk_bf16_f32 v88, v88, v89\n"
      "v_cvt_pk_bf16_f32 v89, v90, v91\n"
      "v_cvt_pk_bf16_f32 v90, v92, v93\n"
      "v_cvt_pk_bf16_f32 v91, v94, v95\n"
      "v_accvgpr_write_b32 a32, v88\n"
      "v_accvgpr_write_b32 a33, v89\n"
      "v_accvgpr_write_b32 a34, v90\n"
      "v_accvgpr_write_b32 a35, v91\n"
      "v_mfma_f32_16x16x32_bf16 v[88:91], v[64:67], a[44:47], v[140:143]\n"
      "v_mfma_f32_16x16x32_bf16 v[92:95], v[68:71], a[44:47], v[140:143]\n"
      "v_cvt_pk_bf16_f32 v72, v72, v73\n"
      "v_cvt_pk_bf16_f32 v73, v74, v75\n"
      "v_cvt_pk_bf16_f32 v74, v76, v77\n"
      "v_cvt_pk_bf16_f32 v75, v78, v79\n"
      "v_accvgpr_write_b32 a36, v72\n"
      "v_accvgpr_write_b32 a37, v73\n"
      "v_accvgpr_write_b32 a38, v74\n"
      "v_accvgpr_write_b32 a39, v75\n"
      "v_mfma_f32_16x16x32_bf16 v[72:75], v[64:67], a[48:51], v[140:143]\n"
      "v_mfma_f32_16x16x32_bf16 v[76:79], v[68:71], a[48:51], v[140:143]\n"
      "v_cvt_pk_bf16_f32 v80, v80, v81\n"
      "v_cvt_pk_bf16_f32 v81, v82, v83\n"
      "v_cvt_pk_bf16_f32 v82, v84, v85\n"
      "v_cvt_pk_bf16_f32 v83, v86, v87\n"
      "v_accvgpr_write_b32 a40, v80\n"
      "v_accvgpr_write_b32 a41, v81\n"
      "v_accvgpr_write_b32 a42, v82\n"
      "v_accvgpr_write_b32 a43, v83\n"
      "v_mfma_f32_16x16x32_bf16 v[80:83], v[64:67], a[52:55], v[140:143]\n"
      "v_mfma_f32_16x16x32_bf16 v[84:87], v[68:71], a[52:55], v[140:143]\n"
      "v_cvt_pk_bf16_f32 v88, v88, v89\n"
      "v_cvt_pk_bf16_f32 v89, v90, v91\n"
      "v_cvt_pk_bf16_f32 v90, v92, v93\n"
      "v_cvt_pk_bf16_f32 v91, v94, v95\n"
      "v_accvgpr_write_b32 a44, v88\n"
      "v_accvgpr_write_b32 a45, v89\n"
      "v_accvgpr_write_b32 a46, v90\n"
      "v_accvgpr_write_b32 a47, v91\n"
      "v_mfma_f32_16x16x32_bf16 v[88:91], v[64:67], a[56:59], v[140:143]\n"
      "v_mfma_f32_16x16x32_bf16 v[92:95], v[68:71], a[56:59], v[140:143]\n"
      "v_cvt_pk_bf16_f32 v72, v72, v73\n"
      "v_cvt_pk_bf16_f32 v73, v74, v75\n"
      "v_cvt_pk_bf16_f32 v74, v76, v77\n"
      "v_cvt_pk_bf16_f32 v75, v78, v79\n"
      "v_accvgpr_write_b32 a48, v72\n"
      "v_accvgpr_write_b32 a49, v73\n"
      "v_accvgpr_write_b32 a50, v74\n"
      "v_accvgpr_write_b32 a51, v75\n"
      "v_mfma_f32_16x16x32_bf16 v[72:75], v[64:67], a[60:63], v[140:143]\n"
      "v_mfma_f32_16x16x32_bf16 v[76:79], v[68:71], a[60:63], v[140:143]\n"
      "v_cvt_pk_bf16_f32 v80, v80, v81\n"
      "v_cvt_pk_bf16_f32 v81, v82, v83\n"
      "v_cvt_pk_bf16_f32 v82, v84, v85\n"
      "v_cvt_pk_bf16_f32 v83, v86, v87\n"
      "v_accvgpr_write_b32 a52, v80\n"
      "v_accvgpr_write_b32 a53, v81\n"
      "v_accvgpr_write_b32 a54, v82\n"
      "v_accvgpr_write_b32 a55, v83\n"
      "v_mfma_f32_16x16x32_bf16 v[80:83], v[64:67], a[64:67], v[140:143]\n"
      "v_mfma_f32_16x16x32_bf16 v[84:87], v[68:71], a[64:67], v[140:143]\n"
      "v_cvt_pk_bf16_f32 v88, v88, v89\n"
      "v_cvt_pk_bf16_f32 v89, v90, v91\n"
      "v_cvt_pk_bf16_f32 v90, v92, v93\n"
      "v_cvt_pk_bf16_f32 v91, v94, v95\n"
      "v_accvgpr_write_b32 a56, v88\n"
      "v_accvgpr_write_b32 a57, v89\n"
      "v_accvgpr_write_b32 a58, v90\n"
      "v_accvgpr_write_b32 a59, v91\n"
      "v_mfma_f32_16x16x32_bf16 v[88:91], v[64:67], a[68:71], v[140:143]\n"
      "v_mfma_f32_16x16x32_bf16 v[92:95], v[68:71], a[68:71], v[140:143]\n"
      "v_cvt_pk_bf16_f32 v72, v72, v73\n"
      "v_cvt_pk_bf16_f32 v73, v74, v75\n"
      "v_cvt_pk_bf16_f32 v74, v76, v77\n"
      "v_cvt_pk_bf16_f32 v75, v78, v79\n"
      "v_accvgpr_write_b32 a60, v72\n"
      "v_accvgpr_write_b32 a61, v73\n"
      "v_accvgpr_write_b32 a62, v74\n"
      "v_accvgpr_write_b32 a63, v75\n"
      "v_mfma_f32_16x16x32_bf16 v[72:75], v[64:67], a[72:75], v[144:147]\n"
      "v_mfma_f32_16x16x32_bf16 v[76:79], v[68:71], a[72:75], v[144:147]\n"
      "v_cvt_pk_bf16_f32 v80, v80, v81\n"
      "v_cvt_pk_bf16_f32 v81, v82, v83\n"
      "v_cvt_pk_bf16_f32 v82, v84, v85\n"
      "v_cvt_pk_bf16_f32 v83, v86, v87\n"
      "v_accvgpr_write_b32 a64, v80\n"
      "v_accvgpr_write_b32 a65, v81\n"
      "v_accvgpr_write_b32 a66, v82\n"
      "v_accvgpr_write_b32 a67, v83\n"
      "v_mfma_f32_16x16x32_bf16 v[80:83], v[64:67], a[76:79], v[144:147]\n"
      "v_mfma_f32_16x16x32_bf16 v[84:87], v[68:71], a[76:79], v[144:147]\n"
      "v_cvt_pk_bf16_f32 v88, v88, v89\n"
      "v_cvt_pk_bf16_f32 v89, v90, v91\n"
      "v_cvt_pk_bf16_f32 v90, v92, v93\n"
      "v_cvt_pk_bf16_f32 v91, v94, v95\n"
      "v_accvgpr_write_b32 a68, v88\n"
      "v_accvgpr_write_b32 a69, v89\n"
      "v_accvgpr_write_b32 a70, v90\n"
      "v_accvgpr_write_b32 a71, v91\n"
      "v_mfma_f32_16x16x32_bf16 v[88:91], v[64:67], a[80:83], v[144:147]\n"
      "v_mfma_f32_16x16x32_bf16 v[92:95], v[68:71], a[80:83], v[144:147]\n"
      "v_cvt_pk_bf16_f32 v72, v72, v73\n"
      "v_cvt_pk_bf16_f32 v73, v74, v75\n"
      "v_cvt_pk_bf16_f32 v74, v76, v77\n"
      "v_cvt_pk_bf16_f32 v75, v78, v79\n"
      "v_accvgpr_write_b32 a72, v72\n"
      "v_accvgpr_write_b32 a73, v73\n"
      "v_accvgpr_write_b32 a74, v74\n"
      "v_accvgpr_write_b32 a75, v75\n"
      "v_mfma_f32_16x16x32_bf16 v[72:75], v[64:67], a[84:87], v[144:147]\n"
      "v_mfma_f32_16x16x32_bf16 v[76:79], v[68:71], a[84:87], v[144:147]\n"
      "v_cvt_pk_bf16_f32 v80, v80, v81\n"
      "v_cvt_pk_bf16_f32 v81, v82, v83\n"
      "v_cvt_pk_bf16_f32 v82, v84, v85\n"
      "v_cvt_pk_bf16_f32 v83, v86, v87\n"
      "v_accvgpr_write_b32 a76, v80\n"
      "v_accvgpr_write_b32 a77, v81\n"
      "v_accvgpr_write_b32 a78, v82\n"
      "v_accvgpr_write_b32 a79, v83\n"
      "v_mfma_f32_16x16x32_bf16 v[80:83], v[64:67], a[88:91], v[144:147]\n"
      "v_mfma_f32_16x16x32_bf16 v[84:87], v[68:71], a[88:91], v[144:147]\n"
      "v_cvt_pk_bf16_f32 v88, v88, v89\n"
      "v_cvt_pk_bf16_f32 v89, v90, v91\n"
      "v_cvt_pk_bf16_f32 v90, v92, v93\n"
      "v_cvt_pk_bf16_f32 v91, v94, v95\n"
      "v_accvgpr_write_b32 a80, v88\n"
      "v_accvgpr_write_b32 a81, v89\n"
      "v_accvgpr_write_b32 a82, v90\n"
      "v_accvgpr_write_b32 a83, v91\n"
      "v_mfma_f32_16x16x32_bf16 v[88:91], v[64:67], a[92:95], v[144:147]\n"
      "v_mfma_f32_16x16x32_bf16 v[92:95], v[68:71], a[92:95], v[144:147]\n"
      "v_cvt_pk_bf16_f32 v72, v72, v73\n"
      "v_cvt_pk_bf16_f32 v73, v74, v75\n"
      "v_cvt_pk_bf16_f32 v74, v76, v77\n"
      "v_cvt_pk_bf16_f32 v75, v78, v79\n"
      "v_accvgpr_write_b32 a84, v72\n"
      "v_accvgpr_write_b32 a85, v73\n"
      "v_accvgpr_write_b32 a86, v74\n"
      "v_accvgpr_write_b32 a87, v75\n"
      "v_mfma_f32_16x16x32_bf16 v[72:75], v[64:67], a[96:99], v[144:147]\n"
      "v_mfma_f32_16x16x32_bf16 v[76:79], v[68:71], a[96:99], v[144:147]\n"
      "v_cvt_pk_bf16_f32 v80, v80, v81\n"
      "v_cvt_pk_bf16_f32 v81, v82, v83\n"
      "v_cvt_pk_bf16_f32 v82, v84, v85\n"
      "v_cvt_pk_bf16_f32 v83, v86, v87\n"
      "v_accvgpr_write_b32 a88, v80\n"
      "v_accvgpr_write_b32 a89, v81\n"
      "v_accvgpr_write_b32 a90, v82\n"
      "v_accvgpr_write_b32 a91, v83\n"
      "v_mfma_f32_16x16x32_bf16 v[80:83], v[64:67], a[100:103], v[144:147]\n"
      "v_mfma_f32_16x16x32_bf16 v[84:87], v[68:71], a[100:103], v[144:147]\n"
      "v_cvt_pk_bf16_f32 v88, v88, v89\n"
      "v_cvt_pk_bf16_f32 v89, v90, v91\n"
      "v_cvt_pk_bf16_f32 v90, v92, v93\n"
      "v_cvt_pk_bf16_f32 v91, v94, v95\n"
      "v_accvgpr_write_b32 a92, v88\n"
      "v_accvgpr_write_b32 a93, v89\n"
      "v_accvgpr_write_b32 a94, v90\n"
      "v_accvgpr_write_b32 a95, v91\n"
      "v_mfma_f32_16x16x32_bf16 v[88:91], v[64:67], a[104:107], v[144:147]\n"
      "v_mfma_f32_16x16x32_bf16 v[92:95], v[68:71], a[104:107], v[144:147]\n"
      "v_cvt_pk_bf16_f32 v72, v72, v73\n"
      "v_cvt_pk_bf16_f32 v73, v74, v75\n"
      "v_cvt_pk_bf16_f32 v74, v76, v77\n"
      "v_cvt_pk_bf16_f32 v75, v78, v79\n"
      "v_accvgpr_write_b32 a96, v72\n"
      "v_accvgpr_write_b32 a97, v73\n"
      "v_accvgpr_write_b32 a98, v74\n"
      "v_accvgpr_write_b32 a99, v75\n"
      "v_mfma_f32_16x16x32_bf16 v[72:75], v[64:67], a[108:111], v[144:147]\n"
      "v_mfma_f32_16x16x32_bf16 v[76:79], v[68:71], a[108:111], v[144:147]\n"
      "v_cvt_pk_bf16_f32 v80, v80, v81\n"
      "v_cvt_pk_bf16_f32 v81, v82, v83\n"
      "v_cvt_pk_bf16_f32 v82, v84, v85\n"
      "v_cvt_pk_bf16_f32 v83, v86, v87\n"
      "v_accvgpr_write_b32 a100, v80\n"
      "v_accvgpr_write_b32 a101, v81\n"
      "v_accvgpr_write_b32 a102, v82\n"
      "v_accvgpr_write_b32 a103, v83\n"
      "v_mfma_f32_16x16x32_bf16 v[80:83], v[64:67], a[112:115], v[144:147]\n"
      "v_mfma_f32_16x16x32_bf16 v[84:87], v[68:71], a[112:115], v[144:147]\n"
      "v_cvt_pk_bf16_f32 v88, v88, v89\n"
      "v_cvt_pk_bf16_f32 v89, v90, v91\n"
      "v_cvt_pk_bf16_f32 v90, v92, v93\n"
      "v_cvt_pk_bf16_f32 v91, v94, v95\n"
      "v_accvgpr_write_b32 a104, v88\n"
      "v_accvgpr_write_b32 a105, v89\n"
      "v_accvgpr_write_b32 a106, v90\n"
      "v_accvgpr_write_b32 a107, v91\n"
      "v_mfma_f32_16x16x32_bf16 v[88:91], v[64:67], a[116:119], v[144:147]\n"
      "v_mfma_f32_16x16x32_bf16 v[92:95], v[68:71], a[116:119], v[144:147]\n"
      "v_cvt_pk_bf16_f32 v72, v72, v73\n"
      "v_cvt_pk_bf16_f32 v73, v74, v75\n"
      "v_cvt_pk_bf16_f32 v74, v76, v77\n"
      "v_cvt_pk_bf16_f32 v75, v78, v79\n"
      "v_accvgpr_write_b32 a108, v72\n"
      "v_accvgpr_write_b32 a109, v73\n"
      "v_accvgpr_write_b32 a110, v74\n"
      "v_accvgpr_write_b32 a111, v75\n"
      "v_mfma_f32_16x16x32_bf16 v[72:75], v[64:67], a[120:123], v[144:147]\n"
      "v_mfma_f32_16x16x32_bf16 v[76:79], v[68:71], a[120:123], v[144:147]\n"
      "v_cvt_pk_bf16_f32 v80, v80, v81\n"
      "v_cvt_pk_bf16_f32 v81, v82, v83\n"
      "v_cvt_pk_bf16_f32 v82, v84, v85\n"
      "v_cvt_pk_bf16_f32 v83, v86, v87\n"
      "v_accvgpr_write_b32 a112, v80\n"
      "v_accvgpr_write_b32 a113, v81\n"
      "v_accvgpr_write_b32 a114, v82\n"
      "v_accvgpr_write_b32 a115, v83\n"
      "v_mfma_f32_16x16x32_bf16 v[80:83], v[64:67], a[124:127], v[144:147]\n"
      "v_mfma_f32_16x16x32_bf16 v[84:87], v[68:71], a[124:127], v[144:147]\n"
      "v_cvt_pk_bf16_f32 v88, v88, v89\n"
      "v_cvt_pk_bf16_f32 v89, v90, v91\n"
      "v_cvt_pk_bf16_f32 v90, v92, v93\n"
      "v_cvt_pk_bf16_f32 v91, v94, v95\n"
      "v_accvgpr_write_b32 a116, v88\n"
      "v_accvgpr_write_b32 a117, v89\n"
      "v_accvgpr_write_b32 a118, v90\n"
      "v_accvgpr_write_b32 a119, v91\n"
      "v_mfma_f32_16x16x32_bf16 v[88:91], v[64:67], a[128:131], v[144:147]\n"
      "v_mfma_f32_16x16x32_bf16 v[92:95], v[68:71], a[128:131], v[144:147]\n"
      "v_cvt_pk_bf16_f32 v72, v72, v73\n"
      "v_cvt_pk_bf16_f32 v73, v74, v75\n"
      "v_cvt_pk_bf16_f32 v74, v76, v77\n"
      "v_cvt_pk_bf16_f32 v75, v78, v79\n"
      "v_accvgpr_write_b32 a120, v72\n"
      "v_accvgpr_write_b32 a121, v73\n"
      "v_accvgpr_write_b32 a122, v74\n"
      "v_accvgpr_write_b32 a123, v75\n"
      "v_mfma_f32_16x16x32_bf16 v[72:75], v[64:67], a[132:135], v[144:147]\n"
      "v_mfma_f32_16x16x32_bf16 v[76:79], v[68:71], a[132:135], v[144:147]\n"
      "v_cvt_pk_bf16_f32 v80, v80, v81\n"
      "v_cvt_pk_bf16_f32 v81, v82, v83\n"
      "v_cvt_pk_bf16_f32 v82, v84, v85\n"
      "v_cvt_pk_bf16_f32 v83, v86, v87\n"
      "v_accvgpr_write_b32 a124, v80\n"
      "v_accvgpr_write_b32 a125, v81\n"
      "v_accvgpr_write_b32 a126, v82\n"
      "v_accvgpr_write_b32 a127, v83\n"
      "v_mfma_f32_16x16x32_bf16 v[80:83], v[64:67], a[136:139], v[144:147]\n"
      "v_mfma_f32_16x16x32_bf16 v[84:87], v[68:71], a[136:139], v[144:147]\n"
      "v_cvt_pk_bf16_f32 v88, v88, v89\n"
      "v_cvt_pk_bf16_f32 v89, v90, v91\n"
      "v_cvt_pk_bf16_f32 v90, v92, v93\n"
      "v_cvt_pk_bf16_f32 v91, v94, v95\n"
      "v_accvgpr_write_b32 a128, v88\n"
      "v_accvgpr_write_b32 a129, v89\n"
      "v_accvgpr_write_b32 a130, v90\n"
      "v_accvgpr_write_b32 a131, v91\n"
      "v_mfma_f32_16x16x32_bf16 v[88:91], v[64:67], a[140:143], v[144:147]\n"
      "v_mfma_f32_16x16x32_bf16 v[92:95], v[68:71], a[140:143], v[144:147]\n"
      "v_cvt_pk_bf16_f32 v72, v72, v73\n"
      "v_cvt_pk_bf16_f32 v73, v74, v75\n"
      "v_cvt_pk_bf16_f32 v74, v76, v77\n"
      "v_cvt_pk_bf16_f32 v75, v78, v79\n"
      "v_accvgpr_write_b32 a132, v72\n"
      "v_accvgpr_write_b32 a133, v73\n"
      "v_accvgpr_write_b32 a134, v74\n"
      "v_accvgpr_write_b32 a135, v75\n"
      "v_mfma_f32_16x16x32_bf16 v[72:75], v[64:67], a[144:147], v[148:151]\n"
      "v_mfma_f32_16x16x32_bf16 v[76:79], v[68:71], a[144:147], v[148:151]\n"
      "v_cvt_pk_bf16_f32 v80, v80, v81\n"
      "v_cvt_pk_bf16_f32 v81, v82, v83\n"
      "v_cvt_pk_bf16_f32 v82, v84, v85\n"
      "v_cvt_pk_bf16_f32 v83, v86, v87\n"
      "v_accvgpr_write_b32 a136, v80\n"
      "v_accvgpr_write_b32 a137, v81\n"
      "v_accvgpr_write_b32 a138, v82\n"
      "v_accvgpr_write_b32 a139, v83\n"
      "v_mfma_f32_16x16x32_bf16 v[80:83], v[64:67], a[148:151], v[148:151]\n"
      "v_mfma_f32_16x16x32_bf16 v[84:87], v[68:71], a[148:151], v[148:151]\n"
      "v_cvt_pk_bf16_f32 v88, v88, v89\n"
      "v_cvt_pk_bf16_f32 v89, v90, v91\n"
      "v_cvt_pk_bf16_f32 v90, v92, v93\n"
      "v_cvt_pk_bf16_f32 v91, v94, v95\n"
      "v_accvgpr_write_b32 a140, v88\n"
      "v_accvgpr_write_b32 a141, v89\n"
      "v_accvgpr_write_b32 a142, v90\n"
      "v_accvgpr_write_b32 a143, v91\n"
      "v_mfma_f32_16x16x32_bf16 v[88:91], v[64:67], a[152:155], v[148:151]\n"
      "v_mfma_f32_16x16x32_bf16 v[92:95], v[68:71], a[152:155], v[148:151]\n"
      "v_cvt_pk_bf16_f32 v72, v72, v73\n"
      "v_cvt_pk_bf16_f32 v73, v74, v75\n"
      "v_cvt_pk_bf16_f32 v74, v76, v77\n"
      "v_cvt_pk_bf16_f32 v75, v78, v79\n"
      "v_accvgpr_write_b32 a144, v72\n"
      "v_accvgpr_write_b32 a145, v73\n"
      "v_accvgpr_write_b32 a146, v74\n"
      "v_accvgpr_write_b32 a147, v75\n"
      "v_mfma_f32_16x16x32_bf16 v[72:75], v[64:67], a[156:159], v[148:151]\n"
      "v_mfma_f32_16x16x32_bf16 v[76:79], v[68:71], a[156:159], v[148:151]\n"
      "v_cvt_pk_bf16_f32 v80, v80, v81\n"
      "v_cvt_pk_bf16_f32 v81, v82, v83\n"
      "v_cvt_pk_bf16_f32 v82, v84, v85\n"
      "v_cvt_pk_bf16_f32 v83, v86, v87\n"
      "v_accvgpr_write_b32 a148, v80\n"
      "v_accvgpr_write_b32 a149, v81\n"
      "v_accvgpr_write_b32 a150, v82\n"
      "v_accvgpr_write_b32 a151, v83\n"
      "v_mfma_f32_16x16x32_bf16 v[80:83], v[64:67], a[160:163], v[148:151]\n"
      "v_mfma_f32_16x16x32_bf16 v[84:87], v[68:71], a[160:163], v[148:151]\n"
      "v_cvt_pk_bf16_f32 v88, v88, v89\n"
      "v_cvt_pk_bf16_f32 v89, v90, v91\n"
      "v_cvt_pk_bf16_f32 v90, v92, v93\n"
      "v_cvt_pk_bf16_f32 v91, v94, v95\n"
      "v_accvgpr_write_b32 a152, v88\n"
      "v_accvgpr_write_b32 a153, v89\n"
      "v_accvgpr_write_b32 a154, v90\n"
      "v_accvgpr_write_b32 a155, v91\n"
      "v_mfma_f32_16x16x32_bf16 v[88:91], v[64:67], a[164:167], v[148:151]\n"
      "v_mfma_f32_16x16x32_bf16 v[92:95], v[68:71], a[164:167], v[148:151]\n"
      "v_cvt_pk_bf16_f32 v72, v72, v73\n"
      "v_cvt_pk_bf16_f32 v73, v74, v75\n"
      "v_cvt_pk_bf16_f32 v74, v76, v77\n"
      "v_cvt_pk_bf16_f32 v75, v78, v79\n"
      "v_accvgpr_write_b32 a156, v72\n"
      "v_accvgpr_write_b32 a157, v73\n"
      "v_accvgpr_write_b32 a158, v74\n"
      "v_accvgpr_write_b32 a159, v75\n"
      "v_mfma_f32_16x16x32_bf16 v[72:75], v[64:67], a[168:171], v[148:151]\n"
      "v_mfma_f32_16x16x32_bf16 v[76:79], v[68:71], a[168:171], v[148:151]\n"
      "v_cvt_pk_bf16_f32 v80, v80, v81\n"
      "v_cvt_pk_bf16_f32 v81, v82, v83\n"
      "v_cvt_pk_bf16_f32 v82, v84, v85\n"
      "v_cvt_pk_bf16_f32 v83, v86, v87\n"
      "v_accvgpr_write_b32 a160, v80\n"
      "v_accvgpr_write_b32 a161, v81\n"
      "v_accvgpr_write_b32 a162, v82\n"
      "v_accvgpr_write_b32 a163, v83\n"
      "v_mfma_f32_16x16x32_bf16 v[80:83], v[64:67], a[172:175], v[148:151]\n"
      "v_mfma_f32_16x16x32_bf16 v[84:87], v[68:71], a[172:175], v[148:151]\n"
      "v_cvt_pk_bf16_f32 v88, v88, v89\n"
      "v_cvt_pk_bf16_f32 v89, v90, v91\n"
      "v_cvt_pk_bf16_f32 v90, v92, v93\n"
      "v_cvt_pk_bf16_f32 v91, v94, v95\n"
      "v_accvgpr_write_b32 a164, v88\n"
      "v_accvgpr_write_b32 a165, v89\n"
      "v_accvgpr_write_b32 a166, v90\n"
      "v_accvgpr_write_b32 a167, v91\n"
      "v_mfma_f32_16x16x32_bf16 v[88:91], v[64:67], a[176:179], v[148:151]\n"
      "v_mfma_f32_16x16x32_bf16 v[92:95], v[68:71], a[176:179], v[148:151]\n"
      "v_cvt_pk_bf16_f32 v72, v72, v73\n"
      "v_cvt_pk_bf16_f32 v73, v74, v75\n"
      "v_cvt_pk_bf16_f32 v74, v76, v77\n"
      "v_cvt_pk_bf16_f32 v75, v78, v79\n"
      "v_accvgpr_write_b32 a168, v72\n"
      "v_accvgpr_write_b32 a169, v73\n"
      "v_accvgpr_write_b32 a170, v74\n"
      "v_accvgpr_write_b32 a171, v75\n"
      "v_mfma_f32_16x16x32_bf16 v[72:75], v[64:67], a[180:183], v[148:151]\n"
      "v_mfma_f32_16x16x32_bf16 v[76:79], v[68:71], a[180:183], v[148:151]\n"
      "v_cvt_pk_bf16_f32 v80, v80, v81\n"
      "v_cvt_pk_bf16_f32 v81, v82, v83\n"
      "v_cvt_pk_bf16_f32 v82, v84, v85\n"
      "v_cvt_pk_bf16_f32 v83, v86, v87\n"
      "v_accvgpr_write_b32 a172, v80\n"
      "v_accvgpr_write_b32 a173, v81\n"
      "v_accvgpr_write_b32 a174, v82\n"
      "v_accvgpr_write_b32 a175, v83\n"
      "v_mfma_f32_16x16x32_bf16 v[80:83], v[64:67], a[184:187], v[148:151]\n"
      "v_mfma_f32_16x16x32_bf16 v[84:87], v[68:71], a[184:187], v[148:151]\n"
      "v_cvt_pk_bf16_f32 v88, v88, v89\n"
      "v_cvt_pk_bf16_f32 v89, v90, v91\n"
      "v_cvt_pk_bf16_f32 v90, v92, v93\n"
      "v_cvt_pk_bf16_f32 v91, v94, v95\n"
      "v_accvgpr_write_b32 a176, v88\n"
      "v_accvgpr_write_b32 a177, v89\n"
      "v_accvgpr_write_b32 a178, v90\n"
      "v_accvgpr_write_b32 a179, v91\n"
      "v_mfma_f32_16x16x32_bf16 v[88:91], v[64:67], a[188:191], v[148:151]\n"
      "v_mfma_f32_16x16x32_bf16 v[92:95], v[68:71], a[188:191], v[148:151]\n"
      "v_cvt_pk_bf16_f32 v72, v72, v73\n"
      "v_cvt_pk_bf16_f32 v73, v74, v75\n"
      "v_cvt_pk_bf16_f32 v74, v76, v77\n"
      "v_cvt_pk_bf16_f32 v75, v78, v79\n"
      "v_accvgpr_write_b32 a180, v72\n"
      "v_accvgpr_write_b32 a181, v73\n"
      "v_accvgpr_write_b32 a182, v74\n"
      "v_accvgpr_write_b32 a183, v75\n"
      "v_mfma_f32_16x16x32_bf16 v[72:75], v[64:67], a[192:195], v[148:151]\n"
      "v_mfma_f32_16x16x32_bf16 v[76:79], v[68:71], a[192:195], v[148:151]\n"
      "v_cvt_pk_bf16_f32 v80, v80, v81\n"
      "v_cvt_pk_bf16_f32 v81, v82, v83\n"
      "v_cvt_pk_bf16_f32 v82, v84, v85\n"
      "v_cvt_pk_bf16_f32 v83, v86, v87\n"
      "v_accvgpr_write_b32 a184, v80\n"
      "v_accvgpr_write_b32 a185, v81\n"
      "v_accvgpr_write_b32 a186, v82\n"
      "v_accvgpr_write_b32 a187, v83\n"
      "v_mfma_f32_16x16x32_bf16 v[80:83], v[64:67], a[196:199], v[148:151]\n"
      "v_mfma_f32_16x16x32_bf16 v[84:87], v[68:71], a[196:199], v[148:151]\n"
      "v_cvt_pk_bf16_f32 v88, v88, v89\n"
      "v_cvt_pk_bf16_f32 v89, v90, v91\n"
      "v_cvt_pk_bf16_f32 v90, v92, v93\n"
      "v_cvt_pk_bf16_f32 v91, v94, v95\n"
      "v_accvgpr_write_b32 a188, v88\n"
      "v_accvgpr_write_b32 a189, v89\n"
      "v_accvgpr_write_b32 a190, v90\n"
      "v_accvgpr_write_b32 a191, v91\n"
      "v_mfma_f32_16x16x32_bf16 v[88:91], v[64:67], a[200:203], v[148:151]\n"
      "v_mfma_f32_16x16x32_bf16 v[92:95], v[68:71], a[200:203], v[148:151]\n"
      "v_cvt_pk_bf16_f32 v72, v72, v73\n"
      "v_cvt_pk_bf16_f32 v73, v74, v75\n"
      "v_cvt_pk_bf16_f32 v74, v76, v77\n"
      "v_cvt_pk_bf16_f32 v75, v78, v79\n"
      "v_accvgpr_write_b32 a192, v72\n"
      "v_accvgpr_write_b32 a193, v73\n"
      "v_accvgpr_write_b32 a194, v74\n"
      "v_accvgpr_write_b32 a195, v75\n"
      "v_mfma_f32_16x16x32_bf16 v[72:75], v[64:67], a[204:207], v[148:151]\n"
      "v_mfma_f32_16x16x32_bf16 v[76:79], v[68:71], a[204:207], v[148:151]\n"
      "v_cvt_pk_bf16_f32 v80, v80, v81\n"
      "v_cvt_pk_bf16_f32 v81, v82, v83\n"
      "v_cvt_pk_bf16_f32 v82, v84, v85\n"
      "v_cvt_pk_bf16_f32 v83, v86, v87\n"
      "v_accvgpr_write_b32 a196, v80\n"
      "v_accvgpr_write_b32 a197, v81\n"
      "v_accvgpr_write_b32 a198, v82\n"
      "v_accvgpr_write_b32 a199, v83\n"
      "v_mfma_f32_16x16x32_bf16 v[80:83], v[64:67], a[208:211], v[148:151]\n"
      "v_mfma_f32_16x16x32_bf16 v[84:87], v[68:71], a[208:211], v[148:151]\n"
      "v_cvt_pk_bf16_f32 v88, v88, v89\n"
      "v_cvt_pk_bf16_f32 v89, v90, v91\n"
      "v_cvt_pk_bf16_f32 v90, v92, v93\n"
      "v_cvt_pk_bf16_f32 v91, v94, v95\n"
      "v_accvgpr_write_b32 a200, v88\n"
      "v_accvgpr_write_b32 a201, v89\n"
      "v_accvgpr_write_b32 a202, v90\n"
      "v_accvgpr_write_b32 a203, v91\n"
      "v_mfma_f32_16x16x32_bf16 v[88:91], v[64:67], a[212:215], v[148:151]\n"
      "v_mfma_f32_16x16x32_bf16 v[92:95], v[68:71], a[212:215], v[148:151]\n"
      "v_cvt_pk_bf16_f32 v72, v72, v73\n"
      "v_cvt_pk_bf16_f32 v73, v74, v75\n"
      "v_cvt_pk_bf16_f32 v74, v76, v77\n"
      "v_cvt_pk_bf16_f32 v75, v78, v79\n"
      "v_accvgpr_write_b32 a204, v72\n"
      "v_accvgpr_write_b32 a205, v73\n"
      "v_accvgpr_write_b32 a206, v74\n"
      "v_accvgpr_write_b32 a207, v75\n"
      "v_mfma_f32_16x16x32_bf16 v[72:75], v[64:67], a[216:219], v[152:155]\n"
      "v_mfma_f32_16x16x32_bf16 v[76:79], v[68:71], a[216:219], v[152:155]\n"
      "v_cvt_pk_bf16_f32 v80, v80, v81\n"
      "v_cvt_pk_bf16_f32 v81, v82, v83\n"
      "v_cvt_pk_bf16_f32 v82, v84, v85\n"
      "v_cvt_pk_bf16_f32 v83, v86, v87\n"
      "v_accvgpr_write_b32 a208, v80\n"
      "v_accvgpr_write_b32 a209, v81\n"
      "v_accvgpr_write_b32 a210, v82\n"
      "v_accvgpr_write_b32 a211, v83\n"
      "v_mfma_f32_16x16x32_bf16 v[80:83], v[64:67], a[220:223], v[152:155]\n"
      "v_mfma_f32_16x16x32_bf16 v[84:87], v[68:71], a[220:223], v[152:155]\n"
      "v_cvt_pk_bf16_f32 v88, v88, v89\n"
      "v_cvt_pk_bf16_f32 v89, v90, v91\n"
      "v_cvt_pk_bf16_f32 v90, v92, v93\n"
      "v_cvt_pk_bf16_f32 v91, v94, v95\n"
      "v_accvgpr_write_b32 a212, v88\n"
      "v_accvgpr_write_b32 a213, v89\n"
      "v_accvgpr_write_b32 a214, v90\n"
      "v_accvgpr_write_b32 a215, v91\n"
      "v_mfma_f32_16x16x32_bf16 v[88:91], v[64:67], a[224:227], v[152:155]\n"
      "v_mfma_f32_16x16x32_bf16 v[92:95], v[68:71], a[224:227], v[152:155]\n"
      "v_cvt_pk_bf16_f32 v72, v72, v73\n"
      "v_cvt_pk_bf16_f32 v73, v74, v75\n"
      "v_cvt_pk_bf16_f32 v74, v76, v77\n"
      "v_cvt_pk_bf16_f32 v75, v78, v79\n"
      "v_accvgpr_write_b32 a216, v72\n"
      "v_accvgpr_write_b32 a217, v73\n"
      "v_accvgpr_write_b32 a218, v74\n"
      "v_accvgpr_write_b32 a219, v75\n"
      "v_mfma_f32_16x16x32_bf16 v[72:75], v[64:67], a[228:231], v[152:155]\n"
      "v_mfma_f32_16x16x32_bf16 v[76:79], v[68:71], a[228:231], v[152:155]\n"
      "v_cvt_pk_bf16_f32 v80, v80, v81\n"
      "v_cvt_pk_bf16_f32 v81, v82, v83\n"
      "v_cvt_pk_bf16_f32 v82, v84, v85\n"
      "v_cvt_pk_bf16_f32 v83, v86, v87\n"
      "v_accvgpr_write_b32 a220, v80\n"
      "v_accvgpr_write_b32 a221, v81\n"
      "v_accvgpr_write_b32 a222, v82\n"
      "v_accvgpr_write_b32 a223, v83\n"
      "v_mfma_f32_16x16x32_bf16 v[80:83], v[64:67], a[232:235], v[152:155]\n"
      "v_mfma_f32_16x16x32_bf16 v[84:87], v[68:71], a[232:235], v[152:155]\n"
      "v_cvt_pk_bf16_f32 v88, v88, v89\n"
      "v_cvt_pk_bf16_f32 v89, v90, v91\n"
      "v_cvt_pk_bf16_f32 v90, v92, v93\n"
      "v_cvt_pk_bf16_f32 v91, v94, v95\n"
      "v_accvgpr_write_b32 a224, v88\n"
      "v_accvgpr_write_b32 a225, v89\n"
      "v_accvgpr_write_b32 a226, v90\n"
      "v_accvgpr_write_b32 a227, v91\n"
      "v_mfma_f32_16x16x32_bf16 v[88:91], v[64:67], a[236:239], v[152:155]\n"
      "v_mfma_f32_16x16x32_bf16 v[92:95], v[68:71], a[236:239], v[152:155]\n"
      "v_cvt_pk_bf16_f32 v72, v72, v73\n"
      "v_cvt_pk_bf16_f32 v73, v74, v75\n"
      "v_cvt_pk_bf16_f32 v74, v76, v77\n"
      "v_cvt_pk_bf16_f32 v75, v78, v79\n"
      "v_accvgpr_write_b32 a228, v72\n"
      "v_accvgpr_write_b32 a229, v73\n"
      "v_accvgpr_write_b32 a230, v74\n"
      "v_accvgpr_write_b32 a231, v75\n"
      "v_mfma_f32_16x16x32_bf16 v[72:75], v[64:67], a[240:243], v[152:155]\n"
      "v_mfma_f32_16x16x32_bf16 v[76:79], v[68:71], a[240:243], v[152:155]\n"
      "v_cvt_pk_bf16_f32 v80, v80, v81\n"
      "v_cvt_pk_bf16_f32 v81, v82, v83\n"
      "v_cvt_pk_bf16_f32 v82, v84, v85\n"
      "v_cvt_pk_bf16_f32 v83, v86, v87\n"
      "v_accvgpr_write_b32 a232, v80\n"
      "v_accvgpr_write_b32 a233, v81\n"
      "v_accvgpr_write_b32 a234, v82\n"
      "v_accvgpr_write_b32 a235, v83\n"
      "v_mfma_f32_16x16x32_bf16 v[80:83], v[64:67], a[244:247], v[152:155]\n"
      "v_mfma_f32_16x16x32_bf16 v[84:87], v[68:71], a[244:247], v[152:155]\n"
      "v_cvt_pk_bf16_f32 v88, v88, v89\n"
      "v_cvt_pk_bf16_f32 v89, v90, v91\n"
      "v_cvt_pk_bf16_f32 v90, v92, v93\n"
      "v_cvt_pk_bf16_f32 v91, v94, v95\n"
      "v_accvgpr_write_b32 a236, v88\n"
      "v_accvgpr_write_b32 a237, v89\n"
      "v_accvgpr_write_b32 a238, v90\n"
      "v_accvgpr_write_b32 a239, v91\n"
      "v_mfma_f32_16x16x32_bf16 v[88:91], v[64:67], a[248:251], v[152:155]\n"
      "v_mfma_f32_16x16x32_bf16 v[92:95], v[68:71], a[248:251], v[152:155]\n"
      "v_cvt_pk_bf16_f32 v72, v72, v73\n"
      "v_cvt_pk_bf16_f32 v73, v74, v75\n"
      "v_cvt_pk_bf16_f32 v74, v76, v77\n"
      "v_cvt_pk_bf16_f32 v75, v78, v79\n"
      "v_accvgpr_write_b32 a240, v72\n"
      "v_accvgpr_write_b32 a241, v73\n"
      "v_accvgpr_write_b32 a242, v74\n"
      "v_accvgpr_write_b32 a243, v75\n"
      "v_mfma_f32_16x16x32_bf16 v[72:75], v[64:67], a[252:255], v[152:155]\n"
      "v_mfma_f32_16x16x32_bf16 v[76:79], v[68:71], a[252:255], v[152:155]\n"
      "v_cvt_pk_bf16_f32 v80, v80, v81\n"
      "v_cvt_pk_bf16_f32 v81, v82, v83\n"
      "v_cvt_pk_bf16_f32 v82, v84, v85\n"
      "v_cvt_pk_bf16_f32 v83, v86, v87\n"
      "v_accvgpr_write_b32 a244, v80\n"
      "v_accvgpr_write_b32 a245, v81\n"
      "v_accvgpr_write_b32 a246, v82\n"
      "v_accvgpr_write_b32 a247, v83\n"
      "v_mfma_f32_16x16x32_bf16 v[80:83], v[64:67], v[20:23], v[152:155]\n"
      "v_mfma_f32_16x16x32_bf16 v[84:87], v[68:71], v[20:23], v[152:155]\n"
      "v_cvt_pk_bf16_f32 v88, v88, v89\n"
      "v_cvt_pk_bf16_f32 v89, v90, v91\n"
      "v_cvt_pk_bf16_f32 v90, v92, v93\n"
      "v_cvt_pk_bf16_f32 v91, v94, v95\n"
      "v_accvgpr_write_b32 a248, v88\n"
      "v_accvgpr_write_b32 a249, v89\n"
      "v_accvgpr_write_b32 a250, v90\n"
      "v_accvgpr_write_b32 a251, v91\n"
      "v_mfma_f32_16x16x32_bf16 v[88:91], v[64:67], v[24:27], v[152:155]\n"
      "v_mfma_f32_16x16x32_bf16 v[92:95], v[68:71], v[24:27], v[152:155]\n"
      "v_cvt_pk_bf16_f32 v72, v72, v73\n"
      "v_cvt_pk_bf16_f32 v73, v74, v75\n"
      "v_cvt_pk_bf16_f32 v74, v76, v77\n"
      "v_cvt_pk_bf16_f32 v75, v78, v79\n"
      "v_accvgpr_write_b32 a252, v72\n"
      "v_accvgpr_write_b32 a253, v73\n"
      "v_accvgpr_write_b32 a254, v74\n"
      "v_accvgpr_write_b32 a255, v75\n"
      "v_mfma_f32_16x16x32_bf16 v[72:75], v[64:67], v[28:31], v[152:155]\n"
      "v_mfma_f32_16x16x32_bf16 v[76:79], v[68:71], v[28:31], v[152:155]\n"
      "v_cvt_pk_bf16_f32 v20, v80, v81\n"
      "v_cvt_pk_bf16_f32 v21, v82, v83\n"
      "v_cvt_pk_bf16_f32 v22, v84, v85\n"
      "v_cvt_pk_bf16_f32 v23, v86, v87\n"
      "v_mfma_f32_16x16x32_bf16 v[80:83], v[64:67], v[32:35], v[152:155]\n"
      "v_mfma_f32_16x16x32_bf16 v[84:87], v[68:71], v[32:35], v[152:155]\n"
      "v_cvt_pk_bf16_f32 v24, v88, v89\n"
      "v_cvt_pk_bf16_f32 v25, v90, v91\n"
      "v_cvt_pk_bf16_f32 v26, v92, v93\n"
      "v_cvt_pk_bf16_f32 v27, v94, v95\n"
      "v_mfma_f32_16x16x32_bf16 v[88:91], v[64:67], v[36:39], v[152:155]\n"
      "v_mfma_f32_16x16x32_bf16 v[92:95], v[68:71], v[36:39], v[152:155]\n"
      "v_cvt_pk_bf16_f32 v28, v72, v73\n"
      "v_cvt_pk_bf16_f32 v29, v74, v75\n"
      "v_cvt_pk_bf16_f32 v30, v76, v77\n"
      "v_cvt_pk_bf16_f32 v31, v78, v79\n"
      "v_mfma_f32_16x16x32_bf16 v[72:75], v[64:67], v[40:43], v[152:155]\n"
      "v_mfma_f32_16x16x32_bf16 v[76:79], v[68:71], v[40:43], v[152:155]\n"
      "v_cvt_pk_bf16_f32 v32, v80, v81\n"
      "v_cvt_pk_bf16_f32 v33, v82, v83\n"
      "v_cvt_pk_bf16_f32 v34, v84, v85\n"
      "v_cvt_pk_bf16_f32 v35, v86, v87\n"
      "v_mfma_f32_16x16x32_bf16 v[80:83], v[64:67], v[44:47], v[152:155]\n"
      "v_mfma_f32_16x16x32_bf16 v[84:87], v[68:71], v[44:47], v[152:155]\n"
      "v_cvt_pk_bf16_f32 v36, v88, v89\n"
      "v_cvt_pk_bf16_f32 v37, v90, v91\n"
      "v_cvt_pk_bf16_f32 v38, v92, v93\n"
      "v_cvt_pk_bf16_f32 v39, v94, v95\n"
      "v_mfma_f32_16x16x32_bf16 v[88:91], v[64:67], v[48:51], v[152:155]\n"
      "v_mfma_f32_16x16x32_bf16 v[92:95], v[68:71], v[48:51], v[152:155]\n"
      "v_cvt_pk_bf16_f32 v40, v72, v73\n"
      "v_cvt_pk_bf16_f32 v41, v74, v75\n"
      "v_cvt_pk_bf16_f32 v42, v76, v77\n"
      "v_cvt_pk_bf16_f32 v43, v78, v79\n"
      "v_cvt_pk_bf16_f32 v44, v80, v81\n"
      "v_cvt_pk_bf16_f32 v45, v82, v83\n"
      "v_cvt_pk_bf16_f32 v46, v84, v85\n"
      "v_cvt_pk_bf16_f32 v47, v86, v87\n"
      "v_cvt_pk_bf16_f32 v48, v88, v89\n"
      "v_cvt_pk_bf16_f32 v49, v90, v91\n"
      "v_cvt_pk_bf16_f32 v50, v92, v93\n"
      "v_cvt_pk_bf16_f32 v51, v94, v95\n"
      "v_add_u32 v17, 40960, v0\n"
      "s_barrier\n"
      "v_mov_b32 v11, 0\n"
      "ds_write_b32 v13, v11\n"
      "s_waitcnt lgkmcnt(0)\n"
      "s_barrier\n"
      "s_add_u32 s6, s36, 1548288\n"
      "s_add_u32 s36, s36, 4294942720\n"
      "s_add_u32 s42, s37, 4294635520\n"
      "s_add_u32 s37, s37, 4294801408\n"
      "s_add_u32 s36, s36, 24576\n"
      "s_min_u32 s36, s36, s6\n"
      "buffer_load_dword v18, v2, s[8:11], s36 offen sc1\n"
      "s_add_u32 m0, s38, 0\n"
      "s_mov_b32 s40, s36\n"
      "s_waitcnt vmcnt(0)\n"
      "v_cmp_ne_u32 vcc, s21, v18\n"
      "s_add_u32 s36, s36, 24576\n"
      "s_cbranch_vccnz .Lsp_g0_1\n"
      ".Lbk_g0_1:\n"
      "buffer_load_dwordx4 v0, s[8:11], s40 offen offset:0 sc1 lds\n"
      "buffer_load_dwordx4 v0, s[8:11], s40 offen offset:1024 sc1 lds\n"
      "buffer_load_dwordx4 v0, s[8:11], s40 offen offset:2048 sc1 lds\n"
      "buffer_load_dwordx4 v0, s[8:11], s40 offen offset:3072 sc1 lds\n"
      "s_add_u32 m0, s39, 0\n"
      "s_nop 0\n"
      "buffer_load_dwordx4 v1, s[8:11], s40 offen offset:0 sc1 lds\n"
      "s_min_u32 s36, s36, s6\n"
      "buffer_load_dword v18, v2, s[8:11], s36 offen sc1\n"
      "v_add_u32 v11, 1, v11\n"
      "s_add_u32 m0, s38, 20480\n"
      "s_mov_b32 s40, s36\n"
      "s_waitcnt vmcnt(1)\n"
      "ds_write_b32 v13, v11\n"
      "s_waitcnt vmcnt(0)\n"
      "v_cmp_ne_u32 vcc, s21, v18\n"
      "s_add_u32 s36, s36, 24576\n"
      "s_cbranch_vccnz .Lsp_g0_2\n"
      ".Lbk_g0_2:\n"
      "buffer_load_dwordx4 v0, s[8:11], s40 offen offset:0 sc1 lds\n"
      "ds_read_b32 v12, v14\n"
      "buffer_load_dwordx4 v0, s[8:11], s40 offen offset:1024 sc1 lds\n"
      "s_waitcnt lgkmcnt(0)\n"
      "v_cmp_gt_i32 vcc, v11, v12\n"
      "buffer_load_dwordx4 v0, s[8:11], s40 offen offset:2048 sc1 lds\n"
      "s_cbranch_vccnz .Lsp_gy0_3\n"
      ".Lbk_gy0_3:\n"
      "ds_read_b128 v[52:55], v0 offset:0\n"
      "ds_read_b128 v[60:63], v0 offset:2048\n"
      "buffer_load_dwordx4 v0, s[8:11], s40 offen offset:3072 sc1 lds\n"
      "s_add_u32 m0, s39, 20480\n"
      "ds_read_b128 v[68:71], v0 offset:4096\n"
      "ds_read2st64_b64 v[168:171], v4 offset0:0 offset1:1\n"
      "ds_read_b128 v[56:59], v0 offset:1024\n"
      "buffer_load_dwordx4 v1, s[8:11], s40 offen offset:0 sc1 lds\n"
      "ds_read_b128 v[64:67], v0 offset:3072\n"
      "s_min_u32 s36, s36, s6\n"
      "buffer_load_dword v18, v2, s[8:11], s36 offen sc1\n"
      "ds_read_b128 v[72:75], v0 offset:5120\n"
      "ds_read_b128 v[76:79], v0 offset:6144\n"
      "buffer_load_dword v16, v13, s[8:11], s31 offen\n"
      "ds_read_b128 v[80:83], v0 offset:7168\n"
      "ds_read_b128 v[92:95], v0 offset:10240\n"
      "s_mov_b32 s20, 15\n"
      "s_waitcnt lgkmcnt(0)\n"
      "s_nop 4\n"
      "s_branch .Lg_loop\n"
      ".p2align 12\n"
      ".Lg_p0:\n"
      "s_branch .Lg_pr\n"
      ".p2align 5\n"
      ".Lg_loop:\n"
      "v_mfma_f32_16x16x32_bf16 v[124:127], a[0:3], v[52:55], 0\n"
      "ds_read_b128 v[84:87], v0 offset:8192\n"
      "ds_read_b128 v[104:107], v0 offset:13312\n"
      "v_mfma_f32_16x16x32_bf16 v[124:127], a[4:7], v[56:59], v[124:127]\n"
      "ds_read_b128 v[88:91], v0 offset:9216\n"
      "v_mul_f32 v130, v130, v158\n"
      "v_mfma_f32_16x16x32_bf16 v[124:127], a[8:11], v[60:63], v[124:127]\n"
      "s_waitcnt lgkmcnt(7)\n"
      "ds_read_b128 v[108:111], v0 offset:14336\n"
      "v_add_f32 v169, v169, v171\n"
      "v_mfma_f32_16x16x32_bf16 v[124:127], a[12:15], v[64:67], v[124:127]\n"
      "ds_read_b128 v[96:99], v0 offset:11264\n"
      "v_fma_f32 v161, v161, v177, v177\n"
      "v_mfma_f32_16x16x32_bf16 v[124:127], a[16:19], v[68:71], v[124:127]\n"
      "s_waitcnt lgkmcnt(7)\n"
      "ds_read_b128 v[100:103], v0 offset:12288\n"
      "v_mul_f32 v128, v128, v156\n"
      "v_mfma_f32_16x16x32_bf16 v[124:127], a[20:23], v[72:75], v[124:127]\n"
      "v_rcp_f32 v161, v161\n"
      "v_mfma_f32_16x16x32_bf16 v[124:127], a[24:27], v[76:79], v[124:127]\n"
      "s_waitcnt lgkmcnt(6)\n"
      "ds_read_b128 v[112:115], v0 offset:15360\n"
      "v_mul_f32 v129, v129, v157\n"
      "v_mfma_f32_16x16x32_bf16 v[124:127], a[28:31], v[80:83], v[124:127]\n"
      "s_waitcnt lgkmcnt(5)\n"
      "ds_read_b128 v[120:123], v0 offset:17408\n"
      "v_mul_f32 v131, v131, v159\n"
      "v_mfma_f32_16x16x32_bf16 v[124:127], a[32:35], v[84:87], v[124:127]\n"
      "s_waitcnt lgkmcnt(5)\n"
      "ds_read_b128 v[116:119], v0 offset:16384\n"
      "v_cvt_pk_bf16_f32 v165, v130, v131\n"
      "v_mfma_f32_16x16x32_bf16 v[124:127], a[36:39], v[88:91], v[124:127]\n"
      "v_cvt_pk_bf16_f32 v164, v128, v129\n"
      "v_mfma_f32_16x16x32_bf16 v[124:127], a[40:43], v[92:95], v[124:127]\n"
      "s_waitcnt lgkmcnt(4)\n"
      "v_fma_f32 v160, v160, v177, v177\n"
      "v_sub_f32 v176, -v168, v170\n"
      "v_mfma_f32_16x16x32_bf16 v[124:127], a[44:47], v[96:99], v[124:127]\n"
      "s_waitcnt lgkmcnt(3)\n"
      "v_rcp_f32 v160, v160\n"
      "v_mfma_f32_16x16x32_bf16 v[124:127], a[48:51], v[100:103], v[124:127]\n"
      "v_mul_f32 v161, v133, v161\n"
      "v_fma_f32 v169, -v176, v176, v169\n"
      "v_mfma_f32_16x16x32_bf16 v[124:127], a[52:55], v[104:107], v[124:127]\n"
      "v_max_f32 v177, 0x3727c5ac, v169\n"
      "v_mfma_f32_16x16x32_bf16 v[124:127], a[56:59], v[108:111], v[124:127]\n"
      "s_waitcnt lgkmcnt(1)\n"
      "v_mul_f32 v162, v134, v162\n"
      "v_mul_f32 v137, v137, v161\n"
      "v_mfma_f32_16x16x32_bf16 v[124:127], a[60:63], v[112:115], v[124:127]\n"
      "s_waitcnt lgkmcnt(0)\n"
      "v_mul_f32 v138, v138, v162\n"
      "v_mul_f32 v139, v139, v163\n"
      "v_mfma_f32_16x16x32_bf16 v[124:127], a[64:67], v[116:119], v[124:127]\n"
      "v_mul_f32 v160, v132, v160\n"
      "v_cvt_pk_bf16_f32 v167, v138, v139\n"
      "v_mfma_f32_16x16x32_bf16 v[124:127], a[68:71], v[120:123], v[124:127]\n"
      "v_mul_f32 v136, v136, v160\n"
      "v_cvt_pk_bf16_f32 v166, v136, v137\n"
      "v_mfma_f32_16x16x32_bf16 v[132:135], a[144:147], v[52:55], 0\n"
      "buffer_store_dwordx4 v[164:167], v0, s[8:11], s37 offen sc1\n"
      "v_mfma_f32_16x16x32_bf16 v[132:135], a[148:151], v[56:59], v[132:135]\n"
      "v_rsq_f32 v170, v177\n"
      "v_mfma_f32_16x16x32_bf16 v[132:135], a[152:155], v[60:63], v[132:135]\n"
      "v_mul_f32 v158, v126, v126\n"
      "v_mfma_f32_16x16x32_bf16 v[132:135], a[156:159], v[64:67], v[132:135]\n"
      "v_mul_f32 v178, 0xc0135761, v170\n"
      "v_mul_f32 v171, 0x3d372713, v178\n"
      "v_mfma_f32_16x16x32_bf16 v[132:135], a[160:163], v[68:71], v[132:135]\n"
      "v_mul_f32 v170, v170, v170\n"
      "v_mul_f32 v179, v171, v170\n"
      "v_mfma_f32_16x16x32_bf16 v[132:135], a[164:167], v[72:75], v[132:135]\n"
      "v_mul_f32 v159, v127, v127\n"
      "v_mfma_f32_16x16x32_bf16 v[132:135], a[168:171], v[76:79], v[132:135]\n"
      "v_mul_f32 v156, v124, v124\n"
      "v_fma_f32 v159, v159, v179, v178\n"
      "v_mfma_f32_16x16x32_bf16 v[132:135], a[172:175], v[80:83], v[132:135]\n"
      "v_mul_f32 v157, v125, v125\n"
      "v_fma_f32 v158, v158, v179, v178\n"
      "v_mfma_f32_16x16x32_bf16 v[132:135], a[176:179], v[84:87], v[132:135]\n"
      "s_add_u32 s42, s42, 165888\n"
      "v_add_u32 v11, 1, v11\n"
      "v_mul_f32 v158, v126, v158\n"
      "v_mfma_f32_16x16x32_bf16 v[132:135], a[180:183], v[88:91], v[132:135]\n"
      "v_fma_f32 v157, v157, v179, v178\n"
      "v_fma_f32 v156, v156, v179, v178\n"
      "v_mfma_f32_16x16x32_bf16 v[132:135], a[184:187], v[92:95], v[132:135]\n"
      "v_exp_f32 v158, v158\n"
      "v_mfma_f32_16x16x32_bf16 v[132:135], a[188:191], v[96:99], v[132:135]\n"
      "s_add_u32 m0, s38, 40960\n"
      "v_mfma_f32_16x16x32_bf16 v[132:135], a[192:195], v[100:103], v[132:135]\n"
      "s_mov_b32 s40, s36\n"
      "v_mul_f32 v156, v124, v156\n"
      "v_fma_f32 v158, v158, v177, v177\n"
      "v_mfma_f32_16x16x32_bf16 v[132:135], a[196:199], v[104:107], v[132:135]\n"
      "v_exp_f32 v156, v156\n"
      "v_mfma_f32_16x16x32_bf16 v[132:135], a[200:203], v[108:111], v[132:135]\n"
      "v_fma_f32 v156, v156, v177, v177\n"
      "v_mul_f32 v159, v127, v159\n"
      "v_mfma_f32_16x16x32_bf16 v[132:135], a[204:207], v[112:115], v[132:135]\n"
      "v_rcp_f32 v156, v156\n"
      "v_mfma_f32_16x16x32_bf16 v[132:135], a[208:211], v[116:119], v[132:135]\n"
      "v_exp_f32 v159, v159\n"
      "v_mfma_f32_16x16x32_bf16 v[132:135], a[212:215], v[120:123], v[132:135]\n"
      "v_fma_f32 v159, v159, v177, v177\n"
      "v_mfma_f32_16x16x32_bf16 v[128:131], a[72:75], v[52:55], 0\n"
      "s_waitcnt vmcnt(3)\n"
      "ds_write_b32 v13, v11\n"
      "v_mfma_f32_16x16x32_bf16 v[128:131], a[76:79], v[56:59], v[128:131]\n"
      "s_add_u32 s37, s37, 165888\n"
      "v_rcp_f32 v158, v158\n"
      "v_mfma_f32_16x16x32_bf16 v[128:131], a[80:83], v[60:63], v[128:131]\n"
      "v_mul_f32 v162, v134, v134\n"
      "v_mul_f32 v163, v135, v135\n"
      "v_mfma_f32_16x16x32_bf16 v[128:131], a[84:87], v[64:67], v[128:131]\n"
      "v_rcp_f32 v159, v159\n"
      "v_mfma_f32_16x16x32_bf16 v[128:131], a[88:91], v[68:71], v[128:131]\n"
      "s_waitcnt vmcnt(2)\n"
      "v_cmp_ne_u32 vcc, s21, v18\n"
      "v_fma_f32 v163, v163, v179, v178\n"
      "v_mfma_f32_16x16x32_bf16 v[128:131], a[92:95], v[72:75], v[128:131]\n"
      "v_fma_f32 v162, v162, v179, v178\n"
      "v_mul_f32 v162, v134, v162\n"
      "v_mfma_f32_16x16x32_bf16 v[128:131], a[96:99], v[76:79], v[128:131]\n"
      "s_add_u32 s36, s36, 24576\n"
      "v_mul_f32 v161, v133, v133\n"
      "v_mul_f32 v159, v127, v159\n"
      "v_mfma_f32_16x16x32_bf16 v[128:131], a[100:103], v[80:83], v[128:131]\n"
      "v_exp_f32 v162, v162\n"
      "v_mfma_f32_16x16x32_bf16 v[128:131], a[104:107], v[84:87], v[128:131]\n"
      "s_cbranch_vccnz .Lsp_g0_4\n"
      ".Lbk_g0_4:\n"
      "v_mul_f32 v160, v132, v132\n"
      "v_mfma_f32_16x16x32_bf16 v[128:131], a[108:111], v[88:91], v[128:131]\n"
      "buffer_load_dwordx4 v0, s[8:11], s40 offen offset:0 sc1 lds\n"
      "v_mfma_f32_16x16x32_bf16 v[128:131], a[112:115], v[92:95], v[128:131]\n"
      "ds_read_b32 v12, v14\n"
      "v_fma_f32 v161, v161, v179, v178\n"
      "v_mfma_f32_16x16x32_bf16 v[128:131], a[116:119], v[96:99], v[128:131]\n"
      "v_mul_f32 v156, v124, v156\n"
      "v_mul_f32 v161, v133, v161\n"
      "v_mfma_f32_16x16x32_bf16 v[128:131], a[120:123], v[100:103], v[128:131]\n"
      "v_fma_f32 v160, v160, v179, v178\n"
      "v_mul_f32 v163, v135, v163\n"
      "v_mfma_f32_16x16x32_bf16 v[128:131], a[124:127], v[104:107], v[128:131]\n"
      "buffer_load_dwordx4 v0, s[8:11], s40 offen offset:1024 sc1 lds\n"
      "v_mfma_f32_16x16x32_bf16 v[128:131], a[128:131], v[108:111], v[128:131]\n"
      "v_exp_f32 v163, v163\n"
      "v_mfma_f32_16x16x32_bf16 v[128:131], a[132:135], v[112:115], v[128:131]\n"
      "v_mul_f32 v157, v125, v157\n"
      "v_fma_f32 v163, v163, v177, v177\n"
      "v_mfma_f32_16x16x32_bf16 v[128:131], a[136:139], v[116:119], v[128:131]\n"
      "s_waitcnt lgkmcnt(0)\n"
      "v_cmp_gt_i32 vcc, v11, v12\n"
      "v_mul_f32 v160, v132, v160\n"
      "v_mfma_f32_16x16x32_bf16 v[128:131], a[140:143], v[120:123], v[128:131]\n"
      "buffer_load_dwordx4 v0, s[8:11], s40 offen offset:2048 sc1 lds\n"
      "v_mfma_f32_16x16x32_bf16 v[136:139], a[216:219], v[52:55], 0\n"
      "v_rcp_f32 v163, v163\n"
      "v_mfma_f32_16x16x32_bf16 v[136:139], a[220:223], v[56:59], v[136:139]\n"
      "s_cbranch_vccnz .Lsp_gy0_5\n"
      ".Lbk_gy0_5:\n"
      "ds_read_b128 v[52:55], v0 offset:20480\n"
      "v_mfma_f32_16x16x32_bf16 v[136:139], a[224:227], v[60:63], v[136:139]\n"
      "v_exp_f32 v157, v157\n"
      "v_mfma_f32_16x16x32_bf16 v[136:139], a[228:231], v[64:67], v[136:139]\n"
      "ds_read_b128 v[60:63], v0 offset:22528\n"
      "v_mul_f32 v163, v135, v163\n"
      "v_mfma_f32_16x16x32_bf16 v[136:139], a[232:235], v[68:71], v[136:139]\n"
      "buffer_load_dwordx4 v0, s[8:11], s40 offen offset:3072 sc1 lds\n"
      "v_mfma_f32_16x16x32_bf16 v[136:139], a[236:239], v[72:75], v[136:139]\n"
      "v_exp_f32 v161, v161\n"
      "v_mfma_f32_16x16x32_bf16 v[136:139], a[240:243], v[76:79], v[136:139]\n"
      "s_add_u32 m0, s39, 40960\n"
      "ds_read_b128 v[68:71], v0 offset:24576\n"
      "ds_read2st64_b64 v[168:171], v4 offset0:40 offset1:41\n"
      "v_mfma_f32_16x16x32_bf16 v[136:139], a[244:247], v[80:83], v[136:139]\n"
      "ds_read_b128 v[56:59], v0 offset:21504\n"
      "v_fma_f32 v162, v162, v177, v177\n"
      "v_mfma_f32_16x16x32_bf16 v[136:139], a[248:251], v[84:87], v[136:139]\n"
      "buffer_load_dwordx4 v1, s[8:11], s40 offen offset:0 sc1 lds\n"
      "v_mfma_f32_16x16x32_bf16 v[136:139], a[252:255], v[88:91], v[136:139]\n"
      "ds_read_b128 v[64:67], v0 offset:23552\n"
      "v_fma_f32 v157, v157, v177, v177\n"
      "v_mfma_f32_16x16x32_bf16 v[136:139], v[20:23], v[92:95], v[136:139]\n"
      "s_min_u32 s36, s36, s6\n"
      "v_rcp_f32 v162, v162\n"
      "v_mfma_f32_16x16x32_bf16 v[136:139], v[24:27], v[96:99], v[136:139]\n"
      "v_exp_f32 v160, v160\n"
      "v_mfma_f32_16x16x32_bf16 v[136:139], v[28:31], v[100:103], v[136:139]\n"
      "v_rcp_f32 v157, v157\n"
      "v_mfma_f32_16x16x32_bf16 v[136:139], v[32:35], v[104:107], v[136:139]\n"
      "buffer_load_dword v18, v2, s[8:11], s36 offen sc1\n"
      "v_mfma_f32_16x16x32_bf16 v[136:139], v[36:39], v[108:111], v[136:139]\n"
      "ds_read_b128 v[72:75], v0 offset:25600\n"
      "ds_read_b128 v[76:79], v0 offset:26624\n"
      "v_mfma_f32_16x16x32_bf16 v[136:139], v[40:43], v[112:115], v[136:139]\n"
      "s_waitcnt vmcnt(6) lgkmcnt(3)\n"
      "buffer_store_dword v15, v3, s[8:11], s42 offen sc1\n"
      "v_mfma_f32_16x16x32_bf16 v[136:139], v[44:47], v[116:119], v[136:139]\n"
      "ds_read_b128 v[80:83], v0 offset:27648\n"
      "v_mul_f32 v158, v126, v158\n"
      "v_mfma_f32_16x16x32_bf16 v[136:139], v[48:51], v[120:123], v[136:139]\n"
      "ds_read_b128 v[92:95], v0 offset:30720\n"
      "v_mul_f32 v157, v125, v157\n"
      "v_mfma_f32_16x16x32_bf16 v[124:127], a[0:3], v[52:55], 0\n"
      "ds_read_b128 v[84:87], v0 offset:28672\n"
      "ds_read_b128 v[104:107], v0 offset:33792\n"
      "v_mfma_f32_16x16x32_bf16 v[124:127], a[4:7], v[56:59], v[124:127]\n"
      "ds_read_b128 v[88:91], v0 offset:29696\n"
      "v_mul_f32 v130, v130, v158\n"
      "v_mfma_f32_16x16x32_bf16 v[124:127], a[8:11], v[60:63], v[124:127]\n"
      "s_waitcnt lgkmcnt(7)\n"
      "ds_read_b128 v[108:111], v0 offset:34816\n"
      "v_add_f32 v169, v169, v171\n"
      "v_mfma_f32_16x16x32_bf16 v[124:127], a[12:15], v[64:67], v[124:127]\n"
      "ds_read_b128 v[96:99], v0 offset:31744\n"
      "v_fma_f32 v161, v161, v177, v177\n"
      "v_mfma_f32_16x16x32_bf16 v[124:127], a[16:19], v[68:71], v[124:127]\n"
      "s_waitcnt lgkmcnt(7)\n"
      "ds_read_b128 v[100:103], v0 offset:32768\n"
      "v_mul_f32 v128, v128, v156\n"
      "v_mfma_f32_16x16x32_bf16 v[124:127], a[20:23], v[72:75], v[124:127]\n"
      "v_rcp_f32 v161, v161\n"
      "v_mfma_f32_16x16x32_bf16 v[124:127], a[24:27], v[76:79], v[124:127]\n"
      "s_waitcnt lgkmcnt(6)\n"
      "ds_read_b128 v[112:115], v0 offset:35840\n"
      "v_mul_f32 v129, v129, v157\n"
      "v_mfma_f32_16x16x32_bf16 v[124:127], a[28:31], v[80:83], v[124:127]\n"
      "s_waitcnt lgkmcnt(5)\n"
      "ds_read_b128 v[120:123], v0 offset:37888\n"
      "v_mul_f32 v131, v131, v159\n"
      "v_mfma_f32_16x16x32_bf16 v[124:127], a[32:35], v[84:87], v[124:127]\n"
      "s_waitcnt lgkmcnt(5)\n"
      "ds_read_b128 v[116:119], v0 offset:36864\n"
      "v_cvt_pk_bf16_f32 v165, v130, v131\n"
      "v_mfma_f32_16x16x32_bf16 v[124:127], a[36:39], v[88:91], v[124:127]\n"
      "v_cvt_pk_bf16_f32 v164, v128, v129\n"
      "v_mfma_f32_16x16x32_bf16 v[124:127], a[40:43], v[92:95], v[124:127]\n"
      "s_waitcnt lgkmcnt(4)\n"
      "v_fma_f32 v160, v160, v177, v177\n"
      "v_sub_f32 v176, -v168, v170\n"
      "v_mfma_f32_16x16x32_bf16 v[124:127], a[44:47], v[96:99], v[124:127]\n"
      "s_waitcnt lgkmcnt(3)\n"
      "v_rcp_f32 v160, v160\n"
      "v_mfma_f32_16x16x32_bf16 v[124:127], a[48:51], v[100:103], v[124:127]\n"
      "v_mul_f32 v161, v133, v161\n"
      "v_fma_f32 v169, -v176, v176, v169\n"
      "v_mfma_f32_16x16x32_bf16 v[124:127], a[52:55], v[104:107], v[124:127]\n"
      "v_max_f32 v177, 0x3727c5ac, v169\n"
      "v_mfma_f32_16x16x32_bf16 v[124:127], a[56:59], v[108:111], v[124:127]\n"
      "s_waitcnt lgkmcnt(1)\n"
      "v_mul_f32 v162, v134, v162\n"
      "v_mul_f32 v137, v137, v161\n"
      "v_mfma_f32_16x16x32_bf16 v[124:127], a[60:63], v[112:115], v[124:127]\n"
      "s_waitcnt lgkmcnt(0)\n"
      "v_mul_f32 v138, v138, v162\n"
      "v_mul_f32 v139, v139, v163\n"
      "v_mfma_f32_16x16x32_bf16 v[124:127], a[64:67], v[116:119], v[124:127]\n"
      "v_mul_f32 v160, v132, v160\n"
      "v_cvt_pk_bf16_f32 v167, v138, v139\n"
      "v_mfma_f32_16x16x32_bf16 v[124:127], a[68:71], v[120:123], v[124:127]\n"
      "v_mul_f32 v136, v136, v160\n"
      "v_cvt_pk_bf16_f32 v166, v136, v137\n"
      "v_mfma_f32_16x16x32_bf16 v[132:135], a[144:147], v[52:55], 0\n"
      "buffer_store_dwordx4 v[164:167], v0, s[8:11], s37 offen sc1\n"
      "v_mfma_f32_16x16x32_bf16 v[132:135], a[148:151], v[56:59], v[132:135]\n"
      "v_rsq_f32 v170, v177\n"
      "v_mfma_f32_16x16x32_bf16 v[132:135], a[152:155], v[60:63], v[132:135]\n"
      "v_mul_f32 v158, v126, v126\n"
      "v_mfma_f32_16x16x32_bf16 v[132:135], a[156:159], v[64:67], v[132:135]\n"
      "v_mul_f32 v178, 0xc0135761, v170\n"
      "v_mul_f32 v171, 0x3d372713, v178\n"
      "v_mfma_f32_16x16x32_bf16 v[132:135], a[160:163], v[68:71], v[132:135]\n"
      "v_mul_f32 v170, v170, v170\n"
      "v_mul_f32 v179, v171, v170\n"
      "v_mfma_f32_16x16x32_bf16 v[132:135], a[164:167], v[72:75], v[132:135]\n"
      "v_mul_f32 v159, v127, v127\n"
      "v_mfma_f32_16x16x32_bf16 v[132:135], a[168:171], v[76:79], v[132:135]\n"
      "v_mul_f32 v156, v124, v124\n"
      "v_fma_f32 v159, v159, v179, v178\n"
      "v_mfma_f32_16x16x32_bf16 v[132:135], a[172:175], v[80:83], v[132:135]\n"
      "v_mul_f32 v157, v125, v125\n"
      "v_fma_f32 v158, v158, v179, v178\n"
      "v_mfma_f32_16x16x32_bf16 v[132:135], a[176:179], v[84:87], v[132:135]\n"
      "s_add_u32 s42, s42, 165888\n"
      "v_add_u32 v11, 1, v11\n"
      "v_mul_f32 v158, v126, v158\n"
      "v_mfma_f32_16x16x32_bf16 v[132:135], a[180:183], v[88:91], v[132:135]\n"
      "v_fma_f32 v157, v157, v179, v178\n"
      "v_fma_f32 v156, v156, v179, v178\n"
      "v_mfma_f32_16x16x32_bf16 v[132:135], a[184:187], v[92:95], v[132:135]\n"
      "v_exp_f32 v158, v158\n"
      "v_mfma_f32_16x16x32_bf16 v[132:135], a[188:191], v[96:99], v[132:135]\n"
      "s_add_u32 m0, s38, 61440\n"
      "v_mfma_f32_16x16x32_bf16 v[132:135], a[192:195], v[100:103], v[132:135]\n"
      "s_mov_b32 s40, s36\n"
      "v_mul_f32 v156, v124, v156\n"
      "v_fma_f32 v158, v158, v177, v177\n"
      "v_mfma_f32_16x16x32_bf16 v[132:135], a[196:199], v[104:107], v[132:135]\n"
      "v_exp_f32 v156, v156\n"
      "v_mfma_f32_16x16x32_bf16 v[132:135], a[200:203], v[108:111], v[132:135]\n"
      "v_fma_f32 v156, v156, v177, v177\n"
      "v_mul_f32 v159, v127, v159\n"
      "v_mfma_f32_16x16x32_bf16 v[132:135], a[204:207], v[112:115], v[132:135]\n"
      "v_rcp_f32 v156, v156\n"
      "v_mfma_f32_16x16x32_bf16 v[132:135], a[208:211], v[116:119], v[132:135]\n"
      "v_exp_f32 v159, v159\n"
      "v_mfma_f32_16x16x32_bf16 v[132:135], a[212:215], v[120:123], v[132:135]\n"
      "v_fma_f32 v159, v159, v177, v177\n"
      "v_mfma_f32_16x16x32_bf16 v[128:131], a[72:75], v[52:55], 0\n"
      "s_waitcnt vmcnt(3)\n"
      "ds_write_b32 v13, v11\n"
      "v_mfma_f32_16x16x32_bf16 v[128:131], a[76:79], v[56:59], v[128:131]\n"
      "s_add_u32 s37, s37, 165888\n"
      "v_rcp_f32 v158, v158\n"
      "v_mfma_f32_16x16x32_bf16 v[128:131], a[80:83], v[60:63], v[128:131]\n"
      "v_mul_f32 v162, v134, v134\n"
      "v_mul_f32 v163, v135, v135\n"
      "v_mfma_f32_16x16x32_bf16 v[128:131], a[84:87], v[64:67], v[128:131]\n"
      "v_rcp_f32 v159, v159\n"
      "v_mfma_f32_16x16x32_bf16 v[128:131], a[88:91], v[68:71], v[128:131]\n"
      "s_waitcnt vmcnt(2)\n"
      "v_cmp_ne_u32 vcc, s21, v18\n"
      "v_fma_f32 v163, v163, v179, v178\n"
      "v_mfma_f32_16x16x32_bf16 v[128:131], a[92:95], v[72:75], v[128:131]\n"
      "v_fma_f32 v162, v162, v179, v178\n"
      "v_mul_f32 v162, v134, v162\n"
      "v_mfma_f32_16x16x32_bf16 v[128:131], a[96:99], v[76:79], v[128:131]\n"
      "s_add_u32 s36, s36, 24576\n"
      "v_mul_f32 v161, v133, v133\n"
      "v_mul_f32 v159, v127, v159\n"
      "v_mfma_f32_16x16x32_bf16 v[128:131], a[100:103], v[80:83], v[128:131]\n"
      "v_exp_f32 v162, v162\n"
      "v_mfma_f32_16x16x32_bf16 v[128:131], a[104:107], v[84:87], v[128:131]\n"
      "s_cbranch_vccnz .Lsp_g0_6\n"
      ".Lbk_g0_6:\n"
      "v_mul_f32 v160, v132, v132\n"
      "v_mfma_f32_16x16x32_bf16 v[128:131], a[108:111], v[88:91], v[128:131]\n"
      "buffer_load_dwordx4 v0, s[8:11], s40 offen offset:0 sc1 lds\n"
      "v_mfma_f32_16x16x32_bf16 v[128:131], a[112:115], v[92:95], v[128:131]\n"
      "ds_read_b32 v12, v14\n"
      "v_fma_f32 v161, v161, v179, v178\n"
      "v_mfma_f32_16x16x32_bf16 v[128:131], a[116:119], v[96:99], v[128:131]\n"
      "v_mul_f32 v156, v124, v156\n"
      "v_mul_f32 v161, v133, v161\n"
      "v_mfma_f32_16x16x32_bf16 v[128:131], a[120:123], v[100:103], v[128:131]\n"
      "v_fma_f32 v160, v160, v179, v178\n"
      "v_mul_f32 v163, v135, v163\n"
      "v_mfma_f32_16x16x32_bf16 v[128:131], a[124:127], v[104:107], v[128:131]\n"
      "buffer_load_dwordx4 v0, s[8:11], s40 offen offset:1024 sc1 lds\n"
      "v_mfma_f32_16x16x32_bf16 v[128:131], a[128:131], v[108:111], v[128:131]\n"
      "v_exp_f32 v163, v163\n"
      "v_mfma_f32_16x16x32_bf16 v[128:131], a[132:135], v[112:115], v[128:131]\n"
      "v_mul_f32 v157, v125, v157\n"
      "v_fma_f32 v163, v163, v177, v177\n"
      "v_mfma_f32_16x16x32_bf16 v[128:131], a[136:139], v[116:119], v[128:131]\n"
      "s_waitcnt lgkmcnt(0)\n"
      "v_cmp_gt_i32 vcc, v11, v12\n"
      "v_mul_f32 v160, v132, v160\n"
      "v_mfma_f32_16x16x32_bf16 v[128:131], a[140:143], v[120:123], v[128:131]\n"
      "buffer_load_dwordx4 v0, s[8:11], s40 offen offset:2048 sc1 lds\n"
      "v_mfma_f32_16x16x32_bf16 v[136:139], a[216:219], v[52:55], 0\n"
      "v_rcp_f32_e64 v163, v163\n"
      "v_mfma_f32_16x16x32_bf16 v[136:139], a[220:223], v[56:59], v[136:139]\n"
      "s_cbranch_vccnz .Lsp_gy0_7\n"
      ".Lbk_gy0_7:\n"
      "ds_read_b128 v[52:55], v17 offset:0\n"
      "v_mfma_f32_16x16x32_bf16 v[136:139], a[224:227], v[60:63], v[136:139]\n"
      "v_exp_f32 v157, v157\n"
      "v_mfma_f32_16x16x32_bf16 v[136:139], a[228:231], v[64:67], v[136:139]\n"
      "ds_read_b128 v[60:63], v17 offset:2048\n"
      "v_mul_f32 v163, v135, v163\n"
      "v_mfma_f32_16x16x32_bf16 v[136:139], a[232:235], v[68:71], v[136:139]\n"
      "buffer_load_dwordx4 v0, s[8:11], s40 offen offset:3072 sc1 lds\n"
      "v_mfma_f32_16x16x32_bf16 v[136:139], a[236:239], v[72:75], v[136:139]\n"
      "v_exp_f32 v161, v161\n"
      "v_mfma_f32_16x16x32_bf16 v[136:139], a[240:243], v[76:79], v[136:139]\n"
      "s_add_u32 m0, s39, 61440\n"
      "ds_read_b128 v[68:71], v17 offset:4096\n"
      "ds_read2st64_b64 v[168:171], v4 offset0:80 offset1:81\n"
      "v_mfma_f32_16x16x32_bf16 v[136:139], a[244:247], v[80:83], v[136:139]\n"
      "ds_read_b128 v[56:59], v17 offset:1024\n"
      "v_fma_f32 v162, v162, v177, v177\n"
      "v_mfma_f32_16x16x32_bf16 v[136:139], a[248:251], v[84:87], v[136:139]\n"
      "buffer_load_dwordx4 v1, s[8:11], s40 offen offset:0 sc1 lds\n"
      "v_mfma_f32_16x16x32_bf16 v[136:139], a[252:255], v[88:91], v[136:139]\n"
      "ds_read_b128 v[64:67], v17 offset:3072\n"
      "v_fma_f32 v157, v157, v177, v177\n"
      "v_mfma_f32_16x16x32_bf16 v[136:139], v[20:23], v[92:95], v[136:139]\n"
      "s_min_u32 s36, s36, s6\n"
      "v_rcp_f32 v162, v162\n"
      "v_mfma_f32_16x16x32_bf16 v[136:139], v[24:27], v[96:99], v[136:139]\n"
      "v_exp_f32 v160, v160\n"
      "v_mfma_f32_16x16x32_bf16 v[136:139], v[28:31], v[100:103], v[136:139]\n"
      "v_rcp_f32 v157, v157\n"
      "v_mfma_f32_16x16x32_bf16 v[136:139], v[32:35], v[104:107], v[136:139]\n"
      "buffer_load_dword v18, v2, s[8:11], s36 offen sc1\n"
      "v_mfma_f32_16x16x32_bf16 v[136:139], v[36:39], v[108:111], v[136:139]\n"
      "ds_read_b128 v[72:75], v17 offset:5120\n"
      "ds_read_b128 v[76:79], v17 offset:6144\n"
      "v_mfma_f32_16x16x32_bf16 v[136:139], v[40:43], v[112:115], v[136:139]\n"
      "s_waitcnt vmcnt(6) lgkmcnt(3)\n"
      "buffer_store_dword v15, v3, s[8:11], s42 offen sc1\n"
      "v_mfma_f32_16x16x32_bf16 v[136:139], v[44:47], v[116:119], v[136:139]\n"
      "ds_read_b128 v[80:83], v17 offset:7168\n"
      "v_mul_f32 v158, v126, v158\n"
      "v_mfma_f32_16x16x32_bf16 v[136:139], v[48:51], v[120:123], v[136:139]\n"
      "ds_read_b128 v[92:95], v17 offset:10240\n"
      "v_mul_f32 v157, v125, v157\n"
      "v_mfma_f32_16x16x32_bf16 v[124:127], a[0:3], v[52:55], 0\n"
      "ds_read_b128 v[84:87], v17 offset:8192\n"
      "ds_read_b128 v[104:107], v17 offset:13312\n"
      "v_mfma_f32_16x16x32_bf16 v[124:127], a[4:7], v[56:59], v[124:127]\n"
      "ds_read_b128 v[88:91], v17 offset:9216\n"
      "v_mul_f32 v130, v130, v158\n"
      "v_mfma_f32_16x16x32_bf16 v[124:127], a[8:11], v[60:63], v[124:127]\n"
      "s_waitcnt lgkmcnt(7)\n"
      "ds_read_b128 v[108:111], v17 offset:14336\n"
      "v_add_f32 v169, v169, v171\n"
      "v_mfma_f32_16x16x32_bf16 v[124:127], a[12:15], v[64:67], v[124:127]\n"
      "ds_read_b128 v[96:99], v17 offset:11264\n"
      "v_fma_f32 v161, v161, v177, v177\n"
      "v_mfma_f32_16x16x32_bf16 v[124:127], a[16:19], v[68:71], v[124:127]\n"
      "s_waitcnt lgkmcnt(7)\n"
      "ds_read_b128 v[100:103], v17 offset:12288\n"
      "v_mul_f32 v128, v128, v156\n"
      "v_mfma_f32_16x16x32_bf16 v[124:127], a[20:23], v[72:75], v[124:127]\n"
      "v_rcp_f32 v161, v161\n"
      "v_mfma_f32_16x16x32_bf16 v[124:127], a[24:27], v[76:79], v[124:127]\n"
      "s_waitcnt lgkmcnt(6)\n"
      "ds_read_b128 v[112:115], v17 offset:15360\n"
      "v_mul_f32 v129, v129, v157\n"
      "v_mfma_f32_16x16x32_bf16 v[124:127], a[28:31], v[80:83], v[124:127]\n"
      "s_waitcnt lgkmcnt(5)\n"
      "ds_read_b128 v[120:123], v17 offset:17408\n"
      "v_mul_f32 v131, v131, v159\n"
      "v_mfma_f32_16x16x32_bf16 v[124:127], a[32:35], v[84:87], v[124:127]\n"
      "s_waitcnt lgkmcnt(5)\n"
      "ds_read_b128 v[116:119], v17 offset:16384\n"
      "v_cvt_pk_bf16_f32 v165, v130, v131\n"
      "v_mfma_f32_16x16x32_bf16 v[124:127], a[36:39], v[88:91], v[124:127]\n"
      "v_cvt_pk_bf16_f32 v164, v128, v129\n"
      "v_mfma_f32_16x16x32_bf16 v[124:127], a[40:43], v[92:95], v[124:127]\n"
      "s_waitcnt lgkmcnt(4)\n"
      "v_fma_f32 v160, v160, v177, v177\n"
      "v_sub_f32 v176, -v168, v170\n"
      "v_mfma_f32_16x16x32_bf16 v[124:127], a[44:47], v[96:99], v[124:127]\n"
      "s_waitcnt lgkmcnt(3)\n"
      "v_rcp_f32 v160, v160\n"
      "v_mfma_f32_16x16x32_bf16 v[124:127], a[48:51], v[100:103], v[124:127]\n"
      "v_mul_f32 v161, v133, v161\n"
      "v_fma_f32 v169, -v176, v176, v169\n"
      "v_mfma_f32_16x16x32_bf16 v[124:127], a[52:55], v[104:107], v[124:127]\n"
      "v_max_f32 v177, 0x3727c5ac, v169\n"
      "v_mfma_f32_16x16x32_bf16 v[124:127], a[56:59], v[108:111], v[124:127]\n"
      "s_waitcnt lgkmcnt(1)\n"
      "v_mul_f32 v162, v134, v162\n"
      "v_mul_f32 v137, v137, v161\n"
      "v_mfma_f32_16x16x32_bf16 v[124:127], a[60:63], v[112:115], v[124:127]\n"
      "s_waitcnt lgkmcnt(0)\n"
      "v_mul_f32 v138, v138, v162\n"
      "v_mul_f32 v139, v139, v163\n"
      "v_mfma_f32_16x16x32_bf16 v[124:127], a[64:67], v[116:119], v[124:127]\n"
      "v_mul_f32 v160, v132, v160\n"
      "v_cvt_pk_bf16_f32 v167, v138, v139\n"
      "v_mfma_f32_16x16x32_bf16 v[124:127], a[68:71], v[120:123], v[124:127]\n"
      "v_mul_f32 v136, v136, v160\n"
      "v_cvt_pk_bf16_f32 v166, v136, v137\n"
      "v_mfma_f32_16x16x32_bf16 v[132:135], a[144:147], v[52:55], 0\n"
      "buffer_store_dwordx4 v[164:167], v0, s[8:11], s37 offen sc1\n"
      "v_mfma_f32_16x16x32_bf16 v[132:135], a[148:151], v[56:59], v[132:135]\n"
      "v_rsq_f32 v170, v177\n"
      "v_mfma_f32_16x16x32_bf16 v[132:135], a[152:155], v[60:63], v[132:135]\n"
      "v_mul_f32 v158, v126, v126\n"
      "v_mfma_f32_16x16x32_bf16 v[132:135], a[156:159], v[64:67], v[132:135]\n"
      "v_mul_f32 v178, 0xc0135761, v170\n"
      "v_mul_f32 v171, 0x3d372713, v178\n"
      "v_mfma_f32_16x16x32_bf16 v[132:135], a[160:163], v[68:71], v[132:135]\n"
      "v_mul_f32 v170, v170, v170\n"
      "v_mul_f32 v179, v171, v170\n"
      "v_mfma_f32_16x16x32_bf16 v[132:135], a[164:167], v[72:75], v[132:135]\n"
      "v_mul_f32 v159, v127, v127\n"
      "v_mfma_f32_16x16x32_bf16 v[132:135], a[168:171], v[76:79], v[132:135]\n"
      "v_mul_f32 v156, v124, v124\n"
      "v_fma_f32 v159, v159, v179, v178\n"
      "v_mfma_f32_16x16x32_bf16 v[132:135], a[172:175], v[80:83], v[132:135]\n"
      "v_mul_f32 v157, v125, v125\n"
      "v_fma_f32 v158, v158, v179, v178\n"
      "v_mfma_f32_16x16x32_bf16 v[132:135], a[176:179], v[84:87], v[132:135]\n"
      "s_add_u32 s42, s42, 165888\n"
      "v_add_u32 v11, 1, v11\n"
      "v_mul_f32 v158, v126, v158\n"
      "v_mfma_f32_16x16x32_bf16 v[132:135], a[180:183], v[88:91], v[132:135]\n"
      "v_fma_f32 v157, v157, v179, v178\n"
      "v_fma_f32 v156, v156, v179, v178\n"
      "v_mfma_f32_16x16x32_bf16 v[132:135], a[184:187], v[92:95], v[132:135]\n"
      "v_exp_f32 v158, v158\n"
      "v_mfma_f32_16x16x32_bf16 v[132:135], a[188:191], v[96:99], v[132:135]\n"
      "s_add_u32 m0, s38, 0\n"
      "v_mfma_f32_16x16x32_bf16 v[132:135], a[192:195], v[100:103], v[132:135]\n"
      "s_mov_b32 s40, s36\n"
      "v_mul_f32 v156, v124, v156\n"
      "v_fma_f32 v158, v158, v177, v177\n"
      "v_mfma_f32_16x16x32_bf16 v[132:135], a[196:199], v[104:107], v[132:135]\n"
      "v_exp_f32 v156, v156\n"
      "v_mfma_f32_16x16x32_bf16 v[132:135], a[200:203], v[108:111], v[132:135]\n"
      "v_fma_f32 v156, v156, v177, v177\n"
      "v_mul_f32 v159, v127, v159\n"
      "v_mfma_f32_16x16x32_bf16 v[132:135], a[204:207], v[112:115], v[132:135]\n"
      "v_rcp_f32 v156, v156\n"
      "v_mfma_f32_16x16x32_bf16 v[132:135], a[208:211], v[116:119], v[132:135]\n"
      "v_exp_f32 v159, v159\n"
      "v_mfma_f32_16x16x32_bf16 v[132:135], a[212:215], v[120:123], v[132:135]\n"
      "v_fma_f32 v159, v159, v177, v177\n"
      "v_mfma_f32_16x16x32_bf16 v[128:131], a[72:75], v[52:55], 0\n"
      "s_waitcnt vmcnt(3)\n"
      "ds_write_b32 v13, v11\n"
      "v_mfma_f32_16x16x32_bf16 v[128:131], a[76:79], v[56:59], v[128:131]\n"
      "s_add_u32 s37, s37, 165888\n"
      "v_rcp_f32 v158, v158\n"
      "v_mfma_f32_16x16x32_bf16 v[128:131], a[80:83], v[60:63], v[128:131]\n"
      "v_mul_f32 v162, v134, v134\n"
      "v_mul_f32 v163, v135, v135\n"
      "v_mfma_f32_16x16x32_bf16 v[128:131], a[84:87], v[64:67], v[128:131]\n"
      "v_rcp_f32 v159, v159\n"
      "v_mfma_f32_16x16x32_bf16 v[128:131], a[88:91], v[68:71], v[128:131]\n"
      "s_waitcnt vmcnt(2)\n"
      "v_cmp_ne_u32 vcc, s21, v18\n"
      "v_fma_f32 v163, v163, v179, v178\n"
      "v_mfma_f32_16x16x32_bf16 v[128:131], a[92:95], v[72:75], v[128:131]\n"
      "v_fma_f32 v162, v162, v179, v178\n"
      "v_mul_f32 v162, v134, v162\n"
      "v_mfma_f32_16x16x32_bf16 v[128:131], a[96:99], v[76:79], v[128:131]\n"
      "s_add_u32 s36, s36, 24576\n"
      "v_mul_f32 v161, v133, v133\n"
      "v_mul_f32_e64 v159, v127, v159\n"
      "v_mfma_f32_16x16x32_bf16 v[128:131], a[100:103], v[80:83], v[128:131]\n"
      "v_exp_f32_e64 v162, v162\n"
      "v_mfma_f32_16x16x32_bf16 v[128:131], a[104:107], v[84:87], v[128:131]\n"
      "s_cbranch_vccnz .Lsp_g0_8\n"
      ".Lbk_g0_8:\n"
      "v_mul_f32 v160, v132, v132\n"
      "v_mfma_f32_16x16x32_bf16 v[128:131], a[108:111], v[88:91], v[128:131]\n"
      "buffer_load_dwordx4 v0, s[8:11], s40 offen offset:0 sc1 lds\n"
      "v_mfma_f32_16x16x32_bf16 v[128:131], a[112:115], v[92:95], v[128:131]\n"
      "ds_read_b32 v12, v14\n"
      "v_fma_f32 v161, v161, v179, v178\n"
      "v_mfma_f32_16x16x32_bf16 v[128:131], a[116:119], v[96:99], v[128:131]\n"
      "v_mul_f32 v156, v124, v156\n"
      "v_mul_f32 v161, v133, v161\n"
      "v_mfma_f32_16x16x32_bf16 v[128:131], a[120:123], v[100:103], v[128:131]\n"
      "v_fma_f32 v160, v160, v179, v178\n"
      "v_mul_f32 v163, v135, v163\n"
      "v_mfma_f32_16x16x32_bf16 v[128:131], a[124:127], v[104:107], v[128:131]\n"
      "buffer_load_dwordx4 v0, s[8:11], s40 offen offset:1024 sc1 lds\n"
      "v_mfma_f32_16x16x32_bf16 v[128:131], a[128:131], v[108:111], v[128:131]\n"
      "v_exp_f32 v163, v163\n"
      "v_mfma_f32_16x16x32_bf16 v[128:131], a[132:135], v[112:115], v[128:131]\n"
      "v_mul_f32 v157, v125, v157\n"
      "v_fma_f32 v163, v163, v177, v177\n"
      "v_mfma_f32_16x16x32_bf16 v[128:131], a[136:139], v[116:119], v[128:131]\n"
      "s_waitcnt lgkmcnt(0)\n"
      "v_cmp_gt_i32 vcc, v11, v12\n"
      "v_mul_f32 v160, v132, v160\n"
      "v_mfma_f32_16x16x32_bf16 v[128:131], a[140:143], v[120:123], v[128:131]\n"
      "buffer_load_dwordx4 v0, s[8:11], s40 offen offset:2048 sc1 lds\n"
      "v_mfma_f32_16x16x32_bf16 v[136:139], a[216:219], v[52:55], 0\n"
      "v_rcp_f32_e64 v163, v163\n"
      "v_mfma_f32_16x16x32_bf16 v[136:139], a[220:223], v[56:59], v[136:139]\n"
      "s_cbranch_vccnz .Lsp_gy0_9\n"
      ".Lbk_gy0_9:\n"
      "ds_read_b128 v[52:55], v17 offset:20480\n"
      "v_mfma_f32_16x16x32_bf16 v[136:139], a[224:227], v[60:63], v[136:139]\n"
      "v_exp_f32 v157, v157\n"
      "v_mfma_f32_16x16x32_bf16 v[136:139], a[228:231], v[64:67], v[136:139]\n"
      "ds_read_b128 v[60:63], v17 offset:22528\n"
      "v_mul_f32 v163, v135, v163\n"
      "v_mfma_f32_16x16x32_bf16 v[136:139], a[232:235], v[68:71], v[136:139]\n"
      "buffer_load_dwordx4 v0, s[8:11], s40 offen offset:3072 sc1 lds\n"
      "v_mfma_f32_16x16x32_bf16 v[136:139], a[236:239], v[72:75], v[136:139]\n"
      "v_exp_f32 v161, v161\n"
      "v_mfma_f32_16x16x32_bf16 v[136:139], a[240:243], v[76:79], v[136:139]\n"
      "s_add_u32 m0, s39, 0\n"
      "ds_read_b128 v[68:71], v17 offset:24576\n"
      "ds_read2st64_b64 v[168:171], v4 offset0:120 offset1:121\n"
      "v_mfma_f32_16x16x32_bf16 v[136:139], a[244:247], v[80:83], v[136:139]\n"
      "ds_read_b128 v[56:59], v17 offset:21504\n"
      "v_fma_f32 v162, v162, v177, v177\n"
      "v_mfma_f32_16x16x32_bf16 v[136:139], a[248:251], v[84:87], v[136:139]\n"
      "buffer_load_dwordx4 v1, s[8:11], s40 offen offset:0 sc1 lds\n"
      "v_mfma_f32_16x16x32_bf16 v[136:139], a[252:255], v[88:91], v[136:139]\n"
      "ds_read_b128 v[64:67], v17 offset:23552\n"
      "v_fma_f32 v157, v157, v177, v177\n"
      "v_mfma_f32_16x16x32_bf16 v[136:139], v[20:23], v[92:95], v[136:139]\n"
      "s_min_u32 s36, s36, s6\n"
      "v_rcp_f32 v162, v162\n"
      "v_mfma_f32_16x16x32_bf16 v[136:139], v[24:27], v[96:99], v[136:139]\n"
      "v_exp_f32 v160, v160\n"
      "v_mfma_f32_16x16x32_bf16 v[136:139], v[28:31], v[100:103], v[136:139]\n"
      "v_rcp_f32 v157, v157\n"
      "v_mfma_f32_16x16x32_bf16 v[136:139], v[32:35], v[104:107], v[136:139]\n"
      "buffer_load_dword v18, v2, s[8:11], s36 offen sc1\n"
      "v_mfma_f32_16x16x32_bf16 v[136:139], v[36:39], v[108:111], v[136:139]\n"
      "ds_read_b128 v[72:75], v17 offset:25600\n"
      "ds_read_b128 v[76:79], v17 offset:26624\n"
      "v_mfma_f32_16x16x32_bf16 v[136:139], v[40:43], v[112:115], v[136:139]\n"
      "s_waitcnt vmcnt(6) lgkmcnt(3)\n"
      "buffer_store_dword v15, v3, s[8:11], s42 offen sc1\n"
      "v_mfma_f32_16x16x32_bf16 v[136:139], v[44:47], v[116:119], v[136:139]\n"
      "ds_read_b128 v[80:83], v17 offset:27648\n"
      "v_mul_f32 v158, v126, v158\n"
      "v_mfma_f32_16x16x32_bf16 v[136:139], v[48:51], v[120:123], v[136:139]\n"
      "ds_read_b128 v[92:95], v17 offset:30720\n"
      "v_mul_f32 v157, v125, v157\n"
      "v_mfma_f32_16x16x32_bf16 v[124:127], a[0:3], v[52:55], 0\n"
      "ds_read_b128 v[84:87], v17 offset:28672\n"
      "ds_read_b128 v[104:107], v17 offset:33792\n"
      "v_mfma_f32_16x16x32_bf16 v[124:127], a[4:7], v[56:59], v[124:127]\n"
      "ds_read_b128 v[88:91], v17 offset:29696\n"
      "v_mul_f32 v130, v130, v158\n"
      "v_mfma_f32_16x16x32_bf16 v[124:127], a[8:11], v[60:63], v[124:127]\n"
      "s_waitcnt lgkmcnt(7)\n"
      "ds_read_b128 v[108:111], v17 offset:34816\n"
      "v_add_f32 v169, v169, v171\n"
      "v_mfma_f32_16x16x32_bf16 v[124:127], a[12:15], v[64:67], v[124:127]\n"
      ".Lg_p1:\n"
      "s_cbranch_execz .Lg_pr\n"
      "ds_read_b128 v[96:99], v17 offset:31744\n"
      "v_fma_f32 v161, v161, v177, v177\n"
      "v_mfma_f32_16x16x32_bf16 v[124:127], a[16:19], v[68:71], v[124:127]\n"
      "s_waitcnt lgkmcnt(7)\n"
      "ds_read_b128 v[100:103], v17 offset:32768\n"
      "v_mul_f32 v128, v128, v156\n"
      "v_mfma_f32_16x16x32_bf16 v[124:127], a[20:23], v[72:75], v[124:127]\n"
      "v_rcp_f32 v161, v161\n"
      "v_mfma_f32_16x16x32_bf16 v[124:127], a[24:27], v[76:79], v[124:127]\n"
      "s_waitcnt lgkmcnt(6)\n"
      "ds_read_b128 v[112:115], v17 offset:35840\n"
      "v_mul_f32 v129, v129, v157\n"
      "v_mfma_f32_16x16x32_bf16 v[124:127], a[28:31], v[80:83], v[124:127]\n"
      "s_waitcnt lgkmcnt(5)\n"
      "ds_read_b128 v[120:123], v17 offset:37888\n"
      "v_mul_f32 v131, v131, v159\n"
      "v_mfma_f32_16x16x32_bf16 v[124:127], a[32:35], v[84:87], v[124:127]\n"
      "s_waitcnt lgkmcnt(5)\n"
      "ds_read_b128 v[116:119], v17 offset:36864\n"
      "v_cvt_pk_bf16_f32 v165, v130, v131\n"
      "v_mfma_f32_16x16x32_bf16 v[124:127], a[36:39], v[88:91], v[124:127]\n"
      "v_cvt_pk_bf16_f32 v164, v128, v129\n"
      "v_mfma_f32_16x16x32_bf16 v[124:127], a[40:43], v[92:95], v[124:127]\n"
      "s_waitcnt lgkmcnt(4)\n"
      "v_fma_f32 v160, v160, v177, v177\n"
      "v_sub_f32 v176, -v168, v170\n"
      "v_mfma_f32_16x16x32_bf16 v[124:127], a[44:47], v[96:99], v[124:127]\n"
      "s_waitcnt lgkmcnt(3)\n"
      "v_rcp_f32 v160, v160\n"
      "v_mfma_f32_16x16x32_bf16 v[124:127], a[48:51], v[100:103], v[124:127]\n"
      "v_mul_f32 v161, v133, v161\n"
      "v_fma_f32 v169, -v176, v176, v169\n"
      "v_mfma_f32_16x16x32_bf16 v[124:127], a[52:55], v[104:107], v[124:127]\n"
      "v_max_f32 v177, 0x3727c5ac, v169\n"
      "v_mfma_f32_16x16x32_bf16 v[124:127], a[56:59], v[108:111], v[124:127]\n"
      "s_waitcnt lgkmcnt(1)\n"
      "v_mul_f32 v162, v134, v162\n"
      "v_mul_f32 v137, v137, v161\n"
      "v_mfma_f32_16x16x32_bf16 v[124:127], a[60:63], v[112:115], v[124:127]\n"
      "s_waitcnt lgkmcnt(0)\n"
      "v_mul_f32 v138, v138, v162\n"
      "v_mul_f32 v139, v139, v163\n"
      "v_mfma_f32_16x16x32_bf16 v[124:127], a[64:67], v[116:119], v[124:127]\n"
      "v_mul_f32 v160, v132, v160\n"
      "v_cvt_pk_bf16_f32 v167, v138, v139\n"
      "v_mfma_f32_16x16x32_bf16 v[124:127], a[68:71], v[120:123], v[124:127]\n"
      "v_mul_f32 v136, v136, v160\n"
      "v_cvt_pk_bf16_f32 v166, v136, v137\n"
      "v_mfma_f32_16x16x32_bf16 v[132:135], a[144:147], v[52:55], 0\n"
      "buffer_store_dwordx4 v[164:167], v0, s[8:11], s37 offen sc1\n"
      "v_mfma_f32_16x16x32_bf16 v[132:135], a[148:151], v[56:59], v[132:135]\n"
      "v_rsq_f32 v170, v177\n"
      "v_mfma_f32_16x16x32_bf16 v[132:135], a[152:155], v[60:63], v[132:135]\n"
      "v_mul_f32 v158, v126, v126\n"
      "v_mfma_f32_16x16x32_bf16 v[132:135], a[156:159], v[64:67], v[132:135]\n"
      "v_mul_f32 v178, 0xc0135761, v170\n"
      "v_mul_f32 v171, 0x3d372713, v178\n"
      "v_mfma_f32_16x16x32_bf16 v[132:135], a[160:163], v[68:71], v[132:135]\n"
      "v_mul_f32 v170, v170, v170\n"
      "v_mul_f32 v179, v171, v170\n"
      "v_mfma_f32_16x16x32_bf16 v[132:135], a[164:167], v[72:75], v[132:135]\n"
      "v_mul_f32 v159, v127, v127\n"
      "v_mfma_f32_16x16x32_bf16 v[132:135], a[168:171], v[76:79], v[132:135]\n"
      "v_mul_f32 v156, v124, v124\n"
      "v_fma_f32 v159, v159, v179, v178\n"
      "v_mfma_f32_16x16x32_bf16 v[132:135], a[172:175], v[80:83], v[132:135]\n"
      "v_mul_f32 v157, v125, v125\n"
      "v_fma_f32 v158, v158, v179, v178\n"
      "v_mfma_f32_16x16x32_bf16 v[132:135], a[176:179], v[84:87], v[132:135]\n"
      "s_add_u32 s42, s42, 165888\n"
      "v_add_u32 v11, 1, v11\n"
      "v_mul_f32 v158, v126, v158\n"
      "v_mfma_f32_16x16x32_bf16 v[132:135], a[180:183], v[88:91], v[132:135]\n"
      "v_fma_f32 v157, v157, v179, v178\n"
      "v_fma_f32 v156, v156, v179, v178\n"
      "v_mfma_f32_16x16x32_bf16 v[132:135], a[184:187], v[92:95], v[132:135]\n"
      "v_exp_f32 v158, v158\n"
      "v_mfma_f32_16x16x32_bf16 v[132:135], a[188:191], v[96:99], v[132:135]\n"
      "s_add_u32 m0, s38, 20480\n"
      "v_mfma_f32_16x16x32_bf16 v[132:135], a[192:195], v[100:103], v[132:135]\n"
      "s_mov_b32 s40, s36\n"
      "v_mul_f32 v156, v124, v156\n"
      "v_fma_f32 v158, v158, v177, v177\n"
      "v_mfma_f32_16x16x32_bf16 v[132:135], a[196:199], v[104:107], v[132:135]\n"
      "v_exp_f32 v156, v156\n"
      "v_mfma_f32_16x16x32_bf16 v[132:135], a[200:203], v[108:111], v[132:135]\n"
      "v_fma_f32 v156, v156, v177, v177\n"
      "v_mul_f32 v159, v127, v159\n"
      "v_mfma_f32_16x16x32_bf16 v[132:135], a[204:207], v[112:115], v[132:135]\n"
      "v_rcp_f32 v156, v156\n"
      "v_mfma_f32_16x16x32_bf16 v[132:135], a[208:211], v[116:119], v[132:135]\n"
      "v_exp_f32 v159, v159\n"
      "v_mfma_f32_16x16x32_bf16 v[132:135], a[212:215], v[120:123], v[132:135]\n"
      "v_fma_f32 v159, v159, v177, v177\n"
      "v_mfma_f32_16x16x32_bf16 v[128:131], a[72:75], v[52:55], 0\n"
      "s_waitcnt vmcnt(3)\n"
      "ds_write_b32 v13, v11\n"
      "v_mfma_f32_16x16x32_bf16 v[128:131], a[76:79], v[56:59], v[128:131]\n"
      "s_add_u32 s37, s37, 165888\n"
      "v_rcp_f32 v158, v158\n"
      "v_mfma_f32_16x16x32_bf16 v[128:131], a[80:83], v[60:63], v[128:131]\n"
      "v_mul_f32 v162, v134, v134\n"
      "v_mul_f32 v163, v135, v135\n"
      "v_mfma_f32_16x16x32_bf16 v[128:131], a[84:87], v[64:67], v[128:131]\n"
      "v_rcp_f32 v159, v159\n"
      "v_mfma_f32_16x16x32_bf16 v[128:131], a[88:91], v[68:71], v[128:131]\n"
      "s_waitcnt vmcnt(2)\n"
      "v_cmp_ne_u32 vcc, s21, v18\n"
      "v_fma_f32 v163, v163, v179, v178\n"
      "v_mfma_f32_16x16x32_bf16 v[128:131], a[92:95], v[72:75], v[128:131]\n"
      "v_fma_f32 v162, v162, v179, v178\n"
      "v_mul_f32 v162, v134, v162\n"
      "v_mfma_f32_16x16x32_bf16 v[128:131], a[96:99], v[76:79], v[128:131]\n"
      "s_add_u32 s36, s36, 24576\n"
      "v_mul_f32 v161, v133, v133\n"
      "v_mul_f32 v159, v127, v159\n"
      "v_mfma_f32_16x16x32_bf16 v[128:131], a[100:103], v[80:83], v[128:131]\n"
      "v_exp_f32_e64 v162, v162\n"
      "v_mfma_f32_16x16x32_bf16 v[128:131], a[104:107], v[84:87], v[128:131]\n"
      "s_cbranch_vccnz .Lsp_g0_10\n"
      ".Lbk_g0_10:\n"
      "v_mul_f32 v160, v132, v132\n"
      "v_mfma_f32_16x16x32_bf16 v[128:131], a[108:111], v[88:91], v[128:131]\n"
      "buffer_load_dwordx4 v0, s[8:11], s40 offen offset:0 sc1 lds\n"
      "v_mfma_f32_16x16x32_bf16 v[128:131], a[112:115], v[92:95], v[128:131]\n"
      "ds_read_b32 v12, v14\n"
      "v_fma_f32 v161, v161, v179, v178\n"
      "v_mfma_f32_16x16x32_bf16 v[128:131], a[116:119], v[96:99], v[128:131]\n"
      "v_mul_f32 v156, v124, v156\n"
      "v_mul_f32 v161, v133, v161\n"
      "v_mfma_f32_16x16x32_bf16 v[128:131], a[120:123], v[100:103], v[128:131]\n"
      "v_fma_f32 v160, v160, v179, v178\n"
      "v_mul_f32 v163, v135, v163\n"
      "v_mfma_f32_16x16x32_bf16 v[128:131], a[124:127], v[104:107], v[128:131]\n"
      "buffer_load_dwordx4 v0, s[8:11], s40 offen offset:1024 sc1 lds\n"
      "v_mfma_f32_16x16x32_bf16 v[128:131], a[128:131], v[108:111], v[128:131]\n"
      "v_exp_f32 v163, v163\n"
      "v_mfma_f32_16x16x32_bf16 v[128:131], a[132:135], v[112:115], v[128:131]\n"
      "v_mul_f32 v157, v125, v157\n"
      "v_fma_f32 v163, v163, v177, v177\n"
      "v_mfma_f32_16x16x32_bf16 v[128:131], a[136:139], v[116:119], v[128:131]\n"
      "s_waitcnt lgkmcnt(0)\n"
      "v_cmp_gt_i32 vcc, v11, v12\n"
      "v_mul_f32 v160, v132, v160\n"
      "v_mfma_f32_16x16x32_bf16 v[128:131], a[140:143], v[120:123], v[128:131]\n"
      "buffer_load_dwordx4 v0, s[8:11], s40 offen offset:2048 sc1 lds\n"
      "v_mfma_f32_16x16x32_bf16 v[136:139], a[216:219], v[52:55], 0\n"
      "v_rcp_f32_e64 v163, v163\n"
      "v_mfma_f32_16x16x32_bf16 v[136:139], a[220:223], v[56:59], v[136:139]\n"
      "s_cbranch_vccnz .Lsp_gy0_11\n"
      ".Lbk_gy0_11:\n"
      "ds_read_b128 v[52:55], v0 offset:0\n"
      "v_mfma_f32_16x16x32_bf16 v[136:139], a[224:227], v[60:63], v[136:139]\n"
      "v_exp_f32 v157, v157\n"
      "v_mfma_f32_16x16x32_bf16 v[136:139], a[228:231], v[64:67], v[136:139]\n"
      "ds_read_b128 v[60:63], v0 offset:2048\n"
      "v_mul_f32 v163, v135, v163\n"
      "v_mfma_f32_16x16x32_bf16 v[136:139], a[232:235], v[68:71], v[136:139]\n"
      "buffer_load_dwordx4 v0, s[8:11], s40 offen offset:3072 sc1 lds\n"
      "v_mfma_f32_16x16x32_bf16 v[136:139], a[236:239], v[72:75], v[136:139]\n"
      "v_exp_f32 v161, v161\n"
      "v_mfma_f32_16x16x32_bf16 v[136:139], a[240:243], v[76:79], v[136:139]\n"
      "s_add_u32 m0, s39, 20480\n"
      "ds_read_b128 v[68:71], v0 offset:4096\n"
      "ds_read2st64_b64 v[168:171], v4 offset0:0 offset1:1\n"
      "v_mfma_f32_16x16x32_bf16 v[136:139], a[244:247], v[80:83], v[136:139]\n"
      "ds_read_b128 v[56:59], v0 offset:1024\n"
      "v_fma_f32 v162, v162, v177, v177\n"
      "v_mfma_f32_16x16x32_bf16 v[136:139], a[248:251], v[84:87], v[136:139]\n"
      "buffer_load_dwordx4 v1, s[8:11], s40 offen offset:0 sc1 lds\n"
      "v_mfma_f32_16x16x32_bf16 v[136:139], a[252:255], v[88:91], v[136:139]\n"
      "ds_read_b128 v[64:67], v0 offset:3072\n"
      "v_fma_f32 v157, v157, v177, v177\n"
      "v_mfma_f32_16x16x32_bf16 v[136:139], v[20:23], v[92:95], v[136:139]\n"
      "s_min_u32 s36, s36, s6\n"
      "v_rcp_f32 v162, v162\n"
      "v_mfma_f32_16x16x32_bf16 v[136:139], v[24:27], v[96:99], v[136:139]\n"
      "v_exp_f32 v160, v160\n"
      "v_mfma_f32_16x16x32_bf16 v[136:139], v[28:31], v[100:103], v[136:139]\n"
      "v_rcp_f32 v157, v157\n"
      "v_mfma_f32_16x16x32_bf16 v[136:139], v[32:35], v[104:107], v[136:139]\n"
      "buffer_load_dword v18, v2, s[8:11], s36 offen sc1\n"
      "v_mfma_f32_16x16x32_bf16 v[136:139], v[36:39], v[108:111], v[136:139]\n"
      "ds_read_b128 v[72:75], v0 offset:5120\n"
      "ds_read_b128 v[76:79], v0 offset:6144\n"
      "v_mfma_f32_16x16x32_bf16 v[136:139], v[40:43], v[112:115], v[136:139]\n"
      "s_waitcnt vmcnt(6) lgkmcnt(3)\n"
      "buffer_store_dword v15, v3, s[8:11], s42 offen sc1\n"
      "v_mfma_f32_16x16x32_bf16 v[136:139], v[44:47], v[116:119], v[136:139]\n"
      "ds_read_b128 v[80:83], v0 offset:7168\n"
      "v_mul_f32 v158, v126, v158\n"
      "v_mfma_f32_16x16x32_bf16 v[136:139], v[48:51], v[120:123], v[136:139]\n"
      "ds_read_b128 v[92:95], v0 offset:10240\n"
      "v_mul_f32 v157, v125, v157\n"
      "s_sub_u32 s20, s20, 1\n"
      "s_cbranch_scc0 .Lg_loop\n"
      ".Lg_tail:\n"
      "v_mul_f32 v130, v130, v158\n"
      "v_fma_f32 v161, v161, v177, v177\n"
      "v_mul_f32 v128, v128, v156\n"
      "v_rcp_f32 v161, v161\n"
      "v_mul_f32 v129, v129, v157\n"
      "v_mul_f32 v131, v131, v159\n"
      "v_cvt_pk_bf16_f32 v165, v130, v131\n"
      "v_cvt_pk_bf16_f32 v164, v128, v129\n"
      "v_fma_f32 v160, v160, v177, v177\n"
      "v_rcp_f32 v160, v160\n"
      "v_mul_f32 v161, v133, v161\n"
      "v_mul_f32 v162, v134, v162\n"
      "v_mul_f32 v137, v137, v161\n"
      "v_mul_f32 v138, v138, v162\n"
      "v_mul_f32 v139, v139, v163\n"
      "v_mul_f32 v160, v132, v160\n"
      "v_cvt_pk_bf16_f32 v167, v138, v139\n"
      "v_mul_f32 v136, v136, v160\n"
      "v_cvt_pk_bf16_f32 v166, v136, v137\n"
      "buffer_store_dwordx4 v[164:167], v0, s[8:11], s37 offen sc1\n"
      "s_add_u32 s42, s42, 165888\n"
      "s_add_u32 s37, s37, 165888\n"
      "s_waitcnt vmcnt(0)\n"
      "buffer_store_dword v15, v3, s[8:11], s42 offen sc1\n"
      "s_add_u32 s42, s42, 165888\n"
      "s_endpgm\n"
      ".Lsp_g0_1:\n"
      "s_mov_b32 s22, s23\n"
      ".Lsl_g0_1:\n"
      "buffer_load_dword v18, v2, s[8:11], s40 offen sc1\n"
      "s_waitcnt vmcnt(0)\n"
      "v_cmp_ne_u32 vcc, s21, v18\n"
      "s_cbranch_vccz .Lbk_g0_1\n"
      "s_sleep 4\n"
      "s_sub_u32 s22, s22, 1\n"
      "s_cbranch_scc0 .Lsl_g0_1\n"
      "s_mov_b32 s23, 0\n"
      "s_branch .Lbk_g0_1\n"
      ".Lsp_g0_2:\n"
      "s_mov_b32 s22, s23\n"
      ".Lsl_g0_2:\n"
      "buffer_load_dword v18, v2, s[8:11], s40 offen sc1\n"
      "s_waitcnt vmcnt(0)\n"
      "v_cmp_ne_u32 vcc, s21, v18\n"
      "s_cbranch_vccz .Lbk_g0_2\n"
      "s_sleep 4\n"
      "s_sub_u32 s22, s22, 1\n"
      "s_cbranch_scc0 .Lsl_g0_2\n"
      "s_mov_b32 s23, 0\n"
      "s_branch .Lbk_g0_2\n"
      ".Lsp_gy0_3:\n"
      "s_lshl_b32 s22, s23, 4\n"
      ".Lsl_gy0_3:\n"
      "ds_read_b32 v12, v14\n"
      "s_waitcnt lgkmcnt(0)\n"
      "v_cmp_gt_i32 vcc, v11, v12\n"
      "s_cbranch_vccz .Lbk_gy0_3\n"
      "s_sub_u32 s22, s22, 1\n"
      "s_cbranch_scc0 .Lsl_gy0_3\n"
      "s_mov_b32 s23, 0\n"
      "s_branch .Lbk_gy0_3\n"
      ".Lsp_g0_4:\n"
      "s_mov_b32 s22, s23\n"
      "buffer_load_dword v18, v2, s[8:11], s40 offen sc1\n"
      "s_waitcnt vmcnt(0)\n"
      "v_cmp_ne_u32 vcc, s21, v18\n"
      "s_add_u32 s30, s42, 0\n"
      "buffer_store_dword v15, v3, s[8:11], s30 offen sc1\n"
      "s_cbranch_vccz .Lbk_g0_4\n"
      ".Lsl_g0_4:\n"
      "buffer_load_dword v18, v2, s[8:11], s40 offen sc1\n"
      "s_waitcnt vmcnt(0)\n"
      "v_cmp_ne_u32 vcc, s21, v18\n"
      "s_cbranch_vccz .Lbk_g0_4\n"
      "s_sleep 4\n"
      "s_sub_u32 s22, s22, 1\n"
      "s_cbranch_scc0 .Lsl_g0_4\n"
      "s_mov_b32 s23, 0\n"
      "s_branch .Lbk_g0_4\n"
      ".Lsp_gy0_5:\n"
      "s_lshl_b32 s22, s23, 4\n"
      ".Lsl_gy0_5:\n"
      "ds_read_b32 v12, v14\n"
      "s_waitcnt lgkmcnt(0)\n"
      "v_cmp_gt_i32 vcc, v11, v12\n"
      "s_cbranch_vccz .Lbk_gy0_5\n"
      "s_sub_u32 s22, s22, 1\n"
      "s_cbranch_scc0 .Lsl_gy0_5\n"
      "s_mov_b32 s23, 0\n"
      "s_branch .Lbk_gy0_5\n"
      ".Lsp_g0_6:\n"
      "s_mov_b32 s22, s23\n"
      "buffer_load_dword v18, v2, s[8:11], s40 offen sc1\n"
      "s_waitcnt vmcnt(0)\n"
      "v_cmp_ne_u32 vcc, s21, v18\n"
      "s_add_u32 s30, s42, 0\n"
      "buffer_store_dword v15, v3, s[8:11], s30 offen sc1\n"
      "s_cbranch_vccz .Lbk_g0_6\n"
      ".Lsl_g0_6:\n"
      "buffer_load_dword v18, v2, s[8:11], s40 offen sc1\n"
      "s_waitcnt vmcnt(0)\n"
      "v_cmp_ne_u32 vcc, s21, v18\n"
      "s_cbranch_vccz .Lbk_g0_6\n"
      "s_sleep 4\n"
      "s_sub_u32 s22, s22, 1\n"
      "s_cbranch_scc0 .Lsl_g0_6\n"
      "s_mov_b32 s23, 0\n"
      "s_branch .Lbk_g0_6\n"
      ".Lsp_gy0_7:\n"
      "s_lshl_b32 s22, s23, 4\n"
      ".Lsl_gy0_7:\n"
      "ds_read_b32 v12, v14\n"
      "s_waitcnt lgkmcnt(0)\n"
      "v_cmp_gt_i32 vcc, v11, v12\n"
      "s_cbranch_vccz .Lbk_gy0_7\n"
      "s_sub_u32 s22, s22, 1\n"
      "s_cbranch_scc0 .Lsl_gy0_7\n"
      "s_mov_b32 s23, 0\n"
      "s_branch .Lbk_gy0_7\n"
      ".Lsp_g0_8:\n"
      "s_mov_b32 s22, s23\n"
      "buffer_load_dword v18, v2, s[8:11], s40 offen sc1\n"
      "s_waitcnt vmcnt(0)\n"
      "v_cmp_ne_u32 vcc, s21, v18\n"
      "s_add_u32 s30, s42, 0\n"
      "buffer_store_dword v15, v3, s[8:11], s30 offen sc1\n"
      "s_cbranch_vccz .Lbk_g0_8\n"
      ".Lsl_g0_8:\n"
      "buffer_load_dword v18, v2, s[8:11], s40 offen sc1\n"
      "s_waitcnt vmcnt(0)\n"
      "v_cmp_ne_u32 vcc, s21, v18\n"
      "s_cbranch_vccz .Lbk_g0_8\n"
      "s_sleep 4\n"
      "s_sub_u32 s22, s22, 1\n"
      "s_cbranch_scc0 .Lsl_g0_8\n"
      "s_mov_b32 s23, 0\n"
      "s_branch .Lbk_g0_8\n"
      ".Lsp_gy0_9:\n"
      "s_lshl_b32 s22, s23, 4\n"
      ".Lsl_gy0_9:\n"
      "ds_read_b32 v12, v14\n"
      "s_waitcnt lgkmcnt(0)\n"
      "v_cmp_gt_i32 vcc, v11, v12\n"
      "s_cbranch_vccz .Lbk_gy0_9\n"
      "s_sub_u32 s22, s22, 1\n"
      "s_cbranch_scc0 .Lsl_gy0_9\n"
      "s_mov_b32 s23, 0\n"
      "s_branch .Lbk_gy0_9\n"
      ".Lsp_g0_10:\n"
      "s_mov_b32 s22, s23\n"
      "buffer_load_dword v18, v2, s[8:11], s40 offen sc1\n"
      "s_waitcnt vmcnt(0)\n"
      "v_cmp_ne_u32 vcc, s21, v18\n"
      "s_add_u32 s30, s42, 0\n"
      "buffer_store_dword v15, v3, s[8:11], s30 offen sc1\n"
      "s_cbranch_vccz .Lbk_g0_10\n"
      ".Lsl_g0_10:\n"
      "buffer_load_dword v18, v2, s[8:11], s40 offen sc1\n"
      "s_waitcnt vmcnt(0)\n"
      "v_cmp_ne_u32 vcc, s21, v18\n"
      "s_cbranch_vccz .Lbk_g0_10\n"
      "s_sleep 4\n"
      "s_sub_u32 s22, s22, 1\n"
      "s_cbranch_scc0 .Lsl_g0_10\n"
      "s_mov_b32 s23, 0\n"
      "s_branch .Lbk_g0_10\n"
      ".Lsp_gy0_11:\n"
      "s_lshl_b32 s22, s23, 4\n"
      ".Lsl_gy0_11:\n"
      "ds_read_b32 v12, v14\n"
      "s_waitcnt lgkmcnt(0)\n"
      "v_cmp_gt_i32 vcc, v11, v12\n"
      "s_cbranch_vccz .Lbk_gy0_11\n"
      "s_sub_u32 s22, s22, 1\n"
      "s_cbranch_scc0 .Lsl_gy0_11\n"
      "s_mov_b32 s23, 0\n"
      "s_branch .Lbk_gy0_11\n"
      ".Lg_last:\n"
      :
      : "{s[8:11]}"(rsWS), "{s[12:15]}"(rsA), "{s[92:95]}"(rsC), "{v0}"(v_l16), "{v1}"(v_l5), "{v2}"(v_fa), "{v3}"(v_ra), "{v4}"(v_st), "{v5}"(v_at), "{v6}"(v_vo0), "{v7}"(v_vo1), "{v8}"(v_vo2), "{v9}"(v_vo3), "{v10}"(v_pw), "{v12}"(v_al), "{v13}"(v_yw), "{v14}"(v_yr), "{v16}"(v_hi), "{v54}"(v_tw[0]), "{v55}"(v_tw[1]), "{v64}"(v_id[0]), "{v65}"(v_id[1]), "{s36}"(s_sx), "{s37}"(s_sh), "{s38}"(s_m4), "{s39}"(s_m5)
      : "v255", "a255", "s99", "vcc", "scc", "memory");

  }
}

extern "C" void kernel_launch(void* const* d_in, const int* in_sizes, int n_in, void* d_out, int out_size,
                              void* d_ws, size_t ws_size, hipStream_t stream) {
  char* ws = (char*)d_ws + ((256 - ((size_t)d_ws & 255)) & 255);
  (void)hipFuncSetAttribute((const void*)k_all, hipFuncAttributeMaxDynamicSharedMemorySize, LDS_BYTES);
  k_all<<<256, 256, LDS_BYTES, stream>>>((const u16*)d_in[0], (const u16*)d_in[1], (const u16*)d_in[2], (const u16*)d_in[4],
                                         (const u16*)d_in[5], (const u16*)d_in[6], ws, (u16*)d_out);
}
```

```cpp
#include <hip/hip_runtime.h>
#include <stdint.h>

typedef unsigned short u16;
typedef __attribute__((ext_vector_type(4))) unsigned u32x4;
constexpr unsigned WS_DBG = 65536u;
constexpr unsigned WS_XIN = 1310720u;
constexpr unsigned WS_OB = 9175040u;
constexpr unsigned WS_X1 = 17039360u;
constexpr unsigned WS_H = 26034176u;
constexpr unsigned WS_PS = 78589952u;
constexpr unsigned WS_END = 109740032u;
constexpr unsigned KSK = 0u;
constexpr unsigned ENT = 1u;
constexpr unsigned ROT = 0u;
constexpr unsigned FLIP = 0u;
constexpr unsigned PW = 1664u;
constexpr unsigned REC = 24576u;
constexpr unsigned HREC = 165888u;
constexpr unsigned PREC = 99840u;
constexpr unsigned NREC = 80u;
constexpr unsigned OOB = 0x40000000u, NEG = 0xff61b1e6u;
constexpr int LDS_BYTES = 160 * 1024;

__device__ __forceinline__ u32x4 srd(const void* p, unsigned bytes) {
  const unsigned long long a = (unsigned long long)p;
  return u32x4{(unsigned)a, (unsigned)(a >> 32) & 0xffffu, bytes, 0x00020000u};
}

extern "C" __global__ __launch_bounds__(256, 1) __attribute__((amdgpu_waves_per_eu(1, 1))) void k_all(
    const u16* X, const u16* Wqkv, const u16* Wo, const u16* Wg, const u16* Wu, const u16* Wd, char* ws, u16* out) {
  const unsigned b = blockIdx.x, xcd = b & 7, slot = b >> 3;
  const unsigned w = __builtin_amdgcn_readfirstlane(threadIdx.x >> 6), lane = threadIdx.x & 63;
  const bool fast = ((xcd ^ FLIP) & 1) == 0;
  auto qvo = [&](unsigned i, unsigned hg) {
    const unsigned c = 64 * i + lane, r = c * 2185 >> 16, cc = c - 30 * r, sc = cc * 57 >> 9;
    const unsigned o = (8 * w + r) * 3456 + sc * (1152 - 144) + 144 * hg + cc * 16 - 1024 * i;
    return o | (c < 240 && cc < 27 ? 0u : OOB);
  };
  if (ENT) {
    constexpr unsigned NL = 41, LR = 9;
    const unsigned wi = slot * 4 + w, n0 = wi * NL + lane, n = n0 < 576 * LR ? n0 : 0, r = n / LR, c = n - LR * r;
    const unsigned lx = lane - NL, m = (xcd * 128 + wi) * 4 + lx, t = m / 144;
    unsigned oc = (wi * 8 + (lane & 7)) * 128;
    asm volatile("s_getpc_b64 vcc\n.Lent_pc:\n\ts_sub_u32 vcc_lo, vcc_lo, .Lent_pc-k_all\n\ts_subb_u32 vcc_hi, vcc_hi, 0\n\ts_mov_b32 m0, %1\n\t"
                 "v_min_u32 %0, .Lw_last-k_all, %0\n\tglobal_load_lds_dword %0, vcc" : "+v"(oc) : "s"(162816 + w * 256) : "vcc", "scc", "memory");
    unsigned oq = r * 3456 + c / (LR / 3) * 1152 + ((xcd & 6) + c % (LR / 3)) * 128, ox = (32 * (t & 3) + 31 - (t >> 2)) * 18432 + (m - 144 * t) * 128;
    asm volatile("" : "+v"(oq), "+v"(ox));
    const bool isq = lane < NL && fast;
    oq = isq ? oq : lx < 4 ? ox : 0;
    asm volatile("" : "+v"(oq));
    __builtin_amdgcn_sched_barrier(0);
    const char* pa = (const char*)(isq ? Wqkv : X) + oq;
    asm volatile("s_mov_b32 m0, %1\n\ts_nop 0\n\tglobal_load_lds_dword %0, off" ::"v"(pa), "s"(162816 + w * 256) : "memory");
    __builtin_amdgcn_sched_barrier(0);
  }
  const unsigned i16 = lane & 15, g = lane >> 4, q = i16 >> 2, p = lane & 3;
  const u32x4 rsWS = srd(ws, WS_END);
  const unsigned v_l16 = lane * 16, e = xcd >> 1;
  const unsigned half = (i16 + 16 * (g >> 1)) * 16 + (g & 1) * 8;
  const unsigned line = lane < 32 ? lane * 4 : OOB, v_al = w ? OOB : line + 128 * (1 + b);
  const unsigned up = lane >> 5, l5 = (lane & 31) * 4, v_hi = 0u - up;
  const unsigned l8 = lane >> 3, l7 = (lane & 7) * 16;
#define COMMON(ID, YB) const unsigned v_dbg = WS_DBG + (((ID) + part * 4 + gr) * 4 + w) * 1024 + lane * 4, v_yw = (YB) + w * 4, v_yr = (YB) + p * 4; (void)v_dbg
  if (__builtin_expect(fast && slot < 8, 1)) {
    const unsigned gr = slot & 3, part = 2 * e + (slot >> 2);
    COMMON(0, 61440);
    const unsigned hg = part, h = 4 * hg + w, par = i16 & 1;
    const u32x4 rsA = srd(Wqkv, 576 * 1728 * 2);
    const unsigned v_l5 = w < 3 || lane < 32 ? v_l16 + 16384 - w * 3072 : OOB;
    const unsigned v_fa = 20480 - w * 4096 + (lane < 48 ? lane >> 2 : 11) * 128 + p * 4, v_ra = up * 8 * REC + l5 + 20480 + h * 128;
    const unsigned v_st = 18432 + i16 * 32 + q * 8, v_tw = 61568 + w * 128 + i16 * 8, v_tr = 61568 + w * 128 + g * 32;
    const unsigned v_om = h * 512 + half, v_oe = g ? OOB : 16384 + h * 128 + i16 * 8;
    const unsigned v_pw = 20480 + h * 128 + l8 * REC + l7;
    unsigned v_vo[4], v_bc[4], v_bp[4], v_mk[4], v_sel[4] = {0, 0, 0, 0}, v_ci[4];
#pragma unroll
    for (unsigned i = 0; i < 4; ++i) {
      const unsigned kk = 4 * g + i, ka = 4 * g + 2 * (i & 1) + par;
      v_bc[i] = (i & 1) == par && kk <= i16 ? 0u : NEG;
      v_bp[i] = (i & 1) == par && kk >= i16 + 2 ? 0u : NEG;
      v_mk[i] = (i < 2 ? ka > i16 : ka <= i16) ? 0xffffu << (16 * par) : 0u;
      v_ci[i] = p == 2 ? 0x3f800000u : 0u;
    }
    v_sel[3] = lane == 16 ? 0x3f80u : lane == 17 ? 0x3f800000u : 0u;
#pragma unroll
    for (unsigned i = 0; i < 4; ++i) v_vo[i] = qvo(i, hg);
    const unsigned rb = 65536 + g * 4224 + q * 480;
    const unsigned v_at = rb + 36 * w + 4 * (w & 1) + 8 * p, v_ax = rb + (p < 2 ? p : 2) * 144 + 72 * (w >> 1) + 32;
    const unsigned v_pm = (16 * g + 4 * (i16 >> 1 & 3) + 2 * (w & 1) + par) * 4, v_ms = i16 < 6 ? ~0u : 0u;
    const unsigned s_sx = WS_XIN + (gr * NREC + KSK) * REC + w * 4096, s_ev = s_sx + (32 - KSK) * REC, s_del = 0u - (gr ? NREC + 32 : 32) * REC;
    const unsigned s_kil = gr ? 0x7f7fffffu : 0xff7fffffu, s_so = WS_OB + (gr * NREC + KSK) * REC;
    const unsigned s_m4 = w * 4096, s_m5 = 16384 + w * 1024, s_ms = w * 4224, s_up = part + gr + w;
    asm volatile(
      "s_buffer_load_dword s21, s[8:11], 0\n"
      "s_mov_b32 s23, 65536\n"
      "s_mov_b32 s31, 0x40000000\n"
      "s_mov_b32 s51, 0\n"
      "s_add_u32 m0, s43, 65536\n"
      "s_nop 0\n"
      "buffer_load_dwordx4 v48, s[12:15], s51 offen offset:0 lds\n"
      "buffer_load_dwordx4 v49, s[12:15], s51 offen offset:1024 lds\n"
      "buffer_load_dwordx4 v50, s[12:15], s51 offen offset:2048 lds\n"
      "buffer_load_dwordx4 v51, s[12:15], s51 offen offset:3072 lds\n"
      "s_add_u32 s51, s51, 110592\n"
      "s_add_u32 m0, s43, 82432\n"
      "s_nop 0\n"
      "buffer_load_dwordx4 v48, s[12:15], s51 offen offset:0 lds\n"
      "buffer_load_dwordx4 v49, s[12:15], s51 offen offset:1024 lds\n"
      "buffer_load_dwordx4 v50, s[12:15], s51 offen offset:2048 lds\n"
      "buffer_load_dwordx4 v51, s[12:15], s51 offen offset:3072 lds\n"
      "s_add_u32 s51, s51, 110592\n"
      "s_add_u32 m0, s43, 99328\n"
      "s_nop 0\n"
      "buffer_load_dwordx4 v48, s[12:15], s51 offen offset:0 lds\n"
      "buffer_load_dwordx4 v49, s[12:15], s51 offen offset:1024 lds\n"
      "buffer_load_dwordx4 v50, s[12:15], s51 offen offset:2048 lds\n"
      "buffer_load_dwordx4 v51, s[12:15], s51 offen offset:3072 lds\n"
      "s_add_u32 s51, s51, 110592\n"
      "s_add_u32 s6, s36, 1572864\n"
      "s_add_u32 s36, s36, 4294942720\n"
      "s_add_u32 s48, s37, 811008\n"
      "s_mov_b32 s53, 0x7f7fffff\n"
      "v_mov_b32 v179, 0\n"
      "s_mov_b32 s54, 0xffff\n"
      "s_mov_b32 s55, 0\n"
      "v_mov_b32 v9, 0\n"
      "ds_write_b32 v11, v9\n"
      "v_mov_b32 v56, 0x3f803f80\n"
      "v_mov_b32 v57, 0x3f803f80\n"
      "v_mov_b32 v58, 0x3f803f80\n"
      "v_mov_b32 v59, 0x3f803f80\n"
      "s_mov_b64 exec, 0\n"
      "s_lshr_b32 s22, s41, 12\n"
      ".Lq_pl:\n"
      "s_cmp_eq_u32 s22, 0\n"
      "s_cbranch_scc1 .Lq_p0\n"
      "s_cmp_eq_u32 s22, 1\n"
      "s_cbranch_scc1 .Lq_p1\n"
      "s_cmp_eq_u32 s22, 3\n"
      "s_cbranch_scc1 .Lq_px\n"
      "s_branch .Lq_pe\n"
      ".Lq_pr:\n"
      "s_add_u32 s22, s22, 4\n"
      "s_branch .Lq_pl\n"
      ".Lq_pe:\n"
      "s_mov_b64 exec, -1\n"
      "s_waitcnt vmcnt(8) lgkmcnt(0)\n"
      "s_barrier\n"
      "ds_read_b64_tr_b16 v[60:61], v53 offset:0\n"
      "ds_read_b64_tr_b16 v[62:63], v53 offset:1920\n"
      "ds_read_b64_tr_b16 a[0:1], v52 offset:144\n"
      "ds_read_b64_tr_b16 a[2:3], v52 offset:2064\n"
      "ds_read_b64_tr_b16 a[72:73], v52 offset:288\n"
      "ds_read_b64_tr_b16 a[74:75], v52 offset:2208\n"
      "ds_read_b64_tr_b16 a[144:145], v52 offset:0\n"
      "ds_read_b64_tr_b16 a[146:147], v52 offset:1920\n"
      "s_add_u32 m0, s43, 116224\n"
      "s_nop 0\n"
      "buffer_load_dwordx4 v48, s[12:15], s51 offen offset:0 lds\n"
      "buffer_load_dwordx4 v49, s[12:15], s51 offen offset:1024 lds\n"
      "buffer_load_dwordx4 v50, s[12:15], s51 offen offset:2048 lds\n"
      "buffer_load_dwordx4 v51, s[12:15], s51 offen offset:3072 lds\n"
      "s_add_u32 s51, s51, 110592\n"
      "s_waitcnt vmcnt(8) lgkmcnt(0)\n"
      "s_barrier\n"
      "ds_read_b64_tr_b16 v[68:69], v53 offset:16896\n"
      "ds_read_b64_tr_b16 v[70:71], v53 offset:18816\n"
      "ds_read_b64_tr_b16 a[4:5], v52 offset:17040\n"
      "ds_read_b64_tr_b16 a[6:7], v52 offset:18960\n"
      "ds_read_b64_tr_b16 a[76:77], v52 offset:17184\n"
      "ds_read_b64_tr_b16 a[78:79], v52 offset:19104\n"
      "ds_read_b64_tr_b16 a[148:149], v52 offset:16896\n"
      "ds_read_b64_tr_b16 a[150:151], v52 offset:18816\n"
      "ds_bpermute_b32 v64, v54, v60\n"
      "ds_bpermute_b32 v65, v54, v61\n"
      "ds_bpermute_b32 v66, v54, v62\n"
      "ds_bpermute_b32 v67, v54, v63\n"
      "v_mfma_f32_16x16x32_bf16 v[136:139], a[0:3], v[56:59], 0\n"
      "v_mfma_f32_16x16x32_bf16 v[144:147], v[56:59], a[72:75], 0\n"
      "v_mfma_f32_16x16x32_bf16 v[148:151], a[144:147], v[56:59], 0\n"
      "s_add_u32 m0, s43, 65536\n"
      "s_nop 0\n"
      "buffer_load_dwordx4 v48, s[12:15], s51 offen offset:0 lds\n"
      "buffer_load_dwordx4 v49, s[12:15], s51 offen offset:1024 lds\n"
      "buffer_load_dwordx4 v50, s[12:15], s51 offen offset:2048 lds\n"
      "buffer_load_dwordx4 v51, s[12:15], s51 offen offset:3072 lds\n"
      "s_add_u32 s51, s51, 110592\n"
      "s_waitcnt vmcnt(8) lgkmcnt(0)\n"
      "s_barrier\n"
      "ds_read_b64_tr_b16 v[60:61], v53 offset:33792\n"
      "ds_read_b64_tr_b16 v[62:63], v53 offset:35712\n"
      "ds_read_b64_tr_b16 a[8:9], v52 offset:33936\n"
      "ds_read_b64_tr_b16 a[10:11], v52 offset:35856\n"
      "ds_read_b64_tr_b16 a[80:81], v52 offset:34080\n"
      "ds_read_b64_tr_b16 a[82:83], v52 offset:36000\n"
      "ds_read_b64_tr_b16 a[152:153], v52 offset:33792\n"
      "ds_read_b64_tr_b16 a[154:155], v52 offset:35712\n"
      "ds_bpermute_b32 v72, v54, v68\n"
      "ds_bpermute_b32 v73, v54, v69\n"
      "ds_bpermute_b32 v74, v54, v70\n"
      "ds_bpermute_b32 v75, v54, v71\n"
      "v_mfma_f32_16x16x32_bf16 v[136:139], a[4:7], v[56:59], v[136:139]\n"
      "v_mfma_f32_16x16x32_bf16 v[144:147], v[56:59], a[76:79], v[144:147]\n"
      "v_mfma_f32_16x16x32_bf16 v[148:151], a[148:151], v[56:59], v[148:151]\n"
      "s_add_u32 m0, s43, 82432\n"
      "s_nop 0\n"
      "buffer_load_dwordx4 v48, s[12:15], s51 offen offset:0 lds\n"
      "buffer_load_dwordx4 v49, s[12:15], s51 offen offset:1024 lds\n"
      "buffer_load_dwordx4 v50, s[12:15], s51 offen offset:2048 lds\n"
      "buffer_load_dwordx4 v51, s[12:15], s51 offen offset:3072 lds\n"
      "s_add_u32 s51, s51, 110592\n"
      "v_and_b32 v64, v55, v64\n"
      "v_and_b32 v65, v55, v65\n"
      "v_and_b32 v66, v55, v66\n"
      "v_and_b32 v67, v55, v67\n"
      "v_accvgpr_write_b32 a216, v64\n"
      "v_accvgpr_write_b32 a217, v65\n"
      "v_accvgpr_write_b32 a218, v66\n"
      "v_accvgpr_write_b32 a219, v67\n"
      "v_mfma_f32_16x16x32_bf16 v[140:143], v[64:67], v[56:59], 0\n"
      "s_waitcnt vmcnt(8) lgkmcnt(0)\n"
      "s_barrier\n"
      "ds_read_b64_tr_b16 v[68:69], v53 offset:50688\n"
      "ds_read_b64_tr_b16 v[70:71], v53 offset:52608\n"
      "ds_read_b64_tr_b16 a[12:13], v52 offset:50832\n"
      "ds_read_b64_tr_b16 a[14:15], v52 offset:52752\n"
      "ds_read_b64_tr_b16 a[84:85], v52 offset:50976\n"
      "ds_read_b64_tr_b16 a[86:87], v52 offset:52896\n"
      "ds_read_b64_tr_b16 a[156:157], v52 offset:50688\n"
      "ds_read_b64_tr_b16 a[158:159], v52 offset:52608\n"
      "ds_bpermute_b32 v64, v54, v60\n"
      "ds_bpermute_b32 v65, v54, v61\n"
      "ds_bpermute_b32 v66, v54, v62\n"
      "ds_bpermute_b32 v67, v54, v63\n"
      "v_mfma_f32_16x16x32_bf16 v[136:139], a[8:11], v[56:59], v[136:139]\n"
      "v_mfma_f32_16x16x32_bf16 v[144:147], v[56:59], a[80:83], v[144:147]\n"
      "v_mfma_f32_16x16x32_bf16 v[148:151], a[152:155], v[56:59], v[148:151]\n"
      "s_add_u32 m0, s43, 99328\n"
      "s_nop 0\n"
      "buffer_load_dwordx4 v48, s[12:15], s51 offen offset:0 lds\n"
      "buffer_load_dwordx4 v49, s[12:15], s51 offen offset:1024 lds\n"
      "buffer_load_dwordx4 v50, s[12:15], s51 offen offset:2048 lds\n"
      "buffer_load_dwordx4 v51, s[12:15], s51 offen offset:3072 lds\n"
      "s_add_u32 s51, s51, 110592\n"
      "v_and_b32 v72, v55, v72\n"
      "v_and_b32 v73, v55, v73\n"
      "v_and_b32 v74, v55, v74\n"
      "v_and_b32 v75, v55, v75\n"
      "v_accvgpr_write_b32 a220, v72\n"
      "v_accvgpr_write_b32 a221, v73\n"
      "v_accvgpr_write_b32 a222, v74\n"
      "v_accvgpr_write_b32 a223, v75\n"
      "v_mfma_f32_16x16x32_bf16 v[140:143], v[72:75], v[56:59], v[140:143]\n"
      "s_waitcnt vmcnt(8) lgkmcnt(0)\n"
      "s_barrier\n"
      "ds_read_b64_tr_b16 v[60:61], v53 offset:0\n"
      "ds_read_b64_tr_b16 v[62:63], v53 offset:1920\n"
      "ds_read_b64_tr_b16 a[16:17], v52 offset:144\n"
      "ds_read_b64_tr_b16 a[18:19], v52 offset:2064\n"
      "ds_read_b64_tr_b16 a[88:89], v52 offset:288\n"
      "ds_read_b64_tr_b16 a[90:91], v52 offset:2208\n"
      "ds_read_b64_tr_b16 a[160:161], v52 offset:0\n"
      "ds_read_b64_tr_b16 a[162:163], v52 offset:1920\n"
      "ds_bpermute_b32 v72, v54, v68\n"
      "ds_bpermute_b32 v73, v54, v69\n"
      "ds_bpermute_b32 v74, v54, v70\n"
      "ds_bpermute_b32 v75, v54, v71\n"
      "v_mfma_f32_16x16x32_bf16 v[136:139], a[12:15], v[56:59], v[136:139]\n"
      "v_mfma_f32_16x16x32_bf16 v[144:147], v[56:59], a[84:87], v[144:147]\n"
      "v_mfma_f32_16x16x32_bf16 v[148:151], a[156:159], v[56:59], v[148:151]\n"
      "s_add_u32 m0, s43, 116224\n"
      "s_nop 0\n"
      "buffer_load_dwordx4 v48, s[12:15], s51 offen offset:0 lds\n"
      "buffer_load_dwordx4 v49, s[12:15], s51 offen offset:1024 lds\n"
      "buffer_load_dwordx4 v50, s[12:15], s51 offen offset:2048 lds\n"
      "buffer_load_dwordx4 v51, s[12:15], s51 offen offset:3072 lds\n"
      "s_add_u32 s51, s51, 110592\n"
      "v_and_b32 v64, v55, v64\n"
      "v_and_b32 v65, v55, v65\n"
      "v_and_b32 v66, v55, v66\n"
      "v_and_b32 v67, v55, v67\n"
      "v_accvgpr_write_b32 a224, v64\n"
      "v_accvgpr_write_b32 a225, v65\n"
      "v_accvgpr_write_b32 a226, v66\n"
      "v_accvgpr_write_b32 a227, v67\n"
      "v_mfma_f32_16x16x32_bf16 v[140:143], v[64:67], v[56:59], v[140:143]\n"
      "s_waitcnt vmcnt(8) lgkmcnt(0)\n"
      "s_barrier\n"
      "ds_read_b64_tr_b16 v[68:69], v53 offset:16896\n"
      "ds_read_b64_tr_b16 v[70:71], v53 offset:18816\n"
      "ds_read_b64_tr_b16 a[20:21], v52 offset:17040\n"
      "ds_read_b64_tr_b16 a[22:23], v52 offset:18960\n"
      "ds_read_b64_tr_b16 a[92:93], v52 offset:17184\n"
      "ds_read_b64_tr_b16 a[94:95], v52 offset:19104\n"
      "ds_read_b64_tr_b16 a[164:165], v52 offset:16896\n"
      "ds_read_b64_tr_b16 a[166:167], v52 offset:18816\n"
      "ds_bpermute_b32 v64, v54, v60\n"
      "ds_bpermute_b32 v65, v54, v61\n"
      "ds_bpermute_b32 v66, v54, v62\n"
      "ds_bpermute_b32 v67, v54, v63\n"
      "v_mfma_f32_16x16x32_bf16 v[136:139], a[16:19], v[56:59], v[136:139]\n"
      "v_mfma_f32_16x16x32_bf16 v[144:147], v[56:59], a[88:91], v[144:147]\n"
      "v_mfma_f32_16x16x32_bf16 v[148:151], a[160:163], v[56:59], v[148:151]\n"
      "s_add_u32 m0, s43, 65536\n"
      "s_nop 0\n"
      "buffer_load_dwordx4 v48, s[12:15], s51 offen offset:0 lds\n"
      "buffer_load_dwordx4 v49, s[12:15], s51 offen offset:1024 lds\n"
      "buffer_load_dwordx4 v50, s[12:15], s51 offen offset:2048 lds\n"
      "buffer_load_dwordx4 v51, s[12:15], s51 offen offset:3072 lds\n"
      "s_add_u32 s51, s51, 110592\n"
      "v_and_b32 v72, v55, v72\n"
      "v_and_b32 v73, v55, v73\n"
      "v_and_b32 v74, v55, v74\n"
      "v_and_b32 v75, v55, v75\n"
      "v_accvgpr_write_b32 a228, v72\n"
      "v_accvgpr_write_b32 a229, v73\n"
      "v_accvgpr_write_b32 a230, v74\n"
      "v_accvgpr_write_b32 a231, v75\n"
      "v_mfma_f32_16x16x32_bf16 v[140:143], v[72:75], v[56:59], v[140:143]\n"
      "s_waitcnt vmcnt(8) lgkmcnt(0)\n"
      "s_barrier\n"
      "ds_read_b64_tr_b16 v[60:61], v53 offset:33792\n"
      "ds_read_b64_tr_b16 v[62:63], v53 offset:35712\n"
      "ds_read_b64_tr_b16 a[24:25], v52 offset:33936\n"
      "ds_read_b64_tr_b16 a[26:27], v52 offset:35856\n"
      "ds_read_b64_tr_b16 a[96:97], v52 offset:34080\n"
      "ds_read_b64_tr_b16 a[98:99], v52 offset:36000\n"
      "ds_read_b64_tr_b16 a[168:169], v52 offset:33792\n"
      "ds_read_b64_tr_b16 a[170:171], v52 offset:35712\n"
      "ds_bpermute_b32 v72, v54, v68\n"
      "ds_bpermute_b32 v73, v54, v69\n"
      "ds_bpermute_b32 v74, v54, v70\n"
      "ds_bpermute_b32 v75, v54, v71\n"
      "v_mfma_f32_16x16x32_bf16 v[136:139], a[20:23], v[56:59], v[136:139]\n"
      "v_mfma_f32_16x16x32_bf16 v[144:147], v[56:59], a[92:95], v[144:147]\n"
      "v_mfma_f32_16x16x32_bf16 v[148:151], a[164:167], v[56:59], v[148:151]\n"
      "s_add_u32 m0, s43, 82432\n"
      "s_nop 0\n"
      "buffer_load_dwordx4 v48, s[12:15], s51 offen offset:0 lds\n"
      "buffer_load_dwordx4 v49, s[12:15], s51 offen offset:1024 lds\n"
      "buffer_load_dwordx4 v50, s[12:15], s51 offen offset:2048 lds\n"
      "buffer_load_dwordx4 v51, s[12:15], s51 offen offset:3072 lds\n"
      "s_add_u32 s51, s51, 110592\n"
      "v_and_b32 v64, v55, v64\n"
      "v_and_b32 v65, v55, v65\n"
      "v_and_b32 v66, v55, v66\n"
      "v_and_b32 v67, v55, v67\n"
      "v_accvgpr_write_b32 a232, v64\n"
      "v_accvgpr_write_b32 a233, v65\n"
      "v_accvgpr_write_b32 a234, v66\n"
      "v_accvgpr_write_b32 a235, v67\n"
      "v_mfma_f32_16x16x32_bf16 v[140:143], v[64:67], v[56:59], v[140:143]\n"
      "s_waitcnt vmcnt(8) lgkmcnt(0)\n"
      "s_barrier\n"
      "ds_read_b64_tr_b16 v[68:69], v53 offset:50688\n"
      "ds_read_b64_tr_b16 v[70:71], v53 offset:52608\n"
      "ds_read_b64_tr_b16 a[28:29], v52 offset:50832\n"
      "ds_read_b64_tr_b16 a[30:31], v52 offset:52752\n"
      "ds_read_b64_tr_b16 a[100:101], v52 offset:50976\n"
      "ds_read_b64_tr_b16 a[102:103], v52 offset:52896\n"
      "ds_read_b64_tr_b16 a[172:173], v52 offset:50688\n"
      "ds_read_b64_tr_b16 a[174:175], v52 offset:52608\n"
      "ds_bpermute_b32 v64, v54, v60\n"
      "ds_bpermute_b32 v65, v54, v61\n"
      "ds_bpermute_b32 v66, v54, v62\n"
      "ds_bpermute_b32 v67, v54, v63\n"
      "v_mfma_f32_16x16x32_bf16 v[136:139], a[24:27], v[56:59], v[136:139]\n"
      "v_mfma_f32_16x16x32_bf16 v[144:147], v[56:59], a[96:99], v[144:147]\n"
      "v_mfma_f32_16x16x32_bf16 v[148:151], a[168:171], v[56:59], v[148:151]\n"
      "s_add_u32 m0, s43, 99328\n"
      "s_nop 0\n"
      "buffer_load_dwordx4 v48, s[12:15], s51 offen offset:0 lds\n"
      "buffer_load_dwordx4 v49, s[12:15], s51 offen offset:1024 lds\n"
      "buffer_load_dwordx4 v50, s[12:15], s51 offen offset:2048 lds\n"
      "buffer_load_dwordx4 v51, s[12:15], s51 offen offset:3072 lds\n"
      "s_add_u32 s51, s51, 110592\n"
      "v_and_b32 v72, v55, v72\n"
      "v_and_b32 v73, v55, v73\n"
      "v_and_b32 v74, v55, v74\n"
      "v_and_b32 v75, v55, v75\n"
      "v_accvgpr_write_b32 a236, v72\n"
      "v_accvgpr_write_b32 a237, v73\n"
      "v_accvgpr_write_b32 a238, v74\n"
      "v_accvgpr_write_b32 a239, v75\n"
      "v_mfma_f32_16x16x32_bf16 v[140:143], v[72:75], v[56:59], v[140:143]\n"
      "s_waitcnt vmcnt(8) lgkmcnt(0)\n"
      "s_barrier\n"
      "ds_read_b64_tr_b16 v[60:61], v53 offset:0\n"
      "ds_read_b64_tr_b16 v[62:63], v53 offset:1920\n"
      "ds_read_b64_tr_b16 a[32:33], v52 offset:144\n"
      "ds_read_b64_tr_b16 a[34:35], v52 offset:2064\n"
      "ds_read_b64_tr_b16 a[104:105], v52 offset:288\n"
      "ds_read_b64_tr_b16 a[106:107], v52 offset:2208\n"
      "ds_read_b64_tr_b16 a[176:177], v52 offset:0\n"
      "ds_read_b64_tr_b16 a[178:179], v52 offset:1920\n"
      "ds_bpermute_b32 v72, v54, v68\n"
      "ds_bpermute_b32 v73, v54, v69\n"
      "ds_bpermute_b32 v74, v54, v70\n"
      ".Lq_px:\n"
      "s_cbranch_execz .Lq_pr\n"
      "ds_bpermute_b32 v75, v54, v71\n"
      "v_mfma_f32_16x16x32_bf16 v[136:139], a[28:31], v[56:59], v[136:139]\n"
      "v_mfma_f32_16x16x32_bf16 v[144:147], v[56:59], a[100:103], v[144:147]\n"
      "v_mfma_f32_16x16x32_bf16 v[148:151], a[172:175], v[56:59], v[148:151]\n"
      "s_add_u32 m0, s43, 116224\n"
      "s_nop 0\n"
      "buffer_load_dwordx4 v48, s[12:15], s51 offen offset:0 lds\n"
      "buffer_load_dwordx4 v49, s[12:15], s51 offen offset:1024 lds\n"
      "buffer_load_dwordx4 v50, s[12:15], s51 offen offset:2048 lds\n"
      "buffer_load_dwordx4 v51, s[12:15], s51 offen offset:3072 lds\n"
      "s_add_u32 s51, s51, 110592\n"
      "v_and_b32 v64, v55, v64\n"
      "v_and_b32 v65, v55, v65\n"
      "v_and_b32 v66, v55, v66\n"
      "v_and_b32 v67, v55, v67\n"
      "v_accvgpr_write_b32 a240, v64\n"
      "v_accvgpr_write_b32 a241, v65\n"
      "v_accvgpr_write_b32 a242, v66\n"
      "v_accvgpr_write_b32 a243, v67\n"
      "v_mfma_f32_16x16x32_bf16 v[140:143], v[64:67], v[56:59], v[140:143]\n"
      "s_waitcnt vmcnt(8) lgkmcnt(0)\n"
      "s_barrier\n"
      "ds_read_b64_tr_b16 v[68:69], v53 offset:16896\n"
      "ds_read_b64_tr_b16 v[70:71], v53 offset:18816\n"
      "ds_read_b64_tr_b16 a[36:37], v52 offset:17040\n"
      "ds_read_b64_tr_b16 a[38:39], v52 offset:18960\n"
      "ds_read_b64_tr_b16 a[108:109], v52 offset:17184\n"
      "ds_read_b64_tr_b16 a[110:111], v52 offset:19104\n"
      "ds_read_b64_tr_b16 a[180:181], v52 offset:16896\n"
      "ds_read_b64_tr_b16 a[182:183], v52 offset:18816\n"
      "ds_bpermute_b32 v64, v54, v60\n"
      "ds_bpermute_b32 v65, v54, v61\n"
      "ds_bpermute_b32 v66, v54, v62\n"
      "ds_bpermute_b32 v67, v54, v63\n"
      "v_mfma_f32_16x16x32_bf16 v[136:139], a[32:35], v[56:59], v[136:139]\n"
      "v_mfma_f32_16x16x32_bf16 v[144:147], v[56:59], a[104:107], v[144:147]\n"
      "v_mfma_f32_16x16x32_bf16 v[148:151], a[176:179], v[56:59], v[148:151]\n"
      "s_add_u32 m0, s43, 65536\n"
      "s_nop 0\n"
      "buffer_load_dwordx4 v48, s[12:15], s51 offen offset:0 lds\n"
      "buffer_load_dwordx4 v49, s[12:15], s51 offen offset:1024 lds\n"
      "buffer_load_dwordx4 v50, s[12:15], s51 offen offset:2048 lds\n"
      "buffer_load_dwordx4 v51, s[12:15], s51 offen offset:3072 lds\n"
      "s_add_u32 s51, s51, 110592\n"
      "v_and_b32 v72, v55, v72\n"
      "v_and_b32 v73, v55, v73\n"
      "v_and_b32 v74, v55, v74\n"
      "v_and_b32 v75, v55, v75\n"
      "v_accvgpr_write_b32 a244, v72\n"
      "v_accvgpr_write_b32 a245, v73\n"
      "v_accvgpr_write_b32 a246, v74\n"
      "v_accvgpr_write_b32 a247, v75\n"
      "v_mfma_f32_16x16x32_bf16 v[140:143], v[72:75], v[56:59], v[140:143]\n"
      "s_waitcnt vmcnt(8) lgkmcnt(0)\n"
      "s_barrier\n"
      "ds_read_b64_tr_b16 v[60:61], v53 offset:33792\n"
      "ds_read_b64_tr_b16 v[62:63], v53 offset:35712\n"
      "ds_read_b64_tr_b16 a[40:41], v52 offset:33936\n"
      "ds_read_b64_tr_b16 a[42:43], v52 offset:35856\n"
      "ds_read_b64_tr_b16 a[112:113], v52 offset:34080\n"
      "ds_read_b64_tr_b16 a[114:115], v52 offset:36000\n"
      "ds_read_b64_tr_b16 a[184:185], v52 offset:33792\n"
      "ds_read_b64_tr_b16 a[186:187], v52 offset:35712\n"
      "ds_bpermute_b32 v72, v54, v68\n"
      "ds_bpermute_b32 v73, v54, v69\n"
      "ds_bpermute_b32 v74, v54, v70\n"
      "ds_bpermute_b32 v75, v54, v71\n"
      "v_mfma_f32_16x16x32_bf16 v[136:139], a[36:39], v[56:59], v[136:139]\n"
      "v_mfma_f32_16x16x32_bf16 v[144:147], v[56:59], a[108:111], v[144:147]\n"
      "v_mfma_f32_16x16x32_bf16 v[148:151], a[180:183], v[56:59], v[148:151]\n"
      "s_add_u32 m0, s43, 82432\n"
      "s_nop 0\n"
      "buffer_load_dwordx4 v48, s[12:15], s51 offen offset:0 lds\n"
      "buffer_load_dwordx4 v49, s[12:15], s51 offen offset:1024 lds\n"
      "buffer_load_dwordx4 v50, s[12:15], s51 offen offset:2048 lds\n"
      "buffer_load_dwordx4 v51, s[12:15], s51 offen offset:3072 lds\n"
      "s_add_u32 s51, s51, 110592\n"
      "v_and_b32 v64, v55, v64\n"
      "v_and_b32 v65, v55, v65\n"
      "v_and_b32 v66, v55, v66\n"
      "v_and_b32 v67, v55, v67\n"
      "v_accvgpr_write_b32 a248, v64\n"
      "v_accvgpr_write_b32 a249, v65\n"
      "v_accvgpr_write_b32 a250, v66\n"
      "v_accvgpr_write_b32 a251, v67\n"
      "v_mfma_f32_16x16x32_bf16 v[140:143], v[64:67], v[56:59], v[140:143]\n"
      "s_waitcnt vmcnt(8) lgkmcnt(0)\n"
      "s_barrier\n"
      "ds_read_b64_tr_b16 v[68:69], v53 offset:50688\n"
      "ds_read_b64_tr_b16 v[70:71], v53 offset:52608\n"
      "ds_read_b64_tr_b16 a[44:45], v52 offset:50832\n"
      "ds_read_b64_tr_b16 a[46:47], v52 offset:52752\n"
      "ds_read_b64_tr_b16 a[116:117], v52 offset:50976\n"
      "ds_read_b64_tr_b16 a[118:119], v52 offset:52896\n"
      "ds_read_b64_tr_b16 a[188:189], v52 offset:50688\n"
      "ds_read_b64_tr_b16 a[190:191], v52 offset:52608\n"
      "ds_bpermute_b32 v64, v54, v60\n"
      "ds_bpermute_b32 v65, v54, v61\n"
      "ds_bpermute_b32 v66, v54, v62\n"
      "ds_bpermute_b32 v67, v54, v63\n"
      "v_mfma_f32_16x16x32_bf16 v[136:139], a[40:43], v[56:59], v[136:139]\n"
      "v_mfma_f32_16x16x32_bf16 v[144:147], v[56:59], a[112:115], v[144:147]\n"
      "v_mfma_f32_16x16x32_bf16 v[148:151], a[184:187], v[56:59], v[148:151]\n"
      "s_add_u32 m0, s43, 99328\n"
      "s_nop 0\n"
      "buffer_load_dwordx4 v48, s[12:15], s51 offen offset:0 lds\n"
      "buffer_load_dwordx4 v49, s[12:15], s51 offen offset:1024 lds\n"
      "buffer_load_dwordx4 v50, s[12:15], s51 offen offset:2048 lds\n"
      "buffer_load_dwordx4 v51, s[12:15], s51 offen offset:3072 lds\n"
      "s_add_u32 s51, s51, 110592\n"
      "v_and_b32 v72, v55, v72\n"
      "v_and_b32 v73, v55, v73\n"
      "v_and_b32 v74, v55, v74\n"
      "v_and_b32 v75, v55, v75\n"
      "v_accvgpr_write_b32 a252, v72\n"
      "v_accvgpr_write_b32 a253, v73\n"
      "v_accvgpr_write_b32 a254, v74\n"
      "v_accvgpr_write_b32 a255, v75\n"
      "v_mfma_f32_16x16x32_bf16 v[140:143], v[72:75], v[56:59], v[140:143]\n"
      "s_waitcnt vmcnt(8) lgkmcnt(0)\n"
      "s_barrier\n"
      "ds_read_b64_tr_b16 v[60:61], v53 offset:0\n"
      "ds_read_b64_tr_b16 v[62:63], v53 offset:1920\n"
      "ds_read_b64_tr_b16 a[48:49], v52 offset:144\n"
      "ds_read_b64_tr_b16 a[50:51], v52 offset:2064\n"
      "ds_read_b64_tr_b16 a[120:121], v52 offset:288\n"
      "ds_read_b64_tr_b16 a[122:123], v52 offset:2208\n"
      "ds_read_b64_tr_b16 a[192:193], v52 offset:0\n"
      "ds_read_b64_tr_b16 a[194:195], v52 offset:1920\n"
      "ds_bpermute_b32 v72, v54, v68\n"
      "ds_bpermute_b32 v73, v54, v69\n"
      "ds_bpermute_b32 v74, v54, v70\n"
      "ds_bpermute_b32 v75, v54, v71\n"
      "v_mfma_f32_16x16x32_bf16 v[136:139], a[44:47], v[56:59], v[136:139]\n"
      "v_mfma_f32_16x16x32_bf16 v[144:147], v[56:59], a[116:119], v[144:147]\n"
      "v_mfma_f32_16x16x32_bf16 v[148:151], a[188:191], v[56:59], v[148:151]\n"
      "s_add_u32 m0, s43, 116224\n"
      "s_nop 0\n"
      "buffer_load_dwordx4 v48, s[12:15], s51 offen offset:0 lds\n"
      "buffer_load_dwordx4 v49, s[12:15], s51 offen offset:1024 lds\n"
      "buffer_load_dwordx4 v50, s[12:15], s51 offen offset:2048 lds\n"
      "buffer_load_dwordx4 v51, s[12:15], s51 offen offset:3072 lds\n"
      "s_add_u32 s51, s51, 110592\n"
      "v_and_b32 v64, v55, v64\n"
      "v_and_b32 v65, v55, v65\n"
      "v_and_b32 v66, v55, v66\n"
      "v_and_b32 v67, v55, v67\n"
      "v_mov_b32 v16, v64\n"
      "v_mov_b32 v17, v65\n"
      "v_mov_b32 v18, v66\n"
      "v_mov_b32 v19, v67\n"
      "v_mfma_f32_16x16x32_bf16 v[140:143], v[64:67], v[56:59], v[140:143]\n"
      "s_add_u32 s30, s36, 24576\n"
      "buffer_load_dword v204, v2, s[8:11], s30 offen sc1\n"
      "s_add_u32 s30, s36, 49152\n"
      "buffer_load_dword v205, v2, s[8:11], s30 offen sc1\n"
      "s_waitcnt vmcnt(10) lgkmcnt(0)\n"
      "s_barrier\n"
      "ds_read_b64_tr_b16 v[68:69], v53 offset:16896\n"
      "ds_read_b64_tr_b16 v[70:71], v53 offset:18816\n"
      "ds_read_b64_tr_b16 a[52:53], v52 offset:17040\n"
      "ds_read_b64_tr_b16 a[54:55], v52 offset:18960\n"
      "ds_read_b64_tr_b16 a[124:125], v52 offset:17184\n"
      "ds_read_b64_tr_b16 a[126:127], v52 offset:19104\n"
      "ds_read_b64_tr_b16 a[196:197], v52 offset:16896\n"
      "ds_read_b64_tr_b16 a[198:199], v52 offset:18816\n"
      "ds_bpermute_b32 v64, v54, v60\n"
      "ds_bpermute_b32 v65, v54, v61\n"
      "ds_bpermute_b32 v66, v54, v62\n"
      "ds_bpermute_b32 v67, v54, v63\n"
      "v_mfma_f32_16x16x32_bf16 v[136:139], a[48:51], v[56:59], v[136:139]\n"
      "v_mfma_f32_16x16x32_bf16 v[144:147], v[56:59], a[120:123], v[144:147]\n"
      "v_mfma_f32_16x16x32_bf16 v[148:151], a[192:195], v[56:59], v[148:151]\n"
      "s_add_u32 m0, s43, 65536\n"
      "s_nop 0\n"
      "buffer_load_dwordx4 v48, s[12:15], s51 offen offset:0 lds\n"
      "buffer_load_dwordx4 v49, s[12:15], s51 offen offset:1024 lds\n"
      "buffer_load_dwordx4 v50, s[12:15], s51 offen offset:2048 lds\n"
      "buffer_load_dwordx4 v51, s[12:15], s51 offen offset:3072 lds\n"
      "s_add_u32 s51, s51, 110592\n"
      "v_and_b32 v72, v55, v72\n"
      "v_and_b32 v73, v55, v73\n"
      "v_and_b32 v74, v55, v74\n"
      "v_and_b32 v75, v55, v75\n"
      "v_mov_b32 v20, v72\n"
      "v_mov_b32 v21, v73\n"
      "v_mov_b32 v22, v74\n"
      "v_mov_b32 v23, v75\n"
      "v_mfma_f32_16x16x32_bf16 v[140:143], v[72:75], v[56:59], v[140:143]\n"
      "s_waitcnt vmcnt(10) lgkmcnt(0)\n"
      "s_barrier\n"
      "ds_read_b64_tr_b16 v[60:61], v53 offset:33792\n"
      "ds_read_b64_tr_b16 v[62:63], v53 offset:35712\n"
      "ds_read_b64_tr_b16 a[56:57], v52 offset:33936\n"
      "ds_read_b64_tr_b16 a[58:59], v52 offset:35856\n"
      "ds_read_b64_tr_b16 a[128:129], v52 offset:34080\n"
      "ds_read_b64_tr_b16 a[130:131], v52 offset:36000\n"
      "ds_read_b64_tr_b16 a[200:201], v52 offset:33792\n"
      "ds_read_b64_tr_b16 a[202:203], v52 offset:35712\n"
      "ds_bpermute_b32 v72, v54, v68\n"
      "ds_bpermute_b32 v73, v54, v69\n"
      "ds_bpermute_b32 v74, v54, v70\n"
      "ds_bpermute_b32 v75, v54, v71\n"
      "v_mfma_f32_16x16x32_bf16 v[136:139], a[52:55], v[56:59], v[136:139]\n"
      "v_mfma_f32_16x16x32_bf16 v[144:147], v[56:59], a[124:127], v[144:147]\n"
      "v_mfma_f32_16x16x32_bf16 v[148:151], a[196:199], v[56:59], v[148:151]\n"
      "s_add_u32 m0, s43, 82432\n"
      "s_nop 0\n"
      "buffer_load_dwordx4 v48, s[12:15], s51 offen offset:0 lds\n"
      "buffer_load_dwordx4 v49, s[12:15], s51 offen offset:1024 lds\n"
      "buffer_load_dwordx4 v50, s[12:15], s51 offen offset:2048 lds\n"
      "buffer_load_dwordx4 v51, s[12:15], s51 offen offset:3072 lds\n"
      "s_add_u32 s51, s51, 110592\n"
      "v_and_b32 v64, v55, v64\n"
      "v_and_b32 v65, v55, v65\n"
      "v_and_b32 v66, v55, v66\n"
      "v_and_b32 v67, v55, v67\n"
      "v_mov_b32 v24, v64\n"
      "v_mov_b32 v25, v65\n"
      "v_mov_b32 v26, v66\n"
      "v_mov_b32 v27, v67\n"
      "v_mfma_f32_16x16x32_bf16 v[140:143], v[64:67], v[56:59], v[140:143]\n"
      "s_mul_i32 s21, s21, 1664525\n"
      "s_add_u32 s21, s21, 1013904223\n"
      "v_mov_b32 v13, s21\n"
      "buffer_store_dword v13, v10, s[8:11], 0 offen sc1\n"
      "v_not_b32 v200, v13\n"
      "v_not_b32 v201, v13\n"
      "v_not_b32 v202, v13\n"
      "v_not_b32 v203, v13\n"
      "s_add_u32 s30, s40, 0\n"
      "buffer_store_dwordx4 v[200:203], v76, s[8:11], s30 offen sc1\n"
      "v_xor_b32 v13, v13, v14\n"
      "s_add_u32 s52, s40, 4294893568\n"
      "s_add_u32 s40, s40, 4294918144\n"
      "s_add_u32 s36, s36, 24576\n"
      "s_mov_b32 s45, s36\n"
      "s_min_u32 s45, s45, s6\n"
      "s_waitcnt vmcnt(11)\n"
      "v_cmp_ne_u32 vcc, s21, v204\n"
      "s_add_u32 m0, s41, 0\n"
      "s_add_u32 s36, s36, 24576\n"
      "s_mov_b32 s46, s45\n"
      "s_cbranch_vccnz .Lsp_q0_1\n"
      ".Lbk_q0_1:\n"
      "buffer_load_dwordx4 v0, s[8:11], s46 offen offset:1024 sc1 lds\n"
      "buffer_load_dwordx4 v0, s[8:11], s46 offen offset:0 sc1 lds\n"
      "buffer_load_dwordx4 v0, s[8:11], s46 offen offset:2048 sc1 lds\n"
      "s_mov_b32 s45, s36\n"
      "s_min_u32 s45, s45, s6\n"
      "buffer_load_dwordx4 v0, s[8:11], s46 offen offset:3072 sc1 lds\n"
      "s_add_u32 m0, s42, 0\n"
      "s_nop 0\n"
      "buffer_load_dwordx4 v1, s[8:11], s46 offen offset:0 sc1 lds\n"
      "s_waitcnt lgkmcnt(0)\n"
      "s_barrier\n"
      "ds_read_b64_tr_b16 v[68:69], v53 offset:50688\n"
      "ds_read_b64_tr_b16 v[70:71], v53 offset:52608\n"
      "ds_read_b64_tr_b16 a[60:61], v52 offset:50832\n"
      "ds_read_b64_tr_b16 a[62:63], v52 offset:52752\n"
      "ds_read_b64_tr_b16 a[132:133], v52 offset:50976\n"
      "ds_read_b64_tr_b16 a[134:135], v52 offset:52896\n"
      "ds_read_b64_tr_b16 a[204:205], v52 offset:50688\n"
      "ds_read_b64_tr_b16 a[206:207], v52 offset:52608\n"
      "ds_bpermute_b32 v64, v54, v60\n"
      "ds_bpermute_b32 v65, v54, v61\n"
      "ds_bpermute_b32 v66, v54, v62\n"
      "ds_bpermute_b32 v67, v54, v63\n"
      "v_mfma_f32_16x16x32_bf16 v[136:139], a[56:59], v[56:59], v[136:139]\n"
      "v_mfma_f32_16x16x32_bf16 v[144:147], v[56:59], a[128:131], v[144:147]\n"
      "v_mfma_f32_16x16x32_bf16 v[148:151], a[200:203], v[56:59], v[148:151]\n"
      "v_and_b32 v72, v55, v72\n"
      "v_and_b32 v73, v55, v73\n"
      "v_and_b32 v74, v55, v74\n"
      "v_and_b32 v75, v55, v75\n"
      "v_mov_b32 v28, v72\n"
      "v_mov_b32 v29, v73\n"
      "v_mov_b32 v30, v74\n"
      "v_mov_b32 v31, v75\n"
      "v_mfma_f32_16x16x32_bf16 v[140:143], v[72:75], v[56:59], v[140:143]\n"
      "s_waitcnt vmcnt(11) lgkmcnt(0)\n"
      "s_barrier\n"
      "ds_read_b64_tr_b16 v[60:61], v53 offset:0\n"
      "ds_read_b64_tr_b16 v[62:63], v53 offset:1920\n"
      "ds_read_b64_tr_b16 a[64:65], v52 offset:144\n"
      "ds_read_b64_tr_b16 a[66:67], v52 offset:2064\n"
      "ds_read_b64_tr_b16 a[136:137], v52 offset:288\n"
      "ds_read_b64_tr_b16 a[138:139], v52 offset:2208\n"
      "ds_read_b64_tr_b16 a[208:209], v52 offset:0\n"
      "ds_read_b64_tr_b16 a[210:211], v52 offset:1920\n"
      "ds_bpermute_b32 v72, v54, v68\n"
      "ds_bpermute_b32 v73, v54, v69\n"
      "ds_bpermute_b32 v74, v54, v70\n"
      "ds_bpermute_b32 v75, v54, v71\n"
      "v_mfma_f32_16x16x32_bf16 v[136:139], a[60:63], v[56:59], v[136:139]\n"
      "v_mfma_f32_16x16x32_bf16 v[144:147], v[56:59], a[132:135], v[144:147]\n"
      "v_mfma_f32_16x16x32_bf16 v[148:151], a[204:207], v[56:59], v[148:151]\n"
      "v_and_b32 v64, v55, v64\n"
      "v_and_b32 v65, v55, v65\n"
      "v_and_b32 v66, v55, v66\n"
      "v_and_b32 v67, v55, v67\n"
      "v_mov_b32 v32, v64\n"
      "v_mov_b32 v33, v65\n"
      "v_mov_b32 v34, v66\n"
      "v_mov_b32 v35, v67\n"
      "v_mfma_f32_16x16x32_bf16 v[140:143], v[64:67], v[56:59], v[140:143]\n"
      "s_waitcnt vmcnt(7) lgkmcnt(0)\n"
      "s_barrier\n"
      "ds_read_b64_tr_b16 v[68:69], v53 offset:16896\n"
      "ds_read_b64_tr_b16 v[70:71], v53 offset:18816\n"
      "ds_read_b64_tr_b16 a[68:69], v52 offset:17040\n"
      "ds_read_b64_tr_b16 a[70:71], v52 offset:18960\n"
      "ds_read_b64_tr_b16 a[140:141], v52 offset:17184\n"
      "ds_read_b64_tr_b16 a[142:143], v52 offset:19104\n"
      "ds_read_b64_tr_b16 a[212:213], v52 offset:16896\n"
      "ds_read_b64_tr_b16 a[214:215], v52 offset:18816\n"
      "ds_bpermute_b32 v64, v54, v60\n"
      "ds_bpermute_b32 v65, v54, v61\n"
      "ds_bpermute_b32 v66, v54, v62\n"
      "ds_bpermute_b32 v67, v54, v63\n"
      "v_mfma_f32_16x16x32_bf16 v[136:139], a[64:67], v[56:59], v[136:139]\n"
      "v_mfma_f32_16x16x32_bf16 v[144:147], v[56:59], a[136:139], v[144:147]\n"
      "v_mfma_f32_16x16x32_bf16 v[148:151], a[208:211], v[56:59], v[148:151]\n"
      "v_and_b32 v72, v55, v72\n"
      "v_and_b32 v73, v55, v73\n"
      "v_and_b32 v74, v55, v74\n"
      "v_and_b32 v75, v55, v75\n"
      "v_mov_b32 v36, v72\n"
      "v_mov_b32 v37, v73\n"
      "v_mov_b32 v38, v74\n"
      "v_mov_b32 v39, v75\n"
      "v_mfma_f32_16x16x32_bf16 v[140:143], v[72:75], v[56:59], v[140:143]\n"
      "s_waitcnt lgkmcnt(0)\n"
      "ds_bpermute_b32 v72, v54, v68\n"
      "ds_bpermute_b32 v73, v54, v69\n"
      "ds_bpermute_b32 v74, v54, v70\n"
      "ds_bpermute_b32 v75, v54, v71\n"
      "v_mfma_f32_16x16x32_bf16 v[136:139], a[68:71], v[56:59], v[136:139]\n"
      "v_mfma_f32_16x16x32_bf16 v[144:147], v[56:59], a[140:143], v[144:147]\n"
      "v_mfma_f32_16x16x32_bf16 v[148:151], a[212:215], v[56:59], v[148:151]\n"
      "v_and_b32 v64, v55, v64\n"
      "v_and_b32 v65, v55, v65\n"
      "v_and_b32 v66, v55, v66\n"
      "v_and_b32 v67, v55, v67\n"
      "v_mov_b32 v40, v64\n"
      "v_mov_b32 v41, v65\n"
      "v_mov_b32 v42, v66\n"
      "v_mov_b32 v43, v67\n"
      "v_mfma_f32_16x16x32_bf16 v[140:143], v[64:67], v[56:59], v[140:143]\n"
      "s_waitcnt lgkmcnt(0)\n"
      "v_and_b32 v72, v55, v72\n"
      "v_and_b32 v73, v55, v73\n"
      "v_and_b32 v74, v55, v74\n"
      "v_and_b32 v75, v55, v75\n"
      "v_mov_b32 v44, v72\n"
      "v_mov_b32 v45, v73\n"
      "v_mov_b32 v46, v74\n"
      "v_mov_b32 v47, v75\n"
      "v_mfma_f32_16x16x32_bf16 v[140:143], v[72:75], v[56:59], v[140:143]\n"
      "s_add_u32 s36, s36, 24576\n"
      "s_cmp_eq_u32 s36, s37\n"
      "s_cselect_b32 s47, s38, 0\n"
      "s_cselect_b32 s37, s48, s37\n"
      "s_cselect_b32 s49, s39, s53\n"
      "s_add_u32 m0, s41, 20480\n"
      "s_mov_b32 s46, s45\n"
      "s_add_u32 s45, s36, s47\n"
      "v_add_u32 v9, 1, v9\n"
      "v_cmp_ne_u32 vcc, s21, v205\n"
      "s_min_u32 s45, s45, s6\n"
      "s_cbranch_vccnz .Lsp_q1_2\n"
      ".Lbk_q1_2:\n"
      "buffer_load_dwordx4 v0, s[8:11], s46 offen offset:1024 sc1 lds\n"
      "s_waitcnt vmcnt(1)\n"
      "ds_write_b32 v11, v9\n"
      "buffer_load_dwordx4 v0, s[8:11], s46 offen offset:0 sc1 lds\n"
      "buffer_load_dwordx4 v0, s[8:11], s46 offen offset:3072 sc1 lds\n"
      "buffer_load_dword v15, v2, s[8:11], s45 offen sc1\n"
      "ds_read_b32 v10, v12\n"
      "buffer_load_dwordx4 v0, s[8:11], s46 offen offset:2048 sc1 lds\n"
      "s_waitcnt lgkmcnt(0)\n"
      "v_cmp_gt_i32 vcc, v9, v10\n"
      "s_add_u32 m0, s42, 20480\n"
      "s_nop 0\n"
      "buffer_load_dwordx4 v1, s[8:11], s46 offen offset:0 sc1 lds\n"
      "s_cbranch_vccnz .Lsp_qy0_3\n"
      ".Lbk_qy0_3:\n"
      "ds_read_b128 v[52:55], v0 offset:1024\n"
      "ds_read_b128 v[48:51], v0 offset:0\n"
      "ds_read_b128 v[56:59], v0 offset:2048\n"
      "ds_read_b128 v[68:71], v0 offset:5120\n"
      "ds_read_b128 v[64:67], v0 offset:4096\n"
      "ds_read_b128 v[60:63], v0 offset:3072\n"
      "buffer_load_dword v14, v11, s[8:11], s31 offen\n"
      "ds_read2st64_b64 v[212:215], v4 offset0:0 offset1:1\n"
      "ds_read_b128 v[72:75], v0 offset:6144\n"
      "ds_read_b128 v[76:79], v0 offset:7168\n"
      "ds_read_b128 v[88:91], v0 offset:10240\n"
      "ds_read_b128 v[108:111], v0 offset:15360\n"
      "s_mov_b32 s20, 21\n"
      "s_waitcnt lgkmcnt(0)\n"
      "s_nop 4\n"
      "s_branch .Lq_loop\n"
      ".p2align 12\n"
      ".Lq_p0:\n"
      "s_branch .Lq_pr\n"
      ".p2align 5\n"
      ".Lq_loop:\n"
      "v_mfma_f32_16x16x32_bf16 v[120:123], a[0:3], v[48:51], 0\n"
      "ds_read_b128 v[80:83], v0 offset:8192\n"
      "ds_read_b128 v[84:87], v0 offset:9216\n"
      "v_mfma_f32_16x16x32_bf16 v[120:123], a[4:7], v[52:55], v[120:123]\n"
      "ds_read_b128 v[96:99], v0 offset:12288\n"
      "v_mul_f32 v232, 0x3eae1a93, v219\n"
      "v_mfma_f32_16x16x32_bf16 v[120:123], a[8:11], v[56:59], v[120:123]\n"
      "s_waitcnt lgkmcnt(7)\n"
      "ds_read_b128 v[104:107], v0 offset:14336\n"
      "v_fma_f32 v135, -v218, v151, v135\n"
      "v_mfma_f32_16x16x32_bf16 v[120:123], a[12:15], v[60:63], v[120:123]\n"
      "v_fma_f32 v133, -v218, v149, v133\n"
      "v_fma_f32 v134, -v218, v150, v134\n"
      "v_mfma_f32_16x16x32_bf16 v[120:123], a[16:19], v[64:67], v[120:123]\n"
      "ds_read_b128 v[92:95], v0 offset:11264\n"
      "v_fma_f32 v132, -v218, v148, v132\n"
      "v_mfma_f32_16x16x32_bf16 v[120:123], a[20:23], v[68:71], v[120:123]\n"
      "s_waitcnt lgkmcnt(5)\n"
      "ds_read_b64 v[216:217], v4 offset:1024\n"
      "v_cvt_pk_bf16_f32 v176, v132, v133\n"
      "v_mfma_f32_16x16x32_bf16 v[120:123], a[24:27], v[72:75], v[120:123]\n"
      "ds_read_b128 v[100:103], v0 offset:13312\n"
      "v_cvt_pk_bf16_f32 v177, v134, v135\n"
      "v_mfma_f32_16x16x32_bf16 v[120:123], a[28:31], v[76:79], v[120:123]\n"
      "s_waitcnt lgkmcnt(5)\n"
      "v_rcp_f32 v210, v210\n"
      "v_mfma_f32_16x16x32_bf16 v[120:123], a[32:35], v[80:83], v[120:123]\n"
      "v_mul_f32 v204, v204, v210\n"
      "v_add_f32 v212, v212, v214\n"
      "v_mfma_f32_16x16x32_bf16 v[188:191], v[172:175], v[176:179], v[152:155]\n"
      "ds_read_b128 v[112:115], v0 offset:16384\n"
      "v_mfma_f32_16x16x32_bf16 v[120:123], a[36:39], v[84:87], v[120:123]\n"
      "ds_read_b128 v[116:119], v0 offset:17408\n"
      "v_mul_f32 v208, v208, v210\n"
      "v_mfma_f32_16x16x32_bf16 v[120:123], a[40:43], v[88:91], v[120:123]\n"
      "s_waitcnt lgkmcnt(4)\n"
      "s_add_u32 s36, s36, 24576\n"
      "v_mul_f32 v207, v207, v210\n"
      "v_mfma_f32_16x16x32_bf16 v[120:123], a[44:47], v[92:95], v[120:123]\n"
      "v_mul_f32 v209, v209, v210\n"
      "v_cvt_pk_bf16_f32 v230, v208, v209\n"
      "v_mfma_f32_16x16x32_bf16 v[120:123], a[48:51], v[96:99], v[120:123]\n"
      "s_waitcnt lgkmcnt(2)\n"
      "buffer_store_dwordx2 v[230:231], v8, s[8:11], s40 offen sc1\n"
      "v_mfma_f32_16x16x32_bf16 v[120:123], a[52:55], v[100:103], v[120:123]\n"
      "v_mul_f32 v206, v206, v210\n"
      "v_add_f32 v218, v212, v216\n"
      "v_mfma_f32_16x16x32_bf16 v[120:123], a[56:59], v[104:107], v[120:123]\n"
      "s_mov_b32 s46, s45\n"
      "v_add_f32 v213, v213, v215\n"
      "v_mfma_f32_16x16x32_bf16 v[120:123], a[60:63], v[108:111], v[120:123]\n"
      "s_waitcnt lgkmcnt(1)\n"
      "v_cvt_pk_bf16_f32 v229, v206, v207\n"
      "v_mfma_f32_16x16x32_bf16 v[120:123], a[64:67], v[112:115], v[120:123]\n"
      "s_waitcnt lgkmcnt(0)\n"
      "v_mul_f32 v205, v205, v210\n"
      "v_cvt_pk_bf16_f32 v228, v204, v205\n"
      "v_mfma_f32_16x16x32_bf16 v[120:123], a[68:71], v[116:119], v[120:123]\n"
      "buffer_store_dwordx2 v[228:229], v7, s[8:11], s40 offen sc1\n"
      "v_mfma_f32_16x16x32_bf16 v[124:127], a[216:219], v[48:51], 0\n"
      "v_add_f32 v213, v213, v217\n"
      "v_fma_f32 v213, -v218, v218, v213\n"
      "v_mfma_f32_16x16x32_bf16 v[124:127], a[220:223], v[52:55], v[124:127]\n"
      "v_max_f32 v213, 0x3727c5ac, v213\n"
      "s_mov_b32 s45, s36\n"
      "v_mfma_f32_16x16x32_bf16 v[124:127], a[224:227], v[56:59], v[124:127]\n"
      "v_mov_b32 v187, v185\n"
      "v_mfma_f32_16x16x32_bf16 v[124:127], a[228:231], v[60:63], v[124:127]\n"
      "v_rsq_f32 v219, v213\n"
      "v_mfma_f32_16x16x32_bf16 v[124:127], a[232:235], v[64:67], v[124:127]\n"
      "s_add_u32 m0, s41, 40960\n"
      "ds_write_b64 v5, v[218:219] offset:0\n"
      "v_mfma_f32_16x16x32_bf16 v[124:127], a[236:239], v[68:71], v[124:127]\n"
      "s_waitcnt vmcnt(5)\n"
      "v_mov_b32 v186, v184\n"
      "v_cmp_ne_u32 vcc, s21, v15\n"
      "v_mfma_f32_16x16x32_bf16 v[124:127], a[240:243], v[72:75], v[124:127]\n"
      "s_min_u32 s45, s45, s6\n"
      "v_mfma_f32_16x16x32_bf16 v[124:127], a[244:247], v[76:79], v[124:127]\n"
      "v_cndmask_b32 v233, v219, 1.0, s[54:55]\n"
      "v_add_u32 v9, 1, v9\n"
      "v_mfma_f32_16x16x32_bf16 v[124:127], a[248:251], v[80:83], v[124:127]\n"
      "v_fma_f32 v123, -v218, v139, v123\n"
      "v_fma_f32 v122, -v218, v138, v122\n"
      "v_mfma_f32_16x16x32_bf16 v[124:127], a[252:255], v[84:87], v[124:127]\n"
      "v_fma_f32 v120, -v218, v136, v120\n"
      "v_fma_f32 v121, -v218, v137, v121\n"
      "v_mfma_f32_16x16x32_bf16 v[124:127], v[16:19], v[88:91], v[124:127]\n"
      "ds_read_b128 v[224:227], v6 offset:16\n"
      "v_mul_f32 v121, v219, v121\n"
      "v_mfma_f32_16x16x32_bf16 v[124:127], v[20:23], v[92:95], v[124:127]\n"
      "v_mul_f32 v120, v219, v120\n"
      "v_mfma_f32_16x16x32_bf16 v[124:127], v[24:27], v[96:99], v[124:127]\n"
      "s_cbranch_vccnz .Lsp_q2_4\n"
      ".Lbk_q2_4:\n"
      "v_mfma_f32_16x16x32_bf16 v[124:127], v[28:31], v[100:103], v[124:127]\n"
      "buffer_load_dword v15, v2, s[8:11], s45 offen sc1\n"
      "v_mfma_f32_16x16x32_bf16 v[124:127], v[32:35], v[104:107], v[124:127]\n"
      "v_mul_f32 v123, v219, v123\n"
      "v_mul_f32 v122, v219, v122\n"
      "v_mfma_f32_16x16x32_bf16 v[124:127], v[36:39], v[108:111], v[124:127]\n"
      "s_waitcnt vmcnt(4) lgkmcnt(1)\n"
      "ds_write_b32 v11, v9\n"
      "v_mfma_f32_16x16x32_bf16 v[124:127], v[40:43], v[112:115], v[124:127]\n"
      "v_mov_b32 v182, v180\n"
      "v_mfma_f32_16x16x32_bf16 v[124:127], v[44:47], v[116:119], v[124:127]\n"
      "v_cvt_pk_bf16_f32 v173, v122, v123\n"
      "v_mfma_f32_16x16x32_bf16 v[128:131], v[48:51], a[72:75], 0\n"
      "buffer_load_dwordx4 v0, s[8:11], s46 offen offset:1024 sc1 lds\n"
      "v_mfma_f32_16x16x32_bf16 v[128:131], v[52:55], a[76:79], v[128:131]\n"
      "ds_read_b128 v[220:223], v6 offset:0\n"
      "v_cvt_pk_bf16_f32 v172, v120, v121\n"
      "v_mfma_f32_16x16x32_bf16 v[128:131], v[56:59], a[80:83], v[128:131]\n"
      "v_fma_f32 v126, -v218, v142, v126\n"
      "v_fma_f32 v124, -v218, v140, v124\n"
      "v_mfma_f32_16x16x32_bf16 v[128:131], v[60:63], a[84:87], v[128:131]\n"
      "s_add_u32 s52, s52, 24576\n"
      "v_fma_f32 v125, -v218, v141, v125\n"
      "v_mul_f32 v124, v233, v124\n"
      "v_mfma_f32_16x16x32_bf16 v[128:131], v[64:67], a[88:91], v[128:131]\n"
      "v_mul_f32 v126, v219, v126\n"
      "v_mul_f32 v125, v233, v125\n"
      "v_mfma_f32_16x16x32_bf16 v[128:131], v[68:71], a[92:95], v[128:131]\n"
      "v_fma_f32 v127, -v218, v143, v127\n"
      "v_cvt_pk_bf16_f32 v175, v124, v125\n"
      "v_mfma_f32_16x16x32_bf16 v[128:131], v[72:75], a[96:99], v[128:131]\n"
      "v_mul_f32 v127, v219, v127\n"
      "v_cvt_pk_bf16_f32 v174, v126, v127\n"
      "v_mfma_f32_16x16x32_bf16 v[128:131], v[76:79], a[100:103], v[128:131]\n"
      "buffer_load_dwordx4 v0, s[8:11], s46 offen offset:2048 sc1 lds\n"
      "v_mfma_f32_16x16x32_bf16 v[192:195], v[172:175], v[176:179], v[156:159]\n"
      "ds_read_b32 v10, v12\n"
      "v_mfma_f32_16x16x32_bf16 v[196:199], v[172:175], v[164:167], v[168:171]\n"
      "v_mov_b32 v183, v181\n"
      "v_mov_b32 v178, v175\n"
      "v_mfma_f32_16x16x32_bf16 v[128:131], v[80:83], a[104:107], v[128:131]\n"
      "buffer_load_dwordx4 v0, s[8:11], s46 offen offset:0 sc1 lds\n"
      "v_mfma_f32_16x16x32_bf16 v[128:131], v[84:87], a[108:111], v[128:131]\n"
      "v_max_f32 v235, v194, v195\n"
      "v_max_f32 v234, v192, v193\n"
      "v_mfma_f32_16x16x32_bf16 v[128:131], v[88:91], a[112:115], v[128:131]\n"
      "v_max3_f32 v234, v188, v189, v234\n"
      "v_max3_f32 v235, v190, v191, v235\n"
      "v_mfma_f32_16x16x32_bf16 v[128:131], v[92:95], a[116:119], v[128:131]\n"
      "v_max_f32 v236, v234, v235\n"
      "v_mov_b32 v237, v236\n"
      "v_mfma_f32_16x16x32_bf16 v[128:131], v[96:99], a[120:123], v[128:131]\n"
      "v_permlane16_swap_b32 v236, v237\n"
      "v_mfma_f32_16x16x32_bf16 v[128:131], v[100:103], a[124:127], v[128:131]\n"
      "v_max_f32 v236, v236, v237\n"
      "v_mov_b32 v237, v236\n"
      "v_mfma_f32_16x16x32_bf16 v[128:131], v[104:107], a[128:131], v[128:131]\n"
      "s_waitcnt lgkmcnt(0)\n"
      "v_permlane32_swap_b32 v236, v237\n"
      "v_cmp_gt_i32 vcc, v9, v10\n"
      "v_mfma_f32_16x16x32_bf16 v[128:131], v[108:111], a[132:135], v[128:131]\n"
      "buffer_load_dwordx4 v0, s[8:11], s46 offen offset:3072 sc1 lds\n"
      "v_mfma_f32_16x16x32_bf16 v[128:131], v[112:115], a[136:139], v[128:131]\n"
      "s_add_u32 m0, s42, 40960\n"
      "v_max_f32 v236, v236, v237\n"
      "v_mul_f32 v236, v236, v232\n"
      "v_mfma_f32_16x16x32_bf16 v[128:131], v[116:119], a[140:143], v[128:131]\n"
      "s_add_u32 s40, s40, 24576\n"
      "v_fma_f32 v234, v234, v232, -v236\n"
      "v_fma_f32 v235, v235, v232, -v236\n"
      "v_mfma_f32_16x16x32_bf16 v[132:135], a[144:147], v[48:51], 0\n"
      "v_exp_f32 v234, v234\n"
      "v_mfma_f32_16x16x32_bf16 v[132:135], a[148:151], v[52:55], v[132:135]\n"
      "s_cbranch_vccnz .Lsp_qy1_5\n"
      ".Lbk_qy1_5:\n"
      "ds_read_b128 v[48:51], v0 offset:20480\n"
      "v_mfma_f32_16x16x32_bf16 v[132:135], a[152:155], v[56:59], v[132:135]\n"
      "v_exp_f32 v235, v235\n"
      "v_mfma_f32_16x16x32_bf16 v[132:135], a[156:159], v[60:63], v[132:135]\n"
      "ds_read_b128 v[56:59], v0 offset:22528\n"
      "v_fma_f32 v130, -v224, v147, v130\n"
      "v_mfma_f32_16x16x32_bf16 v[132:135], a[160:163], v[64:67], v[132:135]\n"
      "ds_read_b64 v[216:217], v4 offset:21504\n"
      "v_fma_f32 v129, -v222, v146, v129\n"
      "v_mfma_f32_16x16x32_bf16 v[132:135], a[164:167], v[68:71], v[132:135]\n"
      "buffer_load_dwordx4 v1, s[8:11], s46 offen offset:0 sc1 lds\n"
      "v_mfma_f32_16x16x32_bf16 v[132:135], a[168:171], v[72:75], v[132:135]\n"
      "v_cvt_pk_bf16_f32 v234, v234, v234\n"
      "v_fma_f32 v131, -v226, v144, v131\n"
      "v_mfma_f32_16x16x32_bf16 v[132:135], a[172:175], v[76:79], v[132:135]\n"
      "ds_read_b128 v[60:63], v0 offset:23552\n"
      "v_and_b32 v200, v160, v234\n"
      "v_mfma_f32_16x16x32_bf16 v[132:135], a[176:179], v[80:83], v[132:135]\n"
      "v_cvt_pk_bf16_f32 v235, v235, v235\n"
      "v_mul_f32 v129, v223, v129\n"
      "v_mfma_f32_16x16x32_bf16 v[132:135], a[180:183], v[84:87], v[132:135]\n"
      "ds_read_b128 v[52:55], v0 offset:21504\n"
      "v_mul_f32 v131, v227, v131\n"
      "v_mfma_f32_16x16x32_bf16 v[132:135], a[184:187], v[88:91], v[132:135]\n"
      "ds_read_b128 v[88:91], v0 offset:30720\n"
      "v_mul_f32 v130, v225, v130\n"
      "v_cvt_pk_bf16_f32 v185, v198, v199\n"
      "v_mfma_f32_16x16x32_bf16 v[132:135], a[188:191], v[92:95], v[132:135]\n"
      "v_cvt_pk_bf16_f32 v184, v196, v197\n"
      "v_and_b32 v201, v161, v235\n"
      "v_fma_f32 v128, -v220, v145, v128\n"
      "v_mfma_f32_16x16x32_bf16 v[132:135], a[192:195], v[96:99], v[132:135]\n"
      "ds_read_b128 v[68:71], v0 offset:25600\n"
      "v_and_b32 v203, v163, v235\n"
      "v_mfma_f32_16x16x32_bf16 v[132:135], a[196:199], v[100:103], v[132:135]\n"
      "v_mul_f32 v128, v221, v128\n"
      "v_and_b32 v202, v162, v234\n"
      "v_mfma_f32_16x16x32_bf16 v[132:135], a[200:203], v[104:107], v[132:135]\n"
      "v_cvt_pk_bf16_f32 v180, v128, v129\n"
      "v_cvt_pk_bf16_f32 v181, v130, v131\n"
      "v_mfma_f32_16x16x32_bf16 v[208:211], v[184:187], v[200:203], 0\n"
      "ds_read_b128 v[64:67], v0 offset:24576\n"
      "ds_read_b128 v[72:75], v0 offset:26624\n"
      "v_mfma_f32_16x16x32_bf16 v[204:207], v[180:183], v[200:203], 0\n"
      "s_waitcnt vmcnt(6) lgkmcnt(4)\n"
      "buffer_store_dword v13, v3, s[8:11], s52 offen sc1\n"
      "v_mfma_f32_16x16x32_bf16 v[132:135], a[204:207], v[108:111], v[132:135]\n"
      "ds_read2st64_b64 v[212:215], v4 offset0:40 offset1:41\n"
      "v_mfma_f32_16x16x32_bf16 v[132:135], a[208:211], v[112:115], v[132:135]\n"
      "v_rcp_f32 v210, v210\n"
      "v_mfma_f32_16x16x32_bf16 v[132:135], a[212:215], v[116:119], v[132:135]\n"
      "ds_read_b128 v[76:79], v0 offset:27648\n"
      "v_mul_f32 v209, v209, v210\n"
      "v_mfma_f32_16x16x32_bf16 v[120:123], a[0:3], v[48:51], 0\n"
      "ds_read_b128 v[80:83], v0 offset:28672\n"
      "ds_read_b128 v[100:103], v0 offset:33792\n"
      "v_mfma_f32_16x16x32_bf16 v[120:123], a[4:7], v[52:55], v[120:123]\n"
      "ds_read_b128 v[92:95], v0 offset:31744\n"
      "v_mul_f32 v208, v208, v210\n"
      "v_mfma_f32_16x16x32_bf16 v[120:123], a[8:11], v[56:59], v[120:123]\n"
      "ds_read_b128 v[84:87], v0 offset:29696\n"
      "v_fma_f32 v134, -v218, v150, v134\n"
      "v_mfma_f32_16x16x32_bf16 v[120:123], a[12:15], v[60:63], v[120:123]\n"
      "s_waitcnt lgkmcnt(6)\n"
      "ds_read_b128 v[104:107], v0 offset:34816\n"
      "v_fma_f32 v132, -v218, v148, v132\n"
      "v_mfma_f32_16x16x32_bf16 v[120:123], a[16:19], v[64:67], v[120:123]\n"
      "s_mov_b32 s50, s49\n"
      "ds_read_b128 v[96:99], v0 offset:32768\n"
      "v_mfma_f32_16x16x32_bf16 v[120:123], a[20:23], v[68:71], v[120:123]\n"
      "ds_read_b128 v[108:111], v0 offset:35840\n"
      "v_fma_f32 v133, -v218, v149, v133\n"
      "v_mfma_f32_16x16x32_bf16 v[120:123], a[24:27], v[72:75], v[120:123]\n"
      "s_waitcnt lgkmcnt(7)\n"
      "v_fma_f32 v135, -v218, v151, v135\n"
      "v_cvt_pk_bf16_f32 v177, v134, v135\n"
      "v_mfma_f32_16x16x32_bf16 v[120:123], a[28:31], v[76:79], v[120:123]\n"
      "s_waitcnt lgkmcnt(5)\n"
      "ds_read_b128 v[112:115], v0 offset:36864\n"
      "v_cvt_pk_bf16_f32 v176, v132, v133\n"
      "v_mfma_f32_16x16x32_bf16 v[120:123], a[32:35], v[80:83], v[120:123]\n"
      "v_mov_b32 v187, v185\n"
      "v_mul_f32 v232, 0x3eae1a93, v219\n"
      "v_mfma_f32_16x16x32_bf16 v[188:191], v[172:175], v[176:179], v[152:155]\n"
      "s_waitcnt lgkmcnt(4)\n"
      "ds_read_b128 v[116:119], v0 offset:37888\n"
      "v_mul_f32 v207, v207, v210\n"
      "v_mfma_f32_16x16x32_bf16 v[120:123], a[36:39], v[84:87], v[120:123]\n"
      "v_mul_f32 v204, v204, v210\n"
      "v_mfma_f32_16x16x32_bf16 v[120:123], a[40:43], v[88:91], v[120:123]\n"
      "v_cvt_pk_bf16_f32 v230, v208, v209\n"
      "v_add_f32 v213, v213, v215\n"
      "v_mfma_f32_16x16x32_bf16 v[120:123], a[44:47], v[92:95], v[120:123]\n"
      "s_waitcnt lgkmcnt(2)\n"
      "v_mul_f32 v205, v205, v210\n"
      "v_mfma_f32_16x16x32_bf16 v[120:123], a[48:51], v[96:99], v[120:123]\n"
      "buffer_store_dwordx2 v[230:231], v8, s[8:11], s40 offen sc1\n"
      "v_mfma_f32_16x16x32_bf16 v[120:123], a[52:55], v[100:103], v[120:123]\n"
      "v_add_f32 v212, v212, v214\n"
      "v_mfma_f32_16x16x32_bf16 v[120:123], a[56:59], v[104:107], v[120:123]\n"
      "v_cvt_pk_bf16_f32 v228, v204, v205\n"
      "v_mul_f32 v206, v206, v210\n"
      "v_mfma_f32_16x16x32_bf16 v[120:123], a[60:63], v[108:111], v[120:123]\n"
      "s_waitcnt vmcnt(7) lgkmcnt(0)\n"
      "v_cvt_pk_bf16_f32 v229, v206, v207\n"
      "v_cmp_ne_u32 vcc, s21, v15\n"
      "v_mfma_f32_16x16x32_bf16 v[120:123], a[64:67], v[112:115], v[120:123]\n"
      "s_add_u32 m0, s41, 0\n"
      "v_mov_b32 v182, v180\n"
      "v_mfma_f32_16x16x32_bf16 v[120:123], a[68:71], v[116:119], v[120:123]\n"
      "buffer_store_dwordx2 v[228:229], v7, s[8:11], s40 offen sc1\n"
      "v_mfma_f32_16x16x32_bf16 v[124:127], a[216:219], v[48:51], 0\n"
      "v_add_f32 v218, v212, v216\n"
      "v_add_f32_e64 v213, v213, v217\n"
      "v_mfma_f32_16x16x32_bf16 v[124:127], a[220:223], v[52:55], v[124:127]\n"
      "s_add_u32 s36, s36, 24576\n"
      "v_fma_f32 v213, -v218, v218, v213\n"
      "v_mfma_f32_16x16x32_bf16 v[124:127], a[224:227], v[56:59], v[124:127]\n"
      "s_mov_b32 s46, s45\n"
      "s_cbranch_vccnz .Lsp_q0_6\n"
      ".Lbk_q0_6:\n"
      "v_mfma_f32_16x16x32_bf16 v[124:127], a[228:231], v[60:63], v[124:127]\n"
      "buffer_load_dwordx4 v0, s[8:11], s46 offen offset:1024 sc1 lds\n"
      "v_mfma_f32_16x16x32_bf16 v[124:127], a[232:235], v[64:67], v[124:127]\n"
      "v_max_f32 v213, 0x3727c5ac, v213\n"
      "v_fma_f32 v120, -v218, v136, v120\n"
      "v_mfma_f32_16x16x32_bf16 v[124:127], a[236:239], v[68:71], v[124:127]\n"
      "v_mov_b32 v186, v184\n"
      "v_mfma_f32_16x16x32_bf16 v[124:127], a[240:243], v[72:75], v[124:127]\n"
      "v_add_u32 v9, 1, v9\n"
      "v_mfma_f32_16x16x32_bf16 v[124:127], a[244:247], v[76:79], v[124:127]\n"
      "s_add_u32 s40, s40, 24576\n"
      "v_fma_f32 v123, -v218, v139, v123\n"
      "v_mfma_f32_16x16x32_bf16 v[124:127], a[248:251], v[80:83], v[124:127]\n"
      "v_rsq_f32 v219, v213\n"
      "v_mfma_f32_16x16x32_bf16 v[124:127], a[252:255], v[84:87], v[124:127]\n"
      "v_mfma_f32_16x16x32_bf16 v[124:127], v[16:19], v[88:91], v[124:127]\n"
      "v_fma_f32 v121, -v218, v137, v121\n"
      "v_fma_f32 v122, -v218, v138, v122\n"
      "v_mfma_f32_16x16x32_bf16 v[124:127], v[20:23], v[92:95], v[124:127]\n"
      "buffer_load_dwordx4 v0, s[8:11], s46 offen offset:0 sc1 lds\n"
      "v_mfma_f32_16x16x32_bf16 v[124:127], v[24:27], v[96:99], v[124:127]\n"
      "v_mul_f32 v120, v219, v120\n"
      "v_mul_f32 v122, v219, v122\n"
      "v_mfma_f32_16x16x32_bf16 v[124:127], v[28:31], v[100:103], v[124:127]\n"
      "ds_write_b64 v5, v[218:219] offset:512\n"
      "v_mfma_f32_16x16x32_bf16 v[124:127], v[32:35], v[104:107], v[124:127]\n"
      "v_mul_f32 v121, v219, v121\n"
      "v_mul_f32 v123, v219, v123\n"
      "v_mfma_f32_16x16x32_bf16 v[124:127], v[36:39], v[108:111], v[124:127]\n"
      "buffer_load_dwordx4 v0, s[8:11], s46 offen offset:2048 sc1 lds\n"
      "v_mfma_f32_16x16x32_bf16 v[124:127], v[40:43], v[112:115], v[124:127]\n"
      "s_mov_b32 s45, s36\n"
      "ds_read_b128 v[224:227], v6 offset:528\n"
      "v_mfma_f32_16x16x32_bf16 v[124:127], v[44:47], v[116:119], v[124:127]\n"
      "s_min_u32 s45, s45, s6\n"
      "v_cndmask_b32 v233, v219, 1.0, s[54:55]\n"
      "v_mfma_f32_16x16x32_bf16 v[128:131], v[48:51], a[72:75], 0\n"
      "s_add_u32 s52, s52, 24576\n"
      "v_cvt_pk_bf16_f32 v172, v120, v121\n"
      "v_cvt_pk_bf16_f32 v173, v122, v123\n"
      "v_mfma_f32_16x16x32_bf16 v[128:131], v[52:55], a[76:79], v[128:131]\n"
      "buffer_load_dwordx4 v0, s[8:11], s46 offen offset:3072 sc1 lds\n"
      "v_mfma_f32_16x16x32_bf16 v[128:131], v[56:59], a[80:83], v[128:131]\n"
      "v_fma_f32 v126, -v218, v142, v126\n"
      "v_fma_f32 v124, -v218, v140, v124\n"
      "v_mul_f32 v126, v219, v126\n"
      "v_mfma_f32_16x16x32_bf16 v[128:131], v[60:63], a[84:87], v[128:131]\n"
      "v_fma_f32 v125, -v218, v141, v125\n"
      "v_mul_f32 v125, v233, v125\n"
      "v_mfma_f32_16x16x32_bf16 v[128:131], v[64:67], a[88:91], v[128:131]\n"
      "s_waitcnt vmcnt(7) lgkmcnt(1)\n"
      "ds_write_b32 v11, v9\n"
      "v_mfma_f32_16x16x32_bf16 v[128:131], v[68:71], a[92:95], v[128:131]\n"
      "v_fma_f32 v127, -v218, v143, v127\n"
      "v_mul_f32 v124, v233, v124\n"
      "v_mul_f32 v127, v219, v127\n"
      "v_mfma_f32_16x16x32_bf16 v[128:131], v[72:75], a[96:99], v[128:131]\n"
      "v_cvt_pk_bf16_f32 v174, v126, v127\n"
      "v_cvt_pk_bf16_f32 v175, v124, v125\n"
      "v_mfma_f32_16x16x32_bf16 v[128:131], v[76:79], a[100:103], v[128:131]\n"
      "buffer_load_dword v15, v2, s[8:11], s45 offen sc1\n"
      "v_mfma_f32_16x16x32_bf16 v[192:195], v[172:175], v[176:179], v[156:159]\n"
      "v_mov_b32 v183, v181\n"
      "v_mfma_f32_16x16x32_bf16 v[196:199], v[172:175], v[164:167], v[168:171]\n"
      "ds_read_b128 v[220:223], v6 offset:512\n"
      "s_add_u32 m0, s42, 0\n"
      "v_mfma_f32_16x16x32_bf16 v[128:131], v[80:83], a[104:107], v[128:131]\n"
      "buffer_load_dwordx4 v1, s[8:11], s46 offen offset:0 sc1 lds\n"
      "v_mfma_f32_16x16x32_bf16 v[128:131], v[84:87], a[108:111], v[128:131]\n"
      "v_max_f32 v235, v194, v195\n"
      "v_max3_f32 v235, v190, v191, v235\n"
      "v_mfma_f32_16x16x32_bf16 v[128:131], v[88:91], a[112:115], v[128:131]\n"
      "v_max_f32 v234, v192, v193\n"
      "v_max3_f32 v234, v188, v189, v234\n"
      "v_mfma_f32_16x16x32_bf16 v[128:131], v[92:95], a[116:119], v[128:131]\n"
      "ds_read_b32 v10, v12\n"
      "v_max_f32 v236, v234, v235\n"
      "v_mfma_f32_16x16x32_bf16 v[128:131], v[96:99], a[120:123], v[128:131]\n"
      "v_mov_b32 v237, v236\n"
      "v_cvt_pk_bf16_f32 v185, v198, v199\n"
      "v_mfma_f32_16x16x32_bf16 v[128:131], v[100:103], a[124:127], v[128:131]\n"
      "v_permlane16_swap_b32 v236, v237\n"
      "v_mfma_f32_16x16x32_bf16 v[128:131], v[104:107], a[128:131], v[128:131]\n"
      "v_max_f32 v236, v236, v237\n"
      "v_mov_b32 v237, v236\n"
      "v_mfma_f32_16x16x32_bf16 v[128:131], v[108:111], a[132:135], v[128:131]\n"
      "v_permlane32_swap_b32 v236, v237\n"
      "v_mfma_f32_16x16x32_bf16 v[128:131], v[112:115], a[136:139], v[128:131]\n"
      "s_waitcnt lgkmcnt(0)\n"
      "v_cmp_gt_i32 vcc, v9, v10\n"
      "v_max_f32 v236, v236, v237\n"
      "v_mfma_f32_16x16x32_bf16 v[128:131], v[116:119], a[140:143], v[128:131]\n"
      "v_mul_f32_e64 v236, v236, v232\n"
      "v_fma_f32 v235, v235, v232, -v236\n"
      "v_mfma_f32_16x16x32_bf16 v[132:135], a[144:147], v[48:51], 0\n"
      "v_exp_f32_e64 v235, v235\n"
      "v_mfma_f32_16x16x32_bf16 v[132:135], a[148:151], v[52:55], v[132:135]\n"
      "s_cbranch_vccnz .Lsp_qy2_7\n"
      ".Lbk_qy2_7:\n"
      "v_cvt_pk_bf16_f32 v235, v235, v235\n"
      "v_mfma_f32_16x16x32_bf16 v[132:135], a[152:155], v[56:59], v[132:135]\n"
      "v_fma_f32 v130, -v224, v147, v130\n"
      "v_mul_f32 v130, v225, v130\n"
      "v_mfma_f32_16x16x32_bf16 v[132:135], a[156:159], v[60:63], v[132:135]\n"
      "v_fma_f32 v129, -v222, v146, v129\n"
      "v_cvt_pk_bf16_f32 v184, v196, v197\n"
      "v_mfma_f32_16x16x32_bf16 v[132:135], a[160:163], v[64:67], v[132:135]\n"
      "v_fma_f32 v234, v234, v232, -v236\n"
      "v_fma_f32 v131, -v226, v144, v131\n"
      "v_mfma_f32_16x16x32_bf16 v[132:135], a[164:167], v[68:71], v[132:135]\n"
      "ds_read_b128 v[52:55], v0 offset:41984\n"
      "ds_read_b128 v[64:67], v0 offset:45056\n"
      "v_mfma_f32_16x16x32_bf16 v[132:135], a[168:171], v[72:75], v[132:135]\n"
      "ds_read_b128 v[56:59], v0 offset:43008\n"
      "v_mul_f32 v129, v223, v129\n"
      "v_mfma_f32_16x16x32_bf16 v[132:135], a[172:175], v[76:79], v[132:135]\n"
      "v_exp_f32 v234, v234\n"
      "v_mfma_f32_16x16x32_bf16 v[132:135], a[176:179], v[80:83], v[132:135]\n"
      "v_fma_f32 v128, -v220, v145, v128\n"
      "v_and_b32 v203, v163, v235\n"
      "v_mfma_f32_16x16x32_bf16 v[132:135], a[180:183], v[84:87], v[132:135]\n"
      "v_cvt_pk_bf16_f32 v234, v234, v234\n"
      "v_and_b32 v201, v161, v235\n"
      "v_mfma_f32_16x16x32_bf16 v[132:135], a[184:187], v[88:91], v[132:135]\n"
      "ds_read_b128 v[48:51], v0 offset:40960\n"
      "v_and_b32 v202, v162, v234\n"
      "v_mfma_f32_16x16x32_bf16 v[132:135], a[188:191], v[92:95], v[132:135]\n"
      "ds_read_b128 v[60:63], v0 offset:44032\n"
      "v_mul_f32 v128, v221, v128\n"
      "v_mfma_f32_16x16x32_bf16 v[132:135], a[192:195], v[96:99], v[132:135]\n"
      "ds_read_b128 v[76:79], v0 offset:48128\n"
      "v_cvt_pk_bf16_f32 v180, v128, v129\n"
      "v_mfma_f32_16x16x32_bf16 v[132:135], a[196:199], v[100:103], v[132:135]\n"
      "ds_read_b128 v[100:103], v0 offset:54272\n"
      "v_and_b32 v200, v160, v234\n"
      "v_mfma_f32_16x16x32_bf16 v[132:135], a[200:203], v[104:107], v[132:135]\n"
      "v_mul_f32 v131, v227, v131\n"
      "v_cvt_pk_bf16_f32 v181, v130, v131\n"
      "v_mfma_f32_16x16x32_bf16 v[208:211], v[184:187], v[200:203], 0\n"
      "s_waitcnt vmcnt(6) lgkmcnt(4)\n"
      "buffer_store_dword v13, v3, s[8:11], s52 offen sc1\n"
      "v_mfma_f32_16x16x32_bf16 v[204:207], v[180:183], v[200:203], 0\n"
      "ds_read_b128 v[84:87], v0 offset:50176\n"
      "ds_read_b128 v[72:75], v0 offset:47104\n"
      "v_mfma_f32_16x16x32_bf16 v[132:135], a[204:207], v[108:111], v[132:135]\n"
      "ds_read_b128 v[68:71], v0 offset:46080\n"
      "v_mov_b32 v178, v175\n"
      "v_mfma_f32_16x16x32_bf16 v[132:135], a[208:211], v[112:115], v[132:135]\n"
      "v_rcp_f32 v210, v210\n"
      "v_mfma_f32_16x16x32_bf16 v[132:135], a[212:215], v[116:119], v[132:135]\n"
      "s_waitcnt lgkmcnt(4)\n"
      "ds_read_b128 v[92:95], v0 offset:52224\n"
      "v_mul_f32 v205, v205, v210\n"
      "v_mfma_f32_16x16x32_bf16 v[120:123], a[0:3], v[48:51], 0\n"
      "s_add_u32 s36, s36, 24576\n"
      "ds_read_b128 v[80:83], v0 offset:49152\n"
      "v_mov_b32 v182, v180\n"
      "v_mfma_f32_16x16x32_bf16 v[120:123], a[4:7], v[52:55], v[120:123]\n"
      "ds_read_b128 v[96:99], v0 offset:53248\n"
      "v_mfma_f32_16x16x32_bf16 v[120:123], a[8:11], v[56:59], v[120:123]\n"
      "ds_read_b128 v[88:91], v0 offset:51200\n"
      "v_fma_f32 v132, -v218, v148, v132\n"
      "v_mfma_f32_16x16x32_bf16 v[120:123], a[12:15], v[60:63], v[120:123]\n"
      "s_cmp_eq_u32 s36, s37\n"
      "v_fma_f32 v133, -v218, v149, v133\n"
      "v_cvt_pk_bf16_f32 v176, v132, v133\n"
      "v_mfma_f32_16x16x32_bf16 v[120:123], a[16:19], v[64:67], v[120:123]\n"
      "s_waitcnt lgkmcnt(4)\n"
      "ds_read2st64_b64 v[212:215], v4 offset0:80 offset1:81\n"
      "s_cselect_b32 s47, s38, 0\n"
      "v_mfma_f32_16x16x32_bf16 v[120:123], a[20:23], v[68:71], v[120:123]\n"
      "s_cselect_b32 s37, s48, s37\n"
      "ds_read_b128 v[112:115], v0 offset:57344\n"
      "v_fma_f32 v135, -v218, v151, v135\n"
      "v_mfma_f32_16x16x32_bf16 v[120:123], a[24:27], v[72:75], v[120:123]\n"
      "s_cselect_b32 s49, s39, s53\n"
      "v_fma_f32 v134, -v218, v150, v134\n"
      "v_mov_b32 v183, v181\n"
      "v_mfma_f32_16x16x32_bf16 v[120:123], a[28:31], v[76:79], v[120:123]\n"
      "s_waitcnt lgkmcnt(4)\n"
      "ds_read_b128 v[104:107], v0 offset:55296\n"
      "v_cvt_pk_bf16_f32 v177, v134, v135\n"
      "v_mfma_f32_16x16x32_bf16 v[120:123], a[32:35], v[80:83], v[120:123]\n"
      "ds_read_b128 v[108:111], v0 offset:56320\n"
      "v_mul_f32 v206, v206, v210\n"
      "v_mfma_f32_16x16x32_bf16 v[188:191], v[172:175], v[176:179], v[152:155]\n"
      "ds_read_b64 v[216:217], v4 offset:41984\n"
      "v_mul_f32 v207, v207, v210\n"
      "v_mfma_f32_16x16x32_bf16 v[120:123], a[36:39], v[84:87], v[120:123]\n"
      "s_waitcnt lgkmcnt(5)\n"
      "s_add_u32 m0, s41, 20480\n"
      "ds_read_b128 v[116:119], v0 offset:58368\n"
      "v_mfma_f32_16x16x32_bf16 v[120:123], a[40:43], v[88:91], v[120:123]\n"
      "s_mov_b32 s46, s45\n"
      "s_add_u32 s45, s36, s47\n"
      "v_mul_f32 v209, v209, v210\n"
      "v_mfma_f32_16x16x32_bf16 v[120:123], a[44:47], v[92:95], v[120:123]\n"
      "v_cvt_pk_bf16_f32 v229, v206, v207\n"
      "v_mfma_f32_16x16x32_bf16 v[120:123], a[48:51], v[96:99], v[120:123]\n"
      "s_waitcnt lgkmcnt(4)\n"
      "v_add_f32 v213, v213, v215\n"
      "v_mfma_f32_16x16x32_bf16 v[120:123], a[52:55], v[100:103], v[120:123]\n"
      "s_waitcnt lgkmcnt(3)\n"
      "v_mul_f32 v232, 0x3eae1a93, v219\n"
      "v_mul_f32 v204, v204, v210\n"
      "v_mfma_f32_16x16x32_bf16 v[120:123], a[56:59], v[104:107], v[120:123]\n"
      "s_waitcnt lgkmcnt(2)\n"
      "v_mul_f32 v208, v208, v210\n"
      "v_cvt_pk_bf16_f32 v230, v208, v209\n"
      "v_mfma_f32_16x16x32_bf16 v[120:123], a[60:63], v[108:111], v[120:123]\n"
      "v_mfma_f32_16x16x32_bf16 v[120:123], a[64:67], v[112:115], v[120:123]\n"
      "s_waitcnt lgkmcnt(0)\n"
      "buffer_store_dwordx2 v[230:231], v8, s[8:11], s40 offen sc1\n"
      "v_mfma_f32_16x16x32_bf16 v[120:123], a[68:71], v[116:119], v[120:123]\n"
      "v_cvt_pk_bf16_f32 v228, v204, v205\n"
      "v_mfma_f32_16x16x32_bf16 v[124:127], a[216:219], v[48:51], 0\n"
      "v_add_f32 v212, v212, v214\n"
      "v_add_f32 v213, v213, v217\n"
      "v_mfma_f32_16x16x32_bf16 v[124:127], a[220:223], v[52:55], v[124:127]\n"
      "v_add_f32 v218, v212, v216\n"
      "v_fma_f32 v213, -v218, v218, v213\n"
      "v_mfma_f32_16x16x32_bf16 v[124:127], a[224:227], v[56:59], v[124:127]\n"
      "buffer_store_dwordx2 v[228:229], v7, s[8:11], s40 offen sc1\n"
      "v_mfma_f32_16x16x32_bf16 v[124:127], a[228:231], v[60:63], v[124:127]\n"
      "s_add_u32 s52, s52, 24576\n"
      "v_fma_f32 v123, -v218, v139, v123\n"
      "v_add_u32 v9, 1, v9\n"
      "v_mfma_f32_16x16x32_bf16 v[124:127], a[232:235], v[64:67], v[124:127]\n"
      "v_fma_f32 v122, -v218, v138, v122\n"
      "v_mfma_f32_16x16x32_bf16 v[124:127], a[236:239], v[68:71], v[124:127]\n"
      "s_waitcnt vmcnt(4)\n"
      "v_max_f32 v213, 0x3727c5ac, v213\n"
      "v_cmp_ne_u32 vcc, s21, v15\n"
      "v_mfma_f32_16x16x32_bf16 v[124:127], a[240:243], v[72:75], v[124:127]\n"
      "v_rsq_f32 v219, v213\n"
      "v_mfma_f32_16x16x32_bf16 v[124:127], a[244:247], v[76:79], v[124:127]\n"
      "v_mov_b32 v186, v184\n"
      "v_fma_f32 v120, -v218, v136, v120\n"
      "v_mfma_f32_16x16x32_bf16 v[124:127], a[248:251], v[80:83], v[124:127]\n"
      "s_min_u32 s45, s45, s6\n"
      "ds_write_b64 v5, v[218:219] offset:1024\n"
      "v_mfma_f32_16x16x32_bf16 v[124:127], a[252:255], v[84:87], v[124:127]\n"
      "s_cbranch_vccnz .Lsp_q1_8\n"
      ".Lbk_q1_8:\n"
      "v_mfma_f32_16x16x32_bf16 v[124:127], v[16:19], v[88:91], v[124:127]\n"
      "buffer_load_dwordx4 v0, s[8:11], s46 offen offset:1024 sc1 lds\n"
      "v_mfma_f32_16x16x32_bf16 v[124:127], v[20:23], v[92:95], v[124:127]\n"
      "v_cndmask_b32 v233, v219, 1.0, s[54:55]\n"
      "v_mul_f32 v123, v219, v123\n"
      "v_mfma_f32_16x16x32_bf16 v[124:127], v[24:27], v[96:99], v[124:127]\n"
      "ds_read_b128 v[220:223], v6 offset:1024\n"
      "v_mul_f32 v120, v219, v120\n"
      "v_mfma_f32_16x16x32_bf16 v[124:127], v[28:31], v[100:103], v[124:127]\n"
      "v_mul_f32 v122, v219, v122\n"
      "v_fma_f32 v121, -v218, v137, v121\n"
      "v_mfma_f32_16x16x32_bf16 v[124:127], v[32:35], v[104:107], v[124:127]\n"
      "s_waitcnt vmcnt(4)\n"
      "ds_write_b32 v11, v9\n"
      "v_mfma_f32_16x16x32_bf16 v[124:127], v[36:39], v[108:111], v[124:127]\n"
      "buffer_load_dwordx4 v0, s[8:11], s46 offen offset:0 sc1 lds\n"
      "v_mfma_f32_16x16x32_bf16 v[124:127], v[40:43], v[112:115], v[124:127]\n"
      "v_mfma_f32_16x16x32_bf16 v[124:127], v[44:47], v[116:119], v[124:127]\n"
      "v_cvt_pk_bf16_f32 v173, v122, v123\n"
      "v_mfma_f32_16x16x32_bf16 v[128:131], v[48:51], a[72:75], 0\n"
      "v_mul_f32 v121, v219, v121\n"
      "v_cvt_pk_bf16_f32 v172, v120, v121\n"
      "v_mfma_f32_16x16x32_bf16 v[128:131], v[52:55], a[76:79], v[128:131]\n"
      "buffer_load_dwordx4 v0, s[8:11], s46 offen offset:3072 sc1 lds\n"
      "v_mfma_f32_16x16x32_bf16 v[128:131], v[56:59], a[80:83], v[128:131]\n"
      "v_fma_f32 v125, -v218, v141, v125\n"
      "v_fma_f32 v124, -v218, v140, v124\n"
      "v_mfma_f32_16x16x32_bf16 v[128:131], v[60:63], a[84:87], v[128:131]\n"
      "v_fma_f32 v127, -v218, v143, v127\n"
      "v_mul_f32 v125, v233, v125\n"
      "v_mfma_f32_16x16x32_bf16 v[128:131], v[64:67], a[88:91], v[128:131]\n"
      "v_mul_f32 v124, v233, v124\n"
      "v_mul_f32 v127, v219, v127\n"
      "v_mfma_f32_16x16x32_bf16 v[128:131], v[68:71], a[92:95], v[128:131]\n"
      "v_fma_f32 v126, -v218, v142, v126\n"
      "v_mul_f32 v126, v219, v126\n"
      "v_mfma_f32_16x16x32_bf16 v[128:131], v[72:75], a[96:99], v[128:131]\n"
      "v_cvt_pk_bf16_f32 v175, v124, v125\n"
      "v_cvt_pk_bf16_f32 v174, v126, v127\n"
      "v_mfma_f32_16x16x32_bf16 v[128:131], v[76:79], a[100:103], v[128:131]\n"
      "buffer_load_dword v15, v2, s[8:11], s45 offen sc1\n"
      "v_mfma_f32_16x16x32_bf16 v[192:195], v[172:175], v[176:179], v[156:159]\n"
      "ds_read_b128 v[224:227], v6 offset:1040\n"
      "v_mov_b32 v187, v185\n"
      "v_mfma_f32_16x16x32_bf16 v[196:199], v[172:175], v[164:167], v[168:171]\n"
      "ds_read_b32 v10, v12\n"
      "v_mfma_f32_16x16x32_bf16 v[128:131], v[80:83], a[104:107], v[128:131]\n"
      "buffer_load_dwordx4 v0, s[8:11], s46 offen offset:2048 sc1 lds\n"
      "v_mfma_f32_16x16x32_bf16 v[128:131], v[84:87], a[108:111], v[128:131]\n"
      "v_med3_f32 v234, v192, v193, s50\n"
      "v_med3_f32 v235, v194, v195, s50\n"
      "v_mfma_f32_16x16x32_bf16 v[128:131], v[88:91], a[112:115], v[128:131]\n"
      "v_cvt_pk_bf16_f32 v185, v198, v199\n"
      "v_max3_f32 v234, v188, v189, v234\n"
      "v_mfma_f32_16x16x32_bf16 v[128:131], v[92:95], a[116:119], v[128:131]\n"
      "v_max3_f32 v235, v190, v191, v235\n"
      "v_max_f32 v236, v234, v235\n"
      "v_mfma_f32_16x16x32_bf16 v[128:131], v[96:99], a[120:123], v[128:131]\n"
      "s_waitcnt lgkmcnt(0)\n"
      "v_mov_b32 v237, v236\n"
      "v_cmp_gt_i32 vcc, v9, v10\n"
      "v_mfma_f32_16x16x32_bf16 v[128:131], v[100:103], a[124:127], v[128:131]\n"
      "s_add_u32 m0, s42, 20480\n"
      "v_permlane16_swap_b32 v236, v237\n"
      "v_mfma_f32_16x16x32_bf16 v[128:131], v[104:107], a[128:131], v[128:131]\n"
      "v_max_f32 v236, v236, v237\n"
      "v_mov_b32 v237, v236\n"
      "v_mfma_f32_16x16x32_bf16 v[128:131], v[108:111], a[132:135], v[128:131]\n"
      "buffer_load_dwordx4 v1, s[8:11], s46 offen offset:0 sc1 lds\n"
      "v_mfma_f32_16x16x32_bf16 v[128:131], v[112:115], a[136:139], v[128:131]\n"
      ".Lq_p1:\n"
      "s_cbranch_execz .Lq_pr\n"
      "v_permlane32_swap_b32 v236, v237\n"
      "v_mfma_f32_16x16x32_bf16 v[128:131], v[116:119], a[140:143], v[128:131]\n"
      "v_max_f32 v236, v236, v237\n"
      "v_mul_f32_e64 v236, v236, v232\n"
      "v_mfma_f32_16x16x32_bf16 v[132:135], a[144:147], v[48:51], 0\n"
      "s_cbranch_vccnz .Lsp_qy0_9\n"
      ".Lbk_qy0_9:\n"
      "v_fma_f32 v234, v234, v232, -v236\n"
      "v_mfma_f32_16x16x32_bf16 v[132:135], a[148:151], v[52:55], v[132:135]\n"
      "ds_read_b128 v[52:55], v0 offset:1024\n"
      "ds_read_b128 v[48:51], v0 offset:0\n"
      "v_mfma_f32_16x16x32_bf16 v[132:135], a[152:155], v[56:59], v[132:135]\n"
      "v_fma_f32 v131, -v226, v144, v131\n"
      "v_fma_f32 v235, v235, v232, -v236\n"
      "v_mfma_f32_16x16x32_bf16 v[132:135], a[156:159], v[60:63], v[132:135]\n"
      "ds_read_b128 v[56:59], v0 offset:2048\n"
      "v_fma_f32 v128, -v220, v145, v128\n"
      "v_mfma_f32_16x16x32_bf16 v[132:135], a[160:163], v[64:67], v[132:135]\n"
      "v_exp_f32 v234, v234\n"
      "v_mfma_f32_16x16x32_bf16 v[132:135], a[164:167], v[68:71], v[132:135]\n"
      "v_exp_f32 v235, v235\n"
      "v_mfma_f32_16x16x32_bf16 v[132:135], a[168:171], v[72:75], v[132:135]\n"
      "v_cvt_pk_bf16_f32 v234, v234, v234\n"
      "v_fma_f32 v129, -v222, v146, v129\n"
      "v_mfma_f32_16x16x32_bf16 v[132:135], a[172:175], v[76:79], v[132:135]\n"
      "v_mul_f32 v129, v223, v129\n"
      "v_fma_f32 v130, -v224, v147, v130\n"
      "v_mfma_f32_16x16x32_bf16 v[132:135], a[176:179], v[80:83], v[132:135]\n"
      "v_cvt_pk_bf16_f32 v235, v235, v235\n"
      "v_and_b32 v200, v160, v234\n"
      "v_mfma_f32_16x16x32_bf16 v[132:135], a[180:183], v[84:87], v[132:135]\n"
      "v_cvt_pk_bf16_f32 v184, v196, v197\n"
      "v_and_b32 v201, v161, v235\n"
      "v_mfma_f32_16x16x32_bf16 v[132:135], a[184:187], v[88:91], v[132:135]\n"
      "ds_read_b128 v[68:71], v0 offset:5120\n"
      "v_mul_f32 v130, v225, v130\n"
      "v_mfma_f32_16x16x32_bf16 v[132:135], a[188:191], v[92:95], v[132:135]\n"
      "v_and_b32 v203, v163, v235\n"
      "v_mul_f32_e64 v131, v227, v131\n"
      "v_mfma_f32_16x16x32_bf16 v[132:135], a[192:195], v[96:99], v[132:135]\n"
      "ds_read_b128 v[64:67], v0 offset:4096\n"
      "v_mul_f32_e64 v128, v221, v128\n"
      "v_mfma_f32_16x16x32_bf16 v[132:135], a[196:199], v[100:103], v[132:135]\n"
      "v_and_b32_e64 v202, v162, v234\n"
      "v_cvt_pk_bf16_f32 v181, v130, v131\n"
      "v_mfma_f32_16x16x32_bf16 v[132:135], a[200:203], v[104:107], v[132:135]\n"
      "s_add_u32 s40, s40, 24576\n"
      "ds_read_b128 v[60:63], v0 offset:3072\n"
      "v_cvt_pk_bf16_f32 v180, v128, v129\n"
      "v_mfma_f32_16x16x32_bf16 v[208:211], v[184:187], v[200:203], 0\n"
      "s_waitcnt vmcnt(6) lgkmcnt(3)\n"
      "buffer_store_dword v13, v3, s[8:11], s52 offen sc1\n"
      "ds_read2st64_b64 v[212:215], v4 offset0:0 offset1:1\n"
      "v_mfma_f32_16x16x32_bf16 v[204:207], v[180:183], v[200:203], 0\n"
      "ds_read_b128 v[72:75], v0 offset:6144\n"
      "v_mfma_f32_16x16x32_bf16 v[132:135], a[204:207], v[108:111], v[132:135]\n"
      "ds_read_b128 v[76:79], v0 offset:7168\n"
      "v_mov_b32_e64 v178, v175\n"
      "v_mfma_f32_16x16x32_bf16 v[132:135], a[208:211], v[112:115], v[132:135]\n"
      "ds_read_b128 v[88:91], v0 offset:10240\n"
      "ds_read_b128 v[108:111], v0 offset:15360\n"
      "v_mfma_f32_16x16x32_bf16 v[132:135], a[212:215], v[116:119], v[132:135]\n"
      "s_sub_u32 s20, s20, 1\n"
      "s_cbranch_scc0 .Lq_loop\n"
      ".Lq_tail:\n"
      "v_mul_f32 v232, 0x3eae1a93, v219\n"
      "s_nop 7\n"
      "v_fma_f32 v135, -v218, v151, v135\n"
      "v_fma_f32 v133, -v218, v149, v133\n"
      "v_fma_f32 v134, -v218, v150, v134\n"
      "v_fma_f32 v132, -v218, v148, v132\n"
      "v_cvt_pk_bf16_f32 v176, v132, v133\n"
      "v_cvt_pk_bf16_f32 v177, v134, v135\n"
      "v_rcp_f32 v210, v210\n"
      "s_nop 0\n"
      "v_mul_f32 v204, v204, v210\n"
      "v_mfma_f32_16x16x32_bf16 v[188:191], v[172:175], v[176:179], v[152:155]\n"
      "v_mul_f32 v208, v208, v210\n"
      "v_mul_f32 v207, v207, v210\n"
      "v_mul_f32 v209, v209, v210\n"
      "v_cvt_pk_bf16_f32 v230, v208, v209\n"
      "buffer_store_dwordx2 v[230:231], v8, s[8:11], s40 offen sc1\n"
      "v_mul_f32 v206, v206, v210\n"
      "v_cvt_pk_bf16_f32 v229, v206, v207\n"
      "v_mul_f32 v205, v205, v210\n"
      "v_cvt_pk_bf16_f32 v228, v204, v205\n"
      "buffer_store_dwordx2 v[228:229], v7, s[8:11], s40 offen sc1\n"
      "v_mov_b32 v187, v185\n"
      "v_mov_b32 v186, v184\n"
      "v_mov_b32 v182, v180\n"
      "s_add_u32 s52, s52, 24576\n"
      "v_mov_b32 v183, v181\n"
      "s_add_u32 s40, s40, 24576\n"
      "s_waitcnt vmcnt(0)\n"
      "buffer_store_dword v13, v3, s[8:11], s52 offen sc1\n"
      "s_add_u32 s52, s52, 24576\n"
      "s_cmp_lg_u32 s44, 0\n"
      "s_cbranch_scc1 .Lq_done\n"
      "v_lshlrev_b32 v48, 3, v0\n"
      "s_mov_b32 s22, s23\n"
      ".Lq_up:\n"
      "s_mov_b32 s30, 128\n"
      "buffer_load_dword v49, v48, s[8:11], s30 offen sc1\n"
      "s_mov_b32 s30, 8320\n"
      "buffer_load_dword v50, v48, s[8:11], s30 offen sc1\n"
      "s_mov_b32 s30, 16512\n"
      "buffer_load_dword v51, v48, s[8:11], s30 offen sc1\n"
      "s_mov_b32 s30, 24704\n"
      "buffer_load_dword v52, v48, s[8:11], s30 offen sc1\n"
      "s_waitcnt vmcnt(0)\n"
      "v_xor_b32 v49, s21, v49\n"
      "v_xor_b32 v50, s21, v50\n"
      "v_xor_b32 v51, s21, v51\n"
      "v_xor_b32 v52, s21, v52\n"
      "v_or3_b32 v49, v49, v50, v51\n"
      "v_or_b32 v49, v49, v52\n"
      "v_cmp_ne_u32 vcc, 0, v49\n"
      "s_cbranch_vccz .Lq_ok\n"
      "s_sleep 4\n"
      "s_sub_u32 s22, s22, 1\n"
      "s_cbranch_scc0 .Lq_up\n"
      ".Lq_ok:\n"
      "v_lshrrev_b32 v48, 2, v0\n"
      "v_and_b32 v48, 0x7c, v48\n"
      "v_mov_b32 v49, s21\n"
      "buffer_store_dword v49, v48, s[8:11], 0 offen sc1\n"
      ".Lq_done:\n"
      "s_endpgm\n"
      ".Lsp_q0_1:\n"
      "s_mov_b32 s22, s23\n"
      ".Lsl_q0_1:\n"
      "buffer_load_dword v15, v2, s[8:11], s46 offen sc1\n"
      "s_waitcnt vmcnt(0)\n"
      "v_cmp_ne_u32 vcc, s21, v15\n"
      "s_cbranch_vccz .Lbk_q0_1\n"
      "s_sleep 4\n"
      "s_sub_u32 s22, s22, 1\n"
      "s_cbranch_scc0 .Lsl_q0_1\n"
      "s_mov_b32 s23, 0\n"
      "s_branch .Lbk_q0_1\n"
      ".Lsp_q1_2:\n"
      "s_mov_b32 s22, s23\n"
      ".Lsl_q1_2:\n"
      "buffer_load_dword v15, v2, s[8:11], s46 offen sc1\n"
      "s_waitcnt vmcnt(0)\n"
      "v_cmp_ne_u32 vcc, s21, v15\n"
      "s_cbranch_vccz .Lbk_q1_2\n"
      "s_sleep 4\n"
      "s_sub_u32 s22, s22, 1\n"
      "s_cbranch_scc0 .Lsl_q1_2\n"
      "s_mov_b32 s23, 0\n"
      "s_branch .Lbk_q1_2\n"
      ".Lsp_qy0_3:\n"
      "s_lshl_b32 s22, s23, 4\n"
      ".Lsl_qy0_3:\n"
      "ds_read_b32 v10, v12\n"
      "s_waitcnt lgkmcnt(0)\n"
      "v_cmp_gt_i32 vcc, v9, v10\n"
      "s_cbranch_vccz .Lbk_qy0_3\n"
      "s_sub_u32 s22, s22, 1\n"
      "s_cbranch_scc0 .Lsl_qy0_3\n"
      "s_mov_b32 s23, 0\n"
      "s_branch .Lbk_qy0_3\n"
      ".Lsp_q2_4:\n"
      "s_mov_b32 s22, s23\n"
      "buffer_load_dword v15, v2, s[8:11], s46 offen sc1\n"
      "s_waitcnt vmcnt(0)\n"
      "v_cmp_ne_u32 vcc, s21, v15\n"
      "s_add_u32 s30, s52, 24576\n"
      "buffer_store_dword v13, v3, s[8:11], s30 offen sc1\n"
      "s_cbranch_vccz .Lbk_q2_4\n"
      ".Lsl_q2_4:\n"
      "buffer_load_dword v15, v2, s[8:11], s46 offen sc1\n"
      "s_waitcnt vmcnt(0)\n"
      "v_cmp_ne_u32 vcc, s21, v15\n"
      "s_cbranch_vccz .Lbk_q2_4\n"
      "s_sleep 4\n"
      "s_sub_u32 s22, s22, 1\n"
      "s_cbranch_scc0 .Lsl_q2_4\n"
      "s_mov_b32 s23, 0\n"
      "s_branch .Lbk_q2_4\n"
      ".Lsp_qy1_5:\n"
      "s_lshl_b32 s22, s23, 4\n"
      ".Lsl_qy1_5:\n"
      "ds_read_b32 v10, v12\n"
      "s_waitcnt lgkmcnt(0)\n"
      "v_cmp_gt_i32 vcc, v9, v10\n"
      "s_cbranch_vccz .Lbk_qy1_5\n"
      "s_sub_u32 s22, s22, 1\n"
      "s_cbranch_scc0 .Lsl_qy1_5\n"
      "s_mov_b32 s23, 0\n"
      "s_branch .Lbk_qy1_5\n"
      ".Lsp_q0_6:\n"
      "s_mov_b32 s22, s23\n"
      "buffer_load_dword v15, v2, s[8:11], s46 offen sc1\n"
      "s_waitcnt vmcnt(0)\n"
      "v_cmp_ne_u32 vcc, s21, v15\n"
      "s_add_u32 s30, s52, 24576\n"
      "buffer_store_dword v13, v3, s[8:11], s30 offen sc1\n"
      "s_cbranch_vccz .Lbk_q0_6\n"
      ".Lsl_q0_6:\n"
      "buffer_load_dword v15, v2, s[8:11], s46 offen sc1\n"
      "s_waitcnt vmcnt(0)\n"
      "v_cmp_ne_u32 vcc, s21, v15\n"
      "s_cbranch_vccz .Lbk_q0_6\n"
      "s_sleep 4\n"
      "s_sub_u32 s22, s22, 1\n"
      "s_cbranch_scc0 .Lsl_q0_6\n"
      "s_mov_b32 s23, 0\n"
      "s_branch .Lbk_q0_6\n"
      ".Lsp_qy2_7:\n"
      "s_lshl_b32 s22, s23, 4\n"
      ".Lsl_qy2_7:\n"
      "ds_read_b32 v10, v12\n"
      "s_waitcnt lgkmcnt(0)\n"
      "v_cmp_gt_i32 vcc, v9, v10\n"
      "s_cbranch_vccz .Lbk_qy2_7\n"
      "s_sub_u32 s22, s22, 1\n"
      "s_cbranch_scc0 .Lsl_qy2_7\n"
      "s_mov_b32 s23, 0\n"
      "s_branch .Lbk_qy2_7\n"
      ".Lsp_q1_8:\n"
      "s_mov_b32 s22, s23\n"
      "buffer_load_dword v15, v2, s[8:11], s46 offen sc1\n"
      "s_waitcnt vmcnt(0)\n"
      "v_cmp_ne_u32 vcc, s21, v15\n"
      "s_add_u32 s30, s52, 0\n"
      "buffer_store_dword v13, v3, s[8:11], s30 offen sc1\n"
      "s_cbranch_vccz .Lbk_q1_8\n"
      ".Lsl_q1_8:\n"
      "buffer_load_dword v15, v2, s[8:11], s46 offen sc1\n"
      "s_waitcnt vmcnt(0)\n"
      "v_cmp_ne_u32 vcc, s21, v15\n"
      "s_cbranch_vccz .Lbk_q1_8\n"
      "s_sleep 4\n"
      "s_sub_u32 s22, s22, 1\n"
      "s_cbranch_scc0 .Lsl_q1_8\n"
      "s_mov_b32 s23, 0\n"
      "s_branch .Lbk_q1_8\n"
      ".Lsp_qy0_9:\n"
      "s_lshl_b32 s22, s23, 4\n"
      ".Lsl_qy0_9:\n"
      "ds_read_b32 v10, v12\n"
      "s_waitcnt lgkmcnt(0)\n"
      "v_cmp_gt_i32 vcc, v9, v10\n"
      "s_cbranch_vccz .Lbk_qy0_9\n"
      "s_sub_u32 s22, s22, 1\n"
      "s_cbranch_scc0 .Lsl_qy0_9\n"
      "s_mov_b32 s23, 0\n"
      "s_branch .Lbk_qy0_9\n"
      ".Lq_last:\n"
      :
      : "{s[8:11]}"(rsWS), "{s[12:15]}"(rsA), "{v0}"(v_l16), "{v1}"(v_l5), "{v2}"(v_fa), "{v3}"(v_ra), "{v4}"(v_st), "{v5}"(v_tw), "{v6}"(v_tr), "{v7}"(v_om), "{v8}"(v_oe), "{v10}"(v_al), "{v11}"(v_yw), "{v12}"(v_yr), "{v14}"(v_hi), "{v152}"(v_bc[0]), "{v153}"(v_bc[1]), "{v154}"(v_bc[2]), "{v155}"(v_bc[3]), "{v156}"(v_bp[0]), "{v157}"(v_bp[1]), "{v158}"(v_bp[2]), "{v159}"(v_bp[3]), "{v160}"(v_mk[0]), "{v161}"(v_mk[1]), "{v162}"(v_mk[2]), "{v163}"(v_mk[3]), "{v164}"(v_sel[0]), "{v165}"(v_sel[1]), "{v166}"(v_sel[2]), "{v167}"(v_sel[3]), "{v168}"(v_ci[0]), "{v169}"(v_ci[1]), "{v170}"(v_ci[2]), "{v171}"(v_ci[3]), "{v48}"(v_vo[0]), "{v49}"(v_vo[1]), "{v50}"(v_vo[2]), "{v51}"(v_vo[3]), "{v52}"(v_at), "{v53}"(v_ax), "{v54}"(v_pm), "{v55}"(v_ms), "{v76}"(v_pw), "{s36}"(s_sx), "{s37}"(s_ev), "{s38}"(s_del), "{s39}"(s_kil), "{s40}"(s_so), "{s41}"(s_m4), "{s42}"(s_m5), "{s43}"(s_ms), "{s44}"(s_up)
      : "v255", "a255", "s99", "vcc", "scc", "memory");

  } else if (fast && slot < 10) {
    const unsigned gr = e, part = slot - 8;
    COMMON(32, 81920);
    const unsigned c = part, o = w & 1;
    const u32x4 rsA = srd(Wo, 576 * 576 * 2);
    const unsigned v_l5 = v_l16 + 16384 - w * 3072, e0 = 16384 + o * 2048 + g * 512 + i16 * 8, e1 = e0 + 2048 - o * 4096;
    const unsigned v_ex[8] = {e0, e1, e0 + 20480, e1 + 20480, e0 + 40960, e1 + 40960, e0 + 61440, e1 + 61440}, v_l2 = v_l16 + 40960;
    const unsigned v_fa = 20480 - w * 4096 + (8 * w + (lane >> 3)) * 128 + (lane & 7) * 4, v_ra = up * 8 * REC + l5 + 19456 + (4 * c + w) * 128;
    const unsigned v_xp = (9 * c + 2 * w) * 1024 + v_l16, v_xs = (9 * c + 8) * 1024 + (w >> 1) * 512 + half;
    const unsigned v_rp = v_xp + WS_XIN, v_rs = v_xs + WS_XIN;
    const unsigned v_ew = 82944 + (w >> 1) * 1024 + lane * 4;
    const unsigned v_sw = 86016 + i16 * 144 + (4 * w + g) * 8, v_sr = 86016 + (8 * (w >> 1) + l8) * 144 + l7;
    const unsigned v_ss = 18432 + c * 512 + (w >> 1) * 256 + l8 * 32 + p * 8;
    const unsigned v_pw = 19456 + (4 * c + w) * 128 + l8 * REC + l7;
    unsigned v_vo[9], v_vp[9];
#pragma unroll
    for (unsigned j = 0; j < 9; ++j) {
      const unsigned ch = 64 * j + lane, r = ch / 36, x = 576 * c + ch % 36 * 16 - 1024 * (j & 3);
      v_vo[j] = (20 * o + r) * 1152 + x;
      v_vp[j] = (36 * (4 * o + (r >> 2)) + 16 + (r & 3)) * 1152 + x;
    }
    const unsigned ro = (8 * g + q) * 576;
    const unsigned v_t0 = ro + o * 18432 + 128 * w + 16 * p, v_t1 = v_t0 + 18432 - 2 * o * 18432, v_ts = ro + o * 18432 + 512 + 32 * (w >> 1) + 8 * p;
    const unsigned s_sx = WS_OB + (gr * NREC + KSK) * REC + w * 4096, s_ev = s_sx + (31 - KSK) * REC, s_sy = WS_X1 + (gr * NREC + KSK) * REC;
    const unsigned s_m4 = w * 4096, s_m5 = 16384 + w * 1024, s_m9 = w * 9216, s_so = (w >> 1) * 8 * 41472;
    const unsigned s_s8 = s_so, s_wg = o, s_rb = WS_OB + w * 4096;
    asm volatile(
      "s_buffer_load_dword s21, s[8:11], 0\n"
      "s_mov_b32 s23, 65536\n"
      "s_mov_b32 s31, 0x40000000\n"
      "s_add_u32 m0, s41, 0\n"
      "s_nop 0\n"
      "buffer_load_dwordx4 v100, s[12:15], s42 offen offset:0 lds\n"
      "buffer_load_dwordx4 v101, s[12:15], s42 offen offset:1024 lds\n"
      "buffer_load_dwordx4 v102, s[12:15], s42 offen offset:2048 lds\n"
      "buffer_load_dwordx4 v103, s[12:15], s42 offen offset:3072 lds\n"
      "s_add_u32 m0, s41, 4096\n"
      "s_nop 0\n"
      "buffer_load_dwordx4 v104, s[12:15], s42 offen offset:0 lds\n"
      "buffer_load_dwordx4 v105, s[12:15], s42 offen offset:1024 lds\n"
      "buffer_load_dwordx4 v106, s[12:15], s42 offen offset:2048 lds\n"
      "buffer_load_dwordx4 v107, s[12:15], s42 offen offset:3072 lds\n"
      "s_add_u32 m0, s41, 8192\n"
      "s_nop 0\n"
      "buffer_load_dwordx4 v108, s[12:15], s42 offen offset:0 lds\n"
      "s_add_u32 s42, s42, 41472\n"
      "s_add_u32 m0, s41, 36864\n"
      "s_nop 0\n"
      "buffer_load_dwordx4 v100, s[12:15], s42 offen offset:0 lds\n"
      "buffer_load_dwordx4 v101, s[12:15], s42 offen offset:1024 lds\n"
      "buffer_load_dwordx4 v102, s[12:15], s42 offen offset:2048 lds\n"
      "buffer_load_dwordx4 v103, s[12:15], s42 offen offset:3072 lds\n"
      "s_add_u32 m0, s41, 40960\n"
      "s_nop 0\n"
      "buffer_load_dwordx4 v104, s[12:15], s42 offen offset:0 lds\n"
      "buffer_load_dwordx4 v105, s[12:15], s42 offen offset:1024 lds\n"
      "buffer_load_dwordx4 v106, s[12:15], s42 offen offset:2048 lds\n"
      "buffer_load_dwordx4 v107, s[12:15], s42 offen offset:3072 lds\n"
      "s_add_u32 m0, s41, 45056\n"
      "s_nop 0\n"
      "buffer_load_dwordx4 v108, s[12:15], s42 offen offset:0 lds\n"
      "s_add_u32 s42, s42, 41472\n"
      "s_add_u32 m0, s41, 73728\n"
      "s_nop 0\n"
      "buffer_load_dwordx4 v100, s[12:15], s42 offen offset:0 lds\n"
      "buffer_load_dwordx4 v101, s[12:15], s42 offen offset:1024 lds\n"
      "buffer_load_dwordx4 v102, s[12:15], s42 offen offset:2048 lds\n"
      "buffer_load_dwordx4 v103, s[12:15], s42 offen offset:3072 lds\n"
      "s_add_u32 m0, s41, 77824\n"
      "s_nop 0\n"
      "buffer_load_dwordx4 v104, s[12:15], s42 offen offset:0 lds\n"
      "buffer_load_dwordx4 v105, s[12:15], s42 offen offset:1024 lds\n"
      "buffer_load_dwordx4 v106, s[12:15], s42 offen offset:2048 lds\n"
      "buffer_load_dwordx4 v107, s[12:15], s42 offen offset:3072 lds\n"
      "s_add_u32 m0, s41, 81920\n"
      "s_nop 0\n"
      "buffer_load_dwordx4 v108, s[12:15], s42 offen offset:0 lds\n"
      "s_add_u32 s42, s42, 41472\n"
      "s_waitcnt vmcnt(18)\n"
      "s_barrier\n"
      "ds_read_b64_tr_b16 v[64:65], v120 offset:0\n"
      "ds_read_b64_tr_b16 v[66:67], v120 offset:2304\n"
      "ds_read_b64_tr_b16 a[0:1], v118 offset:0\n"
      "ds_read_b64_tr_b16 a[2:3], v118 offset:2304\n"
      "ds_read_b64_tr_b16 a[72:73], v118 offset:8\n"
      "ds_read_b64_tr_b16 a[74:75], v118 offset:2312\n"
      "ds_read_b64_tr_b16 a[144:145], v118 offset:64\n"
      "ds_read_b64_tr_b16 a[146:147], v118 offset:2368\n"
      "ds_read_b64_tr_b16 a[216:217], v118 offset:72\n"
      "ds_read_b64_tr_b16 a[218:219], v118 offset:2376\n"
      "ds_read_b64_tr_b16 a[36:37], v119 offset:0\n"
      "ds_read_b64_tr_b16 a[38:39], v119 offset:2304\n"
      "ds_read_b64_tr_b16 a[108:109], v119 offset:8\n"
      "ds_read_b64_tr_b16 a[110:111], v119 offset:2312\n"
      "ds_read_b64_tr_b16 a[180:181], v119 offset:64\n"
      "ds_read_b64_tr_b16 a[182:183], v119 offset:2368\n"
      "ds_read_b64_tr_b16 a[252:253], v119 offset:72\n"
      "ds_read_b64_tr_b16 a[254:255], v119 offset:2376\n"
      "v_add_u32 v118, 36864, v118\n"
      "v_add_u32 v119, 36864, v119\n"
      "v_add_u32 v120, 36864, v120\n"
      "s_add_u32 m0, s41, 110592\n"
      "s_nop 0\n"
      "buffer_load_dwordx4 v100, s[12:15], s42 offen offset:0 lds\n"
      "buffer_load_dwordx4 v101, s[12:15], s42 offen offset:1024 lds\n"
      "buffer_load_dwordx4 v102, s[12:15], s42 offen offset:2048 lds\n"
      "buffer_load_dwordx4 v103, s[12:15], s42 offen offset:3072 lds\n"
      "s_add_u32 m0, s41, 114688\n"
      "s_nop 0\n"
      "buffer_load_dwordx4 v104, s[12:15], s42 offen offset:0 lds\n"
      "buffer_load_dwordx4 v105, s[12:15], s42 offen offset:1024 lds\n"
      "buffer_load_dwordx4 v106, s[12:15], s42 offen offset:2048 lds\n"
      "buffer_load_dwordx4 v107, s[12:15], s42 offen offset:3072 lds\n"
      "s_add_u32 m0, s41, 118784\n"
      "s_nop 0\n"
      "buffer_load_dwordx4 v108, s[12:15], s42 offen offset:0 lds\n"
      "s_add_u32 s42, s42, 41472\n"
      "s_waitcnt vmcnt(18) lgkmcnt(0)\n"
      "s_barrier\n"
      "ds_read_b64_tr_b16 v[68:69], v120 offset:0\n"
      "ds_read_b64_tr_b16 v[70:71], v120 offset:2304\n"
      "ds_read_b64_tr_b16 a[4:5], v118 offset:0\n"
      "ds_read_b64_tr_b16 a[6:7], v118 offset:2304\n"
      "ds_read_b64_tr_b16 a[76:77], v118 offset:8\n"
      "ds_read_b64_tr_b16 a[78:79], v118 offset:2312\n"
      "ds_read_b64_tr_b16 a[148:149], v118 offset:64\n"
      "ds_read_b64_tr_b16 a[150:151], v118 offset:2368\n"
      "ds_read_b64_tr_b16 a[220:221], v118 offset:72\n"
      "ds_read_b64_tr_b16 a[222:223], v118 offset:2376\n"
      "ds_read_b64_tr_b16 a[40:41], v119 offset:0\n"
      "ds_read_b64_tr_b16 a[42:43], v119 offset:2304\n"
      "ds_read_b64_tr_b16 a[112:113], v119 offset:8\n"
      "ds_read_b64_tr_b16 a[114:115], v119 offset:2312\n"
      "ds_read_b64_tr_b16 a[184:185], v119 offset:64\n"
      "ds_read_b64_tr_b16 a[186:187], v119 offset:2368\n"
      "ds_read_b64_tr_b16 v[32:33], v119 offset:72\n"
      "ds_read_b64_tr_b16 v[34:35], v119 offset:2376\n"
      "v_add_u32 v118, 36864, v118\n"
      "v_add_u32 v119, 36864, v119\n"
      "v_add_u32 v120, 36864, v120\n"
      "s_add_u32 m0, s41, 0\n"
      "s_nop 0\n"
      "buffer_load_dwordx4 v100, s[12:15], s42 offen offset:0 lds\n"
      "buffer_load_dwordx4 v101, s[12:15], s42 offen offset:1024 lds\n"
      "buffer_load_dwordx4 v102, s[12:15], s42 offen offset:2048 lds\n"
      "buffer_load_dwordx4 v103, s[12:15], s42 offen offset:3072 lds\n"
      "s_add_u32 m0, s41, 4096\n"
      "s_nop 0\n"
      "buffer_load_dwordx4 v104, s[12:15], s42 offen offset:0 lds\n"
      "buffer_load_dwordx4 v105, s[12:15], s42 offen offset:1024 lds\n"
      "buffer_load_dwordx4 v106, s[12:15], s42 offen offset:2048 lds\n"
      "buffer_load_dwordx4 v107, s[12:15], s42 offen offset:3072 lds\n"
      "s_add_u32 m0, s41, 8192\n"
      "s_nop 0\n"
      "buffer_load_dwordx4 v108, s[12:15], s42 offen offset:0 lds\n"
      "s_add_u32 s42, s42, 41472\n"
      "s_waitcnt vmcnt(18) lgkmcnt(0)\n"
      "s_barrier\n"
      "ds_read_b64_tr_b16 v[72:73], v120 offset:0\n"
      "ds_read_b64_tr_b16 v[74:75], v120 offset:2304\n"
      "ds_read_b64_tr_b16 a[8:9], v118 offset:0\n"
      "ds_read_b64_tr_b16 a[10:11], v118 offset:2304\n"
      "ds_read_b64_tr_b16 a[80:81], v118 offset:8\n"
      "ds_read_b64_tr_b16 a[82:83], v118 offset:2312\n"
      "ds_read_b64_tr_b16 a[152:153], v118 offset:64\n"
      "ds_read_b64_tr_b16 a[154:155], v118 offset:2368\n"
      "ds_read_b64_tr_b16 a[224:225], v118 offset:72\n"
      "ds_read_b64_tr_b16 a[226:227], v118 offset:2376\n"
      "ds_read_b64_tr_b16 a[44:45], v119 offset:0\n"
      "ds_read_b64_tr_b16 a[46:47], v119 offset:2304\n"
      "ds_read_b64_tr_b16 a[116:117], v119 offset:8\n"
      "ds_read_b64_tr_b16 a[118:119], v119 offset:2312\n"
      "ds_read_b64_tr_b16 a[188:189], v119 offset:64\n"
      "ds_read_b64_tr_b16 a[190:191], v119 offset:2368\n"
      "ds_read_b64_tr_b16 v[36:37], v119 offset:72\n"
      "ds_read_b64_tr_b16 v[38:39], v119 offset:2376\n"
      "v_add_u32 v118, 36864, v118\n"
      "v_add_u32 v119, 36864, v119\n"
      "v_add_u32 v120, 36864, v120\n"
      "s_add_u32 m0, s41, 36864\n"
      "s_nop 0\n"
      "buffer_load_dwordx4 v100, s[12:15], s42 offen offset:0 lds\n"
      "buffer_load_dwordx4 v101, s[12:15], s42 offen offset:1024 lds\n"
      "buffer_load_dwordx4 v102, s[12:15], s42 offen offset:2048 lds\n"
      "buffer_load_dwordx4 v103, s[12:15], s42 offen offset:3072 lds\n"
      "s_add_u32 m0, s41, 40960\n"
      "s_nop 0\n"
      "buffer_load_dwordx4 v104, s[12:15], s42 offen offset:0 lds\n"
      "buffer_load_dwordx4 v105, s[12:15], s42 offen offset:1024 lds\n"
      "buffer_load_dwordx4 v106, s[12:15], s42 offen offset:2048 lds\n"
      "buffer_load_dwordx4 v107, s[12:15], s42 offen offset:3072 lds\n"
      "s_add_u32 m0, s41, 45056\n"
      "s_nop 0\n"
      "buffer_load_dwordx4 v108, s[12:15], s42 offen offset:0 lds\n"
      "s_add_u32 s42, s42, 41472\n"
      "s_waitcnt vmcnt(18) lgkmcnt(0)\n"
      "s_barrier\n"
      "ds_read_b64_tr_b16 v[76:77], v120 offset:0\n"
      "ds_read_b64_tr_b16 v[78:79], v120 offset:2304\n"
      "ds_read_b64_tr_b16 a[12:13], v118 offset:0\n"
      "ds_read_b64_tr_b16 a[14:15], v118 offset:2304\n"
      "ds_read_b64_tr_b16 a[84:85], v118 offset:8\n"
      "ds_read_b64_tr_b16 a[86:87], v118 offset:2312\n"
      "ds_read_b64_tr_b16 a[156:157], v118 offset:64\n"
      "ds_read_b64_tr_b16 a[158:159], v118 offset:2368\n"
      "ds_read_b64_tr_b16 a[228:229], v118 offset:72\n"
      "ds_read_b64_tr_b16 a[230:231], v118 offset:2376\n"
      "ds_read_b64_tr_b16 a[48:49], v119 offset:0\n"
      "ds_read_b64_tr_b16 a[50:51], v119 offset:2304\n"
      "ds_read_b64_tr_b16 a[120:121], v119 offset:8\n"
      "ds_read_b64_tr_b16 a[122:123], v119 offset:2312\n"
      "ds_read_b64_tr_b16 a[192:193], v119 offset:64\n"
      "ds_read_b64_tr_b16 a[194:195], v119 offset:2368\n"
      "ds_read_b64_tr_b16 v[40:41], v119 offset:72\n"
      "ds_read_b64_tr_b16 v[42:43], v119 offset:2376\n"
      "v_add_u32 v118, 4294856704, v118\n"
      "v_add_u32 v119, 4294856704, v119\n"
      "v_add_u32 v120, 4294856704, v120\n"
      "s_add_u32 m0, s41, 73728\n"
      "s_nop 0\n"
      "buffer_load_dwordx4 v100, s[12:15], s42 offen offset:0 lds\n"
      "buffer_load_dwordx4 v101, s[12:15], s42 offen offset:1024 lds\n"
      "buffer_load_dwordx4 v102, s[12:15], s42 offen offset:2048 lds\n"
      "buffer_load_dwordx4 v103, s[12:15], s42 offen offset:3072 lds\n"
      "s_add_u32 m0, s41, 77824\n"
      "s_nop 0\n"
      "buffer_load_dwordx4 v104, s[12:15], s42 offen offset:0 lds\n"
      "buffer_load_dwordx4 v105, s[12:15], s42 offen offset:1024 lds\n"
      "buffer_load_dwordx4 v106, s[12:15], s42 offen offset:2048 lds\n"
      "buffer_load_dwordx4 v107, s[12:15], s42 offen offset:3072 lds\n"
      "s_add_u32 m0, s41, 81920\n"
      "s_nop 0\n"
      "buffer_load_dwordx4 v108, s[12:15], s42 offen offset:0 lds\n"
      "s_add_u32 s42, s42, 41472\n"
      "s_waitcnt vmcnt(18) lgkmcnt(0)\n"
      "s_barrier\n"
      "ds_read_b64_tr_b16 v[80:81], v120 offset:0\n"
      "ds_read_b64_tr_b16 v[82:83], v120 offset:2304\n"
      "ds_read_b64_tr_b16 a[16:17], v118 offset:0\n"
      "ds_read_b64_tr_b16 a[18:19], v118 offset:2304\n"
      "ds_read_b64_tr_b16 a[88:89], v118 offset:8\n"
      "ds_read_b64_tr_b16 a[90:91], v118 offset:2312\n"
      "ds_read_b64_tr_b16 a[160:161], v118 offset:64\n"
      "ds_read_b64_tr_b16 a[162:163], v118 offset:2368\n"
      "ds_read_b64_tr_b16 a[232:233], v118 offset:72\n"
      "ds_read_b64_tr_b16 a[234:235], v118 offset:2376\n"
      "ds_read_b64_tr_b16 a[52:53], v119 offset:0\n"
      "ds_read_b64_tr_b16 a[54:55], v119 offset:2304\n"
      "ds_read_b64_tr_b16 a[124:125], v119 offset:8\n"
      "ds_read_b64_tr_b16 a[126:127], v119 offset:2312\n"
      "ds_read_b64_tr_b16 a[196:197], v119 offset:64\n"
      "ds_read_b64_tr_b16 a[198:199], v119 offset:2368\n"
      "ds_read_b64_tr_b16 v[44:45], v119 offset:72\n"
      "ds_read_b64_tr_b16 v[46:47], v119 offset:2376\n"
      "v_add_u32 v118, 36864, v118\n"
      "v_add_u32 v119, 36864, v119\n"
      "v_add_u32 v120, 36864, v120\n"
      "s_add_u32 m0, s41, 110592\n"
      "s_nop 0\n"
      "buffer_load_dwordx4 v100, s[12:15], s42 offen offset:0 lds\n"
      "buffer_load_dwordx4 v101, s[12:15], s42 offen offset:1024 lds\n"
      "buffer_load_dwordx4 v102, s[12:15], s42 offen offset:2048 lds\n"
      "buffer_load_dwordx4 v103, s[12:15], s42 offen offset:3072 lds\n"
      "s_add_u32 m0, s41, 114688\n"
      "s_nop 0\n"
      "buffer_load_dwordx4 v104, s[12:15], s42 offen offset:0 lds\n"
      "buffer_load_dwordx4 v105, s[12:15], s42 offen offset:1024 lds\n"
      "buffer_load_dwordx4 v106, s[12:15], s42 offen offset:2048 lds\n"
      "buffer_load_dwordx4 v107, s[12:15], s42 offen offset:3072 lds\n"
      "s_add_u32 m0, s41, 118784\n"
      "s_nop 0\n"
      "buffer_load_dwordx4 v108, s[12:15], s42 offen offset:0 lds\n"
      "s_add_u32 s42, s42, 41472\n"
      "s_waitcnt vmcnt(18) lgkmcnt(0)\n"
      "s_barrier\n"
      "ds_read_b64_tr_b16 v[84:85], v120 offset:0\n"
      "ds_read_b64_tr_b16 v[86:87], v120 offset:2304\n"
      "ds_read_b64_tr_b16 a[20:21], v118 offset:0\n"
      "ds_read_b64_tr_b16 a[22:23], v118 offset:2304\n"
      "ds_read_b64_tr_b16 a[92:93], v118 offset:8\n"
      "ds_read_b64_tr_b16 a[94:95], v118 offset:2312\n"
      "ds_read_b64_tr_b16 a[164:165], v118 offset:64\n"
      "ds_read_b64_tr_b16 a[166:167], v118 offset:2368\n"
      "ds_read_b64_tr_b16 a[236:237], v118 offset:72\n"
      "ds_read_b64_tr_b16 a[238:239], v118 offset:2376\n"
      "ds_read_b64_tr_b16 a[56:57], v119 offset:0\n"
      "ds_read_b64_tr_b16 a[58:59], v119 offset:2304\n"
      "ds_read_b64_tr_b16 a[128:129], v119 offset:8\n"
      "ds_read_b64_tr_b16 a[130:131], v119 offset:2312\n"
      "ds_read_b64_tr_b16 a[200:201], v119 offset:64\n"
      "ds_read_b64_tr_b16 a[202:203], v119 offset:2368\n"
      "ds_read_b64_tr_b16 v[48:49], v119 offset:72\n"
      "ds_read_b64_tr_b16 v[50:51], v119 offset:2376\n"
      "v_add_u32 v118, 36864, v118\n"
      "v_add_u32 v119, 36864, v119\n"
      "v_add_u32 v120, 36864, v120\n"
      "s_add_u32 m0, s41, 0\n"
      "s_nop 0\n"
      "buffer_load_dwordx4 v109, s[12:15], s43 offen offset:0 lds\n"
      "buffer_load_dwordx4 v110, s[12:15], s43 offen offset:1024 lds\n"
      "buffer_load_dwordx4 v111, s[12:15], s43 offen offset:2048 lds\n"
      "buffer_load_dwordx4 v112, s[12:15], s43 offen offset:3072 lds\n"
      "s_add_u32 m0, s41, 4096\n"
      "s_nop 0\n"
      "buffer_load_dwordx4 v113, s[12:15], s43 offen offset:0 lds\n"
      "buffer_load_dwordx4 v114, s[12:15], s43 offen offset:1024 lds\n"
      "buffer_load_dwordx4 v115, s[12:15], s43 offen offset:2048 lds\n"
      "buffer_load_dwordx4 v116, s[12:15], s43 offen offset:3072 lds\n"
      "s_add_u32 m0, s41, 8192\n"
      "s_nop 0\n"
      "buffer_load_dwordx4 v117, s[12:15], s43 offen offset:0 lds\n"
      "s_add_u32 s42, s42, 41472\n"
      "s_waitcnt vmcnt(18) lgkmcnt(0)\n"
      "s_barrier\n"
      "ds_read_b64_tr_b16 v[88:89], v120 offset:0\n"
      "ds_read_b64_tr_b16 v[90:91], v120 offset:2304\n"
      "ds_read_b64_tr_b16 a[24:25], v118 offset:0\n"
      "ds_read_b64_tr_b16 a[26:27], v118 offset:2304\n"
      "ds_read_b64_tr_b16 a[96:97], v118 offset:8\n"
      "ds_read_b64_tr_b16 a[98:99], v118 offset:2312\n"
      "ds_read_b64_tr_b16 a[168:169], v118 offset:64\n"
      "ds_read_b64_tr_b16 a[170:171], v118 offset:2368\n"
      "ds_read_b64_tr_b16 a[240:241], v118 offset:72\n"
      "ds_read_b64_tr_b16 a[242:243], v118 offset:2376\n"
      "ds_read_b64_tr_b16 a[60:61], v119 offset:0\n"
      "ds_read_b64_tr_b16 a[62:63], v119 offset:2304\n"
      "ds_read_b64_tr_b16 a[132:133], v119 offset:8\n"
      "ds_read_b64_tr_b16 a[134:135], v119 offset:2312\n"
      "ds_read_b64_tr_b16 a[204:205], v119 offset:64\n"
      "ds_read_b64_tr_b16 a[206:207], v119 offset:2368\n"
      "ds_read_b64_tr_b16 v[52:53], v119 offset:72\n"
      "ds_read_b64_tr_b16 v[54:55], v119 offset:2376\n"
      "v_add_u32 v118, 36864, v118\n"
      "v_add_u32 v119, 36864, v119\n"
      "v_add_u32 v120, 36864, v120\n"
      "s_waitcnt vmcnt(9) lgkmcnt(0)\n"
      "s_barrier\n"
      "ds_read_b64_tr_b16 v[92:93], v120 offset:0\n"
      "ds_read_b64_tr_b16 v[94:95], v120 offset:2304\n"
      "ds_read_b64_tr_b16 a[28:29], v118 offset:0\n"
      "ds_read_b64_tr_b16 a[30:31], v118 offset:2304\n"
      "ds_read_b64_tr_b16 a[100:101], v118 offset:8\n"
      "ds_read_b64_tr_b16 a[102:103], v118 offset:2312\n"
      "ds_read_b64_tr_b16 a[172:173], v118 offset:64\n"
      "ds_read_b64_tr_b16 a[174:175], v118 offset:2368\n"
      "ds_read_b64_tr_b16 a[244:245], v118 offset:72\n"
      "ds_read_b64_tr_b16 a[246:247], v118 offset:2376\n"
      "ds_read_b64_tr_b16 a[64:65], v119 offset:0\n"
      "ds_read_b64_tr_b16 a[66:67], v119 offset:2304\n"
      "ds_read_b64_tr_b16 a[136:137], v119 offset:8\n"
      "ds_read_b64_tr_b16 a[138:139], v119 offset:2312\n"
      "ds_read_b64_tr_b16 a[208:209], v119 offset:64\n"
      "ds_read_b64_tr_b16 a[210:211], v119 offset:2368\n"
      "ds_read_b64_tr_b16 v[56:57], v119 offset:72\n"
      "ds_read_b64_tr_b16 v[58:59], v119 offset:2376\n"
      "v_add_u32 v118, 4294856704, v118\n"
      "v_add_u32 v119, 4294856704, v119\n"
      "v_add_u32 v120, 4294856704, v120\n"
      "s_waitcnt vmcnt(0) lgkmcnt(0)\n"
      "s_barrier\n"
      "ds_read_b64_tr_b16 v[96:97], v120 offset:0\n"
      "ds_read_b64_tr_b16 v[98:99], v120 offset:2304\n"
      "ds_read_b64_tr_b16 a[32:33], v118 offset:0\n"
      "ds_read_b64_tr_b16 a[34:35], v118 offset:2304\n"
      "ds_read_b64_tr_b16 a[104:105], v118 offset:8\n"
      "ds_read_b64_tr_b16 a[106:107], v118 offset:2312\n"
      "ds_read_b64_tr_b16 a[176:177], v118 offset:64\n"
      "ds_read_b64_tr_b16 a[178:179], v118 offset:2368\n"
      "ds_read_b64_tr_b16 a[248:249], v118 offset:72\n"
      "ds_read_b64_tr_b16 a[250:251], v118 offset:2376\n"
      "ds_read_b64_tr_b16 a[68:69], v119 offset:0\n"
      "ds_read_b64_tr_b16 a[70:71], v119 offset:2304\n"
      "ds_read_b64_tr_b16 a[140:141], v119 offset:8\n"
      "ds_read_b64_tr_b16 a[142:143], v119 offset:2312\n"
      "ds_read_b64_tr_b16 a[212:213], v119 offset:64\n"
      "ds_read_b64_tr_b16 a[214:215], v119 offset:2368\n"
      "ds_read_b64_tr_b16 v[60:61], v119 offset:72\n"
      "ds_read_b64_tr_b16 v[62:63], v119 offset:2376\n"
      "v_add_u32 v118, 36864, v118\n"
      "v_add_u32 v119, 36864, v119\n"
      "v_add_u32 v120, 36864, v120\n"
      "s_waitcnt lgkmcnt(0)\n"
      "s_getpc_b64 vcc\n"
      ".Lw_ct:\n"
      "s_add_u32 vcc_lo, vcc_lo, .Lw_p0-.Lw_ct\n"
      "s_addc_u32 vcc_hi, vcc_hi, 0\n"
      "s_lshl_b32 s30, s39, 1\n"
      "v_lshl_add_u32 v130, v0, 3, s30\n"
      "v_min_u32 v130, .Lw_last-.Lw_p0-4, v130\n"
      "global_load_dword v130, v130, vcc\n"
      "s_mul_i32 s21, s21, 1664525\n"
      "s_add_u32 s21, s21, 1013904223\n"
      "v_mov_b32 v25, s21\n"
      "buffer_store_dword v25, v22, s[8:11], 0 offen sc1\n"
      "v_not_b32 v124, v25\n"
      "v_not_b32 v125, v25\n"
      "v_not_b32 v126, v25\n"
      "v_not_b32 v127, v25\n"
      "s_add_u32 s30, s38, 0\n"
      "buffer_store_dwordx4 v[124:127], v121, s[8:11], s30 offen sc1\n"
      "v_xor_b32 v25, v25, v26\n"
      "v_mov_b32 v27, 0x3ae38e39\n"
      "s_barrier\n"
      "v_mov_b32 v21, 0\n"
      "ds_write_b32 v23, v21\n"
      "s_waitcnt lgkmcnt(0)\n"
      "s_barrier\n"
      "s_add_u32 s6, s36, 1572864\n"
      "s_add_u32 s36, s36, 4294942720\n"
      "s_mov_b32 s51, 24576\n"
      "s_waitcnt vmcnt(0) lgkmcnt(0)\n"
      "s_cmp_lg_u32 s44, 0\n"
      "s_cbranch_scc1 .Lv_start\n"
      "s_mov_b64 exec, 0\n"
      "s_lshr_b32 s22, s39, 13\n"
      ".Lw_pl:\n"
      "s_cmp_eq_u32 s22, 0\n"
      "s_cbranch_scc1 .Lw_p0\n"
      "s_cmp_eq_u32 s22, 1\n"
      "s_cbranch_scc1 .Lw_p1\n"
      "s_branch .Lw_pe\n"
      ".Lw_pr:\n"
      "s_add_u32 s22, s22, 2\n"
      "s_branch .Lw_pl\n"
      ".Lw_pe:\n"
      "s_mov_b64 exec, -1\n"
      "s_add_u32 s50, s38, 4294893568\n"
      "s_add_u32 s49, s38, 4294893568\n"
      "s_add_u32 s38, s38, 4294942720\n"
      "s_cmp_eq_u32 s36, s37\n"
      "s_cselect_b32 s48, 49152, s51\n"
      "s_add_u32 s36, s36, s48\n"
      "s_min_u32 s36, s36, s6\n"
      "buffer_load_dword v28, v11, s[8:11], s36 offen sc1\n"
      "s_mov_b32 s46, s36\n"
      "s_add_u32 s36, s36, 24576\n"
      "s_min_u32 s36, s36, s6\n"
      "s_waitcnt vmcnt(0)\n"
      "v_cmp_ne_u32 vcc, s21, v28\n"
      "s_add_u32 m0, s39, 0\n"
      "s_cbranch_vccnz .Lsp_w0_1\n"
      ".Lbk_w0_1:\n"
      "buffer_load_dword v28, v11, s[8:11], s36 offen sc1\n"
      "buffer_load_dwordx4 v0, s[8:11], s46 offen offset:0 sc1 lds\n"
      "buffer_load_dwordx4 v0, s[8:11], s46 offen offset:1024 sc1 lds\n"
      "buffer_load_dwordx4 v0, s[8:11], s46 offen offset:3072 sc1 lds\n"
      "buffer_load_dwordx4 v0, s[8:11], s46 offen offset:2048 sc1 lds\n"
      "s_add_u32 m0, s40, 0\n"
      "s_nop 0\n"
      "buffer_load_dwordx4 v1, s[8:11], s46 offen offset:0 sc1 lds\n"
      "s_sub_u32 s47, s46, s45\n"
      "buffer_load_dwordx2 v[200:201], v14, s[8:11], s47 offen sc1\n"
      "buffer_load_dwordx4 v[192:195], v13, s[8:11], s47 offen sc1\n"
      "buffer_load_dword v26, v23, s[8:11], s31 offen\n"
      "s_add_u32 m0, s39, 20480\n"
      "s_mov_b32 s46, s36\n"
      "s_waitcnt vmcnt(8)\n"
      "v_cmp_ne_u32 vcc, s21, v28\n"
      "buffer_load_dword v26, v23, s[8:11], s31 offen\n"
      "s_cmp_eq_u32 s36, s37\n"
      "s_cselect_b32 s48, 49152, s51\n"
      "s_add_u32 s36, s36, s48\n"
      "s_cbranch_vccnz .Lsp_w1_2\n"
      ".Lbk_w1_2:\n"
      "s_min_u32 s36, s36, s6\n"
      "buffer_load_dword v28, v11, s[8:11], s36 offen sc1\n"
      "buffer_load_dwordx4 v0, s[8:11], s46 offen offset:0 sc1 lds\n"
      "buffer_load_dwordx4 v0, s[8:11], s46 offen offset:1024 sc1 lds\n"
      "buffer_load_dwordx4 v0, s[8:11], s46 offen offset:3072 sc1 lds\n"
      "v_add_u32 v21, 1, v21\n"
      "s_waitcnt vmcnt(8)\n"
      "ds_write_b32 v23, v21\n"
      "buffer_load_dwordx4 v0, s[8:11], s46 offen offset:2048 sc1 lds\n"
      "s_add_u32 m0, s40, 20480\n"
      "buffer_load_dwordx4 v[196:199], v13, s[8:11], s47 offen offset:1024 sc1\n"
      "buffer_load_dwordx4 v1, s[8:11], s46 offen offset:0 sc1 lds\n"
      "ds_read_b32 v22, v24\n"
      "s_waitcnt lgkmcnt(0)\n"
      "v_cmp_gt_i32 vcc, v21, v22\n"
      "s_waitcnt vmcnt(10)\n"
      "v_and_b32 v189, 0xffff0000, v200\n"
      "v_lshlrev_b32 v188, 16, v200\n"
      "s_cbranch_vccnz .Lsp_wy0_3\n"
      ".Lbk_wy0_3:\n"
      "ds_read_b128 v[104:107], v0 offset:1024\n"
      "v_lshlrev_b32 v190, 16, v201\n"
      "ds_read_b128 v[100:103], v0 offset:0\n"
      "ds_read_b128 v[120:123], v0 offset:5120\n"
      "s_waitcnt vmcnt(9)\n"
      "v_lshlrev_b32 v178, 16, v195\n"
      "ds_read_b128 v[112:115], v0 offset:3072\n"
      "ds_read_b128 v[116:119], v0 offset:4096\n"
      "v_lshlrev_b32 v174, 16, v193\n"
      "ds_read_b128 v[124:127], v0 offset:6144\n"
      "s_sub_u32 s47, s46, s45\n"
      "ds_read_b128 v[108:111], v0 offset:2048\n"
      "v_and_b32 v191, 0xffff0000, v201\n"
      "v_and_b32 v177, 0xffff0000, v194\n"
      "ds_read_b128 v[136:139], v0 offset:8192\n"
      "v_lshlrev_b32 v176, 16, v194\n"
      "ds_read_b128 v[128:131], v0 offset:7168\n"
      "v_lshlrev_b32 v172, 16, v192\n"
      "v_and_b32 v175, 0xffff0000, v193\n"
      "buffer_load_dwordx2 v[200:201], v14, s[8:11], s47 offen sc1\n"
      "ds_read2_b32 v[132:133], v2 offset0:0 offset1:32\n"
      "v_and_b32 v173, 0xffff0000, v192\n"
      "ds_read2_b32 v[134:135], v2 offset0:64 offset1:96\n"
      "s_mov_b32 s20, 31\n"
      "s_waitcnt lgkmcnt(0)\n"
      "s_nop 4\n"
      "s_branch .Lw_loop\n"
      ".p2align 12\n"
      ".Lw_p0:\n"
      "s_branch .Lw_pr\n"
      ".p2align 5\n"
      ".Lw_loop:\n"
      "v_mfma_f32_16x16x32_bf16 v[188:191], v[64:67], v[100:103], v[188:191]\n"
      "ds_read_b128 v[140:143], v0 offset:9216\n"
      "v_and_b32 v179, 0xffff0000, v195\n"
      "v_mfma_f32_16x16x32_bf16 v[188:191], v[68:71], v[104:107], v[188:191]\n"
      "buffer_load_dwordx4 v[192:195], v13, s[8:11], s47 offen sc1\n"
      "v_mfma_f32_16x16x32_bf16 v[188:191], v[72:75], v[108:111], v[188:191]\n"
      "v_add_f32 v218, v218, v182\n"
      "v_mfma_f32_16x16x32_bf16 v[188:191], v[76:79], v[112:115], v[188:191]\n"
      "ds_read_b128 v[156:159], v0 offset:13312\n"
      "v_fmac_f32 v219, v184, v184\n"
      "v_mfma_f32_16x16x32_bf16 v[188:191], v[80:83], v[116:119], v[188:191]\n"
      "ds_read_b128 v[164:167], v0 offset:15360\n"
      "v_cvt_pk_bf16_f32 v213, v182, v183\n"
      "v_mfma_f32_16x16x32_bf16 v[188:191], v[84:87], v[120:123], v[188:191]\n"
      "ds_read_b128 v[144:147], v0 offset:10240\n"
      "v_mfma_f32_16x16x32_bf16 v[188:191], v[88:91], v[124:127], v[188:191]\n"
      "s_waitcnt lgkmcnt(4)\n"
      "v_cvt_pk_bf16_f32 v215, v186, v187\n"
      "v_add_f32 v218, v218, v183\n"
      "v_mfma_f32_16x16x32_bf16 v[188:191], v[92:95], v[128:131], v[188:191]\n"
      "buffer_store_dwordx4 v[208:211], v15, s[8:11], s38 offen sc1\n"
      "v_mfma_f32_16x16x32_bf16 v[188:191], v[96:99], v[132:135], v[188:191]\n"
      "v_cvt_pk_bf16_f32 v212, v180, v181\n"
      "v_add_f32 v218, v218, v184\n"
      "v_mfma_f32_16x16x32_bf16 v[172:175], a[0:3], v[100:103], v[172:175]\n"
      "s_add_u32 s50, s50, 24576\n"
      "v_cvt_pk_bf16_f32 v214, v184, v185\n"
      "v_add_f32 v218, v218, v185\n"
      "v_mfma_f32_16x16x32_bf16 v[172:175], a[4:7], v[104:107], v[172:175]\n"
      "s_mov_b32 s46, s36\n"
      "v_add_f32 v218, v218, v186\n"
      "v_mfma_f32_16x16x32_bf16 v[172:175], a[8:11], v[108:111], v[172:175]\n"
      "ds_read_b128 v[148:151], v0 offset:11264\n"
      "v_add_f32 v218, v218, v187\n"
      "v_mfma_f32_16x16x32_bf16 v[172:175], a[12:15], v[112:115], v[172:175]\n"
      "v_fmac_f32 v219, v185, v185\n"
      "v_add_u32 v21, 1, v21\n"
      "v_mfma_f32_16x16x32_bf16 v[172:175], a[16:19], v[116:119], v[172:175]\n"
      "s_add_u32 s36, s36, 24576\n"
      "ds_read_b128 v[152:155], v0 offset:12288\n"
      "v_fmac_f32 v219, v186, v186\n"
      "v_mfma_f32_16x16x32_bf16 v[172:175], a[20:23], v[120:123], v[172:175]\n"
      "buffer_store_dwordx2 v[216:217], v16, s[8:11], s38 offen sc1\n"
      "v_mfma_f32_16x16x32_bf16 v[172:175], a[24:27], v[124:127], v[172:175]\n"
      "ds_read_b128 v[160:163], v0 offset:14336\n"
      "v_mfma_f32_16x16x32_bf16 v[172:175], a[28:31], v[128:131], v[172:175]\n"
      "ds_read2_b32 v[170:171], v3 offset0:64 offset1:96\n"
      "v_mfma_f32_16x16x32_bf16 v[172:175], a[32:35], v[132:135], v[172:175]\n"
      "ds_read2_b32 v[168:169], v3 offset0:0 offset1:32\n"
      "s_min_u32 s36, s36, s6\n"
      "v_mfma_f32_16x16x32_bf16 v[172:175], a[36:39], v[136:139], v[172:175]\n"
      "s_waitcnt lgkmcnt(4)\n"
      "v_mfma_f32_16x16x32_bf16 v[172:175], a[40:43], v[140:143], v[172:175]\n"
      "buffer_store_dwordx4 v[212:215], v15, s[8:11], s38 offen offset:1024 sc1\n"
      "v_mfma_f32_16x16x32_bf16 v[172:175], a[44:47], v[144:147], v[172:175]\n"
      "ds_write_b32 v18, v218\n"
      "v_mfma_f32_16x16x32_bf16 v[172:175], a[48:51], v[148:151], v[172:175]\n"
      "s_waitcnt vmcnt(11) lgkmcnt(4)\n"
      "v_fmac_f32_e64 v219, v187, v187\n"
      "v_cmp_ne_u32 vcc, s21, v28\n"
      "v_mfma_f32_16x16x32_bf16 v[172:175], a[52:55], v[152:155], v[172:175]\n"
      "ds_write_b32 v18, v219 offset:4\n"
      "v_mfma_f32_16x16x32_bf16 v[172:175], a[56:59], v[156:159], v[172:175]\n"
      "s_waitcnt lgkmcnt(3)\n"
      "s_add_u32 m0, s39, 40960\n"
      "v_mfma_f32_16x16x32_bf16 v[172:175], a[60:63], v[160:163], v[172:175]\n"
      "ds_write_b32 v23, v21\n"
      "v_mfma_f32_16x16x32_bf16 v[172:175], a[64:67], v[164:167], v[172:175]\n"
      "s_waitcnt lgkmcnt(3)\n"
      "s_cbranch_vccnz .Lsp_w0_4\n"
      ".Lbk_w0_4:\n"
      "v_mfma_f32_16x16x32_bf16 v[172:175], a[68:71], v[168:171], v[172:175]\n"
      "v_mfma_f32_16x16x32_bf16 v[176:179], a[72:75], v[100:103], v[176:179]\n"
      "buffer_load_dword v28, v11, s[8:11], s36 offen sc1\n"
      "v_mfma_f32_16x16x32_bf16 v[176:179], a[76:79], v[104:107], v[176:179]\n"
      "s_waitcnt vmcnt(7) lgkmcnt(2)\n"
      "v_lshlrev_b32 v186, 16, v199\n"
      "v_lshlrev_b32 v184, 16, v198\n"
      "v_mfma_f32_16x16x32_bf16 v[176:179], a[80:83], v[108:111], v[176:179]\n"
      "v_lshlrev_b32 v180, 16, v196\n"
      "v_and_b32 v183, 0xffff0000, v197\n"
      "v_mfma_f32_16x16x32_bf16 v[176:179], a[84:87], v[112:115], v[176:179]\n"
      "buffer_store_dword v25, v12, s[8:11], s50 offen sc1\n"
      "v_mfma_f32_16x16x32_bf16 v[176:179], a[88:91], v[116:119], v[176:179]\n"
      "v_lshlrev_b32 v182, 16, v197\n"
      "v_and_b32 v181, 0xffff0000, v196\n"
      "v_mfma_f32_16x16x32_bf16 v[176:179], a[92:95], v[120:123], v[176:179]\n"
      "v_and_b32 v187, 0xffff0000, v199\n"
      "v_and_b32 v185, 0xffff0000, v198\n"
      "v_mfma_f32_16x16x32_bf16 v[176:179], a[96:99], v[124:127], v[176:179]\n"
      "buffer_load_dwordx4 v[196:199], v13, s[8:11], s47 offen offset:1024 sc1\n"
      "v_mfma_f32_16x16x32_bf16 v[176:179], a[100:103], v[128:131], v[176:179]\n"
      "ds_read_b32 v22, v24\n"
      "v_mul_f32 v219, v172, v172\n"
      "v_mfma_f32_16x16x32_bf16 v[176:179], a[104:107], v[132:135], v[176:179]\n"
      "v_mfma_f32_16x16x32_bf16 v[176:179], a[108:111], v[136:139], v[176:179]\n"
      "v_mfma_f32_16x16x32_bf16 v[176:179], a[112:115], v[140:143], v[176:179]\n"
      "buffer_load_dwordx4 v0, s[8:11], s46 offen offset:0 sc1 lds\n"
      "v_mfma_f32_16x16x32_bf16 v[176:179], a[116:119], v[144:147], v[176:179]\n"
      "v_add_f32 v218, v172, v173\n"
      "v_fmac_f32 v219, v173, v173\n"
      "v_mfma_f32_16x16x32_bf16 v[176:179], a[120:123], v[148:151], v[176:179]\n"
      "s_waitcnt lgkmcnt(0)\n"
      "v_cmp_gt_i32 vcc, v21, v22\n"
      "v_mfma_f32_16x16x32_bf16 v[176:179], a[124:127], v[152:155], v[176:179]\n"
      "v_mfma_f32_16x16x32_bf16 v[176:179], a[128:131], v[156:159], v[176:179]\n"
      "buffer_load_dwordx4 v0, s[8:11], s46 offen offset:1024 sc1 lds\n"
      "v_mfma_f32_16x16x32_bf16 v[176:179], a[132:135], v[160:163], v[176:179]\n"
      "s_cbranch_vccnz .Lsp_wz0_5\n"
      ".Lbk_wz0_5:\n"
      "ds_read2st64_b32 v[204:205], v17 offset1:1\n"
      "v_mfma_f32_16x16x32_bf16 v[176:179], a[136:139], v[164:167], v[176:179]\n"
      "ds_read2st64_b32 v[206:207], v17 offset0:2 offset1:3\n"
      "v_add_u32 v21, 1, v21\n"
      "v_mfma_f32_16x16x32_bf16 v[176:179], a[140:143], v[168:171], v[176:179]\n"
      "s_waitcnt vmcnt(10)\n"
      "ds_write_b32 v23, v21\n"
      "v_mfma_f32_16x16x32_bf16 v[180:183], a[144:147], v[100:103], v[180:183]\n"
      "buffer_load_dwordx4 v0, s[8:11], s46 offen offset:3072 sc1 lds\n"
      "v_mfma_f32_16x16x32_bf16 v[180:183], a[148:151], v[104:107], v[180:183]\n"
      "v_add_f32 v218, v218, v174\n"
      "v_fmac_f32 v219, v174, v174\n"
      "v_mfma_f32_16x16x32_bf16 v[180:183], a[152:155], v[108:111], v[180:183]\n"
      "v_fmac_f32 v219, v175, v175\n"
      "v_fmac_f32 v219, v176, v176\n"
      "v_mfma_f32_16x16x32_bf16 v[180:183], a[156:159], v[112:115], v[180:183]\n"
      "v_add_f32 v218, v218, v175\n"
      "v_add_f32 v218, v218, v176\n"
      "v_mfma_f32_16x16x32_bf16 v[180:183], a[160:163], v[116:119], v[180:183]\n"
      "buffer_load_dwordx4 v0, s[8:11], s46 offen offset:2048 sc1 lds\n"
      "v_mfma_f32_16x16x32_bf16 v[180:183], a[164:167], v[120:123], v[180:183]\n"
      "s_add_u32 m0, s40, 40960\n"
      "v_add_f32 v218, v218, v177\n"
      "v_cvt_pk_bf16_f32 v209, v174, v175\n"
      "v_mfma_f32_16x16x32_bf16 v[180:183], a[168:171], v[124:127], v[180:183]\n"
      "s_waitcnt lgkmcnt(0)\n"
      "v_add_f32 v188, v188, v204\n"
      "v_add_f32 v190, v190, v206\n"
      "v_mfma_f32_16x16x32_bf16 v[180:183], a[172:175], v[128:131], v[180:183]\n"
      "v_cvt_pk_bf16_f32 v208, v172, v173\n"
      "v_fmac_f32 v219, v177, v177\n"
      "v_mfma_f32_16x16x32_bf16 v[180:183], a[176:179], v[132:135], v[180:183]\n"
      "buffer_load_dwordx4 v1, s[8:11], s46 offen offset:0 sc1 lds\n"
      "v_mfma_f32_16x16x32_bf16 v[180:183], a[180:183], v[136:139], v[180:183]\n"
      "s_add_u32 s38, s38, 24576\n"
      "v_cvt_pk_bf16_f32 v210, v176, v177\n"
      "v_mfma_f32_16x16x32_bf16 v[180:183], a[184:187], v[140:143], v[180:183]\n"
      "ds_read_b32 v22, v24\n"
      "v_add_f32 v218, v218, v178\n"
      "v_mfma_f32_16x16x32_bf16 v[180:183], a[188:191], v[144:147], v[180:183]\n"
      "v_add_f32 v218, v218, v179\n"
      "v_add_f32 v218, v218, v188\n"
      "v_mfma_f32_16x16x32_bf16 v[180:183], a[192:195], v[148:151], v[180:183]\n"
      "s_add_u32 s50, s50, 24576\n"
      "v_fmac_f32 v219, v178, v178\n"
      "v_fmac_f32 v219, v179, v179\n"
      "v_mfma_f32_16x16x32_bf16 v[180:183], a[196:199], v[152:155], v[180:183]\n"
      "v_add_f32 v191, v191, v207\n"
      "v_cvt_pk_bf16_f32 v211, v178, v179\n"
      "v_mfma_f32_16x16x32_bf16 v[180:183], a[200:203], v[156:159], v[180:183]\n"
      "v_fmac_f32 v219, v188, v188\n"
      "v_add_f32 v189, v189, v205\n"
      "v_mfma_f32_16x16x32_bf16 v[180:183], a[204:207], v[160:163], v[180:183]\n"
      "s_waitcnt lgkmcnt(0)\n"
      "v_fmac_f32 v219, v189, v189\n"
      "v_cmp_gt_i32 vcc, v21, v22\n"
      "v_mfma_f32_16x16x32_bf16 v[180:183], a[208:211], v[164:167], v[180:183]\n"
      "v_fmac_f32 v219, v190, v190\n"
      "v_add_f32 v218, v218, v189\n"
      "v_mfma_f32_16x16x32_bf16 v[180:183], a[212:215], v[168:171], v[180:183]\n"
      "s_waitcnt vmcnt(11)\n"
      "v_lshlrev_b32 v172, 16, v192\n"
      "v_mfma_f32_16x16x32_bf16 v[184:187], a[216:219], v[100:103], v[184:187]\n"
      "v_and_b32 v175, 0xffff0000, v193\n"
      "v_and_b32 v177, 0xffff0000, v194\n"
      "v_mfma_f32_16x16x32_bf16 v[184:187], a[220:223], v[104:107], v[184:187]\n"
      "s_cbranch_vccnz .Lsp_wy1_6\n"
      ".Lbk_wy1_6:\n"
      "v_add_f32 v218, v218, v190\n"
      "v_mfma_f32_16x16x32_bf16 v[184:187], a[224:227], v[108:111], v[184:187]\n"
      "ds_read_b128 v[104:107], v0 offset:21504\n"
      "v_cvt_pk_bf16_f32 v216, v188, v189\n"
      "v_mfma_f32_16x16x32_bf16 v[184:187], a[228:231], v[112:115], v[184:187]\n"
      "v_add_f32 v218, v218, v191\n"
      "v_add_f32 v218, v218, v180\n"
      "v_mfma_f32_16x16x32_bf16 v[184:187], a[232:235], v[116:119], v[184:187]\n"
      "ds_read_b128 v[116:119], v0 offset:24576\n"
      "v_cvt_pk_bf16_f32 v217, v190, v191\n"
      "v_mfma_f32_16x16x32_bf16 v[184:187], a[236:239], v[120:123], v[184:187]\n"
      "ds_read_b128 v[108:111], v0 offset:22528\n"
      "v_lshlrev_b32 v188, 16, v200\n"
      "v_mfma_f32_16x16x32_bf16 v[184:187], a[240:243], v[124:127], v[184:187]\n"
      "ds_read_b128 v[100:103], v0 offset:20480\n"
      "v_fmac_f32 v219, v191, v191\n"
      "v_mfma_f32_16x16x32_bf16 v[184:187], a[244:247], v[128:131], v[184:187]\n"
      "v_lshlrev_b32 v190, 16, v201\n"
      "v_and_b32 v191, 0xffff0000, v201\n"
      "v_mfma_f32_16x16x32_bf16 v[184:187], a[248:251], v[132:135], v[184:187]\n"
      "ds_read2_b32 v[134:135], v4 offset0:64 offset1:96\n"
      "v_lshlrev_b32 v174, 16, v193\n"
      "v_mfma_f32_16x16x32_bf16 v[184:187], a[252:255], v[136:139], v[184:187]\n"
      "s_sub_u32 s47, s46, s45\n"
      "v_and_b32 v189, 0xffff0000, v200\n"
      "v_fmac_f32 v219, v180, v180\n"
      "v_mfma_f32_16x16x32_bf16 v[184:187], v[32:35], v[140:143], v[184:187]\n"
      "buffer_store_dwordx4 v[208:211], v15, s[8:11], s38 offen sc1\n"
      "v_mfma_f32_16x16x32_bf16 v[184:187], v[36:39], v[144:147], v[184:187]\n"
      "ds_read_b128 v[136:139], v0 offset:28672\n"
      "v_fmac_f32 v219, v181, v181\n"
      "v_mfma_f32_16x16x32_bf16 v[184:187], v[40:43], v[148:151], v[184:187]\n"
      "ds_read2_b32 v[132:133], v4 offset0:0 offset1:32\n"
      "v_lshlrev_b32 v178, 16, v195\n"
      "v_mfma_f32_16x16x32_bf16 v[184:187], v[44:47], v[152:155], v[184:187]\n"
      "ds_read_b128 v[112:115], v0 offset:23552\n"
      "v_add_f32 v218, v218, v181\n"
      "v_mfma_f32_16x16x32_bf16 v[184:187], v[48:51], v[156:159], v[184:187]\n"
      "ds_read_b128 v[120:123], v0 offset:25600\n"
      "v_and_b32 v173, 0xffff0000, v192\n"
      "v_mfma_f32_16x16x32_bf16 v[184:187], v[52:55], v[160:163], v[184:187]\n"
      "ds_read_b128 v[124:127], v0 offset:26624\n"
      "v_lshlrev_b32 v176, 16, v194\n"
      "v_mfma_f32_16x16x32_bf16 v[184:187], v[56:59], v[164:167], v[184:187]\n"
      "buffer_load_dwordx2 v[200:201], v14, s[8:11], s47 offen sc1\n"
      "v_mfma_f32_16x16x32_bf16 v[184:187], v[60:63], v[168:171], v[184:187]\n"
      "s_waitcnt lgkmcnt(5)\n"
      "ds_read_b128 v[128:131], v0 offset:27648\n"
      "v_add_f32 v218, v218, v182\n"
      "v_mfma_f32_16x16x32_bf16 v[188:191], v[64:67], v[100:103], v[188:191]\n"
      "ds_read_b128 v[140:143], v0 offset:29696\n"
      "v_cvt_pk_bf16_f32 v213, v182, v183\n"
      "v_mfma_f32_16x16x32_bf16 v[188:191], v[68:71], v[104:107], v[188:191]\n"
      "ds_read_b128 v[152:155], v0 offset:32768\n"
      "v_mfma_f32_16x16x32_bf16 v[188:191], v[72:75], v[108:111], v[188:191]\n"
      "s_waitcnt lgkmcnt(5)\n"
      "buffer_store_dwordx2 v[216:217], v16, s[8:11], s38 offen sc1\n"
      "v_mfma_f32_16x16x32_bf16 v[188:191], v[76:79], v[112:115], v[188:191]\n"
      "ds_read_b128 v[144:147], v0 offset:30720\n"
      "v_cvt_pk_bf16_f32 v212, v180, v181\n"
      "v_mfma_f32_16x16x32_bf16 v[188:191], v[80:83], v[116:119], v[188:191]\n"
      "s_waitcnt lgkmcnt(4)\n"
      "ds_read_b128 v[164:167], v0 offset:35840\n"
      "v_add_f32 v218, v218, v183\n"
      "v_mfma_f32_16x16x32_bf16 v[188:191], v[84:87], v[120:123], v[188:191]\n"
      "v_fmac_f32 v219, v182, v182\n"
      "v_add_f32 v218, v218, v184\n"
      "v_mfma_f32_16x16x32_bf16 v[188:191], v[88:91], v[124:127], v[188:191]\n"
      "s_waitcnt lgkmcnt(4)\n"
      "ds_read_b128 v[148:151], v0 offset:31744\n"
      "v_add_f32 v218, v218, v185\n"
      "v_mfma_f32_16x16x32_bf16 v[188:191], v[92:95], v[128:131], v[188:191]\n"
      "ds_read_b128 v[160:163], v0 offset:34816\n"
      "v_and_b32 v179, 0xffff0000, v195\n"
      "v_mfma_f32_16x16x32_bf16 v[188:191], v[96:99], v[132:135], v[188:191]\n"
      "buffer_load_dwordx4 v[192:195], v13, s[8:11], s47 offen sc1\n"
      "v_mfma_f32_16x16x32_bf16 v[172:175], a[0:3], v[100:103], v[172:175]\n"
      "v_cvt_pk_bf16_f32 v215, v186, v187\n"
      "v_fmac_f32 v219, v183, v183\n"
      "v_mfma_f32_16x16x32_bf16 v[172:175], a[4:7], v[104:107], v[172:175]\n"
      "v_cvt_pk_bf16_f32 v214, v184, v185\n"
      "v_mfma_f32_16x16x32_bf16 v[172:175], a[8:11], v[108:111], v[172:175]\n"
      "ds_read2_b32 v[168:169], v5 offset0:0 offset1:32\n"
      "v_mfma_f32_16x16x32_bf16 v[172:175], a[12:15], v[112:115], v[172:175]\n"
      "ds_read_b128 v[156:159], v0 offset:33792\n"
      "v_mfma_f32_16x16x32_bf16 v[172:175], a[16:19], v[116:119], v[172:175]\n"
      "buffer_store_dwordx4 v[212:215], v15, s[8:11], s38 offen offset:1024 sc1\n"
      "v_mfma_f32_16x16x32_bf16 v[172:175], a[20:23], v[120:123], v[172:175]\n"
      "v_fmac_f32 v219, v184, v184\n"
      "v_fmac_f32 v219, v185, v185\n"
      "v_mfma_f32_16x16x32_bf16 v[172:175], a[24:27], v[124:127], v[172:175]\n"
      "ds_read2_b32 v[170:171], v5 offset0:64 offset1:96\n"
      "v_add_f32 v218, v218, v186\n"
      "v_mfma_f32_16x16x32_bf16 v[172:175], a[28:31], v[128:131], v[172:175]\n"
      "v_fmac_f32 v219, v186, v186\n"
      "v_add_f32 v218, v218, v187\n"
      "v_mfma_f32_16x16x32_bf16 v[172:175], a[32:35], v[132:135], v[172:175]\n"
      "ds_write_b32 v18, v218\n"
      "v_mfma_f32_16x16x32_bf16 v[172:175], a[36:39], v[136:139], v[172:175]\n"
      "s_waitcnt lgkmcnt(3)\n"
      "s_add_u32 m0, s39, 61440\n"
      "v_add_u32 v21, 1, v21\n"
      "v_mfma_f32_16x16x32_bf16 v[172:175], a[40:43], v[140:143], v[172:175]\n"
      "s_mov_b32 s46, s36\n"
      "v_mfma_f32_16x16x32_bf16 v[172:175], a[44:47], v[144:147], v[172:175]\n"
      "v_fmac_f32 v219, v187, v187\n"
      "v_mfma_f32_16x16x32_bf16 v[172:175], a[48:51], v[148:151], v[172:175]\n"
      "ds_write_b32 v18, v219 offset:4\n"
      "v_mfma_f32_16x16x32_bf16 v[172:175], a[52:55], v[152:155], v[172:175]\n"
      "s_waitcnt vmcnt(10) lgkmcnt(2)\n"
      "v_and_b32 v181, 0xffff0000, v196\n"
      "v_mfma_f32_16x16x32_bf16 v[172:175], a[56:59], v[156:159], v[172:175]\n"
      "ds_write_b32 v23, v21\n"
      "v_mfma_f32_16x16x32_bf16 v[172:175], a[60:63], v[160:163], v[172:175]\n"
      "v_lshlrev_b32 v186, 16, v199\n"
      "v_cmp_ne_u32 vcc, s21, v28\n"
      "v_mfma_f32_16x16x32_bf16 v[172:175], a[64:67], v[164:167], v[172:175]\n"
      "buffer_store_dword v25, v12, s[8:11], s50 offen sc1\n"
      "v_mfma_f32_16x16x32_bf16 v[172:175], a[68:71], v[168:171], v[172:175]\n"
      "s_cmp_eq_u32 s36, s37\n"
      "s_cselect_b32 s48, 49152, s51\n"
      "s_add_u32 s36, s36, s48\n"
      "v_mfma_f32_16x16x32_bf16 v[176:179], a[72:75], v[100:103], v[176:179]\n"
      "s_cbranch_vccnz .Lsp_w1_7\n"
      ".Lbk_w1_7:\n"
      "s_min_u32 s36, s36, s6\n"
      "v_mfma_f32_16x16x32_bf16 v[176:179], a[76:79], v[104:107], v[176:179]\n"
      "buffer_load_dword v28, v11, s[8:11], s36 offen sc1\n"
      "v_mfma_f32_16x16x32_bf16 v[176:179], a[80:83], v[108:111], v[176:179]\n"
      "v_and_b32 v183, 0xffff0000, v197\n"
      "v_mul_f32 v219, v172, v172\n"
      "v_mfma_f32_16x16x32_bf16 v[176:179], a[84:87], v[112:115], v[176:179]\n"
      "buffer_load_dwordx4 v0, s[8:11], s46 offen offset:0 sc1 lds\n"
      "v_mfma_f32_16x16x32_bf16 v[176:179], a[88:91], v[116:119], v[176:179]\n"
      "v_mfma_f32_16x16x32_bf16 v[176:179], a[92:95], v[120:123], v[176:179]\n"
      "v_fmac_f32 v219, v173, v173\n"
      "v_mfma_f32_16x16x32_bf16 v[176:179], a[96:99], v[124:127], v[176:179]\n"
      "v_lshlrev_b32 v184, 16, v198\n"
      "v_and_b32 v185, 0xffff0000, v198\n"
      "v_mfma_f32_16x16x32_bf16 v[176:179], a[100:103], v[128:131], v[176:179]\n"
      "ds_read_b32 v22, v24\n"
      "v_fmac_f32 v219, v174, v174\n"
      "v_mfma_f32_16x16x32_bf16 v[176:179], a[104:107], v[132:135], v[176:179]\n"
      "buffer_load_dwordx4 v0, s[8:11], s46 offen offset:1024 sc1 lds\n"
      "v_mfma_f32_16x16x32_bf16 v[176:179], a[108:111], v[136:139], v[176:179]\n"
      "v_lshlrev_b32 v182, 16, v197\n"
      "v_add_f32 v218, v172, v173\n"
      "v_mfma_f32_16x16x32_bf16 v[176:179], a[112:115], v[140:143], v[176:179]\n"
      "v_add_f32 v218, v218, v174\n"
      "v_fmac_f32 v219, v175, v175\n"
      "v_mfma_f32_16x16x32_bf16 v[176:179], a[116:119], v[144:147], v[176:179]\n"
      "v_cvt_pk_bf16_f32 v208, v172, v173\n"
      "v_mfma_f32_16x16x32_bf16 v[176:179], a[120:123], v[148:151], v[176:179]\n"
      "s_waitcnt lgkmcnt(0)\n"
      "v_and_b32 v187, 0xffff0000, v199\n"
      "v_cmp_gt_i32 vcc, v21, v22\n"
      "v_mfma_f32_16x16x32_bf16 v[176:179], a[124:127], v[152:155], v[176:179]\n"
      "buffer_load_dwordx4 v0, s[8:11], s46 offen offset:3072 sc1 lds\n"
      "v_mfma_f32_16x16x32_bf16 v[176:179], a[128:131], v[156:159], v[176:179]\n"
      "v_lshlrev_b32 v180, 16, v196\n"
      "v_add_f32 v218, v218, v175\n"
      "v_mfma_f32_16x16x32_bf16 v[176:179], a[132:135], v[160:163], v[176:179]\n"
      "s_cbranch_vccnz .Lsp_wz1_8\n"
      ".Lbk_wz1_8:\n"
      "ds_read2st64_b32 v[204:205], v17 offset1:1\n"
      "v_mfma_f32_16x16x32_bf16 v[176:179], a[136:139], v[164:167], v[176:179]\n"
      "ds_read2st64_b32 v[206:207], v17 offset0:2 offset1:3\n"
      "v_add_u32 v21, 1, v21\n"
      "v_mfma_f32_16x16x32_bf16 v[176:179], a[140:143], v[168:171], v[176:179]\n"
      "s_waitcnt vmcnt(10)\n"
      "ds_write_b32 v23, v21\n"
      "v_mfma_f32_16x16x32_bf16 v[180:183], a[144:147], v[100:103], v[180:183]\n"
      "buffer_load_dwordx4 v0, s[8:11], s46 offen offset:2048 sc1 lds\n"
      "v_mfma_f32_16x16x32_bf16 v[180:183], a[148:151], v[104:107], v[180:183]\n"
      "v_mfma_f32_16x16x32_bf16 v[180:183], a[152:155], v[108:111], v[180:183]\n"
      "s_add_u32 m0, s40, 61440\n"
      "v_add_f32 v218, v218, v176\n"
      "v_mfma_f32_16x16x32_bf16 v[180:183], a[156:159], v[112:115], v[180:183]\n"
      "v_fmac_f32 v219, v176, v176\n"
      "v_cvt_pk_bf16_f32 v209, v174, v175\n"
      "v_mfma_f32_16x16x32_bf16 v[180:183], a[160:163], v[116:119], v[180:183]\n"
      "buffer_load_dwordx4 v[196:199], v13, s[8:11], s47 offen offset:1024 sc1\n"
      "v_mfma_f32_16x16x32_bf16 v[180:183], a[164:167], v[120:123], v[180:183]\n"
      "s_waitcnt lgkmcnt(1)\n"
      "v_cvt_pk_bf16_f32 v211, v178, v179\n"
      "v_add_f32 v190, v190, v206\n"
      "v_mfma_f32_16x16x32_bf16 v[180:183], a[168:171], v[124:127], v[180:183]\n"
      "v_add_f32 v218, v218, v177\n"
      "v_add_f32 v218, v218, v178\n"
      "v_mfma_f32_16x16x32_bf16 v[180:183], a[172:175], v[128:131], v[180:183]\n"
      "v_add_f32 v218, v218, v179\n"
      "v_add_f32 v189, v189, v205\n"
      "v_mfma_f32_16x16x32_bf16 v[180:183], a[176:179], v[132:135], v[180:183]\n"
      "buffer_load_dwordx4 v1, s[8:11], s46 offen offset:0 sc1 lds\n"
      "v_mfma_f32_16x16x32_bf16 v[180:183], a[180:183], v[136:139], v[180:183]\n"
      "v_add_f32 v188, v188, v204\n"
      "v_fmac_f32 v219, v177, v177\n"
      "v_mfma_f32_16x16x32_bf16 v[180:183], a[184:187], v[140:143], v[180:183]\n"
      "ds_read_b32 v22, v24\n"
      "v_fmac_f32 v219, v178, v178\n"
      "v_mfma_f32_16x16x32_bf16 v[180:183], a[188:191], v[144:147], v[180:183]\n"
      "v_cvt_pk_bf16_f32 v210, v176, v177\n"
      "v_fmac_f32 v219, v179, v179\n"
      "v_mfma_f32_16x16x32_bf16 v[180:183], a[192:195], v[148:151], v[180:183]\n"
      "v_add_f32 v191, v191, v207\n"
      "v_fmac_f32 v219, v188, v188\n"
      "v_mfma_f32_16x16x32_bf16 v[180:183], a[196:199], v[152:155], v[180:183]\n"
      "v_add_f32 v218, v218, v188\n"
      "v_mfma_f32_16x16x32_bf16 v[180:183], a[200:203], v[156:159], v[180:183]\n"
      "v_add_f32 v218, v218, v189\n"
      "v_add_f32 v218, v218, v190\n"
      "v_mfma_f32_16x16x32_bf16 v[180:183], a[204:207], v[160:163], v[180:183]\n"
      "s_waitcnt lgkmcnt(0)\n"
      "v_fmac_f32 v219, v189, v189\n"
      "v_cmp_gt_i32 vcc, v21, v22\n"
      "v_mfma_f32_16x16x32_bf16 v[180:183], a[208:211], v[164:167], v[180:183]\n"
      "v_cvt_pk_bf16_f32 v216, v188, v189\n"
      "v_fmac_f32 v219, v190, v190\n"
      "v_mfma_f32_16x16x32_bf16 v[180:183], a[212:215], v[168:171], v[180:183]\n"
      "s_waitcnt vmcnt(11)\n"
      "v_and_b32 v189, 0xffff0000, v200\n"
      "v_fmac_f32 v219, v191, v191\n"
      "v_mfma_f32_16x16x32_bf16 v[184:187], a[216:219], v[100:103], v[184:187]\n"
      "s_add_u32 s38, s38, 24576\n"
      "v_cvt_pk_bf16_f32 v217, v190, v191\n"
      "v_lshlrev_b32_e64 v188, 16, v200\n"
      "v_mfma_f32_16x16x32_bf16 v[184:187], a[220:223], v[104:107], v[184:187]\n"
      "s_cbranch_vccnz .Lsp_wy0_9\n"
      ".Lbk_wy0_9:\n"
      "v_mfma_f32_16x16x32_bf16 v[184:187], a[224:227], v[108:111], v[184:187]\n"
      "ds_read_b128 v[104:107], v10 offset:1024\n"
      "v_add_f32 v218, v218, v191\n"
      "v_mfma_f32_16x16x32_bf16 v[184:187], a[228:231], v[112:115], v[184:187]\n"
      "v_fmac_f32 v219, v180, v180\n"
      "v_lshlrev_b32 v190, 16, v201\n"
      "v_mfma_f32_16x16x32_bf16 v[184:187], a[232:235], v[116:119], v[184:187]\n"
      "ds_read_b128 v[100:103], v10 offset:0\n"
      "v_add_f32 v218, v218, v180\n"
      "v_mfma_f32_16x16x32_bf16 v[184:187], a[236:239], v[120:123], v[184:187]\n"
      "s_waitcnt vmcnt(9)\n"
      "ds_read_b128 v[120:123], v10 offset:5120\n"
      "v_lshlrev_b32 v178, 16, v195\n"
      "v_mfma_f32_16x16x32_bf16 v[184:187], a[240:243], v[124:127], v[184:187]\n"
      "ds_read_b128 v[112:115], v10 offset:3072\n"
      "v_fmac_f32 v219, v181, v181\n"
      "v_mfma_f32_16x16x32_bf16 v[184:187], a[244:247], v[128:131], v[184:187]\n"
      "ds_read_b128 v[116:119], v10 offset:4096\n"
      "v_lshlrev_b32 v174, 16, v193\n"
      "v_mfma_f32_16x16x32_bf16 v[184:187], a[248:251], v[132:135], v[184:187]\n"
      "ds_read_b128 v[124:127], v10 offset:6144\n"
      "v_fmac_f32 v219, v182, v182\n"
      "v_mfma_f32_16x16x32_bf16 v[184:187], a[252:255], v[136:139], v[184:187]\n"
      "s_sub_u32 s47, s46, s45\n"
      "ds_read_b128 v[108:111], v10 offset:2048\n"
      "v_mfma_f32_16x16x32_bf16 v[184:187], v[32:35], v[140:143], v[184:187]\n"
      "v_and_b32 v191, 0xffff0000, v201\n"
      "v_and_b32 v177, 0xffff0000, v194\n"
      "v_mfma_f32_16x16x32_bf16 v[184:187], v[36:39], v[144:147], v[184:187]\n"
      "ds_read_b128 v[136:139], v10 offset:8192\n"
      "v_lshlrev_b32 v176, 16, v194\n"
      "v_mfma_f32_16x16x32_bf16 v[184:187], v[40:43], v[148:151], v[184:187]\n"
      "ds_read_b128 v[128:131], v10 offset:7168\n"
      "v_lshlrev_b32 v172, 16, v192\n"
      "v_mfma_f32_16x16x32_bf16 v[184:187], v[44:47], v[152:155], v[184:187]\n"
      "v_fmac_f32 v219, v183, v183\n"
      "v_and_b32 v175, 0xffff0000, v193\n"
      "v_mfma_f32_16x16x32_bf16 v[184:187], v[48:51], v[156:159], v[184:187]\n"
      "buffer_load_dwordx2 v[200:201], v14, s[8:11], s47 offen sc1\n"
      "v_mfma_f32_16x16x32_bf16 v[184:187], v[52:55], v[160:163], v[184:187]\n"
      "ds_read2_b32 v[132:133], v6 offset0:0 offset1:32\n"
      "v_and_b32 v173, 0xffff0000, v192\n"
      "v_mfma_f32_16x16x32_bf16 v[184:187], v[56:59], v[164:167], v[184:187]\n"
      "s_waitcnt lgkmcnt(3)\n"
      "ds_read2_b32 v[134:135], v6 offset0:64 offset1:96\n"
      "v_add_f32 v218, v218, v181\n"
      "v_mfma_f32_16x16x32_bf16 v[184:187], v[60:63], v[168:171], v[184:187]\n"
      "s_sub_u32 s20, s20, 1\n"
      "s_cbranch_scc1 .Lw_tail\n"
      "v_mfma_f32_16x16x32_bf16 v[188:191], v[64:67], v[100:103], v[188:191]\n"
      "ds_read_b128 v[140:143], v10 offset:9216\n"
      "v_and_b32 v179, 0xffff0000, v195\n"
      "v_mfma_f32_16x16x32_bf16 v[188:191], v[68:71], v[104:107], v[188:191]\n"
      "buffer_load_dwordx4 v[192:195], v13, s[8:11], s47 offen sc1\n"
      "v_mfma_f32_16x16x32_bf16 v[188:191], v[72:75], v[108:111], v[188:191]\n"
      "v_add_f32 v218, v218, v182\n"
      "v_mfma_f32_16x16x32_bf16 v[188:191], v[76:79], v[112:115], v[188:191]\n"
      "ds_read_b128 v[156:159], v10 offset:13312\n"
      "v_fmac_f32 v219, v184, v184\n"
      "v_mfma_f32_16x16x32_bf16 v[188:191], v[80:83], v[116:119], v[188:191]\n"
      "ds_read_b128 v[164:167], v10 offset:15360\n"
      "v_cvt_pk_bf16_f32 v213, v182, v183\n"
      "v_mfma_f32_16x16x32_bf16 v[188:191], v[84:87], v[120:123], v[188:191]\n"
      "ds_read_b128 v[144:147], v10 offset:10240\n"
      "v_mfma_f32_16x16x32_bf16 v[188:191], v[88:91], v[124:127], v[188:191]\n"
      "s_waitcnt lgkmcnt(4)\n"
      "v_cvt_pk_bf16_f32 v215, v186, v187\n"
      "v_add_f32 v218, v218, v183\n"
      "v_mfma_f32_16x16x32_bf16 v[188:191], v[92:95], v[128:131], v[188:191]\n"
      "buffer_store_dwordx4 v[208:211], v15, s[8:11], s38 offen sc1\n"
      "v_mfma_f32_16x16x32_bf16 v[188:191], v[96:99], v[132:135], v[188:191]\n"
      "v_cvt_pk_bf16_f32 v212, v180, v181\n"
      "v_add_f32 v218, v218, v184\n"
      "v_mfma_f32_16x16x32_bf16 v[172:175], a[0:3], v[100:103], v[172:175]\n"
      "s_add_u32 s50, s50, 24576\n"
      "v_cvt_pk_bf16_f32 v214, v184, v185\n"
      "v_add_f32 v218, v218, v185\n"
      "v_mfma_f32_16x16x32_bf16 v[172:175], a[4:7], v[104:107], v[172:175]\n"
      "s_mov_b32 s46, s36\n"
      "v_add_f32 v218, v218, v186\n"
      "v_mfma_f32_16x16x32_bf16 v[172:175], a[8:11], v[108:111], v[172:175]\n"
      "ds_read_b128 v[148:151], v10 offset:11264\n"
      "v_add_f32 v218, v218, v187\n"
      "v_mfma_f32_16x16x32_bf16 v[172:175], a[12:15], v[112:115], v[172:175]\n"
      "v_fmac_f32 v219, v185, v185\n"
      "v_add_u32 v21, 1, v21\n"
      "v_mfma_f32_16x16x32_bf16 v[172:175], a[16:19], v[116:119], v[172:175]\n"
      "s_add_u32 s36, s36, 24576\n"
      "ds_read_b128 v[152:155], v10 offset:12288\n"
      "v_fmac_f32 v219, v186, v186\n"
      "v_mfma_f32_16x16x32_bf16 v[172:175], a[20:23], v[120:123], v[172:175]\n"
      "buffer_store_dwordx2 v[216:217], v16, s[8:11], s38 offen sc1\n"
      "v_mfma_f32_16x16x32_bf16 v[172:175], a[24:27], v[124:127], v[172:175]\n"
      "ds_read_b128 v[160:163], v10 offset:14336\n"
      "v_mfma_f32_16x16x32_bf16 v[172:175], a[28:31], v[128:131], v[172:175]\n"
      "ds_read2_b32 v[170:171], v7 offset0:64 offset1:96\n"
      "v_mfma_f32_16x16x32_bf16 v[172:175], a[32:35], v[132:135], v[172:175]\n"
      "ds_read2_b32 v[168:169], v7 offset0:0 offset1:32\n"
      "s_min_u32 s36, s36, s6\n"
      "v_mfma_f32_16x16x32_bf16 v[172:175], a[36:39], v[136:139], v[172:175]\n"
      "s_waitcnt lgkmcnt(4)\n"
      "v_mfma_f32_16x16x32_bf16 v[172:175], a[40:43], v[140:143], v[172:175]\n"
      "buffer_store_dwordx4 v[212:215], v15, s[8:11], s38 offen offset:1024 sc1\n"
      "v_mfma_f32_16x16x32_bf16 v[172:175], a[44:47], v[144:147], v[172:175]\n"
      "ds_write_b32 v18, v218\n"
      "v_mfma_f32_16x16x32_bf16 v[172:175], a[48:51], v[148:151], v[172:175]\n"
      "s_waitcnt vmcnt(11) lgkmcnt(4)\n"
      "v_fmac_f32 v219, v187, v187\n"
      "v_cmp_ne_u32 vcc, s21, v28\n"
      "v_mfma_f32_16x16x32_bf16 v[172:175], a[52:55], v[152:155], v[172:175]\n"
      "ds_write_b32 v18, v219 offset:4\n"
      "v_mfma_f32_16x16x32_bf16 v[172:175], a[56:59], v[156:159], v[172:175]\n"
      "s_waitcnt lgkmcnt(3)\n"
      "s_add_u32 m0, s39, 0\n"
      "v_mfma_f32_16x16x32_bf16 v[172:175], a[60:63], v[160:163], v[172:175]\n"
      "ds_write_b32 v23, v21\n"
      "v_mfma_f32_16x16x32_bf16 v[172:175], a[64:67], v[164:167], v[172:175]\n"
      "s_waitcnt lgkmcnt(3)\n"
      "s_cbranch_vccnz .Lsp_w0_10\n"
      ".Lbk_w0_10:\n"
      "v_mfma_f32_16x16x32_bf16 v[172:175], a[68:71], v[168:171], v[172:175]\n"
      "v_mfma_f32_16x16x32_bf16 v[176:179], a[72:75], v[100:103], v[176:179]\n"
      "buffer_load_dword v28, v11, s[8:11], s36 offen sc1\n"
      "v_mfma_f32_16x16x32_bf16 v[176:179], a[76:79], v[104:107], v[176:179]\n"
      "s_waitcnt vmcnt(7) lgkmcnt(2)\n"
      "v_lshlrev_b32 v186, 16, v199\n"
      "v_lshlrev_b32 v184, 16, v198\n"
      "v_mfma_f32_16x16x32_bf16 v[176:179], a[80:83], v[108:111], v[176:179]\n"
      "v_lshlrev_b32 v180, 16, v196\n"
      "v_and_b32 v183, 0xffff0000, v197\n"
      "v_mfma_f32_16x16x32_bf16 v[176:179], a[84:87], v[112:115], v[176:179]\n"
      "buffer_store_dword v25, v12, s[8:11], s50 offen sc1\n"
      "v_mfma_f32_16x16x32_bf16 v[176:179], a[88:91], v[116:119], v[176:179]\n"
      "v_lshlrev_b32 v182, 16, v197\n"
      "v_and_b32 v181, 0xffff0000, v196\n"
      "v_mfma_f32_16x16x32_bf16 v[176:179], a[92:95], v[120:123], v[176:179]\n"
      "v_and_b32 v187, 0xffff0000, v199\n"
      "v_and_b32 v185, 0xffff0000, v198\n"
      "v_mfma_f32_16x16x32_bf16 v[176:179], a[96:99], v[124:127], v[176:179]\n"
      "buffer_load_dwordx4 v[196:199], v13, s[8:11], s47 offen offset:1024 sc1\n"
      "v_mfma_f32_16x16x32_bf16 v[176:179], a[100:103], v[128:131], v[176:179]\n"
      "ds_read_b32 v22, v24\n"
      "v_mul_f32 v219, v172, v172\n"
      "v_mfma_f32_16x16x32_bf16 v[176:179], a[104:107], v[132:135], v[176:179]\n"
      "v_mfma_f32_16x16x32_bf16 v[176:179], a[108:111], v[136:139], v[176:179]\n"
      "v_mfma_f32_16x16x32_bf16 v[176:179], a[112:115], v[140:143], v[176:179]\n"
      "buffer_load_dwordx4 v0, s[8:11], s46 offen offset:0 sc1 lds\n"
      "v_mfma_f32_16x16x32_bf16 v[176:179], a[116:119], v[144:147], v[176:179]\n"
      "v_add_f32 v218, v172, v173\n"
      "v_fmac_f32 v219, v173, v173\n"
      "v_mfma_f32_16x16x32_bf16 v[176:179], a[120:123], v[148:151], v[176:179]\n"
      "s_waitcnt lgkmcnt(0)\n"
      "v_cmp_gt_i32 vcc, v21, v22\n"
      "v_mfma_f32_16x16x32_bf16 v[176:179], a[124:127], v[152:155], v[176:179]\n"
      "v_mfma_f32_16x16x32_bf16 v[176:179], a[128:131], v[156:159], v[176:179]\n"
      "buffer_load_dwordx4 v0, s[8:11], s46 offen offset:1024 sc1 lds\n"
      "v_mfma_f32_16x16x32_bf16 v[176:179], a[132:135], v[160:163], v[176:179]\n"
      "s_cbranch_vccnz .Lsp_wz0_11\n"
      ".Lbk_wz0_11:\n"
      "ds_read2st64_b32 v[204:205], v17 offset1:1\n"
      "v_mfma_f32_16x16x32_bf16 v[176:179], a[136:139], v[164:167], v[176:179]\n"
      "ds_read2st64_b32 v[206:207], v17 offset0:2 offset1:3\n"
      "v_add_u32 v21, 1, v21\n"
      "v_mfma_f32_16x16x32_bf16 v[176:179], a[140:143], v[168:171], v[176:179]\n"
      "s_waitcnt vmcnt(10)\n"
      "ds_write_b32 v23, v21\n"
      "v_mfma_f32_16x16x32_bf16 v[180:183], a[144:147], v[100:103], v[180:183]\n"
      "buffer_load_dwordx4 v0, s[8:11], s46 offen offset:3072 sc1 lds\n"
      "v_mfma_f32_16x16x32_bf16 v[180:183], a[148:151], v[104:107], v[180:183]\n"
      "v_add_f32 v218, v218, v174\n"
      "v_fmac_f32 v219, v174, v174\n"
      "v_mfma_f32_16x16x32_bf16 v[180:183], a[152:155], v[108:111], v[180:183]\n"
      "v_fmac_f32 v219, v175, v175\n"
      "v_fmac_f32 v219, v176, v176\n"
      "v_mfma_f32_16x16x32_bf16 v[180:183], a[156:159], v[112:115], v[180:183]\n"
      "v_add_f32 v218, v218, v175\n"
      "v_add_f32 v218, v218, v176\n"
      "v_mfma_f32_16x16x32_bf16 v[180:183], a[160:163], v[116:119], v[180:183]\n"
      "buffer_load_dwordx4 v0, s[8:11], s46 offen offset:2048 sc1 lds\n"
      "v_mfma_f32_16x16x32_bf16 v[180:183], a[164:167], v[120:123], v[180:183]\n"
      "s_add_u32 m0, s40, 0\n"
      "v_add_f32 v218, v218, v177\n"
      "v_cvt_pk_bf16_f32 v209, v174, v175\n"
      "v_mfma_f32_16x16x32_bf16 v[180:183], a[168:171], v[124:127], v[180:183]\n"
      "s_waitcnt lgkmcnt(0)\n"
      "v_add_f32 v188, v188, v204\n"
      "v_add_f32 v190, v190, v206\n"
      "v_mfma_f32_16x16x32_bf16 v[180:183], a[172:175], v[128:131], v[180:183]\n"
      "v_cvt_pk_bf16_f32 v208, v172, v173\n"
      "v_fmac_f32 v219, v177, v177\n"
      "v_mfma_f32_16x16x32_bf16 v[180:183], a[176:179], v[132:135], v[180:183]\n"
      "buffer_load_dwordx4 v1, s[8:11], s46 offen offset:0 sc1 lds\n"
      "v_mfma_f32_16x16x32_bf16 v[180:183], a[180:183], v[136:139], v[180:183]\n"
      "s_add_u32 s38, s38, 24576\n"
      "v_cvt_pk_bf16_f32 v210, v176, v177\n"
      "v_mfma_f32_16x16x32_bf16 v[180:183], a[184:187], v[140:143], v[180:183]\n"
      "ds_read_b32 v22, v24\n"
      "v_add_f32 v218, v218, v178\n"
      "v_mfma_f32_16x16x32_bf16 v[180:183], a[188:191], v[144:147], v[180:183]\n"
      "v_add_f32 v218, v218, v179\n"
      "v_add_f32 v218, v218, v188\n"
      "v_mfma_f32_16x16x32_bf16 v[180:183], a[192:195], v[148:151], v[180:183]\n"
      "s_add_u32 s50, s50, 24576\n"
      "v_fmac_f32 v219, v178, v178\n"
      "v_fmac_f32 v219, v179, v179\n"
      "v_mfma_f32_16x16x32_bf16 v[180:183], a[196:199], v[152:155], v[180:183]\n"
      "v_add_f32 v191, v191, v207\n"
      "v_cvt_pk_bf16_f32 v211, v178, v179\n"
      "v_mfma_f32_16x16x32_bf16 v[180:183], a[200:203], v[156:159], v[180:183]\n"
      "v_fmac_f32 v219, v188, v188\n"
      "v_add_f32 v189, v189, v205\n"
      "v_mfma_f32_16x16x32_bf16 v[180:183], a[204:207], v[160:163], v[180:183]\n"
      "s_waitcnt lgkmcnt(0)\n"
      "v_fmac_f32 v219, v189, v189\n"
      "v_cmp_gt_i32 vcc, v21, v22\n"
      "v_mfma_f32_16x16x32_bf16 v[180:183], a[208:211], v[164:167], v[180:183]\n"
      ".Lw_p1:\n"
      "s_cbranch_execz .Lw_pr\n"
      "v_fmac_f32 v219, v190, v190\n"
      "v_add_f32 v218, v218, v189\n"
      "v_mfma_f32_16x16x32_bf16 v[180:183], a[212:215], v[168:171], v[180:183]\n"
      "s_waitcnt vmcnt(11)\n"
      "v_lshlrev_b32 v172, 16, v192\n"
      "v_mfma_f32_16x16x32_bf16 v[184:187], a[216:219], v[100:103], v[184:187]\n"
      "v_and_b32 v175, 0xffff0000, v193\n"
      "v_and_b32 v177, 0xffff0000, v194\n"
      "v_mfma_f32_16x16x32_bf16 v[184:187], a[220:223], v[104:107], v[184:187]\n"
      "s_cbranch_vccnz .Lsp_wy1_12\n"
      ".Lbk_wy1_12:\n"
      "v_add_f32 v218, v218, v190\n"
      "v_mfma_f32_16x16x32_bf16 v[184:187], a[224:227], v[108:111], v[184:187]\n"
      "ds_read_b128 v[104:107], v10 offset:21504\n"
      "v_cvt_pk_bf16_f32 v216, v188, v189\n"
      "v_mfma_f32_16x16x32_bf16 v[184:187], a[228:231], v[112:115], v[184:187]\n"
      "v_add_f32 v218, v218, v191\n"
      "v_add_f32 v218, v218, v180\n"
      "v_mfma_f32_16x16x32_bf16 v[184:187], a[232:235], v[116:119], v[184:187]\n"
      "ds_read_b128 v[116:119], v10 offset:24576\n"
      "v_cvt_pk_bf16_f32 v217, v190, v191\n"
      "v_mfma_f32_16x16x32_bf16 v[184:187], a[236:239], v[120:123], v[184:187]\n"
      "ds_read_b128 v[108:111], v10 offset:22528\n"
      "v_lshlrev_b32 v188, 16, v200\n"
      "v_mfma_f32_16x16x32_bf16 v[184:187], a[240:243], v[124:127], v[184:187]\n"
      "ds_read_b128 v[100:103], v10 offset:20480\n"
      "v_fmac_f32 v219, v191, v191\n"
      "v_mfma_f32_16x16x32_bf16 v[184:187], a[244:247], v[128:131], v[184:187]\n"
      "v_lshlrev_b32 v190, 16, v201\n"
      "v_and_b32 v191, 0xffff0000, v201\n"
      "v_mfma_f32_16x16x32_bf16 v[184:187], a[248:251], v[132:135], v[184:187]\n"
      "ds_read2_b32 v[134:135], v8 offset0:64 offset1:96\n"
      "v_lshlrev_b32 v174, 16, v193\n"
      "v_mfma_f32_16x16x32_bf16 v[184:187], a[252:255], v[136:139], v[184:187]\n"
      "s_sub_u32 s47, s46, s45\n"
      "v_and_b32 v189, 0xffff0000, v200\n"
      "v_fmac_f32 v219, v180, v180\n"
      "v_mfma_f32_16x16x32_bf16 v[184:187], v[32:35], v[140:143], v[184:187]\n"
      "buffer_store_dwordx4 v[208:211], v15, s[8:11], s38 offen sc1\n"
      "v_mfma_f32_16x16x32_bf16 v[184:187], v[36:39], v[144:147], v[184:187]\n"
      "ds_read_b128 v[136:139], v10 offset:28672\n"
      "v_fmac_f32 v219, v181, v181\n"
      "v_mfma_f32_16x16x32_bf16 v[184:187], v[40:43], v[148:151], v[184:187]\n"
      "ds_read2_b32 v[132:133], v8 offset0:0 offset1:32\n"
      "v_lshlrev_b32 v178, 16, v195\n"
      "v_mfma_f32_16x16x32_bf16 v[184:187], v[44:47], v[152:155], v[184:187]\n"
      "ds_read_b128 v[112:115], v10 offset:23552\n"
      "v_add_f32 v218, v218, v181\n"
      "v_mfma_f32_16x16x32_bf16 v[184:187], v[48:51], v[156:159], v[184:187]\n"
      "ds_read_b128 v[120:123], v10 offset:25600\n"
      "v_and_b32 v173, 0xffff0000, v192\n"
      "v_mfma_f32_16x16x32_bf16 v[184:187], v[52:55], v[160:163], v[184:187]\n"
      "ds_read_b128 v[124:127], v10 offset:26624\n"
      "v_lshlrev_b32 v176, 16, v194\n"
      "v_mfma_f32_16x16x32_bf16 v[184:187], v[56:59], v[164:167], v[184:187]\n"
      "buffer_load_dwordx2 v[200:201], v14, s[8:11], s47 offen sc1\n"
      "v_mfma_f32_16x16x32_bf16 v[184:187], v[60:63], v[168:171], v[184:187]\n"
      "s_waitcnt lgkmcnt(5)\n"
      "ds_read_b128 v[128:131], v10 offset:27648\n"
      "v_add_f32 v218, v218, v182\n"
      "v_mfma_f32_16x16x32_bf16 v[188:191], v[64:67], v[100:103], v[188:191]\n"
      "ds_read_b128 v[140:143], v10 offset:29696\n"
      "v_cvt_pk_bf16_f32 v213, v182, v183\n"
      "v_mfma_f32_16x16x32_bf16 v[188:191], v[68:71], v[104:107], v[188:191]\n"
      "ds_read_b128 v[152:155], v10 offset:32768\n"
      "v_mfma_f32_16x16x32_bf16 v[188:191], v[72:75], v[108:111], v[188:191]\n"
      "s_waitcnt lgkmcnt(5)\n"
      "buffer_store_dwordx2 v[216:217], v16, s[8:11], s38 offen sc1\n"
      "v_mfma_f32_16x16x32_bf16 v[188:191], v[76:79], v[112:115], v[188:191]\n"
      "ds_read_b128 v[144:147], v10 offset:30720\n"
      "v_cvt_pk_bf16_f32 v212, v180, v181\n"
      "v_mfma_f32_16x16x32_bf16 v[188:191], v[80:83], v[116:119], v[188:191]\n"
      "s_waitcnt lgkmcnt(4)\n"
      "ds_read_b128 v[164:167], v10 offset:35840\n"
      "v_add_f32 v218, v218, v183\n"
      "v_mfma_f32_16x16x32_bf16 v[188:191], v[84:87], v[120:123], v[188:191]\n"
      "v_fmac_f32 v219, v182, v182\n"
      "v_add_f32 v218, v218, v184\n"
      "v_mfma_f32_16x16x32_bf16 v[188:191], v[88:91], v[124:127], v[188:191]\n"
      "s_waitcnt lgkmcnt(4)\n"
      "ds_read_b128 v[148:151], v10 offset:31744\n"
      "v_add_f32 v218, v218, v185\n"
      "v_mfma_f32_16x16x32_bf16 v[188:191], v[92:95], v[128:131], v[188:191]\n"
      "ds_read_b128 v[160:163], v10 offset:34816\n"
      "v_and_b32 v179, 0xffff0000, v195\n"
      "v_mfma_f32_16x16x32_bf16 v[188:191], v[96:99], v[132:135], v[188:191]\n"
      "buffer_load_dwordx4 v[192:195], v13, s[8:11], s47 offen sc1\n"
      "v_mfma_f32_16x16x32_bf16 v[172:175], a[0:3], v[100:103], v[172:175]\n"
      "v_cvt_pk_bf16_f32 v215, v186, v187\n"
      "v_fmac_f32 v219, v183, v183\n"
      "v_mfma_f32_16x16x32_bf16 v[172:175], a[4:7], v[104:107], v[172:175]\n"
      "v_cvt_pk_bf16_f32 v214, v184, v185\n"
      "v_mfma_f32_16x16x32_bf16 v[172:175], a[8:11], v[108:111], v[172:175]\n"
      "ds_read2_b32 v[168:169], v9 offset0:0 offset1:32\n"
      "v_mfma_f32_16x16x32_bf16 v[172:175], a[12:15], v[112:115], v[172:175]\n"
      "ds_read_b128 v[156:159], v10 offset:33792\n"
      "v_mfma_f32_16x16x32_bf16 v[172:175], a[16:19], v[116:119], v[172:175]\n"
      "buffer_store_dwordx4 v[212:215], v15, s[8:11], s38 offen offset:1024 sc1\n"
      "v_mfma_f32_16x16x32_bf16 v[172:175], a[20:23], v[120:123], v[172:175]\n"
      "v_fmac_f32 v219, v184, v184\n"
      "v_fmac_f32 v219, v185, v185\n"
      "v_mfma_f32_16x16x32_bf16 v[172:175], a[24:27], v[124:127], v[172:175]\n"
      "ds_read2_b32 v[170:171], v9 offset0:64 offset1:96\n"
      "v_add_f32 v218, v218, v186\n"
      "v_mfma_f32_16x16x32_bf16 v[172:175], a[28:31], v[128:131], v[172:175]\n"
      "v_fmac_f32 v219, v186, v186\n"
      "v_add_f32 v218, v218, v187\n"
      "v_mfma_f32_16x16x32_bf16 v[172:175], a[32:35], v[132:135], v[172:175]\n"
      "ds_write_b32 v18, v218\n"
      "v_mfma_f32_16x16x32_bf16 v[172:175], a[36:39], v[136:139], v[172:175]\n"
      "s_waitcnt lgkmcnt(3)\n"
      "s_add_u32 m0, s39, 20480\n"
      "v_add_u32 v21, 1, v21\n"
      "v_mfma_f32_16x16x32_bf16 v[172:175], a[40:43], v[140:143], v[172:175]\n"
      "s_mov_b32 s46, s36\n"
      "v_mfma_f32_16x16x32_bf16 v[172:175], a[44:47], v[144:147], v[172:175]\n"
      "v_fmac_f32 v219, v187, v187\n"
      "v_mfma_f32_16x16x32_bf16 v[172:175], a[48:51], v[148:151], v[172:175]\n"
      "ds_write_b32 v18, v219 offset:4\n"
      "v_mfma_f32_16x16x32_bf16 v[172:175], a[52:55], v[152:155], v[172:175]\n"
      "s_waitcnt vmcnt(10) lgkmcnt(2)\n"
      "v_and_b32 v181, 0xffff0000, v196\n"
      "v_mfma_f32_16x16x32_bf16 v[172:175], a[56:59], v[156:159], v[172:175]\n"
      "ds_write_b32 v23, v21\n"
      "v_mfma_f32_16x16x32_bf16 v[172:175], a[60:63], v[160:163], v[172:175]\n"
      "v_lshlrev_b32 v186, 16, v199\n"
      "v_cmp_ne_u32 vcc, s21, v28\n"
      "v_mfma_f32_16x16x32_bf16 v[172:175], a[64:67], v[164:167], v[172:175]\n"
      "buffer_store_dword v25, v12, s[8:11], s50 offen sc1\n"
      "v_mfma_f32_16x16x32_bf16 v[172:175], a[68:71], v[168:171], v[172:175]\n"
      "s_cmp_eq_u32 s36, s37\n"
      "s_cselect_b32 s48, 49152, s51\n"
      "s_add_u32 s36, s36, s48\n"
      "v_mfma_f32_16x16x32_bf16 v[176:179], a[72:75], v[100:103], v[176:179]\n"
      "s_cbranch_vccnz .Lsp_w1_13\n"
      ".Lbk_w1_13:\n"
      "s_min_u32 s36, s36, s6\n"
      "v_mfma_f32_16x16x32_bf16 v[176:179], a[76:79], v[104:107], v[176:179]\n"
      "buffer_load_dword v28, v11, s[8:11], s36 offen sc1\n"
      "v_mfma_f32_16x16x32_bf16 v[176:179], a[80:83], v[108:111], v[176:179]\n"
      "v_and_b32 v183, 0xffff0000, v197\n"
      "v_mul_f32 v219, v172, v172\n"
      "v_mfma_f32_16x16x32_bf16 v[176:179], a[84:87], v[112:115], v[176:179]\n"
      "buffer_load_dwordx4 v0, s[8:11], s46 offen offset:0 sc1 lds\n"
      "v_mfma_f32_16x16x32_bf16 v[176:179], a[88:91], v[116:119], v[176:179]\n"
      "v_mfma_f32_16x16x32_bf16 v[176:179], a[92:95], v[120:123], v[176:179]\n"
      "v_fmac_f32 v219, v173, v173\n"
      "v_mfma_f32_16x16x32_bf16 v[176:179], a[96:99], v[124:127], v[176:179]\n"
      "v_lshlrev_b32 v184, 16, v198\n"
      "v_and_b32 v185, 0xffff0000, v198\n"
      "v_mfma_f32_16x16x32_bf16 v[176:179], a[100:103], v[128:131], v[176:179]\n"
      "ds_read_b32 v22, v24\n"
      "v_fmac_f32 v219, v174, v174\n"
      "v_mfma_f32_16x16x32_bf16 v[176:179], a[104:107], v[132:135], v[176:179]\n"
      "buffer_load_dwordx4 v0, s[8:11], s46 offen offset:1024 sc1 lds\n"
      "v_mfma_f32_16x16x32_bf16 v[176:179], a[108:111], v[136:139], v[176:179]\n"
      "v_lshlrev_b32 v182, 16, v197\n"
      "v_add_f32 v218, v172, v173\n"
      "v_mfma_f32_16x16x32_bf16 v[176:179], a[112:115], v[140:143], v[176:179]\n"
      "v_add_f32 v218, v218, v174\n"
      "v_fmac_f32 v219, v175, v175\n"
      "v_mfma_f32_16x16x32_bf16 v[176:179], a[116:119], v[144:147], v[176:179]\n"
      "v_cvt_pk_bf16_f32 v208, v172, v173\n"
      "v_mfma_f32_16x16x32_bf16 v[176:179], a[120:123], v[148:151], v[176:179]\n"
      "s_waitcnt lgkmcnt(0)\n"
      "v_and_b32 v187, 0xffff0000, v199\n"
      "v_cmp_gt_i32 vcc, v21, v22\n"
      "v_mfma_f32_16x16x32_bf16 v[176:179], a[124:127], v[152:155], v[176:179]\n"
      "buffer_load_dwordx4 v0, s[8:11], s46 offen offset:3072 sc1 lds\n"
      "v_mfma_f32_16x16x32_bf16 v[176:179], a[128:131], v[156:159], v[176:179]\n"
      "v_lshlrev_b32 v180, 16, v196\n"
      "v_add_f32 v218, v218, v175\n"
      "v_mfma_f32_16x16x32_bf16 v[176:179], a[132:135], v[160:163], v[176:179]\n"
      "s_cbranch_vccnz .Lsp_wz1_14\n"
      ".Lbk_wz1_14:\n"
      "ds_read2st64_b32 v[204:205], v17 offset1:1\n"
      "v_mfma_f32_16x16x32_bf16 v[176:179], a[136:139], v[164:167], v[176:179]\n"
      "ds_read2st64_b32 v[206:207], v17 offset0:2 offset1:3\n"
      "v_add_u32 v21, 1, v21\n"
      "v_mfma_f32_16x16x32_bf16 v[176:179], a[140:143], v[168:171], v[176:179]\n"
      "s_waitcnt vmcnt(10)\n"
      "ds_write_b32 v23, v21\n"
      "v_mfma_f32_16x16x32_bf16 v[180:183], a[144:147], v[100:103], v[180:183]\n"
      "buffer_load_dwordx4 v0, s[8:11], s46 offen offset:2048 sc1 lds\n"
      "v_mfma_f32_16x16x32_bf16 v[180:183], a[148:151], v[104:107], v[180:183]\n"
      "v_mfma_f32_16x16x32_bf16 v[180:183], a[152:155], v[108:111], v[180:183]\n"
      "s_add_u32 m0, s40, 20480\n"
      "v_add_f32 v218, v218, v176\n"
      "v_mfma_f32_16x16x32_bf16 v[180:183], a[156:159], v[112:115], v[180:183]\n"
      "v_fmac_f32 v219, v176, v176\n"
      "v_cvt_pk_bf16_f32 v209, v174, v175\n"
      "v_mfma_f32_16x16x32_bf16 v[180:183], a[160:163], v[116:119], v[180:183]\n"
      "buffer_load_dwordx4 v[196:199], v13, s[8:11], s47 offen offset:1024 sc1\n"
      "v_mfma_f32_16x16x32_bf16 v[180:183], a[164:167], v[120:123], v[180:183]\n"
      "s_waitcnt lgkmcnt(1)\n"
      "v_cvt_pk_bf16_f32 v211, v178, v179\n"
      "v_add_f32 v190, v190, v206\n"
      "v_mfma_f32_16x16x32_bf16 v[180:183], a[168:171], v[124:127], v[180:183]\n"
      "v_add_f32 v218, v218, v177\n"
      "v_add_f32 v218, v218, v178\n"
      "v_mfma_f32_16x16x32_bf16 v[180:183], a[172:175], v[128:131], v[180:183]\n"
      "v_add_f32 v218, v218, v179\n"
      "v_add_f32 v189, v189, v205\n"
      "v_mfma_f32_16x16x32_bf16 v[180:183], a[176:179], v[132:135], v[180:183]\n"
      "buffer_load_dwordx4 v1, s[8:11], s46 offen offset:0 sc1 lds\n"
      "v_mfma_f32_16x16x32_bf16 v[180:183], a[180:183], v[136:139], v[180:183]\n"
      "v_add_f32 v188, v188, v204\n"
      "v_fmac_f32 v219, v177, v177\n"
      "v_mfma_f32_16x16x32_bf16 v[180:183], a[184:187], v[140:143], v[180:183]\n"
      "ds_read_b32 v22, v24\n"
      "v_fmac_f32 v219, v178, v178\n"
      "v_mfma_f32_16x16x32_bf16 v[180:183], a[188:191], v[144:147], v[180:183]\n"
      "v_cvt_pk_bf16_f32 v210, v176, v177\n"
      "v_fmac_f32 v219, v179, v179\n"
      "v_mfma_f32_16x16x32_bf16 v[180:183], a[192:195], v[148:151], v[180:183]\n"
      "v_add_f32 v191, v191, v207\n"
      "v_fmac_f32 v219, v188, v188\n"
      "v_mfma_f32_16x16x32_bf16 v[180:183], a[196:199], v[152:155], v[180:183]\n"
      "v_add_f32 v218, v218, v188\n"
      "v_mfma_f32_16x16x32_bf16 v[180:183], a[200:203], v[156:159], v[180:183]\n"
      "v_add_f32 v218, v218, v189\n"
      "v_add_f32 v218, v218, v190\n"
      "v_mfma_f32_16x16x32_bf16 v[180:183], a[204:207], v[160:163], v[180:183]\n"
      "s_waitcnt lgkmcnt(0)\n"
      "v_fmac_f32 v219, v189, v189\n"
      "v_cmp_gt_i32 vcc, v21, v22\n"
      "v_mfma_f32_16x16x32_bf16 v[180:183], a[208:211], v[164:167], v[180:183]\n"
      "v_cvt_pk_bf16_f32 v216, v188, v189\n"
      "v_fmac_f32 v219, v190, v190\n"
      "v_mfma_f32_16x16x32_bf16 v[180:183], a[212:215], v[168:171], v[180:183]\n"
      "s_waitcnt vmcnt(11)\n"
      "v_and_b32 v189, 0xffff0000, v200\n"
      "v_fmac_f32 v219, v191, v191\n"
      "v_mfma_f32_16x16x32_bf16 v[184:187], a[216:219], v[100:103], v[184:187]\n"
      "s_add_u32 s38, s38, 24576\n"
      "v_cvt_pk_bf16_f32 v217, v190, v191\n"
      "v_lshlrev_b32_e64 v188, 16, v200\n"
      "v_mfma_f32_16x16x32_bf16 v[184:187], a[220:223], v[104:107], v[184:187]\n"
      "s_cbranch_vccnz .Lsp_wy0_15\n"
      ".Lbk_wy0_15:\n"
      "v_mfma_f32_16x16x32_bf16 v[184:187], a[224:227], v[108:111], v[184:187]\n"
      "ds_read_b128 v[104:107], v0 offset:1024\n"
      "v_add_f32 v218, v218, v191\n"
      "v_mfma_f32_16x16x32_bf16 v[184:187], a[228:231], v[112:115], v[184:187]\n"
      "v_fmac_f32 v219, v180, v180\n"
      "v_lshlrev_b32 v190, 16, v201\n"
      "v_mfma_f32_16x16x32_bf16 v[184:187], a[232:235], v[116:119], v[184:187]\n"
      "ds_read_b128 v[100:103], v0 offset:0\n"
      "v_add_f32 v218, v218, v180\n"
      "v_mfma_f32_16x16x32_bf16 v[184:187], a[236:239], v[120:123], v[184:187]\n"
      "s_waitcnt vmcnt(9)\n"
      "ds_read_b128 v[120:123], v0 offset:5120\n"
      "v_lshlrev_b32 v178, 16, v195\n"
      "v_mfma_f32_16x16x32_bf16 v[184:187], a[240:243], v[124:127], v[184:187]\n"
      "ds_read_b128 v[112:115], v0 offset:3072\n"
      "v_fmac_f32 v219, v181, v181\n"
      "v_mfma_f32_16x16x32_bf16 v[184:187], a[244:247], v[128:131], v[184:187]\n"
      "ds_read_b128 v[116:119], v0 offset:4096\n"
      "v_lshlrev_b32 v174, 16, v193\n"
      "v_mfma_f32_16x16x32_bf16 v[184:187], a[248:251], v[132:135], v[184:187]\n"
      "ds_read_b128 v[124:127], v0 offset:6144\n"
      "v_fmac_f32 v219, v182, v182\n"
      "v_mfma_f32_16x16x32_bf16 v[184:187], a[252:255], v[136:139], v[184:187]\n"
      "s_sub_u32 s47, s46, s45\n"
      "ds_read_b128 v[108:111], v0 offset:2048\n"
      "v_mfma_f32_16x16x32_bf16 v[184:187], v[32:35], v[140:143], v[184:187]\n"
      "v_and_b32 v191, 0xffff0000, v201\n"
      "v_and_b32 v177, 0xffff0000, v194\n"
      "v_mfma_f32_16x16x32_bf16 v[184:187], v[36:39], v[144:147], v[184:187]\n"
      "ds_read_b128 v[136:139], v0 offset:8192\n"
      "v_lshlrev_b32 v176, 16, v194\n"
      "v_mfma_f32_16x16x32_bf16 v[184:187], v[40:43], v[148:151], v[184:187]\n"
      "ds_read_b128 v[128:131], v0 offset:7168\n"
      "v_lshlrev_b32 v172, 16, v192\n"
      "v_mfma_f32_16x16x32_bf16 v[184:187], v[44:47], v[152:155], v[184:187]\n"
      "v_fmac_f32 v219, v183, v183\n"
      "v_and_b32 v175, 0xffff0000, v193\n"
      "v_mfma_f32_16x16x32_bf16 v[184:187], v[48:51], v[156:159], v[184:187]\n"
      "buffer_load_dwordx2 v[200:201], v14, s[8:11], s47 offen sc1\n"
      "v_mfma_f32_16x16x32_bf16 v[184:187], v[52:55], v[160:163], v[184:187]\n"
      "ds_read2_b32 v[132:133], v2 offset0:0 offset1:32\n"
      "v_and_b32 v173, 0xffff0000, v192\n"
      "v_mfma_f32_16x16x32_bf16 v[184:187], v[56:59], v[164:167], v[184:187]\n"
      "s_waitcnt lgkmcnt(3)\n"
      "ds_read2_b32 v[134:135], v2 offset0:64 offset1:96\n"
      "v_add_f32 v218, v218, v181\n"
      "v_mfma_f32_16x16x32_bf16 v[184:187], v[60:63], v[168:171], v[184:187]\n"
      "s_sub_u32 s20, s20, 1\n"
      "s_cbranch_scc0 .Lw_loop\n"
      ".Lw_tail:\n"
      "v_add_f32 v218, v218, v182\n"
      "s_nop 7\n"
      "v_fmac_f32 v219, v184, v184\n"
      "v_cvt_pk_bf16_f32 v213, v182, v183\n"
      "v_cvt_pk_bf16_f32 v215, v186, v187\n"
      "v_add_f32 v218, v218, v183\n"
      "buffer_store_dwordx4 v[208:211], v15, s[8:11], s38 offen sc1\n"
      "v_cvt_pk_bf16_f32 v212, v180, v181\n"
      "v_add_f32 v218, v218, v184\n"
      "s_add_u32 s50, s50, 24576\n"
      "v_cvt_pk_bf16_f32 v214, v184, v185\n"
      "v_add_f32 v218, v218, v185\n"
      "v_add_f32 v218, v218, v186\n"
      "v_add_f32 v218, v218, v187\n"
      "v_fmac_f32 v219, v185, v185\n"
      "v_fmac_f32 v219, v186, v186\n"
      "buffer_store_dwordx2 v[216:217], v16, s[8:11], s38 offen sc1\n"
      "buffer_store_dwordx4 v[212:215], v15, s[8:11], s38 offen offset:1024 sc1\n"
      "ds_write_b32 v18, v218\n"
      "v_fmac_f32 v219, v187, v187\n"
      "ds_write_b32 v18, v219 offset:4\n"
      "s_waitcnt lgkmcnt(0)\n"
      "s_barrier\n"
      "s_waitcnt vmcnt(12)\n"
      "buffer_store_dword v25, v12, s[8:11], s50 offen sc1\n"
      "s_add_u32 s38, s38, 24576\n"
      "s_add_u32 s50, s50, 24576\n"
      "s_waitcnt vmcnt(1)\n"
      "buffer_store_dword v25, v12, s[8:11], s50 offen sc1\n"
      "s_add_u32 s50, s50, 24576\n"
      "s_endpgm\n"
      ".Lv_start:\n"
      "s_mov_b64 exec, 0\n"
      "s_lshr_b32 s22, s39, 13\n"
      ".Lv_pl:\n"
      "s_cmp_eq_u32 s22, 0\n"
      "s_cbranch_scc1 .Lv_p0\n"
      "s_cmp_eq_u32 s22, 1\n"
      "s_cbranch_scc1 .Lv_p1\n"
      "s_branch .Lv_pe\n"
      ".Lv_pr:\n"
      "s_add_u32 s22, s22, 2\n"
      "s_branch .Lv_pl\n"
      ".Lv_pe:\n"
      "s_mov_b64 exec, -1\n"
      "s_add_u32 s50, s38, 4294893568\n"
      "s_add_u32 s49, s38, 4294918144\n"
      "s_add_u32 s38, s38, 4294942720\n"
      "s_cmp_eq_u32 s36, s37\n"
      "s_cselect_b32 s48, 49152, s51\n"
      "s_add_u32 s36, s36, s48\n"
      "s_min_u32 s36, s36, s6\n"
      "buffer_load_dword v28, v11, s[8:11], s36 offen sc1\n"
      "s_mov_b32 s46, s36\n"
      "s_add_u32 s36, s36, 24576\n"
      "s_min_u32 s36, s36, s6\n"
      "s_add_u32 m0, s39, 0\n"
      "s_waitcnt vmcnt(0)\n"
      "v_cmp_ne_u32 vcc, s21, v28\n"
      "s_cbranch_vccnz .Lsp_v0_16\n"
      ".Lbk_v0_16:\n"
      "buffer_load_dword v28, v11, s[8:11], s36 offen sc1\n"
      "buffer_load_dwordx4 v0, s[8:11], s46 offen offset:3072 sc1 lds\n"
      "buffer_load_dwordx4 v0, s[8:11], s46 offen offset:0 sc1 lds\n"
      "buffer_load_dwordx4 v0, s[8:11], s46 offen offset:1024 sc1 lds\n"
      "buffer_load_dwordx4 v0, s[8:11], s46 offen offset:2048 sc1 lds\n"
      "s_add_u32 m0, s40, 0\n"
      "s_nop 0\n"
      "buffer_load_dwordx4 v1, s[8:11], s46 offen offset:0 sc1 lds\n"
      "s_sub_u32 s47, s46, s45\n"
      "buffer_load_dwordx4 v[192:195], v13, s[8:11], s47 offen sc1\n"
      "s_mov_b32 s46, s36\n"
      "s_cmp_eq_u32 s36, s37\n"
      "s_cselect_b32 s48, 49152, s51\n"
      "buffer_load_dword v26, v23, s[8:11], s31 offen\n"
      "s_add_u32 s36, s36, s48\n"
      "s_waitcnt vmcnt(7)\n"
      "v_cmp_ne_u32 vcc, s21, v28\n"
      "s_min_u32 s36, s36, s6\n"
      "s_cbranch_vccnz .Lsp_v1_17\n"
      ".Lbk_v1_17:\n"
      "buffer_load_dword v28, v11, s[8:11], s36 offen sc1\n"
      "s_add_u32 m0, s39, 20480\n"
      "s_nop 0\n"
      "buffer_load_dwordx4 v0, s[8:11], s46 offen offset:0 sc1 lds\n"
      "buffer_load_dwordx4 v0, s[8:11], s46 offen offset:1024 sc1 lds\n"
      "buffer_load_dwordx4 v0, s[8:11], s46 offen offset:2048 sc1 lds\n"
      "buffer_load_dwordx4 v[196:199], v13, s[8:11], s47 offen offset:1024 sc1\n"
      "v_add_u32 v21, 1, v21\n"
      "s_waitcnt vmcnt(7)\n"
      "ds_write_b32 v23, v21\n"
      "buffer_load_dwordx4 v0, s[8:11], s46 offen offset:3072 sc1 lds\n"
      "s_add_u32 m0, s40, 20480\n"
      "s_nop 0\n"
      "buffer_load_dwordx4 v1, s[8:11], s46 offen offset:0 sc1 lds\n"
      "ds_read_b32 v22, v24\n"
      "buffer_load_dword v26, v23, s[8:11], s31 offen\n"
      "s_waitcnt lgkmcnt(0)\n"
      "v_cmp_gt_i32 vcc, v21, v22\n"
      "buffer_load_dword v26, v23, s[8:11], s31 offen\n"
      "s_cbranch_vccnz .Lsp_vy0_18\n"
      ".Lbk_vy0_18:\n"
      "s_sub_u32 s47, s46, s45\n"
      "s_waitcnt vmcnt(10)\n"
      "v_lshlrev_b32 v174, 16, v193\n"
      "v_and_b32 v179, 0xffff0000, v195\n"
      "ds_read_b128 v[100:103], v0 offset:8192\n"
      "ds_read_b128 v[108:111], v0 offset:10240\n"
      "v_and_b32 v173, 0xffff0000, v192\n"
      "ds_read_b128 v[120:123], v0 offset:13312\n"
      "ds_read_b128 v[128:131], v0 offset:15360\n"
      "v_lshlrev_b32 v178, 16, v195\n"
      "ds_read_b128 v[104:107], v0 offset:9216\n"
      "v_lshlrev_b32 v172, 16, v192\n"
      "buffer_load_dword v26, v23, s[8:11], s31 offen\n"
      "s_mov_b32 s46, s36\n"
      "ds_read_b128 v[112:115], v0 offset:11264\n"
      "ds_read2_b32 v[134:135], v2 offset0:64 offset1:96\n"
      "ds_read2_b32 v[132:133], v2 offset0:0 offset1:32\n"
      "s_add_u32 s36, s36, 24576\n"
      "ds_read_b128 v[116:119], v0 offset:12288\n"
      "ds_read_b128 v[124:127], v0 offset:14336\n"
      "ds_read_b128 v[160:163], v0 offset:6144\n"
      "v_and_b32 v177, 0xffff0000, v194\n"
      "s_mov_b32 s20, 31\n"
      "s_waitcnt lgkmcnt(0)\n"
      "s_nop 4\n"
      "s_branch .Lv_loop\n"
      ".p2align 12\n"
      ".Lv_p0:\n"
      "s_branch .Lv_pr\n"
      ".p2align 5\n"
      ".Lv_loop:\n"
      "v_mfma_f32_16x16x32_bf16 v[188:191], v[64:67], v[100:103], 0\n"
      "ds_read_b128 v[140:143], v0 offset:1024\n"
      "v_and_b32 v175, 0xffff0000, v193\n"
      "v_mfma_f32_16x16x32_bf16 v[188:191], v[68:71], v[104:107], v[188:191]\n"
      "ds_read2_b32 v[168:169], v3 offset0:0 offset1:32\n"
      "v_lshlrev_b32 v176, 16, v194\n"
      "v_mfma_f32_16x16x32_bf16 v[188:191], v[72:75], v[108:111], v[188:191]\n"
      "s_waitcnt lgkmcnt(5)\n"
      "ds_read_b128 v[164:167], v0 offset:7168\n"
      "v_add_f32 v218, v218, v184\n"
      "v_mfma_f32_16x16x32_bf16 v[188:191], v[76:79], v[112:115], v[188:191]\n"
      "s_waitcnt lgkmcnt(5)\n"
      "ds_read_b128 v[144:147], v0 offset:2048\n"
      "v_add_f32 v218, v218, v185\n"
      "v_mfma_f32_16x16x32_bf16 v[188:191], v[80:83], v[116:119], v[188:191]\n"
      "buffer_load_dwordx4 v[192:195], v13, s[8:11], s47 offen sc1\n"
      "v_mfma_f32_16x16x32_bf16 v[188:191], v[84:87], v[120:123], v[188:191]\n"
      "s_waitcnt lgkmcnt(4)\n"
      "ds_read2_b32 v[170:171], v3 offset0:64 offset1:96\n"
      "v_fmac_f32 v219, v183, v183\n"
      "v_mfma_f32_16x16x32_bf16 v[188:191], v[88:91], v[124:127], v[188:191]\n"
      "s_min_u32 s36, s36, s6\n"
      "ds_read_b128 v[136:139], v0 offset:0\n"
      "v_fmac_f32 v219, v184, v184\n"
      "v_mfma_f32_16x16x32_bf16 v[188:191], v[92:95], v[128:131], v[188:191]\n"
      "ds_read_b128 v[148:151], v0 offset:3072\n"
      "v_mfma_f32_16x16x32_bf16 v[188:191], v[96:99], v[132:135], v[188:191]\n"
      "v_cvt_pk_bf16_f32 v215, v186, v187\n"
      "v_add_f32 v218, v218, v186\n"
      "v_mfma_f32_16x16x32_bf16 v[172:175], a[0:3], v[100:103], v[172:175]\n"
      "ds_read_b128 v[152:155], v0 offset:4096\n"
      "v_add_f32 v218, v218, v187\n"
      "v_mfma_f32_16x16x32_bf16 v[172:175], a[4:7], v[104:107], v[172:175]\n"
      "ds_read_b128 v[156:159], v0 offset:5120\n"
      "v_fmac_f32 v219, v185, v185\n"
      "v_mfma_f32_16x16x32_bf16 v[172:175], a[8:11], v[108:111], v[172:175]\n"
      "s_add_u32 m0, s39, 40960\n"
      "v_cvt_pk_bf16_f32 v214, v184, v185\n"
      "v_mfma_f32_16x16x32_bf16 v[172:175], a[12:15], v[112:115], v[172:175]\n"
      "ds_write_b32 v18, v218\n"
      "v_mfma_f32_16x16x32_bf16 v[172:175], a[16:19], v[116:119], v[172:175]\n"
      "s_waitcnt vmcnt(10) lgkmcnt(4)\n"
      "v_fmac_f32_e64 v219, v186, v186\n"
      "v_cmp_ne_u32 vcc, s21, v28\n"
      "v_mfma_f32_16x16x32_bf16 v[172:175], a[20:23], v[120:123], v[172:175]\n"
      "ds_write_b32 v17, v190 offset:512\n"
      "v_mfma_f32_16x16x32_bf16 v[172:175], a[24:27], v[124:127], v[172:175]\n"
      "buffer_store_dwordx4 v[212:215], v15, s[8:11], s38 offen offset:1024 sc1\n"
      "v_mfma_f32_16x16x32_bf16 v[172:175], a[28:31], v[128:131], v[172:175]\n"
      "ds_write_b32 v17, v188 offset:0\n"
      "v_mfma_f32_16x16x32_bf16 v[172:175], a[32:35], v[132:135], v[172:175]\n"
      "v_fmac_f32_e64 v219, v187, v187\n"
      "v_mfma_f32_16x16x32_bf16 v[172:175], a[36:39], v[136:139], v[172:175]\n"
      "ds_write_b32 v17, v191 offset:768\n"
      "v_mfma_f32_16x16x32_bf16 v[172:175], a[40:43], v[140:143], v[172:175]\n"
      "s_cbranch_vccnz .Lsp_v0_19\n"
      ".Lbk_v0_19:\n"
      "v_add_u32 v21, 1, v21\n"
      "v_mfma_f32_16x16x32_bf16 v[172:175], a[44:47], v[144:147], v[172:175]\n"
      "s_waitcnt lgkmcnt(3)\n"
      "ds_write_b32 v18, v219 offset:4\n"
      "v_mfma_f32_16x16x32_bf16 v[172:175], a[48:51], v[148:151], v[172:175]\n"
      "buffer_load_dword v28, v11, s[8:11], s36 offen sc1\n"
      "v_mfma_f32_16x16x32_bf16 v[172:175], a[52:55], v[152:155], v[172:175]\n"
      "ds_write_b32 v17, v189 offset:256\n"
      "v_mfma_f32_16x16x32_bf16 v[172:175], a[56:59], v[156:159], v[172:175]\n"
      "s_waitcnt vmcnt(8) lgkmcnt(3)\n"
      "v_lshlrev_b32 v186, 16, v199\n"
      "v_and_b32 v185, 0xffff0000, v198\n"
      "v_mfma_f32_16x16x32_bf16 v[172:175], a[60:63], v[160:163], v[172:175]\n"
      "ds_write_b32 v23, v21\n"
      "v_mfma_f32_16x16x32_bf16 v[172:175], a[64:67], v[164:167], v[172:175]\n"
      "v_and_b32 v181, 0xffff0000, v196\n"
      "v_and_b32 v187, 0xffff0000, v199\n"
      "v_mfma_f32_16x16x32_bf16 v[172:175], a[68:71], v[168:171], v[172:175]\n"
      "buffer_load_dwordx4 v0, s[8:11], s46 offen offset:3072 sc1 lds\n"
      "v_mfma_f32_16x16x32_bf16 v[176:179], a[72:75], v[100:103], v[176:179]\n"
      "v_lshlrev_b32 v182, 16, v197\n"
      "s_add_u32 s38, s38, 24576\n"
      "v_mfma_f32_16x16x32_bf16 v[176:179], a[76:79], v[104:107], v[176:179]\n"
      "v_lshlrev_b32 v184, 16, v198\n"
      "v_mfma_f32_16x16x32_bf16 v[176:179], a[80:83], v[108:111], v[176:179]\n"
      "v_cvt_pk_bf16_f32 v208, v172, v173\n"
      "v_mfma_f32_16x16x32_bf16 v[176:179], a[84:87], v[112:115], v[176:179]\n"
      "v_mul_f32 v219, v172, v172\n"
      "v_mfma_f32_16x16x32_bf16 v[176:179], a[88:91], v[116:119], v[176:179]\n"
      "v_lshlrev_b32 v180, 16, v196\n"
      "v_and_b32 v183, 0xffff0000, v197\n"
      "v_mfma_f32_16x16x32_bf16 v[176:179], a[92:95], v[120:123], v[176:179]\n"
      "v_fmac_f32 v219, v173, v173\n"
      "v_mfma_f32_16x16x32_bf16 v[176:179], a[96:99], v[124:127], v[176:179]\n"
      "buffer_load_dwordx4 v[196:199], v13, s[8:11], s47 offen offset:1024 sc1\n"
      "v_mfma_f32_16x16x32_bf16 v[176:179], a[100:103], v[128:131], v[176:179]\n"
      "ds_read_b32 v22, v24\n"
      "v_mfma_f32_16x16x32_bf16 v[176:179], a[104:107], v[132:135], v[176:179]\n"
      "v_fmac_f32 v219, v174, v174\n"
      "v_mfma_f32_16x16x32_bf16 v[176:179], a[108:111], v[136:139], v[176:179]\n"
      "v_fmac_f32 v219, v175, v175\n"
      "v_mfma_f32_16x16x32_bf16 v[176:179], a[112:115], v[140:143], v[176:179]\n"
      "buffer_load_dwordx4 v0, s[8:11], s46 offen offset:0 sc1 lds\n"
      "v_mfma_f32_16x16x32_bf16 v[176:179], a[116:119], v[144:147], v[176:179]\n"
      "v_mfma_f32_16x16x32_bf16 v[176:179], a[120:123], v[148:151], v[176:179]\n"
      "s_waitcnt lgkmcnt(0)\n"
      "v_cmp_gt_i32 vcc, v21, v22\n"
      "v_add_f32_e64 v218, v172, v173\n"
      "v_mfma_f32_16x16x32_bf16 v[176:179], a[124:127], v[152:155], v[176:179]\n"
      "v_add_f32_e64 v218, v218, v174\n"
      "v_add_f32_e64 v218, v218, v175\n"
      "v_mfma_f32_16x16x32_bf16 v[176:179], a[128:131], v[156:159], v[176:179]\n"
      "buffer_load_dwordx4 v0, s[8:11], s46 offen offset:1024 sc1 lds\n"
      "v_mfma_f32_16x16x32_bf16 v[176:179], a[132:135], v[160:163], v[176:179]\n"
      "s_cbranch_vccnz .Lsp_vz0_20\n"
      ".Lbk_vz0_20:\n"
      "ds_read_b128 v[204:207], v19\n"
      "v_mfma_f32_16x16x32_bf16 v[176:179], a[136:139], v[164:167], v[176:179]\n"
      "v_cvt_pk_bf16_f32 v209, v174, v175\n"
      "v_add_u32 v21, 1, v21\n"
      "v_mfma_f32_16x16x32_bf16 v[176:179], a[140:143], v[168:171], v[176:179]\n"
      "s_waitcnt vmcnt(10)\n"
      "ds_write_b32 v23, v21\n"
      "v_mfma_f32_16x16x32_bf16 v[180:183], a[144:147], v[100:103], v[180:183]\n"
      "v_mfma_f32_16x16x32_bf16 v[180:183], a[148:151], v[104:107], v[180:183]\n"
      "buffer_load_dwordx4 v0, s[8:11], s46 offen offset:2048 sc1 lds\n"
      "v_mfma_f32_16x16x32_bf16 v[180:183], a[152:155], v[108:111], v[180:183]\n"
      "s_add_u32 s49, s49, 24576\n"
      "s_add_u32 m0, s40, 40960\n"
      "v_mfma_f32_16x16x32_bf16 v[180:183], a[156:159], v[112:115], v[180:183]\n"
      "v_mfma_f32_16x16x32_bf16 v[180:183], a[160:163], v[116:119], v[180:183]\n"
      "s_waitcnt lgkmcnt(1)\n"
      "v_add_f32 v204, v204, v206\n"
      "v_cvt_pk_bf16_f32 v210, v176, v177\n"
      "v_mfma_f32_16x16x32_bf16 v[180:183], a[164:167], v[120:123], v[180:183]\n"
      "buffer_load_dwordx4 v1, s[8:11], s46 offen offset:0 sc1 lds\n"
      "v_mfma_f32_16x16x32_bf16 v[180:183], a[168:171], v[124:127], v[180:183]\n"
      "v_add_f32 v205, v205, v207\n"
      "v_add_f32_dpp v204, v204, v204 quad_perm:[1,0,3,2] row_mask:0xf bank_mask:0xf\n"
      "v_mfma_f32_16x16x32_bf16 v[180:183], a[172:175], v[128:131], v[180:183]\n"
      "v_fmac_f32 v219, v176, v176\n"
      "v_mfma_f32_16x16x32_bf16 v[180:183], a[176:179], v[132:135], v[180:183]\n"
      "v_add_f32_dpp v204, v204, v204 quad_perm:[2,3,0,1] row_mask:0xf bank_mask:0xf\n"
      "v_cvt_pk_bf16_f32 v211, v178, v179\n"
      "v_mfma_f32_16x16x32_bf16 v[180:183], a[180:183], v[136:139], v[180:183]\n"
      "buffer_store_dwordx4 v[208:211], v15, s[8:11], s38 offen sc1\n"
      "v_mfma_f32_16x16x32_bf16 v[180:183], a[184:187], v[140:143], v[180:183]\n"
      "ds_read_b32 v22, v24\n"
      "v_add_f32_dpp v205, v205, v205 quad_perm:[1,0,3,2] row_mask:0xf bank_mask:0xf\n"
      "v_mfma_f32_16x16x32_bf16 v[180:183], a[188:191], v[144:147], v[180:183]\n"
      "s_add_u32 s50, s50, 24576\n"
      "v_add_f32_dpp v205, v205, v205 quad_perm:[2,3,0,1] row_mask:0xf bank_mask:0xf\n"
      "v_fmac_f32 v219, v177, v177\n"
      "v_mfma_f32_16x16x32_bf16 v[180:183], a[192:195], v[148:151], v[180:183]\n"
      "v_add_f32_dpp v205, v205, v205 row_half_mirror row_mask:0xf bank_mask:0xf\n"
      "v_fmac_f32 v219, v178, v178\n"
      "v_mfma_f32_16x16x32_bf16 v[180:183], a[196:199], v[152:155], v[180:183]\n"
      "v_add_f32_dpp v204, v204, v204 row_half_mirror row_mask:0xf bank_mask:0xf\n"
      "v_mfma_f32_16x16x32_bf16 v[180:183], a[200:203], v[156:159], v[180:183]\n"
      "v_mul_f32 v204, v27, v204\n"
      "v_fmaak_f32 v205, v27, v205, 0x36a7c5ac\n"
      "v_mfma_f32_16x16x32_bf16 v[180:183], a[204:207], v[160:163], v[180:183]\n"
      "s_waitcnt vmcnt(12) lgkmcnt(1)\n"
      "buffer_store_dword v25, v12, s[8:11], s50 offen sc1\n"
      "v_mfma_f32_16x16x32_bf16 v[180:183], a[208:211], v[164:167], v[180:183]\n"
      "s_waitcnt lgkmcnt(0)\n"
      "v_cmp_gt_i32 vcc, v21, v22\n"
      "v_add_f32 v218, v218, v176\n"
      "v_mfma_f32_16x16x32_bf16 v[180:183], a[212:215], v[168:171], v[180:183]\n"
      "v_add_f32 v218, v218, v177\n"
      "v_add_f32_e64 v218, v218, v178\n"
      "v_mfma_f32_16x16x32_bf16 v[184:187], a[216:219], v[100:103], v[184:187]\n"
      "buffer_store_dwordx2 v[204:205], v20, s[8:11], s49 offen sc1\n"
      "v_mfma_f32_16x16x32_bf16 v[184:187], a[220:223], v[104:107], v[184:187]\n"
      "s_sub_u32 s47, s46, s45\n"
      "s_cbranch_vccnz .Lsp_vy1_21\n"
      ".Lbk_vy1_21:\n"
      "v_mfma_f32_16x16x32_bf16 v[184:187], a[224:227], v[108:111], v[184:187]\n"
      "s_waitcnt vmcnt(11)\n"
      "ds_read_b128 v[104:107], v0 offset:29696\n"
      "v_and_b32 v177, 0xffff0000, v194\n"
      "v_mfma_f32_16x16x32_bf16 v[184:187], a[228:231], v[112:115], v[184:187]\n"
      "v_fmac_f32 v219, v179, v179\n"
      "v_mfma_f32_16x16x32_bf16 v[184:187], a[232:235], v[116:119], v[184:187]\n"
      "v_fmac_f32 v219, v180, v180\n"
      "v_add_f32 v218, v218, v179\n"
      "v_mfma_f32_16x16x32_bf16 v[184:187], a[236:239], v[120:123], v[184:187]\n"
      "v_cvt_pk_bf16_f32 v212, v180, v181\n"
      "v_and_b32 v179, 0xffff0000, v195\n"
      "v_mfma_f32_16x16x32_bf16 v[184:187], a[240:243], v[124:127], v[184:187]\n"
      "ds_read_b128 v[120:123], v0 offset:33792\n"
      "v_lshlrev_b32 v174, 16, v193\n"
      "v_mfma_f32_16x16x32_bf16 v[184:187], a[244:247], v[128:131], v[184:187]\n"
      "ds_read_b128 v[112:115], v0 offset:31744\n"
      "v_add_f32 v218, v218, v180\n"
      "v_mfma_f32_16x16x32_bf16 v[184:187], a[248:251], v[132:135], v[184:187]\n"
      "ds_read_b128 v[100:103], v0 offset:28672\n"
      "v_add_f32 v218, v218, v181\n"
      "v_mfma_f32_16x16x32_bf16 v[184:187], a[252:255], v[136:139], v[184:187]\n"
      "ds_read_b128 v[124:127], v0 offset:34816\n"
      "v_mfma_f32_16x16x32_bf16 v[184:187], v[32:35], v[140:143], v[184:187]\n"
      "ds_read_b128 v[108:111], v0 offset:30720\n"
      "v_and_b32 v175, 0xffff0000, v193\n"
      "v_mfma_f32_16x16x32_bf16 v[184:187], v[36:39], v[144:147], v[184:187]\n"
      "ds_read_b128 v[116:119], v0 offset:32768\n"
      "v_fmac_f32 v219, v181, v181\n"
      "v_mfma_f32_16x16x32_bf16 v[184:187], v[40:43], v[148:151], v[184:187]\n"
      "v_add_f32 v218, v218, v182\n"
      "v_mfma_f32_16x16x32_bf16 v[184:187], v[44:47], v[152:155], v[184:187]\n"
      "ds_read2_b32 v[132:133], v4 offset0:0 offset1:32\n"
      "v_and_b32 v173, 0xffff0000, v192\n"
      "v_mfma_f32_16x16x32_bf16 v[184:187], v[48:51], v[156:159], v[184:187]\n"
      "ds_read_b128 v[128:131], v0 offset:35840\n"
      "v_lshlrev_b32 v178, 16, v195\n"
      "v_mfma_f32_16x16x32_bf16 v[184:187], v[52:55], v[160:163], v[184:187]\n"
      "ds_read_b128 v[156:159], v0 offset:25600\n"
      "v_lshlrev_b32 v172, 16, v192\n"
      "v_mfma_f32_16x16x32_bf16 v[184:187], v[56:59], v[164:167], v[184:187]\n"
      "ds_read_b128 v[144:147], v0 offset:22528\n"
      "v_lshlrev_b32 v176, 16, v194\n"
      "v_mfma_f32_16x16x32_bf16 v[184:187], v[60:63], v[168:171], v[184:187]\n"
      "s_waitcnt lgkmcnt(5)\n"
      "buffer_load_dwordx4 v[192:195], v13, s[8:11], s47 offen sc1\n"
      "v_mfma_f32_16x16x32_bf16 v[188:191], v[64:67], v[100:103], 0\n"
      "ds_read2_b32 v[134:135], v4 offset0:64 offset1:96\n"
      "v_fmac_f32 v219, v182, v182\n"
      "v_mfma_f32_16x16x32_bf16 v[188:191], v[68:71], v[104:107], v[188:191]\n"
      "ds_read_b128 v[148:151], v0 offset:23552\n"
      "v_add_f32 v218, v218, v183\n"
      "v_mfma_f32_16x16x32_bf16 v[188:191], v[72:75], v[108:111], v[188:191]\n"
      "s_mov_b32 s46, s36\n"
      "v_cvt_pk_bf16_f32 v213, v182, v183\n"
      "v_cvt_pk_bf16_f32 v215, v186, v187\n"
      "v_mfma_f32_16x16x32_bf16 v[188:191], v[76:79], v[112:115], v[188:191]\n"
      "s_waitcnt lgkmcnt(4)\n"
      "ds_read_b128 v[160:163], v0 offset:26624\n"
      "v_fmac_f32 v219, v183, v183\n"
      "v_mfma_f32_16x16x32_bf16 v[188:191], v[80:83], v[116:119], v[188:191]\n"
      "ds_read_b128 v[140:143], v0 offset:21504\n"
      "v_fmac_f32 v219, v184, v184\n"
      "v_mfma_f32_16x16x32_bf16 v[188:191], v[84:87], v[120:123], v[188:191]\n"
      "ds_read2_b32 v[168:169], v5 offset0:0 offset1:32\n"
      "v_add_f32 v218, v218, v184\n"
      "v_mfma_f32_16x16x32_bf16 v[188:191], v[88:91], v[124:127], v[188:191]\n"
      "ds_read_b128 v[152:155], v0 offset:24576\n"
      "v_fmac_f32 v219, v185, v185\n"
      "v_mfma_f32_16x16x32_bf16 v[188:191], v[92:95], v[128:131], v[188:191]\n"
      "s_waitcnt lgkmcnt(5)\n"
      "ds_read_b128 v[136:139], v0 offset:20480\n"
      "v_add_f32 v218, v218, v185\n"
      "v_mfma_f32_16x16x32_bf16 v[188:191], v[96:99], v[132:135], v[188:191]\n"
      "s_cmp_eq_u32 s36, s37\n"
      "s_cselect_b32 s48, 49152, s51\n"
      "ds_read_b128 v[164:167], v0 offset:27648\n"
      "v_mfma_f32_16x16x32_bf16 v[172:175], a[0:3], v[100:103], v[172:175]\n"
      "ds_read2_b32 v[170:171], v5 offset0:64 offset1:96\n"
      "v_cvt_pk_bf16_f32 v214, v184, v185\n"
      "v_mfma_f32_16x16x32_bf16 v[172:175], a[4:7], v[104:107], v[172:175]\n"
      "v_fmac_f32 v219, v186, v186\n"
      "v_add_f32 v218, v218, v186\n"
      "v_mfma_f32_16x16x32_bf16 v[172:175], a[8:11], v[108:111], v[172:175]\n"
      "ds_write_b32 v17, v189 offset:256\n"
      "v_mfma_f32_16x16x32_bf16 v[172:175], a[12:15], v[112:115], v[172:175]\n"
      "v_add_f32 v218, v218, v187\n"
      "v_add_u32 v21, 1, v21\n"
      "v_mfma_f32_16x16x32_bf16 v[172:175], a[16:19], v[116:119], v[172:175]\n"
      "buffer_store_dwordx4 v[212:215], v15, s[8:11], s38 offen offset:1024 sc1\n"
      "v_mfma_f32_16x16x32_bf16 v[172:175], a[20:23], v[120:123], v[172:175]\n"
      "ds_write_b32 v17, v190 offset:512\n"
      "v_mfma_f32_16x16x32_bf16 v[172:175], a[24:27], v[124:127], v[172:175]\n"
      "v_fmac_f32 v219, v187, v187\n"
      "v_mfma_f32_16x16x32_bf16 v[172:175], a[28:31], v[128:131], v[172:175]\n"
      "s_add_u32 s36, s36, s48\n"
      "ds_write_b32 v18, v218\n"
      "v_mfma_f32_16x16x32_bf16 v[172:175], a[32:35], v[132:135], v[172:175]\n"
      "s_waitcnt vmcnt(11) lgkmcnt(3)\n"
      "v_cmp_ne_u32 vcc, s21, v28\n"
      "v_mfma_f32_16x16x32_bf16 v[172:175], a[36:39], v[136:139], v[172:175]\n"
      "ds_write_b32 v17, v188 offset:0\n"
      "v_mfma_f32_16x16x32_bf16 v[172:175], a[40:43], v[140:143], v[172:175]\n"
      "s_waitcnt vmcnt(9) lgkmcnt(3)\n"
      "s_min_u32 s36, s36, s6\n"
      "v_lshlrev_b32 v182, 16, v197\n"
      "v_mfma_f32_16x16x32_bf16 v[172:175], a[44:47], v[144:147], v[172:175]\n"
      "ds_write_b32 v17, v191 offset:768\n"
      "v_mfma_f32_16x16x32_bf16 v[172:175], a[48:51], v[148:151], v[172:175]\n"
      "s_cbranch_vccnz .Lsp_v1_22\n"
      ".Lbk_v1_22:\n"
      "v_and_b32 v181, 0xffff0000, v196\n"
      "v_mfma_f32_16x16x32_bf16 v[172:175], a[52:55], v[152:155], v[172:175]\n"
      "ds_write_b32 v18, v219 offset:4\n"
      "v_mfma_f32_16x16x32_bf16 v[172:175], a[56:59], v[156:159], v[172:175]\n"
      "buffer_load_dword v28, v11, s[8:11], s36 offen sc1\n"
      "v_mfma_f32_16x16x32_bf16 v[172:175], a[60:63], v[160:163], v[172:175]\n"
      "s_add_u32 m0, s39, 61440\n"
      "ds_write_b32 v23, v21\n"
      "v_mfma_f32_16x16x32_bf16 v[172:175], a[64:67], v[164:167], v[172:175]\n"
      "v_and_b32 v187, 0xffff0000, v199\n"
      "v_and_b32 v183, 0xffff0000, v197\n"
      "v_mfma_f32_16x16x32_bf16 v[172:175], a[68:71], v[168:171], v[172:175]\n"
      "buffer_load_dwordx4 v0, s[8:11], s46 offen offset:0 sc1 lds\n"
      "v_mfma_f32_16x16x32_bf16 v[176:179], a[72:75], v[100:103], v[176:179]\n"
      "s_add_u32 s49, s49, 24576\n"
      "v_mfma_f32_16x16x32_bf16 v[176:179], a[76:79], v[104:107], v[176:179]\n"
      "v_mfma_f32_16x16x32_bf16 v[176:179], a[80:83], v[108:111], v[176:179]\n"
      "v_cvt_pk_bf16_f32 v209, v174, v175\n"
      "v_mfma_f32_16x16x32_bf16 v[176:179], a[84:87], v[112:115], v[176:179]\n"
      "buffer_load_dwordx4 v0, s[8:11], s46 offen offset:1024 sc1 lds\n"
      "v_mfma_f32_16x16x32_bf16 v[176:179], a[88:91], v[116:119], v[176:179]\n"
      "v_add_f32 v218, v172, v173\n"
      "v_mfma_f32_16x16x32_bf16 v[176:179], a[92:95], v[120:123], v[176:179]\n"
      "v_add_f32 v218, v218, v174\n"
      "v_lshlrev_b32 v180, 16, v196\n"
      "v_mfma_f32_16x16x32_bf16 v[176:179], a[96:99], v[124:127], v[176:179]\n"
      "ds_read_b32 v22, v24\n"
      "v_lshlrev_b32 v186, 16, v199\n"
      "v_mfma_f32_16x16x32_bf16 v[176:179], a[100:103], v[128:131], v[176:179]\n"
      "buffer_load_dwordx4 v0, s[8:11], s46 offen offset:2048 sc1 lds\n"
      "v_mfma_f32_16x16x32_bf16 v[176:179], a[104:107], v[132:135], v[176:179]\n"
      "v_mul_f32 v219, v172, v172\n"
      "v_fmac_f32 v219, v173, v173\n"
      "v_mfma_f32_16x16x32_bf16 v[176:179], a[108:111], v[136:139], v[176:179]\n"
      "v_lshlrev_b32 v184, 16, v198\n"
      "v_mfma_f32_16x16x32_bf16 v[176:179], a[112:115], v[140:143], v[176:179]\n"
      "v_fmac_f32_e64 v219, v174, v174\n"
      "v_mfma_f32_16x16x32_bf16 v[176:179], a[116:119], v[144:147], v[176:179]\n"
      "s_waitcnt lgkmcnt(0)\n"
      "v_cmp_gt_i32 vcc, v21, v22\n"
      "v_add_f32_e64 v218, v218, v175\n"
      "v_mfma_f32_16x16x32_bf16 v[176:179], a[120:123], v[148:151], v[176:179]\n"
      "v_mfma_f32_16x16x32_bf16 v[176:179], a[124:127], v[152:155], v[176:179]\n"
      "v_and_b32 v185, 0xffff0000, v198\n"
      "v_cvt_pk_bf16_f32 v208, v172, v173\n"
      "v_mfma_f32_16x16x32_bf16 v[176:179], a[128:131], v[156:159], v[176:179]\n"
      "buffer_load_dwordx4 v[196:199], v13, s[8:11], s47 offen offset:1024 sc1\n"
      "v_mfma_f32_16x16x32_bf16 v[176:179], a[132:135], v[160:163], v[176:179]\n"
      "s_cbranch_vccnz .Lsp_vz1_23\n"
      ".Lbk_vz1_23:\n"
      "ds_read_b128 v[204:207], v19\n"
      "v_mfma_f32_16x16x32_bf16 v[176:179], a[136:139], v[164:167], v[176:179]\n"
      "v_add_u32 v21, 1, v21\n"
      "v_mfma_f32_16x16x32_bf16 v[176:179], a[140:143], v[168:171], v[176:179]\n"
      "s_waitcnt vmcnt(10)\n"
      "ds_write_b32 v23, v21\n"
      "v_mfma_f32_16x16x32_bf16 v[180:183], a[144:147], v[100:103], v[180:183]\n"
      "v_fmac_f32 v219, v175, v175\n"
      "v_mfma_f32_16x16x32_bf16 v[180:183], a[148:151], v[104:107], v[180:183]\n"
      "buffer_load_dwordx4 v0, s[8:11], s46 offen offset:3072 sc1 lds\n"
      "v_mfma_f32_16x16x32_bf16 v[180:183], a[152:155], v[108:111], v[180:183]\n"
      "v_cvt_pk_bf16_f32 v210, v176, v177\n"
      "v_fmac_f32 v219, v176, v176\n"
      "v_mfma_f32_16x16x32_bf16 v[180:183], a[156:159], v[112:115], v[180:183]\n"
      "s_add_u32 m0, s40, 61440\n"
      "v_fmac_f32 v219, v177, v177\n"
      "v_mfma_f32_16x16x32_bf16 v[180:183], a[160:163], v[116:119], v[180:183]\n"
      "s_waitcnt lgkmcnt(1)\n"
      "v_add_f32 v204, v204, v206\n"
      "v_add_f32 v205, v205, v207\n"
      "v_mfma_f32_16x16x32_bf16 v[180:183], a[164:167], v[120:123], v[180:183]\n"
      "v_add_f32_dpp v204, v204, v204 quad_perm:[1,0,3,2] row_mask:0xf bank_mask:0xf\n"
      "v_fmac_f32 v219, v178, v178\n"
      "v_mfma_f32_16x16x32_bf16 v[180:183], a[168:171], v[124:127], v[180:183]\n"
      "buffer_load_dwordx4 v1, s[8:11], s46 offen offset:0 sc1 lds\n"
      "v_mfma_f32_16x16x32_bf16 v[180:183], a[172:175], v[128:131], v[180:183]\n"
      "v_add_f32_dpp v205, v205, v205 quad_perm:[1,0,3,2] row_mask:0xf bank_mask:0xf\n"
      "v_add_f32_dpp v204, v204, v204 quad_perm:[2,3,0,1] row_mask:0xf bank_mask:0xf\n"
      "v_mfma_f32_16x16x32_bf16 v[180:183], a[176:179], v[132:135], v[180:183]\n"
      "v_add_f32_dpp v204, v204, v204 row_half_mirror row_mask:0xf bank_mask:0xf\n"
      "v_cvt_pk_bf16_f32 v211, v178, v179\n"
      "v_mfma_f32_16x16x32_bf16 v[180:183], a[180:183], v[136:139], v[180:183]\n"
      "s_add_u32 s38, s38, 24576\n"
      "v_mul_f32 v204, v27, v204\n"
      "v_add_f32_dpp v205, v205, v205 quad_perm:[2,3,0,1] row_mask:0xf bank_mask:0xf\n"
      "v_mfma_f32_16x16x32_bf16 v[180:183], a[184:187], v[140:143], v[180:183]\n"
      "ds_read_b32 v22, v24\n"
      "v_add_f32 v218, v218, v176\n"
      "v_mfma_f32_16x16x32_bf16 v[180:183], a[188:191], v[144:147], v[180:183]\n"
      "v_add_f32_dpp v205, v205, v205 row_half_mirror row_mask:0xf bank_mask:0xf\n"
      "v_fmaak_f32 v205, v27, v205, 0x36a7c5ac\n"
      "v_mfma_f32_16x16x32_bf16 v[180:183], a[192:195], v[148:151], v[180:183]\n"
      "buffer_store_dwordx2 v[204:205], v20, s[8:11], s49 offen sc1\n"
      "v_mfma_f32_16x16x32_bf16 v[180:183], a[196:199], v[152:155], v[180:183]\n"
      "s_add_u32 s50, s50, 24576\n"
      "v_add_f32 v218, v218, v177\n"
      "v_mfma_f32_16x16x32_bf16 v[180:183], a[200:203], v[156:159], v[180:183]\n"
      "v_add_f32 v218, v218, v178\n"
      "v_mfma_f32_16x16x32_bf16 v[180:183], a[204:207], v[160:163], v[180:183]\n"
      "s_waitcnt lgkmcnt(0)\n"
      "v_add_f32 v218, v218, v179\n"
      "v_cmp_gt_i32 vcc, v21, v22\n"
      "v_mfma_f32_16x16x32_bf16 v[180:183], a[208:211], v[164:167], v[180:183]\n"
      "s_waitcnt vmcnt(10)\n"
      "buffer_store_dword v25, v12, s[8:11], s50 offen sc1\n"
      "v_mfma_f32_16x16x32_bf16 v[180:183], a[212:215], v[168:171], v[180:183]\n"
      "v_fmac_f32 v219, v179, v179\n"
      "v_mfma_f32_16x16x32_bf16 v[184:187], a[216:219], v[100:103], v[184:187]\n"
      "s_cbranch_vccnz .Lsp_vy0_24\n"
      ".Lbk_vy0_24:\n"
      "s_sub_u32 s47, s46, s45\n"
      "v_mfma_f32_16x16x32_bf16 v[184:187], a[220:223], v[104:107], v[184:187]\n"
      "s_waitcnt vmcnt(10)\n"
      "v_lshlrev_b32 v174, 16, v193\n"
      "v_and_b32 v179, 0xffff0000, v195\n"
      "v_mfma_f32_16x16x32_bf16 v[184:187], a[224:227], v[108:111], v[184:187]\n"
      "ds_read_b128 v[100:103], v10 offset:8192\n"
      "v_cvt_pk_bf16_f32 v213, v182, v183\n"
      "v_mfma_f32_16x16x32_bf16 v[184:187], a[228:231], v[112:115], v[184:187]\n"
      "ds_read_b128 v[108:111], v10 offset:10240\n"
      "v_and_b32 v173, 0xffff0000, v192\n"
      "v_mfma_f32_16x16x32_bf16 v[184:187], a[232:235], v[116:119], v[184:187]\n"
      "v_fmac_f32 v219, v180, v180\n"
      "v_mfma_f32_16x16x32_bf16 v[184:187], a[236:239], v[120:123], v[184:187]\n"
      "ds_read_b128 v[120:123], v10 offset:13312\n"
      "v_add_f32 v218, v218, v180\n"
      "v_mfma_f32_16x16x32_bf16 v[184:187], a[240:243], v[124:127], v[184:187]\n"
      "v_add_f32 v218, v218, v181\n"
      "v_add_f32 v218, v218, v182\n"
      "v_mfma_f32_16x16x32_bf16 v[184:187], a[244:247], v[128:131], v[184:187]\n"
      "ds_read_b128 v[128:131], v10 offset:15360\n"
      "v_lshlrev_b32 v178, 16, v195\n"
      "v_mfma_f32_16x16x32_bf16 v[184:187], a[248:251], v[132:135], v[184:187]\n"
      "v_mfma_f32_16x16x32_bf16 v[184:187], a[252:255], v[136:139], v[184:187]\n"
      "ds_read_b128 v[104:107], v10 offset:9216\n"
      "v_lshlrev_b32 v172, 16, v192\n"
      "v_mfma_f32_16x16x32_bf16 v[184:187], v[32:35], v[140:143], v[184:187]\n"
      "buffer_store_dwordx4 v[208:211], v15, s[8:11], s38 offen sc1\n"
      "v_mfma_f32_16x16x32_bf16 v[184:187], v[36:39], v[144:147], v[184:187]\n"
      "s_mov_b32 s46, s36\n"
      "ds_read_b128 v[112:115], v10 offset:11264\n"
      "v_fmac_f32 v219, v181, v181\n"
      "v_mfma_f32_16x16x32_bf16 v[184:187], v[40:43], v[148:151], v[184:187]\n"
      "ds_read2_b32 v[134:135], v6 offset0:64 offset1:96\n"
      "v_cvt_pk_bf16_f32 v212, v180, v181\n"
      "v_mfma_f32_16x16x32_bf16 v[184:187], v[44:47], v[152:155], v[184:187]\n"
      "ds_read2_b32 v[132:133], v6 offset0:0 offset1:32\n"
      "s_add_u32 s36, s36, 24576\n"
      "v_mfma_f32_16x16x32_bf16 v[184:187], v[48:51], v[156:159], v[184:187]\n"
      "ds_read_b128 v[116:119], v10 offset:12288\n"
      "v_fmac_f32 v219, v182, v182\n"
      "v_mfma_f32_16x16x32_bf16 v[184:187], v[52:55], v[160:163], v[184:187]\n"
      "ds_read_b128 v[124:127], v10 offset:14336\n"
      "v_add_f32 v218, v218, v183\n"
      "v_mfma_f32_16x16x32_bf16 v[184:187], v[56:59], v[164:167], v[184:187]\n"
      "s_waitcnt lgkmcnt(5)\n"
      "ds_read_b128 v[160:163], v10 offset:6144\n"
      "v_and_b32 v177, 0xffff0000, v194\n"
      "v_mfma_f32_16x16x32_bf16 v[184:187], v[60:63], v[168:171], v[184:187]\n"
      "s_sub_u32 s20, s20, 1\n"
      "s_cbranch_scc1 .Lv_tail\n"
      "v_mfma_f32_16x16x32_bf16 v[188:191], v[64:67], v[100:103], 0\n"
      "ds_read_b128 v[140:143], v10 offset:1024\n"
      "v_and_b32 v175, 0xffff0000, v193\n"
      "v_mfma_f32_16x16x32_bf16 v[188:191], v[68:71], v[104:107], v[188:191]\n"
      "ds_read2_b32 v[168:169], v7 offset0:0 offset1:32\n"
      "v_lshlrev_b32 v176, 16, v194\n"
      "v_mfma_f32_16x16x32_bf16 v[188:191], v[72:75], v[108:111], v[188:191]\n"
      "s_waitcnt lgkmcnt(5)\n"
      "ds_read_b128 v[164:167], v10 offset:7168\n"
      "v_add_f32 v218, v218, v184\n"
      "v_mfma_f32_16x16x32_bf16 v[188:191], v[76:79], v[112:115], v[188:191]\n"
      "s_waitcnt lgkmcnt(5)\n"
      "ds_read_b128 v[144:147], v10 offset:2048\n"
      "v_add_f32 v218, v218, v185\n"
      "v_mfma_f32_16x16x32_bf16 v[188:191], v[80:83], v[116:119], v[188:191]\n"
      "buffer_load_dwordx4 v[192:195], v13, s[8:11], s47 offen sc1\n"
      "v_mfma_f32_16x16x32_bf16 v[188:191], v[84:87], v[120:123], v[188:191]\n"
      "s_waitcnt lgkmcnt(4)\n"
      "ds_read2_b32 v[170:171], v7 offset0:64 offset1:96\n"
      "v_fmac_f32 v219, v183, v183\n"
      "v_mfma_f32_16x16x32_bf16 v[188:191], v[88:91], v[124:127], v[188:191]\n"
      "s_min_u32 s36, s36, s6\n"
      "ds_read_b128 v[136:139], v10 offset:0\n"
      "v_fmac_f32 v219, v184, v184\n"
      "v_mfma_f32_16x16x32_bf16 v[188:191], v[92:95], v[128:131], v[188:191]\n"
      "ds_read_b128 v[148:151], v10 offset:3072\n"
      "v_mfma_f32_16x16x32_bf16 v[188:191], v[96:99], v[132:135], v[188:191]\n"
      "v_cvt_pk_bf16_f32 v215, v186, v187\n"
      "v_add_f32 v218, v218, v186\n"
      "v_mfma_f32_16x16x32_bf16 v[172:175], a[0:3], v[100:103], v[172:175]\n"
      "ds_read_b128 v[152:155], v10 offset:4096\n"
      "v_add_f32 v218, v218, v187\n"
      "v_mfma_f32_16x16x32_bf16 v[172:175], a[4:7], v[104:107], v[172:175]\n"
      "ds_read_b128 v[156:159], v10 offset:5120\n"
      "v_fmac_f32 v219, v185, v185\n"
      "v_mfma_f32_16x16x32_bf16 v[172:175], a[8:11], v[108:111], v[172:175]\n"
      "s_add_u32 m0, s39, 0\n"
      "v_cvt_pk_bf16_f32 v214, v184, v185\n"
      "v_mfma_f32_16x16x32_bf16 v[172:175], a[12:15], v[112:115], v[172:175]\n"
      "ds_write_b32 v18, v218\n"
      "v_mfma_f32_16x16x32_bf16 v[172:175], a[16:19], v[116:119], v[172:175]\n"
      "s_waitcnt vmcnt(10) lgkmcnt(4)\n"
      "v_fmac_f32 v219, v186, v186\n"
      "v_cmp_ne_u32 vcc, s21, v28\n"
      "v_mfma_f32_16x16x32_bf16 v[172:175], a[20:23], v[120:123], v[172:175]\n"
      "ds_write_b32 v17, v190 offset:512\n"
      "v_mfma_f32_16x16x32_bf16 v[172:175], a[24:27], v[124:127], v[172:175]\n"
      "buffer_store_dwordx4 v[212:215], v15, s[8:11], s38 offen offset:1024 sc1\n"
      "v_mfma_f32_16x16x32_bf16 v[172:175], a[28:31], v[128:131], v[172:175]\n"
      "ds_write_b32 v17, v188 offset:0\n"
      "v_mfma_f32_16x16x32_bf16 v[172:175], a[32:35], v[132:135], v[172:175]\n"
      "v_fmac_f32_e64 v219, v187, v187\n"
      "v_mfma_f32_16x16x32_bf16 v[172:175], a[36:39], v[136:139], v[172:175]\n"
      "ds_write_b32 v17, v191 offset:768\n"
      "v_mfma_f32_16x16x32_bf16 v[172:175], a[40:43], v[140:143], v[172:175]\n"
      "s_cbranch_vccnz .Lsp_v0_25\n"
      ".Lbk_v0_25:\n"
      "v_add_u32 v21, 1, v21\n"
      "v_mfma_f32_16x16x32_bf16 v[172:175], a[44:47], v[144:147], v[172:175]\n"
      "s_waitcnt lgkmcnt(3)\n"
      "ds_write_b32 v18, v219 offset:4\n"
      "v_mfma_f32_16x16x32_bf16 v[172:175], a[48:51], v[148:151], v[172:175]\n"
      "buffer_load_dword v28, v11, s[8:11], s36 offen sc1\n"
      "v_mfma_f32_16x16x32_bf16 v[172:175], a[52:55], v[152:155], v[172:175]\n"
      "ds_write_b32 v17, v189 offset:256\n"
      "v_mfma_f32_16x16x32_bf16 v[172:175], a[56:59], v[156:159], v[172:175]\n"
      "s_waitcnt vmcnt(8) lgkmcnt(3)\n"
      "v_lshlrev_b32 v186, 16, v199\n"
      "v_and_b32 v185, 0xffff0000, v198\n"
      "v_mfma_f32_16x16x32_bf16 v[172:175], a[60:63], v[160:163], v[172:175]\n"
      "ds_write_b32 v23, v21\n"
      "v_mfma_f32_16x16x32_bf16 v[172:175], a[64:67], v[164:167], v[172:175]\n"
      "v_and_b32 v181, 0xffff0000, v196\n"
      "v_and_b32 v187, 0xffff0000, v199\n"
      "v_mfma_f32_16x16x32_bf16 v[172:175], a[68:71], v[168:171], v[172:175]\n"
      "buffer_load_dwordx4 v0, s[8:11], s46 offen offset:3072 sc1 lds\n"
      "v_mfma_f32_16x16x32_bf16 v[176:179], a[72:75], v[100:103], v[176:179]\n"
      "v_lshlrev_b32 v182, 16, v197\n"
      "s_add_u32 s38, s38, 24576\n"
      "v_mfma_f32_16x16x32_bf16 v[176:179], a[76:79], v[104:107], v[176:179]\n"
      "v_lshlrev_b32 v184, 16, v198\n"
      "v_mfma_f32_16x16x32_bf16 v[176:179], a[80:83], v[108:111], v[176:179]\n"
      "v_cvt_pk_bf16_f32 v208, v172, v173\n"
      "v_mfma_f32_16x16x32_bf16 v[176:179], a[84:87], v[112:115], v[176:179]\n"
      "v_mul_f32 v219, v172, v172\n"
      "v_mfma_f32_16x16x32_bf16 v[176:179], a[88:91], v[116:119], v[176:179]\n"
      "v_lshlrev_b32 v180, 16, v196\n"
      "v_and_b32 v183, 0xffff0000, v197\n"
      "v_mfma_f32_16x16x32_bf16 v[176:179], a[92:95], v[120:123], v[176:179]\n"
      "v_fmac_f32 v219, v173, v173\n"
      "v_mfma_f32_16x16x32_bf16 v[176:179], a[96:99], v[124:127], v[176:179]\n"
      "buffer_load_dwordx4 v[196:199], v13, s[8:11], s47 offen offset:1024 sc1\n"
      "v_mfma_f32_16x16x32_bf16 v[176:179], a[100:103], v[128:131], v[176:179]\n"
      "ds_read_b32 v22, v24\n"
      "v_mfma_f32_16x16x32_bf16 v[176:179], a[104:107], v[132:135], v[176:179]\n"
      "v_fmac_f32 v219, v174, v174\n"
      "v_mfma_f32_16x16x32_bf16 v[176:179], a[108:111], v[136:139], v[176:179]\n"
      "v_fmac_f32 v219, v175, v175\n"
      "v_mfma_f32_16x16x32_bf16 v[176:179], a[112:115], v[140:143], v[176:179]\n"
      "buffer_load_dwordx4 v0, s[8:11], s46 offen offset:0 sc1 lds\n"
      "v_mfma_f32_16x16x32_bf16 v[176:179], a[116:119], v[144:147], v[176:179]\n"
      "v_mfma_f32_16x16x32_bf16 v[176:179], a[120:123], v[148:151], v[176:179]\n"
      "s_waitcnt lgkmcnt(0)\n"
      "v_cmp_gt_i32 vcc, v21, v22\n"
      "v_add_f32_e64 v218, v172, v173\n"
      "v_mfma_f32_16x16x32_bf16 v[176:179], a[124:127], v[152:155], v[176:179]\n"
      "v_add_f32_e64 v218, v218, v174\n"
      "v_add_f32_e64 v218, v218, v175\n"
      "v_mfma_f32_16x16x32_bf16 v[176:179], a[128:131], v[156:159], v[176:179]\n"
      "buffer_load_dwordx4 v0, s[8:11], s46 offen offset:1024 sc1 lds\n"
      "v_mfma_f32_16x16x32_bf16 v[176:179], a[132:135], v[160:163], v[176:179]\n"
      "s_cbranch_vccnz .Lsp_vz0_26\n"
      ".Lbk_vz0_26:\n"
      "ds_read_b128 v[204:207], v19\n"
      "v_mfma_f32_16x16x32_bf16 v[176:179], a[136:139], v[164:167], v[176:179]\n"
      "v_cvt_pk_bf16_f32 v209, v174, v175\n"
      "v_add_u32 v21, 1, v21\n"
      "v_mfma_f32_16x16x32_bf16 v[176:179], a[140:143], v[168:171], v[176:179]\n"
      "s_waitcnt vmcnt(10)\n"
      "ds_write_b32 v23, v21\n"
      "v_mfma_f32_16x16x32_bf16 v[180:183], a[144:147], v[100:103], v[180:183]\n"
      "v_mfma_f32_16x16x32_bf16 v[180:183], a[148:151], v[104:107], v[180:183]\n"
      "buffer_load_dwordx4 v0, s[8:11], s46 offen offset:2048 sc1 lds\n"
      "v_mfma_f32_16x16x32_bf16 v[180:183], a[152:155], v[108:111], v[180:183]\n"
      "s_add_u32 s49, s49, 24576\n"
      "s_add_u32 m0, s40, 0\n"
      "v_mfma_f32_16x16x32_bf16 v[180:183], a[156:159], v[112:115], v[180:183]\n"
      "v_mfma_f32_16x16x32_bf16 v[180:183], a[160:163], v[116:119], v[180:183]\n"
      "s_waitcnt lgkmcnt(1)\n"
      "v_add_f32 v204, v204, v206\n"
      "v_cvt_pk_bf16_f32 v210, v176, v177\n"
      "v_mfma_f32_16x16x32_bf16 v[180:183], a[164:167], v[120:123], v[180:183]\n"
      "buffer_load_dwordx4 v1, s[8:11], s46 offen offset:0 sc1 lds\n"
      "v_mfma_f32_16x16x32_bf16 v[180:183], a[168:171], v[124:127], v[180:183]\n"
      "v_add_f32 v205, v205, v207\n"
      "v_add_f32_dpp v204, v204, v204 quad_perm:[1,0,3,2] row_mask:0xf bank_mask:0xf\n"
      "v_mfma_f32_16x16x32_bf16 v[180:183], a[172:175], v[128:131], v[180:183]\n"
      "v_fmac_f32 v219, v176, v176\n"
      "v_mfma_f32_16x16x32_bf16 v[180:183], a[176:179], v[132:135], v[180:183]\n"
      "v_add_f32_dpp v204, v204, v204 quad_perm:[2,3,0,1] row_mask:0xf bank_mask:0xf\n"
      "v_cvt_pk_bf16_f32 v211, v178, v179\n"
      "v_mfma_f32_16x16x32_bf16 v[180:183], a[180:183], v[136:139], v[180:183]\n"
      "buffer_store_dwordx4 v[208:211], v15, s[8:11], s38 offen sc1\n"
      "v_mfma_f32_16x16x32_bf16 v[180:183], a[184:187], v[140:143], v[180:183]\n"
      "ds_read_b32 v22, v24\n"
      "v_add_f32_dpp v205, v205, v205 quad_perm:[1,0,3,2] row_mask:0xf bank_mask:0xf\n"
      "v_mfma_f32_16x16x32_bf16 v[180:183], a[188:191], v[144:147], v[180:183]\n"
      "s_add_u32 s50, s50, 24576\n"
      "v_add_f32_dpp v205, v205, v205 quad_perm:[2,3,0,1] row_mask:0xf bank_mask:0xf\n"
      "v_fmac_f32 v219, v177, v177\n"
      "v_mfma_f32_16x16x32_bf16 v[180:183], a[192:195], v[148:151], v[180:183]\n"
      "v_add_f32_dpp v205, v205, v205 row_half_mirror row_mask:0xf bank_mask:0xf\n"
      "v_fmac_f32 v219, v178, v178\n"
      "v_mfma_f32_16x16x32_bf16 v[180:183], a[196:199], v[152:155], v[180:183]\n"
      ".Lv_p1:\n"
      "s_cbranch_execz .Lv_pr\n"
      "v_add_f32_dpp v204, v204, v204 row_half_mirror row_mask:0xf bank_mask:0xf\n"
      "v_mfma_f32_16x16x32_bf16 v[180:183], a[200:203], v[156:159], v[180:183]\n"
      "v_mul_f32 v204, v27, v204\n"
      "v_fmaak_f32 v205, v27, v205, 0x36a7c5ac\n"
      "v_mfma_f32_16x16x32_bf16 v[180:183], a[204:207], v[160:163], v[180:183]\n"
      "s_waitcnt vmcnt(12) lgkmcnt(1)\n"
      "buffer_store_dword v25, v12, s[8:11], s50 offen sc1\n"
      "v_mfma_f32_16x16x32_bf16 v[180:183], a[208:211], v[164:167], v[180:183]\n"
      "s_waitcnt lgkmcnt(0)\n"
      "v_cmp_gt_i32 vcc, v21, v22\n"
      "v_add_f32 v218, v218, v176\n"
      "v_mfma_f32_16x16x32_bf16 v[180:183], a[212:215], v[168:171], v[180:183]\n"
      "v_add_f32 v218, v218, v177\n"
      "v_add_f32_e64 v218, v218, v178\n"
      "v_mfma_f32_16x16x32_bf16 v[184:187], a[216:219], v[100:103], v[184:187]\n"
      "buffer_store_dwordx2 v[204:205], v20, s[8:11], s49 offen sc1\n"
      "v_mfma_f32_16x16x32_bf16 v[184:187], a[220:223], v[104:107], v[184:187]\n"
      "s_sub_u32 s47, s46, s45\n"
      "s_cbranch_vccnz .Lsp_vy1_27\n"
      ".Lbk_vy1_27:\n"
      "v_mfma_f32_16x16x32_bf16 v[184:187], a[224:227], v[108:111], v[184:187]\n"
      "s_waitcnt vmcnt(11)\n"
      "ds_read_b128 v[104:107], v10 offset:29696\n"
      "v_and_b32 v177, 0xffff0000, v194\n"
      "v_mfma_f32_16x16x32_bf16 v[184:187], a[228:231], v[112:115], v[184:187]\n"
      "v_fmac_f32 v219, v179, v179\n"
      "v_mfma_f32_16x16x32_bf16 v[184:187], a[232:235], v[116:119], v[184:187]\n"
      "v_fmac_f32 v219, v180, v180\n"
      "v_add_f32 v218, v218, v179\n"
      "v_mfma_f32_16x16x32_bf16 v[184:187], a[236:239], v[120:123], v[184:187]\n"
      "v_cvt_pk_bf16_f32 v212, v180, v181\n"
      "v_and_b32 v179, 0xffff0000, v195\n"
      "v_mfma_f32_16x16x32_bf16 v[184:187], a[240:243], v[124:127], v[184:187]\n"
      "ds_read_b128 v[120:123], v10 offset:33792\n"
      "v_lshlrev_b32 v174, 16, v193\n"
      "v_mfma_f32_16x16x32_bf16 v[184:187], a[244:247], v[128:131], v[184:187]\n"
      "ds_read_b128 v[112:115], v10 offset:31744\n"
      "v_add_f32 v218, v218, v180\n"
      "v_mfma_f32_16x16x32_bf16 v[184:187], a[248:251], v[132:135], v[184:187]\n"
      "ds_read_b128 v[100:103], v10 offset:28672\n"
      "v_add_f32 v218, v218, v181\n"
      "v_mfma_f32_16x16x32_bf16 v[184:187], a[252:255], v[136:139], v[184:187]\n"
      "ds_read_b128 v[124:127], v10 offset:34816\n"
      "v_mfma_f32_16x16x32_bf16 v[184:187], v[32:35], v[140:143], v[184:187]\n"
      "ds_read_b128 v[108:111], v10 offset:30720\n"
      "v_and_b32 v175, 0xffff0000, v193\n"
      "v_mfma_f32_16x16x32_bf16 v[184:187], v[36:39], v[144:147], v[184:187]\n"
      "ds_read_b128 v[116:119], v10 offset:32768\n"
      "v_fmac_f32 v219, v181, v181\n"
      "v_mfma_f32_16x16x32_bf16 v[184:187], v[40:43], v[148:151], v[184:187]\n"
      "v_add_f32 v218, v218, v182\n"
      "v_mfma_f32_16x16x32_bf16 v[184:187], v[44:47], v[152:155], v[184:187]\n"
      "ds_read2_b32 v[132:133], v8 offset0:0 offset1:32\n"
      "v_and_b32 v173, 0xffff0000, v192\n"
      "v_mfma_f32_16x16x32_bf16 v[184:187], v[48:51], v[156:159], v[184:187]\n"
      "ds_read_b128 v[128:131], v10 offset:35840\n"
      "v_lshlrev_b32 v178, 16, v195\n"
      "v_mfma_f32_16x16x32_bf16 v[184:187], v[52:55], v[160:163], v[184:187]\n"
      "ds_read_b128 v[156:159], v10 offset:25600\n"
      "v_lshlrev_b32 v172, 16, v192\n"
      "v_mfma_f32_16x16x32_bf16 v[184:187], v[56:59], v[164:167], v[184:187]\n"
      "ds_read_b128 v[144:147], v10 offset:22528\n"
      "v_lshlrev_b32 v176, 16, v194\n"
      "v_mfma_f32_16x16x32_bf16 v[184:187], v[60:63], v[168:171], v[184:187]\n"
      "s_waitcnt lgkmcnt(5)\n"
      "buffer_load_dwordx4 v[192:195], v13, s[8:11], s47 offen sc1\n"
      "v_mfma_f32_16x16x32_bf16 v[188:191], v[64:67], v[100:103], 0\n"
      "ds_read2_b32 v[134:135], v8 offset0:64 offset1:96\n"
      "v_fmac_f32 v219, v182, v182\n"
      "v_mfma_f32_16x16x32_bf16 v[188:191], v[68:71], v[104:107], v[188:191]\n"
      "ds_read_b128 v[148:151], v10 offset:23552\n"
      "v_add_f32 v218, v218, v183\n"
      "v_mfma_f32_16x16x32_bf16 v[188:191], v[72:75], v[108:111], v[188:191]\n"
      "s_mov_b32 s46, s36\n"
      "v_cvt_pk_bf16_f32 v213, v182, v183\n"
      "v_cvt_pk_bf16_f32 v215, v186, v187\n"
      "v_mfma_f32_16x16x32_bf16 v[188:191], v[76:79], v[112:115], v[188:191]\n"
      "s_waitcnt lgkmcnt(4)\n"
      "ds_read_b128 v[160:163], v10 offset:26624\n"
      "v_fmac_f32 v219, v183, v183\n"
      "v_mfma_f32_16x16x32_bf16 v[188:191], v[80:83], v[116:119], v[188:191]\n"
      "ds_read_b128 v[140:143], v10 offset:21504\n"
      "v_fmac_f32 v219, v184, v184\n"
      "v_mfma_f32_16x16x32_bf16 v[188:191], v[84:87], v[120:123], v[188:191]\n"
      "ds_read2_b32 v[168:169], v9 offset0:0 offset1:32\n"
      "v_add_f32 v218, v218, v184\n"
      "v_mfma_f32_16x16x32_bf16 v[188:191], v[88:91], v[124:127], v[188:191]\n"
      "ds_read_b128 v[152:155], v10 offset:24576\n"
      "v_fmac_f32 v219, v185, v185\n"
      "v_mfma_f32_16x16x32_bf16 v[188:191], v[92:95], v[128:131], v[188:191]\n"
      "s_waitcnt lgkmcnt(5)\n"
      "ds_read_b128 v[136:139], v10 offset:20480\n"
      "v_add_f32 v218, v218, v185\n"
      "v_mfma_f32_16x16x32_bf16 v[188:191], v[96:99], v[132:135], v[188:191]\n"
      "s_cmp_eq_u32 s36, s37\n"
      "s_cselect_b32 s48, 49152, s51\n"
      "ds_read_b128 v[164:167], v10 offset:27648\n"
      "v_mfma_f32_16x16x32_bf16 v[172:175], a[0:3], v[100:103], v[172:175]\n"
      "ds_read2_b32 v[170:171], v9 offset0:64 offset1:96\n"
      "v_cvt_pk_bf16_f32 v214, v184, v185\n"
      "v_mfma_f32_16x16x32_bf16 v[172:175], a[4:7], v[104:107], v[172:175]\n"
      "v_fmac_f32 v219, v186, v186\n"
      "v_add_f32 v218, v218, v186\n"
      "v_mfma_f32_16x16x32_bf16 v[172:175], a[8:11], v[108:111], v[172:175]\n"
      "ds_write_b32 v17, v189 offset:256\n"
      "v_mfma_f32_16x16x32_bf16 v[172:175], a[12:15], v[112:115], v[172:175]\n"
      "v_add_f32 v218, v218, v187\n"
      "v_add_u32 v21, 1, v21\n"
      "v_mfma_f32_16x16x32_bf16 v[172:175], a[16:19], v[116:119], v[172:175]\n"
      "buffer_store_dwordx4 v[212:215], v15, s[8:11], s38 offen offset:1024 sc1\n"
      "v_mfma_f32_16x16x32_bf16 v[172:175], a[20:23], v[120:123], v[172:175]\n"
      "ds_write_b32 v17, v190 offset:512\n"
      "v_mfma_f32_16x16x32_bf16 v[172:175], a[24:27], v[124:127], v[172:175]\n"
      "v_fmac_f32 v219, v187, v187\n"
      "v_mfma_f32_16x16x32_bf16 v[172:175], a[28:31], v[128:131], v[172:175]\n"
      "s_add_u32 s36, s36, s48\n"
      "ds_write_b32 v18, v218\n"
      "v_mfma_f32_16x16x32_bf16 v[172:175], a[32:35], v[132:135], v[172:175]\n"
      "s_waitcnt vmcnt(11) lgkmcnt(3)\n"
      "v_cmp_ne_u32 vcc, s21, v28\n"
      "v_mfma_f32_16x16x32_bf16 v[172:175], a[36:39], v[136:139], v[172:175]\n"
      "ds_write_b32 v17, v188 offset:0\n"
      "v_mfma_f32_16x16x32_bf16 v[172:175], a[40:43], v[140:143], v[172:175]\n"
      "s_waitcnt vmcnt(9) lgkmcnt(3)\n"
      "s_min_u32 s36, s36, s6\n"
      "v_lshlrev_b32 v182, 16, v197\n"
      "v_mfma_f32_16x16x32_bf16 v[172:175], a[44:47], v[144:147], v[172:175]\n"
      "ds_write_b32 v17, v191 offset:768\n"
      "v_mfma_f32_16x16x32_bf16 v[172:175], a[48:51], v[148:151], v[172:175]\n"
      "s_cbranch_vccnz .Lsp_v1_28\n"
      ".Lbk_v1_28:\n"
      "v_and_b32 v181, 0xffff0000, v196\n"
      "v_mfma_f32_16x16x32_bf16 v[172:175], a[52:55], v[152:155], v[172:175]\n"
      "ds_write_b32 v18, v219 offset:4\n"
      "v_mfma_f32_16x16x32_bf16 v[172:175], a[56:59], v[156:159], v[172:175]\n"
      "buffer_load_dword v28, v11, s[8:11], s36 offen sc1\n"
      "v_mfma_f32_16x16x32_bf16 v[172:175], a[60:63], v[160:163], v[172:175]\n"
      "s_add_u32 m0, s39, 20480\n"
      "ds_write_b32 v23, v21\n"
      "v_mfma_f32_16x16x32_bf16 v[172:175], a[64:67], v[164:167], v[172:175]\n"
      "v_and_b32 v187, 0xffff0000, v199\n"
      "v_and_b32 v183, 0xffff0000, v197\n"
      "v_mfma_f32_16x16x32_bf16 v[172:175], a[68:71], v[168:171], v[172:175]\n"
      "buffer_load_dwordx4 v0, s[8:11], s46 offen offset:0 sc1 lds\n"
      "v_mfma_f32_16x16x32_bf16 v[176:179], a[72:75], v[100:103], v[176:179]\n"
      "s_add_u32 s49, s49, 24576\n"
      "v_mfma_f32_16x16x32_bf16 v[176:179], a[76:79], v[104:107], v[176:179]\n"
      "v_mfma_f32_16x16x32_bf16 v[176:179], a[80:83], v[108:111], v[176:179]\n"
      "v_cvt_pk_bf16_f32 v209, v174, v175\n"
      "v_mfma_f32_16x16x32_bf16 v[176:179], a[84:87], v[112:115], v[176:179]\n"
      "buffer_load_dwordx4 v0, s[8:11], s46 offen offset:1024 sc1 lds\n"
      "v_mfma_f32_16x16x32_bf16 v[176:179], a[88:91], v[116:119], v[176:179]\n"
      "v_add_f32 v218, v172, v173\n"
      "v_mfma_f32_16x16x32_bf16 v[176:179], a[92:95], v[120:123], v[176:179]\n"
      "v_add_f32 v218, v218, v174\n"
      "v_lshlrev_b32 v180, 16, v196\n"
      "v_mfma_f32_16x16x32_bf16 v[176:179], a[96:99], v[124:127], v[176:179]\n"
      "ds_read_b32 v22, v24\n"
      "v_lshlrev_b32 v186, 16, v199\n"
      "v_mfma_f32_16x16x32_bf16 v[176:179], a[100:103], v[128:131], v[176:179]\n"
      "buffer_load_dwordx4 v0, s[8:11], s46 offen offset:2048 sc1 lds\n"
      "v_mfma_f32_16x16x32_bf16 v[176:179], a[104:107], v[132:135], v[176:179]\n"
      "v_mul_f32 v219, v172, v172\n"
      "v_fmac_f32 v219, v173, v173\n"
      "v_mfma_f32_16x16x32_bf16 v[176:179], a[108:111], v[136:139], v[176:179]\n"
      "v_lshlrev_b32 v184, 16, v198\n"
      "v_mfma_f32_16x16x32_bf16 v[176:179], a[112:115], v[140:143], v[176:179]\n"
      "v_fmac_f32_e64 v219, v174, v174\n"
      "v_mfma_f32_16x16x32_bf16 v[176:179], a[116:119], v[144:147], v[176:179]\n"
      "s_waitcnt lgkmcnt(0)\n"
      "v_cmp_gt_i32 vcc, v21, v22\n"
      "v_add_f32_e64 v218, v218, v175\n"
      "v_mfma_f32_16x16x32_bf16 v[176:179], a[120:123], v[148:151], v[176:179]\n"
      "v_mfma_f32_16x16x32_bf16 v[176:179], a[124:127], v[152:155], v[176:179]\n"
      "v_and_b32 v185, 0xffff0000, v198\n"
      "v_cvt_pk_bf16_f32 v208, v172, v173\n"
      "v_mfma_f32_16x16x32_bf16 v[176:179], a[128:131], v[156:159], v[176:179]\n"
      "buffer_load_dwordx4 v[196:199], v13, s[8:11], s47 offen offset:1024 sc1\n"
      "v_mfma_f32_16x16x32_bf16 v[176:179], a[132:135], v[160:163], v[176:179]\n"
      "s_cbranch_vccnz .Lsp_vz1_29\n"
      ".Lbk_vz1_29:\n"
      "ds_read_b128 v[204:207], v19\n"
      "v_mfma_f32_16x16x32_bf16 v[176:179], a[136:139], v[164:167], v[176:179]\n"
      "v_add_u32 v21, 1, v21\n"
      "v_mfma_f32_16x16x32_bf16 v[176:179], a[140:143], v[168:171], v[176:179]\n"
      "s_waitcnt vmcnt(10)\n"
      "ds_write_b32 v23, v21\n"
      "v_mfma_f32_16x16x32_bf16 v[180:183], a[144:147], v[100:103], v[180:183]\n"
      "v_fmac_f32 v219, v175, v175\n"
      "v_mfma_f32_16x16x32_bf16 v[180:183], a[148:151], v[104:107], v[180:183]\n"
      "buffer_load_dwordx4 v0, s[8:11], s46 offen offset:3072 sc1 lds\n"
      "v_mfma_f32_16x16x32_bf16 v[180:183], a[152:155], v[108:111], v[180:183]\n"
      "v_cvt_pk_bf16_f32 v210, v176, v177\n"
      "v_fmac_f32 v219, v176, v176\n"
      "v_mfma_f32_16x16x32_bf16 v[180:183], a[156:159], v[112:115], v[180:183]\n"
      "s_add_u32 m0, s40, 20480\n"
      "v_fmac_f32 v219, v177, v177\n"
      "v_mfma_f32_16x16x32_bf16 v[180:183], a[160:163], v[116:119], v[180:183]\n"
      "s_waitcnt lgkmcnt(1)\n"
      "v_add_f32 v204, v204, v206\n"
      "v_add_f32 v205, v205, v207\n"
      "v_mfma_f32_16x16x32_bf16 v[180:183], a[164:167], v[120:123], v[180:183]\n"
      "v_add_f32_dpp v204, v204, v204 quad_perm:[1,0,3,2] row_mask:0xf bank_mask:0xf\n"
      "v_fmac_f32 v219, v178, v178\n"
      "v_mfma_f32_16x16x32_bf16 v[180:183], a[168:171], v[124:127], v[180:183]\n"
      "buffer_load_dwordx4 v1, s[8:11], s46 offen offset:0 sc1 lds\n"
      "v_mfma_f32_16x16x32_bf16 v[180:183], a[172:175], v[128:131], v[180:183]\n"
      "v_add_f32_dpp v205, v205, v205 quad_perm:[1,0,3,2] row_mask:0xf bank_mask:0xf\n"
      "v_add_f32_dpp v204, v204, v204 quad_perm:[2,3,0,1] row_mask:0xf bank_mask:0xf\n"
      "v_mfma_f32_16x16x32_bf16 v[180:183], a[176:179], v[132:135], v[180:183]\n"
      "v_add_f32_dpp v204, v204, v204 row_half_mirror row_mask:0xf bank_mask:0xf\n"
      "v_cvt_pk_bf16_f32 v211, v178, v179\n"
      "v_mfma_f32_16x16x32_bf16 v[180:183], a[180:183], v[136:139], v[180:183]\n"
      "s_add_u32 s38, s38, 24576\n"
      "v_mul_f32 v204, v27, v204\n"
      "v_add_f32_dpp v205, v205, v205 quad_perm:[2,3,0,1] row_mask:0xf bank_mask:0xf\n"
      "v_mfma_f32_16x16x32_bf16 v[180:183], a[184:187], v[140:143], v[180:183]\n"
      "ds_read_b32 v22, v24\n"
      "v_add_f32 v218, v218, v176\n"
      "v_mfma_f32_16x16x32_bf16 v[180:183], a[188:191], v[144:147], v[180:183]\n"
      "v_add_f32_dpp v205, v205, v205 row_half_mirror row_mask:0xf bank_mask:0xf\n"
      "v_fmaak_f32 v205, v27, v205, 0x36a7c5ac\n"
      "v_mfma_f32_16x16x32_bf16 v[180:183], a[192:195], v[148:151], v[180:183]\n"
      "buffer_store_dwordx2 v[204:205], v20, s[8:11], s49 offen sc1\n"
      "v_mfma_f32_16x16x32_bf16 v[180:183], a[196:199], v[152:155], v[180:183]\n"
      "s_add_u32 s50, s50, 24576\n"
      "v_add_f32 v218, v218, v177\n"
      "v_mfma_f32_16x16x32_bf16 v[180:183], a[200:203], v[156:159], v[180:183]\n"
      "v_add_f32 v218, v218, v178\n"
      "v_mfma_f32_16x16x32_bf16 v[180:183], a[204:207], v[160:163], v[180:183]\n"
      "s_waitcnt lgkmcnt(0)\n"
      "v_add_f32 v218, v218, v179\n"
      "v_cmp_gt_i32 vcc, v21, v22\n"
      "v_mfma_f32_16x16x32_bf16 v[180:183], a[208:211], v[164:167], v[180:183]\n"
      "s_waitcnt vmcnt(10)\n"
      "buffer_store_dword v25, v12, s[8:11], s50 offen sc1\n"
      "v_mfma_f32_16x16x32_bf16 v[180:183], a[212:215], v[168:171], v[180:183]\n"
      "v_fmac_f32 v219, v179, v179\n"
      "v_mfma_f32_16x16x32_bf16 v[184:187], a[216:219], v[100:103], v[184:187]\n"
      "s_cbranch_vccnz .Lsp_vy0_30\n"
      ".Lbk_vy0_30:\n"
      "s_sub_u32 s47, s46, s45\n"
      "v_mfma_f32_16x16x32_bf16 v[184:187], a[220:223], v[104:107], v[184:187]\n"
      "s_waitcnt vmcnt(10)\n"
      "v_lshlrev_b32 v174, 16, v193\n"
      "v_and_b32 v179, 0xffff0000, v195\n"
      "v_mfma_f32_16x16x32_bf16 v[184:187], a[224:227], v[108:111], v[184:187]\n"
      "ds_read_b128 v[100:103], v0 offset:8192\n"
      "v_cvt_pk_bf16_f32 v213, v182, v183\n"
      "v_mfma_f32_16x16x32_bf16 v[184:187], a[228:231], v[112:115], v[184:187]\n"
      "ds_read_b128 v[108:111], v0 offset:10240\n"
      "v_and_b32 v173, 0xffff0000, v192\n"
      "v_mfma_f32_16x16x32_bf16 v[184:187], a[232:235], v[116:119], v[184:187]\n"
      "v_fmac_f32 v219, v180, v180\n"
      "v_mfma_f32_16x16x32_bf16 v[184:187], a[236:239], v[120:123], v[184:187]\n"
      "ds_read_b128 v[120:123], v0 offset:13312\n"
      "v_add_f32 v218, v218, v180\n"
      "v_mfma_f32_16x16x32_bf16 v[184:187], a[240:243], v[124:127], v[184:187]\n"
      "v_add_f32 v218, v218, v181\n"
      "v_add_f32 v218, v218, v182\n"
      "v_mfma_f32_16x16x32_bf16 v[184:187], a[244:247], v[128:131], v[184:187]\n"
      "ds_read_b128 v[128:131], v0 offset:15360\n"
      "v_lshlrev_b32 v178, 16, v195\n"
      "v_mfma_f32_16x16x32_bf16 v[184:187], a[248:251], v[132:135], v[184:187]\n"
      "v_mfma_f32_16x16x32_bf16 v[184:187], a[252:255], v[136:139], v[184:187]\n"
      "ds_read_b128 v[104:107], v0 offset:9216\n"
      "v_lshlrev_b32 v172, 16, v192\n"
      "v_mfma_f32_16x16x32_bf16 v[184:187], v[32:35], v[140:143], v[184:187]\n"
      "buffer_store_dwordx4 v[208:211], v15, s[8:11], s38 offen sc1\n"
      "v_mfma_f32_16x16x32_bf16 v[184:187], v[36:39], v[144:147], v[184:187]\n"
      "s_mov_b32 s46, s36\n"
      "ds_read_b128 v[112:115], v0 offset:11264\n"
      "v_fmac_f32 v219, v181, v181\n"
      "v_mfma_f32_16x16x32_bf16 v[184:187], v[40:43], v[148:151], v[184:187]\n"
      "ds_read2_b32 v[134:135], v2 offset0:64 offset1:96\n"
      "v_cvt_pk_bf16_f32 v212, v180, v181\n"
      "v_mfma_f32_16x16x32_bf16 v[184:187], v[44:47], v[152:155], v[184:187]\n"
      "ds_read2_b32 v[132:133], v2 offset0:0 offset1:32\n"
      "s_add_u32 s36, s36, 24576\n"
      "v_mfma_f32_16x16x32_bf16 v[184:187], v[48:51], v[156:159], v[184:187]\n"
      "ds_read_b128 v[116:119], v0 offset:12288\n"
      "v_fmac_f32 v219, v182, v182\n"
      "v_mfma_f32_16x16x32_bf16 v[184:187], v[52:55], v[160:163], v[184:187]\n"
      "ds_read_b128 v[124:127], v0 offset:14336\n"
      "v_add_f32 v218, v218, v183\n"
      "v_mfma_f32_16x16x32_bf16 v[184:187], v[56:59], v[164:167], v[184:187]\n"
      "s_waitcnt lgkmcnt(5)\n"
      "ds_read_b128 v[160:163], v0 offset:6144\n"
      "v_and_b32 v177, 0xffff0000, v194\n"
      "v_mfma_f32_16x16x32_bf16 v[184:187], v[60:63], v[168:171], v[184:187]\n"
      "s_sub_u32 s20, s20, 1\n"
      "s_cbranch_scc0 .Lv_loop\n"
      ".Lv_tail:\n"
      "s_nop 8\n"
      "v_add_f32 v218, v218, v184\n"
      "v_add_f32 v218, v218, v185\n"
      "v_fmac_f32 v219, v183, v183\n"
      "v_fmac_f32 v219, v184, v184\n"
      "v_cvt_pk_bf16_f32 v215, v186, v187\n"
      "v_add_f32 v218, v218, v186\n"
      "v_add_f32 v218, v218, v187\n"
      "v_fmac_f32 v219, v185, v185\n"
      "v_cvt_pk_bf16_f32 v214, v184, v185\n"
      "ds_write_b32 v18, v218\n"
      "v_fmac_f32 v219, v186, v186\n"
      "buffer_store_dwordx4 v[212:215], v15, s[8:11], s38 offen offset:1024 sc1\n"
      "v_fmac_f32 v219, v187, v187\n"
      "ds_write_b32 v18, v219 offset:4\n"
      "s_waitcnt lgkmcnt(0)\n"
      "s_barrier\n"
      "s_add_u32 s38, s38, 24576\n"
      "ds_read_b128 v[204:207], v19\n"
      "s_add_u32 s49, s49, 24576\n"
      "s_waitcnt lgkmcnt(0)\n"
      "v_add_f32 v204, v204, v206\n"
      "v_add_f32 v205, v205, v207\n"
      "s_nop 0\n"
      "v_add_f32_dpp v204, v204, v204 quad_perm:[1,0,3,2] row_mask:0xf bank_mask:0xf\n"
      "s_nop 1\n"
      "v_add_f32_dpp v204, v204, v204 quad_perm:[2,3,0,1] row_mask:0xf bank_mask:0xf\n"
      "v_add_f32_dpp v205, v205, v205 quad_perm:[1,0,3,2] row_mask:0xf bank_mask:0xf\n"
      "s_add_u32 s50, s50, 24576\n"
      "s_nop 0\n"
      "v_add_f32_dpp v205, v205, v205 quad_perm:[2,3,0,1] row_mask:0xf bank_mask:0xf\n"
      "s_nop 1\n"
      "v_add_f32_dpp v205, v205, v205 row_half_mirror row_mask:0xf bank_mask:0xf\n"
      "v_add_f32_dpp v204, v204, v204 row_half_mirror row_mask:0xf bank_mask:0xf\n"
      "v_mul_f32 v204, v27, v204\n"
      "v_fmaak_f32 v205, v27, v205, 0x36a7c5ac\n"
      "s_waitcnt vmcnt(3)\n"
      "buffer_store_dword v25, v12, s[8:11], s50 offen sc1\n"
      "buffer_store_dwordx2 v[204:205], v20, s[8:11], s49 offen sc1\n"
      "s_add_u32 s49, s49, 24576\n"
      "s_add_u32 s50, s50, 24576\n"
      "s_waitcnt vmcnt(0)\n"
      "buffer_store_dword v25, v12, s[8:11], s50 offen sc1\n"
      "s_add_u32 s50, s50, 24576\n"
      "s_endpgm\n"
      ".Lsp_w0_1:\n"
      "s_mov_b32 s22, s23\n"
      ".Lsl_w0_1:\n"
      "buffer_load_dword v28, v11, s[8:11], s46 offen sc1\n"
      "s_waitcnt vmcnt(0)\n"
      "v_cmp_ne_u32 vcc, s21, v28\n"
      "s_cbranch_vccz .Lbk_w0_1\n"
      "s_sleep 4\n"
      "s_sub_u32 s22, s22, 1\n"
      "s_cbranch_scc0 .Lsl_w0_1\n"
      "s_mov_b32 s23, 0\n"
      "s_branch .Lbk_w0_1\n"
      ".Lsp_w1_2:\n"
      "s_mov_b32 s22, s23\n"
      ".Lsl_w1_2:\n"
      "buffer_load_dword v28, v11, s[8:11], s46 offen sc1\n"
      "s_waitcnt vmcnt(0)\n"
      "v_cmp_ne_u32 vcc, s21, v28\n"
      "s_cbranch_vccz .Lbk_w1_2\n"
      "s_sleep 4\n"
      "s_sub_u32 s22, s22, 1\n"
      "s_cbranch_scc0 .Lsl_w1_2\n"
      "s_mov_b32 s23, 0\n"
      "s_branch .Lbk_w1_2\n"
      ".Lsp_wy0_3:\n"
      "s_lshl_b32 s22, s23, 4\n"
      ".Lsl_wy0_3:\n"
      "ds_read_b32 v22, v24\n"
      "s_waitcnt lgkmcnt(0)\n"
      "v_cmp_gt_i32 vcc, v21, v22\n"
      "s_cbranch_vccz .Lbk_wy0_3\n"
      "s_sub_u32 s22, s22, 1\n"
      "s_cbranch_scc0 .Lsl_wy0_3\n"
      "s_mov_b32 s23, 0\n"
      "s_branch .Lbk_wy0_3\n"
      ".Lsp_w0_4:\n"
      "s_mov_b32 s22, s23\n"
      "buffer_load_dword v28, v11, s[8:11], s46 offen sc1\n"
      "s_waitcnt vmcnt(0)\n"
      "v_cmp_ne_u32 vcc, s21, v28\n"
      "s_add_u32 s30, s50, 0\n"
      "buffer_store_dword v25, v12, s[8:11], s30 offen sc1\n"
      "s_add_u32 s30, s50, 24576\n"
      "buffer_store_dword v25, v12, s[8:11], s30 offen sc1\n"
      "s_cbranch_vccz .Lbk_w0_4\n"
      ".Lsl_w0_4:\n"
      "buffer_load_dword v28, v11, s[8:11], s46 offen sc1\n"
      "s_waitcnt vmcnt(0)\n"
      "v_cmp_ne_u32 vcc, s21, v28\n"
      "s_cbranch_vccz .Lbk_w0_4\n"
      "s_sleep 4\n"
      "s_sub_u32 s22, s22, 1\n"
      "s_cbranch_scc0 .Lsl_w0_4\n"
      "s_mov_b32 s23, 0\n"
      "s_branch .Lbk_w0_4\n"
      ".Lsp_wz0_5:\n"
      "s_lshl_b32 s22, s23, 4\n"
      ".Lsl_wz0_5:\n"
      "ds_read_b32 v22, v24\n"
      "s_waitcnt lgkmcnt(0)\n"
      "v_cmp_gt_i32 vcc, v21, v22\n"
      "s_cbranch_vccz .Lbk_wz0_5\n"
      "s_sub_u32 s22, s22, 1\n"
      "s_cbranch_scc0 .Lsl_wz0_5\n"
      "s_mov_b32 s23, 0\n"
      "s_branch .Lbk_wz0_5\n"
      ".Lsp_wy1_6:\n"
      "s_lshl_b32 s22, s23, 4\n"
      ".Lsl_wy1_6:\n"
      "ds_read_b32 v22, v24\n"
      "s_waitcnt lgkmcnt(0)\n"
      "v_cmp_gt_i32 vcc, v21, v22\n"
      "s_cbranch_vccz .Lbk_wy1_6\n"
      "s_sub_u32 s22, s22, 1\n"
      "s_cbranch_scc0 .Lsl_wy1_6\n"
      "s_mov_b32 s23, 0\n"
      "s_branch .Lbk_wy1_6\n"
      ".Lsp_w1_7:\n"
      "s_mov_b32 s22, s23\n"
      "buffer_load_dword v28, v11, s[8:11], s46 offen sc1\n"
      "s_waitcnt vmcnt(0)\n"
      "v_cmp_ne_u32 vcc, s21, v28\n"
      "s_add_u32 s30, s50, 24576\n"
      "buffer_store_dword v25, v12, s[8:11], s30 offen sc1\n"
      "s_cbranch_vccz .Lbk_w1_7\n"
      ".Lsl_w1_7:\n"
      "buffer_load_dword v28, v11, s[8:11], s46 offen sc1\n"
      "s_waitcnt vmcnt(0)\n"
      "v_cmp_ne_u32 vcc, s21, v28\n"
      "s_cbranch_vccz .Lbk_w1_7\n"
      "s_sleep 4\n"
      "s_sub_u32 s22, s22, 1\n"
      "s_cbranch_scc0 .Lsl_w1_7\n"
      "s_mov_b32 s23, 0\n"
      "s_branch .Lbk_w1_7\n"
      ".Lsp_wz1_8:\n"
      "s_lshl_b32 s22, s23, 4\n"
      ".Lsl_wz1_8:\n"
      "ds_read_b32 v22, v24\n"
      "s_waitcnt lgkmcnt(0)\n"
      "v_cmp_gt_i32 vcc, v21, v22\n"
      "s_cbranch_vccz .Lbk_wz1_8\n"
      "s_sub_u32 s22, s22, 1\n"
      "s_cbranch_scc0 .Lsl_wz1_8\n"
      "s_mov_b32 s23, 0\n"
      "s_branch .Lbk_wz1_8\n"
      ".Lsp_wy0_9:\n"
      "s_lshl_b32 s22, s23, 4\n"
      ".Lsl_wy0_9:\n"
      "ds_read_b32 v22, v24\n"
      "s_waitcnt lgkmcnt(0)\n"
      "v_cmp_gt_i32 vcc, v21, v22\n"
      "s_cbranch_vccz .Lbk_wy0_9\n"
      "s_sub_u32 s22, s22, 1\n"
      "s_cbranch_scc0 .Lsl_wy0_9\n"
      "s_mov_b32 s23, 0\n"
      "s_branch .Lbk_wy0_9\n"
      ".Lsp_w0_10:\n"
      "s_mov_b32 s22, s23\n"
      "buffer_load_dword v28, v11, s[8:11], s46 offen sc1\n"
      "s_waitcnt vmcnt(0)\n"
      "v_cmp_ne_u32 vcc, s21, v28\n"
      "s_add_u32 s30, s50, 0\n"
      "buffer_store_dword v25, v12, s[8:11], s30 offen sc1\n"
      "s_add_u32 s30, s50, 24576\n"
      "buffer_store_dword v25, v12, s[8:11], s30 offen sc1\n"
      "s_cbranch_vccz .Lbk_w0_10\n"
      ".Lsl_w0_10:\n"
      "buffer_load_dword v28, v11, s[8:11], s46 offen sc1\n"
      "s_waitcnt vmcnt(0)\n"
      "v_cmp_ne_u32 vcc, s21, v28\n"
      "s_cbranch_vccz .Lbk_w0_10\n"
      "s_sleep 4\n"
      "s_sub_u32 s22, s22, 1\n"
      "s_cbranch_scc0 .Lsl_w0_10\n"
      "s_mov_b32 s23, 0\n"
      "s_branch .Lbk_w0_10\n"
      ".Lsp_wz0_11:\n"
      "s_lshl_b32 s22, s23, 4\n"
      ".Lsl_wz0_11:\n"
      "ds_read_b32 v22, v24\n"
      "s_waitcnt lgkmcnt(0)\n"
      "v_cmp_gt_i32 vcc, v21, v22\n"
      "s_cbranch_vccz .Lbk_wz0_11\n"
      "s_sub_u32 s22, s22, 1\n"
      "s_cbranch_scc0 .Lsl_wz0_11\n"
      "s_mov_b32 s23, 0\n"
      "s_branch .Lbk_wz0_11\n"
      ".Lsp_wy1_12:\n"
      "s_lshl_b32 s22, s23, 4\n"
      ".Lsl_wy1_12:\n"
      "ds_read_b32 v22, v24\n"
      "s_waitcnt lgkmcnt(0)\n"
      "v_cmp_gt_i32 vcc, v21, v22\n"
      "s_cbranch_vccz .Lbk_wy1_12\n"
      "s_sub_u32 s22, s22, 1\n"
      "s_cbranch_scc0 .Lsl_wy1_12\n"
      "s_mov_b32 s23, 0\n"
      "s_branch .Lbk_wy1_12\n"
      ".Lsp_w1_13:\n"
      "s_mov_b32 s22, s23\n"
      "buffer_load_dword v28, v11, s[8:11], s46 offen sc1\n"
      "s_waitcnt vmcnt(0)\n"
      "v_cmp_ne_u32 vcc, s21, v28\n"
      "s_add_u32 s30, s50, 24576\n"
      "buffer_store_dword v25, v12, s[8:11], s30 offen sc1\n"
      "s_cbranch_vccz .Lbk_w1_13\n"
      ".Lsl_w1_13:\n"
      "buffer_load_dword v28, v11, s[8:11], s46 offen sc1\n"
      "s_waitcnt vmcnt(0)\n"
      "v_cmp_ne_u32 vcc, s21, v28\n"
      "s_cbranch_vccz .Lbk_w1_13\n"
      "s_sleep 4\n"
      "s_sub_u32 s22, s22, 1\n"
      "s_cbranch_scc0 .Lsl_w1_13\n"
      "s_mov_b32 s23, 0\n"
      "s_branch .Lbk_w1_13\n"
      ".Lsp_wz1_14:\n"
      "s_lshl_b32 s22, s23, 4\n"
      ".Lsl_wz1_14:\n"
      "ds_read_b32 v22, v24\n"
      "s_waitcnt lgkmcnt(0)\n"
      "v_cmp_gt_i32 vcc, v21, v22\n"
      "s_cbranch_vccz .Lbk_wz1_14\n"
      "s_sub_u32 s22, s22, 1\n"
      "s_cbranch_scc0 .Lsl_wz1_14\n"
      "s_mov_b32 s23, 0\n"
      "s_branch .Lbk_wz1_14\n"
      ".Lsp_wy0_15:\n"
      "s_lshl_b32 s22, s23, 4\n"
      ".Lsl_wy0_15:\n"
      "ds_read_b32 v22, v24\n"
      "s_waitcnt lgkmcnt(0)\n"
      "v_cmp_gt_i32 vcc, v21, v22\n"
      "s_cbranch_vccz .Lbk_wy0_15\n"
      "s_sub_u32 s22, s22, 1\n"
      "s_cbranch_scc0 .Lsl_wy0_15\n"
      "s_mov_b32 s23, 0\n"
      "s_branch .Lbk_wy0_15\n"
      ".Lsp_v0_16:\n"
      "s_mov_b32 s22, s23\n"
      ".Lsl_v0_16:\n"
      "buffer_load_dword v28, v11, s[8:11], s46 offen sc1\n"
      "s_waitcnt vmcnt(0)\n"
      "v_cmp_ne_u32 vcc, s21, v28\n"
      "s_cbranch_vccz .Lbk_v0_16\n"
      "s_sleep 4\n"
      "s_sub_u32 s22, s22, 1\n"
      "s_cbranch_scc0 .Lsl_v0_16\n"
      "s_mov_b32 s23, 0\n"
      "s_branch .Lbk_v0_16\n"
      ".Lsp_v1_17:\n"
      "s_mov_b32 s22, s23\n"
      ".Lsl_v1_17:\n"
      "buffer_load_dword v28, v11, s[8:11], s46 offen sc1\n"
      "s_waitcnt vmcnt(0)\n"
      "v_cmp_ne_u32 vcc, s21, v28\n"
      "s_cbranch_vccz .Lbk_v1_17\n"
      "s_sleep 4\n"
      "s_sub_u32 s22, s22, 1\n"
      "s_cbranch_scc0 .Lsl_v1_17\n"
      "s_mov_b32 s23, 0\n"
      "s_branch .Lbk_v1_17\n"
      ".Lsp_vy0_18:\n"
      "s_lshl_b32 s22, s23, 4\n"
      ".Lsl_vy0_18:\n"
      "ds_read_b32 v22, v24\n"
      "s_waitcnt lgkmcnt(0)\n"
      "v_cmp_gt_i32 vcc, v21, v22\n"
      "s_cbranch_vccz .Lbk_vy0_18\n"
      "s_sub_u32 s22, s22, 1\n"
      "s_cbranch_scc0 .Lsl_vy0_18\n"
      "s_mov_b32 s23, 0\n"
      "s_branch .Lbk_vy0_18\n"
      ".Lsp_v0_19:\n"
      "s_mov_b32 s22, s23\n"
      "buffer_load_dword v28, v11, s[8:11], s46 offen sc1\n"
      "s_waitcnt vmcnt(0)\n"
      "v_cmp_ne_u32 vcc, s21, v28\n"
      "s_add_u32 s30, s50, 24576\n"
      "buffer_store_dword v25, v12, s[8:11], s30 offen sc1\n"
      "s_cbranch_vccz .Lbk_v0_19\n"
      ".Lsl_v0_19:\n"
      "buffer_load_dword v28, v11, s[8:11], s46 offen sc1\n"
      "s_waitcnt vmcnt(0)\n"
      "v_cmp_ne_u32 vcc, s21, v28\n"
      "s_cbranch_vccz .Lbk_v0_19\n"
      "s_sleep 4\n"
      "s_sub_u32 s22, s22, 1\n"
      "s_cbranch_scc0 .Lsl_v0_19\n"
      "s_mov_b32 s23, 0\n"
      "s_branch .Lbk_v0_19\n"
      ".Lsp_vz0_20:\n"
      "s_lshl_b32 s22, s23, 4\n"
      ".Lsl_vz0_20:\n"
      "ds_read_b32 v22, v24\n"
      "s_waitcnt lgkmcnt(0)\n"
      "v_cmp_gt_i32 vcc, v21, v22\n"
      "s_cbranch_vccz .Lbk_vz0_20\n"
      "s_sub_u32 s22, s22, 1\n"
      "s_cbranch_scc0 .Lsl_vz0_20\n"
      "s_mov_b32 s23, 0\n"
      "s_branch .Lbk_vz0_20\n"
      ".Lsp_vy1_21:\n"
      "s_lshl_b32 s22, s23, 4\n"
      ".Lsl_vy1_21:\n"
      "ds_read_b32 v22, v24\n"
      "s_waitcnt lgkmcnt(0)\n"
      "v_cmp_gt_i32 vcc, v21, v22\n"
      "s_cbranch_vccz .Lbk_vy1_21\n"
      "s_sub_u32 s22, s22, 1\n"
      "s_cbranch_scc0 .Lsl_vy1_21\n"
      "s_mov_b32 s23, 0\n"
      "s_branch .Lbk_vy1_21\n"
      ".Lsp_v1_22:\n"
      "s_mov_b32 s22, s23\n"
      "buffer_load_dword v28, v11, s[8:11], s46 offen sc1\n"
      "s_waitcnt vmcnt(0)\n"
      "v_cmp_ne_u32 vcc, s21, v28\n"
      "s_add_u32 s30, s50, 24576\n"
      "buffer_store_dword v25, v12, s[8:11], s30 offen sc1\n"
      "s_cbranch_vccz .Lbk_v1_22\n"
      ".Lsl_v1_22:\n"
      "buffer_load_dword v28, v11, s[8:11], s46 offen sc1\n"
      "s_waitcnt vmcnt(0)\n"
      "v_cmp_ne_u32 vcc, s21, v28\n"
      "s_cbranch_vccz .Lbk_v1_22\n"
      "s_sleep 4\n"
      "s_sub_u32 s22, s22, 1\n"
      "s_cbranch_scc0 .Lsl_v1_22\n"
      "s_mov_b32 s23, 0\n"
      "s_branch .Lbk_v1_22\n"
      ".Lsp_vz1_23:\n"
      "s_lshl_b32 s22, s23, 4\n"
      ".Lsl_vz1_23:\n"
      "ds_read_b32 v22, v24\n"
      "s_waitcnt lgkmcnt(0)\n"
      "v_cmp_gt_i32 vcc, v21, v22\n"
      "s_cbranch_vccz .Lbk_vz1_23\n"
      "s_sub_u32 s22, s22, 1\n"
      "s_cbranch_scc0 .Lsl_vz1_23\n"
      "s_mov_b32 s23, 0\n"
      "s_branch .Lbk_vz1_23\n"
      ".Lsp_vy0_24:\n"
      "s_lshl_b32 s22, s23, 4\n"
      ".Lsl_vy0_24:\n"
      "ds_read_b32 v22, v24\n"
      "s_waitcnt lgkmcnt(0)\n"
      "v_cmp_gt_i32 vcc, v21, v22\n"
      "s_cbranch_vccz .Lbk_vy0_24\n"
      "s_sub_u32 s22, s22, 1\n"
      "s_cbranch_scc0 .Lsl_vy0_24\n"
      "s_mov_b32 s23, 0\n"
      "s_branch .Lbk_vy0_24\n"
      ".Lsp_v0_25:\n"
      "s_mov_b32 s22, s23\n"
      "buffer_load_dword v28, v11, s[8:11], s46 offen sc1\n"
      "s_waitcnt vmcnt(0)\n"
      "v_cmp_ne_u32 vcc, s21, v28\n"
      "s_add_u32 s30, s50, 24576\n"
      "buffer_store_dword v25, v12, s[8:11], s30 offen sc1\n"
      "s_cbranch_vccz .Lbk_v0_25\n"
      ".Lsl_v0_25:\n"
      "buffer_load_dword v28, v11, s[8:11], s46 offen sc1\n"
      "s_waitcnt vmcnt(0)\n"
      "v_cmp_ne_u32 vcc, s21, v28\n"
      "s_cbranch_vccz .Lbk_v0_25\n"
      "s_sleep 4\n"
      "s_sub_u32 s22, s22, 1\n"
      "s_cbranch_scc0 .Lsl_v0_25\n"
      "s_mov_b32 s23, 0\n"
      "s_branch .Lbk_v0_25\n"
      ".Lsp_vz0_26:\n"
      "s_lshl_b32 s22, s23, 4\n"
      ".Lsl_vz0_26:\n"
      "ds_read_b32 v22, v24\n"
      "s_waitcnt lgkmcnt(0)\n"
      "v_cmp_gt_i32 vcc, v21, v22\n"
      "s_cbranch_vccz .Lbk_vz0_26\n"
      "s_sub_u32 s22, s22, 1\n"
      "s_cbranch_scc0 .Lsl_vz0_26\n"
      "s_mov_b32 s23, 0\n"
      "s_branch .Lbk_vz0_26\n"
      ".Lsp_vy1_27:\n"
      "s_lshl_b32 s22, s23, 4\n"
      ".Lsl_vy1_27:\n"
      "ds_read_b32 v22, v24\n"
      "s_waitcnt lgkmcnt(0)\n"
      "v_cmp_gt_i32 vcc, v21, v22\n"
      "s_cbranch_vccz .Lbk_vy1_27\n"
      "s_sub_u32 s22, s22, 1\n"
      "s_cbranch_scc0 .Lsl_vy1_27\n"
      "s_mov_b32 s23, 0\n"
      "s_branch .Lbk_vy1_27\n"
      ".Lsp_v1_28:\n"
      "s_mov_b32 s22, s23\n"
      "buffer_load_dword v28, v11, s[8:11], s46 offen sc1\n"
      "s_waitcnt vmcnt(0)\n"
      "v_cmp_ne_u32 vcc, s21, v28\n"
      "s_add_u32 s30, s50, 24576\n"
      "buffer_store_dword v25, v12, s[8:11], s30 offen sc1\n"
      "s_cbranch_vccz .Lbk_v1_28\n"
      ".Lsl_v1_28:\n"
      "buffer_load_dword v28, v11, s[8:11], s46 offen sc1\n"
      "s_waitcnt vmcnt(0)\n"
      "v_cmp_ne_u32 vcc, s21, v28\n"
      "s_cbranch_vccz .Lbk_v1_28\n"
      "s_sleep 4\n"
      "s_sub_u32 s22, s22, 1\n"
      "s_cbranch_scc0 .Lsl_v1_28\n"
      "s_mov_b32 s23, 0\n"
      "s_branch .Lbk_v1_28\n"
      ".Lsp_vz1_29:\n"
      "s_lshl_b32 s22, s23, 4\n"
      ".Lsl_vz1_29:\n"
      "ds_read_b32 v22, v24\n"
      "s_waitcnt lgkmcnt(0)\n"
      "v_cmp_gt_i32 vcc, v21, v22\n"
      "s_cbranch_vccz .Lbk_vz1_29\n"
      "s_sub_u32 s22, s22, 1\n"
      "s_cbranch_scc0 .Lsl_vz1_29\n"
      "s_mov_b32 s23, 0\n"
      "s_branch .Lbk_vz1_29\n"
      ".Lsp_vy0_30:\n"
      "s_lshl_b32 s22, s23, 4\n"
      ".Lsl_vy0_30:\n"
      "ds_read_b32 v22, v24\n"
      "s_waitcnt lgkmcnt(0)\n"
      "v_cmp_gt_i32 vcc, v21, v22\n"
      "s_cbranch_vccz .Lbk_vy0_30\n"
      "s_sub_u32 s22, s22, 1\n"
      "s_cbranch_scc0 .Lsl_vy0_30\n"
      "s_mov_b32 s23, 0\n"
      "s_branch .Lbk_vy0_30\n"
      ".Lw_last:\n"
      :
      : "{s[8:11]}"(rsWS), "{s[12:15]}"(rsA), "{v0}"(v_l16), "{v1}"(v_l5), "{v2}"(v_ex[0]), "{v3}"(v_ex[1]), "{v4}"(v_ex[2]), "{v5}"(v_ex[3]), "{v6}"(v_ex[4]), "{v7}"(v_ex[5]), "{v8}"(v_ex[6]), "{v9}"(v_ex[7]), "{v10}"(v_l2), "{v11}"(v_fa), "{v12}"(v_ra), "{v13}"(v_rp), "{v14}"(v_rs), "{v15}"(v_xp), "{v16}"(v_xs), "{v17}"(v_ew), "{v18}"(v_sw), "{v19}"(v_sr), "{v20}"(v_ss), "{v22}"(v_al), "{v23}"(v_yw), "{v24}"(v_yr), "{v26}"(v_hi), "{v100}"(v_vo[0]), "{v101}"(v_vo[1]), "{v102}"(v_vo[2]), "{v103}"(v_vo[3]), "{v104}"(v_vo[4]), "{v105}"(v_vo[5]), "{v106}"(v_vo[6]), "{v107}"(v_vo[7]), "{v108}"(v_vo[8]), "{v109}"(v_vp[0]), "{v110}"(v_vp[1]), "{v111}"(v_vp[2]), "{v112}"(v_vp[3]), "{v113}"(v_vp[4]), "{v114}"(v_vp[5]), "{v115}"(v_vp[6]), "{v116}"(v_vp[7]), "{v117}"(v_vp[8]), "{v118}"(v_t0), "{v119}"(v_t1), "{v120}"(v_ts), "{v121}"(v_pw), "{s36}"(s_sx), "{s37}"(s_ev), "{s38}"(s_sy), "{s39}"(s_m4), "{s40}"(s_m5), "{s41}"(s_m9), "{s42}"(s_so), "{s43}"(s_s8), "{s44}"(s_wg), "{s45}"(s_rb)
      : "v255", "a255", "s99", "vcc", "scc", "memory");

  } else if (fast && slot < 28) {
    const unsigned td = (slot - 10) / 3, gr = td < 4 ? td : (e + 5 - td) & 3, part = (td < 4 ? e : td) * 3 + (slot - 10) % 3;
    COMMON(40, 149760);
    const unsigned ks = part / 3, n3 = part % 3;
    const u32x4 rsA = srd(Wd, 4608 * 576 * 2), rsB = srd(out, 2048 * 576 * 2), rsC = srd(X, 2048 * 576 * 2);
    const unsigned v_l16b = v_l16 + 4096, v_l8 = 1024 + lane * 8, f0 = 24 * ks + 6 * w;
    const unsigned v_fa = 147456 - f0 * 1024 + (f0 + (l8 < 5 ? l8 : 5)) * 128 + l7 / 4, v_ga = (l8 < 4 ? l8 : 4) * 4 * PW + 1536 + l7 / 4;
    const unsigned v_ra = up * 6 * PREC + l5 + 1536, v_rf = up * 33 * REC + l5 + 20480 + (4 * n3 + w) * 128;
    const unsigned v_pw = l8 < 5 ? (l8 + 7) * PREC + l8 * 4 * PW + 1536 + l7 : OOB, v_pu = ks && l8 + ks < 5 ? OOB : v_pw - 6 * PREC;
    const unsigned pf = 3 * (w >> 1) + 2 * (w & 1), sf = 3 * (w >> 1) + 1;
    const unsigned v_at = (8 * g + q) * 384 + 64 * pf + 16 * p, v_as = (8 * g + q) * 384 + 64 * sf + 32 * (w & 1) + 8 * p;
    auto vo = [&](unsigned i) { const unsigned c = 64 * i + lane; return (8 * w + c / 24) * 1152 + 384 * n3 + c % 24 * 16 - 1024 * i; };
    const unsigned v_vo0 = vo(0), v_vo1 = vo(1), v_vo2 = vo(2);
    const unsigned v_xp = (6 * n3 + pf) * 1024 + v_l16, v_xs = (6 * n3 + sf) * 1024 + (w & 1) * 512 + half;
    const unsigned v_op = i16 * 1152 + 384 * n3 + 64 * pf + 16 * g, v_os = i16 * 1152 + 384 * n3 + 64 * sf + 32 * (w & 1) + 8 * g;
    const unsigned v_sw = 147456 + i16 * 136 + (4 * w + g) * 8, v_sr = 147456 + (4 * w + g) * 136 + i16 * 8;
    const unsigned v_ss = 18432 + n3 * 512 + w * 128 + g * 32 + p * 8;
    const unsigned s_ph = ks ? 6 - ks : 0, s_p0 = 0u - s_ph, r0 = gr * NREC + s_p0;
    const unsigned s_sh = WS_H + r0 * HREC + f0 * 1024, s_ps = WS_PS + r0 * PREC + (n3 * 20 + w) * PW;
    const unsigned s_x1 = WS_X1 + r0 * REC, s_xr = WS_XIN + (r0 + 33) * REC, s_or = (32 * gr + 63 - s_p0) * 18432;
    const unsigned s_k0 = ROT * 6 * gr;
    const unsigned s_m3 = w * 3072, s_m6 = w * 6144, s_so = ks * 768 * 1152;
    const unsigned s_i0 = (32 * gr + 31 - ks) * 18432, s_i5 = ks < 2 ? s_i0 - 30 * 18432 : OOB;
    const unsigned s_j0 = WS_XIN + (gr * NREC + ks) * REC, s_j5 = ks < 2 ? s_j0 + 30 * REC : OOB, s_he = s_sh + (63 + s_ph) * HREC;
    asm volatile(
      "s_buffer_load_dword s21, s[8:11], 0\n"
      "s_mov_b32 s23, 65536\n"
      "s_mov_b32 s31, 0x40000000\n"
      "buffer_load_dwordx4 v[56:59], v8, s[92:95], s46 offen\n"
      "buffer_load_dwordx2 v[60:61], v9, s[92:95], s46 offen\n"
      "s_waitcnt lgkmcnt(0)\n"
      "s_mul_i32 s21, s21, 1664525\n"
      "s_add_u32 s21, s21, 1013904223\n"
      "v_mov_b32 v18, s21\n"
      "v_mov_b32 v21, v18\n"
      "v_not_b32 v224, v18\n"
      "v_not_b32 v225, v18\n"
      "v_not_b32 v226, v18\n"
      "v_not_b32 v227, v18\n"
      "v_xor_b32 v18, v18, v19\n"
      "s_waitcnt vmcnt(0)\n"
      "buffer_store_dwordx4 v[56:59], v6, s[8:11], s48 offen sc1\n"
      "buffer_store_dwordx2 v[60:61], v7, s[8:11], s48 offen sc1\n"
      "v_lshlrev_b32 v164, 16, v56\n"
      "v_and_b32 v165, 0xffff0000, v56\n"
      "v_lshlrev_b32 v166, 16, v57\n"
      "v_and_b32 v167, 0xffff0000, v57\n"
      "v_lshlrev_b32 v168, 16, v58\n"
      "v_and_b32 v169, 0xffff0000, v58\n"
      "v_lshlrev_b32 v170, 16, v59\n"
      "v_and_b32 v171, 0xffff0000, v59\n"
      "v_lshlrev_b32 v172, 16, v60\n"
      "v_and_b32 v173, 0xffff0000, v60\n"
      "v_lshlrev_b32 v174, 16, v61\n"
      "v_and_b32 v175, 0xffff0000, v61\n"
      "v_add_f32 v62, v164, v165\n"
      "v_mul_f32 v63, v164, v164\n"
      "v_fmac_f32 v63, v165, v165\n"
      "v_add_f32 v62, v62, v166\n"
      "v_fmac_f32 v63, v166, v166\n"
      "v_add_f32 v62, v62, v167\n"
      "v_fmac_f32 v63, v167, v167\n"
      "v_add_f32 v62, v62, v168\n"
      "v_fmac_f32 v63, v168, v168\n"
      "v_add_f32 v62, v62, v169\n"
      "v_fmac_f32 v63, v169, v169\n"
      "v_add_f32 v62, v62, v170\n"
      "v_fmac_f32 v63, v170, v170\n"
      "v_add_f32 v62, v62, v171\n"
      "v_fmac_f32 v63, v171, v171\n"
      "v_add_f32 v62, v62, v172\n"
      "v_fmac_f32 v63, v172, v172\n"
      "v_add_f32 v62, v62, v173\n"
      "v_fmac_f32 v63, v173, v173\n"
      "v_add_f32 v62, v62, v174\n"
      "v_fmac_f32 v63, v174, v174\n"
      "v_add_f32 v62, v62, v175\n"
      "v_fmac_f32 v63, v175, v175\n"
      "ds_write_b64 v10, v[62:63] offset:0\n"
      "s_waitcnt lgkmcnt(0)\n"
      "s_barrier\n"
      "ds_read_b64 v[152:153], v11 offset:0\n"
      "s_waitcnt lgkmcnt(0)\n"
      "v_add_f32_dpp v152, v152, v152 row_ror:8 row_mask:0xf bank_mask:0xf\n"
      "v_add_f32_dpp v153, v153, v153 row_ror:8 row_mask:0xf bank_mask:0xf\n"
      "s_nop 0\n"
      "v_add_f32_dpp v152, v152, v152 row_ror:4 row_mask:0xf bank_mask:0xf\n"
      "v_add_f32_dpp v153, v153, v153 row_ror:4 row_mask:0xf bank_mask:0xf\n"
      "s_nop 0\n"
      "v_add_f32_dpp v152, v152, v152 row_ror:2 row_mask:0xf bank_mask:0xf\n"
      "v_add_f32_dpp v153, v153, v153 row_ror:2 row_mask:0xf bank_mask:0xf\n"
      "s_nop 0\n"
      "v_add_f32_dpp v152, v152, v152 row_ror:1 row_mask:0xf bank_mask:0xf\n"
      "v_add_f32_dpp v153, v153, v153 row_ror:1 row_mask:0xf bank_mask:0xf\n"
      "v_mul_f32 v152, 0x3ae38e39, v152\n"
      "v_add_f32 v153, 0x3afba882, v153\n"
      "v_mul_f32 v153, 0x3ae38e39, v153\n"
      "buffer_store_dwordx2 v[152:153], v12, s[8:11], s48 offen sc1\n"
      "s_add_u32 s30, s46, 4294856704\n"
      "buffer_load_dwordx4 v[64:67], v8, s[92:95], s30 offen\n"
      "buffer_load_dwordx2 v[68:69], v9, s[92:95], s30 offen\n"
      "s_add_u32 s30, s46, 4294746112\n"
      "buffer_load_dwordx4 v[72:75], v8, s[92:95], s30 offen\n"
      "buffer_load_dwordx2 v[76:77], v9, s[92:95], s30 offen\n"
      "s_waitcnt vmcnt(4)\n"
      "buffer_store_dword v18, v5, s[8:11], s48 offen sc1\n"
      "s_add_u32 s30, s46, 4294635520\n"
      "buffer_load_dwordx4 v[80:83], v8, s[92:95], s30 offen\n"
      "buffer_load_dwordx2 v[84:85], v9, s[92:95], s30 offen\n"
      "s_add_u32 s30, s46, 4294524928\n"
      "buffer_load_dwordx4 v[88:91], v8, s[92:95], s30 offen\n"
      "buffer_load_dwordx2 v[92:93], v9, s[92:95], s30 offen\n"
      "buffer_load_dwordx4 v[96:99], v8, s[92:95], s47 offen\n"
      "buffer_load_dwordx2 v[100:101], v9, s[92:95], s47 offen\n"
      "buffer_store_dword v18, v15, s[8:11], 0 offen sc1\n"
      "s_cmp_eq_u32 s45, 0\n"
      "s_cselect_b32 s30, 0x40000000, s37\n"
      "buffer_store_dword v21, v3, s[8:11], s30 offen sc1\n"
      "s_waitcnt vmcnt(11)\n"
      "s_add_u32 s30, s48, 147456\n"
      "buffer_store_dwordx4 v[64:67], v6, s[8:11], s30 offen sc1\n"
      "buffer_store_dwordx2 v[68:69], v7, s[8:11], s30 offen sc1\n"
      "v_lshlrev_b32 v164, 16, v64\n"
      "v_and_b32 v165, 0xffff0000, v64\n"
      "v_lshlrev_b32 v166, 16, v65\n"
      "v_and_b32 v167, 0xffff0000, v65\n"
      "v_lshlrev_b32 v168, 16, v66\n"
      "v_and_b32 v169, 0xffff0000, v66\n"
      "v_lshlrev_b32 v170, 16, v67\n"
      "v_and_b32 v171, 0xffff0000, v67\n"
      "v_lshlrev_b32 v172, 16, v68\n"
      "v_and_b32 v173, 0xffff0000, v68\n"
      "v_lshlrev_b32 v174, 16, v69\n"
      "v_and_b32 v175, 0xffff0000, v69\n"
      "v_add_f32 v70, v164, v165\n"
      "v_mul_f32 v71, v164, v164\n"
      "v_fmac_f32 v71, v165, v165\n"
      "v_add_f32 v70, v70, v166\n"
      "v_fmac_f32 v71, v166, v166\n"
      "v_add_f32 v70, v70, v167\n"
      "v_fmac_f32 v71, v167, v167\n"
      "v_add_f32 v70, v70, v168\n"
      "v_fmac_f32 v71, v168, v168\n"
      "v_add_f32 v70, v70, v169\n"
      "v_fmac_f32 v71, v169, v169\n"
      "v_add_f32 v70, v70, v170\n"
      "v_fmac_f32 v71, v170, v170\n"
      "v_add_f32 v70, v70, v171\n"
      "v_fmac_f32 v71, v171, v171\n"
      "v_add_f32 v70, v70, v172\n"
      "v_fmac_f32 v71, v172, v172\n"
      "v_add_f32 v70, v70, v173\n"
      "v_fmac_f32 v71, v173, v173\n"
      "v_add_f32 v70, v70, v174\n"
      "v_fmac_f32 v71, v174, v174\n"
      "v_add_f32 v70, v70, v175\n"
      "v_fmac_f32 v71, v175, v175\n"
      "ds_write_b64 v10, v[70:71] offset:2176\n"
      "s_waitcnt vmcnt(11)\n"
      "s_add_u32 s30, s48, 294912\n"
      "buffer_store_dwordx4 v[72:75], v6, s[8:11], s30 offen sc1\n"
      "buffer_store_dwordx2 v[76:77], v7, s[8:11], s30 offen sc1\n"
      "v_lshlrev_b32 v164, 16, v72\n"
      "v_and_b32 v165, 0xffff0000, v72\n"
      "v_lshlrev_b32 v166, 16, v73\n"
      "v_and_b32 v167, 0xffff0000, v73\n"
      "v_lshlrev_b32 v168, 16, v74\n"
      "v_and_b32 v169, 0xffff0000, v74\n"
      "v_lshlrev_b32 v170, 16, v75\n"
      "v_and_b32 v171, 0xffff0000, v75\n"
      "v_lshlrev_b32 v172, 16, v76\n"
      "v_and_b32 v173, 0xffff0000, v76\n"
      "v_lshlrev_b32 v174, 16, v77\n"
      "v_and_b32 v175, 0xffff0000, v77\n"
      "v_add_f32 v78, v164, v165\n"
      "v_mul_f32 v79, v164, v164\n"
      "v_fmac_f32 v79, v165, v165\n"
      "v_add_f32 v78, v78, v166\n"
      "v_fmac_f32 v79, v166, v166\n"
      "v_add_f32 v78, v78, v167\n"
      "v_fmac_f32 v79, v167, v167\n"
      "v_add_f32 v78, v78, v168\n"
      "v_fmac_f32 v79, v168, v168\n"
      "v_add_f32 v78, v78, v169\n"
      "v_fmac_f32 v79, v169, v169\n"
      "v_add_f32 v78, v78, v170\n"
      "v_fmac_f32 v79, v170, v170\n"
      "v_add_f32 v78, v78, v171\n"
      "v_fmac_f32 v79, v171, v171\n"
      "v_add_f32 v78, v78, v172\n"
      "v_fmac_f32 v79, v172, v172\n"
      "v_add_f32 v78, v78, v173\n"
      "v_fmac_f32 v79, v173, v173\n"
      "v_add_f32 v78, v78, v174\n"
      "v_fmac_f32 v79, v174, v174\n"
      "v_add_f32 v78, v78, v175\n"
      "v_fmac_f32 v79, v175, v175\n"
      "ds_write_b64 v10, v[78:79] offset:4352\n"
      "s_waitcnt vmcnt(10)\n"
      "s_add_u32 s30, s48, 442368\n"
      "buffer_store_dwordx4 v[80:83], v6, s[8:11], s30 offen sc1\n"
      "buffer_store_dwordx2 v[84:85], v7, s[8:11], s30 offen sc1\n"
      "v_lshlrev_b32 v164, 16, v80\n"
      "v_and_b32 v165, 0xffff0000, v80\n"
      "v_lshlrev_b32 v166, 16, v81\n"
      "v_and_b32 v167, 0xffff0000, v81\n"
      "v_lshlrev_b32 v168, 16, v82\n"
      "v_and_b32 v169, 0xffff0000, v82\n"
      "v_lshlrev_b32 v170, 16, v83\n"
      "v_and_b32 v171, 0xffff0000, v83\n"
      "v_lshlrev_b32 v172, 16, v84\n"
      "v_and_b32 v173, 0xffff0000, v84\n"
      "v_lshlrev_b32 v174, 16, v85\n"
      "v_and_b32 v175, 0xffff0000, v85\n"
      "v_add_f32 v86, v164, v165\n"
      "v_mul_f32 v87, v164, v164\n"
      "v_fmac_f32 v87, v165, v165\n"
      "v_add_f32 v86, v86, v166\n"
      "v_fmac_f32 v87, v166, v166\n"
      "v_add_f32 v86, v86, v167\n"
      "v_fmac_f32 v87, v167, v167\n"
      "v_add_f32 v86, v86, v168\n"
      "v_fmac_f32 v87, v168, v168\n"
      "v_add_f32 v86, v86, v169\n"
      "v_fmac_f32 v87, v169, v169\n"
      "v_add_f32 v86, v86, v170\n"
      "v_fmac_f32 v87, v170, v170\n"
      "v_add_f32 v86, v86, v171\n"
      "v_fmac_f32 v87, v171, v171\n"
      "v_add_f32 v86, v86, v172\n"
      "v_fmac_f32 v87, v172, v172\n"
      "v_add_f32 v86, v86, v173\n"
      "v_fmac_f32 v87, v173, v173\n"
      "v_add_f32 v86, v86, v174\n"
      "v_fmac_f32 v87, v174, v174\n"
      "v_add_f32 v86, v86, v175\n"
      "v_fmac_f32 v87, v175, v175\n"
      "ds_write_b64 v10, v[86:87] offset:6528\n"
      "s_waitcnt vmcnt(10)\n"
      "s_add_u32 s30, s48, 589824\n"
      "buffer_store_dwordx4 v[88:91], v6, s[8:11], s30 offen sc1\n"
      "buffer_store_dwordx2 v[92:93], v7, s[8:11], s30 offen sc1\n"
      "v_lshlrev_b32 v164, 16, v88\n"
      "v_and_b32 v165, 0xffff0000, v88\n"
      "v_lshlrev_b32 v166, 16, v89\n"
      "v_and_b32 v167, 0xffff0000, v89\n"
      "v_lshlrev_b32 v168, 16, v90\n"
      "v_and_b32 v169, 0xffff0000, v90\n"
      "v_lshlrev_b32 v170, 16, v91\n"
      "v_and_b32 v171, 0xffff0000, v91\n"
      "v_lshlrev_b32 v172, 16, v92\n"
      "v_and_b32 v173, 0xffff0000, v92\n"
      "v_lshlrev_b32 v174, 16, v93\n"
      "v_and_b32 v175, 0xffff0000, v93\n"
      "v_add_f32 v94, v164, v165\n"
      "v_mul_f32 v95, v164, v164\n"
      "v_fmac_f32 v95, v165, v165\n"
      "v_add_f32 v94, v94, v166\n"
      "v_fmac_f32 v95, v166, v166\n"
      "v_add_f32 v94, v94, v167\n"
      "v_fmac_f32 v95, v167, v167\n"
      "v_add_f32 v94, v94, v168\n"
      "v_fmac_f32 v95, v168, v168\n"
      "v_add_f32 v94, v94, v169\n"
      "v_fmac_f32 v95, v169, v169\n"
      "v_add_f32 v94, v94, v170\n"
      "v_fmac_f32 v95, v170, v170\n"
      "v_add_f32 v94, v94, v171\n"
      "v_fmac_f32 v95, v171, v171\n"
      "v_add_f32 v94, v94, v172\n"
      "v_fmac_f32 v95, v172, v172\n"
      "v_add_f32 v94, v94, v173\n"
      "v_fmac_f32 v95, v173, v173\n"
      "v_add_f32 v94, v94, v174\n"
      "v_fmac_f32 v95, v174, v174\n"
      "v_add_f32 v94, v94, v175\n"
      "v_fmac_f32 v95, v175, v175\n"
      "ds_write_b64 v10, v[94:95] offset:8704\n"
      "s_waitcnt vmcnt(10)\n"
      "buffer_store_dwordx4 v[96:99], v6, s[8:11], s49 offen sc1\n"
      "buffer_store_dwordx2 v[100:101], v7, s[8:11], s49 offen sc1\n"
      "v_lshlrev_b32 v164, 16, v96\n"
      "v_and_b32 v165, 0xffff0000, v96\n"
      "v_lshlrev_b32 v166, 16, v97\n"
      "v_and_b32 v167, 0xffff0000, v97\n"
      "v_lshlrev_b32 v168, 16, v98\n"
      "v_and_b32 v169, 0xffff0000, v98\n"
      "v_lshlrev_b32 v170, 16, v99\n"
      "v_and_b32 v171, 0xffff0000, v99\n"
      "v_lshlrev_b32 v172, 16, v100\n"
      "v_and_b32 v173, 0xffff0000, v100\n"
      "v_lshlrev_b32 v174, 16, v101\n"
      "v_and_b32 v175, 0xffff0000, v101\n"
      "v_add_f32 v102, v164, v165\n"
      "v_mul_f32 v103, v164, v164\n"
      "v_fmac_f32 v103, v165, v165\n"
      "v_add_f32 v102, v102, v166\n"
      "v_fmac_f32 v103, v166, v166\n"
      "v_add_f32 v102, v102, v167\n"
      "v_fmac_f32 v103, v167, v167\n"
      "v_add_f32 v102, v102, v168\n"
      "v_fmac_f32 v103, v168, v168\n"
      "v_add_f32 v102, v102, v169\n"
      "v_fmac_f32 v103, v169, v169\n"
      "v_add_f32 v102, v102, v170\n"
      "v_fmac_f32 v103, v170, v170\n"
      "v_add_f32 v102, v102, v171\n"
      "v_fmac_f32 v103, v171, v171\n"
      "v_add_f32 v102, v102, v172\n"
      "v_fmac_f32 v103, v172, v172\n"
      "v_add_f32 v102, v102, v173\n"
      "v_fmac_f32 v103, v173, v173\n"
      "v_add_f32 v102, v102, v174\n"
      "v_fmac_f32 v103, v174, v174\n"
      "v_add_f32 v102, v102, v175\n"
      "v_fmac_f32 v103, v175, v175\n"
      "ds_write_b64 v10, v[102:103] offset:10880\n"
      "s_waitcnt lgkmcnt(0)\n"
      "s_barrier\n"
      "ds_read_b64 v[154:155], v11 offset:2176\n"
      "ds_read_b64 v[156:157], v11 offset:4352\n"
      "ds_read_b64 v[158:159], v11 offset:6528\n"
      "ds_read_b64 v[160:161], v11 offset:8704\n"
      "ds_read_b64 v[162:163], v11 offset:10880\n"
      "s_waitcnt lgkmcnt(0)\n"
      "v_add_f32_dpp v154, v154, v154 row_ror:8 row_mask:0xf bank_mask:0xf\n"
      "v_add_f32_dpp v155, v155, v155 row_ror:8 row_mask:0xf bank_mask:0xf\n"
      "v_add_f32_dpp v156, v156, v156 row_ror:8 row_mask:0xf bank_mask:0xf\n"
      "v_add_f32_dpp v157, v157, v157 row_ror:8 row_mask:0xf bank_mask:0xf\n"
      "v_add_f32_dpp v158, v158, v158 row_ror:8 row_mask:0xf bank_mask:0xf\n"
      "v_add_f32_dpp v159, v159, v159 row_ror:8 row_mask:0xf bank_mask:0xf\n"
      "v_add_f32_dpp v160, v160, v160 row_ror:8 row_mask:0xf bank_mask:0xf\n"
      "v_add_f32_dpp v161, v161, v161 row_ror:8 row_mask:0xf bank_mask:0xf\n"
      "v_add_f32_dpp v162, v162, v162 row_ror:8 row_mask:0xf bank_mask:0xf\n"
      "v_add_f32_dpp v163, v163, v163 row_ror:8 row_mask:0xf bank_mask:0xf\n"
      "v_add_f32_dpp v154, v154, v154 row_ror:4 row_mask:0xf bank_mask:0xf\n"
      "v_add_f32_dpp v155, v155, v155 row_ror:4 row_mask:0xf bank_mask:0xf\n"
      "v_add_f32_dpp v156, v156, v156 row_ror:4 row_mask:0xf bank_mask:0xf\n"
      "v_add_f32_dpp v157, v157, v157 row_ror:4 row_mask:0xf bank_mask:0xf\n"
      "v_add_f32_dpp v158, v158, v158 row_ror:4 row_mask:0xf bank_mask:0xf\n"
      "v_add_f32_dpp v159, v159, v159 row_ror:4 row_mask:0xf bank_mask:0xf\n"
      "v_add_f32_dpp v160, v160, v160 row_ror:4 row_mask:0xf bank_mask:0xf\n"
      "v_add_f32_dpp v161, v161, v161 row_ror:4 row_mask:0xf bank_mask:0xf\n"
      "v_add_f32_dpp v162, v162, v162 row_ror:4 row_mask:0xf bank_mask:0xf\n"
      "v_add_f32_dpp v163, v163, v163 row_ror:4 row_mask:0xf bank_mask:0xf\n"
      "v_add_f32_dpp v154, v154, v154 row_ror:2 row_mask:0xf bank_mask:0xf\n"
      "v_add_f32_dpp v155, v155, v155 row_ror:2 row_mask:0xf bank_mask:0xf\n"
      "v_add_f32_dpp v156, v156, v156 row_ror:2 row_mask:0xf bank_mask:0xf\n"
      "v_add_f32_dpp v157, v157, v157 row_ror:2 row_mask:0xf bank_mask:0xf\n"
      "v_add_f32_dpp v158, v158, v158 row_ror:2 row_mask:0xf bank_mask:0xf\n"
      "v_add_f32_dpp v159, v159, v159 row_ror:2 row_mask:0xf bank_mask:0xf\n"
      "v_add_f32_dpp v160, v160, v160 row_ror:2 row_mask:0xf bank_mask:0xf\n"
      "v_add_f32_dpp v161, v161, v161 row_ror:2 row_mask:0xf bank_mask:0xf\n"
      "v_add_f32_dpp v162, v162, v162 row_ror:2 row_mask:0xf bank_mask:0xf\n"
      "v_add_f32_dpp v163, v163, v163 row_ror:2 row_mask:0xf bank_mask:0xf\n"
      "v_add_f32_dpp v154, v154, v154 row_ror:1 row_mask:0xf bank_mask:0xf\n"
      "v_add_f32_dpp v155, v155, v155 row_ror:1 row_mask:0xf bank_mask:0xf\n"
      "v_add_f32_dpp v156, v156, v156 row_ror:1 row_mask:0xf bank_mask:0xf\n"
      "v_add_f32_dpp v157, v157, v157 row_ror:1 row_mask:0xf bank_mask:0xf\n"
      "v_add_f32_dpp v158, v158, v158 row_ror:1 row_mask:0xf bank_mask:0xf\n"
      "v_add_f32_dpp v159, v159, v159 row_ror:1 row_mask:0xf bank_mask:0xf\n"
      "v_add_f32_dpp v160, v160, v160 row_ror:1 row_mask:0xf bank_mask:0xf\n"
      "v_add_f32_dpp v161, v161, v161 row_ror:1 row_mask:0xf bank_mask:0xf\n"
      "v_add_f32_dpp v162, v162, v162 row_ror:1 row_mask:0xf bank_mask:0xf\n"
      "v_add_f32_dpp v163, v163, v163 row_ror:1 row_mask:0xf bank_mask:0xf\n"
      "v_mul_f32 v154, 0x3ae38e39, v154\n"
      "v_add_f32 v155, 0x3afba882, v155\n"
      "v_mul_f32 v155, 0x3ae38e39, v155\n"
      "v_mul_f32 v156, 0x3ae38e39, v156\n"
      "v_add_f32 v157, 0x3afba882, v157\n"
      "v_mul_f32 v157, 0x3ae38e39, v157\n"
      "v_mul_f32 v158, 0x3ae38e39, v158\n"
      "v_add_f32 v159, 0x3afba882, v159\n"
      "v_mul_f32 v159, 0x3ae38e39, v159\n"
      "v_mul_f32 v160, 0x3ae38e39, v160\n"
      "v_add_f32 v161, 0x3afba882, v161\n"
      "v_mul_f32 v161, 0x3ae38e39, v161\n"
      "v_mul_f32 v162, 0x3ae38e39, v162\n"
      "v_add_f32 v163, 0x3afba882, v163\n"
      "v_mul_f32 v163, 0x3ae38e39, v163\n"
      "s_add_u32 s30, s48, 147456\n"
      "buffer_store_dwordx2 v[154:155], v12, s[8:11], s30 offen sc1\n"
      "s_add_u32 s30, s48, 294912\n"
      "buffer_store_dwordx2 v[156:157], v12, s[8:11], s30 offen sc1\n"
      "s_add_u32 s30, s48, 442368\n"
      "buffer_store_dwordx2 v[158:159], v12, s[8:11], s30 offen sc1\n"
      "s_add_u32 s30, s48, 589824\n"
      "buffer_store_dwordx2 v[160:161], v12, s[8:11], s30 offen sc1\n"
      "buffer_store_dwordx2 v[162:163], v12, s[8:11], s49 offen sc1\n"
      "s_waitcnt vmcnt(0)\n"
      "s_add_u32 s30, s48, 147456\n"
      "buffer_store_dword v18, v5, s[8:11], s30 offen sc1\n"
      "s_add_u32 s30, s48, 294912\n"
      "buffer_store_dword v18, v5, s[8:11], s30 offen sc1\n"
      "s_add_u32 s30, s48, 442368\n"
      "buffer_store_dword v18, v5, s[8:11], s30 offen sc1\n"
      "s_add_u32 s30, s48, 589824\n"
      "buffer_store_dword v18, v5, s[8:11], s30 offen sc1\n"
      "buffer_store_dword v18, v5, s[8:11], s49 offen sc1\n"
      "v_and_b32 v20, 0x40000000, v19\n"
      "v_or_b32 v5, v20, v5\n"
      "s_add_u32 s30, s37, 0\n"
      "buffer_store_dwordx4 v[224:227], v183, s[8:11], s30 offen sc1\n"
      "s_add_u32 s30, s37, 0\n"
      "buffer_store_dwordx4 v[224:227], v181, s[8:11], s30 offen sc1\n"
      "s_sleep 32\n"
      "s_waitcnt vmcnt(0) lgkmcnt(0)\n"
      "s_mov_b32 s61, s60\n"
      "s_mov_b32 s62, 6\n"
      ".Ld_r0:\n"
      "s_mul_i32 s63, s61, 36864\n"
      "s_add_u32 s63, s63, s44\n"
      "s_mul_i32 s30, s61, 8192\n"
      "s_lshr_b32 s30, s30, 16\n"
      "s_mul_i32 s30, s30, 8\n"
      "s_sub_u32 s30, s61, s30\n"
      "s_mul_i32 s30, s30, 12288\n"
      "s_add_u32 s30, s30, 0\n"
      "s_add_u32 m0, s42, s30\n"
      "s_add_u32 s61, s61, 1\n"
      "buffer_load_dwordx4 v178, s[12:15], s63 offen offset:0 lds\n"
      "buffer_load_dwordx4 v179, s[12:15], s63 offen offset:1024 lds\n"
      "buffer_load_dwordx4 v180, s[12:15], s63 offen offset:2048 lds\n"
      "s_cmp_eq_u32 s61, 24\n"
      "s_cselect_b32 s61, 0, s61\n"
      "s_sub_u32 s62, s62, 1\n"
      "s_cbranch_scc0 .Ld_r0\n"
      "v_add_u32 v20, 49152, v176\n"
      "v_add_u32 v21, 49152, v177\n"
      "v_add_u32 v22, 98304, v176\n"
      "v_add_u32 v23, 98304, v177\n"
      "s_mov_b32 s62, 23\n"
      "s_cmp_eq_u32 s60, 6\n"
      "s_cbranch_scc1 .Ld_rs6\n"
      "s_cmp_eq_u32 s60, 12\n"
      "s_cbranch_scc1 .Ld_rs12\n"
      "s_cmp_eq_u32 s60, 18\n"
      "s_cbranch_scc1 .Ld_rs18\n"
      ".Ld_rs0:\n"
      "s_add_u32 s63, s44, 258048\n"
      "s_cmp_ge_u32 s62, 7\n"
      "s_cselect_b32 s63, s63, s31\n"
      "s_add_u32 m0, s42, 86016\n"
      "s_waitcnt vmcnt(18) lgkmcnt(0)\n"
      "s_barrier\n"
      "ds_read_b64_tr_b16 a[0:1], v176 offset:0\n"
      "ds_read_b64_tr_b16 a[2:3], v176 offset:1536\n"
      "ds_read_b64_tr_b16 a[96:97], v176 offset:8\n"
      "ds_read_b64_tr_b16 a[98:99], v176 offset:1544\n"
      "ds_read_b64_tr_b16 a[192:193], v177 offset:0\n"
      "ds_read_b64_tr_b16 a[194:195], v177 offset:1536\n"
      "buffer_load_dwordx4 v178, s[12:15], s63 offen offset:0 lds\n"
      "buffer_load_dwordx4 v179, s[12:15], s63 offen offset:1024 lds\n"
      "buffer_load_dwordx4 v180, s[12:15], s63 offen offset:2048 lds\n"
      "s_sub_u32 s62, s62, 1\n"
      ".Ld_rs1:\n"
      "s_add_u32 s63, s44, 294912\n"
      "s_cmp_ge_u32 s62, 7\n"
      "s_cselect_b32 s63, s63, s31\n"
      "s_add_u32 m0, s42, 0\n"
      "s_waitcnt vmcnt(18) lgkmcnt(0)\n"
      "s_barrier\n"
      "ds_read_b64_tr_b16 a[4:5], v176 offset:12288\n"
      "ds_read_b64_tr_b16 a[6:7], v176 offset:13824\n"
      "ds_read_b64_tr_b16 a[100:101], v176 offset:12296\n"
      "ds_read_b64_tr_b16 a[102:103], v176 offset:13832\n"
      "ds_read_b64_tr_b16 a[196:197], v177 offset:12288\n"
      "ds_read_b64_tr_b16 a[198:199], v177 offset:13824\n"
      "buffer_load_dwordx4 v178, s[12:15], s63 offen offset:0 lds\n"
      "buffer_load_dwordx4 v179, s[12:15], s63 offen offset:1024 lds\n"
      "buffer_load_dwordx4 v180, s[12:15], s63 offen offset:2048 lds\n"
      "s_sub_u32 s62, s62, 1\n"
      ".Ld_rs2:\n"
      "s_add_u32 s63, s44, 331776\n"
      "s_cmp_ge_u32 s62, 7\n"
      "s_cselect_b32 s63, s63, s31\n"
      "s_add_u32 m0, s42, 12288\n"
      "s_waitcnt vmcnt(18) lgkmcnt(0)\n"
      "s_barrier\n"
      "ds_read_b64_tr_b16 a[8:9], v176 offset:24576\n"
      "ds_read_b64_tr_b16 a[10:11], v176 offset:26112\n"
      "ds_read_b64_tr_b16 a[104:105], v176 offset:24584\n"
      "ds_read_b64_tr_b16 a[106:107], v176 offset:26120\n"
      "ds_read_b64_tr_b16 a[200:201], v177 offset:24576\n"
      "ds_read_b64_tr_b16 a[202:203], v177 offset:26112\n"
      "buffer_load_dwordx4 v178, s[12:15], s63 offen offset:0 lds\n"
      "buffer_load_dwordx4 v179, s[12:15], s63 offen offset:1024 lds\n"
      "buffer_load_dwordx4 v180, s[12:15], s63 offen offset:2048 lds\n"
      "s_sub_u32 s62, s62, 1\n"
      ".Ld_rs3:\n"
      "s_add_u32 s63, s44, 368640\n"
      "s_cmp_ge_u32 s62, 7\n"
      "s_cselect_b32 s63, s63, s31\n"
      "s_add_u32 m0, s42, 24576\n"
      "s_waitcnt vmcnt(18) lgkmcnt(0)\n"
      "s_barrier\n"
      "ds_read_b64_tr_b16 a[12:13], v176 offset:36864\n"
      "ds_read_b64_tr_b16 a[14:15], v176 offset:38400\n"
      "ds_read_b64_tr_b16 a[108:109], v176 offset:36872\n"
      "ds_read_b64_tr_b16 a[110:111], v176 offset:38408\n"
      "ds_read_b64_tr_b16 a[204:205], v177 offset:36864\n"
      "ds_read_b64_tr_b16 a[206:207], v177 offset:38400\n"
      "buffer_load_dwordx4 v178, s[12:15], s63 offen offset:0 lds\n"
      "buffer_load_dwordx4 v179, s[12:15], s63 offen offset:1024 lds\n"
      "buffer_load_dwordx4 v180, s[12:15], s63 offen offset:2048 lds\n"
      "s_sub_u32 s62, s62, 1\n"
      ".Ld_rs4:\n"
      "s_add_u32 s63, s44, 405504\n"
      "s_cmp_ge_u32 s62, 7\n"
      "s_cselect_b32 s63, s63, s31\n"
      "s_add_u32 m0, s42, 36864\n"
      "s_waitcnt vmcnt(18) lgkmcnt(0)\n"
      "s_barrier\n"
      "ds_read_b64_tr_b16 a[16:17], v20 offset:0\n"
      "ds_read_b64_tr_b16 a[18:19], v20 offset:1536\n"
      "ds_read_b64_tr_b16 a[112:113], v20 offset:8\n"
      "ds_read_b64_tr_b16 a[114:115], v20 offset:1544\n"
      "ds_read_b64_tr_b16 a[208:209], v21 offset:0\n"
      "ds_read_b64_tr_b16 a[210:211], v21 offset:1536\n"
      "buffer_load_dwordx4 v178, s[12:15], s63 offen offset:0 lds\n"
      "buffer_load_dwordx4 v179, s[12:15], s63 offen offset:1024 lds\n"
      "buffer_load_dwordx4 v180, s[12:15], s63 offen offset:2048 lds\n"
      "s_sub_u32 s62, s62, 1\n"
      ".Ld_rs5:\n"
      "s_add_u32 s63, s44, 442368\n"
      "s_cmp_ge_u32 s62, 7\n"
      "s_cselect_b32 s63, s63, s31\n"
      "s_add_u32 m0, s42, 49152\n"
      "s_waitcnt vmcnt(18) lgkmcnt(0)\n"
      "s_barrier\n"
      "ds_read_b64_tr_b16 a[20:21], v20 offset:12288\n"
      "ds_read_b64_tr_b16 a[22:23], v20 offset:13824\n"
      "ds_read_b64_tr_b16 a[116:117], v20 offset:12296\n"
      "ds_read_b64_tr_b16 a[118:119], v20 offset:13832\n"
      "ds_read_b64_tr_b16 a[212:213], v21 offset:12288\n"
      "ds_read_b64_tr_b16 a[214:215], v21 offset:13824\n"
      "buffer_load_dwordx4 v178, s[12:15], s63 offen offset:0 lds\n"
      "buffer_load_dwordx4 v179, s[12:15], s63 offen offset:1024 lds\n"
      "buffer_load_dwordx4 v180, s[12:15], s63 offen offset:2048 lds\n"
      "s_sub_u32 s62, s62, 1\n"
      "s_cbranch_scc1 .Ld_re\n"
      ".Ld_rs6:\n"
      "s_add_u32 s63, s44, 479232\n"
      "s_cmp_ge_u32 s62, 7\n"
      "s_cselect_b32 s63, s63, s31\n"
      "s_add_u32 m0, s42, 61440\n"
      "s_waitcnt vmcnt(18) lgkmcnt(0)\n"
      "s_barrier\n"
      "ds_read_b64_tr_b16 a[24:25], v20 offset:24576\n"
      "ds_read_b64_tr_b16 a[26:27], v20 offset:26112\n"
      "ds_read_b64_tr_b16 a[120:121], v20 offset:24584\n"
      "ds_read_b64_tr_b16 a[122:123], v20 offset:26120\n"
      "ds_read_b64_tr_b16 a[216:217], v21 offset:24576\n"
      "ds_read_b64_tr_b16 a[218:219], v21 offset:26112\n"
      "buffer_load_dwordx4 v178, s[12:15], s63 offen offset:0 lds\n"
      "buffer_load_dwordx4 v179, s[12:15], s63 offen offset:1024 lds\n"
      "buffer_load_dwordx4 v180, s[12:15], s63 offen offset:2048 lds\n"
      "s_sub_u32 s62, s62, 1\n"
      ".Ld_rs7:\n"
      "s_add_u32 s63, s44, 516096\n"
      "s_cmp_ge_u32 s62, 7\n"
      "s_cselect_b32 s63, s63, s31\n"
      "s_add_u32 m0, s42, 73728\n"
      "s_waitcnt vmcnt(18) lgkmcnt(0)\n"
      "s_barrier\n"
      "ds_read_b64_tr_b16 a[28:29], v20 offset:36864\n"
      "ds_read_b64_tr_b16 a[30:31], v20 offset:38400\n"
      "ds_read_b64_tr_b16 a[124:125], v20 offset:36872\n"
      "ds_read_b64_tr_b16 a[126:127], v20 offset:38408\n"
      "ds_read_b64_tr_b16 a[220:221], v21 offset:36864\n"
      "ds_read_b64_tr_b16 a[222:223], v21 offset:38400\n"
      "buffer_load_dwordx4 v178, s[12:15], s63 offen offset:0 lds\n"
      "buffer_load_dwordx4 v179, s[12:15], s63 offen offset:1024 lds\n"
      "buffer_load_dwordx4 v180, s[12:15], s63 offen offset:2048 lds\n"
      "s_sub_u32 s62, s62, 1\n"
      ".Ld_rs8:\n"
      "s_add_u32 s63, s44, 552960\n"
      "s_cmp_ge_u32 s62, 7\n"
      "s_cselect_b32 s63, s63, s31\n"
      "s_add_u32 m0, s42, 86016\n"
      "s_waitcnt vmcnt(18) lgkmcnt(0)\n"
      "s_barrier\n"
      "ds_read_b64_tr_b16 a[32:33], v176 offset:0\n"
      "ds_read_b64_tr_b16 a[34:35], v176 offset:1536\n"
      "ds_read_b64_tr_b16 a[128:129], v176 offset:8\n"
      "ds_read_b64_tr_b16 a[130:131], v176 offset:1544\n"
      "ds_read_b64_tr_b16 a[224:225], v177 offset:0\n"
      "ds_read_b64_tr_b16 a[226:227], v177 offset:1536\n"
      "buffer_load_dwordx4 v178, s[12:15], s63 offen offset:0 lds\n"
      "buffer_load_dwordx4 v179, s[12:15], s63 offen offset:1024 lds\n"
      "buffer_load_dwordx4 v180, s[12:15], s63 offen offset:2048 lds\n"
      "s_sub_u32 s62, s62, 1\n"
      ".Ld_rs9:\n"
      "s_add_u32 s63, s44, 589824\n"
      "s_cmp_ge_u32 s62, 7\n"
      "s_cselect_b32 s63, s63, s31\n"
      "s_add_u32 m0, s42, 0\n"
      "s_waitcnt vmcnt(18) lgkmcnt(0)\n"
      "s_barrier\n"
      "ds_read_b64_tr_b16 a[36:37], v176 offset:12288\n"
      "ds_read_b64_tr_b16 a[38:39], v176 offset:13824\n"
      "ds_read_b64_tr_b16 a[132:133], v176 offset:12296\n"
      "ds_read_b64_tr_b16 a[134:135], v176 offset:13832\n"
      "ds_read_b64_tr_b16 a[228:229], v177 offset:12288\n"
      "ds_read_b64_tr_b16 a[230:231], v177 offset:13824\n"
      "buffer_load_dwordx4 v178, s[12:15], s63 offen offset:0 lds\n"
      "buffer_load_dwordx4 v179, s[12:15], s63 offen offset:1024 lds\n"
      "buffer_load_dwordx4 v180, s[12:15], s63 offen offset:2048 lds\n"
      "s_sub_u32 s62, s62, 1\n"
      ".Ld_rs10:\n"
      "s_add_u32 s63, s44, 626688\n"
      "s_cmp_ge_u32 s62, 7\n"
      "s_cselect_b32 s63, s63, s31\n"
      "s_add_u32 m0, s42, 12288\n"
      "s_waitcnt vmcnt(18) lgkmcnt(0)\n"
      "s_barrier\n"
      "ds_read_b64_tr_b16 a[40:41], v176 offset:24576\n"
      "ds_read_b64_tr_b16 a[42:43], v176 offset:26112\n"
      "ds_read_b64_tr_b16 a[136:137], v176 offset:24584\n"
      "ds_read_b64_tr_b16 a[138:139], v176 offset:26120\n"
      "ds_read_b64_tr_b16 a[232:233], v177 offset:24576\n"
      "ds_read_b64_tr_b16 a[234:235], v177 offset:26112\n"
      "buffer_load_dwordx4 v178, s[12:15], s63 offen offset:0 lds\n"
      "buffer_load_dwordx4 v179, s[12:15], s63 offen offset:1024 lds\n"
      "buffer_load_dwordx4 v180, s[12:15], s63 offen offset:2048 lds\n"
      "s_sub_u32 s62, s62, 1\n"
      ".Ld_rs11:\n"
      "s_add_u32 s63, s44, 663552\n"
      "s_cmp_ge_u32 s62, 7\n"
      "s_cselect_b32 s63, s63, s31\n"
      "s_add_u32 m0, s42, 24576\n"
      "s_waitcnt vmcnt(18) lgkmcnt(0)\n"
      "s_barrier\n"
      "ds_read_b64_tr_b16 a[44:45], v176 offset:36864\n"
      "ds_read_b64_tr_b16 a[46:47], v176 offset:38400\n"
      "ds_read_b64_tr_b16 a[140:141], v176 offset:36872\n"
      "ds_read_b64_tr_b16 a[142:143], v176 offset:38408\n"
      "ds_read_b64_tr_b16 a[236:237], v177 offset:36864\n"
      "ds_read_b64_tr_b16 a[238:239], v177 offset:38400\n"
      "buffer_load_dwordx4 v178, s[12:15], s63 offen offset:0 lds\n"
      "buffer_load_dwordx4 v179, s[12:15], s63 offen offset:1024 lds\n"
      "buffer_load_dwordx4 v180, s[12:15], s63 offen offset:2048 lds\n"
      "s_sub_u32 s62, s62, 1\n"
      "s_cbranch_scc1 .Ld_re\n"
      ".Ld_rs12:\n"
      "s_add_u32 s63, s44, 700416\n"
      "s_cmp_ge_u32 s62, 7\n"
      "s_cselect_b32 s63, s63, s31\n"
      "s_add_u32 m0, s42, 36864\n"
      "s_waitcnt vmcnt(18) lgkmcnt(0)\n"
      "s_barrier\n"
      "ds_read_b64_tr_b16 a[48:49], v20 offset:0\n"
      "ds_read_b64_tr_b16 a[50:51], v20 offset:1536\n"
      "ds_read_b64_tr_b16 a[144:145], v20 offset:8\n"
      "ds_read_b64_tr_b16 a[146:147], v20 offset:1544\n"
      "ds_read_b64_tr_b16 a[240:241], v21 offset:0\n"
      "ds_read_b64_tr_b16 a[242:243], v21 offset:1536\n"
      "buffer_load_dwordx4 v178, s[12:15], s63 offen offset:0 lds\n"
      "buffer_load_dwordx4 v179, s[12:15], s63 offen offset:1024 lds\n"
      "buffer_load_dwordx4 v180, s[12:15], s63 offen offset:2048 lds\n"
      "s_sub_u32 s62, s62, 1\n"
      ".Ld_rs13:\n"
      "s_add_u32 s63, s44, 737280\n"
      "s_cmp_ge_u32 s62, 7\n"
      "s_cselect_b32 s63, s63, s31\n"
      "s_add_u32 m0, s42, 49152\n"
      "s_waitcnt vmcnt(18) lgkmcnt(0)\n"
      "s_barrier\n"
      "ds_read_b64_tr_b16 a[52:53], v20 offset:12288\n"
      "ds_read_b64_tr_b16 a[54:55], v20 offset:13824\n"
      "ds_read_b64_tr_b16 a[148:149], v20 offset:12296\n"
      "ds_read_b64_tr_b16 a[150:151], v20 offset:13832\n"
      "ds_read_b64_tr_b16 a[244:245], v21 offset:12288\n"
      "ds_read_b64_tr_b16 a[246:247], v21 offset:13824\n"
      "buffer_load_dwordx4 v178, s[12:15], s63 offen offset:0 lds\n"
      "buffer_load_dwordx4 v179, s[12:15], s63 offen offset:1024 lds\n"
      "buffer_load_dwordx4 v180, s[12:15], s63 offen offset:2048 lds\n"
      "s_sub_u32 s62, s62, 1\n"
      ".Ld_rs14:\n"
      "s_add_u32 s63, s44, 774144\n"
      "s_cmp_ge_u32 s62, 7\n"
      "s_cselect_b32 s63, s63, s31\n"
      "s_add_u32 m0, s42, 61440\n"
      "s_waitcnt vmcnt(18) lgkmcnt(0)\n"
      "s_barrier\n"
      "ds_read_b64_tr_b16 a[56:57], v20 offset:24576\n"
      "ds_read_b64_tr_b16 a[58:59], v20 offset:26112\n"
      "ds_read_b64_tr_b16 a[152:153], v20 offset:24584\n"
      "ds_read_b64_tr_b16 a[154:155], v20 offset:26120\n"
      "ds_read_b64_tr_b16 a[248:249], v21 offset:24576\n"
      "ds_read_b64_tr_b16 a[250:251], v21 offset:26112\n"
      "buffer_load_dwordx4 v178, s[12:15], s63 offen offset:0 lds\n"
      "buffer_load_dwordx4 v179, s[12:15], s63 offen offset:1024 lds\n"
      "buffer_load_dwordx4 v180, s[12:15], s63 offen offset:2048 lds\n"
      "s_sub_u32 s62, s62, 1\n"
      ".Ld_rs15:\n"
      "s_add_u32 s63, s44, 811008\n"
      "s_cmp_ge_u32 s62, 7\n"
      "s_cselect_b32 s63, s63, s31\n"
      "s_add_u32 m0, s42, 73728\n"
      "s_waitcnt vmcnt(18) lgkmcnt(0)\n"
      "s_barrier\n"
      "ds_read_b64_tr_b16 a[60:61], v20 offset:36864\n"
      "ds_read_b64_tr_b16 a[62:63], v20 offset:38400\n"
      "ds_read_b64_tr_b16 a[156:157], v20 offset:36872\n"
      "ds_read_b64_tr_b16 a[158:159], v20 offset:38408\n"
      "ds_read_b64_tr_b16 a[252:253], v21 offset:36864\n"
      "ds_read_b64_tr_b16 a[254:255], v21 offset:38400\n"
      "buffer_load_dwordx4 v178, s[12:15], s63 offen offset:0 lds\n"
      "buffer_load_dwordx4 v179, s[12:15], s63 offen offset:1024 lds\n"
      "buffer_load_dwordx4 v180, s[12:15], s63 offen offset:2048 lds\n"
      "s_sub_u32 s62, s62, 1\n"
      ".Ld_rs16:\n"
      "s_add_u32 s63, s44, 847872\n"
      "s_cmp_ge_u32 s62, 7\n"
      "s_cselect_b32 s63, s63, s31\n"
      "s_add_u32 m0, s42, 86016\n"
      "s_waitcnt vmcnt(18) lgkmcnt(0)\n"
      "s_barrier\n"
      "ds_read_b64_tr_b16 a[64:65], v176 offset:0\n"
      "ds_read_b64_tr_b16 a[66:67], v176 offset:1536\n"
      "ds_read_b64_tr_b16 a[160:161], v176 offset:8\n"
      "ds_read_b64_tr_b16 a[162:163], v176 offset:1544\n"
      "ds_read_b64_tr_b16 v[24:25], v177 offset:0\n"
      "ds_read_b64_tr_b16 v[26:27], v177 offset:1536\n"
      "buffer_load_dwordx4 v178, s[12:15], s63 offen offset:0 lds\n"
      "buffer_load_dwordx4 v179, s[12:15], s63 offen offset:1024 lds\n"
      "buffer_load_dwordx4 v180, s[12:15], s63 offen offset:2048 lds\n"
      "s_sub_u32 s62, s62, 1\n"
      ".Ld_rs17:\n"
      "s_add_u32 s63, s44, 0\n"
      "s_cmp_ge_u32 s62, 7\n"
      "s_cselect_b32 s63, s63, s31\n"
      "s_add_u32 m0, s42, 0\n"
      "s_waitcnt vmcnt(18) lgkmcnt(0)\n"
      "s_barrier\n"
      "ds_read_b64_tr_b16 a[68:69], v176 offset:12288\n"
      "ds_read_b64_tr_b16 a[70:71], v176 offset:13824\n"
      "ds_read_b64_tr_b16 a[164:165], v176 offset:12296\n"
      "ds_read_b64_tr_b16 a[166:167], v176 offset:13832\n"
      "ds_read_b64_tr_b16 v[28:29], v177 offset:12288\n"
      "ds_read_b64_tr_b16 v[30:31], v177 offset:13824\n"
      "buffer_load_dwordx4 v178, s[12:15], s63 offen offset:0 lds\n"
      "buffer_load_dwordx4 v179, s[12:15], s63 offen offset:1024 lds\n"
      "buffer_load_dwordx4 v180, s[12:15], s63 offen offset:2048 lds\n"
      "s_sub_u32 s62, s62, 1\n"
      "s_cbranch_scc1 .Ld_re\n"
      ".Ld_rs18:\n"
      "s_add_u32 s63, s44, 36864\n"
      "s_cmp_ge_u32 s62, 7\n"
      "s_cselect_b32 s63, s63, s31\n"
      "s_add_u32 m0, s42, 12288\n"
      "s_waitcnt vmcnt(18) lgkmcnt(0)\n"
      "s_barrier\n"
      "ds_read_b64_tr_b16 a[72:73], v176 offset:24576\n"
      "ds_read_b64_tr_b16 a[74:75], v176 offset:26112\n"
      "ds_read_b64_tr_b16 a[168:169], v176 offset:24584\n"
      "ds_read_b64_tr_b16 a[170:171], v176 offset:26120\n"
      "ds_read_b64_tr_b16 v[32:33], v177 offset:24576\n"
      "ds_read_b64_tr_b16 v[34:35], v177 offset:26112\n"
      "buffer_load_dwordx4 v178, s[12:15], s63 offen offset:0 lds\n"
      "buffer_load_dwordx4 v179, s[12:15], s63 offen offset:1024 lds\n"
      "buffer_load_dwordx4 v180, s[12:15], s63 offen offset:2048 lds\n"
      "s_sub_u32 s62, s62, 1\n"
      ".Ld_rs19:\n"
      "s_add_u32 s63, s44, 73728\n"
      "s_cmp_ge_u32 s62, 7\n"
      "s_cselect_b32 s63, s63, s31\n"
      "s_add_u32 m0, s42, 24576\n"
      "s_waitcnt vmcnt(18) lgkmcnt(0)\n"
      "s_barrier\n"
      "ds_read_b64_tr_b16 a[76:77], v176 offset:36864\n"
      "ds_read_b64_tr_b16 a[78:79], v176 offset:38400\n"
      "ds_read_b64_tr_b16 a[172:173], v176 offset:36872\n"
      "ds_read_b64_tr_b16 a[174:175], v176 offset:38408\n"
      "ds_read_b64_tr_b16 v[36:37], v177 offset:36864\n"
      "ds_read_b64_tr_b16 v[38:39], v177 offset:38400\n"
      "buffer_load_dwordx4 v178, s[12:15], s63 offen offset:0 lds\n"
      "buffer_load_dwordx4 v179, s[12:15], s63 offen offset:1024 lds\n"
      "buffer_load_dwordx4 v180, s[12:15], s63 offen offset:2048 lds\n"
      "s_sub_u32 s62, s62, 1\n"
      ".Ld_rs20:\n"
      "s_add_u32 s63, s44, 110592\n"
      "s_cmp_ge_u32 s62, 7\n"
      "s_cselect_b32 s63, s63, s31\n"
      "s_add_u32 m0, s42, 36864\n"
      "s_waitcnt vmcnt(18) lgkmcnt(0)\n"
      "s_barrier\n"
      "ds_read_b64_tr_b16 a[80:81], v20 offset:0\n"
      "ds_read_b64_tr_b16 a[82:83], v20 offset:1536\n"
      "ds_read_b64_tr_b16 a[176:177], v20 offset:8\n"
      "ds_read_b64_tr_b16 a[178:179], v20 offset:1544\n"
      "ds_read_b64_tr_b16 v[40:41], v21 offset:0\n"
      "ds_read_b64_tr_b16 v[42:43], v21 offset:1536\n"
      "buffer_load_dwordx4 v178, s[12:15], s63 offen offset:0 lds\n"
      "buffer_load_dwordx4 v179, s[12:15], s63 offen offset:1024 lds\n"
      "buffer_load_dwordx4 v180, s[12:15], s63 offen offset:2048 lds\n"
      "s_sub_u32 s62, s62, 1\n"
      ".Ld_rs21:\n"
      "s_add_u32 s63, s44, 147456\n"
      "s_cmp_ge_u32 s62, 7\n"
      "s_cselect_b32 s63, s63, s31\n"
      "s_add_u32 m0, s42, 49152\n"
      "s_waitcnt vmcnt(18) lgkmcnt(0)\n"
      "s_barrier\n"
      "ds_read_b64_tr_b16 a[84:85], v20 offset:12288\n"
      "ds_read_b64_tr_b16 a[86:87], v20 offset:13824\n"
      "ds_read_b64_tr_b16 a[180:181], v20 offset:12296\n"
      "ds_read_b64_tr_b16 a[182:183], v20 offset:13832\n"
      "ds_read_b64_tr_b16 v[44:45], v21 offset:12288\n"
      "ds_read_b64_tr_b16 v[46:47], v21 offset:13824\n"
      "buffer_load_dwordx4 v178, s[12:15], s63 offen offset:0 lds\n"
      "buffer_load_dwordx4 v179, s[12:15], s63 offen offset:1024 lds\n"
      "buffer_load_dwordx4 v180, s[12:15], s63 offen offset:2048 lds\n"
      "s_sub_u32 s62, s62, 1\n"
      ".Ld_rs22:\n"
      "s_add_u32 s63, s44, 184320\n"
      "s_cmp_ge_u32 s62, 7\n"
      "s_cselect_b32 s63, s63, s31\n"
      "s_add_u32 m0, s42, 61440\n"
      "s_waitcnt vmcnt(18) lgkmcnt(0)\n"
      "s_barrier\n"
      "ds_read_b64_tr_b16 a[88:89], v20 offset:24576\n"
      "ds_read_b64_tr_b16 a[90:91], v20 offset:26112\n"
      "ds_read_b64_tr_b16 a[184:185], v20 offset:24584\n"
      "ds_read_b64_tr_b16 a[186:187], v20 offset:26120\n"
      "ds_read_b64_tr_b16 v[48:49], v21 offset:24576\n"
      "ds_read_b64_tr_b16 v[50:51], v21 offset:26112\n"
      "buffer_load_dwordx4 v178, s[12:15], s63 offen offset:0 lds\n"
      "buffer_load_dwordx4 v179, s[12:15], s63 offen offset:1024 lds\n"
      "buffer_load_dwordx4 v180, s[12:15], s63 offen offset:2048 lds\n"
      "s_sub_u32 s62, s62, 1\n"
      ".Ld_rs23:\n"
      "s_add_u32 s63, s44, 221184\n"
      "s_cmp_ge_u32 s62, 7\n"
      "s_cselect_b32 s63, s63, s31\n"
      "s_add_u32 m0, s42, 73728\n"
      "s_waitcnt vmcnt(18) lgkmcnt(0)\n"
      "s_barrier\n"
      "ds_read_b64_tr_b16 a[92:93], v20 offset:36864\n"
      "ds_read_b64_tr_b16 a[94:95], v20 offset:38400\n"
      "ds_read_b64_tr_b16 a[188:189], v20 offset:36872\n"
      "ds_read_b64_tr_b16 a[190:191], v20 offset:38408\n"
      "ds_read_b64_tr_b16 v[52:53], v21 offset:36864\n"
      "ds_read_b64_tr_b16 v[54:55], v21 offset:38400\n"
      "buffer_load_dwordx4 v178, s[12:15], s63 offen offset:0 lds\n"
      "buffer_load_dwordx4 v179, s[12:15], s63 offen offset:1024 lds\n"
      "buffer_load_dwordx4 v180, s[12:15], s63 offen offset:2048 lds\n"
      "s_sub_u32 s62, s62, 1\n"
      "s_cbranch_scc0 .Ld_rs0\n"
      ".Ld_re:\n"
      "s_waitcnt vmcnt(0) lgkmcnt(0)\n"
      "v_add_u32 v22, 49152, v0\n"
      "v_add_u32 v23, 98304, v0\n"
      "s_lshr_b32 s58, s42, 10\n"
      "s_mul_i32 s58, s58, 1366\n"
      "s_and_b32 s58, s58, 0x7000\n"
      "s_getpc_b64 vcc\n"
      ".Ld_ct:\n"
      "s_add_u32 vcc_lo, vcc_lo, .Ld_p0-.Ld_ct\n"
      "s_addc_u32 vcc_hi, vcc_hi, 0\n"
      "s_lshl_b32 s30, s58, 1\n"
      "v_lshl_add_u32 v56, v0, 3, s30\n"
      "v_min_u32 v56, .Ld_last-.Ld_p0-4, v56\n"
      "global_load_dword v56, v56, vcc\n"
      "s_waitcnt vmcnt(0) lgkmcnt(0)\n"
      "s_mov_b64 exec, 0\n"
      "s_lshr_b32 s22, s58, 12\n"
      ".Ld_pl:\n"
      "s_cmp_eq_u32 s22, 0\n"
      "s_cbranch_scc1 .Ld_p0\n"
      "s_cmp_eq_u32 s22, 1\n"
      "s_cbranch_scc1 .Ld_p1\n"
      "s_branch .Ld_pe\n"
      ".Ld_pr:\n"
      "s_add_u32 s22, s22, 4\n"
      "s_branch .Ld_pl\n"
      ".Ld_pe:\n"
      "s_mov_b64 exec, -1\n"
      "s_barrier\n"
      "v_mov_b32 v14, 0\n"
      "ds_write_b32 v16, v14\n"
      "s_waitcnt lgkmcnt(0)\n"
      "s_barrier\n"
      "s_mov_b32 s6, s50\n"
      "s_add_u32 s36, s36, 4294801408\n"
      "s_add_u32 s53, s37, 0\n"
      "s_add_u32 s52, s37, 4294787584\n"
      "s_add_u32 s37, s37, 4294894080\n"
      "s_mov_b32 s56, 0x40000000\n"
      "s_mov_b32 s57, 0x40000000\n"
      "s_mov_b32 s59, 63\n"
      "s_cmp_eq_u32 s45, 1\n"
      "s_cbranch_scc1 .Ld_in1\n"
      "s_cmp_eq_u32 s45, 2\n"
      "s_cbranch_scc1 .Ld_in2\n"
      "s_cmp_eq_u32 s45, 3\n"
      "s_cbranch_scc1 .Ld_in3\n"
      "s_cmp_eq_u32 s45, 4\n"
      "s_cbranch_scc1 .Ld_in4\n"
      "s_cmp_eq_u32 s45, 5\n"
      "s_cbranch_scc1 .Ld_in5\n"
      "s_add_u32 s36, s36, 165888\n"
      "s_min_u32 s36, s36, s6\n"
      "buffer_load_dword v20, v2, s[8:11], s36 offen sc1\n"
      "s_add_u32 m0, s43, 0\n"
      "s_mov_b32 s51, s36\n"
      "s_add_u32 s36, s36, 165888\n"
      "s_min_u32 s36, s36, s6\n"
      "s_waitcnt vmcnt(0)\n"
      "v_cmp_ne_u32 vcc, s21, v20\n"
      "s_cbranch_vccnz .Lsp_d0_1\n"
      ".Lbk_d0_1:\n"
      "buffer_load_dwordx4 v0, s[8:11], s51 offen offset:0 sc1 lds\n"
      "buffer_load_dwordx4 v0, s[8:11], s51 offen offset:1024 sc1 lds\n"
      "buffer_load_dword v20, v2, s[8:11], s36 offen sc1\n"
      "buffer_load_dwordx4 v0, s[8:11], s51 offen offset:2048 sc1 lds\n"
      "buffer_load_dwordx4 v0, s[8:11], s51 offen offset:3072 sc1 lds\n"
      "s_add_u32 m0, s43, 4096\n"
      "s_nop 0\n"
      "buffer_load_dwordx4 v1, s[8:11], s51 offen offset:1024 sc1 lds\n"
      "buffer_load_dwordx4 v1, s[8:11], s51 offen offset:0 sc1 lds\n"
      "s_mov_b32 s51, s36\n"
      "s_add_u32 m0, s43, 24576\n"
      "v_add_u32 v14, 1, v14\n"
      "s_add_u32 s36, s36, 165888\n"
      "buffer_load_dwordx4 v[216:219], v6, s[8:11], s38 offen sc1\n"
      "s_waitcnt vmcnt(5)\n"
      "v_cmp_ne_u32 vcc, s21, v20\n"
      "buffer_load_dwordx2 v[220:221], v7, s[8:11], s38 offen sc1\n"
      "s_cbranch_vccnz .Lsp_d1_2\n"
      ".Lbk_d1_2:\n"
      "buffer_load_dwordx4 v0, s[8:11], s51 offen offset:0 sc1 lds\n"
      "buffer_load_dwordx4 v0, s[8:11], s51 offen offset:1024 sc1 lds\n"
      "s_waitcnt vmcnt(4)\n"
      "ds_write_b32 v16, v14\n"
      "buffer_load_dwordx4 v0, s[8:11], s51 offen offset:2048 sc1 lds\n"
      "s_min_u32 s36, s36, s6\n"
      "buffer_load_dword v20, v2, s[8:11], s36 offen sc1\n"
      "buffer_load_dwordx4 v0, s[8:11], s51 offen offset:3072 sc1 lds\n"
      "ds_read_b32 v15, v17\n"
      "s_add_u32 m0, s43, 28672\n"
      "s_nop 0\n"
      "buffer_load_dwordx4 v1, s[8:11], s51 offen offset:0 sc1 lds\n"
      "s_waitcnt lgkmcnt(0)\n"
      "v_cmp_gt_i32 vcc, v14, v15\n"
      "buffer_load_dwordx4 v1, s[8:11], s51 offen offset:1024 sc1 lds\n"
      "s_cbranch_vccnz .Lsp_dy0_3\n"
      ".Lbk_dy0_3:\n"
      "ds_read_b128 v[56:59], v0 offset:0\n"
      "ds_read_b128 v[60:63], v0 offset:1024\n"
      "ds_read_b128 v[84:87], v0 offset:7168\n"
      "ds_read_b128 v[72:75], v0 offset:4096\n"
      "ds_read_b128 v[88:91], v0 offset:8192\n"
      "ds_read_b128 v[68:71], v0 offset:3072\n"
      "ds_read_b128 v[104:107], v0 offset:12288\n"
      "ds_read_b128 v[96:99], v0 offset:10240\n"
      "ds_read_b128 v[80:83], v0 offset:6144\n"
      "ds_read_b128 v[64:67], v0 offset:2048\n"
      "ds_read_b128 v[92:95], v0 offset:9216\n"
      "ds_read_b128 v[76:79], v0 offset:5120\n"
      "ds_read_b128 v[132:135], v0 offset:19456\n"
      "ds_read_b128 v[100:103], v0 offset:11264\n"
      "s_waitcnt vmcnt(0) lgkmcnt(0)\n"
      "s_nop 4\n"
      "s_branch .Ld_t0\n"
      ".p2align 12\n"
      ".Ld_p0:\n"
      "s_branch .Ld_pr\n"
      ".p2align 5\n"
      ".Ld_t0:\n"
      "v_mfma_f32_16x16x32_bf16 v[152:155], a[0:3], v[56:59], 0\n"
      "ds_read_b128 v[108:111], v0 offset:13312\n"
      "ds_read_b128 v[120:123], v0 offset:16384\n"
      "v_mfma_f32_16x16x32_bf16 v[152:155], a[4:7], v[60:63], v[152:155]\n"
      "s_waitcnt lgkmcnt(6)\n"
      "ds_read_b128 v[116:119], v0 offset:15360\n"
      "v_add_f32 v236, v236, v168\n"
      "v_mfma_f32_16x16x32_bf16 v[152:155], a[8:11], v[64:67], v[152:155]\n"
      "v_cvt_pk_bf16_f32 v228, v160, v161\n"
      "v_cvt_pk_bf16_f32 v229, v162, v163\n"
      "v_mfma_f32_16x16x32_bf16 v[152:155], a[12:15], v[68:71], v[152:155]\n"
      "buffer_store_dwordx2 v[228:229], v13, s[8:11], s37 offen sc1\n"
      "v_mfma_f32_16x16x32_bf16 v[152:155], a[16:19], v[72:75], v[152:155]\n"
      "s_waitcnt lgkmcnt(4)\n"
      "v_cvt_pk_bf16_f32 v178, v168, v169\n"
      "v_add_f32 v175, v175, v213\n"
      "v_mfma_f32_16x16x32_bf16 v[152:155], a[20:23], v[76:79], v[152:155]\n"
      "ds_read_b128 v[112:115], v0 offset:14336\n"
      "v_add_f32 v171, v171, v211\n"
      "v_mfma_f32_16x16x32_bf16 v[152:155], a[24:27], v[80:83], v[152:155]\n"
      "ds_read_b128 v[124:127], v0 offset:17408\n"
      "v_add_f32 v236, v236, v169\n"
      "v_mfma_f32_16x16x32_bf16 v[152:155], a[28:31], v[84:87], v[152:155]\n"
      "v_add_f32 v173, v173, v204\n"
      "v_cvt_pk_bf16_f32 v179, v170, v171\n"
      "v_mfma_f32_16x16x32_bf16 v[152:155], a[32:35], v[88:91], v[152:155]\n"
      "ds_read_b128 v[128:131], v0 offset:18432\n"
      "v_add_f32 v173, v173, v212\n"
      "v_mfma_f32_16x16x32_bf16 v[152:155], a[36:39], v[92:95], v[152:155]\n"
      "buffer_store_dwordx4 v[176:179], v6, s[8:11], s56 offen sc1\n"
      "v_mfma_f32_16x16x32_bf16 v[152:155], a[40:43], v[96:99], v[152:155]\n"
      "s_waitcnt lgkmcnt(3)\n"
      "ds_read_b128 v[148:151], v0 offset:23552\n"
      "v_cvt_pk_bf16_f32 v180, v172, v173\n"
      "v_mfma_f32_16x16x32_bf16 v[152:155], a[44:47], v[100:103], v[152:155]\n"
      "ds_read_b128 v[144:147], v0 offset:22528\n"
      "v_fmac_f32 v237, v168, v168\n"
      "v_mfma_f32_16x16x32_bf16 v[152:155], a[48:51], v[104:107], v[152:155]\n"
      "s_add_u32 m0, s43, 49152\n"
      "ds_read_b128 v[136:139], v0 offset:20480\n"
      "v_fmac_f32 v237, v169, v169\n"
      "v_mfma_f32_16x16x32_bf16 v[152:155], a[52:55], v[108:111], v[152:155]\n"
      "s_waitcnt lgkmcnt(4)\n"
      "s_mov_b32 s51, s36\n"
      "ds_read_b128 v[140:143], v0 offset:21504\n"
      "v_mfma_f32_16x16x32_bf16 v[152:155], a[56:59], v[112:115], v[152:155]\n"
      "v_cvt_pk_bf16_f32 v181, v174, v175\n"
      "v_fmac_f32 v237, v170, v170\n"
      "v_mfma_f32_16x16x32_bf16 v[152:155], a[60:63], v[116:119], v[152:155]\n"
      "buffer_store_dwordx2 v[180:181], v7, s[8:11], s56 offen sc1\n"
      "v_mfma_f32_16x16x32_bf16 v[152:155], a[64:67], v[120:123], v[152:155]\n"
      "v_add_u32 v14, 1, v14\n"
      "v_mfma_f32_16x16x32_bf16 v[152:155], a[68:71], v[124:127], v[152:155]\n"
      "s_waitcnt lgkmcnt(3)\n"
      "v_add_f32 v236, v236, v170\n"
      "v_fmac_f32 v237, v171, v171\n"
      "v_mfma_f32_16x16x32_bf16 v[152:155], a[72:75], v[128:131], v[152:155]\n"
      "v_fmac_f32 v237, v172, v172\n"
      "v_fmac_f32 v237, v173, v173\n"
      "v_mfma_f32_16x16x32_bf16 v[152:155], a[76:79], v[132:135], v[152:155]\n"
      "s_waitcnt lgkmcnt(1)\n"
      "buffer_store_dwordx2 v[180:181], v9, s[16:19], s57 offen\n"
      "v_mfma_f32_16x16x32_bf16 v[152:155], a[80:83], v[136:139], v[152:155]\n"
      "s_waitcnt lgkmcnt(0)\n"
      "v_add_f32 v236, v236, v171\n"
      "v_fmac_f32 v237, v174, v174\n"
      "v_mfma_f32_16x16x32_bf16 v[152:155], a[84:87], v[140:143], v[152:155]\n"
      "v_add_f32 v236, v236, v172\n"
      "v_fmac_f32 v237, v175, v175\n"
      "v_mfma_f32_16x16x32_bf16 v[152:155], a[88:91], v[144:147], v[152:155]\n"
      "v_add_f32 v236, v236, v173\n"
      "v_add_f32 v236, v236, v174\n"
      "v_mfma_f32_16x16x32_bf16 v[152:155], a[92:95], v[148:151], v[152:155]\n"
      "buffer_store_dwordx4 v[176:179], v8, s[16:19], s57 offen\n"
      "v_mfma_f32_16x16x32_bf16 v[156:159], a[96:99], v[56:59], 0\n"
      "s_waitcnt vmcnt(15)\n"
      "v_add_f32 v236, v236, v175\n"
      "v_lshlrev_b32 v166, 16, v217\n"
      "v_mfma_f32_16x16x32_bf16 v[156:159], a[100:103], v[60:63], v[156:159]\n"
      "s_waitcnt vmcnt(10)\n"
      "v_cmp_ne_u32 vcc, s21, v20\n"
      "v_mfma_f32_16x16x32_bf16 v[156:159], a[104:107], v[64:67], v[156:159]\n"
      "ds_write_b64 v10, v[236:237]\n"
      "v_mfma_f32_16x16x32_bf16 v[156:159], a[108:111], v[68:71], v[156:159]\n"
      "s_add_u32 s36, s36, 165888\n"
      "v_mfma_f32_16x16x32_bf16 v[156:159], a[112:115], v[72:75], v[156:159]\n"
      "s_min_u32 s36, s36, s6\n"
      "s_cbranch_vccnz .Lsp_d2_4\n"
      ".Lbk_d2_4:\n"
      "v_mfma_f32_16x16x32_bf16 v[156:159], a[116:119], v[76:79], v[156:159]\n"
      "s_waitcnt vmcnt(7)\n"
      "ds_write_b32 v16, v14\n"
      "v_mfma_f32_16x16x32_bf16 v[156:159], a[120:123], v[80:83], v[156:159]\n"
      "buffer_load_dword v20, v2, s[8:11], s36 offen sc1\n"
      "v_mfma_f32_16x16x32_bf16 v[156:159], a[124:127], v[84:87], v[156:159]\n"
      "v_and_b32 v167, 0xffff0000, v217\n"
      "v_lshlrev_b32 v164, 16, v216\n"
      "v_mfma_f32_16x16x32_bf16 v[156:159], a[128:131], v[88:91], v[156:159]\n"
      "v_mfma_f32_16x16x32_bf16 v[156:159], a[132:135], v[92:95], v[156:159]\n"
      "buffer_load_dwordx4 v0, s[8:11], s51 offen offset:3072 sc1 lds\n"
      "v_mfma_f32_16x16x32_bf16 v[156:159], a[136:139], v[96:99], v[156:159]\n"
      "s_add_u32 s52, s52, 106496\n"
      "v_mfma_f32_16x16x32_bf16 v[156:159], a[140:143], v[100:103], v[156:159]\n"
      "v_mfma_f32_16x16x32_bf16 v[156:159], a[144:147], v[104:107], v[156:159]\n"
      "s_add_u32 s37, s37, 173056\n"
      "v_and_b32 v165, 0xffff0000, v216\n"
      "v_mfma_f32_16x16x32_bf16 v[156:159], a[148:151], v[108:111], v[156:159]\n"
      "buffer_load_dwordx4 v0, s[8:11], s51 offen offset:1024 sc1 lds\n"
      "v_mfma_f32_16x16x32_bf16 v[156:159], a[152:155], v[112:115], v[156:159]\n"
      "ds_read_b32 v15, v17\n"
      "v_mfma_f32_16x16x32_bf16 v[156:159], a[156:159], v[116:119], v[156:159]\n"
      "v_mfma_f32_16x16x32_bf16 v[156:159], a[160:163], v[120:123], v[156:159]\n"
      "v_lshlrev_b32 v170, 16, v219\n"
      "v_mfma_f32_16x16x32_bf16 v[156:159], a[164:167], v[124:127], v[156:159]\n"
      "buffer_load_dwordx4 v0, s[8:11], s51 offen offset:2048 sc1 lds\n"
      "v_mfma_f32_16x16x32_bf16 v[156:159], a[168:171], v[128:131], v[156:159]\n"
      "v_mfma_f32_16x16x32_bf16 v[156:159], a[172:175], v[132:135], v[156:159]\n"
      "s_waitcnt lgkmcnt(0)\n"
      "v_cmp_gt_i32 vcc, v14, v15\n"
      "v_mfma_f32_16x16x32_bf16 v[156:159], a[176:179], v[136:139], v[156:159]\n"
      "v_add_f32 v165, v165, v153\n"
      "v_mfma_f32_16x16x32_bf16 v[156:159], a[180:183], v[140:143], v[156:159]\n"
      "buffer_load_dwordx4 v0, s[8:11], s51 offen offset:0 sc1 lds\n"
      "v_mfma_f32_16x16x32_bf16 v[156:159], a[184:187], v[144:147], v[156:159]\n"
      "v_add_f32 v164, v164, v152\n"
      "s_add_u32 m0, s43, 53248\n"
      "v_mfma_f32_16x16x32_bf16 v[156:159], a[188:191], v[148:151], v[156:159]\n"
      "s_cbranch_vccnz .Lsp_dy1_5\n"
      ".Lbk_dy1_5:\n"
      "ds_read_b64 v[238:239], v11\n"
      "v_mfma_f32_16x16x32_bf16 v[160:163], a[192:195], v[56:59], 0\n"
      "v_lshlrev_b32 v168, 16, v218\n"
      "v_mfma_f32_16x16x32_bf16 v[160:163], a[196:199], v[60:63], v[160:163]\n"
      "buffer_load_dwordx4 v1, s[8:11], s51 offen offset:0 sc1 lds\n"
      "v_mfma_f32_16x16x32_bf16 v[160:163], a[200:203], v[64:67], v[160:163]\n"
      "v_mfma_f32_16x16x32_bf16 v[160:163], a[204:207], v[68:71], v[160:163]\n"
      "ds_read_b128 v[56:59], v0 offset:24576\n"
      "v_mfma_f32_16x16x32_bf16 v[160:163], a[208:211], v[72:75], v[160:163]\n"
      "ds_read_b128 v[64:67], v0 offset:26624\n"
      "v_mfma_f32_16x16x32_bf16 v[160:163], a[212:215], v[76:79], v[160:163]\n"
      "buffer_load_dwordx4 v1, s[8:11], s51 offen offset:1024 sc1 lds\n"
      "v_mfma_f32_16x16x32_bf16 v[160:163], a[216:219], v[80:83], v[160:163]\n"
      "s_waitcnt lgkmcnt(2)\n"
      "ds_read_b128 v[68:71], v0 offset:27648\n"
      "v_add_f32_dpp v238, v238, v238 row_ror:8 row_mask:0xf bank_mask:0xf\n"
      "v_mfma_f32_16x16x32_bf16 v[160:163], a[220:223], v[84:87], v[160:163]\n"
      "ds_read_b128 v[80:83], v0 offset:30720\n"
      "v_add_f32_dpp v238, v238, v238 row_ror:4 row_mask:0xf bank_mask:0xf\n"
      "v_mfma_f32_16x16x32_bf16 v[160:163], a[224:227], v[88:91], v[160:163]\n"
      "ds_read_b128 v[60:63], v0 offset:25600\n"
      "v_add_f32_dpp v238, v238, v238 row_ror:2 row_mask:0xf bank_mask:0xf\n"
      "v_mfma_f32_16x16x32_bf16 v[160:163], a[228:231], v[92:95], v[160:163]\n"
      "ds_read_b128 v[92:95], v0 offset:33792\n"
      "v_add_f32_dpp v239, v239, v239 row_ror:8 row_mask:0xf bank_mask:0xf\n"
      "v_mfma_f32_16x16x32_bf16 v[160:163], a[232:235], v[96:99], v[160:163]\n"
      "ds_read_b128 v[84:87], v0 offset:31744\n"
      "v_add_f32_dpp v239, v239, v239 row_ror:4 row_mask:0xf bank_mask:0xf\n"
      "v_mfma_f32_16x16x32_bf16 v[160:163], a[236:239], v[100:103], v[160:163]\n"
      "s_waitcnt vmcnt(11) lgkmcnt(5)\n"
      "buffer_store_dword v18, v4, s[8:11], s52 offen sc1\n"
      "v_mfma_f32_16x16x32_bf16 v[160:163], a[240:243], v[104:107], v[160:163]\n"
      "ds_read_b128 v[72:75], v0 offset:28672\n"
      "v_add_f32_dpp v239, v239, v239 row_ror:2 row_mask:0xf bank_mask:0xf\n"
      "v_mfma_f32_16x16x32_bf16 v[160:163], a[244:247], v[108:111], v[160:163]\n"
      "ds_read_b128 v[76:79], v0 offset:29696\n"
      "v_add_f32_dpp v239, v239, v239 row_ror:1 row_mask:0xf bank_mask:0xf\n"
      "v_mfma_f32_16x16x32_bf16 v[160:163], a[248:251], v[112:115], v[160:163]\n"
      "v_add_f32_dpp v238, v238, v238 row_ror:1 row_mask:0xf bank_mask:0xf\n"
      "v_add_f32 v168, v168, v156\n"
      "v_mfma_f32_16x16x32_bf16 v[160:163], a[252:255], v[116:119], v[160:163]\n"
      "ds_read_b128 v[100:103], v0 offset:35840\n"
      "v_mul_f32 v238, 0x3ae38e39, v238\n"
      "v_mfma_f32_16x16x32_bf16 v[160:163], v[24:27], v[120:123], v[160:163]\n"
      "v_and_b32 v169, 0xffff0000, v218\n"
      "v_mfma_f32_16x16x32_bf16 v[160:163], v[28:31], v[124:127], v[160:163]\n"
      "ds_read_b128 v[96:99], v0 offset:34816\n"
      "v_and_b32 v175, 0xffff0000, v221\n"
      "v_mfma_f32_16x16x32_bf16 v[160:163], v[32:35], v[128:131], v[160:163]\n"
      "ds_read_b128 v[116:119], v0 offset:39936\n"
      "v_lshlrev_b32 v174, 16, v221\n"
      "v_mfma_f32_16x16x32_bf16 v[160:163], v[36:39], v[132:135], v[160:163]\n"
      "ds_read_b128 v[120:123], v0 offset:40960\n"
      "v_add_f32 v167, v167, v155\n"
      "v_mfma_f32_16x16x32_bf16 v[160:163], v[40:43], v[136:139], v[160:163]\n"
      "ds_read_b128 v[108:111], v0 offset:37888\n"
      "v_and_b32 v171, 0xffff0000, v219\n"
      "v_mfma_f32_16x16x32_bf16 v[160:163], v[44:47], v[140:143], v[160:163]\n"
      "ds_read_b128 v[88:91], v0 offset:32768\n"
      "v_add_f32 v166, v166, v154\n"
      "v_mfma_f32_16x16x32_bf16 v[160:163], v[48:51], v[144:147], v[160:163]\n"
      "v_add_f32 v239, 0x3afba882, v239\n"
      "v_mul_f32 v239, 0x3ae38e39, v239\n"
      "v_mfma_f32_16x16x32_bf16 v[160:163], v[52:55], v[148:151], v[160:163]\n"
      "s_sub_u32 s59, s59, 1\n"
      "s_cbranch_scc1 .Ld_out0\n"
      ".Ld_t1:\n"
      "v_mfma_f32_16x16x32_bf16 v[152:155], a[0:3], v[56:59], 0\n"
      "s_waitcnt lgkmcnt(4)\n"
      "buffer_store_dwordx2 v[238:239], v12, s[8:11], s56 offen sc1\n"
      "v_mfma_f32_16x16x32_bf16 v[152:155], a[4:7], v[60:63], v[152:155]\n"
      "ds_read_b128 v[104:107], v0 offset:36864\n"
      "v_mfma_f32_16x16x32_bf16 v[152:155], a[8:11], v[64:67], v[152:155]\n"
      "ds_read_b128 v[140:143], v0 offset:46080\n"
      "v_mfma_f32_16x16x32_bf16 v[152:155], a[12:15], v[68:71], v[152:155]\n"
      "ds_read_b128 v[112:115], v0 offset:38912\n"
      "v_mfma_f32_16x16x32_bf16 v[152:155], a[16:19], v[72:75], v[152:155]\n"
      "ds_read_b128 v[124:127], v0 offset:41984\n"
      "v_mfma_f32_16x16x32_bf16 v[152:155], a[20:23], v[76:79], v[152:155]\n"
      "ds_read_b128 v[132:135], v0 offset:44032\n"
      "v_mfma_f32_16x16x32_bf16 v[152:155], a[24:27], v[80:83], v[152:155]\n"
      "s_mov_b32 s51, s36\n"
      "v_add_f32 v174, v174, v162\n"
      "s_add_u32 s36, s36, 165888\n"
      "v_mfma_f32_16x16x32_bf16 v[152:155], a[28:31], v[84:87], v[152:155]\n"
      "s_waitcnt lgkmcnt(5)\n"
      "ds_read_b128 v[148:151], v0 offset:48128\n"
      "v_mfma_f32_16x16x32_bf16 v[152:155], a[32:35], v[88:91], v[152:155]\n"
      "ds_read_b128 v[128:131], v0 offset:43008\n"
      "s_add_u32 m0, s43, 73728\n"
      "v_mfma_f32_16x16x32_bf16 v[152:155], a[36:39], v[92:95], v[152:155]\n"
      "ds_read_b128 v[136:139], v0 offset:45056\n"
      "v_mfma_f32_16x16x32_bf16 v[152:155], a[40:43], v[96:99], v[152:155]\n"
      "v_mfma_f32_16x16x32_bf16 v[152:155], a[44:47], v[100:103], v[152:155]\n"
      "s_waitcnt lgkmcnt(4)\n"
      "v_add_f32 v170, v170, v158\n"
      "v_mfma_f32_16x16x32_bf16 v[152:155], a[48:51], v[104:107], v[152:155]\n"
      "v_mfma_f32_16x16x32_bf16 v[152:155], a[52:55], v[108:111], v[152:155]\n"
      "s_waitcnt vmcnt(8) lgkmcnt(3)\n"
      "v_lshlrev_b32 v172, 16, v220\n"
      "v_cmp_ne_u32 vcc, s21, v20\n"
      "v_mfma_f32_16x16x32_bf16 v[152:155], a[56:59], v[112:115], v[152:155]\n"
      "ds_read_b128 v[144:147], v0 offset:47104\n"
      "s_add_u32 s38, s38, 147456\n"
      "v_mfma_f32_16x16x32_bf16 v[152:155], a[60:63], v[116:119], v[152:155]\n"
      "s_min_u32 s36, s36, s6\n"
      "v_add_f32 v171, v171, v159\n"
      "v_mfma_f32_16x16x32_bf16 v[152:155], a[64:67], v[120:123], v[152:155]\n"
      "v_mfma_f32_16x16x32_bf16 v[152:155], a[68:71], v[124:127], v[152:155]\n"
      "s_waitcnt lgkmcnt(1)\n"
      "v_mfma_f32_16x16x32_bf16 v[152:155], a[72:75], v[128:131], v[152:155]\n"
      "v_add_f32 v169, v169, v157\n"
      "v_add_f32 v172, v172, v160\n"
      "v_mfma_f32_16x16x32_bf16 v[152:155], a[76:79], v[132:135], v[152:155]\n"
      "v_mfma_f32_16x16x32_bf16 v[152:155], a[80:83], v[136:139], v[152:155]\n"
      "s_cbranch_vccnz .Lsp_d3_6\n"
      ".Lbk_d3_6:\n"
      "v_mfma_f32_16x16x32_bf16 v[152:155], a[84:87], v[140:143], v[152:155]\n"
      "s_waitcnt lgkmcnt(0)\n"
      "v_mfma_f32_16x16x32_bf16 v[152:155], a[88:91], v[144:147], v[152:155]\n"
      "v_mfma_f32_16x16x32_bf16 v[152:155], a[92:95], v[148:151], v[152:155]\n"
      "buffer_load_dwordx4 v0, s[8:11], s51 offen offset:2048 sc1 lds\n"
      "v_mfma_f32_16x16x32_bf16 v[156:159], a[96:99], v[56:59], 0\n"
      "v_mfma_f32_16x16x32_bf16 v[156:159], a[100:103], v[60:63], v[156:159]\n"
      "v_add_u32 v14, 1, v14\n"
      "v_mfma_f32_16x16x32_bf16 v[156:159], a[104:107], v[64:67], v[156:159]\n"
      "v_cvt_pk_bf16_f32 v225, v154, v155\n"
      "v_mfma_f32_16x16x32_bf16 v[156:159], a[108:111], v[68:71], v[156:159]\n"
      "v_add_f32 v175, v175, v163\n"
      "v_mfma_f32_16x16x32_bf16 v[156:159], a[112:115], v[72:75], v[156:159]\n"
      "buffer_load_dwordx4 v0, s[8:11], s51 offen offset:1024 sc1 lds\n"
      "v_mfma_f32_16x16x32_bf16 v[156:159], a[116:119], v[76:79], v[156:159]\n"
      "v_mfma_f32_16x16x32_bf16 v[156:159], a[120:123], v[80:83], v[156:159]\n"
      "v_mfma_f32_16x16x32_bf16 v[156:159], a[124:127], v[84:87], v[156:159]\n"
      "v_and_b32 v173, 0xffff0000, v220\n"
      "v_mfma_f32_16x16x32_bf16 v[156:159], a[128:131], v[88:91], v[156:159]\n"
      "buffer_load_dword v20, v2, s[8:11], s36 offen sc1\n"
      "v_mfma_f32_16x16x32_bf16 v[156:159], a[132:135], v[92:95], v[156:159]\n"
      "s_waitcnt vmcnt(5)\n"
      "ds_write_b32 v16, v14\n"
      "v_mfma_f32_16x16x32_bf16 v[156:159], a[136:139], v[96:99], v[156:159]\n"
      "buffer_load_dwordx4 v0, s[8:11], s51 offen offset:0 sc1 lds\n"
      "v_mfma_f32_16x16x32_bf16 v[156:159], a[140:143], v[100:103], v[156:159]\n"
      "v_add_f32 v173, v173, v161\n"
      "v_mfma_f32_16x16x32_bf16 v[156:159], a[144:147], v[104:107], v[156:159]\n"
      "v_mfma_f32_16x16x32_bf16 v[156:159], a[148:151], v[108:111], v[156:159]\n"
      "v_mfma_f32_16x16x32_bf16 v[156:159], a[152:155], v[112:115], v[156:159]\n"
      "v_mfma_f32_16x16x32_bf16 v[156:159], a[156:159], v[116:119], v[156:159]\n"
      "buffer_load_dwordx4 v0, s[8:11], s51 offen offset:3072 sc1 lds\n"
      "v_mfma_f32_16x16x32_bf16 v[156:159], a[160:163], v[120:123], v[156:159]\n"
      "s_add_u32 m0, s43, 77824\n"
      "v_mfma_f32_16x16x32_bf16 v[156:159], a[164:167], v[124:127], v[156:159]\n"
      "v_mfma_f32_16x16x32_bf16 v[156:159], a[168:171], v[128:131], v[156:159]\n"
      "ds_read_b32 v15, v17\n"
      "v_mfma_f32_16x16x32_bf16 v[156:159], a[172:175], v[132:135], v[156:159]\n"
      "s_add_u32 s52, s52, 173056\n"
      "v_mfma_f32_16x16x32_bf16 v[156:159], a[176:179], v[136:139], v[156:159]\n"
      "buffer_load_dwordx4 v1, s[8:11], s51 offen offset:0 sc1 lds\n"
      "v_mfma_f32_16x16x32_bf16 v[156:159], a[180:183], v[140:143], v[156:159]\n"
      "v_mfma_f32_16x16x32_bf16 v[156:159], a[184:187], v[144:147], v[156:159]\n"
      "v_cvt_pk_bf16_f32 v224, v152, v153\n"
      "v_mfma_f32_16x16x32_bf16 v[156:159], a[188:191], v[148:151], v[156:159]\n"
      "s_waitcnt lgkmcnt(0)\n"
      "v_cmp_gt_i32 vcc, v14, v15\n"
      "v_mfma_f32_16x16x32_bf16 v[160:163], a[192:195], v[56:59], 0\n"
      "buffer_load_dwordx4 v1, s[8:11], s51 offen offset:1024 sc1 lds\n"
      "v_mfma_f32_16x16x32_bf16 v[160:163], a[196:199], v[60:63], v[160:163]\n"
      "v_mfma_f32_16x16x32_bf16 v[160:163], a[200:203], v[64:67], v[160:163]\n"
      "s_cbranch_vccnz .Lsp_dy2_7\n"
      ".Lbk_dy2_7:\n"
      "v_mfma_f32_16x16x32_bf16 v[160:163], a[204:207], v[68:71], v[160:163]\n"
      "ds_read_b128 v[68:71], v22 offset:3072\n"
      "v_mfma_f32_16x16x32_bf16 v[160:163], a[208:211], v[72:75], v[160:163]\n"
      "ds_read_b128 v[60:63], v22 offset:1024\n"
      "v_mfma_f32_16x16x32_bf16 v[160:163], a[212:215], v[76:79], v[160:163]\n"
      "s_waitcnt vmcnt(7)\n"
      "buffer_store_dword v18, v5, s[8:11], s56 offen sc1\n"
      "v_mfma_f32_16x16x32_bf16 v[160:163], a[216:219], v[80:83], v[160:163]\n"
      "v_cvt_pk_bf16_f32 v227, v158, v159\n"
      "v_mfma_f32_16x16x32_bf16 v[160:163], a[220:223], v[84:87], v[160:163]\n"
      "ds_read_b128 v[80:83], v22 offset:6144\n"
      "v_cvt_pk_bf16_f32 v226, v156, v157\n"
      "v_mfma_f32_16x16x32_bf16 v[160:163], a[224:227], v[88:91], v[160:163]\n"
      "ds_read_b128 v[72:75], v22 offset:4096\n"
      "v_mfma_f32_16x16x32_bf16 v[160:163], a[228:231], v[92:95], v[160:163]\n"
      "ds_read_b128 v[76:79], v22 offset:5120\n"
      "v_mfma_f32_16x16x32_bf16 v[160:163], a[232:235], v[96:99], v[160:163]\n"
      "ds_read_b128 v[96:99], v22 offset:10240\n"
      "v_mfma_f32_16x16x32_bf16 v[160:163], a[236:239], v[100:103], v[160:163]\n"
      "v_mfma_f32_16x16x32_bf16 v[160:163], a[240:243], v[104:107], v[160:163]\n"
      "buffer_store_dwordx4 v[224:227], v0, s[8:11], s37 offen sc1\n"
      "v_mfma_f32_16x16x32_bf16 v[160:163], a[244:247], v[108:111], v[160:163]\n"
      "v_mfma_f32_16x16x32_bf16 v[160:163], a[248:251], v[112:115], v[160:163]\n"
      "ds_read_b128 v[84:87], v22 offset:7168\n"
      "v_mfma_f32_16x16x32_bf16 v[160:163], a[252:255], v[116:119], v[160:163]\n"
      "v_mfma_f32_16x16x32_bf16 v[160:163], v[24:27], v[120:123], v[160:163]\n"
      "ds_read_b128 v[56:59], v22 offset:0\n"
      "v_mfma_f32_16x16x32_bf16 v[160:163], v[28:31], v[124:127], v[160:163]\n"
      "ds_read_b128 v[92:95], v22 offset:9216\n"
      "s_mov_b32 s51, s36\n"
      "v_mfma_f32_16x16x32_bf16 v[160:163], v[32:35], v[128:131], v[160:163]\n"
      "ds_read_b128 v[64:67], v22 offset:2048\n"
      "v_mfma_f32_16x16x32_bf16 v[160:163], v[36:39], v[132:135], v[160:163]\n"
      "ds_read_b128 v[100:103], v22 offset:11264\n"
      "v_mfma_f32_16x16x32_bf16 v[160:163], v[40:43], v[136:139], v[160:163]\n"
      "ds_read_b128 v[132:135], v22 offset:19456\n"
      "v_mfma_f32_16x16x32_bf16 v[160:163], v[44:47], v[140:143], v[160:163]\n"
      "v_mfma_f32_16x16x32_bf16 v[160:163], v[48:51], v[144:147], v[160:163]\n"
      "s_waitcnt lgkmcnt(4)\n"
      "ds_read_b128 v[112:115], v22 offset:14336\n"
      "s_add_u32 m0, s43, 98304\n"
      "v_mfma_f32_16x16x32_bf16 v[160:163], v[52:55], v[148:151], v[160:163]\n"
      "s_sub_u32 s59, s59, 1\n"
      "s_cbranch_scc1 .Ld_out1\n"
      ".Ld_t2:\n"
      "v_mfma_f32_16x16x32_bf16 v[152:155], a[0:3], v[56:59], 0\n"
      "ds_read_b128 v[88:91], v22 offset:8192\n"
      "v_mfma_f32_16x16x32_bf16 v[152:155], a[4:7], v[60:63], v[152:155]\n"
      "s_waitcnt lgkmcnt(4)\n"
      "ds_read_b128 v[120:123], v22 offset:16384\n"
      "v_mfma_f32_16x16x32_bf16 v[152:155], a[8:11], v[64:67], v[152:155]\n"
      "s_add_u32 s36, s36, 165888\n"
      "ds_read_b128 v[104:107], v22 offset:12288\n"
      "v_cvt_pk_bf16_f32 v229, v162, v163\n"
      "v_mfma_f32_16x16x32_bf16 v[152:155], a[12:15], v[68:71], v[152:155]\n"
      "ds_read_b128 v[148:151], v22 offset:23552\n"
      "v_cvt_pk_bf16_f32 v228, v160, v161\n"
      "v_mfma_f32_16x16x32_bf16 v[152:155], a[16:19], v[72:75], v[152:155]\n"
      "buffer_store_dwordx2 v[228:229], v13, s[8:11], s37 offen sc1\n"
      "v_mfma_f32_16x16x32_bf16 v[152:155], a[20:23], v[76:79], v[152:155]\n"
      "ds_read_b128 v[108:111], v22 offset:13312\n"
      "v_mfma_f32_16x16x32_bf16 v[152:155], a[24:27], v[80:83], v[152:155]\n"
      "ds_read_b128 v[116:119], v22 offset:15360\n"
      "v_mfma_f32_16x16x32_bf16 v[152:155], a[28:31], v[84:87], v[152:155]\n"
      "s_waitcnt lgkmcnt(5)\n"
      "ds_read_b128 v[124:127], v22 offset:17408\n"
      "v_mfma_f32_16x16x32_bf16 v[152:155], a[32:35], v[88:91], v[152:155]\n"
      "ds_read_b128 v[140:143], v22 offset:21504\n"
      "v_mfma_f32_16x16x32_bf16 v[152:155], a[36:39], v[92:95], v[152:155]\n"
      "ds_read_b128 v[128:131], v22 offset:18432\n"
      "v_mfma_f32_16x16x32_bf16 v[152:155], a[40:43], v[96:99], v[152:155]\n"
      "v_mfma_f32_16x16x32_bf16 v[152:155], a[44:47], v[100:103], v[152:155]\n"
      "s_waitcnt lgkmcnt(5)\n"
      "ds_read_b128 v[136:139], v22 offset:20480\n"
      "v_mfma_f32_16x16x32_bf16 v[152:155], a[48:51], v[104:107], v[152:155]\n"
      "s_waitcnt lgkmcnt(5)\n"
      "v_mfma_f32_16x16x32_bf16 v[152:155], a[52:55], v[108:111], v[152:155]\n"
      "v_mfma_f32_16x16x32_bf16 v[152:155], a[56:59], v[112:115], v[152:155]\n"
      "s_waitcnt lgkmcnt(3)\n"
      "ds_read_b128 v[144:147], v22 offset:22528\n"
      "v_mfma_f32_16x16x32_bf16 v[152:155], a[60:63], v[116:119], v[152:155]\n"
      "v_add_u32 v14, 1, v14\n"
      "v_mfma_f32_16x16x32_bf16 v[152:155], a[64:67], v[120:123], v[152:155]\n"
      "s_waitcnt vmcnt(7) lgkmcnt(2)\n"
      "v_cmp_ne_u32 vcc, s21, v20\n"
      "v_mfma_f32_16x16x32_bf16 v[152:155], a[68:71], v[124:127], v[152:155]\n"
      "v_mfma_f32_16x16x32_bf16 v[152:155], a[72:75], v[128:131], v[152:155]\n"
      "v_mfma_f32_16x16x32_bf16 v[152:155], a[76:79], v[132:135], v[152:155]\n"
      "s_waitcnt lgkmcnt(1)\n"
      "v_mfma_f32_16x16x32_bf16 v[152:155], a[80:83], v[136:139], v[152:155]\n"
      "v_mfma_f32_16x16x32_bf16 v[152:155], a[84:87], v[140:143], v[152:155]\n"
      "s_waitcnt lgkmcnt(0)\n"
      "s_cbranch_vccnz .Lsp_d4_8\n"
      ".Lbk_d4_8:\n"
      "v_mfma_f32_16x16x32_bf16 v[152:155], a[88:91], v[144:147], v[152:155]\n"
      "v_mfma_f32_16x16x32_bf16 v[152:155], a[92:95], v[148:151], v[152:155]\n"
      "s_min_u32 s36, s36, s6\n"
      "v_mfma_f32_16x16x32_bf16 v[156:159], a[96:99], v[56:59], 0\n"
      "buffer_load_dwordx4 v0, s[8:11], s51 offen offset:0 sc1 lds\n"
      "v_mfma_f32_16x16x32_bf16 v[156:159], a[100:103], v[60:63], v[156:159]\n"
      "v_mfma_f32_16x16x32_bf16 v[156:159], a[104:107], v[64:67], v[156:159]\n"
      "v_mfma_f32_16x16x32_bf16 v[156:159], a[108:111], v[68:71], v[156:159]\n"
      "v_mfma_f32_16x16x32_bf16 v[156:159], a[112:115], v[72:75], v[156:159]\n"
      "buffer_load_dwordx4 v0, s[8:11], s51 offen offset:2048 sc1 lds\n"
      "v_mfma_f32_16x16x32_bf16 v[156:159], a[116:119], v[76:79], v[156:159]\n"
      "s_waitcnt vmcnt(5)\n"
      "ds_write_b32 v16, v14\n"
      "v_mfma_f32_16x16x32_bf16 v[156:159], a[120:123], v[80:83], v[156:159]\n"
      "v_mfma_f32_16x16x32_bf16 v[156:159], a[124:127], v[84:87], v[156:159]\n"
      "v_mfma_f32_16x16x32_bf16 v[156:159], a[128:131], v[88:91], v[156:159]\n"
      "v_mfma_f32_16x16x32_bf16 v[156:159], a[132:135], v[92:95], v[156:159]\n"
      "buffer_load_dword v20, v2, s[8:11], s36 offen sc1\n"
      "v_mfma_f32_16x16x32_bf16 v[156:159], a[136:139], v[96:99], v[156:159]\n"
      "v_mfma_f32_16x16x32_bf16 v[156:159], a[140:143], v[100:103], v[156:159]\n"
      "v_cvt_pk_bf16_f32 v225, v154, v155\n"
      "v_mfma_f32_16x16x32_bf16 v[156:159], a[144:147], v[104:107], v[156:159]\n"
      "buffer_load_dwordx4 v0, s[8:11], s51 offen offset:3072 sc1 lds\n"
      "v_mfma_f32_16x16x32_bf16 v[156:159], a[148:151], v[108:111], v[156:159]\n"
      "v_mfma_f32_16x16x32_bf16 v[156:159], a[152:155], v[112:115], v[156:159]\n"
      "v_mfma_f32_16x16x32_bf16 v[156:159], a[156:159], v[116:119], v[156:159]\n"
      "ds_read_b32 v15, v17\n"
      "v_mfma_f32_16x16x32_bf16 v[156:159], a[160:163], v[120:123], v[156:159]\n"
      "buffer_load_dwordx4 v0, s[8:11], s51 offen offset:1024 sc1 lds\n"
      "v_mfma_f32_16x16x32_bf16 v[156:159], a[164:167], v[124:127], v[156:159]\n"
      "s_add_u32 m0, s43, 102400\n"
      "v_mfma_f32_16x16x32_bf16 v[156:159], a[168:171], v[128:131], v[156:159]\n"
      "v_mfma_f32_16x16x32_bf16 v[156:159], a[172:175], v[132:135], v[156:159]\n"
      "v_mfma_f32_16x16x32_bf16 v[156:159], a[176:179], v[136:139], v[156:159]\n"
      "buffer_load_dwordx4 v1, s[8:11], s51 offen offset:1024 sc1 lds\n"
      "v_mfma_f32_16x16x32_bf16 v[156:159], a[180:183], v[140:143], v[156:159]\n"
      "v_mfma_f32_16x16x32_bf16 v[156:159], a[184:187], v[144:147], v[156:159]\n"
      "s_waitcnt lgkmcnt(0)\n"
      "v_cmp_gt_i32 vcc, v14, v15\n"
      "v_mfma_f32_16x16x32_bf16 v[156:159], a[188:191], v[148:151], v[156:159]\n"
      "v_mfma_f32_16x16x32_bf16 v[160:163], a[192:195], v[56:59], 0\n"
      "buffer_load_dwordx4 v1, s[8:11], s51 offen offset:0 sc1 lds\n"
      "v_mfma_f32_16x16x32_bf16 v[160:163], a[196:199], v[60:63], v[160:163]\n"
      "s_cbranch_vccnz .Lsp_dy3_9\n"
      ".Lbk_dy3_9:\n"
      "ds_read_b128 v[60:63], v22 offset:25600\n"
      "v_mfma_f32_16x16x32_bf16 v[160:163], a[200:203], v[64:67], v[160:163]\n"
      "v_mfma_f32_16x16x32_bf16 v[160:163], a[204:207], v[68:71], v[160:163]\n"
      "v_cvt_pk_bf16_f32 v227, v158, v159\n"
      "v_mfma_f32_16x16x32_bf16 v[160:163], a[208:211], v[72:75], v[160:163]\n"
      "ds_read_b128 v[64:67], v22 offset:26624\n"
      "v_mfma_f32_16x16x32_bf16 v[160:163], a[212:215], v[76:79], v[160:163]\n"
      "s_mov_b32 s51, s36\n"
      "ds_read_b128 v[72:75], v22 offset:28672\n"
      "v_mfma_f32_16x16x32_bf16 v[160:163], a[216:219], v[80:83], v[160:163]\n"
      "ds_read_b128 v[68:71], v22 offset:27648\n"
      "v_mfma_f32_16x16x32_bf16 v[160:163], a[220:223], v[84:87], v[160:163]\n"
      "v_cvt_pk_bf16_f32 v224, v152, v153\n"
      "v_mfma_f32_16x16x32_bf16 v[160:163], a[224:227], v[88:91], v[160:163]\n"
      "s_waitcnt vmcnt(7) lgkmcnt(3)\n"
      "buffer_store_dword v18, v4, s[8:11], s52 offen sc1\n"
      "v_mfma_f32_16x16x32_bf16 v[160:163], a[228:231], v[92:95], v[160:163]\n"
      "ds_read_b128 v[92:95], v22 offset:33792\n"
      "v_mfma_f32_16x16x32_bf16 v[160:163], a[232:235], v[96:99], v[160:163]\n"
      "ds_read_b128 v[56:59], v22 offset:24576\n"
      "v_mfma_f32_16x16x32_bf16 v[160:163], a[236:239], v[100:103], v[160:163]\n"
      "buffer_load_dword v21, v3, s[8:11], s53 offen sc1\n"
      "v_mfma_f32_16x16x32_bf16 v[160:163], a[240:243], v[104:107], v[160:163]\n"
      "ds_read_b128 v[100:103], v22 offset:35840\n"
      "v_mfma_f32_16x16x32_bf16 v[160:163], a[244:247], v[108:111], v[160:163]\n"
      "ds_read_b128 v[80:83], v22 offset:30720\n"
      "v_mfma_f32_16x16x32_bf16 v[160:163], a[248:251], v[112:115], v[160:163]\n"
      "ds_read_b128 v[112:115], v22 offset:38912\n"
      "v_mfma_f32_16x16x32_bf16 v[160:163], a[252:255], v[116:119], v[160:163]\n"
      "s_add_u32 s37, s37, 106496\n"
      "ds_read_b128 v[88:91], v22 offset:32768\n"
      "v_mfma_f32_16x16x32_bf16 v[160:163], v[24:27], v[120:123], v[160:163]\n"
      "ds_read_b128 v[96:99], v22 offset:34816\n"
      "v_cvt_pk_bf16_f32 v226, v156, v157\n"
      "v_mfma_f32_16x16x32_bf16 v[160:163], v[28:31], v[124:127], v[160:163]\n"
      "ds_read_b128 v[76:79], v22 offset:29696\n"
      "v_mfma_f32_16x16x32_bf16 v[160:163], v[32:35], v[128:131], v[160:163]\n"
      "v_mfma_f32_16x16x32_bf16 v[160:163], v[36:39], v[132:135], v[160:163]\n"
      "buffer_store_dwordx4 v[224:227], v0, s[8:11], s37 offen sc1\n"
      "v_mfma_f32_16x16x32_bf16 v[160:163], v[40:43], v[136:139], v[160:163]\n"
      "ds_read_b128 v[116:119], v22 offset:39936\n"
      "v_mfma_f32_16x16x32_bf16 v[160:163], v[44:47], v[140:143], v[160:163]\n"
      "ds_read_b128 v[84:87], v22 offset:31744\n"
      "v_mfma_f32_16x16x32_bf16 v[160:163], v[48:51], v[144:147], v[160:163]\n"
      "s_waitcnt lgkmcnt(4)\n"
      "ds_read_b128 v[104:107], v22 offset:36864\n"
      "v_mfma_f32_16x16x32_bf16 v[160:163], v[52:55], v[148:151], v[160:163]\n"
      "s_sub_u32 s59, s59, 1\n"
      "s_cbranch_scc1 .Ld_out2\n"
      ".Ld_t3:\n"
      "v_mfma_f32_16x16x32_bf16 v[152:155], a[0:3], v[56:59], 0\n"
      "ds_read_b128 v[120:123], v22 offset:40960\n"
      "v_mfma_f32_16x16x32_bf16 v[152:155], a[4:7], v[60:63], v[152:155]\n"
      "ds_read_b128 v[108:111], v22 offset:37888\n"
      "v_mfma_f32_16x16x32_bf16 v[152:155], a[8:11], v[64:67], v[152:155]\n"
      "ds_read_b128 v[132:135], v22 offset:44032\n"
      "v_mfma_f32_16x16x32_bf16 v[152:155], a[12:15], v[68:71], v[152:155]\n"
      "ds_read_b128 v[136:139], v22 offset:45056\n"
      "v_cvt_pk_bf16_f32 v229, v162, v163\n"
      "v_mfma_f32_16x16x32_bf16 v[152:155], a[16:19], v[72:75], v[152:155]\n"
      "s_waitcnt lgkmcnt(5)\n"
      "v_mfma_f32_16x16x32_bf16 v[152:155], a[20:23], v[76:79], v[152:155]\n"
      "ds_read_b128 v[124:127], v22 offset:41984\n"
      "v_mfma_f32_16x16x32_bf16 v[152:155], a[24:27], v[80:83], v[152:155]\n"
      "s_add_u32 m0, s43, 122880\n"
      "v_cvt_pk_bf16_f32 v228, v160, v161\n"
      "v_mfma_f32_16x16x32_bf16 v[152:155], a[28:31], v[84:87], v[152:155]\n"
      "buffer_store_dwordx2 v[228:229], v13, s[8:11], s37 offen sc1\n"
      "v_mfma_f32_16x16x32_bf16 v[152:155], a[32:35], v[88:91], v[152:155]\n"
      "v_mfma_f32_16x16x32_bf16 v[152:155], a[36:39], v[92:95], v[152:155]\n"
      "ds_read_b128 v[128:131], v22 offset:43008\n"
      "v_mfma_f32_16x16x32_bf16 v[152:155], a[40:43], v[96:99], v[152:155]\n"
      "ds_read_b128 v[144:147], v22 offset:47104\n"
      "v_mfma_f32_16x16x32_bf16 v[152:155], a[44:47], v[100:103], v[152:155]\n"
      "s_waitcnt lgkmcnt(3)\n"
      "ds_read_b128 v[148:151], v22 offset:48128\n"
      "v_mfma_f32_16x16x32_bf16 v[152:155], a[48:51], v[104:107], v[152:155]\n"
      "ds_read_b128 v[140:143], v22 offset:46080\n"
      "v_mfma_f32_16x16x32_bf16 v[152:155], a[52:55], v[108:111], v[152:155]\n"
      "v_mfma_f32_16x16x32_bf16 v[152:155], a[56:59], v[112:115], v[152:155]\n"
      "s_waitcnt vmcnt(8) lgkmcnt(4)\n"
      "v_cmp_ne_u32 vcc, s21, v20\n"
      "v_mfma_f32_16x16x32_bf16 v[152:155], a[60:63], v[116:119], v[152:155]\n"
      "v_mfma_f32_16x16x32_bf16 v[152:155], a[64:67], v[120:123], v[152:155]\n"
      "s_add_u32 s36, s36, 165888\n"
      "s_add_u32 s52, s52, 106496\n"
      "v_mfma_f32_16x16x32_bf16 v[152:155], a[68:71], v[124:127], v[152:155]\n"
      "s_waitcnt lgkmcnt(2)\n"
      "v_mfma_f32_16x16x32_bf16 v[152:155], a[72:75], v[128:131], v[152:155]\n"
      "s_cbranch_vccnz .Lsp_d5_10\n"
      ".Lbk_d5_10:\n"
      "v_mfma_f32_16x16x32_bf16 v[152:155], a[76:79], v[132:135], v[152:155]\n"
      "buffer_load_dwordx4 v0, s[8:11], s51 offen offset:1024 sc1 lds\n"
      "v_mfma_f32_16x16x32_bf16 v[152:155], a[80:83], v[136:139], v[152:155]\n"
      "s_waitcnt lgkmcnt(0)\n"
      "v_mfma_f32_16x16x32_bf16 v[152:155], a[84:87], v[140:143], v[152:155]\n"
      "v_add_u32 v14, 1, v14\n"
      "v_mfma_f32_16x16x32_bf16 v[152:155], a[88:91], v[144:147], v[152:155]\n"
      "v_mfma_f32_16x16x32_bf16 v[152:155], a[92:95], v[148:151], v[152:155]\n"
      "buffer_load_dwordx4 v0, s[8:11], s51 offen offset:0 sc1 lds\n"
      "v_mfma_f32_16x16x32_bf16 v[156:159], a[96:99], v[56:59], 0\n"
      "v_mfma_f32_16x16x32_bf16 v[156:159], a[100:103], v[60:63], v[156:159]\n"
      "v_mfma_f32_16x16x32_bf16 v[156:159], a[104:107], v[64:67], v[156:159]\n"
      "v_mfma_f32_16x16x32_bf16 v[156:159], a[108:111], v[68:71], v[156:159]\n"
      "buffer_load_dwordx4 v0, s[8:11], s51 offen offset:2048 sc1 lds\n"
      "v_mfma_f32_16x16x32_bf16 v[156:159], a[112:115], v[72:75], v[156:159]\n"
      "s_waitcnt vmcnt(7)\n"
      "ds_write_b32 v16, v14\n"
      "v_mfma_f32_16x16x32_bf16 v[156:159], a[116:119], v[76:79], v[156:159]\n"
      "v_mfma_f32_16x16x32_bf16 v[156:159], a[120:123], v[80:83], v[156:159]\n"
      "v_mfma_f32_16x16x32_bf16 v[156:159], a[124:127], v[84:87], v[156:159]\n"
      "buffer_load_dwordx4 v0, s[8:11], s51 offen offset:3072 sc1 lds\n"
      "v_mfma_f32_16x16x32_bf16 v[156:159], a[128:131], v[88:91], v[156:159]\n"
      "v_mfma_f32_16x16x32_bf16 v[156:159], a[132:135], v[92:95], v[156:159]\n"
      "v_cvt_pk_bf16_f32 v224, v152, v153\n"
      "v_mfma_f32_16x16x32_bf16 v[156:159], a[136:139], v[96:99], v[156:159]\n"
      "v_mfma_f32_16x16x32_bf16 v[156:159], a[140:143], v[100:103], v[156:159]\n"
      "s_min_u32 s36, s36, s6\n"
      "s_add_u32 m0, s43, 126976\n"
      "v_mfma_f32_16x16x32_bf16 v[156:159], a[144:147], v[104:107], v[156:159]\n"
      "buffer_load_dwordx4 v1, s[8:11], s51 offen offset:1024 sc1 lds\n"
      "v_mfma_f32_16x16x32_bf16 v[156:159], a[148:151], v[108:111], v[156:159]\n"
      "ds_read_b32 v15, v17\n"
      "v_mfma_f32_16x16x32_bf16 v[156:159], a[152:155], v[112:115], v[156:159]\n"
      "v_mfma_f32_16x16x32_bf16 v[156:159], a[156:159], v[116:119], v[156:159]\n"
      "v_mfma_f32_16x16x32_bf16 v[156:159], a[160:163], v[120:123], v[156:159]\n"
      "buffer_load_dword v20, v2, s[8:11], s36 offen sc1\n"
      "v_mfma_f32_16x16x32_bf16 v[156:159], a[164:167], v[124:127], v[156:159]\n"
      "v_mfma_f32_16x16x32_bf16 v[156:159], a[168:171], v[128:131], v[156:159]\n"
      "s_waitcnt lgkmcnt(0)\n"
      "v_cmp_gt_i32 vcc, v14, v15\n"
      "v_mfma_f32_16x16x32_bf16 v[156:159], a[172:175], v[132:135], v[156:159]\n"
      "buffer_load_dwordx4 v1, s[8:11], s51 offen offset:0 sc1 lds\n"
      "v_mfma_f32_16x16x32_bf16 v[156:159], a[176:179], v[136:139], v[156:159]\n"
      "s_add_u32 s55, s53, 6656\n"
      "v_mfma_f32_16x16x32_bf16 v[156:159], a[180:183], v[140:143], v[156:159]\n"
      "s_add_u32 s54, s53, 13312\n"
      "v_mfma_f32_16x16x32_bf16 v[156:159], a[184:187], v[144:147], v[156:159]\n"
      "s_waitcnt vmcnt(9)\n"
      "s_cbranch_vccnz .Lsp_dy4_11\n"
      ".Lbk_dy4_11:\n"
      "v_cmp_ne_u32 vcc, s21, v21\n"
      "v_mfma_f32_16x16x32_bf16 v[156:159], a[188:191], v[148:151], v[156:159]\n"
      ".Ld_p1:\n"
      "s_cbranch_execz .Ld_pr\n"
      "v_mfma_f32_16x16x32_bf16 v[160:163], a[192:195], v[56:59], 0\n"
      "v_mfma_f32_16x16x32_bf16 v[160:163], a[196:199], v[60:63], v[160:163]\n"
      "s_cbranch_vccnz .Lsp_df_12\n"
      ".Lbk_df_12:\n"
      "buffer_load_dwordx4 v[176:179], v0, s[8:11], s53 offen sc1\n"
      "v_mfma_f32_16x16x32_bf16 v[160:163], a[200:203], v[64:67], v[160:163]\n"
      "ds_read_b128 v[60:63], v23 offset:1024\n"
      "v_mfma_f32_16x16x32_bf16 v[160:163], a[204:207], v[68:71], v[160:163]\n"
      "ds_read_b128 v[64:67], v23 offset:2048\n"
      "v_mfma_f32_16x16x32_bf16 v[160:163], a[208:211], v[72:75], v[160:163]\n"
      "buffer_load_dwordx2 v[180:181], v13, s[8:11], s53 offen sc1\n"
      "v_mfma_f32_16x16x32_bf16 v[160:163], a[212:215], v[76:79], v[160:163]\n"
      "ds_read_b128 v[72:75], v23 offset:4096\n"
      "v_mfma_f32_16x16x32_bf16 v[160:163], a[216:219], v[80:83], v[160:163]\n"
      "ds_read_b128 v[68:71], v23 offset:3072\n"
      "s_add_u32 s58, s41, 4294967264\n"
      "v_mfma_f32_16x16x32_bf16 v[160:163], a[220:223], v[84:87], v[160:163]\n"
      "ds_read_b128 v[80:83], v23 offset:6144\n"
      "v_mfma_f32_16x16x32_bf16 v[160:163], a[224:227], v[88:91], v[160:163]\n"
      "v_cvt_pk_bf16_f32 v225, v154, v155\n"
      "v_mfma_f32_16x16x32_bf16 v[160:163], a[228:231], v[92:95], v[160:163]\n"
      "buffer_load_dwordx4 v[184:187], v0, s[8:11], s55 offen sc1\n"
      "v_mfma_f32_16x16x32_bf16 v[160:163], a[232:235], v[96:99], v[160:163]\n"
      "ds_read_b128 v[84:87], v23 offset:7168\n"
      "v_cvt_pk_bf16_f32 v226, v156, v157\n"
      "v_mfma_f32_16x16x32_bf16 v[160:163], a[236:239], v[100:103], v[160:163]\n"
      "ds_read_b128 v[76:79], v23 offset:5120\n"
      "v_mfma_f32_16x16x32_bf16 v[160:163], a[240:243], v[104:107], v[160:163]\n"
      "s_add_u32 s37, s37, 106496\n"
      "ds_read_b128 v[56:59], v23 offset:0\n"
      "v_mfma_f32_16x16x32_bf16 v[160:163], a[244:247], v[108:111], v[160:163]\n"
      "s_waitcnt vmcnt(10) lgkmcnt(4)\n"
      "buffer_store_dword v18, v4, s[8:11], s52 offen sc1\n"
      "v_mfma_f32_16x16x32_bf16 v[160:163], a[248:251], v[112:115], v[160:163]\n"
      "ds_read_b128 v[88:91], v23 offset:8192\n"
      "v_mfma_f32_16x16x32_bf16 v[160:163], a[252:255], v[116:119], v[160:163]\n"
      "ds_read_b128 v[96:99], v23 offset:10240\n"
      "v_mfma_f32_16x16x32_bf16 v[160:163], v[24:27], v[120:123], v[160:163]\n"
      "buffer_load_dwordx4 v[192:195], v0, s[8:11], s54 offen sc1\n"
      "v_mfma_f32_16x16x32_bf16 v[160:163], v[28:31], v[124:127], v[160:163]\n"
      "v_mfma_f32_16x16x32_bf16 v[160:163], v[32:35], v[128:131], v[160:163]\n"
      "buffer_load_dwordx2 v[188:189], v13, s[8:11], s55 offen sc1\n"
      "v_mfma_f32_16x16x32_bf16 v[160:163], v[36:39], v[132:135], v[160:163]\n"
      "ds_read_b128 v[100:103], v23 offset:11264\n"
      "v_mfma_f32_16x16x32_bf16 v[160:163], v[40:43], v[136:139], v[160:163]\n"
      "s_add_u32 s55, s53, 19968\n"
      "ds_read_b128 v[132:135], v23 offset:19456\n"
      "v_mfma_f32_16x16x32_bf16 v[160:163], v[44:47], v[140:143], v[160:163]\n"
      "ds_read_b128 v[104:107], v23 offset:12288\n"
      "v_mfma_f32_16x16x32_bf16 v[160:163], v[48:51], v[144:147], v[160:163]\n"
      "s_waitcnt lgkmcnt(3)\n"
      "buffer_load_dwordx2 v[196:197], v13, s[8:11], s54 offen sc1\n"
      "v_mfma_f32_16x16x32_bf16 v[160:163], v[52:55], v[148:151], v[160:163]\n"
      "s_sub_u32 s59, s59, 1\n"
      "s_cbranch_scc1 .Ld_out3\n"
      ".Ld_t4:\n"
      "v_mfma_f32_16x16x32_bf16 v[152:155], a[0:3], v[56:59], 0\n"
      "ds_read_b128 v[92:95], v23 offset:9216\n"
      "v_mfma_f32_16x16x32_bf16 v[152:155], a[4:7], v[60:63], v[152:155]\n"
      "ds_read_b128 v[108:111], v23 offset:13312\n"
      "s_add_u32 m0, s43, 0\n"
      "v_mfma_f32_16x16x32_bf16 v[152:155], a[8:11], v[64:67], v[152:155]\n"
      "ds_read_b128 v[128:131], v23 offset:18432\n"
      "v_cvt_pk_bf16_f32 v228, v160, v161\n"
      "v_mfma_f32_16x16x32_bf16 v[152:155], a[12:15], v[68:71], v[152:155]\n"
      "buffer_load_dwordx4 v[200:203], v0, s[8:11], s55 offen sc1\n"
      "v_mfma_f32_16x16x32_bf16 v[152:155], a[16:19], v[72:75], v[152:155]\n"
      "ds_read_b128 v[116:119], v23 offset:15360\n"
      "v_cvt_pk_bf16_f32 v227, v158, v159\n"
      "v_mfma_f32_16x16x32_bf16 v[152:155], a[20:23], v[76:79], v[152:155]\n"
      "ds_read_b128 v[112:115], v23 offset:14336\n"
      "v_mfma_f32_16x16x32_bf16 v[152:155], a[24:27], v[80:83], v[152:155]\n"
      "s_mov_b32 s51, s36\n"
      "ds_read_b128 v[120:123], v23 offset:16384\n"
      "v_mfma_f32_16x16x32_bf16 v[152:155], a[28:31], v[84:87], v[152:155]\n"
      "buffer_store_dwordx4 v[224:227], v0, s[8:11], s37 offen sc1\n"
      "v_mfma_f32_16x16x32_bf16 v[152:155], a[32:35], v[88:91], v[152:155]\n"
      "s_waitcnt lgkmcnt(4)\n"
      "ds_read_b128 v[140:143], v23 offset:21504\n"
      "v_mfma_f32_16x16x32_bf16 v[152:155], a[36:39], v[92:95], v[152:155]\n"
      "ds_read_b128 v[124:127], v23 offset:17408\n"
      "v_mfma_f32_16x16x32_bf16 v[152:155], a[40:43], v[96:99], v[152:155]\n"
      "ds_read_b128 v[136:139], v23 offset:20480\n"
      "v_mfma_f32_16x16x32_bf16 v[152:155], a[44:47], v[100:103], v[152:155]\n"
      "v_cvt_pk_bf16_f32 v229, v162, v163\n"
      "v_mfma_f32_16x16x32_bf16 v[152:155], a[48:51], v[104:107], v[152:155]\n"
      "buffer_store_dwordx2 v[228:229], v13, s[8:11], s37 offen sc1\n"
      "v_mfma_f32_16x16x32_bf16 v[152:155], a[52:55], v[108:111], v[152:155]\n"
      "s_waitcnt lgkmcnt(3)\n"
      "ds_read_b128 v[148:151], v23 offset:23552\n"
      "v_add_u32_e64 v14, 1, v14\n"
      "v_mfma_f32_16x16x32_bf16 v[152:155], a[56:59], v[112:115], v[152:155]\n"
      "s_add_u32 s36, s36, 165888\n"
      "s_add_u32 s37, s37, 106496\n"
      "ds_read_b128 v[144:147], v23 offset:22528\n"
      "v_mfma_f32_16x16x32_bf16 v[152:155], a[60:63], v[116:119], v[152:155]\n"
      "v_mfma_f32_16x16x32_bf16 v[152:155], a[64:67], v[120:123], v[152:155]\n"
      "s_waitcnt lgkmcnt(3)\n"
      "s_min_u32 s36, s36, s6\n"
      "v_mfma_f32_16x16x32_bf16 v[152:155], a[68:71], v[124:127], v[152:155]\n"
      "buffer_load_dwordx2 v[204:205], v13, s[8:11], s55 offen sc1\n"
      "v_mfma_f32_16x16x32_bf16 v[152:155], a[72:75], v[128:131], v[152:155]\n"
      "s_cmp_lt_u32 s41, 32\n"
      "s_cselect_b32 s56, s39, 0x40000000\n"
      "v_mfma_f32_16x16x32_bf16 v[152:155], a[76:79], v[132:135], v[152:155]\n"
      "s_waitcnt lgkmcnt(2)\n"
      "s_add_u32 s54, s53, 26624\n"
      "v_mfma_f32_16x16x32_bf16 v[152:155], a[80:83], v[136:139], v[152:155]\n"
      "s_waitcnt vmcnt(12) lgkmcnt(1)\n"
      "s_add_u32 s53, s53, 599040\n"
      "v_cmp_ne_u32 vcc, s21, v20\n"
      "v_mfma_f32_16x16x32_bf16 v[152:155], a[84:87], v[140:143], v[152:155]\n"
      "s_waitcnt lgkmcnt(0)\n"
      "buffer_load_dwordx2 v[212:213], v13, s[8:11], s54 offen sc1\n"
      "v_mfma_f32_16x16x32_bf16 v[152:155], a[88:91], v[144:147], v[152:155]\n"
      "v_mfma_f32_16x16x32_bf16 v[152:155], a[92:95], v[148:151], v[152:155]\n"
      "s_add_u32 s39, s39, 147456\n"
      "v_mfma_f32_16x16x32_bf16 v[156:159], a[96:99], v[56:59], 0\n"
      "s_waitcnt vmcnt(12)\n"
      "ds_write_b32 v16, v14\n"
      "v_mfma_f32_16x16x32_bf16 v[156:159], a[100:103], v[60:63], v[156:159]\n"
      "buffer_load_dwordx4 v[208:211], v0, s[8:11], s54 offen sc1\n"
      "v_mfma_f32_16x16x32_bf16 v[156:159], a[104:107], v[64:67], v[156:159]\n"
      "s_cmp_lt_u32 s58, 32\n"
      "s_cselect_b32 s57, s40, 0x40000000\n"
      "v_mfma_f32_16x16x32_bf16 v[156:159], a[108:111], v[68:71], v[156:159]\n"
      "s_cbranch_vccnz .Lsp_d0_13\n"
      ".Lbk_d0_13:\n"
      "v_cvt_pk_bf16_f32 v224, v152, v153\n"
      "v_mfma_f32_16x16x32_bf16 v[156:159], a[112:115], v[72:75], v[156:159]\n"
      "v_cvt_pk_bf16_f32 v225, v154, v155\n"
      "v_mfma_f32_16x16x32_bf16 v[156:159], a[116:119], v[76:79], v[156:159]\n"
      "buffer_load_dwordx4 v0, s[8:11], s51 offen offset:0 sc1 lds\n"
      "v_mfma_f32_16x16x32_bf16 v[156:159], a[120:123], v[80:83], v[156:159]\n"
      "s_waitcnt vmcnt(13)\n"
      "v_lshlrev_b32 v234, 16, v178\n"
      "v_lshlrev_b32 v233, 16, v177\n"
      "v_mfma_f32_16x16x32_bf16 v[156:159], a[124:127], v[84:87], v[156:159]\n"
      "v_lshlrev_b32 v232, 16, v176\n"
      "v_add_f32 v166, v166, v233\n"
      "v_mfma_f32_16x16x32_bf16 v[156:159], a[128:131], v[88:91], v[156:159]\n"
      "s_add_u32 s52, s52, 106496\n"
      "v_add_f32 v164, v164, v232\n"
      "v_mfma_f32_16x16x32_bf16 v[156:159], a[132:135], v[92:95], v[156:159]\n"
      "buffer_load_dwordx4 v0, s[8:11], s51 offen offset:1024 sc1 lds\n"
      "v_mfma_f32_16x16x32_bf16 v[156:159], a[136:139], v[96:99], v[156:159]\n"
      "s_waitcnt vmcnt(13) lgkmcnt(0)\n"
      "v_lshlrev_b32 v233, 16, v181\n"
      "v_lshlrev_b32 v235, 16, v179\n"
      "v_mfma_f32_16x16x32_bf16 v[156:159], a[140:143], v[100:103], v[156:159]\n"
      "v_and_b32 v176, 0xffff0000, v176\n"
      "v_lshlrev_b32 v232, 16, v180\n"
      "v_mfma_f32_16x16x32_bf16 v[156:159], a[144:147], v[104:107], v[156:159]\n"
      "v_add_f32 v172, v172, v232\n"
      "v_and_b32 v181, 0xffff0000, v181\n"
      "v_mfma_f32_16x16x32_bf16 v[156:159], a[148:151], v[108:111], v[156:159]\n"
      "v_add_f32 v168, v168, v234\n"
      "v_add_f32 v174, v174, v233\n"
      "v_mfma_f32_16x16x32_bf16 v[156:159], a[152:155], v[112:115], v[156:159]\n"
      "buffer_load_dword v20, v2, s[8:11], s36 offen sc1\n"
      "v_mfma_f32_16x16x32_bf16 v[156:159], a[156:159], v[116:119], v[156:159]\n"
      "ds_read_b32 v15, v17\n"
      "v_add_f32 v165, v165, v176\n"
      "v_mfma_f32_16x16x32_bf16 v[156:159], a[160:163], v[120:123], v[156:159]\n"
      "buffer_load_dwordx4 v0, s[8:11], s51 offen offset:2048 sc1 lds\n"
      "v_mfma_f32_16x16x32_bf16 v[156:159], a[164:167], v[124:127], v[156:159]\n"
      "s_waitcnt vmcnt(14)\n"
      "v_lshlrev_b32 v232, 16, v184\n"
      "v_and_b32 v177, 0xffff0000, v177\n"
      "v_mfma_f32_16x16x32_bf16 v[156:159], a[168:171], v[128:131], v[156:159]\n"
      "s_add_u32 s40, s40, 4294856704\n"
      "v_add_f32 v167, v167, v177\n"
      "v_and_b32 v180, 0xffff0000, v180\n"
      "v_mfma_f32_16x16x32_bf16 v[156:159], a[172:175], v[132:135], v[156:159]\n"
      "v_add_f32 v164, v164, v232\n"
      "v_lshlrev_b32 v233, 16, v185\n"
      "v_mfma_f32_16x16x32_bf16 v[156:159], a[176:179], v[136:139], v[156:159]\n"
      "buffer_load_dwordx4 v0, s[8:11], s51 offen offset:3072 sc1 lds\n"
      "v_mfma_f32_16x16x32_bf16 v[156:159], a[180:183], v[140:143], v[156:159]\n"
      "s_waitcnt lgkmcnt(0)\n"
      "v_add_f32 v166, v166, v233\n"
      "v_cmp_gt_i32 vcc, v14, v15\n"
      "v_mfma_f32_16x16x32_bf16 v[156:159], a[184:187], v[144:147], v[156:159]\n"
      "s_waitcnt vmcnt(12)\n"
      "v_lshlrev_b32 v232, 16, v188\n"
      "v_add_f32 v172, v172, v232\n"
      "v_mfma_f32_16x16x32_bf16 v[156:159], a[188:191], v[148:151], v[156:159]\n"
      "v_and_b32 v185, 0xffff0000, v185\n"
      "v_lshlrev_b32 v232, 16, v192\n"
      "v_mfma_f32_16x16x32_bf16 v[160:163], a[192:195], v[56:59], 0\n"
      "s_add_u32 m0, s43, 4096\n"
      "v_lshlrev_b32 v234, 16, v186\n"
      "v_add_f32 v164, v164, v232\n"
      "v_mfma_f32_16x16x32_bf16 v[160:163], a[196:199], v[60:63], v[160:163]\n"
      "buffer_load_dwordx4 v1, s[8:11], s51 offen offset:1024 sc1 lds\n"
      "v_mfma_f32_16x16x32_bf16 v[160:163], a[200:203], v[64:67], v[160:163]\n"
      "s_cbranch_vccnz .Lsp_dy5_14\n"
      ".Lbk_dy5_14:\n"
      "v_cvt_pk_bf16_f32 v227, v158, v159\n"
      "v_mfma_f32_16x16x32_bf16 v[160:163], a[204:207], v[68:71], v[160:163]\n"
      "ds_read_b128 v[56:59], v23 offset:24576\n"
      "v_lshlrev_b32 v233, 16, v189\n"
      "v_mfma_f32_16x16x32_bf16 v[160:163], a[208:211], v[72:75], v[160:163]\n"
      "s_waitcnt vmcnt(12)\n"
      "ds_read_b128 v[64:67], v23 offset:26624\n"
      "v_lshlrev_b32 v232, 16, v196\n"
      "v_mfma_f32_16x16x32_bf16 v[160:163], a[212:215], v[76:79], v[160:163]\n"
      "buffer_load_dwordx4 v1, s[8:11], s51 offen offset:0 sc1 lds\n"
      "v_mfma_f32_16x16x32_bf16 v[160:163], a[216:219], v[80:83], v[160:163]\n"
      "ds_read_b128 v[76:79], v23 offset:29696\n"
      "v_add_f32 v174, v174, v233\n"
      "v_mfma_f32_16x16x32_bf16 v[160:163], a[220:223], v[84:87], v[160:163]\n"
      "v_and_b32 v184, 0xffff0000, v184\n"
      "v_add_f32 v172, v172, v232\n"
      "v_mfma_f32_16x16x32_bf16 v[160:163], a[224:227], v[88:91], v[160:163]\n"
      "v_add_f32 v170, v170, v235\n"
      "v_cvt_pk_bf16_f32 v226, v156, v157\n"
      "v_mfma_f32_16x16x32_bf16 v[160:163], a[228:231], v[92:95], v[160:163]\n"
      "s_add_u32 s41, s41, 6\n"
      "ds_read_b128 v[88:91], v23 offset:32768\n"
      "v_add_f32 v165, v165, v184\n"
      "v_mfma_f32_16x16x32_bf16 v[160:163], a[232:235], v[96:99], v[160:163]\n"
      "ds_read_b128 v[68:71], v23 offset:27648\n"
      "v_mfma_f32_16x16x32_bf16 v[160:163], a[236:239], v[100:103], v[160:163]\n"
      "ds_read_b128 v[96:99], v23 offset:34816\n"
      "v_add_f32 v168, v168, v234\n"
      "v_mfma_f32_16x16x32_bf16 v[160:163], a[240:243], v[104:107], v[160:163]\n"
      "ds_read_b128 v[80:83], v23 offset:30720\n"
      "v_lshlrev_b32 v233, 16, v193\n"
      "v_mfma_f32_16x16x32_bf16 v[160:163], a[244:247], v[108:111], v[160:163]\n"
      "ds_read_b128 v[72:75], v23 offset:28672\n"
      "v_and_b32 v178, 0xffff0000, v178\n"
      "v_mfma_f32_16x16x32_bf16 v[160:163], a[248:251], v[112:115], v[160:163]\n"
      "s_waitcnt vmcnt(12) lgkmcnt(5)\n"
      "s_mov_b32 s51, s36\n"
      "ds_read_b128 v[112:115], v23 offset:38912\n"
      "v_lshlrev_b32 v232, 16, v200\n"
      "v_mfma_f32_16x16x32_bf16 v[160:163], a[252:255], v[116:119], v[160:163]\n"
      "v_add_f32 v164, v164, v232\n"
      "v_and_b32 v186, 0xffff0000, v186\n"
      "v_mfma_f32_16x16x32_bf16 v[160:163], v[24:27], v[120:123], v[160:163]\n"
      "s_waitcnt vmcnt(10) lgkmcnt(5)\n"
      "buffer_store_dword v18, v4, s[8:11], s52 offen sc1\n"
      "v_mfma_f32_16x16x32_bf16 v[160:163], v[28:31], v[124:127], v[160:163]\n"
      "ds_read_b128 v[60:63], v23 offset:25600\n"
      "v_add_f32 v169, v169, v178\n"
      "v_mfma_f32_16x16x32_bf16 v[160:163], v[32:35], v[128:131], v[160:163]\n"
      "buffer_store_dwordx4 v[224:227], v0, s[8:11], s37 offen sc1\n"
      "v_mfma_f32_16x16x32_bf16 v[160:163], v[36:39], v[132:135], v[160:163]\n"
      "ds_read_b128 v[104:107], v23 offset:36864\n"
      "v_and_b32 v200, 0xffff0000, v200\n"
      "v_mfma_f32_16x16x32_bf16 v[160:163], v[40:43], v[136:139], v[160:163]\n"
      "ds_read_b128 v[84:87], v23 offset:31744\n"
      "v_add_f32 v173, v173, v180\n"
      "v_mfma_f32_16x16x32_bf16 v[160:163], v[44:47], v[140:143], v[160:163]\n"
      "ds_read_b128 v[140:143], v23 offset:46080\n"
      "v_add_f32 v166, v166, v233\n"
      "v_mfma_f32_16x16x32_bf16 v[160:163], v[48:51], v[144:147], v[160:163]\n"
      "ds_read_b128 v[100:103], v23 offset:35840\n"
      "v_lshlrev_b32_e64 v234, 16, v194\n"
      "v_mfma_f32_16x16x32_bf16 v[160:163], v[52:55], v[148:151], v[160:163]\n"
      "s_sub_u32 s59, s59, 1\n"
      "s_cbranch_scc1 .Ld_out4\n"
      ".Ld_t5:\n"
      "v_mfma_f32_16x16x32_bf16 v[152:155], a[0:3], v[56:59], 0\n"
      "s_waitcnt lgkmcnt(4)\n"
      "ds_read_b128 v[92:95], v23 offset:33792\n"
      "ds_read_b128 v[116:119], v23 offset:39936\n"
      "v_mfma_f32_16x16x32_bf16 v[152:155], a[4:7], v[60:63], v[152:155]\n"
      "ds_read_b128 v[108:111], v23 offset:37888\n"
      "v_and_b32 v192, 0xffff0000, v192\n"
      "v_mfma_f32_16x16x32_bf16 v[152:155], a[8:11], v[64:67], v[152:155]\n"
      "ds_read_b128 v[128:131], v23 offset:43008\n"
      "v_cvt_pk_bf16_f32 v229, v162, v163\n"
      "v_mfma_f32_16x16x32_bf16 v[152:155], a[12:15], v[68:71], v[152:155]\n"
      "ds_read_b128 v[136:139], v23 offset:45056\n"
      "v_add_f32 v175, v175, v181\n"
      "v_mfma_f32_16x16x32_bf16 v[152:155], a[16:19], v[72:75], v[152:155]\n"
      "v_cvt_pk_bf16_f32 v228, v160, v161\n"
      "v_and_b32 v189, 0xffff0000, v189\n"
      "v_mfma_f32_16x16x32_bf16 v[152:155], a[20:23], v[76:79], v[152:155]\n"
      "s_waitcnt vmcnt(11) lgkmcnt(5)\n"
      "ds_read_b128 v[144:147], v23 offset:47104\n"
      "v_lshlrev_b32 v232, 16, v204\n"
      "v_mfma_f32_16x16x32_bf16 v[152:155], a[24:27], v[80:83], v[152:155]\n"
      "buffer_store_dwordx2 v[228:229], v13, s[8:11], s37 offen sc1\n"
      "v_mfma_f32_16x16x32_bf16 v[152:155], a[28:31], v[84:87], v[152:155]\n"
      "v_lshlrev_b32 v235, 16, v187\n"
      "v_add_f32 v172, v172, v232\n"
      "v_mfma_f32_16x16x32_bf16 v[152:155], a[32:35], v[88:91], v[152:155]\n"
      "s_waitcnt vmcnt(10) lgkmcnt(3)\n"
      "ds_read_b128 v[120:123], v23 offset:40960\n"
      "v_lshlrev_b32 v232, 16, v208\n"
      "v_mfma_f32_16x16x32_bf16 v[152:155], a[36:39], v[92:95], v[152:155]\n"
      "ds_read_b128 v[124:127], v23 offset:41984\n"
      "v_add_f32 v170, v170, v235\n"
      "v_mfma_f32_16x16x32_bf16 v[152:155], a[40:43], v[96:99], v[152:155]\n"
      "s_add_u32 m0, s43, 24576\n"
      "v_and_b32 v194, 0xffff0000, v194\n"
      "v_and_b32 v208, 0xffff0000, v208\n"
      "v_mfma_f32_16x16x32_bf16 v[152:155], a[44:47], v[100:103], v[152:155]\n"
      "ds_read_b128 v[132:135], v23 offset:44032\n"
      "v_add_f32 v167, v167, v185\n"
      "v_mfma_f32_16x16x32_bf16 v[152:155], a[48:51], v[104:107], v[152:155]\n"
      "v_lshlrev_b32 v233, 16, v197\n"
      "v_add_u32 v14, 1, v14\n"
      "v_mfma_f32_16x16x32_bf16 v[152:155], a[52:55], v[108:111], v[152:155]\n"
      "s_add_u32 s36, s36, 165888\n"
      "v_lshlrev_b32 v235, 16, v195\n"
      "v_add_f32 v168, v168, v234\n"
      "v_mfma_f32_16x16x32_bf16 v[152:155], a[56:59], v[112:115], v[152:155]\n"
      "buffer_load_dwordx4 v[216:219], v6, s[8:11], s38 offen sc1\n"
      "v_mfma_f32_16x16x32_bf16 v[152:155], a[60:63], v[116:119], v[152:155]\n"
      "s_waitcnt lgkmcnt(2)\n"
      "ds_read_b128 v[148:151], v23 offset:48128\n"
      "v_add_f32 v164, v164, v232\n"
      "v_mfma_f32_16x16x32_bf16 v[152:155], a[64:67], v[120:123], v[152:155]\n"
      "s_waitcnt lgkmcnt(2)\n"
      "v_add_f32 v165, v165, v192\n"
      "v_lshlrev_b32 v234, 16, v202\n"
      "v_mfma_f32_16x16x32_bf16 v[152:155], a[68:71], v[124:127], v[152:155]\n"
      "v_add_f32 v174, v174, v233\n"
      "v_lshlrev_b32 v232, 16, v212\n"
      "v_mfma_f32_16x16x32_bf16 v[152:155], a[72:75], v[128:131], v[152:155]\n"
      "s_waitcnt lgkmcnt(1)\n"
      "v_and_b32 v179, 0xffff0000, v179\n"
      "v_add_f32 v169, v169, v186\n"
      "v_mfma_f32_16x16x32_bf16 v[152:155], a[76:79], v[132:135], v[152:155]\n"
      "s_waitcnt vmcnt(8)\n"
      "v_lshlrev_b32 v233, 16, v201\n"
      "v_cmp_ne_u32 vcc, s21, v20\n"
      "v_mfma_f32_16x16x32_bf16 v[152:155], a[80:83], v[136:139], v[152:155]\n"
      "buffer_load_dwordx2 v[220:221], v7, s[8:11], s38 offen sc1\n"
      "v_mfma_f32_16x16x32_bf16 v[152:155], a[84:87], v[140:143], v[152:155]\n"
      "v_add_f32 v166, v166, v233\n"
      "v_and_b32 v193, 0xffff0000, v193\n"
      "v_mfma_f32_16x16x32_bf16 v[152:155], a[88:91], v[144:147], v[152:155]\n"
      "s_waitcnt lgkmcnt(0)\n"
      "v_and_b32 v197, 0xffff0000, v197\n"
      "v_lshlrev_b32 v233, 16, v205\n"
      "v_mfma_f32_16x16x32_bf16 v[152:155], a[92:95], v[148:151], v[152:155]\n"
      "v_add_f32 v170, v170, v235\n"
      "v_add_f32 v174, v174, v233\n"
      "v_mfma_f32_16x16x32_bf16 v[156:159], a[96:99], v[56:59], 0\n"
      "s_cbranch_vccnz .Lsp_d1_15\n"
      ".Lbk_d1_15:\n"
      "v_lshlrev_b32 v235, 16, v203\n"
      "v_mfma_f32_16x16x32_bf16 v[156:159], a[100:103], v[60:63], v[156:159]\n"
      "buffer_load_dwordx4 v0, s[8:11], s51 offen offset:0 sc1 lds\n"
      "v_mfma_f32_16x16x32_bf16 v[156:159], a[104:107], v[64:67], v[156:159]\n"
      "v_and_b32 v187, 0xffff0000, v187\n"
      "v_lshlrev_b32 v233, 16, v209\n"
      "v_mfma_f32_16x16x32_bf16 v[156:159], a[108:111], v[68:71], v[156:159]\n"
      "v_add_f32 v170, v170, v235\n"
      "v_add_f32 v166, v166, v233\n"
      "v_mfma_f32_16x16x32_bf16 v[156:159], a[112:115], v[72:75], v[156:159]\n"
      "v_and_b32 v201, 0xffff0000, v201\n"
      "v_add_f32 v165, v165, v200\n"
      "v_mfma_f32_16x16x32_bf16 v[156:159], a[116:119], v[76:79], v[156:159]\n"
      "v_and_b32 v188, 0xffff0000, v188\n"
      "v_add_f32 v165, v165, v208\n"
      "v_mfma_f32_16x16x32_bf16 v[156:159], a[120:123], v[80:83], v[156:159]\n"
      "buffer_load_dwordx4 v0, s[8:11], s51 offen offset:1024 sc1 lds\n"
      "v_mfma_f32_16x16x32_bf16 v[156:159], a[124:127], v[84:87], v[156:159]\n"
      "v_add_f32 v168, v168, v234\n"
      "v_mul_f32 v237, v164, v164\n"
      "v_mfma_f32_16x16x32_bf16 v[156:159], a[128:131], v[88:91], v[156:159]\n"
      "v_add_f32 v167, v167, v193\n"
      "v_lshlrev_b32 v233, 16, v213\n"
      "v_mfma_f32_16x16x32_bf16 v[156:159], a[132:135], v[92:95], v[156:159]\n"
      "s_waitcnt vmcnt(7)\n"
      "ds_write_b32 v16, v14\n"
      "v_mfma_f32_16x16x32_bf16 v[156:159], a[136:139], v[96:99], v[156:159]\n"
      "buffer_load_dwordx4 v0, s[8:11], s51 offen offset:2048 sc1 lds\n"
      "v_mfma_f32_16x16x32_bf16 v[156:159], a[140:143], v[100:103], v[156:159]\n"
      "v_add_f32 v171, v171, v179\n"
      "v_add_f32 v167, v167, v201\n"
      "v_mfma_f32_16x16x32_bf16 v[156:159], a[144:147], v[104:107], v[156:159]\n"
      "v_lshlrev_b32 v234, 16, v210\n"
      "v_and_b32 v213, 0xffff0000, v213\n"
      "v_mfma_f32_16x16x32_bf16 v[156:159], a[148:151], v[108:111], v[156:159]\n"
      "s_min_u32 s36, s36, s6\n"
      "v_add_f32 v173, v173, v188\n"
      "v_and_b32 v209, 0xffff0000, v209\n"
      "v_mfma_f32_16x16x32_bf16 v[156:159], a[152:155], v[112:115], v[156:159]\n"
      "buffer_load_dword v20, v2, s[8:11], s36 offen sc1\n"
      "v_mfma_f32_16x16x32_bf16 v[156:159], a[156:159], v[116:119], v[156:159]\n"
      "v_add_f32 v236, v164, v165\n"
      "v_add_f32 v175, v175, v189\n"
      "v_mfma_f32_16x16x32_bf16 v[156:159], a[160:163], v[120:123], v[156:159]\n"
      "buffer_load_dwordx4 v0, s[8:11], s51 offen offset:3072 sc1 lds\n"
      "v_mfma_f32_16x16x32_bf16 v[156:159], a[164:167], v[124:127], v[156:159]\n"
      "v_add_f32 v167, v167, v209\n"
      "v_and_b32 v203, 0xffff0000, v203\n"
      "v_mfma_f32_16x16x32_bf16 v[156:159], a[168:171], v[128:131], v[156:159]\n"
      "ds_read_b32 v15, v17\n"
      "v_fmac_f32 v237, v165, v165\n"
      "v_mfma_f32_16x16x32_bf16 v[156:159], a[172:175], v[132:135], v[156:159]\n"
      "s_add_u32 m0, s43, 28672\n"
      "v_add_f32 v175, v175, v197\n"
      "v_and_b32 v204, 0xffff0000, v204\n"
      "v_mfma_f32_16x16x32_bf16 v[156:159], a[176:179], v[136:139], v[156:159]\n"
      "v_and_b32 v195, 0xffff0000, v195\n"
      "v_add_f32 v172, v172, v232\n"
      "v_mfma_f32_16x16x32_bf16 v[156:159], a[180:183], v[140:143], v[156:159]\n"
      "buffer_load_dwordx4 v1, s[8:11], s51 offen offset:0 sc1 lds\n"
      "v_mfma_f32_16x16x32_bf16 v[156:159], a[184:187], v[144:147], v[156:159]\n"
      "v_fmac_f32 v237, v166, v166\n"
      "v_cvt_pk_bf16_f32 v225, v154, v155\n"
      "v_mfma_f32_16x16x32_bf16 v[156:159], a[188:191], v[148:151], v[156:159]\n"
      "s_waitcnt lgkmcnt(0)\n"
      "v_add_f32 v169, v169, v194\n"
      "v_cmp_gt_i32 vcc, v14, v15\n"
      "v_mfma_f32_16x16x32_bf16 v[160:163], a[192:195], v[56:59], 0\n"
      "v_and_b32 v196, 0xffff0000, v196\n"
      "v_and_b32 v212, 0xffff0000, v212\n"
      "v_mfma_f32_16x16x32_bf16 v[160:163], a[196:199], v[60:63], v[160:163]\n"
      "buffer_load_dwordx4 v1, s[8:11], s51 offen offset:1024 sc1 lds\n"
      "v_mfma_f32_16x16x32_bf16 v[160:163], a[200:203], v[64:67], v[160:163]\n"
      "s_cbranch_vccnz .Lsp_dy0_16\n"
      ".Lbk_dy0_16:\n"
      "v_mfma_f32_16x16x32_bf16 v[160:163], a[204:207], v[68:71], v[160:163]\n"
      "ds_read_b128 v[56:59], v0 offset:0\n"
      "v_cvt_pk_bf16_f32 v177, v166, v167\n"
      "v_mfma_f32_16x16x32_bf16 v[160:163], a[208:211], v[72:75], v[160:163]\n"
      "v_add_f32 v171, v171, v187\n"
      "v_and_b32 v205, 0xffff0000, v205\n"
      "v_mfma_f32_16x16x32_bf16 v[160:163], a[212:215], v[76:79], v[160:163]\n"
      "ds_read_b128 v[60:63], v0 offset:1024\n"
      "v_cvt_pk_bf16_f32 v224, v152, v153\n"
      "v_mfma_f32_16x16x32_bf16 v[160:163], a[216:219], v[80:83], v[160:163]\n"
      "s_add_u32 s52, s52, 106496\n"
      "v_and_b32 v202, 0xffff0000, v202\n"
      "v_add_f32 v169, v169, v202\n"
      "v_mfma_f32_16x16x32_bf16 v[160:163], a[220:223], v[84:87], v[160:163]\n"
      "ds_read_b128 v[84:87], v0 offset:7168\n"
      "v_add_f32 v236, v236, v166\n"
      "v_mfma_f32_16x16x32_bf16 v[160:163], a[224:227], v[88:91], v[160:163]\n"
      "ds_read_b128 v[72:75], v0 offset:4096\n"
      "v_and_b32 v210, 0xffff0000, v210\n"
      "v_mfma_f32_16x16x32_bf16 v[160:163], a[228:231], v[92:95], v[160:163]\n"
      "v_add_f32 v173, v173, v196\n"
      "v_lshlrev_b32 v235, 16, v211\n"
      "v_mfma_f32_16x16x32_bf16 v[160:163], a[232:235], v[96:99], v[160:163]\n"
      "v_add_f32 v170, v170, v235\n"
      "v_add_f32 v171, v171, v195\n"
      "v_mfma_f32_16x16x32_bf16 v[160:163], a[236:239], v[100:103], v[160:163]\n"
      "s_add_u32 s37, s37, 106496\n"
      "ds_read_b128 v[88:91], v0 offset:8192\n"
      "v_cvt_pk_bf16_f32 v227, v158, v159\n"
      "v_mfma_f32_16x16x32_bf16 v[160:163], a[240:243], v[104:107], v[160:163]\n"
      "s_waitcnt vmcnt(9) lgkmcnt(3)\n"
      "buffer_store_dword v18, v4, s[8:11], s52 offen sc1\n"
      "v_mfma_f32_16x16x32_bf16 v[160:163], a[244:247], v[108:111], v[160:163]\n"
      "ds_read_b128 v[68:71], v0 offset:3072\n"
      "v_add_f32 v174, v174, v233\n"
      "v_mfma_f32_16x16x32_bf16 v[160:163], a[248:251], v[112:115], v[160:163]\n"
      "ds_read_b128 v[104:107], v0 offset:12288\n"
      "v_add_f32 v169, v169, v210\n"
      "v_mfma_f32_16x16x32_bf16 v[160:163], a[252:255], v[116:119], v[160:163]\n"
      "ds_read_b128 v[96:99], v0 offset:10240\n"
      "v_fmac_f32 v237, v167, v167\n"
      "v_mfma_f32_16x16x32_bf16 v[160:163], v[24:27], v[120:123], v[160:163]\n"
      "ds_read_b128 v[80:83], v0 offset:6144\n"
      "v_add_f32 v175, v175, v205\n"
      "v_mfma_f32_16x16x32_bf16 v[160:163], v[28:31], v[124:127], v[160:163]\n"
      "ds_read_b128 v[64:67], v0 offset:2048\n"
      "v_add_f32 v168, v168, v234\n"
      "v_mfma_f32_16x16x32_bf16 v[160:163], v[32:35], v[128:131], v[160:163]\n"
      "v_add_f32 v236, v236, v167\n"
      "v_cvt_pk_bf16_f32 v226, v156, v157\n"
      "v_mfma_f32_16x16x32_bf16 v[160:163], v[36:39], v[132:135], v[160:163]\n"
      "ds_read_b128 v[92:95], v0 offset:9216\n"
      "v_cvt_pk_bf16_f32 v176, v164, v165\n"
      "v_mfma_f32_16x16x32_bf16 v[160:163], v[40:43], v[136:139], v[160:163]\n"
      "buffer_store_dwordx4 v[224:227], v0, s[8:11], s37 offen sc1\n"
      "ds_read_b128 v[76:79], v0 offset:5120\n"
      "v_mfma_f32_16x16x32_bf16 v[160:163], v[44:47], v[140:143], v[160:163]\n"
      "ds_read_b128 v[132:135], v0 offset:19456\n"
      "v_and_b32 v211, 0xffff0000, v211\n"
      "v_mfma_f32_16x16x32_bf16 v[160:163], v[48:51], v[144:147], v[160:163]\n"
      "ds_read_b128 v[100:103], v0 offset:11264\n"
      "v_add_f32 v171, v171, v203\n"
      "v_mfma_f32_16x16x32_bf16 v[160:163], v[52:55], v[148:151], v[160:163]\n"
      "s_sub_u32 s59, s59, 1\n"
      "s_cbranch_scc0 .Ld_t0\n"
      ".Ld_out5:\n"
      "s_nop 8\n"
      "v_cvt_pk_bf16_f32 v228, v160, v161\n"
      "v_cvt_pk_bf16_f32 v229, v162, v163\n"
      "buffer_store_dwordx2 v[228:229], v13, s[8:11], s37 offen sc1\n"
      "v_cvt_pk_bf16_f32 v178, v168, v169\n"
      "v_add_f32 v175, v175, v213\n"
      "v_add_f32 v171, v171, v211\n"
      "v_add_f32 v173, v173, v204\n"
      "v_cvt_pk_bf16_f32 v179, v170, v171\n"
      "v_add_f32 v173, v173, v212\n"
      "v_cvt_pk_bf16_f32 v180, v172, v173\n"
      "v_cvt_pk_bf16_f32 v181, v174, v175\n"
      "buffer_store_dwordx2 v[180:181], v9, s[16:19], s57 offen\n"
      "buffer_store_dwordx4 v[176:179], v8, s[16:19], s57 offen\n"
      "s_add_u32 s52, s52, 106496\n"
      "s_add_u32 s37, s37, 173056\n"
      "s_waitcnt vmcnt(2)\n"
      "buffer_store_dword v18, v4, s[8:11], s52 offen sc1\n"
      "s_add_u32 s52, s52, 173056\n"
      "s_branch .Ld_end\n"
      ".Ld_out0:\n"
      "s_nop 8\n"
      "v_add_f32 v174, v174, v162\n"
      "v_add_f32 v170, v170, v158\n"
      "v_lshlrev_b32 v172, 16, v220\n"
      "s_add_u32 s38, s38, 147456\n"
      "v_add_f32 v171, v171, v159\n"
      "v_add_f32 v169, v169, v157\n"
      "v_add_f32 v172, v172, v160\n"
      "v_add_f32 v175, v175, v163\n"
      "v_and_b32 v173, 0xffff0000, v220\n"
      "v_add_f32 v173, v173, v161\n"
      "s_add_u32 s52, s52, 173056\n"
      "buffer_load_dword v21, v3, s[8:11], s53 offen sc1\n"
      "s_add_u32 s55, s53, 6656\n"
      "s_add_u32 s54, s53, 13312\n"
      "s_waitcnt vmcnt(0)\n"
      "v_cmp_ne_u32 vcc, s21, v21\n"
      "s_cbranch_vccnz .Lsp_df_17\n"
      ".Lbk_df_17:\n"
      "buffer_load_dwordx4 v[176:179], v0, s[8:11], s53 offen sc1\n"
      "buffer_load_dwordx2 v[180:181], v13, s[8:11], s53 offen sc1\n"
      "s_add_u32 s58, s41, 4294967264\n"
      "buffer_load_dwordx4 v[184:187], v0, s[8:11], s55 offen sc1\n"
      "buffer_load_dwordx4 v[192:195], v0, s[8:11], s54 offen sc1\n"
      "buffer_load_dwordx2 v[188:189], v13, s[8:11], s55 offen sc1\n"
      "s_add_u32 s55, s53, 19968\n"
      "buffer_load_dwordx2 v[196:197], v13, s[8:11], s54 offen sc1\n"
      "buffer_load_dwordx4 v[200:203], v0, s[8:11], s55 offen sc1\n"
      "buffer_load_dwordx2 v[204:205], v13, s[8:11], s55 offen sc1\n"
      "s_cmp_lt_u32 s41, 32\n"
      "s_cselect_b32 s56, s39, 0x40000000\n"
      "s_add_u32 s54, s53, 26624\n"
      "s_add_u32 s53, s53, 599040\n"
      "buffer_load_dwordx2 v[212:213], v13, s[8:11], s54 offen sc1\n"
      "s_add_u32 s39, s39, 147456\n"
      "buffer_load_dwordx4 v[208:211], v0, s[8:11], s54 offen sc1\n"
      "s_cmp_lt_u32 s58, 32\n"
      "s_cselect_b32 s57, s40, 0x40000000\n"
      "s_waitcnt vmcnt(9)\n"
      "v_lshlrev_b32 v234, 16, v178\n"
      "v_lshlrev_b32 v233, 16, v177\n"
      "v_lshlrev_b32 v232, 16, v176\n"
      "v_add_f32 v166, v166, v233\n"
      "v_add_f32 v164, v164, v232\n"
      "s_waitcnt vmcnt(8)\n"
      "v_lshlrev_b32 v233, 16, v181\n"
      "v_lshlrev_b32 v235, 16, v179\n"
      "v_and_b32 v176, 0xffff0000, v176\n"
      "v_lshlrev_b32 v232, 16, v180\n"
      "v_add_f32 v172, v172, v232\n"
      "v_and_b32 v181, 0xffff0000, v181\n"
      "v_add_f32 v168, v168, v234\n"
      "v_add_f32 v174, v174, v233\n"
      "v_add_f32 v165, v165, v176\n"
      "s_waitcnt vmcnt(7)\n"
      "v_lshlrev_b32 v232, 16, v184\n"
      "v_and_b32 v177, 0xffff0000, v177\n"
      "s_add_u32 s40, s40, 4294856704\n"
      "v_add_f32 v167, v167, v177\n"
      "v_and_b32 v180, 0xffff0000, v180\n"
      "v_add_f32 v164, v164, v232\n"
      "v_lshlrev_b32 v233, 16, v185\n"
      "v_add_f32 v166, v166, v233\n"
      "s_waitcnt vmcnt(5)\n"
      "v_lshlrev_b32 v232, 16, v188\n"
      "v_add_f32 v172, v172, v232\n"
      "v_and_b32 v185, 0xffff0000, v185\n"
      "v_lshlrev_b32 v232, 16, v192\n"
      "v_lshlrev_b32 v234, 16, v186\n"
      "v_add_f32 v164, v164, v232\n"
      "v_lshlrev_b32 v233, 16, v189\n"
      "s_waitcnt vmcnt(4)\n"
      "v_lshlrev_b32 v232, 16, v196\n"
      "v_add_f32 v174, v174, v233\n"
      "v_and_b32 v184, 0xffff0000, v184\n"
      "v_add_f32 v172, v172, v232\n"
      "v_add_f32 v170, v170, v235\n"
      "s_add_u32 s41, s41, 6\n"
      "v_add_f32 v165, v165, v184\n"
      "v_add_f32 v168, v168, v234\n"
      "v_lshlrev_b32 v233, 16, v193\n"
      "v_and_b32 v178, 0xffff0000, v178\n"
      "s_waitcnt vmcnt(3)\n"
      "v_lshlrev_b32 v232, 16, v200\n"
      "v_add_f32 v164, v164, v232\n"
      "v_and_b32 v186, 0xffff0000, v186\n"
      "v_add_f32 v169, v169, v178\n"
      "v_and_b32 v200, 0xffff0000, v200\n"
      "v_add_f32 v173, v173, v180\n"
      "v_add_f32 v166, v166, v233\n"
      "v_lshlrev_b32 v234, 16, v194\n"
      "v_and_b32 v192, 0xffff0000, v192\n"
      "v_add_f32 v175, v175, v181\n"
      "v_and_b32 v189, 0xffff0000, v189\n"
      "s_waitcnt vmcnt(2)\n"
      "v_lshlrev_b32 v232, 16, v204\n"
      "v_lshlrev_b32 v235, 16, v187\n"
      "v_add_f32 v172, v172, v232\n"
      "s_waitcnt vmcnt(0)\n"
      "v_lshlrev_b32 v232, 16, v208\n"
      "v_add_f32 v170, v170, v235\n"
      "v_and_b32 v194, 0xffff0000, v194\n"
      "v_and_b32 v208, 0xffff0000, v208\n"
      "v_add_f32 v167, v167, v185\n"
      "v_lshlrev_b32 v233, 16, v197\n"
      "v_lshlrev_b32 v235, 16, v195\n"
      "v_add_f32 v168, v168, v234\n"
      "v_add_f32 v164, v164, v232\n"
      "v_add_f32 v165, v165, v192\n"
      "v_lshlrev_b32 v234, 16, v202\n"
      "v_add_f32 v174, v174, v233\n"
      "v_lshlrev_b32 v232, 16, v212\n"
      "v_and_b32 v179, 0xffff0000, v179\n"
      "v_add_f32 v169, v169, v186\n"
      "v_lshlrev_b32 v233, 16, v201\n"
      "v_add_f32 v166, v166, v233\n"
      "v_and_b32 v193, 0xffff0000, v193\n"
      "v_and_b32 v197, 0xffff0000, v197\n"
      "v_lshlrev_b32 v233, 16, v205\n"
      "v_add_f32 v170, v170, v235\n"
      "v_add_f32 v174, v174, v233\n"
      "v_lshlrev_b32 v235, 16, v203\n"
      "v_and_b32 v187, 0xffff0000, v187\n"
      "v_lshlrev_b32 v233, 16, v209\n"
      "v_add_f32 v170, v170, v235\n"
      "v_add_f32 v166, v166, v233\n"
      "v_and_b32 v201, 0xffff0000, v201\n"
      "v_add_f32 v165, v165, v200\n"
      "v_and_b32 v188, 0xffff0000, v188\n"
      "v_add_f32 v165, v165, v208\n"
      "v_add_f32 v168, v168, v234\n"
      "v_add_f32 v167, v167, v193\n"
      "v_lshlrev_b32 v233, 16, v213\n"
      "v_add_f32 v171, v171, v179\n"
      "v_add_f32 v167, v167, v201\n"
      "v_lshlrev_b32 v234, 16, v210\n"
      "v_and_b32 v213, 0xffff0000, v213\n"
      "v_add_f32 v173, v173, v188\n"
      "v_and_b32 v209, 0xffff0000, v209\n"
      "v_add_f32 v175, v175, v189\n"
      "v_add_f32 v167, v167, v209\n"
      "v_and_b32 v203, 0xffff0000, v203\n"
      "v_add_f32 v175, v175, v197\n"
      "v_and_b32 v204, 0xffff0000, v204\n"
      "v_and_b32 v195, 0xffff0000, v195\n"
      "v_add_f32 v172, v172, v232\n"
      "v_add_f32 v169, v169, v194\n"
      "v_and_b32 v196, 0xffff0000, v196\n"
      "v_and_b32 v212, 0xffff0000, v212\n"
      "v_cvt_pk_bf16_f32 v177, v166, v167\n"
      "v_add_f32 v171, v171, v187\n"
      "v_and_b32 v205, 0xffff0000, v205\n"
      "v_and_b32 v202, 0xffff0000, v202\n"
      "v_add_f32 v169, v169, v202\n"
      "v_and_b32 v210, 0xffff0000, v210\n"
      "v_add_f32 v173, v173, v196\n"
      "v_lshlrev_b32 v235, 16, v211\n"
      "v_add_f32 v170, v170, v235\n"
      "v_add_f32 v171, v171, v195\n"
      "v_add_f32 v174, v174, v233\n"
      "v_add_f32 v169, v169, v210\n"
      "v_add_f32 v175, v175, v205\n"
      "v_add_f32 v168, v168, v234\n"
      "v_cvt_pk_bf16_f32 v176, v164, v165\n"
      "v_and_b32 v211, 0xffff0000, v211\n"
      "v_add_f32 v171, v171, v203\n"
      "v_cvt_pk_bf16_f32 v178, v168, v169\n"
      "v_add_f32 v175, v175, v213\n"
      "v_add_f32 v171, v171, v211\n"
      "v_add_f32 v173, v173, v204\n"
      "v_cvt_pk_bf16_f32 v179, v170, v171\n"
      "v_add_f32 v173, v173, v212\n"
      "v_cvt_pk_bf16_f32 v180, v172, v173\n"
      "v_cvt_pk_bf16_f32 v181, v174, v175\n"
      "buffer_store_dwordx2 v[180:181], v9, s[16:19], s57 offen\n"
      "buffer_store_dwordx4 v[176:179], v8, s[16:19], s57 offen\n"
      "s_branch .Ld_end\n"
      ".Ld_out1:\n"
      "s_nop 8\n"
      "v_cvt_pk_bf16_f32 v229, v162, v163\n"
      "v_cvt_pk_bf16_f32 v228, v160, v161\n"
      "buffer_store_dwordx2 v[228:229], v13, s[8:11], s37 offen sc1\n"
      "s_waitcnt vmcnt(0)\n"
      "buffer_store_dword v18, v4, s[8:11], s52 offen sc1\n"
      "buffer_load_dword v21, v3, s[8:11], s53 offen sc1\n"
      "s_add_u32 s37, s37, 106496\n"
      "s_add_u32 s52, s52, 106496\n"
      "s_add_u32 s55, s53, 6656\n"
      "s_add_u32 s54, s53, 13312\n"
      "s_waitcnt vmcnt(0)\n"
      "v_cmp_ne_u32 vcc, s21, v21\n"
      "s_cbranch_vccnz .Lsp_df_18\n"
      ".Lbk_df_18:\n"
      "buffer_load_dwordx4 v[176:179], v0, s[8:11], s53 offen sc1\n"
      "buffer_load_dwordx2 v[180:181], v13, s[8:11], s53 offen sc1\n"
      "s_add_u32 s58, s41, 4294967264\n"
      "buffer_load_dwordx4 v[184:187], v0, s[8:11], s55 offen sc1\n"
      "buffer_load_dwordx4 v[192:195], v0, s[8:11], s54 offen sc1\n"
      "buffer_load_dwordx2 v[188:189], v13, s[8:11], s55 offen sc1\n"
      "s_add_u32 s55, s53, 19968\n"
      "buffer_load_dwordx2 v[196:197], v13, s[8:11], s54 offen sc1\n"
      "buffer_load_dwordx4 v[200:203], v0, s[8:11], s55 offen sc1\n"
      "buffer_load_dwordx2 v[204:205], v13, s[8:11], s55 offen sc1\n"
      "s_cmp_lt_u32 s41, 32\n"
      "s_cselect_b32 s56, s39, 0x40000000\n"
      "s_add_u32 s54, s53, 26624\n"
      "s_add_u32 s53, s53, 599040\n"
      "buffer_load_dwordx2 v[212:213], v13, s[8:11], s54 offen sc1\n"
      "s_add_u32 s39, s39, 147456\n"
      "buffer_load_dwordx4 v[208:211], v0, s[8:11], s54 offen sc1\n"
      "s_cmp_lt_u32 s58, 32\n"
      "s_cselect_b32 s57, s40, 0x40000000\n"
      "s_waitcnt vmcnt(9)\n"
      "v_lshlrev_b32 v234, 16, v178\n"
      "v_lshlrev_b32 v233, 16, v177\n"
      "v_lshlrev_b32 v232, 16, v176\n"
      "v_add_f32 v166, v166, v233\n"
      "v_add_f32 v164, v164, v232\n"
      "s_waitcnt vmcnt(8)\n"
      "v_lshlrev_b32 v233, 16, v181\n"
      "v_lshlrev_b32 v235, 16, v179\n"
      "v_and_b32 v176, 0xffff0000, v176\n"
      "v_lshlrev_b32 v232, 16, v180\n"
      "v_add_f32 v172, v172, v232\n"
      "v_and_b32 v181, 0xffff0000, v181\n"
      "v_add_f32 v168, v168, v234\n"
      "v_add_f32 v174, v174, v233\n"
      "v_add_f32 v165, v165, v176\n"
      "s_waitcnt vmcnt(7)\n"
      "v_lshlrev_b32 v232, 16, v184\n"
      "v_and_b32 v177, 0xffff0000, v177\n"
      "s_add_u32 s40, s40, 4294856704\n"
      "v_add_f32 v167, v167, v177\n"
      "v_and_b32 v180, 0xffff0000, v180\n"
      "v_add_f32 v164, v164, v232\n"
      "v_lshlrev_b32 v233, 16, v185\n"
      "v_add_f32 v166, v166, v233\n"
      "s_waitcnt vmcnt(5)\n"
      "v_lshlrev_b32 v232, 16, v188\n"
      "v_add_f32 v172, v172, v232\n"
      "v_and_b32 v185, 0xffff0000, v185\n"
      "v_lshlrev_b32 v232, 16, v192\n"
      "v_lshlrev_b32 v234, 16, v186\n"
      "v_add_f32 v164, v164, v232\n"
      "v_lshlrev_b32 v233, 16, v189\n"
      "s_waitcnt vmcnt(4)\n"
      "v_lshlrev_b32 v232, 16, v196\n"
      "v_add_f32 v174, v174, v233\n"
      "v_and_b32 v184, 0xffff0000, v184\n"
      "v_add_f32 v172, v172, v232\n"
      "v_add_f32 v170, v170, v235\n"
      "s_add_u32 s41, s41, 6\n"
      "v_add_f32 v165, v165, v184\n"
      "v_add_f32 v168, v168, v234\n"
      "v_lshlrev_b32 v233, 16, v193\n"
      "v_and_b32 v178, 0xffff0000, v178\n"
      "s_waitcnt vmcnt(3)\n"
      "v_lshlrev_b32 v232, 16, v200\n"
      "v_add_f32 v164, v164, v232\n"
      "v_and_b32 v186, 0xffff0000, v186\n"
      "v_add_f32 v169, v169, v178\n"
      "v_and_b32 v200, 0xffff0000, v200\n"
      "v_add_f32 v173, v173, v180\n"
      "v_add_f32 v166, v166, v233\n"
      "v_lshlrev_b32 v234, 16, v194\n"
      "v_and_b32 v192, 0xffff0000, v192\n"
      "v_add_f32 v175, v175, v181\n"
      "v_and_b32 v189, 0xffff0000, v189\n"
      "s_waitcnt vmcnt(2)\n"
      "v_lshlrev_b32 v232, 16, v204\n"
      "v_lshlrev_b32 v235, 16, v187\n"
      "v_add_f32 v172, v172, v232\n"
      "s_waitcnt vmcnt(0)\n"
      "v_lshlrev_b32 v232, 16, v208\n"
      "v_add_f32 v170, v170, v235\n"
      "v_and_b32 v194, 0xffff0000, v194\n"
      "v_and_b32 v208, 0xffff0000, v208\n"
      "v_add_f32 v167, v167, v185\n"
      "v_lshlrev_b32 v233, 16, v197\n"
      "v_lshlrev_b32 v235, 16, v195\n"
      "v_add_f32 v168, v168, v234\n"
      "v_add_f32 v164, v164, v232\n"
      "v_add_f32 v165, v165, v192\n"
      "v_lshlrev_b32 v234, 16, v202\n"
      "v_add_f32 v174, v174, v233\n"
      "v_lshlrev_b32 v232, 16, v212\n"
      "v_and_b32 v179, 0xffff0000, v179\n"
      "v_add_f32 v169, v169, v186\n"
      "v_lshlrev_b32 v233, 16, v201\n"
      "v_add_f32 v166, v166, v233\n"
      "v_and_b32 v193, 0xffff0000, v193\n"
      "v_and_b32 v197, 0xffff0000, v197\n"
      "v_lshlrev_b32 v233, 16, v205\n"
      "v_add_f32 v170, v170, v235\n"
      "v_add_f32 v174, v174, v233\n"
      "v_lshlrev_b32 v235, 16, v203\n"
      "v_and_b32 v187, 0xffff0000, v187\n"
      "v_lshlrev_b32 v233, 16, v209\n"
      "v_add_f32 v170, v170, v235\n"
      "v_add_f32 v166, v166, v233\n"
      "v_and_b32 v201, 0xffff0000, v201\n"
      "v_add_f32 v165, v165, v200\n"
      "v_and_b32 v188, 0xffff0000, v188\n"
      "v_add_f32 v165, v165, v208\n"
      "v_add_f32 v168, v168, v234\n"
      "v_add_f32 v167, v167, v193\n"
      "v_lshlrev_b32 v233, 16, v213\n"
      "v_add_f32 v171, v171, v179\n"
      "v_add_f32 v167, v167, v201\n"
      "v_lshlrev_b32 v234, 16, v210\n"
      "v_and_b32 v213, 0xffff0000, v213\n"
      "v_add_f32 v173, v173, v188\n"
      "v_and_b32 v209, 0xffff0000, v209\n"
      "v_add_f32 v175, v175, v189\n"
      "v_add_f32 v167, v167, v209\n"
      "v_and_b32 v203, 0xffff0000, v203\n"
      "v_add_f32 v175, v175, v197\n"
      "v_and_b32 v204, 0xffff0000, v204\n"
      "v_and_b32 v195, 0xffff0000, v195\n"
      "v_add_f32 v172, v172, v232\n"
      "v_add_f32 v169, v169, v194\n"
      "v_and_b32 v196, 0xffff0000, v196\n"
      "v_and_b32 v212, 0xffff0000, v212\n"
      "v_cvt_pk_bf16_f32 v177, v166, v167\n"
      "v_add_f32 v171, v171, v187\n"
      "v_and_b32 v205, 0xffff0000, v205\n"
      "v_and_b32 v202, 0xffff0000, v202\n"
      "v_add_f32 v169, v169, v202\n"
      "v_and_b32 v210, 0xffff0000, v210\n"
      "v_add_f32 v173, v173, v196\n"
      "v_lshlrev_b32 v235, 16, v211\n"
      "v_add_f32 v170, v170, v235\n"
      "v_add_f32 v171, v171, v195\n"
      "v_add_f32 v174, v174, v233\n"
      "v_add_f32 v169, v169, v210\n"
      "v_add_f32 v175, v175, v205\n"
      "v_add_f32 v168, v168, v234\n"
      "v_cvt_pk_bf16_f32 v176, v164, v165\n"
      "v_and_b32 v211, 0xffff0000, v211\n"
      "v_add_f32 v171, v171, v203\n"
      "v_cvt_pk_bf16_f32 v178, v168, v169\n"
      "v_add_f32 v175, v175, v213\n"
      "v_add_f32 v171, v171, v211\n"
      "v_add_f32 v173, v173, v204\n"
      "v_cvt_pk_bf16_f32 v179, v170, v171\n"
      "v_add_f32 v173, v173, v212\n"
      "v_cvt_pk_bf16_f32 v180, v172, v173\n"
      "v_cvt_pk_bf16_f32 v181, v174, v175\n"
      "buffer_store_dwordx2 v[180:181], v9, s[16:19], s57 offen\n"
      "buffer_store_dwordx4 v[176:179], v8, s[16:19], s57 offen\n"
      "s_branch .Ld_end\n"
      ".Ld_out2:\n"
      "s_nop 8\n"
      "v_cvt_pk_bf16_f32 v229, v162, v163\n"
      "v_cvt_pk_bf16_f32 v228, v160, v161\n"
      "buffer_store_dwordx2 v[228:229], v13, s[8:11], s37 offen sc1\n"
      "s_add_u32 s52, s52, 106496\n"
      "s_add_u32 s55, s53, 6656\n"
      "s_add_u32 s54, s53, 13312\n"
      "s_waitcnt vmcnt(2)\n"
      "v_cmp_ne_u32 vcc, s21, v21\n"
      "s_cbranch_vccnz .Lsp_df_19\n"
      ".Lbk_df_19:\n"
      "buffer_load_dwordx4 v[176:179], v0, s[8:11], s53 offen sc1\n"
      "buffer_load_dwordx2 v[180:181], v13, s[8:11], s53 offen sc1\n"
      "s_add_u32 s58, s41, 4294967264\n"
      "buffer_load_dwordx4 v[184:187], v0, s[8:11], s55 offen sc1\n"
      "s_add_u32 s37, s37, 106496\n"
      "s_waitcnt vmcnt(3)\n"
      "buffer_store_dword v18, v4, s[8:11], s52 offen sc1\n"
      "buffer_load_dwordx4 v[192:195], v0, s[8:11], s54 offen sc1\n"
      "buffer_load_dwordx2 v[188:189], v13, s[8:11], s55 offen sc1\n"
      "s_add_u32 s55, s53, 19968\n"
      "buffer_load_dwordx2 v[196:197], v13, s[8:11], s54 offen sc1\n"
      "buffer_load_dwordx4 v[200:203], v0, s[8:11], s55 offen sc1\n"
      "buffer_load_dwordx2 v[204:205], v13, s[8:11], s55 offen sc1\n"
      "s_cmp_lt_u32 s41, 32\n"
      "s_cselect_b32 s56, s39, 0x40000000\n"
      "s_add_u32 s54, s53, 26624\n"
      "s_add_u32 s53, s53, 599040\n"
      "buffer_load_dwordx2 v[212:213], v13, s[8:11], s54 offen sc1\n"
      "s_add_u32 s39, s39, 147456\n"
      "buffer_load_dwordx4 v[208:211], v0, s[8:11], s54 offen sc1\n"
      "s_cmp_lt_u32 s58, 32\n"
      "s_cselect_b32 s57, s40, 0x40000000\n"
      "s_waitcnt vmcnt(10)\n"
      "v_lshlrev_b32 v234, 16, v178\n"
      "v_lshlrev_b32 v233, 16, v177\n"
      "v_lshlrev_b32 v232, 16, v176\n"
      "v_add_f32 v166, v166, v233\n"
      "s_add_u32 s52, s52, 106496\n"
      "v_add_f32 v164, v164, v232\n"
      "s_waitcnt vmcnt(9)\n"
      "v_lshlrev_b32 v233, 16, v181\n"
      "v_lshlrev_b32 v235, 16, v179\n"
      "v_and_b32 v176, 0xffff0000, v176\n"
      "v_lshlrev_b32 v232, 16, v180\n"
      "v_add_f32 v172, v172, v232\n"
      "v_and_b32 v181, 0xffff0000, v181\n"
      "v_add_f32 v168, v168, v234\n"
      "v_add_f32 v174, v174, v233\n"
      "v_add_f32 v165, v165, v176\n"
      "s_waitcnt vmcnt(8)\n"
      "v_lshlrev_b32 v232, 16, v184\n"
      "v_and_b32 v177, 0xffff0000, v177\n"
      "s_add_u32 s40, s40, 4294856704\n"
      "v_add_f32 v167, v167, v177\n"
      "v_and_b32 v180, 0xffff0000, v180\n"
      "v_add_f32 v164, v164, v232\n"
      "v_lshlrev_b32 v233, 16, v185\n"
      "v_add_f32 v166, v166, v233\n"
      "s_waitcnt vmcnt(5)\n"
      "v_lshlrev_b32 v232, 16, v188\n"
      "v_add_f32 v172, v172, v232\n"
      "v_and_b32 v185, 0xffff0000, v185\n"
      "v_lshlrev_b32 v232, 16, v192\n"
      "v_lshlrev_b32 v234, 16, v186\n"
      "v_add_f32 v164, v164, v232\n"
      "v_lshlrev_b32 v233, 16, v189\n"
      "s_waitcnt vmcnt(4)\n"
      "v_lshlrev_b32 v232, 16, v196\n"
      "v_add_f32 v174, v174, v233\n"
      "v_and_b32 v184, 0xffff0000, v184\n"
      "v_add_f32 v172, v172, v232\n"
      "v_add_f32 v170, v170, v235\n"
      "s_add_u32 s41, s41, 6\n"
      "v_add_f32 v165, v165, v184\n"
      "v_add_f32 v168, v168, v234\n"
      "v_lshlrev_b32 v233, 16, v193\n"
      "v_and_b32 v178, 0xffff0000, v178\n"
      "s_waitcnt vmcnt(3)\n"
      "v_lshlrev_b32 v232, 16, v200\n"
      "v_add_f32 v164, v164, v232\n"
      "v_and_b32 v186, 0xffff0000, v186\n"
      "v_add_f32 v169, v169, v178\n"
      "v_and_b32 v200, 0xffff0000, v200\n"
      "v_add_f32 v173, v173, v180\n"
      "v_add_f32 v166, v166, v233\n"
      "v_lshlrev_b32 v234, 16, v194\n"
      "v_and_b32 v192, 0xffff0000, v192\n"
      "v_add_f32 v175, v175, v181\n"
      "v_and_b32 v189, 0xffff0000, v189\n"
      "s_waitcnt vmcnt(2)\n"
      "v_lshlrev_b32 v232, 16, v204\n"
      "v_lshlrev_b32 v235, 16, v187\n"
      "v_add_f32 v172, v172, v232\n"
      "s_waitcnt vmcnt(0)\n"
      "v_lshlrev_b32 v232, 16, v208\n"
      "v_add_f32 v170, v170, v235\n"
      "v_and_b32 v194, 0xffff0000, v194\n"
      "v_and_b32 v208, 0xffff0000, v208\n"
      "v_add_f32 v167, v167, v185\n"
      "v_lshlrev_b32 v233, 16, v197\n"
      "v_lshlrev_b32 v235, 16, v195\n"
      "v_add_f32 v168, v168, v234\n"
      "v_add_f32 v164, v164, v232\n"
      "v_add_f32 v165, v165, v192\n"
      "v_lshlrev_b32 v234, 16, v202\n"
      "v_add_f32 v174, v174, v233\n"
      "v_lshlrev_b32 v232, 16, v212\n"
      "v_and_b32 v179, 0xffff0000, v179\n"
      "v_add_f32 v169, v169, v186\n"
      "v_lshlrev_b32 v233, 16, v201\n"
      "v_add_f32 v166, v166, v233\n"
      "v_and_b32 v193, 0xffff0000, v193\n"
      "v_and_b32 v197, 0xffff0000, v197\n"
      "v_lshlrev_b32 v233, 16, v205\n"
      "v_add_f32 v170, v170, v235\n"
      "v_add_f32 v174, v174, v233\n"
      "v_lshlrev_b32 v235, 16, v203\n"
      "v_and_b32 v187, 0xffff0000, v187\n"
      "v_lshlrev_b32 v233, 16, v209\n"
      "v_add_f32 v170, v170, v235\n"
      "v_add_f32 v166, v166, v233\n"
      "v_and_b32 v201, 0xffff0000, v201\n"
      "v_add_f32 v165, v165, v200\n"
      "v_and_b32 v188, 0xffff0000, v188\n"
      "v_add_f32 v165, v165, v208\n"
      "v_add_f32 v168, v168, v234\n"
      "v_add_f32 v167, v167, v193\n"
      "v_lshlrev_b32 v233, 16, v213\n"
      "v_add_f32 v171, v171, v179\n"
      "v_add_f32 v167, v167, v201\n"
      "v_lshlrev_b32 v234, 16, v210\n"
      "v_and_b32 v213, 0xffff0000, v213\n"
      "v_add_f32 v173, v173, v188\n"
      "v_and_b32 v209, 0xffff0000, v209\n"
      "v_add_f32 v175, v175, v189\n"
      "v_add_f32 v167, v167, v209\n"
      "v_and_b32 v203, 0xffff0000, v203\n"
      "v_add_f32 v175, v175, v197\n"
      "v_and_b32 v204, 0xffff0000, v204\n"
      "v_and_b32 v195, 0xffff0000, v195\n"
      "v_add_f32 v172, v172, v232\n"
      "v_add_f32 v169, v169, v194\n"
      "v_and_b32 v196, 0xffff0000, v196\n"
      "v_and_b32 v212, 0xffff0000, v212\n"
      "v_cvt_pk_bf16_f32 v177, v166, v167\n"
      "v_add_f32 v171, v171, v187\n"
      "v_and_b32 v205, 0xffff0000, v205\n"
      "v_and_b32 v202, 0xffff0000, v202\n"
      "v_add_f32 v169, v169, v202\n"
      "v_and_b32 v210, 0xffff0000, v210\n"
      "v_add_f32 v173, v173, v196\n"
      "v_lshlrev_b32 v235, 16, v211\n"
      "v_add_f32 v170, v170, v235\n"
      "v_add_f32 v171, v171, v195\n"
      "v_add_f32 v174, v174, v233\n"
      "v_add_f32 v169, v169, v210\n"
      "v_add_f32 v175, v175, v205\n"
      "v_add_f32 v168, v168, v234\n"
      "v_cvt_pk_bf16_f32 v176, v164, v165\n"
      "v_and_b32 v211, 0xffff0000, v211\n"
      "v_add_f32 v171, v171, v203\n"
      "v_cvt_pk_bf16_f32 v178, v168, v169\n"
      "v_add_f32 v175, v175, v213\n"
      "v_add_f32 v171, v171, v211\n"
      "v_add_f32 v173, v173, v204\n"
      "v_cvt_pk_bf16_f32 v179, v170, v171\n"
      "v_add_f32 v173, v173, v212\n"
      "v_cvt_pk_bf16_f32 v180, v172, v173\n"
      "v_cvt_pk_bf16_f32 v181, v174, v175\n"
      "buffer_store_dwordx2 v[180:181], v9, s[16:19], s57 offen\n"
      "buffer_store_dwordx4 v[176:179], v8, s[16:19], s57 offen\n"
      "s_branch .Ld_end\n"
      ".Ld_out3:\n"
      "s_nop 8\n"
      "v_cvt_pk_bf16_f32 v228, v160, v161\n"
      "buffer_load_dwordx4 v[200:203], v0, s[8:11], s55 offen sc1\n"
      "v_cvt_pk_bf16_f32 v227, v158, v159\n"
      "buffer_store_dwordx4 v[224:227], v0, s[8:11], s37 offen sc1\n"
      "v_cvt_pk_bf16_f32 v229, v162, v163\n"
      "buffer_store_dwordx2 v[228:229], v13, s[8:11], s37 offen sc1\n"
      "s_add_u32 s37, s37, 106496\n"
      "buffer_load_dwordx2 v[204:205], v13, s[8:11], s55 offen sc1\n"
      "s_cmp_lt_u32 s41, 32\n"
      "s_cselect_b32 s56, s39, 0x40000000\n"
      "s_add_u32 s54, s53, 26624\n"
      "s_add_u32 s53, s53, 599040\n"
      "buffer_load_dwordx2 v[212:213], v13, s[8:11], s54 offen sc1\n"
      "s_add_u32 s39, s39, 147456\n"
      "buffer_load_dwordx4 v[208:211], v0, s[8:11], s54 offen sc1\n"
      "s_cmp_lt_u32 s58, 32\n"
      "s_cselect_b32 s57, s40, 0x40000000\n"
      "s_waitcnt vmcnt(12)\n"
      "v_lshlrev_b32 v234, 16, v178\n"
      "v_lshlrev_b32 v233, 16, v177\n"
      "v_lshlrev_b32 v232, 16, v176\n"
      "v_add_f32 v166, v166, v233\n"
      "s_add_u32 s52, s52, 106496\n"
      "v_add_f32 v164, v164, v232\n"
      "s_waitcnt vmcnt(11)\n"
      "v_lshlrev_b32 v233, 16, v181\n"
      "v_lshlrev_b32 v235, 16, v179\n"
      "v_and_b32 v176, 0xffff0000, v176\n"
      "v_lshlrev_b32 v232, 16, v180\n"
      "v_add_f32 v172, v172, v232\n"
      "v_and_b32 v181, 0xffff0000, v181\n"
      "v_add_f32 v168, v168, v234\n"
      "v_add_f32 v174, v174, v233\n"
      "v_add_f32 v165, v165, v176\n"
      "s_waitcnt vmcnt(10)\n"
      "v_lshlrev_b32 v232, 16, v184\n"
      "v_and_b32 v177, 0xffff0000, v177\n"
      "s_add_u32 s40, s40, 4294856704\n"
      "v_add_f32 v167, v167, v177\n"
      "v_and_b32 v180, 0xffff0000, v180\n"
      "v_add_f32 v164, v164, v232\n"
      "v_lshlrev_b32 v233, 16, v185\n"
      "v_add_f32 v166, v166, v233\n"
      "s_waitcnt vmcnt(7)\n"
      "v_lshlrev_b32 v232, 16, v188\n"
      "v_add_f32 v172, v172, v232\n"
      "v_and_b32 v185, 0xffff0000, v185\n"
      "v_lshlrev_b32 v232, 16, v192\n"
      "v_lshlrev_b32 v234, 16, v186\n"
      "v_add_f32 v164, v164, v232\n"
      "v_lshlrev_b32 v233, 16, v189\n"
      "s_waitcnt vmcnt(6)\n"
      "v_lshlrev_b32 v232, 16, v196\n"
      "v_add_f32 v174, v174, v233\n"
      "v_and_b32 v184, 0xffff0000, v184\n"
      "v_add_f32 v172, v172, v232\n"
      "v_add_f32 v170, v170, v235\n"
      "s_add_u32 s41, s41, 6\n"
      "v_add_f32 v165, v165, v184\n"
      "v_add_f32 v168, v168, v234\n"
      "v_lshlrev_b32 v233, 16, v193\n"
      "v_and_b32 v178, 0xffff0000, v178\n"
      "s_waitcnt vmcnt(5)\n"
      "v_lshlrev_b32 v232, 16, v200\n"
      "v_add_f32 v164, v164, v232\n"
      "v_and_b32 v186, 0xffff0000, v186\n"
      "s_waitcnt vmcnt(3)\n"
      "buffer_store_dword v18, v4, s[8:11], s52 offen sc1\n"
      "v_add_f32 v169, v169, v178\n"
      "v_and_b32 v200, 0xffff0000, v200\n"
      "v_add_f32 v173, v173, v180\n"
      "v_add_f32 v166, v166, v233\n"
      "v_lshlrev_b32 v234, 16, v194\n"
      "v_and_b32 v192, 0xffff0000, v192\n"
      "v_add_f32 v175, v175, v181\n"
      "v_and_b32 v189, 0xffff0000, v189\n"
      "s_waitcnt vmcnt(3)\n"
      "v_lshlrev_b32 v232, 16, v204\n"
      "v_lshlrev_b32 v235, 16, v187\n"
      "v_add_f32 v172, v172, v232\n"
      "s_waitcnt vmcnt(1)\n"
      "v_lshlrev_b32 v232, 16, v208\n"
      "v_add_f32 v170, v170, v235\n"
      "v_and_b32 v194, 0xffff0000, v194\n"
      "v_and_b32 v208, 0xffff0000, v208\n"
      "v_add_f32 v167, v167, v185\n"
      "v_lshlrev_b32 v233, 16, v197\n"
      "v_lshlrev_b32 v235, 16, v195\n"
      "v_add_f32 v168, v168, v234\n"
      "v_add_f32 v164, v164, v232\n"
      "v_add_f32 v165, v165, v192\n"
      "v_lshlrev_b32 v234, 16, v202\n"
      "v_add_f32 v174, v174, v233\n"
      "v_lshlrev_b32 v232, 16, v212\n"
      "v_and_b32 v179, 0xffff0000, v179\n"
      "v_add_f32 v169, v169, v186\n"
      "v_lshlrev_b32 v233, 16, v201\n"
      "v_add_f32 v166, v166, v233\n"
      "v_and_b32 v193, 0xffff0000, v193\n"
      "v_and_b32 v197, 0xffff0000, v197\n"
      "v_lshlrev_b32 v233, 16, v205\n"
      "v_add_f32 v170, v170, v235\n"
      "v_add_f32 v174, v174, v233\n"
      "v_lshlrev_b32 v235, 16, v203\n"
      "v_and_b32 v187, 0xffff0000, v187\n"
      "v_lshlrev_b32 v233, 16, v209\n"
      "v_add_f32 v170, v170, v235\n"
      "v_add_f32 v166, v166, v233\n"
      "v_and_b32 v201, 0xffff0000, v201\n"
      "v_add_f32 v165, v165, v200\n"
      "v_and_b32 v188, 0xffff0000, v188\n"
      "v_add_f32 v165, v165, v208\n"
      "v_add_f32 v168, v168, v234\n"
      "v_add_f32 v167, v167, v193\n"
      "v_lshlrev_b32 v233, 16, v213\n"
      "v_add_f32 v171, v171, v179\n"
      "v_add_f32 v167, v167, v201\n"
      "v_lshlrev_b32 v234, 16, v210\n"
      "v_and_b32 v213, 0xffff0000, v213\n"
      "v_add_f32 v173, v173, v188\n"
      "v_and_b32 v209, 0xffff0000, v209\n"
      "v_add_f32 v175, v175, v189\n"
      "v_add_f32 v167, v167, v209\n"
      "v_and_b32 v203, 0xffff0000, v203\n"
      "v_add_f32 v175, v175, v197\n"
      "v_and_b32 v204, 0xffff0000, v204\n"
      "v_and_b32 v195, 0xffff0000, v195\n"
      "v_add_f32 v172, v172, v232\n"
      "v_add_f32 v169, v169, v194\n"
      "v_and_b32 v196, 0xffff0000, v196\n"
      "v_and_b32 v212, 0xffff0000, v212\n"
      "v_cvt_pk_bf16_f32 v177, v166, v167\n"
      "v_add_f32 v171, v171, v187\n"
      "v_and_b32 v205, 0xffff0000, v205\n"
      "s_add_u32 s52, s52, 106496\n"
      "v_and_b32 v202, 0xffff0000, v202\n"
      "v_add_f32 v169, v169, v202\n"
      "v_and_b32 v210, 0xffff0000, v210\n"
      "v_add_f32 v173, v173, v196\n"
      "v_lshlrev_b32 v235, 16, v211\n"
      "v_add_f32 v170, v170, v235\n"
      "v_add_f32 v171, v171, v195\n"
      "v_add_f32 v174, v174, v233\n"
      "v_add_f32 v169, v169, v210\n"
      "v_add_f32 v175, v175, v205\n"
      "v_add_f32 v168, v168, v234\n"
      "v_cvt_pk_bf16_f32 v176, v164, v165\n"
      "v_and_b32 v211, 0xffff0000, v211\n"
      "v_add_f32 v171, v171, v203\n"
      "v_cvt_pk_bf16_f32 v178, v168, v169\n"
      "v_add_f32 v175, v175, v213\n"
      "v_add_f32 v171, v171, v211\n"
      "v_add_f32 v173, v173, v204\n"
      "v_cvt_pk_bf16_f32 v179, v170, v171\n"
      "v_add_f32 v173, v173, v212\n"
      "v_cvt_pk_bf16_f32 v180, v172, v173\n"
      "v_cvt_pk_bf16_f32 v181, v174, v175\n"
      "buffer_store_dwordx2 v[180:181], v9, s[16:19], s57 offen\n"
      "buffer_store_dwordx4 v[176:179], v8, s[16:19], s57 offen\n"
      "s_branch .Ld_end\n"
      ".Ld_out4:\n"
      "v_and_b32 v192, 0xffff0000, v192\n"
      "s_nop 7\n"
      "v_cvt_pk_bf16_f32 v229, v162, v163\n"
      "v_add_f32 v175, v175, v181\n"
      "v_cvt_pk_bf16_f32 v228, v160, v161\n"
      "v_and_b32 v189, 0xffff0000, v189\n"
      "s_waitcnt vmcnt(11)\n"
      "v_lshlrev_b32 v232, 16, v204\n"
      "buffer_store_dwordx2 v[228:229], v13, s[8:11], s37 offen sc1\n"
      "v_lshlrev_b32 v235, 16, v187\n"
      "v_add_f32 v172, v172, v232\n"
      "s_waitcnt vmcnt(10)\n"
      "v_lshlrev_b32 v232, 16, v208\n"
      "v_add_f32 v170, v170, v235\n"
      "v_and_b32 v194, 0xffff0000, v194\n"
      "v_and_b32 v208, 0xffff0000, v208\n"
      "v_add_f32 v167, v167, v185\n"
      "v_lshlrev_b32 v233, 16, v197\n"
      "v_lshlrev_b32 v235, 16, v195\n"
      "v_add_f32 v168, v168, v234\n"
      "v_add_f32 v164, v164, v232\n"
      "v_add_f32 v165, v165, v192\n"
      "v_lshlrev_b32 v234, 16, v202\n"
      "v_add_f32 v174, v174, v233\n"
      "v_lshlrev_b32 v232, 16, v212\n"
      "v_and_b32 v179, 0xffff0000, v179\n"
      "v_add_f32 v169, v169, v186\n"
      "v_lshlrev_b32 v233, 16, v201\n"
      "v_add_f32 v166, v166, v233\n"
      "v_and_b32 v193, 0xffff0000, v193\n"
      "v_and_b32 v197, 0xffff0000, v197\n"
      "v_lshlrev_b32 v233, 16, v205\n"
      "v_add_f32 v170, v170, v235\n"
      "v_add_f32 v174, v174, v233\n"
      "v_lshlrev_b32 v235, 16, v203\n"
      "v_and_b32 v187, 0xffff0000, v187\n"
      "v_lshlrev_b32 v233, 16, v209\n"
      "v_add_f32 v170, v170, v235\n"
      "v_add_f32 v166, v166, v233\n"
      "v_and_b32 v201, 0xffff0000, v201\n"
      "v_add_f32 v165, v165, v200\n"
      "v_and_b32 v188, 0xffff0000, v188\n"
      "v_add_f32 v165, v165, v208\n"
      "v_add_f32 v168, v168, v234\n"
      "v_add_f32 v167, v167, v193\n"
      "v_lshlrev_b32 v233, 16, v213\n"
      "v_add_f32 v171, v171, v179\n"
      "v_add_f32 v167, v167, v201\n"
      "v_lshlrev_b32 v234, 16, v210\n"
      "v_and_b32 v213, 0xffff0000, v213\n"
      "v_add_f32 v173, v173, v188\n"
      "v_and_b32 v209, 0xffff0000, v209\n"
      "v_add_f32 v175, v175, v189\n"
      "v_add_f32 v167, v167, v209\n"
      "v_and_b32 v203, 0xffff0000, v203\n"
      "v_add_f32 v175, v175, v197\n"
      "v_and_b32 v204, 0xffff0000, v204\n"
      "v_and_b32 v195, 0xffff0000, v195\n"
      "v_add_f32 v172, v172, v232\n"
      "v_add_f32 v169, v169, v194\n"
      "v_and_b32 v196, 0xffff0000, v196\n"
      "v_and_b32 v212, 0xffff0000, v212\n"
      "v_cvt_pk_bf16_f32 v177, v166, v167\n"
      "v_add_f32 v171, v171, v187\n"
      "v_and_b32 v205, 0xffff0000, v205\n"
      "s_add_u32 s52, s52, 106496\n"
      "v_and_b32 v202, 0xffff0000, v202\n"
      "v_add_f32 v169, v169, v202\n"
      "v_and_b32 v210, 0xffff0000, v210\n"
      "v_add_f32 v173, v173, v196\n"
      "v_lshlrev_b32 v235, 16, v211\n"
      "v_add_f32 v170, v170, v235\n"
      "v_add_f32 v171, v171, v195\n"
      "s_add_u32 s37, s37, 106496\n"
      "s_waitcnt vmcnt(0)\n"
      "buffer_store_dword v18, v4, s[8:11], s52 offen sc1\n"
      "v_add_f32 v174, v174, v233\n"
      "v_add_f32 v169, v169, v210\n"
      "v_add_f32 v175, v175, v205\n"
      "v_add_f32 v168, v168, v234\n"
      "v_cvt_pk_bf16_f32 v176, v164, v165\n"
      "v_and_b32 v211, 0xffff0000, v211\n"
      "v_add_f32 v171, v171, v203\n"
      "v_cvt_pk_bf16_f32 v178, v168, v169\n"
      "v_add_f32 v175, v175, v213\n"
      "v_add_f32 v171, v171, v211\n"
      "v_add_f32 v173, v173, v204\n"
      "v_cvt_pk_bf16_f32 v179, v170, v171\n"
      "v_add_f32 v173, v173, v212\n"
      "v_cvt_pk_bf16_f32 v180, v172, v173\n"
      "v_cvt_pk_bf16_f32 v181, v174, v175\n"
      "buffer_store_dwordx2 v[180:181], v9, s[16:19], s57 offen\n"
      "buffer_store_dwordx4 v[176:179], v8, s[16:19], s57 offen\n"
      "s_add_u32 s52, s52, 106496\n"
      "s_branch .Ld_end\n"
      ".Ld_in1:\n"
      "s_add_u32 s36, s36, 165888\n"
      "s_min_u32 s36, s36, s6\n"
      "s_mov_b32 s51, s36\n"
      "s_add_u32 s36, s36, 165888\n"
      "s_min_u32 s36, s36, s6\n"
      "buffer_load_dword v20, v2, s[8:11], s36 offen sc1\n"
      "s_mov_b32 s51, s36\n"
      "s_add_u32 m0, s43, 24576\n"
      "s_add_u32 s36, s36, 165888\n"
      "s_waitcnt vmcnt(0)\n"
      "v_cmp_ne_u32 vcc, s21, v20\n"
      "s_cbranch_vccnz .Lsp_d1_20\n"
      ".Lbk_d1_20:\n"
      "buffer_load_dwordx4 v0, s[8:11], s51 offen offset:0 sc1 lds\n"
      "buffer_load_dwordx4 v0, s[8:11], s51 offen offset:1024 sc1 lds\n"
      "buffer_load_dwordx4 v0, s[8:11], s51 offen offset:2048 sc1 lds\n"
      "s_min_u32 s36, s36, s6\n"
      "buffer_load_dword v20, v2, s[8:11], s36 offen sc1\n"
      "buffer_load_dwordx4 v0, s[8:11], s51 offen offset:3072 sc1 lds\n"
      "s_add_u32 m0, s43, 28672\n"
      "s_nop 0\n"
      "buffer_load_dwordx4 v1, s[8:11], s51 offen offset:0 sc1 lds\n"
      "buffer_load_dwordx4 v1, s[8:11], s51 offen offset:1024 sc1 lds\n"
      "s_add_u32 m0, s43, 49152\n"
      "s_mov_b32 s51, s36\n"
      "v_add_u32 v14, 1, v14\n"
      "s_waitcnt vmcnt(3)\n"
      "v_cmp_ne_u32 vcc, s21, v20\n"
      "s_add_u32 s36, s36, 165888\n"
      "s_min_u32 s36, s36, s6\n"
      "s_cbranch_vccnz .Lsp_d2_21\n"
      ".Lbk_d2_21:\n"
      "s_waitcnt vmcnt(0)\n"
      "ds_write_b32 v16, v14\n"
      "buffer_load_dword v20, v2, s[8:11], s36 offen sc1\n"
      "buffer_load_dwordx4 v0, s[8:11], s51 offen offset:3072 sc1 lds\n"
      "s_add_u32 s52, s52, 106496\n"
      "s_add_u32 s37, s37, 173056\n"
      "buffer_load_dwordx4 v0, s[8:11], s51 offen offset:1024 sc1 lds\n"
      "ds_read_b32 v15, v17\n"
      "buffer_load_dwordx4 v0, s[8:11], s51 offen offset:2048 sc1 lds\n"
      "s_waitcnt lgkmcnt(0)\n"
      "v_cmp_gt_i32 vcc, v14, v15\n"
      "buffer_load_dwordx4 v0, s[8:11], s51 offen offset:0 sc1 lds\n"
      "s_add_u32 m0, s43, 53248\n"
      "s_cbranch_vccnz .Lsp_dy1_22\n"
      ".Lbk_dy1_22:\n"
      "buffer_load_dwordx4 v1, s[8:11], s51 offen offset:0 sc1 lds\n"
      "ds_read_b128 v[56:59], v0 offset:24576\n"
      "ds_read_b128 v[64:67], v0 offset:26624\n"
      "buffer_load_dwordx4 v1, s[8:11], s51 offen offset:1024 sc1 lds\n"
      "ds_read_b128 v[68:71], v0 offset:27648\n"
      "ds_read_b128 v[80:83], v0 offset:30720\n"
      "ds_read_b128 v[60:63], v0 offset:25600\n"
      "ds_read_b128 v[92:95], v0 offset:33792\n"
      "ds_read_b128 v[84:87], v0 offset:31744\n"
      "ds_read_b128 v[72:75], v0 offset:28672\n"
      "ds_read_b128 v[76:79], v0 offset:29696\n"
      "ds_read_b128 v[100:103], v0 offset:35840\n"
      "ds_read_b128 v[96:99], v0 offset:34816\n"
      "ds_read_b128 v[116:119], v0 offset:39936\n"
      "ds_read_b128 v[120:123], v0 offset:40960\n"
      "ds_read_b128 v[108:111], v0 offset:37888\n"
      "ds_read_b128 v[88:91], v0 offset:32768\n"
      "s_waitcnt vmcnt(0) lgkmcnt(0)\n"
      "s_nop 4\n"
      "s_branch .Ld_t1\n"
      ".Ld_in2:\n"
      "s_add_u32 s36, s36, 165888\n"
      "s_min_u32 s36, s36, s6\n"
      "s_mov_b32 s51, s36\n"
      "s_add_u32 s36, s36, 165888\n"
      "s_min_u32 s36, s36, s6\n"
      "s_mov_b32 s51, s36\n"
      "s_add_u32 s36, s36, 165888\n"
      "s_min_u32 s36, s36, s6\n"
      "buffer_load_dword v20, v2, s[8:11], s36 offen sc1\n"
      "s_add_u32 m0, s43, 49152\n"
      "s_mov_b32 s51, s36\n"
      "s_waitcnt vmcnt(0)\n"
      "v_cmp_ne_u32 vcc, s21, v20\n"
      "s_add_u32 s36, s36, 165888\n"
      "s_min_u32 s36, s36, s6\n"
      "s_cbranch_vccnz .Lsp_d2_23\n"
      ".Lbk_d2_23:\n"
      "buffer_load_dword v20, v2, s[8:11], s36 offen sc1\n"
      "buffer_load_dwordx4 v0, s[8:11], s51 offen offset:3072 sc1 lds\n"
      "s_add_u32 s52, s52, 106496\n"
      "s_add_u32 s37, s37, 173056\n"
      "buffer_load_dwordx4 v0, s[8:11], s51 offen offset:1024 sc1 lds\n"
      "buffer_load_dwordx4 v0, s[8:11], s51 offen offset:2048 sc1 lds\n"
      "buffer_load_dwordx4 v0, s[8:11], s51 offen offset:0 sc1 lds\n"
      "s_add_u32 m0, s43, 53248\n"
      "s_nop 0\n"
      "buffer_load_dwordx4 v1, s[8:11], s51 offen offset:0 sc1 lds\n"
      "buffer_load_dwordx4 v1, s[8:11], s51 offen offset:1024 sc1 lds\n"
      "s_mov_b32 s51, s36\n"
      "s_add_u32 s36, s36, 165888\n"
      "s_add_u32 m0, s43, 73728\n"
      "s_waitcnt vmcnt(6)\n"
      "v_cmp_ne_u32 vcc, s21, v20\n"
      "s_add_u32 s38, s38, 147456\n"
      "s_min_u32 s36, s36, s6\n"
      "s_cbranch_vccnz .Lsp_d3_24\n"
      ".Lbk_d3_24:\n"
      "buffer_load_dwordx4 v0, s[8:11], s51 offen offset:2048 sc1 lds\n"
      "v_add_u32 v14, 1, v14\n"
      "buffer_load_dwordx4 v0, s[8:11], s51 offen offset:1024 sc1 lds\n"
      "buffer_load_dword v20, v2, s[8:11], s36 offen sc1\n"
      "s_waitcnt vmcnt(3)\n"
      "ds_write_b32 v16, v14\n"
      "buffer_load_dwordx4 v0, s[8:11], s51 offen offset:0 sc1 lds\n"
      "buffer_load_dwordx4 v0, s[8:11], s51 offen offset:3072 sc1 lds\n"
      "s_add_u32 m0, s43, 77824\n"
      "ds_read_b32 v15, v17\n"
      "s_add_u32 s52, s52, 173056\n"
      "buffer_load_dwordx4 v1, s[8:11], s51 offen offset:0 sc1 lds\n"
      "s_waitcnt lgkmcnt(0)\n"
      "v_cmp_gt_i32 vcc, v14, v15\n"
      "buffer_load_dwordx4 v1, s[8:11], s51 offen offset:1024 sc1 lds\n"
      "s_cbranch_vccnz .Lsp_dy2_25\n"
      ".Lbk_dy2_25:\n"
      "ds_read_b128 v[68:71], v22 offset:3072\n"
      "ds_read_b128 v[60:63], v22 offset:1024\n"
      "ds_read_b128 v[80:83], v22 offset:6144\n"
      "ds_read_b128 v[72:75], v22 offset:4096\n"
      "ds_read_b128 v[76:79], v22 offset:5120\n"
      "ds_read_b128 v[96:99], v22 offset:10240\n"
      "ds_read_b128 v[84:87], v22 offset:7168\n"
      "ds_read_b128 v[56:59], v22 offset:0\n"
      "ds_read_b128 v[92:95], v22 offset:9216\n"
      "s_mov_b32 s51, s36\n"
      "ds_read_b128 v[64:67], v22 offset:2048\n"
      "ds_read_b128 v[100:103], v22 offset:11264\n"
      "ds_read_b128 v[132:135], v22 offset:19456\n"
      "ds_read_b128 v[112:115], v22 offset:14336\n"
      "s_add_u32 m0, s43, 98304\n"
      "s_waitcnt vmcnt(0) lgkmcnt(0)\n"
      "s_nop 4\n"
      "s_branch .Ld_t2\n"
      ".Ld_in3:\n"
      "s_add_u32 s36, s36, 165888\n"
      "s_min_u32 s36, s36, s6\n"
      "s_mov_b32 s51, s36\n"
      "s_add_u32 s36, s36, 165888\n"
      "s_min_u32 s36, s36, s6\n"
      "s_mov_b32 s51, s36\n"
      "s_add_u32 s36, s36, 165888\n"
      "s_min_u32 s36, s36, s6\n"
      "s_mov_b32 s51, s36\n"
      "s_add_u32 s36, s36, 165888\n"
      "s_min_u32 s36, s36, s6\n"
      "buffer_load_dword v20, v2, s[8:11], s36 offen sc1\n"
      "s_add_u32 s52, s52, 106496\n"
      "s_add_u32 s37, s37, 173056\n"
      "s_mov_b32 s51, s36\n"
      "s_add_u32 s36, s36, 165888\n"
      "s_add_u32 m0, s43, 73728\n"
      "s_waitcnt vmcnt(0)\n"
      "v_cmp_ne_u32 vcc, s21, v20\n"
      "s_add_u32 s38, s38, 147456\n"
      "s_min_u32 s36, s36, s6\n"
      "s_cbranch_vccnz .Lsp_d3_26\n"
      ".Lbk_d3_26:\n"
      "buffer_load_dwordx4 v0, s[8:11], s51 offen offset:2048 sc1 lds\n"
      "buffer_load_dwordx4 v0, s[8:11], s51 offen offset:1024 sc1 lds\n"
      "buffer_load_dword v20, v2, s[8:11], s36 offen sc1\n"
      "buffer_load_dwordx4 v0, s[8:11], s51 offen offset:0 sc1 lds\n"
      "buffer_load_dwordx4 v0, s[8:11], s51 offen offset:3072 sc1 lds\n"
      "s_add_u32 m0, s43, 77824\n"
      "s_add_u32 s52, s52, 173056\n"
      "buffer_load_dwordx4 v1, s[8:11], s51 offen offset:0 sc1 lds\n"
      "buffer_load_dwordx4 v1, s[8:11], s51 offen offset:1024 sc1 lds\n"
      "s_mov_b32 s51, s36\n"
      "s_add_u32 m0, s43, 98304\n"
      "s_add_u32 s36, s36, 165888\n"
      "v_add_u32 v14, 1, v14\n"
      "s_waitcnt vmcnt(4)\n"
      "v_cmp_ne_u32 vcc, s21, v20\n"
      "s_cbranch_vccnz .Lsp_d4_27\n"
      ".Lbk_d4_27:\n"
      "s_min_u32 s36, s36, s6\n"
      "buffer_load_dwordx4 v0, s[8:11], s51 offen offset:0 sc1 lds\n"
      "buffer_load_dwordx4 v0, s[8:11], s51 offen offset:2048 sc1 lds\n"
      "s_waitcnt vmcnt(2)\n"
      "ds_write_b32 v16, v14\n"
      "buffer_load_dword v20, v2, s[8:11], s36 offen sc1\n"
      "buffer_load_dwordx4 v0, s[8:11], s51 offen offset:3072 sc1 lds\n"
      "ds_read_b32 v15, v17\n"
      "buffer_load_dwordx4 v0, s[8:11], s51 offen offset:1024 sc1 lds\n"
      "s_add_u32 m0, s43, 102400\n"
      "s_nop 0\n"
      "buffer_load_dwordx4 v1, s[8:11], s51 offen offset:1024 sc1 lds\n"
      "s_waitcnt lgkmcnt(0)\n"
      "v_cmp_gt_i32 vcc, v14, v15\n"
      "buffer_load_dwordx4 v1, s[8:11], s51 offen offset:0 sc1 lds\n"
      "s_cbranch_vccnz .Lsp_dy3_28\n"
      ".Lbk_dy3_28:\n"
      "ds_read_b128 v[60:63], v22 offset:25600\n"
      "ds_read_b128 v[64:67], v22 offset:26624\n"
      "s_mov_b32 s51, s36\n"
      "ds_read_b128 v[72:75], v22 offset:28672\n"
      "ds_read_b128 v[68:71], v22 offset:27648\n"
      "ds_read_b128 v[92:95], v22 offset:33792\n"
      "ds_read_b128 v[56:59], v22 offset:24576\n"
      "ds_read_b128 v[100:103], v22 offset:35840\n"
      "ds_read_b128 v[80:83], v22 offset:30720\n"
      "ds_read_b128 v[112:115], v22 offset:38912\n"
      "s_add_u32 s37, s37, 106496\n"
      "ds_read_b128 v[88:91], v22 offset:32768\n"
      "ds_read_b128 v[96:99], v22 offset:34816\n"
      "ds_read_b128 v[76:79], v22 offset:29696\n"
      "ds_read_b128 v[116:119], v22 offset:39936\n"
      "ds_read_b128 v[84:87], v22 offset:31744\n"
      "ds_read_b128 v[104:107], v22 offset:36864\n"
      "s_waitcnt vmcnt(0) lgkmcnt(0)\n"
      "s_nop 4\n"
      "s_branch .Ld_t3\n"
      ".Ld_in4:\n"
      "s_add_u32 s36, s36, 165888\n"
      "s_min_u32 s36, s36, s6\n"
      "s_mov_b32 s51, s36\n"
      "s_add_u32 s36, s36, 165888\n"
      "s_min_u32 s36, s36, s6\n"
      "s_mov_b32 s51, s36\n"
      "s_add_u32 s36, s36, 165888\n"
      "s_min_u32 s36, s36, s6\n"
      "s_mov_b32 s51, s36\n"
      "s_add_u32 s36, s36, 165888\n"
      "s_min_u32 s36, s36, s6\n"
      "s_add_u32 s52, s52, 106496\n"
      "s_add_u32 s37, s37, 173056\n"
      "s_mov_b32 s51, s36\n"
      "s_add_u32 s36, s36, 165888\n"
      "s_add_u32 s38, s38, 147456\n"
      "s_min_u32 s36, s36, s6\n"
      "buffer_load_dword v20, v2, s[8:11], s36 offen sc1\n"
      "s_add_u32 s52, s52, 173056\n"
      "s_mov_b32 s51, s36\n"
      "s_add_u32 m0, s43, 98304\n"
      "s_add_u32 s36, s36, 165888\n"
      "s_waitcnt vmcnt(0)\n"
      "v_cmp_ne_u32 vcc, s21, v20\n"
      "s_cbranch_vccnz .Lsp_d4_29\n"
      ".Lbk_d4_29:\n"
      "s_min_u32 s36, s36, s6\n"
      "buffer_load_dwordx4 v0, s[8:11], s51 offen offset:0 sc1 lds\n"
      "buffer_load_dwordx4 v0, s[8:11], s51 offen offset:2048 sc1 lds\n"
      "buffer_load_dword v20, v2, s[8:11], s36 offen sc1\n"
      "buffer_load_dwordx4 v0, s[8:11], s51 offen offset:3072 sc1 lds\n"
      "buffer_load_dwordx4 v0, s[8:11], s51 offen offset:1024 sc1 lds\n"
      "s_add_u32 m0, s43, 102400\n"
      "s_nop 0\n"
      "buffer_load_dwordx4 v1, s[8:11], s51 offen offset:1024 sc1 lds\n"
      "buffer_load_dwordx4 v1, s[8:11], s51 offen offset:0 sc1 lds\n"
      "s_mov_b32 s51, s36\n"
      "s_add_u32 s37, s37, 106496\n"
      "s_add_u32 m0, s43, 122880\n"
      "s_waitcnt vmcnt(4)\n"
      "v_cmp_ne_u32 vcc, s21, v20\n"
      "s_add_u32 s36, s36, 165888\n"
      "s_add_u32 s52, s52, 106496\n"
      "s_cbranch_vccnz .Lsp_d5_30\n"
      ".Lbk_d5_30:\n"
      "buffer_load_dwordx4 v0, s[8:11], s51 offen offset:1024 sc1 lds\n"
      "v_add_u32 v14, 1, v14\n"
      "buffer_load_dwordx4 v0, s[8:11], s51 offen offset:0 sc1 lds\n"
      "buffer_load_dwordx4 v0, s[8:11], s51 offen offset:2048 sc1 lds\n"
      "s_waitcnt vmcnt(3)\n"
      "ds_write_b32 v16, v14\n"
      "buffer_load_dwordx4 v0, s[8:11], s51 offen offset:3072 sc1 lds\n"
      "s_min_u32 s36, s36, s6\n"
      "s_add_u32 m0, s43, 126976\n"
      "s_nop 0\n"
      "buffer_load_dwordx4 v1, s[8:11], s51 offen offset:1024 sc1 lds\n"
      "ds_read_b32 v15, v17\n"
      "buffer_load_dword v20, v2, s[8:11], s36 offen sc1\n"
      "s_waitcnt lgkmcnt(0)\n"
      "v_cmp_gt_i32 vcc, v14, v15\n"
      "buffer_load_dwordx4 v1, s[8:11], s51 offen offset:0 sc1 lds\n"
      "s_cbranch_vccnz .Lsp_dy4_31\n"
      ".Lbk_dy4_31:\n"
      "ds_read_b128 v[60:63], v23 offset:1024\n"
      "ds_read_b128 v[64:67], v23 offset:2048\n"
      "ds_read_b128 v[72:75], v23 offset:4096\n"
      "ds_read_b128 v[68:71], v23 offset:3072\n"
      "s_add_u32 s58, s41, 4294967264\n"
      "ds_read_b128 v[80:83], v23 offset:6144\n"
      "ds_read_b128 v[84:87], v23 offset:7168\n"
      "ds_read_b128 v[76:79], v23 offset:5120\n"
      "s_add_u32 s37, s37, 106496\n"
      "ds_read_b128 v[56:59], v23 offset:0\n"
      "ds_read_b128 v[88:91], v23 offset:8192\n"
      "ds_read_b128 v[96:99], v23 offset:10240\n"
      "ds_read_b128 v[100:103], v23 offset:11264\n"
      "ds_read_b128 v[132:135], v23 offset:19456\n"
      "ds_read_b128 v[104:107], v23 offset:12288\n"
      "s_waitcnt vmcnt(0) lgkmcnt(0)\n"
      "s_nop 4\n"
      "s_branch .Ld_t4\n"
      ".Ld_in5:\n"
      "s_add_u32 s36, s36, 165888\n"
      "s_min_u32 s36, s36, s6\n"
      "s_mov_b32 s51, s36\n"
      "s_add_u32 s36, s36, 165888\n"
      "s_min_u32 s36, s36, s6\n"
      "s_mov_b32 s51, s36\n"
      "s_add_u32 s36, s36, 165888\n"
      "s_min_u32 s36, s36, s6\n"
      "s_mov_b32 s51, s36\n"
      "s_add_u32 s36, s36, 165888\n"
      "s_min_u32 s36, s36, s6\n"
      "s_add_u32 s52, s52, 106496\n"
      "s_add_u32 s37, s37, 173056\n"
      "s_mov_b32 s51, s36\n"
      "s_add_u32 s36, s36, 165888\n"
      "s_add_u32 s38, s38, 147456\n"
      "s_min_u32 s36, s36, s6\n"
      "s_add_u32 s52, s52, 173056\n"
      "s_mov_b32 s51, s36\n"
      "s_add_u32 s36, s36, 165888\n"
      "s_min_u32 s36, s36, s6\n"
      "buffer_load_dword v20, v2, s[8:11], s36 offen sc1\n"
      "s_mov_b32 s51, s36\n"
      "s_add_u32 s37, s37, 106496\n"
      "s_add_u32 m0, s43, 122880\n"
      "s_waitcnt vmcnt(0)\n"
      "v_cmp_ne_u32 vcc, s21, v20\n"
      "s_add_u32 s36, s36, 165888\n"
      "s_add_u32 s52, s52, 106496\n"
      "s_cbranch_vccnz .Lsp_d5_32\n"
      ".Lbk_d5_32:\n"
      "buffer_load_dwordx4 v0, s[8:11], s51 offen offset:1024 sc1 lds\n"
      "buffer_load_dwordx4 v0, s[8:11], s51 offen offset:0 sc1 lds\n"
      "buffer_load_dwordx4 v0, s[8:11], s51 offen offset:2048 sc1 lds\n"
      "buffer_load_dwordx4 v0, s[8:11], s51 offen offset:3072 sc1 lds\n"
      "s_min_u32 s36, s36, s6\n"
      "s_add_u32 m0, s43, 126976\n"
      "s_nop 0\n"
      "buffer_load_dwordx4 v1, s[8:11], s51 offen offset:1024 sc1 lds\n"
      "buffer_load_dword v20, v2, s[8:11], s36 offen sc1\n"
      "buffer_load_dwordx4 v1, s[8:11], s51 offen offset:0 sc1 lds\n"
      "s_add_u32 s58, s41, 4294967264\n"
      "s_add_u32 s37, s37, 106496\n"
      "s_add_u32 m0, s43, 0\n"
      "s_mov_b32 s51, s36\n"
      "v_add_u32 v14, 1, v14\n"
      "s_add_u32 s36, s36, 165888\n"
      "s_add_u32 s37, s37, 106496\n"
      "s_min_u32 s36, s36, s6\n"
      "s_cmp_lt_u32 s41, 32\n"
      "s_cselect_b32 s56, s39, 0x40000000\n"
      "s_add_u32 s53, s53, 599040\n"
      "s_waitcnt vmcnt(1)\n"
      "v_cmp_ne_u32 vcc, s21, v20\n"
      "s_add_u32 s39, s39, 147456\n"
      "s_waitcnt vmcnt(0)\n"
      "ds_write_b32 v16, v14\n"
      "s_cmp_lt_u32 s58, 32\n"
      "s_cselect_b32 s57, s40, 0x40000000\n"
      "s_cbranch_vccnz .Lsp_d0_33\n"
      ".Lbk_d0_33:\n"
      "buffer_load_dwordx4 v0, s[8:11], s51 offen offset:0 sc1 lds\n"
      "s_add_u32 s52, s52, 106496\n"
      "buffer_load_dwordx4 v0, s[8:11], s51 offen offset:1024 sc1 lds\n"
      "buffer_load_dword v20, v2, s[8:11], s36 offen sc1\n"
      "ds_read_b32 v15, v17\n"
      "buffer_load_dwordx4 v0, s[8:11], s51 offen offset:2048 sc1 lds\n"
      "s_add_u32 s40, s40, 4294856704\n"
      "buffer_load_dwordx4 v0, s[8:11], s51 offen offset:3072 sc1 lds\n"
      "s_waitcnt lgkmcnt(0)\n"
      "v_cmp_gt_i32 vcc, v14, v15\n"
      "s_add_u32 m0, s43, 4096\n"
      "s_nop 0\n"
      "buffer_load_dwordx4 v1, s[8:11], s51 offen offset:1024 sc1 lds\n"
      "s_cbranch_vccnz .Lsp_dy5_34\n"
      ".Lbk_dy5_34:\n"
      "ds_read_b128 v[56:59], v23 offset:24576\n"
      "ds_read_b128 v[64:67], v23 offset:26624\n"
      "buffer_load_dwordx4 v1, s[8:11], s51 offen offset:0 sc1 lds\n"
      "ds_read_b128 v[76:79], v23 offset:29696\n"
      "s_add_u32 s41, s41, 6\n"
      "ds_read_b128 v[88:91], v23 offset:32768\n"
      "ds_read_b128 v[68:71], v23 offset:27648\n"
      "ds_read_b128 v[96:99], v23 offset:34816\n"
      "ds_read_b128 v[80:83], v23 offset:30720\n"
      "ds_read_b128 v[72:75], v23 offset:28672\n"
      "s_mov_b32 s51, s36\n"
      "ds_read_b128 v[112:115], v23 offset:38912\n"
      "ds_read_b128 v[60:63], v23 offset:25600\n"
      "ds_read_b128 v[104:107], v23 offset:36864\n"
      "ds_read_b128 v[84:87], v23 offset:31744\n"
      "ds_read_b128 v[140:143], v23 offset:46080\n"
      "ds_read_b128 v[100:103], v23 offset:35840\n"
      "s_waitcnt vmcnt(0) lgkmcnt(0)\n"
      "s_nop 4\n"
      "s_branch .Ld_t5\n"
      ".Ld_end:\n"
      "s_endpgm\n"
      ".Lsp_d0_1:\n"
      "s_mov_b32 s22, s23\n"
      ".Lsl_d0_1:\n"
      "buffer_load_dword v20, v2, s[8:11], s51 offen sc1\n"
      "s_waitcnt vmcnt(0)\n"
      "v_cmp_ne_u32 vcc, s21, v20\n"
      "s_cbranch_vccz .Lbk_d0_1\n"
      "s_sleep 4\n"
      "s_sub_u32 s22, s22, 1\n"
      "s_cbranch_scc0 .Lsl_d0_1\n"
      "s_mov_b32 s23, 0\n"
      "s_branch .Lbk_d0_1\n"
      ".Lsp_d1_2:\n"
      "s_mov_b32 s22, s23\n"
      ".Lsl_d1_2:\n"
      "buffer_load_dword v20, v2, s[8:11], s51 offen sc1\n"
      "s_waitcnt vmcnt(0)\n"
      "v_cmp_ne_u32 vcc, s21, v20\n"
      "s_cbranch_vccz .Lbk_d1_2\n"
      "s_sleep 4\n"
      "s_sub_u32 s22, s22, 1\n"
      "s_cbranch_scc0 .Lsl_d1_2\n"
      "s_mov_b32 s23, 0\n"
      "s_branch .Lbk_d1_2\n"
      ".Lsp_dy0_3:\n"
      "s_lshl_b32 s22, s23, 4\n"
      ".Lsl_dy0_3:\n"
      "ds_read_b32 v15, v17\n"
      "s_waitcnt lgkmcnt(0)\n"
      "v_cmp_gt_i32 vcc, v14, v15\n"
      "s_cbranch_vccz .Lbk_dy0_3\n"
      "s_sub_u32 s22, s22, 1\n"
      "s_cbranch_scc0 .Lsl_dy0_3\n"
      "s_mov_b32 s23, 0\n"
      "s_branch .Lbk_dy0_3\n"
      ".Lsp_d2_4:\n"
      "s_mov_b32 s22, s23\n"
      "buffer_load_dword v20, v2, s[8:11], s51 offen sc1\n"
      "s_waitcnt vmcnt(0)\n"
      "v_cmp_ne_u32 vcc, s21, v20\n"
      "s_add_u32 s30, s52, 106496\n"
      "buffer_store_dword v18, v4, s[8:11], s30 offen sc1\n"
      "s_cbranch_vccz .Lbk_d2_4\n"
      ".Lsl_d2_4:\n"
      "buffer_load_dword v20, v2, s[8:11], s51 offen sc1\n"
      "s_waitcnt vmcnt(0)\n"
      "v_cmp_ne_u32 vcc, s21, v20\n"
      "s_cbranch_vccz .Lbk_d2_4\n"
      "s_sleep 4\n"
      "s_sub_u32 s22, s22, 1\n"
      "s_cbranch_scc0 .Lsl_d2_4\n"
      "s_mov_b32 s23, 0\n"
      "s_branch .Lbk_d2_4\n"
      ".Lsp_dy1_5:\n"
      "s_lshl_b32 s22, s23, 4\n"
      ".Lsl_dy1_5:\n"
      "ds_read_b32 v15, v17\n"
      "s_waitcnt lgkmcnt(0)\n"
      "v_cmp_gt_i32 vcc, v14, v15\n"
      "s_cbranch_vccz .Lbk_dy1_5\n"
      "s_sub_u32 s22, s22, 1\n"
      "s_cbranch_scc0 .Lsl_dy1_5\n"
      "s_mov_b32 s23, 0\n"
      "s_branch .Lbk_dy1_5\n"
      ".Lsp_d3_6:\n"
      "s_mov_b32 s22, s23\n"
      "buffer_load_dword v20, v2, s[8:11], s51 offen sc1\n"
      "s_waitcnt vmcnt(0)\n"
      "v_cmp_ne_u32 vcc, s21, v20\n"
      "buffer_store_dword v18, v5, s[8:11], s56 offen sc1\n"
      "s_cbranch_vccz .Lbk_d3_6\n"
      ".Lsl_d3_6:\n"
      "buffer_load_dword v20, v2, s[8:11], s51 offen sc1\n"
      "s_waitcnt vmcnt(0)\n"
      "v_cmp_ne_u32 vcc, s21, v20\n"
      "s_cbranch_vccz .Lbk_d3_6\n"
      "s_sleep 4\n"
      "s_sub_u32 s22, s22, 1\n"
      "s_cbranch_scc0 .Lsl_d3_6\n"
      "s_mov_b32 s23, 0\n"
      "s_branch .Lbk_d3_6\n"
      ".Lsp_dy2_7:\n"
      "s_lshl_b32 s22, s23, 4\n"
      ".Lsl_dy2_7:\n"
      "ds_read_b32 v15, v17\n"
      "s_waitcnt lgkmcnt(0)\n"
      "v_cmp_gt_i32 vcc, v14, v15\n"
      "s_cbranch_vccz .Lbk_dy2_7\n"
      "s_sub_u32 s22, s22, 1\n"
      "s_cbranch_scc0 .Lsl_dy2_7\n"
      "s_mov_b32 s23, 0\n"
      "s_branch .Lbk_dy2_7\n"
      ".Lsp_d4_8:\n"
      "s_mov_b32 s22, s23\n"
      "buffer_load_dword v20, v2, s[8:11], s51 offen sc1\n"
      "s_waitcnt vmcnt(0)\n"
      "v_cmp_ne_u32 vcc, s21, v20\n"
      "s_add_u32 s30, s52, 0\n"
      "buffer_store_dword v18, v4, s[8:11], s30 offen sc1\n"
      "s_cbranch_vccz .Lbk_d4_8\n"
      ".Lsl_d4_8:\n"
      "buffer_load_dword v20, v2, s[8:11], s51 offen sc1\n"
      "s_waitcnt vmcnt(0)\n"
      "v_cmp_ne_u32 vcc, s21, v20\n"
      "s_cbranch_vccz .Lbk_d4_8\n"
      "s_sleep 4\n"
      "s_sub_u32 s22, s22, 1\n"
      "s_cbranch_scc0 .Lsl_d4_8\n"
      "s_mov_b32 s23, 0\n"
      "s_branch .Lbk_d4_8\n"
      ".Lsp_dy3_9:\n"
      "s_lshl_b32 s22, s23, 4\n"
      ".Lsl_dy3_9:\n"
      "ds_read_b32 v15, v17\n"
      "s_waitcnt lgkmcnt(0)\n"
      "v_cmp_gt_i32 vcc, v14, v15\n"
      "s_cbranch_vccz .Lbk_dy3_9\n"
      "s_sub_u32 s22, s22, 1\n"
      "s_cbranch_scc0 .Lsl_dy3_9\n"
      "s_mov_b32 s23, 0\n"
      "s_branch .Lbk_dy3_9\n"
      ".Lsp_d5_10:\n"
      "s_mov_b32 s22, s23\n"
      "buffer_load_dword v20, v2, s[8:11], s51 offen sc1\n"
      "s_waitcnt vmcnt(0)\n"
      "v_cmp_ne_u32 vcc, s21, v20\n"
      "s_add_u32 s30, s52, 0\n"
      "buffer_store_dword v18, v4, s[8:11], s30 offen sc1\n"
      "s_cbranch_vccz .Lbk_d5_10\n"
      ".Lsl_d5_10:\n"
      "buffer_load_dword v20, v2, s[8:11], s51 offen sc1\n"
      "s_waitcnt vmcnt(0)\n"
      "v_cmp_ne_u32 vcc, s21, v20\n"
      "s_cbranch_vccz .Lbk_d5_10\n"
      "s_sleep 4\n"
      "s_sub_u32 s22, s22, 1\n"
      "s_cbranch_scc0 .Lsl_d5_10\n"
      "s_mov_b32 s23, 0\n"
      "s_branch .Lbk_d5_10\n"
      ".Lsp_dy4_11:\n"
      "s_lshl_b32 s22, s23, 4\n"
      ".Lsl_dy4_11:\n"
      "ds_read_b32 v15, v17\n"
      "s_waitcnt lgkmcnt(0)\n"
      "v_cmp_gt_i32 vcc, v14, v15\n"
      "s_cbranch_vccz .Lbk_dy4_11\n"
      "s_sub_u32 s22, s22, 1\n"
      "s_cbranch_scc0 .Lsl_dy4_11\n"
      "s_mov_b32 s23, 0\n"
      "s_branch .Lbk_dy4_11\n"
      ".Lsp_df_12:\n"
      "s_mov_b32 s22, s23\n"
      "buffer_load_dword v21, v3, s[8:11], s53 offen sc1\n"
      "s_waitcnt vmcnt(0)\n"
      "v_cmp_ne_u32 vcc, s21, v21\n"
      "s_add_u32 s30, s52, 0\n"
      "buffer_store_dword v18, v4, s[8:11], s30 offen sc1\n"
      "s_cbranch_vccz .Lbk_df_12\n"
      ".Lsl_df_12:\n"
      "buffer_load_dword v21, v3, s[8:11], s53 offen sc1\n"
      "s_waitcnt vmcnt(0)\n"
      "v_cmp_ne_u32 vcc, s21, v21\n"
      "s_cbranch_vccz .Lbk_df_12\n"
      "s_sleep 4\n"
      "s_sub_u32 s22, s22, 1\n"
      "s_cbranch_scc0 .Lsl_df_12\n"
      "s_mov_b32 s23, 0\n"
      "s_branch .Lbk_df_12\n"
      ".Lsp_d0_13:\n"
      "s_mov_b32 s22, s23\n"
      "buffer_load_dword v20, v2, s[8:11], s51 offen sc1\n"
      "s_waitcnt vmcnt(0)\n"
      "v_cmp_ne_u32 vcc, s21, v20\n"
      "s_add_u32 s30, s52, 106496\n"
      "buffer_store_dword v18, v4, s[8:11], s30 offen sc1\n"
      "s_cbranch_vccz .Lbk_d0_13\n"
      ".Lsl_d0_13:\n"
      "buffer_load_dword v20, v2, s[8:11], s51 offen sc1\n"
      "s_waitcnt vmcnt(0)\n"
      "v_cmp_ne_u32 vcc, s21, v20\n"
      "s_cbranch_vccz .Lbk_d0_13\n"
      "s_sleep 4\n"
      "s_sub_u32 s22, s22, 1\n"
      "s_cbranch_scc0 .Lsl_d0_13\n"
      "s_mov_b32 s23, 0\n"
      "s_branch .Lbk_d0_13\n"
      ".Lsp_dy5_14:\n"
      "s_lshl_b32 s22, s23, 4\n"
      ".Lsl_dy5_14:\n"
      "ds_read_b32 v15, v17\n"
      "s_waitcnt lgkmcnt(0)\n"
      "v_cmp_gt_i32 vcc, v14, v15\n"
      "s_cbranch_vccz .Lbk_dy5_14\n"
      "s_sub_u32 s22, s22, 1\n"
      "s_cbranch_scc0 .Lsl_dy5_14\n"
      "s_mov_b32 s23, 0\n"
      "s_branch .Lbk_dy5_14\n"
      ".Lsp_d1_15:\n"
      "s_mov_b32 s22, s23\n"
      "buffer_load_dword v20, v2, s[8:11], s51 offen sc1\n"
      "s_waitcnt vmcnt(0)\n"
      "v_cmp_ne_u32 vcc, s21, v20\n"
      "s_add_u32 s30, s52, 106496\n"
      "buffer_store_dword v18, v4, s[8:11], s30 offen sc1\n"
      "s_cbranch_vccz .Lbk_d1_15\n"
      ".Lsl_d1_15:\n"
      "buffer_load_dword v20, v2, s[8:11], s51 offen sc1\n"
      "s_waitcnt vmcnt(0)\n"
      "v_cmp_ne_u32 vcc, s21, v20\n"
      "s_cbranch_vccz .Lbk_d1_15\n"
      "s_sleep 4\n"
      "s_sub_u32 s22, s22, 1\n"
      "s_cbranch_scc0 .Lsl_d1_15\n"
      "s_mov_b32 s23, 0\n"
      "s_branch .Lbk_d1_15\n"
      ".Lsp_dy0_16:\n"
      "s_lshl_b32 s22, s23, 4\n"
      ".Lsl_dy0_16:\n"
      "ds_read_b32 v15, v17\n"
      "s_waitcnt lgkmcnt(0)\n"
      "v_cmp_gt_i32 vcc, v14, v15\n"
      "s_cbranch_vccz .Lbk_dy0_16\n"
      "s_sub_u32 s22, s22, 1\n"
      "s_cbranch_scc0 .Lsl_dy0_16\n"
      "s_mov_b32 s23, 0\n"
      "s_branch .Lbk_dy0_16\n"
      ".Lsp_df_17:\n"
      "s_mov_b32 s22, s23\n"
      "buffer_load_dword v21, v3, s[8:11], s53 offen sc1\n"
      "s_waitcnt vmcnt(0)\n"
      "v_cmp_ne_u32 vcc, s21, v21\n"
      "s_add_u32 s30, s52, 0\n"
      "buffer_store_dword v18, v4, s[8:11], s30 offen sc1\n"
      "s_cbranch_vccz .Lbk_df_17\n"
      ".Lsl_df_17:\n"
      "buffer_load_dword v21, v3, s[8:11], s53 offen sc1\n"
      "s_waitcnt vmcnt(0)\n"
      "v_cmp_ne_u32 vcc, s21, v21\n"
      "s_cbranch_vccz .Lbk_df_17\n"
      "s_sleep 4\n"
      "s_sub_u32 s22, s22, 1\n"
      "s_cbranch_scc0 .Lsl_df_17\n"
      "s_mov_b32 s23, 0\n"
      "s_branch .Lbk_df_17\n"
      ".Lsp_df_18:\n"
      "s_mov_b32 s22, s23\n"
      "buffer_load_dword v21, v3, s[8:11], s53 offen sc1\n"
      "s_waitcnt vmcnt(0)\n"
      "v_cmp_ne_u32 vcc, s21, v21\n"
      "s_add_u32 s30, s52, 0\n"
      "buffer_store_dword v18, v4, s[8:11], s30 offen sc1\n"
      "s_cbranch_vccz .Lbk_df_18\n"
      ".Lsl_df_18:\n"
      "buffer_load_dword v21, v3, s[8:11], s53 offen sc1\n"
      "s_waitcnt vmcnt(0)\n"
      "v_cmp_ne_u32 vcc, s21, v21\n"
      "s_cbranch_vccz .Lbk_df_18\n"
      "s_sleep 4\n"
      "s_sub_u32 s22, s22, 1\n"
      "s_cbranch_scc0 .Lsl_df_18\n"
      "s_mov_b32 s23, 0\n"
      "s_branch .Lbk_df_18\n"
      ".Lsp_df_19:\n"
      "s_mov_b32 s22, s23\n"
      "buffer_load_dword v21, v3, s[8:11], s53 offen sc1\n"
      "s_waitcnt vmcnt(0)\n"
      "v_cmp_ne_u32 vcc, s21, v21\n"
      "s_add_u32 s30, s52, 0\n"
      "buffer_store_dword v18, v4, s[8:11], s30 offen sc1\n"
      "s_cbranch_vccz .Lbk_df_19\n"
      ".Lsl_df_19:\n"
      "buffer_load_dword v21, v3, s[8:11], s53 offen sc1\n"
      "s_waitcnt vmcnt(0)\n"
      "v_cmp_ne_u32 vcc, s21, v21\n"
      "s_cbranch_vccz .Lbk_df_19\n"
      "s_sleep 4\n"
      "s_sub_u32 s22, s22, 1\n"
      "s_cbranch_scc0 .Lsl_df_19\n"
      "s_mov_b32 s23, 0\n"
      "s_branch .Lbk_df_19\n"
      ".Lsp_d1_20:\n"
      "s_mov_b32 s22, s23\n"
      ".Lsl_d1_20:\n"
      "buffer_load_dword v20, v2, s[8:11], s51 offen sc1\n"
      "s_waitcnt vmcnt(0)\n"
      "v_cmp_ne_u32 vcc, s21, v20\n"
      "s_cbranch_vccz .Lbk_d1_20\n"
      "s_sleep 4\n"
      "s_sub_u32 s22, s22, 1\n"
      "s_cbranch_scc0 .Lsl_d1_20\n"
      "s_mov_b32 s23, 0\n"
      "s_branch .Lbk_d1_20\n"
      ".Lsp_d2_21:\n"
      "s_mov_b32 s22, s23\n"
      ".Lsl_d2_21:\n"
      "buffer_load_dword v20, v2, s[8:11], s51 offen sc1\n"
      "s_waitcnt vmcnt(0)\n"
      "v_cmp_ne_u32 vcc, s21, v20\n"
      "s_cbranch_vccz .Lbk_d2_21\n"
      "s_sleep 4\n"
      "s_sub_u32 s22, s22, 1\n"
      "s_cbranch_scc0 .Lsl_d2_21\n"
      "s_mov_b32 s23, 0\n"
      "s_branch .Lbk_d2_21\n"
      ".Lsp_dy1_22:\n"
      "s_lshl_b32 s22, s23, 4\n"
      ".Lsl_dy1_22:\n"
      "ds_read_b32 v15, v17\n"
      "s_waitcnt lgkmcnt(0)\n"
      "v_cmp_gt_i32 vcc, v14, v15\n"
      "s_cbranch_vccz .Lbk_dy1_22\n"
      "s_sub_u32 s22, s22, 1\n"
      "s_cbranch_scc0 .Lsl_dy1_22\n"
      "s_mov_b32 s23, 0\n"
      "s_branch .Lbk_dy1_22\n"
      ".Lsp_d2_23:\n"
      "s_mov_b32 s22, s23\n"
      ".Lsl_d2_23:\n"
      "buffer_load_dword v20, v2, s[8:11], s51 offen sc1\n"
      "s_waitcnt vmcnt(0)\n"
      "v_cmp_ne_u32 vcc, s21, v20\n"
      "s_cbranch_vccz .Lbk_d2_23\n"
      "s_sleep 4\n"
      "s_sub_u32 s22, s22, 1\n"
      "s_cbranch_scc0 .Lsl_d2_23\n"
      "s_mov_b32 s23, 0\n"
      "s_branch .Lbk_d2_23\n"
      ".Lsp_d3_24:\n"
      "s_mov_b32 s22, s23\n"
      ".Lsl_d3_24:\n"
      "buffer_load_dword v20, v2, s[8:11], s51 offen sc1\n"
      "s_waitcnt vmcnt(0)\n"
      "v_cmp_ne_u32 vcc, s21, v20\n"
      "s_cbranch_vccz .Lbk_d3_24\n"
      "s_sleep 4\n"
      "s_sub_u32 s22, s22, 1\n"
      "s_cbranch_scc0 .Lsl_d3_24\n"
      "s_mov_b32 s23, 0\n"
      "s_branch .Lbk_d3_24\n"
      ".Lsp_dy2_25:\n"
      "s_lshl_b32 s22, s23, 4\n"
      ".Lsl_dy2_25:\n"
      "ds_read_b32 v15, v17\n"
      "s_waitcnt lgkmcnt(0)\n"
      "v_cmp_gt_i32 vcc, v14, v15\n"
      "s_cbranch_vccz .Lbk_dy2_25\n"
      "s_sub_u32 s22, s22, 1\n"
      "s_cbranch_scc0 .Lsl_dy2_25\n"
      "s_mov_b32 s23, 0\n"
      "s_branch .Lbk_dy2_25\n"
      ".Lsp_d3_26:\n"
      "s_mov_b32 s22, s23\n"
      ".Lsl_d3_26:\n"
      "buffer_load_dword v20, v2, s[8:11], s51 offen sc1\n"
      "s_waitcnt vmcnt(0)\n"
      "v_cmp_ne_u32 vcc, s21, v20\n"
      "s_cbranch_vccz .Lbk_d3_26\n"
      "s_sleep 4\n"
      "s_sub_u32 s22, s22, 1\n"
      "s_cbranch_scc0 .Lsl_d3_26\n"
      "s_mov_b32 s23, 0\n"
      "s_branch .Lbk_d3_26\n"
      ".Lsp_d4_27:\n"
      "s_mov_b32 s22, s23\n"
      ".Lsl_d4_27:\n"
      "buffer_load_dword v20, v2, s[8:11], s51 offen sc1\n"
      "s_waitcnt vmcnt(0)\n"
      "v_cmp_ne_u32 vcc, s21, v20\n"
      "s_cbranch_vccz .Lbk_d4_27\n"
      "s_sleep 4\n"
      "s_sub_u32 s22, s22, 1\n"
      "s_cbranch_scc0 .Lsl_d4_27\n"
      "s_mov_b32 s23, 0\n"
      "s_branch .Lbk_d4_27\n"
      ".Lsp_dy3_28:\n"
      "s_lshl_b32 s22, s23, 4\n"
      ".Lsl_dy3_28:\n"
      "ds_read_b32 v15, v17\n"
      "s_waitcnt lgkmcnt(0)\n"
      "v_cmp_gt_i32 vcc, v14, v15\n"
      "s_cbranch_vccz .Lbk_dy3_28\n"
      "s_sub_u32 s22, s22, 1\n"
      "s_cbranch_scc0 .Lsl_dy3_28\n"
      "s_mov_b32 s23, 0\n"
      "s_branch .Lbk_dy3_28\n"
      ".Lsp_d4_29:\n"
      "s_mov_b32 s22, s23\n"
      ".Lsl_d4_29:\n"
      "buffer_load_dword v20, v2, s[8:11], s51 offen sc1\n"
      "s_waitcnt vmcnt(0)\n"
      "v_cmp_ne_u32 vcc, s21, v20\n"
      "s_cbranch_vccz .Lbk_d4_29\n"
      "s_sleep 4\n"
      "s_sub_u32 s22, s22, 1\n"
      "s_cbranch_scc0 .Lsl_d4_29\n"
      "s_mov_b32 s23, 0\n"
      "s_branch .Lbk_d4_29\n"
      ".Lsp_d5_30:\n"
      "s_mov_b32 s22, s23\n"
      ".Lsl_d5_30:\n"
      "buffer_load_dword v20, v2, s[8:11], s51 offen sc1\n"
      "s_waitcnt vmcnt(0)\n"
      "v_cmp_ne_u32 vcc, s21, v20\n"
      "s_cbranch_vccz .Lbk_d5_30\n"
      "s_sleep 4\n"
      "s_sub_u32 s22, s22, 1\n"
      "s_cbranch_scc0 .Lsl_d5_30\n"
      "s_mov_b32 s23, 0\n"
      "s_branch .Lbk_d5_30\n"
      ".Lsp_dy4_31:\n"
      "s_lshl_b32 s22, s23, 4\n"
      ".Lsl_dy4_31:\n"
      "ds_read_b32 v15, v17\n"
      "s_waitcnt lgkmcnt(0)\n"
      "v_cmp_gt_i32 vcc, v14, v15\n"
      "s_cbranch_vccz .Lbk_dy4_31\n"
      "s_sub_u32 s22, s22, 1\n"
      "s_cbranch_scc0 .Lsl_dy4_31\n"
      "s_mov_b32 s23, 0\n"
      "s_branch .Lbk_dy4_31\n"
      ".Lsp_d5_32:\n"
      "s_mov_b32 s22, s23\n"
      ".Lsl_d5_32:\n"
      "buffer_load_dword v20, v2, s[8:11], s51 offen sc1\n"
      "s_waitcnt vmcnt(0)\n"
      "v_cmp_ne_u32 vcc, s21, v20\n"
      "s_cbranch_vccz .Lbk_d5_32\n"
      "s_sleep 4\n"
      "s_sub_u32 s22, s22, 1\n"
      "s_cbranch_scc0 .Lsl_d5_32\n"
      "s_mov_b32 s23, 0\n"
      "s_branch .Lbk_d5_32\n"
      ".Lsp_d0_33:\n"
      "s_mov_b32 s22, s23\n"
      ".Lsl_d0_33:\n"
      "buffer_load_dword v20, v2, s[8:11], s51 offen sc1\n"
      "s_waitcnt vmcnt(0)\n"
      "v_cmp_ne_u32 vcc, s21, v20\n"
      "s_cbranch_vccz .Lbk_d0_33\n"
      "s_sleep 4\n"
      "s_sub_u32 s22, s22, 1\n"
      "s_cbranch_scc0 .Lsl_d0_33\n"
      "s_mov_b32 s23, 0\n"
      "s_branch .Lbk_d0_33\n"
      ".Lsp_dy5_34:\n"
      "s_lshl_b32 s22, s23, 4\n"
      ".Lsl_dy5_34:\n"
      "ds_read_b32 v15, v17\n"
      "s_waitcnt lgkmcnt(0)\n"
      "v_cmp_gt_i32 vcc, v14, v15\n"
      "s_cbranch_vccz .Lbk_dy5_34\n"
      "s_sub_u32 s22, s22, 1\n"
      "s_cbranch_scc0 .Lsl_dy5_34\n"
      "s_mov_b32 s23, 0\n"
      "s_branch .Lbk_dy5_34\n"
      ".Ld_last:\n"
      :
      : "{s[8:11]}"(rsWS), "{s[12:15]}"(rsA), "{s[16:19]}"(rsB), "{s[92:95]}"(rsC), "{v0}"(v_l16), "{v1}"(v_l16b), "{v2}"(v_fa), "{v3}"(v_ga), "{v4}"(v_ra), "{v5}"(v_rf), "{v6}"(v_xp), "{v7}"(v_xs), "{v8}"(v_op), "{v9}"(v_os), "{v10}"(v_sw), "{v11}"(v_sr), "{v12}"(v_ss), "{v13}"(v_l8), "{v15}"(v_al), "{v16}"(v_yw), "{v17}"(v_yr), "{v19}"(v_hi), "{v176}"(v_at), "{v177}"(v_as), "{v178}"(v_vo0), "{v179}"(v_vo1), "{v180}"(v_vo2), "{v181}"(v_pw), "{v183}"(v_pu), "{s36}"(s_sh), "{s37}"(s_ps), "{s38}"(s_x1), "{s39}"(s_xr), "{s40}"(s_or), "{s41}"(s_p0), "{s42}"(s_m3), "{s43}"(s_m6), "{s44}"(s_so), "{s45}"(s_ph), "{s46}"(s_i0), "{s47}"(s_i5), "{s48}"(s_j0), "{s49}"(s_j5), "{s50}"(s_he), "{s60}"(s_k0)
      : "v255", "a255", "s99", "vcc", "scc", "memory");

  } else {
    const unsigned e1 = (e - 1) & 3, gr = !fast ? slot & 3 : slot < 31 ? (e + slot - 27) & 3 : e1, part = !fast ? 8 * e + (slot >> 2) : slot < 31 ? 32 + e : 32 + e1;
    COMMON(112, 81920);
    const unsigned j = part, f = 4 * j + w;
    const u32x4 rsA = srd(w >> 1 ? Wu : Wg, 576 * 4608 * 2), rsC = srd(Wo, 576 * 576 * 2);
    const unsigned tw0 = !fast && slot < 4 ? ((e * 16 + slot * 4 + w) * 81 + lane) * 128 : OOB, v_tw[2] = {tw0, lane < 17 ? tw0 + 8192 : OOB};
    const unsigned v_l5 = w < 3 ? v_l16 + 16384 - w * 3072 : OOB;
    const unsigned v_fa = 19456 - w * 4096 + (lane >> 3) * 128 + (lane & 7) * 4, v_ra = up * 8 * HREC + l5 + 147456 - f * 1024 + f * 128;
    const unsigned one = q == g ? 0x3f80u << (16 * (p & 1)) : 0u, v_id[2] = {p < 2 ? one : 0u, p < 2 ? 0u : one};
    const unsigned v_st = 18432 + i16 * 32 + q * 8, v_pw = 147456 - f * 1024 + f * 128 + l8 * HREC + l7;
    const unsigned v_at = 65536 + (8 * g + q) * 256 + ((4 * w + p) ^ (4 * q)) * 16;
    const unsigned v_vo0 = (16 * (w & 1) + g) * 9216 + 256 * j + (i16 ^ (4 * g)) * 16;
    const unsigned v_vo1 = v_vo0 + 4 * 9216 - 1024, v_vo2 = v_vo1 + 4 * 9216 - 1024, v_vo3 = v_vo2 + 4 * 9216 - 1024;
    const unsigned s_sx = WS_X1 + gr * NREC * REC + w * 4096, s_sh = WS_H + gr * NREC * HREC + f * 1024;
    const unsigned s_m4 = w * 4096, s_m5 = 16384 + w * 1024;
    asm volatile(
      "s_buffer_load_dword s21, s[8:11], 0\n"
      "s_mov_b32 s23, 65536\n"
      "s_mov_b32 s31, 0x40000000\n"
      "s_mov_b32 s41, 0\n"
      "s_sleep 16\n"
      "buffer_load_dword v58, v54, s[92:95], s41 offen\n"
      "buffer_load_dword v59, v55, s[92:95], s41 offen\n"
      "s_waitcnt vmcnt(0)\n"
      "s_add_u32 m0, s38, 65536\n"
      "s_nop 0\n"
      "buffer_load_dwordx4 v6, s[12:15], s41 offen offset:0 lds\n"
      "buffer_load_dwordx4 v7, s[12:15], s41 offen offset:1024 lds\n"
      "buffer_load_dwordx4 v8, s[12:15], s41 offen offset:2048 lds\n"
      "buffer_load_dwordx4 v9, s[12:15], s41 offen offset:3072 lds\n"
      "s_add_u32 s41, s41, 294912\n"
      "s_add_u32 m0, s38, 81920\n"
      "s_nop 0\n"
      "buffer_load_dwordx4 v6, s[12:15], s41 offen offset:0 lds\n"
      "buffer_load_dwordx4 v7, s[12:15], s41 offen offset:1024 lds\n"
      "buffer_load_dwordx4 v8, s[12:15], s41 offen offset:2048 lds\n"
      "buffer_load_dwordx4 v9, s[12:15], s41 offen offset:3072 lds\n"
      "s_add_u32 s41, s41, 294912\n"
      "s_add_u32 m0, s38, 98304\n"
      "s_nop 0\n"
      "buffer_load_dwordx4 v6, s[12:15], s41 offen offset:0 lds\n"
      "buffer_load_dwordx4 v7, s[12:15], s41 offen offset:1024 lds\n"
      "buffer_load_dwordx4 v8, s[12:15], s41 offen offset:2048 lds\n"
      "buffer_load_dwordx4 v9, s[12:15], s41 offen offset:3072 lds\n"
      "s_add_u32 s41, s41, 294912\n"
      "s_add_u32 m0, s38, 114688\n"
      "s_nop 0\n"
      "buffer_load_dwordx4 v6, s[12:15], s41 offen offset:0 lds\n"
      "buffer_load_dwordx4 v7, s[12:15], s41 offen offset:1024 lds\n"
      "buffer_load_dwordx4 v8, s[12:15], s41 offen offset:2048 lds\n"
      "buffer_load_dwordx4 v9, s[12:15], s41 offen offset:3072 lds\n"
      "s_add_u32 s41, s41, 294912\n"
      "s_add_u32 m0, s38, 131072\n"
      "s_nop 0\n"
      "buffer_load_dwordx4 v6, s[12:15], s41 offen offset:0 lds\n"
      "buffer_load_dwordx4 v7, s[12:15], s41 offen offset:1024 lds\n"
      "buffer_load_dwordx4 v8, s[12:15], s41 offen offset:2048 lds\n"
      "buffer_load_dwordx4 v9, s[12:15], s41 offen offset:3072 lds\n"
      "s_add_u32 s41, s41, 294912\n"
      "v_mov_b32 v172, 0x3f803f80\n"
      "v_mov_b32 v173, 0x3f803f80\n"
      "v_mov_b32 v174, 0x3f803f80\n"
      "v_mov_b32 v175, 0x3f803f80\n"
      "v_add_u32 v19, 49152, v5\n"
      "s_mov_b64 exec, 0\n"
      "s_lshr_b32 s22, s38, 12\n"
      ".Lg_pl:\n"
      "s_cmp_eq_u32 s22, 0\n"
      "s_cbranch_scc1 .Lg_p0\n"
      "s_cmp_eq_u32 s22, 1\n"
      "s_cbranch_scc1 .Lg_p1\n"
      "s_branch .Lg_pe\n"
      ".Lg_pr:\n"
      "s_add_u32 s22, s22, 4\n"
      "s_branch .Lg_pl\n"
      ".Lg_pe:\n"
      "s_mov_b64 exec, -1\n"
      "s_waitcnt vmcnt(16)\n"
      "s_barrier\n"
      "ds_read_b64_tr_b16 a[0:1], v5 offset:0\n"
      "ds_read_b64_tr_b16 a[2:3], v5 offset:1024\n"
      "ds_read_b64_tr_b16 a[72:73], v5 offset:8192\n"
      "ds_read_b64_tr_b16 a[74:75], v5 offset:9216\n"
      "ds_read_b64_tr_b16 a[144:145], v5 offset:8\n"
      "ds_read_b64_tr_b16 a[146:147], v5 offset:1032\n"
      "ds_read_b64_tr_b16 a[216:217], v5 offset:8200\n"
      "ds_read_b64_tr_b16 a[218:219], v5 offset:9224\n"
      "s_add_u32 m0, s38, 147456\n"
      "s_nop 0\n"
      "buffer_load_dwordx4 v6, s[12:15], s41 offen offset:0 lds\n"
      "buffer_load_dwordx4 v7, s[12:15], s41 offen offset:1024 lds\n"
      "buffer_load_dwordx4 v8, s[12:15], s41 offen offset:2048 lds\n"
      "buffer_load_dwordx4 v9, s[12:15], s41 offen offset:3072 lds\n"
      "s_add_u32 s41, s41, 294912\n"
      "s_waitcnt vmcnt(16) lgkmcnt(0)\n"
      "s_barrier\n"
      "ds_read_b64_tr_b16 a[4:5], v5 offset:16384\n"
      "ds_read_b64_tr_b16 a[6:7], v5 offset:17408\n"
      "ds_read_b64_tr_b16 a[76:77], v5 offset:24576\n"
      "ds_read_b64_tr_b16 a[78:79], v5 offset:25600\n"
      "ds_read_b64_tr_b16 a[148:149], v5 offset:16392\n"
      "ds_read_b64_tr_b16 a[150:151], v5 offset:17416\n"
      "ds_read_b64_tr_b16 a[220:221], v5 offset:24584\n"
      "ds_read_b64_tr_b16 a[222:223], v5 offset:25608\n"
      "v_mfma_f32_16x16x32_bf16 v[76:79], v[172:175], a[0:3], 0\n"
      "v_mfma_f32_16x16x32_bf16 v[80:83], v[172:175], a[72:75], 0\n"
      "v_mfma_f32_16x16x32_bf16 v[84:87], v[172:175], a[144:147], 0\n"
      "v_mfma_f32_16x16x32_bf16 v[88:91], v[172:175], a[216:219], 0\n"
      "s_add_u32 m0, s38, 65536\n"
      "s_nop 0\n"
      "buffer_load_dwordx4 v6, s[12:15], s41 offen offset:0 lds\n"
      "buffer_load_dwordx4 v7, s[12:15], s41 offen offset:1024 lds\n"
      "buffer_load_dwordx4 v8, s[12:15], s41 offen offset:2048 lds\n"
      "buffer_load_dwordx4 v9, s[12:15], s41 offen offset:3072 lds\n"
      "s_add_u32 s41, s41, 294912\n"
      "s_waitcnt vmcnt(16) lgkmcnt(0)\n"
      "s_barrier\n"
      "ds_read_b64_tr_b16 a[8:9], v5 offset:32768\n"
      "ds_read_b64_tr_b16 a[10:11], v5 offset:33792\n"
      "ds_read_b64_tr_b16 a[80:81], v5 offset:40960\n"
      "ds_read_b64_tr_b16 a[82:83], v5 offset:41984\n"
      "ds_read_b64_tr_b16 a[152:153], v5 offset:32776\n"
      "ds_read_b64_tr_b16 a[154:155], v5 offset:33800\n"
      "ds_read_b64_tr_b16 a[224:225], v5 offset:40968\n"
      "ds_read_b64_tr_b16 a[226:227], v5 offset:41992\n"
      "v_mfma_f32_16x16x32_bf16 v[76:79], v[172:175], a[4:7], v[76:79]\n"
      "v_mfma_f32_16x16x32_bf16 v[80:83], v[172:175], a[76:79], v[80:83]\n"
      "v_mfma_f32_16x16x32_bf16 v[84:87], v[172:175], a[148:151], v[84:87]\n"
      "v_mfma_f32_16x16x32_bf16 v[88:91], v[172:175], a[220:223], v[88:91]\n"
      "s_add_u32 m0, s38, 81920\n"
      "s_nop 0\n"
      "buffer_load_dwordx4 v6, s[12:15], s41 offen offset:0 lds\n"
      "buffer_load_dwordx4 v7, s[12:15], s41 offen offset:1024 lds\n"
      "buffer_load_dwordx4 v8, s[12:15], s41 offen offset:2048 lds\n"
      "buffer_load_dwordx4 v9, s[12:15], s41 offen offset:3072 lds\n"
      "s_add_u32 s41, s41, 294912\n"
      "s_waitcnt vmcnt(16) lgkmcnt(0)\n"
      "s_barrier\n"
      "ds_read_b64_tr_b16 a[12:13], v19 offset:0\n"
      "ds_read_b64_tr_b16 a[14:15], v19 offset:1024\n"
      "ds_read_b64_tr_b16 a[84:85], v19 offset:8192\n"
      "ds_read_b64_tr_b16 a[86:87], v19 offset:9216\n"
      "ds_read_b64_tr_b16 a[156:157], v19 offset:8\n"
      "ds_read_b64_tr_b16 a[158:159], v19 offset:1032\n"
      "ds_read_b64_tr_b16 a[228:229], v19 offset:8200\n"
      "ds_read_b64_tr_b16 a[230:231], v19 offset:9224\n"
      "v_mfma_f32_16x16x32_bf16 v[76:79], v[172:175], a[8:11], v[76:79]\n"
      "v_mfma_f32_16x16x32_bf16 v[80:83], v[172:175], a[80:83], v[80:83]\n"
      "v_mfma_f32_16x16x32_bf16 v[84:87], v[172:175], a[152:155], v[84:87]\n"
      "v_mfma_f32_16x16x32_bf16 v[88:91], v[172:175], a[224:227], v[88:91]\n"
      "s_add_u32 m0, s38, 98304\n"
      "s_nop 0\n"
      "buffer_load_dwordx4 v6, s[12:15], s41 offen offset:0 lds\n"
      "buffer_load_dwordx4 v7, s[12:15], s41 offen offset:1024 lds\n"
      "buffer_load_dwordx4 v8, s[12:15], s41 offen offset:2048 lds\n"
      "buffer_load_dwordx4 v9, s[12:15], s41 offen offset:3072 lds\n"
      "s_add_u32 s41, s41, 294912\n"
      "s_waitcnt vmcnt(16) lgkmcnt(0)\n"
      "s_barrier\n"
      "ds_read_b64_tr_b16 a[16:17], v19 offset:16384\n"
      "ds_read_b64_tr_b16 a[18:19], v19 offset:17408\n"
      "ds_read_b64_tr_b16 a[88:89], v19 offset:24576\n"
      "ds_read_b64_tr_b16 a[90:91], v19 offset:25600\n"
      "ds_read_b64_tr_b16 a[160:161], v19 offset:16392\n"
      "ds_read_b64_tr_b16 a[162:163], v19 offset:17416\n"
      "ds_read_b64_tr_b16 a[232:233], v19 offset:24584\n"
      "ds_read_b64_tr_b16 a[234:235], v19 offset:25608\n"
      "v_mfma_f32_16x16x32_bf16 v[76:79], v[172:175], a[12:15], v[76:79]\n"
      "v_mfma_f32_16x16x32_bf16 v[80:83], v[172:175], a[84:87], v[80:83]\n"
      "v_mfma_f32_16x16x32_bf16 v[84:87], v[172:175], a[156:159], v[84:87]\n"
      "v_mfma_f32_16x16x32_bf16 v[88:91], v[172:175], a[228:231], v[88:91]\n"
      "s_add_u32 m0, s38, 114688\n"
      "s_nop 0\n"
      "buffer_load_dwordx4 v6, s[12:15], s41 offen offset:0 lds\n"
      "buffer_load_dwordx4 v7, s[12:15], s41 offen offset:1024 lds\n"
      "buffer_load_dwordx4 v8, s[12:15], s41 offen offset:2048 lds\n"
      "buffer_load_dwordx4 v9, s[12:15], s41 offen offset:3072 lds\n"
      "s_add_u32 s41, s41, 294912\n"
      "s_waitcnt vmcnt(16) lgkmcnt(0)\n"
      "s_barrier\n"
      "ds_read_b64_tr_b16 a[20:21], v19 offset:32768\n"
      "ds_read_b64_tr_b16 a[22:23], v19 offset:33792\n"
      "ds_read_b64_tr_b16 a[92:93], v19 offset:40960\n"
      "ds_read_b64_tr_b16 a[94:95], v19 offset:41984\n"
      "ds_read_b64_tr_b16 a[164:165], v19 offset:32776\n"
      "ds_read_b64_tr_b16 a[166:167], v19 offset:33800\n"
      "ds_read_b64_tr_b16 a[236:237], v19 offset:40968\n"
      "ds_read_b64_tr_b16 a[238:239], v19 offset:41992\n"
      "v_mfma_f32_16x16x32_bf16 v[76:79], v[172:175], a[16:19], v[76:79]\n"
      "v_mfma_f32_16x16x32_bf16 v[80:83], v[172:175], a[88:91], v[80:83]\n"
      "v_mfma_f32_16x16x32_bf16 v[84:87], v[172:175], a[160:163], v[84:87]\n"
      "v_mfma_f32_16x16x32_bf16 v[88:91], v[172:175], a[232:235], v[88:91]\n"
      "s_add_u32 m0, s38, 131072\n"
      "s_nop 0\n"
      "buffer_load_dwordx4 v6, s[12:15], s41 offen offset:0 lds\n"
      "buffer_load_dwordx4 v7, s[12:15], s41 offen offset:1024 lds\n"
      "buffer_load_dwordx4 v8, s[12:15], s41 offen offset:2048 lds\n"
      "buffer_load_dwordx4 v9, s[12:15], s41 offen offset:3072 lds\n"
      "s_add_u32 s41, s41, 294912\n"
      "s_waitcnt vmcnt(16) lgkmcnt(0)\n"
      "s_barrier\n"
      "ds_read_b64_tr_b16 a[24:25], v5 offset:0\n"
      "ds_read_b64_tr_b16 a[26:27], v5 offset:1024\n"
      "ds_read_b64_tr_b16 a[96:97], v5 offset:8192\n"
      "ds_read_b64_tr_b16 a[98:99], v5 offset:9216\n"
      "ds_read_b64_tr_b16 a[168:169], v5 offset:8\n"
      "ds_read_b64_tr_b16 a[170:171], v5 offset:1032\n"
      "ds_read_b64_tr_b16 a[240:241], v5 offset:8200\n"
      "ds_read_b64_tr_b16 a[242:243], v5 offset:9224\n"
      "v_mfma_f32_16x16x32_bf16 v[76:79], v[172:175], a[20:23], v[76:79]\n"
      "v_mfma_f32_16x16x32_bf16 v[80:83], v[172:175], a[92:95], v[80:83]\n"
      "v_mfma_f32_16x16x32_bf16 v[84:87], v[172:175], a[164:167], v[84:87]\n"
      "v_mfma_f32_16x16x32_bf16 v[88:91], v[172:175], a[236:239], v[88:91]\n"
      "s_add_u32 m0, s38, 147456\n"
      "s_nop 0\n"
      "buffer_load_dwordx4 v6, s[12:15], s41 offen offset:0 lds\n"
      "buffer_load_dwordx4 v7, s[12:15], s41 offen offset:1024 lds\n"
      "buffer_load_dwordx4 v8, s[12:15], s41 offen offset:2048 lds\n"
      "buffer_load_dwordx4 v9, s[12:15], s41 offen offset:3072 lds\n"
      "s_add_u32 s41, s41, 294912\n"
      "s_waitcnt vmcnt(16) lgkmcnt(0)\n"
      "s_barrier\n"
      "ds_read_b64_tr_b16 a[28:29], v5 offset:16384\n"
      "ds_read_b64_tr_b16 a[30:31], v5 offset:17408\n"
      "ds_read_b64_tr_b16 a[100:101], v5 offset:24576\n"
      "ds_read_b64_tr_b16 a[102:103], v5 offset:25600\n"
      "ds_read_b64_tr_b16 a[172:173], v5 offset:16392\n"
      "ds_read_b64_tr_b16 a[174:175], v5 offset:17416\n"
      "ds_read_b64_tr_b16 a[244:245], v5 offset:24584\n"
      "ds_read_b64_tr_b16 a[246:247], v5 offset:25608\n"
      "v_mfma_f32_16x16x32_bf16 v[76:79], v[172:175], a[24:27], v[76:79]\n"
      "v_mfma_f32_16x16x32_bf16 v[80:83], v[172:175], a[96:99], v[80:83]\n"
      "v_mfma_f32_16x16x32_bf16 v[84:87], v[172:175], a[168:171], v[84:87]\n"
      "v_mfma_f32_16x16x32_bf16 v[88:91], v[172:175], a[240:243], v[88:91]\n"
      "s_add_u32 m0, s38, 65536\n"
      "s_nop 0\n"
      "buffer_load_dwordx4 v6, s[12:15], s41 offen offset:0 lds\n"
      "buffer_load_dwordx4 v7, s[12:15], s41 offen offset:1024 lds\n"
      "buffer_load_dwordx4 v8, s[12:15], s41 offen offset:2048 lds\n"
      "buffer_load_dwordx4 v9, s[12:15], s41 offen offset:3072 lds\n"
      "s_add_u32 s41, s41, 294912\n"
      "s_waitcnt vmcnt(16) lgkmcnt(0)\n"
      "s_barrier\n"
      "ds_read_b64_tr_b16 a[32:33], v5 offset:32768\n"
      "ds_read_b64_tr_b16 a[34:35], v5 offset:33792\n"
      "ds_read_b64_tr_b16 a[104:105], v5 offset:40960\n"
      "ds_read_b64_tr_b16 a[106:107], v5 offset:41984\n"
      "ds_read_b64_tr_b16 a[176:177], v5 offset:32776\n"
      "ds_read_b64_tr_b16 a[178:179], v5 offset:33800\n"
      "ds_read_b64_tr_b16 a[248:249], v5 offset:40968\n"
      "ds_read_b64_tr_b16 a[250:251], v5 offset:41992\n"
      "v_mfma_f32_16x16x32_bf16 v[76:79], v[172:175], a[28:31], v[76:79]\n"
      "v_mfma_f32_16x16x32_bf16 v[80:83], v[172:175], a[100:103], v[80:83]\n"
      "v_mfma_f32_16x16x32_bf16 v[84:87], v[172:175], a[172:175], v[84:87]\n"
      "v_mfma_f32_16x16x32_bf16 v[88:91], v[172:175], a[244:247], v[88:91]\n"
      "s_add_u32 m0, s38, 81920\n"
      "s_nop 0\n"
      "buffer_load_dwordx4 v6, s[12:15], s41 offen offset:0 lds\n"
      "buffer_load_dwordx4 v7, s[12:15], s41 offen offset:1024 lds\n"
      "buffer_load_dwordx4 v8, s[12:15], s41 offen offset:2048 lds\n"
      "buffer_load_dwordx4 v9, s[12:15], s41 offen offset:3072 lds\n"
      "s_add_u32 s41, s41, 294912\n"
      "s_waitcnt vmcnt(16) lgkmcnt(0)\n"
      "s_barrier\n"
      "ds_read_b64_tr_b16 a[36:37], v19 offset:0\n"
      "ds_read_b64_tr_b16 a[38:39], v19 offset:1024\n"
      "ds_read_b64_tr_b16 a[108:109], v19 offset:8192\n"
      "ds_read_b64_tr_b16 a[110:111], v19 offset:9216\n"
      "ds_read_b64_tr_b16 a[180:181], v19 offset:8\n"
      "ds_read_b64_tr_b16 a[182:183], v19 offset:1032\n"
      "ds_read_b64_tr_b16 a[252:253], v19 offset:8200\n"
      "ds_read_b64_tr_b16 a[254:255], v19 offset:9224\n"
      "v_mfma_f32_16x16x32_bf16 v[76:79], v[172:175], a[32:35], v[76:79]\n"
      "v_mfma_f32_16x16x32_bf16 v[80:83], v[172:175], a[104:107], v[80:83]\n"
      "v_mfma_f32_16x16x32_bf16 v[84:87], v[172:175], a[176:179], v[84:87]\n"
      "v_mfma_f32_16x16x32_bf16 v[88:91], v[172:175], a[248:251], v[88:91]\n"
      "s_add_u32 m0, s38, 98304\n"
      "s_nop 0\n"
      "buffer_load_dwordx4 v6, s[12:15], s41 offen offset:0 lds\n"
      "buffer_load_dwordx4 v7, s[12:15], s41 offen offset:1024 lds\n"
      "buffer_load_dwordx4 v8, s[12:15], s41 offen offset:2048 lds\n"
      "buffer_load_dwordx4 v9, s[12:15], s41 offen offset:3072 lds\n"
      "s_add_u32 s41, s41, 294912\n"
      "s_waitcnt vmcnt(16) lgkmcnt(0)\n"
      "s_barrier\n"
      "ds_read_b64_tr_b16 a[40:41], v19 offset:16384\n"
      "ds_read_b64_tr_b16 a[42:43], v19 offset:17408\n"
      "ds_read_b64_tr_b16 a[112:113], v19 offset:24576\n"
      "ds_read_b64_tr_b16 a[114:115], v19 offset:25600\n"
      "ds_read_b64_tr_b16 a[184:185], v19 offset:16392\n"
      "ds_read_b64_tr_b16 a[186:187], v19 offset:17416\n"
      "ds_read_b64_tr_b16 v[20:21], v19 offset:24584\n"
      "ds_read_b64_tr_b16 v[22:23], v19 offset:25608\n"
      "v_mfma_f32_16x16x32_bf16 v[76:79], v[172:175], a[36:39], v[76:79]\n"
      "v_mfma_f32_16x16x32_bf16 v[80:83], v[172:175], a[108:111], v[80:83]\n"
      "v_mfma_f32_16x16x32_bf16 v[84:87], v[172:175], a[180:183], v[84:87]\n"
      "v_mfma_f32_16x16x32_bf16 v[88:91], v[172:175], a[252:255], v[88:91]\n"
      "s_add_u32 m0, s38, 114688\n"
      "s_nop 0\n"
      "buffer_load_dwordx4 v6, s[12:15], s41 offen offset:0 lds\n"
      "buffer_load_dwordx4 v7, s[12:15], s41 offen offset:1024 lds\n"
      "buffer_load_dwordx4 v8, s[12:15], s41 offen offset:2048 lds\n"
      "buffer_load_dwordx4 v9, s[12:15], s41 offen offset:3072 lds\n"
      "s_add_u32 s41, s41, 294912\n"
      "s_waitcnt vmcnt(16) lgkmcnt(0)\n"
      "s_barrier\n"
      "ds_read_b64_tr_b16 a[44:45], v19 offset:32768\n"
      "ds_read_b64_tr_b16 a[46:47], v19 offset:33792\n"
      "ds_read_b64_tr_b16 a[116:117], v19 offset:40960\n"
      "ds_read_b64_tr_b16 a[118:119], v19 offset:41984\n"
      "ds_read_b64_tr_b16 a[188:189], v19 offset:32776\n"
      "ds_read_b64_tr_b16 a[190:191], v19 offset:33800\n"
      "ds_read_b64_tr_b16 v[24:25], v19 offset:40968\n"
      "ds_read_b64_tr_b16 v[26:27], v19 offset:41992\n"
      "v_mfma_f32_16x16x32_bf16 v[92:95], v[172:175], a[40:43], v[76:79]\n"
      "v_mfma_f32_16x16x32_bf16 v[96:99], v[172:175], a[112:115], v[80:83]\n"
      "v_mfma_f32_16x16x32_bf16 v[100:103], v[172:175], a[184:187], v[84:87]\n"
      "v_mfma_f32_16x16x32_bf16 v[104:107], v[172:175], v[20:23], v[88:91]\n"
      "s_add_u32 m0, s38, 131072\n"
      "s_nop 0\n"
      "buffer_load_dwordx4 v6, s[12:15], s41 offen offset:0 lds\n"
      "buffer_load_dwordx4 v7, s[12:15], s41 offen offset:1024 lds\n"
      "buffer_load_dwordx4 v8, s[12:15], s41 offen offset:2048 lds\n"
      "buffer_load_dwordx4 v9, s[12:15], s41 offen offset:3072 lds\n"
      "s_add_u32 s41, s41, 294912\n"
      "s_waitcnt vmcnt(16) lgkmcnt(0)\n"
      "s_barrier\n"
      "ds_read_b64_tr_b16 a[48:49], v5 offset:0\n"
      "ds_read_b64_tr_b16 a[50:51], v5 offset:1024\n"
      "ds_read_b64_tr_b16 a[120:121], v5 offset:8192\n"
      "ds_read_b64_tr_b16 a[122:123], v5 offset:9216\n"
      "ds_read_b64_tr_b16 a[192:193], v5 offset:8\n"
      "ds_read_b64_tr_b16 a[194:195], v5 offset:1032\n"
      "ds_read_b64_tr_b16 v[28:29], v5 offset:8200\n"
      "ds_read_b64_tr_b16 v[30:31], v5 offset:9224\n"
      "v_mfma_f32_16x16x32_bf16 v[92:95], v[172:175], a[44:47], v[92:95]\n"
      "v_mfma_f32_16x16x32_bf16 v[96:99], v[172:175], a[116:119], v[96:99]\n"
      "v_mfma_f32_16x16x32_bf16 v[100:103], v[172:175], a[188:191], v[100:103]\n"
      "v_mfma_f32_16x16x32_bf16 v[104:107], v[172:175], v[24:27], v[104:107]\n"
      "s_add_u32 m0, s38, 147456\n"
      "s_nop 0\n"
      "buffer_load_dwordx4 v6, s[12:15], s41 offen offset:0 lds\n"
      "buffer_load_dwordx4 v7, s[12:15], s41 offen offset:1024 lds\n"
      "buffer_load_dwordx4 v8, s[12:15], s41 offen offset:2048 lds\n"
      "buffer_load_dwordx4 v9, s[12:15], s41 offen offset:3072 lds\n"
      "s_add_u32 s41, s41, 294912\n"
      "s_waitcnt vmcnt(16) lgkmcnt(0)\n"
      "s_barrier\n"
      "ds_read_b64_tr_b16 a[52:53], v5 offset:16384\n"
      "ds_read_b64_tr_b16 a[54:55], v5 offset:17408\n"
      "ds_read_b64_tr_b16 a[124:125], v5 offset:24576\n"
      "ds_read_b64_tr_b16 a[126:127], v5 offset:25600\n"
      "ds_read_b64_tr_b16 a[196:197], v5 offset:16392\n"
      "ds_read_b64_tr_b16 a[198:199], v5 offset:17416\n"
      "ds_read_b64_tr_b16 v[32:33], v5 offset:24584\n"
      "ds_read_b64_tr_b16 v[34:35], v5 offset:25608\n"
      "v_mfma_f32_16x16x32_bf16 v[92:95], v[172:175], a[48:51], v[92:95]\n"
      "v_mfma_f32_16x16x32_bf16 v[96:99], v[172:175], a[120:123], v[96:99]\n"
      "v_mfma_f32_16x16x32_bf16 v[100:103], v[172:175], a[192:195], v[100:103]\n"
      "v_mfma_f32_16x16x32_bf16 v[104:107], v[172:175], v[28:31], v[104:107]\n"
      "s_waitcnt vmcnt(12) lgkmcnt(0)\n"
      "s_barrier\n"
      "ds_read_b64_tr_b16 a[56:57], v5 offset:32768\n"
      "ds_read_b64_tr_b16 a[58:59], v5 offset:33792\n"
      "ds_read_b64_tr_b16 a[128:129], v5 offset:40960\n"
      "ds_read_b64_tr_b16 a[130:131], v5 offset:41984\n"
      "ds_read_b64_tr_b16 a[200:201], v5 offset:32776\n"
      "ds_read_b64_tr_b16 a[202:203], v5 offset:33800\n"
      "ds_read_b64_tr_b16 v[36:37], v5 offset:40968\n"
      "ds_read_b64_tr_b16 v[38:39], v5 offset:41992\n"
      "v_mfma_f32_16x16x32_bf16 v[92:95], v[172:175], a[52:55], v[92:95]\n"
      "v_mfma_f32_16x16x32_bf16 v[96:99], v[172:175], a[124:127], v[96:99]\n"
      "v_mfma_f32_16x16x32_bf16 v[100:103], v[172:175], a[196:199], v[100:103]\n"
      "v_mfma_f32_16x16x32_bf16 v[104:107], v[172:175], v[32:35], v[104:107]\n"
      "s_waitcnt vmcnt(8) lgkmcnt(0)\n"
      "s_barrier\n"
      "ds_read_b64_tr_b16 a[60:61], v19 offset:0\n"
      "ds_read_b64_tr_b16 a[62:63], v19 offset:1024\n"
      "ds_read_b64_tr_b16 a[132:133], v19 offset:8192\n"
      "ds_read_b64_tr_b16 a[134:135], v19 offset:9216\n"
      "ds_read_b64_tr_b16 a[204:205], v19 offset:8\n"
      "ds_read_b64_tr_b16 a[206:207], v19 offset:1032\n"
      "ds_read_b64_tr_b16 v[40:41], v19 offset:8200\n"
      "ds_read_b64_tr_b16 v[42:43], v19 offset:9224\n"
      "v_mfma_f32_16x16x32_bf16 v[92:95], v[172:175], a[56:59], v[92:95]\n"
      "v_mfma_f32_16x16x32_bf16 v[96:99], v[172:175], a[128:131], v[96:99]\n"
      "v_mfma_f32_16x16x32_bf16 v[100:103], v[172:175], a[200:203], v[100:103]\n"
      "v_mfma_f32_16x16x32_bf16 v[104:107], v[172:175], v[36:39], v[104:107]\n"
      "s_waitcnt vmcnt(4) lgkmcnt(0)\n"
      "s_barrier\n"
      "ds_read_b64_tr_b16 a[64:65], v19 offset:16384\n"
      "ds_read_b64_tr_b16 a[66:67], v19 offset:17408\n"
      "ds_read_b64_tr_b16 a[136:137], v19 offset:24576\n"
      "ds_read_b64_tr_b16 a[138:139], v19 offset:25600\n"
      "ds_read_b64_tr_b16 a[208:209], v19 offset:16392\n"
      "ds_read_b64_tr_b16 a[210:211], v19 offset:17416\n"
      "ds_read_b64_tr_b16 v[44:45], v19 offset:24584\n"
      "ds_read_b64_tr_b16 v[46:47], v19 offset:25608\n"
      "v_mfma_f32_16x16x32_bf16 v[108:111], v[172:175], a[60:63], v[92:95]\n"
      "v_mfma_f32_16x16x32_bf16 v[112:115], v[172:175], a[132:135], v[96:99]\n"
      "v_mfma_f32_16x16x32_bf16 v[116:119], v[172:175], a[204:207], v[100:103]\n"
      "v_mfma_f32_16x16x32_bf16 v[120:123], v[172:175], v[40:43], v[104:107]\n"
      "s_waitcnt vmcnt(0) lgkmcnt(0)\n"
      "s_barrier\n"
      "ds_read_b64_tr_b16 a[68:69], v19 offset:32768\n"
      "ds_read_b64_tr_b16 a[70:71], v19 offset:33792\n"
      "ds_read_b64_tr_b16 a[140:141], v19 offset:40960\n"
      "ds_read_b64_tr_b16 a[142:143], v19 offset:41984\n"
      "ds_read_b64_tr_b16 a[212:213], v19 offset:32776\n"
      "ds_read_b64_tr_b16 a[214:215], v19 offset:33800\n"
      "ds_read_b64_tr_b16 v[48:49], v19 offset:40968\n"
      "ds_read_b64_tr_b16 v[50:51], v19 offset:41992\n"
      "v_mfma_f32_16x16x32_bf16 v[108:111], v[172:175], a[64:67], v[108:111]\n"
      "v_mfma_f32_16x16x32_bf16 v[112:115], v[172:175], a[136:139], v[112:115]\n"
      "v_mfma_f32_16x16x32_bf16 v[116:119], v[172:175], a[208:211], v[116:119]\n"
      "v_mfma_f32_16x16x32_bf16 v[120:123], v[172:175], v[44:47], v[120:123]\n"
      "s_waitcnt lgkmcnt(0)\n"
      "v_mfma_f32_16x16x32_bf16 v[140:143], v[172:175], a[68:71], v[108:111]\n"
      "v_mfma_f32_16x16x32_bf16 v[144:147], v[172:175], a[140:143], v[112:115]\n"
      "v_mfma_f32_16x16x32_bf16 v[148:151], v[172:175], a[212:215], v[116:119]\n"
      "v_mfma_f32_16x16x32_bf16 v[152:155], v[172:175], v[48:51], v[120:123]\n"
      "s_getpc_b64 vcc\n"
      ".Lg_ct:\n"
      "s_add_u32 vcc_lo, vcc_lo, .Lg_p0-.Lg_ct\n"
      "s_addc_u32 vcc_hi, vcc_hi, 0\n"
      "s_lshl_b32 s30, s38, 1\n"
      "v_lshl_add_u32 v60, v0, 3, s30\n"
      "v_min_u32 v60, .Lg_last-.Lg_p0-4, v60\n"
      "global_load_dword v60, v60, vcc\n"
      "s_mul_i32 s21, s21, 1664525\n"
      "s_add_u32 s21, s21, 1013904223\n"
      "v_mov_b32 v15, s21\n"
      "buffer_store_dword v15, v12, s[8:11], 0 offen sc1\n"
      "v_not_b32 v52, v15\n"
      "v_not_b32 v53, v15\n"
      "v_not_b32 v54, v15\n"
      "v_not_b32 v55, v15\n"
      "s_add_u32 s30, s37, 0\n"
      "buffer_store_dwordx4 v[52:55], v10, s[8:11], s30 offen sc1\n"
      "v_xor_b32 v15, v15, v16\n"
      "v_mov_b32 v66, 0\n"
      "v_mov_b32 v67, 0\n"
      "v_mov_b32 v68, 0\n"
      "v_mov_b32 v69, 0\n"
      "v_mov_b32 v70, v64\n"
      "v_mov_b32 v71, v65\n"
      "v_sub_f32 v156, v140, v76\n"
      "v_sub_f32 v157, v144, v80\n"
      "v_sub_f32 v158, v148, v84\n"
      "v_sub_f32 v159, v152, v88\n"
      "v_sub_f32 v160, v140, v92\n"
      "v_sub_f32 v161, v144, v96\n"
      "v_sub_f32 v162, v148, v100\n"
      "v_sub_f32 v163, v152, v104\n"
      "v_sub_f32 v164, v140, v108\n"
      "v_sub_f32 v165, v144, v112\n"
      "v_sub_f32 v166, v148, v116\n"
      "v_sub_f32 v167, v152, v120\n"
      "v_mul_f32 v140, 0xbae38e39, v140\n"
      "v_mul_f32 v141, 0xbae38e39, v141\n"
      "v_mul_f32 v142, 0xbae38e39, v142\n"
      "v_mul_f32 v143, 0xbae38e39, v143\n"
      "v_mul_f32 v144, 0xbae38e39, v144\n"
      "v_mul_f32 v145, 0xbae38e39, v145\n"
      "v_mul_f32 v146, 0xbae38e39, v146\n"
      "v_mul_f32 v147, 0xbae38e39, v147\n"
      "v_mul_f32 v148, 0xbae38e39, v148\n"
      "v_mul_f32 v149, 0xbae38e39, v149\n"
      "v_mul_f32 v150, 0xbae38e39, v150\n"
      "v_mul_f32 v151, 0xbae38e39, v151\n"
      "v_mul_f32 v152, 0xbae38e39, v152\n"
      "v_mul_f32 v153, 0xbae38e39, v153\n"
      "v_mul_f32 v154, 0xbae38e39, v154\n"
      "v_mul_f32 v155, 0xbae38e39, v155\n"
      "v_mfma_f32_16x16x32_bf16 v[72:75], v[64:67], a[0:3], v[140:143]\n"
      "v_mfma_f32_16x16x32_bf16 v[76:79], v[68:71], a[0:3], v[140:143]\n"
      "v_mfma_f32_16x16x32_bf16 v[80:83], v[64:67], a[72:75], v[144:147]\n"
      "v_mfma_f32_16x16x32_bf16 v[84:87], v[68:71], a[72:75], v[144:147]\n"
      "v_mfma_f32_16x16x32_bf16 v[88:91], v[64:67], a[144:147], v[148:151]\n"
      "s_nop 4\n"
      "v_cvt_pk_bf16_f32 v72, v72, v73\n"
      "v_cvt_pk_bf16_f32 v73, v74, v75\n"
      "v_cvt_pk_bf16_f32 v74, v76, v77\n"
      "v_mfma_f32_16x16x32_bf16 v[92:95], v[68:71], a[144:147], v[148:151]\n"
      "v_cvt_pk_bf16_f32 v75, v78, v79\n"
      "v_accvgpr_write_b32 a0, v72\n"
      "v_accvgpr_write_b32 a1, v73\n"
      "v_accvgpr_write_b32 a2, v74\n"
      "v_accvgpr_write_b32 a3, v75\n"
      "v_mfma_f32_16x16x32_bf16 v[96:99], v[64:67], a[216:219], v[152:155]\n"
      "v_cvt_pk_bf16_f32 v80, v80, v81\n"
      "v_cvt_pk_bf16_f32 v81, v82, v83\n"
      "v_cvt_pk_bf16_f32 v82, v84, v85\n"
      "v_mfma_f32_16x16x32_bf16 v[100:103], v[68:71], a[216:219], v[152:155]\n"
      "v_cvt_pk_bf16_f32 v83, v86, v87\n"
      "v_accvgpr_write_b32 a72, v80\n"
      "v_accvgpr_write_b32 a73, v81\n"
      "v_mfma_f32_16x16x32_bf16 v[124:127], v[172:175], a[0:3], 0\n"
      "v_accvgpr_write_b32 a74, v82\n"
      "v_accvgpr_write_b32 a75, v83\n"
      "v_mfma_f32_16x16x32_bf16 v[72:75], v[64:67], a[4:7], v[140:143]\n"
      "v_cvt_pk_bf16_f32 v88, v88, v89\n"
      "v_cvt_pk_bf16_f32 v89, v90, v91\n"
      "v_cvt_pk_bf16_f32 v90, v92, v93\n"
      "v_mfma_f32_16x16x32_bf16 v[76:79], v[68:71], a[4:7], v[140:143]\n"
      "v_cvt_pk_bf16_f32 v91, v94, v95\n"
      "v_accvgpr_write_b32 a144, v88\n"
      "v_accvgpr_write_b32 a145, v89\n"
      "v_mfma_f32_16x16x32_bf16 v[128:131], v[172:175], a[72:75], 0\n"
      "v_accvgpr_write_b32 a146, v90\n"
      "v_accvgpr_write_b32 a147, v91\n"
      "v_mfma_f32_16x16x32_bf16 v[80:83], v[64:67], a[76:79], v[144:147]\n"
      "v_cvt_pk_bf16_f32 v96, v96, v97\n"
      "v_cvt_pk_bf16_f32 v97, v98, v99\n"
      "v_cvt_pk_bf16_f32 v98, v100, v101\n"
      "v_mfma_f32_16x16x32_bf16 v[84:87], v[68:71], a[76:79], v[144:147]\n"
      "v_cvt_pk_bf16_f32 v99, v102, v103\n"
      "v_accvgpr_write_b32 a216, v96\n"
      "v_accvgpr_write_b32 a217, v97\n"
      "v_mfma_f32_16x16x32_bf16 v[132:135], v[172:175], a[144:147], 0\n"
      "v_accvgpr_write_b32 a218, v98\n"
      "v_accvgpr_write_b32 a219, v99\n"
      "v_mfma_f32_16x16x32_bf16 v[88:91], v[64:67], a[148:151], v[148:151]\n"
      "v_cvt_pk_bf16_f32 v72, v72, v73\n"
      "v_cvt_pk_bf16_f32 v73, v74, v75\n"
      "v_cvt_pk_bf16_f32 v74, v76, v77\n"
      "v_mfma_f32_16x16x32_bf16 v[92:95], v[68:71], a[148:151], v[148:151]\n"
      "v_cvt_pk_bf16_f32 v75, v78, v79\n"
      "v_accvgpr_write_b32 a4, v72\n"
      "v_accvgpr_write_b32 a5, v73\n"
      "v_mfma_f32_16x16x32_bf16 v[136:139], v[172:175], a[216:219], 0\n"
      "v_accvgpr_write_b32 a6, v74\n"
      "v_accvgpr_write_b32 a7, v75\n"
      "v_mfma_f32_16x16x32_bf16 v[96:99], v[64:67], a[220:223], v[152:155]\n"
      "v_cvt_pk_bf16_f32 v80, v80, v81\n"
      "v_cvt_pk_bf16_f32 v81, v82, v83\n"
      "v_cvt_pk_bf16_f32 v82, v84, v85\n"
      "v_mfma_f32_16x16x32_bf16 v[100:103], v[68:71], a[220:223], v[152:155]\n"
      "v_cvt_pk_bf16_f32 v83, v86, v87\n"
      "v_accvgpr_write_b32 a76, v80\n"
      "v_accvgpr_write_b32 a77, v81\n"
      "v_mfma_f32_16x16x32_bf16 v[124:127], v[172:175], a[4:7], v[124:127]\n"
      "v_accvgpr_write_b32 a78, v82\n"
      "v_accvgpr_write_b32 a79, v83\n"
      "v_mfma_f32_16x16x32_bf16 v[72:75], v[64:67], a[8:11], v[140:143]\n"
      "v_cvt_pk_bf16_f32 v88, v88, v89\n"
      "v_cvt_pk_bf16_f32 v89, v90, v91\n"
      "v_cvt_pk_bf16_f32 v90, v92, v93\n"
      "v_mfma_f32_16x16x32_bf16 v[76:79], v[68:71], a[8:11], v[140:143]\n"
      "v_cvt_pk_bf16_f32 v91, v94, v95\n"
      "v_accvgpr_write_b32 a148, v88\n"
      "v_accvgpr_write_b32 a149, v89\n"
      "v_mfma_f32_16x16x32_bf16 v[128:131], v[172:175], a[76:79], v[128:131]\n"
      "v_accvgpr_write_b32 a150, v90\n"
      "v_accvgpr_write_b32 a151, v91\n"
      "v_mfma_f32_16x16x32_bf16 v[80:83], v[64:67], a[80:83], v[144:147]\n"
      "v_cvt_pk_bf16_f32 v96, v96, v97\n"
      "v_cvt_pk_bf16_f32 v97, v98, v99\n"
      "v_cvt_pk_bf16_f32 v98, v100, v101\n"
      "v_mfma_f32_16x16x32_bf16 v[84:87], v[68:71], a[80:83], v[144:147]\n"
      "v_cvt_pk_bf16_f32 v99, v102, v103\n"
      "v_accvgpr_write_b32 a220, v96\n"
      "v_accvgpr_write_b32 a221, v97\n"
      "v_mfma_f32_16x16x32_bf16 v[132:135], v[172:175], a[148:151], v[132:135]\n"
      "v_accvgpr_write_b32 a222, v98\n"
      "v_accvgpr_write_b32 a223, v99\n"
      "v_mfma_f32_16x16x32_bf16 v[88:91], v[64:67], a[152:155], v[148:151]\n"
      "v_cvt_pk_bf16_f32 v72, v72, v73\n"
      "v_cvt_pk_bf16_f32 v73, v74, v75\n"
      "v_cvt_pk_bf16_f32 v74, v76, v77\n"
      "v_mfma_f32_16x16x32_bf16 v[92:95], v[68:71], a[152:155], v[148:151]\n"
      "v_cvt_pk_bf16_f32 v75, v78, v79\n"
      "v_accvgpr_write_b32 a8, v72\n"
      "v_accvgpr_write_b32 a9, v73\n"
      "v_mfma_f32_16x16x32_bf16 v[136:139], v[172:175], a[220:223], v[136:139]\n"
      "v_accvgpr_write_b32 a10, v74\n"
      "v_accvgpr_write_b32 a11, v75\n"
      "v_mfma_f32_16x16x32_bf16 v[96:99], v[64:67], a[224:227], v[152:155]\n"
      "v_cvt_pk_bf16_f32 v80, v80, v81\n"
      "v_cvt_pk_bf16_f32 v81, v82, v83\n"
      "v_cvt_pk_bf16_f32 v82, v84, v85\n"
      "v_mfma_f32_16x16x32_bf16 v[100:103], v[68:71], a[224:227], v[152:155]\n"
      "v_cvt_pk_bf16_f32 v83, v86, v87\n"
      "v_accvgpr_write_b32 a80, v80\n"
      "v_accvgpr_write_b32 a81, v81\n"
      "v_mfma_f32_16x16x32_bf16 v[124:127], v[172:175], a[8:11], v[124:127]\n"
      "v_accvgpr_write_b32 a82, v82\n"
      "v_accvgpr_write_b32 a83, v83\n"
      "v_mfma_f32_16x16x32_bf16 v[72:75], v[64:67], a[12:15], v[140:143]\n"
      "v_cvt_pk_bf16_f32 v88, v88, v89\n"
      "v_cvt_pk_bf16_f32 v89, v90, v91\n"
      "v_cvt_pk_bf16_f32 v90, v92, v93\n"
      "v_mfma_f32_16x16x32_bf16 v[76:79], v[68:71], a[12:15], v[140:143]\n"
      "v_cvt_pk_bf16_f32 v91, v94, v95\n"
      "v_accvgpr_write_b32 a152, v88\n"
      "v_accvgpr_write_b32 a153, v89\n"
      "v_mfma_f32_16x16x32_bf16 v[128:131], v[172:175], a[80:83], v[128:131]\n"
      "v_accvgpr_write_b32 a154, v90\n"
      "v_accvgpr_write_b32 a155, v91\n"
      "v_mfma_f32_16x16x32_bf16 v[80:83], v[64:67], a[84:87], v[144:147]\n"
      "v_cvt_pk_bf16_f32 v96, v96, v97\n"
      "v_cvt_pk_bf16_f32 v97, v98, v99\n"
      "v_cvt_pk_bf16_f32 v98, v100, v101\n"
      "v_mfma_f32_16x16x32_bf16 v[84:87], v[68:71], a[84:87], v[144:147]\n"
      "v_cvt_pk_bf16_f32 v99, v102, v103\n"
      "v_accvgpr_write_b32 a224, v96\n"
      "v_accvgpr_write_b32 a225, v97\n"
      "v_mfma_f32_16x16x32_bf16 v[132:135], v[172:175], a[152:155], v[132:135]\n"
      "v_accvgpr_write_b32 a226, v98\n"
      "v_accvgpr_write_b32 a227, v99\n"
      "v_mfma_f32_16x16x32_bf16 v[88:91], v[64:67], a[156:159], v[148:151]\n"
      "v_cvt_pk_bf16_f32 v72, v72, v73\n"
      "v_cvt_pk_bf16_f32 v73, v74, v75\n"
      "v_cvt_pk_bf16_f32 v74, v76, v77\n"
      "v_mfma_f32_16x16x32_bf16 v[92:95], v[68:71], a[156:159], v[148:151]\n"
      "v_cvt_pk_bf16_f32 v75, v78, v79\n"
      "v_accvgpr_write_b32 a12, v72\n"
      "v_accvgpr_write_b32 a13, v73\n"
      "v_mfma_f32_16x16x32_bf16 v[136:139], v[172:175], a[224:227], v[136:139]\n"
      "v_accvgpr_write_b32 a14, v74\n"
      "v_accvgpr_write_b32 a15, v75\n"
      "v_mfma_f32_16x16x32_bf16 v[96:99], v[64:67], a[228:231], v[152:155]\n"
      "v_cvt_pk_bf16_f32 v80, v80, v81\n"
      "v_cvt_pk_bf16_f32 v81, v82, v83\n"
      "v_cvt_pk_bf16_f32 v82, v84, v85\n"
      "v_mfma_f32_16x16x32_bf16 v[100:103], v[68:71], a[228:231], v[152:155]\n"
      "v_cvt_pk_bf16_f32 v83, v86, v87\n"
      "v_accvgpr_write_b32 a84, v80\n"
      "v_accvgpr_write_b32 a85, v81\n"
      "v_mfma_f32_16x16x32_bf16 v[124:127], v[172:175], a[12:15], v[124:127]\n"
      "v_accvgpr_write_b32 a86, v82\n"
      "v_accvgpr_write_b32 a87, v83\n"
      "v_mfma_f32_16x16x32_bf16 v[72:75], v[64:67], a[16:19], v[140:143]\n"
      "v_cvt_pk_bf16_f32 v88, v88, v89\n"
      "v_cvt_pk_bf16_f32 v89, v90, v91\n"
      "v_cvt_pk_bf16_f32 v90, v92, v93\n"
      "v_mfma_f32_16x16x32_bf16 v[76:79], v[68:71], a[16:19], v[140:143]\n"
      "v_cvt_pk_bf16_f32 v91, v94, v95\n"
      "v_accvgpr_write_b32 a156, v88\n"
      "v_accvgpr_write_b32 a157, v89\n"
      "v_mfma_f32_16x16x32_bf16 v[128:131], v[172:175], a[84:87], v[128:131]\n"
      "v_accvgpr_write_b32 a158, v90\n"
      "v_accvgpr_write_b32 a159, v91\n"
      "v_mfma_f32_16x16x32_bf16 v[80:83], v[64:67], a[88:91], v[144:147]\n"
      "v_cvt_pk_bf16_f32 v96, v96, v97\n"
      "v_cvt_pk_bf16_f32 v97, v98, v99\n"
      "v_cvt_pk_bf16_f32 v98, v100, v101\n"
      "v_mfma_f32_16x16x32_bf16 v[84:87], v[68:71], a[88:91], v[144:147]\n"
      "v_cvt_pk_bf16_f32 v99, v102, v103\n"
      "v_accvgpr_write_b32 a228, v96\n"
      "v_accvgpr_write_b32 a229, v97\n"
      "v_mfma_f32_16x16x32_bf16 v[132:135], v[172:175], a[156:159], v[132:135]\n"
      "v_accvgpr_write_b32 a230, v98\n"
      "v_accvgpr_write_b32 a231, v99\n"
      "v_mfma_f32_16x16x32_bf16 v[88:91], v[64:67], a[160:163], v[148:151]\n"
      "v_cvt_pk_bf16_f32 v72, v72, v73\n"
      "v_cvt_pk_bf16_f32 v73, v74, v75\n"
      "v_cvt_pk_bf16_f32 v74, v76, v77\n"
      "v_mfma_f32_16x16x32_bf16 v[92:95], v[68:71], a[160:163], v[148:151]\n"
      "v_cvt_pk_bf16_f32 v75, v78, v79\n"
      "v_accvgpr_write_b32 a16, v72\n"
      "v_accvgpr_write_b32 a17, v73\n"
      "v_mfma_f32_16x16x32_bf16 v[136:139], v[172:175], a[228:231], v[136:139]\n"
      "v_accvgpr_write_b32 a18, v74\n"
      "v_accvgpr_write_b32 a19, v75\n"
      "v_mfma_f32_16x16x32_bf16 v[96:99], v[64:67], a[232:235], v[152:155]\n"
      "v_cvt_pk_bf16_f32 v80, v80, v81\n"
      "v_cvt_pk_bf16_f32 v81, v82, v83\n"
      "v_cvt_pk_bf16_f32 v82, v84, v85\n"
      "v_mfma_f32_16x16x32_bf16 v[100:103], v[68:71], a[232:235], v[152:155]\n"
      "v_cvt_pk_bf16_f32 v83, v86, v87\n"
      "v_accvgpr_write_b32 a88, v80\n"
      "v_accvgpr_write_b32 a89, v81\n"
      "v_mfma_f32_16x16x32_bf16 v[124:127], v[172:175], a[16:19], v[124:127]\n"
      "v_accvgpr_write_b32 a90, v82\n"
      "v_accvgpr_write_b32 a91, v83\n"
      "v_mfma_f32_16x16x32_bf16 v[72:75], v[64:67], a[20:23], v[140:143]\n"
      "v_cvt_pk_bf16_f32 v88, v88, v89\n"
      "v_cvt_pk_bf16_f32 v89, v90, v91\n"
      "v_cvt_pk_bf16_f32 v90, v92, v93\n"
      "v_mfma_f32_16x16x32_bf16 v[76:79], v[68:71], a[20:23], v[140:143]\n"
      "v_cvt_pk_bf16_f32 v91, v94, v95\n"
      "v_accvgpr_write_b32 a160, v88\n"
      "v_accvgpr_write_b32 a161, v89\n"
      "v_mfma_f32_16x16x32_bf16 v[128:131], v[172:175], a[88:91], v[128:131]\n"
      "v_accvgpr_write_b32 a162, v90\n"
      "v_accvgpr_write_b32 a163, v91\n"
      "v_mfma_f32_16x16x32_bf16 v[80:83], v[64:67], a[92:95], v[144:147]\n"
      "v_cvt_pk_bf16_f32 v96, v96, v97\n"
      "v_cvt_pk_bf16_f32 v97, v98, v99\n"
      "v_cvt_pk_bf16_f32 v98, v100, v101\n"
      "v_mfma_f32_16x16x32_bf16 v[84:87], v[68:71], a[92:95], v[144:147]\n"
      "v_cvt_pk_bf16_f32 v99, v102, v103\n"
      "v_accvgpr_write_b32 a232, v96\n"
      "v_accvgpr_write_b32 a233, v97\n"
      "v_mfma_f32_16x16x32_bf16 v[132:135], v[172:175], a[160:163], v[132:135]\n"
      "v_accvgpr_write_b32 a234, v98\n"
      "v_accvgpr_write_b32 a235, v99\n"
      "v_mfma_f32_16x16x32_bf16 v[88:91], v[64:67], a[164:167], v[148:151]\n"
      "v_cvt_pk_bf16_f32 v72, v72, v73\n"
      "v_cvt_pk_bf16_f32 v73, v74, v75\n"
      "v_cvt_pk_bf16_f32 v74, v76, v77\n"
      "v_mfma_f32_16x16x32_bf16 v[92:95], v[68:71], a[164:167], v[148:151]\n"
      "v_cvt_pk_bf16_f32 v75, v78, v79\n"
      "v_accvgpr_write_b32 a20, v72\n"
      "v_accvgpr_write_b32 a21, v73\n"
      "v_mfma_f32_16x16x32_bf16 v[136:139], v[172:175], a[232:235], v[136:139]\n"
      "v_accvgpr_write_b32 a22, v74\n"
      "v_accvgpr_write_b32 a23, v75\n"
      "v_mfma_f32_16x16x32_bf16 v[96:99], v[64:67], a[236:239], v[152:155]\n"
      "v_cvt_pk_bf16_f32 v80, v80, v81\n"
      "v_cvt_pk_bf16_f32 v81, v82, v83\n"
      "v_cvt_pk_bf16_f32 v82, v84, v85\n"
      "v_mfma_f32_16x16x32_bf16 v[100:103], v[68:71], a[236:239], v[152:155]\n"
      "v_cvt_pk_bf16_f32 v83, v86, v87\n"
      "v_accvgpr_write_b32 a92, v80\n"
      "v_accvgpr_write_b32 a93, v81\n"
      "v_mfma_f32_16x16x32_bf16 v[124:127], v[172:175], a[20:23], v[124:127]\n"
      "v_accvgpr_write_b32 a94, v82\n"
      "v_accvgpr_write_b32 a95, v83\n"
      "v_mfma_f32_16x16x32_bf16 v[72:75], v[64:67], a[24:27], v[140:143]\n"
      "v_cvt_pk_bf16_f32 v88, v88, v89\n"
      "v_cvt_pk_bf16_f32 v89, v90, v91\n"
      "v_cvt_pk_bf16_f32 v90, v92, v93\n"
      "v_mfma_f32_16x16x32_bf16 v[76:79], v[68:71], a[24:27], v[140:143]\n"
      "v_cvt_pk_bf16_f32 v91, v94, v95\n"
      "v_accvgpr_write_b32 a164, v88\n"
      "v_accvgpr_write_b32 a165, v89\n"
      "v_mfma_f32_16x16x32_bf16 v[128:131], v[172:175], a[92:95], v[128:131]\n"
      "v_accvgpr_write_b32 a166, v90\n"
      "v_accvgpr_write_b32 a167, v91\n"
      "v_mfma_f32_16x16x32_bf16 v[80:83], v[64:67], a[96:99], v[144:147]\n"
      "v_cvt_pk_bf16_f32 v96, v96, v97\n"
      "v_cvt_pk_bf16_f32 v97, v98, v99\n"
      "v_cvt_pk_bf16_f32 v98, v100, v101\n"
      "v_mfma_f32_16x16x32_bf16 v[84:87], v[68:71], a[96:99], v[144:147]\n"
      "v_cvt_pk_bf16_f32 v99, v102, v103\n"
      "v_accvgpr_write_b32 a236, v96\n"
      "v_accvgpr_write_b32 a237, v97\n"
      "v_mfma_f32_16x16x32_bf16 v[132:135], v[172:175], a[164:167], v[132:135]\n"
      "v_accvgpr_write_b32 a238, v98\n"
      "v_accvgpr_write_b32 a239, v99\n"
      "v_mfma_f32_16x16x32_bf16 v[88:91], v[64:67], a[168:171], v[148:151]\n"
      "v_cvt_pk_bf16_f32 v72, v72, v73\n"
      "v_cvt_pk_bf16_f32 v73, v74, v75\n"
      "v_cvt_pk_bf16_f32 v74, v76, v77\n"
      "v_mfma_f32_16x16x32_bf16 v[92:95], v[68:71], a[168:171], v[148:151]\n"
      "v_cvt_pk_bf16_f32 v75, v78, v79\n"
      "v_accvgpr_write_b32 a24, v72\n"
      "v_accvgpr_write_b32 a25, v73\n"
      "v_mfma_f32_16x16x32_bf16 v[136:139], v[172:175], a[236:239], v[136:139]\n"
      "v_accvgpr_write_b32 a26, v74\n"
      "v_accvgpr_write_b32 a27, v75\n"
      "v_mfma_f32_16x16x32_bf16 v[96:99], v[64:67], a[240:243], v[152:155]\n"
      "v_cvt_pk_bf16_f32 v80, v80, v81\n"
      "v_cvt_pk_bf16_f32 v81, v82, v83\n"
      "v_cvt_pk_bf16_f32 v82, v84, v85\n"
      "v_mfma_f32_16x16x32_bf16 v[100:103], v[68:71], a[240:243], v[152:155]\n"
      "v_cvt_pk_bf16_f32 v83, v86, v87\n"
      "v_accvgpr_write_b32 a96, v80\n"
      "v_accvgpr_write_b32 a97, v81\n"
      "v_mfma_f32_16x16x32_bf16 v[124:127], v[172:175], a[24:27], v[124:127]\n"
      "v_accvgpr_write_b32 a98, v82\n"
      "v_accvgpr_write_b32 a99, v83\n"
      "v_mfma_f32_16x16x32_bf16 v[72:75], v[64:67], a[28:31], v[140:143]\n"
      "v_cvt_pk_bf16_f32 v88, v88, v89\n"
      "v_cvt_pk_bf16_f32 v89, v90, v91\n"
      "v_cvt_pk_bf16_f32 v90, v92, v93\n"
      "v_mfma_f32_16x16x32_bf16 v[76:79], v[68:71], a[28:31], v[140:143]\n"
      "v_cvt_pk_bf16_f32 v91, v94, v95\n"
      "v_accvgpr_write_b32 a168, v88\n"
      "v_accvgpr_write_b32 a169, v89\n"
      "v_mfma_f32_16x16x32_bf16 v[128:131], v[172:175], a[96:99], v[128:131]\n"
      "v_accvgpr_write_b32 a170, v90\n"
      "v_accvgpr_write_b32 a171, v91\n"
      "v_mfma_f32_16x16x32_bf16 v[80:83], v[64:67], a[100:103], v[144:147]\n"
      "v_cvt_pk_bf16_f32 v96, v96, v97\n"
      "v_cvt_pk_bf16_f32 v97, v98, v99\n"
      "v_cvt_pk_bf16_f32 v98, v100, v101\n"
      "v_mfma_f32_16x16x32_bf16 v[84:87], v[68:71], a[100:103], v[144:147]\n"
      "v_cvt_pk_bf16_f32 v99, v102, v103\n"
      "v_accvgpr_write_b32 a240, v96\n"
      "v_accvgpr_write_b32 a241, v97\n"
      "v_mfma_f32_16x16x32_bf16 v[132:135], v[172:175], a[168:171], v[132:135]\n"
      "v_accvgpr_write_b32 a242, v98\n"
      "v_accvgpr_write_b32 a243, v99\n"
      "v_mfma_f32_16x16x32_bf16 v[88:91], v[64:67], a[172:175], v[148:151]\n"
      "v_cvt_pk_bf16_f32 v72, v72, v73\n"
      "v_cvt_pk_bf16_f32 v73, v74, v75\n"
      "v_cvt_pk_bf16_f32 v74, v76, v77\n"
      "v_mfma_f32_16x16x32_bf16 v[92:95], v[68:71], a[172:175], v[148:151]\n"
      "v_cvt_pk_bf16_f32 v75, v78, v79\n"
      "v_accvgpr_write_b32 a28, v72\n"
      "v_accvgpr_write_b32 a29, v73\n"
      "v_mfma_f32_16x16x32_bf16 v[136:139], v[172:175], a[240:243], v[136:139]\n"
      "v_accvgpr_write_b32 a30, v74\n"
      "v_accvgpr_write_b32 a31, v75\n"
      "v_mfma_f32_16x16x32_bf16 v[96:99], v[64:67], a[244:247], v[152:155]\n"
      "v_cvt_pk_bf16_f32 v80, v80, v81\n"
      "v_cvt_pk_bf16_f32 v81, v82, v83\n"
      "v_cvt_pk_bf16_f32 v82, v84, v85\n"
      "v_mfma_f32_16x16x32_bf16 v[100:103], v[68:71], a[244:247], v[152:155]\n"
      "v_cvt_pk_bf16_f32 v83, v86, v87\n"
      "v_accvgpr_write_b32 a100, v80\n"
      "v_accvgpr_write_b32 a101, v81\n"
      "v_mfma_f32_16x16x32_bf16 v[124:127], v[172:175], a[28:31], v[124:127]\n"
      "v_accvgpr_write_b32 a102, v82\n"
      "v_accvgpr_write_b32 a103, v83\n"
      "v_mfma_f32_16x16x32_bf16 v[72:75], v[64:67], a[32:35], v[140:143]\n"
      "v_cvt_pk_bf16_f32 v88, v88, v89\n"
      "v_cvt_pk_bf16_f32 v89, v90, v91\n"
      "v_cvt_pk_bf16_f32 v90, v92, v93\n"
      "v_mfma_f32_16x16x32_bf16 v[76:79], v[68:71], a[32:35], v[140:143]\n"
      "v_cvt_pk_bf16_f32 v91, v94, v95\n"
      "v_accvgpr_write_b32 a172, v88\n"
      "v_accvgpr_write_b32 a173, v89\n"
      "v_mfma_f32_16x16x32_bf16 v[128:131], v[172:175], a[100:103], v[128:131]\n"
      "v_accvgpr_write_b32 a174, v90\n"
      "v_accvgpr_write_b32 a175, v91\n"
      "v_mfma_f32_16x16x32_bf16 v[80:83], v[64:67], a[104:107], v[144:147]\n"
      "v_cvt_pk_bf16_f32 v96, v96, v97\n"
      "v_cvt_pk_bf16_f32 v97, v98, v99\n"
      "v_cvt_pk_bf16_f32 v98, v100, v101\n"
      "v_mfma_f32_16x16x32_bf16 v[84:87], v[68:71], a[104:107], v[144:147]\n"
      "v_cvt_pk_bf16_f32 v99, v102, v103\n"
      "v_accvgpr_write_b32 a244, v96\n"
      "v_accvgpr_write_b32 a245, v97\n"
      "v_mfma_f32_16x16x32_bf16 v[132:135], v[172:175], a[172:175], v[132:135]\n"
      "v_accvgpr_write_b32 a246, v98\n"
      "v_accvgpr_write_b32 a247, v99\n"
      "v_mfma_f32_16x16x32_bf16 v[88:91], v[64:67], a[176:179], v[148:151]\n"
      "v_cvt_pk_bf16_f32 v72, v72, v73\n"
      "v_cvt_pk_bf16_f32 v73, v74, v75\n"
      "v_cvt_pk_bf16_f32 v74, v76, v77\n"
      "v_mfma_f32_16x16x32_bf16 v[92:95], v[68:71], a[176:179], v[148:151]\n"
      "v_cvt_pk_bf16_f32 v75, v78, v79\n"
      "v_accvgpr_write_b32 a32, v72\n"
      "v_accvgpr_write_b32 a33, v73\n"
      "v_mfma_f32_16x16x32_bf16 v[136:139], v[172:175], a[244:247], v[136:139]\n"
      "v_accvgpr_write_b32 a34, v74\n"
      "v_accvgpr_write_b32 a35, v75\n"
      "v_mfma_f32_16x16x32_bf16 v[96:99], v[64:67], a[248:251], v[152:155]\n"
      "v_cvt_pk_bf16_f32 v80, v80, v81\n"
      "v_cvt_pk_bf16_f32 v81, v82, v83\n"
      "v_cvt_pk_bf16_f32 v82, v84, v85\n"
      "v_mfma_f32_16x16x32_bf16 v[100:103], v[68:71], a[248:251], v[152:155]\n"
      "v_cvt_pk_bf16_f32 v83, v86, v87\n"
      "v_accvgpr_write_b32 a104, v80\n"
      "v_accvgpr_write_b32 a105, v81\n"
      "v_mfma_f32_16x16x32_bf16 v[124:127], v[172:175], a[32:35], v[124:127]\n"
      "v_accvgpr_write_b32 a106, v82\n"
      "v_accvgpr_write_b32 a107, v83\n"
      "v_mfma_f32_16x16x32_bf16 v[72:75], v[64:67], a[36:39], v[140:143]\n"
      "v_cvt_pk_bf16_f32 v88, v88, v89\n"
      "v_cvt_pk_bf16_f32 v89, v90, v91\n"
      "v_cvt_pk_bf16_f32 v90, v92, v93\n"
      "v_mfma_f32_16x16x32_bf16 v[76:79], v[68:71], a[36:39], v[140:143]\n"
      "v_cvt_pk_bf16_f32 v91, v94, v95\n"
      "v_accvgpr_write_b32 a176, v88\n"
      "v_accvgpr_write_b32 a177, v89\n"
      "v_mfma_f32_16x16x32_bf16 v[128:131], v[172:175], a[104:107], v[128:131]\n"
      "v_accvgpr_write_b32 a178, v90\n"
      "v_accvgpr_write_b32 a179, v91\n"
      "v_mfma_f32_16x16x32_bf16 v[80:83], v[64:67], a[108:111], v[144:147]\n"
      "v_cvt_pk_bf16_f32 v96, v96, v97\n"
      "v_cvt_pk_bf16_f32 v97, v98, v99\n"
      "v_cvt_pk_bf16_f32 v98, v100, v101\n"
      "v_mfma_f32_16x16x32_bf16 v[84:87], v[68:71], a[108:111], v[144:147]\n"
      "v_cvt_pk_bf16_f32 v99, v102, v103\n"
      "v_accvgpr_write_b32 a248, v96\n"
      "v_accvgpr_write_b32 a249, v97\n"
      "v_mfma_f32_16x16x32_bf16 v[132:135], v[172:175], a[176:179], v[132:135]\n"
      "v_accvgpr_write_b32 a250, v98\n"
      "v_accvgpr_write_b32 a251, v99\n"
      "v_mfma_f32_16x16x32_bf16 v[88:91], v[64:67], a[180:183], v[148:151]\n"
      "v_cvt_pk_bf16_f32 v72, v72, v73\n"
      "v_cvt_pk_bf16_f32 v73, v74, v75\n"
      "v_cvt_pk_bf16_f32 v74, v76, v77\n"
      "v_mfma_f32_16x16x32_bf16 v[92:95], v[68:71], a[180:183], v[148:151]\n"
      "v_cvt_pk_bf16_f32 v75, v78, v79\n"
      "v_accvgpr_write_b32 a36, v72\n"
      "v_accvgpr_write_b32 a37, v73\n"
      "v_mfma_f32_16x16x32_bf16 v[136:139], v[172:175], a[248:251], v[136:139]\n"
      "v_accvgpr_write_b32 a38, v74\n"
      "v_accvgpr_write_b32 a39, v75\n"
      "v_mfma_f32_16x16x32_bf16 v[96:99], v[64:67], a[252:255], v[152:155]\n"
      "v_cvt_pk_bf16_f32 v80, v80, v81\n"
      "v_cvt_pk_bf16_f32 v81, v82, v83\n"
      "v_cvt_pk_bf16_f32 v82, v84, v85\n"
      "v_mfma_f32_16x16x32_bf16 v[100:103], v[68:71], a[252:255], v[152:155]\n"
      "v_cvt_pk_bf16_f32 v83, v86, v87\n"
      "v_accvgpr_write_b32 a108, v80\n"
      "v_accvgpr_write_b32 a109, v81\n"
      "v_mfma_f32_16x16x32_bf16 v[124:127], v[172:175], a[36:39], v[124:127]\n"
      "v_accvgpr_write_b32 a110, v82\n"
      "v_accvgpr_write_b32 a111, v83\n"
      "s_nop 1\n"
      "v_mfma_f32_16x16x32_bf16 v[128:131], v[172:175], a[108:111], v[128:131]\n"
      "v_cvt_pk_bf16_f32 v88, v88, v89\n"
      "v_cvt_pk_bf16_f32 v89, v90, v91\n"
      "v_cvt_pk_bf16_f32 v90, v92, v93\n"
      "v_cvt_pk_bf16_f32 v91, v94, v95\n"
      "v_accvgpr_write_b32 a180, v88\n"
      "v_accvgpr_write_b32 a181, v89\n"
      "v_accvgpr_write_b32 a182, v90\n"
      "v_accvgpr_write_b32 a183, v91\n"
      "s_nop 1\n"
      "v_mfma_f32_16x16x32_bf16 v[132:135], v[172:175], a[180:183], v[132:135]\n"
      "v_cvt_pk_bf16_f32 v96, v96, v97\n"
      "v_cvt_pk_bf16_f32 v97, v98, v99\n"
      "v_cvt_pk_bf16_f32 v98, v100, v101\n"
      "v_cvt_pk_bf16_f32 v99, v102, v103\n"
      "v_accvgpr_write_b32 a252, v96\n"
      "v_accvgpr_write_b32 a253, v97\n"
      "v_accvgpr_write_b32 a254, v98\n"
      "v_accvgpr_write_b32 a255, v99\n"
      "s_nop 1\n"
      "v_mfma_f32_16x16x32_bf16 v[136:139], v[172:175], a[252:255], v[136:139]\n"
      "v_add_f32 v168, v124, v156\n"
      "v_add_f32 v169, v128, v157\n"
      "v_add_f32 v170, v132, v158\n"
      "s_nop 5\n"
      "v_add_f32 v171, v136, v159\n"
      "v_mul_f32 v140, 0xbb800000, v168\n"
      "v_mul_f32 v141, 0xbb800000, v168\n"
      "v_mul_f32 v142, 0xbb800000, v168\n"
      "v_mul_f32 v143, 0xbb800000, v168\n"
      "v_mul_f32 v144, 0xbb800000, v169\n"
      "v_mul_f32 v145, 0xbb800000, v169\n"
      "v_mul_f32 v146, 0xbb800000, v169\n"
      "v_mul_f32 v147, 0xbb800000, v169\n"
      "v_mul_f32 v148, 0xbb800000, v170\n"
      "v_mul_f32 v149, 0xbb800000, v170\n"
      "v_mul_f32 v150, 0xbb800000, v170\n"
      "v_mul_f32 v151, 0xbb800000, v170\n"
      "v_mul_f32 v152, 0xbb800000, v171\n"
      "v_mul_f32 v153, 0xbb800000, v171\n"
      "v_mul_f32 v154, 0xbb800000, v171\n"
      "v_mul_f32 v155, 0xbb800000, v171\n"
      "v_mfma_f32_16x16x32_bf16 v[72:75], v[64:67], a[40:43], v[140:143]\n"
      "v_mfma_f32_16x16x32_bf16 v[76:79], v[68:71], a[40:43], v[140:143]\n"
      "v_mfma_f32_16x16x32_bf16 v[80:83], v[64:67], a[112:115], v[144:147]\n"
      "v_mfma_f32_16x16x32_bf16 v[84:87], v[68:71], a[112:115], v[144:147]\n"
      "v_mfma_f32_16x16x32_bf16 v[88:91], v[64:67], a[184:187], v[148:151]\n"
      "s_nop 4\n"
      "v_cvt_pk_bf16_f32 v72, v72, v73\n"
      "v_cvt_pk_bf16_f32 v73, v74, v75\n"
      "v_cvt_pk_bf16_f32 v74, v76, v77\n"
      "v_mfma_f32_16x16x32_bf16 v[92:95], v[68:71], a[184:187], v[148:151]\n"
      "v_cvt_pk_bf16_f32 v75, v78, v79\n"
      "v_accvgpr_write_b32 a40, v72\n"
      "v_accvgpr_write_b32 a41, v73\n"
      "v_accvgpr_write_b32 a42, v74\n"
      "v_accvgpr_write_b32 a43, v75\n"
      "v_mfma_f32_16x16x32_bf16 v[96:99], v[64:67], v[20:23], v[152:155]\n"
      "v_cvt_pk_bf16_f32 v80, v80, v81\n"
      "v_cvt_pk_bf16_f32 v81, v82, v83\n"
      "v_cvt_pk_bf16_f32 v82, v84, v85\n"
      "v_mfma_f32_16x16x32_bf16 v[100:103], v[68:71], v[20:23], v[152:155]\n"
      "v_cvt_pk_bf16_f32 v83, v86, v87\n"
      "v_accvgpr_write_b32 a112, v80\n"
      "v_accvgpr_write_b32 a113, v81\n"
      "v_mfma_f32_16x16x32_bf16 v[124:127], v[172:175], a[40:43], v[124:127]\n"
      "v_accvgpr_write_b32 a114, v82\n"
      "v_accvgpr_write_b32 a115, v83\n"
      "v_mfma_f32_16x16x32_bf16 v[72:75], v[64:67], a[44:47], v[140:143]\n"
      "v_cvt_pk_bf16_f32 v88, v88, v89\n"
      "v_cvt_pk_bf16_f32 v89, v90, v91\n"
      "v_cvt_pk_bf16_f32 v90, v92, v93\n"
      "v_mfma_f32_16x16x32_bf16 v[76:79], v[68:71], a[44:47], v[140:143]\n"
      "v_cvt_pk_bf16_f32 v91, v94, v95\n"
      "v_accvgpr_write_b32 a184, v88\n"
      "v_accvgpr_write_b32 a185, v89\n"
      "v_mfma_f32_16x16x32_bf16 v[128:131], v[172:175], a[112:115], v[128:131]\n"
      "v_accvgpr_write_b32 a186, v90\n"
      "v_accvgpr_write_b32 a187, v91\n"
      "v_mfma_f32_16x16x32_bf16 v[80:83], v[64:67], a[116:119], v[144:147]\n"
      "v_cvt_pk_bf16_f32 v20, v96, v97\n"
      "v_cvt_pk_bf16_f32 v21, v98, v99\n"
      "v_cvt_pk_bf16_f32 v22, v100, v101\n"
      "v_mfma_f32_16x16x32_bf16 v[84:87], v[68:71], a[116:119], v[144:147]\n"
      "v_cvt_pk_bf16_f32 v23, v102, v103\n"
      "v_mfma_f32_16x16x32_bf16 v[132:135], v[172:175], a[184:187], v[132:135]\n"
      "v_mfma_f32_16x16x32_bf16 v[88:91], v[64:67], a[188:191], v[148:151]\n"
      "v_cvt_pk_bf16_f32 v72, v72, v73\n"
      "v_cvt_pk_bf16_f32 v73, v74, v75\n"
      "v_cvt_pk_bf16_f32 v74, v76, v77\n"
      "v_mfma_f32_16x16x32_bf16 v[92:95], v[68:71], a[188:191], v[148:151]\n"
      "v_cvt_pk_bf16_f32 v75, v78, v79\n"
      "v_accvgpr_write_b32 a44, v72\n"
      "v_accvgpr_write_b32 a45, v73\n"
      "v_mfma_f32_16x16x32_bf16 v[136:139], v[172:175], v[20:23], v[136:139]\n"
      "v_accvgpr_write_b32 a46, v74\n"
      "v_accvgpr_write_b32 a47, v75\n"
      "v_mfma_f32_16x16x32_bf16 v[96:99], v[64:67], v[24:27], v[152:155]\n"
      "v_cvt_pk_bf16_f32 v80, v80, v81\n"
      "v_cvt_pk_bf16_f32 v81, v82, v83\n"
      "v_cvt_pk_bf16_f32 v82, v84, v85\n"
      "v_mfma_f32_16x16x32_bf16 v[100:103], v[68:71], v[24:27], v[152:155]\n"
      "v_cvt_pk_bf16_f32 v83, v86, v87\n"
      "v_accvgpr_write_b32 a116, v80\n"
      "v_accvgpr_write_b32 a117, v81\n"
      "v_mfma_f32_16x16x32_bf16 v[124:127], v[172:175], a[44:47], v[124:127]\n"
      "v_accvgpr_write_b32 a118, v82\n"
      "v_accvgpr_write_b32 a119, v83\n"
      "v_mfma_f32_16x16x32_bf16 v[72:75], v[64:67], a[48:51], v[140:143]\n"
      "v_cvt_pk_bf16_f32 v88, v88, v89\n"
      "v_cvt_pk_bf16_f32 v89, v90, v91\n"
      "v_cvt_pk_bf16_f32 v90, v92, v93\n"
      "v_mfma_f32_16x16x32_bf16 v[76:79], v[68:71], a[48:51], v[140:143]\n"
      "v_cvt_pk_bf16_f32 v91, v94, v95\n"
      "v_accvgpr_write_b32 a188, v88\n"
      "v_accvgpr_write_b32 a189, v89\n"
      "v_mfma_f32_16x16x32_bf16 v[128:131], v[172:175], a[116:119], v[128:131]\n"
      "v_accvgpr_write_b32 a190, v90\n"
      "v_accvgpr_write_b32 a191, v91\n"
      "v_mfma_f32_16x16x32_bf16 v[80:83], v[64:67], a[120:123], v[144:147]\n"
      "v_cvt_pk_bf16_f32 v24, v96, v97\n"
      "v_cvt_pk_bf16_f32 v25, v98, v99\n"
      "v_cvt_pk_bf16_f32 v26, v100, v101\n"
      "v_mfma_f32_16x16x32_bf16 v[84:87], v[68:71], a[120:123], v[144:147]\n"
      "v_cvt_pk_bf16_f32 v27, v102, v103\n"
      "v_mfma_f32_16x16x32_bf16 v[132:135], v[172:175], a[188:191], v[132:135]\n"
      "v_mfma_f32_16x16x32_bf16 v[88:91], v[64:67], a[192:195], v[148:151]\n"
      "v_cvt_pk_bf16_f32 v72, v72, v73\n"
      "v_cvt_pk_bf16_f32 v73, v74, v75\n"
      "v_cvt_pk_bf16_f32 v74, v76, v77\n"
      "v_mfma_f32_16x16x32_bf16 v[92:95], v[68:71], a[192:195], v[148:151]\n"
      "v_cvt_pk_bf16_f32 v75, v78, v79\n"
      "v_accvgpr_write_b32 a48, v72\n"
      "v_accvgpr_write_b32 a49, v73\n"
      "v_mfma_f32_16x16x32_bf16 v[136:139], v[172:175], v[24:27], v[136:139]\n"
      "v_accvgpr_write_b32 a50, v74\n"
      "v_accvgpr_write_b32 a51, v75\n"
      "v_mfma_f32_16x16x32_bf16 v[96:99], v[64:67], v[28:31], v[152:155]\n"
      "v_cvt_pk_bf16_f32 v80, v80, v81\n"
      "v_cvt_pk_bf16_f32 v81, v82, v83\n"
      "v_cvt_pk_bf16_f32 v82, v84, v85\n"
      "v_mfma_f32_16x16x32_bf16 v[100:103], v[68:71], v[28:31], v[152:155]\n"
      "v_cvt_pk_bf16_f32 v83, v86, v87\n"
      "v_accvgpr_write_b32 a120, v80\n"
      "v_accvgpr_write_b32 a121, v81\n"
      "v_mfma_f32_16x16x32_bf16 v[124:127], v[172:175], a[48:51], v[124:127]\n"
      "v_accvgpr_write_b32 a122, v82\n"
      "v_accvgpr_write_b32 a123, v83\n"
      "v_mfma_f32_16x16x32_bf16 v[72:75], v[64:67], a[52:55], v[140:143]\n"
      "v_cvt_pk_bf16_f32 v88, v88, v89\n"
      "v_cvt_pk_bf16_f32 v89, v90, v91\n"
      "v_cvt_pk_bf16_f32 v90, v92, v93\n"
      "v_mfma_f32_16x16x32_bf16 v[76:79], v[68:71], a[52:55], v[140:143]\n"
      "v_cvt_pk_bf16_f32 v91, v94, v95\n"
      "v_accvgpr_write_b32 a192, v88\n"
      "v_accvgpr_write_b32 a193, v89\n"
      "v_mfma_f32_16x16x32_bf16 v[128:131], v[172:175], a[120:123], v[128:131]\n"
      "v_accvgpr_write_b32 a194, v90\n"
      "v_accvgpr_write_b32 a195, v91\n"
      "v_mfma_f32_16x16x32_bf16 v[80:83], v[64:67], a[124:127], v[144:147]\n"
      "v_cvt_pk_bf16_f32 v28, v96, v97\n"
      "v_cvt_pk_bf16_f32 v29, v98, v99\n"
      "v_cvt_pk_bf16_f32 v30, v100, v101\n"
      "v_mfma_f32_16x16x32_bf16 v[84:87], v[68:71], a[124:127], v[144:147]\n"
      "v_cvt_pk_bf16_f32 v31, v102, v103\n"
      "v_mfma_f32_16x16x32_bf16 v[132:135], v[172:175], a[192:195], v[132:135]\n"
      "v_mfma_f32_16x16x32_bf16 v[88:91], v[64:67], a[196:199], v[148:151]\n"
      "v_cvt_pk_bf16_f32 v72, v72, v73\n"
      "v_cvt_pk_bf16_f32 v73, v74, v75\n"
      "v_cvt_pk_bf16_f32 v74, v76, v77\n"
      "v_mfma_f32_16x16x32_bf16 v[92:95], v[68:71], a[196:199], v[148:151]\n"
      "v_cvt_pk_bf16_f32 v75, v78, v79\n"
      "v_accvgpr_write_b32 a52, v72\n"
      "v_accvgpr_write_b32 a53, v73\n"
      "v_mfma_f32_16x16x32_bf16 v[136:139], v[172:175], v[28:31], v[136:139]\n"
      "v_accvgpr_write_b32 a54, v74\n"
      "v_accvgpr_write_b32 a55, v75\n"
      "v_mfma_f32_16x16x32_bf16 v[96:99], v[64:67], v[32:35], v[152:155]\n"
      "v_cvt_pk_bf16_f32 v80, v80, v81\n"
      "v_cvt_pk_bf16_f32 v81, v82, v83\n"
      "v_cvt_pk_bf16_f32 v82, v84, v85\n"
      "v_mfma_f32_16x16x32_bf16 v[100:103], v[68:71], v[32:35], v[152:155]\n"
      "v_cvt_pk_bf16_f32 v83, v86, v87\n"
      "v_accvgpr_write_b32 a124, v80\n"
      "v_accvgpr_write_b32 a125, v81\n"
      "v_mfma_f32_16x16x32_bf16 v[124:127], v[172:175], a[52:55], v[124:127]\n"
      "v_accvgpr_write_b32 a126, v82\n"
      "v_accvgpr_write_b32 a127, v83\n"
      "v_mfma_f32_16x16x32_bf16 v[72:75], v[64:67], a[56:59], v[140:143]\n"
      "v_cvt_pk_bf16_f32 v88, v88, v89\n"
      "v_cvt_pk_bf16_f32 v89, v90, v91\n"
      "v_cvt_pk_bf16_f32 v90, v92, v93\n"
      "v_mfma_f32_16x16x32_bf16 v[76:79], v[68:71], a[56:59], v[140:143]\n"
      "v_cvt_pk_bf16_f32 v91, v94, v95\n"
      "v_accvgpr_write_b32 a196, v88\n"
      "v_accvgpr_write_b32 a197, v89\n"
      "v_mfma_f32_16x16x32_bf16 v[128:131], v[172:175], a[124:127], v[128:131]\n"
      "v_accvgpr_write_b32 a198, v90\n"
      "v_accvgpr_write_b32 a199, v91\n"
      "v_mfma_f32_16x16x32_bf16 v[80:83], v[64:67], a[128:131], v[144:147]\n"
      "v_cvt_pk_bf16_f32 v32, v96, v97\n"
      "v_cvt_pk_bf16_f32 v33, v98, v99\n"
      "v_cvt_pk_bf16_f32 v34, v100, v101\n"
      "v_mfma_f32_16x16x32_bf16 v[84:87], v[68:71], a[128:131], v[144:147]\n"
      "v_cvt_pk_bf16_f32 v35, v102, v103\n"
      "v_mfma_f32_16x16x32_bf16 v[132:135], v[172:175], a[196:199], v[132:135]\n"
      "v_mfma_f32_16x16x32_bf16 v[88:91], v[64:67], a[200:203], v[148:151]\n"
      "v_cvt_pk_bf16_f32 v72, v72, v73\n"
      "v_cvt_pk_bf16_f32 v73, v74, v75\n"
      "v_cvt_pk_bf16_f32 v74, v76, v77\n"
      "v_mfma_f32_16x16x32_bf16 v[92:95], v[68:71], a[200:203], v[148:151]\n"
      "v_cvt_pk_bf16_f32 v75, v78, v79\n"
      "v_accvgpr_write_b32 a56, v72\n"
      "v_accvgpr_write_b32 a57, v73\n"
      "v_mfma_f32_16x16x32_bf16 v[136:139], v[172:175], v[32:35], v[136:139]\n"
      "v_accvgpr_write_b32 a58, v74\n"
      "v_accvgpr_write_b32 a59, v75\n"
      "v_mfma_f32_16x16x32_bf16 v[96:99], v[64:67], v[36:39], v[152:155]\n"
      "v_cvt_pk_bf16_f32 v80, v80, v81\n"
      "v_cvt_pk_bf16_f32 v81, v82, v83\n"
      "v_cvt_pk_bf16_f32 v82, v84, v85\n"
      "v_mfma_f32_16x16x32_bf16 v[100:103], v[68:71], v[36:39], v[152:155]\n"
      "v_cvt_pk_bf16_f32 v83, v86, v87\n"
      "v_accvgpr_write_b32 a128, v80\n"
      "v_accvgpr_write_b32 a129, v81\n"
      "v_mfma_f32_16x16x32_bf16 v[124:127], v[172:175], a[56:59], v[124:127]\n"
      "v_accvgpr_write_b32 a130, v82\n"
      "v_accvgpr_write_b32 a131, v83\n"
      "s_nop 1\n"
      "v_mfma_f32_16x16x32_bf16 v[128:131], v[172:175], a[128:131], v[128:131]\n"
      "v_cvt_pk_bf16_f32 v88, v88, v89\n"
      "v_cvt_pk_bf16_f32 v89, v90, v91\n"
      "v_cvt_pk_bf16_f32 v90, v92, v93\n"
      "v_cvt_pk_bf16_f32 v91, v94, v95\n"
      "v_accvgpr_write_b32 a200, v88\n"
      "v_accvgpr_write_b32 a201, v89\n"
      "v_accvgpr_write_b32 a202, v90\n"
      "v_accvgpr_write_b32 a203, v91\n"
      "s_nop 1\n"
      "v_mfma_f32_16x16x32_bf16 v[132:135], v[172:175], a[200:203], v[132:135]\n"
      "v_cvt_pk_bf16_f32 v36, v96, v97\n"
      "v_cvt_pk_bf16_f32 v37, v98, v99\n"
      "v_cvt_pk_bf16_f32 v38, v100, v101\n"
      "v_cvt_pk_bf16_f32 v39, v102, v103\n"
      "s_nop 1\n"
      "v_mfma_f32_16x16x32_bf16 v[136:139], v[172:175], v[36:39], v[136:139]\n"
      "v_add_f32 v168, v124, v160\n"
      "v_add_f32 v169, v128, v161\n"
      "v_add_f32 v170, v132, v162\n"
      "s_nop 5\n"
      "v_add_f32 v171, v136, v163\n"
      "v_mul_f32 v140, 0xbc2aaaab, v168\n"
      "v_mul_f32 v141, 0xbc2aaaab, v168\n"
      "v_mul_f32 v142, 0xbc2aaaab, v168\n"
      "v_mul_f32 v143, 0xbc2aaaab, v168\n"
      "v_mul_f32 v144, 0xbc2aaaab, v169\n"
      "v_mul_f32 v145, 0xbc2aaaab, v169\n"
      "v_mul_f32 v146, 0xbc2aaaab, v169\n"
      "v_mul_f32 v147, 0xbc2aaaab, v169\n"
      "v_mul_f32 v148, 0xbc2aaaab, v170\n"
      "v_mul_f32 v149, 0xbc2aaaab, v170\n"
      "v_mul_f32 v150, 0xbc2aaaab, v170\n"
      "v_mul_f32 v151, 0xbc2aaaab, v170\n"
      "v_mul_f32 v152, 0xbc2aaaab, v171\n"
      "v_mul_f32 v153, 0xbc2aaaab, v171\n"
      "v_mul_f32 v154, 0xbc2aaaab, v171\n"
      "v_mul_f32 v155, 0xbc2aaaab, v171\n"
      "v_mfma_f32_16x16x32_bf16 v[72:75], v[64:67], a[60:63], v[140:143]\n"
      "v_mfma_f32_16x16x32_bf16 v[76:79], v[68:71], a[60:63], v[140:143]\n"
      "v_mfma_f32_16x16x32_bf16 v[80:83], v[64:67], a[132:135], v[144:147]\n"
      "v_mfma_f32_16x16x32_bf16 v[84:87], v[68:71], a[132:135], v[144:147]\n"
      "v_mfma_f32_16x16x32_bf16 v[88:91], v[64:67], a[204:207], v[148:151]\n"
      "s_nop 4\n"
      "v_cvt_pk_bf16_f32 v72, v72, v73\n"
      "v_cvt_pk_bf16_f32 v73, v74, v75\n"
      "v_cvt_pk_bf16_f32 v74, v76, v77\n"
      "v_mfma_f32_16x16x32_bf16 v[92:95], v[68:71], a[204:207], v[148:151]\n"
      "v_cvt_pk_bf16_f32 v75, v78, v79\n"
      "v_accvgpr_write_b32 a60, v72\n"
      "v_accvgpr_write_b32 a61, v73\n"
      "v_accvgpr_write_b32 a62, v74\n"
      "v_accvgpr_write_b32 a63, v75\n"
      "v_mfma_f32_16x16x32_bf16 v[96:99], v[64:67], v[40:43], v[152:155]\n"
      "v_cvt_pk_bf16_f32 v80, v80, v81\n"
      "v_cvt_pk_bf16_f32 v81, v82, v83\n"
      "v_cvt_pk_bf16_f32 v82, v84, v85\n"
      "v_mfma_f32_16x16x32_bf16 v[100:103], v[68:71], v[40:43], v[152:155]\n"
      "v_cvt_pk_bf16_f32 v83, v86, v87\n"
      "v_accvgpr_write_b32 a132, v80\n"
      "v_accvgpr_write_b32 a133, v81\n"
      "v_mfma_f32_16x16x32_bf16 v[124:127], v[172:175], a[60:63], v[124:127]\n"
      "v_accvgpr_write_b32 a134, v82\n"
      "v_accvgpr_write_b32 a135, v83\n"
      "v_mfma_f32_16x16x32_bf16 v[72:75], v[64:67], a[64:67], v[140:143]\n"
      "v_cvt_pk_bf16_f32 v88, v88, v89\n"
      "v_cvt_pk_bf16_f32 v89, v90, v91\n"
      "v_cvt_pk_bf16_f32 v90, v92, v93\n"
      "v_mfma_f32_16x16x32_bf16 v[76:79], v[68:71], a[64:67], v[140:143]\n"
      "v_cvt_pk_bf16_f32 v91, v94, v95\n"
      "v_accvgpr_write_b32 a204, v88\n"
      "v_accvgpr_write_b32 a205, v89\n"
      "v_mfma_f32_16x16x32_bf16 v[128:131], v[172:175], a[132:135], v[128:131]\n"
      "v_accvgpr_write_b32 a206, v90\n"
      "v_accvgpr_write_b32 a207, v91\n"
      "v_mfma_f32_16x16x32_bf16 v[80:83], v[64:67], a[136:139], v[144:147]\n"
      "v_cvt_pk_bf16_f32 v40, v96, v97\n"
      "v_cvt_pk_bf16_f32 v41, v98, v99\n"
      "v_cvt_pk_bf16_f32 v42, v100, v101\n"
      "v_mfma_f32_16x16x32_bf16 v[84:87], v[68:71], a[136:139], v[144:147]\n"
      "v_cvt_pk_bf16_f32 v43, v102, v103\n"
      "v_mfma_f32_16x16x32_bf16 v[132:135], v[172:175], a[204:207], v[132:135]\n"
      "v_mfma_f32_16x16x32_bf16 v[88:91], v[64:67], a[208:211], v[148:151]\n"
      "v_cvt_pk_bf16_f32 v72, v72, v73\n"
      "v_cvt_pk_bf16_f32 v73, v74, v75\n"
      "v_cvt_pk_bf16_f32 v74, v76, v77\n"
      "v_mfma_f32_16x16x32_bf16 v[92:95], v[68:71], a[208:211], v[148:151]\n"
      "v_cvt_pk_bf16_f32 v75, v78, v79\n"
      "v_accvgpr_write_b32 a64, v72\n"
      "v_accvgpr_write_b32 a65, v73\n"
      "v_mfma_f32_16x16x32_bf16 v[136:139], v[172:175], v[40:43], v[136:139]\n"
      "v_accvgpr_write_b32 a66, v74\n"
      "v_accvgpr_write_b32 a67, v75\n"
      "v_mfma_f32_16x16x32_bf16 v[96:99], v[64:67], v[44:47], v[152:155]\n"
      "v_cvt_pk_bf16_f32 v80, v80, v81\n"
      "v_cvt_pk_bf16_f32 v81, v82, v83\n"
      "v_cvt_pk_bf16_f32 v82, v84, v85\n"
      "v_mfma_f32_16x16x32_bf16 v[100:103], v[68:71], v[44:47], v[152:155]\n"
      "v_cvt_pk_bf16_f32 v83, v86, v87\n"
      "v_accvgpr_write_b32 a136, v80\n"
      "v_accvgpr_write_b32 a137, v81\n"
      "v_mfma_f32_16x16x32_bf16 v[124:127], v[172:175], a[64:67], v[124:127]\n"
      "v_accvgpr_write_b32 a138, v82\n"
      "v_accvgpr_write_b32 a139, v83\n"
      "s_nop 1\n"
      "v_mfma_f32_16x16x32_bf16 v[128:131], v[172:175], a[136:139], v[128:131]\n"
      "v_cvt_pk_bf16_f32 v88, v88, v89\n"
      "v_cvt_pk_bf16_f32 v89, v90, v91\n"
      "v_cvt_pk_bf16_f32 v90, v92, v93\n"
      "v_cvt_pk_bf16_f32 v91, v94, v95\n"
      "v_accvgpr_write_b32 a208, v88\n"
      "v_accvgpr_write_b32 a209, v89\n"
      "v_accvgpr_write_b32 a210, v90\n"
      "v_accvgpr_write_b32 a211, v91\n"
      "s_nop 1\n"
      "v_mfma_f32_16x16x32_bf16 v[132:135], v[172:175], a[208:211], v[132:135]\n"
      "v_cvt_pk_bf16_f32 v44, v96, v97\n"
      "v_cvt_pk_bf16_f32 v45, v98, v99\n"
      "v_cvt_pk_bf16_f32 v46, v100, v101\n"
      "v_cvt_pk_bf16_f32 v47, v102, v103\n"
      "s_nop 1\n"
      "v_mfma_f32_16x16x32_bf16 v[136:139], v[172:175], v[44:47], v[136:139]\n"
      "v_add_f32 v168, v124, v164\n"
      "v_add_f32 v169, v128, v165\n"
      "v_add_f32 v170, v132, v166\n"
      "s_nop 5\n"
      "v_add_f32 v171, v136, v167\n"
      "v_mul_f32 v140, 0xbd000000, v168\n"
      "v_mul_f32 v141, 0xbd000000, v168\n"
      "v_mul_f32 v142, 0xbd000000, v168\n"
      "v_mul_f32 v143, 0xbd000000, v168\n"
      "v_mul_f32 v144, 0xbd000000, v169\n"
      "v_mul_f32 v145, 0xbd000000, v169\n"
      "v_mul_f32 v146, 0xbd000000, v169\n"
      "v_mul_f32 v147, 0xbd000000, v169\n"
      "v_mul_f32 v148, 0xbd000000, v170\n"
      "v_mul_f32 v149, 0xbd000000, v170\n"
      "v_mul_f32 v150, 0xbd000000, v170\n"
      "v_mul_f32 v151, 0xbd000000, v170\n"
      "v_mul_f32 v152, 0xbd000000, v171\n"
      "v_mul_f32 v153, 0xbd000000, v171\n"
      "v_mul_f32 v154, 0xbd000000, v171\n"
      "v_mul_f32 v155, 0xbd000000, v171\n"
      "v_mfma_f32_16x16x32_bf16 v[72:75], v[64:67], a[68:71], v[140:143]\n"
      "v_mfma_f32_16x16x32_bf16 v[76:79], v[68:71], a[68:71], v[140:143]\n"
      "v_mfma_f32_16x16x32_bf16 v[80:83], v[64:67], a[140:143], v[144:147]\n"
      "v_mfma_f32_16x16x32_bf16 v[84:87], v[68:71], a[140:143], v[144:147]\n"
      "v_mfma_f32_16x16x32_bf16 v[88:91], v[64:67], a[212:215], v[148:151]\n"
      "s_nop 4\n"
      "v_cvt_pk_bf16_f32 v72, v72, v73\n"
      "v_cvt_pk_bf16_f32 v73, v74, v75\n"
      "v_cvt_pk_bf16_f32 v74, v76, v77\n"
      "v_mfma_f32_16x16x32_bf16 v[92:95], v[68:71], a[212:215], v[148:151]\n"
      "v_cvt_pk_bf16_f32 v75, v78, v79\n"
      "v_accvgpr_write_b32 a68, v72\n"
      "v_accvgpr_write_b32 a69, v73\n"
      "v_accvgpr_write_b32 a70, v74\n"
      "v_accvgpr_write_b32 a71, v75\n"
      "v_mfma_f32_16x16x32_bf16 v[96:99], v[64:67], v[48:51], v[152:155]\n"
      "v_cvt_pk_bf16_f32 v80, v80, v81\n"
      "v_cvt_pk_bf16_f32 v81, v82, v83\n"
      "v_cvt_pk_bf16_f32 v82, v84, v85\n"
      "v_mfma_f32_16x16x32_bf16 v[100:103], v[68:71], v[48:51], v[152:155]\n"
      "v_cvt_pk_bf16_f32 v83, v86, v87\n"
      "v_accvgpr_write_b32 a140, v80\n"
      "v_accvgpr_write_b32 a141, v81\n"
      "v_accvgpr_write_b32 a142, v82\n"
      "v_accvgpr_write_b32 a143, v83\n"
      "v_cvt_pk_bf16_f32 v88, v88, v89\n"
      "v_cvt_pk_bf16_f32 v89, v90, v91\n"
      "v_cvt_pk_bf16_f32 v90, v92, v93\n"
      "v_cvt_pk_bf16_f32 v91, v94, v95\n"
      "v_accvgpr_write_b32 a212, v88\n"
      "v_accvgpr_write_b32 a213, v89\n"
      "v_accvgpr_write_b32 a214, v90\n"
      "v_accvgpr_write_b32 a215, v91\n"
      "v_cvt_pk_bf16_f32 v48, v96, v97\n"
      "v_cvt_pk_bf16_f32 v49, v98, v99\n"
      "v_cvt_pk_bf16_f32 v50, v100, v101\n"
      "v_cvt_pk_bf16_f32 v51, v102, v103\n"
      "v_add_u32 v17, 40960, v0\n"
      "s_barrier\n"
      "v_mov_b32 v11, 0\n"
      "ds_write_b32 v13, v11\n"
      "s_waitcnt lgkmcnt(0)\n"
      "s_barrier\n"
      "s_add_u32 s6, s36, 1548288\n"
      "s_add_u32 s36, s36, 4294942720\n"
      "s_add_u32 s42, s37, 4294635520\n"
      "s_add_u32 s37, s37, 4294801408\n"
      "s_add_u32 s36, s36, 24576\n"
      "s_min_u32 s36, s36, s6\n"
      "buffer_load_dword v18, v2, s[8:11], s36 offen sc1\n"
      "s_add_u32 m0, s38, 0\n"
      "s_mov_b32 s40, s36\n"
      "s_waitcnt vmcnt(0)\n"
      "v_cmp_ne_u32 vcc, s21, v18\n"
      "s_add_u32 s36, s36, 24576\n"
      "s_cbranch_vccnz .Lsp_g0_1\n"
      ".Lbk_g0_1:\n"
      "buffer_load_dwordx4 v0, s[8:11], s40 offen offset:0 sc1 lds\n"
      "buffer_load_dwordx4 v0, s[8:11], s40 offen offset:1024 sc1 lds\n"
      "buffer_load_dwordx4 v0, s[8:11], s40 offen offset:2048 sc1 lds\n"
      "buffer_load_dwordx4 v0, s[8:11], s40 offen offset:3072 sc1 lds\n"
      "s_add_u32 m0, s39, 0\n"
      "s_nop 0\n"
      "buffer_load_dwordx4 v1, s[8:11], s40 offen offset:0 sc1 lds\n"
      "s_min_u32 s36, s36, s6\n"
      "buffer_load_dword v18, v2, s[8:11], s36 offen sc1\n"
      "v_add_u32 v11, 1, v11\n"
      "s_add_u32 m0, s38, 20480\n"
      "s_mov_b32 s40, s36\n"
      "s_waitcnt vmcnt(1)\n"
      "ds_write_b32 v13, v11\n"
      "s_waitcnt vmcnt(0)\n"
      "v_cmp_ne_u32 vcc, s21, v18\n"
      "s_add_u32 s36, s36, 24576\n"
      "s_cbranch_vccnz .Lsp_g0_2\n"
      ".Lbk_g0_2:\n"
      "buffer_load_dwordx4 v0, s[8:11], s40 offen offset:0 sc1 lds\n"
      "ds_read_b32 v12, v14\n"
      "buffer_load_dwordx4 v0, s[8:11], s40 offen offset:1024 sc1 lds\n"
      "s_waitcnt lgkmcnt(0)\n"
      "v_cmp_gt_i32 vcc, v11, v12\n"
      "buffer_load_dwordx4 v0, s[8:11], s40 offen offset:2048 sc1 lds\n"
      "s_cbranch_vccnz .Lsp_gy0_3\n"
      ".Lbk_gy0_3:\n"
      "ds_read_b128 v[52:55], v0 offset:0\n"
      "ds_read_b128 v[60:63], v0 offset:2048\n"
      "buffer_load_dwordx4 v0, s[8:11], s40 offen offset:3072 sc1 lds\n"
      "s_add_u32 m0, s39, 20480\n"
      "ds_read_b128 v[68:71], v0 offset:4096\n"
      "ds_read2st64_b64 v[168:171], v4 offset0:0 offset1:1\n"
      "ds_read_b128 v[56:59], v0 offset:1024\n"
      "buffer_load_dwordx4 v1, s[8:11], s40 offen offset:0 sc1 lds\n"
      "ds_read_b128 v[64:67], v0 offset:3072\n"
      "s_min_u32 s36, s36, s6\n"
      "buffer_load_dword v18, v2, s[8:11], s36 offen sc1\n"
      "ds_read_b128 v[72:75], v0 offset:5120\n"
      "ds_read_b128 v[76:79], v0 offset:6144\n"
      "buffer_load_dword v16, v13, s[8:11], s31 offen\n"
      "ds_read_b128 v[80:83], v0 offset:7168\n"
      "ds_read_b128 v[92:95], v0 offset:10240\n"
      "s_mov_b32 s20, 15\n"
      "s_waitcnt lgkmcnt(0)\n"
      "s_nop 4\n"
      "s_branch .Lg_loop\n"
      ".p2align 12\n"
      ".Lg_p0:\n"
      "s_branch .Lg_pr\n"
      ".p2align 5\n"
      ".Lg_loop:\n"
      "v_mfma_f32_16x16x32_bf16 v[124:127], a[0:3], v[52:55], 0\n"
      "ds_read_b128 v[84:87], v0 offset:8192\n"
      "ds_read_b128 v[104:107], v0 offset:13312\n"
      "v_mfma_f32_16x16x32_bf16 v[124:127], a[4:7], v[56:59], v[124:127]\n"
      "ds_read_b128 v[88:91], v0 offset:9216\n"
      "v_mul_f32 v130, v130, v158\n"
      "v_mfma_f32_16x16x32_bf16 v[124:127], a[8:11], v[60:63], v[124:127]\n"
      "s_waitcnt lgkmcnt(7)\n"
      "ds_read_b128 v[108:111], v0 offset:14336\n"
      "v_add_f32 v169, v169, v171\n"
      "v_mfma_f32_16x16x32_bf16 v[124:127], a[12:15], v[64:67], v[124:127]\n"
      "ds_read_b128 v[96:99], v0 offset:11264\n"
      "v_fma_f32 v161, v161, v177, v177\n"
      "v_mfma_f32_16x16x32_bf16 v[124:127], a[16:19], v[68:71], v[124:127]\n"
      "s_waitcnt lgkmcnt(7)\n"
      "ds_read_b128 v[100:103], v0 offset:12288\n"
      "v_mul_f32 v128, v128, v156\n"
      "v_mfma_f32_16x16x32_bf16 v[124:127], a[20:23], v[72:75], v[124:127]\n"
      "v_rcp_f32 v161, v161\n"
      "v_mfma_f32_16x16x32_bf16 v[124:127], a[24:27], v[76:79], v[124:127]\n"
      "s_waitcnt lgkmcnt(6)\n"
      "ds_read_b128 v[112:115], v0 offset:15360\n"
      "v_mul_f32 v129, v129, v157\n"
      "v_mfma_f32_16x16x32_bf16 v[124:127], a[28:31], v[80:83], v[124:127]\n"
      "s_waitcnt lgkmcnt(5)\n"
      "ds_read_b128 v[120:123], v0 offset:17408\n"
      "v_mul_f32 v131, v131, v159\n"
      "v_mfma_f32_16x16x32_bf16 v[124:127], a[32:35], v[84:87], v[124:127]\n"
      "s_waitcnt lgkmcnt(5)\n"
      "ds_read_b128 v[116:119], v0 offset:16384\n"
      "v_cvt_pk_bf16_f32 v165, v130, v131\n"
      "v_mfma_f32_16x16x32_bf16 v[124:127], a[36:39], v[88:91], v[124:127]\n"
      "v_cvt_pk_bf16_f32 v164, v128, v129\n"
      "v_mfma_f32_16x16x32_bf16 v[124:127], a[40:43], v[92:95], v[124:127]\n"
      "s_waitcnt lgkmcnt(4)\n"
      "v_fma_f32 v160, v160, v177, v177\n"
      "v_sub_f32 v176, -v168, v170\n"
      "v_mfma_f32_16x16x32_bf16 v[124:127], a[44:47], v[96:99], v[124:127]\n"
      "s_waitcnt lgkmcnt(3)\n"
      "v_rcp_f32 v160, v160\n"
      "v_mfma_f32_16x16x32_bf16 v[124:127], a[48:51], v[100:103], v[124:127]\n"
      "v_mul_f32 v161, v133, v161\n"
      "v_fma_f32 v169, -v176, v176, v169\n"
      "v_mfma_f32_16x16x32_bf16 v[124:127], a[52:55], v[104:107], v[124:127]\n"
      "v_max_f32 v177, 0x3727c5ac, v169\n"
      "v_mfma_f32_16x16x32_bf16 v[124:127], a[56:59], v[108:111], v[124:127]\n"
      "s_waitcnt lgkmcnt(1)\n"
      "v_mul_f32 v162, v134, v162\n"
      "v_mul_f32 v137, v137, v161\n"
      "v_mfma_f32_16x16x32_bf16 v[124:127], a[60:63], v[112:115], v[124:127]\n"
      "s_waitcnt lgkmcnt(0)\n"
      "v_mul_f32 v138, v138, v162\n"
      "v_mul_f32 v139, v139, v163\n"
      "v_mfma_f32_16x16x32_bf16 v[124:127], a[64:67], v[116:119], v[124:127]\n"
      "v_mul_f32 v160, v132, v160\n"
      "v_cvt_pk_bf16_f32 v167, v138, v139\n"
      "v_mfma_f32_16x16x32_bf16 v[124:127], a[68:71], v[120:123], v[124:127]\n"
      "v_mul_f32 v136, v136, v160\n"
      "v_cvt_pk_bf16_f32 v166, v136, v137\n"
      "v_mfma_f32_16x16x32_bf16 v[132:135], a[144:147], v[52:55], 0\n"
      "buffer_store_dwordx4 v[164:167], v0, s[8:11], s37 offen sc1\n"
      "v_mfma_f32_16x16x32_bf16 v[132:135], a[148:151], v[56:59], v[132:135]\n"
      "v_rsq_f32 v170, v177\n"
      "v_mfma_f32_16x16x32_bf16 v[132:135], a[152:155], v[60:63], v[132:135]\n"
      "v_mul_f32 v158, v126, v126\n"
      "v_mfma_f32_16x16x32_bf16 v[132:135], a[156:159], v[64:67], v[132:135]\n"
      "v_mul_f32 v178, 0xc0135761, v170\n"
      "v_mul_f32 v171, 0x3d372713, v178\n"
      "v_mfma_f32_16x16x32_bf16 v[132:135], a[160:163], v[68:71], v[132:135]\n"
      "v_mul_f32 v170, v170, v170\n"
      "v_mul_f32 v179, v171, v170\n"
      "v_mfma_f32_16x16x32_bf16 v[132:135], a[164:167], v[72:75], v[132:135]\n"
      "v_mul_f32 v159, v127, v127\n"
      "v_mfma_f32_16x16x32_bf16 v[132:135], a[168:171], v[76:79], v[132:135]\n"
      "v_mul_f32 v156, v124, v124\n"
      "v_fma_f32 v159, v159, v179, v178\n"
      "v_mfma_f32_16x16x32_bf16 v[132:135], a[172:175], v[80:83], v[132:135]\n"
      "v_mul_f32 v157, v125, v125\n"
      "v_fma_f32 v158, v158, v179, v178\n"
      "v_mfma_f32_16x16x32_bf16 v[132:135], a[176:179], v[84:87], v[132:135]\n"
      "s_add_u32 s42, s42, 165888\n"
      "v_add_u32 v11, 1, v11\n"
      "v_mul_f32 v158, v126, v158\n"
      "v_mfma_f32_16x16x32_bf16 v[132:135], a[180:183], v[88:91], v[132:135]\n"
      "v_fma_f32 v157, v157, v179, v178\n"
      "v_fma_f32 v156, v156, v179, v178\n"
      "v_mfma_f32_16x16x32_bf16 v[132:135], a[184:187], v[92:95], v[132:135]\n"
      "v_exp_f32 v158, v158\n"
      "v_mfma_f32_16x16x32_bf16 v[132:135], a[188:191], v[96:99], v[132:135]\n"
      "s_add_u32 m0, s38, 40960\n"
      "v_mfma_f32_16x16x32_bf16 v[132:135], a[192:195], v[100:103], v[132:135]\n"
      "s_mov_b32 s40, s36\n"
      "v_mul_f32 v156, v124, v156\n"
      "v_fma_f32 v158, v158, v177, v177\n"
      "v_mfma_f32_16x16x32_bf16 v[132:135], a[196:199], v[104:107], v[132:135]\n"
      "v_exp_f32 v156, v156\n"
      "v_mfma_f32_16x16x32_bf16 v[132:135], a[200:203], v[108:111], v[132:135]\n"
      "v_fma_f32 v156, v156, v177, v177\n"
      "v_mul_f32 v159, v127, v159\n"
      "v_mfma_f32_16x16x32_bf16 v[132:135], a[204:207], v[112:115], v[132:135]\n"
      "v_rcp_f32 v156, v156\n"
      "v_mfma_f32_16x16x32_bf16 v[132:135], a[208:211], v[116:119], v[132:135]\n"
      "v_exp_f32 v159, v159\n"
      "v_mfma_f32_16x16x32_bf16 v[132:135], a[212:215], v[120:123], v[132:135]\n"
      "v_fma_f32 v159, v159, v177, v177\n"
      "v_mfma_f32_16x16x32_bf16 v[128:131], a[72:75], v[52:55], 0\n"
      "s_waitcnt vmcnt(3)\n"
      "ds_write_b32 v13, v11\n"
      "v_mfma_f32_16x16x32_bf16 v[128:131], a[76:79], v[56:59], v[128:131]\n"
      "s_add_u32 s37, s37, 165888\n"
      "v_rcp_f32 v158, v158\n"
      "v_mfma_f32_16x16x32_bf16 v[128:131], a[80:83], v[60:63], v[128:131]\n"
      "v_mul_f32 v162, v134, v134\n"
      "v_mul_f32 v163, v135, v135\n"
      "v_mfma_f32_16x16x32_bf16 v[128:131], a[84:87], v[64:67], v[128:131]\n"
      "v_rcp_f32 v159, v159\n"
      "v_mfma_f32_16x16x32_bf16 v[128:131], a[88:91], v[68:71], v[128:131]\n"
      "s_waitcnt vmcnt(2)\n"
      "v_cmp_ne_u32 vcc, s21, v18\n"
      "v_fma_f32 v163, v163, v179, v178\n"
      "v_mfma_f32_16x16x32_bf16 v[128:131], a[92:95], v[72:75], v[128:131]\n"
      "v_fma_f32 v162, v162, v179, v178\n"
      "v_mul_f32 v162, v134, v162\n"
      "v_mfma_f32_16x16x32_bf16 v[128:131], a[96:99], v[76:79], v[128:131]\n"
      "s_add_u32 s36, s36, 24576\n"
      "v_mul_f32 v161, v133, v133\n"
      "v_mul_f32 v159, v127, v159\n"
      "v_mfma_f32_16x16x32_bf16 v[128:131], a[100:103], v[80:83], v[128:131]\n"
      "v_exp_f32 v162, v162\n"
      "v_mfma_f32_16x16x32_bf16 v[128:131], a[104:107], v[84:87], v[128:131]\n"
      "s_cbranch_vccnz .Lsp_g0_4\n"
      ".Lbk_g0_4:\n"
      "v_mul_f32 v160, v132, v132\n"
      "v_mfma_f32_16x16x32_bf16 v[128:131], a[108:111], v[88:91], v[128:131]\n"
      "buffer_load_dwordx4 v0, s[8:11], s40 offen offset:0 sc1 lds\n"
      "v_mfma_f32_16x16x32_bf16 v[128:131], a[112:115], v[92:95], v[128:131]\n"
      "ds_read_b32 v12, v14\n"
      "v_fma_f32 v161, v161, v179, v178\n"
      "v_mfma_f32_16x16x32_bf16 v[128:131], a[116:119], v[96:99], v[128:131]\n"
      "v_mul_f32 v156, v124, v156\n"
      "v_mul_f32 v161, v133, v161\n"
      "v_mfma_f32_16x16x32_bf16 v[128:131], a[120:123], v[100:103], v[128:131]\n"
      "v_fma_f32 v160, v160, v179, v178\n"
      "v_mul_f32 v163, v135, v163\n"
      "v_mfma_f32_16x16x32_bf16 v[128:131], a[124:127], v[104:107], v[128:131]\n"
      "buffer_load_dwordx4 v0, s[8:11], s40 offen offset:1024 sc1 lds\n"
      "v_mfma_f32_16x16x32_bf16 v[128:131], a[128:131], v[108:111], v[128:131]\n"
      "v_exp_f32 v163, v163\n"
      "v_mfma_f32_16x16x32_bf16 v[128:131], a[132:135], v[112:115], v[128:131]\n"
      "v_mul_f32 v157, v125, v157\n"
      "v_fma_f32 v163, v163, v177, v177\n"
      "v_mfma_f32_16x16x32_bf16 v[128:131], a[136:139], v[116:119], v[128:131]\n"
      "s_waitcnt lgkmcnt(0)\n"
      "v_cmp_gt_i32 vcc, v11, v12\n"
      "v_mul_f32 v160, v132, v160\n"
      "v_mfma_f32_16x16x32_bf16 v[128:131], a[140:143], v[120:123], v[128:131]\n"
      "buffer_load_dwordx4 v0, s[8:11], s40 offen offset:2048 sc1 lds\n"
      "v_mfma_f32_16x16x32_bf16 v[136:139], a[216:219], v[52:55], 0\n"
      "v_rcp_f32 v163, v163\n"
      "v_mfma_f32_16x16x32_bf16 v[136:139], a[220:223], v[56:59], v[136:139]\n"
      "s_cbranch_vccnz .Lsp_gy0_5\n"
      ".Lbk_gy0_5:\n"
      "ds_read_b128 v[52:55], v0 offset:20480\n"
      "v_mfma_f32_16x16x32_bf16 v[136:139], a[224:227], v[60:63], v[136:139]\n"
      "v_exp_f32 v157, v157\n"
      "v_mfma_f32_16x16x32_bf16 v[136:139], a[228:231], v[64:67], v[136:139]\n"
      "ds_read_b128 v[60:63], v0 offset:22528\n"
      "v_mul_f32 v163, v135, v163\n"
      "v_mfma_f32_16x16x32_bf16 v[136:139], a[232:235], v[68:71], v[136:139]\n"
      "buffer_load_dwordx4 v0, s[8:11], s40 offen offset:3072 sc1 lds\n"
      "v_mfma_f32_16x16x32_bf16 v[136:139], a[236:239], v[72:75], v[136:139]\n"
      "v_exp_f32 v161, v161\n"
      "v_mfma_f32_16x16x32_bf16 v[136:139], a[240:243], v[76:79], v[136:139]\n"
      "s_add_u32 m0, s39, 40960\n"
      "ds_read_b128 v[68:71], v0 offset:24576\n"
      "ds_read2st64_b64 v[168:171], v4 offset0:40 offset1:41\n"
      "v_mfma_f32_16x16x32_bf16 v[136:139], a[244:247], v[80:83], v[136:139]\n"
      "ds_read_b128 v[56:59], v0 offset:21504\n"
      "v_fma_f32 v162, v162, v177, v177\n"
      "v_mfma_f32_16x16x32_bf16 v[136:139], a[248:251], v[84:87], v[136:139]\n"
      "buffer_load_dwordx4 v1, s[8:11], s40 offen offset:0 sc1 lds\n"
      "v_mfma_f32_16x16x32_bf16 v[136:139], a[252:255], v[88:91], v[136:139]\n"
      "ds_read_b128 v[64:67], v0 offset:23552\n"
      "v_fma_f32 v157, v157, v177, v177\n"
      "v_mfma_f32_16x16x32_bf16 v[136:139], v[20:23], v[92:95], v[136:139]\n"
      "s_min_u32 s36, s36, s6\n"
      "v_rcp_f32 v162, v162\n"
      "v_mfma_f32_16x16x32_bf16 v[136:139], v[24:27], v[96:99], v[136:139]\n"
      "v_exp_f32 v160, v160\n"
      "v_mfma_f32_16x16x32_bf16 v[136:139], v[28:31], v[100:103], v[136:139]\n"
      "v_rcp_f32 v157, v157\n"
      "v_mfma_f32_16x16x32_bf16 v[136:139], v[32:35], v[104:107], v[136:139]\n"
      "buffer_load_dword v18, v2, s[8:11], s36 offen sc1\n"
      "v_mfma_f32_16x16x32_bf16 v[136:139], v[36:39], v[108:111], v[136:139]\n"
      "ds_read_b128 v[72:75], v0 offset:25600\n"
      "ds_read_b128 v[76:79], v0 offset:26624\n"
      "v_mfma_f32_16x16x32_bf16 v[136:139], v[40:43], v[112:115], v[136:139]\n"
      "s_waitcnt vmcnt(6) lgkmcnt(3)\n"
      "buffer_store_dword v15, v3, s[8:11], s42 offen sc1\n"
      "v_mfma_f32_16x16x32_bf16 v[136:139], v[44:47], v[116:119], v[136:139]\n"
      "ds_read_b128 v[80:83], v0 offset:27648\n"
      "v_mul_f32 v158, v126, v158\n"
      "v_mfma_f32_16x16x32_bf16 v[136:139], v[48:51], v[120:123], v[136:139]\n"
      "ds_read_b128 v[92:95], v0 offset:30720\n"
      "v_mul_f32 v157, v125, v157\n"
      "v_mfma_f32_16x16x32_bf16 v[124:127], a[0:3], v[52:55], 0\n"
      "ds_read_b128 v[84:87], v0 offset:28672\n"
      "ds_read_b128 v[104:107], v0 offset:33792\n"
      "v_mfma_f32_16x16x32_bf16 v[124:127], a[4:7], v[56:59], v[124:127]\n"
      "ds_read_b128 v[88:91], v0 offset:29696\n"
      "v_mul_f32 v130, v130, v158\n"
      "v_mfma_f32_16x16x32_bf16 v[124:127], a[8:11], v[60:63], v[124:127]\n"
      "s_waitcnt lgkmcnt(7)\n"
      "ds_read_b128 v[108:111], v0 offset:34816\n"
      "v_add_f32 v169, v169, v171\n"
      "v_mfma_f32_16x16x32_bf16 v[124:127], a[12:15], v[64:67], v[124:127]\n"
      "ds_read_b128 v[96:99], v0 offset:31744\n"
      "v_fma_f32 v161, v161, v177, v177\n"
      "v_mfma_f32_16x16x32_bf16 v[124:127], a[16:19], v[68:71], v[124:127]\n"
      "s_waitcnt lgkmcnt(7)\n"
      "ds_read_b128 v[100:103], v0 offset:32768\n"
      "v_mul_f32 v128, v128, v156\n"
      "v_mfma_f32_16x16x32_bf16 v[124:127], a[20:23], v[72:75], v[124:127]\n"
      "v_rcp_f32 v161, v161\n"
      "v_mfma_f32_16x16x32_bf16 v[124:127], a[24:27], v[76:79], v[124:127]\n"
      "s_waitcnt lgkmcnt(6)\n"
      "ds_read_b128 v[112:115], v0 offset:35840\n"
      "v_mul_f32 v129, v129, v157\n"
      "v_mfma_f32_16x16x32_bf16 v[124:127], a[28:31], v[80:83], v[124:127]\n"
      "s_waitcnt lgkmcnt(5)\n"
      "ds_read_b128 v[120:123], v0 offset:37888\n"
      "v_mul_f32 v131, v131, v159\n"
      "v_mfma_f32_16x16x32_bf16 v[124:127], a[32:35], v[84:87], v[124:127]\n"
      "s_waitcnt lgkmcnt(5)\n"
      "ds_read_b128 v[116:119], v0 offset:36864\n"
      "v_cvt_pk_bf16_f32 v165, v130, v131\n"
      "v_mfma_f32_16x16x32_bf16 v[124:127], a[36:39], v[88:91], v[124:127]\n"
      "v_cvt_pk_bf16_f32 v164, v128, v129\n"
      "v_mfma_f32_16x16x32_bf16 v[124:127], a[40:43], v[92:95], v[124:127]\n"
      "s_waitcnt lgkmcnt(4)\n"
      "v_fma_f32 v160, v160, v177, v177\n"
      "v_sub_f32 v176, -v168, v170\n"
      "v_mfma_f32_16x16x32_bf16 v[124:127], a[44:47], v[96:99], v[124:127]\n"
      "s_waitcnt lgkmcnt(3)\n"
      "v_rcp_f32 v160, v160\n"
      "v_mfma_f32_16x16x32_bf16 v[124:127], a[48:51], v[100:103], v[124:127]\n"
      "v_mul_f32 v161, v133, v161\n"
      "v_fma_f32 v169, -v176, v176, v169\n"
      "v_mfma_f32_16x16x32_bf16 v[124:127], a[52:55], v[104:107], v[124:127]\n"
      "v_max_f32 v177, 0x3727c5ac, v169\n"
      "v_mfma_f32_16x16x32_bf16 v[124:127], a[56:59], v[108:111], v[124:127]\n"
      "s_waitcnt lgkmcnt(1)\n"
      "v_mul_f32 v162, v134, v162\n"
      "v_mul_f32 v137, v137, v161\n"
      "v_mfma_f32_16x16x32_bf16 v[124:127], a[60:63], v[112:115], v[124:127]\n"
      "s_waitcnt lgkmcnt(0)\n"
      "v_mul_f32 v138, v138, v162\n"
      "v_mul_f32 v139, v139, v163\n"
      "v_mfma_f32_16x16x32_bf16 v[124:127], a[64:67], v[116:119], v[124:127]\n"
      "v_mul_f32 v160, v132, v160\n"
      "v_cvt_pk_bf16_f32 v167, v138, v139\n"
      "v_mfma_f32_16x16x32_bf16 v[124:127], a[68:71], v[120:123], v[124:127]\n"
      "v_mul_f32 v136, v136, v160\n"
      "v_cvt_pk_bf16_f32 v166, v136, v137\n"
      "v_mfma_f32_16x16x32_bf16 v[132:135], a[144:147], v[52:55], 0\n"
      "buffer_store_dwordx4 v[164:167], v0, s[8:11], s37 offen sc1\n"
      "v_mfma_f32_16x16x32_bf16 v[132:135], a[148:151], v[56:59], v[132:135]\n"
      "v_rsq_f32 v170, v177\n"
      "v_mfma_f32_16x16x32_bf16 v[132:135], a[152:155], v[60:63], v[132:135]\n"
      "v_mul_f32 v158, v126, v126\n"
      "v_mfma_f32_16x16x32_bf16 v[132:135], a[156:159], v[64:67], v[132:135]\n"
      "v_mul_f32 v178, 0xc0135761, v170\n"
      "v_mul_f32 v171, 0x3d372713, v178\n"
      "v_mfma_f32_16x16x32_bf16 v[132:135], a[160:163], v[68:71], v[132:135]\n"
      "v_mul_f32 v170, v170, v170\n"
      "v_mul_f32 v179, v171, v170\n"
      "v_mfma_f32_16x16x32_bf16 v[132:135], a[164:167], v[72:75], v[132:135]\n"
      "v_mul_f32 v159, v127, v127\n"
      "v_mfma_f32_16x16x32_bf16 v[132:135], a[168:171], v[76:79], v[132:135]\n"
      "v_mul_f32 v156, v124, v124\n"
      "v_fma_f32 v159, v159, v179, v178\n"
      "v_mfma_f32_16x16x32_bf16 v[132:135], a[172:175], v[80:83], v[132:135]\n"
      "v_mul_f32 v157, v125, v125\n"
      "v_fma_f32 v158, v158, v179, v178\n"
      "v_mfma_f32_16x16x32_bf16 v[132:135], a[176:179], v[84:87], v[132:135]\n"
      "s_add_u32 s42, s42, 165888\n"
      "v_add_u32 v11, 1, v11\n"
      "v_mul_f32 v158, v126, v158\n"
      "v_mfma_f32_16x16x32_bf16 v[132:135], a[180:183], v[88:91], v[132:135]\n"
      "v_fma_f32 v157, v157, v179, v178\n"
      "v_fma_f32 v156, v156, v179, v178\n"
      "v_mfma_f32_16x16x32_bf16 v[132:135], a[184:187], v[92:95], v[132:135]\n"
      "v_exp_f32 v158, v158\n"
      "v_mfma_f32_16x16x32_bf16 v[132:135], a[188:191], v[96:99], v[132:135]\n"
      "s_add_u32 m0, s38, 61440\n"
      "v_mfma_f32_16x16x32_bf16 v[132:135], a[192:195], v[100:103], v[132:135]\n"
      "s_mov_b32 s40, s36\n"
      "v_mul_f32 v156, v124, v156\n"
      "v_fma_f32 v158, v158, v177, v177\n"
      "v_mfma_f32_16x16x32_bf16 v[132:135], a[196:199], v[104:107], v[132:135]\n"
      "v_exp_f32 v156, v156\n"
      "v_mfma_f32_16x16x32_bf16 v[132:135], a[200:203], v[108:111], v[132:135]\n"
      "v_fma_f32 v156, v156, v177, v177\n"
      "v_mul_f32 v159, v127, v159\n"
      "v_mfma_f32_16x16x32_bf16 v[132:135], a[204:207], v[112:115], v[132:135]\n"
      "v_rcp_f32 v156, v156\n"
      "v_mfma_f32_16x16x32_bf16 v[132:135], a[208:211], v[116:119], v[132:135]\n"
      "v_exp_f32 v159, v159\n"
      "v_mfma_f32_16x16x32_bf16 v[132:135], a[212:215], v[120:123], v[132:135]\n"
      "v_fma_f32 v159, v159, v177, v177\n"
      "v_mfma_f32_16x16x32_bf16 v[128:131], a[72:75], v[52:55], 0\n"
      "s_waitcnt vmcnt(3)\n"
      "ds_write_b32 v13, v11\n"
      "v_mfma_f32_16x16x32_bf16 v[128:131], a[76:79], v[56:59], v[128:131]\n"
      "s_add_u32 s37, s37, 165888\n"
      "v_rcp_f32 v158, v158\n"
      "v_mfma_f32_16x16x32_bf16 v[128:131], a[80:83], v[60:63], v[128:131]\n"
      "v_mul_f32 v162, v134, v134\n"
      "v_mul_f32 v163, v135, v135\n"
      "v_mfma_f32_16x16x32_bf16 v[128:131], a[84:87], v[64:67], v[128:131]\n"
      "v_rcp_f32 v159, v159\n"
      "v_mfma_f32_16x16x32_bf16 v[128:131], a[88:91], v[68:71], v[128:131]\n"
      "s_waitcnt vmcnt(2)\n"
      "v_cmp_ne_u32 vcc, s21, v18\n"
      "v_fma_f32 v163, v163, v179, v178\n"
      "v_mfma_f32_16x16x32_bf16 v[128:131], a[92:95], v[72:75], v[128:131]\n"
      "v_fma_f32 v162, v162, v179, v178\n"
      "v_mul_f32 v162, v134, v162\n"
      "v_mfma_f32_16x16x32_bf16 v[128:131], a[96:99], v[76:79], v[128:131]\n"
      "s_add_u32 s36, s36, 24576\n"
      "v_mul_f32 v161, v133, v133\n"
      "v_mul_f32 v159, v127, v159\n"
      "v_mfma_f32_16x16x32_bf16 v[128:131], a[100:103], v[80:83], v[128:131]\n"
      "v_exp_f32 v162, v162\n"
      "v_mfma_f32_16x16x32_bf16 v[128:131], a[104:107], v[84:87], v[128:131]\n"
      "s_cbranch_vccnz .Lsp_g0_6\n"
      ".Lbk_g0_6:\n"
      "v_mul_f32 v160, v132, v132\n"
      "v_mfma_f32_16x16x32_bf16 v[128:131], a[108:111], v[88:91], v[128:131]\n"
      "buffer_load_dwordx4 v0, s[8:11], s40 offen offset:0 sc1 lds\n"
      "v_mfma_f32_16x16x32_bf16 v[128:131], a[112:115], v[92:95], v[128:131]\n"
      "ds_read_b32 v12, v14\n"
      "v_fma_f32 v161, v161, v179, v178\n"
      "v_mfma_f32_16x16x32_bf16 v[128:131], a[116:119], v[96:99], v[128:131]\n"
      "v_mul_f32 v156, v124, v156\n"
      "v_mul_f32 v161, v133, v161\n"
      "v_mfma_f32_16x16x32_bf16 v[128:131], a[120:123], v[100:103], v[128:131]\n"
      "v_fma_f32 v160, v160, v179, v178\n"
      "v_mul_f32 v163, v135, v163\n"
      "v_mfma_f32_16x16x32_bf16 v[128:131], a[124:127], v[104:107], v[128:131]\n"
      "buffer_load_dwordx4 v0, s[8:11], s40 offen offset:1024 sc1 lds\n"
      "v_mfma_f32_16x16x32_bf16 v[128:131], a[128:131], v[108:111], v[128:131]\n"
      "v_exp_f32 v163, v163\n"
      "v_mfma_f32_16x16x32_bf16 v[128:131], a[132:135], v[112:115], v[128:131]\n"
      "v_mul_f32 v157, v125, v157\n"
      "v_fma_f32 v163, v163, v177, v177\n"
      "v_mfma_f32_16x16x32_bf16 v[128:131], a[136:139], v[116:119], v[128:131]\n"
      "s_waitcnt lgkmcnt(0)\n"
      "v_cmp_gt_i32 vcc, v11, v12\n"
      "v_mul_f32 v160, v132, v160\n"
      "v_mfma_f32_16x16x32_bf16 v[128:131], a[140:143], v[120:123], v[128:131]\n"
      "buffer_load_dwordx4 v0, s[8:11], s40 offen offset:2048 sc1 lds\n"
      "v_mfma_f32_16x16x32_bf16 v[136:139], a[216:219], v[52:55], 0\n"
      "v_rcp_f32_e64 v163, v163\n"
      "v_mfma_f32_16x16x32_bf16 v[136:139], a[220:223], v[56:59], v[136:139]\n"
      "s_cbranch_vccnz .Lsp_gy0_7\n"
      ".Lbk_gy0_7:\n"
      "ds_read_b128 v[52:55], v17 offset:0\n"
      "v_mfma_f32_16x16x32_bf16 v[136:139], a[224:227], v[60:63], v[136:139]\n"
      "v_exp_f32 v157, v157\n"
      "v_mfma_f32_16x16x32_bf16 v[136:139], a[228:231], v[64:67], v[136:139]\n"
      "ds_read_b128 v[60:63], v17 offset:2048\n"
      "v_mul_f32 v163, v135, v163\n"
      "v_mfma_f32_16x16x32_bf16 v[136:139], a[232:235], v[68:71], v[136:139]\n"
      "buffer_load_dwordx4 v0, s[8:11], s40 offen offset:3072 sc1 lds\n"
      "v_mfma_f32_16x16x32_bf16 v[136:139], a[236:239], v[72:75], v[136:139]\n"
      "v_exp_f32 v161, v161\n"
      "v_mfma_f32_16x16x32_bf16 v[136:139], a[240:243], v[76:79], v[136:139]\n"
      "s_add_u32 m0, s39, 61440\n"
      "ds_read_b128 v[68:71], v17 offset:4096\n"
      "ds_read2st64_b64 v[168:171], v4 offset0:80 offset1:81\n"
      "v_mfma_f32_16x16x32_bf16 v[136:139], a[244:247], v[80:83], v[136:139]\n"
      "ds_read_b128 v[56:59], v17 offset:1024\n"
      "v_fma_f32 v162, v162, v177, v177\n"
      "v_mfma_f32_16x16x32_bf16 v[136:139], a[248:251], v[84:87], v[136:139]\n"
      "buffer_load_dwordx4 v1, s[8:11], s40 offen offset:0 sc1 lds\n"
      "v_mfma_f32_16x16x32_bf16 v[136:139], a[252:255], v[88:91], v[136:139]\n"
      "ds_read_b128 v[64:67], v17 offset:3072\n"
      "v_fma_f32 v157, v157, v177, v177\n"
      "v_mfma_f32_16x16x32_bf16 v[136:139], v[20:23], v[92:95], v[136:139]\n"
      "s_min_u32 s36, s36, s6\n"
      "v_rcp_f32 v162, v162\n"
      "v_mfma_f32_16x16x32_bf16 v[136:139], v[24:27], v[96:99], v[136:139]\n"
      "v_exp_f32 v160, v160\n"
      "v_mfma_f32_16x16x32_bf16 v[136:139], v[28:31], v[100:103], v[136:139]\n"
      "v_rcp_f32 v157, v157\n"
      "v_mfma_f32_16x16x32_bf16 v[136:139], v[32:35], v[104:107], v[136:139]\n"
      "buffer_load_dword v18, v2, s[8:11], s36 offen sc1\n"
      "v_mfma_f32_16x16x32_bf16 v[136:139], v[36:39], v[108:111], v[136:139]\n"
      "ds_read_b128 v[72:75], v17 offset:5120\n"
      "ds_read_b128 v[76:79], v17 offset:6144\n"
      "v_mfma_f32_16x16x32_bf16 v[136:139], v[40:43], v[112:115], v[136:139]\n"
      "s_waitcnt vmcnt(6) lgkmcnt(3)\n"
      "buffer_store_dword v15, v3, s[8:11], s42 offen sc1\n"
      "v_mfma_f32_16x16x32_bf16 v[136:139], v[44:47], v[116:119], v[136:139]\n"
      "ds_read_b128 v[80:83], v17 offset:7168\n"
      "v_mul_f32 v158, v126, v158\n"
      "v_mfma_f32_16x16x32_bf16 v[136:139], v[48:51], v[120:123], v[136:139]\n"
      "ds_read_b128 v[92:95], v17 offset:10240\n"
      "v_mul_f32 v157, v125, v157\n"
      "v_mfma_f32_16x16x32_bf16 v[124:127], a[0:3], v[52:55], 0\n"
      "ds_read_b128 v[84:87], v17 offset:8192\n"
      "ds_read_b128 v[104:107], v17 offset:13312\n"
      "v_mfma_f32_16x16x32_bf16 v[124:127], a[4:7], v[56:59], v[124:127]\n"
      "ds_read_b128 v[88:91], v17 offset:9216\n"
      "v_mul_f32 v130, v130, v158\n"
      "v_mfma_f32_16x16x32_bf16 v[124:127], a[8:11], v[60:63], v[124:127]\n"
      "s_waitcnt lgkmcnt(7)\n"
      "ds_read_b128 v[108:111], v17 offset:14336\n"
      "v_add_f32 v169, v169, v171\n"
      "v_mfma_f32_16x16x32_bf16 v[124:127], a[12:15], v[64:67], v[124:127]\n"
      "ds_read_b128 v[96:99], v17 offset:11264\n"
      "v_fma_f32 v161, v161, v177, v177\n"
      "v_mfma_f32_16x16x32_bf16 v[124:127], a[16:19], v[68:71], v[124:127]\n"
      "s_waitcnt lgkmcnt(7)\n"
      "ds_read_b128 v[100:103], v17 offset:12288\n"
      "v_mul_f32 v128, v128, v156\n"
      "v_mfma_f32_16x16x32_bf16 v[124:127], a[20:23], v[72:75], v[124:127]\n"
      "v_rcp_f32 v161, v161\n"
      "v_mfma_f32_16x16x32_bf16 v[124:127], a[24:27], v[76:79], v[124:127]\n"
      "s_waitcnt lgkmcnt(6)\n"
      "ds_read_b128 v[112:115], v17 offset:15360\n"
      "v_mul_f32 v129, v129, v157\n"
      "v_mfma_f32_16x16x32_bf16 v[124:127], a[28:31], v[80:83], v[124:127]\n"
      "s_waitcnt lgkmcnt(5)\n"
      "ds_read_b128 v[120:123], v17 offset:17408\n"
      "v_mul_f32 v131, v131, v159\n"
      "v_mfma_f32_16x16x32_bf16 v[124:127], a[32:35], v[84:87], v[124:127]\n"
      "s_waitcnt lgkmcnt(5)\n"
      "ds_read_b128 v[116:119], v17 offset:16384\n"
      "v_cvt_pk_bf16_f32 v165, v130, v131\n"
      "v_mfma_f32_16x16x32_bf16 v[124:127], a[36:39], v[88:91], v[124:127]\n"
      "v_cvt_pk_bf16_f32 v164, v128, v129\n"
      "v_mfma_f32_16x16x32_bf16 v[124:127], a[40:43], v[92:95], v[124:127]\n"
      "s_waitcnt lgkmcnt(4)\n"
      "v_fma_f32 v160, v160, v177, v177\n"
      "v_sub_f32 v176, -v168, v170\n"
      "v_mfma_f32_16x16x32_bf16 v[124:127], a[44:47], v[96:99], v[124:127]\n"
      "s_waitcnt lgkmcnt(3)\n"
      "v_rcp_f32 v160, v160\n"
      "v_mfma_f32_16x16x32_bf16 v[124:127], a[48:51], v[100:103], v[124:127]\n"
      "v_mul_f32 v161, v133, v161\n"
      "v_fma_f32 v169, -v176, v176, v169\n"
      "v_mfma_f32_16x16x32_bf16 v[124:127], a[52:55], v[104:107], v[124:127]\n"
      "v_max_f32 v177, 0x3727c5ac, v169\n"
      "v_mfma_f32_16x16x32_bf16 v[124:127], a[56:59], v[108:111], v[124:127]\n"
      "s_waitcnt lgkmcnt(1)\n"
      "v_mul_f32 v162, v134, v162\n"
      "v_mul_f32 v137, v137, v161\n"
      "v_mfma_f32_16x16x32_bf16 v[124:127], a[60:63], v[112:115], v[124:127]\n"
      "s_waitcnt lgkmcnt(0)\n"
      "v_mul_f32 v138, v138, v162\n"
      "v_mul_f32 v139, v139, v163\n"
      "v_mfma_f32_16x16x32_bf16 v[124:127], a[64:67], v[116:119], v[124:127]\n"
      "v_mul_f32 v160, v132, v160\n"
      "v_cvt_pk_bf16_f32 v167, v138, v139\n"
      "v_mfma_f32_16x16x32_bf16 v[124:127], a[68:71], v[120:123], v[124:127]\n"
      "v_mul_f32 v136, v136, v160\n"
      "v_cvt_pk_bf16_f32 v166, v136, v137\n"
      "v_mfma_f32_16x16x32_bf16 v[132:135], a[144:147], v[52:55], 0\n"
      "buffer_store_dwordx4 v[164:167], v0, s[8:11], s37 offen sc1\n"
      "v_mfma_f32_16x16x32_bf16 v[132:135], a[148:151], v[56:59], v[132:135]\n"
      "v_rsq_f32 v170, v177\n"
      "v_mfma_f32_16x16x32_bf16 v[132:135], a[152:155], v[60:63], v[132:135]\n"
      "v_mul_f32 v158, v126, v126\n"
      "v_mfma_f32_16x16x32_bf16 v[132:135], a[156:159], v[64:67], v[132:135]\n"
      "v_mul_f32 v178, 0xc0135761, v170\n"
      "v_mul_f32 v171, 0x3d372713, v178\n"
      "v_mfma_f32_16x16x32_bf16 v[132:135], a[160:163], v[68:71], v[132:135]\n"
      "v_mul_f32 v170, v170, v170\n"
      "v_mul_f32 v179, v171, v170\n"
      "v_mfma_f32_16x16x32_bf16 v[132:135], a[164:167], v[72:75], v[132:135]\n"
      "v_mul_f32 v159, v127, v127\n"
      "v_mfma_f32_16x16x32_bf16 v[132:135], a[168:171], v[76:79], v[132:135]\n"
      "v_mul_f32 v156, v124, v124\n"
      "v_fma_f32 v159, v159, v179, v178\n"
      "v_mfma_f32_16x16x32_bf16 v[132:135], a[172:175], v[80:83], v[132:135]\n"
      "v_mul_f32 v157, v125, v125\n"
      "v_fma_f32 v158, v158, v179, v178\n"
      "v_mfma_f32_16x16x32_bf16 v[132:135], a[176:179], v[84:87], v[132:135]\n"
      "s_add_u32 s42, s42, 165888\n"
      "v_add_u32 v11, 1, v11\n"
      "v_mul_f32 v158, v126, v158\n"
      "v_mfma_f32_16x16x32_bf16 v[132:135], a[180:183], v[88:91], v[132:135]\n"
      "v_fma_f32 v157, v157, v179, v178\n"
      "v_fma_f32 v156, v156, v179, v178\n"
      "v_mfma_f32_16x16x32_bf16 v[132:135], a[184:187], v[92:95], v[132:135]\n"
      "v_exp_f32 v158, v158\n"
      "v_mfma_f32_16x16x32_bf16 v[132:135], a[188:191], v[96:99], v[132:135]\n"
      "s_add_u32 m0, s38, 0\n"
      "v_mfma_f32_16x16x32_bf16 v[132:135], a[192:195], v[100:103], v[132:135]\n"
      "s_mov_b32 s40, s36\n"
      "v_mul_f32 v156, v124, v156\n"
      "v_fma_f32 v158, v158, v177, v177\n"
      "v_mfma_f32_16x16x32_bf16 v[132:135], a[196:199], v[104:107], v[132:135]\n"
      "v_exp_f32 v156, v156\n"
      "v_mfma_f32_16x16x32_bf16 v[132:135], a[200:203], v[108:111], v[132:135]\n"
      "v_fma_f32 v156, v156, v177, v177\n"
      "v_mul_f32 v159, v127, v159\n"
      "v_mfma_f32_16x16x32_bf16 v[132:135], a[204:207], v[112:115], v[132:135]\n"
      "v_rcp_f32 v156, v156\n"
      "v_mfma_f32_16x16x32_bf16 v[132:135], a[208:211], v[116:119], v[132:135]\n"
      "v_exp_f32 v159, v159\n"
      "v_mfma_f32_16x16x32_bf16 v[132:135], a[212:215], v[120:123], v[132:135]\n"
      "v_fma_f32 v159, v159, v177, v177\n"
      "v_mfma_f32_16x16x32_bf16 v[128:131], a[72:75], v[52:55], 0\n"
      "s_waitcnt vmcnt(3)\n"
      "ds_write_b32 v13, v11\n"
      "v_mfma_f32_16x16x32_bf16 v[128:131], a[76:79], v[56:59], v[128:131]\n"
      "s_add_u32 s37, s37, 165888\n"
      "v_rcp_f32 v158, v158\n"
      "v_mfma_f32_16x16x32_bf16 v[128:131], a[80:83], v[60:63], v[128:131]\n"
      "v_mul_f32 v162, v134, v134\n"
      "v_mul_f32 v163, v135, v135\n"
      "v_mfma_f32_16x16x32_bf16 v[128:131], a[84:87], v[64:67], v[128:131]\n"
      "v_rcp_f32 v159, v159\n"
      "v_mfma_f32_16x16x32_bf16 v[128:131], a[88:91], v[68:71], v[128:131]\n"
      "s_waitcnt vmcnt(2)\n"
      "v_cmp_ne_u32 vcc, s21, v18\n"
      "v_fma_f32 v163, v163, v179, v178\n"
      "v_mfma_f32_16x16x32_bf16 v[128:131], a[92:95], v[72:75], v[128:131]\n"
      "v_fma_f32 v162, v162, v179, v178\n"
      "v_mul_f32 v162, v134, v162\n"
      "v_mfma_f32_16x16x32_bf16 v[128:131], a[96:99], v[76:79], v[128:131]\n"
      "s_add_u32 s36, s36, 24576\n"
      "v_mul_f32 v161, v133, v133\n"
      "v_mul_f32_e64 v159, v127, v159\n"
      "v_mfma_f32_16x16x32_bf16 v[128:131], a[100:103], v[80:83], v[128:131]\n"
      "v_exp_f32_e64 v162, v162\n"
      "v_mfma_f32_16x16x32_bf16 v[128:131], a[104:107], v[84:87], v[128:131]\n"
      "s_cbranch_vccnz .Lsp_g0_8\n"
      ".Lbk_g0_8:\n"
      "v_mul_f32 v160, v132, v132\n"
      "v_mfma_f32_16x16x32_bf16 v[128:131], a[108:111], v[88:91], v[128:131]\n"
      "buffer_load_dwordx4 v0, s[8:11], s40 offen offset:0 sc1 lds\n"
      "v_mfma_f32_16x16x32_bf16 v[128:131], a[112:115], v[92:95], v[128:131]\n"
      "ds_read_b32 v12, v14\n"
      "v_fma_f32 v161, v161, v179, v178\n"
      "v_mfma_f32_16x16x32_bf16 v[128:131], a[116:119], v[96:99], v[128:131]\n"
      "v_mul_f32 v156, v124, v156\n"
      "v_mul_f32 v161, v133, v161\n"
      "v_mfma_f32_16x16x32_bf16 v[128:131], a[120:123], v[100:103], v[128:131]\n"
      "v_fma_f32 v160, v160, v179, v178\n"
      "v_mul_f32 v163, v135, v163\n"
      "v_mfma_f32_16x16x32_bf16 v[128:131], a[124:127], v[104:107], v[128:131]\n"
      "buffer_load_dwordx4 v0, s[8:11], s40 offen offset:1024 sc1 lds\n"
      "v_mfma_f32_16x16x32_bf16 v[128:131], a[128:131], v[108:111], v[128:131]\n"
      "v_exp_f32 v163, v163\n"
      "v_mfma_f32_16x16x32_bf16 v[128:131], a[132:135], v[112:115], v[128:131]\n"
      "v_mul_f32 v157, v125, v157\n"
      "v_fma_f32 v163, v163, v177, v177\n"
      "v_mfma_f32_16x16x32_bf16 v[128:131], a[136:139], v[116:119], v[128:131]\n"
      "s_waitcnt lgkmcnt(0)\n"
      "v_cmp_gt_i32 vcc, v11, v12\n"
      "v_mul_f32 v160, v132, v160\n"
      "v_mfma_f32_16x16x32_bf16 v[128:131], a[140:143], v[120:123], v[128:131]\n"
      "buffer_load_dwordx4 v0, s[8:11], s40 offen offset:2048 sc1 lds\n"
      "v_mfma_f32_16x16x32_bf16 v[136:139], a[216:219], v[52:55], 0\n"
      "v_rcp_f32_e64 v163, v163\n"
      "v_mfma_f32_16x16x32_bf16 v[136:139], a[220:223], v[56:59], v[136:139]\n"
      "s_cbranch_vccnz .Lsp_gy0_9\n"
      ".Lbk_gy0_9:\n"
      "ds_read_b128 v[52:55], v17 offset:20480\n"
      "v_mfma_f32_16x16x32_bf16 v[136:139], a[224:227], v[60:63], v[136:139]\n"
      "v_exp_f32 v157, v157\n"
      "v_mfma_f32_16x16x32_bf16 v[136:139], a[228:231], v[64:67], v[136:139]\n"
      "ds_read_b128 v[60:63], v17 offset:22528\n"
      "v_mul_f32 v163, v135, v163\n"
      "v_mfma_f32_16x16x32_bf16 v[136:139], a[232:235], v[68:71], v[136:139]\n"
      "buffer_load_dwordx4 v0, s[8:11], s40 offen offset:3072 sc1 lds\n"
      "v_mfma_f32_16x16x32_bf16 v[136:139], a[236:239], v[72:75], v[136:139]\n"
      "v_exp_f32 v161, v161\n"
      "v_mfma_f32_16x16x32_bf16 v[136:139], a[240:243], v[76:79], v[136:139]\n"
      "s_add_u32 m0, s39, 0\n"
      "ds_read_b128 v[68:71], v17 offset:24576\n"
      "ds_read2st64_b64 v[168:171], v4 offset0:120 offset1:121\n"
      "v_mfma_f32_16x16x32_bf16 v[136:139], a[244:247], v[80:83], v[136:139]\n"
      "ds_read_b128 v[56:59], v17 offset:21504\n"
      "v_fma_f32 v162, v162, v177, v177\n"
      "v_mfma_f32_16x16x32_bf16 v[136:139], a[248:251], v[84:87], v[136:139]\n"
      "buffer_load_dwordx4 v1, s[8:11], s40 offen offset:0 sc1 lds\n"
      "v_mfma_f32_16x16x32_bf16 v[136:139], a[252:255], v[88:91], v[136:139]\n"
      "ds_read_b128 v[64:67], v17 offset:23552\n"
      "v_fma_f32 v157, v157, v177, v177\n"
      "v_mfma_f32_16x16x32_bf16 v[136:139], v[20:23], v[92:95], v[136:139]\n"
      "s_min_u32 s36, s36, s6\n"
      "v_rcp_f32 v162, v162\n"
      "v_mfma_f32_16x16x32_bf16 v[136:139], v[24:27], v[96:99], v[136:139]\n"
      "v_exp_f32 v160, v160\n"
      "v_mfma_f32_16x16x32_bf16 v[136:139], v[28:31], v[100:103], v[136:139]\n"
      "v_rcp_f32 v157, v157\n"
      "v_mfma_f32_16x16x32_bf16 v[136:139], v[32:35], v[104:107], v[136:139]\n"
      "buffer_load_dword v18, v2, s[8:11], s36 offen sc1\n"
      "v_mfma_f32_16x16x32_bf16 v[136:139], v[36:39], v[108:111], v[136:139]\n"
      "ds_read_b128 v[72:75], v17 offset:25600\n"
      "ds_read_b128 v[76:79], v17 offset:26624\n"
      "v_mfma_f32_16x16x32_bf16 v[136:139], v[40:43], v[112:115], v[136:139]\n"
      "s_waitcnt vmcnt(6) lgkmcnt(3)\n"
      "buffer_store_dword v15, v3, s[8:11], s42 offen sc1\n"
      "v_mfma_f32_16x16x32_bf16 v[136:139], v[44:47], v[116:119], v[136:139]\n"
      "ds_read_b128 v[80:83], v17 offset:27648\n"
      "v_mul_f32 v158, v126, v158\n"
      "v_mfma_f32_16x16x32_bf16 v[136:139], v[48:51], v[120:123], v[136:139]\n"
      "ds_read_b128 v[92:95], v17 offset:30720\n"
      "v_mul_f32 v157, v125, v157\n"
      "v_mfma_f32_16x16x32_bf16 v[124:127], a[0:3], v[52:55], 0\n"
      "ds_read_b128 v[84:87], v17 offset:28672\n"
      "ds_read_b128 v[104:107], v17 offset:33792\n"
      "v_mfma_f32_16x16x32_bf16 v[124:127], a[4:7], v[56:59], v[124:127]\n"
      "ds_read_b128 v[88:91], v17 offset:29696\n"
      "v_mul_f32 v130, v130, v158\n"
      "v_mfma_f32_16x16x32_bf16 v[124:127], a[8:11], v[60:63], v[124:127]\n"
      "s_waitcnt lgkmcnt(7)\n"
      "ds_read_b128 v[108:111], v17 offset:34816\n"
      "v_add_f32 v169, v169, v171\n"
      "v_mfma_f32_16x16x32_bf16 v[124:127], a[12:15], v[64:67], v[124:127]\n"
      ".Lg_p1:\n"
      "s_cbranch_execz .Lg_pr\n"
      "ds_read_b128 v[96:99], v17 offset:31744\n"
      "v_fma_f32 v161, v161, v177, v177\n"
      "v_mfma_f32_16x16x32_bf16 v[124:127], a[16:19], v[68:71], v[124:127]\n"
      "s_waitcnt lgkmcnt(7)\n"
      "ds_read_b128 v[100:103], v17 offset:32768\n"
      "v_mul_f32 v128, v128, v156\n"
      "v_mfma_f32_16x16x32_bf16 v[124:127], a[20:23], v[72:75], v[124:127]\n"
      "v_rcp_f32 v161, v161\n"
      "v_mfma_f32_16x16x32_bf16 v[124:127], a[24:27], v[76:79], v[124:127]\n"
      "s_waitcnt lgkmcnt(6)\n"
      "ds_read_b128 v[112:115], v17 offset:35840\n"
      "v_mul_f32 v129, v129, v157\n"
      "v_mfma_f32_16x16x32_bf16 v[124:127], a[28:31], v[80:83], v[124:127]\n"
      "s_waitcnt lgkmcnt(5)\n"
      "ds_read_b128 v[120:123], v17 offset:37888\n"
      "v_mul_f32 v131, v131, v159\n"
      "v_mfma_f32_16x16x32_bf16 v[124:127], a[32:35], v[84:87], v[124:127]\n"
      "s_waitcnt lgkmcnt(5)\n"
      "ds_read_b128 v[116:119], v17 offset:36864\n"
      "v_cvt_pk_bf16_f32 v165, v130, v131\n"
      "v_mfma_f32_16x16x32_bf16 v[124:127], a[36:39], v[88:91], v[124:127]\n"
      "v_cvt_pk_bf16_f32 v164, v128, v129\n"
      "v_mfma_f32_16x16x32_bf16 v[124:127], a[40:43], v[92:95], v[124:127]\n"
      "s_waitcnt lgkmcnt(4)\n"
      "v_fma_f32 v160, v160, v177, v177\n"
      "v_sub_f32 v176, -v168, v170\n"
      "v_mfma_f32_16x16x32_bf16 v[124:127], a[44:47], v[96:99], v[124:127]\n"
      "s_waitcnt lgkmcnt(3)\n"
      "v_rcp_f32 v160, v160\n"
      "v_mfma_f32_16x16x32_bf16 v[124:127], a[48:51], v[100:103], v[124:127]\n"
      "v_mul_f32 v161, v133, v161\n"
      "v_fma_f32 v169, -v176, v176, v169\n"
      "v_mfma_f32_16x16x32_bf16 v[124:127], a[52:55], v[104:107], v[124:127]\n"
      "v_max_f32 v177, 0x3727c5ac, v169\n"
      "v_mfma_f32_16x16x32_bf16 v[124:127], a[56:59], v[108:111], v[124:127]\n"
      "s_waitcnt lgkmcnt(1)\n"
      "v_mul_f32 v162, v134, v162\n"
      "v_mul_f32 v137, v137, v161\n"
      "v_mfma_f32_16x16x32_bf16 v[124:127], a[60:63], v[112:115], v[124:127]\n"
      "s_waitcnt lgkmcnt(0)\n"
      "v_mul_f32 v138, v138, v162\n"
      "v_mul_f32 v139, v139, v163\n"
      "v_mfma_f32_16x16x32_bf16 v[124:127], a[64:67], v[116:119], v[124:127]\n"
      "v_mul_f32 v160, v132, v160\n"
      "v_cvt_pk_bf16_f32 v167, v138, v139\n"
      "v_mfma_f32_16x16x32_bf16 v[124:127], a[68:71], v[120:123], v[124:127]\n"
      "v_mul_f32 v136, v136, v160\n"
      "v_cvt_pk_bf16_f32 v166, v136, v137\n"
      "v_mfma_f32_16x16x32_bf16 v[132:135], a[144:147], v[52:55], 0\n"
      "buffer_store_dwordx4 v[164:167], v0, s[8:11], s37 offen sc1\n"
      "v_mfma_f32_16x16x32_bf16 v[132:135], a[148:151], v[56:59], v[132:135]\n"
      "v_rsq_f32 v170, v177\n"
      "v_mfma_f32_16x16x32_bf16 v[132:135], a[152:155], v[60:63], v[132:135]\n"
      "v_mul_f32 v158, v126, v126\n"
      "v_mfma_f32_16x16x32_bf16 v[132:135], a[156:159], v[64:67], v[132:135]\n"
      "v_mul_f32 v178, 0xc0135761, v170\n"
      "v_mul_f32 v171, 0x3d372713, v178\n"
      "v_mfma_f32_16x16x32_bf16 v[132:135], a[160:163], v[68:71], v[132:135]\n"
      "v_mul_f32 v170, v170, v170\n"
      "v_mul_f32 v179, v171, v170\n"
      "v_mfma_f32_16x16x32_bf16 v[132:135], a[164:167], v[72:75], v[132:135]\n"
      "v_mul_f32 v159, v127, v127\n"
      "v_mfma_f32_16x16x32_bf16 v[132:135], a[168:171], v[76:79], v[132:135]\n"
      "v_mul_f32 v156, v124, v124\n"
      "v_fma_f32 v159, v159, v179, v178\n"
      "v_mfma_f32_16x16x32_bf16 v[132:135], a[172:175], v[80:83], v[132:135]\n"
      "v_mul_f32 v157, v125, v125\n"
      "v_fma_f32 v158, v158, v179, v178\n"
      "v_mfma_f32_16x16x32_bf16 v[132:135], a[176:179], v[84:87], v[132:135]\n"
      "s_add_u32 s42, s42, 165888\n"
      "v_add_u32 v11, 1, v11\n"
      "v_mul_f32 v158, v126, v158\n"
      "v_mfma_f32_16x16x32_bf16 v[132:135], a[180:183], v[88:91], v[132:135]\n"
      "v_fma_f32 v157, v157, v179, v178\n"
      "v_fma_f32 v156, v156, v179, v178\n"
      "v_mfma_f32_16x16x32_bf16 v[132:135], a[184:187], v[92:95], v[132:135]\n"
      "v_exp_f32 v158, v158\n"
      "v_mfma_f32_16x16x32_bf16 v[132:135], a[188:191], v[96:99], v[132:135]\n"
      "s_add_u32 m0, s38, 20480\n"
      "v_mfma_f32_16x16x32_bf16 v[132:135], a[192:195], v[100:103], v[132:135]\n"
      "s_mov_b32 s40, s36\n"
      "v_mul_f32 v156, v124, v156\n"
      "v_fma_f32 v158, v158, v177, v177\n"
      "v_mfma_f32_16x16x32_bf16 v[132:135], a[196:199], v[104:107], v[132:135]\n"
      "v_exp_f32 v156, v156\n"
      "v_mfma_f32_16x16x32_bf16 v[132:135], a[200:203], v[108:111], v[132:135]\n"
      "v_fma_f32 v156, v156, v177, v177\n"
      "v_mul_f32 v159, v127, v159\n"
      "v_mfma_f32_16x16x32_bf16 v[132:135], a[204:207], v[112:115], v[132:135]\n"
      "v_rcp_f32 v156, v156\n"
      "v_mfma_f32_16x16x32_bf16 v[132:135], a[208:211], v[116:119], v[132:135]\n"
      "v_exp_f32 v159, v159\n"
      "v_mfma_f32_16x16x32_bf16 v[132:135], a[212:215], v[120:123], v[132:135]\n"
      "v_fma_f32 v159, v159, v177, v177\n"
      "v_mfma_f32_16x16x32_bf16 v[128:131], a[72:75], v[52:55], 0\n"
      "s_waitcnt vmcnt(3)\n"
      "ds_write_b32 v13, v11\n"
      "v_mfma_f32_16x16x32_bf16 v[128:131], a[76:79], v[56:59], v[128:131]\n"
      "s_add_u32 s37, s37, 165888\n"
      "v_rcp_f32 v158, v158\n"
      "v_mfma_f32_16x16x32_bf16 v[128:131], a[80:83], v[60:63], v[128:131]\n"
      "v_mul_f32 v162, v134, v134\n"
      "v_mul_f32 v163, v135, v135\n"
      "v_mfma_f32_16x16x32_bf16 v[128:131], a[84:87], v[64:67], v[128:131]\n"
      "v_rcp_f32 v159, v159\n"
      "v_mfma_f32_16x16x32_bf16 v[128:131], a[88:91], v[68:71], v[128:131]\n"
      "s_waitcnt vmcnt(2)\n"
      "v_cmp_ne_u32 vcc, s21, v18\n"
      "v_fma_f32 v163, v163, v179, v178\n"
      "v_mfma_f32_16x16x32_bf16 v[128:131], a[92:95], v[72:75], v[128:131]\n"
      "v_fma_f32 v162, v162, v179, v178\n"
      "v_mul_f32 v162, v134, v162\n"
      "v_mfma_f32_16x16x32_bf16 v[128:131], a[96:99], v[76:79], v[128:131]\n"
      "s_add_u32 s36, s36, 24576\n"
      "v_mul_f32 v161, v133, v133\n"
      "v_mul_f32 v159, v127, v159\n"
      "v_mfma_f32_16x16x32_bf16 v[128:131], a[100:103], v[80:83], v[128:131]\n"
      "v_exp_f32_e64 v162, v162\n"
      "v_mfma_f32_16x16x32_bf16 v[128:131], a[104:107], v[84:87], v[128:131]\n"
      "s_cbranch_vccnz .Lsp_g0_10\n"
      ".Lbk_g0_10:\n"
      "v_mul_f32 v160, v132, v132\n"
      "v_mfma_f32_16x16x32_bf16 v[128:131], a[108:111], v[88:91], v[128:131]\n"
      "buffer_load_dwordx4 v0, s[8:11], s40 offen offset:0 sc1 lds\n"
      "v_mfma_f32_16x16x32_bf16 v[128:131], a[112:115], v[92:95], v[128:131]\n"
      "ds_read_b32 v12, v14\n"
      "v_fma_f32 v161, v161, v179, v178\n"
      "v_mfma_f32_16x16x32_bf16 v[128:131], a[116:119], v[96:99], v[128:131]\n"
      "v_mul_f32 v156, v124, v156\n"
      "v_mul_f32 v161, v133, v161\n"
      "v_mfma_f32_16x16x32_bf16 v[128:131], a[120:123], v[100:103], v[128:131]\n"
      "v_fma_f32 v160, v160, v179, v178\n"
      "v_mul_f32 v163, v135, v163\n"
      "v_mfma_f32_16x16x32_bf16 v[128:131], a[124:127], v[104:107], v[128:131]\n"
      "buffer_load_dwordx4 v0, s[8:11], s40 offen offset:1024 sc1 lds\n"
      "v_mfma_f32_16x16x32_bf16 v[128:131], a[128:131], v[108:111], v[128:131]\n"
      "v_exp_f32 v163, v163\n"
      "v_mfma_f32_16x16x32_bf16 v[128:131], a[132:135], v[112:115], v[128:131]\n"
      "v_mul_f32 v157, v125, v157\n"
      "v_fma_f32 v163, v163, v177, v177\n"
      "v_mfma_f32_16x16x32_bf16 v[128:131], a[136:139], v[116:119], v[128:131]\n"
      "s_waitcnt lgkmcnt(0)\n"
      "v_cmp_gt_i32 vcc, v11, v12\n"
      "v_mul_f32 v160, v132, v160\n"
      "v_mfma_f32_16x16x32_bf16 v[128:131], a[140:143], v[120:123], v[128:131]\n"
      "buffer_load_dwordx4 v0, s[8:11], s40 offen offset:2048 sc1 lds\n"
      "v_mfma_f32_16x16x32_bf16 v[136:139], a[216:219], v[52:55], 0\n"
      "v_rcp_f32_e64 v163, v163\n"
      "v_mfma_f32_16x16x32_bf16 v[136:139], a[220:223], v[56:59], v[136:139]\n"
      "s_cbranch_vccnz .Lsp_gy0_11\n"
      ".Lbk_gy0_11:\n"
      "ds_read_b128 v[52:55], v0 offset:0\n"
      "v_mfma_f32_16x16x32_bf16 v[136:139], a[224:227], v[60:63], v[136:139]\n"
      "v_exp_f32 v157, v157\n"
      "v_mfma_f32_16x16x32_bf16 v[136:139], a[228:231], v[64:67], v[136:139]\n"
      "ds_read_b128 v[60:63], v0 offset:2048\n"
      "v_mul_f32 v163, v135, v163\n"
      "v_mfma_f32_16x16x32_bf16 v[136:139], a[232:235], v[68:71], v[136:139]\n"
      "buffer_load_dwordx4 v0, s[8:11], s40 offen offset:3072 sc1 lds\n"
      "v_mfma_f32_16x16x32_bf16 v[136:139], a[236:239], v[72:75], v[136:139]\n"
      "v_exp_f32 v161, v161\n"
      "v_mfma_f32_16x16x32_bf16 v[136:139], a[240:243], v[76:79], v[136:139]\n"
      "s_add_u32 m0, s39, 20480\n"
      "ds_read_b128 v[68:71], v0 offset:4096\n"
      "ds_read2st64_b64 v[168:171], v4 offset0:0 offset1:1\n"
      "v_mfma_f32_16x16x32_bf16 v[136:139], a[244:247], v[80:83], v[136:139]\n"
      "ds_read_b128 v[56:59], v0 offset:1024\n"
      "v_fma_f32 v162, v162, v177, v177\n"
      "v_mfma_f32_16x16x32_bf16 v[136:139], a[248:251], v[84:87], v[136:139]\n"
      "buffer_load_dwordx4 v1, s[8:11], s40 offen offset:0 sc1 lds\n"
      "v_mfma_f32_16x16x32_bf16 v[136:139], a[252:255], v[88:91], v[136:139]\n"
      "ds_read_b128 v[64:67], v0 offset:3072\n"
      "v_fma_f32 v157, v157, v177, v177\n"
      "v_mfma_f32_16x16x32_bf16 v[136:139], v[20:23], v[92:95], v[136:139]\n"
      "s_min_u32 s36, s36, s6\n"
      "v_rcp_f32 v162, v162\n"
      "v_mfma_f32_16x16x32_bf16 v[136:139], v[24:27], v[96:99], v[136:139]\n"
      "v_exp_f32 v160, v160\n"
      "v_mfma_f32_16x16x32_bf16 v[136:139], v[28:31], v[100:103], v[136:139]\n"
      "v_rcp_f32 v157, v157\n"
      "v_mfma_f32_16x16x32_bf16 v[136:139], v[32:35], v[104:107], v[136:139]\n"
      "buffer_load_dword v18, v2, s[8:11], s36 offen sc1\n"
      "v_mfma_f32_16x16x32_bf16 v[136:139], v[36:39], v[108:111], v[136:139]\n"
      "ds_read_b128 v[72:75], v0 offset:5120\n"
      "ds_read_b128 v[76:79], v0 offset:6144\n"
      "v_mfma_f32_16x16x32_bf16 v[136:139], v[40:43], v[112:115], v[136:139]\n"
      "s_waitcnt vmcnt(6) lgkmcnt(3)\n"
      "buffer_store_dword v15, v3, s[8:11], s42 offen sc1\n"
      "v_mfma_f32_16x16x32_bf16 v[136:139], v[44:47], v[116:119], v[136:139]\n"
      "ds_read_b128 v[80:83], v0 offset:7168\n"
      "v_mul_f32 v158, v126, v158\n"
      "v_mfma_f32_16x16x32_bf16 v[136:139], v[48:51], v[120:123], v[136:139]\n"
      "ds_read_b128 v[92:95], v0 offset:10240\n"
      "v_mul_f32 v157, v125, v157\n"
      "s_sub_u32 s20, s20, 1\n"
      "s_cbranch_scc0 .Lg_loop\n"
      ".Lg_tail:\n"
      "v_mul_f32 v130, v130, v158\n"
      "v_fma_f32 v161, v161, v177, v177\n"
      "v_mul_f32 v128, v128, v156\n"
      "v_rcp_f32 v161, v161\n"
      "v_mul_f32 v129, v129, v157\n"
      "v_mul_f32 v131, v131, v159\n"
      "v_cvt_pk_bf16_f32 v165, v130, v131\n"
      "v_cvt_pk_bf16_f32 v164, v128, v129\n"
      "v_fma_f32 v160, v160, v177, v177\n"
      "v_rcp_f32 v160, v160\n"
      "v_mul_f32 v161, v133, v161\n"
      "v_mul_f32 v162, v134, v162\n"
      "v_mul_f32 v137, v137, v161\n"
      "v_mul_f32 v138, v138, v162\n"
      "v_mul_f32 v139, v139, v163\n"
      "v_mul_f32 v160, v132, v160\n"
      "v_cvt_pk_bf16_f32 v167, v138, v139\n"
      "v_mul_f32 v136, v136, v160\n"
      "v_cvt_pk_bf16_f32 v166, v136, v137\n"
      "buffer_store_dwordx4 v[164:167], v0, s[8:11], s37 offen sc1\n"
      "s_add_u32 s42, s42, 165888\n"
      "s_add_u32 s37, s37, 165888\n"
      "s_waitcnt vmcnt(0)\n"
      "buffer_store_dword v15, v3, s[8:11], s42 offen sc1\n"
      "s_add_u32 s42, s42, 165888\n"
      "s_endpgm\n"
      ".Lsp_g0_1:\n"
      "s_mov_b32 s22, s23\n"
      ".Lsl_g0_1:\n"
      "buffer_load_dword v18, v2, s[8:11], s40 offen sc1\n"
      "s_waitcnt vmcnt(0)\n"
      "v_cmp_ne_u32 vcc, s21, v18\n"
      "s_cbranch_vccz .Lbk_g0_1\n"
      "s_sleep 4\n"
      "s_sub_u32 s22, s22, 1\n"
      "s_cbranch_scc0 .Lsl_g0_1\n"
      "s_mov_b32 s23, 0\n"
      "s_branch .Lbk_g0_1\n"
      ".Lsp_g0_2:\n"
      "s_mov_b32 s22, s23\n"
      ".Lsl_g0_2:\n"
      "buffer_load_dword v18, v2, s[8:11], s40 offen sc1\n"
      "s_waitcnt vmcnt(0)\n"
      "v_cmp_ne_u32 vcc, s21, v18\n"
      "s_cbranch_vccz .Lbk_g0_2\n"
      "s_sleep 4\n"
      "s_sub_u32 s22, s22, 1\n"
      "s_cbranch_scc0 .Lsl_g0_2\n"
      "s_mov_b32 s23, 0\n"
      "s_branch .Lbk_g0_2\n"
      ".Lsp_gy0_3:\n"
      "s_lshl_b32 s22, s23, 4\n"
      ".Lsl_gy0_3:\n"
      "ds_read_b32 v12, v14\n"
      "s_waitcnt lgkmcnt(0)\n"
      "v_cmp_gt_i32 vcc, v11, v12\n"
      "s_cbranch_vccz .Lbk_gy0_3\n"
      "s_sub_u32 s22, s22, 1\n"
      "s_cbranch_scc0 .Lsl_gy0_3\n"
      "s_mov_b32 s23, 0\n"
      "s_branch .Lbk_gy0_3\n"
      ".Lsp_g0_4:\n"
      "s_mov_b32 s22, s23\n"
      "buffer_load_dword v18, v2, s[8:11], s40 offen sc1\n"
      "s_waitcnt vmcnt(0)\n"
      "v_cmp_ne_u32 vcc, s21, v18\n"
      "s_add_u32 s30, s42, 0\n"
      "buffer_store_dword v15, v3, s[8:11], s30 offen sc1\n"
      "s_cbranch_vccz .Lbk_g0_4\n"
      ".Lsl_g0_4:\n"
      "buffer_load_dword v18, v2, s[8:11], s40 offen sc1\n"
      "s_waitcnt vmcnt(0)\n"
      "v_cmp_ne_u32 vcc, s21, v18\n"
      "s_cbranch_vccz .Lbk_g0_4\n"
      "s_sleep 4\n"
      "s_sub_u32 s22, s22, 1\n"
      "s_cbranch_scc0 .Lsl_g0_4\n"
      "s_mov_b32 s23, 0\n"
      "s_branch .Lbk_g0_4\n"
      ".Lsp_gy0_5:\n"
      "s_lshl_b32 s22, s23, 4\n"
      ".Lsl_gy0_5:\n"
      "ds_read_b32 v12, v14\n"
      "s_waitcnt lgkmcnt(0)\n"
      "v_cmp_gt_i32 vcc, v11, v12\n"
      "s_cbranch_vccz .Lbk_gy0_5\n"
      "s_sub_u32 s22, s22, 1\n"
      "s_cbranch_scc0 .Lsl_gy0_5\n"
      "s_mov_b32 s23, 0\n"
      "s_branch .Lbk_gy0_5\n"
      ".Lsp_g0_6:\n"
      "s_mov_b32 s22, s23\n"
      "buffer_load_dword v18, v2, s[8:11], s40 offen sc1\n"
      "s_waitcnt vmcnt(0)\n"
      "v_cmp_ne_u32 vcc, s21, v18\n"
      "s_add_u32 s30, s42, 0\n"
      "buffer_store_dword v15, v3, s[8:11], s30 offen sc1\n"
      "s_cbranch_vccz .Lbk_g0_6\n"
      ".Lsl_g0_6:\n"
      "buffer_load_dword v18, v2, s[8:11], s40 offen sc1\n"
      "s_waitcnt vmcnt(0)\n"
      "v_cmp_ne_u32 vcc, s21, v18\n"
      "s_cbranch_vccz .Lbk_g0_6\n"
      "s_sleep 4\n"
      "s_sub_u32 s22, s22, 1\n"
      "s_cbranch_scc0 .Lsl_g0_6\n"
      "s_mov_b32 s23, 0\n"
      "s_branch .Lbk_g0_6\n"
      ".Lsp_gy0_7:\n"
      "s_lshl_b32 s22, s23, 4\n"
      ".Lsl_gy0_7:\n"
      "ds_read_b32 v12, v14\n"
      "s_waitcnt lgkmcnt(0)\n"
      "v_cmp_gt_i32 vcc, v11, v12\n"
      "s_cbranch_vccz .Lbk_gy0_7\n"
      "s_sub_u32 s22, s22, 1\n"
      "s_cbranch_scc0 .Lsl_gy0_7\n"
      "s_mov_b32 s23, 0\n"
      "s_branch .Lbk_gy0_7\n"
      ".Lsp_g0_8:\n"
      "s_mov_b32 s22, s23\n"
      "buffer_load_dword v18, v2, s[8:11], s40 offen sc1\n"
      "s_waitcnt vmcnt(0)\n"
      "v_cmp_ne_u32 vcc, s21, v18\n"
      "s_add_u32 s30, s42, 0\n"
      "buffer_store_dword v15, v3, s[8:11], s30 offen sc1\n"
      "s_cbranch_vccz .Lbk_g0_8\n"
      ".Lsl_g0_8:\n"
      "buffer_load_dword v18, v2, s[8:11], s40 offen sc1\n"
      "s_waitcnt vmcnt(0)\n"
      "v_cmp_ne_u32 vcc, s21, v18\n"
      "s_cbranch_vccz .Lbk_g0_8\n"
      "s_sleep 4\n"
      "s_sub_u32 s22, s22, 1\n"
      "s_cbranch_scc0 .Lsl_g0_8\n"
      "s_mov_b32 s23, 0\n"
      "s_branch .Lbk_g0_8\n"
      ".Lsp_gy0_9:\n"
      "s_lshl_b32 s22, s23, 4\n"
      ".Lsl_gy0_9:\n"
      "ds_read_b32 v12, v14\n"
      "s_waitcnt lgkmcnt(0)\n"
      "v_cmp_gt_i32 vcc, v11, v12\n"
      "s_cbranch_vccz .Lbk_gy0_9\n"
      "s_sub_u32 s22, s22, 1\n"
      "s_cbranch_scc0 .Lsl_gy0_9\n"
      "s_mov_b32 s23, 0\n"
      "s_branch .Lbk_gy0_9\n"
      ".Lsp_g0_10:\n"
      "s_mov_b32 s22, s23\n"
      "buffer_load_dword v18, v2, s[8:11], s40 offen sc1\n"
      "s_waitcnt vmcnt(0)\n"
      "v_cmp_ne_u32 vcc, s21, v18\n"
      "s_add_u32 s30, s42, 0\n"
      "buffer_store_dword v15, v3, s[8:11], s30 offen sc1\n"
      "s_cbranch_vccz .Lbk_g0_10\n"
      ".Lsl_g0_10:\n"
      "buffer_load_dword v18, v2, s[8:11], s40 offen sc1\n"
      "s_waitcnt vmcnt(0)\n"
      "v_cmp_ne_u32 vcc, s21, v18\n"
      "s_cbranch_vccz .Lbk_g0_10\n"
      "s_sleep 4\n"
      "s_sub_u32 s22, s22, 1\n"
      "s_cbranch_scc0 .Lsl_g0_10\n"
      "s_mov_b32 s23, 0\n"
      "s_branch .Lbk_g0_10\n"
      ".Lsp_gy0_11:\n"
      "s_lshl_b32 s22, s23, 4\n"
      ".Lsl_gy0_11:\n"
      "ds_read_b32 v12, v14\n"
      "s_waitcnt lgkmcnt(0)\n"
      "v_cmp_gt_i32 vcc, v11, v12\n"
      "s_cbranch_vccz .Lbk_gy0_11\n"
      "s_sub_u32 s22, s22, 1\n"
      "s_cbranch_scc0 .Lsl_gy0_11\n"
      "s_mov_b32 s23, 0\n"
      "s_branch .Lbk_gy0_11\n"
      ".Lg_last:\n"
      :
      : "{s[8:11]}"(rsWS), "{s[12:15]}"(rsA), "{s[92:95]}"(rsC), "{v0}"(v_l16), "{v1}"(v_l5), "{v2}"(v_fa), "{v3}"(v_ra), "{v4}"(v_st), "{v5}"(v_at), "{v6}"(v_vo0), "{v7}"(v_vo1), "{v8}"(v_vo2), "{v9}"(v_vo3), "{v10}"(v_pw), "{v12}"(v_al), "{v13}"(v_yw), "{v14}"(v_yr), "{v16}"(v_hi), "{v54}"(v_tw[0]), "{v55}"(v_tw[1]), "{v64}"(v_id[0]), "{v65}"(v_id[1]), "{s36}"(s_sx), "{s37}"(s_sh), "{s38}"(s_m4), "{s39}"(s_m5)
      : "v255", "a255", "s99", "vcc", "scc", "memory");

  }
}

extern "C" void kernel_launch(void* const* d_in, const int* in_sizes, int n_in, void* d_out, int out_size,
                              void* d_ws, size_t ws_size, hipStream_t stream) {
  char* ws = (char*)d_ws + ((256 - ((size_t)d_ws & 255)) & 255);
  (void)hipFuncSetAttribute((const void*)k_all, hipFuncAttributeMaxDynamicSharedMemorySize, LDS_BYTES);
  k_all<<<256, 256, LDS_BYTES, stream>>>((const u16*)d_in[0], (const u16*)d_in[1], (const u16*)d_in[2], (const u16*)d_in[4],
                                         (const u16*)d_in[5], (const u16*)d_in[6], ws, (u16*)d_out);
}
```

```cpp
#include <hip/hip_runtime.h>
#include <stdint.h>

typedef unsigned short u16;
typedef __attribute__((ext_vector_type(4))) unsigned u32x4;
constexpr unsigned WS_DBG = 65536u;
constexpr unsigned WS_XIN = 1310720u;
constexpr unsigned WS_OB = 9175040u;
constexpr unsigned WS_X1 = 17039360u;
constexpr unsigned WS_H = 26034176u;
constexpr unsigned WS_PS = 78589952u;
constexpr unsigned WS_END = 109740032u;
constexpr unsigned KSK = 0u;
constexpr unsigned ENT = 1u;
constexpr unsigned ROT = 0u;
constexpr unsigned FLIP = 0u;
constexpr unsigned PW = 1664u;
constexpr unsigned REC = 24576u;
constexpr unsigned HREC = 165888u;
constexpr unsigned PREC = 99840u;
constexpr unsigned NREC = 80u;
constexpr unsigned OOB = 0x40000000u, NEG = 0xff61b1e6u;
constexpr int LDS_BYTES = 160 * 1024;

__device__ __forceinline__ u32x4 srd(const void* p, unsigned bytes) {
  const unsigned long long a = (unsigned long long)p;
  return u32x4{(unsigned)a, (unsigned)(a >> 32) & 0xffffu, bytes, 0x00020000u};
}

extern "C" __global__ __launch_bounds__(256, 1) __attribute__((amdgpu_waves_per_eu(1, 1))) void k_all(
    const u16* X, const u16* Wqkv, const u16* Wo, const u16* Wg, const u16* Wu, const u16* Wd, char* ws, u16* out) {
  const unsigned b = blockIdx.x, xcd = b & 7, slot = b >> 3;
  const unsigned w = __builtin_amdgcn_readfirstlane(threadIdx.x >> 6), lane = threadIdx.x & 63;
  const bool fast = ((xcd ^ FLIP) & 1) == 0;
  auto qvo = [&](unsigned i, unsigned hg) {
    const unsigned c = 64 * i + lane, r = c * 2185 >> 16, cc = c - 30 * r, sc = cc * 57 >> 9;
    const unsigned o = (8 * w + r) * 3456 + sc * (1152 - 144) + 144 * hg + cc * 16 - 1024 * i;
    return o | (c < 240 && cc < 27 ? 0u : OOB);
  };
  if (ENT) {
    constexpr unsigned NL = 41, LR = 9;
    const unsigned wi = slot * 4 + w, n0 = wi * NL + lane, n = n0 < 576 * LR ? n0 : 0, r = n / LR, c = n - LR * r;
    const unsigned lx = lane - NL, m = (xcd * 128 + wi) * 4 + lx, t = m / 144;
    unsigned oc = (wi * 8 + (lane & 7)) * 128;
    asm volatile("s_getpc_b64 vcc\n.Lent_pc:\n\ts_sub_u32 vcc_lo, vcc_lo, .Lent_pc-k_all\n\ts_subb_u32 vcc_hi, vcc_hi, 0\n\ts_mov_b32 m0, %1\n\t"
                 "v_min_u32 %0, .Lw_last-k_all, %0\n\tglobal_load_lds_dword %0, vcc" : "+v"(oc) : "s"(162816 + w * 256) : "vcc", "scc", "memory");
    unsigned oq = r * 3456 + c / (LR / 3) * 1152 + ((xcd & 6) + c % (LR / 3)) * 128, ox = (32 * (t & 3) + 31 - (t >> 2)) * 18432 + (m - 144 * t) * 128;
    asm volatile("" : "+v"(oq), "+v"(ox));
    const bool isq = lane < NL && fast;
    oq = isq ? oq : lx < 4 ? ox : 0;
    asm volatile("" : "+v"(oq));
    __builtin_amdgcn_sched_barrier(0);
    const char* pa = (const char*)(isq ? Wqkv : X) + oq;
    asm volatile("s_mov_b32 m0, %1\n\ts_nop 0\n\tglobal_load_lds_dword %0, off" ::"v"(pa), "s"(162816 + w * 256) : "memory");
    __builtin_amdgcn_sched_barrier(0);
  }
  const unsigned i16 = lane & 15, g = lane >> 4, q = i16 >> 2, p = lane & 3;
  const u32x4 rsWS = srd(ws, WS_END);
  const unsigned v_l16 = lane * 16, e = xcd >> 1;
  const unsigned half = (i16 + 16 * (g >> 1)) * 16 + (g & 1) * 8;
  const unsigned line = lane < 32 ? lane * 4 : OOB, v_al = w ? OOB : line + 128 * (1 + b);
  const unsigned up = lane >> 5, l5 = (lane & 31) * 4, v_hi = 0u - up;
  const unsigned l8 = lane >> 3, l7 = (lane & 7) * 16;
#define COMMON(ID, YB) const unsigned v_dbg = WS_DBG + (((ID) + part * 4 + gr) * 4 + w) * 1024 + lane * 4, v_yw = (YB) + w * 4, v_yr = (YB) + p * 4; (void)v_dbg
  if (__builtin_expect(fast && slot < 8, 1)) {
    const unsigned gr = slot & 3, part = 2 * e + (slot >> 2);
    COMMON(0, 61440);
    const unsigned hg = part, h = 4 * hg + w, par = i16 & 1;
    const u32x4 rsA = srd(Wqkv, 576 * 1728 * 2);
    const unsigned v_l5 = w < 3 || lane < 32 ? v_l16 + 16384 - w * 3072 : OOB;
    const unsigned v_fa = 20480 - w * 4096 + (lane < 48 ? lane >> 2 : 11) * 128 + p * 4, v_ra = up * 8 * REC + l5 + 20480 + h * 128;
    const unsigned v_st = 18432 + i16 * 32 + q * 8, v_tw = 61568 + w * 128 + i16 * 8, v_tr = 61568 + w * 128 + g * 32;
    const unsigned v_om = h * 512 + half, v_oe = g ? OOB : 16384 + h * 128 + i16 * 8;
    const unsigned v_pw = 20480 + h * 128 + l8 * REC + l7;
    unsigned v_vo[4], v_bc[4], v_bp[4], v_mk[4], v_sel[4] = {0, 0, 0, 0}, v_ci[4];
#pragma unroll
    for (unsigned i = 0; i < 4; ++i) {
      const unsigned kk = 4 * g + i, ka = 4 * g + 2 * (i & 1) + par;
      v_bc[i] = (i & 1) == par && kk <= i16 ? 0u : NEG;
      v_bp[i] = (i & 1) == par && kk >= i16 + 2 ? 0u : NEG;
      v_mk[i] = (i < 2 ? ka > i16 : ka <= i16) ? 0xffffu << (16 * par) : 0u;
      v_ci[i] = p == 2 ? 0x3f800000u : 0u;
    }
    v_sel[3] = lane == 16 ? 0x3f80u : lane == 17 ? 0x3f800000u : 0u;
#pragma unroll
    for (unsigned i = 0; i < 4; ++i) v_vo[i] = qvo(i, hg);
    const unsigned rb = 65536 + g * 4224 + q * 480;
    const unsigned v_at = rb + 36 * w + 4 * (w & 1) + 8 * p, v_ax = rb + (p < 2 ? p : 2) * 144 + 72 * (w >> 1) + 32;
    const unsigned v_pm = (16 * g + 4 * (i16 >> 1 & 3) + 2 * (w & 1) + par) * 4, v_ms = i16 < 6 ? ~0u : 0u;
    const unsigned s_sx = WS_XIN + (gr * NREC + KSK) * REC + w * 4096, s_ev = s_sx + (32 - KSK) * REC, s_del = 0u - (gr ? NREC + 32 : 32) * REC;
    const unsigned s_kil = gr ? 0x7f7fffffu : 0xff7fffffu, s_so = WS_OB + (gr * NREC + KSK) * REC;
    const unsigned s_m4 = w * 4096, s_m5 = 16384 + w * 1024, s_ms = w * 4224, s_up = part + gr + w;
    asm volatile(
      "s_buffer_load_dword s21, s[8:11], 0\n"
      "s_mov_b32 s23, 65536\n"
      "s_mov_b32 s31, 0x40000000\n"
      "s_mov_b32 s51, 0\n"
      "s_add_u32 m0, s43, 65536\n"
      "s_nop 0\n"
      "buffer_load_dwordx4 v48, s[12:15], s51 offen offset:0 lds\n"
      "buffer_load_dwordx4 v49, s[12:15], s51 offen offset:1024 lds\n"
      "buffer_load_dwordx4 v50, s[12:15], s51 offen offset:2048 lds\n"
      "buffer_load_dwordx4 v51, s[12:15], s51 offen offset:3072 lds\n"
      "s_add_u32 s51, s51, 110592\n"
      "s_add_u32 m0, s43, 82432\n"
      "s_nop 0\n"
      "buffer_load_dwordx4 v48, s[12:15], s51 offen offset:0 lds\n"
      "buffer_load_dwordx4 v49, s[12:15], s51 offen offset:1024 lds\n"
      "buffer_load_dwordx4 v50, s[12:15], s51 offen offset:2048 lds\n"
      "buffer_load_dwordx4 v51, s[12:15], s51 offen offset:3072 lds\n"
      "s_add_u32 s51, s51, 110592\n"
      "s_add_u32 m0, s43, 99328\n"
      "s_nop 0\n"
      "buffer_load_dwordx4 v48, s[12:15], s51 offen offset:0 lds\n"
      "buffer_load_dwordx4 v49, s[12:15], s51 offen offset:1024 lds\n"
      "buffer_load_dwordx4 v50, s[12:15], s51 offen offset:2048 lds\n"
      "buffer_load_dwordx4 v51, s[12:15], s51 offen offset:3072 lds\n"
      "s_add_u32 s51, s51, 110592\n"
      "s_add_u32 s6, s36, 1572864\n"
      "s_add_u32 s36, s36, 4294942720\n"
      "s_add_u32 s48, s37, 811008\n"
      "s_mov_b32 s53, 0x7f7fffff\n"
      "v_mov_b32 v179, 0\n"
      "s_mov_b32 s54, 0xffff\n"
      "s_mov_b32 s55, 0\n"
      "v_mov_b32 v9, 0\n"
      "ds_write_b32 v11, v9\n"
      "v_mov_b32 v56, 0x3f803f80\n"
      "v_mov_b32 v57, 0x3f803f80\n"
      "v_mov_b32 v58, 0x3f803f80\n"
      "v_mov_b32 v59, 0x3f803f80\n"
      "s_mov_b64 exec, 0\n"
      "s_lshr_b32 s22, s41, 12\n"
      ".Lq_pl:\n"
      "s_cmp_eq_u32 s22, 0\n"
      "s_cbranch_scc1 .Lq_p0\n"
      "s_cmp_eq_u32 s22, 1\n"
      "s_cbranch_scc1 .Lq_p1\n"
      "s_cmp_eq_u32 s22, 3\n"
      "s_cbranch_scc1 .Lq_px\n"
      "s_branch .Lq_pe\n"
      ".Lq_pr:\n"
      "s_add_u32 s22, s22, 4\n"
      "s_branch .Lq_pl\n"
      ".Lq_pe:\n"
      "s_mov_b64 exec, -1\n"
      "s_waitcnt vmcnt(8) lgkmcnt(0)\n"
      "s_barrier\n"
      "ds_read_b64_tr_b16 v[60:61], v53 offset:0\n"
      "ds_read_b64_tr_b16 v[62:63], v53 offset:1920\n"
      "ds_read_b64_tr_b16 a[0:1], v52 offset:144\n"
      "ds_read_b64_tr_b16 a[2:3], v52 offset:2064\n"
      "ds_read_b64_tr_b16 a[72:73], v52 offset:288\n"
      "ds_read_b64_tr_b16 a[74:75], v52 offset:2208\n"
      "ds_read_b64_tr_b16 a[144:145], v52 offset:0\n"
      "ds_read_b64_tr_b16 a[146:147], v52 offset:1920\n"
      "s_add_u32 m0, s43, 116224\n"
      "s_nop 0\n"
      "buffer_load_dwordx4 v48, s[12:15], s51 offen offset:0 lds\n"
      "buffer_load_dwordx4 v49, s[12:15], s51 offen offset:1024 lds\n"
      "buffer_load_dwordx4 v50, s[12:15], s51 offen offset:2048 lds\n"
      "buffer_load_dwordx4 v51, s[12:15], s51 offen offset:3072 lds\n"
      "s_add_u32 s51, s51, 110592\n"
      "s_waitcnt vmcnt(8) lgkmcnt(0)\n"
      "s_barrier\n"
      "ds_read_b64_tr_b16 v[68:69], v53 offset:16896\n"
      "ds_read_b64_tr_b16 v[70:71], v53 offset:18816\n"
      "ds_read_b64_tr_b16 a[4:5], v52 offset:17040\n"
      "ds_read_b64_tr_b16 a[6:7], v52 offset:18960\n"
      "ds_read_b64_tr_b16 a[76:77], v52 offset:17184\n"
      "ds_read_b64_tr_b16 a[78:79], v52 offset:19104\n"
      "ds_read_b64_tr_b16 a[148:149], v52 offset:16896\n"
      "ds_read_b64_tr_b16 a[150:151], v52 offset:18816\n"
      "ds_bpermute_b32 v64, v54, v60\n"
      "ds_bpermute_b32 v65, v54, v61\n"
      "ds_bpermute_b32 v66, v54, v62\n"
      "ds_bpermute_b32 v67, v54, v63\n"
      "v_mfma_f32_16x16x32_bf16 v[136:139], a[0:3], v[56:59], 0\n"
      "v_mfma_f32_16x16x32_bf16 v[144:147], v[56:59], a[72:75], 0\n"
      "v_mfma_f32_16x16x32_bf16 v[148:151], a[144:147], v[56:59], 0\n"
      "s_add_u32 m0, s43, 65536\n"
      "s_nop 0\n"
      "buffer_load_dwordx4 v48, s[12:15], s51 offen offset:0 lds\n"
      "buffer_load_dwordx4 v49, s[12:15], s51 offen offset:1024 lds\n"
      "buffer_load_dwordx4 v50, s[12:15], s51 offen offset:2048 lds\n"
      "buffer_load_dwordx4 v51, s[12:15], s51 offen offset:3072 lds\n"
      "s_add_u32 s51, s51, 110592\n"
      "s_waitcnt vmcnt(8) lgkmcnt(0)\n"
      "s_barrier\n"
      "ds_read_b64_tr_b16 v[60:61], v53 offset:33792\n"
      "ds_read_b64_tr_b16 v[62:63], v53 offset:35712\n"
      "ds_read_b64_tr_b16 a[8:9], v52 offset:33936\n"
      "ds_read_b64_tr_b16 a[10:11], v52 offset:35856\n"
      "ds_read_b64_tr_b16 a[80:81], v52 offset:34080\n"
      "ds_read_b64_tr_b16 a[82:83], v52 offset:36000\n"
      "ds_read_b64_tr_b16 a[152:153], v52 offset:33792\n"
      "ds_read_b64_tr_b16 a[154:155], v52 offset:35712\n"
      "ds_bpermute_b32 v72, v54, v68\n"
      "ds_bpermute_b32 v73, v54, v69\n"
      "ds_bpermute_b32 v74, v54, v70\n"
      "ds_bpermute_b32 v75, v54, v71\n"
      "v_mfma_f32_16x16x32_bf16 v[136:139], a[4:7], v[56:59], v[136:139]\n"
      "v_mfma_f32_16x16x32_bf16 v[144:147], v[56:59], a[76:79], v[144:147]\n"
      "v_mfma_f32_16x16x32_bf16 v[148:151], a[148:151], v[56:59], v[148:151]\n"
      "s_add_u32 m0, s43, 82432\n"
      "s_nop 0\n"
      "buffer_load_dwordx4 v48, s[12:15], s51 offen offset:0 lds\n"
      "buffer_load_dwordx4 v49, s[12:15], s51 offen offset:1024 lds\n"
      "buffer_load_dwordx4 v50, s[12:15], s51 offen offset:2048 lds\n"
      "buffer_load_dwordx4 v51, s[12:15], s51 offen offset:3072 lds\n"
      "s_add_u32 s51, s51, 110592\n"
      "v_and_b32 v64, v55, v64\n"
      "v_and_b32 v65, v55, v65\n"
      "v_and_b32 v66, v55, v66\n"
      "v_and_b32 v67, v55, v67\n"
      "v_accvgpr_write_b32 a216, v64\n"
      "v_accvgpr_write_b32 a217, v65\n"
      "v_accvgpr_write_b32 a218, v66\n"
      "v_accvgpr_write_b32 a219, v67\n"
      "v_mfma_f32_16x16x32_bf16 v[140:143], v[64:67], v[56:59], 0\n"
      "s_waitcnt vmcnt(8) lgkmcnt(0)\n"
      "s_barrier\n"
      "ds_read_b64_tr_b16 v[68:69], v53 offset:50688\n"
      "ds_read_b64_tr_b16 v[70:71], v53 offset:52608\n"
      "ds_read_b64_tr_b16 a[12:13], v52 offset:50832\n"
      "ds_read_b64_tr_b16 a[14:15], v52 offset:52752\n"
      "ds_read_b64_tr_b16 a[84:85], v52 offset:50976\n"
      "ds_read_b64_tr_b16 a[86:87], v52 offset:52896\n"
      "ds_read_b64_tr_b16 a[156:157], v52 offset:50688\n"
      "ds_read_b64_tr_b16 a[158:159], v52 offset:52608\n"
      "ds_bpermute_b32 v64, v54, v60\n"
      "ds_bpermute_b32 v65, v54, v61\n"
      "ds_bpermute_b32 v66, v54, v62\n"
      "ds_bpermute_b32 v67, v54, v63\n"
      "v_mfma_f32_16x16x32_bf16 v[136:139], a[8:11], v[56:59], v[136:139]\n"
      "v_mfma_f32_16x16x32_bf16 v[144:147], v[56:59], a[80:83], v[144:147]\n"
      "v_mfma_f32_16x16x32_bf16 v[148:151], a[152:155], v[56:59], v[148:151]\n"
      "s_add_u32 m0, s43, 99328\n"
      "s_nop 0\n"
      "buffer_load_dwordx4 v48, s[12:15], s51 offen offset:0 lds\n"
      "buffer_load_dwordx4 v49, s[12:15], s51 offen offset:1024 lds\n"
      "buffer_load_dwordx4 v50, s[12:15], s51 offen offset:2048 lds\n"
      "buffer_load_dwordx4 v51, s[12:15], s51 offen offset:3072 lds\n"
      "s_add_u32 s51, s51, 110592\n"
      "v_and_b32 v72, v55, v72\n"
      "v_and_b32 v73, v55, v73\n"
      "v_and_b32 v74, v55, v74\n"
      "v_and_b32 v75, v55, v75\n"
      "v_accvgpr_write_b32 a220, v72\n"
      "v_accvgpr_write_b32 a221, v73\n"
      "v_accvgpr_write_b32 a222, v74\n"
      "v_accvgpr_write_b32 a223, v75\n"
      "v_mfma_f32_16x16x32_bf16 v[140:143], v[72:75], v[56:59], v[140:143]\n"
      "s_waitcnt vmcnt(8) lgkmcnt(0)\n"
      "s_barrier\n"
      "ds_read_b64_tr_b16 v[60:61], v53 offset:0\n"
      "ds_read_b64_tr_b16 v[62:63], v53 offset:1920\n"
      "ds_read_b64_tr_b16 a[16:17], v52 offset:144\n"
      "ds_read_b64_tr_b16 a[18:19], v52 offset:2064\n"
      "ds_read_b64_tr_b16 a[88:89], v52 offset:288\n"
      "ds_read_b64_tr_b16 a[90:91], v52 offset:2208\n"
      "ds_read_b64_tr_b16 a[160:161], v52 offset:0\n"
      "ds_read_b64_tr_b16 a[162:163], v52 offset:1920\n"
      "ds_bpermute_b32 v72, v54, v68\n"
      "ds_bpermute_b32 v73, v54, v69\n"
      "ds_bpermute_b32 v74, v54, v70\n"
      "ds_bpermute_b32 v75, v54, v71\n"
      "v_mfma_f32_16x16x32_bf16 v[136:139], a[12:15], v[56:59], v[136:139]\n"
      "v_mfma_f32_16x16x32_bf16 v[144:147], v[56:59], a[84:87], v[144:147]\n"
      "v_mfma_f32_16x16x32_bf16 v[148:151], a[156:159], v[56:59], v[148:151]\n"
      "s_add_u32 m0, s43, 116224\n"
      "s_nop 0\n"
      "buffer_load_dwordx4 v48, s[12:15], s51 offen offset:0 lds\n"
      "buffer_load_dwordx4 v49, s[12:15], s51 offen offset:1024 lds\n"
      "buffer_load_dwordx4 v50, s[12:15], s51 offen offset:2048 lds\n"
      "buffer_load_dwordx4 v51, s[12:15], s51 offen offset:3072 lds\n"
      "s_add_u32 s51, s51, 110592\n"
      "v_and_b32 v64, v55, v64\n"
      "v_and_b32 v65, v55, v65\n"
      "v_and_b32 v66, v55, v66\n"
      "v_and_b32 v67, v55, v67\n"
      "v_accvgpr_write_b32 a224, v64\n"
      "v_accvgpr_write_b32 a225, v65\n"
      "v_accvgpr_write_b32 a226, v66\n"
      "v_accvgpr_write_b32 a227, v67\n"
      "v_mfma_f32_16x16x32_bf16 v[140:143], v[64:67], v[56:59], v[140:143]\n"
      "s_waitcnt vmcnt(8) lgkmcnt(0)\n"
      "s_barrier\n"
      "ds_read_b64_tr_b16 v[68:69], v53 offset:16896\n"
      "ds_read_b64_tr_b16 v[70:71], v53 offset:18816\n"
      "ds_read_b64_tr_b16 a[20:21], v52 offset:17040\n"
      "ds_read_b64_tr_b16 a[22:23], v52 offset:18960\n"
      "ds_read_b64_tr_b16 a[92:93], v52 offset:17184\n"
      "ds_read_b64_tr_b16 a[94:95], v52 offset:19104\n"
      "ds_read_b64_tr_b16 a[164:165], v52 offset:16896\n"
      "ds_read_b64_tr_b16 a[166:167], v52 offset:18816\n"
      "ds_bpermute_b32 v64, v54, v60\n"
      "ds_bpermute_b32 v65, v54, v61\n"
      "ds_bpermute_b32 v66, v54, v62\n"
      "ds_bpermute_b32 v67, v54, v63\n"
      "v_mfma_f32_16x16x32_bf16 v[136:139], a[16:19], v[56:59], v[136:139]\n"
      "v_mfma_f32_16x16x32_bf16 v[144:147], v[56:59], a[88:91], v[144:147]\n"
      "v_mfma_f32_16x16x32_bf16 v[148:151], a[160:163], v[56:59], v[148:151]\n"
      "s_add_u32 m0, s43, 65536\n"
      "s_nop 0\n"
      "buffer_load_dwordx4 v48, s[12:15], s51 offen offset:0 lds\n"
      "buffer_load_dwordx4 v49, s[12:15], s51 offen offset:1024 lds\n"
      "buffer_load_dwordx4 v50, s[12:15], s51 offen offset:2048 lds\n"
      "buffer_load_dwordx4 v51, s[12:15], s51 offen offset:3072 lds\n"
      "s_add_u32 s51, s51, 110592\n"
      "v_and_b32 v72, v55, v72\n"
      "v_and_b32 v73, v55, v73\n"
      "v_and_b32 v74, v55, v74\n"
      "v_and_b32 v75, v55, v75\n"
      "v_accvgpr_write_b32 a228, v72\n"
      "v_accvgpr_write_b32 a229, v73\n"
      "v_accvgpr_write_b32 a230, v74\n"
      "v_accvgpr_write_b32 a231, v75\n"
      "v_mfma_f32_16x16x32_bf16 v[140:143], v[72:75], v[56:59], v[140:143]\n"
      "s_waitcnt vmcnt(8) lgkmcnt(0)\n"
      "s_barrier\n"
      "ds_read_b64_tr_b16 v[60:61], v53 offset:33792\n"
      "ds_read_b64_tr_b16 v[62:63], v53 offset:35712\n"
      "ds_read_b64_tr_b16 a[24:25], v52 offset:33936\n"
      "ds_read_b64_tr_b16 a[26:27], v52 offset:35856\n"
      "ds_read_b64_tr_b16 a[96:97], v52 offset:34080\n"
      "ds_read_b64_tr_b16 a[98:99], v52 offset:36000\n"
      "ds_read_b64_tr_b16 a[168:169], v52 offset:33792\n"
      "ds_read_b64_tr_b16 a[170:171], v52 offset:35712\n"
      "ds_bpermute_b32 v72, v54, v68\n"
      "ds_bpermute_b32 v73, v54, v69\n"
      "ds_bpermute_b32 v74, v54, v70\n"
      "ds_bpermute_b32 v75, v54, v71\n"
      "v_mfma_f32_16x16x32_bf16 v[136:139], a[20:23], v[56:59], v[136:139]\n"
      "v_mfma_f32_16x16x32_bf16 v[144:147], v[56:59], a[92:95], v[144:147]\n"
      "v_mfma_f32_16x16x32_bf16 v[148:151], a[164:167], v[56:59], v[148:151]\n"
      "s_add_u32 m0, s43, 82432\n"
      "s_nop 0\n"
      "buffer_load_dwordx4 v48, s[12:15], s51 offen offset:0 lds\n"
      "buffer_load_dwordx4 v49, s[12:15], s51 offen offset:1024 lds\n"
      "buffer_load_dwordx4 v50, s[12:15], s51 offen offset:2048 lds\n"
      "buffer_load_dwordx4 v51, s[12:15], s51 offen offset:3072 lds\n"
      "s_add_u32 s51, s51, 110592\n"
      "v_and_b32 v64, v55, v64\n"
      "v_and_b32 v65, v55, v65\n"
      "v_and_b32 v66, v55, v66\n"
      "v_and_b32 v67, v55, v67\n"
      "v_accvgpr_write_b32 a232, v64\n"
      "v_accvgpr_write_b32 a233, v65\n"
      "v_accvgpr_write_b32 a234, v66\n"
      "v_accvgpr_write_b32 a235, v67\n"
      "v_mfma_f32_16x16x32_bf16 v[140:143], v[64:67], v[56:59], v[140:143]\n"
      "s_waitcnt vmcnt(8) lgkmcnt(0)\n"
      "s_barrier\n"
      "ds_read_b64_tr_b16 v[68:69], v53 offset:50688\n"
      "ds_read_b64_tr_b16 v[70:71], v53 offset:52608\n"
      "ds_read_b64_tr_b16 a[28:29], v52 offset:50832\n"
      "ds_read_b64_tr_b16 a[30:31], v52 offset:52752\n"
      "ds_read_b64_tr_b16 a[100:101], v52 offset:50976\n"
      "ds_read_b64_tr_b16 a[102:103], v52 offset:52896\n"
      "ds_read_b64_tr_b16 a[172:173], v52 offset:50688\n"
      "ds_read_b64_tr_b16 a[174:175], v52 offset:52608\n"
      "ds_bpermute_b32 v64, v54, v60\n"
      "ds_bpermute_b32 v65, v54, v61\n"
      "ds_bpermute_b32 v66, v54, v62\n"
      "ds_bpermute_b32 v67, v54, v63\n"
      "v_mfma_f32_16x16x32_bf16 v[136:139], a[24:27], v[56:59], v[136:139]\n"
      "v_mfma_f32_16x16x32_bf16 v[144:147], v[56:59], a[96:99], v[144:147]\n"
      "v_mfma_f32_16x16x32_bf16 v[148:151], a[168:171], v[56:59], v[148:151]\n"
      "s_add_u32 m0, s43, 99328\n"
      "s_nop 0\n"
      "buffer_load_dwordx4 v48, s[12:15], s51 offen offset:0 lds\n"
      "buffer_load_dwordx4 v49, s[12:15], s51 offen offset:1024 lds\n"
      "buffer_load_dwordx4 v50, s[12:15], s51 offen offset:2048 lds\n"
      "buffer_load_dwordx4 v51, s[12:15], s51 offen offset:3072 lds\n"
      "s_add_u32 s51, s51, 110592\n"
      "v_and_b32 v72, v55, v72\n"
      "v_and_b32 v73, v55, v73\n"
      "v_and_b32 v74, v55, v74\n"
      "v_and_b32 v75, v55, v75\n"
      "v_accvgpr_write_b32 a236, v72\n"
      "v_accvgpr_write_b32 a237, v73\n"
      "v_accvgpr_write_b32 a238, v74\n"
      "v_accvgpr_write_b32 a239, v75\n"
      "v_mfma_f32_16x16x32_bf16 v[140:143], v[72:75], v[56:59], v[140:143]\n"
      "s_waitcnt vmcnt(8) lgkmcnt(0)\n"
      "s_barrier\n"
      "ds_read_b64_tr_b16 v[60:61], v53 offset:0\n"
      "ds_read_b64_tr_b16 v[62:63], v53 offset:1920\n"
      "ds_read_b64_tr_b16 a[32:33], v52 offset:144\n"
      "ds_read_b64_tr_b16 a[34:35], v52 offset:2064\n"
      "ds_read_b64_tr_b16 a[104:105], v52 offset:288\n"
      "ds_read_b64_tr_b16 a[106:107], v52 offset:2208\n"
      "ds_read_b64_tr_b16 a[176:177], v52 offset:0\n"
      "ds_read_b64_tr_b16 a[178:179], v52 offset:1920\n"
      "ds_bpermute_b32 v72, v54, v68\n"
      "ds_bpermute_b32 v73, v54, v69\n"
      "ds_bpermute_b32 v74, v54, v70\n"
      ".Lq_px:\n"
      "s_cbranch_execz .Lq_pr\n"
      "ds_bpermute_b32 v75, v54, v71\n"
      "v_mfma_f32_16x16x32_bf16 v[136:139], a[28:31], v[56:59], v[136:139]\n"
      "v_mfma_f32_16x16x32_bf16 v[144:147], v[56:59], a[100:103], v[144:147]\n"
      "v_mfma_f32_16x16x32_bf16 v[148:151], a[172:175], v[56:59], v[148:151]\n"
      "s_add_u32 m0, s43, 116224\n"
      "s_nop 0\n"
      "buffer_load_dwordx4 v48, s[12:15], s51 offen offset:0 lds\n"
      "buffer_load_dwordx4 v49, s[12:15], s51 offen offset:1024 lds\n"
      "buffer_load_dwordx4 v50, s[12:15], s51 offen offset:2048 lds\n"
      "buffer_load_dwordx4 v51, s[12:15], s51 offen offset:3072 lds\n"
      "s_add_u32 s51, s51, 110592\n"
      "v_and_b32 v64, v55, v64\n"
      "v_and_b32 v65, v55, v65\n"
      "v_and_b32 v66, v55, v66\n"
      "v_and_b32 v67, v55, v67\n"
      "v_accvgpr_write_b32 a240, v64\n"
      "v_accvgpr_write_b32 a241, v65\n"
      "v_accvgpr_write_b32 a242, v66\n"
      "v_accvgpr_write_b32 a243, v67\n"
      "v_mfma_f32_16x16x32_bf16 v[140:143], v[64:67], v[56:59], v[140:143]\n"
      "s_waitcnt vmcnt(8) lgkmcnt(0)\n"
      "s_barrier\n"
      "ds_read_b64_tr_b16 v[68:69], v53 offset:16896\n"
      "ds_read_b64_tr_b16 v[70:71], v53 offset:18816\n"
      "ds_read_b64_tr_b16 a[36:37], v52 offset:17040\n"
      "ds_read_b64_tr_b16 a[38:39], v52 offset:18960\n"
      "ds_read_b64_tr_b16 a[108:109], v52 offset:17184\n"
      "ds_read_b64_tr_b16 a[110:111], v52 offset:19104\n"
      "ds_read_b64_tr_b16 a[180:181], v52 offset:16896\n"
      "ds_read_b64_tr_b16 a[182:183], v52 offset:18816\n"
      "ds_bpermute_b32 v64, v54, v60\n"
      "ds_bpermute_b32 v65, v54, v61\n"
      "ds_bpermute_b32 v66, v54, v62\n"
      "ds_bpermute_b32 v67, v54, v63\n"
      "v_mfma_f32_16x16x32_bf16 v[136:139], a[32:35], v[56:59], v[136:139]\n"
      "v_mfma_f32_16x16x32_bf16 v[144:147], v[56:59], a[104:107], v[144:147]\n"
      "v_mfma_f32_16x16x32_bf16 v[148:151], a[176:179], v[56:59], v[148:151]\n"
      "s_add_u32 m0, s43, 65536\n"
      "s_nop 0\n"
      "buffer_load_dwordx4 v48, s[12:15], s51 offen offset:0 lds\n"
      "buffer_load_dwordx4 v49, s[12:15], s51 offen offset:1024 lds\n"
      "buffer_load_dwordx4 v50, s[12:15], s51 offen offset:2048 lds\n"
      "buffer_load_dwordx4 v51, s[12:15], s51 offen offset:3072 lds\n"
      "s_add_u32 s51, s51, 110592\n"
      "v_and_b32 v72, v55, v72\n"
      "v_and_b32 v73, v55, v73\n"
      "v_and_b32 v74, v55, v74\n"
      "v_and_b32 v75, v55, v75\n"
      "v_accvgpr_write_b32 a244, v72\n"
      "v_accvgpr_write_b32 a245, v73\n"
      "v_accvgpr_write_b32 a246, v74\n"
      "v_accvgpr_write_b32 a247, v75\n"
      "v_mfma_f32_16x16x32_bf16 v[140:143], v[72:75], v[56:59], v[140:143]\n"
      "s_waitcnt vmcnt(8) lgkmcnt(0)\n"
      "s_barrier\n"
      "ds_read_b64_tr_b16 v[60:61], v53 offset:33792\n"
      "ds_read_b64_tr_b16 v[62:63], v53 offset:35712\n"
      "ds_read_b64_tr_b16 a[40:41], v52 offset:33936\n"
      "ds_read_b64_tr_b16 a[42:43], v52 offset:35856\n"
      "ds_read_b64_tr_b16 a[112:113], v52 offset:34080\n"
      "ds_read_b64_tr_b16 a[114:115], v52 offset:36000\n"
      "ds_read_b64_tr_b16 a[184:185], v52 offset:33792\n"
      "ds_read_b64_tr_b16 a[186:187], v52 offset:35712\n"
      "ds_bpermute_b32 v72, v54, v68\n"
      "ds_bpermute_b32 v73, v54, v69\n"
      "ds_bpermute_b32 v74, v54, v70\n"
      "ds_bpermute_b32 v75, v54, v71\n"
      "v_mfma_f32_16x16x32_bf16 v[136:139], a[36:39], v[56:59], v[136:139]\n"
      "v_mfma_f32_16x16x32_bf16 v[144:147], v[56:59], a[108:111], v[144:147]\n"
      "v_mfma_f32_16x16x32_bf16 v[148:151], a[180:183], v[56:59], v[148:151]\n"
      "s_add_u32 m0, s43, 82432\n"
      "s_nop 0\n"
      "buffer_load_dwordx4 v48, s[12:15], s51 offen offset:0 lds\n"
      "buffer_load_dwordx4 v49, s[12:15], s51 offen offset:1024 lds\n"
      "buffer_load_dwordx4 v50, s[12:15], s51 offen offset:2048 lds\n"
      "buffer_load_dwordx4 v51, s[12:15], s51 offen offset:3072 lds\n"
      "s_add_u32 s51, s51, 110592\n"
      "v_and_b32 v64, v55, v64\n"
      "v_and_b32 v65, v55, v65\n"
      "v_and_b32 v66, v55, v66\n"
      "v_and_b32 v67, v55, v67\n"
      "v_accvgpr_write_b32 a248, v64\n"
      "v_accvgpr_write_b32 a249, v65\n"
      "v_accvgpr_write_b32 a250, v66\n"
      "v_accvgpr_write_b32 a251, v67\n"
      "v_mfma_f32_16x16x32_bf16 v[140:143], v[64:67], v[56:59], v[140:143]\n"
      "s_waitcnt vmcnt(8) lgkmcnt(0)\n"
      "s_barrier\n"
      "ds_read_b64_tr_b16 v[68:69], v53 offset:50688\n"
      "ds_read_b64_tr_b16 v[70:71], v53 offset:52608\n"
      "ds_read_b64_tr_b16 a[44:45], v52 offset:50832\n"
      "ds_read_b64_tr_b16 a[46:47], v52 offset:52752\n"
      "ds_read_b64_tr_b16 a[116:117], v52 offset:50976\n"
      "ds_read_b64_tr_b16 a[118:119], v52 offset:52896\n"
      "ds_read_b64_tr_b16 a[188:189], v52 offset:50688\n"
      "ds_read_b64_tr_b16 a[190:191], v52 offset:52608\n"
      "ds_bpermute_b32 v64, v54, v60\n"
      "ds_bpermute_b32 v65, v54, v61\n"
      "ds_bpermute_b32 v66, v54, v62\n"
      "ds_bpermute_b32 v67, v54, v63\n"
      "v_mfma_f32_16x16x32_bf16 v[136:139], a[40:43], v[56:59], v[136:139]\n"
      "v_mfma_f32_16x16x32_bf16 v[144:147], v[56:59], a[112:115], v[144:147]\n"
      "v_mfma_f32_16x16x32_bf16 v[148:151], a[184:187], v[56:59], v[148:151]\n"
      "s_add_u32 m0, s43, 99328\n"
      "s_nop 0\n"
      "buffer_load_dwordx4 v48, s[12:15], s51 offen offset:0 lds\n"
      "buffer_load_dwordx4 v49, s[12:15], s51 offen offset:1024 lds\n"
      "buffer_load_dwordx4 v50, s[12:15], s51 offen offset:2048 lds\n"
      "buffer_load_dwordx4 v51, s[12:15], s51 offen offset:3072 lds\n"
      "s_add_u32 s51, s51, 110592\n"
      "v_and_b32 v72, v55, v72\n"
      "v_and_b32 v73, v55, v73\n"
      "v_and_b32 v74, v55, v74\n"
      "v_and_b32 v75, v55, v75\n"
      "v_accvgpr_write_b32 a252, v72\n"
      "v_accvgpr_write_b32 a253, v73\n"
      "v_accvgpr_write_b32 a254, v74\n"
      "v_accvgpr_write_b32 a255, v75\n"
      "v_mfma_f32_16x16x32_bf16 v[140:143], v[72:75], v[56:59], v[140:143]\n"
      "s_waitcnt vmcnt(8) lgkmcnt(0)\n"
      "s_barrier\n"
      "ds_read_b64_tr_b16 v[60:61], v53 offset:0\n"
      "ds_read_b64_tr_b16 v[62:63], v53 offset:1920\n"
      "ds_read_b64_tr_b16 a[48:49], v52 offset:144\n"
      "ds_read_b64_tr_b16 a[50:51], v52 offset:2064\n"
      "ds_read_b64_tr_b16 a[120:121], v52 offset:288\n"
      "ds_read_b64_tr_b16 a[122:123], v52 offset:2208\n"
      "ds_read_b64_tr_b16 a[192:193], v52 offset:0\n"
      "ds_read_b64_tr_b16 a[194:195], v52 offset:1920\n"
      "ds_bpermute_b32 v72, v54, v68\n"
      "ds_bpermute_b32 v73, v54, v69\n"
      "ds_bpermute_b32 v74, v54, v70\n"
      "ds_bpermute_b32 v75, v54, v71\n"
      "v_mfma_f32_16x16x32_bf16 v[136:139], a[44:47], v[56:59], v[136:139]\n"
      "v_mfma_f32_16x16x32_bf16 v[144:147], v[56:59], a[116:119], v[144:147]\n"
      "v_mfma_f32_16x16x32_bf16 v[148:151], a[188:191], v[56:59], v[148:151]\n"
      "s_add_u32 m0, s43, 116224\n"
      "s_nop 0\n"
      "buffer_load_dwordx4 v48, s[12:15], s51 offen offset:0 lds\n"
      "buffer_load_dwordx4 v49, s[12:15], s51 offen offset:1024 lds\n"
      "buffer_load_dwordx4 v50, s[12:15], s51 offen offset:2048 lds\n"
      "buffer_load_dwordx4 v51, s[12:15], s51 offen offset:3072 lds\n"
      "s_add_u32 s51, s51, 110592\n"
      "v_and_b32 v64, v55, v64\n"
      "v_and_b32 v65, v55, v65\n"
      "v_and_b32 v66, v55, v66\n"
      "v_and_b32 v67, v55, v67\n"
      "v_mov_b32 v16, v64\n"
      "v_mov_b32 v17, v65\n"
      "v_mov_b32 v18, v66\n"
      "v_mov_b32 v19, v67\n"
      "v_mfma_f32_16x16x32_bf16 v[140:143], v[64:67], v[56:59], v[140:143]\n"
      "s_add_u32 s30, s36, 24576\n"
      "buffer_load_dword v204, v2, s[8:11], s30 offen sc1\n"
      "s_add_u32 s30, s36, 49152\n"
      "buffer_load_dword v205, v2, s[8:11], s30 offen sc1\n"
      "s_waitcnt vmcnt(10) lgkmcnt(0)\n"
      "s_barrier\n"
      "ds_read_b64_tr_b16 v[68:69], v53 offset:16896\n"
      "ds_read_b64_tr_b16 v[70:71], v53 offset:18816\n"
      "ds_read_b64_tr_b16 a[52:53], v52 offset:17040\n"
      "ds_read_b64_tr_b16 a[54:55], v52 offset:18960\n"
      "ds_read_b64_tr_b16 a[124:125], v52 offset:17184\n"
      "ds_read_b64_tr_b16 a[126:127], v52 offset:19104\n"
      "ds_read_b64_tr_b16 a[196:197], v52 offset:16896\n"
      "ds_read_b64_tr_b16 a[198:199], v52 offset:18816\n"
      "ds_bpermute_b32 v64, v54, v60\n"
      "ds_bpermute_b32 v65, v54, v61\n"
      "ds_bpermute_b32 v66, v54, v62\n"
      "ds_bpermute_b32 v67, v54, v63\n"
      "v_mfma_f32_16x16x32_bf16 v[136:139], a[48:51], v[56:59], v[136:139]\n"
      "v_mfma_f32_16x16x32_bf16 v[144:147], v[56:59], a[120:123], v[144:147]\n"
      "v_mfma_f32_16x16x32_bf16 v[148:151], a[192:195], v[56:59], v[148:151]\n"
      "s_add_u32 m0, s43, 65536\n"
      "s_nop 0\n"
      "buffer_load_dwordx4 v48, s[12:15], s51 offen offset:0 lds\n"
      "buffer_load_dwordx4 v49, s[12:15], s51 offen offset:1024 lds\n"
      "buffer_load_dwordx4 v50, s[12:15], s51 offen offset:2048 lds\n"
      "buffer_load_dwordx4 v51, s[12:15], s51 offen offset:3072 lds\n"
      "s_add_u32 s51, s51, 110592\n"
      "v_and_b32 v72, v55, v72\n"
      "v_and_b32 v73, v55, v73\n"
      "v_and_b32 v74, v55, v74\n"
      "v_and_b32 v75, v55, v75\n"
      "v_mov_b32 v20, v72\n"
      "v_mov_b32 v21, v73\n"
      "v_mov_b32 v22, v74\n"
      "v_mov_b32 v23, v75\n"
      "v_mfma_f32_16x16x32_bf16 v[140:143], v[72:75], v[56:59], v[140:143]\n"
      "s_waitcnt vmcnt(10) lgkmcnt(0)\n"
      "s_barrier\n"
      "ds_read_b64_tr_b16 v[60:61], v53 offset:33792\n"
      "ds_read_b64_tr_b16 v[62:63], v53 offset:35712\n"
      "ds_read_b64_tr_b16 a[56:57], v52 offset:33936\n"
      "ds_read_b64_tr_b16 a[58:59], v52 offset:35856\n"
      "ds_read_b64_tr_b16 a[128:129], v52 offset:34080\n"
      "ds_read_b64_tr_b16 a[130:131], v52 offset:36000\n"
      "ds_read_b64_tr_b16 a[200:201], v52 offset:33792\n"
      "ds_read_b64_tr_b16 a[202:203], v52 offset:35712\n"
      "ds_bpermute_b32 v72, v54, v68\n"
      "ds_bpermute_b32 v73, v54, v69\n"
      "ds_bpermute_b32 v74, v54, v70\n"
      "ds_bpermute_b32 v75, v54, v71\n"
      "v_mfma_f32_16x16x32_bf16 v[136:139], a[52:55], v[56:59], v[136:139]\n"
      "v_mfma_f32_16x16x32_bf16 v[144:147], v[56:59], a[124:127], v[144:147]\n"
      "v_mfma_f32_16x16x32_bf16 v[148:151], a[196:199], v[56:59], v[148:151]\n"
      "s_add_u32 m0, s43, 82432\n"
      "s_nop 0\n"
      "buffer_load_dwordx4 v48, s[12:15], s51 offen offset:0 lds\n"
      "buffer_load_dwordx4 v49, s[12:15], s51 offen offset:1024 lds\n"
      "buffer_load_dwordx4 v50, s[12:15], s51 offen offset:2048 lds\n"
      "buffer_load_dwordx4 v51, s[12:15], s51 offen offset:3072 lds\n"
      "s_add_u32 s51, s51, 110592\n"
      "v_and_b32 v64, v55, v64\n"
      "v_and_b32 v65, v55, v65\n"
      "v_and_b32 v66, v55, v66\n"
      "v_and_b32 v67, v55, v67\n"
      "v_mov_b32 v24, v64\n"
      "v_mov_b32 v25, v65\n"
      "v_mov_b32 v26, v66\n"
      "v_mov_b32 v27, v67\n"
      "v_mfma_f32_16x16x32_bf16 v[140:143], v[64:67], v[56:59], v[140:143]\n"
      "s_mul_i32 s21, s21, 1664525\n"
      "s_add_u32 s21, s21, 1013904223\n"
      "v_mov_b32 v13, s21\n"
      "buffer_store_dword v13, v10, s[8:11], 0 offen sc1\n"
      "v_not_b32 v200, v13\n"
      "v_not_b32 v201, v13\n"
      "v_not_b32 v202, v13\n"
      "v_not_b32 v203, v13\n"
      "s_add_u32 s30, s40, 0\n"
      "buffer_store_dwordx4 v[200:203], v76, s[8:11], s30 offen sc1\n"
      "v_xor_b32 v13, v13, v14\n"
      "s_add_u32 s52, s40, 4294893568\n"
      "s_add_u32 s40, s40, 4294918144\n"
      "s_add_u32 s36, s36, 24576\n"
      "s_mov_b32 s45, s36\n"
      "s_min_u32 s45, s45, s6\n"
      "s_waitcnt vmcnt(11)\n"
      "v_cmp_ne_u32 vcc, s21, v204\n"
      "s_add_u32 m0, s41, 0\n"
      "s_add_u32 s36, s36, 24576\n"
      "s_mov_b32 s46, s45\n"
      "s_cbranch_vccnz .Lsp_q0_1\n"
      ".Lbk_q0_1:\n"
      "buffer_load_dwordx4 v0, s[8:11], s46 offen offset:1024 sc1 lds\n"
      "buffer_load_dwordx4 v0, s[8:11], s46 offen offset:0 sc1 lds\n"
      "buffer_load_dwordx4 v0, s[8:11], s46 offen offset:2048 sc1 lds\n"
      "s_mov_b32 s45, s36\n"
      "s_min_u32 s45, s45, s6\n"
      "buffer_load_dwordx4 v0, s[8:11], s46 offen offset:3072 sc1 lds\n"
      "s_add_u32 m0, s42, 0\n"
      "s_nop 0\n"
      "buffer_load_dwordx4 v1, s[8:11], s46 offen offset:0 sc1 lds\n"
      "s_waitcnt lgkmcnt(0)\n"
      "s_barrier\n"
      "ds_read_b64_tr_b16 v[68:69], v53 offset:50688\n"
      "ds_read_b64_tr_b16 v[70:71], v53 offset:52608\n"
      "ds_read_b64_tr_b16 a[60:61], v52 offset:50832\n"
      "ds_read_b64_tr_b16 a[62:63], v52 offset:52752\n"
      "ds_read_b64_tr_b16 a[132:133], v52 offset:50976\n"
      "ds_read_b64_tr_b16 a[134:135], v52 offset:52896\n"
      "ds_read_b64_tr_b16 a[204:205], v52 offset:50688\n"
      "ds_read_b64_tr_b16 a[206:207], v52 offset:52608\n"
      "ds_bpermute_b32 v64, v54, v60\n"
      "ds_bpermute_b32 v65, v54, v61\n"
      "ds_bpermute_b32 v66, v54, v62\n"
      "ds_bpermute_b32 v67, v54, v63\n"
      "v_mfma_f32_16x16x32_bf16 v[136:139], a[56:59], v[56:59], v[136:139]\n"
      "v_mfma_f32_16x16x32_bf16 v[144:147], v[56:59], a[128:131], v[144:147]\n"
      "v_mfma_f32_16x16x32_bf16 v[148:151], a[200:203], v[56:59], v[148:151]\n"
      "v_and_b32 v72, v55, v72\n"
      "v_and_b32 v73, v55, v73\n"
      "v_and_b32 v74, v55, v74\n"
      "v_and_b32 v75, v55, v75\n"
      "v_mov_b32 v28, v72\n"
      "v_mov_b32 v29, v73\n"
      "v_mov_b32 v30, v74\n"
      "v_mov_b32 v31, v75\n"
      "v_mfma_f32_16x16x32_bf16 v[140:143], v[72:75], v[56:59], v[140:143]\n"
      "s_waitcnt vmcnt(11) lgkmcnt(0)\n"
      "s_barrier\n"
      "ds_read_b64_tr_b16 v[60:61], v53 offset:0\n"
      "ds_read_b64_tr_b16 v[62:63], v53 offset:1920\n"
      "ds_read_b64_tr_b16 a[64:65], v52 offset:144\n"
      "ds_read_b64_tr_b16 a[66:67], v52 offset:2064\n"
      "ds_read_b64_tr_b16 a[136:137], v52 offset:288\n"
      "ds_read_b64_tr_b16 a[138:139], v52 offset:2208\n"
      "ds_read_b64_tr_b16 a[208:209], v52 offset:0\n"
      "ds_read_b64_tr_b16 a[210:211], v52 offset:1920\n"
      "ds_bpermute_b32 v72, v54, v68\n"
      "ds_bpermute_b32 v73, v54, v69\n"
      "ds_bpermute_b32 v74, v54, v70\n"
      "ds_bpermute_b32 v75, v54, v71\n"
      "v_mfma_f32_16x16x32_bf16 v[136:139], a[60:63], v[56:59], v[136:139]\n"
      "v_mfma_f32_16x16x32_bf16 v[144:147], v[56:59], a[132:135], v[144:147]\n"
      "v_mfma_f32_16x16x32_bf16 v[148:151], a[204:207], v[56:59], v[148:151]\n"
      "v_and_b32 v64, v55, v64\n"
      "v_and_b32 v65, v55, v65\n"
      "v_and_b32 v66, v55, v66\n"
      "v_and_b32 v67, v55, v67\n"
      "v_mov_b32 v32, v64\n"
      "v_mov_b32 v33, v65\n"
      "v_mov_b32 v34, v66\n"
      "v_mov_b32 v35, v67\n"
      "v_mfma_f32_16x16x32_bf16 v[140:143], v[64:67], v[56:59], v[140:143]\n"
      "s_waitcnt vmcnt(7) lgkmcnt(0)\n"
      "s_barrier\n"
      "ds_read_b64_tr_b16 v[68:69], v53 offset:16896\n"
      "ds_read_b64_tr_b16 v[70:71], v53 offset:18816\n"
      "ds_read_b64_tr_b16 a[68:69], v52 offset:17040\n"
      "ds_read_b64_tr_b16 a[70:71], v52 offset:18960\n"
      "ds_read_b64_tr_b16 a[140:141], v52 offset:17184\n"
      "ds_read_b64_tr_b16 a[142:143], v52 offset:19104\n"
      "ds_read_b64_tr_b16 a[212:213], v52 offset:16896\n"
      "ds_read_b64_tr_b16 a[214:215], v52 offset:18816\n"
      "ds_bpermute_b32 v64, v54, v60\n"
      "ds_bpermute_b32 v65, v54, v61\n"
      "ds_bpermute_b32 v66, v54, v62\n"
      "ds_bpermute_b32 v67, v54, v63\n"
      "v_mfma_f32_16x16x32_bf16 v[136:139], a[64:67], v[56:59], v[136:139]\n"
      "v_mfma_f32_16x16x32_bf16 v[144:147], v[56:59], a[136:139], v[144:147]\n"
      "v_mfma_f32_16x16x32_bf16 v[148:151], a[208:211], v[56:59], v[148:151]\n"
      "v_and_b32 v72, v55, v72\n"
      "v_and_b32 v73, v55, v73\n"
      "v_and_b32 v74, v55, v74\n"
      "v_and_b32 v75, v55, v75\n"
      "v_mov_b32 v36, v72\n"
      "v_mov_b32 v37, v73\n"
      "v_mov_b32 v38, v74\n"
      "v_mov_b32 v39, v75\n"
      "v_mfma_f32_16x16x32_bf16 v[140:143], v[72:75], v[56:59], v[140:143]\n"
      "s_waitcnt lgkmcnt(0)\n"
      "ds_bpermute_b32 v72, v54, v68\n"
      "ds_bpermute_b32 v73, v54, v69\n"
      "ds_bpermute_b32 v74, v54, v70\n"
      "ds_bpermute_b32 v75, v54, v71\n"
      "v_mfma_f32_16x16x32_bf16 v[136:139], a[68:71], v[56:59], v[136:139]\n"
      "v_mfma_f32_16x16x32_bf16 v[144:147], v[56:59], a[140:143], v[144:147]\n"
      "v_mfma_f32_16x16x32_bf16 v[148:151], a[212:215], v[56:59], v[148:151]\n"
      "v_and_b32 v64, v55, v64\n"
      "v_and_b32 v65, v55, v65\n"
      "v_and_b32 v66, v55, v66\n"
      "v_and_b32 v67, v55, v67\n"
      "v_mov_b32 v40, v64\n"
      "v_mov_b32 v41, v65\n"
      "v_mov_b32 v42, v66\n"
      "v_mov_b32 v43, v67\n"
      "v_mfma_f32_16x16x32_bf16 v[140:143], v[64:67], v[56:59], v[140:143]\n"
      "s_waitcnt lgkmcnt(0)\n"
      "v_and_b32 v72, v55, v72\n"
      "v_and_b32 v73, v55, v73\n"
      "v_and_b32 v74, v55, v74\n"
      "v_and_b32 v75, v55, v75\n"
      "v_mov_b32 v44, v72\n"
      "v_mov_b32 v45, v73\n"
      "v_mov_b32 v46, v74\n"
      "v_mov_b32 v47, v75\n"
      "v_mfma_f32_16x16x32_bf16 v[140:143], v[72:75], v[56:59], v[140:143]\n"
      "s_add_u32 s36, s36, 24576\n"
      "s_cmp_eq_u32 s36, s37\n"
      "s_cselect_b32 s47, s38, 0\n"
      "s_cselect_b32 s37, s48, s37\n"
      "s_cselect_b32 s49, s39, s53\n"
      "s_add_u32 m0, s41, 20480\n"
      "s_mov_b32 s46, s45\n"
      "s_add_u32 s45, s36, s47\n"
      "v_add_u32 v9, 1, v9\n"
      "v_cmp_ne_u32 vcc, s21, v205\n"
      "s_min_u32 s45, s45, s6\n"
      "s_cbranch_vccnz .Lsp_q1_2\n"
      ".Lbk_q1_2:\n"
      "buffer_load_dwordx4 v0, s[8:11], s46 offen offset:1024 sc1 lds\n"
      "s_waitcnt vmcnt(1)\n"
      "ds_write_b32 v11, v9\n"
      "buffer_load_dwordx4 v0, s[8:11], s46 offen offset:0 sc1 lds\n"
      "buffer_load_dwordx4 v0, s[8:11], s46 offen offset:3072 sc1 lds\n"
      "buffer_load_dword v15, v2, s[8:11], s45 offen sc1\n"
      "ds_read_b32 v10, v12\n"
      "buffer_load_dwordx4 v0, s[8:11], s46 offen offset:2048 sc1 lds\n"
      "s_waitcnt lgkmcnt(0)\n"
      "v_cmp_gt_i32 vcc, v9, v10\n"
      "s_add_u32 m0, s42, 20480\n"
      "s_nop 0\n"
      "buffer_load_dwordx4 v1, s[8:11], s46 offen offset:0 sc1 lds\n"
      "s_cbranch_vccnz .Lsp_qy0_3\n"
      ".Lbk_qy0_3:\n"
      "ds_read_b128 v[52:55], v0 offset:1024\n"
      "ds_read_b128 v[48:51], v0 offset:0\n"
      "ds_read_b128 v[56:59], v0 offset:2048\n"
      "ds_read_b128 v[68:71], v0 offset:5120\n"
      "ds_read_b128 v[64:67], v0 offset:4096\n"
      "ds_read_b128 v[60:63], v0 offset:3072\n"
      "buffer_load_dword v14, v11, s[8:11], s31 offen\n"
      "ds_read2st64_b64 v[212:215], v4 offset0:0 offset1:1\n"
      "ds_read_b128 v[72:75], v0 offset:6144\n"
      "ds_read_b128 v[76:79], v0 offset:7168\n"
      "ds_read_b128 v[88:91], v0 offset:10240\n"
      "ds_read_b128 v[108:111], v0 offset:15360\n"
      "s_mov_b32 s20, 21\n"
      "s_waitcnt lgkmcnt(0)\n"
      "s_nop 4\n"
      "s_branch .Lq_loop\n"
      ".p2align 12\n"
      ".Lq_p0:\n"
      "s_branch .Lq_pr\n"
      ".p2align 5\n"
      ".Lq_loop:\n"
      "v_mfma_f32_16x16x32_bf16 v[120:123], a[0:3], v[48:51], 0\n"
      "ds_read_b128 v[80:83], v0 offset:8192\n"
      "ds_read_b128 v[84:87], v0 offset:9216\n"
      "v_mfma_f32_16x16x32_bf16 v[120:123], a[4:7], v[52:55], v[120:123]\n"
      "ds_read_b128 v[96:99], v0 offset:12288\n"
      "v_mul_f32 v232, 0x3eae1a93, v219\n"
      "v_mfma_f32_16x16x32_bf16 v[120:123], a[8:11], v[56:59], v[120:123]\n"
      "s_waitcnt lgkmcnt(7)\n"
      "ds_read_b128 v[104:107], v0 offset:14336\n"
      "v_fma_f32 v135, -v218, v151, v135\n"
      "v_mfma_f32_16x16x32_bf16 v[120:123], a[12:15], v[60:63], v[120:123]\n"
      "v_fma_f32 v133, -v218, v149, v133\n"
      "v_fma_f32 v134, -v218, v150, v134\n"
      "v_mfma_f32_16x16x32_bf16 v[120:123], a[16:19], v[64:67], v[120:123]\n"
      "ds_read_b128 v[92:95], v0 offset:11264\n"
      "v_fma_f32 v132, -v218, v148, v132\n"
      "v_mfma_f32_16x16x32_bf16 v[120:123], a[20:23], v[68:71], v[120:123]\n"
      "s_waitcnt lgkmcnt(5)\n"
      "ds_read_b64 v[216:217], v4 offset:1024\n"
      "v_cvt_pk_bf16_f32 v176, v132, v133\n"
      "v_mfma_f32_16x16x32_bf16 v[120:123], a[24:27], v[72:75], v[120:123]\n"
      "ds_read_b128 v[100:103], v0 offset:13312\n"
      "v_cvt_pk_bf16_f32 v177, v134, v135\n"
      "v_mfma_f32_16x16x32_bf16 v[120:123], a[28:31], v[76:79], v[120:123]\n"
      "s_waitcnt lgkmcnt(5)\n"
      "v_rcp_f32 v210, v210\n"
      "v_mfma_f32_16x16x32_bf16 v[120:123], a[32:35], v[80:83], v[120:123]\n"
      "v_mul_f32 v204, v204, v210\n"
      "v_add_f32 v212, v212, v214\n"
      "v_mfma_f32_16x16x32_bf16 v[188:191], v[172:175], v[176:179], v[152:155]\n"
      "ds_read_b128 v[112:115], v0 offset:16384\n"
      "v_mfma_f32_16x16x32_bf16 v[120:123], a[36:39], v[84:87], v[120:123]\n"
      "ds_read_b128 v[116:119], v0 offset:17408\n"
      "v_mul_f32 v208, v208, v210\n"
      "v_mfma_f32_16x16x32_bf16 v[120:123], a[40:43], v[88:91], v[120:123]\n"
      "s_waitcnt lgkmcnt(4)\n"
      "s_add_u32 s36, s36, 24576\n"
      "v_mul_f32 v207, v207, v210\n"
      "v_mfma_f32_16x16x32_bf16 v[120:123], a[44:47], v[92:95], v[120:123]\n"
      "v_mul_f32 v209, v209, v210\n"
      "v_cvt_pk_bf16_f32 v230, v208, v209\n"
      "v_mfma_f32_16x16x32_bf16 v[120:123], a[48:51], v[96:99], v[120:123]\n"
      "s_waitcnt lgkmcnt(2)\n"
      "buffer_store_dwordx2 v[230:231], v8, s[8:11], s40 offen sc1\n"
      "v_mfma_f32_16x16x32_bf16 v[120:123], a[52:55], v[100:103], v[120:123]\n"
      "v_mul_f32 v206, v206, v210\n"
      "v_add_f32 v218, v212, v216\n"
      "v_mfma_f32_16x16x32_bf16 v[120:123], a[56:59], v[104:107], v[120:123]\n"
      "s_mov_b32 s46, s45\n"
      "v_add_f32 v213, v213, v215\n"
      "v_mfma_f32_16x16x32_bf16 v[120:123], a[60:63], v[108:111], v[120:123]\n"
      "s_waitcnt lgkmcnt(1)\n"
      "v_cvt_pk_bf16_f32 v229, v206, v207\n"
      "v_mfma_f32_16x16x32_bf16 v[120:123], a[64:67], v[112:115], v[120:123]\n"
      "s_waitcnt lgkmcnt(0)\n"
      "v_mul_f32 v205, v205, v210\n"
      "v_cvt_pk_bf16_f32 v228, v204, v205\n"
      "v_mfma_f32_16x16x32_bf16 v[120:123], a[68:71], v[116:119], v[120:123]\n"
      "buffer_store_dwordx2 v[228:229], v7, s[8:11], s40 offen sc1\n"
      "v_mfma_f32_16x16x32_bf16 v[124:127], a[216:219], v[48:51], 0\n"
      "v_add_f32 v213, v213, v217\n"
      "v_fma_f32 v213, -v218, v218, v213\n"
      "v_mfma_f32_16x16x32_bf16 v[124:127], a[220:223], v[52:55], v[124:127]\n"
      "v_max_f32 v213, 0x3727c5ac, v213\n"
      "s_mov_b32 s45, s36\n"
      "v_mfma_f32_16x16x32_bf16 v[124:127], a[224:227], v[56:59], v[124:127]\n"
      "v_mov_b32 v187, v185\n"
      "v_mfma_f32_16x16x32_bf16 v[124:127], a[228:231], v[60:63], v[124:127]\n"
      "v_rsq_f32 v219, v213\n"
      "v_mfma_f32_16x16x32_bf16 v[124:127], a[232:235], v[64:67], v[124:127]\n"
      "s_add_u32 m0, s41, 40960\n"
      "ds_write_b64 v5, v[218:219] offset:0\n"
      "v_mfma_f32_16x16x32_bf16 v[124:127], a[236:239], v[68:71], v[124:127]\n"
      "s_waitcnt vmcnt(5)\n"
      "v_mov_b32 v186, v184\n"
      "v_cmp_ne_u32 vcc, s21, v15\n"
      "v_mfma_f32_16x16x32_bf16 v[124:127], a[240:243], v[72:75], v[124:127]\n"
      "s_min_u32 s45, s45, s6\n"
      "v_mfma_f32_16x16x32_bf16 v[124:127], a[244:247], v[76:79], v[124:127]\n"
      "v_cndmask_b32 v233, v219, 1.0, s[54:55]\n"
      "v_add_u32 v9, 1, v9\n"
      "v_mfma_f32_16x16x32_bf16 v[124:127], a[248:251], v[80:83], v[124:127]\n"
      "v_fma_f32 v123, -v218, v139, v123\n"
      "v_fma_f32 v122, -v218, v138, v122\n"
      "v_mfma_f32_16x16x32_bf16 v[124:127], a[252:255], v[84:87], v[124:127]\n"
      "v_fma_f32 v120, -v218, v136, v120\n"
      "v_fma_f32 v121, -v218, v137, v121\n"
      "v_mfma_f32_16x16x32_bf16 v[124:127], v[16:19], v[88:91], v[124:127]\n"
      "ds_read_b128 v[224:227], v6 offset:16\n"
      "v_mul_f32 v121, v219, v121\n"
      "v_mfma_f32_16x16x32_bf16 v[124:127], v[20:23], v[92:95], v[124:127]\n"
      "v_mul_f32 v120, v219, v120\n"
      "v_mfma_f32_16x16x32_bf16 v[124:127], v[24:27], v[96:99], v[124:127]\n"
      "s_cbranch_vccnz .Lsp_q2_4\n"
      ".Lbk_q2_4:\n"
      "v_mfma_f32_16x16x32_bf16 v[124:127], v[28:31], v[100:103], v[124:127]\n"
      "buffer_load_dword v15, v2, s[8:11], s45 offen sc1\n"
      "v_mfma_f32_16x16x32_bf16 v[124:127], v[32:35], v[104:107], v[124:127]\n"
      "v_mul_f32 v123, v219, v123\n"
      "v_mul_f32 v122, v219, v122\n"
      "v_mfma_f32_16x16x32_bf16 v[124:127], v[36:39], v[108:111], v[124:127]\n"
      "s_waitcnt vmcnt(4) lgkmcnt(1)\n"
      "ds_write_b32 v11, v9\n"
      "v_mfma_f32_16x16x32_bf16 v[124:127], v[40:43], v[112:115], v[124:127]\n"
      "v_mov_b32 v182, v180\n"
      "v_mfma_f32_16x16x32_bf16 v[124:127], v[44:47], v[116:119], v[124:127]\n"
      "v_cvt_pk_bf16_f32 v173, v122, v123\n"
      "v_mfma_f32_16x16x32_bf16 v[128:131], v[48:51], a[72:75], 0\n"
      "buffer_load_dwordx4 v0, s[8:11], s46 offen offset:1024 sc1 lds\n"
      "v_mfma_f32_16x16x32_bf16 v[128:131], v[52:55], a[76:79], v[128:131]\n"
      "ds_read_b128 v[220:223], v6 offset:0\n"
      "v_cvt_pk_bf16_f32 v172, v120, v121\n"
      "v_mfma_f32_16x16x32_bf16 v[128:131], v[56:59], a[80:83], v[128:131]\n"
      "v_fma_f32 v126, -v218, v142, v126\n"
      "v_fma_f32 v124, -v218, v140, v124\n"
      "v_mfma_f32_16x16x32_bf16 v[128:131], v[60:63], a[84:87], v[128:131]\n"
      "s_add_u32 s52, s52, 24576\n"
      "v_fma_f32 v125, -v218, v141, v125\n"
      "v_mul_f32 v124, v233, v124\n"
      "v_mfma_f32_16x16x32_bf16 v[128:131], v[64:67], a[88:91], v[128:131]\n"
      "v_mul_f32 v126, v219, v126\n"
      "v_mul_f32 v125, v233, v125\n"
      "v_mfma_f32_16x16x32_bf16 v[128:131], v[68:71], a[92:95], v[128:131]\n"
      "v_fma_f32 v127, -v218, v143, v127\n"
      "v_cvt_pk_bf16_f32 v175, v124, v125\n"
      "v_mfma_f32_16x16x32_bf16 v[128:131], v[72:75], a[96:99], v[128:131]\n"
      "v_mul_f32 v127, v219, v127\n"
      "v_cvt_pk_bf16_f32 v174, v126, v127\n"
      "v_mfma_f32_16x16x32_bf16 v[128:131], v[76:79], a[100:103], v[128:131]\n"
      "buffer_load_dwordx4 v0, s[8:11], s46 offen offset:2048 sc1 lds\n"
      "v_mfma_f32_16x16x32_bf16 v[192:195], v[172:175], v[176:179], v[156:159]\n"
      "ds_read_b32 v10, v12\n"
      "v_mfma_f32_16x16x32_bf16 v[196:199], v[172:175], v[164:167], v[168:171]\n"
      "v_mov_b32 v183, v181\n"
      "v_mov_b32 v178, v175\n"
      "v_mfma_f32_16x16x32_bf16 v[128:131], v[80:83], a[104:107], v[128:131]\n"
      "buffer_load_dwordx4 v0, s[8:11], s46 offen offset:0 sc1 lds\n"
      "v_mfma_f32_16x16x32_bf16 v[128:131], v[84:87], a[108:111], v[128:131]\n"
      "v_max_f32 v235, v194, v195\n"
      "v_max_f32 v234, v192, v193\n"
      "v_mfma_f32_16x16x32_bf16 v[128:131], v[88:91], a[112:115], v[128:131]\n"
      "v_max3_f32 v234, v188, v189, v234\n"
      "v_max3_f32 v235, v190, v191, v235\n"
      "v_mfma_f32_16x16x32_bf16 v[128:131], v[92:95], a[116:119], v[128:131]\n"
      "v_max_f32 v236, v234, v235\n"
      "v_mov_b32 v237, v236\n"
      "v_mfma_f32_16x16x32_bf16 v[128:131], v[96:99], a[120:123], v[128:131]\n"
      "v_permlane16_swap_b32 v236, v237\n"
      "v_mfma_f32_16x16x32_bf16 v[128:131], v[100:103], a[124:127], v[128:131]\n"
      "v_max_f32 v236, v236, v237\n"
      "v_mov_b32 v237, v236\n"
      "v_mfma_f32_16x16x32_bf16 v[128:131], v[104:107], a[128:131], v[128:131]\n"
      "s_waitcnt lgkmcnt(0)\n"
      "v_permlane32_swap_b32 v236, v237\n"
      "v_cmp_gt_i32 vcc, v9, v10\n"
      "v_mfma_f32_16x16x32_bf16 v[128:131], v[108:111], a[132:135], v[128:131]\n"
      "buffer_load_dwordx4 v0, s[8:11], s46 offen offset:3072 sc1 lds\n"
      "v_mfma_f32_16x16x32_bf16 v[128:131], v[112:115], a[136:139], v[128:131]\n"
      "s_add_u32 m0, s42, 40960\n"
      "v_max_f32 v236, v236, v237\n"
      "v_mul_f32 v236, v236, v232\n"
      "v_mfma_f32_16x16x32_bf16 v[128:131], v[116:119], a[140:143], v[128:131]\n"
      "s_add_u32 s40, s40, 24576\n"
      "v_fma_f32 v234, v234, v232, -v236\n"
      "v_fma_f32 v235, v235, v232, -v236\n"
      "v_mfma_f32_16x16x32_bf16 v[132:135], a[144:147], v[48:51], 0\n"
      "v_exp_f32 v234, v234\n"
      "v_mfma_f32_16x16x32_bf16 v[132:135], a[148:151], v[52:55], v[132:135]\n"
      "s_cbranch_vccnz .Lsp_qy1_5\n"
      ".Lbk_qy1_5:\n"
      "ds_read_b128 v[48:51], v0 offset:20480\n"
      "v_mfma_f32_16x16x32_bf16 v[132:135], a[152:155], v[56:59], v[132:135]\n"
      "v_exp_f32 v235, v235\n"
      "v_mfma_f32_16x16x32_bf16 v[132:135], a[156:159], v[60:63], v[132:135]\n"
      "ds_read_b128 v[56:59], v0 offset:22528\n"
      "v_fma_f32 v130, -v224, v147, v130\n"
      "v_mfma_f32_16x16x32_bf16 v[132:135], a[160:163], v[64:67], v[132:135]\n"
      "ds_read_b64 v[216:217], v4 offset:21504\n"
      "v_fma_f32 v129, -v222, v146, v129\n"
      "v_mfma_f32_16x16x32_bf16 v[132:135], a[164:167], v[68:71], v[132:135]\n"
      "buffer_load_dwordx4 v1, s[8:11], s46 offen offset:0 sc1 lds\n"
      "v_mfma_f32_16x16x32_bf16 v[132:135], a[168:171], v[72:75], v[132:135]\n"
      "v_cvt_pk_bf16_f32 v234, v234, v234\n"
      "v_fma_f32 v131, -v226, v144, v131\n"
      "v_mfma_f32_16x16x32_bf16 v[132:135], a[172:175], v[76:79], v[132:135]\n"
      "ds_read_b128 v[60:63], v0 offset:23552\n"
      "v_and_b32 v200, v160, v234\n"
      "v_mfma_f32_16x16x32_bf16 v[132:135], a[176:179], v[80:83], v[132:135]\n"
      "v_cvt_pk_bf16_f32 v235, v235, v235\n"
      "v_mul_f32 v129, v223, v129\n"
      "v_mfma_f32_16x16x32_bf16 v[132:135], a[180:183], v[84:87], v[132:135]\n"
      "ds_read_b128 v[52:55], v0 offset:21504\n"
      "v_mul_f32 v131, v227, v131\n"
      "v_mfma_f32_16x16x32_bf16 v[132:135], a[184:187], v[88:91], v[132:135]\n"
      "ds_read_b128 v[88:91], v0 offset:30720\n"
      "v_mul_f32 v130, v225, v130\n"
      "v_cvt_pk_bf16_f32 v185, v198, v199\n"
      "v_mfma_f32_16x16x32_bf16 v[132:135], a[188:191], v[92:95], v[132:135]\n"
      "v_cvt_pk_bf16_f32 v184, v196, v197\n"
      "v_and_b32 v201, v161, v235\n"
      "v_fma_f32 v128, -v220, v145, v128\n"
      "v_mfma_f32_16x16x32_bf16 v[132:135], a[192:195], v[96:99], v[132:135]\n"
      "ds_read_b128 v[68:71], v0 offset:25600\n"
      "v_and_b32 v203, v163, v235\n"
      "v_mfma_f32_16x16x32_bf16 v[132:135], a[196:199], v[100:103], v[132:135]\n"
      "v_mul_f32 v128, v221, v128\n"
      "v_and_b32 v202, v162, v234\n"
      "v_mfma_f32_16x16x32_bf16 v[132:135], a[200:203], v[104:107], v[132:135]\n"
      "v_cvt_pk_bf16_f32 v180, v128, v129\n"
      "v_cvt_pk_bf16_f32 v181, v130, v131\n"
      "v_mfma_f32_16x16x32_bf16 v[208:211], v[184:187], v[200:203], 0\n"
      "ds_read_b128 v[64:67], v0 offset:24576\n"
      "ds_read_b128 v[72:75], v0 offset:26624\n"
      "v_mfma_f32_16x16x32_bf16 v[204:207], v[180:183], v[200:203], 0\n"
      "s_waitcnt vmcnt(6) lgkmcnt(4)\n"
      "buffer_store_dword v13, v3, s[8:11], s52 offen sc1\n"
      "v_mfma_f32_16x16x32_bf16 v[132:135], a[204:207], v[108:111], v[132:135]\n"
      "ds_read2st64_b64 v[212:215], v4 offset0:40 offset1:41\n"
      "v_mfma_f32_16x16x32_bf16 v[132:135], a[208:211], v[112:115], v[132:135]\n"
      "v_rcp_f32 v210, v210\n"
      "v_mfma_f32_16x16x32_bf16 v[132:135], a[212:215], v[116:119], v[132:135]\n"
      "ds_read_b128 v[76:79], v0 offset:27648\n"
      "v_mul_f32 v209, v209, v210\n"
      "v_mfma_f32_16x16x32_bf16 v[120:123], a[0:3], v[48:51], 0\n"
      "ds_read_b128 v[80:83], v0 offset:28672\n"
      "ds_read_b128 v[100:103], v0 offset:33792\n"
      "v_mfma_f32_16x16x32_bf16 v[120:123], a[4:7], v[52:55], v[120:123]\n"
      "ds_read_b128 v[92:95], v0 offset:31744\n"
      "v_mul_f32 v208, v208, v210\n"
      "v_mfma_f32_16x16x32_bf16 v[120:123], a[8:11], v[56:59], v[120:123]\n"
      "ds_read_b128 v[84:87], v0 offset:29696\n"
      "v_fma_f32 v134, -v218, v150, v134\n"
      "v_mfma_f32_16x16x32_bf16 v[120:123], a[12:15], v[60:63], v[120:123]\n"
      "s_waitcnt lgkmcnt(6)\n"
      "ds_read_b128 v[104:107], v0 offset:34816\n"
      "v_fma_f32 v132, -v218, v148, v132\n"
      "v_mfma_f32_16x16x32_bf16 v[120:123], a[16:19], v[64:67], v[120:123]\n"
      "s_mov_b32 s50, s49\n"
      "ds_read_b128 v[96:99], v0 offset:32768\n"
      "v_mfma_f32_16x16x32_bf16 v[120:123], a[20:23], v[68:71], v[120:123]\n"
      "ds_read_b128 v[108:111], v0 offset:35840\n"
      "v_fma_f32 v133, -v218, v149, v133\n"
      "v_mfma_f32_16x16x32_bf16 v[120:123], a[24:27], v[72:75], v[120:123]\n"
      "s_waitcnt lgkmcnt(7)\n"
      "v_fma_f32 v135, -v218, v151, v135\n"
      "v_cvt_pk_bf16_f32 v177, v134, v135\n"
      "v_mfma_f32_16x16x32_bf16 v[120:123], a[28:31], v[76:79], v[120:123]\n"
      "s_waitcnt lgkmcnt(5)\n"
      "ds_read_b128 v[112:115], v0 offset:36864\n"
      "v_cvt_pk_bf16_f32 v176, v132, v133\n"
      "v_mfma_f32_16x16x32_bf16 v[120:123], a[32:35], v[80:83], v[120:123]\n"
      "v_mov_b32 v187, v185\n"
      "v_mul_f32 v232, 0x3eae1a93, v219\n"
      "v_mfma_f32_16x16x32_bf16 v[188:191], v[172:175], v[176:179], v[152:155]\n"
      "s_waitcnt lgkmcnt(4)\n"
      "ds_read_b128 v[116:119], v0 offset:37888\n"
      "v_mul_f32 v207, v207, v210\n"
      "v_mfma_f32_16x16x32_bf16 v[120:123], a[36:39], v[84:87], v[120:123]\n"
      "v_mul_f32 v204, v204, v210\n"
      "v_mfma_f32_16x16x32_bf16 v[120:123], a[40:43], v[88:91], v[120:123]\n"
      "v_cvt_pk_bf16_f32 v230, v208, v209\n"
      "v_add_f32 v213, v213, v215\n"
      "v_mfma_f32_16x16x32_bf16 v[120:123], a[44:47], v[92:95], v[120:123]\n"
      "s_waitcnt lgkmcnt(2)\n"
      "v_mul_f32 v205, v205, v210\n"
      "v_mfma_f32_16x16x32_bf16 v[120:123], a[48:51], v[96:99], v[120:123]\n"
      "buffer_store_dwordx2 v[230:231], v8, s[8:11], s40 offen sc1\n"
      "v_mfma_f32_16x16x32_bf16 v[120:123], a[52:55], v[100:103], v[120:123]\n"
      "v_add_f32 v212, v212, v214\n"
      "v_mfma_f32_16x16x32_bf16 v[120:123], a[56:59], v[104:107], v[120:123]\n"
      "v_cvt_pk_bf16_f32 v228, v204, v205\n"
      "v_mul_f32 v206, v206, v210\n"
      "v_mfma_f32_16x16x32_bf16 v[120:123], a[60:63], v[108:111], v[120:123]\n"
      "s_waitcnt vmcnt(7) lgkmcnt(0)\n"
      "v_cvt_pk_bf16_f32 v229, v206, v207\n"
      "v_cmp_ne_u32 vcc, s21, v15\n"
      "v_mfma_f32_16x16x32_bf16 v[120:123], a[64:67], v[112:115], v[120:123]\n"
      "s_add_u32 m0, s41, 0\n"
      "v_mov_b32 v182, v180\n"
      "v_mfma_f32_16x16x32_bf16 v[120:123], a[68:71], v[116:119], v[120:123]\n"
      "buffer_store_dwordx2 v[228:229], v7, s[8:11], s40 offen sc1\n"
      "v_mfma_f32_16x16x32_bf16 v[124:127], a[216:219], v[48:51], 0\n"
      "v_add_f32 v218, v212, v216\n"
      "v_add_f32_e64 v213, v213, v217\n"
      "v_mfma_f32_16x16x32_bf16 v[124:127], a[220:223], v[52:55], v[124:127]\n"
      "s_add_u32 s36, s36, 24576\n"
      "v_fma_f32 v213, -v218, v218, v213\n"
      "v_mfma_f32_16x16x32_bf16 v[124:127], a[224:227], v[56:59], v[124:127]\n"
      "s_mov_b32 s46, s45\n"
      "s_cbranch_vccnz .Lsp_q0_6\n"
      ".Lbk_q0_6:\n"
      "v_mfma_f32_16x16x32_bf16 v[124:127], a[228:231], v[60:63], v[124:127]\n"
      "buffer_load_dwordx4 v0, s[8:11], s46 offen offset:1024 sc1 lds\n"
      "v_mfma_f32_16x16x32_bf16 v[124:127], a[232:235], v[64:67], v[124:127]\n"
      "v_max_f32 v213, 0x3727c5ac, v213\n"
      "v_fma_f32 v120, -v218, v136, v120\n"
      "v_mfma_f32_16x16x32_bf16 v[124:127], a[236:239], v[68:71], v[124:127]\n"
      "v_mov_b32 v186, v184\n"
      "v_mfma_f32_16x16x32_bf16 v[124:127], a[240:243], v[72:75], v[124:127]\n"
      "v_add_u32 v9, 1, v9\n"
      "v_mfma_f32_16x16x32_bf16 v[124:127], a[244:247], v[76:79], v[124:127]\n"
      "s_add_u32 s40, s40, 24576\n"
      "v_fma_f32 v123, -v218, v139, v123\n"
      "v_mfma_f32_16x16x32_bf16 v[124:127], a[248:251], v[80:83], v[124:127]\n"
      "v_rsq_f32 v219, v213\n"
      "v_mfma_f32_16x16x32_bf16 v[124:127], a[252:255], v[84:87], v[124:127]\n"
      "v_mfma_f32_16x16x32_bf16 v[124:127], v[16:19], v[88:91], v[124:127]\n"
      "v_fma_f32 v121, -v218, v137, v121\n"
      "v_fma_f32 v122, -v218, v138, v122\n"
      "v_mfma_f32_16x16x32_bf16 v[124:127], v[20:23], v[92:95], v[124:127]\n"
      "buffer_load_dwordx4 v0, s[8:11], s46 offen offset:0 sc1 lds\n"
      "v_mfma_f32_16x16x32_bf16 v[124:127], v[24:27], v[96:99], v[124:127]\n"
      "v_mul_f32 v120, v219, v120\n"
      "v_mul_f32 v122, v219, v122\n"
      "v_mfma_f32_16x16x32_bf16 v[124:127], v[28:31], v[100:103], v[124:127]\n"
      "ds_write_b64 v5, v[218:219] offset:512\n"
      "v_mfma_f32_16x16x32_bf16 v[124:127], v[32:35], v[104:107], v[124:127]\n"
      "v_mul_f32 v121, v219, v121\n"
      "v_mul_f32 v123, v219, v123\n"
      "v_mfma_f32_16x16x32_bf16 v[124:127], v[36:39], v[108:111], v[124:127]\n"
      "buffer_load_dwordx4 v0, s[8:11], s46 offen offset:2048 sc1 lds\n"
      "v_mfma_f32_16x16x32_bf16 v[124:127], v[40:43], v[112:115], v[124:127]\n"
      "s_mov_b32 s45, s36\n"
      "ds_read_b128 v[224:227], v6 offset:528\n"
      "v_mfma_f32_16x16x32_bf16 v[124:127], v[44:47], v[116:119], v[124:127]\n"
      "s_min_u32 s45, s45, s6\n"
      "v_cndmask_b32 v233, v219, 1.0, s[54:55]\n"
      "v_mfma_f32_16x16x32_bf16 v[128:131], v[48:51], a[72:75], 0\n"
      "s_add_u32 s52, s52, 24576\n"
      "v_cvt_pk_bf16_f32 v172, v120, v121\n"
      "v_cvt_pk_bf16_f32 v173, v122, v123\n"
      "v_mfma_f32_16x16x32_bf16 v[128:131], v[52:55], a[76:79], v[128:131]\n"
      "buffer_load_dwordx4 v0, s[8:11], s46 offen offset:3072 sc1 lds\n"
      "v_mfma_f32_16x16x32_bf16 v[128:131], v[56:59], a[80:83], v[128:131]\n"
      "v_fma_f32 v126, -v218, v142, v126\n"
      "v_fma_f32 v124, -v218, v140, v124\n"
      "v_mul_f32 v126, v219, v126\n"
      "v_mfma_f32_16x16x32_bf16 v[128:131], v[60:63], a[84:87], v[128:131]\n"
      "v_fma_f32 v125, -v218, v141, v125\n"
      "v_mul_f32 v125, v233, v125\n"
      "v_mfma_f32_16x16x32_bf16 v[128:131], v[64:67], a[88:91], v[128:131]\n"
      "s_waitcnt vmcnt(7) lgkmcnt(1)\n"
      "ds_write_b32 v11, v9\n"
      "v_mfma_f32_16x16x32_bf16 v[128:131], v[68:71], a[92:95], v[128:131]\n"
      "v_fma_f32 v127, -v218, v143, v127\n"
      "v_mul_f32 v124, v233, v124\n"
      "v_mul_f32 v127, v219, v127\n"
      "v_mfma_f32_16x16x32_bf16 v[128:131], v[72:75], a[96:99], v[128:131]\n"
      "v_cvt_pk_bf16_f32 v174, v126, v127\n"
      "v_cvt_pk_bf16_f32 v175, v124, v125\n"
      "v_mfma_f32_16x16x32_bf16 v[128:131], v[76:79], a[100:103], v[128:131]\n"
      "buffer_load_dword v15, v2, s[8:11], s45 offen sc1\n"
      "v_mfma_f32_16x16x32_bf16 v[192:195], v[172:175], v[176:179], v[156:159]\n"
      "v_mov_b32 v183, v181\n"
      "v_mfma_f32_16x16x32_bf16 v[196:199], v[172:175], v[164:167], v[168:171]\n"
      "ds_read_b128 v[220:223], v6 offset:512\n"
      "s_add_u32 m0, s42, 0\n"
      "v_mfma_f32_16x16x32_bf16 v[128:131], v[80:83], a[104:107], v[128:131]\n"
      "buffer_load_dwordx4 v1, s[8:11], s46 offen offset:0 sc1 lds\n"
      "v_mfma_f32_16x16x32_bf16 v[128:131], v[84:87], a[108:111], v[128:131]\n"
      "v_max_f32 v235, v194, v195\n"
      "v_max3_f32 v235, v190, v191, v235\n"
      "v_mfma_f32_16x16x32_bf16 v[128:131], v[88:91], a[112:115], v[128:131]\n"
      "v_max_f32 v234, v192, v193\n"
      "v_max3_f32 v234, v188, v189, v234\n"
      "v_mfma_f32_16x16x32_bf16 v[128:131], v[92:95], a[116:119], v[128:131]\n"
      "ds_read_b32 v10, v12\n"
      "v_max_f32 v236, v234, v235\n"
      "v_mfma_f32_16x16x32_bf16 v[128:131], v[96:99], a[120:123], v[128:131]\n"
      "v_mov_b32 v237, v236\n"
      "v_cvt_pk_bf16_f32 v185, v198, v199\n"
      "v_mfma_f32_16x16x32_bf16 v[128:131], v[100:103], a[124:127], v[128:131]\n"
      "v_permlane16_swap_b32 v236, v237\n"
      "v_mfma_f32_16x16x32_bf16 v[128:131], v[104:107], a[128:131], v[128:131]\n"
      "v_max_f32 v236, v236, v237\n"
      "v_mov_b32 v237, v236\n"
      "v_mfma_f32_16x16x32_bf16 v[128:131], v[108:111], a[132:135], v[128:131]\n"
      "v_permlane32_swap_b32 v236, v237\n"
      "v_mfma_f32_16x16x32_bf16 v[128:131], v[112:115], a[136:139], v[128:131]\n"
      "s_waitcnt lgkmcnt(0)\n"
      "v_cmp_gt_i32 vcc, v9, v10\n"
      "v_max_f32 v236, v236, v237\n"
      "v_mfma_f32_16x16x32_bf16 v[128:131], v[116:119], a[140:143], v[128:131]\n"
      "v_mul_f32_e64 v236, v236, v232\n"
      "v_fma_f32 v235, v235, v232, -v236\n"
      "v_mfma_f32_16x16x32_bf16 v[132:135], a[144:147], v[48:51], 0\n"
      "v_exp_f32_e64 v235, v235\n"
      "v_mfma_f32_16x16x32_bf16 v[132:135], a[148:151], v[52:55], v[132:135]\n"
      "s_cbranch_vccnz .Lsp_qy2_7\n"
      ".Lbk_qy2_7:\n"
      "v_cvt_pk_bf16_f32 v235, v235, v235\n"
      "v_mfma_f32_16x16x32_bf16 v[132:135], a[152:155], v[56:59], v[132:135]\n"
      "v_fma_f32 v130, -v224, v147, v130\n"
      "v_mul_f32 v130, v225, v130\n"
      "v_mfma_f32_16x16x32_bf16 v[132:135], a[156:159], v[60:63], v[132:135]\n"
      "v_fma_f32 v129, -v222, v146, v129\n"
      "v_cvt_pk_bf16_f32 v184, v196, v197\n"
      "v_mfma_f32_16x16x32_bf16 v[132:135], a[160:163], v[64:67], v[132:135]\n"
      "v_fma_f32 v234, v234, v232, -v236\n"
      "v_fma_f32 v131, -v226, v144, v131\n"
      "v_mfma_f32_16x16x32_bf16 v[132:135], a[164:167], v[68:71], v[132:135]\n"
      "ds_read_b128 v[52:55], v0 offset:41984\n"
      "ds_read_b128 v[64:67], v0 offset:45056\n"
      "v_mfma_f32_16x16x32_bf16 v[132:135], a[168:171], v[72:75], v[132:135]\n"
      "ds_read_b128 v[56:59], v0 offset:43008\n"
      "v_mul_f32 v129, v223, v129\n"
      "v_mfma_f32_16x16x32_bf16 v[132:135], a[172:175], v[76:79], v[132:135]\n"
      "v_exp_f32 v234, v234\n"
      "v_mfma_f32_16x16x32_bf16 v[132:135], a[176:179], v[80:83], v[132:135]\n"
      "v_fma_f32 v128, -v220, v145, v128\n"
      "v_and_b32 v203, v163, v235\n"
      "v_mfma_f32_16x16x32_bf16 v[132:135], a[180:183], v[84:87], v[132:135]\n"
      "v_cvt_pk_bf16_f32 v234, v234, v234\n"
      "v_and_b32 v201, v161, v235\n"
      "v_mfma_f32_16x16x32_bf16 v[132:135], a[184:187], v[88:91], v[132:135]\n"
      "ds_read_b128 v[48:51], v0 offset:40960\n"
      "v_and_b32 v202, v162, v234\n"
      "v_mfma_f32_16x16x32_bf16 v[132:135], a[188:191], v[92:95], v[132:135]\n"
      "ds_read_b128 v[60:63], v0 offset:44032\n"
      "v_mul_f32 v128, v221, v128\n"
      "v_mfma_f32_16x16x32_bf16 v[132:135], a[192:195], v[96:99], v[132:135]\n"
      "ds_read_b128 v[76:79], v0 offset:48128\n"
      "v_cvt_pk_bf16_f32 v180, v128, v129\n"
      "v_mfma_f32_16x16x32_bf16 v[132:135], a[196:199], v[100:103], v[132:135]\n"
      "ds_read_b128 v[100:103], v0 offset:54272\n"
      "v_and_b32 v200, v160, v234\n"
      "v_mfma_f32_16x16x32_bf16 v[132:135], a[200:203], v[104:107], v[132:135]\n"
      "v_mul_f32 v131, v227, v131\n"
      "v_cvt_pk_bf16_f32 v181, v130, v131\n"
      "v_mfma_f32_16x16x32_bf16 v[208:211], v[184:187], v[200:203], 0\n"
      "s_waitcnt vmcnt(6) lgkmcnt(4)\n"
      "buffer_store_dword v13, v3, s[8:11], s52 offen sc1\n"
      "v_mfma_f32_16x16x32_bf16 v[204:207], v[180:183], v[200:203], 0\n"
      "ds_read_b128 v[84:87], v0 offset:50176\n"
      "ds_read_b128 v[72:75], v0 offset:47104\n"
      "v_mfma_f32_16x16x32_bf16 v[132:135], a[204:207], v[108:111], v[132:135]\n"
      "ds_read_b128 v[68:71], v0 offset:46080\n"
      "v_mov_b32 v178, v175\n"
      "v_mfma_f32_16x16x32_bf16 v[132:135], a[208:211], v[112:115], v[132:135]\n"
      "v_rcp_f32 v210, v210\n"
      "v_mfma_f32_16x16x32_bf16 v[132:135], a[212:215], v[116:119], v[132:135]\n"
      "s_waitcnt lgkmcnt(4)\n"
      "ds_read_b128 v[92:95], v0 offset:52224\n"
      "v_mul_f32 v205, v205, v210\n"
      "v_mfma_f32_16x16x32_bf16 v[120:123], a[0:3], v[48:51], 0\n"
      "s_add_u32 s36, s36, 24576\n"
      "ds_read_b128 v[80:83], v0 offset:49152\n"
      "v_mov_b32 v182, v180\n"
      "v_mfma_f32_16x16x32_bf16 v[120:123], a[4:7], v[52:55], v[120:123]\n"
      "ds_read_b128 v[96:99], v0 offset:53248\n"
      "v_mfma_f32_16x16x32_bf16 v[120:123], a[8:11], v[56:59], v[120:123]\n"
      "ds_read_b128 v[88:91], v0 offset:51200\n"
      "v_fma_f32 v132, -v218, v148, v132\n"
      "v_mfma_f32_16x16x32_bf16 v[120:123], a[12:15], v[60:63], v[120:123]\n"
      "s_cmp_eq_u32 s36, s37\n"
      "v_fma_f32 v133, -v218, v149, v133\n"
      "v_cvt_pk_bf16_f32 v176, v132, v133\n"
      "v_mfma_f32_16x16x32_bf16 v[120:123], a[16:19], v[64:67], v[120:123]\n"
      "s_waitcnt lgkmcnt(4)\n"
      "ds_read2st64_b64 v[212:215], v4 offset0:80 offset1:81\n"
      "s_cselect_b32 s47, s38, 0\n"
      "v_mfma_f32_16x16x32_bf16 v[120:123], a[20:23], v[68:71], v[120:123]\n"
      "s_cselect_b32 s37, s48, s37\n"
      "ds_read_b128 v[112:115], v0 offset:57344\n"
      "v_fma_f32 v135, -v218, v151, v135\n"
      "v_mfma_f32_16x16x32_bf16 v[120:123], a[24:27], v[72:75], v[120:123]\n"
      "s_cselect_b32 s49, s39, s53\n"
      "v_fma_f32 v134, -v218, v150, v134\n"
      "v_mov_b32 v183, v181\n"
      "v_mfma_f32_16x16x32_bf16 v[120:123], a[28:31], v[76:79], v[120:123]\n"
      "s_waitcnt lgkmcnt(4)\n"
      "ds_read_b128 v[104:107], v0 offset:55296\n"
      "v_cvt_pk_bf16_f32 v177, v134, v135\n"
      "v_mfma_f32_16x16x32_bf16 v[120:123], a[32:35], v[80:83], v[120:123]\n"
      "ds_read_b128 v[108:111], v0 offset:56320\n"
      "v_mul_f32 v206, v206, v210\n"
      "v_mfma_f32_16x16x32_bf16 v[188:191], v[172:175], v[176:179], v[152:155]\n"
      "ds_read_b64 v[216:217], v4 offset:41984\n"
      "v_mul_f32 v207, v207, v210\n"
      "v_mfma_f32_16x16x32_bf16 v[120:123], a[36:39], v[84:87], v[120:123]\n"
      "s_waitcnt lgkmcnt(5)\n"
      "s_add_u32 m0, s41, 20480\n"
      "ds_read_b128 v[116:119], v0 offset:58368\n"
      "v_mfma_f32_16x16x32_bf16 v[120:123], a[40:43], v[88:91], v[120:123]\n"
      "s_mov_b32 s46, s45\n"
      "s_add_u32 s45, s36, s47\n"
      "v_mul_f32 v209, v209, v210\n"
      "v_mfma_f32_16x16x32_bf16 v[120:123], a[44:47], v[92:95], v[120:123]\n"
      "v_cvt_pk_bf16_f32 v229, v206, v207\n"
      "v_mfma_f32_16x16x32_bf16 v[120:123], a[48:51], v[96:99], v[120:123]\n"
      "s_waitcnt lgkmcnt(4)\n"
      "v_add_f32 v213, v213, v215\n"
      "v_mfma_f32_16x16x32_bf16 v[120:123], a[52:55], v[100:103], v[120:123]\n"
      "s_waitcnt lgkmcnt(3)\n"
      "v_mul_f32 v232, 0x3eae1a93, v219\n"
      "v_mul_f32 v204, v204, v210\n"
      "v_mfma_f32_16x16x32_bf16 v[120:123], a[56:59], v[104:107], v[120:123]\n"
      "s_waitcnt lgkmcnt(2)\n"
      "v_mul_f32 v208, v208, v210\n"
      "v_cvt_pk_bf16_f32 v230, v208, v209\n"
      "v_mfma_f32_16x16x32_bf16 v[120:123], a[60:63], v[108:111], v[120:123]\n"
      "v_mfma_f32_16x16x32_bf16 v[120:123], a[64:67], v[112:115], v[120:123]\n"
      "s_waitcnt lgkmcnt(0)\n"
      "buffer_store_dwordx2 v[230:231], v8, s[8:11], s40 offen sc1\n"
      "v_mfma_f32_16x16x32_bf16 v[120:123], a[68:71], v[116:119], v[120:123]\n"
      "v_cvt_pk_bf16_f32 v228, v204, v205\n"
      "v_mfma_f32_16x16x32_bf16 v[124:127], a[216:219], v[48:51], 0\n"
      "v_add_f32 v212, v212, v214\n"
      "v_add_f32 v213, v213, v217\n"
      "v_mfma_f32_16x16x32_bf16 v[124:127], a[220:223], v[52:55], v[124:127]\n"
      "v_add_f32 v218, v212, v216\n"
      "v_fma_f32 v213, -v218, v218, v213\n"
      "v_mfma_f32_16x16x32_bf16 v[124:127], a[224:227], v[56:59], v[124:127]\n"
      "buffer_store_dwordx2 v[228:229], v7, s[8:11], s40 offen sc1\n"
      "v_mfma_f32_16x16x32_bf16 v[124:127], a[228:231], v[60:63], v[124:127]\n"
      "s_add_u32 s52, s52, 24576\n"
      "v_fma_f32 v123, -v218, v139, v123\n"
      "v_add_u32 v9, 1, v9\n"
      "v_mfma_f32_16x16x32_bf16 v[124:127], a[232:235], v[64:67], v[124:127]\n"
      "v_fma_f32 v122, -v218, v138, v122\n"
      "v_mfma_f32_16x16x32_bf16 v[124:127], a[236:239], v[68:71], v[124:127]\n"
      "s_waitcnt vmcnt(4)\n"
      "v_max_f32 v213, 0x3727c5ac, v213\n"
      "v_cmp_ne_u32 vcc, s21, v15\n"
      "v_mfma_f32_16x16x32_bf16 v[124:127], a[240:243], v[72:75], v[124:127]\n"
      "v_rsq_f32 v219, v213\n"
      "v_mfma_f32_16x16x32_bf16 v[124:127], a[244:247], v[76:79], v[124:127]\n"
      "v_mov_b32 v186, v184\n"
      "v_fma_f32 v120, -v218, v136, v120\n"
      "v_mfma_f32_16x16x32_bf16 v[124:127], a[248:251], v[80:83], v[124:127]\n"
      "s_min_u32 s45, s45, s6\n"
      "ds_write_b64 v5, v[218:219] offset:1024\n"
      "v_mfma_f32_16x16x32_bf16 v[124:127], a[252:255], v[84:87], v[124:127]\n"
      "s_cbranch_vccnz .Lsp_q1_8\n"
      ".Lbk_q1_8:\n"
      "v_mfma_f32_16x16x32_bf16 v[124:127], v[16:19], v[88:91], v[124:127]\n"
      "buffer_load_dwordx4 v0, s[8:11], s46 offen offset:1024 sc1 lds\n"
      "v_mfma_f32_16x16x32_bf16 v[124:127], v[20:23], v[92:95], v[124:127]\n"
      "v_cndmask_b32 v233, v219, 1.0, s[54:55]\n"
      "v_mul_f32 v123, v219, v123\n"
      "v_mfma_f32_16x16x32_bf16 v[124:127], v[24:27], v[96:99], v[124:127]\n"
      "ds_read_b128 v[220:223], v6 offset:1024\n"
      "v_mul_f32 v120, v219, v120\n"
      "v_mfma_f32_16x16x32_bf16 v[124:127], v[28:31], v[100:103], v[124:127]\n"
      "v_mul_f32 v122, v219, v122\n"
      "v_fma_f32 v121, -v218, v137, v121\n"
      "v_mfma_f32_16x16x32_bf16 v[124:127], v[32:35], v[104:107], v[124:127]\n"
      "s_waitcnt vmcnt(4)\n"
      "ds_write_b32 v11, v9\n"
      "v_mfma_f32_16x16x32_bf16 v[124:127], v[36:39], v[108:111], v[124:127]\n"
      "buffer_load_dwordx4 v0, s[8:11], s46 offen offset:0 sc1 lds\n"
      "v_mfma_f32_16x16x32_bf16 v[124:127], v[40:43], v[112:115], v[124:127]\n"
      "v_mfma_f32_16x16x32_bf16 v[124:127], v[44:47], v[116:119], v[124:127]\n"
      "v_cvt_pk_bf16_f32 v173, v122, v123\n"
      "v_mfma_f32_16x16x32_bf16 v[128:131], v[48:51], a[72:75], 0\n"
      "v_mul_f32 v121, v219, v121\n"
      "v_cvt_pk_bf16_f32 v172, v120, v121\n"
      "v_mfma_f32_16x16x32_bf16 v[128:131], v[52:55], a[76:79], v[128:131]\n"
      "buffer_load_dwordx4 v0, s[8:11], s46 offen offset:3072 sc1 lds\n"
      "v_mfma_f32_16x16x32_bf16 v[128:131], v[56:59], a[80:83], v[128:131]\n"
      "v_fma_f32 v125, -v218, v141, v125\n"
      "v_fma_f32 v124, -v218, v140, v124\n"
      "v_mfma_f32_16x16x32_bf16 v[128:131], v[60:63], a[84:87], v[128:131]\n"
      "v_fma_f32 v127, -v218, v143, v127\n"
      "v_mul_f32 v125, v233, v125\n"
      "v_mfma_f32_16x16x32_bf16 v[128:131], v[64:67], a[88:91], v[128:131]\n"
      "v_mul_f32 v124, v233, v124\n"
      "v_mul_f32 v127, v219, v127\n"
      "v_mfma_f32_16x16x32_bf16 v[128:131], v[68:71], a[92:95], v[128:131]\n"
      "v_fma_f32 v126, -v218, v142, v126\n"
      "v_mul_f32 v126, v219, v126\n"
      "v_mfma_f32_16x16x32_bf16 v[128:131], v[72:75], a[96:99], v[128:131]\n"
      "v_cvt_pk_bf16_f32 v175, v124, v125\n"
      "v_cvt_pk_bf16_f32 v174, v126, v127\n"
      "v_mfma_f32_16x16x32_bf16 v[128:131], v[76:79], a[100:103], v[128:131]\n"
      "buffer_load_dword v15, v2, s[8:11], s45 offen sc1\n"
      "v_mfma_f32_16x16x32_bf16 v[192:195], v[172:175], v[176:179], v[156:159]\n"
      "ds_read_b128 v[224:227], v6 offset:1040\n"
      "v_mov_b32 v187, v185\n"
      "v_mfma_f32_16x16x32_bf16 v[196:199], v[172:175], v[164:167], v[168:171]\n"
      "ds_read_b32 v10, v12\n"
      "v_mfma_f32_16x16x32_bf16 v[128:131], v[80:83], a[104:107], v[128:131]\n"
      "buffer_load_dwordx4 v0, s[8:11], s46 offen offset:2048 sc1 lds\n"
      "v_mfma_f32_16x16x32_bf16 v[128:131], v[84:87], a[108:111], v[128:131]\n"
      "v_med3_f32 v234, v192, v193, s50\n"
      "v_med3_f32 v235, v194, v195, s50\n"
      "v_mfma_f32_16x16x32_bf16 v[128:131], v[88:91], a[112:115], v[128:131]\n"
      "v_cvt_pk_bf16_f32 v185, v198, v199\n"
      "v_max3_f32 v234, v188, v189, v234\n"
      "v_mfma_f32_16x16x32_bf16 v[128:131], v[92:95], a[116:119], v[128:131]\n"
      "v_max3_f32 v235, v190, v191, v235\n"
      "v_max_f32 v236, v234, v235\n"
      "v_mfma_f32_16x16x32_bf16 v[128:131], v[96:99], a[120:123], v[128:131]\n"
      "s_waitcnt lgkmcnt(0)\n"
      "v_mov_b32 v237, v236\n"
      "v_cmp_gt_i32 vcc, v9, v10\n"
      "v_mfma_f32_16x16x32_bf16 v[128:131], v[100:103], a[124:127], v[128:131]\n"
      "s_add_u32 m0, s42, 20480\n"
      "v_permlane16_swap_b32 v236, v237\n"
      "v_mfma_f32_16x16x32_bf16 v[128:131], v[104:107], a[128:131], v[128:131]\n"
      "v_max_f32 v236, v236, v237\n"
      "v_mov_b32 v237, v236\n"
      "v_mfma_f32_16x16x32_bf16 v[128:131], v[108:111], a[132:135], v[128:131]\n"
      "buffer_load_dwordx4 v1, s[8:11], s46 offen offset:0 sc1 lds\n"
      "v_mfma_f32_16x16x32_bf16 v[128:131], v[112:115], a[136:139], v[128:131]\n"
      ".Lq_p1:\n"
      "s_cbranch_execz .Lq_pr\n"
      "v_permlane32_swap_b32 v236, v237\n"
      "v_mfma_f32_16x16x32_bf16 v[128:131], v[116:119], a[140:143], v[128:131]\n"
      "v_max_f32 v236, v236, v237\n"
      "v_mul_f32_e64 v236, v236, v232\n"
      "v_mfma_f32_16x16x32_bf16 v[132:135], a[144:147], v[48:51], 0\n"
      "s_cbranch_vccnz .Lsp_qy0_9\n"
      ".Lbk_qy0_9:\n"
      "v_fma_f32 v234, v234, v232, -v236\n"
      "v_mfma_f32_16x16x32_bf16 v[132:135], a[148:151], v[52:55], v[132:135]\n"
      "ds_read_b128 v[52:55], v0 offset:1024\n"
      "ds_read_b128 v[48:51], v0 offset:0\n"
      "v_mfma_f32_16x16x32_bf16 v[132:135], a[152:155], v[56:59], v[132:135]\n"
      "v_fma_f32 v131, -v226, v144, v131\n"
      "v_fma_f32 v235, v235, v232, -v236\n"
      "v_mfma_f32_16x16x32_bf16 v[132:135], a[156:159], v[60:63], v[132:135]\n"
      "ds_read_b128 v[56:59], v0 offset:2048\n"
      "v_fma_f32 v128, -v220, v145, v128\n"
      "v_mfma_f32_16x16x32_bf16 v[132:135], a[160:163], v[64:67], v[132:135]\n"
      "v_exp_f32 v234, v234\n"
      "v_mfma_f32_16x16x32_bf16 v[132:135], a[164:167], v[68:71], v[132:135]\n"
      "v_exp_f32 v235, v235\n"
      "v_mfma_f32_16x16x32_bf16 v[132:135], a[168:171], v[72:75], v[132:135]\n"
      "v_cvt_pk_bf16_f32 v234, v234, v234\n"
      "v_fma_f32 v129, -v222, v146, v129\n"
      "v_mfma_f32_16x16x32_bf16 v[132:135], a[172:175], v[76:79], v[132:135]\n"
      "v_mul_f32 v129, v223, v129\n"
      "v_fma_f32 v130, -v224, v147, v130\n"
      "v_mfma_f32_16x16x32_bf16 v[132:135], a[176:179], v[80:83], v[132:135]\n"
      "v_cvt_pk_bf16_f32 v235, v235, v235\n"
      "v_and_b32 v200, v160, v234\n"
      "v_mfma_f32_16x16x32_bf16 v[132:135], a[180:183], v[84:87], v[132:135]\n"
      "v_cvt_pk_bf16_f32 v184, v196, v197\n"
      "v_and_b32 v201, v161, v235\n"
      "v_mfma_f32_16x16x32_bf16 v[132:135], a[184:187], v[88:91], v[132:135]\n"
      "ds_read_b128 v[68:71], v0 offset:5120\n"
      "v_mul_f32 v130, v225, v130\n"
      "v_mfma_f32_16x16x32_bf16 v[132:135], a[188:191], v[92:95], v[132:135]\n"
      "v_and_b32 v203, v163, v235\n"
      "v_mul_f32_e64 v131, v227, v131\n"
      "v_mfma_f32_16x16x32_bf16 v[132:135], a[192:195], v[96:99], v[132:135]\n"
      "ds_read_b128 v[64:67], v0 offset:4096\n"
      "v_mul_f32_e64 v128, v221, v128\n"
      "v_mfma_f32_16x16x32_bf16 v[132:135], a[196:199], v[100:103], v[132:135]\n"
      "v_and_b32_e64 v202, v162, v234\n"
      "v_cvt_pk_bf16_f32 v181, v130, v131\n"
      "v_mfma_f32_16x16x32_bf16 v[132:135], a[200:203], v[104:107], v[132:135]\n"
      "s_add_u32 s40, s40, 24576\n"
      "ds_read_b128 v[60:63], v0 offset:3072\n"
      "v_cvt_pk_bf16_f32 v180, v128, v129\n"
      "v_mfma_f32_16x16x32_bf16 v[208:211], v[184:187], v[200:203], 0\n"
      "s_waitcnt vmcnt(6) lgkmcnt(3)\n"
      "buffer_store_dword v13, v3, s[8:11], s52 offen sc1\n"
      "ds_read2st64_b64 v[212:215], v4 offset0:0 offset1:1\n"
      "v_mfma_f32_16x16x32_bf16 v[204:207], v[180:183], v[200:203], 0\n"
      "ds_read_b128 v[72:75], v0 offset:6144\n"
      "v_mfma_f32_16x16x32_bf16 v[132:135], a[204:207], v[108:111], v[132:135]\n"
      "ds_read_b128 v[76:79], v0 offset:7168\n"
      "v_mov_b32_e64 v178, v175\n"
      "v_mfma_f32_16x16x32_bf16 v[132:135], a[208:211], v[112:115], v[132:135]\n"
      "ds_read_b128 v[88:91], v0 offset:10240\n"
      "ds_read_b128 v[108:111], v0 offset:15360\n"
      "v_mfma_f32_16x16x32_bf16 v[132:135], a[212:215], v[116:119], v[132:135]\n"
      "s_sub_u32 s20, s20, 1\n"
      "s_cbranch_scc0 .Lq_loop\n"
      ".Lq_tail:\n"
      "v_mul_f32 v232, 0x3eae1a93, v219\n"
      "s_nop 7\n"
      "v_fma_f32 v135, -v218, v151, v135\n"
      "v_fma_f32 v133, -v218, v149, v133\n"
      "v_fma_f32 v134, -v218, v150, v134\n"
      "v_fma_f32 v132, -v218, v148, v132\n"
      "v_cvt_pk_bf16_f32 v176, v132, v133\n"
      "v_cvt_pk_bf16_f32 v177, v134, v135\n"
      "v_rcp_f32 v210, v210\n"
      "s_nop 0\n"
      "v_mul_f32 v204, v204, v210\n"
      "v_mfma_f32_16x16x32_bf16 v[188:191], v[172:175], v[176:179], v[152:155]\n"
      "v_mul_f32 v208, v208, v210\n"
      "v_mul_f32 v207, v207, v210\n"
      "v_mul_f32 v209, v209, v210\n"
      "v_cvt_pk_bf16_f32 v230, v208, v209\n"
      "buffer_store_dwordx2 v[230:231], v8, s[8:11], s40 offen sc1\n"
      "v_mul_f32 v206, v206, v210\n"
      "v_cvt_pk_bf16_f32 v229, v206, v207\n"
      "v_mul_f32 v205, v205, v210\n"
      "v_cvt_pk_bf16_f32 v228, v204, v205\n"
      "buffer_store_dwordx2 v[228:229], v7, s[8:11], s40 offen sc1\n"
      "v_mov_b32 v187, v185\n"
      "v_mov_b32 v186, v184\n"
      "v_mov_b32 v182, v180\n"
      "s_add_u32 s52, s52, 24576\n"
      "v_mov_b32 v183, v181\n"
      "s_add_u32 s40, s40, 24576\n"
      "s_waitcnt vmcnt(0)\n"
      "buffer_store_dword v13, v3, s[8:11], s52 offen sc1\n"
      "s_add_u32 s52, s52, 24576\n"
      "s_cmp_lg_u32 s44, 0\n"
      "s_cbranch_scc1 .Lq_done\n"
      "v_lshlrev_b32 v48, 3, v0\n"
      "s_mov_b32 s22, s23\n"
      ".Lq_up:\n"
      "s_mov_b32 s30, 128\n"
      "buffer_load_dword v49, v48, s[8:11], s30 offen sc1\n"
      "s_mov_b32 s30, 8320\n"
      "buffer_load_dword v50, v48, s[8:11], s30 offen sc1\n"
      "s_mov_b32 s30, 16512\n"
      "buffer_load_dword v51, v48, s[8:11], s30 offen sc1\n"
      "s_mov_b32 s30, 24704\n"
      "buffer_load_dword v52, v48, s[8:11], s30 offen sc1\n"
      "s_waitcnt vmcnt(0)\n"
      "v_xor_b32 v49, s21, v49\n"
      "v_xor_b32 v50, s21, v50\n"
      "v_xor_b32 v51, s21, v51\n"
      "v_xor_b32 v52, s21, v52\n"
      "v_or3_b32 v49, v49, v50, v51\n"
      "v_or_b32 v49, v49, v52\n"
      "v_cmp_ne_u32 vcc, 0, v49\n"
      "s_cbranch_vccz .Lq_ok\n"
      "s_sleep 4\n"
      "s_sub_u32 s22, s22, 1\n"
      "s_cbranch_scc0 .Lq_up\n"
      ".Lq_ok:\n"
      "v_lshrrev_b32 v48, 2, v0\n"
      "v_and_b32 v48, 0x7c, v48\n"
      "v_mov_b32 v49, s21\n"
      "buffer_store_dword v49, v48, s[8:11], 0 offen sc1\n"
      ".Lq_done:\n"
      "s_endpgm\n"
      ".Lsp_q0_1:\n"
      "s_mov_b32 s22, s23\n"
      ".Lsl_q0_1:\n"
      "buffer_load_dword v15, v2, s[8:11], s46 offen sc1\n"
      "s_waitcnt vmcnt(0)\n"
      "v_cmp_ne_u32 vcc, s21, v15\n"
      "s_cbranch_vccz .Lbk_q0_1\n"
      "s_sleep 4\n"
      "s_sub_u32 s22, s22, 1\n"
      "s_cbranch_scc0 .Lsl_q0_1\n"
      "s_mov_b32 s23, 0\n"
      "s_branch .Lbk_q0_1\n"
      ".Lsp_q1_2:\n"
      "s_mov_b32 s22, s23\n"
      ".Lsl_q1_2:\n"
      "buffer_load_dword v15, v2, s[8:11], s46 offen sc1\n"
      "s_waitcnt vmcnt(0)\n"
      "v_cmp_ne_u32 vcc, s21, v15\n"
      "s_cbranch_vccz .Lbk_q1_2\n"
      "s_sleep 4\n"
      "s_sub_u32 s22, s22, 1\n"
      "s_cbranch_scc0 .Lsl_q1_2\n"
      "s_mov_b32 s23, 0\n"
      "s_branch .Lbk_q1_2\n"
      ".Lsp_qy0_3:\n"
      "s_lshl_b32 s22, s23, 4\n"
      ".Lsl_qy0_3:\n"
      "ds_read_b32 v10, v12\n"
      "s_waitcnt lgkmcnt(0)\n"
      "v_cmp_gt_i32 vcc, v9, v10\n"
      "s_cbranch_vccz .Lbk_qy0_3\n"
      "s_sub_u32 s22, s22, 1\n"
      "s_cbranch_scc0 .Lsl_qy0_3\n"
      "s_mov_b32 s23, 0\n"
      "s_branch .Lbk_qy0_3\n"
      ".Lsp_q2_4:\n"
      "s_mov_b32 s22, s23\n"
      "buffer_load_dword v15, v2, s[8:11], s46 offen sc1\n"
      "s_waitcnt vmcnt(0)\n"
      "v_cmp_ne_u32 vcc, s21, v15\n"
      "s_add_u32 s30, s52, 24576\n"
      "buffer_store_dword v13, v3, s[8:11], s30 offen sc1\n"
      "s_cbranch_vccz .Lbk_q2_4\n"
      ".Lsl_q2_4:\n"
      "buffer_load_dword v15, v2, s[8:11], s46 offen sc1\n"
      "s_waitcnt vmcnt(0)\n"
      "v_cmp_ne_u32 vcc, s21, v15\n"
      "s_cbranch_vccz .Lbk_q2_4\n"
      "s_sleep 4\n"
      "s_sub_u32 s22, s22, 1\n"
      "s_cbranch_scc0 .Lsl_q2_4\n"
      "s_mov_b32 s23, 0\n"
      "s_branch .Lbk_q2_4\n"
      ".Lsp_qy1_5:\n"
      "s_lshl_b32 s22, s23, 4\n"
      ".Lsl_qy1_5:\n"
      "ds_read_b32 v10, v12\n"
      "s_waitcnt lgkmcnt(0)\n"
      "v_cmp_gt_i32 vcc, v9, v10\n"
      "s_cbranch_vccz .Lbk_qy1_5\n"
      "s_sub_u32 s22, s22, 1\n"
      "s_cbranch_scc0 .Lsl_qy1_5\n"
      "s_mov_b32 s23, 0\n"
      "s_branch .Lbk_qy1_5\n"
      ".Lsp_q0_6:\n"
      "s_mov_b32 s22, s23\n"
      "buffer_load_dword v15, v2, s[8:11], s46 offen sc1\n"
      "s_waitcnt vmcnt(0)\n"
      "v_cmp_ne_u32 vcc, s21, v15\n"
      "s_add_u32 s30, s52, 24576\n"
      "buffer_store_dword v13, v3, s[8:11], s30 offen sc1\n"
      "s_cbranch_vccz .Lbk_q0_6\n"
      ".Lsl_q0_6:\n"
      "buffer_load_dword v15, v2, s[8:11], s46 offen sc1\n"
      "s_waitcnt vmcnt(0)\n"
      "v_cmp_ne_u32 vcc, s21, v15\n"
      "s_cbranch_vccz .Lbk_q0_6\n"
      "s_sleep 4\n"
      "s_sub_u32 s22, s22, 1\n"
      "s_cbranch_scc0 .Lsl_q0_6\n"
      "s_mov_b32 s23, 0\n"
      "s_branch .Lbk_q0_6\n"
      ".Lsp_qy2_7:\n"
      "s_lshl_b32 s22, s23, 4\n"
      ".Lsl_qy2_7:\n"
      "ds_read_b32 v10, v12\n"
      "s_waitcnt lgkmcnt(0)\n"
      "v_cmp_gt_i32 vcc, v9, v10\n"
      "s_cbranch_vccz .Lbk_qy2_7\n"
      "s_sub_u32 s22, s22, 1\n"
      "s_cbranch_scc0 .Lsl_qy2_7\n"
      "s_mov_b32 s23, 0\n"
      "s_branch .Lbk_qy2_7\n"
      ".Lsp_q1_8:\n"
      "s_mov_b32 s22, s23\n"
      "buffer_load_dword v15, v2, s[8:11], s46 offen sc1\n"
      "s_waitcnt vmcnt(0)\n"
      "v_cmp_ne_u32 vcc, s21, v15\n"
      "s_add_u32 s30, s52, 0\n"
      "buffer_store_dword v13, v3, s[8:11], s30 offen sc1\n"
      "s_cbranch_vccz .Lbk_q1_8\n"
      ".Lsl_q1_8:\n"
      "buffer_load_dword v15, v2, s[8:11], s46 offen sc1\n"
      "s_waitcnt vmcnt(0)\n"
      "v_cmp_ne_u32 vcc, s21, v15\n"
      "s_cbranch_vccz .Lbk_q1_8\n"
      "s_sleep 4\n"
      "s_sub_u32 s22, s22, 1\n"
      "s_cbranch_scc0 .Lsl_q1_8\n"
      "s_mov_b32 s23, 0\n"
      "s_branch .Lbk_q1_8\n"
      ".Lsp_qy0_9:\n"
      "s_lshl_b32 s22, s23, 4\n"
      ".Lsl_qy0_9:\n"
      "ds_read_b32 v10, v12\n"
      "s_waitcnt lgkmcnt(0)\n"
      "v_cmp_gt_i32 vcc, v9, v10\n"
      "s_cbranch_vccz .Lbk_qy0_9\n"
      "s_sub_u32 s22, s22, 1\n"
      "s_cbranch_scc0 .Lsl_qy0_9\n"
      "s_mov_b32 s23, 0\n"
      "s_branch .Lbk_qy0_9\n"
      ".Lq_last:\n"
      :
      : "{s[8:11]}"(rsWS), "{s[12:15]}"(rsA), "{v0}"(v_l16), "{v1}"(v_l5), "{v2}"(v_fa), "{v3}"(v_ra), "{v4}"(v_st), "{v5}"(v_tw), "{v6}"(v_tr), "{v7}"(v_om), "{v8}"(v_oe), "{v10}"(v_al), "{v11}"(v_yw), "{v12}"(v_yr), "{v14}"(v_hi), "{v152}"(v_bc[0]), "{v153}"(v_bc[1]), "{v154}"(v_bc[2]), "{v155}"(v_bc[3]), "{v156}"(v_bp[0]), "{v157}"(v_bp[1]), "{v158}"(v_bp[2]), "{v159}"(v_bp[3]), "{v160}"(v_mk[0]), "{v161}"(v_mk[1]), "{v162}"(v_mk[2]), "{v163}"(v_mk[3]), "{v164}"(v_sel[0]), "{v165}"(v_sel[1]), "{v166}"(v_sel[2]), "{v167}"(v_sel[3]), "{v168}"(v_ci[0]), "{v169}"(v_ci[1]), "{v170}"(v_ci[2]), "{v171}"(v_ci[3]), "{v48}"(v_vo[0]), "{v49}"(v_vo[1]), "{v50}"(v_vo[2]), "{v51}"(v_vo[3]), "{v52}"(v_at), "{v53}"(v_ax), "{v54}"(v_pm), "{v55}"(v_ms), "{v76}"(v_pw), "{s36}"(s_sx), "{s37}"(s_ev), "{s38}"(s_del), "{s39}"(s_kil), "{s40}"(s_so), "{s41}"(s_m4), "{s42}"(s_m5), "{s43}"(s_ms), "{s44}"(s_up)
      : "v255", "a255", "s99", "vcc", "scc", "memory");

  } else if (fast && slot < 10) {
    const unsigned gr = e, part = slot - 8;
    COMMON(32, 81920);
    const unsigned c = part, o = w & 1;
    const u32x4 rsA = srd(Wo, 576 * 576 * 2);
    const unsigned v_l5 = v_l16 + 16384 - w * 3072, e0 = 16384 + o * 2048 + g * 512 + i16 * 8, e1 = e0 + 2048 - o * 4096;
    const unsigned v_ex[8] = {e0, e1, e0 + 20480, e1 + 20480, e0 + 40960, e1 + 40960, e0 + 61440, e1 + 61440}, v_l2 = v_l16 + 40960;
    const unsigned v_fa = 20480 - w * 4096 + (8 * w + (lane >> 3)) * 128 + (lane & 7) * 4, v_ra = up * 8 * REC + l5 + 19456 + (4 * c + w) * 128;
    const unsigned v_xp = (9 * c + 2 * w) * 1024 + v_l16, v_xs = (9 * c + 8) * 1024 + (w >> 1) * 512 + half;
    const unsigned v_rp = v_xp + WS_XIN, v_rs = v_xs + WS_XIN;
    const unsigned v_ew = 82944 + (w >> 1) * 1024 + lane * 4;
    const unsigned v_sw = 86016 + i16 * 144 + (4 * w + g) * 8, v_sr = 86016 + (8 * (w >> 1) + l8) * 144 + l7;
    const unsigned v_ss = 18432 + c * 512 + (w >> 1) * 256 + l8 * 32 + p * 8;
    const unsigned v_pw = 19456 + (4 * c + w) * 128 + l8 * REC + l7;
    unsigned v_vo[9], v_vp[9];
#pragma unroll
    for (unsigned j = 0; j < 9; ++j) {
      const unsigned ch = 64 * j + lane, r = ch / 36, x = 576 * c + ch % 36 * 16 - 1024 * (j & 3);
      v_vo[j] = (20 * o + r) * 1152 + x;
      v_vp[j] = (36 * (4 * o + (r >> 2)) + 16 + (r & 3)) * 1152 + x;
    }
    const unsigned ro = (8 * g + q) * 576;
    const unsigned v_t0 = ro + o * 18432 + 128 * w + 16 * p, v_t1 = v_t0 + 18432 - 2 * o * 18432, v_ts = ro + o * 18432 + 512 + 32 * (w >> 1) + 8 * p;
    const unsigned s_sx = WS_OB + (gr * NREC + KSK) * REC + w * 4096, s_ev = s_sx + (31 - KSK) * REC, s_sy = WS_X1 + (gr * NREC + KSK) * REC;
    const unsigned s_m4 = w * 4096, s_m5 = 16384 + w * 1024, s_m9 = w * 9216, s_so = (w >> 1) * 8 * 41472;
    const unsigned s_s8 = s_so, s_wg = o, s_rb = WS_OB + w * 4096;
    asm volatile(
      "s_buffer_load_dword s21, s[8:11], 0\n"
      "s_mov_b32 s23, 65536\n"
      "s_mov_b32 s31, 0x40000000\n"
      "s_add_u32 m0, s41, 0\n"
      "s_nop 0\n"
      "buffer_load_dwordx4 v100, s[12:15], s42 offen offset:0 lds\n"
      "buffer_load_dwordx4 v101, s[12:15], s42 offen offset:1024 lds\n"
      "buffer_load_dwordx4 v102, s[12:15], s42 offen offset:2048 lds\n"
      "buffer_load_dwordx4 v103, s[12:15], s42 offen offset:3072 lds\n"
      "s_add_u32 m0, s41, 4096\n"
      "s_nop 0\n"
      "buffer_load_dwordx4 v104, s[12:15], s42 offen offset:0 lds\n"
      "buffer_load_dwordx4 v105, s[12:15], s42 offen offset:1024 lds\n"
      "buffer_load_dwordx4 v106, s[12:15], s42 offen offset:2048 lds\n"
      "buffer_load_dwordx4 v107, s[12:15], s42 offen offset:3072 lds\n"
      "s_add_u32 m0, s41, 8192\n"
      "s_nop 0\n"
      "buffer_load_dwordx4 v108, s[12:15], s42 offen offset:0 lds\n"
      "s_add_u32 s42, s42, 41472\n"
      "s_add_u32 m0, s41, 36864\n"
      "s_nop 0\n"
      "buffer_load_dwordx4 v100, s[12:15], s42 offen offset:0 lds\n"
      "buffer_load_dwordx4 v101, s[12:15], s42 offen offset:1024 lds\n"
      "buffer_load_dwordx4 v102, s[12:15], s42 offen offset:2048 lds\n"
      "buffer_load_dwordx4 v103, s[12:15], s42 offen offset:3072 lds\n"
      "s_add_u32 m0, s41, 40960\n"
      "s_nop 0\n"
      "buffer_load_dwordx4 v104, s[12:15], s42 offen offset:0 lds\n"
      "buffer_load_dwordx4 v105, s[12:15], s42 offen offset:1024 lds\n"
      "buffer_load_dwordx4 v106, s[12:15], s42 offen offset:2048 lds\n"
      "buffer_load_dwordx4 v107, s[12:15], s42 offen offset:3072 lds\n"
      "s_add_u32 m0, s41, 45056\n"
      "s_nop 0\n"
      "buffer_load_dwordx4 v108, s[12:15], s42 offen offset:0 lds\n"
      "s_add_u32 s42, s42, 41472\n"
      "s_add_u32 m0, s41, 73728\n"
      "s_nop 0\n"
      "buffer_load_dwordx4 v100, s[12:15], s42 offen offset:0 lds\n"
      "buffer_load_dwordx4 v101, s[12:15], s42 offen offset:1024 lds\n"
      "buffer_load_dwordx4 v102, s[12:15], s42 offen offset:2048 lds\n"
      "buffer_load_dwordx4 v103, s[12:15], s42 offen offset:3072 lds\n"
      "s_add_u32 m0, s41, 77824\n"
      "s_nop 0\n"
      "buffer_load_dwordx4 v104, s[12:15], s42 offen offset:0 lds\n"
      "buffer_load_dwordx4 v105, s[12:15], s42 offen offset:1024 lds\n"
      "buffer_load_dwordx4 v106, s[12:15], s42 offen offset:2048 lds\n"
      "buffer_load_dwordx4 v107, s[12:15], s42 offen offset:3072 lds\n"
      "s_add_u32 m0, s41, 81920\n"
      "s_nop 0\n"
      "buffer_load_dwordx4 v108, s[12:15], s42 offen offset:0 lds\n"
      "s_add_u32 s42, s42, 41472\n"
      "s_waitcnt vmcnt(18)\n"
      "s_barrier\n"
      "ds_read_b64_tr_b16 v[64:65], v120 offset:0\n"
      "ds_read_b64_tr_b16 v[66:67], v120 offset:2304\n"
      "ds_read_b64_tr_b16 a[0:1], v118 offset:0\n"
      "ds_read_b64_tr_b16 a[2:3], v118 offset:2304\n"
      "ds_read_b64_tr_b16 a[72:73], v118 offset:8\n"
      "ds_read_b64_tr_b16 a[74:75], v118 offset:2312\n"
      "ds_read_b64_tr_b16 a[144:145], v118 offset:64\n"
      "ds_read_b64_tr_b16 a[146:147], v118 offset:2368\n"
      "ds_read_b64_tr_b16 a[216:217], v118 offset:72\n"
      "ds_read_b64_tr_b16 a[218:219], v118 offset:2376\n"
      "ds_read_b64_tr_b16 a[36:37], v119 offset:0\n"
      "ds_read_b64_tr_b16 a[38:39], v119 offset:2304\n"
      "ds_read_b64_tr_b16 a[108:109], v119 offset:8\n"
      "ds_read_b64_tr_b16 a[110:111], v119 offset:2312\n"
      "ds_read_b64_tr_b16 a[180:181], v119 offset:64\n"
      "ds_read_b64_tr_b16 a[182:183], v119 offset:2368\n"
      "ds_read_b64_tr_b16 a[252:253], v119 offset:72\n"
      "ds_read_b64_tr_b16 a[254:255], v119 offset:2376\n"
      "v_add_u32 v118, 36864, v118\n"
      "v_add_u32 v119, 36864, v119\n"
      "v_add_u32 v120, 36864, v120\n"
      "s_add_u32 m0, s41, 110592\n"
      "s_nop 0\n"
      "buffer_load_dwordx4 v100, s[12:15], s42 offen offset:0 lds\n"
      "buffer_load_dwordx4 v101, s[12:15], s42 offen offset:1024 lds\n"
      "buffer_load_dwordx4 v102, s[12:15], s42 offen offset:2048 lds\n"
      "buffer_load_dwordx4 v103, s[12:15], s42 offen offset:3072 lds\n"
      "s_add_u32 m0, s41, 114688\n"
      "s_nop 0\n"
      "buffer_load_dwordx4 v104, s[12:15], s42 offen offset:0 lds\n"
      "buffer_load_dwordx4 v105, s[12:15], s42 offen offset:1024 lds\n"
      "buffer_load_dwordx4 v106, s[12:15], s42 offen offset:2048 lds\n"
      "buffer_load_dwordx4 v107, s[12:15], s42 offen offset:3072 lds\n"
      "s_add_u32 m0, s41, 118784\n"
      "s_nop 0\n"
      "buffer_load_dwordx4 v108, s[12:15], s42 offen offset:0 lds\n"
      "s_add_u32 s42, s42, 41472\n"
      "s_waitcnt vmcnt(18) lgkmcnt(0)\n"
      "s_barrier\n"
      "ds_read_b64_tr_b16 v[68:69], v120 offset:0\n"
      "ds_read_b64_tr_b16 v[70:71], v120 offset:2304\n"
      "ds_read_b64_tr_b16 a[4:5], v118 offset:0\n"
      "ds_read_b64_tr_b16 a[6:7], v118 offset:2304\n"
      "ds_read_b64_tr_b16 a[76:77], v118 offset:8\n"
      "ds_read_b64_tr_b16 a[78:79], v118 offset:2312\n"
      "ds_read_b64_tr_b16 a[148:149], v118 offset:64\n"
      "ds_read_b64_tr_b16 a[150:151], v118 offset:2368\n"
      "ds_read_b64_tr_b16 a[220:221], v118 offset:72\n"
      "ds_read_b64_tr_b16 a[222:223], v118 offset:2376\n"
      "ds_read_b64_tr_b16 a[40:41], v119 offset:0\n"
      "ds_read_b64_tr_b16 a[42:43], v119 offset:2304\n"
      "ds_read_b64_tr_b16 a[112:113], v119 offset:8\n"
      "ds_read_b64_tr_b16 a[114:115], v119 offset:2312\n"
      "ds_read_b64_tr_b16 a[184:185], v119 offset:64\n"
      "ds_read_b64_tr_b16 a[186:187], v119 offset:2368\n"
      "ds_read_b64_tr_b16 v[32:33], v119 offset:72\n"
      "ds_read_b64_tr_b16 v[34:35], v119 offset:2376\n"
      "v_add_u32 v118, 36864, v118\n"
      "v_add_u32 v119, 36864, v119\n"
      "v_add_u32 v120, 36864, v120\n"
      "s_add_u32 m0, s41, 0\n"
      "s_nop 0\n"
      "buffer_load_dwordx4 v100, s[12:15], s42 offen offset:0 lds\n"
      "buffer_load_dwordx4 v101, s[12:15], s42 offen offset:1024 lds\n"
      "buffer_load_dwordx4 v102, s[12:15], s42 offen offset:2048 lds\n"
      "buffer_load_dwordx4 v103, s[12:15], s42 offen offset:3072 lds\n"
      "s_add_u32 m0, s41, 4096\n"
      "s_nop 0\n"
      "buffer_load_dwordx4 v104, s[12:15], s42 offen offset:0 lds\n"
      "buffer_load_dwordx4 v105, s[12:15], s42 offen offset:1024 lds\n"
      "buffer_load_dwordx4 v106, s[12:15], s42 offen offset:2048 lds\n"
      "buffer_load_dwordx4 v107, s[12:15], s42 offen offset:3072 lds\n"
      "s_add_u32 m0, s41, 8192\n"
      "s_nop 0\n"
      "buffer_load_dwordx4 v108, s[12:15], s42 offen offset:0 lds\n"
      "s_add_u32 s42, s42, 41472\n"
      "s_waitcnt vmcnt(18) lgkmcnt(0)\n"
      "s_barrier\n"
      "ds_read_b64_tr_b16 v[72:73], v120 offset:0\n"
      "ds_read_b64_tr_b16 v[74:75], v120 offset:2304\n"
      "ds_read_b64_tr_b16 a[8:9], v118 offset:0\n"
      "ds_read_b64_tr_b16 a[10:11], v118 offset:2304\n"
      "ds_read_b64_tr_b16 a[80:81], v118 offset:8\n"
      "ds_read_b64_tr_b16 a[82:83], v118 offset:2312\n"
      "ds_read_b64_tr_b16 a[152:153], v118 offset:64\n"
      "ds_read_b64_tr_b16 a[154:155], v118 offset:2368\n"
      "ds_read_b64_tr_b16 a[224:225], v118 offset:72\n"
      "ds_read_b64_tr_b16 a[226:227], v118 offset:2376\n"
      "ds_read_b64_tr_b16 a[44:45], v119 offset:0\n"
      "ds_read_b64_tr_b16 a[46:47], v119 offset:2304\n"
      "ds_read_b64_tr_b16 a[116:117], v119 offset:8\n"
      "ds_read_b64_tr_b16 a[118:119], v119 offset:2312\n"
      "ds_read_b64_tr_b16 a[188:189], v119 offset:64\n"
      "ds_read_b64_tr_b16 a[190:191], v119 offset:2368\n"
      "ds_read_b64_tr_b16 v[36:37], v119 offset:72\n"
      "ds_read_b64_tr_b16 v[38:39], v119 offset:2376\n"
      "v_add_u32 v118, 36864, v118\n"
      "v_add_u32 v119, 36864, v119\n"
      "v_add_u32 v120, 36864, v120\n"
      "s_add_u32 m0, s41, 36864\n"
      "s_nop 0\n"
      "buffer_load_dwordx4 v100, s[12:15], s42 offen offset:0 lds\n"
      "buffer_load_dwordx4 v101, s[12:15], s42 offen offset:1024 lds\n"
      "buffer_load_dwordx4 v102, s[12:15], s42 offen offset:2048 lds\n"
      "buffer_load_dwordx4 v103, s[12:15], s42 offen offset:3072 lds\n"
      "s_add_u32 m0, s41, 40960\n"
      "s_nop 0\n"
      "buffer_load_dwordx4 v104, s[12:15], s42 offen offset:0 lds\n"
      "buffer_load_dwordx4 v105, s[12:15], s42 offen offset:1024 lds\n"
      "buffer_load_dwordx4 v106, s[12:15], s42 offen offset:2048 lds\n"
      "buffer_load_dwordx4 v107, s[12:15], s42 offen offset:3072 lds\n"
      "s_add_u32 m0, s41, 45056\n"
      "s_nop 0\n"
      "buffer_load_dwordx4 v108, s[12:15], s42 offen offset:0 lds\n"
      "s_add_u32 s42, s42, 41472\n"
      "s_waitcnt vmcnt(18) lgkmcnt(0)\n"
      "s_barrier\n"
      "ds_read_b64_tr_b16 v[76:77], v120 offset:0\n"
      "ds_read_b64_tr_b16 v[78:79], v120 offset:2304\n"
      "ds_read_b64_tr_b16 a[12:13], v118 offset:0\n"
      "ds_read_b64_tr_b16 a[14:15], v118 offset:2304\n"
      "ds_read_b64_tr_b16 a[84:85], v118 offset:8\n"
      "ds_read_b64_tr_b16 a[86:87], v118 offset:2312\n"
      "ds_read_b64_tr_b16 a[156:157], v118 offset:64\n"
      "ds_read_b64_tr_b16 a[158:159], v118 offset:2368\n"
      "ds_read_b64_tr_b16 a[228:229], v118 offset:72\n"
      "ds_read_b64_tr_b16 a[230:231], v118 offset:2376\n"
      "ds_read_b64_tr_b16 a[48:49], v119 offset:0\n"
      "ds_read_b64_tr_b16 a[50:51], v119 offset:2304\n"
      "ds_read_b64_tr_b16 a[120:121], v119 offset:8\n"
      "ds_read_b64_tr_b16 a[122:123], v119 offset:2312\n"
      "ds_read_b64_tr_b16 a[192:193], v119 offset:64\n"
      "ds_read_b64_tr_b16 a[194:195], v119 offset:2368\n"
      "ds_read_b64_tr_b16 v[40:41], v119 offset:72\n"
      "ds_read_b64_tr_b16 v[42:43], v119 offset:2376\n"
      "v_add_u32 v118, 4294856704, v118\n"
      "v_add_u32 v119, 4294856704, v119\n"
      "v_add_u32 v120, 4294856704, v120\n"
      "s_add_u32 m0, s41, 73728\n"
      "s_nop 0\n"
      "buffer_load_dwordx4 v100, s[12:15], s42 offen offset:0 lds\n"
      "buffer_load_dwordx4 v101, s[12:15], s42 offen offset:1024 lds\n"
      "buffer_load_dwordx4 v102, s[12:15], s42 offen offset:2048 lds\n"
      "buffer_load_dwordx4 v103, s[12:15], s42 offen offset:3072 lds\n"
      "s_add_u32 m0, s41, 77824\n"
      "s_nop 0\n"
      "buffer_load_dwordx4 v104, s[12:15], s42 offen offset:0 lds\n"
      "buffer_load_dwordx4 v105, s[12:15], s42 offen offset:1024 lds\n"
      "buffer_load_dwordx4 v106, s[12:15], s42 offen offset:2048 lds\n"
      "buffer_load_dwordx4 v107, s[12:15], s42 offen offset:3072 lds\n"
      "s_add_u32 m0, s41, 81920\n"
      "s_nop 0\n"
      "buffer_load_dwordx4 v108, s[12:15], s42 offen offset:0 lds\n"
      "s_add_u32 s42, s42, 41472\n"
      "s_waitcnt vmcnt(18) lgkmcnt(0)\n"
      "s_barrier\n"
      "ds_read_b64_tr_b16 v[80:81], v120 offset:0\n"
      "ds_read_b64_tr_b16 v[82:83], v120 offset:2304\n"
      "ds_read_b64_tr_b16 a[16:17], v118 offset:0\n"
      "ds_read_b64_tr_b16 a[18:19], v118 offset:2304\n"
      "ds_read_b64_tr_b16 a[88:89], v118 offset:8\n"
      "ds_read_b64_tr_b16 a[90:91], v118 offset:2312\n"
      "ds_read_b64_tr_b16 a[160:161], v118 offset:64\n"
      "ds_read_b64_tr_b16 a[162:163], v118 offset:2368\n"
      "ds_read_b64_tr_b16 a[232:233], v118 offset:72\n"
      "ds_read_b64_tr_b16 a[234:235], v118 offset:2376\n"
      "ds_read_b64_tr_b16 a[52:53], v119 offset:0\n"
      "ds_read_b64_tr_b16 a[54:55], v119 offset:2304\n"
      "ds_read_b64_tr_b16 a[124:125], v119 offset:8\n"
      "ds_read_b64_tr_b16 a[126:127], v119 offset:2312\n"
      "ds_read_b64_tr_b16 a[196:197], v119 offset:64\n"
      "ds_read_b64_tr_b16 a[198:199], v119 offset:2368\n"
      "ds_read_b64_tr_b16 v[44:45], v119 offset:72\n"
      "ds_read_b64_tr_b16 v[46:47], v119 offset:2376\n"
      "v_add_u32 v118, 36864, v118\n"
      "v_add_u32 v119, 36864, v119\n"
      "v_add_u32 v120, 36864, v120\n"
      "s_add_u32 m0, s41, 110592\n"
      "s_nop 0\n"
      "buffer_load_dwordx4 v100, s[12:15], s42 offen offset:0 lds\n"
      "buffer_load_dwordx4 v101, s[12:15], s42 offen offset:1024 lds\n"
      "buffer_load_dwordx4 v102, s[12:15], s42 offen offset:2048 lds\n"
      "buffer_load_dwordx4 v103, s[12:15], s42 offen offset:3072 lds\n"
      "s_add_u32 m0, s41, 114688\n"
      "s_nop 0\n"
      "buffer_load_dwordx4 v104, s[12:15], s42 offen offset:0 lds\n"
      "buffer_load_dwordx4 v105, s[12:15], s42 offen offset:1024 lds\n"
      "buffer_load_dwordx4 v106, s[12:15], s42 offen offset:2048 lds\n"
      "buffer_load_dwordx4 v107, s[12:15], s42 offen offset:3072 lds\n"
      "s_add_u32 m0, s41, 118784\n"
      "s_nop 0\n"
      "buffer_load_dwordx4 v108, s[12:15], s42 offen offset:0 lds\n"
      "s_add_u32 s42, s42, 41472\n"
      "s_waitcnt vmcnt(18) lgkmcnt(0)\n"
      "s_barrier\n"
      "ds_read_b64_tr_b16 v[84:85], v120 offset:0\n"
      "ds_read_b64_tr_b16 v[86:87], v120 offset:2304\n"
      "ds_read_b64_tr_b16 a[20:21], v118 offset:0\n"
      "ds_read_b64_tr_b16 a[22:23], v118 offset:2304\n"
      "ds_read_b64_tr_b16 a[92:93], v118 offset:8\n"
      "ds_read_b64_tr_b16 a[94:95], v118 offset:2312\n"
      "ds_read_b64_tr_b16 a[164:165], v118 offset:64\n"
      "ds_read_b64_tr_b16 a[166:167], v118 offset:2368\n"
      "ds_read_b64_tr_b16 a[236:237], v118 offset:72\n"
      "ds_read_b64_tr_b16 a[238:239], v118 offset:2376\n"
      "ds_read_b64_tr_b16 a[56:57], v119 offset:0\n"
      "ds_read_b64_tr_b16 a[58:59], v119 offset:2304\n"
      "ds_read_b64_tr_b16 a[128:129], v119 offset:8\n"
      "ds_read_b64_tr_b16 a[130:131], v119 offset:2312\n"
      "ds_read_b64_tr_b16 a[200:201], v119 offset:64\n"
      "ds_read_b64_tr_b16 a[202:203], v119 offset:2368\n"
      "ds_read_b64_tr_b16 v[48:49], v119 offset:72\n"
      "ds_read_b64_tr_b16 v[50:51], v119 offset:2376\n"
      "v_add_u32 v118, 36864, v118\n"
      "v_add_u32 v119, 36864, v119\n"
      "v_add_u32 v120, 36864, v120\n"
      "s_add_u32 m0, s41, 0\n"
      "s_nop 0\n"
      "buffer_load_dwordx4 v109, s[12:15], s43 offen offset:0 lds\n"
      "buffer_load_dwordx4 v110, s[12:15], s43 offen offset:1024 lds\n"
      "buffer_load_dwordx4 v111, s[12:15], s43 offen offset:2048 lds\n"
      "buffer_load_dwordx4 v112, s[12:15], s43 offen offset:3072 lds\n"
      "s_add_u32 m0, s41, 4096\n"
      "s_nop 0\n"
      "buffer_load_dwordx4 v113, s[12:15], s43 offen offset:0 lds\n"
      "buffer_load_dwordx4 v114, s[12:15], s43 offen offset:1024 lds\n"
      "buffer_load_dwordx4 v115, s[12:15], s43 offen offset:2048 lds\n"
      "buffer_load_dwordx4 v116, s[12:15], s43 offen offset:3072 lds\n"
      "s_add_u32 m0, s41, 8192\n"
      "s_nop 0\n"
      "buffer_load_dwordx4 v117, s[12:15], s43 offen offset:0 lds\n"
      "s_add_u32 s42, s42, 41472\n"
      "s_waitcnt vmcnt(18) lgkmcnt(0)\n"
      "s_barrier\n"
      "ds_read_b64_tr_b16 v[88:89], v120 offset:0\n"
      "ds_read_b64_tr_b16 v[90:91], v120 offset:2304\n"
      "ds_read_b64_tr_b16 a[24:25], v118 offset:0\n"
      "ds_read_b64_tr_b16 a[26:27], v118 offset:2304\n"
      "ds_read_b64_tr_b16 a[96:97], v118 offset:8\n"
      "ds_read_b64_tr_b16 a[98:99], v118 offset:2312\n"
      "ds_read_b64_tr_b16 a[168:169], v118 offset:64\n"
      "ds_read_b64_tr_b16 a[170:171], v118 offset:2368\n"
      "ds_read_b64_tr_b16 a[240:241], v118 offset:72\n"
      "ds_read_b64_tr_b16 a[242:243], v118 offset:2376\n"
      "ds_read_b64_tr_b16 a[60:61], v119 offset:0\n"
      "ds_read_b64_tr_b16 a[62:63], v119 offset:2304\n"
      "ds_read_b64_tr_b16 a[132:133], v119 offset:8\n"
      "ds_read_b64_tr_b16 a[134:135], v119 offset:2312\n"
      "ds_read_b64_tr_b16 a[204:205], v119 offset:64\n"
      "ds_read_b64_tr_b16 a[206:207], v119 offset:2368\n"
      "ds_read_b64_tr_b16 v[52:53], v119 offset:72\n"
      "ds_read_b64_tr_b16 v[54:55], v119 offset:2376\n"
      "v_add_u32 v118, 36864, v118\n"
      "v_add_u32 v119, 36864, v119\n"
      "v_add_u32 v120, 36864, v120\n"
      "s_waitcnt vmcnt(9) lgkmcnt(0)\n"
      "s_barrier\n"
      "ds_read_b64_tr_b16 v[92:93], v120 offset:0\n"
      "ds_read_b64_tr_b16 v[94:95], v120 offset:2304\n"
      "ds_read_b64_tr_b16 a[28:29], v118 offset:0\n"
      "ds_read_b64_tr_b16 a[30:31], v118 offset:2304\n"
      "ds_read_b64_tr_b16 a[100:101], v118 offset:8\n"
      "ds_read_b64_tr_b16 a[102:103], v118 offset:2312\n"
      "ds_read_b64_tr_b16 a[172:173], v118 offset:64\n"
      "ds_read_b64_tr_b16 a[174:175], v118 offset:2368\n"
      "ds_read_b64_tr_b16 a[244:245], v118 offset:72\n"
      "ds_read_b64_tr_b16 a[246:247], v118 offset:2376\n"
      "ds_read_b64_tr_b16 a[64:65], v119 offset:0\n"
      "ds_read_b64_tr_b16 a[66:67], v119 offset:2304\n"
      "ds_read_b64_tr_b16 a[136:137], v119 offset:8\n"
      "ds_read_b64_tr_b16 a[138:139], v119 offset:2312\n"
      "ds_read_b64_tr_b16 a[208:209], v119 offset:64\n"
      "ds_read_b64_tr_b16 a[210:211], v119 offset:2368\n"
      "ds_read_b64_tr_b16 v[56:57], v119 offset:72\n"
      "ds_read_b64_tr_b16 v[58:59], v119 offset:2376\n"
      "v_add_u32 v118, 4294856704, v118\n"
      "v_add_u32 v119, 4294856704, v119\n"
      "v_add_u32 v120, 4294856704, v120\n"
      "s_waitcnt vmcnt(0) lgkmcnt(0)\n"
      "s_barrier\n"
      "ds_read_b64_tr_b16 v[96:97], v120 offset:0\n"
      "ds_read_b64_tr_b16 v[98:99], v120 offset:2304\n"
      "ds_read_b64_tr_b16 a[32:33], v118 offset:0\n"
      "ds_read_b64_tr_b16 a[34:35], v118 offset:2304\n"
      "ds_read_b64_tr_b16 a[104:105], v118 offset:8\n"
      "ds_read_b64_tr_b16 a[106:107], v118 offset:2312\n"
      "ds_read_b64_tr_b16 a[176:177], v118 offset:64\n"
      "ds_read_b64_tr_b16 a[178:179], v118 offset:2368\n"
      "ds_read_b64_tr_b16 a[248:249], v118 offset:72\n"
      "ds_read_b64_tr_b16 a[250:251], v118 offset:2376\n"
      "ds_read_b64_tr_b16 a[68:69], v119 offset:0\n"
      "ds_read_b64_tr_b16 a[70:71], v119 offset:2304\n"
      "ds_read_b64_tr_b16 a[140:141], v119 offset:8\n"
      "ds_read_b64_tr_b16 a[142:143], v119 offset:2312\n"
      "ds_read_b64_tr_b16 a[212:213], v119 offset:64\n"
      "ds_read_b64_tr_b16 a[214:215], v119 offset:2368\n"
      "ds_read_b64_tr_b16 v[60:61], v119 offset:72\n"
      "ds_read_b64_tr_b16 v[62:63], v119 offset:2376\n"
      "v_add_u32 v118, 36864, v118\n"
      "v_add_u32 v119, 36864, v119\n"
      "v_add_u32 v120, 36864, v120\n"
      "s_waitcnt lgkmcnt(0)\n"
      "s_getpc_b64 vcc\n"
      ".Lw_ct:\n"
      "s_add_u32 vcc_lo, vcc_lo, .Lw_p0-.Lw_ct\n"
      "s_addc_u32 vcc_hi, vcc_hi, 0\n"
      "s_lshl_b32 s30, s39, 1\n"
      "v_lshl_add_u32 v130, v0, 3, s30\n"
      "v_min_u32 v130, .Lw_last-.Lw_p0-4, v130\n"
      "global_load_dword v130, v130, vcc\n"
      "s_mul_i32 s21, s21, 1664525\n"
      "s_add_u32 s21, s21, 1013904223\n"
      "v_mov_b32 v25, s21\n"
      "buffer_store_dword v25, v22, s[8:11], 0 offen sc1\n"
      "v_not_b32 v124, v25\n"
      "v_not_b32 v125, v25\n"
      "v_not_b32 v126, v25\n"
      "v_not_b32 v127, v25\n"
      "s_add_u32 s30, s38, 0\n"
      "buffer_store_dwordx4 v[124:127], v121, s[8:11], s30 offen sc1\n"
      "v_xor_b32 v25, v25, v26\n"
      "v_mov_b32 v27, 0x3ae38e39\n"
      "s_barrier\n"
      "v_mov_b32 v21, 0\n"
      "ds_write_b32 v23, v21\n"
      "s_waitcnt lgkmcnt(0)\n"
      "s_barrier\n"
      "s_add_u32 s6, s36, 1572864\n"
      "s_add_u32 s36, s36, 4294942720\n"
      "s_mov_b32 s51, 24576\n"
      "s_waitcnt vmcnt(0) lgkmcnt(0)\n"
      "s_cmp_lg_u32 s44, 0\n"
      "s_cbranch_scc1 .Lv_start\n"
      "s_mov_b64 exec, 0\n"
      "s_lshr_b32 s22, s39, 13\n"
      ".Lw_pl:\n"
      "s_cmp_eq_u32 s22, 0\n"
      "s_cbranch_scc1 .Lw_p0\n"
      "s_cmp_eq_u32 s22, 1\n"
      "s_cbranch_scc1 .Lw_p1\n"
      "s_branch .Lw_pe\n"
      ".Lw_pr:\n"
      "s_add_u32 s22, s22, 2\n"
      "s_branch .Lw_pl\n"
      ".Lw_pe:\n"
      "s_mov_b64 exec, -1\n"
      "s_add_u32 s50, s38, 4294893568\n"
      "s_add_u32 s49, s38, 4294893568\n"
      "s_add_u32 s38, s38, 4294942720\n"
      "s_cmp_eq_u32 s36, s37\n"
      "s_cselect_b32 s48, 49152, s51\n"
      "s_add_u32 s36, s36, s48\n"
      "s_min_u32 s36, s36, s6\n"
      "buffer_load_dword v28, v11, s[8:11], s36 offen sc1\n"
      "s_mov_b32 s46, s36\n"
      "s_add_u32 s36, s36, 24576\n"
      "s_min_u32 s36, s36, s6\n"
      "s_waitcnt vmcnt(0)\n"
      "v_cmp_ne_u32 vcc, s21, v28\n"
      "s_add_u32 m0, s39, 0\n"
      "s_cbranch_vccnz .Lsp_w0_1\n"
      ".Lbk_w0_1:\n"
      "buffer_load_dword v28, v11, s[8:11], s36 offen sc1\n"
      "buffer_load_dwordx4 v0, s[8:11], s46 offen offset:0 sc1 lds\n"
      "buffer_load_dwordx4 v0, s[8:11], s46 offen offset:1024 sc1 lds\n"
      "buffer_load_dwordx4 v0, s[8:11], s46 offen offset:3072 sc1 lds\n"
      "buffer_load_dwordx4 v0, s[8:11], s46 offen offset:2048 sc1 lds\n"
      "s_add_u32 m0, s40, 0\n"
      "s_nop 0\n"
      "buffer_load_dwordx4 v1, s[8:11], s46 offen offset:0 sc1 lds\n"
      "s_sub_u32 s47, s46, s45\n"
      "buffer_load_dwordx2 v[200:201], v14, s[8:11], s47 offen sc1\n"
      "buffer_load_dwordx4 v[192:195], v13, s[8:11], s47 offen sc1\n"
      "buffer_load_dword v26, v23, s[8:11], s31 offen\n"
      "s_add_u32 m0, s39, 20480\n"
      "s_mov_b32 s46, s36\n"
      "s_waitcnt vmcnt(8)\n"
      "v_cmp_ne_u32 vcc, s21, v28\n"
      "buffer_load_dword v26, v23, s[8:11], s31 offen\n"
      "s_cmp_eq_u32 s36, s37\n"
      "s_cselect_b32 s48, 49152, s51\n"
      "s_add_u32 s36, s36, s48\n"
      "s_cbranch_vccnz .Lsp_w1_2\n"
      ".Lbk_w1_2:\n"
      "s_min_u32 s36, s36, s6\n"
      "buffer_load_dword v28, v11, s[8:11], s36 offen sc1\n"
      "buffer_load_dwordx4 v0, s[8:11], s46 offen offset:0 sc1 lds\n"
      "buffer_load_dwordx4 v0, s[8:11], s46 offen offset:1024 sc1 lds\n"
      "buffer_load_dwordx4 v0, s[8:11], s46 offen offset:3072 sc1 lds\n"
      "v_add_u32 v21, 1, v21\n"
      "s_waitcnt vmcnt(8)\n"
      "ds_write_b32 v23, v21\n"
      "buffer_load_dwordx4 v0, s[8:11], s46 offen offset:2048 sc1 lds\n"
      "s_add_u32 m0, s40, 20480\n"
      "buffer_load_dwordx4 v[196:199], v13, s[8:11], s47 offen offset:1024 sc1\n"
      "buffer_load_dwordx4 v1, s[8:11], s46 offen offset:0 sc1 lds\n"
      "ds_read_b32 v22, v24\n"
      "s_waitcnt lgkmcnt(0)\n"
      "v_cmp_gt_i32 vcc, v21, v22\n"
      "s_waitcnt vmcnt(10)\n"
      "v_and_b32 v189, 0xffff0000, v200\n"
      "v_lshlrev_b32 v188, 16, v200\n"
      "s_cbranch_vccnz .Lsp_wy0_3\n"
      ".Lbk_wy0_3:\n"
      "ds_read_b128 v[104:107], v0 offset:1024\n"
      "v_lshlrev_b32 v190, 16, v201\n"
      "ds_read_b128 v[100:103], v0 offset:0\n"
      "ds_read_b128 v[120:123], v0 offset:5120\n"
      "s_waitcnt vmcnt(9)\n"
      "v_lshlrev_b32 v178, 16, v195\n"
      "ds_read_b128 v[112:115], v0 offset:3072\n"
      "ds_read_b128 v[116:119], v0 offset:4096\n"
      "v_lshlrev_b32 v174, 16, v193\n"
      "ds_read_b128 v[124:127], v0 offset:6144\n"
      "s_sub_u32 s47, s46, s45\n"
      "ds_read_b128 v[108:111], v0 offset:2048\n"
      "v_and_b32 v191, 0xffff0000, v201\n"
      "v_and_b32 v177, 0xffff0000, v194\n"
      "ds_read_b128 v[136:139], v0 offset:8192\n"
      "v_lshlrev_b32 v176, 16, v194\n"
      "ds_read_b128 v[128:131], v0 offset:7168\n"
      "v_lshlrev_b32 v172, 16, v192\n"
      "v_and_b32 v175, 0xffff0000, v193\n"
      "buffer_load_dwordx2 v[200:201], v14, s[8:11], s47 offen sc1\n"
      "ds_read2_b32 v[132:133], v2 offset0:0 offset1:32\n"
      "v_and_b32 v173, 0xffff0000, v192\n"
      "ds_read2_b32 v[134:135], v2 offset0:64 offset1:96\n"
      "s_mov_b32 s20, 15\n"
      "s_waitcnt lgkmcnt(0)\n"
      "s_nop 4\n"
      "s_branch .Lw_loop\n"
      ".p2align 12\n"
      ".Lw_p0:\n"
      "s_branch .Lw_pr\n"
      ".p2align 5\n"
      ".Lw_loop:\n"
      "v_mfma_f32_16x16x32_bf16 v[188:191], v[64:67], v[100:103], v[188:191]\n"
      "ds_read_b128 v[140:143], v0 offset:9216\n"
      "v_and_b32 v179, 0xffff0000, v195\n"
      "v_mfma_f32_16x16x32_bf16 v[188:191], v[68:71], v[104:107], v[188:191]\n"
      "buffer_load_dwordx4 v[192:195], v13, s[8:11], s47 offen sc1\n"
      "v_mfma_f32_16x16x32_bf16 v[188:191], v[72:75], v[108:111], v[188:191]\n"
      "v_add_f32 v218, v218, v182\n"
      "v_mfma_f32_16x16x32_bf16 v[188:191], v[76:79], v[112:115], v[188:191]\n"
      "ds_read_b128 v[156:159], v0 offset:13312\n"
      "v_fmac_f32 v219, v184, v184\n"
      "v_mfma_f32_16x16x32_bf16 v[188:191], v[80:83], v[116:119], v[188:191]\n"
      "ds_read_b128 v[164:167], v0 offset:15360\n"
      "v_cvt_pk_bf16_f32 v213, v182, v183\n"
      "v_mfma_f32_16x16x32_bf16 v[188:191], v[84:87], v[120:123], v[188:191]\n"
      "ds_read_b128 v[144:147], v0 offset:10240\n"
      "v_mfma_f32_16x16x32_bf16 v[188:191], v[88:91], v[124:127], v[188:191]\n"
      "s_waitcnt lgkmcnt(4)\n"
      "v_cvt_pk_bf16_f32 v215, v186, v187\n"
      "v_add_f32 v218, v218, v183\n"
      "v_mfma_f32_16x16x32_bf16 v[188:191], v[92:95], v[128:131], v[188:191]\n"
      "buffer_store_dwordx4 v[208:211], v15, s[8:11], s38 offen sc1\n"
      "v_mfma_f32_16x16x32_bf16 v[188:191], v[96:99], v[132:135], v[188:191]\n"
      "v_cvt_pk_bf16_f32 v212, v180, v181\n"
      "v_add_f32 v218, v218, v184\n"
      "v_mfma_f32_16x16x32_bf16 v[172:175], a[0:3], v[100:103], v[172:175]\n"
      "s_add_u32 s50, s50, 24576\n"
      "v_cvt_pk_bf16_f32 v214, v184, v185\n"
      "v_add_f32 v218, v218, v185\n"
      "v_mfma_f32_16x16x32_bf16 v[172:175], a[4:7], v[104:107], v[172:175]\n"
      "s_mov_b32 s46, s36\n"
      "v_add_f32 v218, v218, v186\n"
      "v_mfma_f32_16x16x32_bf16 v[172:175], a[8:11], v[108:111], v[172:175]\n"
      "ds_read_b128 v[148:151], v0 offset:11264\n"
      "v_add_f32 v218, v218, v187\n"
      "v_mfma_f32_16x16x32_bf16 v[172:175], a[12:15], v[112:115], v[172:175]\n"
      "v_fmac_f32 v219, v185, v185\n"
      "v_add_u32 v21, 1, v21\n"
      "v_mfma_f32_16x16x32_bf16 v[172:175], a[16:19], v[116:119], v[172:175]\n"
      "s_add_u32 s36, s36, 24576\n"
      "ds_read_b128 v[152:155], v0 offset:12288\n"
      "v_fmac_f32 v219, v186, v186\n"
      "v_mfma_f32_16x16x32_bf16 v[172:175], a[20:23], v[120:123], v[172:175]\n"
      "buffer_store_dwordx2 v[216:217], v16, s[8:11], s38 offen sc1\n"
      "v_mfma_f32_16x16x32_bf16 v[172:175], a[24:27], v[124:127], v[172:175]\n"
      "ds_read_b128 v[160:163], v0 offset:14336\n"
      "v_mfma_f32_16x16x32_bf16 v[172:175], a[28:31], v[128:131], v[172:175]\n"
      "ds_read2_b32 v[170:171], v3 offset0:64 offset1:96\n"
      "v_mfma_f32_16x16x32_bf16 v[172:175], a[32:35], v[132:135], v[172:175]\n"
      "ds_read2_b32 v[168:169], v3 offset0:0 offset1:32\n"
      "s_min_u32 s36, s36, s6\n"
      "v_mfma_f32_16x16x32_bf16 v[172:175], a[36:39], v[136:139], v[172:175]\n"
      "s_waitcnt lgkmcnt(4)\n"
      "v_mfma_f32_16x16x32_bf16 v[172:175], a[40:43], v[140:143], v[172:175]\n"
      "buffer_store_dwordx4 v[212:215], v15, s[8:11], s38 offen offset:1024 sc1\n"
      "v_mfma_f32_16x16x32_bf16 v[172:175], a[44:47], v[144:147], v[172:175]\n"
      "ds_write_b32 v18, v218\n"
      "v_mfma_f32_16x16x32_bf16 v[172:175], a[48:51], v[148:151], v[172:175]\n"
      "s_waitcnt vmcnt(11) lgkmcnt(4)\n"
      "v_fmac_f32_e64 v219, v187, v187\n"
      "v_cmp_ne_u32 vcc, s21, v28\n"
      "v_mfma_f32_16x16x32_bf16 v[172:175], a[52:55], v[152:155], v[172:175]\n"
      "ds_write_b32 v18, v219 offset:4\n"
      "v_mfma_f32_16x16x32_bf16 v[172:175], a[56:59], v[156:159], v[172:175]\n"
      "s_waitcnt lgkmcnt(3)\n"
      "s_add_u32 m0, s39, 40960\n"
      "v_mfma_f32_16x16x32_bf16 v[172:175], a[60:63], v[160:163], v[172:175]\n"
      "ds_write_b32 v23, v21\n"
      "v_mfma_f32_16x16x32_bf16 v[172:175], a[64:67], v[164:167], v[172:175]\n"
      "s_waitcnt lgkmcnt(3)\n"
      "s_cbranch_vccnz .Lsp_w0_4\n"
      ".Lbk_w0_4:\n"
      "v_mfma_f32_16x16x32_bf16 v[172:175], a[68:71], v[168:171], v[172:175]\n"
      "v_mfma_f32_16x16x32_bf16 v[176:179], a[72:75], v[100:103], v[176:179]\n"
      "buffer_load_dword v28, v11, s[8:11], s36 offen sc1\n"
      "v_mfma_f32_16x16x32_bf16 v[176:179], a[76:79], v[104:107], v[176:179]\n"
      "s_waitcnt vmcnt(7) lgkmcnt(2)\n"
      "v_lshlrev_b32 v186, 16, v199\n"
      "v_lshlrev_b32 v184, 16, v198\n"
      "v_mfma_f32_16x16x32_bf16 v[176:179], a[80:83], v[108:111], v[176:179]\n"
      "v_lshlrev_b32 v180, 16, v196\n"
      "v_and_b32 v183, 0xffff0000, v197\n"
      "v_mfma_f32_16x16x32_bf16 v[176:179], a[84:87], v[112:115], v[176:179]\n"
      "buffer_store_dword v25, v12, s[8:11], s50 offen sc1\n"
      "v_mfma_f32_16x16x32_bf16 v[176:179], a[88:91], v[116:119], v[176:179]\n"
      "v_lshlrev_b32 v182, 16, v197\n"
      "v_and_b32 v181, 0xffff0000, v196\n"
      "v_mfma_f32_16x16x32_bf16 v[176:179], a[92:95], v[120:123], v[176:179]\n"
      "v_and_b32 v187, 0xffff0000, v199\n"
      "v_and_b32 v185, 0xffff0000, v198\n"
      "v_mfma_f32_16x16x32_bf16 v[176:179], a[96:99], v[124:127], v[176:179]\n"
      "buffer_load_dwordx4 v[196:199], v13, s[8:11], s47 offen offset:1024 sc1\n"
      "v_mfma_f32_16x16x32_bf16 v[176:179], a[100:103], v[128:131], v[176:179]\n"
      "ds_read_b32 v22, v24\n"
      "v_mul_f32 v219, v172, v172\n"
      "v_mfma_f32_16x16x32_bf16 v[176:179], a[104:107], v[132:135], v[176:179]\n"
      "v_mfma_f32_16x16x32_bf16 v[176:179], a[108:111], v[136:139], v[176:179]\n"
      "v_mfma_f32_16x16x32_bf16 v[176:179], a[112:115], v[140:143], v[176:179]\n"
      "buffer_load_dwordx4 v0, s[8:11], s46 offen offset:0 sc1 lds\n"
      "v_mfma_f32_16x16x32_bf16 v[176:179], a[116:119], v[144:147], v[176:179]\n"
      "v_add_f32 v218, v172, v173\n"
      "v_fmac_f32 v219, v173, v173\n"
      "v_mfma_f32_16x16x32_bf16 v[176:179], a[120:123], v[148:151], v[176:179]\n"
      "s_waitcnt lgkmcnt(0)\n"
      "v_cmp_gt_i32 vcc, v21, v22\n"
      "v_mfma_f32_16x16x32_bf16 v[176:179], a[124:127], v[152:155], v[176:179]\n"
      "v_mfma_f32_16x16x32_bf16 v[176:179], a[128:131], v[156:159], v[176:179]\n"
      "buffer_load_dwordx4 v0, s[8:11], s46 offen offset:1024 sc1 lds\n"
      "v_mfma_f32_16x16x32_bf16 v[176:179], a[132:135], v[160:163], v[176:179]\n"
      "s_cbranch_vccnz .Lsp_wz0_5\n"
      ".Lbk_wz0_5:\n"
      "ds_read2st64_b32 v[204:205], v17 offset1:1\n"
      "v_mfma_f32_16x16x32_bf16 v[176:179], a[136:139], v[164:167], v[176:179]\n"
      "ds_read2st64_b32 v[206:207], v17 offset0:2 offset1:3\n"
      "v_add_u32 v21, 1, v21\n"
      "v_mfma_f32_16x16x32_bf16 v[176:179], a[140:143], v[168:171], v[176:179]\n"
      "s_waitcnt vmcnt(10)\n"
      "ds_write_b32 v23, v21\n"
      "v_mfma_f32_16x16x32_bf16 v[180:183], a[144:147], v[100:103], v[180:183]\n"
      "buffer_load_dwordx4 v0, s[8:11], s46 offen offset:3072 sc1 lds\n"
      "v_mfma_f32_16x16x32_bf16 v[180:183], a[148:151], v[104:107], v[180:183]\n"
      "v_add_f32 v218, v218, v174\n"
      "v_fmac_f32 v219, v174, v174\n"
      "v_mfma_f32_16x16x32_bf16 v[180:183], a[152:155], v[108:111], v[180:183]\n"
      "v_fmac_f32 v219, v175, v175\n"
      "v_fmac_f32 v219, v176, v176\n"
      "v_mfma_f32_16x16x32_bf16 v[180:183], a[156:159], v[112:115], v[180:183]\n"
      "v_add_f32 v218, v218, v175\n"
      "v_add_f32 v218, v218, v176\n"
      "v_mfma_f32_16x16x32_bf16 v[180:183], a[160:163], v[116:119], v[180:183]\n"
      "buffer_load_dwordx4 v0, s[8:11], s46 offen offset:2048 sc1 lds\n"
      "v_mfma_f32_16x16x32_bf16 v[180:183], a[164:167], v[120:123], v[180:183]\n"
      "s_add_u32 m0, s40, 40960\n"
      "v_add_f32 v218, v218, v177\n"
      "v_cvt_pk_bf16_f32 v209, v174, v175\n"
      "v_mfma_f32_16x16x32_bf16 v[180:183], a[168:171], v[124:127], v[180:183]\n"
      "s_waitcnt lgkmcnt(0)\n"
      "v_add_f32 v188, v188, v204\n"
      "v_add_f32 v190, v190, v206\n"
      "v_mfma_f32_16x16x32_bf16 v[180:183], a[172:175], v[128:131], v[180:183]\n"
      "v_cvt_pk_bf16_f32 v208, v172, v173\n"
      "v_fmac_f32 v219, v177, v177\n"
      "v_mfma_f32_16x16x32_bf16 v[180:183], a[176:179], v[132:135], v[180:183]\n"
      "buffer_load_dwordx4 v1, s[8:11], s46 offen offset:0 sc1 lds\n"
      "v_mfma_f32_16x16x32_bf16 v[180:183], a[180:183], v[136:139], v[180:183]\n"
      "s_add_u32 s38, s38, 24576\n"
      "v_cvt_pk_bf16_f32 v210, v176, v177\n"
      "v_mfma_f32_16x16x32_bf16 v[180:183], a[184:187], v[140:143], v[180:183]\n"
      "ds_read_b32 v22, v24\n"
      "v_add_f32 v218, v218, v178\n"
      "v_mfma_f32_16x16x32_bf16 v[180:183], a[188:191], v[144:147], v[180:183]\n"
      "v_add_f32 v218, v218, v179\n"
      "v_add_f32 v218, v218, v188\n"
      "v_mfma_f32_16x16x32_bf16 v[180:183], a[192:195], v[148:151], v[180:183]\n"
      "s_add_u32 s50, s50, 24576\n"
      "v_fmac_f32 v219, v178, v178\n"
      "v_fmac_f32 v219, v179, v179\n"
      "v_mfma_f32_16x16x32_bf16 v[180:183], a[196:199], v[152:155], v[180:183]\n"
      "v_add_f32 v191, v191, v207\n"
      "v_cvt_pk_bf16_f32 v211, v178, v179\n"
      "v_mfma_f32_16x16x32_bf16 v[180:183], a[200:203], v[156:159], v[180:183]\n"
      "v_fmac_f32 v219, v188, v188\n"
      "v_add_f32 v189, v189, v205\n"
      "v_mfma_f32_16x16x32_bf16 v[180:183], a[204:207], v[160:163], v[180:183]\n"
      "s_waitcnt lgkmcnt(0)\n"
      "v_fmac_f32 v219, v189, v189\n"
      "v_cmp_gt_i32 vcc, v21, v22\n"
      "v_mfma_f32_16x16x32_bf16 v[180:183], a[208:211], v[164:167], v[180:183]\n"
      "v_fmac_f32 v219, v190, v190\n"
      "v_add_f32 v218, v218, v189\n"
      "v_mfma_f32_16x16x32_bf16 v[180:183], a[212:215], v[168:171], v[180:183]\n"
      "s_waitcnt vmcnt(11)\n"
      "v_lshlrev_b32 v172, 16, v192\n"
      "v_mfma_f32_16x16x32_bf16 v[184:187], a[216:219], v[100:103], v[184:187]\n"
      "v_and_b32 v175, 0xffff0000, v193\n"
      "v_and_b32 v177, 0xffff0000, v194\n"
      "v_mfma_f32_16x16x32_bf16 v[184:187], a[220:223], v[104:107], v[184:187]\n"
      "s_cbranch_vccnz .Lsp_wy1_6\n"
      ".Lbk_wy1_6:\n"
      "v_add_f32 v218, v218, v190\n"
      "v_mfma_f32_16x16x32_bf16 v[184:187], a[224:227], v[108:111], v[184:187]\n"
      "ds_read_b128 v[104:107], v0 offset:21504\n"
      "v_cvt_pk_bf16_f32 v216, v188, v189\n"
      "v_mfma_f32_16x16x32_bf16 v[184:187], a[228:231], v[112:115], v[184:187]\n"
      "v_add_f32 v218, v218, v191\n"
      "v_add_f32 v218, v218, v180\n"
      "v_mfma_f32_16x16x32_bf16 v[184:187], a[232:235], v[116:119], v[184:187]\n"
      "ds_read_b128 v[116:119], v0 offset:24576\n"
      "v_cvt_pk_bf16_f32 v217, v190, v191\n"
      "v_mfma_f32_16x16x32_bf16 v[184:187], a[236:239], v[120:123], v[184:187]\n"
      "ds_read_b128 v[108:111], v0 offset:22528\n"
      "v_lshlrev_b32 v188, 16, v200\n"
      "v_mfma_f32_16x16x32_bf16 v[184:187], a[240:243], v[124:127], v[184:187]\n"
      "ds_read_b128 v[100:103], v0 offset:20480\n"
      "v_fmac_f32 v219, v191, v191\n"
      "v_mfma_f32_16x16x32_bf16 v[184:187], a[244:247], v[128:131], v[184:187]\n"
      "v_lshlrev_b32 v190, 16, v201\n"
      "v_and_b32 v191, 0xffff0000, v201\n"
      "v_mfma_f32_16x16x32_bf16 v[184:187], a[248:251], v[132:135], v[184:187]\n"
      "ds_read2_b32 v[134:135], v4 offset0:64 offset1:96\n"
      "v_lshlrev_b32 v174, 16, v193\n"
      "v_mfma_f32_16x16x32_bf16 v[184:187], a[252:255], v[136:139], v[184:187]\n"
      "s_sub_u32 s47, s46, s45\n"
      "v_and_b32 v189, 0xffff0000, v200\n"
      "v_fmac_f32 v219, v180, v180\n"
      "v_mfma_f32_16x16x32_bf16 v[184:187], v[32:35], v[140:143], v[184:187]\n"
      "buffer_store_dwordx4 v[208:211], v15, s[8:11], s38 offen sc1\n"
      "v_mfma_f32_16x16x32_bf16 v[184:187], v[36:39], v[144:147], v[184:187]\n"
      "ds_read_b128 v[136:139], v0 offset:28672\n"
      "v_fmac_f32 v219, v181, v181\n"
      "v_mfma_f32_16x16x32_bf16 v[184:187], v[40:43], v[148:151], v[184:187]\n"
      "ds_read2_b32 v[132:133], v4 offset0:0 offset1:32\n"
      "v_lshlrev_b32 v178, 16, v195\n"
      "v_mfma_f32_16x16x32_bf16 v[184:187], v[44:47], v[152:155], v[184:187]\n"
      "ds_read_b128 v[112:115], v0 offset:23552\n"
      "v_add_f32 v218, v218, v181\n"
      "v_mfma_f32_16x16x32_bf16 v[184:187], v[48:51], v[156:159], v[184:187]\n"
      "ds_read_b128 v[120:123], v0 offset:25600\n"
      "v_and_b32 v173, 0xffff0000, v192\n"
      "v_mfma_f32_16x16x32_bf16 v[184:187], v[52:55], v[160:163], v[184:187]\n"
      "ds_read_b128 v[124:127], v0 offset:26624\n"
      "v_lshlrev_b32 v176, 16, v194\n"
      "v_mfma_f32_16x16x32_bf16 v[184:187], v[56:59], v[164:167], v[184:187]\n"
      "buffer_load_dwordx2 v[200:201], v14, s[8:11], s47 offen sc1\n"
      "v_mfma_f32_16x16x32_bf16 v[184:187], v[60:63], v[168:171], v[184:187]\n"
      "s_waitcnt lgkmcnt(5)\n"
      "ds_read_b128 v[128:131], v0 offset:27648\n"
      "v_add_f32 v218, v218, v182\n"
      "v_mfma_f32_16x16x32_bf16 v[188:191], v[64:67], v[100:103], v[188:191]\n"
      "ds_read_b128 v[140:143], v0 offset:29696\n"
      "v_cvt_pk_bf16_f32 v213, v182, v183\n"
      "v_mfma_f32_16x16x32_bf16 v[188:191], v[68:71], v[104:107], v[188:191]\n"
      "ds_read_b128 v[152:155], v0 offset:32768\n"
      "v_mfma_f32_16x16x32_bf16 v[188:191], v[72:75], v[108:111], v[188:191]\n"
      "s_waitcnt lgkmcnt(5)\n"
      "buffer_store_dwordx2 v[216:217], v16, s[8:11], s38 offen sc1\n"
      "v_mfma_f32_16x16x32_bf16 v[188:191], v[76:79], v[112:115], v[188:191]\n"
      "ds_read_b128 v[144:147], v0 offset:30720\n"
      "v_cvt_pk_bf16_f32 v212, v180, v181\n"
      "v_mfma_f32_16x16x32_bf16 v[188:191], v[80:83], v[116:119], v[188:191]\n"
      "s_waitcnt lgkmcnt(4)\n"
      "ds_read_b128 v[164:167], v0 offset:35840\n"
      "v_add_f32 v218, v218, v183\n"
      "v_mfma_f32_16x16x32_bf16 v[188:191], v[84:87], v[120:123], v[188:191]\n"
      "v_fmac_f32 v219, v182, v182\n"
      "v_add_f32 v218, v218, v184\n"
      "v_mfma_f32_16x16x32_bf16 v[188:191], v[88:91], v[124:127], v[188:191]\n"
      "s_waitcnt lgkmcnt(4)\n"
      "ds_read_b128 v[148:151], v0 offset:31744\n"
      "v_add_f32 v218, v218, v185\n"
      "v_mfma_f32_16x16x32_bf16 v[188:191], v[92:95], v[128:131], v[188:191]\n"
      "ds_read_b128 v[160:163], v0 offset:34816\n"
      "v_and_b32 v179, 0xffff0000, v195\n"
      "v_mfma_f32_16x16x32_bf16 v[188:191], v[96:99], v[132:135], v[188:191]\n"
      "buffer_load_dwordx4 v[192:195], v13, s[8:11], s47 offen sc1\n"
      "v_mfma_f32_16x16x32_bf16 v[172:175], a[0:3], v[100:103], v[172:175]\n"
      "v_cvt_pk_bf16_f32 v215, v186, v187\n"
      "v_fmac_f32 v219, v183, v183\n"
      "v_mfma_f32_16x16x32_bf16 v[172:175], a[4:7], v[104:107], v[172:175]\n"
      "v_cvt_pk_bf16_f32 v214, v184, v185\n"
      "v_mfma_f32_16x16x32_bf16 v[172:175], a[8:11], v[108:111], v[172:175]\n"
      "ds_read2_b32 v[168:169], v5 offset0:0 offset1:32\n"
      "v_mfma_f32_16x16x32_bf16 v[172:175], a[12:15], v[112:115], v[172:175]\n"
      "ds_read_b128 v[156:159], v0 offset:33792\n"
      "v_mfma_f32_16x16x32_bf16 v[172:175], a[16:19], v[116:119], v[172:175]\n"
      "buffer_store_dwordx4 v[212:215], v15, s[8:11], s38 offen offset:1024 sc1\n"
      "v_mfma_f32_16x16x32_bf16 v[172:175], a[20:23], v[120:123], v[172:175]\n"
      "v_fmac_f32 v219, v184, v184\n"
      "v_fmac_f32 v219, v185, v185\n"
      "v_mfma_f32_16x16x32_bf16 v[172:175], a[24:27], v[124:127], v[172:175]\n"
      "ds_read2_b32 v[170:171], v5 offset0:64 offset1:96\n"
      "v_add_f32 v218, v218, v186\n"
      "v_mfma_f32_16x16x32_bf16 v[172:175], a[28:31], v[128:131], v[172:175]\n"
      "v_fmac_f32 v219, v186, v186\n"
      "v_add_f32 v218, v218, v187\n"
      "v_mfma_f32_16x16x32_bf16 v[172:175], a[32:35], v[132:135], v[172:175]\n"
      "ds_write_b32 v18, v218\n"
      "v_mfma_f32_16x16x32_bf16 v[172:175], a[36:39], v[136:139], v[172:175]\n"
      "s_waitcnt lgkmcnt(3)\n"
      "s_add_u32 m0, s39, 61440\n"
      "v_add_u32 v21, 1, v21\n"
      "v_mfma_f32_16x16x32_bf16 v[172:175], a[40:43], v[140:143], v[172:175]\n"
      "s_mov_b32 s46, s36\n"
      "v_mfma_f32_16x16x32_bf16 v[172:175], a[44:47], v[144:147], v[172:175]\n"
      "v_fmac_f32 v219, v187, v187\n"
      "v_mfma_f32_16x16x32_bf16 v[172:175], a[48:51], v[148:151], v[172:175]\n"
      "ds_write_b32 v18, v219 offset:4\n"
      "v_mfma_f32_16x16x32_bf16 v[172:175], a[52:55], v[152:155], v[172:175]\n"
      "s_waitcnt vmcnt(10) lgkmcnt(2)\n"
      "v_and_b32 v181, 0xffff0000, v196\n"
      "v_mfma_f32_16x16x32_bf16 v[172:175], a[56:59], v[156:159], v[172:175]\n"
      "ds_write_b32 v23, v21\n"
      "v_mfma_f32_16x16x32_bf16 v[172:175], a[60:63], v[160:163], v[172:175]\n"
      "v_lshlrev_b32 v186, 16, v199\n"
      "v_cmp_ne_u32 vcc, s21, v28\n"
      "v_mfma_f32_16x16x32_bf16 v[172:175], a[64:67], v[164:167], v[172:175]\n"
      "buffer_store_dword v25, v12, s[8:11], s50 offen sc1\n"
      "v_mfma_f32_16x16x32_bf16 v[172:175], a[68:71], v[168:171], v[172:175]\n"
      "s_cmp_eq_u32 s36, s37\n"
      "s_cselect_b32 s48, 49152, s51\n"
      "s_add_u32 s36, s36, s48\n"
      "v_mfma_f32_16x16x32_bf16 v[176:179], a[72:75], v[100:103], v[176:179]\n"
      "s_cbranch_vccnz .Lsp_w1_7\n"
      ".Lbk_w1_7:\n"
      "s_min_u32 s36, s36, s6\n"
      "v_mfma_f32_16x16x32_bf16 v[176:179], a[76:79], v[104:107], v[176:179]\n"
      "buffer_load_dword v28, v11, s[8:11], s36 offen sc1\n"
      "v_mfma_f32_16x16x32_bf16 v[176:179], a[80:83], v[108:111], v[176:179]\n"
      "v_and_b32 v183, 0xffff0000, v197\n"
      "v_mul_f32 v219, v172, v172\n"
      "v_mfma_f32_16x16x32_bf16 v[176:179], a[84:87], v[112:115], v[176:179]\n"
      "buffer_load_dwordx4 v0, s[8:11], s46 offen offset:0 sc1 lds\n"
      "v_mfma_f32_16x16x32_bf16 v[176:179], a[88:91], v[116:119], v[176:179]\n"
      "v_mfma_f32_16x16x32_bf16 v[176:179], a[92:95], v[120:123], v[176:179]\n"
      "v_fmac_f32 v219, v173, v173\n"
      "v_mfma_f32_16x16x32_bf16 v[176:179], a[96:99], v[124:127], v[176:179]\n"
      "v_lshlrev_b32 v184, 16, v198\n"
      "v_and_b32 v185, 0xffff0000, v198\n"
      "v_mfma_f32_16x16x32_bf16 v[176:179], a[100:103], v[128:131], v[176:179]\n"
      "ds_read_b32 v22, v24\n"
      "v_fmac_f32 v219, v174, v174\n"
      "v_mfma_f32_16x16x32_bf16 v[176:179], a[104:107], v[132:135], v[176:179]\n"
      "buffer_load_dwordx4 v0, s[8:11], s46 offen offset:1024 sc1 lds\n"
      "v_mfma_f32_16x16x32_bf16 v[176:179], a[108:111], v[136:139], v[176:179]\n"
      "v_lshlrev_b32 v182, 16, v197\n"
      "v_add_f32 v218, v172, v173\n"
      "v_mfma_f32_16x16x32_bf16 v[176:179], a[112:115], v[140:143], v[176:179]\n"
      "v_add_f32 v218, v218, v174\n"
      "v_fmac_f32 v219, v175, v175\n"
      "v_mfma_f32_16x16x32_bf16 v[176:179], a[116:119], v[144:147], v[176:179]\n"
      "v_cvt_pk_bf16_f32 v208, v172, v173\n"
      "v_mfma_f32_16x16x32_bf16 v[176:179], a[120:123], v[148:151], v[176:179]\n"
      "s_waitcnt lgkmcnt(0)\n"
      "v_and_b32 v187, 0xffff0000, v199\n"
      "v_cmp_gt_i32 vcc, v21, v22\n"
      "v_mfma_f32_16x16x32_bf16 v[176:179], a[124:127], v[152:155], v[176:179]\n"
      "buffer_load_dwordx4 v0, s[8:11], s46 offen offset:3072 sc1 lds\n"
      "v_mfma_f32_16x16x32_bf16 v[176:179], a[128:131], v[156:159], v[176:179]\n"
      "v_lshlrev_b32 v180, 16, v196\n"
      "v_add_f32 v218, v218, v175\n"
      "v_mfma_f32_16x16x32_bf16 v[176:179], a[132:135], v[160:163], v[176:179]\n"
      "s_cbranch_vccnz .Lsp_wz1_8\n"
      ".Lbk_wz1_8:\n"
      "ds_read2st64_b32 v[204:205], v17 offset1:1\n"
      "v_mfma_f32_16x16x32_bf16 v[176:179], a[136:139], v[164:167], v[176:179]\n"
      "ds_read2st64_b32 v[206:207], v17 offset0:2 offset1:3\n"
      "v_add_u32 v21, 1, v21\n"
      "v_mfma_f32_16x16x32_bf16 v[176:179], a[140:143], v[168:171], v[176:179]\n"
      "s_waitcnt vmcnt(10)\n"
      "ds_write_b32 v23, v21\n"
      "v_mfma_f32_16x16x32_bf16 v[180:183], a[144:147], v[100:103], v[180:183]\n"
      "buffer_load_dwordx4 v0, s[8:11], s46 offen offset:2048 sc1 lds\n"
      "v_mfma_f32_16x16x32_bf16 v[180:183], a[148:151], v[104:107], v[180:183]\n"
      "v_mfma_f32_16x16x32_bf16 v[180:183], a[152:155], v[108:111], v[180:183]\n"
      "s_add_u32 m0, s40, 61440\n"
      "v_add_f32 v218, v218, v176\n"
      "v_mfma_f32_16x16x32_bf16 v[180:183], a[156:159], v[112:115], v[180:183]\n"
      "v_fmac_f32 v219, v176, v176\n"
      "v_cvt_pk_bf16_f32 v209, v174, v175\n"
      "v_mfma_f32_16x16x32_bf16 v[180:183], a[160:163], v[116:119], v[180:183]\n"
      "buffer_load_dwordx4 v[196:199], v13, s[8:11], s47 offen offset:1024 sc1\n"
      "v_mfma_f32_16x16x32_bf16 v[180:183], a[164:167], v[120:123], v[180:183]\n"
      "s_waitcnt lgkmcnt(1)\n"
      "v_cvt_pk_bf16_f32 v211, v178, v179\n"
      "v_add_f32 v190, v190, v206\n"
      "v_mfma_f32_16x16x32_bf16 v[180:183], a[168:171], v[124:127], v[180:183]\n"
      "v_add_f32 v218, v218, v177\n"
      "v_add_f32 v218, v218, v178\n"
      "v_mfma_f32_16x16x32_bf16 v[180:183], a[172:175], v[128:131], v[180:183]\n"
      "v_add_f32 v218, v218, v179\n"
      "v_add_f32 v189, v189, v205\n"
      "v_mfma_f32_16x16x32_bf16 v[180:183], a[176:179], v[132:135], v[180:183]\n"
      "buffer_load_dwordx4 v1, s[8:11], s46 offen offset:0 sc1 lds\n"
      "v_mfma_f32_16x16x32_bf16 v[180:183], a[180:183], v[136:139], v[180:183]\n"
      "v_add_f32 v188, v188, v204\n"
      "v_fmac_f32 v219, v177, v177\n"
      "v_mfma_f32_16x16x32_bf16 v[180:183], a[184:187], v[140:143], v[180:183]\n"
      "ds_read_b32 v22, v24\n"
      "v_fmac_f32 v219, v178, v178\n"
      "v_mfma_f32_16x16x32_bf16 v[180:183], a[188:191], v[144:147], v[180:183]\n"
      "v_cvt_pk_bf16_f32 v210, v176, v177\n"
      "v_fmac_f32 v219, v179, v179\n"
      "v_mfma_f32_16x16x32_bf16 v[180:183], a[192:195], v[148:151], v[180:183]\n"
      "v_add_f32 v191, v191, v207\n"
      "v_fmac_f32 v219, v188, v188\n"
      "v_mfma_f32_16x16x32_bf16 v[180:183], a[196:199], v[152:155], v[180:183]\n"
      "v_add_f32 v218, v218, v188\n"
      "v_mfma_f32_16x16x32_bf16 v[180:183], a[200:203], v[156:159], v[180:183]\n"
      "v_add_f32 v218, v218, v189\n"
      "v_add_f32 v218, v218, v190\n"
      "v_mfma_f32_16x16x32_bf16 v[180:183], a[204:207], v[160:163], v[180:183]\n"
      "s_waitcnt lgkmcnt(0)\n"
      "v_fmac_f32 v219, v189, v189\n"
      "v_cmp_gt_i32 vcc, v21, v22\n"
      "v_mfma_f32_16x16x32_bf16 v[180:183], a[208:211], v[164:167], v[180:183]\n"
      "v_cvt_pk_bf16_f32 v216, v188, v189\n"
      "v_fmac_f32 v219, v190, v190\n"
      "v_mfma_f32_16x16x32_bf16 v[180:183], a[212:215], v[168:171], v[180:183]\n"
      "s_waitcnt vmcnt(11)\n"
      "v_and_b32 v189, 0xffff0000, v200\n"
      "v_fmac_f32 v219, v191, v191\n"
      "v_mfma_f32_16x16x32_bf16 v[184:187], a[216:219], v[100:103], v[184:187]\n"
      "s_add_u32 s38, s38, 24576\n"
      "v_cvt_pk_bf16_f32 v217, v190, v191\n"
      "v_lshlrev_b32_e64 v188, 16, v200\n"
      "v_mfma_f32_16x16x32_bf16 v[184:187], a[220:223], v[104:107], v[184:187]\n"
      "s_cbranch_vccnz .Lsp_wy0_9\n"
      ".Lbk_wy0_9:\n"
      "v_mfma_f32_16x16x32_bf16 v[184:187], a[224:227], v[108:111], v[184:187]\n"
      "ds_read_b128 v[104:107], v10 offset:1024\n"
      "v_add_f32 v218, v218, v191\n"
      "v_mfma_f32_16x16x32_bf16 v[184:187], a[228:231], v[112:115], v[184:187]\n"
      "v_fmac_f32 v219, v180, v180\n"
      "v_lshlrev_b32 v190, 16, v201\n"
      "v_mfma_f32_16x16x32_bf16 v[184:187], a[232:235], v[116:119], v[184:187]\n"
      "ds_read_b128 v[100:103], v10 offset:0\n"
      "v_add_f32 v218, v218, v180\n"
      "v_mfma_f32_16x16x32_bf16 v[184:187], a[236:239], v[120:123], v[184:187]\n"
      "s_waitcnt vmcnt(9)\n"
      "ds_read_b128 v[120:123], v10 offset:5120\n"
      "v_lshlrev_b32 v178, 16, v195\n"
      "v_mfma_f32_16x16x32_bf16 v[184:187], a[240:243], v[124:127], v[184:187]\n"
      "ds_read_b128 v[112:115], v10 offset:3072\n"
      "v_fmac_f32 v219, v181, v181\n"
      "v_mfma_f32_16x16x32_bf16 v[184:187], a[244:247], v[128:131], v[184:187]\n"
      "ds_read_b128 v[116:119], v10 offset:4096\n"
      "v_lshlrev_b32 v174, 16, v193\n"
      "v_mfma_f32_16x16x32_bf16 v[184:187], a[248:251], v[132:135], v[184:187]\n"
      "ds_read_b128 v[124:127], v10 offset:6144\n"
      "v_fmac_f32 v219, v182, v182\n"
      "v_mfma_f32_16x16x32_bf16 v[184:187], a[252:255], v[136:139], v[184:187]\n"
      "s_sub_u32 s47, s46, s45\n"
      "ds_read_b128 v[108:111], v10 offset:2048\n"
      "v_mfma_f32_16x16x32_bf16 v[184:187], v[32:35], v[140:143], v[184:187]\n"
      "v_and_b32 v191, 0xffff0000, v201\n"
      "v_and_b32 v177, 0xffff0000, v194\n"
      "v_mfma_f32_16x16x32_bf16 v[184:187], v[36:39], v[144:147], v[184:187]\n"
      "ds_read_b128 v[136:139], v10 offset:8192\n"
      "v_lshlrev_b32 v176, 16, v194\n"
      "v_mfma_f32_16x16x32_bf16 v[184:187], v[40:43], v[148:151], v[184:187]\n"
      "ds_read_b128 v[128:131], v10 offset:7168\n"
      "v_lshlrev_b32 v172, 16, v192\n"
      "v_mfma_f32_16x16x32_bf16 v[184:187], v[44:47], v[152:155], v[184:187]\n"
      "v_fmac_f32 v219, v183, v183\n"
      "v_and_b32 v175, 0xffff0000, v193\n"
      "v_mfma_f32_16x16x32_bf16 v[184:187], v[48:51], v[156:159], v[184:187]\n"
      "buffer_load_dwordx2 v[200:201], v14, s[8:11], s47 offen sc1\n"
      "v_mfma_f32_16x16x32_bf16 v[184:187], v[52:55], v[160:163], v[184:187]\n"
      "ds_read2_b32 v[132:133], v6 offset0:0 offset1:32\n"
      "v_and_b32 v173, 0xffff0000, v192\n"
      "v_mfma_f32_16x16x32_bf16 v[184:187], v[56:59], v[164:167], v[184:187]\n"
      "s_waitcnt lgkmcnt(3)\n"
      "ds_read2_b32 v[134:135], v6 offset0:64 offset1:96\n"
      "v_add_f32 v218, v218, v181\n"
      "v_mfma_f32_16x16x32_bf16 v[184:187], v[60:63], v[168:171], v[184:187]\n"
      "v_mfma_f32_16x16x32_bf16 v[188:191], v[64:67], v[100:103], v[188:191]\n"
      "ds_read_b128 v[140:143], v10 offset:9216\n"
      "v_and_b32 v179, 0xffff0000, v195\n"
      "v_mfma_f32_16x16x32_bf16 v[188:191], v[68:71], v[104:107], v[188:191]\n"
      "buffer_load_dwordx4 v[192:195], v13, s[8:11], s47 offen sc1\n"
      "v_mfma_f32_16x16x32_bf16 v[188:191], v[72:75], v[108:111], v[188:191]\n"
      "v_add_f32 v218, v218, v182\n"
      "v_mfma_f32_16x16x32_bf16 v[188:191], v[76:79], v[112:115], v[188:191]\n"
      "ds_read_b128 v[156:159], v10 offset:13312\n"
      "v_fmac_f32 v219, v184, v184\n"
      "v_mfma_f32_16x16x32_bf16 v[188:191], v[80:83], v[116:119], v[188:191]\n"
      "ds_read_b128 v[164:167], v10 offset:15360\n"
      "v_cvt_pk_bf16_f32 v213, v182, v183\n"
      "v_mfma_f32_16x16x32_bf16 v[188:191], v[84:87], v[120:123], v[188:191]\n"
      "ds_read_b128 v[144:147], v10 offset:10240\n"
      "v_mfma_f32_16x16x32_bf16 v[188:191], v[88:91], v[124:127], v[188:191]\n"
      "s_waitcnt lgkmcnt(4)\n"
      "v_cvt_pk_bf16_f32 v215, v186, v187\n"
      "v_add_f32 v218, v218, v183\n"
      "v_mfma_f32_16x16x32_bf16 v[188:191], v[92:95], v[128:131], v[188:191]\n"
      "buffer_store_dwordx4 v[208:211], v15, s[8:11], s38 offen sc1\n"
      "v_mfma_f32_16x16x32_bf16 v[188:191], v[96:99], v[132:135], v[188:191]\n"
      "v_cvt_pk_bf16_f32 v212, v180, v181\n"
      "v_add_f32 v218, v218, v184\n"
      "v_mfma_f32_16x16x32_bf16 v[172:175], a[0:3], v[100:103], v[172:175]\n"
      "s_add_u32 s50, s50, 24576\n"
      "v_cvt_pk_bf16_f32 v214, v184, v185\n"
      "v_add_f32 v218, v218, v185\n"
      "v_mfma_f32_16x16x32_bf16 v[172:175], a[4:7], v[104:107], v[172:175]\n"
      "s_mov_b32 s46, s36\n"
      "v_add_f32 v218, v218, v186\n"
      "v_mfma_f32_16x16x32_bf16 v[172:175], a[8:11], v[108:111], v[172:175]\n"
      "ds_read_b128 v[148:151], v10 offset:11264\n"
      "v_add_f32 v218, v218, v187\n"
      "v_mfma_f32_16x16x32_bf16 v[172:175], a[12:15], v[112:115], v[172:175]\n"
      "v_fmac_f32 v219, v185, v185\n"
      "v_add_u32 v21, 1, v21\n"
      "v_mfma_f32_16x16x32_bf16 v[172:175], a[16:19], v[116:119], v[172:175]\n"
      "s_add_u32 s36, s36, 24576\n"
      "ds_read_b128 v[152:155], v10 offset:12288\n"
      "v_fmac_f32_e64 v219, v186, v186\n"
      "v_mfma_f32_16x16x32_bf16 v[172:175], a[20:23], v[120:123], v[172:175]\n"
      "buffer_store_dwordx2 v[216:217], v16, s[8:11], s38 offen sc1\n"
      "v_mfma_f32_16x16x32_bf16 v[172:175], a[24:27], v[124:127], v[172:175]\n"
      "ds_read_b128 v[160:163], v10 offset:14336\n"
      "v_mfma_f32_16x16x32_bf16 v[172:175], a[28:31], v[128:131], v[172:175]\n"
      "ds_read2_b32 v[170:171], v7 offset0:64 offset1:96\n"
      "v_mfma_f32_16x16x32_bf16 v[172:175], a[32:35], v[132:135], v[172:175]\n"
      "ds_read2_b32 v[168:169], v7 offset0:0 offset1:32\n"
      "s_min_u32 s36, s36, s6\n"
      "v_mfma_f32_16x16x32_bf16 v[172:175], a[36:39], v[136:139], v[172:175]\n"
      "s_waitcnt lgkmcnt(4)\n"
      "v_mfma_f32_16x16x32_bf16 v[172:175], a[40:43], v[140:143], v[172:175]\n"
      "buffer_store_dwordx4 v[212:215], v15, s[8:11], s38 offen offset:1024 sc1\n"
      "v_mfma_f32_16x16x32_bf16 v[172:175], a[44:47], v[144:147], v[172:175]\n"
      "ds_write_b32 v18, v218\n"
      "v_mfma_f32_16x16x32_bf16 v[172:175], a[48:51], v[148:151], v[172:175]\n"
      "s_waitcnt vmcnt(11) lgkmcnt(4)\n"
      "v_fmac_f32_e64 v219, v187, v187\n"
      "v_cmp_ne_u32 vcc, s21, v28\n"
      "v_mfma_f32_16x16x32_bf16 v[172:175], a[52:55], v[152:155], v[172:175]\n"
      "ds_write_b32 v18, v219 offset:4\n"
      "v_mfma_f32_16x16x32_bf16 v[172:175], a[56:59], v[156:159], v[172:175]\n"
      "s_waitcnt lgkmcnt(3)\n"
      "s_add_u32 m0, s39, 0\n"
      "v_mfma_f32_16x16x32_bf16 v[172:175], a[60:63], v[160:163], v[172:175]\n"
      "ds_write_b32 v23, v21\n"
      "v_mfma_f32_16x16x32_bf16 v[172:175], a[64:67], v[164:167], v[172:175]\n"
      "s_waitcnt lgkmcnt(3)\n"
      "s_cbranch_vccnz .Lsp_w0_10\n"
      ".Lbk_w0_10:\n"
      "v_mfma_f32_16x16x32_bf16 v[172:175], a[68:71], v[168:171], v[172:175]\n"
      "v_mfma_f32_16x16x32_bf16 v[176:179], a[72:75], v[100:103], v[176:179]\n"
      "buffer_load_dword v28, v11, s[8:11], s36 offen sc1\n"
      "v_mfma_f32_16x16x32_bf16 v[176:179], a[76:79], v[104:107], v[176:179]\n"
      "s_waitcnt vmcnt(7) lgkmcnt(2)\n"
      "v_lshlrev_b32 v186, 16, v199\n"
      "v_lshlrev_b32 v184, 16, v198\n"
      "v_mfma_f32_16x16x32_bf16 v[176:179], a[80:83], v[108:111], v[176:179]\n"
      "v_lshlrev_b32 v180, 16, v196\n"
      "v_and_b32 v183, 0xffff0000, v197\n"
      "v_mfma_f32_16x16x32_bf16 v[176:179], a[84:87], v[112:115], v[176:179]\n"
      "buffer_store_dword v25, v12, s[8:11], s50 offen sc1\n"
      "v_mfma_f32_16x16x32_bf16 v[176:179], a[88:91], v[116:119], v[176:179]\n"
      "v_lshlrev_b32 v182, 16, v197\n"
      "v_and_b32 v181, 0xffff0000, v196\n"
      "v_mfma_f32_16x16x32_bf16 v[176:179], a[92:95], v[120:123], v[176:179]\n"
      "v_and_b32 v187, 0xffff0000, v199\n"
      "v_and_b32 v185, 0xffff0000, v198\n"
      "v_mfma_f32_16x16x32_bf16 v[176:179], a[96:99], v[124:127], v[176:179]\n"
      "buffer_load_dwordx4 v[196:199], v13, s[8:11], s47 offen offset:1024 sc1\n"
      "v_mfma_f32_16x16x32_bf16 v[176:179], a[100:103], v[128:131], v[176:179]\n"
      "ds_read_b32 v22, v24\n"
      "v_mul_f32 v219, v172, v172\n"
      "v_mfma_f32_16x16x32_bf16 v[176:179], a[104:107], v[132:135], v[176:179]\n"
      "v_mfma_f32_16x16x32_bf16 v[176:179], a[108:111], v[136:139], v[176:179]\n"
      "v_mfma_f32_16x16x32_bf16 v[176:179], a[112:115], v[140:143], v[176:179]\n"
      "buffer_load_dwordx4 v0, s[8:11], s46 offen offset:0 sc1 lds\n"
      "v_mfma_f32_16x16x32_bf16 v[176:179], a[116:119], v[144:147], v[176:179]\n"
      "v_add_f32 v218, v172, v173\n"
      "v_fmac_f32 v219, v173, v173\n"
      "v_mfma_f32_16x16x32_bf16 v[176:179], a[120:123], v[148:151], v[176:179]\n"
      "s_waitcnt lgkmcnt(0)\n"
      "v_cmp_gt_i32 vcc, v21, v22\n"
      "v_mfma_f32_16x16x32_bf16 v[176:179], a[124:127], v[152:155], v[176:179]\n"
      "v_mfma_f32_16x16x32_bf16 v[176:179], a[128:131], v[156:159], v[176:179]\n"
      "buffer_load_dwordx4 v0, s[8:11], s46 offen offset:1024 sc1 lds\n"
      "v_mfma_f32_16x16x32_bf16 v[176:179], a[132:135], v[160:163], v[176:179]\n"
      "s_cbranch_vccnz .Lsp_wz0_11\n"
      ".Lbk_wz0_11:\n"
      "ds_read2st64_b32 v[204:205], v17 offset1:1\n"
      "v_mfma_f32_16x16x32_bf16 v[176:179], a[136:139], v[164:167], v[176:179]\n"
      "ds_read2st64_b32 v[206:207], v17 offset0:2 offset1:3\n"
      "v_add_u32 v21, 1, v21\n"
      "v_mfma_f32_16x16x32_bf16 v[176:179], a[140:143], v[168:171], v[176:179]\n"
      "s_waitcnt vmcnt(10)\n"
      "ds_write_b32 v23, v21\n"
      "v_mfma_f32_16x16x32_bf16 v[180:183], a[144:147], v[100:103], v[180:183]\n"
      "buffer_load_dwordx4 v0, s[8:11], s46 offen offset:3072 sc1 lds\n"
      "v_mfma_f32_16x16x32_bf16 v[180:183], a[148:151], v[104:107], v[180:183]\n"
      "v_add_f32 v218, v218, v174\n"
      "v_fmac_f32 v219, v174, v174\n"
      "v_mfma_f32_16x16x32_bf16 v[180:183], a[152:155], v[108:111], v[180:183]\n"
      "v_fmac_f32 v219, v175, v175\n"
      "v_fmac_f32 v219, v176, v176\n"
      "v_mfma_f32_16x16x32_bf16 v[180:183], a[156:159], v[112:115], v[180:183]\n"
      "v_add_f32 v218, v218, v175\n"
      "v_add_f32 v218, v218, v176\n"
      "v_mfma_f32_16x16x32_bf16 v[180:183], a[160:163], v[116:119], v[180:183]\n"
      "buffer_load_dwordx4 v0, s[8:11], s46 offen offset:2048 sc1 lds\n"
      "v_mfma_f32_16x16x32_bf16 v[180:183], a[164:167], v[120:123], v[180:183]\n"
      "s_add_u32 m0, s40, 0\n"
      "v_add_f32 v218, v218, v177\n"
      "v_cvt_pk_bf16_f32 v209, v174, v175\n"
      "v_mfma_f32_16x16x32_bf16 v[180:183], a[168:171], v[124:127], v[180:183]\n"
      "s_waitcnt lgkmcnt(0)\n"
      "v_add_f32 v188, v188, v204\n"
      "v_add_f32 v190, v190, v206\n"
      "v_mfma_f32_16x16x32_bf16 v[180:183], a[172:175], v[128:131], v[180:183]\n"
      "v_cvt_pk_bf16_f32 v208, v172, v173\n"
      "v_fmac_f32 v219, v177, v177\n"
      "v_mfma_f32_16x16x32_bf16 v[180:183], a[176:179], v[132:135], v[180:183]\n"
      "buffer_load_dwordx4 v1, s[8:11], s46 offen offset:0 sc1 lds\n"
      "v_mfma_f32_16x16x32_bf16 v[180:183], a[180:183], v[136:139], v[180:183]\n"
      "s_add_u32 s38, s38, 24576\n"
      "v_cvt_pk_bf16_f32 v210, v176, v177\n"
      "v_mfma_f32_16x16x32_bf16 v[180:183], a[184:187], v[140:143], v[180:183]\n"
      "ds_read_b32 v22, v24\n"
      "v_add_f32 v218, v218, v178\n"
      "v_mfma_f32_16x16x32_bf16 v[180:183], a[188:191], v[144:147], v[180:183]\n"
      "v_add_f32 v218, v218, v179\n"
      "v_add_f32 v218, v218, v188\n"
      "v_mfma_f32_16x16x32_bf16 v[180:183], a[192:195], v[148:151], v[180:183]\n"
      "s_add_u32 s50, s50, 24576\n"
      "v_fmac_f32 v219, v178, v178\n"
      "v_fmac_f32 v219, v179, v179\n"
      "v_mfma_f32_16x16x32_bf16 v[180:183], a[196:199], v[152:155], v[180:183]\n"
      "v_add_f32 v191, v191, v207\n"
      "v_cvt_pk_bf16_f32 v211, v178, v179\n"
      "v_mfma_f32_16x16x32_bf16 v[180:183], a[200:203], v[156:159], v[180:183]\n"
      "v_fmac_f32 v219, v188, v188\n"
      "v_add_f32 v189, v189, v205\n"
      "v_mfma_f32_16x16x32_bf16 v[180:183], a[204:207], v[160:163], v[180:183]\n"
      "s_waitcnt lgkmcnt(0)\n"
      "v_fmac_f32 v219, v189, v189\n"
      "v_cmp_gt_i32 vcc, v21, v22\n"
      "v_mfma_f32_16x16x32_bf16 v[180:183], a[208:211], v[164:167], v[180:183]\n"
      ".Lw_p1:\n"
      "s_cbranch_execz .Lw_pr\n"
      "v_fmac_f32 v219, v190, v190\n"
      "v_add_f32 v218, v218, v189\n"
      "v_mfma_f32_16x16x32_bf16 v[180:183], a[212:215], v[168:171], v[180:183]\n"
      "s_waitcnt vmcnt(11)\n"
      "v_lshlrev_b32 v172, 16, v192\n"
      "v_mfma_f32_16x16x32_bf16 v[184:187], a[216:219], v[100:103], v[184:187]\n"
      "v_and_b32 v175, 0xffff0000, v193\n"
      "v_and_b32 v177, 0xffff0000, v194\n"
      "v_mfma_f32_16x16x32_bf16 v[184:187], a[220:223], v[104:107], v[184:187]\n"
      "s_cbranch_vccnz .Lsp_wy1_12\n"
      ".Lbk_wy1_12:\n"
      "v_add_f32 v218, v218, v190\n"
      "v_mfma_f32_16x16x32_bf16 v[184:187], a[224:227], v[108:111], v[184:187]\n"
      "ds_read_b128 v[104:107], v10 offset:21504\n"
      "v_cvt_pk_bf16_f32 v216, v188, v189\n"
      "v_mfma_f32_16x16x32_bf16 v[184:187], a[228:231], v[112:115], v[184:187]\n"
      "v_add_f32 v218, v218, v191\n"
      "v_add_f32 v218, v218, v180\n"
      "v_mfma_f32_16x16x32_bf16 v[184:187], a[232:235], v[116:119], v[184:187]\n"
      "ds_read_b128 v[116:119], v10 offset:24576\n"
      "v_cvt_pk_bf16_f32 v217, v190, v191\n"
      "v_mfma_f32_16x16x32_bf16 v[184:187], a[236:239], v[120:123], v[184:187]\n"
      "ds_read_b128 v[108:111], v10 offset:22528\n"
      "v_lshlrev_b32 v188, 16, v200\n"
      "v_mfma_f32_16x16x32_bf16 v[184:187], a[240:243], v[124:127], v[184:187]\n"
      "ds_read_b128 v[100:103], v10 offset:20480\n"
      "v_fmac_f32 v219, v191, v191\n"
      "v_mfma_f32_16x16x32_bf16 v[184:187], a[244:247], v[128:131], v[184:187]\n"
      "v_lshlrev_b32 v190, 16, v201\n"
      "v_and_b32 v191, 0xffff0000, v201\n"
      "v_mfma_f32_16x16x32_bf16 v[184:187], a[248:251], v[132:135], v[184:187]\n"
      "ds_read2_b32 v[134:135], v8 offset0:64 offset1:96\n"
      "v_lshlrev_b32 v174, 16, v193\n"
      "v_mfma_f32_16x16x32_bf16 v[184:187], a[252:255], v[136:139], v[184:187]\n"
      "s_sub_u32 s47, s46, s45\n"
      "v_and_b32 v189, 0xffff0000, v200\n"
      "v_fmac_f32 v219, v180, v180\n"
      "v_mfma_f32_16x16x32_bf16 v[184:187], v[32:35], v[140:143], v[184:187]\n"
      "buffer_store_dwordx4 v[208:211], v15, s[8:11], s38 offen sc1\n"
      "v_mfma_f32_16x16x32_bf16 v[184:187], v[36:39], v[144:147], v[184:187]\n"
      "ds_read_b128 v[136:139], v10 offset:28672\n"
      "v_fmac_f32 v219, v181, v181\n"
      "v_mfma_f32_16x16x32_bf16 v[184:187], v[40:43], v[148:151], v[184:187]\n"
      "ds_read2_b32 v[132:133], v8 offset0:0 offset1:32\n"
      "v_lshlrev_b32 v178, 16, v195\n"
      "v_mfma_f32_16x16x32_bf16 v[184:187], v[44:47], v[152:155], v[184:187]\n"
      "ds_read_b128 v[112:115], v10 offset:23552\n"
      "v_add_f32 v218, v218, v181\n"
      "v_mfma_f32_16x16x32_bf16 v[184:187], v[48:51], v[156:159], v[184:187]\n"
      "ds_read_b128 v[120:123], v10 offset:25600\n"
      "v_and_b32 v173, 0xffff0000, v192\n"
      "v_mfma_f32_16x16x32_bf16 v[184:187], v[52:55], v[160:163], v[184:187]\n"
      "ds_read_b128 v[124:127], v10 offset:26624\n"
      "v_lshlrev_b32 v176, 16, v194\n"
      "v_mfma_f32_16x16x32_bf16 v[184:187], v[56:59], v[164:167], v[184:187]\n"
      "buffer_load_dwordx2 v[200:201], v14, s[8:11], s47 offen sc1\n"
      "v_mfma_f32_16x16x32_bf16 v[184:187], v[60:63], v[168:171], v[184:187]\n"
      "s_waitcnt lgkmcnt(5)\n"
      "ds_read_b128 v[128:131], v10 offset:27648\n"
      "v_add_f32 v218, v218, v182\n"
      "v_mfma_f32_16x16x32_bf16 v[188:191], v[64:67], v[100:103], v[188:191]\n"
      "ds_read_b128 v[140:143], v10 offset:29696\n"
      "v_cvt_pk_bf16_f32 v213, v182, v183\n"
      "v_mfma_f32_16x16x32_bf16 v[188:191], v[68:71], v[104:107], v[188:191]\n"
      "ds_read_b128 v[152:155], v10 offset:32768\n"
      "v_mfma_f32_16x16x32_bf16 v[188:191], v[72:75], v[108:111], v[188:191]\n"
      "s_waitcnt lgkmcnt(5)\n"
      "buffer_store_dwordx2 v[216:217], v16, s[8:11], s38 offen sc1\n"
      "v_mfma_f32_16x16x32_bf16 v[188:191], v[76:79], v[112:115], v[188:191]\n"
      "ds_read_b128 v[144:147], v10 offset:30720\n"
      "v_cvt_pk_bf16_f32 v212, v180, v181\n"
      "v_mfma_f32_16x16x32_bf16 v[188:191], v[80:83], v[116:119], v[188:191]\n"
      "s_waitcnt lgkmcnt(4)\n"
      "ds_read_b128 v[164:167], v10 offset:35840\n"
      "v_add_f32 v218, v218, v183\n"
      "v_mfma_f32_16x16x32_bf16 v[188:191], v[84:87], v[120:123], v[188:191]\n"
      "v_fmac_f32 v219, v182, v182\n"
      "v_add_f32 v218, v218, v184\n"
      "v_mfma_f32_16x16x32_bf16 v[188:191], v[88:91], v[124:127], v[188:191]\n"
      "s_waitcnt lgkmcnt(4)\n"
      "ds_read_b128 v[148:151], v10 offset:31744\n"
      "v_add_f32 v218, v218, v185\n"
      "v_mfma_f32_16x16x32_bf16 v[188:191], v[92:95], v[128:131], v[188:191]\n"
      "ds_read_b128 v[160:163], v10 offset:34816\n"
      "v_and_b32 v179, 0xffff0000, v195\n"
      "v_mfma_f32_16x16x32_bf16 v[188:191], v[96:99], v[132:135], v[188:191]\n"
      "buffer_load_dwordx4 v[192:195], v13, s[8:11], s47 offen sc1\n"
      "v_mfma_f32_16x16x32_bf16 v[172:175], a[0:3], v[100:103], v[172:175]\n"
      "v_cvt_pk_bf16_f32 v215, v186, v187\n"
      "v_fmac_f32 v219, v183, v183\n"
      "v_mfma_f32_16x16x32_bf16 v[172:175], a[4:7], v[104:107], v[172:175]\n"
      "v_cvt_pk_bf16_f32 v214, v184, v185\n"
      "v_mfma_f32_16x16x32_bf16 v[172:175], a[8:11], v[108:111], v[172:175]\n"
      "ds_read2_b32 v[168:169], v9 offset0:0 offset1:32\n"
      "v_mfma_f32_16x16x32_bf16 v[172:175], a[12:15], v[112:115], v[172:175]\n"
      "ds_read_b128 v[156:159], v10 offset:33792\n"
      "v_mfma_f32_16x16x32_bf16 v[172:175], a[16:19], v[116:119], v[172:175]\n"
      "buffer_store_dwordx4 v[212:215], v15, s[8:11], s38 offen offset:1024 sc1\n"
      "v_mfma_f32_16x16x32_bf16 v[172:175], a[20:23], v[120:123], v[172:175]\n"
      "v_fmac_f32 v219, v184, v184\n"
      "v_fmac_f32 v219, v185, v185\n"
      "v_mfma_f32_16x16x32_bf16 v[172:175], a[24:27], v[124:127], v[172:175]\n"
      "ds_read2_b32 v[170:171], v9 offset0:64 offset1:96\n"
      "v_add_f32 v218, v218, v186\n"
      "v_mfma_f32_16x16x32_bf16 v[172:175], a[28:31], v[128:131], v[172:175]\n"
      "v_fmac_f32 v219, v186, v186\n"
      "v_add_f32 v218, v218, v187\n"
      "v_mfma_f32_16x16x32_bf16 v[172:175], a[32:35], v[132:135], v[172:175]\n"
      "ds_write_b32 v18, v218\n"
      "v_mfma_f32_16x16x32_bf16 v[172:175], a[36:39], v[136:139], v[172:175]\n"
      "s_waitcnt lgkmcnt(3)\n"
      "s_add_u32 m0, s39, 20480\n"
      "v_add_u32 v21, 1, v21\n"
      "v_mfma_f32_16x16x32_bf16 v[172:175], a[40:43], v[140:143], v[172:175]\n"
      "s_mov_b32 s46, s36\n"
      "v_mfma_f32_16x16x32_bf16 v[172:175], a[44:47], v[144:147], v[172:175]\n"
      "v_fmac_f32 v219, v187, v187\n"
      "v_mfma_f32_16x16x32_bf16 v[172:175], a[48:51], v[148:151], v[172:175]\n"
      "ds_write_b32 v18, v219 offset:4\n"
      "v_mfma_f32_16x16x32_bf16 v[172:175], a[52:55], v[152:155], v[172:175]\n"
      "s_waitcnt vmcnt(10) lgkmcnt(2)\n"
      "v_and_b32 v181, 0xffff0000, v196\n"
      "v_mfma_f32_16x16x32_bf16 v[172:175], a[56:59], v[156:159], v[172:175]\n"
      "ds_write_b32 v23, v21\n"
      "v_mfma_f32_16x16x32_bf16 v[172:175], a[60:63], v[160:163], v[172:175]\n"
      "v_lshlrev_b32 v186, 16, v199\n"
      "v_cmp_ne_u32 vcc, s21, v28\n"
      "v_mfma_f32_16x16x32_bf16 v[172:175], a[64:67], v[164:167], v[172:175]\n"
      "buffer_store_dword v25, v12, s[8:11], s50 offen sc1\n"
      "v_mfma_f32_16x16x32_bf16 v[172:175], a[68:71], v[168:171], v[172:175]\n"
      "s_cmp_eq_u32 s36, s37\n"
      "s_cselect_b32 s48, 49152, s51\n"
      "s_add_u32 s36, s36, s48\n"
      "v_mfma_f32_16x16x32_bf16 v[176:179], a[72:75], v[100:103], v[176:179]\n"
      "s_cbranch_vccnz .Lsp_w1_13\n"
      ".Lbk_w1_13:\n"
      "s_min_u32 s36, s36, s6\n"
      "v_mfma_f32_16x16x32_bf16 v[176:179], a[76:79], v[104:107], v[176:179]\n"
      "buffer_load_dword v28, v11, s[8:11], s36 offen sc1\n"
      "v_mfma_f32_16x16x32_bf16 v[176:179], a[80:83], v[108:111], v[176:179]\n"
      "v_and_b32 v183, 0xffff0000, v197\n"
      "v_mul_f32 v219, v172, v172\n"
      "v_mfma_f32_16x16x32_bf16 v[176:179], a[84:87], v[112:115], v[176:179]\n"
      "buffer_load_dwordx4 v0, s[8:11], s46 offen offset:0 sc1 lds\n"
      "v_mfma_f32_16x16x32_bf16 v[176:179], a[88:91], v[116:119], v[176:179]\n"
      "v_mfma_f32_16x16x32_bf16 v[176:179], a[92:95], v[120:123], v[176:179]\n"
      "v_fmac_f32 v219, v173, v173\n"
      "v_mfma_f32_16x16x32_bf16 v[176:179], a[96:99], v[124:127], v[176:179]\n"
      "v_lshlrev_b32 v184, 16, v198\n"
      "v_and_b32 v185, 0xffff0000, v198\n"
      "v_mfma_f32_16x16x32_bf16 v[176:179], a[100:103], v[128:131], v[176:179]\n"
      "ds_read_b32 v22, v24\n"
      "v_fmac_f32 v219, v174, v174\n"
      "v_mfma_f32_16x16x32_bf16 v[176:179], a[104:107], v[132:135], v[176:179]\n"
      "buffer_load_dwordx4 v0, s[8:11], s46 offen offset:1024 sc1 lds\n"
      "v_mfma_f32_16x16x32_bf16 v[176:179], a[108:111], v[136:139], v[176:179]\n"
      "v_lshlrev_b32 v182, 16, v197\n"
      "v_add_f32 v218, v172, v173\n"
      "v_mfma_f32_16x16x32_bf16 v[176:179], a[112:115], v[140:143], v[176:179]\n"
      "v_add_f32 v218, v218, v174\n"
      "v_fmac_f32 v219, v175, v175\n"
      "v_mfma_f32_16x16x32_bf16 v[176:179], a[116:119], v[144:147], v[176:179]\n"
      "v_cvt_pk_bf16_f32 v208, v172, v173\n"
      "v_mfma_f32_16x16x32_bf16 v[176:179], a[120:123], v[148:151], v[176:179]\n"
      "s_waitcnt lgkmcnt(0)\n"
      "v_and_b32 v187, 0xffff0000, v199\n"
      "v_cmp_gt_i32 vcc, v21, v22\n"
      "v_mfma_f32_16x16x32_bf16 v[176:179], a[124:127], v[152:155], v[176:179]\n"
      "buffer_load_dwordx4 v0, s[8:11], s46 offen offset:3072 sc1 lds\n"
      "v_mfma_f32_16x16x32_bf16 v[176:179], a[128:131], v[156:159], v[176:179]\n"
      "v_lshlrev_b32 v180, 16, v196\n"
      "v_add_f32 v218, v218, v175\n"
      "v_mfma_f32_16x16x32_bf16 v[176:179], a[132:135], v[160:163], v[176:179]\n"
      "s_cbranch_vccnz .Lsp_wz1_14\n"
      ".Lbk_wz1_14:\n"
      "ds_read2st64_b32 v[204:205], v17 offset1:1\n"
      "v_mfma_f32_16x16x32_bf16 v[176:179], a[136:139], v[164:167], v[176:179]\n"
      "ds_read2st64_b32 v[206:207], v17 offset0:2 offset1:3\n"
      "v_add_u32 v21, 1, v21\n"
      "v_mfma_f32_16x16x32_bf16 v[176:179], a[140:143], v[168:171], v[176:179]\n"
      "s_waitcnt vmcnt(10)\n"
      "ds_write_b32 v23, v21\n"
      "v_mfma_f32_16x16x32_bf16 v[180:183], a[144:147], v[100:103], v[180:183]\n"
      "buffer_load_dwordx4 v0, s[8:11], s46 offen offset:2048 sc1 lds\n"
      "v_mfma_f32_16x16x32_bf16 v[180:183], a[148:151], v[104:107], v[180:183]\n"
      "v_mfma_f32_16x16x32_bf16 v[180:183], a[152:155], v[108:111], v[180:183]\n"
      "s_add_u32 m0, s40, 20480\n"
      "v_add_f32 v218, v218, v176\n"
      "v_mfma_f32_16x16x32_bf16 v[180:183], a[156:159], v[112:115], v[180:183]\n"
      "v_fmac_f32 v219, v176, v176\n"
      "v_cvt_pk_bf16_f32 v209, v174, v175\n"
      "v_mfma_f32_16x16x32_bf16 v[180:183], a[160:163], v[116:119], v[180:183]\n"
      "buffer_load_dwordx4 v[196:199], v13, s[8:11], s47 offen offset:1024 sc1\n"
      "v_mfma_f32_16x16x32_bf16 v[180:183], a[164:167], v[120:123], v[180:183]\n"
      "s_waitcnt lgkmcnt(1)\n"
      "v_cvt_pk_bf16_f32 v211, v178, v179\n"
      "v_add_f32 v190, v190, v206\n"
      "v_mfma_f32_16x16x32_bf16 v[180:183], a[168:171], v[124:127], v[180:183]\n"
      "v_add_f32 v218, v218, v177\n"
      "v_add_f32 v218, v218, v178\n"
      "v_mfma_f32_16x16x32_bf16 v[180:183], a[172:175], v[128:131], v[180:183]\n"
      "v_add_f32 v218, v218, v179\n"
      "v_add_f32 v189, v189, v205\n"
      "v_mfma_f32_16x16x32_bf16 v[180:183], a[176:179], v[132:135], v[180:183]\n"
      "buffer_load_dwordx4 v1, s[8:11], s46 offen offset:0 sc1 lds\n"
      "v_mfma_f32_16x16x32_bf16 v[180:183], a[180:183], v[136:139], v[180:183]\n"
      "v_add_f32 v188, v188, v204\n"
      "v_fmac_f32 v219, v177, v177\n"
      "v_mfma_f32_16x16x32_bf16 v[180:183], a[184:187], v[140:143], v[180:183]\n"
      "ds_read_b32 v22, v24\n"
      "v_fmac_f32 v219, v178, v178\n"
      "v_mfma_f32_16x16x32_bf16 v[180:183], a[188:191], v[144:147], v[180:183]\n"
      "v_cvt_pk_bf16_f32 v210, v176, v177\n"
      "v_fmac_f32 v219, v179, v179\n"
      "v_mfma_f32_16x16x32_bf16 v[180:183], a[192:195], v[148:151], v[180:183]\n"
      "v_add_f32 v191, v191, v207\n"
      "v_fmac_f32 v219, v188, v188\n"
      "v_mfma_f32_16x16x32_bf16 v[180:183], a[196:199], v[152:155], v[180:183]\n"
      "v_add_f32 v218, v218, v188\n"
      "v_mfma_f32_16x16x32_bf16 v[180:183], a[200:203], v[156:159], v[180:183]\n"
      "v_add_f32 v218, v218, v189\n"
      "v_add_f32 v218, v218, v190\n"
      "v_mfma_f32_16x16x32_bf16 v[180:183], a[204:207], v[160:163], v[180:183]\n"
      "s_waitcnt lgkmcnt(0)\n"
      "v_fmac_f32 v219, v189, v189\n"
      "v_cmp_gt_i32 vcc, v21, v22\n"
      "v_mfma_f32_16x16x32_bf16 v[180:183], a[208:211], v[164:167], v[180:183]\n"
      "v_cvt_pk_bf16_f32 v216, v188, v189\n"
      "v_fmac_f32 v219, v190, v190\n"
      "v_mfma_f32_16x16x32_bf16 v[180:183], a[212:215], v[168:171], v[180:183]\n"
      "s_waitcnt vmcnt(11)\n"
      "v_and_b32 v189, 0xffff0000, v200\n"
      "v_fmac_f32 v219, v191, v191\n"
      "v_mfma_f32_16x16x32_bf16 v[184:187], a[216:219], v[100:103], v[184:187]\n"
      "s_add_u32 s38, s38, 24576\n"
      "v_cvt_pk_bf16_f32 v217, v190, v191\n"
      "v_lshlrev_b32_e64 v188, 16, v200\n"
      "v_mfma_f32_16x16x32_bf16 v[184:187], a[220:223], v[104:107], v[184:187]\n"
      "s_cbranch_vccnz .Lsp_wy0_15\n"
      ".Lbk_wy0_15:\n"
      "v_mfma_f32_16x16x32_bf16 v[184:187], a[224:227], v[108:111], v[184:187]\n"
      "ds_read_b128 v[104:107], v0 offset:1024\n"
      "v_add_f32 v218, v218, v191\n"
      "v_mfma_f32_16x16x32_bf16 v[184:187], a[228:231], v[112:115], v[184:187]\n"
      "v_fmac_f32 v219, v180, v180\n"
      "v_lshlrev_b32 v190, 16, v201\n"
      "v_mfma_f32_16x16x32_bf16 v[184:187], a[232:235], v[116:119], v[184:187]\n"
      "ds_read_b128 v[100:103], v0 offset:0\n"
      "v_add_f32 v218, v218, v180\n"
      "v_mfma_f32_16x16x32_bf16 v[184:187], a[236:239], v[120:123], v[184:187]\n"
      "s_waitcnt vmcnt(9)\n"
      "ds_read_b128 v[120:123], v0 offset:5120\n"
      "v_lshlrev_b32 v178, 16, v195\n"
      "v_mfma_f32_16x16x32_bf16 v[184:187], a[240:243], v[124:127], v[184:187]\n"
      "ds_read_b128 v[112:115], v0 offset:3072\n"
      "v_fmac_f32 v219, v181, v181\n"
      "v_mfma_f32_16x16x32_bf16 v[184:187], a[244:247], v[128:131], v[184:187]\n"
      "ds_read_b128 v[116:119], v0 offset:4096\n"
      "v_lshlrev_b32 v174, 16, v193\n"
      "v_mfma_f32_16x16x32_bf16 v[184:187], a[248:251], v[132:135], v[184:187]\n"
      "ds_read_b128 v[124:127], v0 offset:6144\n"
      "v_fmac_f32 v219, v182, v182\n"
      "v_mfma_f32_16x16x32_bf16 v[184:187], a[252:255], v[136:139], v[184:187]\n"
      "s_sub_u32 s47, s46, s45\n"
      "ds_read_b128 v[108:111], v0 offset:2048\n"
      "v_mfma_f32_16x16x32_bf16 v[184:187], v[32:35], v[140:143], v[184:187]\n"
      "v_and_b32 v191, 0xffff0000, v201\n"
      "v_and_b32 v177, 0xffff0000, v194\n"
      "v_mfma_f32_16x16x32_bf16 v[184:187], v[36:39], v[144:147], v[184:187]\n"
      "ds_read_b128 v[136:139], v0 offset:8192\n"
      "v_lshlrev_b32 v176, 16, v194\n"
      "v_mfma_f32_16x16x32_bf16 v[184:187], v[40:43], v[148:151], v[184:187]\n"
      "ds_read_b128 v[128:131], v0 offset:7168\n"
      "v_lshlrev_b32 v172, 16, v192\n"
      "v_mfma_f32_16x16x32_bf16 v[184:187], v[44:47], v[152:155], v[184:187]\n"
      "v_fmac_f32 v219, v183, v183\n"
      "v_and_b32 v175, 0xffff0000, v193\n"
      "v_mfma_f32_16x16x32_bf16 v[184:187], v[48:51], v[156:159], v[184:187]\n"
      "buffer_load_dwordx2 v[200:201], v14, s[8:11], s47 offen sc1\n"
      "v_mfma_f32_16x16x32_bf16 v[184:187], v[52:55], v[160:163], v[184:187]\n"
      "ds_read2_b32 v[132:133], v2 offset0:0 offset1:32\n"
      "v_and_b32 v173, 0xffff0000, v192\n"
      "v_mfma_f32_16x16x32_bf16 v[184:187], v[56:59], v[164:167], v[184:187]\n"
      "s_waitcnt lgkmcnt(3)\n"
      "ds_read2_b32 v[134:135], v2 offset0:64 offset1:96\n"
      "v_add_f32 v218, v218, v181\n"
      "v_mfma_f32_16x16x32_bf16 v[184:187], v[60:63], v[168:171], v[184:187]\n"
      "s_sub_u32 s20, s20, 1\n"
      "s_cbranch_scc0 .Lw_loop\n"
      ".Lw_tail:\n"
      "v_add_f32 v218, v218, v182\n"
      "s_nop 7\n"
      "v_fmac_f32 v219, v184, v184\n"
      "v_cvt_pk_bf16_f32 v213, v182, v183\n"
      "v_cvt_pk_bf16_f32 v215, v186, v187\n"
      "v_add_f32 v218, v218, v183\n"
      "buffer_store_dwordx4 v[208:211], v15, s[8:11], s38 offen sc1\n"
      "v_cvt_pk_bf16_f32 v212, v180, v181\n"
      "v_add_f32 v218, v218, v184\n"
      "s_add_u32 s50, s50, 24576\n"
      "v_cvt_pk_bf16_f32 v214, v184, v185\n"
      "v_add_f32 v218, v218, v185\n"
      "v_add_f32 v218, v218, v186\n"
      "v_add_f32 v218, v218, v187\n"
      "v_fmac_f32 v219, v185, v185\n"
      "v_fmac_f32 v219, v186, v186\n"
      "buffer_store_dwordx2 v[216:217], v16, s[8:11], s38 offen sc1\n"
      "buffer_store_dwordx4 v[212:215], v15, s[8:11], s38 offen offset:1024 sc1\n"
      "ds_write_b32 v18, v218\n"
      "v_fmac_f32 v219, v187, v187\n"
      "ds_write_b32 v18, v219 offset:4\n"
      "s_waitcnt lgkmcnt(0)\n"
      "s_barrier\n"
      "s_waitcnt vmcnt(12)\n"
      "buffer_store_dword v25, v12, s[8:11], s50 offen sc1\n"
      "s_add_u32 s38, s38, 24576\n"
      "s_add_u32 s50, s50, 24576\n"
      "s_waitcnt vmcnt(1)\n"
      "buffer_store_dword v25, v12, s[8:11], s50 offen sc1\n"
      "s_add_u32 s50, s50, 24576\n"
      "s_endpgm\n"
      ".Lv_start:\n"
      "s_mov_b64 exec, 0\n"
      "s_lshr_b32 s22, s39, 13\n"
      ".Lv_pl:\n"
      "s_cmp_eq_u32 s22, 0\n"
      "s_cbranch_scc1 .Lv_p0\n"
      "s_cmp_eq_u32 s22, 1\n"
      "s_cbranch_scc1 .Lv_p1\n"
      "s_branch .Lv_pe\n"
      ".Lv_pr:\n"
      "s_add_u32 s22, s22, 2\n"
      "s_branch .Lv_pl\n"
      ".Lv_pe:\n"
      "s_mov_b64 exec, -1\n"
      "s_add_u32 s50, s38, 4294893568\n"
      "s_add_u32 s49, s38, 4294918144\n"
      "s_add_u32 s38, s38, 4294942720\n"
      "s_cmp_eq_u32 s36, s37\n"
      "s_cselect_b32 s48, 49152, s51\n"
      "s_add_u32 s36, s36, s48\n"
      "s_min_u32 s36, s36, s6\n"
      "buffer_load_dword v28, v11, s[8:11], s36 offen sc1\n"
      "s_mov_b32 s46, s36\n"
      "s_add_u32 s36, s36, 24576\n"
      "s_min_u32 s36, s36, s6\n"
      "s_add_u32 m0, s39, 0\n"
      "s_waitcnt vmcnt(0)\n"
      "v_cmp_ne_u32 vcc, s21, v28\n"
      "s_cbranch_vccnz .Lsp_v0_16\n"
      ".Lbk_v0_16:\n"
      "buffer_load_dword v28, v11, s[8:11], s36 offen sc1\n"
      "buffer_load_dwordx4 v0, s[8:11], s46 offen offset:3072 sc1 lds\n"
      "buffer_load_dwordx4 v0, s[8:11], s46 offen offset:0 sc1 lds\n"
      "buffer_load_dwordx4 v0, s[8:11], s46 offen offset:1024 sc1 lds\n"
      "buffer_load_dwordx4 v0, s[8:11], s46 offen offset:2048 sc1 lds\n"
      "s_add_u32 m0, s40, 0\n"
      "s_nop 0\n"
      "buffer_load_dwordx4 v1, s[8:11], s46 offen offset:0 sc1 lds\n"
      "s_sub_u32 s47, s46, s45\n"
      "buffer_load_dwordx4 v[192:195], v13, s[8:11], s47 offen sc1\n"
      "s_mov_b32 s46, s36\n"
      "s_cmp_eq_u32 s36, s37\n"
      "s_cselect_b32 s48, 49152, s51\n"
      "buffer_load_dword v26, v23, s[8:11], s31 offen\n"
      "s_add_u32 s36, s36, s48\n"
      "s_waitcnt vmcnt(7)\n"
      "v_cmp_ne_u32 vcc, s21, v28\n"
      "s_min_u32 s36, s36, s6\n"
      "s_cbranch_vccnz .Lsp_v1_17\n"
      ".Lbk_v1_17:\n"
      "buffer_load_dword v28, v11, s[8:11], s36 offen sc1\n"
      "s_add_u32 m0, s39, 20480\n"
      "s_nop 0\n"
      "buffer_load_dwordx4 v0, s[8:11], s46 offen offset:0 sc1 lds\n"
      "buffer_load_dwordx4 v0, s[8:11], s46 offen offset:1024 sc1 lds\n"
      "buffer_load_dwordx4 v0, s[8:11], s46 offen offset:2048 sc1 lds\n"
      "buffer_load_dwordx4 v[196:199], v13, s[8:11], s47 offen offset:1024 sc1\n"
      "v_add_u32 v21, 1, v21\n"
      "s_waitcnt vmcnt(7)\n"
      "ds_write_b32 v23, v21\n"
      "buffer_load_dwordx4 v0, s[8:11], s46 offen offset:3072 sc1 lds\n"
      "s_add_u32 m0, s40, 20480\n"
      "s_nop 0\n"
      "buffer_load_dwordx4 v1, s[8:11], s46 offen offset:0 sc1 lds\n"
      "ds_read_b32 v22, v24\n"
      "buffer_load_dword v26, v23, s[8:11], s31 offen\n"
      "s_waitcnt lgkmcnt(0)\n"
      "v_cmp_gt_i32 vcc, v21, v22\n"
      "buffer_load_dword v26, v23, s[8:11], s31 offen\n"
      "s_cbranch_vccnz .Lsp_vy0_18\n"
      ".Lbk_vy0_18:\n"
      "s_sub_u32 s47, s46, s45\n"
      "s_waitcnt vmcnt(10)\n"
      "v_lshlrev_b32 v174, 16, v193\n"
      "v_and_b32 v179, 0xffff0000, v195\n"
      "ds_read_b128 v[100:103], v0 offset:8192\n"
      "ds_read_b128 v[108:111], v0 offset:10240\n"
      "v_and_b32 v173, 0xffff0000, v192\n"
      "ds_read_b128 v[120:123], v0 offset:13312\n"
      "ds_read_b128 v[128:131], v0 offset:15360\n"
      "v_lshlrev_b32 v178, 16, v195\n"
      "ds_read_b128 v[104:107], v0 offset:9216\n"
      "v_lshlrev_b32 v172, 16, v192\n"
      "buffer_load_dword v26, v23, s[8:11], s31 offen\n"
      "s_mov_b32 s46, s36\n"
      "ds_read_b128 v[112:115], v0 offset:11264\n"
      "ds_read2_b32 v[134:135], v2 offset0:64 offset1:96\n"
      "ds_read2_b32 v[132:133], v2 offset0:0 offset1:32\n"
      "s_add_u32 s36, s36, 24576\n"
      "ds_read_b128 v[116:119], v0 offset:12288\n"
      "ds_read_b128 v[124:127], v0 offset:14336\n"
      "ds_read_b128 v[160:163], v0 offset:6144\n"
      "v_and_b32 v177, 0xffff0000, v194\n"
      "s_mov_b32 s20, 15\n"
      "s_waitcnt lgkmcnt(0)\n"
      "s_nop 4\n"
      "s_branch .Lv_loop\n"
      ".p2align 12\n"
      ".Lv_p0:\n"
      "s_branch .Lv_pr\n"
      ".p2align 5\n"
      ".Lv_loop:\n"
      "v_mfma_f32_16x16x32_bf16 v[188:191], v[64:67], v[100:103], 0\n"
      "ds_read_b128 v[140:143], v0 offset:1024\n"
      "v_and_b32 v175, 0xffff0000, v193\n"
      "v_mfma_f32_16x16x32_bf16 v[188:191], v[68:71], v[104:107], v[188:191]\n"
      "ds_read2_b32 v[168:169], v3 offset0:0 offset1:32\n"
      "v_lshlrev_b32 v176, 16, v194\n"
      "v_mfma_f32_16x16x32_bf16 v[188:191], v[72:75], v[108:111], v[188:191]\n"
      "s_waitcnt lgkmcnt(5)\n"
      "ds_read_b128 v[164:167], v0 offset:7168\n"
      "v_add_f32 v218, v218, v184\n"
      "v_mfma_f32_16x16x32_bf16 v[188:191], v[76:79], v[112:115], v[188:191]\n"
      "s_waitcnt lgkmcnt(5)\n"
      "ds_read_b128 v[144:147], v0 offset:2048\n"
      "v_add_f32 v218, v218, v185\n"
      "v_mfma_f32_16x16x32_bf16 v[188:191], v[80:83], v[116:119], v[188:191]\n"
      "buffer_load_dwordx4 v[192:195], v13, s[8:11], s47 offen sc1\n"
      "v_mfma_f32_16x16x32_bf16 v[188:191], v[84:87], v[120:123], v[188:191]\n"
      "s_waitcnt lgkmcnt(4)\n"
      "ds_read2_b32 v[170:171], v3 offset0:64 offset1:96\n"
      "v_fmac_f32 v219, v183, v183\n"
      "v_mfma_f32_16x16x32_bf16 v[188:191], v[88:91], v[124:127], v[188:191]\n"
      "s_min_u32 s36, s36, s6\n"
      "ds_read_b128 v[136:139], v0 offset:0\n"
      "v_fmac_f32 v219, v184, v184\n"
      "v_mfma_f32_16x16x32_bf16 v[188:191], v[92:95], v[128:131], v[188:191]\n"
      "ds_read_b128 v[148:151], v0 offset:3072\n"
      "v_mfma_f32_16x16x32_bf16 v[188:191], v[96:99], v[132:135], v[188:191]\n"
      "v_cvt_pk_bf16_f32 v215, v186, v187\n"
      "v_add_f32 v218, v218, v186\n"
      "v_mfma_f32_16x16x32_bf16 v[172:175], a[0:3], v[100:103], v[172:175]\n"
      "ds_read_b128 v[152:155], v0 offset:4096\n"
      "v_add_f32 v218, v218, v187\n"
      "v_mfma_f32_16x16x32_bf16 v[172:175], a[4:7], v[104:107], v[172:175]\n"
      "ds_read_b128 v[156:159], v0 offset:5120\n"
      "v_fmac_f32 v219, v185, v185\n"
      "v_mfma_f32_16x16x32_bf16 v[172:175], a[8:11], v[108:111], v[172:175]\n"
      "s_add_u32 m0, s39, 40960\n"
      "v_cvt_pk_bf16_f32 v214, v184, v185\n"
      "v_mfma_f32_16x16x32_bf16 v[172:175], a[12:15], v[112:115], v[172:175]\n"
      "ds_write_b32 v18, v218\n"
      "v_mfma_f32_16x16x32_bf16 v[172:175], a[16:19], v[116:119], v[172:175]\n"
      "s_waitcnt vmcnt(10) lgkmcnt(4)\n"
      "v_fmac_f32_e64 v219, v186, v186\n"
      "v_cmp_ne_u32 vcc, s21, v28\n"
      "v_mfma_f32_16x16x32_bf16 v[172:175], a[20:23], v[120:123], v[172:175]\n"
      "ds_write_b32 v17, v190 offset:512\n"
      "v_mfma_f32_16x16x32_bf16 v[172:175], a[24:27], v[124:127], v[172:175]\n"
      "buffer_store_dwordx4 v[212:215], v15, s[8:11], s38 offen offset:1024 sc1\n"
      "v_mfma_f32_16x16x32_bf16 v[172:175], a[28:31], v[128:131], v[172:175]\n"
      "ds_write_b32 v17, v188 offset:0\n"
      "v_mfma_f32_16x16x32_bf16 v[172:175], a[32:35], v[132:135], v[172:175]\n"
      "v_fmac_f32_e64 v219, v187, v187\n"
      "v_mfma_f32_16x16x32_bf16 v[172:175], a[36:39], v[136:139], v[172:175]\n"
      "ds_write_b32 v17, v191 offset:768\n"
      "v_mfma_f32_16x16x32_bf16 v[172:175], a[40:43], v[140:143], v[172:175]\n"
      "s_cbranch_vccnz .Lsp_v0_19\n"
      ".Lbk_v0_19:\n"
      "v_add_u32 v21, 1, v21\n"
      "v_mfma_f32_16x16x32_bf16 v[172:175], a[44:47], v[144:147], v[172:175]\n"
      "s_waitcnt lgkmcnt(3)\n"
      "ds_write_b32 v18, v219 offset:4\n"
      "v_mfma_f32_16x16x32_bf16 v[172:175], a[48:51], v[148:151], v[172:175]\n"
      "buffer_load_dword v28, v11, s[8:11], s36 offen sc1\n"
      "v_mfma_f32_16x16x32_bf16 v[172:175], a[52:55], v[152:155], v[172:175]\n"
      "ds_write_b32 v17, v189 offset:256\n"
      "v_mfma_f32_16x16x32_bf16 v[172:175], a[56:59], v[156:159], v[172:175]\n"
      "s_waitcnt vmcnt(8) lgkmcnt(3)\n"
      "v_lshlrev_b32 v186, 16, v199\n"
      "v_and_b32 v185, 0xffff0000, v198\n"
      "v_mfma_f32_16x16x32_bf16 v[172:175], a[60:63], v[160:163], v[172:175]\n"
      "ds_write_b32 v23, v21\n"
      "v_mfma_f32_16x16x32_bf16 v[172:175], a[64:67], v[164:167], v[172:175]\n"
      "v_and_b32 v181, 0xffff0000, v196\n"
      "v_and_b32 v187, 0xffff0000, v199\n"
      "v_mfma_f32_16x16x32_bf16 v[172:175], a[68:71], v[168:171], v[172:175]\n"
      "buffer_load_dwordx4 v0, s[8:11], s46 offen offset:3072 sc1 lds\n"
      "v_mfma_f32_16x16x32_bf16 v[176:179], a[72:75], v[100:103], v[176:179]\n"
      "v_lshlrev_b32 v182, 16, v197\n"
      "s_add_u32 s38, s38, 24576\n"
      "v_mfma_f32_16x16x32_bf16 v[176:179], a[76:79], v[104:107], v[176:179]\n"
      "v_lshlrev_b32 v184, 16, v198\n"
      "v_mfma_f32_16x16x32_bf16 v[176:179], a[80:83], v[108:111], v[176:179]\n"
      "v_cvt_pk_bf16_f32 v208, v172, v173\n"
      "v_mfma_f32_16x16x32_bf16 v[176:179], a[84:87], v[112:115], v[176:179]\n"
      "v_mul_f32 v219, v172, v172\n"
      "v_mfma_f32_16x16x32_bf16 v[176:179], a[88:91], v[116:119], v[176:179]\n"
      "v_lshlrev_b32 v180, 16, v196\n"
      "v_and_b32 v183, 0xffff0000, v197\n"
      "v_mfma_f32_16x16x32_bf16 v[176:179], a[92:95], v[120:123], v[176:179]\n"
      "v_fmac_f32 v219, v173, v173\n"
      "v_mfma_f32_16x16x32_bf16 v[176:179], a[96:99], v[124:127], v[176:179]\n"
      "buffer_load_dwordx4 v[196:199], v13, s[8:11], s47 offen offset:1024 sc1\n"
      "v_mfma_f32_16x16x32_bf16 v[176:179], a[100:103], v[128:131], v[176:179]\n"
      "ds_read_b32 v22, v24\n"
      "v_mfma_f32_16x16x32_bf16 v[176:179], a[104:107], v[132:135], v[176:179]\n"
      "v_fmac_f32 v219, v174, v174\n"
      "v_mfma_f32_16x16x32_bf16 v[176:179], a[108:111], v[136:139], v[176:179]\n"
      "v_fmac_f32 v219, v175, v175\n"
      "v_mfma_f32_16x16x32_bf16 v[176:179], a[112:115], v[140:143], v[176:179]\n"
      "buffer_load_dwordx4 v0, s[8:11], s46 offen offset:0 sc1 lds\n"
      "v_mfma_f32_16x16x32_bf16 v[176:179], a[116:119], v[144:147], v[176:179]\n"
      "v_mfma_f32_16x16x32_bf16 v[176:179], a[120:123], v[148:151], v[176:179]\n"
      "s_waitcnt lgkmcnt(0)\n"
      "v_cmp_gt_i32 vcc, v21, v22\n"
      "v_add_f32_e64 v218, v172, v173\n"
      "v_mfma_f32_16x16x32_bf16 v[176:179], a[124:127], v[152:155], v[176:179]\n"
      "v_add_f32_e64 v218, v218, v174\n"
      "v_add_f32_e64 v218, v218, v175\n"
      "v_mfma_f32_16x16x32_bf16 v[176:179], a[128:131], v[156:159], v[176:179]\n"
      "buffer_load_dwordx4 v0, s[8:11], s46 offen offset:1024 sc1 lds\n"
      "v_mfma_f32_16x16x32_bf16 v[176:179], a[132:135], v[160:163], v[176:179]\n"
      "s_cbranch_vccnz .Lsp_vz0_20\n"
      ".Lbk_vz0_20:\n"
      "ds_read_b128 v[204:207], v19\n"
      "v_mfma_f32_16x16x32_bf16 v[176:179], a[136:139], v[164:167], v[176:179]\n"
      "v_cvt_pk_bf16_f32 v209, v174, v175\n"
      "v_add_u32 v21, 1, v21\n"
      "v_mfma_f32_16x16x32_bf16 v[176:179], a[140:143], v[168:171], v[176:179]\n"
      "s_waitcnt vmcnt(10)\n"
      "ds_write_b32 v23, v21\n"
      "v_mfma_f32_16x16x32_bf16 v[180:183], a[144:147], v[100:103], v[180:183]\n"
      "v_mfma_f32_16x16x32_bf16 v[180:183], a[148:151], v[104:107], v[180:183]\n"
      "buffer_load_dwordx4 v0, s[8:11], s46 offen offset:2048 sc1 lds\n"
      "v_mfma_f32_16x16x32_bf16 v[180:183], a[152:155], v[108:111], v[180:183]\n"
      "s_add_u32 s49, s49, 24576\n"
      "s_add_u32 m0, s40, 40960\n"
      "v_mfma_f32_16x16x32_bf16 v[180:183], a[156:159], v[112:115], v[180:183]\n"
      "v_mfma_f32_16x16x32_bf16 v[180:183], a[160:163], v[116:119], v[180:183]\n"
      "s_waitcnt lgkmcnt(1)\n"
      "v_add_f32 v204, v204, v206\n"
      "v_cvt_pk_bf16_f32 v210, v176, v177\n"
      "v_mfma_f32_16x16x32_bf16 v[180:183], a[164:167], v[120:123], v[180:183]\n"
      "buffer_load_dwordx4 v1, s[8:11], s46 offen offset:0 sc1 lds\n"
      "v_mfma_f32_16x16x32_bf16 v[180:183], a[168:171], v[124:127], v[180:183]\n"
      "v_add_f32 v205, v205, v207\n"
      "v_add_f32_dpp v204, v204, v204 quad_perm:[1,0,3,2] row_mask:0xf bank_mask:0xf\n"
      "v_mfma_f32_16x16x32_bf16 v[180:183], a[172:175], v[128:131], v[180:183]\n"
      "v_fmac_f32 v219, v176, v176\n"
      "v_mfma_f32_16x16x32_bf16 v[180:183], a[176:179], v[132:135], v[180:183]\n"
      "v_add_f32_dpp v204, v204, v204 quad_perm:[2,3,0,1] row_mask:0xf bank_mask:0xf\n"
      "v_cvt_pk_bf16_f32 v211, v178, v179\n"
      "v_mfma_f32_16x16x32_bf16 v[180:183], a[180:183], v[136:139], v[180:183]\n"
      "buffer_store_dwordx4 v[208:211], v15, s[8:11], s38 offen sc1\n"
      "v_mfma_f32_16x16x32_bf16 v[180:183], a[184:187], v[140:143], v[180:183]\n"
      "ds_read_b32 v22, v24\n"
      "v_add_f32_dpp v205, v205, v205 quad_perm:[1,0,3,2] row_mask:0xf bank_mask:0xf\n"
      "v_mfma_f32_16x16x32_bf16 v[180:183], a[188:191], v[144:147], v[180:183]\n"
      "s_add_u32 s50, s50, 24576\n"
      "v_add_f32_dpp v205, v205, v205 quad_perm:[2,3,0,1] row_mask:0xf bank_mask:0xf\n"
      "v_fmac_f32 v219, v177, v177\n"
      "v_mfma_f32_16x16x32_bf16 v[180:183], a[192:195], v[148:151], v[180:183]\n"
      "v_add_f32_dpp v205, v205, v205 row_half_mirror row_mask:0xf bank_mask:0xf\n"
      "v_fmac_f32 v219, v178, v178\n"
      "v_mfma_f32_16x16x32_bf16 v[180:183], a[196:199], v[152:155], v[180:183]\n"
      "v_add_f32_dpp v204, v204, v204 row_half_mirror row_mask:0xf bank_mask:0xf\n"
      "v_mfma_f32_16x16x32_bf16 v[180:183], a[200:203], v[156:159], v[180:183]\n"
      "v_mul_f32 v204, v27, v204\n"
      "v_fmaak_f32 v205, v27, v205, 0x36a7c5ac\n"
      "v_mfma_f32_16x16x32_bf16 v[180:183], a[204:207], v[160:163], v[180:183]\n"
      "s_waitcnt vmcnt(12) lgkmcnt(1)\n"
      "buffer_store_dword v25, v12, s[8:11], s50 offen sc1\n"
      "v_mfma_f32_16x16x32_bf16 v[180:183], a[208:211], v[164:167], v[180:183]\n"
      "s_waitcnt lgkmcnt(0)\n"
      "v_cmp_gt_i32 vcc, v21, v22\n"
      "v_add_f32 v218, v218, v176\n"
      "v_mfma_f32_16x16x32_bf16 v[180:183], a[212:215], v[168:171], v[180:183]\n"
      "v_add_f32 v218, v218, v177\n"
      "v_add_f32_e64 v218, v218, v178\n"
      "v_mfma_f32_16x16x32_bf16 v[184:187], a[216:219], v[100:103], v[184:187]\n"
      "buffer_store_dwordx2 v[204:205], v20, s[8:11], s49 offen sc1\n"
      "v_mfma_f32_16x16x32_bf16 v[184:187], a[220:223], v[104:107], v[184:187]\n"
      "s_sub_u32 s47, s46, s45\n"
      "s_cbranch_vccnz .Lsp_vy1_21\n"
      ".Lbk_vy1_21:\n"
      "v_mfma_f32_16x16x32_bf16 v[184:187], a[224:227], v[108:111], v[184:187]\n"
      "s_waitcnt vmcnt(11)\n"
      "ds_read_b128 v[104:107], v0 offset:29696\n"
      "v_and_b32 v177, 0xffff0000, v194\n"
      "v_mfma_f32_16x16x32_bf16 v[184:187], a[228:231], v[112:115], v[184:187]\n"
      "v_fmac_f32 v219, v179, v179\n"
      "v_mfma_f32_16x16x32_bf16 v[184:187], a[232:235], v[116:119], v[184:187]\n"
      "v_fmac_f32 v219, v180, v180\n"
      "v_add_f32 v218, v218, v179\n"
      "v_mfma_f32_16x16x32_bf16 v[184:187], a[236:239], v[120:123], v[184:187]\n"
      "v_cvt_pk_bf16_f32 v212, v180, v181\n"
      "v_and_b32 v179, 0xffff0000, v195\n"
      "v_mfma_f32_16x16x32_bf16 v[184:187], a[240:243], v[124:127], v[184:187]\n"
      "ds_read_b128 v[120:123], v0 offset:33792\n"
      "v_lshlrev_b32 v174, 16, v193\n"
      "v_mfma_f32_16x16x32_bf16 v[184:187], a[244:247], v[128:131], v[184:187]\n"
      "ds_read_b128 v[112:115], v0 offset:31744\n"
      "v_add_f32 v218, v218, v180\n"
      "v_mfma_f32_16x16x32_bf16 v[184:187], a[248:251], v[132:135], v[184:187]\n"
      "ds_read_b128 v[100:103], v0 offset:28672\n"
      "v_add_f32 v218, v218, v181\n"
      "v_mfma_f32_16x16x32_bf16 v[184:187], a[252:255], v[136:139], v[184:187]\n"
      "ds_read_b128 v[124:127], v0 offset:34816\n"
      "v_mfma_f32_16x16x32_bf16 v[184:187], v[32:35], v[140:143], v[184:187]\n"
      "ds_read_b128 v[108:111], v0 offset:30720\n"
      "v_and_b32 v175, 0xffff0000, v193\n"
      "v_mfma_f32_16x16x32_bf16 v[184:187], v[36:39], v[144:147], v[184:187]\n"
      "ds_read_b128 v[116:119], v0 offset:32768\n"
      "v_fmac_f32 v219, v181, v181\n"
      "v_mfma_f32_16x16x32_bf16 v[184:187], v[40:43], v[148:151], v[184:187]\n"
      "v_add_f32 v218, v218, v182\n"
      "v_mfma_f32_16x16x32_bf16 v[184:187], v[44:47], v[152:155], v[184:187]\n"
      "ds_read2_b32 v[132:133], v4 offset0:0 offset1:32\n"
      "v_and_b32 v173, 0xffff0000, v192\n"
      "v_mfma_f32_16x16x32_bf16 v[184:187], v[48:51], v[156:159], v[184:187]\n"
      "ds_read_b128 v[128:131], v0 offset:35840\n"
      "v_lshlrev_b32 v178, 16, v195\n"
      "v_mfma_f32_16x16x32_bf16 v[184:187], v[52:55], v[160:163], v[184:187]\n"
      "ds_read_b128 v[156:159], v0 offset:25600\n"
      "v_lshlrev_b32 v172, 16, v192\n"
      "v_mfma_f32_16x16x32_bf16 v[184:187], v[56:59], v[164:167], v[184:187]\n"
      "ds_read_b128 v[144:147], v0 offset:22528\n"
      "v_lshlrev_b32 v176, 16, v194\n"
      "v_mfma_f32_16x16x32_bf16 v[184:187], v[60:63], v[168:171], v[184:187]\n"
      "s_waitcnt lgkmcnt(5)\n"
      "buffer_load_dwordx4 v[192:195], v13, s[8:11], s47 offen sc1\n"
      "v_mfma_f32_16x16x32_bf16 v[188:191], v[64:67], v[100:103], 0\n"
      "ds_read2_b32 v[134:135], v4 offset0:64 offset1:96\n"
      "v_fmac_f32 v219, v182, v182\n"
      "v_mfma_f32_16x16x32_bf16 v[188:191], v[68:71], v[104:107], v[188:191]\n"
      "ds_read_b128 v[148:151], v0 offset:23552\n"
      "v_add_f32 v218, v218, v183\n"
      "v_mfma_f32_16x16x32_bf16 v[188:191], v[72:75], v[108:111], v[188:191]\n"
      "s_mov_b32 s46, s36\n"
      "v_cvt_pk_bf16_f32 v213, v182, v183\n"
      "v_cvt_pk_bf16_f32 v215, v186, v187\n"
      "v_mfma_f32_16x16x32_bf16 v[188:191], v[76:79], v[112:115], v[188:191]\n"
      "s_waitcnt lgkmcnt(4)\n"
      "ds_read_b128 v[160:163], v0 offset:26624\n"
      "v_fmac_f32 v219, v183, v183\n"
      "v_mfma_f32_16x16x32_bf16 v[188:191], v[80:83], v[116:119], v[188:191]\n"
      "ds_read_b128 v[140:143], v0 offset:21504\n"
      "v_fmac_f32 v219, v184, v184\n"
      "v_mfma_f32_16x16x32_bf16 v[188:191], v[84:87], v[120:123], v[188:191]\n"
      "ds_read2_b32 v[168:169], v5 offset0:0 offset1:32\n"
      "v_add_f32 v218, v218, v184\n"
      "v_mfma_f32_16x16x32_bf16 v[188:191], v[88:91], v[124:127], v[188:191]\n"
      "ds_read_b128 v[152:155], v0 offset:24576\n"
      "v_fmac_f32 v219, v185, v185\n"
      "v_mfma_f32_16x16x32_bf16 v[188:191], v[92:95], v[128:131], v[188:191]\n"
      "s_waitcnt lgkmcnt(5)\n"
      "ds_read_b128 v[136:139], v0 offset:20480\n"
      "v_add_f32 v218, v218, v185\n"
      "v_mfma_f32_16x16x32_bf16 v[188:191], v[96:99], v[132:135], v[188:191]\n"
      "s_cmp_eq_u32 s36, s37\n"
      "s_cselect_b32 s48, 49152, s51\n"
      "ds_read_b128 v[164:167], v0 offset:27648\n"
      "v_mfma_f32_16x16x32_bf16 v[172:175], a[0:3], v[100:103], v[172:175]\n"
      "ds_read2_b32 v[170:171], v5 offset0:64 offset1:96\n"
      "v_cvt_pk_bf16_f32 v214, v184, v185\n"
      "v_mfma_f32_16x16x32_bf16 v[172:175], a[4:7], v[104:107], v[172:175]\n"
      "v_fmac_f32 v219, v186, v186\n"
      "v_add_f32 v218, v218, v186\n"
      "v_mfma_f32_16x16x32_bf16 v[172:175], a[8:11], v[108:111], v[172:175]\n"
      "ds_write_b32 v17, v189 offset:256\n"
      "v_mfma_f32_16x16x32_bf16 v[172:175], a[12:15], v[112:115], v[172:175]\n"
      "v_add_f32 v218, v218, v187\n"
      "v_add_u32 v21, 1, v21\n"
      "v_mfma_f32_16x16x32_bf16 v[172:175], a[16:19], v[116:119], v[172:175]\n"
      "buffer_store_dwordx4 v[212:215], v15, s[8:11], s38 offen offset:1024 sc1\n"
      "v_mfma_f32_16x16x32_bf16 v[172:175], a[20:23], v[120:123], v[172:175]\n"
      "ds_write_b32 v17, v190 offset:512\n"
      "v_mfma_f32_16x16x32_bf16 v[172:175], a[24:27], v[124:127], v[172:175]\n"
      "v_fmac_f32 v219, v187, v187\n"
      "v_mfma_f32_16x16x32_bf16 v[172:175], a[28:31], v[128:131], v[172:175]\n"
      "s_add_u32 s36, s36, s48\n"
      "ds_write_b32 v18, v218\n"
      "v_mfma_f32_16x16x32_bf16 v[172:175], a[32:35], v[132:135], v[172:175]\n"
      "s_waitcnt vmcnt(11) lgkmcnt(3)\n"
      "v_cmp_ne_u32 vcc, s21, v28\n"
      "v_mfma_f32_16x16x32_bf16 v[172:175], a[36:39], v[136:139], v[172:175]\n"
      "ds_write_b32 v17, v188 offset:0\n"
      "v_mfma_f32_16x16x32_bf16 v[172:175], a[40:43], v[140:143], v[172:175]\n"
      "s_waitcnt vmcnt(9) lgkmcnt(3)\n"
      "s_min_u32 s36, s36, s6\n"
      "v_lshlrev_b32 v182, 16, v197\n"
      "v_mfma_f32_16x16x32_bf16 v[172:175], a[44:47], v[144:147], v[172:175]\n"
      "ds_write_b32 v17, v191 offset:768\n"
      "v_mfma_f32_16x16x32_bf16 v[172:175], a[48:51], v[148:151], v[172:175]\n"
      "s_cbranch_vccnz .Lsp_v1_22\n"
      ".Lbk_v1_22:\n"
      "v_and_b32 v181, 0xffff0000, v196\n"
      "v_mfma_f32_16x16x32_bf16 v[172:175], a[52:55], v[152:155], v[172:175]\n"
      "ds_write_b32 v18, v219 offset:4\n"
      "v_mfma_f32_16x16x32_bf16 v[172:175], a[56:59], v[156:159], v[172:175]\n"
      "buffer_load_dword v28, v11, s[8:11], s36 offen sc1\n"
      "v_mfma_f32_16x16x32_bf16 v[172:175], a[60:63], v[160:163], v[172:175]\n"
      "s_add_u32 m0, s39, 61440\n"
      "ds_write_b32 v23, v21\n"
      "v_mfma_f32_16x16x32_bf16 v[172:175], a[64:67], v[164:167], v[172:175]\n"
      "v_and_b32 v187, 0xffff0000, v199\n"
      "v_and_b32 v183, 0xffff0000, v197\n"
      "v_mfma_f32_16x16x32_bf16 v[172:175], a[68:71], v[168:171], v[172:175]\n"
      "buffer_load_dwordx4 v0, s[8:11], s46 offen offset:0 sc1 lds\n"
      "v_mfma_f32_16x16x32_bf16 v[176:179], a[72:75], v[100:103], v[176:179]\n"
      "s_add_u32 s49, s49, 24576\n"
      "v_mfma_f32_16x16x32_bf16 v[176:179], a[76:79], v[104:107], v[176:179]\n"
      "v_mfma_f32_16x16x32_bf16 v[176:179], a[80:83], v[108:111], v[176:179]\n"
      "v_cvt_pk_bf16_f32 v209, v174, v175\n"
      "v_mfma_f32_16x16x32_bf16 v[176:179], a[84:87], v[112:115], v[176:179]\n"
      "buffer_load_dwordx4 v0, s[8:11], s46 offen offset:1024 sc1 lds\n"
      "v_mfma_f32_16x16x32_bf16 v[176:179], a[88:91], v[116:119], v[176:179]\n"
      "v_add_f32 v218, v172, v173\n"
      "v_mfma_f32_16x16x32_bf16 v[176:179], a[92:95], v[120:123], v[176:179]\n"
      "v_add_f32 v218, v218, v174\n"
      "v_lshlrev_b32 v180, 16, v196\n"
      "v_mfma_f32_16x16x32_bf16 v[176:179], a[96:99], v[124:127], v[176:179]\n"
      "ds_read_b32 v22, v24\n"
      "v_lshlrev_b32 v186, 16, v199\n"
      "v_mfma_f32_16x16x32_bf16 v[176:179], a[100:103], v[128:131], v[176:179]\n"
      "buffer_load_dwordx4 v0, s[8:11], s46 offen offset:2048 sc1 lds\n"
      "v_mfma_f32_16x16x32_bf16 v[176:179], a[104:107], v[132:135], v[176:179]\n"
      "v_mul_f32 v219, v172, v172\n"
      "v_fmac_f32 v219, v173, v173\n"
      "v_mfma_f32_16x16x32_bf16 v[176:179], a[108:111], v[136:139], v[176:179]\n"
      "v_lshlrev_b32 v184, 16, v198\n"
      "v_mfma_f32_16x16x32_bf16 v[176:179], a[112:115], v[140:143], v[176:179]\n"
      "v_fmac_f32_e64 v219, v174, v174\n"
      "v_mfma_f32_16x16x32_bf16 v[176:179], a[116:119], v[144:147], v[176:179]\n"
      "s_waitcnt lgkmcnt(0)\n"
      "v_cmp_gt_i32 vcc, v21, v22\n"
      "v_add_f32_e64 v218, v218, v175\n"
      "v_mfma_f32_16x16x32_bf16 v[176:179], a[120:123], v[148:151], v[176:179]\n"
      "v_mfma_f32_16x16x32_bf16 v[176:179], a[124:127], v[152:155], v[176:179]\n"
      "v_and_b32 v185, 0xffff0000, v198\n"
      "v_cvt_pk_bf16_f32 v208, v172, v173\n"
      "v_mfma_f32_16x16x32_bf16 v[176:179], a[128:131], v[156:159], v[176:179]\n"
      "buffer_load_dwordx4 v[196:199], v13, s[8:11], s47 offen offset:1024 sc1\n"
      "v_mfma_f32_16x16x32_bf16 v[176:179], a[132:135], v[160:163], v[176:179]\n"
      "s_cbranch_vccnz .Lsp_vz1_23\n"
      ".Lbk_vz1_23:\n"
      "ds_read_b128 v[204:207], v19\n"
      "v_mfma_f32_16x16x32_bf16 v[176:179], a[136:139], v[164:167], v[176:179]\n"
      "v_add_u32 v21, 1, v21\n"
      "v_mfma_f32_16x16x32_bf16 v[176:179], a[140:143], v[168:171], v[176:179]\n"
      "s_waitcnt vmcnt(10)\n"
      "ds_write_b32 v23, v21\n"
      "v_mfma_f32_16x16x32_bf16 v[180:183], a[144:147], v[100:103], v[180:183]\n"
      "v_fmac_f32 v219, v175, v175\n"
      "v_mfma_f32_16x16x32_bf16 v[180:183], a[148:151], v[104:107], v[180:183]\n"
      "buffer_load_dwordx4 v0, s[8:11], s46 offen offset:3072 sc1 lds\n"
      "v_mfma_f32_16x16x32_bf16 v[180:183], a[152:155], v[108:111], v[180:183]\n"
      "v_cvt_pk_bf16_f32 v210, v176, v177\n"
      "v_fmac_f32 v219, v176, v176\n"
      "v_mfma_f32_16x16x32_bf16 v[180:183], a[156:159], v[112:115], v[180:183]\n"
      "s_add_u32 m0, s40, 61440\n"
      "v_fmac_f32 v219, v177, v177\n"
      "v_mfma_f32_16x16x32_bf16 v[180:183], a[160:163], v[116:119], v[180:183]\n"
      "s_waitcnt lgkmcnt(1)\n"
      "v_add_f32 v204, v204, v206\n"
      "v_add_f32 v205, v205, v207\n"
      "v_mfma_f32_16x16x32_bf16 v[180:183], a[164:167], v[120:123], v[180:183]\n"
      "v_add_f32_dpp v204, v204, v204 quad_perm:[1,0,3,2] row_mask:0xf bank_mask:0xf\n"
      "v_fmac_f32 v219, v178, v178\n"
      "v_mfma_f32_16x16x32_bf16 v[180:183], a[168:171], v[124:127], v[180:183]\n"
      "buffer_load_dwordx4 v1, s[8:11], s46 offen offset:0 sc1 lds\n"
      "v_mfma_f32_16x16x32_bf16 v[180:183], a[172:175], v[128:131], v[180:183]\n"
      "v_add_f32_dpp v205, v205, v205 quad_perm:[1,0,3,2] row_mask:0xf bank_mask:0xf\n"
      "v_add_f32_dpp v204, v204, v204 quad_perm:[2,3,0,1] row_mask:0xf bank_mask:0xf\n"
      "v_mfma_f32_16x16x32_bf16 v[180:183], a[176:179], v[132:135], v[180:183]\n"
      "v_add_f32_dpp v204, v204, v204 row_half_mirror row_mask:0xf bank_mask:0xf\n"
      "v_cvt_pk_bf16_f32 v211, v178, v179\n"
      "v_mfma_f32_16x16x32_bf16 v[180:183], a[180:183], v[136:139], v[180:183]\n"
      "s_add_u32 s38, s38, 24576\n"
      "v_mul_f32 v204, v27, v204\n"
      "v_add_f32_dpp v205, v205, v205 quad_perm:[2,3,0,1] row_mask:0xf bank_mask:0xf\n"
      "v_mfma_f32_16x16x32_bf16 v[180:183], a[184:187], v[140:143], v[180:183]\n"
      "ds_read_b32 v22, v24\n"
      "v_add_f32 v218, v218, v176\n"
      "v_mfma_f32_16x16x32_bf16 v[180:183], a[188:191], v[144:147], v[180:183]\n"
      "v_add_f32_dpp v205, v205, v205 row_half_mirror row_mask:0xf bank_mask:0xf\n"
      "v_fmaak_f32 v205, v27, v205, 0x36a7c5ac\n"
      "v_mfma_f32_16x16x32_bf16 v[180:183], a[192:195], v[148:151], v[180:183]\n"
      "buffer_store_dwordx2 v[204:205], v20, s[8:11], s49 offen sc1\n"
      "v_mfma_f32_16x16x32_bf16 v[180:183], a[196:199], v[152:155], v[180:183]\n"
      "s_add_u32 s50, s50, 24576\n"
      "v_add_f32 v218, v218, v177\n"
      "v_mfma_f32_16x16x32_bf16 v[180:183], a[200:203], v[156:159], v[180:183]\n"
      "v_add_f32 v218, v218, v178\n"
      "v_mfma_f32_16x16x32_bf16 v[180:183], a[204:207], v[160:163], v[180:183]\n"
      "s_waitcnt lgkmcnt(0)\n"
      "v_add_f32 v218, v218, v179\n"
      "v_cmp_gt_i32 vcc, v21, v22\n"
      "v_mfma_f32_16x16x32_bf16 v[180:183], a[208:211], v[164:167], v[180:183]\n"
      "s_waitcnt vmcnt(10)\n"
      "buffer_store_dword v25, v12, s[8:11], s50 offen sc1\n"
      "v_mfma_f32_16x16x32_bf16 v[180:183], a[212:215], v[168:171], v[180:183]\n"
      "v_fmac_f32 v219, v179, v179\n"
      "v_mfma_f32_16x16x32_bf16 v[184:187], a[216:219], v[100:103], v[184:187]\n"
      "s_cbranch_vccnz .Lsp_vy0_24\n"
      ".Lbk_vy0_24:\n"
      "s_sub_u32 s47, s46, s45\n"
      "v_mfma_f32_16x16x32_bf16 v[184:187], a[220:223], v[104:107], v[184:187]\n"
      "s_waitcnt vmcnt(10)\n"
      "v_lshlrev_b32 v174, 16, v193\n"
      "v_and_b32 v179, 0xffff0000, v195\n"
      "v_mfma_f32_16x16x32_bf16 v[184:187], a[224:227], v[108:111], v[184:187]\n"
      "ds_read_b128 v[100:103], v10 offset:8192\n"
      "v_cvt_pk_bf16_f32 v213, v182, v183\n"
      "v_mfma_f32_16x16x32_bf16 v[184:187], a[228:231], v[112:115], v[184:187]\n"
      "ds_read_b128 v[108:111], v10 offset:10240\n"
      "v_and_b32 v173, 0xffff0000, v192\n"
      "v_mfma_f32_16x16x32_bf16 v[184:187], a[232:235], v[116:119], v[184:187]\n"
      "v_fmac_f32 v219, v180, v180\n"
      "v_mfma_f32_16x16x32_bf16 v[184:187], a[236:239], v[120:123], v[184:187]\n"
      "ds_read_b128 v[120:123], v10 offset:13312\n"
      "v_add_f32 v218, v218, v180\n"
      "v_mfma_f32_16x16x32_bf16 v[184:187], a[240:243], v[124:127], v[184:187]\n"
      "v_add_f32 v218, v218, v181\n"
      "v_add_f32 v218, v218, v182\n"
      "v_mfma_f32_16x16x32_bf16 v[184:187], a[244:247], v[128:131], v[184:187]\n"
      "ds_read_b128 v[128:131], v10 offset:15360\n"
      "v_lshlrev_b32 v178, 16, v195\n"
      "v_mfma_f32_16x16x32_bf16 v[184:187], a[248:251], v[132:135], v[184:187]\n"
      "v_mfma_f32_16x16x32_bf16 v[184:187], a[252:255], v[136:139], v[184:187]\n"
      "ds_read_b128 v[104:107], v10 offset:9216\n"
      "v_lshlrev_b32 v172, 16, v192\n"
      "v_mfma_f32_16x16x32_bf16 v[184:187], v[32:35], v[140:143], v[184:187]\n"
      "buffer_store_dwordx4 v[208:211], v15, s[8:11], s38 offen sc1\n"
      "v_mfma_f32_16x16x32_bf16 v[184:187], v[36:39], v[144:147], v[184:187]\n"
      "s_mov_b32 s46, s36\n"
      "ds_read_b128 v[112:115], v10 offset:11264\n"
      "v_fmac_f32 v219, v181, v181\n"
      "v_mfma_f32_16x16x32_bf16 v[184:187], v[40:43], v[148:151], v[184:187]\n"
      "ds_read2_b32 v[134:135], v6 offset0:64 offset1:96\n"
      "v_cvt_pk_bf16_f32 v212, v180, v181\n"
      "v_mfma_f32_16x16x32_bf16 v[184:187], v[44:47], v[152:155], v[184:187]\n"
      "ds_read2_b32 v[132:133], v6 offset0:0 offset1:32\n"
      "s_add_u32 s36, s36, 24576\n"
      "v_mfma_f32_16x16x32_bf16 v[184:187], v[48:51], v[156:159], v[184:187]\n"
      "ds_read_b128 v[116:119], v10 offset:12288\n"
      "v_fmac_f32 v219, v182, v182\n"
      "v_mfma_f32_16x16x32_bf16 v[184:187], v[52:55], v[160:163], v[184:187]\n"
      "ds_read_b128 v[124:127], v10 offset:14336\n"
      "v_add_f32 v218, v218, v183\n"
      "v_mfma_f32_16x16x32_bf16 v[184:187], v[56:59], v[164:167], v[184:187]\n"
      "s_waitcnt lgkmcnt(5)\n"
      "ds_read_b128 v[160:163], v10 offset:6144\n"
      "v_and_b32 v177, 0xffff0000, v194\n"
      "v_mfma_f32_16x16x32_bf16 v[184:187], v[60:63], v[168:171], v[184:187]\n"
      "v_mfma_f32_16x16x32_bf16 v[188:191], v[64:67], v[100:103], 0\n"
      "ds_read_b128 v[140:143], v10 offset:1024\n"
      "v_and_b32 v175, 0xffff0000, v193\n"
      "v_mfma_f32_16x16x32_bf16 v[188:191], v[68:71], v[104:107], v[188:191]\n"
      "ds_read2_b32 v[168:169], v7 offset0:0 offset1:32\n"
      "v_lshlrev_b32 v176, 16, v194\n"
      "v_mfma_f32_16x16x32_bf16 v[188:191], v[72:75], v[108:111], v[188:191]\n"
      "s_waitcnt lgkmcnt(5)\n"
      "ds_read_b128 v[164:167], v10 offset:7168\n"
      "v_add_f32 v218, v218, v184\n"
      "v_mfma_f32_16x16x32_bf16 v[188:191], v[76:79], v[112:115], v[188:191]\n"
      "s_waitcnt lgkmcnt(5)\n"
      "ds_read_b128 v[144:147], v10 offset:2048\n"
      "v_add_f32 v218, v218, v185\n"
      "v_mfma_f32_16x16x32_bf16 v[188:191], v[80:83], v[116:119], v[188:191]\n"
      "buffer_load_dwordx4 v[192:195], v13, s[8:11], s47 offen sc1\n"
      "v_mfma_f32_16x16x32_bf16 v[188:191], v[84:87], v[120:123], v[188:191]\n"
      "s_waitcnt lgkmcnt(4)\n"
      "ds_read2_b32 v[170:171], v7 offset0:64 offset1:96\n"
      "v_fmac_f32 v219, v183, v183\n"
      "v_mfma_f32_16x16x32_bf16 v[188:191], v[88:91], v[124:127], v[188:191]\n"
      "s_min_u32 s36, s36, s6\n"
      "ds_read_b128 v[136:139], v10 offset:0\n"
      "v_fmac_f32 v219, v184, v184\n"
      "v_mfma_f32_16x16x32_bf16 v[188:191], v[92:95], v[128:131], v[188:191]\n"
      "ds_read_b128 v[148:151], v10 offset:3072\n"
      "v_mfma_f32_16x16x32_bf16 v[188:191], v[96:99], v[132:135], v[188:191]\n"
      "v_cvt_pk_bf16_f32 v215, v186, v187\n"
      "v_add_f32 v218, v218, v186\n"
      "v_mfma_f32_16x16x32_bf16 v[172:175], a[0:3], v[100:103], v[172:175]\n"
      "ds_read_b128 v[152:155], v10 offset:4096\n"
      "v_add_f32 v218, v218, v187\n"
      "v_mfma_f32_16x16x32_bf16 v[172:175], a[4:7], v[104:107], v[172:175]\n"
      "ds_read_b128 v[156:159], v10 offset:5120\n"
      "v_fmac_f32_e64 v219, v185, v185\n"
      "v_mfma_f32_16x16x32_bf16 v[172:175], a[8:11], v[108:111], v[172:175]\n"
      "s_add_u32 m0, s39, 0\n"
      "v_cvt_pk_bf16_f32 v214, v184, v185\n"
      "v_mfma_f32_16x16x32_bf16 v[172:175], a[12:15], v[112:115], v[172:175]\n"
      "ds_write_b32 v18, v218\n"
      "v_mfma_f32_16x16x32_bf16 v[172:175], a[16:19], v[116:119], v[172:175]\n"
      "s_waitcnt vmcnt(10) lgkmcnt(4)\n"
      "v_fmac_f32_e64 v219, v186, v186\n"
      "v_cmp_ne_u32 vcc, s21, v28\n"
      "v_mfma_f32_16x16x32_bf16 v[172:175], a[20:23], v[120:123], v[172:175]\n"
      "ds_write_b32 v17, v190 offset:512\n"
      "v_mfma_f32_16x16x32_bf16 v[172:175], a[24:27], v[124:127], v[172:175]\n"
      "buffer_store_dwordx4 v[212:215], v15, s[8:11], s38 offen offset:1024 sc1\n"
      "v_mfma_f32_16x16x32_bf16 v[172:175], a[28:31], v[128:131], v[172:175]\n"
      "ds_write_b32 v17, v188 offset:0\n"
      "v_mfma_f32_16x16x32_bf16 v[172:175], a[32:35], v[132:135], v[172:175]\n"
      "v_fmac_f32_e64 v219, v187, v187\n"
      "v_mfma_f32_16x16x32_bf16 v[172:175], a[36:39], v[136:139], v[172:175]\n"
      "ds_write_b32 v17, v191 offset:768\n"
      "v_mfma_f32_16x16x32_bf16 v[172:175], a[40:43], v[140:143], v[172:175]\n"
      "s_cbranch_vccnz .Lsp_v0_25\n"
      ".Lbk_v0_25:\n"
      "v_add_u32 v21, 1, v21\n"
      "v_mfma_f32_16x16x32_bf16 v[172:175], a[44:47], v[144:147], v[172:175]\n"
      "s_waitcnt lgkmcnt(3)\n"
      "ds_write_b32 v18, v219 offset:4\n"
      "v_mfma_f32_16x16x32_bf16 v[172:175], a[48:51], v[148:151], v[172:175]\n"
      "buffer_load_dword v28, v11, s[8:11], s36 offen sc1\n"
      "v_mfma_f32_16x16x32_bf16 v[172:175], a[52:55], v[152:155], v[172:175]\n"
      "ds_write_b32 v17, v189 offset:256\n"
      "v_mfma_f32_16x16x32_bf16 v[172:175], a[56:59], v[156:159], v[172:175]\n"
      "s_waitcnt vmcnt(8) lgkmcnt(3)\n"
      "v_lshlrev_b32 v186, 16, v199\n"
      "v_and_b32 v185, 0xffff0000, v198\n"
      "v_mfma_f32_16x16x32_bf16 v[172:175], a[60:63], v[160:163], v[172:175]\n"
      "ds_write_b32 v23, v21\n"
      "v_mfma_f32_16x16x32_bf16 v[172:175], a[64:67], v[164:167], v[172:175]\n"
      "v_and_b32 v181, 0xffff0000, v196\n"
      "v_and_b32 v187, 0xffff0000, v199\n"
      "v_mfma_f32_16x16x32_bf16 v[172:175], a[68:71], v[168:171], v[172:175]\n"
      "buffer_load_dwordx4 v0, s[8:11], s46 offen offset:3072 sc1 lds\n"
      "v_mfma_f32_16x16x32_bf16 v[176:179], a[72:75], v[100:103], v[176:179]\n"
      "v_lshlrev_b32 v182, 16, v197\n"
      "s_add_u32 s38, s38, 24576\n"
      "v_mfma_f32_16x16x32_bf16 v[176:179], a[76:79], v[104:107], v[176:179]\n"
      "v_lshlrev_b32 v184, 16, v198\n"
      "v_mfma_f32_16x16x32_bf16 v[176:179], a[80:83], v[108:111], v[176:179]\n"
      "v_cvt_pk_bf16_f32 v208, v172, v173\n"
      "v_mfma_f32_16x16x32_bf16 v[176:179], a[84:87], v[112:115], v[176:179]\n"
      "v_mul_f32 v219, v172, v172\n"
      "v_mfma_f32_16x16x32_bf16 v[176:179], a[88:91], v[116:119], v[176:179]\n"
      "v_lshlrev_b32 v180, 16, v196\n"
      "v_and_b32 v183, 0xffff0000, v197\n"
      "v_mfma_f32_16x16x32_bf16 v[176:179], a[92:95], v[120:123], v[176:179]\n"
      "v_fmac_f32 v219, v173, v173\n"
      "v_mfma_f32_16x16x32_bf16 v[176:179], a[96:99], v[124:127], v[176:179]\n"
      "buffer_load_dwordx4 v[196:199], v13, s[8:11], s47 offen offset:1024 sc1\n"
      "v_mfma_f32_16x16x32_bf16 v[176:179], a[100:103], v[128:131], v[176:179]\n"
      "ds_read_b32 v22, v24\n"
      "v_mfma_f32_16x16x32_bf16 v[176:179], a[104:107], v[132:135], v[176:179]\n"
      "v_fmac_f32 v219, v174, v174\n"
      "v_mfma_f32_16x16x32_bf16 v[176:179], a[108:111], v[136:139], v[176:179]\n"
      "v_fmac_f32 v219, v175, v175\n"
      "v_mfma_f32_16x16x32_bf16 v[176:179], a[112:115], v[140:143], v[176:179]\n"
      "buffer_load_dwordx4 v0, s[8:11], s46 offen offset:0 sc1 lds\n"
      "v_mfma_f32_16x16x32_bf16 v[176:179], a[116:119], v[144:147], v[176:179]\n"
      "v_mfma_f32_16x16x32_bf16 v[176:179], a[120:123], v[148:151], v[176:179]\n"
      "s_waitcnt lgkmcnt(0)\n"
      "v_cmp_gt_i32 vcc, v21, v22\n"
      "v_add_f32_e64 v218, v172, v173\n"
      "v_mfma_f32_16x16x32_bf16 v[176:179], a[124:127], v[152:155], v[176:179]\n"
      "v_add_f32_e64 v218, v218, v174\n"
      "v_add_f32_e64 v218, v218, v175\n"
      "v_mfma_f32_16x16x32_bf16 v[176:179], a[128:131], v[156:159], v[176:179]\n"
      "buffer_load_dwordx4 v0, s[8:11], s46 offen offset:1024 sc1 lds\n"
      "v_mfma_f32_16x16x32_bf16 v[176:179], a[132:135], v[160:163], v[176:179]\n"
      "s_cbranch_vccnz .Lsp_vz0_26\n"
      ".Lbk_vz0_26:\n"
      "ds_read_b128 v[204:207], v19\n"
      "v_mfma_f32_16x16x32_bf16 v[176:179], a[136:139], v[164:167], v[176:179]\n"
      "v_cvt_pk_bf16_f32 v209, v174, v175\n"
      "v_add_u32 v21, 1, v21\n"
      "v_mfma_f32_16x16x32_bf16 v[176:179], a[140:143], v[168:171], v[176:179]\n"
      "s_waitcnt vmcnt(10)\n"
      "ds_write_b32 v23, v21\n"
      "v_mfma_f32_16x16x32_bf16 v[180:183], a[144:147], v[100:103], v[180:183]\n"
      "v_mfma_f32_16x16x32_bf16 v[180:183], a[148:151], v[104:107], v[180:183]\n"
      "buffer_load_dwordx4 v0, s[8:11], s46 offen offset:2048 sc1 lds\n"
      "v_mfma_f32_16x16x32_bf16 v[180:183], a[152:155], v[108:111], v[180:183]\n"
      "s_add_u32 s49, s49, 24576\n"
      "s_add_u32 m0, s40, 0\n"
      "v_mfma_f32_16x16x32_bf16 v[180:183], a[156:159], v[112:115], v[180:183]\n"
      "v_mfma_f32_16x16x32_bf16 v[180:183], a[160:163], v[116:119], v[180:183]\n"
      "s_waitcnt lgkmcnt(1)\n"
      "v_add_f32 v204, v204, v206\n"
      "v_cvt_pk_bf16_f32 v210, v176, v177\n"
      "v_mfma_f32_16x16x32_bf16 v[180:183], a[164:167], v[120:123], v[180:183]\n"
      "buffer_load_dwordx4 v1, s[8:11], s46 offen offset:0 sc1 lds\n"
      "v_mfma_f32_16x16x32_bf16 v[180:183], a[168:171], v[124:127], v[180:183]\n"
      "v_add_f32 v205, v205, v207\n"
      "v_add_f32_dpp v204, v204, v204 quad_perm:[1,0,3,2] row_mask:0xf bank_mask:0xf\n"
      "v_mfma_f32_16x16x32_bf16 v[180:183], a[172:175], v[128:131], v[180:183]\n"
      "v_fmac_f32 v219, v176, v176\n"
      "v_mfma_f32_16x16x32_bf16 v[180:183], a[176:179], v[132:135], v[180:183]\n"
      "v_add_f32_dpp v204, v204, v204 quad_perm:[2,3,0,1] row_mask:0xf bank_mask:0xf\n"
      "v_cvt_pk_bf16_f32 v211, v178, v179\n"
      "v_mfma_f32_16x16x32_bf16 v[180:183], a[180:183], v[136:139], v[180:183]\n"
      "buffer_store_dwordx4 v[208:211], v15, s[8:11], s38 offen sc1\n"
      "v_mfma_f32_16x16x32_bf16 v[180:183], a[184:187], v[140:143], v[180:183]\n"
      "ds_read_b32 v22, v24\n"
      "v_add_f32_dpp v205, v205, v205 quad_perm:[1,0,3,2] row_mask:0xf bank_mask:0xf\n"
      "v_mfma_f32_16x16x32_bf16 v[180:183], a[188:191], v[144:147], v[180:183]\n"
      "s_add_u32 s50, s50, 24576\n"
      "v_add_f32_dpp v205, v205, v205 quad_perm:[2,3,0,1] row_mask:0xf bank_mask:0xf\n"
      "v_fmac_f32 v219, v177, v177\n"
      "v_mfma_f32_16x16x32_bf16 v[180:183], a[192:195], v[148:151], v[180:183]\n"
      "v_add_f32_dpp v205, v205, v205 row_half_mirror row_mask:0xf bank_mask:0xf\n"
      "v_fmac_f32 v219, v178, v178\n"
      "v_mfma_f32_16x16x32_bf16 v[180:183], a[196:199], v[152:155], v[180:183]\n"
      ".Lv_p1:\n"
      "s_cbranch_execz .Lv_pr\n"
      "v_add_f32_dpp v204, v204, v204 row_half_mirror row_mask:0xf bank_mask:0xf\n"
      "v_mfma_f32_16x16x32_bf16 v[180:183], a[200:203], v[156:159], v[180:183]\n"
      "v_mul_f32 v204, v27, v204\n"
      "v_fmaak_f32 v205, v27, v205, 0x36a7c5ac\n"
      "v_mfma_f32_16x16x32_bf16 v[180:183], a[204:207], v[160:163], v[180:183]\n"
      "s_waitcnt vmcnt(12) lgkmcnt(1)\n"
      "buffer_store_dword v25, v12, s[8:11], s50 offen sc1\n"
      "v_mfma_f32_16x16x32_bf16 v[180:183], a[208:211], v[164:167], v[180:183]\n"
      "s_waitcnt lgkmcnt(0)\n"
      "v_cmp_gt_i32 vcc, v21, v22\n"
      "v_add_f32 v218, v218, v176\n"
      "v_mfma_f32_16x16x32_bf16 v[180:183], a[212:215], v[168:171], v[180:183]\n"
      "v_add_f32 v218, v218, v177\n"
      "v_add_f32_e64 v218, v218, v178\n"
      "v_mfma_f32_16x16x32_bf16 v[184:187], a[216:219], v[100:103], v[184:187]\n"
      "buffer_store_dwordx2 v[204:205], v20, s[8:11], s49 offen sc1\n"
      "v_mfma_f32_16x16x32_bf16 v[184:187], a[220:223], v[104:107], v[184:187]\n"
      "s_sub_u32 s47, s46, s45\n"
      "s_cbranch_vccnz .Lsp_vy1_27\n"
      ".Lbk_vy1_27:\n"
      "v_mfma_f32_16x16x32_bf16 v[184:187], a[224:227], v[108:111], v[184:187]\n"
      "s_waitcnt vmcnt(11)\n"
      "ds_read_b128 v[104:107], v10 offset:29696\n"
      "v_and_b32 v177, 0xffff0000, v194\n"
      "v_mfma_f32_16x16x32_bf16 v[184:187], a[228:231], v[112:115], v[184:187]\n"
      "v_fmac_f32 v219, v179, v179\n"
      "v_mfma_f32_16x16x32_bf16 v[184:187], a[232:235], v[116:119], v[184:187]\n"
      "v_fmac_f32 v219, v180, v180\n"
      "v_add_f32 v218, v218, v179\n"
      "v_mfma_f32_16x16x32_bf16 v[184:187], a[236:239], v[120:123], v[184:187]\n"
      "v_cvt_pk_bf16_f32 v212, v180, v181\n"
      "v_and_b32 v179, 0xffff0000, v195\n"
      "v_mfma_f32_16x16x32_bf16 v[184:187], a[240:243], v[124:127], v[184:187]\n"
      "ds_read_b128 v[120:123], v10 offset:33792\n"
      "v_lshlrev_b32 v174, 16, v193\n"
      "v_mfma_f32_16x16x32_bf16 v[184:187], a[244:247], v[128:131], v[184:187]\n"
      "ds_read_b128 v[112:115], v10 offset:31744\n"
      "v_add_f32 v218, v218, v180\n"
      "v_mfma_f32_16x16x32_bf16 v[184:187], a[248:251], v[132:135], v[184:187]\n"
      "ds_read_b128 v[100:103], v10 offset:28672\n"
      "v_add_f32 v218, v218, v181\n"
      "v_mfma_f32_16x16x32_bf16 v[184:187], a[252:255], v[136:139], v[184:187]\n"
      "ds_read_b128 v[124:127], v10 offset:34816\n"
      "v_mfma_f32_16x16x32_bf16 v[184:187], v[32:35], v[140:143], v[184:187]\n"
      "ds_read_b128 v[108:111], v10 offset:30720\n"
      "v_and_b32 v175, 0xffff0000, v193\n"
      "v_mfma_f32_16x16x32_bf16 v[184:187], v[36:39], v[144:147], v[184:187]\n"
      "ds_read_b128 v[116:119], v10 offset:32768\n"
      "v_fmac_f32 v219, v181, v181\n"
      "v_mfma_f32_16x16x32_bf16 v[184:187], v[40:43], v[148:151], v[184:187]\n"
      "v_add_f32 v218, v218, v182\n"
      "v_mfma_f32_16x16x32_bf16 v[184:187], v[44:47], v[152:155], v[184:187]\n"
      "ds_read2_b32 v[132:133], v8 offset0:0 offset1:32\n"
      "v_and_b32 v173, 0xffff0000, v192\n"
      "v_mfma_f32_16x16x32_bf16 v[184:187], v[48:51], v[156:159], v[184:187]\n"
      "ds_read_b128 v[128:131], v10 offset:35840\n"
      "v_lshlrev_b32 v178, 16, v195\n"
      "v_mfma_f32_16x16x32_bf16 v[184:187], v[52:55], v[160:163], v[184:187]\n"
      "ds_read_b128 v[156:159], v10 offset:25600\n"
      "v_lshlrev_b32 v172, 16, v192\n"
      "v_mfma_f32_16x16x32_bf16 v[184:187], v[56:59], v[164:167], v[184:187]\n"
      "ds_read_b128 v[144:147], v10 offset:22528\n"
      "v_lshlrev_b32 v176, 16, v194\n"
      "v_mfma_f32_16x16x32_bf16 v[184:187], v[60:63], v[168:171], v[184:187]\n"
      "s_waitcnt lgkmcnt(5)\n"
      "buffer_load_dwordx4 v[192:195], v13, s[8:11], s47 offen sc1\n"
      "v_mfma_f32_16x16x32_bf16 v[188:191], v[64:67], v[100:103], 0\n"
      "ds_read2_b32 v[134:135], v8 offset0:64 offset1:96\n"
      "v_fmac_f32 v219, v182, v182\n"
      "v_mfma_f32_16x16x32_bf16 v[188:191], v[68:71], v[104:107], v[188:191]\n"
      "ds_read_b128 v[148:151], v10 offset:23552\n"
      "v_add_f32 v218, v218, v183\n"
      "v_mfma_f32_16x16x32_bf16 v[188:191], v[72:75], v[108:111], v[188:191]\n"
      "s_mov_b32 s46, s36\n"
      "v_cvt_pk_bf16_f32 v213, v182, v183\n"
      "v_cvt_pk_bf16_f32 v215, v186, v187\n"
      "v_mfma_f32_16x16x32_bf16 v[188:191], v[76:79], v[112:115], v[188:191]\n"
      "s_waitcnt lgkmcnt(4)\n"
      "ds_read_b128 v[160:163], v10 offset:26624\n"
      "v_fmac_f32 v219, v183, v183\n"
      "v_mfma_f32_16x16x32_bf16 v[188:191], v[80:83], v[116:119], v[188:191]\n"
      "ds_read_b128 v[140:143], v10 offset:21504\n"
      "v_fmac_f32 v219, v184, v184\n"
      "v_mfma_f32_16x16x32_bf16 v[188:191], v[84:87], v[120:123], v[188:191]\n"
      "ds_read2_b32 v[168:169], v9 offset0:0 offset1:32\n"
      "v_add_f32 v218, v218, v184\n"
      "v_mfma_f32_16x16x32_bf16 v[188:191], v[88:91], v[124:127], v[188:191]\n"
      "ds_read_b128 v[152:155], v10 offset:24576\n"
      "v_fmac_f32 v219, v185, v185\n"
      "v_mfma_f32_16x16x32_bf16 v[188:191], v[92:95], v[128:131], v[188:191]\n"
      "s_waitcnt lgkmcnt(5)\n"
      "ds_read_b128 v[136:139], v10 offset:20480\n"
      "v_add_f32 v218, v218, v185\n"
      "v_mfma_f32_16x16x32_bf16 v[188:191], v[96:99], v[132:135], v[188:191]\n"
      "s_cmp_eq_u32 s36, s37\n"
      "s_cselect_b32 s48, 49152, s51\n"
      "ds_read_b128 v[164:167], v10 offset:27648\n"
      "v_mfma_f32_16x16x32_bf16 v[172:175], a[0:3], v[100:103], v[172:175]\n"
      "ds_read2_b32 v[170:171], v9 offset0:64 offset1:96\n"
      "v_cvt_pk_bf16_f32 v214, v184, v185\n"
      "v_mfma_f32_16x16x32_bf16 v[172:175], a[4:7], v[104:107], v[172:175]\n"
      "v_fmac_f32 v219, v186, v186\n"
      "v_add_f32 v218, v218, v186\n"
      "v_mfma_f32_16x16x32_bf16 v[172:175], a[8:11], v[108:111], v[172:175]\n"
      "ds_write_b32 v17, v189 offset:256\n"
      "v_mfma_f32_16x16x32_bf16 v[172:175], a[12:15], v[112:115], v[172:175]\n"
      "v_add_f32 v218, v218, v187\n"
      "v_add_u32 v21, 1, v21\n"
      "v_mfma_f32_16x16x32_bf16 v[172:175], a[16:19], v[116:119], v[172:175]\n"
      "buffer_store_dwordx4 v[212:215], v15, s[8:11], s38 offen offset:1024 sc1\n"
      "v_mfma_f32_16x16x32_bf16 v[172:175], a[20:23], v[120:123], v[172:175]\n"
      "ds_write_b32 v17, v190 offset:512\n"
      "v_mfma_f32_16x16x32_bf16 v[172:175], a[24:27], v[124:127], v[172:175]\n"
      "v_fmac_f32 v219, v187, v187\n"
      "v_mfma_f32_16x16x32_bf16 v[172:175], a[28:31], v[128:131], v[172:175]\n"
      "s_add_u32 s36, s36, s48\n"
      "ds_write_b32 v18, v218\n"
      "v_mfma_f32_16x16x32_bf16 v[172:175], a[32:35], v[132:135], v[172:175]\n"
      "s_waitcnt vmcnt(11) lgkmcnt(3)\n"
      "v_cmp_ne_u32 vcc, s21, v28\n"
      "v_mfma_f32_16x16x32_bf16 v[172:175], a[36:39], v[136:139], v[172:175]\n"
      "ds_write_b32 v17, v188 offset:0\n"
      "v_mfma_f32_16x16x32_bf16 v[172:175], a[40:43], v[140:143], v[172:175]\n"
      "s_waitcnt vmcnt(9) lgkmcnt(3)\n"
      "s_min_u32 s36, s36, s6\n"
      "v_lshlrev_b32 v182, 16, v197\n"
      "v_mfma_f32_16x16x32_bf16 v[172:175], a[44:47], v[144:147], v[172:175]\n"
      "ds_write_b32 v17, v191 offset:768\n"
      "v_mfma_f32_16x16x32_bf16 v[172:175], a[48:51], v[148:151], v[172:175]\n"
      "s_cbranch_vccnz .Lsp_v1_28\n"
      ".Lbk_v1_28:\n"
      "v_and_b32 v181, 0xffff0000, v196\n"
      "v_mfma_f32_16x16x32_bf16 v[172:175], a[52:55], v[152:155], v[172:175]\n"
      "ds_write_b32 v18, v219 offset:4\n"
      "v_mfma_f32_16x16x32_bf16 v[172:175], a[56:59], v[156:159], v[172:175]\n"
      "buffer_load_dword v28, v11, s[8:11], s36 offen sc1\n"
      "v_mfma_f32_16x16x32_bf16 v[172:175], a[60:63], v[160:163], v[172:175]\n"
      "s_add_u32 m0, s39, 20480\n"
      "ds_write_b32 v23, v21\n"
      "v_mfma_f32_16x16x32_bf16 v[172:175], a[64:67], v[164:167], v[172:175]\n"
      "v_and_b32 v187, 0xffff0000, v199\n"
      "v_and_b32 v183, 0xffff0000, v197\n"
      "v_mfma_f32_16x16x32_bf16 v[172:175], a[68:71], v[168:171], v[172:175]\n"
      "buffer_load_dwordx4 v0, s[8:11], s46 offen offset:0 sc1 lds\n"
      "v_mfma_f32_16x16x32_bf16 v[176:179], a[72:75], v[100:103], v[176:179]\n"
      "s_add_u32 s49, s49, 24576\n"
      "v_mfma_f32_16x16x32_bf16 v[176:179], a[76:79], v[104:107], v[176:179]\n"
      "v_mfma_f32_16x16x32_bf16 v[176:179], a[80:83], v[108:111], v[176:179]\n"
      "v_cvt_pk_bf16_f32 v209, v174, v175\n"
      "v_mfma_f32_16x16x32_bf16 v[176:179], a[84:87], v[112:115], v[176:179]\n"
      "buffer_load_dwordx4 v0, s[8:11], s46 offen offset:1024 sc1 lds\n"
      "v_mfma_f32_16x16x32_bf16 v[176:179], a[88:91], v[116:119], v[176:179]\n"
      "v_add_f32 v218, v172, v173\n"
      "v_mfma_f32_16x16x32_bf16 v[176:179], a[92:95], v[120:123], v[176:179]\n"
      "v_add_f32 v218, v218, v174\n"
      "v_lshlrev_b32 v180, 16, v196\n"
      "v_mfma_f32_16x16x32_bf16 v[176:179], a[96:99], v[124:127], v[176:179]\n"
      "ds_read_b32 v22, v24\n"
      "v_lshlrev_b32 v186, 16, v199\n"
      "v_mfma_f32_16x16x32_bf16 v[176:179], a[100:103], v[128:131], v[176:179]\n"
      "buffer_load_dwordx4 v0, s[8:11], s46 offen offset:2048 sc1 lds\n"
      "v_mfma_f32_16x16x32_bf16 v[176:179], a[104:107], v[132:135], v[176:179]\n"
      "v_mul_f32 v219, v172, v172\n"
      "v_fmac_f32 v219, v173, v173\n"
      "v_mfma_f32_16x16x32_bf16 v[176:179], a[108:111], v[136:139], v[176:179]\n"
      "v_lshlrev_b32 v184, 16, v198\n"
      "v_mfma_f32_16x16x32_bf16 v[176:179], a[112:115], v[140:143], v[176:179]\n"
      "v_fmac_f32_e64 v219, v174, v174\n"
      "v_mfma_f32_16x16x32_bf16 v[176:179], a[116:119], v[144:147], v[176:179]\n"
      "s_waitcnt lgkmcnt(0)\n"
      "v_cmp_gt_i32 vcc, v21, v22\n"
      "v_add_f32_e64 v218, v218, v175\n"
      "v_mfma_f32_16x16x32_bf16 v[176:179], a[120:123], v[148:151], v[176:179]\n"
      "v_mfma_f32_16x16x32_bf16 v[176:179], a[124:127], v[152:155], v[176:179]\n"
      "v_and_b32 v185, 0xffff0000, v198\n"
      "v_cvt_pk_bf16_f32 v208, v172, v173\n"
      "v_mfma_f32_16x16x32_bf16 v[176:179], a[128:131], v[156:159], v[176:179]\n"
      "buffer_load_dwordx4 v[196:199], v13, s[8:11], s47 offen offset:1024 sc1\n"
      "v_mfma_f32_16x16x32_bf16 v[176:179], a[132:135], v[160:163], v[176:179]\n"
      "s_cbranch_vccnz .Lsp_vz1_29\n"
      ".Lbk_vz1_29:\n"
      "ds_read_b128 v[204:207], v19\n"
      "v_mfma_f32_16x16x32_bf16 v[176:179], a[136:139], v[164:167], v[176:179]\n"
      "v_add_u32 v21, 1, v21\n"
      "v_mfma_f32_16x16x32_bf16 v[176:179], a[140:143], v[168:171], v[176:179]\n"
      "s_waitcnt vmcnt(10)\n"
      "ds_write_b32 v23, v21\n"
      "v_mfma_f32_16x16x32_bf16 v[180:183], a[144:147], v[100:103], v[180:183]\n"
      "v_fmac_f32 v219, v175, v175\n"
      "v_mfma_f32_16x16x32_bf16 v[180:183], a[148:151], v[104:107], v[180:183]\n"
      "buffer_load_dwordx4 v0, s[8:11], s46 offen offset:3072 sc1 lds\n"
      "v_mfma_f32_16x16x32_bf16 v[180:183], a[152:155], v[108:111], v[180:183]\n"
      "v_cvt_pk_bf16_f32 v210, v176, v177\n"
      "v_fmac_f32 v219, v176, v176\n"
      "v_mfma_f32_16x16x32_bf16 v[180:183], a[156:159], v[112:115], v[180:183]\n"
      "s_add_u32 m0, s40, 20480\n"
      "v_fmac_f32 v219, v177, v177\n"
      "v_mfma_f32_16x16x32_bf16 v[180:183], a[160:163], v[116:119], v[180:183]\n"
      "s_waitcnt lgkmcnt(1)\n"
      "v_add_f32 v204, v204, v206\n"
      "v_add_f32 v205, v205, v207\n"
      "v_mfma_f32_16x16x32_bf16 v[180:183], a[164:167], v[120:123], v[180:183]\n"
      "v_add_f32_dpp v204, v204, v204 quad_perm:[1,0,3,2] row_mask:0xf bank_mask:0xf\n"
      "v_fmac_f32 v219, v178, v178\n"
      "v_mfma_f32_16x16x32_bf16 v[180:183], a[168:171], v[124:127], v[180:183]\n"
      "buffer_load_dwordx4 v1, s[8:11], s46 offen offset:0 sc1 lds\n"
      "v_mfma_f32_16x16x32_bf16 v[180:183], a[172:175], v[128:131], v[180:183]\n"
      "v_add_f32_dpp v205, v205, v205 quad_perm:[1,0,3,2] row_mask:0xf bank_mask:0xf\n"
      "v_add_f32_dpp v204, v204, v204 quad_perm:[2,3,0,1] row_mask:0xf bank_mask:0xf\n"
      "v_mfma_f32_16x16x32_bf16 v[180:183], a[176:179], v[132:135], v[180:183]\n"
      "v_add_f32_dpp v204, v204, v204 row_half_mirror row_mask:0xf bank_mask:0xf\n"
      "v_cvt_pk_bf16_f32 v211, v178, v179\n"
      "v_mfma_f32_16x16x32_bf16 v[180:183], a[180:183], v[136:139], v[180:183]\n"
      "s_add_u32 s38, s38, 24576\n"
      "v_mul_f32 v204, v27, v204\n"
      "v_add_f32_dpp v205, v205, v205 quad_perm:[2,3,0,1] row_mask:0xf bank_mask:0xf\n"
      "v_mfma_f32_16x16x32_bf16 v[180:183], a[184:187], v[140:143], v[180:183]\n"
      "ds_read_b32 v22, v24\n"
      "v_add_f32 v218, v218, v176\n"
      "v_mfma_f32_16x16x32_bf16 v[180:183], a[188:191], v[144:147], v[180:183]\n"
      "v_add_f32_dpp v205, v205, v205 row_half_mirror row_mask:0xf bank_mask:0xf\n"
      "v_fmaak_f32 v205, v27, v205, 0x36a7c5ac\n"
      "v_mfma_f32_16x16x32_bf16 v[180:183], a[192:195], v[148:151], v[180:183]\n"
      "buffer_store_dwordx2 v[204:205], v20, s[8:11], s49 offen sc1\n"
      "v_mfma_f32_16x16x32_bf16 v[180:183], a[196:199], v[152:155], v[180:183]\n"
      "s_add_u32 s50, s50, 24576\n"
      "v_add_f32 v218, v218, v177\n"
      "v_mfma_f32_16x16x32_bf16 v[180:183], a[200:203], v[156:159], v[180:183]\n"
      "v_add_f32 v218, v218, v178\n"
      "v_mfma_f32_16x16x32_bf16 v[180:183], a[204:207], v[160:163], v[180:183]\n"
      "s_waitcnt lgkmcnt(0)\n"
      "v_add_f32 v218, v218, v179\n"
      "v_cmp_gt_i32 vcc, v21, v22\n"
      "v_mfma_f32_16x16x32_bf16 v[180:183], a[208:211], v[164:167], v[180:183]\n"
      "s_waitcnt vmcnt(10)\n"
      "buffer_store_dword v25, v12, s[8:11], s50 offen sc1\n"
      "v_mfma_f32_16x16x32_bf16 v[180:183], a[212:215], v[168:171], v[180:183]\n"
      "v_fmac_f32 v219, v179, v179\n"
      "v_mfma_f32_16x16x32_bf16 v[184:187], a[216:219], v[100:103], v[184:187]\n"
      "s_cbranch_vccnz .Lsp_vy0_30\n"
      ".Lbk_vy0_30:\n"
      "s_sub_u32 s47, s46, s45\n"
      "v_mfma_f32_16x16x32_bf16 v[184:187], a[220:223], v[104:107], v[184:187]\n"
      "s_waitcnt vmcnt(10)\n"
      "v_lshlrev_b32 v174, 16, v193\n"
      "v_and_b32 v179, 0xffff0000, v195\n"
      "v_mfma_f32_16x16x32_bf16 v[184:187], a[224:227], v[108:111], v[184:187]\n"
      "ds_read_b128 v[100:103], v0 offset:8192\n"
      "v_cvt_pk_bf16_f32 v213, v182, v183\n"
      "v_mfma_f32_16x16x32_bf16 v[184:187], a[228:231], v[112:115], v[184:187]\n"
      "ds_read_b128 v[108:111], v0 offset:10240\n"
      "v_and_b32 v173, 0xffff0000, v192\n"
      "v_mfma_f32_16x16x32_bf16 v[184:187], a[232:235], v[116:119], v[184:187]\n"
      "v_fmac_f32 v219, v180, v180\n"
      "v_mfma_f32_16x16x32_bf16 v[184:187], a[236:239], v[120:123], v[184:187]\n"
      "ds_read_b128 v[120:123], v0 offset:13312\n"
      "v_add_f32 v218, v218, v180\n"
      "v_mfma_f32_16x16x32_bf16 v[184:187], a[240:243], v[124:127], v[184:187]\n"
      "v_add_f32 v218, v218, v181\n"
      "v_add_f32 v218, v218, v182\n"
      "v_mfma_f32_16x16x32_bf16 v[184:187], a[244:247], v[128:131], v[184:187]\n"
      "ds_read_b128 v[128:131], v0 offset:15360\n"
      "v_lshlrev_b32 v178, 16, v195\n"
      "v_mfma_f32_16x16x32_bf16 v[184:187], a[248:251], v[132:135], v[184:187]\n"
      "v_mfma_f32_16x16x32_bf16 v[184:187], a[252:255], v[136:139], v[184:187]\n"
      "ds_read_b128 v[104:107], v0 offset:9216\n"
      "v_lshlrev_b32 v172, 16, v192\n"
      "v_mfma_f32_16x16x32_bf16 v[184:187], v[32:35], v[140:143], v[184:187]\n"
      "buffer_store_dwordx4 v[208:211], v15, s[8:11], s38 offen sc1\n"
      "v_mfma_f32_16x16x32_bf16 v[184:187], v[36:39], v[144:147], v[184:187]\n"
      "s_mov_b32 s46, s36\n"
      "ds_read_b128 v[112:115], v0 offset:11264\n"
      "v_fmac_f32 v219, v181, v181\n"
      "v_mfma_f32_16x16x32_bf16 v[184:187], v[40:43], v[148:151], v[184:187]\n"
      "ds_read2_b32 v[134:135], v2 offset0:64 offset1:96\n"
      "v_cvt_pk_bf16_f32 v212, v180, v181\n"
      "v_mfma_f32_16x16x32_bf16 v[184:187], v[44:47], v[152:155], v[184:187]\n"
      "ds_read2_b32 v[132:133], v2 offset0:0 offset1:32\n"
      "s_add_u32 s36, s36, 24576\n"
      "v_mfma_f32_16x16x32_bf16 v[184:187], v[48:51], v[156:159], v[184:187]\n"
      "ds_read_b128 v[116:119], v0 offset:12288\n"
      "v_fmac_f32 v219, v182, v182\n"
      "v_mfma_f32_16x16x32_bf16 v[184:187], v[52:55], v[160:163], v[184:187]\n"
      "ds_read_b128 v[124:127], v0 offset:14336\n"
      "v_add_f32 v218, v218, v183\n"
      "v_mfma_f32_16x16x32_bf16 v[184:187], v[56:59], v[164:167], v[184:187]\n"
      "s_waitcnt lgkmcnt(5)\n"
      "ds_read_b128 v[160:163], v0 offset:6144\n"
      "v_and_b32 v177, 0xffff0000, v194\n"
      "v_mfma_f32_16x16x32_bf16 v[184:187], v[60:63], v[168:171], v[184:187]\n"
      "s_sub_u32 s20, s20, 1\n"
      "s_cbranch_scc0 .Lv_loop\n"
      ".Lv_tail:\n"
      "s_nop 8\n"
      "v_add_f32 v218, v218, v184\n"
      "v_add_f32 v218, v218, v185\n"
      "v_fmac_f32 v219, v183, v183\n"
      "v_fmac_f32 v219, v184, v184\n"
      "v_cvt_pk_bf16_f32 v215, v186, v187\n"
      "v_add_f32 v218, v218, v186\n"
      "v_add_f32 v218, v218, v187\n"
      "v_fmac_f32 v219, v185, v185\n"
      "v_cvt_pk_bf16_f32 v214, v184, v185\n"
      "ds_write_b32 v18, v218\n"
      "v_fmac_f32 v219, v186, v186\n"
      "buffer_store_dwordx4 v[212:215], v15, s[8:11], s38 offen offset:1024 sc1\n"
      "v_fmac_f32 v219, v187, v187\n"
      "ds_write_b32 v18, v219 offset:4\n"
      "s_waitcnt lgkmcnt(0)\n"
      "s_barrier\n"
      "s_add_u32 s38, s38, 24576\n"
      "ds_read_b128 v[204:207], v19\n"
      "s_add_u32 s49, s49, 24576\n"
      "s_waitcnt lgkmcnt(0)\n"
      "v_add_f32 v204, v204, v206\n"
      "v_add_f32 v205, v205, v207\n"
      "s_nop 0\n"
      "v_add_f32_dpp v204, v204, v204 quad_perm:[1,0,3,2] row_mask:0xf bank_mask:0xf\n"
      "s_nop 1\n"
      "v_add_f32_dpp v204, v204, v204 quad_perm:[2,3,0,1] row_mask:0xf bank_mask:0xf\n"
      "v_add_f32_dpp v205, v205, v205 quad_perm:[1,0,3,2] row_mask:0xf bank_mask:0xf\n"
      "s_add_u32 s50, s50, 24576\n"
      "s_nop 0\n"
      "v_add_f32_dpp v205, v205, v205 quad_perm:[2,3,0,1] row_mask:0xf bank_mask:0xf\n"
      "s_nop 1\n"
      "v_add_f32_dpp v205, v205, v205 row_half_mirror row_mask:0xf bank_mask:0xf\n"
      "v_add_f32_dpp v204, v204, v204 row_half_mirror row_mask:0xf bank_mask:0xf\n"
      "v_mul_f32 v204, v27, v204\n"
      "v_fmaak_f32 v205, v27, v205, 0x36a7c5ac\n"
      "s_waitcnt vmcnt(3)\n"
      "buffer_store_dword v25, v12, s[8:11], s50 offen sc1\n"
      "buffer_store_dwordx2 v[204:205], v20, s[8:11], s49 offen sc1\n"
      "s_add_u32 s49, s49, 24576\n"
      "s_add_u32 s50, s50, 24576\n"
      "s_waitcnt vmcnt(0)\n"
      "buffer_store_dword v25, v12, s[8:11], s50 offen sc1\n"
      "s_add_u32 s50, s50, 24576\n"
      "s_endpgm\n"
      ".Lsp_w0_1:\n"
      "s_mov_b32 s22, s23\n"
      ".Lsl_w0_1:\n"
      "buffer_load_dword v28, v11, s[8:11], s46 offen sc1\n"
      "s_waitcnt vmcnt(0)\n"
      "v_cmp_ne_u32 vcc, s21, v28\n"
      "s_cbranch_vccz .Lbk_w0_1\n"
      "s_sleep 4\n"
      "s_sub_u32 s22, s22, 1\n"
      "s_cbranch_scc0 .Lsl_w0_1\n"
      "s_mov_b32 s23, 0\n"
      "s_branch .Lbk_w0_1\n"
      ".Lsp_w1_2:\n"
      "s_mov_b32 s22, s23\n"
      ".Lsl_w1_2:\n"
      "buffer_load_dword v28, v11, s[8:11], s46 offen sc1\n"
      "s_waitcnt vmcnt(0)\n"
      "v_cmp_ne_u32 vcc, s21, v28\n"
      "s_cbranch_vccz .Lbk_w1_2\n"
      "s_sleep 4\n"
      "s_sub_u32 s22, s22, 1\n"
      "s_cbranch_scc0 .Lsl_w1_2\n"
      "s_mov_b32 s23, 0\n"
      "s_branch .Lbk_w1_2\n"
      ".Lsp_wy0_3:\n"
      "s_lshl_b32 s22, s23, 4\n"
      ".Lsl_wy0_3:\n"
      "ds_read_b32 v22, v24\n"
      "s_waitcnt lgkmcnt(0)\n"
      "v_cmp_gt_i32 vcc, v21, v22\n"
      "s_cbranch_vccz .Lbk_wy0_3\n"
      "s_sub_u32 s22, s22, 1\n"
      "s_cbranch_scc0 .Lsl_wy0_3\n"
      "s_mov_b32 s23, 0\n"
      "s_branch .Lbk_wy0_3\n"
      ".Lsp_w0_4:\n"
      "s_mov_b32 s22, s23\n"
      "buffer_load_dword v28, v11, s[8:11], s46 offen sc1\n"
      "s_waitcnt vmcnt(0)\n"
      "v_cmp_ne_u32 vcc, s21, v28\n"
      "s_add_u32 s30, s50, 0\n"
      "buffer_store_dword v25, v12, s[8:11], s30 offen sc1\n"
      "s_add_u32 s30, s50, 24576\n"
      "buffer_store_dword v25, v12, s[8:11], s30 offen sc1\n"
      "s_cbranch_vccz .Lbk_w0_4\n"
      ".Lsl_w0_4:\n"
      "buffer_load_dword v28, v11, s[8:11], s46 offen sc1\n"
      "s_waitcnt vmcnt(0)\n"
      "v_cmp_ne_u32 vcc, s21, v28\n"
      "s_cbranch_vccz .Lbk_w0_4\n"
      "s_sleep 4\n"
      "s_sub_u32 s22, s22, 1\n"
      "s_cbranch_scc0 .Lsl_w0_4\n"
      "s_mov_b32 s23, 0\n"
      "s_branch .Lbk_w0_4\n"
      ".Lsp_wz0_5:\n"
      "s_lshl_b32 s22, s23, 4\n"
      ".Lsl_wz0_5:\n"
      "ds_read_b32 v22, v24\n"
      "s_waitcnt lgkmcnt(0)\n"
      "v_cmp_gt_i32 vcc, v21, v22\n"
      "s_cbranch_vccz .Lbk_wz0_5\n"
      "s_sub_u32 s22, s22, 1\n"
      "s_cbranch_scc0 .Lsl_wz0_5\n"
      "s_mov_b32 s23, 0\n"
      "s_branch .Lbk_wz0_5\n"
      ".Lsp_wy1_6:\n"
      "s_lshl_b32 s22, s23, 4\n"
      ".Lsl_wy1_6:\n"
      "ds_read_b32 v22, v24\n"
      "s_waitcnt lgkmcnt(0)\n"
      "v_cmp_gt_i32 vcc, v21, v22\n"
      "s_cbranch_vccz .Lbk_wy1_6\n"
      "s_sub_u32 s22, s22, 1\n"
      "s_cbranch_scc0 .Lsl_wy1_6\n"
      "s_mov_b32 s23, 0\n"
      "s_branch .Lbk_wy1_6\n"
      ".Lsp_w1_7:\n"
      "s_mov_b32 s22, s23\n"
      "buffer_load_dword v28, v11, s[8:11], s46 offen sc1\n"
      "s_waitcnt vmcnt(0)\n"
      "v_cmp_ne_u32 vcc, s21, v28\n"
      "s_add_u32 s30, s50, 24576\n"
      "buffer_store_dword v25, v12, s[8:11], s30 offen sc1\n"
      "s_cbranch_vccz .Lbk_w1_7\n"
      ".Lsl_w1_7:\n"
      "buffer_load_dword v28, v11, s[8:11], s46 offen sc1\n"
      "s_waitcnt vmcnt(0)\n"
      "v_cmp_ne_u32 vcc, s21, v28\n"
      "s_cbranch_vccz .Lbk_w1_7\n"
      "s_sleep 4\n"
      "s_sub_u32 s22, s22, 1\n"
      "s_cbranch_scc0 .Lsl_w1_7\n"
      "s_mov_b32 s23, 0\n"
      "s_branch .Lbk_w1_7\n"
      ".Lsp_wz1_8:\n"
      "s_lshl_b32 s22, s23, 4\n"
      ".Lsl_wz1_8:\n"
      "ds_read_b32 v22, v24\n"
      "s_waitcnt lgkmcnt(0)\n"
      "v_cmp_gt_i32 vcc, v21, v22\n"
      "s_cbranch_vccz .Lbk_wz1_8\n"
      "s_sub_u32 s22, s22, 1\n"
      "s_cbranch_scc0 .Lsl_wz1_8\n"
      "s_mov_b32 s23, 0\n"
      "s_branch .Lbk_wz1_8\n"
      ".Lsp_wy0_9:\n"
      "s_lshl_b32 s22, s23, 4\n"
      ".Lsl_wy0_9:\n"
      "ds_read_b32 v22, v24\n"
      "s_waitcnt lgkmcnt(0)\n"
      "v_cmp_gt_i32 vcc, v21, v22\n"
      "s_cbranch_vccz .Lbk_wy0_9\n"
      "s_sub_u32 s22, s22, 1\n"
      "s_cbranch_scc0 .Lsl_wy0_9\n"
      "s_mov_b32 s23, 0\n"
      "s_branch .Lbk_wy0_9\n"
      ".Lsp_w0_10:\n"
      "s_mov_b32 s22, s23\n"
      "buffer_load_dword v28, v11, s[8:11], s46 offen sc1\n"
      "s_waitcnt vmcnt(0)\n"
      "v_cmp_ne_u32 vcc, s21, v28\n"
      "s_add_u32 s30, s50, 0\n"
      "buffer_store_dword v25, v12, s[8:11], s30 offen sc1\n"
      "s_add_u32 s30, s50, 24576\n"
      "buffer_store_dword v25, v12, s[8:11], s30 offen sc1\n"
      "s_cbranch_vccz .Lbk_w0_10\n"
      ".Lsl_w0_10:\n"
      "buffer_load_dword v28, v11, s[8:11], s46 offen sc1\n"
      "s_waitcnt vmcnt(0)\n"
      "v_cmp_ne_u32 vcc, s21, v28\n"
      "s_cbranch_vccz .Lbk_w0_10\n"
      "s_sleep 4\n"
      "s_sub_u32 s22, s22, 1\n"
      "s_cbranch_scc0 .Lsl_w0_10\n"
      "s_mov_b32 s23, 0\n"
      "s_branch .Lbk_w0_10\n"
      ".Lsp_wz0_11:\n"
      "s_lshl_b32 s22, s23, 4\n"
      ".Lsl_wz0_11:\n"
      "ds_read_b32 v22, v24\n"
      "s_waitcnt lgkmcnt(0)\n"
      "v_cmp_gt_i32 vcc, v21, v22\n"
      "s_cbranch_vccz .Lbk_wz0_11\n"
      "s_sub_u32 s22, s22, 1\n"
      "s_cbranch_scc0 .Lsl_wz0_11\n"
      "s_mov_b32 s23, 0\n"
      "s_branch .Lbk_wz0_11\n"
      ".Lsp_wy1_12:\n"
      "s_lshl_b32 s22, s23, 4\n"
      ".Lsl_wy1_12:\n"
      "ds_read_b32 v22, v24\n"
      "s_waitcnt lgkmcnt(0)\n"
      "v_cmp_gt_i32 vcc, v21, v22\n"
      "s_cbranch_vccz .Lbk_wy1_12\n"
      "s_sub_u32 s22, s22, 1\n"
      "s_cbranch_scc0 .Lsl_wy1_12\n"
      "s_mov_b32 s23, 0\n"
      "s_branch .Lbk_wy1_12\n"
      ".Lsp_w1_13:\n"
      "s_mov_b32 s22, s23\n"
      "buffer_load_dword v28, v11, s[8:11], s46 offen sc1\n"
      "s_waitcnt vmcnt(0)\n"
      "v_cmp_ne_u32 vcc, s21, v28\n"
      "s_add_u32 s30, s50, 24576\n"
      "buffer_store_dword v25, v12, s[8:11], s30 offen sc1\n"
      "s_cbranch_vccz .Lbk_w1_13\n"
      ".Lsl_w1_13:\n"
      "buffer_load_dword v28, v11, s[8:11], s46 offen sc1\n"
      "s_waitcnt vmcnt(0)\n"
      "v_cmp_ne_u32 vcc, s21, v28\n"
      "s_cbranch_vccz .Lbk_w1_13\n"
      "s_sleep 4\n"
      "s_sub_u32 s22, s22, 1\n"
      "s_cbranch_scc0 .Lsl_w1_13\n"
      "s_mov_b32 s23, 0\n"
      "s_branch .Lbk_w1_13\n"
      ".Lsp_wz1_14:\n"
      "s_lshl_b32 s22, s23, 4\n"
      ".Lsl_wz1_14:\n"
      "ds_read_b32 v22, v24\n"
      "s_waitcnt lgkmcnt(0)\n"
      "v_cmp_gt_i32 vcc, v21, v22\n"
      "s_cbranch_vccz .Lbk_wz1_14\n"
      "s_sub_u32 s22, s22, 1\n"
      "s_cbranch_scc0 .Lsl_wz1_14\n"
      "s_mov_b32 s23, 0\n"
      "s_branch .Lbk_wz1_14\n"
      ".Lsp_wy0_15:\n"
      "s_lshl_b32 s22, s23, 4\n"
      ".Lsl_wy0_15:\n"
      "ds_read_b32 v22, v24\n"
      "s_waitcnt lgkmcnt(0)\n"
      "v_cmp_gt_i32 vcc, v21, v22\n"
      "s_cbranch_vccz .Lbk_wy0_15\n"
      "s_sub_u32 s22, s22, 1\n"
      "s_cbranch_scc0 .Lsl_wy0_15\n"
      "s_mov_b32 s23, 0\n"
      "s_branch .Lbk_wy0_15\n"
      ".Lsp_v0_16:\n"
      "s_mov_b32 s22, s23\n"
      ".Lsl_v0_16:\n"
      "buffer_load_dword v28, v11, s[8:11], s46 offen sc1\n"
      "s_waitcnt vmcnt(0)\n"
      "v_cmp_ne_u32 vcc, s21, v28\n"
      "s_cbranch_vccz .Lbk_v0_16\n"
      "s_sleep 4\n"
      "s_sub_u32 s22, s22, 1\n"
      "s_cbranch_scc0 .Lsl_v0_16\n"
      "s_mov_b32 s23, 0\n"
      "s_branch .Lbk_v0_16\n"
      ".Lsp_v1_17:\n"
      "s_mov_b32 s22, s23\n"
      ".Lsl_v1_17:\n"
      "buffer_load_dword v28, v11, s[8:11], s46 offen sc1\n"
      "s_waitcnt vmcnt(0)\n"
      "v_cmp_ne_u32 vcc, s21, v28\n"
      "s_cbranch_vccz .Lbk_v1_17\n"
      "s_sleep 4\n"
      "s_sub_u32 s22, s22, 1\n"
      "s_cbranch_scc0 .Lsl_v1_17\n"
      "s_mov_b32 s23, 0\n"
      "s_branch .Lbk_v1_17\n"
      ".Lsp_vy0_18:\n"
      "s_lshl_b32 s22, s23, 4\n"
      ".Lsl_vy0_18:\n"
      "ds_read_b32 v22, v24\n"
      "s_waitcnt lgkmcnt(0)\n"
      "v_cmp_gt_i32 vcc, v21, v22\n"
      "s_cbranch_vccz .Lbk_vy0_18\n"
      "s_sub_u32 s22, s22, 1\n"
      "s_cbranch_scc0 .Lsl_vy0_18\n"
      "s_mov_b32 s23, 0\n"
      "s_branch .Lbk_vy0_18\n"
      ".Lsp_v0_19:\n"
      "s_mov_b32 s22, s23\n"
      "buffer_load_dword v28, v11, s[8:11], s46 offen sc1\n"
      "s_waitcnt vmcnt(0)\n"
      "v_cmp_ne_u32 vcc, s21, v28\n"
      "s_add_u32 s30, s50, 24576\n"
      "buffer_store_dword v25, v12, s[8:11], s30 offen sc1\n"
      "s_cbranch_vccz .Lbk_v0_19\n"
      ".Lsl_v0_19:\n"
      "buffer_load_dword v28, v11, s[8:11], s46 offen sc1\n"
      "s_waitcnt vmcnt(0)\n"
      "v_cmp_ne_u32 vcc, s21, v28\n"
      "s_cbranch_vccz .Lbk_v0_19\n"
      "s_sleep 4\n"
      "s_sub_u32 s22, s22, 1\n"
      "s_cbranch_scc0 .Lsl_v0_19\n"
      "s_mov_b32 s23, 0\n"
      "s_branch .Lbk_v0_19\n"
      ".Lsp_vz0_20:\n"
      "s_lshl_b32 s22, s23, 4\n"
      ".Lsl_vz0_20:\n"
      "ds_read_b32 v22, v24\n"
      "s_waitcnt lgkmcnt(0)\n"
      "v_cmp_gt_i32 vcc, v21, v22\n"
      "s_cbranch_vccz .Lbk_vz0_20\n"
      "s_sub_u32 s22, s22, 1\n"
      "s_cbranch_scc0 .Lsl_vz0_20\n"
      "s_mov_b32 s23, 0\n"
      "s_branch .Lbk_vz0_20\n"
      ".Lsp_vy1_21:\n"
      "s_lshl_b32 s22, s23, 4\n"
      ".Lsl_vy1_21:\n"
      "ds_read_b32 v22, v24\n"
      "s_waitcnt lgkmcnt(0)\n"
      "v_cmp_gt_i32 vcc, v21, v22\n"
      "s_cbranch_vccz .Lbk_vy1_21\n"
      "s_sub_u32 s22, s22, 1\n"
      "s_cbranch_scc0 .Lsl_vy1_21\n"
      "s_mov_b32 s23, 0\n"
      "s_branch .Lbk_vy1_21\n"
      ".Lsp_v1_22:\n"
      "s_mov_b32 s22, s23\n"
      "buffer_load_dword v28, v11, s[8:11], s46 offen sc1\n"
      "s_waitcnt vmcnt(0)\n"
      "v_cmp_ne_u32 vcc, s21, v28\n"
      "s_add_u32 s30, s50, 24576\n"
      "buffer_store_dword v25, v12, s[8:11], s30 offen sc1\n"
      "s_cbranch_vccz .Lbk_v1_22\n"
      ".Lsl_v1_22:\n"
      "buffer_load_dword v28, v11, s[8:11], s46 offen sc1\n"
      "s_waitcnt vmcnt(0)\n"
      "v_cmp_ne_u32 vcc, s21, v28\n"
      "s_cbranch_vccz .Lbk_v1_22\n"
      "s_sleep 4\n"
      "s_sub_u32 s22, s22, 1\n"
      "s_cbranch_scc0 .Lsl_v1_22\n"
      "s_mov_b32 s23, 0\n"
      "s_branch .Lbk_v1_22\n"
      ".Lsp_vz1_23:\n"
      "s_lshl_b32 s22, s23, 4\n"
      ".Lsl_vz1_23:\n"
      "ds_read_b32 v22, v24\n"
      "s_waitcnt lgkmcnt(0)\n"
      "v_cmp_gt_i32 vcc, v21, v22\n"
      "s_cbranch_vccz .Lbk_vz1_23\n"
      "s_sub_u32 s22, s22, 1\n"
      "s_cbranch_scc0 .Lsl_vz1_23\n"
      "s_mov_b32 s23, 0\n"
      "s_branch .Lbk_vz1_23\n"
      ".Lsp_vy0_24:\n"
      "s_lshl_b32 s22, s23, 4\n"
      ".Lsl_vy0_24:\n"
      "ds_read_b32 v22, v24\n"
      "s_waitcnt lgkmcnt(0)\n"
      "v_cmp_gt_i32 vcc, v21, v22\n"
      "s_cbranch_vccz .Lbk_vy0_24\n"
      "s_sub_u32 s22, s22, 1\n"
      "s_cbranch_scc0 .Lsl_vy0_24\n"
      "s_mov_b32 s23, 0\n"
      "s_branch .Lbk_vy0_24\n"
      ".Lsp_v0_25:\n"
      "s_mov_b32 s22, s23\n"
      "buffer_load_dword v28, v11, s[8:11], s46 offen sc1\n"
      "s_waitcnt vmcnt(0)\n"
      "v_cmp_ne_u32 vcc, s21, v28\n"
      "s_add_u32 s30, s50, 24576\n"
      "buffer_store_dword v25, v12, s[8:11], s30 offen sc1\n"
      "s_cbranch_vccz .Lbk_v0_25\n"
      ".Lsl_v0_25:\n"
      "buffer_load_dword v28, v11, s[8:11], s46 offen sc1\n"
      "s_waitcnt vmcnt(0)\n"
      "v_cmp_ne_u32 vcc, s21, v28\n"
      "s_cbranch_vccz .Lbk_v0_25\n"
      "s_sleep 4\n"
      "s_sub_u32 s22, s22, 1\n"
      "s_cbranch_scc0 .Lsl_v0_25\n"
      "s_mov_b32 s23, 0\n"
      "s_branch .Lbk_v0_25\n"
      ".Lsp_vz0_26:\n"
      "s_lshl_b32 s22, s23, 4\n"
      ".Lsl_vz0_26:\n"
      "ds_read_b32 v22, v24\n"
      "s_waitcnt lgkmcnt(0)\n"
      "v_cmp_gt_i32 vcc, v21, v22\n"
      "s_cbranch_vccz .Lbk_vz0_26\n"
      "s_sub_u32 s22, s22, 1\n"
      "s_cbranch_scc0 .Lsl_vz0_26\n"
      "s_mov_b32 s23, 0\n"
      "s_branch .Lbk_vz0_26\n"
      ".Lsp_vy1_27:\n"
      "s_lshl_b32 s22, s23, 4\n"
      ".Lsl_vy1_27:\n"
      "ds_read_b32 v22, v24\n"
      "s_waitcnt lgkmcnt(0)\n"
      "v_cmp_gt_i32 vcc, v21, v22\n"
      "s_cbranch_vccz .Lbk_vy1_27\n"
      "s_sub_u32 s22, s22, 1\n"
      "s_cbranch_scc0 .Lsl_vy1_27\n"
      "s_mov_b32 s23, 0\n"
      "s_branch .Lbk_vy1_27\n"
      ".Lsp_v1_28:\n"
      "s_mov_b32 s22, s23\n"
      "buffer_load_dword v28, v11, s[8:11], s46 offen sc1\n"
      "s_waitcnt vmcnt(0)\n"
      "v_cmp_ne_u32 vcc, s21, v28\n"
      "s_add_u32 s30, s50, 24576\n"
      "buffer_store_dword v25, v12, s[8:11], s30 offen sc1\n"
      "s_cbranch_vccz .Lbk_v1_28\n"
      ".Lsl_v1_28:\n"
      "buffer_load_dword v28, v11, s[8:11], s46 offen sc1\n"
      "s_waitcnt vmcnt(0)\n"
      "v_cmp_ne_u32 vcc, s21, v28\n"
      "s_cbranch_vccz .Lbk_v1_28\n"
      "s_sleep 4\n"
      "s_sub_u32 s22, s22, 1\n"
      "s_cbranch_scc0 .Lsl_v1_28\n"
      "s_mov_b32 s23, 0\n"
      "s_branch .Lbk_v1_28\n"
      ".Lsp_vz1_29:\n"
      "s_lshl_b32 s22, s23, 4\n"
      ".Lsl_vz1_29:\n"
      "ds_read_b32 v22, v24\n"
      "s_waitcnt lgkmcnt(0)\n"
      "v_cmp_gt_i32 vcc, v21, v22\n"
      "s_cbranch_vccz .Lbk_vz1_29\n"
      "s_sub_u32 s22, s22, 1\n"
      "s_cbranch_scc0 .Lsl_vz1_29\n"
      "s_mov_b32 s23, 0\n"
      "s_branch .Lbk_vz1_29\n"
      ".Lsp_vy0_30:\n"
      "s_lshl_b32 s22, s23, 4\n"
      ".Lsl_vy0_30:\n"
      "ds_read_b32 v22, v24\n"
      "s_waitcnt lgkmcnt(0)\n"
      "v_cmp_gt_i32 vcc, v21, v22\n"
      "s_cbranch_vccz .Lbk_vy0_30\n"
      "s_sub_u32 s22, s22, 1\n"
      "s_cbranch_scc0 .Lsl_vy0_30\n"
      "s_mov_b32 s23, 0\n"
      "s_branch .Lbk_vy0_30\n"
      ".Lw_last:\n"
      :
      : "{s[8:11]}"(rsWS), "{s[12:15]}"(rsA), "{v0}"(v_l16), "{v1}"(v_l5), "{v2}"(v_ex[0]), "{v3}"(v_ex[1]), "{v4}"(v_ex[2]), "{v5}"(v_ex[3]), "{v6}"(v_ex[4]), "{v7}"(v_ex[5]), "{v8}"(v_ex[6]), "{v9}"(v_ex[7]), "{v10}"(v_l2), "{v11}"(v_fa), "{v12}"(v_ra), "{v13}"(v_rp), "{v14}"(v_rs), "{v15}"(v_xp), "{v16}"(v_xs), "{v17}"(v_ew), "{v18}"(v_sw), "{v19}"(v_sr), "{v20}"(v_ss), "{v22}"(v_al), "{v23}"(v_yw), "{v24}"(v_yr), "{v26}"(v_hi), "{v100}"(v_vo[0]), "{v101}"(v_vo[1]), "{v102}"(v_vo[2]), "{v103}"(v_vo[3]), "{v104}"(v_vo[4]), "{v105}"(v_vo[5]), "{v106}"(v_vo[6]), "{v107}"(v_vo[7]), "{v108}"(v_vo[8]), "{v109}"(v_vp[0]), "{v110}"(v_vp[1]), "{v111}"(v_vp[2]), "{v112}"(v_vp[3]), "{v113}"(v_vp[4]), "{v114}"(v_vp[5]), "{v115}"(v_vp[6]), "{v116}"(v_vp[7]), "{v117}"(v_vp[8]), "{v118}"(v_t0), "{v119}"(v_t1), "{v120}"(v_ts), "{v121}"(v_pw), "{s36}"(s_sx), "{s37}"(s_ev), "{s38}"(s_sy), "{s39}"(s_m4), "{s40}"(s_m5), "{s41}"(s_m9), "{s42}"(s_so), "{s43}"(s_s8), "{s44}"(s_wg), "{s45}"(s_rb)
      : "v255", "a255", "s99", "vcc", "scc", "memory");

  } else if (fast && slot < 28) {
    const unsigned td = (slot - 10) / 3, gr = td < 4 ? td : (e + 5 - td) & 3, part = (td < 4 ? e : td) * 3 + (slot - 10) % 3;
    COMMON(40, 149760);
    const unsigned ks = part / 3, n3 = part % 3;
    const u32x4 rsA = srd(Wd, 4608 * 576 * 2), rsB = srd(out, 2048 * 576 * 2), rsC = srd(X, 2048 * 576 * 2);
    const unsigned v_l16b = v_l16 + 4096, v_l8 = 1024 + lane * 8, f0 = 24 * ks + 6 * w;
    const unsigned v_fa = 147456 - f0 * 1024 + (f0 + (l8 < 5 ? l8 : 5)) * 128 + l7 / 4, v_ga = (l8 < 4 ? l8 : 4) * 4 * PW + 1536 + l7 / 4;
    const unsigned v_ra = up * 6 * PREC + l5 + 1536, v_rf = up * 33 * REC + l5 + 20480 + (4 * n3 + w) * 128;
    const unsigned v_pw = l8 < 5 ? (l8 + 7) * PREC + l8 * 4 * PW + 1536 + l7 : OOB, v_pu = ks && l8 + ks < 5 ? OOB : v_pw - 6 * PREC;
    const unsigned pf = 3 * (w >> 1) + 2 * (w & 1), sf = 3 * (w >> 1) + 1;
    const unsigned v_at = (8 * g + q) * 384 + 64 * pf + 16 * p, v_as = (8 * g + q) * 384 + 64 * sf + 32 * (w & 1) + 8 * p;
    auto vo = [&](unsigned i) { const unsigned c = 64 * i + lane; return (8 * w + c / 24) * 1152 + 384 * n3 + c % 24 * 16 - 1024 * i; };
    const unsigned v_vo0 = vo(0), v_vo1 = vo(1), v_vo2 = vo(2);
    const unsigned v_xp = (6 * n3 + pf) * 1024 + v_l16, v_xs = (6 * n3 + sf) * 1024 + (w & 1) * 512 + half;
    const unsigned v_op = i16 * 1152 + 384 * n3 + 64 * pf + 16 * g, v_os = i16 * 1152 + 384 * n3 + 64 * sf + 32 * (w & 1) + 8 * g;
    const unsigned v_sw = 147456 + i16 * 136 + (4 * w + g) * 8, v_sr = 147456 + (4 * w + g) * 136 + i16 * 8;
    const unsigned v_ss = 18432 + n3 * 512 + w * 128 + g * 32 + p * 8;
    const unsigned s_ph = ks ? 6 - ks : 0, s_p0 = 0u - s_ph, r0 = gr * NREC + s_p0;
    const unsigned s_sh = WS_H + r0 * HREC + f0 * 1024, s_ps = WS_PS + r0 * PREC + (n3 * 20 + w) * PW;
    const unsigned s_x1 = WS_X1 + r0 * REC, s_xr = WS_XIN + (r0 + 33) * REC, s_or = (32 * gr + 63 - s_p0) * 18432;
    const unsigned s_k0 = ROT * 6 * gr;
    const unsigned s_m3 = w * 3072, s_m6 = w * 6144, s_so = ks * 768 * 1152;
    const unsigned s_i0 = (32 * gr + 31 - ks) * 18432, s_i5 = ks < 2 ? s_i0 - 30 * 18432 : OOB;
    const unsigned s_j0 = WS_XIN + (gr * NREC + ks) * REC, s_j5 = ks < 2 ? s_j0 + 30 * REC : OOB, s_he = s_sh + (63 + s_ph) * HREC;
    asm volatile(
      "s_buffer_load_dword s21, s[8:11], 0\n"
      "s_mov_b32 s23, 65536\n"
      "s_mov_b32 s31, 0x40000000\n"
      "buffer_load_dwordx4 v[56:59], v8, s[92:95], s46 offen\n"
      "buffer_load_dwordx2 v[60:61], v9, s[92:95], s46 offen\n"
      "s_waitcnt lgkmcnt(0)\n"
      "s_mul_i32 s21, s21, 1664525\n"
      "s_add_u32 s21, s21, 1013904223\n"
      "v_mov_b32 v18, s21\n"
      "v_mov_b32 v21, v18\n"
      "v_not_b32 v224, v18\n"
      "v_not_b32 v225, v18\n"
      "v_not_b32 v226, v18\n"
      "v_not_b32 v227, v18\n"
      "v_xor_b32 v18, v18, v19\n"
      "s_waitcnt vmcnt(0)\n"
      "buffer_store_dwordx4 v[56:59], v6, s[8:11], s48 offen sc1\n"
      "buffer_store_dwordx2 v[60:61], v7, s[8:11], s48 offen sc1\n"
      "v_lshlrev_b32 v164, 16, v56\n"
      "v_and_b32 v165, 0xffff0000, v56\n"
      "v_lshlrev_b32 v166, 16, v57\n"
      "v_and_b32 v167, 0xffff0000, v57\n"
      "v_lshlrev_b32 v168, 16, v58\n"
      "v_and_b32 v169, 0xffff0000, v58\n"
      "v_lshlrev_b32 v170, 16, v59\n"
      "v_and_b32 v171, 0xffff0000, v59\n"
      "v_lshlrev_b32 v172, 16, v60\n"
      "v_and_b32 v173, 0xffff0000, v60\n"
      "v_lshlrev_b32 v174, 16, v61\n"
      "v_and_b32 v175, 0xffff0000, v61\n"
      "v_add_f32 v62, v164, v165\n"
      "v_mul_f32 v63, v164, v164\n"
      "v_fmac_f32 v63, v165, v165\n"
      "v_add_f32 v62, v62, v166\n"
      "v_fmac_f32 v63, v166, v166\n"
      "v_add_f32 v62, v62, v167\n"
      "v_fmac_f32 v63, v167, v167\n"
      "v_add_f32 v62, v62, v168\n"
      "v_fmac_f32 v63, v168, v168\n"
      "v_add_f32 v62, v62, v169\n"
      "v_fmac_f32 v63, v169, v169\n"
      "v_add_f32 v62, v62, v170\n"
      "v_fmac_f32 v63, v170, v170\n"
      "v_add_f32 v62, v62, v171\n"
      "v_fmac_f32 v63, v171, v171\n"
      "v_add_f32 v62, v62, v172\n"
      "v_fmac_f32 v63, v172, v172\n"
      "v_add_f32 v62, v62, v173\n"
      "v_fmac_f32 v63, v173, v173\n"
      "v_add_f32 v62, v62, v174\n"
      "v_fmac_f32 v63, v174, v174\n"
      "v_add_f32 v62, v62, v175\n"
      "v_fmac_f32 v63, v175, v175\n"
      "ds_write_b64 v10, v[62:63] offset:0\n"
      "s_waitcnt lgkmcnt(0)\n"
      "s_barrier\n"
      "ds_read_b64 v[152:153], v11 offset:0\n"
      "s_waitcnt lgkmcnt(0)\n"
      "v_add_f32_dpp v152, v152, v152 row_ror:8 row_mask:0xf bank_mask:0xf\n"
      "v_add_f32_dpp v153, v153, v153 row_ror:8 row_mask:0xf bank_mask:0xf\n"
      "s_nop 0\n"
      "v_add_f32_dpp v152, v152, v152 row_ror:4 row_mask:0xf bank_mask:0xf\n"
      "v_add_f32_dpp v153, v153, v153 row_ror:4 row_mask:0xf bank_mask:0xf\n"
      "s_nop 0\n"
      "v_add_f32_dpp v152, v152, v152 row_ror:2 row_mask:0xf bank_mask:0xf\n"
      "v_add_f32_dpp v153, v153, v153 row_ror:2 row_mask:0xf bank_mask:0xf\n"
      "s_nop 0\n"
      "v_add_f32_dpp v152, v152, v152 row_ror:1 row_mask:0xf bank_mask:0xf\n"
      "v_add_f32_dpp v153, v153, v153 row_ror:1 row_mask:0xf bank_mask:0xf\n"
      "v_mul_f32 v152, 0x3ae38e39, v152\n"
      "v_add_f32 v153, 0x3afba882, v153\n"
      "v_mul_f32 v153, 0x3ae38e39, v153\n"
      "buffer_store_dwordx2 v[152:153], v12, s[8:11], s48 offen sc1\n"
      "s_add_u32 s30, s46, 4294856704\n"
      "buffer_load_dwordx4 v[64:67], v8, s[92:95], s30 offen\n"
      "buffer_load_dwordx2 v[68:69], v9, s[92:95], s30 offen\n"
      "s_add_u32 s30, s46, 4294746112\n"
      "buffer_load_dwordx4 v[72:75], v8, s[92:95], s30 offen\n"
      "buffer_load_dwordx2 v[76:77], v9, s[92:95], s30 offen\n"
      "s_waitcnt vmcnt(4)\n"
      "buffer_store_dword v18, v5, s[8:11], s48 offen sc1\n"
      "s_add_u32 s30, s46, 4294635520\n"
      "buffer_load_dwordx4 v[80:83], v8, s[92:95], s30 offen\n"
      "buffer_load_dwordx2 v[84:85], v9, s[92:95], s30 offen\n"
      "s_add_u32 s30, s46, 4294524928\n"
      "buffer_load_dwordx4 v[88:91], v8, s[92:95], s30 offen\n"
      "buffer_load_dwordx2 v[92:93], v9, s[92:95], s30 offen\n"
      "buffer_load_dwordx4 v[96:99], v8, s[92:95], s47 offen\n"
      "buffer_load_dwordx2 v[100:101], v9, s[92:95], s47 offen\n"
      "buffer_store_dword v18, v15, s[8:11], 0 offen sc1\n"
      "s_cmp_eq_u32 s45, 0\n"
      "s_cselect_b32 s30, 0x40000000, s37\n"
      "buffer_store_dword v21, v3, s[8:11], s30 offen sc1\n"
      "s_waitcnt vmcnt(11)\n"
      "s_add_u32 s30, s48, 147456\n"
      "buffer_store_dwordx4 v[64:67], v6, s[8:11], s30 offen sc1\n"
      "buffer_store_dwordx2 v[68:69], v7, s[8:11], s30 offen sc1\n"
      "v_lshlrev_b32 v164, 16, v64\n"
      "v_and_b32 v165, 0xffff0000, v64\n"
      "v_lshlrev_b32 v166, 16, v65\n"
      "v_and_b32 v167, 0xffff0000, v65\n"
      "v_lshlrev_b32 v168, 16, v66\n"
      "v_and_b32 v169, 0xffff0000, v66\n"
      "v_lshlrev_b32 v170, 16, v67\n"
      "v_and_b32 v171, 0xffff0000, v67\n"
      "v_lshlrev_b32 v172, 16, v68\n"
      "v_and_b32 v173, 0xffff0000, v68\n"
      "v_lshlrev_b32 v174, 16, v69\n"
      "v_and_b32 v175, 0xffff0000, v69\n"
      "v_add_f32 v70, v164, v165\n"
      "v_mul_f32 v71, v164, v164\n"
      "v_fmac_f32 v71, v165, v165\n"
      "v_add_f32 v70, v70, v166\n"
      "v_fmac_f32 v71, v166, v166\n"
      "v_add_f32 v70, v70, v167\n"
      "v_fmac_f32 v71, v167, v167\n"
      "v_add_f32 v70, v70, v168\n"
      "v_fmac_f32 v71, v168, v168\n"
      "v_add_f32 v70, v70, v169\n"
      "v_fmac_f32 v71, v169, v169\n"
      "v_add_f32 v70, v70, v170\n"
      "v_fmac_f32 v71, v170, v170\n"
      "v_add_f32 v70, v70, v171\n"
      "v_fmac_f32 v71, v171, v171\n"
      "v_add_f32 v70, v70, v172\n"
      "v_fmac_f32 v71, v172, v172\n"
      "v_add_f32 v70, v70, v173\n"
      "v_fmac_f32 v71, v173, v173\n"
      "v_add_f32 v70, v70, v174\n"
      "v_fmac_f32 v71, v174, v174\n"
      "v_add_f32 v70, v70, v175\n"
      "v_fmac_f32 v71, v175, v175\n"
      "ds_write_b64 v10, v[70:71] offset:2176\n"
      "s_waitcnt vmcnt(11)\n"
      "s_add_u32 s30, s48, 294912\n"
      "buffer_store_dwordx4 v[72:75], v6, s[8:11], s30 offen sc1\n"
      "buffer_store_dwordx2 v[76:77], v7, s[8:11], s30 offen sc1\n"
      "v_lshlrev_b32 v164, 16, v72\n"
      "v_and_b32 v165, 0xffff0000, v72\n"
      "v_lshlrev_b32 v166, 16, v73\n"
      "v_and_b32 v167, 0xffff0000, v73\n"
      "v_lshlrev_b32 v168, 16, v74\n"
      "v_and_b32 v169, 0xffff0000, v74\n"
      "v_lshlrev_b32 v170, 16, v75\n"
      "v_and_b32 v171, 0xffff0000, v75\n"
      "v_lshlrev_b32 v172, 16, v76\n"
      "v_and_b32 v173, 0xffff0000, v76\n"
      "v_lshlrev_b32 v174, 16, v77\n"
      "v_and_b32 v175, 0xffff0000, v77\n"
      "v_add_f32 v78, v164, v165\n"
      "v_mul_f32 v79, v164, v164\n"
      "v_fmac_f32 v79, v165, v165\n"
      "v_add_f32 v78, v78, v166\n"
      "v_fmac_f32 v79, v166, v166\n"
      "v_add_f32 v78, v78, v167\n"
      "v_fmac_f32 v79, v167, v167\n"
      "v_add_f32 v78, v78, v168\n"
      "v_fmac_f32 v79, v168, v168\n"
      "v_add_f32 v78, v78, v169\n"
      "v_fmac_f32 v79, v169, v169\n"
      "v_add_f32 v78, v78, v170\n"
      "v_fmac_f32 v79, v170, v170\n"
      "v_add_f32 v78, v78, v171\n"
      "v_fmac_f32 v79, v171, v171\n"
      "v_add_f32 v78, v78, v172\n"
      "v_fmac_f32 v79, v172, v172\n"
      "v_add_f32 v78, v78, v173\n"
      "v_fmac_f32 v79, v173, v173\n"
      "v_add_f32 v78, v78, v174\n"
      "v_fmac_f32 v79, v174, v174\n"
      "v_add_f32 v78, v78, v175\n"
      "v_fmac_f32 v79, v175, v175\n"
      "ds_write_b64 v10, v[78:79] offset:4352\n"
      "s_waitcnt vmcnt(10)\n"
      "s_add_u32 s30, s48, 442368\n"
      "buffer_store_dwordx4 v[80:83], v6, s[8:11], s30 offen sc1\n"
      "buffer_store_dwordx2 v[84:85], v7, s[8:11], s30 offen sc1\n"
      "v_lshlrev_b32 v164, 16, v80\n"
      "v_and_b32 v165, 0xffff0000, v80\n"
      "v_lshlrev_b32 v166, 16, v81\n"
      "v_and_b32 v167, 0xffff0000, v81\n"
      "v_lshlrev_b32 v168, 16, v82\n"
      "v_and_b32 v169, 0xffff0000, v82\n"
      "v_lshlrev_b32 v170, 16, v83\n"
      "v_and_b32 v171, 0xffff0000, v83\n"
      "v_lshlrev_b32 v172, 16, v84\n"
      "v_and_b32 v173, 0xffff0000, v84\n"
      "v_lshlrev_b32 v174, 16, v85\n"
      "v_and_b32 v175, 0xffff0000, v85\n"
      "v_add_f32 v86, v164, v165\n"
      "v_mul_f32 v87, v164, v164\n"
      "v_fmac_f32 v87, v165, v165\n"
      "v_add_f32 v86, v86, v166\n"
      "v_fmac_f32 v87, v166, v166\n"
      "v_add_f32 v86, v86, v167\n"
      "v_fmac_f32 v87, v167, v167\n"
      "v_add_f32 v86, v86, v168\n"
      "v_fmac_f32 v87, v168, v168\n"
      "v_add_f32 v86, v86, v169\n"
      "v_fmac_f32 v87, v169, v169\n"
      "v_add_f32 v86, v86, v170\n"
      "v_fmac_f32 v87, v170, v170\n"
      "v_add_f32 v86, v86, v171\n"
      "v_fmac_f32 v87, v171, v171\n"
      "v_add_f32 v86, v86, v172\n"
      "v_fmac_f32 v87, v172, v172\n"
      "v_add_f32 v86, v86, v173\n"
      "v_fmac_f32 v87, v173, v173\n"
      "v_add_f32 v86, v86, v174\n"
      "v_fmac_f32 v87, v174, v174\n"
      "v_add_f32 v86, v86, v175\n"
      "v_fmac_f32 v87, v175, v175\n"
      "ds_write_b64 v10, v[86:87] offset:6528\n"
      "s_waitcnt vmcnt(10)\n"
      "s_add_u32 s30, s48, 589824\n"
      "buffer_store_dwordx4 v[88:91], v6, s[8:11], s30 offen sc1\n"
      "buffer_store_dwordx2 v[92:93], v7, s[8:11], s30 offen sc1\n"
      "v_lshlrev_b32 v164, 16, v88\n"
      "v_and_b32 v165, 0xffff0000, v88\n"
      "v_lshlrev_b32 v166, 16, v89\n"
      "v_and_b32 v167, 0xffff0000, v89\n"
      "v_lshlrev_b32 v168, 16, v90\n"
      "v_and_b32 v169, 0xffff0000, v90\n"
      "v_lshlrev_b32 v170, 16, v91\n"
      "v_and_b32 v171, 0xffff0000, v91\n"
      "v_lshlrev_b32 v172, 16, v92\n"
      "v_and_b32 v173, 0xffff0000, v92\n"
      "v_lshlrev_b32 v174, 16, v93\n"
      "v_and_b32 v175, 0xffff0000, v93\n"
      "v_add_f32 v94, v164, v165\n"
      "v_mul_f32 v95, v164, v164\n"
      "v_fmac_f32 v95, v165, v165\n"
      "v_add_f32 v94, v94, v166\n"
      "v_fmac_f32 v95, v166, v166\n"
      "v_add_f32 v94, v94, v167\n"
      "v_fmac_f32 v95, v167, v167\n"
      "v_add_f32 v94, v94, v168\n"
      "v_fmac_f32 v95, v168, v168\n"
      "v_add_f32 v94, v94, v169\n"
      "v_fmac_f32 v95, v169, v169\n"
      "v_add_f32 v94, v94, v170\n"
      "v_fmac_f32 v95, v170, v170\n"
      "v_add_f32 v94, v94, v171\n"
      "v_fmac_f32 v95, v171, v171\n"
      "v_add_f32 v94, v94, v172\n"
      "v_fmac_f32 v95, v172, v172\n"
      "v_add_f32 v94, v94, v173\n"
      "v_fmac_f32 v95, v173, v173\n"
      "v_add_f32 v94, v94, v174\n"
      "v_fmac_f32 v95, v174, v174\n"
      "v_add_f32 v94, v94, v175\n"
      "v_fmac_f32 v95, v175, v175\n"
      "ds_write_b64 v10, v[94:95] offset:8704\n"
      "s_waitcnt vmcnt(10)\n"
      "buffer_store_dwordx4 v[96:99], v6, s[8:11], s49 offen sc1\n"
      "buffer_store_dwordx2 v[100:101], v7, s[8:11], s49 offen sc1\n"
      "v_lshlrev_b32 v164, 16, v96\n"
      "v_and_b32 v165, 0xffff0000, v96\n"
      "v_lshlrev_b32 v166, 16, v97\n"
      "v_and_b32 v167, 0xffff0000, v97\n"
      "v_lshlrev_b32 v168, 16, v98\n"
      "v_and_b32 v169, 0xffff0000, v98\n"
      "v_lshlrev_b32 v170, 16, v99\n"
      "v_and_b32 v171, 0xffff0000, v99\n"
      "v_lshlrev_b32 v172, 16, v100\n"
      "v_and_b32 v173, 0xffff0000, v100\n"
      "v_lshlrev_b32 v174, 16, v101\n"
      "v_and_b32 v175, 0xffff0000, v101\n"
      "v_add_f32 v102, v164, v165\n"
      "v_mul_f32 v103, v164, v164\n"
      "v_fmac_f32 v103, v165, v165\n"
      "v_add_f32 v102, v102, v166\n"
      "v_fmac_f32 v103, v166, v166\n"
      "v_add_f32 v102, v102, v167\n"
      "v_fmac_f32 v103, v167, v167\n"
      "v_add_f32 v102, v102, v168\n"
      "v_fmac_f32 v103, v168, v168\n"
      "v_add_f32 v102, v102, v169\n"
      "v_fmac_f32 v103, v169, v169\n"
      "v_add_f32 v102, v102, v170\n"
      "v_fmac_f32 v103, v170, v170\n"
      "v_add_f32 v102, v102, v171\n"
      "v_fmac_f32 v103, v171, v171\n"
      "v_add_f32 v102, v102, v172\n"
      "v_fmac_f32 v103, v172, v172\n"
      "v_add_f32 v102, v102, v173\n"
      "v_fmac_f32 v103, v173, v173\n"
      "v_add_f32 v102, v102, v174\n"
      "v_fmac_f32 v103, v174, v174\n"
      "v_add_f32 v102, v102, v175\n"
      "v_fmac_f32 v103, v175, v175\n"
      "ds_write_b64 v10, v[102:103] offset:10880\n"
      "s_waitcnt lgkmcnt(0)\n"
      "s_barrier\n"
      "ds_read_b64 v[154:155], v11 offset:2176\n"
      "ds_read_b64 v[156:157], v11 offset:4352\n"
      "ds_read_b64 v[158:159], v11 offset:6528\n"
      "ds_read_b64 v[160:161], v11 offset:8704\n"
      "ds_read_b64 v[162:163], v11 offset:10880\n"
      "s_waitcnt lgkmcnt(0)\n"
      "v_add_f32_dpp v154, v154, v154 row_ror:8 row_mask:0xf bank_mask:0xf\n"
      "v_add_f32_dpp v155, v155, v155 row_ror:8 row_mask:0xf bank_mask:0xf\n"
      "v_add_f32_dpp v156, v156, v156 row_ror:8 row_mask:0xf bank_mask:0xf\n"
      "v_add_f32_dpp v157, v157, v157 row_ror:8 row_mask:0xf bank_mask:0xf\n"
      "v_add_f32_dpp v158, v158, v158 row_ror:8 row_mask:0xf bank_mask:0xf\n"
      "v_add_f32_dpp v159, v159, v159 row_ror:8 row_mask:0xf bank_mask:0xf\n"
      "v_add_f32_dpp v160, v160, v160 row_ror:8 row_mask:0xf bank_mask:0xf\n"
      "v_add_f32_dpp v161, v161, v161 row_ror:8 row_mask:0xf bank_mask:0xf\n"
      "v_add_f32_dpp v162, v162, v162 row_ror:8 row_mask:0xf bank_mask:0xf\n"
      "v_add_f32_dpp v163, v163, v163 row_ror:8 row_mask:0xf bank_mask:0xf\n"
      "v_add_f32_dpp v154, v154, v154 row_ror:4 row_mask:0xf bank_mask:0xf\n"
      "v_add_f32_dpp v155, v155, v155 row_ror:4 row_mask:0xf bank_mask:0xf\n"
      "v_add_f32_dpp v156, v156, v156 row_ror:4 row_mask:0xf bank_mask:0xf\n"
      "v_add_f32_dpp v157, v157, v157 row_ror:4 row_mask:0xf bank_mask:0xf\n"
      "v_add_f32_dpp v158, v158, v158 row_ror:4 row_mask:0xf bank_mask:0xf\n"
      "v_add_f32_dpp v159, v159, v159 row_ror:4 row_mask:0xf bank_mask:0xf\n"
      "v_add_f32_dpp v160, v160, v160 row_ror:4 row_mask:0xf bank_mask:0xf\n"
      "v_add_f32_dpp v161, v161, v161 row_ror:4 row_mask:0xf bank_mask:0xf\n"
      "v_add_f32_dpp v162, v162, v162 row_ror:4 row_mask:0xf bank_mask:0xf\n"
      "v_add_f32_dpp v163, v163, v163 row_ror:4 row_mask:0xf bank_mask:0xf\n"
      "v_add_f32_dpp v154, v154, v154 row_ror:2 row_mask:0xf bank_mask:0xf\n"
      "v_add_f32_dpp v155, v155, v155 row_ror:2 row_mask:0xf bank_mask:0xf\n"
      "v_add_f32_dpp v156, v156, v156 row_ror:2 row_mask:0xf bank_mask:0xf\n"
      "v_add_f32_dpp v157, v157, v157 row_ror:2 row_mask:0xf bank_mask:0xf\n"
      "v_add_f32_dpp v158, v158, v158 row_ror:2 row_mask:0xf bank_mask:0xf\n"
      "v_add_f32_dpp v159, v159, v159 row_ror:2 row_mask:0xf bank_mask:0xf\n"
      "v_add_f32_dpp v160, v160, v160 row_ror:2 row_mask:0xf bank_mask:0xf\n"
      "v_add_f32_dpp v161, v161, v161 row_ror:2 row_mask:0xf bank_mask:0xf\n"
      "v_add_f32_dpp v162, v162, v162 row_ror:2 row_mask:0xf bank_mask:0xf\n"
      "v_add_f32_dpp v163, v163, v163 row_ror:2 row_mask:0xf bank_mask:0xf\n"
      "v_add_f32_dpp v154, v154, v154 row_ror:1 row_mask:0xf bank_mask:0xf\n"
      "v_add_f32_dpp v155, v155, v155 row_ror:1 row_mask:0xf bank_mask:0xf\n"
      "v_add_f32_dpp v156, v156, v156 row_ror:1 row_mask:0xf bank_mask:0xf\n"
      "v_add_f32_dpp v157, v157, v157 row_ror:1 row_mask:0xf bank_mask:0xf\n"
      "v_add_f32_dpp v158, v158, v158 row_ror:1 row_mask:0xf bank_mask:0xf\n"
      "v_add_f32_dpp v159, v159, v159 row_ror:1 row_mask:0xf bank_mask:0xf\n"
      "v_add_f32_dpp v160, v160, v160 row_ror:1 row_mask:0xf bank_mask:0xf\n"
      "v_add_f32_dpp v161, v161, v161 row_ror:1 row_mask:0xf bank_mask:0xf\n"
      "v_add_f32_dpp v162, v162, v162 row_ror:1 row_mask:0xf bank_mask:0xf\n"
      "v_add_f32_dpp v163, v163, v163 row_ror:1 row_mask:0xf bank_mask:0xf\n"
      "v_mul_f32 v154, 0x3ae38e39, v154\n"
      "v_add_f32 v155, 0x3afba882, v155\n"
      "v_mul_f32 v155, 0x3ae38e39, v155\n"
      "v_mul_f32 v156, 0x3ae38e39, v156\n"
      "v_add_f32 v157, 0x3afba882, v157\n"
      "v_mul_f32 v157, 0x3ae38e39, v157\n"
      "v_mul_f32 v158, 0x3ae38e39, v158\n"
      "v_add_f32 v159, 0x3afba882, v159\n"
      "v_mul_f32 v159, 0x3ae38e39, v159\n"
      "v_mul_f32 v160, 0x3ae38e39, v160\n"
      "v_add_f32 v161, 0x3afba882, v161\n"
      "v_mul_f32 v161, 0x3ae38e39, v161\n"
      "v_mul_f32 v162, 0x3ae38e39, v162\n"
      "v_add_f32 v163, 0x3afba882, v163\n"
      "v_mul_f32 v163, 0x3ae38e39, v163\n"
      "s_add_u32 s30, s48, 147456\n"
      "buffer_store_dwordx2 v[154:155], v12, s[8:11], s30 offen sc1\n"
      "s_add_u32 s30, s48, 294912\n"
      "buffer_store_dwordx2 v[156:157], v12, s[8:11], s30 offen sc1\n"
      "s_add_u32 s30, s48, 442368\n"
      "buffer_store_dwordx2 v[158:159], v12, s[8:11], s30 offen sc1\n"
      "s_add_u32 s30, s48, 589824\n"
      "buffer_store_dwordx2 v[160:161], v12, s[8:11], s30 offen sc1\n"
      "buffer_store_dwordx2 v[162:163], v12, s[8:11], s49 offen sc1\n"
      "s_waitcnt vmcnt(0)\n"
      "s_add_u32 s30, s48, 147456\n"
      "buffer_store_dword v18, v5, s[8:11], s30 offen sc1\n"
      "s_add_u32 s30, s48, 294912\n"
      "buffer_store_dword v18, v5, s[8:11], s30 offen sc1\n"
      "s_add_u32 s30, s48, 442368\n"
      "buffer_store_dword v18, v5, s[8:11], s30 offen sc1\n"
      "s_add_u32 s30, s48, 589824\n"
      "buffer_store_dword v18, v5, s[8:11], s30 offen sc1\n"
      "buffer_store_dword v18, v5, s[8:11], s49 offen sc1\n"
      "v_and_b32 v20, 0x40000000, v19\n"
      "v_or_b32 v5, v20, v5\n"
      "s_add_u32 s30, s37, 0\n"
      "buffer_store_dwordx4 v[224:227], v183, s[8:11], s30 offen sc1\n"
      "s_add_u32 s30, s37, 0\n"
      "buffer_store_dwordx4 v[224:227], v181, s[8:11], s30 offen sc1\n"
      "s_sleep 32\n"
      "s_waitcnt vmcnt(0) lgkmcnt(0)\n"
      "s_mov_b32 s61, s60\n"
      "s_mov_b32 s62, 6\n"
      ".Ld_r0:\n"
      "s_mul_i32 s63, s61, 36864\n"
      "s_add_u32 s63, s63, s44\n"
      "s_mul_i32 s30, s61, 8192\n"
      "s_lshr_b32 s30, s30, 16\n"
      "s_mul_i32 s30, s30, 8\n"
      "s_sub_u32 s30, s61, s30\n"
      "s_mul_i32 s30, s30, 12288\n"
      "s_add_u32 s30, s30, 0\n"
      "s_add_u32 m0, s42, s30\n"
      "s_add_u32 s61, s61, 1\n"
      "buffer_load_dwordx4 v178, s[12:15], s63 offen offset:0 lds\n"
      "buffer_load_dwordx4 v179, s[12:15], s63 offen offset:1024 lds\n"
      "buffer_load_dwordx4 v180, s[12:15], s63 offen offset:2048 lds\n"
      "s_cmp_eq_u32 s61, 24\n"
      "s_cselect_b32 s61, 0, s61\n"
      "s_sub_u32 s62, s62, 1\n"
      "s_cbranch_scc0 .Ld_r0\n"
      "v_add_u32 v20, 49152, v176\n"
      "v_add_u32 v21, 49152, v177\n"
      "v_add_u32 v22, 98304, v176\n"
      "v_add_u32 v23, 98304, v177\n"
      "s_mov_b32 s62, 23\n"
      "s_cmp_eq_u32 s60, 6\n"
      "s_cbranch_scc1 .Ld_rs6\n"
      "s_cmp_eq_u32 s60, 12\n"
      "s_cbranch_scc1 .Ld_rs12\n"
      "s_cmp_eq_u32 s60, 18\n"
      "s_cbranch_scc1 .Ld_rs18\n"
      ".Ld_rs0:\n"
      "s_add_u32 s63, s44, 258048\n"
      "s_cmp_ge_u32 s62, 7\n"
      "s_cselect_b32 s63, s63, s31\n"
      "s_add_u32 m0, s42, 86016\n"
      "s_waitcnt vmcnt(18) lgkmcnt(0)\n"
      "s_barrier\n"
      "ds_read_b64_tr_b16 a[0:1], v176 offset:0\n"
      "ds_read_b64_tr_b16 a[2:3], v176 offset:1536\n"
      "ds_read_b64_tr_b16 a[96:97], v176 offset:8\n"
      "ds_read_b64_tr_b16 a[98:99], v176 offset:1544\n"
      "ds_read_b64_tr_b16 a[192:193], v177 offset:0\n"
      "ds_read_b64_tr_b16 a[194:195], v177 offset:1536\n"
      "buffer_load_dwordx4 v178, s[12:15], s63 offen offset:0 lds\n"
      "buffer_load_dwordx4 v179, s[12:15], s63 offen offset:1024 lds\n"
      "buffer_load_dwordx4 v180, s[12:15], s63 offen offset:2048 lds\n"
      "s_sub_u32 s62, s62, 1\n"
      ".Ld_rs1:\n"
      "s_add_u32 s63, s44, 294912\n"
      "s_cmp_ge_u32 s62, 7\n"
      "s_cselect_b32 s63, s63, s31\n"
      "s_add_u32 m0, s42, 0\n"
      "s_waitcnt vmcnt(18) lgkmcnt(0)\n"
      "s_barrier\n"
      "ds_read_b64_tr_b16 a[4:5], v176 offset:12288\n"
      "ds_read_b64_tr_b16 a[6:7], v176 offset:13824\n"
      "ds_read_b64_tr_b16 a[100:101], v176 offset:12296\n"
      "ds_read_b64_tr_b16 a[102:103], v176 offset:13832\n"
      "ds_read_b64_tr_b16 a[196:197], v177 offset:12288\n"
      "ds_read_b64_tr_b16 a[198:199], v177 offset:13824\n"
      "buffer_load_dwordx4 v178, s[12:15], s63 offen offset:0 lds\n"
      "buffer_load_dwordx4 v179, s[12:15], s63 offen offset:1024 lds\n"
      "buffer_load_dwordx4 v180, s[12:15], s63 offen offset:2048 lds\n"
      "s_sub_u32 s62, s62, 1\n"
      ".Ld_rs2:\n"
      "s_add_u32 s63, s44, 331776\n"
      "s_cmp_ge_u32 s62, 7\n"
      "s_cselect_b32 s63, s63, s31\n"
      "s_add_u32 m0, s42, 12288\n"
      "s_waitcnt vmcnt(18) lgkmcnt(0)\n"
      "s_barrier\n"
      "ds_read_b64_tr_b16 a[8:9], v176 offset:24576\n"
      "ds_read_b64_tr_b16 a[10:11], v176 offset:26112\n"
      "ds_read_b64_tr_b16 a[104:105], v176 offset:24584\n"
      "ds_read_b64_tr_b16 a[106:107], v176 offset:26120\n"
      "ds_read_b64_tr_b16 a[200:201], v177 offset:24576\n"
      "ds_read_b64_tr_b16 a[202:203], v177 offset:26112\n"
      "buffer_load_dwordx4 v178, s[12:15], s63 offen offset:0 lds\n"
      "buffer_load_dwordx4 v179, s[12:15], s63 offen offset:1024 lds\n"
      "buffer_load_dwordx4 v180, s[12:15], s63 offen offset:2048 lds\n"
      "s_sub_u32 s62, s62, 1\n"
      ".Ld_rs3:\n"
      "s_add_u32 s63, s44, 368640\n"
      "s_cmp_ge_u32 s62, 7\n"
      "s_cselect_b32 s63, s63, s31\n"
      "s_add_u32 m0, s42, 24576\n"
      "s_waitcnt vmcnt(18) lgkmcnt(0)\n"
      "s_barrier\n"
      "ds_read_b64_tr_b16 a[12:13], v176 offset:36864\n"
      "ds_read_b64_tr_b16 a[14:15], v176 offset:38400\n"
      "ds_read_b64_tr_b16 a[108:109], v176 offset:36872\n"
      "ds_read_b64_tr_b16 a[110:111], v176 offset:38408\n"
      "ds_read_b64_tr_b16 a[204:205], v177 offset:36864\n"
      "ds_read_b64_tr_b16 a[206:207], v177 offset:38400\n"
      "buffer_load_dwordx4 v178, s[12:15], s63 offen offset:0 lds\n"
      "buffer_load_dwordx4 v179, s[12:15], s63 offen offset:1024 lds\n"
      "buffer_load_dwordx4 v180, s[12:15], s63 offen offset:2048 lds\n"
      "s_sub_u32 s62, s62, 1\n"
      ".Ld_rs4:\n"
      "s_add_u32 s63, s44, 405504\n"
      "s_cmp_ge_u32 s62, 7\n"
      "s_cselect_b32 s63, s63, s31\n"
      "s_add_u32 m0, s42, 36864\n"
      "s_waitcnt vmcnt(18) lgkmcnt(0)\n"
      "s_barrier\n"
      "ds_read_b64_tr_b16 a[16:17], v20 offset:0\n"
      "ds_read_b64_tr_b16 a[18:19], v20 offset:1536\n"
      "ds_read_b64_tr_b16 a[112:113], v20 offset:8\n"
      "ds_read_b64_tr_b16 a[114:115], v20 offset:1544\n"
      "ds_read_b64_tr_b16 a[208:209], v21 offset:0\n"
      "ds_read_b64_tr_b16 a[210:211], v21 offset:1536\n"
      "buffer_load_dwordx4 v178, s[12:15], s63 offen offset:0 lds\n"
      "buffer_load_dwordx4 v179, s[12:15], s63 offen offset:1024 lds\n"
      "buffer_load_dwordx4 v180, s[12:15], s63 offen offset:2048 lds\n"
      "s_sub_u32 s62, s62, 1\n"
      ".Ld_rs5:\n"
      "s_add_u32 s63, s44, 442368\n"
      "s_cmp_ge_u32 s62, 7\n"
      "s_cselect_b32 s63, s63, s31\n"
      "s_add_u32 m0, s42, 49152\n"
      "s_waitcnt vmcnt(18) lgkmcnt(0)\n"
      "s_barrier\n"
      "ds_read_b64_tr_b16 a[20:21], v20 offset:12288\n"
      "ds_read_b64_tr_b16 a[22:23], v20 offset:13824\n"
      "ds_read_b64_tr_b16 a[116:117], v20 offset:12296\n"
      "ds_read_b64_tr_b16 a[118:119], v20 offset:13832\n"
      "ds_read_b64_tr_b16 a[212:213], v21 offset:12288\n"
      "ds_read_b64_tr_b16 a[214:215], v21 offset:13824\n"
      "buffer_load_dwordx4 v178, s[12:15], s63 offen offset:0 lds\n"
      "buffer_load_dwordx4 v179, s[12:15], s63 offen offset:1024 lds\n"
      "buffer_load_dwordx4 v180, s[12:15], s63 offen offset:2048 lds\n"
      "s_sub_u32 s62, s62, 1\n"
      "s_cbranch_scc1 .Ld_re\n"
      ".Ld_rs6:\n"
      "s_add_u32 s63, s44, 479232\n"
      "s_cmp_ge_u32 s62, 7\n"
      "s_cselect_b32 s63, s63, s31\n"
      "s_add_u32 m0, s42, 61440\n"
      "s_waitcnt vmcnt(18) lgkmcnt(0)\n"
      "s_barrier\n"
      "ds_read_b64_tr_b16 a[24:25], v20 offset:24576\n"
      "ds_read_b64_tr_b16 a[26:27], v20 offset:26112\n"
      "ds_read_b64_tr_b16 a[120:121], v20 offset:24584\n"
      "ds_read_b64_tr_b16 a[122:123], v20 offset:26120\n"
      "ds_read_b64_tr_b16 a[216:217], v21 offset:24576\n"
      "ds_read_b64_tr_b16 a[218:219], v21 offset:26112\n"
      "buffer_load_dwordx4 v178, s[12:15], s63 offen offset:0 lds\n"
      "buffer_load_dwordx4 v179, s[12:15], s63 offen offset:1024 lds\n"
      "buffer_load_dwordx4 v180, s[12:15], s63 offen offset:2048 lds\n"
      "s_sub_u32 s62, s62, 1\n"
      ".Ld_rs7:\n"
      "s_add_u32 s63, s44, 516096\n"
      "s_cmp_ge_u32 s62, 7\n"
      "s_cselect_b32 s63, s63, s31\n"
      "s_add_u32 m0, s42, 73728\n"
      "s_waitcnt vmcnt(18) lgkmcnt(0)\n"
      "s_barrier\n"
      "ds_read_b64_tr_b16 a[28:29], v20 offset:36864\n"
      "ds_read_b64_tr_b16 a[30:31], v20 offset:38400\n"
      "ds_read_b64_tr_b16 a[124:125], v20 offset:36872\n"
      "ds_read_b64_tr_b16 a[126:127], v20 offset:38408\n"
      "ds_read_b64_tr_b16 a[220:221], v21 offset:36864\n"
      "ds_read_b64_tr_b16 a[222:223], v21 offset:38400\n"
      "buffer_load_dwordx4 v178, s[12:15], s63 offen offset:0 lds\n"
      "buffer_load_dwordx4 v179, s[12:15], s63 offen offset:1024 lds\n"
      "buffer_load_dwordx4 v180, s[12:15], s63 offen offset:2048 lds\n"
      "s_sub_u32 s62, s62, 1\n"
      ".Ld_rs8:\n"
      "s_add_u32 s63, s44, 552960\n"
      "s_cmp_ge_u32 s62, 7\n"
      "s_cselect_b32 s63, s63, s31\n"
      "s_add_u32 m0, s42, 86016\n"
      "s_waitcnt vmcnt(18) lgkmcnt(0)\n"
      "s_barrier\n"
      "ds_read_b64_tr_b16 a[32:33], v176 offset:0\n"
      "ds_read_b64_tr_b16 a[34:35], v176 offset:1536\n"
      "ds_read_b64_tr_b16 a[128:129], v176 offset:8\n"
      "ds_read_b64_tr_b16 a[130:131], v176 offset:1544\n"
      "ds_read_b64_tr_b16 a[224:225], v177 offset:0\n"
      "ds_read_b64_tr_b16 a[226:227], v177 offset:1536\n"
      "buffer_load_dwordx4 v178, s[12:15], s63 offen offset:0 lds\n"
      "buffer_load_dwordx4 v179, s[12:15], s63 offen offset:1024 lds\n"
      "buffer_load_dwordx4 v180, s[12:15], s63 offen offset:2048 lds\n"
      "s_sub_u32 s62, s62, 1\n"
      ".Ld_rs9:\n"
      "s_add_u32 s63, s44, 589824\n"
      "s_cmp_ge_u32 s62, 7\n"
      "s_cselect_b32 s63, s63, s31\n"
      "s_add_u32 m0, s42, 0\n"
      "s_waitcnt vmcnt(18) lgkmcnt(0)\n"
      "s_barrier\n"
      "ds_read_b64_tr_b16 a[36:37], v176 offset:12288\n"
      "ds_read_b64_tr_b16 a[38:39], v176 offset:13824\n"
      "ds_read_b64_tr_b16 a[132:133], v176 offset:12296\n"
      "ds_read_b64_tr_b16 a[134:135], v176 offset:13832\n"
      "ds_read_b64_tr_b16 a[228:229], v177 offset:12288\n"
      "ds_read_b64_tr_b16 a[230:231], v177 offset:13824\n"
      "buffer_load_dwordx4 v178, s[12:15], s63 offen offset:0 lds\n"
      "buffer_load_dwordx4 v179, s[12:15], s63 offen offset:1024 lds\n"
      "buffer_load_dwordx4 v180, s[12:15], s63 offen offset:2048 lds\n"
      "s_sub_u32 s62, s62, 1\n"
      ".Ld_rs10:\n"
      "s_add_u32 s63, s44, 626688\n"
      "s_cmp_ge_u32 s62, 7\n"
      "s_cselect_b32 s63, s63, s31\n"
      "s_add_u32 m0, s42, 12288\n"
      "s_waitcnt vmcnt(18) lgkmcnt(0)\n"
      "s_barrier\n"
      "ds_read_b64_tr_b16 a[40:41], v176 offset:24576\n"
      "ds_read_b64_tr_b16 a[42:43], v176 offset:26112\n"
      "ds_read_b64_tr_b16 a[136:137], v176 offset:24584\n"
      "ds_read_b64_tr_b16 a[138:139], v176 offset:26120\n"
      "ds_read_b64_tr_b16 a[232:233], v177 offset:24576\n"
      "ds_read_b64_tr_b16 a[234:235], v177 offset:26112\n"
      "buffer_load_dwordx4 v178, s[12:15], s63 offen offset:0 lds\n"
      "buffer_load_dwordx4 v179, s[12:15], s63 offen offset:1024 lds\n"
      "buffer_load_dwordx4 v180, s[12:15], s63 offen offset:2048 lds\n"
      "s_sub_u32 s62, s62, 1\n"
      ".Ld_rs11:\n"
      "s_add_u32 s63, s44, 663552\n"
      "s_cmp_ge_u32 s62, 7\n"
      "s_cselect_b32 s63, s63, s31\n"
      "s_add_u32 m0, s42, 24576\n"
      "s_waitcnt vmcnt(18) lgkmcnt(0)\n"
      "s_barrier\n"
      "ds_read_b64_tr_b16 a[44:45], v176 offset:36864\n"
      "ds_read_b64_tr_b16 a[46:47], v176 offset:38400\n"
      "ds_read_b64_tr_b16 a[140:141], v176 offset:36872\n"
      "ds_read_b64_tr_b16 a[142:143], v176 offset:38408\n"
      "ds_read_b64_tr_b16 a[236:237], v177 offset:36864\n"
      "ds_read_b64_tr_b16 a[238:239], v177 offset:38400\n"
      "buffer_load_dwordx4 v178, s[12:15], s63 offen offset:0 lds\n"
      "buffer_load_dwordx4 v179, s[12:15], s63 offen offset:1024 lds\n"
      "buffer_load_dwordx4 v180, s[12:15], s63 offen offset:2048 lds\n"
      "s_sub_u32 s62, s62, 1\n"
      "s_cbranch_scc1 .Ld_re\n"
      ".Ld_rs12:\n"
      "s_add_u32 s63, s44, 700416\n"
      "s_cmp_ge_u32 s62, 7\n"
      "s_cselect_b32 s63, s63, s31\n"
      "s_add_u32 m0, s42, 36864\n"
      "s_waitcnt vmcnt(18) lgkmcnt(0)\n"
      "s_barrier\n"
      "ds_read_b64_tr_b16 a[48:49], v20 offset:0\n"
      "ds_read_b64_tr_b16 a[50:51], v20 offset:1536\n"
      "ds_read_b64_tr_b16 a[144:145], v20 offset:8\n"
      "ds_read_b64_tr_b16 a[146:147], v20 offset:1544\n"
      "ds_read_b64_tr_b16 a[240:241], v21 offset:0\n"
      "ds_read_b64_tr_b16 a[242:243], v21 offset:1536\n"
      "buffer_load_dwordx4 v178, s[12:15], s63 offen offset:0 lds\n"
      "buffer_load_dwordx4 v179, s[12:15], s63 offen offset:1024 lds\n"
      "buffer_load_dwordx4 v180, s[12:15], s63 offen offset:2048 lds\n"
      "s_sub_u32 s62, s62, 1\n"
      ".Ld_rs13:\n"
      "s_add_u32 s63, s44, 737280\n"
      "s_cmp_ge_u32 s62, 7\n"
      "s_cselect_b32 s63, s63, s31\n"
      "s_add_u32 m0, s42, 49152\n"
      "s_waitcnt vmcnt(18) lgkmcnt(0)\n"
      "s_barrier\n"
      "ds_read_b64_tr_b16 a[52:53], v20 offset:12288\n"
      "ds_read_b64_tr_b16 a[54:55], v20 offset:13824\n"
      "ds_read_b64_tr_b16 a[148:149], v20 offset:12296\n"
      "ds_read_b64_tr_b16 a[150:151], v20 offset:13832\n"
      "ds_read_b64_tr_b16 a[244:245], v21 offset:12288\n"
      "ds_read_b64_tr_b16 a[246:247], v21 offset:13824\n"
      "buffer_load_dwordx4 v178, s[12:15], s63 offen offset:0 lds\n"
      "buffer_load_dwordx4 v179, s[12:15], s63 offen offset:1024 lds\n"
      "buffer_load_dwordx4 v180, s[12:15], s63 offen offset:2048 lds\n"
      "s_sub_u32 s62, s62, 1\n"
      ".Ld_rs14:\n"
      "s_add_u32 s63, s44, 774144\n"
      "s_cmp_ge_u32 s62, 7\n"
      "s_cselect_b32 s63, s63, s31\n"
      "s_add_u32 m0, s42, 61440\n"
      "s_waitcnt vmcnt(18) lgkmcnt(0)\n"
      "s_barrier\n"
      "ds_read_b64_tr_b16 a[56:57], v20 offset:24576\n"
      "ds_read_b64_tr_b16 a[58:59], v20 offset:26112\n"
      "ds_read_b64_tr_b16 a[152:153], v20 offset:24584\n"
      "ds_read_b64_tr_b16 a[154:155], v20 offset:26120\n"
      "ds_read_b64_tr_b16 a[248:249], v21 offset:24576\n"
      "ds_read_b64_tr_b16 a[250:251], v21 offset:26112\n"
      "buffer_load_dwordx4 v178, s[12:15], s63 offen offset:0 lds\n"
      "buffer_load_dwordx4 v179, s[12:15], s63 offen offset:1024 lds\n"
      "buffer_load_dwordx4 v180, s[12:15], s63 offen offset:2048 lds\n"
      "s_sub_u32 s62, s62, 1\n"
      ".Ld_rs15:\n"
      "s_add_u32 s63, s44, 811008\n"
      "s_cmp_ge_u32 s62, 7\n"
      "s_cselect_b32 s63, s63, s31\n"
      "s_add_u32 m0, s42, 73728\n"
      "s_waitcnt vmcnt(18) lgkmcnt(0)\n"
      "s_barrier\n"
      "ds_read_b64_tr_b16 a[60:61], v20 offset:36864\n"
      "ds_read_b64_tr_b16 a[62:63], v20 offset:38400\n"
      "ds_read_b64_tr_b16 a[156:157], v20 offset:36872\n"
      "ds_read_b64_tr_b16 a[158:159], v20 offset:38408\n"
      "ds_read_b64_tr_b16 a[252:253], v21 offset:36864\n"
      "ds_read_b64_tr_b16 a[254:255], v21 offset:38400\n"
      "buffer_load_dwordx4 v178, s[12:15], s63 offen offset:0 lds\n"
      "buffer_load_dwordx4 v179, s[12:15], s63 offen offset:1024 lds\n"
      "buffer_load_dwordx4 v180, s[12:15], s63 offen offset:2048 lds\n"
      "s_sub_u32 s62, s62, 1\n"
      ".Ld_rs16:\n"
      "s_add_u32 s63, s44, 847872\n"
      "s_cmp_ge_u32 s62, 7\n"
      "s_cselect_b32 s63, s63, s31\n"
      "s_add_u32 m0, s42, 86016\n"
      "s_waitcnt vmcnt(18) lgkmcnt(0)\n"
      "s_barrier\n"
      "ds_read_b64_tr_b16 a[64:65], v176 offset:0\n"
      "ds_read_b64_tr_b16 a[66:67], v176 offset:1536\n"
      "ds_read_b64_tr_b16 a[160:161], v176 offset:8\n"
      "ds_read_b64_tr_b16 a[162:163], v176 offset:1544\n"
      "ds_read_b64_tr_b16 v[24:25], v177 offset:0\n"
      "ds_read_b64_tr_b16 v[26:27], v177 offset:1536\n"
      "buffer_load_dwordx4 v178, s[12:15], s63 offen offset:0 lds\n"
      "buffer_load_dwordx4 v179, s[12:15], s63 offen offset:1024 lds\n"
      "buffer_load_dwordx4 v180, s[12:15], s63 offen offset:2048 lds\n"
      "s_sub_u32 s62, s62, 1\n"
      ".Ld_rs17:\n"
      "s_add_u32 s63, s44, 0\n"
      "s_cmp_ge_u32 s62, 7\n"
      "s_cselect_b32 s63, s63, s31\n"
      "s_add_u32 m0, s42, 0\n"
      "s_waitcnt vmcnt(18) lgkmcnt(0)\n"
      "s_barrier\n"
      "ds_read_b64_tr_b16 a[68:69], v176 offset:12288\n"
      "ds_read_b64_tr_b16 a[70:71], v176 offset:13824\n"
      "ds_read_b64_tr_b16 a[164:165], v176 offset:12296\n"
      "ds_read_b64_tr_b16 a[166:167], v176 offset:13832\n"
      "ds_read_b64_tr_b16 v[28:29], v177 offset:12288\n"
      "ds_read_b64_tr_b16 v[30:31], v177 offset:13824\n"
      "buffer_load_dwordx4 v178, s[12:15], s63 offen offset:0 lds\n"
      "buffer_load_dwordx4 v179, s[12:15], s63 offen offset:1024 lds\n"
      "buffer_load_dwordx4 v180, s[12:15], s63 offen offset:2048 lds\n"
      "s_sub_u32 s62, s62, 1\n"
      "s_cbranch_scc1 .Ld_re\n"
      ".Ld_rs18:\n"
      "s_add_u32 s63, s44, 36864\n"
      "s_cmp_ge_u32 s62, 7\n"
      "s_cselect_b32 s63, s63, s31\n"
      "s_add_u32 m0, s42, 12288\n"
      "s_waitcnt vmcnt(18) lgkmcnt(0)\n"
      "s_barrier\n"
      "ds_read_b64_tr_b16 a[72:73], v176 offset:24576\n"
      "ds_read_b64_tr_b16 a[74:75], v176 offset:26112\n"
      "ds_read_b64_tr_b16 a[168:169], v176 offset:24584\n"
      "ds_read_b64_tr_b16 a[170:171], v176 offset:26120\n"
      "ds_read_b64_tr_b16 v[32:33], v177 offset:24576\n"
      "ds_read_b64_tr_b16 v[34:35], v177 offset:26112\n"
      "buffer_load_dwordx4 v178, s[12:15], s63 offen offset:0 lds\n"
      "buffer_load_dwordx4 v179, s[12:15], s63 offen offset:1024 lds\n"
      "buffer_load_dwordx4 v180, s[12:15], s63 offen offset:2048 lds\n"
      "s_sub_u32 s62, s62, 1\n"
      ".Ld_rs19:\n"
      "s_add_u32 s63, s44, 73728\n"
      "s_cmp_ge_u32 s62, 7\n"
      "s_cselect_b32 s63, s63, s31\n"
      "s_add_u32 m0, s42, 24576\n"
      "s_waitcnt vmcnt(18) lgkmcnt(0)\n"
      "s_barrier\n"
      "ds_read_b64_tr_b16 a[76:77], v176 offset:36864\n"
      "ds_read_b64_tr_b16 a[78:79], v176 offset:38400\n"
      "ds_read_b64_tr_b16 a[172:173], v176 offset:36872\n"
      "ds_read_b64_tr_b16 a[174:175], v176 offset:38408\n"
      "ds_read_b64_tr_b16 v[36:37], v177 offset:36864\n"
      "ds_read_b64_tr_b16 v[38:39], v177 offset:38400\n"
      "buffer_load_dwordx4 v178, s[12:15], s63 offen offset:0 lds\n"
      "buffer_load_dwordx4 v179, s[12:15], s63 offen offset:1024 lds\n"
      "buffer_load_dwordx4 v180, s[12:15], s63 offen offset:2048 lds\n"
      "s_sub_u32 s62, s62, 1\n"
      ".Ld_rs20:\n"
      "s_add_u32 s63, s44, 110592\n"
      "s_cmp_ge_u32 s62, 7\n"
      "s_cselect_b32 s63, s63, s31\n"
      "s_add_u32 m0, s42, 36864\n"
      "s_waitcnt vmcnt(18) lgkmcnt(0)\n"
      "s_barrier\n"
      "ds_read_b64_tr_b16 a[80:81], v20 offset:0\n"
      "ds_read_b64_tr_b16 a[82:83], v20 offset:1536\n"
      "ds_read_b64_tr_b16 a[176:177], v20 offset:8\n"
      "ds_read_b64_tr_b16 a[178:179], v20 offset:1544\n"
      "ds_read_b64_tr_b16 v[40:41], v21 offset:0\n"
      "ds_read_b64_tr_b16 v[42:43], v21 offset:1536\n"
      "buffer_load_dwordx4 v178, s[12:15], s63 offen offset:0 lds\n"
      "buffer_load_dwordx4 v179, s[12:15], s63 offen offset:1024 lds\n"
      "buffer_load_dwordx4 v180, s[12:15], s63 offen offset:2048 lds\n"
      "s_sub_u32 s62, s62, 1\n"
      ".Ld_rs21:\n"
      "s_add_u32 s63, s44, 147456\n"
      "s_cmp_ge_u32 s62, 7\n"
      "s_cselect_b32 s63, s63, s31\n"
      "s_add_u32 m0, s42, 49152\n"
      "s_waitcnt vmcnt(18) lgkmcnt(0)\n"
      "s_barrier\n"
      "ds_read_b64_tr_b16 a[84:85], v20 offset:12288\n"
      "ds_read_b64_tr_b16 a[86:87], v20 offset:13824\n"
      "ds_read_b64_tr_b16 a[180:181], v20 offset:12296\n"
      "ds_read_b64_tr_b16 a[182:183], v20 offset:13832\n"
      "ds_read_b64_tr_b16 v[44:45], v21 offset:12288\n"
      "ds_read_b64_tr_b16 v[46:47], v21 offset:13824\n"
      "buffer_load_dwordx4 v178, s[12:15], s63 offen offset:0 lds\n"
      "buffer_load_dwordx4 v179, s[12:15], s63 offen offset:1024 lds\n"
      "buffer_load_dwordx4 v180, s[12:15], s63 offen offset:2048 lds\n"
      "s_sub_u32 s62, s62, 1\n"
      ".Ld_rs22:\n"
      "s_add_u32 s63, s44, 184320\n"
      "s_cmp_ge_u32 s62, 7\n"
      "s_cselect_b32 s63, s63, s31\n"
      "s_add_u32 m0, s42, 61440\n"
      "s_waitcnt vmcnt(18) lgkmcnt(0)\n"
      "s_barrier\n"
      "ds_read_b64_tr_b16 a[88:89], v20 offset:24576\n"
      "ds_read_b64_tr_b16 a[90:91], v20 offset:26112\n"
      "ds_read_b64_tr_b16 a[184:185], v20 offset:24584\n"
      "ds_read_b64_tr_b16 a[186:187], v20 offset:26120\n"
      "ds_read_b64_tr_b16 v[48:49], v21 offset:24576\n"
      "ds_read_b64_tr_b16 v[50:51], v21 offset:26112\n"
      "buffer_load_dwordx4 v178, s[12:15], s63 offen offset:0 lds\n"
      "buffer_load_dwordx4 v179, s[12:15], s63 offen offset:1024 lds\n"
      "buffer_load_dwordx4 v180, s[12:15], s63 offen offset:2048 lds\n"
      "s_sub_u32 s62, s62, 1\n"
      ".Ld_rs23:\n"
      "s_add_u32 s63, s44, 221184\n"
      "s_cmp_ge_u32 s62, 7\n"
      "s_cselect_b32 s63, s63, s31\n"
      "s_add_u32 m0, s42, 73728\n"
      "s_waitcnt vmcnt(18) lgkmcnt(0)\n"
      "s_barrier\n"
      "ds_read_b64_tr_b16 a[92:93], v20 offset:36864\n"
      "ds_read_b64_tr_b16 a[94:95], v20 offset:38400\n"
      "ds_read_b64_tr_b16 a[188:189], v20 offset:36872\n"
      "ds_read_b64_tr_b16 a[190:191], v20 offset:38408\n"
      "ds_read_b64_tr_b16 v[52:53], v21 offset:36864\n"
      "ds_read_b64_tr_b16 v[54:55], v21 offset:38400\n"
      "buffer_load_dwordx4 v178, s[12:15], s63 offen offset:0 lds\n"
      "buffer_load_dwordx4 v179, s[12:15], s63 offen offset:1024 lds\n"
      "buffer_load_dwordx4 v180, s[12:15], s63 offen offset:2048 lds\n"
      "s_sub_u32 s62, s62, 1\n"
      "s_cbranch_scc0 .Ld_rs0\n"
      ".Ld_re:\n"
      "s_waitcnt vmcnt(0) lgkmcnt(0)\n"
      "v_add_u32 v22, 49152, v0\n"
      "v_add_u32 v23, 98304, v0\n"
      "s_lshr_b32 s58, s42, 10\n"
      "s_mul_i32 s58, s58, 1366\n"
      "s_and_b32 s58, s58, 0x7000\n"
      "s_getpc_b64 vcc\n"
      ".Ld_ct:\n"
      "s_add_u32 vcc_lo, vcc_lo, .Ld_p0-.Ld_ct\n"
      "s_addc_u32 vcc_hi, vcc_hi, 0\n"
      "s_lshl_b32 s30, s58, 1\n"
      "v_lshl_add_u32 v56, v0, 3, s30\n"
      "v_min_u32 v56, .Ld_last-.Ld_p0-4, v56\n"
      "global_load_dword v56, v56, vcc\n"
      "s_waitcnt vmcnt(0) lgkmcnt(0)\n"
      "s_mov_b64 exec, 0\n"
      "s_lshr_b32 s22, s58, 12\n"
      ".Ld_pl:\n"
      "s_cmp_eq_u32 s22, 0\n"
      "s_cbranch_scc1 .Ld_p0\n"
      "s_cmp_eq_u32 s22, 1\n"
      "s_cbranch_scc1 .Ld_p1\n"
      "s_branch .Ld_pe\n"
      ".Ld_pr:\n"
      "s_add_u32 s22, s22, 4\n"
      "s_branch .Ld_pl\n"
      ".Ld_pe:\n"
      "s_mov_b64 exec, -1\n"
      "s_barrier\n"
      "v_mov_b32 v14, 0\n"
      "ds_write_b32 v16, v14\n"
      "s_waitcnt lgkmcnt(0)\n"
      "s_barrier\n"
      "s_mov_b32 s6, s50\n"
      "s_add_u32 s36, s36, 4294801408\n"
      "s_add_u32 s53, s37, 0\n"
      "s_add_u32 s52, s37, 4294787584\n"
      "s_add_u32 s37, s37, 4294894080\n"
      "s_mov_b32 s56, 0x40000000\n"
      "s_mov_b32 s57, 0x40000000\n"
      "s_mov_b32 s59, 63\n"
      "s_cmp_eq_u32 s45, 1\n"
      "s_cbranch_scc1 .Ld_in1\n"
      "s_cmp_eq_u32 s45, 2\n"
      "s_cbranch_scc1 .Ld_in2\n"
      "s_cmp_eq_u32 s45, 3\n"
      "s_cbranch_scc1 .Ld_in3\n"
      "s_cmp_eq_u32 s45, 4\n"
      "s_cbranch_scc1 .Ld_in4\n"
      "s_cmp_eq_u32 s45, 5\n"
      "s_cbranch_scc1 .Ld_in5\n"
      "s_add_u32 s36, s36, 165888\n"
      "s_min_u32 s36, s36, s6\n"
      "buffer_load_dword v20, v2, s[8:11], s36 offen sc1\n"
      "s_add_u32 m0, s43, 0\n"
      "s_mov_b32 s51, s36\n"
      "s_add_u32 s36, s36, 165888\n"
      "s_min_u32 s36, s36, s6\n"
      "s_waitcnt vmcnt(0)\n"
      "v_cmp_ne_u32 vcc, s21, v20\n"
      "s_cbranch_vccnz .Lsp_d0_1\n"
      ".Lbk_d0_1:\n"
      "buffer_load_dwordx4 v0, s[8:11], s51 offen offset:0 sc1 lds\n"
      "buffer_load_dwordx4 v0, s[8:11], s51 offen offset:1024 sc1 lds\n"
      "buffer_load_dword v20, v2, s[8:11], s36 offen sc1\n"
      "buffer_load_dwordx4 v0, s[8:11], s51 offen offset:2048 sc1 lds\n"
      "buffer_load_dwordx4 v0, s[8:11], s51 offen offset:3072 sc1 lds\n"
      "s_add_u32 m0, s43, 4096\n"
      "s_nop 0\n"
      "buffer_load_dwordx4 v1, s[8:11], s51 offen offset:1024 sc1 lds\n"
      "buffer_load_dwordx4 v1, s[8:11], s51 offen offset:0 sc1 lds\n"
      "s_mov_b32 s51, s36\n"
      "s_add_u32 m0, s43, 24576\n"
      "v_add_u32 v14, 1, v14\n"
      "s_add_u32 s36, s36, 165888\n"
      "buffer_load_dwordx4 v[216:219], v6, s[8:11], s38 offen sc1\n"
      "s_waitcnt vmcnt(5)\n"
      "v_cmp_ne_u32 vcc, s21, v20\n"
      "buffer_load_dwordx2 v[220:221], v7, s[8:11], s38 offen sc1\n"
      "s_cbranch_vccnz .Lsp_d1_2\n"
      ".Lbk_d1_2:\n"
      "buffer_load_dwordx4 v0, s[8:11], s51 offen offset:0 sc1 lds\n"
      "buffer_load_dwordx4 v0, s[8:11], s51 offen offset:1024 sc1 lds\n"
      "s_waitcnt vmcnt(4)\n"
      "ds_write_b32 v16, v14\n"
      "buffer_load_dwordx4 v0, s[8:11], s51 offen offset:2048 sc1 lds\n"
      "s_min_u32 s36, s36, s6\n"
      "buffer_load_dword v20, v2, s[8:11], s36 offen sc1\n"
      "buffer_load_dwordx4 v0, s[8:11], s51 offen offset:3072 sc1 lds\n"
      "ds_read_b32 v15, v17\n"
      "s_add_u32 m0, s43, 28672\n"
      "s_nop 0\n"
      "buffer_load_dwordx4 v1, s[8:11], s51 offen offset:0 sc1 lds\n"
      "s_waitcnt lgkmcnt(0)\n"
      "v_cmp_gt_i32 vcc, v14, v15\n"
      "buffer_load_dwordx4 v1, s[8:11], s51 offen offset:1024 sc1 lds\n"
      "s_cbranch_vccnz .Lsp_dy0_3\n"
      ".Lbk_dy0_3:\n"
      "ds_read_b128 v[56:59], v0 offset:0\n"
      "ds_read_b128 v[60:63], v0 offset:1024\n"
      "ds_read_b128 v[84:87], v0 offset:7168\n"
      "ds_read_b128 v[72:75], v0 offset:4096\n"
      "ds_read_b128 v[88:91], v0 offset:8192\n"
      "ds_read_b128 v[68:71], v0 offset:3072\n"
      "ds_read_b128 v[104:107], v0 offset:12288\n"
      "ds_read_b128 v[96:99], v0 offset:10240\n"
      "ds_read_b128 v[80:83], v0 offset:6144\n"
      "ds_read_b128 v[64:67], v0 offset:2048\n"
      "ds_read_b128 v[92:95], v0 offset:9216\n"
      "ds_read_b128 v[76:79], v0 offset:5120\n"
      "ds_read_b128 v[132:135], v0 offset:19456\n"
      "ds_read_b128 v[100:103], v0 offset:11264\n"
      "s_waitcnt vmcnt(0) lgkmcnt(0)\n"
      "s_nop 4\n"
      "s_branch .Ld_t0\n"
      ".p2align 12\n"
      ".Ld_p0:\n"
      "s_branch .Ld_pr\n"
      ".p2align 5\n"
      ".Ld_t0:\n"
      "v_mfma_f32_16x16x32_bf16 v[152:155], a[0:3], v[56:59], 0\n"
      "ds_read_b128 v[108:111], v0 offset:13312\n"
      "ds_read_b128 v[120:123], v0 offset:16384\n"
      "v_mfma_f32_16x16x32_bf16 v[152:155], a[4:7], v[60:63], v[152:155]\n"
      "s_waitcnt lgkmcnt(6)\n"
      "ds_read_b128 v[116:119], v0 offset:15360\n"
      "v_add_f32 v236, v236, v168\n"
      "v_mfma_f32_16x16x32_bf16 v[152:155], a[8:11], v[64:67], v[152:155]\n"
      "v_cvt_pk_bf16_f32 v228, v160, v161\n"
      "v_cvt_pk_bf16_f32 v229, v162, v163\n"
      "v_mfma_f32_16x16x32_bf16 v[152:155], a[12:15], v[68:71], v[152:155]\n"
      "buffer_store_dwordx2 v[228:229], v13, s[8:11], s37 offen sc1\n"
      "v_mfma_f32_16x16x32_bf16 v[152:155], a[16:19], v[72:75], v[152:155]\n"
      "s_waitcnt lgkmcnt(4)\n"
      "v_cvt_pk_bf16_f32 v178, v168, v169\n"
      "v_add_f32 v175, v175, v213\n"
      "v_mfma_f32_16x16x32_bf16 v[152:155], a[20:23], v[76:79], v[152:155]\n"
      "ds_read_b128 v[112:115], v0 offset:14336\n"
      "v_add_f32 v171, v171, v211\n"
      "v_mfma_f32_16x16x32_bf16 v[152:155], a[24:27], v[80:83], v[152:155]\n"
      "ds_read_b128 v[124:127], v0 offset:17408\n"
      "v_add_f32 v236, v236, v169\n"
      "v_mfma_f32_16x16x32_bf16 v[152:155], a[28:31], v[84:87], v[152:155]\n"
      "v_add_f32 v173, v173, v204\n"
      "v_cvt_pk_bf16_f32 v179, v170, v171\n"
      "v_mfma_f32_16x16x32_bf16 v[152:155], a[32:35], v[88:91], v[152:155]\n"
      "ds_read_b128 v[128:131], v0 offset:18432\n"
      "v_add_f32 v173, v173, v212\n"
      "v_mfma_f32_16x16x32_bf16 v[152:155], a[36:39], v[92:95], v[152:155]\n"
      "buffer_store_dwordx4 v[176:179], v6, s[8:11], s56 offen sc1\n"
      "v_mfma_f32_16x16x32_bf16 v[152:155], a[40:43], v[96:99], v[152:155]\n"
      "s_waitcnt lgkmcnt(3)\n"
      "ds_read_b128 v[148:151], v0 offset:23552\n"
      "v_cvt_pk_bf16_f32 v180, v172, v173\n"
      "v_mfma_f32_16x16x32_bf16 v[152:155], a[44:47], v[100:103], v[152:155]\n"
      "ds_read_b128 v[144:147], v0 offset:22528\n"
      "v_fmac_f32 v237, v168, v168\n"
      "v_mfma_f32_16x16x32_bf16 v[152:155], a[48:51], v[104:107], v[152:155]\n"
      "s_add_u32 m0, s43, 49152\n"
      "ds_read_b128 v[136:139], v0 offset:20480\n"
      "v_fmac_f32 v237, v169, v169\n"
      "v_mfma_f32_16x16x32_bf16 v[152:155], a[52:55], v[108:111], v[152:155]\n"
      "s_waitcnt lgkmcnt(4)\n"
      "s_mov_b32 s51, s36\n"
      "ds_read_b128 v[140:143], v0 offset:21504\n"
      "v_mfma_f32_16x16x32_bf16 v[152:155], a[56:59], v[112:115], v[152:155]\n"
      "v_cvt_pk_bf16_f32 v181, v174, v175\n"
      "v_fmac_f32 v237, v170, v170\n"
      "v_mfma_f32_16x16x32_bf16 v[152:155], a[60:63], v[116:119], v[152:155]\n"
      "buffer_store_dwordx2 v[180:181], v7, s[8:11], s56 offen sc1\n"
      "v_mfma_f32_16x16x32_bf16 v[152:155], a[64:67], v[120:123], v[152:155]\n"
      "v_add_u32 v14, 1, v14\n"
      "v_mfma_f32_16x16x32_bf16 v[152:155], a[68:71], v[124:127], v[152:155]\n"
      "s_waitcnt lgkmcnt(3)\n"
      "v_add_f32 v236, v236, v170\n"
      "v_fmac_f32 v237, v171, v171\n"
      "v_mfma_f32_16x16x32_bf16 v[152:155], a[72:75], v[128:131], v[152:155]\n"
      "v_fmac_f32 v237, v172, v172\n"
      "v_fmac_f32 v237, v173, v173\n"
      "v_mfma_f32_16x16x32_bf16 v[152:155], a[76:79], v[132:135], v[152:155]\n"
      "s_waitcnt lgkmcnt(1)\n"
      "buffer_store_dwordx2 v[180:181], v9, s[16:19], s57 offen\n"
      "v_mfma_f32_16x16x32_bf16 v[152:155], a[80:83], v[136:139], v[152:155]\n"
      "s_waitcnt lgkmcnt(0)\n"
      "v_add_f32 v236, v236, v171\n"
      "v_fmac_f32 v237, v174, v174\n"
      "v_mfma_f32_16x16x32_bf16 v[152:155], a[84:87], v[140:143], v[152:155]\n"
      "v_add_f32 v236, v236, v172\n"
      "v_fmac_f32 v237, v175, v175\n"
      "v_mfma_f32_16x16x32_bf16 v[152:155], a[88:91], v[144:147], v[152:155]\n"
      "v_add_f32 v236, v236, v173\n"
      "v_add_f32 v236, v236, v174\n"
      "v_mfma_f32_16x16x32_bf16 v[152:155], a[92:95], v[148:151], v[152:155]\n"
      "buffer_store_dwordx4 v[176:179], v8, s[16:19], s57 offen\n"
      "v_mfma_f32_16x16x32_bf16 v[156:159], a[96:99], v[56:59], 0\n"
      "s_waitcnt vmcnt(15)\n"
      "v_add_f32 v236, v236, v175\n"
      "v_lshlrev_b32 v166, 16, v217\n"
      "v_mfma_f32_16x16x32_bf16 v[156:159], a[100:103], v[60:63], v[156:159]\n"
      "s_waitcnt vmcnt(10)\n"
      "v_cmp_ne_u32 vcc, s21, v20\n"
      "v_mfma_f32_16x16x32_bf16 v[156:159], a[104:107], v[64:67], v[156:159]\n"
      "ds_write_b64 v10, v[236:237]\n"
      "v_mfma_f32_16x16x32_bf16 v[156:159], a[108:111], v[68:71], v[156:159]\n"
      "s_add_u32 s36, s36, 165888\n"
      "v_mfma_f32_16x16x32_bf16 v[156:159], a[112:115], v[72:75], v[156:159]\n"
      "s_min_u32 s36, s36, s6\n"
      "s_cbranch_vccnz .Lsp_d2_4\n"
      ".Lbk_d2_4:\n"
      "v_mfma_f32_16x16x32_bf16 v[156:159], a[116:119], v[76:79], v[156:159]\n"
      "s_waitcnt vmcnt(7)\n"
      "ds_write_b32 v16, v14\n"
      "v_mfma_f32_16x16x32_bf16 v[156:159], a[120:123], v[80:83], v[156:159]\n"
      "buffer_load_dword v20, v2, s[8:11], s36 offen sc1\n"
      "v_mfma_f32_16x16x32_bf16 v[156:159], a[124:127], v[84:87], v[156:159]\n"
      "v_and_b32 v167, 0xffff0000, v217\n"
      "v_lshlrev_b32 v164, 16, v216\n"
      "v_mfma_f32_16x16x32_bf16 v[156:159], a[128:131], v[88:91], v[156:159]\n"
      "v_mfma_f32_16x16x32_bf16 v[156:159], a[132:135], v[92:95], v[156:159]\n"
      "buffer_load_dwordx4 v0, s[8:11], s51 offen offset:3072 sc1 lds\n"
      "v_mfma_f32_16x16x32_bf16 v[156:159], a[136:139], v[96:99], v[156:159]\n"
      "s_add_u32 s52, s52, 106496\n"
      "v_mfma_f32_16x16x32_bf16 v[156:159], a[140:143], v[100:103], v[156:159]\n"
      "v_mfma_f32_16x16x32_bf16 v[156:159], a[144:147], v[104:107], v[156:159]\n"
      "s_add_u32 s37, s37, 173056\n"
      "v_and_b32 v165, 0xffff0000, v216\n"
      "v_mfma_f32_16x16x32_bf16 v[156:159], a[148:151], v[108:111], v[156:159]\n"
      "buffer_load_dwordx4 v0, s[8:11], s51 offen offset:1024 sc1 lds\n"
      "v_mfma_f32_16x16x32_bf16 v[156:159], a[152:155], v[112:115], v[156:159]\n"
      "ds_read_b32 v15, v17\n"
      "v_mfma_f32_16x16x32_bf16 v[156:159], a[156:159], v[116:119], v[156:159]\n"
      "v_mfma_f32_16x16x32_bf16 v[156:159], a[160:163], v[120:123], v[156:159]\n"
      "v_lshlrev_b32 v170, 16, v219\n"
      "v_mfma_f32_16x16x32_bf16 v[156:159], a[164:167], v[124:127], v[156:159]\n"
      "buffer_load_dwordx4 v0, s[8:11], s51 offen offset:2048 sc1 lds\n"
      "v_mfma_f32_16x16x32_bf16 v[156:159], a[168:171], v[128:131], v[156:159]\n"
      "v_mfma_f32_16x16x32_bf16 v[156:159], a[172:175], v[132:135], v[156:159]\n"
      "s_waitcnt lgkmcnt(0)\n"
      "v_cmp_gt_i32 vcc, v14, v15\n"
      "v_mfma_f32_16x16x32_bf16 v[156:159], a[176:179], v[136:139], v[156:159]\n"
      "v_add_f32 v165, v165, v153\n"
      "v_mfma_f32_16x16x32_bf16 v[156:159], a[180:183], v[140:143], v[156:159]\n"
      "buffer_load_dwordx4 v0, s[8:11], s51 offen offset:0 sc1 lds\n"
      "v_mfma_f32_16x16x32_bf16 v[156:159], a[184:187], v[144:147], v[156:159]\n"
      "v_add_f32 v164, v164, v152\n"
      "s_add_u32 m0, s43, 53248\n"
      "v_mfma_f32_16x16x32_bf16 v[156:159], a[188:191], v[148:151], v[156:159]\n"
      "s_cbranch_vccnz .Lsp_dy1_5\n"
      ".Lbk_dy1_5:\n"
      "ds_read_b64 v[238:239], v11\n"
      "v_mfma_f32_16x16x32_bf16 v[160:163], a[192:195], v[56:59], 0\n"
      "v_lshlrev_b32 v168, 16, v218\n"
      "v_mfma_f32_16x16x32_bf16 v[160:163], a[196:199], v[60:63], v[160:163]\n"
      "buffer_load_dwordx4 v1, s[8:11], s51 offen offset:0 sc1 lds\n"
      "v_mfma_f32_16x16x32_bf16 v[160:163], a[200:203], v[64:67], v[160:163]\n"
      "v_mfma_f32_16x16x32_bf16 v[160:163], a[204:207], v[68:71], v[160:163]\n"
      "ds_read_b128 v[56:59], v0 offset:24576\n"
      "v_mfma_f32_16x16x32_bf16 v[160:163], a[208:211], v[72:75], v[160:163]\n"
      "ds_read_b128 v[64:67], v0 offset:26624\n"
      "v_mfma_f32_16x16x32_bf16 v[160:163], a[212:215], v[76:79], v[160:163]\n"
      "buffer_load_dwordx4 v1, s[8:11], s51 offen offset:1024 sc1 lds\n"
      "v_mfma_f32_16x16x32_bf16 v[160:163], a[216:219], v[80:83], v[160:163]\n"
      "s_waitcnt lgkmcnt(2)\n"
      "ds_read_b128 v[68:71], v0 offset:27648\n"
      "v_add_f32_dpp v238, v238, v238 row_ror:8 row_mask:0xf bank_mask:0xf\n"
      "v_mfma_f32_16x16x32_bf16 v[160:163], a[220:223], v[84:87], v[160:163]\n"
      "ds_read_b128 v[80:83], v0 offset:30720\n"
      "v_add_f32_dpp v238, v238, v238 row_ror:4 row_mask:0xf bank_mask:0xf\n"
      "v_mfma_f32_16x16x32_bf16 v[160:163], a[224:227], v[88:91], v[160:163]\n"
      "ds_read_b128 v[60:63], v0 offset:25600\n"
      "v_add_f32_dpp v238, v238, v238 row_ror:2 row_mask:0xf bank_mask:0xf\n"
      "v_mfma_f32_16x16x32_bf16 v[160:163], a[228:231], v[92:95], v[160:163]\n"
      "ds_read_b128 v[92:95], v0 offset:33792\n"
      "v_add_f32_dpp v239, v239, v239 row_ror:8 row_mask:0xf bank_mask:0xf\n"
      "v_mfma_f32_16x16x32_bf16 v[160:163], a[232:235], v[96:99], v[160:163]\n"
      "ds_read_b128 v[84:87], v0 offset:31744\n"
      "v_add_f32_dpp v239, v239, v239 row_ror:4 row_mask:0xf bank_mask:0xf\n"
      "v_mfma_f32_16x16x32_bf16 v[160:163], a[236:239], v[100:103], v[160:163]\n"
      "s_waitcnt vmcnt(11) lgkmcnt(5)\n"
      "buffer_store_dword v18, v4, s[8:11], s52 offen sc1\n"
      "v_mfma_f32_16x16x32_bf16 v[160:163], a[240:243], v[104:107], v[160:163]\n"
      "ds_read_b128 v[72:75], v0 offset:28672\n"
      "v_add_f32_dpp v239, v239, v239 row_ror:2 row_mask:0xf bank_mask:0xf\n"
      "v_mfma_f32_16x16x32_bf16 v[160:163], a[244:247], v[108:111], v[160:163]\n"
      "ds_read_b128 v[76:79], v0 offset:29696\n"
      "v_add_f32_dpp v239, v239, v239 row_ror:1 row_mask:0xf bank_mask:0xf\n"
      "v_mfma_f32_16x16x32_bf16 v[160:163], a[248:251], v[112:115], v[160:163]\n"
      "v_add_f32_dpp v238, v238, v238 row_ror:1 row_mask:0xf bank_mask:0xf\n"
      "v_add_f32 v168, v168, v156\n"
      "v_mfma_f32_16x16x32_bf16 v[160:163], a[252:255], v[116:119], v[160:163]\n"
      "ds_read_b128 v[100:103], v0 offset:35840\n"
      "v_mul_f32 v238, 0x3ae38e39, v238\n"
      "v_mfma_f32_16x16x32_bf16 v[160:163], v[24:27], v[120:123], v[160:163]\n"
      "v_and_b32 v169, 0xffff0000, v218\n"
      "v_mfma_f32_16x16x32_bf16 v[160:163], v[28:31], v[124:127], v[160:163]\n"
      "ds_read_b128 v[96:99], v0 offset:34816\n"
      "v_and_b32 v175, 0xffff0000, v221\n"
      "v_mfma_f32_16x16x32_bf16 v[160:163], v[32:35], v[128:131], v[160:163]\n"
      "ds_read_b128 v[116:119], v0 offset:39936\n"
      "v_lshlrev_b32 v174, 16, v221\n"
      "v_mfma_f32_16x16x32_bf16 v[160:163], v[36:39], v[132:135], v[160:163]\n"
      "ds_read_b128 v[120:123], v0 offset:40960\n"
      "v_add_f32 v167, v167, v155\n"
      "v_mfma_f32_16x16x32_bf16 v[160:163], v[40:43], v[136:139], v[160:163]\n"
      "ds_read_b128 v[108:111], v0 offset:37888\n"
      "v_and_b32 v171, 0xffff0000, v219\n"
      "v_mfma_f32_16x16x32_bf16 v[160:163], v[44:47], v[140:143], v[160:163]\n"
      "ds_read_b128 v[88:91], v0 offset:32768\n"
      "v_add_f32 v166, v166, v154\n"
      "v_mfma_f32_16x16x32_bf16 v[160:163], v[48:51], v[144:147], v[160:163]\n"
      "v_add_f32 v239, 0x3afba882, v239\n"
      "v_mul_f32 v239, 0x3ae38e39, v239\n"
      "v_mfma_f32_16x16x32_bf16 v[160:163], v[52:55], v[148:151], v[160:163]\n"
      "s_sub_u32 s59, s59, 1\n"
      "s_cbranch_scc1 .Ld_out0\n"
      ".Ld_t1:\n"
      "v_mfma_f32_16x16x32_bf16 v[152:155], a[0:3], v[56:59], 0\n"
      "s_waitcnt lgkmcnt(4)\n"
      "buffer_store_dwordx2 v[238:239], v12, s[8:11], s56 offen sc1\n"
      "v_mfma_f32_16x16x32_bf16 v[152:155], a[4:7], v[60:63], v[152:155]\n"
      "ds_read_b128 v[104:107], v0 offset:36864\n"
      "v_mfma_f32_16x16x32_bf16 v[152:155], a[8:11], v[64:67], v[152:155]\n"
      "ds_read_b128 v[140:143], v0 offset:46080\n"
      "v_mfma_f32_16x16x32_bf16 v[152:155], a[12:15], v[68:71], v[152:155]\n"
      "ds_read_b128 v[112:115], v0 offset:38912\n"
      "v_mfma_f32_16x16x32_bf16 v[152:155], a[16:19], v[72:75], v[152:155]\n"
      "ds_read_b128 v[124:127], v0 offset:41984\n"
      "v_mfma_f32_16x16x32_bf16 v[152:155], a[20:23], v[76:79], v[152:155]\n"
      "ds_read_b128 v[132:135], v0 offset:44032\n"
      "v_mfma_f32_16x16x32_bf16 v[152:155], a[24:27], v[80:83], v[152:155]\n"
      "s_mov_b32 s51, s36\n"
      "v_add_f32 v174, v174, v162\n"
      "s_add_u32 s36, s36, 165888\n"
      "v_mfma_f32_16x16x32_bf16 v[152:155], a[28:31], v[84:87], v[152:155]\n"
      "s_waitcnt lgkmcnt(5)\n"
      "ds_read_b128 v[148:151], v0 offset:48128\n"
      "v_mfma_f32_16x16x32_bf16 v[152:155], a[32:35], v[88:91], v[152:155]\n"
      "ds_read_b128 v[128:131], v0 offset:43008\n"
      "s_add_u32 m0, s43, 73728\n"
      "v_mfma_f32_16x16x32_bf16 v[152:155], a[36:39], v[92:95], v[152:155]\n"
      "ds_read_b128 v[136:139], v0 offset:45056\n"
      "v_mfma_f32_16x16x32_bf16 v[152:155], a[40:43], v[96:99], v[152:155]\n"
      "v_mfma_f32_16x16x32_bf16 v[152:155], a[44:47], v[100:103], v[152:155]\n"
      "s_waitcnt lgkmcnt(4)\n"
      "v_add_f32 v170, v170, v158\n"
      "v_mfma_f32_16x16x32_bf16 v[152:155], a[48:51], v[104:107], v[152:155]\n"
      "v_mfma_f32_16x16x32_bf16 v[152:155], a[52:55], v[108:111], v[152:155]\n"
      "s_waitcnt vmcnt(8) lgkmcnt(3)\n"
      "v_lshlrev_b32 v172, 16, v220\n"
      "v_cmp_ne_u32 vcc, s21, v20\n"
      "v_mfma_f32_16x16x32_bf16 v[152:155], a[56:59], v[112:115], v[152:155]\n"
      "ds_read_b128 v[144:147], v0 offset:47104\n"
      "s_add_u32 s38, s38, 147456\n"
      "v_mfma_f32_16x16x32_bf16 v[152:155], a[60:63], v[116:119], v[152:155]\n"
      "s_min_u32 s36, s36, s6\n"
      "v_add_f32 v171, v171, v159\n"
      "v_mfma_f32_16x16x32_bf16 v[152:155], a[64:67], v[120:123], v[152:155]\n"
      "v_mfma_f32_16x16x32_bf16 v[152:155], a[68:71], v[124:127], v[152:155]\n"
      "s_waitcnt lgkmcnt(1)\n"
      "v_mfma_f32_16x16x32_bf16 v[152:155], a[72:75], v[128:131], v[152:155]\n"
      "v_add_f32 v169, v169, v157\n"
      "v_add_f32 v172, v172, v160\n"
      "v_mfma_f32_16x16x32_bf16 v[152:155], a[76:79], v[132:135], v[152:155]\n"
      "v_mfma_f32_16x16x32_bf16 v[152:155], a[80:83], v[136:139], v[152:155]\n"
      "s_cbranch_vccnz .Lsp_d3_6\n"
      ".Lbk_d3_6:\n"
      "v_mfma_f32_16x16x32_bf16 v[152:155], a[84:87], v[140:143], v[152:155]\n"
      "s_waitcnt lgkmcnt(0)\n"
      "v_mfma_f32_16x16x32_bf16 v[152:155], a[88:91], v[144:147], v[152:155]\n"
      "v_mfma_f32_16x16x32_bf16 v[152:155], a[92:95], v[148:151], v[152:155]\n"
      "buffer_load_dwordx4 v0, s[8:11], s51 offen offset:2048 sc1 lds\n"
      "v_mfma_f32_16x16x32_bf16 v[156:159], a[96:99], v[56:59], 0\n"
      "v_mfma_f32_16x16x32_bf16 v[156:159], a[100:103], v[60:63], v[156:159]\n"
      "v_add_u32 v14, 1, v14\n"
      "v_mfma_f32_16x16x32_bf16 v[156:159], a[104:107], v[64:67], v[156:159]\n"
      "v_cvt_pk_bf16_f32 v225, v154, v155\n"
      "v_mfma_f32_16x16x32_bf16 v[156:159], a[108:111], v[68:71], v[156:159]\n"
      "v_add_f32 v175, v175, v163\n"
      "v_mfma_f32_16x16x32_bf16 v[156:159], a[112:115], v[72:75], v[156:159]\n"
      "buffer_load_dwordx4 v0, s[8:11], s51 offen offset:1024 sc1 lds\n"
      "v_mfma_f32_16x16x32_bf16 v[156:159], a[116:119], v[76:79], v[156:159]\n"
      "v_mfma_f32_16x16x32_bf16 v[156:159], a[120:123], v[80:83], v[156:159]\n"
      "v_mfma_f32_16x16x32_bf16 v[156:159], a[124:127], v[84:87], v[156:159]\n"
      "v_and_b32 v173, 0xffff0000, v220\n"
      "v_mfma_f32_16x16x32_bf16 v[156:159], a[128:131], v[88:91], v[156:159]\n"
      "buffer_load_dword v20, v2, s[8:11], s36 offen sc1\n"
      "v_mfma_f32_16x16x32_bf16 v[156:159], a[132:135], v[92:95], v[156:159]\n"
      "s_waitcnt vmcnt(5)\n"
      "ds_write_b32 v16, v14\n"
      "v_mfma_f32_16x16x32_bf16 v[156:159], a[136:139], v[96:99], v[156:159]\n"
      "buffer_load_dwordx4 v0, s[8:11], s51 offen offset:0 sc1 lds\n"
      "v_mfma_f32_16x16x32_bf16 v[156:159], a[140:143], v[100:103], v[156:159]\n"
      "v_add_f32 v173, v173, v161\n"
      "v_mfma_f32_16x16x32_bf16 v[156:159], a[144:147], v[104:107], v[156:159]\n"
      "v_mfma_f32_16x16x32_bf16 v[156:159], a[148:151], v[108:111], v[156:159]\n"
      "v_mfma_f32_16x16x32_bf16 v[156:159], a[152:155], v[112:115], v[156:159]\n"
      "v_mfma_f32_16x16x32_bf16 v[156:159], a[156:159], v[116:119], v[156:159]\n"
      "buffer_load_dwordx4 v0, s[8:11], s51 offen offset:3072 sc1 lds\n"
      "v_mfma_f32_16x16x32_bf16 v[156:159], a[160:163], v[120:123], v[156:159]\n"
      "s_add_u32 m0, s43, 77824\n"
      "v_mfma_f32_16x16x32_bf16 v[156:159], a[164:167], v[124:127], v[156:159]\n"
      "v_mfma_f32_16x16x32_bf16 v[156:159], a[168:171], v[128:131], v[156:159]\n"
      "ds_read_b32 v15, v17\n"
      "v_mfma_f32_16x16x32_bf16 v[156:159], a[172:175], v[132:135], v[156:159]\n"
      "s_add_u32 s52, s52, 173056\n"
      "v_mfma_f32_16x16x32_bf16 v[156:159], a[176:179], v[136:139], v[156:159]\n"
      "buffer_load_dwordx4 v1, s[8:11], s51 offen offset:0 sc1 lds\n"
      "v_mfma_f32_16x16x32_bf16 v[156:159], a[180:183], v[140:143], v[156:159]\n"
      "v_mfma_f32_16x16x32_bf16 v[156:159], a[184:187], v[144:147], v[156:159]\n"
      "v_cvt_pk_bf16_f32 v224, v152, v153\n"
      "v_mfma_f32_16x16x32_bf16 v[156:159], a[188:191], v[148:151], v[156:159]\n"
      "s_waitcnt lgkmcnt(0)\n"
      "v_cmp_gt_i32 vcc, v14, v15\n"
      "v_mfma_f32_16x16x32_bf16 v[160:163], a[192:195], v[56:59], 0\n"
      "buffer_load_dwordx4 v1, s[8:11], s51 offen offset:1024 sc1 lds\n"
      "v_mfma_f32_16x16x32_bf16 v[160:163], a[196:199], v[60:63], v[160:163]\n"
      "v_mfma_f32_16x16x32_bf16 v[160:163], a[200:203], v[64:67], v[160:163]\n"
      "s_cbranch_vccnz .Lsp_dy2_7\n"
      ".Lbk_dy2_7:\n"
      "v_mfma_f32_16x16x32_bf16 v[160:163], a[204:207], v[68:71], v[160:163]\n"
      "ds_read_b128 v[68:71], v22 offset:3072\n"
      "v_mfma_f32_16x16x32_bf16 v[160:163], a[208:211], v[72:75], v[160:163]\n"
      "ds_read_b128 v[60:63], v22 offset:1024\n"
      "v_mfma_f32_16x16x32_bf16 v[160:163], a[212:215], v[76:79], v[160:163]\n"
      "s_waitcnt vmcnt(7)\n"
      "buffer_store_dword v18, v5, s[8:11], s56 offen sc1\n"
      "v_mfma_f32_16x16x32_bf16 v[160:163], a[216:219], v[80:83], v[160:163]\n"
      "v_cvt_pk_bf16_f32 v227, v158, v159\n"
      "v_mfma_f32_16x16x32_bf16 v[160:163], a[220:223], v[84:87], v[160:163]\n"
      "ds_read_b128 v[80:83], v22 offset:6144\n"
      "v_cvt_pk_bf16_f32 v226, v156, v157\n"
      "v_mfma_f32_16x16x32_bf16 v[160:163], a[224:227], v[88:91], v[160:163]\n"
      "ds_read_b128 v[72:75], v22 offset:4096\n"
      "v_mfma_f32_16x16x32_bf16 v[160:163], a[228:231], v[92:95], v[160:163]\n"
      "ds_read_b128 v[76:79], v22 offset:5120\n"
      "v_mfma_f32_16x16x32_bf16 v[160:163], a[232:235], v[96:99], v[160:163]\n"
      "ds_read_b128 v[96:99], v22 offset:10240\n"
      "v_mfma_f32_16x16x32_bf16 v[160:163], a[236:239], v[100:103], v[160:163]\n"
      "v_mfma_f32_16x16x32_bf16 v[160:163], a[240:243], v[104:107], v[160:163]\n"
      "buffer_store_dwordx4 v[224:227], v0, s[8:11], s37 offen sc1\n"
      "v_mfma_f32_16x16x32_bf16 v[160:163], a[244:247], v[108:111], v[160:163]\n"
      "v_mfma_f32_16x16x32_bf16 v[160:163], a[248:251], v[112:115], v[160:163]\n"
      "ds_read_b128 v[84:87], v22 offset:7168\n"
      "v_mfma_f32_16x16x32_bf16 v[160:163], a[252:255], v[116:119], v[160:163]\n"
      "v_mfma_f32_16x16x32_bf16 v[160:163], v[24:27], v[120:123], v[160:163]\n"
      "ds_read_b128 v[56:59], v22 offset:0\n"
      "v_mfma_f32_16x16x32_bf16 v[160:163], v[28:31], v[124:127], v[160:163]\n"
      "ds_read_b128 v[92:95], v22 offset:9216\n"
      "s_mov_b32 s51, s36\n"
      "v_mfma_f32_16x16x32_bf16 v[160:163], v[32:35], v[128:131], v[160:163]\n"
      "ds_read_b128 v[64:67], v22 offset:2048\n"
      "v_mfma_f32_16x16x32_bf16 v[160:163], v[36:39], v[132:135], v[160:163]\n"
      "ds_read_b128 v[100:103], v22 offset:11264\n"
      "v_mfma_f32_16x16x32_bf16 v[160:163], v[40:43], v[136:139], v[160:163]\n"
      "ds_read_b128 v[132:135], v22 offset:19456\n"
      "v_mfma_f32_16x16x32_bf16 v[160:163], v[44:47], v[140:143], v[160:163]\n"
      "v_mfma_f32_16x16x32_bf16 v[160:163], v[48:51], v[144:147], v[160:163]\n"
      "s_waitcnt lgkmcnt(4)\n"
      "ds_read_b128 v[112:115], v22 offset:14336\n"
      "s_add_u32 m0, s43, 98304\n"
      "v_mfma_f32_16x16x32_bf16 v[160:163], v[52:55], v[148:151], v[160:163]\n"
      "s_sub_u32 s59, s59, 1\n"
      "s_cbranch_scc1 .Ld_out1\n"
      ".Ld_t2:\n"
      "v_mfma_f32_16x16x32_bf16 v[152:155], a[0:3], v[56:59], 0\n"
      "ds_read_b128 v[88:91], v22 offset:8192\n"
      "v_mfma_f32_16x16x32_bf16 v[152:155], a[4:7], v[60:63], v[152:155]\n"
      "s_waitcnt lgkmcnt(4)\n"
      "ds_read_b128 v[120:123], v22 offset:16384\n"
      "v_mfma_f32_16x16x32_bf16 v[152:155], a[8:11], v[64:67], v[152:155]\n"
      "s_add_u32 s36, s36, 165888\n"
      "ds_read_b128 v[104:107], v22 offset:12288\n"
      "v_cvt_pk_bf16_f32 v229, v162, v163\n"
      "v_mfma_f32_16x16x32_bf16 v[152:155], a[12:15], v[68:71], v[152:155]\n"
      "ds_read_b128 v[148:151], v22 offset:23552\n"
      "v_cvt_pk_bf16_f32 v228, v160, v161\n"
      "v_mfma_f32_16x16x32_bf16 v[152:155], a[16:19], v[72:75], v[152:155]\n"
      "buffer_store_dwordx2 v[228:229], v13, s[8:11], s37 offen sc1\n"
      "v_mfma_f32_16x16x32_bf16 v[152:155], a[20:23], v[76:79], v[152:155]\n"
      "ds_read_b128 v[108:111], v22 offset:13312\n"
      "v_mfma_f32_16x16x32_bf16 v[152:155], a[24:27], v[80:83], v[152:155]\n"
      "ds_read_b128 v[116:119], v22 offset:15360\n"
      "v_mfma_f32_16x16x32_bf16 v[152:155], a[28:31], v[84:87], v[152:155]\n"
      "s_waitcnt lgkmcnt(5)\n"
      "ds_read_b128 v[124:127], v22 offset:17408\n"
      "v_mfma_f32_16x16x32_bf16 v[152:155], a[32:35], v[88:91], v[152:155]\n"
      "ds_read_b128 v[140:143], v22 offset:21504\n"
      "v_mfma_f32_16x16x32_bf16 v[152:155], a[36:39], v[92:95], v[152:155]\n"
      "ds_read_b128 v[128:131], v22 offset:18432\n"
      "v_mfma_f32_16x16x32_bf16 v[152:155], a[40:43], v[96:99], v[152:155]\n"
      "v_mfma_f32_16x16x32_bf16 v[152:155], a[44:47], v[100:103], v[152:155]\n"
      "s_waitcnt lgkmcnt(5)\n"
      "ds_read_b128 v[136:139], v22 offset:20480\n"
      "v_mfma_f32_16x16x32_bf16 v[152:155], a[48:51], v[104:107], v[152:155]\n"
      "s_waitcnt lgkmcnt(5)\n"
      "v_mfma_f32_16x16x32_bf16 v[152:155], a[52:55], v[108:111], v[152:155]\n"
      "v_mfma_f32_16x16x32_bf16 v[152:155], a[56:59], v[112:115], v[152:155]\n"
      "s_waitcnt lgkmcnt(3)\n"
      "ds_read_b128 v[144:147], v22 offset:22528\n"
      "v_mfma_f32_16x16x32_bf16 v[152:155], a[60:63], v[116:119], v[152:155]\n"
      "v_add_u32 v14, 1, v14\n"
      "v_mfma_f32_16x16x32_bf16 v[152:155], a[64:67], v[120:123], v[152:155]\n"
      "s_waitcnt vmcnt(7) lgkmcnt(2)\n"
      "v_cmp_ne_u32 vcc, s21, v20\n"
      "v_mfma_f32_16x16x32_bf16 v[152:155], a[68:71], v[124:127], v[152:155]\n"
      "v_mfma_f32_16x16x32_bf16 v[152:155], a[72:75], v[128:131], v[152:155]\n"
      "v_mfma_f32_16x16x32_bf16 v[152:155], a[76:79], v[132:135], v[152:155]\n"
      "s_waitcnt lgkmcnt(1)\n"
      "v_mfma_f32_16x16x32_bf16 v[152:155], a[80:83], v[136:139], v[152:155]\n"
      "v_mfma_f32_16x16x32_bf16 v[152:155], a[84:87], v[140:143], v[152:155]\n"
      "s_waitcnt lgkmcnt(0)\n"
      "s_cbranch_vccnz .Lsp_d4_8\n"
      ".Lbk_d4_8:\n"
      "v_mfma_f32_16x16x32_bf16 v[152:155], a[88:91], v[144:147], v[152:155]\n"
      "v_mfma_f32_16x16x32_bf16 v[152:155], a[92:95], v[148:151], v[152:155]\n"
      "s_min_u32 s36, s36, s6\n"
      "v_mfma_f32_16x16x32_bf16 v[156:159], a[96:99], v[56:59], 0\n"
      "buffer_load_dwordx4 v0, s[8:11], s51 offen offset:0 sc1 lds\n"
      "v_mfma_f32_16x16x32_bf16 v[156:159], a[100:103], v[60:63], v[156:159]\n"
      "v_mfma_f32_16x16x32_bf16 v[156:159], a[104:107], v[64:67], v[156:159]\n"
      "v_mfma_f32_16x16x32_bf16 v[156:159], a[108:111], v[68:71], v[156:159]\n"
      "v_mfma_f32_16x16x32_bf16 v[156:159], a[112:115], v[72:75], v[156:159]\n"
      "buffer_load_dwordx4 v0, s[8:11], s51 offen offset:2048 sc1 lds\n"
      "v_mfma_f32_16x16x32_bf16 v[156:159], a[116:119], v[76:79], v[156:159]\n"
      "s_waitcnt vmcnt(5)\n"
      "ds_write_b32 v16, v14\n"
      "v_mfma_f32_16x16x32_bf16 v[156:159], a[120:123], v[80:83], v[156:159]\n"
      "v_mfma_f32_16x16x32_bf16 v[156:159], a[124:127], v[84:87], v[156:159]\n"
      "v_mfma_f32_16x16x32_bf16 v[156:159], a[128:131], v[88:91], v[156:159]\n"
      "v_mfma_f32_16x16x32_bf16 v[156:159], a[132:135], v[92:95], v[156:159]\n"
      "buffer_load_dword v20, v2, s[8:11], s36 offen sc1\n"
      "v_mfma_f32_16x16x32_bf16 v[156:159], a[136:139], v[96:99], v[156:159]\n"
      "v_mfma_f32_16x16x32_bf16 v[156:159], a[140:143], v[100:103], v[156:159]\n"
      "v_cvt_pk_bf16_f32 v225, v154, v155\n"
      "v_mfma_f32_16x16x32_bf16 v[156:159], a[144:147], v[104:107], v[156:159]\n"
      "buffer_load_dwordx4 v0, s[8:11], s51 offen offset:3072 sc1 lds\n"
      "v_mfma_f32_16x16x32_bf16 v[156:159], a[148:151], v[108:111], v[156:159]\n"
      "v_mfma_f32_16x16x32_bf16 v[156:159], a[152:155], v[112:115], v[156:159]\n"
      "v_mfma_f32_16x16x32_bf16 v[156:159], a[156:159], v[116:119], v[156:159]\n"
      "ds_read_b32 v15, v17\n"
      "v_mfma_f32_16x16x32_bf16 v[156:159], a[160:163], v[120:123], v[156:159]\n"
      "buffer_load_dwordx4 v0, s[8:11], s51 offen offset:1024 sc1 lds\n"
      "v_mfma_f32_16x16x32_bf16 v[156:159], a[164:167], v[124:127], v[156:159]\n"
      "s_add_u32 m0, s43, 102400\n"
      "v_mfma_f32_16x16x32_bf16 v[156:159], a[168:171], v[128:131], v[156:159]\n"
      "v_mfma_f32_16x16x32_bf16 v[156:159], a[172:175], v[132:135], v[156:159]\n"
      "v_mfma_f32_16x16x32_bf16 v[156:159], a[176:179], v[136:139], v[156:159]\n"
      "buffer_load_dwordx4 v1, s[8:11], s51 offen offset:1024 sc1 lds\n"
      "v_mfma_f32_16x16x32_bf16 v[156:159], a[180:183], v[140:143], v[156:159]\n"
      "v_mfma_f32_16x16x32_bf16 v[156:159], a[184:187], v[144:147], v[156:159]\n"
      "s_waitcnt lgkmcnt(0)\n"
      "v_cmp_gt_i32 vcc, v14, v15\n"
      "v_mfma_f32_16x16x32_bf16 v[156:159], a[188:191], v[148:151], v[156:159]\n"
      "v_mfma_f32_16x16x32_bf16 v[160:163], a[192:195], v[56:59], 0\n"
      "buffer_load_dwordx4 v1, s[8:11], s51 offen offset:0 sc1 lds\n"
      "v_mfma_f32_16x16x32_bf16 v[160:163], a[196:199], v[60:63], v[160:163]\n"
      "s_cbranch_vccnz .Lsp_dy3_9\n"
      ".Lbk_dy3_9:\n"
      "ds_read_b128 v[60:63], v22 offset:25600\n"
      "v_mfma_f32_16x16x32_bf16 v[160:163], a[200:203], v[64:67], v[160:163]\n"
      "v_mfma_f32_16x16x32_bf16 v[160:163], a[204:207], v[68:71], v[160:163]\n"
      "v_cvt_pk_bf16_f32 v227, v158, v159\n"
      "v_mfma_f32_16x16x32_bf16 v[160:163], a[208:211], v[72:75], v[160:163]\n"
      "ds_read_b128 v[64:67], v22 offset:26624\n"
      "v_mfma_f32_16x16x32_bf16 v[160:163], a[212:215], v[76:79], v[160:163]\n"
      "s_mov_b32 s51, s36\n"
      "ds_read_b128 v[72:75], v22 offset:28672\n"
      "v_mfma_f32_16x16x32_bf16 v[160:163], a[216:219], v[80:83], v[160:163]\n"
      "ds_read_b128 v[68:71], v22 offset:27648\n"
      "v_mfma_f32_16x16x32_bf16 v[160:163], a[220:223], v[84:87], v[160:163]\n"
      "v_cvt_pk_bf16_f32 v224, v152, v153\n"
      "v_mfma_f32_16x16x32_bf16 v[160:163], a[224:227], v[88:91], v[160:163]\n"
      "s_waitcnt vmcnt(7) lgkmcnt(3)\n"
      "buffer_store_dword v18, v4, s[8:11], s52 offen sc1\n"
      "v_mfma_f32_16x16x32_bf16 v[160:163], a[228:231], v[92:95], v[160:163]\n"
      "ds_read_b128 v[92:95], v22 offset:33792\n"
      "v_mfma_f32_16x16x32_bf16 v[160:163], a[232:235], v[96:99], v[160:163]\n"
      "ds_read_b128 v[56:59], v22 offset:24576\n"
      "v_mfma_f32_16x16x32_bf16 v[160:163], a[236:239], v[100:103], v[160:163]\n"
      "buffer_load_dword v21, v3, s[8:11], s53 offen sc1\n"
      "v_mfma_f32_16x16x32_bf16 v[160:163], a[240:243], v[104:107], v[160:163]\n"
      "ds_read_b128 v[100:103], v22 offset:35840\n"
      "v_mfma_f32_16x16x32_bf16 v[160:163], a[244:247], v[108:111], v[160:163]\n"
      "ds_read_b128 v[80:83], v22 offset:30720\n"
      "v_mfma_f32_16x16x32_bf16 v[160:163], a[248:251], v[112:115], v[160:163]\n"
      "ds_read_b128 v[112:115], v22 offset:38912\n"
      "v_mfma_f32_16x16x32_bf16 v[160:163], a[252:255], v[116:119], v[160:163]\n"
      "s_add_u32 s37, s37, 106496\n"
      "ds_read_b128 v[88:91], v22 offset:32768\n"
      "v_mfma_f32_16x16x32_bf16 v[160:163], v[24:27], v[120:123], v[160:163]\n"
      "ds_read_b128 v[96:99], v22 offset:34816\n"
      "v_cvt_pk_bf16_f32 v226, v156, v157\n"
      "v_mfma_f32_16x16x32_bf16 v[160:163], v[28:31], v[124:127], v[160:163]\n"
      "ds_read_b128 v[76:79], v22 offset:29696\n"
      "v_mfma_f32_16x16x32_bf16 v[160:163], v[32:35], v[128:131], v[160:163]\n"
      "v_mfma_f32_16x16x32_bf16 v[160:163], v[36:39], v[132:135], v[160:163]\n"
      "buffer_store_dwordx4 v[224:227], v0, s[8:11], s37 offen sc1\n"
      "v_mfma_f32_16x16x32_bf16 v[160:163], v[40:43], v[136:139], v[160:163]\n"
      "ds_read_b128 v[116:119], v22 offset:39936\n"
      "v_mfma_f32_16x16x32_bf16 v[160:163], v[44:47], v[140:143], v[160:163]\n"
      "ds_read_b128 v[84:87], v22 offset:31744\n"
      "v_mfma_f32_16x16x32_bf16 v[160:163], v[48:51], v[144:147], v[160:163]\n"
      "s_waitcnt lgkmcnt(4)\n"
      "ds_read_b128 v[104:107], v22 offset:36864\n"
      "v_mfma_f32_16x16x32_bf16 v[160:163], v[52:55], v[148:151], v[160:163]\n"
      "s_sub_u32 s59, s59, 1\n"
      "s_cbranch_scc1 .Ld_out2\n"
      ".Ld_t3:\n"
      "v_mfma_f32_16x16x32_bf16 v[152:155], a[0:3], v[56:59], 0\n"
      "ds_read_b128 v[120:123], v22 offset:40960\n"
      "v_mfma_f32_16x16x32_bf16 v[152:155], a[4:7], v[60:63], v[152:155]\n"
      "ds_read_b128 v[108:111], v22 offset:37888\n"
      "v_mfma_f32_16x16x32_bf16 v[152:155], a[8:11], v[64:67], v[152:155]\n"
      "ds_read_b128 v[132:135], v22 offset:44032\n"
      "v_mfma_f32_16x16x32_bf16 v[152:155], a[12:15], v[68:71], v[152:155]\n"
      "ds_read_b128 v[136:139], v22 offset:45056\n"
      "v_cvt_pk_bf16_f32 v229, v162, v163\n"
      "v_mfma_f32_16x16x32_bf16 v[152:155], a[16:19], v[72:75], v[152:155]\n"
      "s_waitcnt lgkmcnt(5)\n"
      "v_mfma_f32_16x16x32_bf16 v[152:155], a[20:23], v[76:79], v[152:155]\n"
      "ds_read_b128 v[124:127], v22 offset:41984\n"
      "v_mfma_f32_16x16x32_bf16 v[152:155], a[24:27], v[80:83], v[152:155]\n"
      "s_add_u32 m0, s43, 122880\n"
      "v_cvt_pk_bf16_f32 v228, v160, v161\n"
      "v_mfma_f32_16x16x32_bf16 v[152:155], a[28:31], v[84:87], v[152:155]\n"
      "buffer_store_dwordx2 v[228:229], v13, s[8:11], s37 offen sc1\n"
      "v_mfma_f32_16x16x32_bf16 v[152:155], a[32:35], v[88:91], v[152:155]\n"
      "v_mfma_f32_16x16x32_bf16 v[152:155], a[36:39], v[92:95], v[152:155]\n"
      "ds_read_b128 v[128:131], v22 offset:43008\n"
      "v_mfma_f32_16x16x32_bf16 v[152:155], a[40:43], v[96:99], v[152:155]\n"
      "ds_read_b128 v[144:147], v22 offset:47104\n"
      "v_mfma_f32_16x16x32_bf16 v[152:155], a[44:47], v[100:103], v[152:155]\n"
      "s_waitcnt lgkmcnt(3)\n"
      "ds_read_b128 v[148:151], v22 offset:48128\n"
      "v_mfma_f32_16x16x32_bf16 v[152:155], a[48:51], v[104:107], v[152:155]\n"
      "ds_read_b128 v[140:143], v22 offset:46080\n"
      "v_mfma_f32_16x16x32_bf16 v[152:155], a[52:55], v[108:111], v[152:155]\n"
      "v_mfma_f32_16x16x32_bf16 v[152:155], a[56:59], v[112:115], v[152:155]\n"
      "s_waitcnt vmcnt(8) lgkmcnt(4)\n"
      "v_cmp_ne_u32 vcc, s21, v20\n"
      "v_mfma_f32_16x16x32_bf16 v[152:155], a[60:63], v[116:119], v[152:155]\n"
      "v_mfma_f32_16x16x32_bf16 v[152:155], a[64:67], v[120:123], v[152:155]\n"
      "s_add_u32 s36, s36, 165888\n"
      "s_add_u32 s52, s52, 106496\n"
      "v_mfma_f32_16x16x32_bf16 v[152:155], a[68:71], v[124:127], v[152:155]\n"
      "s_waitcnt lgkmcnt(2)\n"
      "v_mfma_f32_16x16x32_bf16 v[152:155], a[72:75], v[128:131], v[152:155]\n"
      "s_cbranch_vccnz .Lsp_d5_10\n"
      ".Lbk_d5_10:\n"
      "v_mfma_f32_16x16x32_bf16 v[152:155], a[76:79], v[132:135], v[152:155]\n"
      "buffer_load_dwordx4 v0, s[8:11], s51 offen offset:1024 sc1 lds\n"
      "v_mfma_f32_16x16x32_bf16 v[152:155], a[80:83], v[136:139], v[152:155]\n"
      "s_waitcnt lgkmcnt(0)\n"
      "v_mfma_f32_16x16x32_bf16 v[152:155], a[84:87], v[140:143], v[152:155]\n"
      "v_add_u32 v14, 1, v14\n"
      "v_mfma_f32_16x16x32_bf16 v[152:155], a[88:91], v[144:147], v[152:155]\n"
      "v_mfma_f32_16x16x32_bf16 v[152:155], a[92:95], v[148:151], v[152:155]\n"
      "buffer_load_dwordx4 v0, s[8:11], s51 offen offset:0 sc1 lds\n"
      "v_mfma_f32_16x16x32_bf16 v[156:159], a[96:99], v[56:59], 0\n"
      "v_mfma_f32_16x16x32_bf16 v[156:159], a[100:103], v[60:63], v[156:159]\n"
      "v_mfma_f32_16x16x32_bf16 v[156:159], a[104:107], v[64:67], v[156:159]\n"
      "v_mfma_f32_16x16x32_bf16 v[156:159], a[108:111], v[68:71], v[156:159]\n"
      "buffer_load_dwordx4 v0, s[8:11], s51 offen offset:2048 sc1 lds\n"
      "v_mfma_f32_16x16x32_bf16 v[156:159], a[112:115], v[72:75], v[156:159]\n"
      "s_waitcnt vmcnt(7)\n"
      "ds_write_b32 v16, v14\n"
      "v_mfma_f32_16x16x32_bf16 v[156:159], a[116:119], v[76:79], v[156:159]\n"
      "v_mfma_f32_16x16x32_bf16 v[156:159], a[120:123], v[80:83], v[156:159]\n"
      "v_mfma_f32_16x16x32_bf16 v[156:159], a[124:127], v[84:87], v[156:159]\n"
      "buffer_load_dwordx4 v0, s[8:11], s51 offen offset:3072 sc1 lds\n"
      "v_mfma_f32_16x16x32_bf16 v[156:159], a[128:131], v[88:91], v[156:159]\n"
      "v_mfma_f32_16x16x32_bf16 v[156:159], a[132:135], v[92:95], v[156:159]\n"
      "v_cvt_pk_bf16_f32 v224, v152, v153\n"
      "v_mfma_f32_16x16x32_bf16 v[156:159], a[136:139], v[96:99], v[156:159]\n"
      "v_mfma_f32_16x16x32_bf16 v[156:159], a[140:143], v[100:103], v[156:159]\n"
      "s_min_u32 s36, s36, s6\n"
      "s_add_u32 m0, s43, 126976\n"
      "v_mfma_f32_16x16x32_bf16 v[156:159], a[144:147], v[104:107], v[156:159]\n"
      "buffer_load_dwordx4 v1, s[8:11], s51 offen offset:1024 sc1 lds\n"
      "v_mfma_f32_16x16x32_bf16 v[156:159], a[148:151], v[108:111], v[156:159]\n"
      "ds_read_b32 v15, v17\n"
      "v_mfma_f32_16x16x32_bf16 v[156:159], a[152:155], v[112:115], v[156:159]\n"
      "v_mfma_f32_16x16x32_bf16 v[156:159], a[156:159], v[116:119], v[156:159]\n"
      "v_mfma_f32_16x16x32_bf16 v[156:159], a[160:163], v[120:123], v[156:159]\n"
      "buffer_load_dword v20, v2, s[8:11], s36 offen sc1\n"
      "v_mfma_f32_16x16x32_bf16 v[156:159], a[164:167], v[124:127], v[156:159]\n"
      "v_mfma_f32_16x16x32_bf16 v[156:159], a[168:171], v[128:131], v[156:159]\n"
      "s_waitcnt lgkmcnt(0)\n"
      "v_cmp_gt_i32 vcc, v14, v15\n"
      "v_mfma_f32_16x16x32_bf16 v[156:159], a[172:175], v[132:135], v[156:159]\n"
      "buffer_load_dwordx4 v1, s[8:11], s51 offen offset:0 sc1 lds\n"
      "v_mfma_f32_16x16x32_bf16 v[156:159], a[176:179], v[136:139], v[156:159]\n"
      "s_add_u32 s55, s53, 6656\n"
      "v_mfma_f32_16x16x32_bf16 v[156:159], a[180:183], v[140:143], v[156:159]\n"
      "s_add_u32 s54, s53, 13312\n"
      "v_mfma_f32_16x16x32_bf16 v[156:159], a[184:187], v[144:147], v[156:159]\n"
      "s_waitcnt vmcnt(9)\n"
      "s_cbranch_vccnz .Lsp_dy4_11\n"
      ".Lbk_dy4_11:\n"
      "v_cmp_ne_u32 vcc, s21, v21\n"
      "v_mfma_f32_16x16x32_bf16 v[156:159], a[188:191], v[148:151], v[156:159]\n"
      ".Ld_p1:\n"
      "s_cbranch_execz .Ld_pr\n"
      "v_mfma_f32_16x16x32_bf16 v[160:163], a[192:195], v[56:59], 0\n"
      "v_mfma_f32_16x16x32_bf16 v[160:163], a[196:199], v[60:63], v[160:163]\n"
      "s_cbranch_vccnz .Lsp_df_12\n"
      ".Lbk_df_12:\n"
      "buffer_load_dwordx4 v[176:179], v0, s[8:11], s53 offen sc1\n"
      "v_mfma_f32_16x16x32_bf16 v[160:163], a[200:203], v[64:67], v[160:163]\n"
      "ds_read_b128 v[60:63], v23 offset:1024\n"
      "v_mfma_f32_16x16x32_bf16 v[160:163], a[204:207], v[68:71], v[160:163]\n"
      "ds_read_b128 v[64:67], v23 offset:2048\n"
      "v_mfma_f32_16x16x32_bf16 v[160:163], a[208:211], v[72:75], v[160:163]\n"
      "buffer_load_dwordx2 v[180:181], v13, s[8:11], s53 offen sc1\n"
      "v_mfma_f32_16x16x32_bf16 v[160:163], a[212:215], v[76:79], v[160:163]\n"
      "ds_read_b128 v[72:75], v23 offset:4096\n"
      "v_mfma_f32_16x16x32_bf16 v[160:163], a[216:219], v[80:83], v[160:163]\n"
      "ds_read_b128 v[68:71], v23 offset:3072\n"
      "s_add_u32 s58, s41, 4294967264\n"
      "v_mfma_f32_16x16x32_bf16 v[160:163], a[220:223], v[84:87], v[160:163]\n"
      "ds_read_b128 v[80:83], v23 offset:6144\n"
      "v_mfma_f32_16x16x32_bf16 v[160:163], a[224:227], v[88:91], v[160:163]\n"
      "v_cvt_pk_bf16_f32 v225, v154, v155\n"
      "v_mfma_f32_16x16x32_bf16 v[160:163], a[228:231], v[92:95], v[160:163]\n"
      "buffer_load_dwordx4 v[184:187], v0, s[8:11], s55 offen sc1\n"
      "v_mfma_f32_16x16x32_bf16 v[160:163], a[232:235], v[96:99], v[160:163]\n"
      "ds_read_b128 v[84:87], v23 offset:7168\n"
      "v_cvt_pk_bf16_f32 v226, v156, v157\n"
      "v_mfma_f32_16x16x32_bf16 v[160:163], a[236:239], v[100:103], v[160:163]\n"
      "ds_read_b128 v[76:79], v23 offset:5120\n"
      "v_mfma_f32_16x16x32_bf16 v[160:163], a[240:243], v[104:107], v[160:163]\n"
      "s_add_u32 s37, s37, 106496\n"
      "ds_read_b128 v[56:59], v23 offset:0\n"
      "v_mfma_f32_16x16x32_bf16 v[160:163], a[244:247], v[108:111], v[160:163]\n"
      "s_waitcnt vmcnt(10) lgkmcnt(4)\n"
      "buffer_store_dword v18, v4, s[8:11], s52 offen sc1\n"
      "v_mfma_f32_16x16x32_bf16 v[160:163], a[248:251], v[112:115], v[160:163]\n"
      "ds_read_b128 v[88:91], v23 offset:8192\n"
      "v_mfma_f32_16x16x32_bf16 v[160:163], a[252:255], v[116:119], v[160:163]\n"
      "ds_read_b128 v[96:99], v23 offset:10240\n"
      "v_mfma_f32_16x16x32_bf16 v[160:163], v[24:27], v[120:123], v[160:163]\n"
      "buffer_load_dwordx4 v[192:195], v0, s[8:11], s54 offen sc1\n"
      "v_mfma_f32_16x16x32_bf16 v[160:163], v[28:31], v[124:127], v[160:163]\n"
      "v_mfma_f32_16x16x32_bf16 v[160:163], v[32:35], v[128:131], v[160:163]\n"
      "buffer_load_dwordx2 v[188:189], v13, s[8:11], s55 offen sc1\n"
      "v_mfma_f32_16x16x32_bf16 v[160:163], v[36:39], v[132:135], v[160:163]\n"
      "ds_read_b128 v[100:103], v23 offset:11264\n"
      "v_mfma_f32_16x16x32_bf16 v[160:163], v[40:43], v[136:139], v[160:163]\n"
      "s_add_u32 s55, s53, 19968\n"
      "ds_read_b128 v[132:135], v23 offset:19456\n"
      "v_mfma_f32_16x16x32_bf16 v[160:163], v[44:47], v[140:143], v[160:163]\n"
      "ds_read_b128 v[104:107], v23 offset:12288\n"
      "v_mfma_f32_16x16x32_bf16 v[160:163], v[48:51], v[144:147], v[160:163]\n"
      "s_waitcnt lgkmcnt(3)\n"
      "buffer_load_dwordx2 v[196:197], v13, s[8:11], s54 offen sc1\n"
      "v_mfma_f32_16x16x32_bf16 v[160:163], v[52:55], v[148:151], v[160:163]\n"
      "s_sub_u32 s59, s59, 1\n"
      "s_cbranch_scc1 .Ld_out3\n"
      ".Ld_t4:\n"
      "v_mfma_f32_16x16x32_bf16 v[152:155], a[0:3], v[56:59], 0\n"
      "ds_read_b128 v[92:95], v23 offset:9216\n"
      "v_mfma_f32_16x16x32_bf16 v[152:155], a[4:7], v[60:63], v[152:155]\n"
      "ds_read_b128 v[108:111], v23 offset:13312\n"
      "s_add_u32 m0, s43, 0\n"
      "v_mfma_f32_16x16x32_bf16 v[152:155], a[8:11], v[64:67], v[152:155]\n"
      "ds_read_b128 v[128:131], v23 offset:18432\n"
      "v_cvt_pk_bf16_f32 v228, v160, v161\n"
      "v_mfma_f32_16x16x32_bf16 v[152:155], a[12:15], v[68:71], v[152:155]\n"
      "buffer_load_dwordx4 v[200:203], v0, s[8:11], s55 offen sc1\n"
      "v_mfma_f32_16x16x32_bf16 v[152:155], a[16:19], v[72:75], v[152:155]\n"
      "ds_read_b128 v[116:119], v23 offset:15360\n"
      "v_cvt_pk_bf16_f32 v227, v158, v159\n"
      "v_mfma_f32_16x16x32_bf16 v[152:155], a[20:23], v[76:79], v[152:155]\n"
      "ds_read_b128 v[112:115], v23 offset:14336\n"
      "v_mfma_f32_16x16x32_bf16 v[152:155], a[24:27], v[80:83], v[152:155]\n"
      "s_mov_b32 s51, s36\n"
      "ds_read_b128 v[120:123], v23 offset:16384\n"
      "v_mfma_f32_16x16x32_bf16 v[152:155], a[28:31], v[84:87], v[152:155]\n"
      "buffer_store_dwordx4 v[224:227], v0, s[8:11], s37 offen sc1\n"
      "v_mfma_f32_16x16x32_bf16 v[152:155], a[32:35], v[88:91], v[152:155]\n"
      "s_waitcnt lgkmcnt(4)\n"
      "ds_read_b128 v[140:143], v23 offset:21504\n"
      "v_mfma_f32_16x16x32_bf16 v[152:155], a[36:39], v[92:95], v[152:155]\n"
      "ds_read_b128 v[124:127], v23 offset:17408\n"
      "v_mfma_f32_16x16x32_bf16 v[152:155], a[40:43], v[96:99], v[152:155]\n"
      "ds_read_b128 v[136:139], v23 offset:20480\n"
      "v_mfma_f32_16x16x32_bf16 v[152:155], a[44:47], v[100:103], v[152:155]\n"
      "v_cvt_pk_bf16_f32 v229, v162, v163\n"
      "v_mfma_f32_16x16x32_bf16 v[152:155], a[48:51], v[104:107], v[152:155]\n"
      "buffer_store_dwordx2 v[228:229], v13, s[8:11], s37 offen sc1\n"
      "v_mfma_f32_16x16x32_bf16 v[152:155], a[52:55], v[108:111], v[152:155]\n"
      "s_waitcnt lgkmcnt(3)\n"
      "ds_read_b128 v[148:151], v23 offset:23552\n"
      "v_add_u32_e64 v14, 1, v14\n"
      "v_mfma_f32_16x16x32_bf16 v[152:155], a[56:59], v[112:115], v[152:155]\n"
      "s_add_u32 s36, s36, 165888\n"
      "s_add_u32 s37, s37, 106496\n"
      "ds_read_b128 v[144:147], v23 offset:22528\n"
      "v_mfma_f32_16x16x32_bf16 v[152:155], a[60:63], v[116:119], v[152:155]\n"
      "v_mfma_f32_16x16x32_bf16 v[152:155], a[64:67], v[120:123], v[152:155]\n"
      "s_waitcnt lgkmcnt(3)\n"
      "s_min_u32 s36, s36, s6\n"
      "v_mfma_f32_16x16x32_bf16 v[152:155], a[68:71], v[124:127], v[152:155]\n"
      "buffer_load_dwordx2 v[204:205], v13, s[8:11], s55 offen sc1\n"
      "v_mfma_f32_16x16x32_bf16 v[152:155], a[72:75], v[128:131], v[152:155]\n"
      "s_cmp_lt_u32 s41, 32\n"
      "s_cselect_b32 s56, s39, 0x40000000\n"
      "v_mfma_f32_16x16x32_bf16 v[152:155], a[76:79], v[132:135], v[152:155]\n"
      "s_waitcnt lgkmcnt(2)\n"
      "s_add_u32 s54, s53, 26624\n"
      "v_mfma_f32_16x16x32_bf16 v[152:155], a[80:83], v[136:139], v[152:155]\n"
      "s_waitcnt vmcnt(12) lgkmcnt(1)\n"
      "s_add_u32 s53, s53, 599040\n"
      "v_cmp_ne_u32 vcc, s21, v20\n"
      "v_mfma_f32_16x16x32_bf16 v[152:155], a[84:87], v[140:143], v[152:155]\n"
      "s_waitcnt lgkmcnt(0)\n"
      "buffer_load_dwordx2 v[212:213], v13, s[8:11], s54 offen sc1\n"
      "v_mfma_f32_16x16x32_bf16 v[152:155], a[88:91], v[144:147], v[152:155]\n"
      "v_mfma_f32_16x16x32_bf16 v[152:155], a[92:95], v[148:151], v[152:155]\n"
      "s_add_u32 s39, s39, 147456\n"
      "v_mfma_f32_16x16x32_bf16 v[156:159], a[96:99], v[56:59], 0\n"
      "s_waitcnt vmcnt(12)\n"
      "ds_write_b32 v16, v14\n"
      "v_mfma_f32_16x16x32_bf16 v[156:159], a[100:103], v[60:63], v[156:159]\n"
      "buffer_load_dwordx4 v[208:211], v0, s[8:11], s54 offen sc1\n"
      "v_mfma_f32_16x16x32_bf16 v[156:159], a[104:107], v[64:67], v[156:159]\n"
      "s_cmp_lt_u32 s58, 32\n"
      "s_cselect_b32 s57, s40, 0x40000000\n"
      "v_mfma_f32_16x16x32_bf16 v[156:159], a[108:111], v[68:71], v[156:159]\n"
      "s_cbranch_vccnz .Lsp_d0_13\n"
      ".Lbk_d0_13:\n"
      "v_cvt_pk_bf16_f32 v224, v152, v153\n"
      "v_mfma_f32_16x16x32_bf16 v[156:159], a[112:115], v[72:75], v[156:159]\n"
      "v_cvt_pk_bf16_f32 v225, v154, v155\n"
      "v_mfma_f32_16x16x32_bf16 v[156:159], a[116:119], v[76:79], v[156:159]\n"
      "buffer_load_dwordx4 v0, s[8:11], s51 offen offset:0 sc1 lds\n"
      "v_mfma_f32_16x16x32_bf16 v[156:159], a[120:123], v[80:83], v[156:159]\n"
      "s_waitcnt vmcnt(13)\n"
      "v_lshlrev_b32 v234, 16, v178\n"
      "v_lshlrev_b32 v233, 16, v177\n"
      "v_mfma_f32_16x16x32_bf16 v[156:159], a[124:127], v[84:87], v[156:159]\n"
      "v_lshlrev_b32 v232, 16, v176\n"
      "v_add_f32 v166, v166, v233\n"
      "v_mfma_f32_16x16x32_bf16 v[156:159], a[128:131], v[88:91], v[156:159]\n"
      "s_add_u32 s52, s52, 106496\n"
      "v_add_f32 v164, v164, v232\n"
      "v_mfma_f32_16x16x32_bf16 v[156:159], a[132:135], v[92:95], v[156:159]\n"
      "buffer_load_dwordx4 v0, s[8:11], s51 offen offset:1024 sc1 lds\n"
      "v_mfma_f32_16x16x32_bf16 v[156:159], a[136:139], v[96:99], v[156:159]\n"
      "s_waitcnt vmcnt(13) lgkmcnt(0)\n"
      "v_lshlrev_b32 v233, 16, v181\n"
      "v_lshlrev_b32 v235, 16, v179\n"
      "v_mfma_f32_16x16x32_bf16 v[156:159], a[140:143], v[100:103], v[156:159]\n"
      "v_and_b32 v176, 0xffff0000, v176\n"
      "v_lshlrev_b32 v232, 16, v180\n"
      "v_mfma_f32_16x16x32_bf16 v[156:159], a[144:147], v[104:107], v[156:159]\n"
      "v_add_f32 v172, v172, v232\n"
      "v_and_b32 v181, 0xffff0000, v181\n"
      "v_mfma_f32_16x16x32_bf16 v[156:159], a[148:151], v[108:111], v[156:159]\n"
      "v_add_f32 v168, v168, v234\n"
      "v_add_f32 v174, v174, v233\n"
      "v_mfma_f32_16x16x32_bf16 v[156:159], a[152:155], v[112:115], v[156:159]\n"
      "buffer_load_dword v20, v2, s[8:11], s36 offen sc1\n"
      "v_mfma_f32_16x16x32_bf16 v[156:159], a[156:159], v[116:119], v[156:159]\n"
      "ds_read_b32 v15, v17\n"
      "v_add_f32 v165, v165, v176\n"
      "v_mfma_f32_16x16x32_bf16 v[156:159], a[160:163], v[120:123], v[156:159]\n"
      "buffer_load_dwordx4 v0, s[8:11], s51 offen offset:2048 sc1 lds\n"
      "v_mfma_f32_16x16x32_bf16 v[156:159], a[164:167], v[124:127], v[156:159]\n"
      "s_waitcnt vmcnt(14)\n"
      "v_lshlrev_b32 v232, 16, v184\n"
      "v_and_b32 v177, 0xffff0000, v177\n"
      "v_mfma_f32_16x16x32_bf16 v[156:159], a[168:171], v[128:131], v[156:159]\n"
      "s_add_u32 s40, s40, 4294856704\n"
      "v_add_f32 v167, v167, v177\n"
      "v_and_b32 v180, 0xffff0000, v180\n"
      "v_mfma_f32_16x16x32_bf16 v[156:159], a[172:175], v[132:135], v[156:159]\n"
      "v_add_f32 v164, v164, v232\n"
      "v_lshlrev_b32 v233, 16, v185\n"
      "v_mfma_f32_16x16x32_bf16 v[156:159], a[176:179], v[136:139], v[156:159]\n"
      "buffer_load_dwordx4 v0, s[8:11], s51 offen offset:3072 sc1 lds\n"
      "v_mfma_f32_16x16x32_bf16 v[156:159], a[180:183], v[140:143], v[156:159]\n"
      "s_waitcnt lgkmcnt(0)\n"
      "v_add_f32 v166, v166, v233\n"
      "v_cmp_gt_i32 vcc, v14, v15\n"
      "v_mfma_f32_16x16x32_bf16 v[156:159], a[184:187], v[144:147], v[156:159]\n"
      "s_waitcnt vmcnt(12)\n"
      "v_lshlrev_b32 v232, 16, v188\n"
      "v_add_f32 v172, v172, v232\n"
      "v_mfma_f32_16x16x32_bf16 v[156:159], a[188:191], v[148:151], v[156:159]\n"
      "v_and_b32 v185, 0xffff0000, v185\n"
      "v_lshlrev_b32 v232, 16, v192\n"
      "v_mfma_f32_16x16x32_bf16 v[160:163], a[192:195], v[56:59], 0\n"
      "s_add_u32 m0, s43, 4096\n"
      "v_lshlrev_b32 v234, 16, v186\n"
      "v_add_f32 v164, v164, v232\n"
      "v_mfma_f32_16x16x32_bf16 v[160:163], a[196:199], v[60:63], v[160:163]\n"
      "buffer_load_dwordx4 v1, s[8:11], s51 offen offset:1024 sc1 lds\n"
      "v_mfma_f32_16x16x32_bf16 v[160:163], a[200:203], v[64:67], v[160:163]\n"
      "s_cbranch_vccnz .Lsp_dy5_14\n"
      ".Lbk_dy5_14:\n"
      "v_cvt_pk_bf16_f32 v227, v158, v159\n"
      "v_mfma_f32_16x16x32_bf16 v[160:163], a[204:207], v[68:71], v[160:163]\n"
      "ds_read_b128 v[56:59], v23 offset:24576\n"
      "v_lshlrev_b32 v233, 16, v189\n"
      "v_mfma_f32_16x16x32_bf16 v[160:163], a[208:211], v[72:75], v[160:163]\n"
      "s_waitcnt vmcnt(12)\n"
      "ds_read_b128 v[64:67], v23 offset:26624\n"
      "v_lshlrev_b32 v232, 16, v196\n"
      "v_mfma_f32_16x16x32_bf16 v[160:163], a[212:215], v[76:79], v[160:163]\n"
      "buffer_load_dwordx4 v1, s[8:11], s51 offen offset:0 sc1 lds\n"
      "v_mfma_f32_16x16x32_bf16 v[160:163], a[216:219], v[80:83], v[160:163]\n"
      "ds_read_b128 v[76:79], v23 offset:29696\n"
      "v_add_f32 v174, v174, v233\n"
      "v_mfma_f32_16x16x32_bf16 v[160:163], a[220:223], v[84:87], v[160:163]\n"
      "v_and_b32 v184, 0xffff0000, v184\n"
      "v_add_f32 v172, v172, v232\n"
      "v_mfma_f32_16x16x32_bf16 v[160:163], a[224:227], v[88:91], v[160:163]\n"
      "v_add_f32 v170, v170, v235\n"
      "v_cvt_pk_bf16_f32 v226, v156, v157\n"
      "v_mfma_f32_16x16x32_bf16 v[160:163], a[228:231], v[92:95], v[160:163]\n"
      "s_add_u32 s41, s41, 6\n"
      "ds_read_b128 v[88:91], v23 offset:32768\n"
      "v_add_f32 v165, v165, v184\n"
      "v_mfma_f32_16x16x32_bf16 v[160:163], a[232:235], v[96:99], v[160:163]\n"
      "ds_read_b128 v[68:71], v23 offset:27648\n"
      "v_mfma_f32_16x16x32_bf16 v[160:163], a[236:239], v[100:103], v[160:163]\n"
      "ds_read_b128 v[96:99], v23 offset:34816\n"
      "v_add_f32 v168, v168, v234\n"
      "v_mfma_f32_16x16x32_bf16 v[160:163], a[240:243], v[104:107], v[160:163]\n"
      "ds_read_b128 v[80:83], v23 offset:30720\n"
      "v_lshlrev_b32 v233, 16, v193\n"
      "v_mfma_f32_16x16x32_bf16 v[160:163], a[244:247], v[108:111], v[160:163]\n"
      "ds_read_b128 v[72:75], v23 offset:28672\n"
      "v_and_b32 v178, 0xffff0000, v178\n"
      "v_mfma_f32_16x16x32_bf16 v[160:163], a[248:251], v[112:115], v[160:163]\n"
      "s_waitcnt vmcnt(12) lgkmcnt(5)\n"
      "s_mov_b32 s51, s36\n"
      "ds_read_b128 v[112:115], v23 offset:38912\n"
      "v_lshlrev_b32 v232, 16, v200\n"
      "v_mfma_f32_16x16x32_bf16 v[160:163], a[252:255], v[116:119], v[160:163]\n"
      "v_add_f32 v164, v164, v232\n"
      "v_and_b32 v186, 0xffff0000, v186\n"
      "v_mfma_f32_16x16x32_bf16 v[160:163], v[24:27], v[120:123], v[160:163]\n"
      "s_waitcnt vmcnt(10) lgkmcnt(5)\n"
      "buffer_store_dword v18, v4, s[8:11], s52 offen sc1\n"
      "v_mfma_f32_16x16x32_bf16 v[160:163], v[28:31], v[124:127], v[160:163]\n"
      "ds_read_b128 v[60:63], v23 offset:25600\n"
      "v_add_f32 v169, v169, v178\n"
      "v_mfma_f32_16x16x32_bf16 v[160:163], v[32:35], v[128:131], v[160:163]\n"
      "buffer_store_dwordx4 v[224:227], v0, s[8:11], s37 offen sc1\n"
      "v_mfma_f32_16x16x32_bf16 v[160:163], v[36:39], v[132:135], v[160:163]\n"
      "ds_read_b128 v[104:107], v23 offset:36864\n"
      "v_and_b32 v200, 0xffff0000, v200\n"
      "v_mfma_f32_16x16x32_bf16 v[160:163], v[40:43], v[136:139], v[160:163]\n"
      "ds_read_b128 v[84:87], v23 offset:31744\n"
      "v_add_f32 v173, v173, v180\n"
      "v_mfma_f32_16x16x32_bf16 v[160:163], v[44:47], v[140:143], v[160:163]\n"
      "ds_read_b128 v[140:143], v23 offset:46080\n"
      "v_add_f32 v166, v166, v233\n"
      "v_mfma_f32_16x16x32_bf16 v[160:163], v[48:51], v[144:147], v[160:163]\n"
      "ds_read_b128 v[100:103], v23 offset:35840\n"
      "v_lshlrev_b32_e64 v234, 16, v194\n"
      "v_mfma_f32_16x16x32_bf16 v[160:163], v[52:55], v[148:151], v[160:163]\n"
      "s_sub_u32 s59, s59, 1\n"
      "s_cbranch_scc1 .Ld_out4\n"
      ".Ld_t5:\n"
      "v_mfma_f32_16x16x32_bf16 v[152:155], a[0:3], v[56:59], 0\n"
      "s_waitcnt lgkmcnt(4)\n"
      "ds_read_b128 v[92:95], v23 offset:33792\n"
      "ds_read_b128 v[116:119], v23 offset:39936\n"
      "v_mfma_f32_16x16x32_bf16 v[152:155], a[4:7], v[60:63], v[152:155]\n"
      "ds_read_b128 v[108:111], v23 offset:37888\n"
      "v_and_b32 v192, 0xffff0000, v192\n"
      "v_mfma_f32_16x16x32_bf16 v[152:155], a[8:11], v[64:67], v[152:155]\n"
      "ds_read_b128 v[128:131], v23 offset:43008\n"
      "v_cvt_pk_bf16_f32 v229, v162, v163\n"
      "v_mfma_f32_16x16x32_bf16 v[152:155], a[12:15], v[68:71], v[152:155]\n"
      "ds_read_b128 v[136:139], v23 offset:45056\n"
      "v_add_f32 v175, v175, v181\n"
      "v_mfma_f32_16x16x32_bf16 v[152:155], a[16:19], v[72:75], v[152:155]\n"
      "v_cvt_pk_bf16_f32 v228, v160, v161\n"
      "v_and_b32 v189, 0xffff0000, v189\n"
      "v_mfma_f32_16x16x32_bf16 v[152:155], a[20:23], v[76:79], v[152:155]\n"
      "s_waitcnt vmcnt(11) lgkmcnt(5)\n"
      "ds_read_b128 v[144:147], v23 offset:47104\n"
      "v_lshlrev_b32 v232, 16, v204\n"
      "v_mfma_f32_16x16x32_bf16 v[152:155], a[24:27], v[80:83], v[152:155]\n"
      "buffer_store_dwordx2 v[228:229], v13, s[8:11], s37 offen sc1\n"
      "v_mfma_f32_16x16x32_bf16 v[152:155], a[28:31], v[84:87], v[152:155]\n"
      "v_lshlrev_b32 v235, 16, v187\n"
      "v_add_f32 v172, v172, v232\n"
      "v_mfma_f32_16x16x32_bf16 v[152:155], a[32:35], v[88:91], v[152:155]\n"
      "s_waitcnt vmcnt(10) lgkmcnt(3)\n"
      "ds_read_b128 v[120:123], v23 offset:40960\n"
      "v_lshlrev_b32 v232, 16, v208\n"
      "v_mfma_f32_16x16x32_bf16 v[152:155], a[36:39], v[92:95], v[152:155]\n"
      "ds_read_b128 v[124:127], v23 offset:41984\n"
      "v_add_f32 v170, v170, v235\n"
      "v_mfma_f32_16x16x32_bf16 v[152:155], a[40:43], v[96:99], v[152:155]\n"
      "s_add_u32 m0, s43, 24576\n"
      "v_and_b32 v194, 0xffff0000, v194\n"
      "v_and_b32 v208, 0xffff0000, v208\n"
      "v_mfma_f32_16x16x32_bf16 v[152:155], a[44:47], v[100:103], v[152:155]\n"
      "ds_read_b128 v[132:135], v23 offset:44032\n"
      "v_add_f32 v167, v167, v185\n"
      "v_mfma_f32_16x16x32_bf16 v[152:155], a[48:51], v[104:107], v[152:155]\n"
      "v_lshlrev_b32 v233, 16, v197\n"
      "v_add_u32 v14, 1, v14\n"
      "v_mfma_f32_16x16x32_bf16 v[152:155], a[52:55], v[108:111], v[152:155]\n"
      "s_add_u32 s36, s36, 165888\n"
      "v_lshlrev_b32 v235, 16, v195\n"
      "v_add_f32 v168, v168, v234\n"
      "v_mfma_f32_16x16x32_bf16 v[152:155], a[56:59], v[112:115], v[152:155]\n"
      "buffer_load_dwordx4 v[216:219], v6, s[8:11], s38 offen sc1\n"
      "v_mfma_f32_16x16x32_bf16 v[152:155], a[60:63], v[116:119], v[152:155]\n"
      "s_waitcnt lgkmcnt(2)\n"
      "ds_read_b128 v[148:151], v23 offset:48128\n"
      "v_add_f32 v164, v164, v232\n"
      "v_mfma_f32_16x16x32_bf16 v[152:155], a[64:67], v[120:123], v[152:155]\n"
      "s_waitcnt lgkmcnt(2)\n"
      "v_add_f32 v165, v165, v192\n"
      "v_lshlrev_b32 v234, 16, v202\n"
      "v_mfma_f32_16x16x32_bf16 v[152:155], a[68:71], v[124:127], v[152:155]\n"
      "v_add_f32 v174, v174, v233\n"
      "v_lshlrev_b32 v232, 16, v212\n"
      "v_mfma_f32_16x16x32_bf16 v[152:155], a[72:75], v[128:131], v[152:155]\n"
      "s_waitcnt lgkmcnt(1)\n"
      "v_and_b32 v179, 0xffff0000, v179\n"
      "v_add_f32 v169, v169, v186\n"
      "v_mfma_f32_16x16x32_bf16 v[152:155], a[76:79], v[132:135], v[152:155]\n"
      "s_waitcnt vmcnt(8)\n"
      "v_lshlrev_b32 v233, 16, v201\n"
      "v_cmp_ne_u32 vcc, s21, v20\n"
      "v_mfma_f32_16x16x32_bf16 v[152:155], a[80:83], v[136:139], v[152:155]\n"
      "buffer_load_dwordx2 v[220:221], v7, s[8:11], s38 offen sc1\n"
      "v_mfma_f32_16x16x32_bf16 v[152:155], a[84:87], v[140:143], v[152:155]\n"
      "v_add_f32 v166, v166, v233\n"
      "v_and_b32 v193, 0xffff0000, v193\n"
      "v_mfma_f32_16x16x32_bf16 v[152:155], a[88:91], v[144:147], v[152:155]\n"
      "s_waitcnt lgkmcnt(0)\n"
      "v_and_b32 v197, 0xffff0000, v197\n"
      "v_lshlrev_b32 v233, 16, v205\n"
      "v_mfma_f32_16x16x32_bf16 v[152:155], a[92:95], v[148:151], v[152:155]\n"
      "v_add_f32 v170, v170, v235\n"
      "v_add_f32 v174, v174, v233\n"
      "v_mfma_f32_16x16x32_bf16 v[156:159], a[96:99], v[56:59], 0\n"
      "s_cbranch_vccnz .Lsp_d1_15\n"
      ".Lbk_d1_15:\n"
      "v_lshlrev_b32 v235, 16, v203\n"
      "v_mfma_f32_16x16x32_bf16 v[156:159], a[100:103], v[60:63], v[156:159]\n"
      "buffer_load_dwordx4 v0, s[8:11], s51 offen offset:0 sc1 lds\n"
      "v_mfma_f32_16x16x32_bf16 v[156:159], a[104:107], v[64:67], v[156:159]\n"
      "v_and_b32 v187, 0xffff0000, v187\n"
      "v_lshlrev_b32 v233, 16, v209\n"
      "v_mfma_f32_16x16x32_bf16 v[156:159], a[108:111], v[68:71], v[156:159]\n"
      "v_add_f32 v170, v170, v235\n"
      "v_add_f32 v166, v166, v233\n"
      "v_mfma_f32_16x16x32_bf16 v[156:159], a[112:115], v[72:75], v[156:159]\n"
      "v_and_b32 v201, 0xffff0000, v201\n"
      "v_add_f32 v165, v165, v200\n"
      "v_mfma_f32_16x16x32_bf16 v[156:159], a[116:119], v[76:79], v[156:159]\n"
      "v_and_b32 v188, 0xffff0000, v188\n"
      "v_add_f32 v165, v165, v208\n"
      "v_mfma_f32_16x16x32_bf16 v[156:159], a[120:123], v[80:83], v[156:159]\n"
      "buffer_load_dwordx4 v0, s[8:11], s51 offen offset:1024 sc1 lds\n"
      "v_mfma_f32_16x16x32_bf16 v[156:159], a[124:127], v[84:87], v[156:159]\n"
      "v_add_f32 v168, v168, v234\n"
      "v_mul_f32 v237, v164, v164\n"
      "v_mfma_f32_16x16x32_bf16 v[156:159], a[128:131], v[88:91], v[156:159]\n"
      "v_add_f32 v167, v167, v193\n"
      "v_lshlrev_b32 v233, 16, v213\n"
      "v_mfma_f32_16x16x32_bf16 v[156:159], a[132:135], v[92:95], v[156:159]\n"
      "s_waitcnt vmcnt(7)\n"
      "ds_write_b32 v16, v14\n"
      "v_mfma_f32_16x16x32_bf16 v[156:159], a[136:139], v[96:99], v[156:159]\n"
      "buffer_load_dwordx4 v0, s[8:11], s51 offen offset:2048 sc1 lds\n"
      "v_mfma_f32_16x16x32_bf16 v[156:159], a[140:143], v[100:103], v[156:159]\n"
      "v_add_f32 v171, v171, v179\n"
      "v_add_f32 v167, v167, v201\n"
      "v_mfma_f32_16x16x32_bf16 v[156:159], a[144:147], v[104:107], v[156:159]\n"
      "v_lshlrev_b32 v234, 16, v210\n"
      "v_and_b32 v213, 0xffff0000, v213\n"
      "v_mfma_f32_16x16x32_bf16 v[156:159], a[148:151], v[108:111], v[156:159]\n"
      "s_min_u32 s36, s36, s6\n"
      "v_add_f32 v173, v173, v188\n"
      "v_and_b32 v209, 0xffff0000, v209\n"
      "v_mfma_f32_16x16x32_bf16 v[156:159], a[152:155], v[112:115], v[156:159]\n"
      "buffer_load_dword v20, v2, s[8:11], s36 offen sc1\n"
      "v_mfma_f32_16x16x32_bf16 v[156:159], a[156:159], v[116:119], v[156:159]\n"
      "v_add_f32 v236, v164, v165\n"
      "v_add_f32 v175, v175, v189\n"
      "v_mfma_f32_16x16x32_bf16 v[156:159], a[160:163], v[120:123], v[156:159]\n"
      "buffer_load_dwordx4 v0, s[8:11], s51 offen offset:3072 sc1 lds\n"
      "v_mfma_f32_16x16x32_bf16 v[156:159], a[164:167], v[124:127], v[156:159]\n"
      "v_add_f32 v167, v167, v209\n"
      "v_and_b32 v203, 0xffff0000, v203\n"
      "v_mfma_f32_16x16x32_bf16 v[156:159], a[168:171], v[128:131], v[156:159]\n"
      "ds_read_b32 v15, v17\n"
      "v_fmac_f32 v237, v165, v165\n"
      "v_mfma_f32_16x16x32_bf16 v[156:159], a[172:175], v[132:135], v[156:159]\n"
      "s_add_u32 m0, s43, 28672\n"
      "v_add_f32 v175, v175, v197\n"
      "v_and_b32 v204, 0xffff0000, v204\n"
      "v_mfma_f32_16x16x32_bf16 v[156:159], a[176:179], v[136:139], v[156:159]\n"
      "v_and_b32 v195, 0xffff0000, v195\n"
      "v_add_f32 v172, v172, v232\n"
      "v_mfma_f32_16x16x32_bf16 v[156:159], a[180:183], v[140:143], v[156:159]\n"
      "buffer_load_dwordx4 v1, s[8:11], s51 offen offset:0 sc1 lds\n"
      "v_mfma_f32_16x16x32_bf16 v[156:159], a[184:187], v[144:147], v[156:159]\n"
      "v_fmac_f32 v237, v166, v166\n"
      "v_cvt_pk_bf16_f32 v225, v154, v155\n"
      "v_mfma_f32_16x16x32_bf16 v[156:159], a[188:191], v[148:151], v[156:159]\n"
      "s_waitcnt lgkmcnt(0)\n"
      "v_add_f32 v169, v169, v194\n"
      "v_cmp_gt_i32 vcc, v14, v15\n"
      "v_mfma_f32_16x16x32_bf16 v[160:163], a[192:195], v[56:59], 0\n"
      "v_and_b32 v196, 0xffff0000, v196\n"
      "v_and_b32 v212, 0xffff0000, v212\n"
      "v_mfma_f32_16x16x32_bf16 v[160:163], a[196:199], v[60:63], v[160:163]\n"
      "buffer_load_dwordx4 v1, s[8:11], s51 offen offset:1024 sc1 lds\n"
      "v_mfma_f32_16x16x32_bf16 v[160:163], a[200:203], v[64:67], v[160:163]\n"
      "s_cbranch_vccnz .Lsp_dy0_16\n"
      ".Lbk_dy0_16:\n"
      "v_mfma_f32_16x16x32_bf16 v[160:163], a[204:207], v[68:71], v[160:163]\n"
      "ds_read_b128 v[56:59], v0 offset:0\n"
      "v_cvt_pk_bf16_f32 v177, v166, v167\n"
      "v_mfma_f32_16x16x32_bf16 v[160:163], a[208:211], v[72:75], v[160:163]\n"
      "v_add_f32 v171, v171, v187\n"
      "v_and_b32 v205, 0xffff0000, v205\n"
      "v_mfma_f32_16x16x32_bf16 v[160:163], a[212:215], v[76:79], v[160:163]\n"
      "ds_read_b128 v[60:63], v0 offset:1024\n"
      "v_cvt_pk_bf16_f32 v224, v152, v153\n"
      "v_mfma_f32_16x16x32_bf16 v[160:163], a[216:219], v[80:83], v[160:163]\n"
      "s_add_u32 s52, s52, 106496\n"
      "v_and_b32 v202, 0xffff0000, v202\n"
      "v_add_f32 v169, v169, v202\n"
      "v_mfma_f32_16x16x32_bf16 v[160:163], a[220:223], v[84:87], v[160:163]\n"
      "ds_read_b128 v[84:87], v0 offset:7168\n"
      "v_add_f32 v236, v236, v166\n"
      "v_mfma_f32_16x16x32_bf16 v[160:163], a[224:227], v[88:91], v[160:163]\n"
      "ds_read_b128 v[72:75], v0 offset:4096\n"
      "v_and_b32 v210, 0xffff0000, v210\n"
      "v_mfma_f32_16x16x32_bf16 v[160:163], a[228:231], v[92:95], v[160:163]\n"
      "v_add_f32 v173, v173, v196\n"
      "v_lshlrev_b32 v235, 16, v211\n"
      "v_mfma_f32_16x16x32_bf16 v[160:163], a[232:235], v[96:99], v[160:163]\n"
      "v_add_f32 v170, v170, v235\n"
      "v_add_f32 v171, v171, v195\n"
      "v_mfma_f32_16x16x32_bf16 v[160:163], a[236:239], v[100:103], v[160:163]\n"
      "s_add_u32 s37, s37, 106496\n"
      "ds_read_b128 v[88:91], v0 offset:8192\n"
      "v_cvt_pk_bf16_f32 v227, v158, v159\n"
      "v_mfma_f32_16x16x32_bf16 v[160:163], a[240:243], v[104:107], v[160:163]\n"
      "s_waitcnt vmcnt(9) lgkmcnt(3)\n"
      "buffer_store_dword v18, v4, s[8:11], s52 offen sc1\n"
      "v_mfma_f32_16x16x32_bf16 v[160:163], a[244:247], v[108:111], v[160:163]\n"
      "ds_read_b128 v[68:71], v0 offset:3072\n"
      "v_add_f32 v174, v174, v233\n"
      "v_mfma_f32_16x16x32_bf16 v[160:163], a[248:251], v[112:115], v[160:163]\n"
      "ds_read_b128 v[104:107], v0 offset:12288\n"
      "v_add_f32 v169, v169, v210\n"
      "v_mfma_f32_16x16x32_bf16 v[160:163], a[252:255], v[116:119], v[160:163]\n"
      "ds_read_b128 v[96:99], v0 offset:10240\n"
      "v_fmac_f32 v237, v167, v167\n"
      "v_mfma_f32_16x16x32_bf16 v[160:163], v[24:27], v[120:123], v[160:163]\n"
      "ds_read_b128 v[80:83], v0 offset:6144\n"
      "v_add_f32 v175, v175, v205\n"
      "v_mfma_f32_16x16x32_bf16 v[160:163], v[28:31], v[124:127], v[160:163]\n"
      "ds_read_b128 v[64:67], v0 offset:2048\n"
      "v_add_f32 v168, v168, v234\n"
      "v_mfma_f32_16x16x32_bf16 v[160:163], v[32:35], v[128:131], v[160:163]\n"
      "v_add_f32 v236, v236, v167\n"
      "v_cvt_pk_bf16_f32 v226, v156, v157\n"
      "v_mfma_f32_16x16x32_bf16 v[160:163], v[36:39], v[132:135], v[160:163]\n"
      "ds_read_b128 v[92:95], v0 offset:9216\n"
      "v_cvt_pk_bf16_f32 v176, v164, v165\n"
      "v_mfma_f32_16x16x32_bf16 v[160:163], v[40:43], v[136:139], v[160:163]\n"
      "buffer_store_dwordx4 v[224:227], v0, s[8:11], s37 offen sc1\n"
      "ds_read_b128 v[76:79], v0 offset:5120\n"
      "v_mfma_f32_16x16x32_bf16 v[160:163], v[44:47], v[140:143], v[160:163]\n"
      "ds_read_b128 v[132:135], v0 offset:19456\n"
      "v_and_b32 v211, 0xffff0000, v211\n"
      "v_mfma_f32_16x16x32_bf16 v[160:163], v[48:51], v[144:147], v[160:163]\n"
      "ds_read_b128 v[100:103], v0 offset:11264\n"
      "v_add_f32 v171, v171, v203\n"
      "v_mfma_f32_16x16x32_bf16 v[160:163], v[52:55], v[148:151], v[160:163]\n"
      "s_sub_u32 s59, s59, 1\n"
      "s_cbranch_scc0 .Ld_t0\n"
      ".Ld_out5:\n"
      "s_nop 8\n"
      "v_cvt_pk_bf16_f32 v228, v160, v161\n"
      "v_cvt_pk_bf16_f32 v229, v162, v163\n"
      "buffer_store_dwordx2 v[228:229], v13, s[8:11], s37 offen sc1\n"
      "v_cvt_pk_bf16_f32 v178, v168, v169\n"
      "v_add_f32 v175, v175, v213\n"
      "v_add_f32 v171, v171, v211\n"
      "v_add_f32 v173, v173, v204\n"
      "v_cvt_pk_bf16_f32 v179, v170, v171\n"
      "v_add_f32 v173, v173, v212\n"
      "v_cvt_pk_bf16_f32 v180, v172, v173\n"
      "v_cvt_pk_bf16_f32 v181, v174, v175\n"
      "buffer_store_dwordx2 v[180:181], v9, s[16:19], s57 offen\n"
      "buffer_store_dwordx4 v[176:179], v8, s[16:19], s57 offen\n"
      "s_add_u32 s52, s52, 106496\n"
      "s_add_u32 s37, s37, 173056\n"
      "s_waitcnt vmcnt(2)\n"
      "buffer_store_dword v18, v4, s[8:11], s52 offen sc1\n"
      "s_add_u32 s52, s52, 173056\n"
      "s_branch .Ld_end\n"
      ".Ld_out0:\n"
      "s_nop 8\n"
      "v_add_f32 v174, v174, v162\n"
      "v_add_f32 v170, v170, v158\n"
      "v_lshlrev_b32 v172, 16, v220\n"
      "s_add_u32 s38, s38, 147456\n"
      "v_add_f32 v171, v171, v159\n"
      "v_add_f32 v169, v169, v157\n"
      "v_add_f32 v172, v172, v160\n"
      "v_add_f32 v175, v175, v163\n"
      "v_and_b32 v173, 0xffff0000, v220\n"
      "v_add_f32 v173, v173, v161\n"
      "s_add_u32 s52, s52, 173056\n"
      "buffer_load_dword v21, v3, s[8:11], s53 offen sc1\n"
      "s_add_u32 s55, s53, 6656\n"
      "s_add_u32 s54, s53, 13312\n"
      "s_waitcnt vmcnt(0)\n"
      "v_cmp_ne_u32 vcc, s21, v21\n"
      "s_cbranch_vccnz .Lsp_df_17\n"
      ".Lbk_df_17:\n"
      "buffer_load_dwordx4 v[176:179], v0, s[8:11], s53 offen sc1\n"
      "buffer_load_dwordx2 v[180:181], v13, s[8:11], s53 offen sc1\n"
      "s_add_u32 s58, s41, 4294967264\n"
      "buffer_load_dwordx4 v[184:187], v0, s[8:11], s55 offen sc1\n"
      "buffer_load_dwordx4 v[192:195], v0, s[8:11], s54 offen sc1\n"
      "buffer_load_dwordx2 v[188:189], v13, s[8:11], s55 offen sc1\n"
      "s_add_u32 s55, s53, 19968\n"
      "buffer_load_dwordx2 v[196:197], v13, s[8:11], s54 offen sc1\n"
      "buffer_load_dwordx4 v[200:203], v0, s[8:11], s55 offen sc1\n"
      "buffer_load_dwordx2 v[204:205], v13, s[8:11], s55 offen sc1\n"
      "s_cmp_lt_u32 s41, 32\n"
      "s_cselect_b32 s56, s39, 0x40000000\n"
      "s_add_u32 s54, s53, 26624\n"
      "s_add_u32 s53, s53, 599040\n"
      "buffer_load_dwordx2 v[212:213], v13, s[8:11], s54 offen sc1\n"
      "s_add_u32 s39, s39, 147456\n"
      "buffer_load_dwordx4 v[208:211], v0, s[8:11], s54 offen sc1\n"
      "s_cmp_lt_u32 s58, 32\n"
      "s_cselect_b32 s57, s40, 0x40000000\n"
      "s_waitcnt vmcnt(9)\n"
      "v_lshlrev_b32 v234, 16, v178\n"
      "v_lshlrev_b32 v233, 16, v177\n"
      "v_lshlrev_b32 v232, 16, v176\n"
      "v_add_f32 v166, v166, v233\n"
      "v_add_f32 v164, v164, v232\n"
      "s_waitcnt vmcnt(8)\n"
      "v_lshlrev_b32 v233, 16, v181\n"
      "v_lshlrev_b32 v235, 16, v179\n"
      "v_and_b32 v176, 0xffff0000, v176\n"
      "v_lshlrev_b32 v232, 16, v180\n"
      "v_add_f32 v172, v172, v232\n"
      "v_and_b32 v181, 0xffff0000, v181\n"
      "v_add_f32 v168, v168, v234\n"
      "v_add_f32 v174, v174, v233\n"
      "v_add_f32 v165, v165, v176\n"
      "s_waitcnt vmcnt(7)\n"
      "v_lshlrev_b32 v232, 16, v184\n"
      "v_and_b32 v177, 0xffff0000, v177\n"
      "s_add_u32 s40, s40, 4294856704\n"
      "v_add_f32 v167, v167, v177\n"
      "v_and_b32 v180, 0xffff0000, v180\n"
      "v_add_f32 v164, v164, v232\n"
      "v_lshlrev_b32 v233, 16, v185\n"
      "v_add_f32 v166, v166, v233\n"
      "s_waitcnt vmcnt(5)\n"
      "v_lshlrev_b32 v232, 16, v188\n"
      "v_add_f32 v172, v172, v232\n"
      "v_and_b32 v185, 0xffff0000, v185\n"
      "v_lshlrev_b32 v232, 16, v192\n"
      "v_lshlrev_b32 v234, 16, v186\n"
      "v_add_f32 v164, v164, v232\n"
      "v_lshlrev_b32 v233, 16, v189\n"
      "s_waitcnt vmcnt(4)\n"
      "v_lshlrev_b32 v232, 16, v196\n"
      "v_add_f32 v174, v174, v233\n"
      "v_and_b32 v184, 0xffff0000, v184\n"
      "v_add_f32 v172, v172, v232\n"
      "v_add_f32 v170, v170, v235\n"
      "s_add_u32 s41, s41, 6\n"
      "v_add_f32 v165, v165, v184\n"
      "v_add_f32 v168, v168, v234\n"
      "v_lshlrev_b32 v233, 16, v193\n"
      "v_and_b32 v178, 0xffff0000, v178\n"
      "s_waitcnt vmcnt(3)\n"
      "v_lshlrev_b32 v232, 16, v200\n"
      "v_add_f32 v164, v164, v232\n"
      "v_and_b32 v186, 0xffff0000, v186\n"
      "v_add_f32 v169, v169, v178\n"
      "v_and_b32 v200, 0xffff0000, v200\n"
      "v_add_f32 v173, v173, v180\n"
      "v_add_f32 v166, v166, v233\n"
      "v_lshlrev_b32 v234, 16, v194\n"
      "v_and_b32 v192, 0xffff0000, v192\n"
      "v_add_f32 v175, v175, v181\n"
      "v_and_b32 v189, 0xffff0000, v189\n"
      "s_waitcnt vmcnt(2)\n"
      "v_lshlrev_b32 v232, 16, v204\n"
      "v_lshlrev_b32 v235, 16, v187\n"
      "v_add_f32 v172, v172, v232\n"
      "s_waitcnt vmcnt(0)\n"
      "v_lshlrev_b32 v232, 16, v208\n"
      "v_add_f32 v170, v170, v235\n"
      "v_and_b32 v194, 0xffff0000, v194\n"
      "v_and_b32 v208, 0xffff0000, v208\n"
      "v_add_f32 v167, v167, v185\n"
      "v_lshlrev_b32 v233, 16, v197\n"
      "v_lshlrev_b32 v235, 16, v195\n"
      "v_add_f32 v168, v168, v234\n"
      "v_add_f32 v164, v164, v232\n"
      "v_add_f32 v165, v165, v192\n"
      "v_lshlrev_b32 v234, 16, v202\n"
      "v_add_f32 v174, v174, v233\n"
      "v_lshlrev_b32 v232, 16, v212\n"
      "v_and_b32 v179, 0xffff0000, v179\n"
      "v_add_f32 v169, v169, v186\n"
      "v_lshlrev_b32 v233, 16, v201\n"
      "v_add_f32 v166, v166, v233\n"
      "v_and_b32 v193, 0xffff0000, v193\n"
      "v_and_b32 v197, 0xffff0000, v197\n"
      "v_lshlrev_b32 v233, 16, v205\n"
      "v_add_f32 v170, v170, v235\n"
      "v_add_f32 v174, v174, v233\n"
      "v_lshlrev_b32 v235, 16, v203\n"
      "v_and_b32 v187, 0xffff0000, v187\n"
      "v_lshlrev_b32 v233, 16, v209\n"
      "v_add_f32 v170, v170, v235\n"
      "v_add_f32 v166, v166, v233\n"
      "v_and_b32 v201, 0xffff0000, v201\n"
      "v_add_f32 v165, v165, v200\n"
      "v_and_b32 v188, 0xffff0000, v188\n"
      "v_add_f32 v165, v165, v208\n"
      "v_add_f32 v168, v168, v234\n"
      "v_add_f32 v167, v167, v193\n"
      "v_lshlrev_b32 v233, 16, v213\n"
      "v_add_f32 v171, v171, v179\n"
      "v_add_f32 v167, v167, v201\n"
      "v_lshlrev_b32 v234, 16, v210\n"
      "v_and_b32 v213, 0xffff0000, v213\n"
      "v_add_f32 v173, v173, v188\n"
      "v_and_b32 v209, 0xffff0000, v209\n"
      "v_add_f32 v175, v175, v189\n"
      "v_add_f32 v167, v167, v209\n"
      "v_and_b32 v203, 0xffff0000, v203\n"
      "v_add_f32 v175, v175, v197\n"
      "v_and_b32 v204, 0xffff0000, v204\n"
      "v_and_b32 v195, 0xffff0000, v195\n"
      "v_add_f32 v172, v172, v232\n"
      "v_add_f32 v169, v169, v194\n"
      "v_and_b32 v196, 0xffff0000, v196\n"
      "v_and_b32 v212, 0xffff0000, v212\n"
      "v_cvt_pk_bf16_f32 v177, v166, v167\n"
      "v_add_f32 v171, v171, v187\n"
      "v_and_b32 v205, 0xffff0000, v205\n"
      "v_and_b32 v202, 0xffff0000, v202\n"
      "v_add_f32 v169, v169, v202\n"
      "v_and_b32 v210, 0xffff0000, v210\n"
      "v_add_f32 v173, v173, v196\n"
      "v_lshlrev_b32 v235, 16, v211\n"
      "v_add_f32 v170, v170, v235\n"
      "v_add_f32 v171, v171, v195\n"
      "v_add_f32 v174, v174, v233\n"
      "v_add_f32 v169, v169, v210\n"
      "v_add_f32 v175, v175, v205\n"
      "v_add_f32 v168, v168, v234\n"
      "v_cvt_pk_bf16_f32 v176, v164, v165\n"
      "v_and_b32 v211, 0xffff0000, v211\n"
      "v_add_f32 v171, v171, v203\n"
      "v_cvt_pk_bf16_f32 v178, v168, v169\n"
      "v_add_f32 v175, v175, v213\n"
      "v_add_f32 v171, v171, v211\n"
      "v_add_f32 v173, v173, v204\n"
      "v_cvt_pk_bf16_f32 v179, v170, v171\n"
      "v_add_f32 v173, v173, v212\n"
      "v_cvt_pk_bf16_f32 v180, v172, v173\n"
      "v_cvt_pk_bf16_f32 v181, v174, v175\n"
      "buffer_store_dwordx2 v[180:181], v9, s[16:19], s57 offen\n"
      "buffer_store_dwordx4 v[176:179], v8, s[16:19], s57 offen\n"
      "s_branch .Ld_end\n"
      ".Ld_out1:\n"
      "s_nop 8\n"
      "v_cvt_pk_bf16_f32 v229, v162, v163\n"
      "v_cvt_pk_bf16_f32 v228, v160, v161\n"
      "buffer_store_dwordx2 v[228:229], v13, s[8:11], s37 offen sc1\n"
      "s_waitcnt vmcnt(0)\n"
      "buffer_store_dword v18, v4, s[8:11], s52 offen sc1\n"
      "buffer_load_dword v21, v3, s[8:11], s53 offen sc1\n"
      "s_add_u32 s37, s37, 106496\n"
      "s_add_u32 s52, s52, 106496\n"
      "s_add_u32 s55, s53, 6656\n"
      "s_add_u32 s54, s53, 13312\n"
      "s_waitcnt vmcnt(0)\n"
      "v_cmp_ne_u32 vcc, s21, v21\n"
      "s_cbranch_vccnz .Lsp_df_18\n"
      ".Lbk_df_18:\n"
      "buffer_load_dwordx4 v[176:179], v0, s[8:11], s53 offen sc1\n"
      "buffer_load_dwordx2 v[180:181], v13, s[8:11], s53 offen sc1\n"
      "s_add_u32 s58, s41, 4294967264\n"
      "buffer_load_dwordx4 v[184:187], v0, s[8:11], s55 offen sc1\n"
      "buffer_load_dwordx4 v[192:195], v0, s[8:11], s54 offen sc1\n"
      "buffer_load_dwordx2 v[188:189], v13, s[8:11], s55 offen sc1\n"
      "s_add_u32 s55, s53, 19968\n"
      "buffer_load_dwordx2 v[196:197], v13, s[8:11], s54 offen sc1\n"
      "buffer_load_dwordx4 v[200:203], v0, s[8:11], s55 offen sc1\n"
      "buffer_load_dwordx2 v[204:205], v13, s[8:11], s55 offen sc1\n"
      "s_cmp_lt_u32 s41, 32\n"
      "s_cselect_b32 s56, s39, 0x40000000\n"
      "s_add_u32 s54, s53, 26624\n"
      "s_add_u32 s53, s53, 599040\n"
      "buffer_load_dwordx2 v[212:213], v13, s[8:11], s54 offen sc1\n"
      "s_add_u32 s39, s39, 147456\n"
      "buffer_load_dwordx4 v[208:211], v0, s[8:11], s54 offen sc1\n"
      "s_cmp_lt_u32 s58, 32\n"
      "s_cselect_b32 s57, s40, 0x40000000\n"
      "s_waitcnt vmcnt(9)\n"
      "v_lshlrev_b32 v234, 16, v178\n"
      "v_lshlrev_b32 v233, 16, v177\n"
      "v_lshlrev_b32 v232, 16, v176\n"
      "v_add_f32 v166, v166, v233\n"
      "v_add_f32 v164, v164, v232\n"
      "s_waitcnt vmcnt(8)\n"
      "v_lshlrev_b32 v233, 16, v181\n"
      "v_lshlrev_b32 v235, 16, v179\n"
      "v_and_b32 v176, 0xffff0000, v176\n"
      "v_lshlrev_b32 v232, 16, v180\n"
      "v_add_f32 v172, v172, v232\n"
      "v_and_b32 v181, 0xffff0000, v181\n"
      "v_add_f32 v168, v168, v234\n"
      "v_add_f32 v174, v174, v233\n"
      "v_add_f32 v165, v165, v176\n"
      "s_waitcnt vmcnt(7)\n"
      "v_lshlrev_b32 v232, 16, v184\n"
      "v_and_b32 v177, 0xffff0000, v177\n"
      "s_add_u32 s40, s40, 4294856704\n"
      "v_add_f32 v167, v167, v177\n"
      "v_and_b32 v180, 0xffff0000, v180\n"
      "v_add_f32 v164, v164, v232\n"
      "v_lshlrev_b32 v233, 16, v185\n"
      "v_add_f32 v166, v166, v233\n"
      "s_waitcnt vmcnt(5)\n"
      "v_lshlrev_b32 v232, 16, v188\n"
      "v_add_f32 v172, v172, v232\n"
      "v_and_b32 v185, 0xffff0000, v185\n"
      "v_lshlrev_b32 v232, 16, v192\n"
      "v_lshlrev_b32 v234, 16, v186\n"
      "v_add_f32 v164, v164, v232\n"
      "v_lshlrev_b32 v233, 16, v189\n"
      "s_waitcnt vmcnt(4)\n"
      "v_lshlrev_b32 v232, 16, v196\n"
      "v_add_f32 v174, v174, v233\n"
      "v_and_b32 v184, 0xffff0000, v184\n"
      "v_add_f32 v172, v172, v232\n"
      "v_add_f32 v170, v170, v235\n"
      "s_add_u32 s41, s41, 6\n"
      "v_add_f32 v165, v165, v184\n"
      "v_add_f32 v168, v168, v234\n"
      "v_lshlrev_b32 v233, 16, v193\n"
      "v_and_b32 v178, 0xffff0000, v178\n"
      "s_waitcnt vmcnt(3)\n"
      "v_lshlrev_b32 v232, 16, v200\n"
      "v_add_f32 v164, v164, v232\n"
      "v_and_b32 v186, 0xffff0000, v186\n"
      "v_add_f32 v169, v169, v178\n"
      "v_and_b32 v200, 0xffff0000, v200\n"
      "v_add_f32 v173, v173, v180\n"
      "v_add_f32 v166, v166, v233\n"
      "v_lshlrev_b32 v234, 16, v194\n"
      "v_and_b32 v192, 0xffff0000, v192\n"
      "v_add_f32 v175, v175, v181\n"
      "v_and_b32 v189, 0xffff0000, v189\n"
      "s_waitcnt vmcnt(2)\n"
      "v_lshlrev_b32 v232, 16, v204\n"
      "v_lshlrev_b32 v235, 16, v187\n"
      "v_add_f32 v172, v172, v232\n"
      "s_waitcnt vmcnt(0)\n"
      "v_lshlrev_b32 v232, 16, v208\n"
      "v_add_f32 v170, v170, v235\n"
      "v_and_b32 v194, 0xffff0000, v194\n"
      "v_and_b32 v208, 0xffff0000, v208\n"
      "v_add_f32 v167, v167, v185\n"
      "v_lshlrev_b32 v233, 16, v197\n"
      "v_lshlrev_b32 v235, 16, v195\n"
      "v_add_f32 v168, v168, v234\n"
      "v_add_f32 v164, v164, v232\n"
      "v_add_f32 v165, v165, v192\n"
      "v_lshlrev_b32 v234, 16, v202\n"
      "v_add_f32 v174, v174, v233\n"
      "v_lshlrev_b32 v232, 16, v212\n"
      "v_and_b32 v179, 0xffff0000, v179\n"
      "v_add_f32 v169, v169, v186\n"
      "v_lshlrev_b32 v233, 16, v201\n"
      "v_add_f32 v166, v166, v233\n"
      "v_and_b32 v193, 0xffff0000, v193\n"
      "v_and_b32 v197, 0xffff0000, v197\n"
      "v_lshlrev_b32 v233, 16, v205\n"
      "v_add_f32 v170, v170, v235\n"
      "v_add_f32 v174, v174, v233\n"
      "v_lshlrev_b32 v235, 16, v203\n"
      "v_and_b32 v187, 0xffff0000, v187\n"
      "v_lshlrev_b32 v233, 16, v209\n"
      "v_add_f32 v170, v170, v235\n"
      "v_add_f32 v166, v166, v233\n"
      "v_and_b32 v201, 0xffff0000, v201\n"
      "v_add_f32 v165, v165, v200\n"
      "v_and_b32 v188, 0xffff0000, v188\n"
      "v_add_f32 v165, v165, v208\n"
      "v_add_f32 v168, v168, v234\n"
      "v_add_f32 v167, v167, v193\n"
      "v_lshlrev_b32 v233, 16, v213\n"
      "v_add_f32 v171, v171, v179\n"
      "v_add_f32 v167, v167, v201\n"
      "v_lshlrev_b32 v234, 16, v210\n"
      "v_and_b32 v213, 0xffff0000, v213\n"
      "v_add_f32 v173, v173, v188\n"
      "v_and_b32 v209, 0xffff0000, v209\n"
      "v_add_f32 v175, v175, v189\n"
      "v_add_f32 v167, v167, v209\n"
      "v_and_b32 v203, 0xffff0000, v203\n"
      "v_add_f32 v175, v175, v197\n"
      "v_and_b32 v204, 0xffff0000, v204\n"
      "v_and_b32 v195, 0xffff0000, v195\n"
      "v_add_f32 v172, v172, v232\n"
      "v_add_f32 v169, v169, v194\n"
      "v_and_b32 v196, 0xffff0000, v196\n"
      "v_and_b32 v212, 0xffff0000, v212\n"
      "v_cvt_pk_bf16_f32 v177, v166, v167\n"
      "v_add_f32 v171, v171, v187\n"
      "v_and_b32 v205, 0xffff0000, v205\n"
      "v_and_b32 v202, 0xffff0000, v202\n"
      "v_add_f32 v169, v169, v202\n"
      "v_and_b32 v210, 0xffff0000, v210\n"
      "v_add_f32 v173, v173, v196\n"
      "v_lshlrev_b32 v235, 16, v211\n"
      "v_add_f32 v170, v170, v235\n"
      "v_add_f32 v171, v171, v195\n"
      "v_add_f32 v174, v174, v233\n"
      "v_add_f32 v169, v169, v210\n"
      "v_add_f32 v175, v175, v205\n"
      "v_add_f32 v168, v168, v234\n"
      "v_cvt_pk_bf16_f32 v176, v164, v165\n"
      "v_and_b32 v211, 0xffff0000, v211\n"
      "v_add_f32 v171, v171, v203\n"
      "v_cvt_pk_bf16_f32 v178, v168, v169\n"
      "v_add_f32 v175, v175, v213\n"
      "v_add_f32 v171, v171, v211\n"
      "v_add_f32 v173, v173, v204\n"
      "v_cvt_pk_bf16_f32 v179, v170, v171\n"
      "v_add_f32 v173, v173, v212\n"
      "v_cvt_pk_bf16_f32 v180, v172, v173\n"
      "v_cvt_pk_bf16_f32 v181, v174, v175\n"
      "buffer_store_dwordx2 v[180:181], v9, s[16:19], s57 offen\n"
      "buffer_store_dwordx4 v[176:179], v8, s[16:19], s57 offen\n"
      "s_branch .Ld_end\n"
      ".Ld_out2:\n"
      "s_nop 8\n"
      "v_cvt_pk_bf16_f32 v229, v162, v163\n"
      "v_cvt_pk_bf16_f32 v228, v160, v161\n"
      "buffer_store_dwordx2 v[228:229], v13, s[8:11], s37 offen sc1\n"
      "s_add_u32 s52, s52, 106496\n"
      "s_add_u32 s55, s53, 6656\n"
      "s_add_u32 s54, s53, 13312\n"
      "s_waitcnt vmcnt(2)\n"
      "v_cmp_ne_u32 vcc, s21, v21\n"
      "s_cbranch_vccnz .Lsp_df_19\n"
      ".Lbk_df_19:\n"
      "buffer_load_dwordx4 v[176:179], v0, s[8:11], s53 offen sc1\n"
      "buffer_load_dwordx2 v[180:181], v13, s[8:11], s53 offen sc1\n"
      "s_add_u32 s58, s41, 4294967264\n"
      "buffer_load_dwordx4 v[184:187], v0, s[8:11], s55 offen sc1\n"
      "s_add_u32 s37, s37, 106496\n"
      "s_waitcnt vmcnt(3)\n"
      "buffer_store_dword v18, v4, s[8:11], s52 offen sc1\n"
      "buffer_load_dwordx4 v[192:195], v0, s[8:11], s54 offen sc1\n"
      "buffer_load_dwordx2 v[188:189], v13, s[8:11], s55 offen sc1\n"
      "s_add_u32 s55, s53, 19968\n"
      "buffer_load_dwordx2 v[196:197], v13, s[8:11], s54 offen sc1\n"
      "buffer_load_dwordx4 v[200:203], v0, s[8:11], s55 offen sc1\n"
      "buffer_load_dwordx2 v[204:205], v13, s[8:11], s55 offen sc1\n"
      "s_cmp_lt_u32 s41, 32\n"
      "s_cselect_b32 s56, s39, 0x40000000\n"
      "s_add_u32 s54, s53, 26624\n"
      "s_add_u32 s53, s53, 599040\n"
      "buffer_load_dwordx2 v[212:213], v13, s[8:11], s54 offen sc1\n"
      "s_add_u32 s39, s39, 147456\n"
      "buffer_load_dwordx4 v[208:211], v0, s[8:11], s54 offen sc1\n"
      "s_cmp_lt_u32 s58, 32\n"
      "s_cselect_b32 s57, s40, 0x40000000\n"
      "s_waitcnt vmcnt(10)\n"
      "v_lshlrev_b32 v234, 16, v178\n"
      "v_lshlrev_b32 v233, 16, v177\n"
      "v_lshlrev_b32 v232, 16, v176\n"
      "v_add_f32 v166, v166, v233\n"
      "s_add_u32 s52, s52, 106496\n"
      "v_add_f32 v164, v164, v232\n"
      "s_waitcnt vmcnt(9)\n"
      "v_lshlrev_b32 v233, 16, v181\n"
      "v_lshlrev_b32 v235, 16, v179\n"
      "v_and_b32 v176, 0xffff0000, v176\n"
      "v_lshlrev_b32 v232, 16, v180\n"
      "v_add_f32 v172, v172, v232\n"
      "v_and_b32 v181, 0xffff0000, v181\n"
      "v_add_f32 v168, v168, v234\n"
      "v_add_f32 v174, v174, v233\n"
      "v_add_f32 v165, v165, v176\n"
      "s_waitcnt vmcnt(8)\n"
      "v_lshlrev_b32 v232, 16, v184\n"
      "v_and_b32 v177, 0xffff0000, v177\n"
      "s_add_u32 s40, s40, 4294856704\n"
      "v_add_f32 v167, v167, v177\n"
      "v_and_b32 v180, 0xffff0000, v180\n"
      "v_add_f32 v164, v164, v232\n"
      "v_lshlrev_b32 v233, 16, v185\n"
      "v_add_f32 v166, v166, v233\n"
      "s_waitcnt vmcnt(5)\n"
      "v_lshlrev_b32 v232, 16, v188\n"
      "v_add_f32 v172, v172, v232\n"
      "v_and_b32 v185, 0xffff0000, v185\n"
      "v_lshlrev_b32 v232, 16, v192\n"
      "v_lshlrev_b32 v234, 16, v186\n"
      "v_add_f32 v164, v164, v232\n"
      "v_lshlrev_b32 v233, 16, v189\n"
      "s_waitcnt vmcnt(4)\n"
      "v_lshlrev_b32 v232, 16, v196\n"
      "v_add_f32 v174, v174, v233\n"
      "v_and_b32 v184, 0xffff0000, v184\n"
      "v_add_f32 v172, v172, v232\n"
      "v_add_f32 v170, v170, v235\n"
      "s_add_u32 s41, s41, 6\n"
      "v_add_f32 v165, v165, v184\n"
      "v_add_f32 v168, v168, v234\n"
      "v_lshlrev_b32 v233, 16, v193\n"
      "v_and_b32 v178, 0xffff0000, v178\n"
      "s_waitcnt vmcnt(3)\n"
      "v_lshlrev_b32 v232, 16, v200\n"
      "v_add_f32 v164, v164, v232\n"
      "v_and_b32 v186, 0xffff0000, v186\n"
      "v_add_f32 v169, v169, v178\n"
      "v_and_b32 v200, 0xffff0000, v200\n"
      "v_add_f32 v173, v173, v180\n"
      "v_add_f32 v166, v166, v233\n"
      "v_lshlrev_b32 v234, 16, v194\n"
      "v_and_b32 v192, 0xffff0000, v192\n"
      "v_add_f32 v175, v175, v181\n"
      "v_and_b32 v189, 0xffff0000, v189\n"
      "s_waitcnt vmcnt(2)\n"
      "v_lshlrev_b32 v232, 16, v204\n"
      "v_lshlrev_b32 v235, 16, v187\n"
      "v_add_f32 v172, v172, v232\n"
      "s_waitcnt vmcnt(0)\n"
      "v_lshlrev_b32 v232, 16, v208\n"
      "v_add_f32 v170, v170, v235\n"
      "v_and_b32 v194, 0xffff0000, v194\n"
      "v_and_b32 v208, 0xffff0000, v208\n"
      "v_add_f32 v167, v167, v185\n"
      "v_lshlrev_b32 v233, 16, v197\n"
      "v_lshlrev_b32 v235, 16, v195\n"
      "v_add_f32 v168, v168, v234\n"
      "v_add_f32 v164, v164, v232\n"
      "v_add_f32 v165, v165, v192\n"
      "v_lshlrev_b32 v234, 16, v202\n"
      "v_add_f32 v174, v174, v233\n"
      "v_lshlrev_b32 v232, 16, v212\n"
      "v_and_b32 v179, 0xffff0000, v179\n"
      "v_add_f32 v169, v169, v186\n"
      "v_lshlrev_b32 v233, 16, v201\n"
      "v_add_f32 v166, v166, v233\n"
      "v_and_b32 v193, 0xffff0000, v193\n"
      "v_and_b32 v197, 0xffff0000, v197\n"
      "v_lshlrev_b32 v233, 16, v205\n"
      "v_add_f32 v170, v170, v235\n"
      "v_add_f32 v174, v174, v233\n"
      "v_lshlrev_b32 v235, 16, v203\n"
      "v_and_b32 v187, 0xffff0000, v187\n"
      "v_lshlrev_b32 v233, 16, v209\n"
      "v_add_f32 v170, v170, v235\n"
      "v_add_f32 v166, v166, v233\n"
      "v_and_b32 v201, 0xffff0000, v201\n"
      "v_add_f32 v165, v165, v200\n"
      "v_and_b32 v188, 0xffff0000, v188\n"
      "v_add_f32 v165, v165, v208\n"
      "v_add_f32 v168, v168, v234\n"
      "v_add_f32 v167, v167, v193\n"
      "v_lshlrev_b32 v233, 16, v213\n"
      "v_add_f32 v171, v171, v179\n"
      "v_add_f32 v167, v167, v201\n"
      "v_lshlrev_b32 v234, 16, v210\n"
      "v_and_b32 v213, 0xffff0000, v213\n"
      "v_add_f32 v173, v173, v188\n"
      "v_and_b32 v209, 0xffff0000, v209\n"
      "v_add_f32 v175, v175, v189\n"
      "v_add_f32 v167, v167, v209\n"
      "v_and_b32 v203, 0xffff0000, v203\n"
      "v_add_f32 v175, v175, v197\n"
      "v_and_b32 v204, 0xffff0000, v204\n"
      "v_and_b32 v195, 0xffff0000, v195\n"
      "v_add_f32 v172, v172, v232\n"
      "v_add_f32 v169, v169, v194\n"
      "v_and_b32 v196, 0xffff0000, v196\n"
      "v_and_b32 v212, 0xffff0000, v212\n"
      "v_cvt_pk_bf16_f32 v177, v166, v167\n"
      "v_add_f32 v171, v171, v187\n"
      "v_and_b32 v205, 0xffff0000, v205\n"
      "v_and_b32 v202, 0xffff0000, v202\n"
      "v_add_f32 v169, v169, v202\n"
      "v_and_b32 v210, 0xffff0000, v210\n"
      "v_add_f32 v173, v173, v196\n"
      "v_lshlrev_b32 v235, 16, v211\n"
      "v_add_f32 v170, v170, v235\n"
      "v_add_f32 v171, v171, v195\n"
      "v_add_f32 v174, v174, v233\n"
      "v_add_f32 v169, v169, v210\n"
      "v_add_f32 v175, v175, v205\n"
      "v_add_f32 v168, v168, v234\n"
      "v_cvt_pk_bf16_f32 v176, v164, v165\n"
      "v_and_b32 v211, 0xffff0000, v211\n"
      "v_add_f32 v171, v171, v203\n"
      "v_cvt_pk_bf16_f32 v178, v168, v169\n"
      "v_add_f32 v175, v175, v213\n"
      "v_add_f32 v171, v171, v211\n"
      "v_add_f32 v173, v173, v204\n"
      "v_cvt_pk_bf16_f32 v179, v170, v171\n"
      "v_add_f32 v173, v173, v212\n"
      "v_cvt_pk_bf16_f32 v180, v172, v173\n"
      "v_cvt_pk_bf16_f32 v181, v174, v175\n"
      "buffer_store_dwordx2 v[180:181], v9, s[16:19], s57 offen\n"
      "buffer_store_dwordx4 v[176:179], v8, s[16:19], s57 offen\n"
      "s_branch .Ld_end\n"
      ".Ld_out3:\n"
      "s_nop 8\n"
      "v_cvt_pk_bf16_f32 v228, v160, v161\n"
      "buffer_load_dwordx4 v[200:203], v0, s[8:11], s55 offen sc1\n"
      "v_cvt_pk_bf16_f32 v227, v158, v159\n"
      "buffer_store_dwordx4 v[224:227], v0, s[8:11], s37 offen sc1\n"
      "v_cvt_pk_bf16_f32 v229, v162, v163\n"
      "buffer_store_dwordx2 v[228:229], v13, s[8:11], s37 offen sc1\n"
      "s_add_u32 s37, s37, 106496\n"
      "buffer_load_dwordx2 v[204:205], v13, s[8:11], s55 offen sc1\n"
      "s_cmp_lt_u32 s41, 32\n"
      "s_cselect_b32 s56, s39, 0x40000000\n"
      "s_add_u32 s54, s53, 26624\n"
      "s_add_u32 s53, s53, 599040\n"
      "buffer_load_dwordx2 v[212:213], v13, s[8:11], s54 offen sc1\n"
      "s_add_u32 s39, s39, 147456\n"
      "buffer_load_dwordx4 v[208:211], v0, s[8:11], s54 offen sc1\n"
      "s_cmp_lt_u32 s58, 32\n"
      "s_cselect_b32 s57, s40, 0x40000000\n"
      "s_waitcnt vmcnt(12)\n"
      "v_lshlrev_b32 v234, 16, v178\n"
      "v_lshlrev_b32 v233, 16, v177\n"
      "v_lshlrev_b32 v232, 16, v176\n"
      "v_add_f32 v166, v166, v233\n"
      "s_add_u32 s52, s52, 106496\n"
      "v_add_f32 v164, v164, v232\n"
      "s_waitcnt vmcnt(11)\n"
      "v_lshlrev_b32 v233, 16, v181\n"
      "v_lshlrev_b32 v235, 16, v179\n"
      "v_and_b32 v176, 0xffff0000, v176\n"
      "v_lshlrev_b32 v232, 16, v180\n"
      "v_add_f32 v172, v172, v232\n"
      "v_and_b32 v181, 0xffff0000, v181\n"
      "v_add_f32 v168, v168, v234\n"
      "v_add_f32 v174, v174, v233\n"
      "v_add_f32 v165, v165, v176\n"
      "s_waitcnt vmcnt(10)\n"
      "v_lshlrev_b32 v232, 16, v184\n"
      "v_and_b32 v177, 0xffff0000, v177\n"
      "s_add_u32 s40, s40, 4294856704\n"
      "v_add_f32 v167, v167, v177\n"
      "v_and_b32 v180, 0xffff0000, v180\n"
      "v_add_f32 v164, v164, v232\n"
      "v_lshlrev_b32 v233, 16, v185\n"
      "v_add_f32 v166, v166, v233\n"
      "s_waitcnt vmcnt(7)\n"
      "v_lshlrev_b32 v232, 16, v188\n"
      "v_add_f32 v172, v172, v232\n"
      "v_and_b32 v185, 0xffff0000, v185\n"
      "v_lshlrev_b32 v232, 16, v192\n"
      "v_lshlrev_b32 v234, 16, v186\n"
      "v_add_f32 v164, v164, v232\n"
      "v_lshlrev_b32 v233, 16, v189\n"
      "s_waitcnt vmcnt(6)\n"
      "v_lshlrev_b32 v232, 16, v196\n"
      "v_add_f32 v174, v174, v233\n"
      "v_and_b32 v184, 0xffff0000, v184\n"
      "v_add_f32 v172, v172, v232\n"
      "v_add_f32 v170, v170, v235\n"
      "s_add_u32 s41, s41, 6\n"
      "v_add_f32 v165, v165, v184\n"
      "v_add_f32 v168, v168, v234\n"
      "v_lshlrev_b32 v233, 16, v193\n"
      "v_and_b32 v178, 0xffff0000, v178\n"
      "s_waitcnt vmcnt(5)\n"
      "v_lshlrev_b32 v232, 16, v200\n"
      "v_add_f32 v164, v164, v232\n"
      "v_and_b32 v186, 0xffff0000, v186\n"
      "s_waitcnt vmcnt(3)\n"
      "buffer_store_dword v18, v4, s[8:11], s52 offen sc1\n"
      "v_add_f32 v169, v169, v178\n"
      "v_and_b32 v200, 0xffff0000, v200\n"
      "v_add_f32 v173, v173, v180\n"
      "v_add_f32 v166, v166, v233\n"
      "v_lshlrev_b32 v234, 16, v194\n"
      "v_and_b32 v192, 0xffff0000, v192\n"
      "v_add_f32 v175, v175, v181\n"
      "v_and_b32 v189, 0xffff0000, v189\n"
      "s_waitcnt vmcnt(3)\n"
      "v_lshlrev_b32 v232, 16, v204\n"
      "v_lshlrev_b32 v235, 16, v187\n"
      "v_add_f32 v172, v172, v232\n"
      "s_waitcnt vmcnt(1)\n"
      "v_lshlrev_b32 v232, 16, v208\n"
      "v_add_f32 v170, v170, v235\n"
      "v_and_b32 v194, 0xffff0000, v194\n"
      "v_and_b32 v208, 0xffff0000, v208\n"
      "v_add_f32 v167, v167, v185\n"
      "v_lshlrev_b32 v233, 16, v197\n"
      "v_lshlrev_b32 v235, 16, v195\n"
      "v_add_f32 v168, v168, v234\n"
      "v_add_f32 v164, v164, v232\n"
      "v_add_f32 v165, v165, v192\n"
      "v_lshlrev_b32 v234, 16, v202\n"
      "v_add_f32 v174, v174, v233\n"
      "v_lshlrev_b32 v232, 16, v212\n"
      "v_and_b32 v179, 0xffff0000, v179\n"
      "v_add_f32 v169, v169, v186\n"
      "v_lshlrev_b32 v233, 16, v201\n"
      "v_add_f32 v166, v166, v233\n"
      "v_and_b32 v193, 0xffff0000, v193\n"
      "v_and_b32 v197, 0xffff0000, v197\n"
      "v_lshlrev_b32 v233, 16, v205\n"
      "v_add_f32 v170, v170, v235\n"
      "v_add_f32 v174, v174, v233\n"
      "v_lshlrev_b32 v235, 16, v203\n"
      "v_and_b32 v187, 0xffff0000, v187\n"
      "v_lshlrev_b32 v233, 16, v209\n"
      "v_add_f32 v170, v170, v235\n"
      "v_add_f32 v166, v166, v233\n"
      "v_and_b32 v201, 0xffff0000, v201\n"
      "v_add_f32 v165, v165, v200\n"
      "v_and_b32 v188, 0xffff0000, v188\n"
      "v_add_f32 v165, v165, v208\n"
      "v_add_f32 v168, v168, v234\n"
      "v_add_f32 v167, v167, v193\n"
      "v_lshlrev_b32 v233, 16, v213\n"
      "v_add_f32 v171, v171, v179\n"
      "v_add_f32 v167, v167, v201\n"
      "v_lshlrev_b32 v234, 16, v210\n"
      "v_and_b32 v213, 0xffff0000, v213\n"
      "v_add_f32 v173, v173, v188\n"
      "v_and_b32 v209, 0xffff0000, v209\n"
      "v_add_f32 v175, v175, v189\n"
      "v_add_f32 v167, v167, v209\n"
      "v_and_b32 v203, 0xffff0000, v203\n"
      "v_add_f32 v175, v175, v197\n"
      "v_and_b32 v204, 0xffff0000, v204\n"
      "v_and_b32 v195, 0xffff0000, v195\n"
      "v_add_f32 v172, v172, v232\n"
      "v_add_f32 v169, v169, v194\n"
      "v_and_b32 v196, 0xffff0000, v196\n"
      "v_and_b32 v212, 0xffff0000, v212\n"
      "v_cvt_pk_bf16_f32 v177, v166, v167\n"
      "v_add_f32 v171, v171, v187\n"
      "v_and_b32 v205, 0xffff0000, v205\n"
      "s_add_u32 s52, s52, 106496\n"
      "v_and_b32 v202, 0xffff0000, v202\n"
      "v_add_f32 v169, v169, v202\n"
      "v_and_b32 v210, 0xffff0000, v210\n"
      "v_add_f32 v173, v173, v196\n"
      "v_lshlrev_b32 v235, 16, v211\n"
      "v_add_f32 v170, v170, v235\n"
      "v_add_f32 v171, v171, v195\n"
      "v_add_f32 v174, v174, v233\n"
      "v_add_f32 v169, v169, v210\n"
      "v_add_f32 v175, v175, v205\n"
      "v_add_f32 v168, v168, v234\n"
      "v_cvt_pk_bf16_f32 v176, v164, v165\n"
      "v_and_b32 v211, 0xffff0000, v211\n"
      "v_add_f32 v171, v171, v203\n"
      "v_cvt_pk_bf16_f32 v178, v168, v169\n"
      "v_add_f32 v175, v175, v213\n"
      "v_add_f32 v171, v171, v211\n"
      "v_add_f32 v173, v173, v204\n"
      "v_cvt_pk_bf16_f32 v179, v170, v171\n"
      "v_add_f32 v173, v173, v212\n"
      "v_cvt_pk_bf16_f32 v180, v172, v173\n"
      "v_cvt_pk_bf16_f32 v181, v174, v175\n"
      "buffer_store_dwordx2 v[180:181], v9, s[16:19], s57 offen\n"
      "buffer_store_dwordx4 v[176:179], v8, s[16:19], s57 offen\n"
      "s_branch .Ld_end\n"
      ".Ld_out4:\n"
      "v_and_b32 v192, 0xffff0000, v192\n"
      "s_nop 7\n"
      "v_cvt_pk_bf16_f32 v229, v162, v163\n"
      "v_add_f32 v175, v175, v181\n"
      "v_cvt_pk_bf16_f32 v228, v160, v161\n"
      "v_and_b32 v189, 0xffff0000, v189\n"
      "s_waitcnt vmcnt(11)\n"
      "v_lshlrev_b32 v232, 16, v204\n"
      "buffer_store_dwordx2 v[228:229], v13, s[8:11], s37 offen sc1\n"
      "v_lshlrev_b32 v235, 16, v187\n"
      "v_add_f32 v172, v172, v232\n"
      "s_waitcnt vmcnt(10)\n"
      "v_lshlrev_b32 v232, 16, v208\n"
      "v_add_f32 v170, v170, v235\n"
      "v_and_b32 v194, 0xffff0000, v194\n"
      "v_and_b32 v208, 0xffff0000, v208\n"
      "v_add_f32 v167, v167, v185\n"
      "v_lshlrev_b32 v233, 16, v197\n"
      "v_lshlrev_b32 v235, 16, v195\n"
      "v_add_f32 v168, v168, v234\n"
      "v_add_f32 v164, v164, v232\n"
      "v_add_f32 v165, v165, v192\n"
      "v_lshlrev_b32 v234, 16, v202\n"
      "v_add_f32 v174, v174, v233\n"
      "v_lshlrev_b32 v232, 16, v212\n"
      "v_and_b32 v179, 0xffff0000, v179\n"
      "v_add_f32 v169, v169, v186\n"
      "v_lshlrev_b32 v233, 16, v201\n"
      "v_add_f32 v166, v166, v233\n"
      "v_and_b32 v193, 0xffff0000, v193\n"
      "v_and_b32 v197, 0xffff0000, v197\n"
      "v_lshlrev_b32 v233, 16, v205\n"
      "v_add_f32 v170, v170, v235\n"
      "v_add_f32 v174, v174, v233\n"
      "v_lshlrev_b32 v235, 16, v203\n"
      "v_and_b32 v187, 0xffff0000, v187\n"
      "v_lshlrev_b32 v233, 16, v209\n"
      "v_add_f32 v170, v170, v235\n"
      "v_add_f32 v166, v166, v233\n"
      "v_and_b32 v201, 0xffff0000, v201\n"
      "v_add_f32 v165, v165, v200\n"
      "v_and_b32 v188, 0xffff0000, v188\n"
      "v_add_f32 v165, v165, v208\n"
      "v_add_f32 v168, v168, v234\n"
      "v_add_f32 v167, v167, v193\n"
      "v_lshlrev_b32 v233, 16, v213\n"
      "v_add_f32 v171, v171, v179\n"
      "v_add_f32 v167, v167, v201\n"
      "v_lshlrev_b32 v234, 16, v210\n"
      "v_and_b32 v213, 0xffff0000, v213\n"
      "v_add_f32 v173, v173, v188\n"
      "v_and_b32 v209, 0xffff0000, v209\n"
      "v_add_f32 v175, v175, v189\n"
      "v_add_f32 v167, v167, v209\n"
      "v_and_b32 v203, 0xffff0000, v203\n"
      "v_add_f32 v175, v175, v197\n"
      "v_and_b32 v204, 0xffff0000, v204\n"
      "v_and_b32 v195, 0xffff0000, v195\n"
      "v_add_f32 v172, v172, v232\n"
      "v_add_f32 v169, v169, v194\n"
      "v_and_b32 v196, 0xffff0000, v196\n"
      "v_and_b32 v212, 0xffff0000, v212\n"
      "v_cvt_pk_bf16_f32 v177, v166, v167\n"
      "v_add_f32 v171, v171, v187\n"
      "v_and_b32 v205, 0xffff0000, v205\n"
      "s_add_u32 s52, s52, 106496\n"
      "v_and_b32 v202, 0xffff0000, v202\n"
      "v_add_f32 v169, v169, v202\n"
      "v_and_b32 v210, 0xffff0000, v210\n"
      "v_add_f32 v173, v173, v196\n"
      "v_lshlrev_b32 v235, 16, v211\n"
      "v_add_f32 v170, v170, v235\n"
      "v_add_f32 v171, v171, v195\n"
      "s_add_u32 s37, s37, 106496\n"
      "s_waitcnt vmcnt(0)\n"
      "buffer_store_dword v18, v4, s[8:11], s52 offen sc1\n"
      "v_add_f32 v174, v174, v233\n"
      "v_add_f32 v169, v169, v210\n"
      "v_add_f32 v175, v175, v205\n"
      "v_add_f32 v168, v168, v234\n"
      "v_cvt_pk_bf16_f32 v176, v164, v165\n"
      "v_and_b32 v211, 0xffff0000, v211\n"
      "v_add_f32 v171, v171, v203\n"
      "v_cvt_pk_bf16_f32 v178, v168, v169\n"
      "v_add_f32 v175, v175, v213\n"
      "v_add_f32 v171, v171, v211\n"
      "v_add_f32 v173, v173, v204\n"
      "v_cvt_pk_bf16_f32 v179, v170, v171\n"
      "v_add_f32 v173, v173, v212\n"
      "v_cvt_pk_bf16_f32 v180, v172, v173\n"
      "v_cvt_pk_bf16_f32 v181, v174, v175\n"
      "buffer_store_dwordx2 v[180:181], v9, s[16:19], s57 offen\n"
      "buffer_store_dwordx4 v[176:179], v8, s[16:19], s57 offen\n"
      "s_add_u32 s52, s52, 106496\n"
      "s_branch .Ld_end\n"
      ".Ld_in1:\n"
      "s_add_u32 s36, s36, 165888\n"
      "s_min_u32 s36, s36, s6\n"
      "s_mov_b32 s51, s36\n"
      "s_add_u32 s36, s36, 165888\n"
      "s_min_u32 s36, s36, s6\n"
      "buffer_load_dword v20, v2, s[8:11], s36 offen sc1\n"
      "s_mov_b32 s51, s36\n"
      "s_add_u32 m0, s43, 24576\n"
      "s_add_u32 s36, s36, 165888\n"
      "s_waitcnt vmcnt(0)\n"
      "v_cmp_ne_u32 vcc, s21, v20\n"
      "s_cbranch_vccnz .Lsp_d1_20\n"
      ".Lbk_d1_20:\n"
      "buffer_load_dwordx4 v0, s[8:11], s51 offen offset:0 sc1 lds\n"
      "buffer_load_dwordx4 v0, s[8:11], s51 offen offset:1024 sc1 lds\n"
      "buffer_load_dwordx4 v0, s[8:11], s51 offen offset:2048 sc1 lds\n"
      "s_min_u32 s36, s36, s6\n"
      "buffer_load_dword v20, v2, s[8:11], s36 offen sc1\n"
      "buffer_load_dwordx4 v0, s[8:11], s51 offen offset:3072 sc1 lds\n"
      "s_add_u32 m0, s43, 28672\n"
      "s_nop 0\n"
      "buffer_load_dwordx4 v1, s[8:11], s51 offen offset:0 sc1 lds\n"
      "buffer_load_dwordx4 v1, s[8:11], s51 offen offset:1024 sc1 lds\n"
      "s_add_u32 m0, s43, 49152\n"
      "s_mov_b32 s51, s36\n"
      "v_add_u32 v14, 1, v14\n"
      "s_waitcnt vmcnt(3)\n"
      "v_cmp_ne_u32 vcc, s21, v20\n"
      "s_add_u32 s36, s36, 165888\n"
      "s_min_u32 s36, s36, s6\n"
      "s_cbranch_vccnz .Lsp_d2_21\n"
      ".Lbk_d2_21:\n"
      "s_waitcnt vmcnt(0)\n"
      "ds_write_b32 v16, v14\n"
      "buffer_load_dword v20, v2, s[8:11], s36 offen sc1\n"
      "buffer_load_dwordx4 v0, s[8:11], s51 offen offset:3072 sc1 lds\n"
      "s_add_u32 s52, s52, 106496\n"
      "s_add_u32 s37, s37, 173056\n"
      "buffer_load_dwordx4 v0, s[8:11], s51 offen offset:1024 sc1 lds\n"
      "ds_read_b32 v15, v17\n"
      "buffer_load_dwordx4 v0, s[8:11], s51 offen offset:2048 sc1 lds\n"
      "s_waitcnt lgkmcnt(0)\n"
      "v_cmp_gt_i32 vcc, v14, v15\n"
      "buffer_load_dwordx4 v0, s[8:11], s51 offen offset:0 sc1 lds\n"
      "s_add_u32 m0, s43, 53248\n"
      "s_cbranch_vccnz .Lsp_dy1_22\n"
      ".Lbk_dy1_22:\n"
      "buffer_load_dwordx4 v1, s[8:11], s51 offen offset:0 sc1 lds\n"
      "ds_read_b128 v[56:59], v0 offset:24576\n"
      "ds_read_b128 v[64:67], v0 offset:26624\n"
      "buffer_load_dwordx4 v1, s[8:11], s51 offen offset:1024 sc1 lds\n"
      "ds_read_b128 v[68:71], v0 offset:27648\n"
      "ds_read_b128 v[80:83], v0 offset:30720\n"
      "ds_read_b128 v[60:63], v0 offset:25600\n"
      "ds_read_b128 v[92:95], v0 offset:33792\n"
      "ds_read_b128 v[84:87], v0 offset:31744\n"
      "ds_read_b128 v[72:75], v0 offset:28672\n"
      "ds_read_b128 v[76:79], v0 offset:29696\n"
      "ds_read_b128 v[100:103], v0 offset:35840\n"
      "ds_read_b128 v[96:99], v0 offset:34816\n"
      "ds_read_b128 v[116:119], v0 offset:39936\n"
      "ds_read_b128 v[120:123], v0 offset:40960\n"
      "ds_read_b128 v[108:111], v0 offset:37888\n"
      "ds_read_b128 v[88:91], v0 offset:32768\n"
      "s_waitcnt vmcnt(0) lgkmcnt(0)\n"
      "s_nop 4\n"
      "s_branch .Ld_t1\n"
      ".Ld_in2:\n"
      "s_add_u32 s36, s36, 165888\n"
      "s_min_u32 s36, s36, s6\n"
      "s_mov_b32 s51, s36\n"
      "s_add_u32 s36, s36, 165888\n"
      "s_min_u32 s36, s36, s6\n"
      "s_mov_b32 s51, s36\n"
      "s_add_u32 s36, s36, 165888\n"
      "s_min_u32 s36, s36, s6\n"
      "buffer_load_dword v20, v2, s[8:11], s36 offen sc1\n"
      "s_add_u32 m0, s43, 49152\n"
      "s_mov_b32 s51, s36\n"
      "s_waitcnt vmcnt(0)\n"
      "v_cmp_ne_u32 vcc, s21, v20\n"
      "s_add_u32 s36, s36, 165888\n"
      "s_min_u32 s36, s36, s6\n"
      "s_cbranch_vccnz .Lsp_d2_23\n"
      ".Lbk_d2_23:\n"
      "buffer_load_dword v20, v2, s[8:11], s36 offen sc1\n"
      "buffer_load_dwordx4 v0, s[8:11], s51 offen offset:3072 sc1 lds\n"
      "s_add_u32 s52, s52, 106496\n"
      "s_add_u32 s37, s37, 173056\n"
      "buffer_load_dwordx4 v0, s[8:11], s51 offen offset:1024 sc1 lds\n"
      "buffer_load_dwordx4 v0, s[8:11], s51 offen offset:2048 sc1 lds\n"
      "buffer_load_dwordx4 v0, s[8:11], s51 offen offset:0 sc1 lds\n"
      "s_add_u32 m0, s43, 53248\n"
      "s_nop 0\n"
      "buffer_load_dwordx4 v1, s[8:11], s51 offen offset:0 sc1 lds\n"
      "buffer_load_dwordx4 v1, s[8:11], s51 offen offset:1024 sc1 lds\n"
      "s_mov_b32 s51, s36\n"
      "s_add_u32 s36, s36, 165888\n"
      "s_add_u32 m0, s43, 73728\n"
      "s_waitcnt vmcnt(6)\n"
      "v_cmp_ne_u32 vcc, s21, v20\n"
      "s_add_u32 s38, s38, 147456\n"
      "s_min_u32 s36, s36, s6\n"
      "s_cbranch_vccnz .Lsp_d3_24\n"
      ".Lbk_d3_24:\n"
      "buffer_load_dwordx4 v0, s[8:11], s51 offen offset:2048 sc1 lds\n"
      "v_add_u32 v14, 1, v14\n"
      "buffer_load_dwordx4 v0, s[8:11], s51 offen offset:1024 sc1 lds\n"
      "buffer_load_dword v20, v2, s[8:11], s36 offen sc1\n"
      "s_waitcnt vmcnt(3)\n"
      "ds_write_b32 v16, v14\n"
      "buffer_load_dwordx4 v0, s[8:11], s51 offen offset:0 sc1 lds\n"
      "buffer_load_dwordx4 v0, s[8:11], s51 offen offset:3072 sc1 lds\n"
      "s_add_u32 m0, s43, 77824\n"
      "ds_read_b32 v15, v17\n"
      "s_add_u32 s52, s52, 173056\n"
      "buffer_load_dwordx4 v1, s[8:11], s51 offen offset:0 sc1 lds\n"
      "s_waitcnt lgkmcnt(0)\n"
      "v_cmp_gt_i32 vcc, v14, v15\n"
      "buffer_load_dwordx4 v1, s[8:11], s51 offen offset:1024 sc1 lds\n"
      "s_cbranch_vccnz .Lsp_dy2_25\n"
      ".Lbk_dy2_25:\n"
      "ds_read_b128 v[68:71], v22 offset:3072\n"
      "ds_read_b128 v[60:63], v22 offset:1024\n"
      "ds_read_b128 v[80:83], v22 offset:6144\n"
      "ds_read_b128 v[72:75], v22 offset:4096\n"
      "ds_read_b128 v[76:79], v22 offset:5120\n"
      "ds_read_b128 v[96:99], v22 offset:10240\n"
      "ds_read_b128 v[84:87], v22 offset:7168\n"
      "ds_read_b128 v[56:59], v22 offset:0\n"
      "ds_read_b128 v[92:95], v22 offset:9216\n"
      "s_mov_b32 s51, s36\n"
      "ds_read_b128 v[64:67], v22 offset:2048\n"
      "ds_read_b128 v[100:103], v22 offset:11264\n"
      "ds_read_b128 v[132:135], v22 offset:19456\n"
      "ds_read_b128 v[112:115], v22 offset:14336\n"
      "s_add_u32 m0, s43, 98304\n"
      "s_waitcnt vmcnt(0) lgkmcnt(0)\n"
      "s_nop 4\n"
      "s_branch .Ld_t2\n"
      ".Ld_in3:\n"
      "s_add_u32 s36, s36, 165888\n"
      "s_min_u32 s36, s36, s6\n"
      "s_mov_b32 s51, s36\n"
      "s_add_u32 s36, s36, 165888\n"
      "s_min_u32 s36, s36, s6\n"
      "s_mov_b32 s51, s36\n"
      "s_add_u32 s36, s36, 165888\n"
      "s_min_u32 s36, s36, s6\n"
      "s_mov_b32 s51, s36\n"
      "s_add_u32 s36, s36, 165888\n"
      "s_min_u32 s36, s36, s6\n"
      "buffer_load_dword v20, v2, s[8:11], s36 offen sc1\n"
      "s_add_u32 s52, s52, 106496\n"
      "s_add_u32 s37, s37, 173056\n"
      "s_mov_b32 s51, s36\n"
      "s_add_u32 s36, s36, 165888\n"
      "s_add_u32 m0, s43, 73728\n"
      "s_waitcnt vmcnt(0)\n"
      "v_cmp_ne_u32 vcc, s21, v20\n"
      "s_add_u32 s38, s38, 147456\n"
      "s_min_u32 s36, s36, s6\n"
      "s_cbranch_vccnz .Lsp_d3_26\n"
      ".Lbk_d3_26:\n"
      "buffer_load_dwordx4 v0, s[8:11], s51 offen offset:2048 sc1 lds\n"
      "buffer_load_dwordx4 v0, s[8:11], s51 offen offset:1024 sc1 lds\n"
      "buffer_load_dword v20, v2, s[8:11], s36 offen sc1\n"
      "buffer_load_dwordx4 v0, s[8:11], s51 offen offset:0 sc1 lds\n"
      "buffer_load_dwordx4 v0, s[8:11], s51 offen offset:3072 sc1 lds\n"
      "s_add_u32 m0, s43, 77824\n"
      "s_add_u32 s52, s52, 173056\n"
      "buffer_load_dwordx4 v1, s[8:11], s51 offen offset:0 sc1 lds\n"
      "buffer_load_dwordx4 v1, s[8:11], s51 offen offset:1024 sc1 lds\n"
      "s_mov_b32 s51, s36\n"
      "s_add_u32 m0, s43, 98304\n"
      "s_add_u32 s36, s36, 165888\n"
      "v_add_u32 v14, 1, v14\n"
      "s_waitcnt vmcnt(4)\n"
      "v_cmp_ne_u32 vcc, s21, v20\n"
      "s_cbranch_vccnz .Lsp_d4_27\n"
      ".Lbk_d4_27:\n"
      "s_min_u32 s36, s36, s6\n"
      "buffer_load_dwordx4 v0, s[8:11], s51 offen offset:0 sc1 lds\n"
      "buffer_load_dwordx4 v0, s[8:11], s51 offen offset:2048 sc1 lds\n"
      "s_waitcnt vmcnt(2)\n"
      "ds_write_b32 v16, v14\n"
      "buffer_load_dword v20, v2, s[8:11], s36 offen sc1\n"
      "buffer_load_dwordx4 v0, s[8:11], s51 offen offset:3072 sc1 lds\n"
      "ds_read_b32 v15, v17\n"
      "buffer_load_dwordx4 v0, s[8:11], s51 offen offset:1024 sc1 lds\n"
      "s_add_u32 m0, s43, 102400\n"
      "s_nop 0\n"
      "buffer_load_dwordx4 v1, s[8:11], s51 offen offset:1024 sc1 lds\n"
      "s_waitcnt lgkmcnt(0)\n"
      "v_cmp_gt_i32 vcc, v14, v15\n"
      "buffer_load_dwordx4 v1, s[8:11], s51 offen offset:0 sc1 lds\n"
      "s_cbranch_vccnz .Lsp_dy3_28\n"
      ".Lbk_dy3_28:\n"
      "ds_read_b128 v[60:63], v22 offset:25600\n"
      "ds_read_b128 v[64:67], v22 offset:26624\n"
      "s_mov_b32 s51, s36\n"
      "ds_read_b128 v[72:75], v22 offset:28672\n"
      "ds_read_b128 v[68:71], v22 offset:27648\n"
      "ds_read_b128 v[92:95], v22 offset:33792\n"
      "ds_read_b128 v[56:59], v22 offset:24576\n"
      "ds_read_b128 v[100:103], v22 offset:35840\n"
      "ds_read_b128 v[80:83], v22 offset:30720\n"
      "ds_read_b128 v[112:115], v22 offset:38912\n"
      "s_add_u32 s37, s37, 106496\n"
      "ds_read_b128 v[88:91], v22 offset:32768\n"
      "ds_read_b128 v[96:99], v22 offset:34816\n"
      "ds_read_b128 v[76:79], v22 offset:29696\n"
      "ds_read_b128 v[116:119], v22 offset:39936\n"
      "ds_read_b128 v[84:87], v22 offset:31744\n"
      "ds_read_b128 v[104:107], v22 offset:36864\n"
      "s_waitcnt vmcnt(0) lgkmcnt(0)\n"
      "s_nop 4\n"
      "s_branch .Ld_t3\n"
      ".Ld_in4:\n"
      "s_add_u32 s36, s36, 165888\n"
      "s_min_u32 s36, s36, s6\n"
      "s_mov_b32 s51, s36\n"
      "s_add_u32 s36, s36, 165888\n"
      "s_min_u32 s36, s36, s6\n"
      "s_mov_b32 s51, s36\n"
      "s_add_u32 s36, s36, 165888\n"
      "s_min_u32 s36, s36, s6\n"
      "s_mov_b32 s51, s36\n"
      "s_add_u32 s36, s36, 165888\n"
      "s_min_u32 s36, s36, s6\n"
      "s_add_u32 s52, s52, 106496\n"
      "s_add_u32 s37, s37, 173056\n"
      "s_mov_b32 s51, s36\n"
      "s_add_u32 s36, s36, 165888\n"
      "s_add_u32 s38, s38, 147456\n"
      "s_min_u32 s36, s36, s6\n"
      "buffer_load_dword v20, v2, s[8:11], s36 offen sc1\n"
      "s_add_u32 s52, s52, 173056\n"
      "s_mov_b32 s51, s36\n"
      "s_add_u32 m0, s43, 98304\n"
      "s_add_u32 s36, s36, 165888\n"
      "s_waitcnt vmcnt(0)\n"
      "v_cmp_ne_u32 vcc, s21, v20\n"
      "s_cbranch_vccnz .Lsp_d4_29\n"
      ".Lbk_d4_29:\n"
      "s_min_u32 s36, s36, s6\n"
      "buffer_load_dwordx4 v0, s[8:11], s51 offen offset:0 sc1 lds\n"
      "buffer_load_dwordx4 v0, s[8:11], s51 offen offset:2048 sc1 lds\n"
      "buffer_load_dword v20, v2, s[8:11], s36 offen sc1\n"
      "buffer_load_dwordx4 v0, s[8:11], s51 offen offset:3072 sc1 lds\n"
      "buffer_load_dwordx4 v0, s[8:11], s51 offen offset:1024 sc1 lds\n"
      "s_add_u32 m0, s43, 102400\n"
      "s_nop 0\n"
      "buffer_load_dwordx4 v1, s[8:11], s51 offen offset:1024 sc1 lds\n"
      "buffer_load_dwordx4 v1, s[8:11], s51 offen offset:0 sc1 lds\n"
      "s_mov_b32 s51, s36\n"
      "s_add_u32 s37, s37, 106496\n"
      "s_add_u32 m0, s43, 122880\n"
      "s_waitcnt vmcnt(4)\n"
      "v_cmp_ne_u32 vcc, s21, v20\n"
      "s_add_u32 s36, s36, 165888\n"
      "s_add_u32 s52, s52, 106496\n"
      "s_cbranch_vccnz .Lsp_d5_30\n"
      ".Lbk_d5_30:\n"
      "buffer_load_dwordx4 v0, s[8:11], s51 offen offset:1024 sc1 lds\n"
      "v_add_u32 v14, 1, v14\n"
      "buffer_load_dwordx4 v0, s[8:11], s51 offen offset:0 sc1 lds\n"
      "buffer_load_dwordx4 v0, s[8:11], s51 offen offset:2048 sc1 lds\n"
      "s_waitcnt vmcnt(3)\n"
      "ds_write_b32 v16, v14\n"
      "buffer_load_dwordx4 v0, s[8:11], s51 offen offset:3072 sc1 lds\n"
      "s_min_u32 s36, s36, s6\n"
      "s_add_u32 m0, s43, 126976\n"
      "s_nop 0\n"
      "buffer_load_dwordx4 v1, s[8:11], s51 offen offset:1024 sc1 lds\n"
      "ds_read_b32 v15, v17\n"
      "buffer_load_dword v20, v2, s[8:11], s36 offen sc1\n"
      "s_waitcnt lgkmcnt(0)\n"
      "v_cmp_gt_i32 vcc, v14, v15\n"
      "buffer_load_dwordx4 v1, s[8:11], s51 offen offset:0 sc1 lds\n"
      "s_cbranch_vccnz .Lsp_dy4_31\n"
      ".Lbk_dy4_31:\n"
      "ds_read_b128 v[60:63], v23 offset:1024\n"
      "ds_read_b128 v[64:67], v23 offset:2048\n"
      "ds_read_b128 v[72:75], v23 offset:4096\n"
      "ds_read_b128 v[68:71], v23 offset:3072\n"
      "s_add_u32 s58, s41, 4294967264\n"
      "ds_read_b128 v[80:83], v23 offset:6144\n"
      "ds_read_b128 v[84:87], v23 offset:7168\n"
      "ds_read_b128 v[76:79], v23 offset:5120\n"
      "s_add_u32 s37, s37, 106496\n"
      "ds_read_b128 v[56:59], v23 offset:0\n"
      "ds_read_b128 v[88:91], v23 offset:8192\n"
      "ds_read_b128 v[96:99], v23 offset:10240\n"
      "ds_read_b128 v[100:103], v23 offset:11264\n"
      "ds_read_b128 v[132:135], v23 offset:19456\n"
      "ds_read_b128 v[104:107], v23 offset:12288\n"
      "s_waitcnt vmcnt(0) lgkmcnt(0)\n"
      "s_nop 4\n"
      "s_branch .Ld_t4\n"
      ".Ld_in5:\n"
      "s_add_u32 s36, s36, 165888\n"
      "s_min_u32 s36, s36, s6\n"
      "s_mov_b32 s51, s36\n"
      "s_add_u32 s36, s36, 165888\n"
      "s_min_u32 s36, s36, s6\n"
      "s_mov_b32 s51, s36\n"
      "s_add_u32 s36, s36, 165888\n"
      "s_min_u32 s36, s36, s6\n"
      "s_mov_b32 s51, s36\n"
      "s_add_u32 s36, s36, 165888\n"
      "s_min_u32 s36, s36, s6\n"
      "s_add_u32 s52, s52, 106496\n"
      "s_add_u32 s37, s37, 173056\n"
      "s_mov_b32 s51, s36\n"
      "s_add_u32 s36, s36, 165888\n"
      "s_add_u32 s38, s38, 147456\n"
      "s_min_u32 s36, s36, s6\n"
      "s_add_u32 s52, s52, 173056\n"
      "s_mov_b32 s51, s36\n"
      "s_add_u32 s36, s36, 165888\n"
      "s_min_u32 s36, s36, s6\n"
      "buffer_load_dword v20, v2, s[8:11], s36 offen sc1\n"
      "s_mov_b32 s51, s36\n"
      "s_add_u32 s37, s37, 106496\n"
      "s_add_u32 m0, s43, 122880\n"
      "s_waitcnt vmcnt(0)\n"
      "v_cmp_ne_u32 vcc, s21, v20\n"
      "s_add_u32 s36, s36, 165888\n"
      "s_add_u32 s52, s52, 106496\n"
      "s_cbranch_vccnz .Lsp_d5_32\n"
      ".Lbk_d5_32:\n"
      "buffer_load_dwordx4 v0, s[8:11], s51 offen offset:1024 sc1 lds\n"
      "buffer_load_dwordx4 v0, s[8:11], s51 offen offset:0 sc1 lds\n"
      "buffer_load_dwordx4 v0, s[8:11], s51 offen offset:2048 sc1 lds\n"
      "buffer_load_dwordx4 v0, s[8:11], s51 offen offset:3072 sc1 lds\n"
      "s_min_u32 s36, s36, s6\n"
      "s_add_u32 m0, s43, 126976\n"
      "s_nop 0\n"
      "buffer_load_dwordx4 v1, s[8:11], s51 offen offset:1024 sc1 lds\n"
      "buffer_load_dword v20, v2, s[8:11], s36 offen sc1\n"
      "buffer_load_dwordx4 v1, s[8:11], s51 offen offset:0 sc1 lds\n"
      "s_add_u32 s58, s41, 4294967264\n"
      "s_add_u32 s37, s37, 106496\n"
      "s_add_u32 m0, s43, 0\n"
      "s_mov_b32 s51, s36\n"
      "v_add_u32 v14, 1, v14\n"
      "s_add_u32 s36, s36, 165888\n"
      "s_add_u32 s37, s37, 106496\n"
      "s_min_u32 s36, s36, s6\n"
      "s_cmp_lt_u32 s41, 32\n"
      "s_cselect_b32 s56, s39, 0x40000000\n"
      "s_add_u32 s53, s53, 599040\n"
      "s_waitcnt vmcnt(1)\n"
      "v_cmp_ne_u32 vcc, s21, v20\n"
      "s_add_u32 s39, s39, 147456\n"
      "s_waitcnt vmcnt(0)\n"
      "ds_write_b32 v16, v14\n"
      "s_cmp_lt_u32 s58, 32\n"
      "s_cselect_b32 s57, s40, 0x40000000\n"
      "s_cbranch_vccnz .Lsp_d0_33\n"
      ".Lbk_d0_33:\n"
      "buffer_load_dwordx4 v0, s[8:11], s51 offen offset:0 sc1 lds\n"
      "s_add_u32 s52, s52, 106496\n"
      "buffer_load_dwordx4 v0, s[8:11], s51 offen offset:1024 sc1 lds\n"
      "buffer_load_dword v20, v2, s[8:11], s36 offen sc1\n"
      "ds_read_b32 v15, v17\n"
      "buffer_load_dwordx4 v0, s[8:11], s51 offen offset:2048 sc1 lds\n"
      "s_add_u32 s40, s40, 4294856704\n"
      "buffer_load_dwordx4 v0, s[8:11], s51 offen offset:3072 sc1 lds\n"
      "s_waitcnt lgkmcnt(0)\n"
      "v_cmp_gt_i32 vcc, v14, v15\n"
      "s_add_u32 m0, s43, 4096\n"
      "s_nop 0\n"
      "buffer_load_dwordx4 v1, s[8:11], s51 offen offset:1024 sc1 lds\n"
      "s_cbranch_vccnz .Lsp_dy5_34\n"
      ".Lbk_dy5_34:\n"
      "ds_read_b128 v[56:59], v23 offset:24576\n"
      "ds_read_b128 v[64:67], v23 offset:26624\n"
      "buffer_load_dwordx4 v1, s[8:11], s51 offen offset:0 sc1 lds\n"
      "ds_read_b128 v[76:79], v23 offset:29696\n"
      "s_add_u32 s41, s41, 6\n"
      "ds_read_b128 v[88:91], v23 offset:32768\n"
      "ds_read_b128 v[68:71], v23 offset:27648\n"
      "ds_read_b128 v[96:99], v23 offset:34816\n"
      "ds_read_b128 v[80:83], v23 offset:30720\n"
      "ds_read_b128 v[72:75], v23 offset:28672\n"
      "s_mov_b32 s51, s36\n"
      "ds_read_b128 v[112:115], v23 offset:38912\n"
      "ds_read_b128 v[60:63], v23 offset:25600\n"
      "ds_read_b128 v[104:107], v23 offset:36864\n"
      "ds_read_b128 v[84:87], v23 offset:31744\n"
      "ds_read_b128 v[140:143], v23 offset:46080\n"
      "ds_read_b128 v[100:103], v23 offset:35840\n"
      "s_waitcnt vmcnt(0) lgkmcnt(0)\n"
      "s_nop 4\n"
      "s_branch .Ld_t5\n"
      ".Ld_end:\n"
      "s_endpgm\n"
      ".Lsp_d0_1:\n"
      "s_mov_b32 s22, s23\n"
      ".Lsl_d0_1:\n"
      "buffer_load_dword v20, v2, s[8:11], s51 offen sc1\n"
      "s_waitcnt vmcnt(0)\n"
      "v_cmp_ne_u32 vcc, s21, v20\n"
      "s_cbranch_vccz .Lbk_d0_1\n"
      "s_sleep 4\n"
      "s_sub_u32 s22, s22, 1\n"
      "s_cbranch_scc0 .Lsl_d0_1\n"
      "s_mov_b32 s23, 0\n"
      "s_branch .Lbk_d0_1\n"
      ".Lsp_d1_2:\n"
      "s_mov_b32 s22, s23\n"
      ".Lsl_d1_2:\n"
      "buffer_load_dword v20, v2, s[8:11], s51 offen sc1\n"
      "s_waitcnt vmcnt(0)\n"
      "v_cmp_ne_u32 vcc, s21, v20\n"
      "s_cbranch_vccz .Lbk_d1_2\n"
      "s_sleep 4\n"
      "s_sub_u32 s22, s22, 1\n"
      "s_cbranch_scc0 .Lsl_d1_2\n"
      "s_mov_b32 s23, 0\n"
      "s_branch .Lbk_d1_2\n"
      ".Lsp_dy0_3:\n"
      "s_lshl_b32 s22, s23, 4\n"
      ".Lsl_dy0_3:\n"
      "ds_read_b32 v15, v17\n"
      "s_waitcnt lgkmcnt(0)\n"
      "v_cmp_gt_i32 vcc, v14, v15\n"
      "s_cbranch_vccz .Lbk_dy0_3\n"
      "s_sub_u32 s22, s22, 1\n"
      "s_cbranch_scc0 .Lsl_dy0_3\n"
      "s_mov_b32 s23, 0\n"
      "s_branch .Lbk_dy0_3\n"
      ".Lsp_d2_4:\n"
      "s_mov_b32 s22, s23\n"
      "buffer_load_dword v20, v2, s[8:11], s51 offen sc1\n"
      "s_waitcnt vmcnt(0)\n"
      "v_cmp_ne_u32 vcc, s21, v20\n"
      "s_add_u32 s30, s52, 106496\n"
      "buffer_store_dword v18, v4, s[8:11], s30 offen sc1\n"
      "s_cbranch_vccz .Lbk_d2_4\n"
      ".Lsl_d2_4:\n"
      "buffer_load_dword v20, v2, s[8:11], s51 offen sc1\n"
      "s_waitcnt vmcnt(0)\n"
      "v_cmp_ne_u32 vcc, s21, v20\n"
      "s_cbranch_vccz .Lbk_d2_4\n"
      "s_sleep 4\n"
      "s_sub_u32 s22, s22, 1\n"
      "s_cbranch_scc0 .Lsl_d2_4\n"
      "s_mov_b32 s23, 0\n"
      "s_branch .Lbk_d2_4\n"
      ".Lsp_dy1_5:\n"
      "s_lshl_b32 s22, s23, 4\n"
      ".Lsl_dy1_5:\n"
      "ds_read_b32 v15, v17\n"
      "s_waitcnt lgkmcnt(0)\n"
      "v_cmp_gt_i32 vcc, v14, v15\n"
      "s_cbranch_vccz .Lbk_dy1_5\n"
      "s_sub_u32 s22, s22, 1\n"
      "s_cbranch_scc0 .Lsl_dy1_5\n"
      "s_mov_b32 s23, 0\n"
      "s_branch .Lbk_dy1_5\n"
      ".Lsp_d3_6:\n"
      "s_mov_b32 s22, s23\n"
      "buffer_load_dword v20, v2, s[8:11], s51 offen sc1\n"
      "s_waitcnt vmcnt(0)\n"
      "v_cmp_ne_u32 vcc, s21, v20\n"
      "buffer_store_dword v18, v5, s[8:11], s56 offen sc1\n"
      "s_cbranch_vccz .Lbk_d3_6\n"
      ".Lsl_d3_6:\n"
      "buffer_load_dword v20, v2, s[8:11], s51 offen sc1\n"
      "s_waitcnt vmcnt(0)\n"
      "v_cmp_ne_u32 vcc, s21, v20\n"
      "s_cbranch_vccz .Lbk_d3_6\n"
      "s_sleep 4\n"
      "s_sub_u32 s22, s22, 1\n"
      "s_cbranch_scc0 .Lsl_d3_6\n"
      "s_mov_b32 s23, 0\n"
      "s_branch .Lbk_d3_6\n"
      ".Lsp_dy2_7:\n"
      "s_lshl_b32 s22, s23, 4\n"
      ".Lsl_dy2_7:\n"
      "ds_read_b32 v15, v17\n"
      "s_waitcnt lgkmcnt(0)\n"
      "v_cmp_gt_i32 vcc, v14, v15\n"
      "s_cbranch_vccz .Lbk_dy2_7\n"
      "s_sub_u32 s22, s22, 1\n"
      "s_cbranch_scc0 .Lsl_dy2_7\n"
      "s_mov_b32 s23, 0\n"
      "s_branch .Lbk_dy2_7\n"
      ".Lsp_d4_8:\n"
      "s_mov_b32 s22, s23\n"
      "buffer_load_dword v20, v2, s[8:11], s51 offen sc1\n"
      "s_waitcnt vmcnt(0)\n"
      "v_cmp_ne_u32 vcc, s21, v20\n"
      "s_add_u32 s30, s52, 0\n"
      "buffer_store_dword v18, v4, s[8:11], s30 offen sc1\n"
      "s_cbranch_vccz .Lbk_d4_8\n"
      ".Lsl_d4_8:\n"
      "buffer_load_dword v20, v2, s[8:11], s51 offen sc1\n"
      "s_waitcnt vmcnt(0)\n"
      "v_cmp_ne_u32 vcc, s21, v20\n"
      "s_cbranch_vccz .Lbk_d4_8\n"
      "s_sleep 4\n"
      "s_sub_u32 s22, s22, 1\n"
      "s_cbranch_scc0 .Lsl_d4_8\n"
      "s_mov_b32 s23, 0\n"
      "s_branch .Lbk_d4_8\n"
      ".Lsp_dy3_9:\n"
      "s_lshl_b32 s22, s23, 4\n"
      ".Lsl_dy3_9:\n"
      "ds_read_b32 v15, v17\n"
      "s_waitcnt lgkmcnt(0)\n"
      "v_cmp_gt_i32 vcc, v14, v15\n"
      "s_cbranch_vccz .Lbk_dy3_9\n"
      "s_sub_u32 s22, s22, 1\n"
      "s_cbranch_scc0 .Lsl_dy3_9\n"
      "s_mov_b32 s23, 0\n"
      "s_branch .Lbk_dy3_9\n"
      ".Lsp_d5_10:\n"
      "s_mov_b32 s22, s23\n"
      "buffer_load_dword v20, v2, s[8:11], s51 offen sc1\n"
      "s_waitcnt vmcnt(0)\n"
      "v_cmp_ne_u32 vcc, s21, v20\n"
      "s_add_u32 s30, s52, 0\n"
      "buffer_store_dword v18, v4, s[8:11], s30 offen sc1\n"
      "s_cbranch_vccz .Lbk_d5_10\n"
      ".Lsl_d5_10:\n"
      "buffer_load_dword v20, v2, s[8:11], s51 offen sc1\n"
      "s_waitcnt vmcnt(0)\n"
      "v_cmp_ne_u32 vcc, s21, v20\n"
      "s_cbranch_vccz .Lbk_d5_10\n"
      "s_sleep 4\n"
      "s_sub_u32 s22, s22, 1\n"
      "s_cbranch_scc0 .Lsl_d5_10\n"
      "s_mov_b32 s23, 0\n"
      "s_branch .Lbk_d5_10\n"
      ".Lsp_dy4_11:\n"
      "s_lshl_b32 s22, s23, 4\n"
      ".Lsl_dy4_11:\n"
      "ds_read_b32 v15, v17\n"
      "s_waitcnt lgkmcnt(0)\n"
      "v_cmp_gt_i32 vcc, v14, v15\n"
      "s_cbranch_vccz .Lbk_dy4_11\n"
      "s_sub_u32 s22, s22, 1\n"
      "s_cbranch_scc0 .Lsl_dy4_11\n"
      "s_mov_b32 s23, 0\n"
      "s_branch .Lbk_dy4_11\n"
      ".Lsp_df_12:\n"
      "s_mov_b32 s22, s23\n"
      "buffer_load_dword v21, v3, s[8:11], s53 offen sc1\n"
      "s_waitcnt vmcnt(0)\n"
      "v_cmp_ne_u32 vcc, s21, v21\n"
      "s_add_u32 s30, s52, 0\n"
      "buffer_store_dword v18, v4, s[8:11], s30 offen sc1\n"
      "s_cbranch_vccz .Lbk_df_12\n"
      ".Lsl_df_12:\n"
      "buffer_load_dword v21, v3, s[8:11], s53 offen sc1\n"
      "s_waitcnt vmcnt(0)\n"
      "v_cmp_ne_u32 vcc, s21, v21\n"
      "s_cbranch_vccz .Lbk_df_12\n"
      "s_sleep 4\n"
      "s_sub_u32 s22, s22, 1\n"
      "s_cbranch_scc0 .Lsl_df_12\n"
      "s_mov_b32 s23, 0\n"
      "s_branch .Lbk_df_12\n"
      ".Lsp_d0_13:\n"
      "s_mov_b32 s22, s23\n"
      "buffer_load_dword v20, v2, s[8:11], s51 offen sc1\n"
      "s_waitcnt vmcnt(0)\n"
      "v_cmp_ne_u32 vcc, s21, v20\n"
      "s_add_u32 s30, s52, 106496\n"
      "buffer_store_dword v18, v4, s[8:11], s30 offen sc1\n"
      "s_cbranch_vccz .Lbk_d0_13\n"
      ".Lsl_d0_13:\n"
      "buffer_load_dword v20, v2, s[8:11], s51 offen sc1\n"
      "s_waitcnt vmcnt(0)\n"
      "v_cmp_ne_u32 vcc, s21, v20\n"
      "s_cbranch_vccz .Lbk_d0_13\n"
      "s_sleep 4\n"
      "s_sub_u32 s22, s22, 1\n"
      "s_cbranch_scc0 .Lsl_d0_13\n"
      "s_mov_b32 s23, 0\n"
      "s_branch .Lbk_d0_13\n"
      ".Lsp_dy5_14:\n"
      "s_lshl_b32 s22, s23, 4\n"
      ".Lsl_dy5_14:\n"
      "ds_read_b32 v15, v17\n"
      "s_waitcnt lgkmcnt(0)\n"
      "v_cmp_gt_i32 vcc, v14, v15\n"
      "s_cbranch_vccz .Lbk_dy5_14\n"
      "s_sub_u32 s22, s22, 1\n"
      "s_cbranch_scc0 .Lsl_dy5_14\n"
      "s_mov_b32 s23, 0\n"
      "s_branch .Lbk_dy5_14\n"
      ".Lsp_d1_15:\n"
      "s_mov_b32 s22, s23\n"
      "buffer_load_dword v20, v2, s[8:11], s51 offen sc1\n"
      "s_waitcnt vmcnt(0)\n"
      "v_cmp_ne_u32 vcc, s21, v20\n"
      "s_add_u32 s30, s52, 106496\n"
      "buffer_store_dword v18, v4, s[8:11], s30 offen sc1\n"
      "s_cbranch_vccz .Lbk_d1_15\n"
      ".Lsl_d1_15:\n"
      "buffer_load_dword v20, v2, s[8:11], s51 offen sc1\n"
      "s_waitcnt vmcnt(0)\n"
      "v_cmp_ne_u32 vcc, s21, v20\n"
      "s_cbranch_vccz .Lbk_d1_15\n"
      "s_sleep 4\n"
      "s_sub_u32 s22, s22, 1\n"
      "s_cbranch_scc0 .Lsl_d1_15\n"
      "s_mov_b32 s23, 0\n"
      "s_branch .Lbk_d1_15\n"
      ".Lsp_dy0_16:\n"
      "s_lshl_b32 s22, s23, 4\n"
      ".Lsl_dy0_16:\n"
      "ds_read_b32 v15, v17\n"
      "s_waitcnt lgkmcnt(0)\n"
      "v_cmp_gt_i32 vcc, v14, v15\n"
      "s_cbranch_vccz .Lbk_dy0_16\n"
      "s_sub_u32 s22, s22, 1\n"
      "s_cbranch_scc0 .Lsl_dy0_16\n"
      "s_mov_b32 s23, 0\n"
      "s_branch .Lbk_dy0_16\n"
      ".Lsp_df_17:\n"
      "s_mov_b32 s22, s23\n"
      "buffer_load_dword v21, v3, s[8:11], s53 offen sc1\n"
      "s_waitcnt vmcnt(0)\n"
      "v_cmp_ne_u32 vcc, s21, v21\n"
      "s_add_u32 s30, s52, 0\n"
      "buffer_store_dword v18, v4, s[8:11], s30 offen sc1\n"
      "s_cbranch_vccz .Lbk_df_17\n"
      ".Lsl_df_17:\n"
      "buffer_load_dword v21, v3, s[8:11], s53 offen sc1\n"
      "s_waitcnt vmcnt(0)\n"
      "v_cmp_ne_u32 vcc, s21, v21\n"
      "s_cbranch_vccz .Lbk_df_17\n"
      "s_sleep 4\n"
      "s_sub_u32 s22, s22, 1\n"
      "s_cbranch_scc0 .Lsl_df_17\n"
      "s_mov_b32 s23, 0\n"
      "s_branch .Lbk_df_17\n"
      ".Lsp_df_18:\n"
      "s_mov_b32 s22, s23\n"
      "buffer_load_dword v21, v3, s[8:11], s53 offen sc1\n"
      "s_waitcnt vmcnt(0)\n"
      "v_cmp_ne_u32 vcc, s21, v21\n"
      "s_add_u32 s30, s52, 0\n"
      "buffer_store_dword v18, v4, s[8:11], s30 offen sc1\n"
      "s_cbranch_vccz .Lbk_df_18\n"
      ".Lsl_df_18:\n"
      "buffer_load_dword v21, v3, s[8:11], s53 offen sc1\n"
      "s_waitcnt vmcnt(0)\n"
      "v_cmp_ne_u32 vcc, s21, v21\n"
      "s_cbranch_vccz .Lbk_df_18\n"
      "s_sleep 4\n"
      "s_sub_u32 s22, s22, 1\n"
      "s_cbranch_scc0 .Lsl_df_18\n"
      "s_mov_b32 s23, 0\n"
      "s_branch .Lbk_df_18\n"
      ".Lsp_df_19:\n"
      "s_mov_b32 s22, s23\n"
      "buffer_load_dword v21, v3, s[8:11], s53 offen sc1\n"
      "s_waitcnt vmcnt(0)\n"
      "v_cmp_ne_u32 vcc, s21, v21\n"
      "s_add_u32 s30, s52, 0\n"
      "buffer_store_dword v18, v4, s[8:11], s30 offen sc1\n"
      "s_cbranch_vccz .Lbk_df_19\n"
      ".Lsl_df_19:\n"
      "buffer_load_dword v21, v3, s[8:11], s53 offen sc1\n"
      "s_waitcnt vmcnt(0)\n"
      "v_cmp_ne_u32 vcc, s21, v21\n"
      "s_cbranch_vccz .Lbk_df_19\n"
      "s_sleep 4\n"
      "s_sub_u32 s22, s22, 1\n"
      "s_cbranch_scc0 .Lsl_df_19\n"
      "s_mov_b32 s23, 0\n"
      "s_branch .Lbk_df_19\n"
      ".Lsp_d1_20:\n"
      "s_mov_b32 s22, s23\n"
      ".Lsl_d1_20:\n"
      "buffer_load_dword v20, v2, s[8:11], s51 offen sc1\n"
      "s_waitcnt vmcnt(0)\n"
      "v_cmp_ne_u32 vcc, s21, v20\n"
      "s_cbranch_vccz .Lbk_d1_20\n"
      "s_sleep 4\n"
      "s_sub_u32 s22, s22, 1\n"
      "s_cbranch_scc0 .Lsl_d1_20\n"
      "s_mov_b32 s23, 0\n"
      "s_branch .Lbk_d1_20\n"
      ".Lsp_d2_21:\n"
      "s_mov_b32 s22, s23\n"
      ".Lsl_d2_21:\n"
      "buffer_load_dword v20, v2, s[8:11], s51 offen sc1\n"
      "s_waitcnt vmcnt(0)\n"
      "v_cmp_ne_u32 vcc, s21, v20\n"
      "s_cbranch_vccz .Lbk_d2_21\n"
      "s_sleep 4\n"
      "s_sub_u32 s22, s22, 1\n"
      "s_cbranch_scc0 .Lsl_d2_21\n"
      "s_mov_b32 s23, 0\n"
      "s_branch .Lbk_d2_21\n"
      ".Lsp_dy1_22:\n"
      "s_lshl_b32 s22, s23, 4\n"
      ".Lsl_dy1_22:\n"
      "ds_read_b32 v15, v17\n"
      "s_waitcnt lgkmcnt(0)\n"
      "v_cmp_gt_i32 vcc, v14, v15\n"
      "s_cbranch_vccz .Lbk_dy1_22\n"
      "s_sub_u32 s22, s22, 1\n"
      "s_cbranch_scc0 .Lsl_dy1_22\n"
      "s_mov_b32 s23, 0\n"
      "s_branch .Lbk_dy1_22\n"
      ".Lsp_d2_23:\n"
      "s_mov_b32 s22, s23\n"
      ".Lsl_d2_23:\n"
      "buffer_load_dword v20, v2, s[8:11], s51 offen sc1\n"
      "s_waitcnt vmcnt(0)\n"
      "v_cmp_ne_u32 vcc, s21, v20\n"
      "s_cbranch_vccz .Lbk_d2_23\n"
      "s_sleep 4\n"
      "s_sub_u32 s22, s22, 1\n"
      "s_cbranch_scc0 .Lsl_d2_23\n"
      "s_mov_b32 s23, 0\n"
      "s_branch .Lbk_d2_23\n"
      ".Lsp_d3_24:\n"
      "s_mov_b32 s22, s23\n"
      ".Lsl_d3_24:\n"
      "buffer_load_dword v20, v2, s[8:11], s51 offen sc1\n"
      "s_waitcnt vmcnt(0)\n"
      "v_cmp_ne_u32 vcc, s21, v20\n"
      "s_cbranch_vccz .Lbk_d3_24\n"
      "s_sleep 4\n"
      "s_sub_u32 s22, s22, 1\n"
      "s_cbranch_scc0 .Lsl_d3_24\n"
      "s_mov_b32 s23, 0\n"
      "s_branch .Lbk_d3_24\n"
      ".Lsp_dy2_25:\n"
      "s_lshl_b32 s22, s23, 4\n"
      ".Lsl_dy2_25:\n"
      "ds_read_b32 v15, v17\n"
      "s_waitcnt lgkmcnt(0)\n"
      "v_cmp_gt_i32 vcc, v14, v15\n"
      "s_cbranch_vccz .Lbk_dy2_25\n"
      "s_sub_u32 s22, s22, 1\n"
      "s_cbranch_scc0 .Lsl_dy2_25\n"
      "s_mov_b32 s23, 0\n"
      "s_branch .Lbk_dy2_25\n"
      ".Lsp_d3_26:\n"
      "s_mov_b32 s22, s23\n"
      ".Lsl_d3_26:\n"
      "buffer_load_dword v20, v2, s[8:11], s51 offen sc1\n"
      "s_waitcnt vmcnt(0)\n"
      "v_cmp_ne_u32 vcc, s21, v20\n"
      "s_cbranch_vccz .Lbk_d3_26\n"
      "s_sleep 4\n"
      "s_sub_u32 s22, s22, 1\n"
      "s_cbranch_scc0 .Lsl_d3_26\n"
      "s_mov_b32 s23, 0\n"
      "s_branch .Lbk_d3_26\n"
      ".Lsp_d4_27:\n"
      "s_mov_b32 s22, s23\n"
      ".Lsl_d4_27:\n"
      "buffer_load_dword v20, v2, s[8:11], s51 offen sc1\n"
      "s_waitcnt vmcnt(0)\n"
      "v_cmp_ne_u32 vcc, s21, v20\n"
      "s_cbranch_vccz .Lbk_d4_27\n"
      "s_sleep 4\n"
      "s_sub_u32 s22, s22, 1\n"
      "s_cbranch_scc0 .Lsl_d4_27\n"
      "s_mov_b32 s23, 0\n"
      "s_branch .Lbk_d4_27\n"
      ".Lsp_dy3_28:\n"
      "s_lshl_b32 s22, s23, 4\n"
      ".Lsl_dy3_28:\n"
      "ds_read_b32 v15, v17\n"
      "s_waitcnt lgkmcnt(0)\n"
      "v_cmp_gt_i32 vcc, v14, v15\n"
      "s_cbranch_vccz .Lbk_dy3_28\n"
      "s_sub_u32 s22, s22, 1\n"
      "s_cbranch_scc0 .Lsl_dy3_28\n"
      "s_mov_b32 s23, 0\n"
      "s_branch .Lbk_dy3_28\n"
      ".Lsp_d4_29:\n"
      "s_mov_b32 s22, s23\n"
      ".Lsl_d4_29:\n"
      "buffer_load_dword v20, v2, s[8:11], s51 offen sc1\n"
      "s_waitcnt vmcnt(0)\n"
      "v_cmp_ne_u32 vcc, s21, v20\n"
      "s_cbranch_vccz .Lbk_d4_29\n"
      "s_sleep 4\n"
      "s_sub_u32 s22, s22, 1\n"
      "s_cbranch_scc0 .Lsl_d4_29\n"
      "s_mov_b32 s23, 0\n"
      "s_branch .Lbk_d4_29\n"
      ".Lsp_d5_30:\n"
      "s_mov_b32 s22, s23\n"
      ".Lsl_d5_30:\n"
      "buffer_load_dword v20, v2, s[8:11], s51 offen sc1\n"
      "s_waitcnt vmcnt(0)\n"
      "v_cmp_ne_u32 vcc, s21, v20\n"
      "s_cbranch_vccz .Lbk_d5_30\n"
      "s_sleep 4\n"
      "s_sub_u32 s22, s22, 1\n"
      "s_cbranch_scc0 .Lsl_d5_30\n"
      "s_mov_b32 s23, 0\n"
      "s_branch .Lbk_d5_30\n"
      ".Lsp_dy4_31:\n"
      "s_lshl_b32 s22, s23, 4\n"
      ".Lsl_dy4_31:\n"
      "ds_read_b32 v15, v17\n"
      "s_waitcnt lgkmcnt(0)\n"
      "v_cmp_gt_i32 vcc, v14, v15\n"
      "s_cbranch_vccz .Lbk_dy4_31\n"
      "s_sub_u32 s22, s22, 1\n"
      "s_cbranch_scc0 .Lsl_dy4_31\n"
      "s_mov_b32 s23, 0\n"
      "s_branch .Lbk_dy4_31\n"
      ".Lsp_d5_32:\n"
      "s_mov_b32 s22, s23\n"
      ".Lsl_d5_32:\n"
      "buffer_load_dword v20, v2, s[8:11], s51 offen sc1\n"
      "s_waitcnt vmcnt(0)\n"
      "v_cmp_ne_u32 vcc, s21, v20\n"
      "s_cbranch_vccz .Lbk_d5_32\n"
      "s_sleep 4\n"
      "s_sub_u32 s22, s22, 1\n"
      "s_cbranch_scc0 .Lsl_d5_32\n"
      "s_mov_b32 s23, 0\n"
      "s_branch .Lbk_d5_32\n"
      ".Lsp_d0_33:\n"
      "s_mov_b32 s22, s23\n"
      ".Lsl_d0_33:\n"
      "buffer_load_dword v20, v2, s[8:11], s51 offen sc1\n"
      "s_waitcnt vmcnt(0)\n"
      "v_cmp_ne_u32 vcc, s21, v20\n"
      "s_cbranch_vccz .Lbk_d0_33\n"
      "s_sleep 4\n"
      "s_sub_u32 s22, s22, 1\n"
      "s_cbranch_scc0 .Lsl_d0_33\n"
      "s_mov_b32 s23, 0\n"
      "s_branch .Lbk_d0_33\n"
      ".Lsp_dy5_34:\n"
      "s_lshl_b32 s22, s23, 4\n"
      ".Lsl_dy5_34:\n"
      "ds_read_b32 v15, v17\n"
      "s_waitcnt lgkmcnt(0)\n"
      "v_cmp_gt_i32 vcc, v14, v15\n"
      "s_cbranch_vccz .Lbk_dy5_34\n"
      "s_sub_u32 s22, s22, 1\n"
      "s_cbranch_scc0 .Lsl_dy5_34\n"
      "s_mov_b32 s23, 0\n"
      "s_branch .Lbk_dy5_34\n"
      ".Ld_last:\n"
      :
      : "{s[8:11]}"(rsWS), "{s[12:15]}"(rsA), "{s[16:19]}"(rsB), "{s[92:95]}"(rsC), "{v0}"(v_l16), "{v1}"(v_l16b), "{v2}"(v_fa), "{v3}"(v_ga), "{v4}"(v_ra), "{v5}"(v_rf), "{v6}"(v_xp), "{v7}"(v_xs), "{v8}"(v_op), "{v9}"(v_os), "{v10}"(v_sw), "{v11}"(v_sr), "{v12}"(v_ss), "{v13}"(v_l8), "{v15}"(v_al), "{v16}"(v_yw), "{v17}"(v_yr), "{v19}"(v_hi), "{v176}"(v_at), "{v177}"(v_as), "{v178}"(v_vo0), "{v179}"(v_vo1), "{v180}"(v_vo2), "{v181}"(v_pw), "{v183}"(v_pu), "{s36}"(s_sh), "{s37}"(s_ps), "{s38}"(s_x1), "{s39}"(s_xr), "{s40}"(s_or), "{s41}"(s_p0), "{s42}"(s_m3), "{s43}"(s_m6), "{s44}"(s_so), "{s45}"(s_ph), "{s46}"(s_i0), "{s47}"(s_i5), "{s48}"(s_j0), "{s49}"(s_j5), "{s50}"(s_he), "{s60}"(s_k0)
      : "v255", "a255", "s99", "vcc", "scc", "memory");

  } else {
    const unsigned e1 = (e - 1) & 3, gr = !fast ? slot & 3 : slot < 31 ? (e + slot - 27) & 3 : e1, part = !fast ? 8 * e + (slot >> 2) : slot < 31 ? 32 + e : 32 + e1;
    COMMON(112, 81920);
    const unsigned j = part, f = 4 * j + w;
    const u32x4 rsA = srd(w >> 1 ? Wu : Wg, 576 * 4608 * 2), rsC = srd(Wo, 576 * 576 * 2);
    const unsigned tw0 = !fast && slot < 4 ? ((e * 16 + slot * 4 + w) * 81 + lane) * 128 : OOB, v_tw[2] = {tw0, lane < 17 ? tw0 + 8192 : OOB};
    const unsigned v_l5 = w < 3 ? v_l16 + 16384 - w * 3072 : OOB;
    const unsigned v_fa = 19456 - w * 4096 + (lane >> 3) * 128 + (lane & 7) * 4, v_ra = up * 8 * HREC + l5 + 147456 - f * 1024 + f * 128;
    const unsigned one = q == g ? 0x3f80u << (16 * (p & 1)) : 0u, v_id[2] = {p < 2 ? one : 0u, p < 2 ? 0u : one};
    const unsigned v_st = 18432 + i16 * 32 + q * 8, v_pw = 147456 - f * 1024 + f * 128 + l8 * HREC + l7;
    const unsigned v_at = 65536 + (8 * g + q) * 256 + ((4 * w + p) ^ (4 * q)) * 16;
    const unsigned v_vo0 = (16 * (w & 1) + g) * 9216 + 256 * j + (i16 ^ (4 * g)) * 16;
    const unsigned v_vo1 = v_vo0 + 4 * 9216 - 1024, v_vo2 = v_vo1 + 4 * 9216 - 1024, v_vo3 = v_vo2 + 4 * 9216 - 1024;
    const unsigned s_sx = WS_X1 + gr * NREC * REC + w * 4096, s_sh = WS_H + gr * NREC * HREC + f * 1024;
    const unsigned s_m4 = w * 4096, s_m5 = 16384 + w * 1024;
    asm volatile(
      "s_buffer_load_dword s21, s[8:11], 0\n"
      "s_mov_b32 s23, 65536\n"
      "s_mov_b32 s31, 0x40000000\n"
      "s_mov_b32 s41, 0\n"
      "s_sleep 16\n"
      "buffer_load_dword v58, v54, s[92:95], s41 offen\n"
      "buffer_load_dword v59, v55, s[92:95], s41 offen\n"
      "s_waitcnt vmcnt(0)\n"
      "s_add_u32 m0, s38, 65536\n"
      "s_nop 0\n"
      "buffer_load_dwordx4 v6, s[12:15], s41 offen offset:0 lds\n"
      "buffer_load_dwordx4 v7, s[12:15], s41 offen offset:1024 lds\n"
      "buffer_load_dwordx4 v8, s[12:15], s41 offen offset:2048 lds\n"
      "buffer_load_dwordx4 v9, s[12:15], s41 offen offset:3072 lds\n"
      "s_add_u32 s41, s41, 294912\n"
      "s_add_u32 m0, s38, 81920\n"
      "s_nop 0\n"
      "buffer_load_dwordx4 v6, s[12:15], s41 offen offset:0 lds\n"
      "buffer_load_dwordx4 v7, s[12:15], s41 offen offset:1024 lds\n"
      "buffer_load_dwordx4 v8, s[12:15], s41 offen offset:2048 lds\n"
      "buffer_load_dwordx4 v9, s[12:15], s41 offen offset:3072 lds\n"
      "s_add_u32 s41, s41, 294912\n"
      "s_add_u32 m0, s38, 98304\n"
      "s_nop 0\n"
      "buffer_load_dwordx4 v6, s[12:15], s41 offen offset:0 lds\n"
      "buffer_load_dwordx4 v7, s[12:15], s41 offen offset:1024 lds\n"
      "buffer_load_dwordx4 v8, s[12:15], s41 offen offset:2048 lds\n"
      "buffer_load_dwordx4 v9, s[12:15], s41 offen offset:3072 lds\n"
      "s_add_u32 s41, s41, 294912\n"
      "s_add_u32 m0, s38, 114688\n"
      "s_nop 0\n"
      "buffer_load_dwordx4 v6, s[12:15], s41 offen offset:0 lds\n"
      "buffer_load_dwordx4 v7, s[12:15], s41 offen offset:1024 lds\n"
      "buffer_load_dwordx4 v8, s[12:15], s41 offen offset:2048 lds\n"
      "buffer_load_dwordx4 v9, s[12:15], s41 offen offset:3072 lds\n"
      "s_add_u32 s41, s41, 294912\n"
      "s_add_u32 m0, s38, 131072\n"
      "s_nop 0\n"
      "buffer_load_dwordx4 v6, s[12:15], s41 offen offset:0 lds\n"
      "buffer_load_dwordx4 v7, s[12:15], s41 offen offset:1024 lds\n"
      "buffer_load_dwordx4 v8, s[12:15], s41 offen offset:2048 lds\n"
      "buffer_load_dwordx4 v9, s[12:15], s41 offen offset:3072 lds\n"
      "s_add_u32 s41, s41, 294912\n"
      "v_mov_b32 v172, 0x3f803f80\n"
      "v_mov_b32 v173, 0x3f803f80\n"
      "v_mov_b32 v174, 0x3f803f80\n"
      "v_mov_b32 v175, 0x3f803f80\n"
      "v_add_u32 v19, 49152, v5\n"
      "s_mov_b64 exec, 0\n"
      "s_lshr_b32 s22, s38, 12\n"
      ".Lg_pl:\n"
      "s_cmp_eq_u32 s22, 0\n"
      "s_cbranch_scc1 .Lg_p0\n"
      "s_cmp_eq_u32 s22, 1\n"
      "s_cbranch_scc1 .Lg_p1\n"
      "s_branch .Lg_pe\n"
      ".Lg_pr:\n"
      "s_add_u32 s22, s22, 4\n"
      "s_branch .Lg_pl\n"
      ".Lg_pe:\n"
      "s_mov_b64 exec, -1\n"
      "s_waitcnt vmcnt(16)\n"
      "s_barrier\n"
      "ds_read_b64_tr_b16 a[0:1], v5 offset:0\n"
      "ds_read_b64_tr_b16 a[2:3], v5 offset:1024\n"
      "ds_read_b64_tr_b16 a[72:73], v5 offset:8192\n"
      "ds_read_b64_tr_b16 a[74:75], v5 offset:9216\n"
      "ds_read_b64_tr_b16 a[144:145], v5 offset:8\n"
      "ds_read_b64_tr_b16 a[146:147], v5 offset:1032\n"
      "ds_read_b64_tr_b16 a[216:217], v5 offset:8200\n"
      "ds_read_b64_tr_b16 a[218:219], v5 offset:9224\n"
      "s_add_u32 m0, s38, 147456\n"
      "s_nop 0\n"
      "buffer_load_dwordx4 v6, s[12:15], s41 offen offset:0 lds\n"
      "buffer_load_dwordx4 v7, s[12:15], s41 offen offset:1024 lds\n"
      "buffer_load_dwordx4 v8, s[12:15], s41 offen offset:2048 lds\n"
      "buffer_load_dwordx4 v9, s[12:15], s41 offen offset:3072 lds\n"
      "s_add_u32 s41, s41, 294912\n"
      "s_waitcnt vmcnt(16) lgkmcnt(0)\n"
      "s_barrier\n"
      "ds_read_b64_tr_b16 a[4:5], v5 offset:16384\n"
      "ds_read_b64_tr_b16 a[6:7], v5 offset:17408\n"
      "ds_read_b64_tr_b16 a[76:77], v5 offset:24576\n"
      "ds_read_b64_tr_b16 a[78:79], v5 offset:25600\n"
      "ds_read_b64_tr_b16 a[148:149], v5 offset:16392\n"
      "ds_read_b64_tr_b16 a[150:151], v5 offset:17416\n"
      "ds_read_b64_tr_b16 a[220:221], v5 offset:24584\n"
      "ds_read_b64_tr_b16 a[222:223], v5 offset:25608\n"
      "v_mfma_f32_16x16x32_bf16 v[76:79], v[172:175], a[0:3], 0\n"
      "v_mfma_f32_16x16x32_bf16 v[80:83], v[172:175], a[72:75], 0\n"
      "v_mfma_f32_16x16x32_bf16 v[84:87], v[172:175], a[144:147], 0\n"
      "v_mfma_f32_16x16x32_bf16 v[88:91], v[172:175], a[216:219], 0\n"
      "s_add_u32 m0, s38, 65536\n"
      "s_nop 0\n"
      "buffer_load_dwordx4 v6, s[12:15], s41 offen offset:0 lds\n"
      "buffer_load_dwordx4 v7, s[12:15], s41 offen offset:1024 lds\n"
      "buffer_load_dwordx4 v8, s[12:15], s41 offen offset:2048 lds\n"
      "buffer_load_dwordx4 v9, s[12:15], s41 offen offset:3072 lds\n"
      "s_add_u32 s41, s41, 294912\n"
      "s_waitcnt vmcnt(16) lgkmcnt(0)\n"
      "s_barrier\n"
      "ds_read_b64_tr_b16 a[8:9], v5 offset:32768\n"
      "ds_read_b64_tr_b16 a[10:11], v5 offset:33792\n"
      "ds_read_b64_tr_b16 a[80:81], v5 offset:40960\n"
      "ds_read_b64_tr_b16 a[82:83], v5 offset:41984\n"
      "ds_read_b64_tr_b16 a[152:153], v5 offset:32776\n"
      "ds_read_b64_tr_b16 a[154:155], v5 offset:33800\n"
      "ds_read_b64_tr_b16 a[224:225], v5 offset:40968\n"
      "ds_read_b64_tr_b16 a[226:227], v5 offset:41992\n"
      "v_mfma_f32_16x16x32_bf16 v[76:79], v[172:175], a[4:7], v[76:79]\n"
      "v_mfma_f32_16x16x32_bf16 v[80:83], v[172:175], a[76:79], v[80:83]\n"
      "v_mfma_f32_16x16x32_bf16 v[84:87], v[172:175], a[148:151], v[84:87]\n"
      "v_mfma_f32_16x16x32_bf16 v[88:91], v[172:175], a[220:223], v[88:91]\n"
      "s_add_u32 m0, s38, 81920\n"
      "s_nop 0\n"
      "buffer_load_dwordx4 v6, s[12:15], s41 offen offset:0 lds\n"
      "buffer_load_dwordx4 v7, s[12:15], s41 offen offset:1024 lds\n"
      "buffer_load_dwordx4 v8, s[12:15], s41 offen offset:2048 lds\n"
      "buffer_load_dwordx4 v9, s[12:15], s41 offen offset:3072 lds\n"
      "s_add_u32 s41, s41, 294912\n"
      "s_waitcnt vmcnt(16) lgkmcnt(0)\n"
      "s_barrier\n"
      "ds_read_b64_tr_b16 a[12:13], v19 offset:0\n"
      "ds_read_b64_tr_b16 a[14:15], v19 offset:1024\n"
      "ds_read_b64_tr_b16 a[84:85], v19 offset:8192\n"
      "ds_read_b64_tr_b16 a[86:87], v19 offset:9216\n"
      "ds_read_b64_tr_b16 a[156:157], v19 offset:8\n"
      "ds_read_b64_tr_b16 a[158:159], v19 offset:1032\n"
      "ds_read_b64_tr_b16 a[228:229], v19 offset:8200\n"
      "ds_read_b64_tr_b16 a[230:231], v19 offset:9224\n"
      "v_mfma_f32_16x16x32_bf16 v[76:79], v[172:175], a[8:11], v[76:79]\n"
      "v_mfma_f32_16x16x32_bf16 v[80:83], v[172:175], a[80:83], v[80:83]\n"
      "v_mfma_f32_16x16x32_bf16 v[84:87], v[172:175], a[152:155], v[84:87]\n"
      "v_mfma_f32_16x16x32_bf16 v[88:91], v[172:175], a[224:227], v[88:91]\n"
      "s_add_u32 m0, s38, 98304\n"
      "s_nop 0\n"
      "buffer_load_dwordx4 v6, s[12:15], s41 offen offset:0 lds\n"
      "buffer_load_dwordx4 v7, s[12:15], s41 offen offset:1024 lds\n"
      "buffer_load_dwordx4 v8, s[12:15], s41 offen offset:2048 lds\n"
      "buffer_load_dwordx4 v9, s[12:15], s41 offen offset:3072 lds\n"
      "s_add_u32 s41, s41, 294912\n"
      "s_waitcnt vmcnt(16) lgkmcnt(0)\n"
      "s_barrier\n"
      "ds_read_b64_tr_b16 a[16:17], v19 offset:16384\n"
      "ds_read_b64_tr_b16 a[18:19], v19 offset:17408\n"
      "ds_read_b64_tr_b16 a[88:89], v19 offset:24576\n"
      "ds_read_b64_tr_b16 a[90:91], v19 offset:25600\n"
      "ds_read_b64_tr_b16 a[160:161], v19 offset:16392\n"
      "ds_read_b64_tr_b16 a[162:163], v19 offset:17416\n"
      "ds_read_b64_tr_b16 a[232:233], v19 offset:24584\n"
      "ds_read_b64_tr_b16 a[234:235], v19 offset:25608\n"
      "v_mfma_f32_16x16x32_bf16 v[76:79], v[172:175], a[12:15], v[76:79]\n"
      "v_mfma_f32_16x16x32_bf16 v[80:83], v[172:175], a[84:87], v[80:83]\n"
      "v_mfma_f32_16x16x32_bf16 v[84:87], v[172:175], a[156:159], v[84:87]\n"
      "v_mfma_f32_16x16x32_bf16 v[88:91], v[172:175], a[228:231], v[88:91]\n"
      "s_add_u32 m0, s38, 114688\n"
      "s_nop 0\n"
      "buffer_load_dwordx4 v6, s[12:15], s41 offen offset:0 lds\n"
      "buffer_load_dwordx4 v7, s[12:15], s41 offen offset:1024 lds\n"
      "buffer_load_dwordx4 v8, s[12:15], s41 offen offset:2048 lds\n"
      "buffer_load_dwordx4 v9, s[12:15], s41 offen offset:3072 lds\n"
      "s_add_u32 s41, s41, 294912\n"
      "s_waitcnt vmcnt(16) lgkmcnt(0)\n"
      "s_barrier\n"
      "ds_read_b64_tr_b16 a[20:21], v19 offset:32768\n"
      "ds_read_b64_tr_b16 a[22:23], v19 offset:33792\n"
      "ds_read_b64_tr_b16 a[92:93], v19 offset:40960\n"
      "ds_read_b64_tr_b16 a[94:95], v19 offset:41984\n"
      "ds_read_b64_tr_b16 a[164:165], v19 offset:32776\n"
      "ds_read_b64_tr_b16 a[166:167], v19 offset:33800\n"
      "ds_read_b64_tr_b16 a[236:237], v19 offset:40968\n"
      "ds_read_b64_tr_b16 a[238:239], v19 offset:41992\n"
      "v_mfma_f32_16x16x32_bf16 v[76:79], v[172:175], a[16:19], v[76:79]\n"
      "v_mfma_f32_16x16x32_bf16 v[80:83], v[172:175], a[88:91], v[80:83]\n"
      "v_mfma_f32_16x16x32_bf16 v[84:87], v[172:175], a[160:163], v[84:87]\n"
      "v_mfma_f32_16x16x32_bf16 v[88:91], v[172:175], a[232:235], v[88:91]\n"
      "s_add_u32 m0, s38, 131072\n"
      "s_nop 0\n"
      "buffer_load_dwordx4 v6, s[12:15], s41 offen offset:0 lds\n"
      "buffer_load_dwordx4 v7, s[12:15], s41 offen offset:1024 lds\n"
      "buffer_load_dwordx4 v8, s[12:15], s41 offen offset:2048 lds\n"
      "buffer_load_dwordx4 v9, s[12:15], s41 offen offset:3072 lds\n"
      "s_add_u32 s41, s41, 294912\n"
      "s_waitcnt vmcnt(16) lgkmcnt(0)\n"
      "s_barrier\n"
      "ds_read_b64_tr_b16 a[24:25], v5 offset:0\n"
      "ds_read_b64_tr_b16 a[26:27], v5 offset:1024\n"
      "ds_read_b64_tr_b16 a[96:97], v5 offset:8192\n"
      "ds_read_b64_tr_b16 a[98:99], v5 offset:9216\n"
      "ds_read_b64_tr_b16 a[168:169], v5 offset:8\n"
      "ds_read_b64_tr_b16 a[170:171], v5 offset:1032\n"
      "ds_read_b64_tr_b16 a[240:241], v5 offset:8200\n"
      "ds_read_b64_tr_b16 a[242:243], v5 offset:9224\n"
      "v_mfma_f32_16x16x32_bf16 v[76:79], v[172:175], a[20:23], v[76:79]\n"
      "v_mfma_f32_16x16x32_bf16 v[80:83], v[172:175], a[92:95], v[80:83]\n"
      "v_mfma_f32_16x16x32_bf16 v[84:87], v[172:175], a[164:167], v[84:87]\n"
      "v_mfma_f32_16x16x32_bf16 v[88:91], v[172:175], a[236:239], v[88:91]\n"
      "s_add_u32 m0, s38, 147456\n"
      "s_nop 0\n"
      "buffer_load_dwordx4 v6, s[12:15], s41 offen offset:0 lds\n"
      "buffer_load_dwordx4 v7, s[12:15], s41 offen offset:1024 lds\n"
      "buffer_load_dwordx4 v8, s[12:15], s41 offen offset:2048 lds\n"
      "buffer_load_dwordx4 v9, s[12:15], s41 offen offset:3072 lds\n"
      "s_add_u32 s41, s41, 294912\n"
      "s_waitcnt vmcnt(16) lgkmcnt(0)\n"
      "s_barrier\n"
      "ds_read_b64_tr_b16 a[28:29], v5 offset:16384\n"
      "ds_read_b64_tr_b16 a[30:31], v5 offset:17408\n"
      "ds_read_b64_tr_b16 a[100:101], v5 offset:24576\n"
      "ds_read_b64_tr_b16 a[102:103], v5 offset:25600\n"
      "ds_read_b64_tr_b16 a[172:173], v5 offset:16392\n"
      "ds_read_b64_tr_b16 a[174:175], v5 offset:17416\n"
      "ds_read_b64_tr_b16 a[244:245], v5 offset:24584\n"
      "ds_read_b64_tr_b16 a[246:247], v5 offset:25608\n"
      "v_mfma_f32_16x16x32_bf16 v[76:79], v[172:175], a[24:27], v[76:79]\n"
      "v_mfma_f32_16x16x32_bf16 v[80:83], v[172:175], a[96:99], v[80:83]\n"
      "v_mfma_f32_16x16x32_bf16 v[84:87], v[172:175], a[168:171], v[84:87]\n"
      "v_mfma_f32_16x16x32_bf16 v[88:91], v[172:175], a[240:243], v[88:91]\n"
      "s_add_u32 m0, s38, 65536\n"
      "s_nop 0\n"
      "buffer_load_dwordx4 v6, s[12:15], s41 offen offset:0 lds\n"
      "buffer_load_dwordx4 v7, s[12:15], s41 offen offset:1024 lds\n"
      "buffer_load_dwordx4 v8, s[12:15], s41 offen offset:2048 lds\n"
      "buffer_load_dwordx4 v9, s[12:15], s41 offen offset:3072 lds\n"
      "s_add_u32 s41, s41, 294912\n"
      "s_waitcnt vmcnt(16) lgkmcnt(0)\n"
      "s_barrier\n"
      "ds_read_b64_tr_b16 a[32:33], v5 offset:32768\n"
      "ds_read_b64_tr_b16 a[34:35], v5 offset:33792\n"
      "ds_read_b64_tr_b16 a[104:105], v5 offset:40960\n"
      "ds_read_b64_tr_b16 a[106:107], v5 offset:41984\n"
      "ds_read_b64_tr_b16 a[176:177], v5 offset:32776\n"
      "ds_read_b64_tr_b16 a[178:179], v5 offset:33800\n"
      "ds_read_b64_tr_b16 a[248:249], v5 offset:40968\n"
      "ds_read_b64_tr_b16 a[250:251], v5 offset:41992\n"
      "v_mfma_f32_16x16x32_bf16 v[76:79], v[172:175], a[28:31], v[76:79]\n"
      "v_mfma_f32_16x16x32_bf16 v[80:83], v[172:175], a[100:103], v[80:83]\n"
      "v_mfma_f32_16x16x32_bf16 v[84:87], v[172:175], a[172:175], v[84:87]\n"
      "v_mfma_f32_16x16x32_bf16 v[88:91], v[172:175], a[244:247], v[88:91]\n"
      "s_add_u32 m0, s38, 81920\n"
      "s_nop 0\n"
      "buffer_load_dwordx4 v6, s[12:15], s41 offen offset:0 lds\n"
      "buffer_load_dwordx4 v7, s[12:15], s41 offen offset:1024 lds\n"
      "buffer_load_dwordx4 v8, s[12:15], s41 offen offset:2048 lds\n"
      "buffer_load_dwordx4 v9, s[12:15], s41 offen offset:3072 lds\n"
      "s_add_u32 s41, s41, 294912\n"
      "s_waitcnt vmcnt(16) lgkmcnt(0)\n"
      "s_barrier\n"
      "ds_read_b64_tr_b16 a[36:37], v19 offset:0\n"
      "ds_read_b64_tr_b16 a[38:39], v19 offset:1024\n"
      "ds_read_b64_tr_b16 a[108:109], v19 offset:8192\n"
      "ds_read_b64_tr_b16 a[110:111], v19 offset:9216\n"
      "ds_read_b64_tr_b16 a[180:181], v19 offset:8\n"
      "ds_read_b64_tr_b16 a[182:183], v19 offset:1032\n"
      "ds_read_b64_tr_b16 a[252:253], v19 offset:8200\n"
      "ds_read_b64_tr_b16 a[254:255], v19 offset:9224\n"
      "v_mfma_f32_16x16x32_bf16 v[76:79], v[172:175], a[32:35], v[76:79]\n"
      "v_mfma_f32_16x16x32_bf16 v[80:83], v[172:175], a[104:107], v[80:83]\n"
      "v_mfma_f32_16x16x32_bf16 v[84:87], v[172:175], a[176:179], v[84:87]\n"
      "v_mfma_f32_16x16x32_bf16 v[88:91], v[172:175], a[248:251], v[88:91]\n"
      "s_add_u32 m0, s38, 98304\n"
      "s_nop 0\n"
      "buffer_load_dwordx4 v6, s[12:15], s41 offen offset:0 lds\n"
      "buffer_load_dwordx4 v7, s[12:15], s41 offen offset:1024 lds\n"
      "buffer_load_dwordx4 v8, s[12:15], s41 offen offset:2048 lds\n"
      "buffer_load_dwordx4 v9, s[12:15], s41 offen offset:3072 lds\n"
      "s_add_u32 s41, s41, 294912\n"
      "s_waitcnt vmcnt(16) lgkmcnt(0)\n"
      "s_barrier\n"
      "ds_read_b64_tr_b16 a[40:41], v19 offset:16384\n"
      "ds_read_b64_tr_b16 a[42:43], v19 offset:17408\n"
      "ds_read_b64_tr_b16 a[112:113], v19 offset:24576\n"
      "ds_read_b64_tr_b16 a[114:115], v19 offset:25600\n"
      "ds_read_b64_tr_b16 a[184:185], v19 offset:16392\n"
      "ds_read_b64_tr_b16 a[186:187], v19 offset:17416\n"
      "ds_read_b64_tr_b16 v[20:21], v19 offset:24584\n"
      "ds_read_b64_tr_b16 v[22:23], v19 offset:25608\n"
      "v_mfma_f32_16x16x32_bf16 v[76:79], v[172:175], a[36:39], v[76:79]\n"
      "v_mfma_f32_16x16x32_bf16 v[80:83], v[172:175], a[108:111], v[80:83]\n"
      "v_mfma_f32_16x16x32_bf16 v[84:87], v[172:175], a[180:183], v[84:87]\n"
      "v_mfma_f32_16x16x32_bf16 v[88:91], v[172:175], a[252:255], v[88:91]\n"
      "s_add_u32 m0, s38, 114688\n"
      "s_nop 0\n"
      "buffer_load_dwordx4 v6, s[12:15], s41 offen offset:0 lds\n"
      "buffer_load_dwordx4 v7, s[12:15], s41 offen offset:1024 lds\n"
      "buffer_load_dwordx4 v8, s[12:15], s41 offen offset:2048 lds\n"
      "buffer_load_dwordx4 v9, s[12:15], s41 offen offset:3072 lds\n"
      "s_add_u32 s41, s41, 294912\n"
      "s_waitcnt vmcnt(16) lgkmcnt(0)\n"
      "s_barrier\n"
      "ds_read_b64_tr_b16 a[44:45], v19 offset:32768\n"
      "ds_read_b64_tr_b16 a[46:47], v19 offset:33792\n"
      "ds_read_b64_tr_b16 a[116:117], v19 offset:40960\n"
      "ds_read_b64_tr_b16 a[118:119], v19 offset:41984\n"
      "ds_read_b64_tr_b16 a[188:189], v19 offset:32776\n"
      "ds_read_b64_tr_b16 a[190:191], v19 offset:33800\n"
      "ds_read_b64_tr_b16 v[24:25], v19 offset:40968\n"
      "ds_read_b64_tr_b16 v[26:27], v19 offset:41992\n"
      "v_mfma_f32_16x16x32_bf16 v[92:95], v[172:175], a[40:43], v[76:79]\n"
      "v_mfma_f32_16x16x32_bf16 v[96:99], v[172:175], a[112:115], v[80:83]\n"
      "v_mfma_f32_16x16x32_bf16 v[100:103], v[172:175], a[184:187], v[84:87]\n"
      "v_mfma_f32_16x16x32_bf16 v[104:107], v[172:175], v[20:23], v[88:91]\n"
      "s_add_u32 m0, s38, 131072\n"
      "s_nop 0\n"
      "buffer_load_dwordx4 v6, s[12:15], s41 offen offset:0 lds\n"
      "buffer_load_dwordx4 v7, s[12:15], s41 offen offset:1024 lds\n"
      "buffer_load_dwordx4 v8, s[12:15], s41 offen offset:2048 lds\n"
      "buffer_load_dwordx4 v9, s[12:15], s41 offen offset:3072 lds\n"
      "s_add_u32 s41, s41, 294912\n"
      "s_waitcnt vmcnt(16) lgkmcnt(0)\n"
      "s_barrier\n"
      "ds_read_b64_tr_b16 a[48:49], v5 offset:0\n"
      "ds_read_b64_tr_b16 a[50:51], v5 offset:1024\n"
      "ds_read_b64_tr_b16 a[120:121], v5 offset:8192\n"
      "ds_read_b64_tr_b16 a[122:123], v5 offset:9216\n"
      "ds_read_b64_tr_b16 a[192:193], v5 offset:8\n"
      "ds_read_b64_tr_b16 a[194:195], v5 offset:1032\n"
      "ds_read_b64_tr_b16 v[28:29], v5 offset:8200\n"
      "ds_read_b64_tr_b16 v[30:31], v5 offset:9224\n"
      "v_mfma_f32_16x16x32_bf16 v[92:95], v[172:175], a[44:47], v[92:95]\n"
      "v_mfma_f32_16x16x32_bf16 v[96:99], v[172:175], a[116:119], v[96:99]\n"
      "v_mfma_f32_16x16x32_bf16 v[100:103], v[172:175], a[188:191], v[100:103]\n"
      "v_mfma_f32_16x16x32_bf16 v[104:107], v[172:175], v[24:27], v[104:107]\n"
      "s_add_u32 m0, s38, 147456\n"
      "s_nop 0\n"
      "buffer_load_dwordx4 v6, s[12:15], s41 offen offset:0 lds\n"
      "buffer_load_dwordx4 v7, s[12:15], s41 offen offset:1024 lds\n"
      "buffer_load_dwordx4 v8, s[12:15], s41 offen offset:2048 lds\n"
      "buffer_load_dwordx4 v9, s[12:15], s41 offen offset:3072 lds\n"
      "s_add_u32 s41, s41, 294912\n"
      "s_waitcnt vmcnt(16) lgkmcnt(0)\n"
      "s_barrier\n"
      "ds_read_b64_tr_b16 a[52:53], v5 offset:16384\n"
      "ds_read_b64_tr_b16 a[54:55], v5 offset:17408\n"
      "ds_read_b64_tr_b16 a[124:125], v5 offset:24576\n"
      "ds_read_b64_tr_b16 a[126:127], v5 offset:25600\n"
      "ds_read_b64_tr_b16 a[196:197], v5 offset:16392\n"
      "ds_read_b64_tr_b16 a[198:199], v5 offset:17416\n"
      "ds_read_b64_tr_b16 v[32:33], v5 offset:24584\n"
      "ds_read_b64_tr_b16 v[34:35], v5 offset:25608\n"
      "v_mfma_f32_16x16x32_bf16 v[92:95], v[172:175], a[48:51], v[92:95]\n"
      "v_mfma_f32_16x16x32_bf16 v[96:99], v[172:175], a[120:123], v[96:99]\n"
      "v_mfma_f32_16x16x32_bf16 v[100:103], v[172:175], a[192:195], v[100:103]\n"
      "v_mfma_f32_16x16x32_bf16 v[104:107], v[172:175], v[28:31], v[104:107]\n"
      "s_waitcnt vmcnt(12) lgkmcnt(0)\n"
      "s_barrier\n"
      "ds_read_b64_tr_b16 a[56:57], v5 offset:32768\n"
      "ds_read_b64_tr_b16 a[58:59], v5 offset:33792\n"
      "ds_read_b64_tr_b16 a[128:129], v5 offset:40960\n"
      "ds_read_b64_tr_b16 a[130:131], v5 offset:41984\n"
      "ds_read_b64_tr_b16 a[200:201], v5 offset:32776\n"
      "ds_read_b64_tr_b16 a[202:203], v5 offset:33800\n"
      "ds_read_b64_tr_b16 v[36:37], v5 offset:40968\n"
      "ds_read_b64_tr_b16 v[38:39], v5 offset:41992\n"
      "v_mfma_f32_16x16x32_bf16 v[92:95], v[172:175], a[52:55], v[92:95]\n"
      "v_mfma_f32_16x16x32_bf16 v[96:99], v[172:175], a[124:127], v[96:99]\n"
      "v_mfma_f32_16x16x32_bf16 v[100:103], v[172:175], a[196:199], v[100:103]\n"
      "v_mfma_f32_16x16x32_bf16 v[104:107], v[172:175], v[32:35], v[104:107]\n"
      "s_waitcnt vmcnt(8) lgkmcnt(0)\n"
      "s_barrier\n"
      "ds_read_b64_tr_b16 a[60:61], v19 offset:0\n"
      "ds_read_b64_tr_b16 a[62:63], v19 offset:1024\n"
      "ds_read_b64_tr_b16 a[132:133], v19 offset:8192\n"
      "ds_read_b64_tr_b16 a[134:135], v19 offset:9216\n"
      "ds_read_b64_tr_b16 a[204:205], v19 offset:8\n"
      "ds_read_b64_tr_b16 a[206:207], v19 offset:1032\n"
      "ds_read_b64_tr_b16 v[40:41], v19 offset:8200\n"
      "ds_read_b64_tr_b16 v[42:43], v19 offset:9224\n"
      "v_mfma_f32_16x16x32_bf16 v[92:95], v[172:175], a[56:59], v[92:95]\n"
      "v_mfma_f32_16x16x32_bf16 v[96:99], v[172:175], a[128:131], v[96:99]\n"
      "v_mfma_f32_16x16x32_bf16 v[100:103], v[172:175], a[200:203], v[100:103]\n"
      "v_mfma_f32_16x16x32_bf16 v[104:107], v[172:175], v[36:39], v[104:107]\n"
      "s_waitcnt vmcnt(4) lgkmcnt(0)\n"
      "s_barrier\n"
      "ds_read_b64_tr_b16 a[64:65], v19 offset:16384\n"
      "ds_read_b64_tr_b16 a[66:67], v19 offset:17408\n"
      "ds_read_b64_tr_b16 a[136:137], v19 offset:24576\n"
      "ds_read_b64_tr_b16 a[138:139], v19 offset:25600\n"
      "ds_read_b64_tr_b16 a[208:209], v19 offset:16392\n"
      "ds_read_b64_tr_b16 a[210:211], v19 offset:17416\n"
      "ds_read_b64_tr_b16 v[44:45], v19 offset:24584\n"
      "ds_read_b64_tr_b16 v[46:47], v19 offset:25608\n"
      "v_mfma_f32_16x16x32_bf16 v[108:111], v[172:175], a[60:63], v[92:95]\n"
      "v_mfma_f32_16x16x32_bf16 v[112:115], v[172:175], a[132:135], v[96:99]\n"
      "v_mfma_f32_16x16x32_bf16 v[116:119], v[172:175], a[204:207], v[100:103]\n"
      "v_mfma_f32_16x16x32_bf16 v[120:123], v[172:175], v[40:43], v[104:107]\n"
      "s_waitcnt vmcnt(0) lgkmcnt(0)\n"
      "s_barrier\n"
      "ds_read_b64_tr_b16 a[68:69], v19 offset:32768\n"
      "ds_read_b64_tr_b16 a[70:71], v19 offset:33792\n"
      "ds_read_b64_tr_b16 a[140:141], v19 offset:40960\n"
      "ds_read_b64_tr_b16 a[142:143], v19 offset:41984\n"
      "ds_read_b64_tr_b16 a[212:213], v19 offset:32776\n"
      "ds_read_b64_tr_b16 a[214:215], v19 offset:33800\n"
      "ds_read_b64_tr_b16 v[48:49], v19 offset:40968\n"
      "ds_read_b64_tr_b16 v[50:51], v19 offset:41992\n"
      "v_mfma_f32_16x16x32_bf16 v[108:111], v[172:175], a[64:67], v[108:111]\n"
      "v_mfma_f32_16x16x32_bf16 v[112:115], v[172:175], a[136:139], v[112:115]\n"
      "v_mfma_f32_16x16x32_bf16 v[116:119], v[172:175], a[208:211], v[116:119]\n"
      "v_mfma_f32_16x16x32_bf16 v[120:123], v[172:175], v[44:47], v[120:123]\n"
      "s_waitcnt lgkmcnt(0)\n"
      "v_mfma_f32_16x16x32_bf16 v[140:143], v[172:175], a[68:71], v[108:111]\n"
      "v_mfma_f32_16x16x32_bf16 v[144:147], v[172:175], a[140:143], v[112:115]\n"
      "v_mfma_f32_16x16x32_bf16 v[148:151], v[172:175], a[212:215], v[116:119]\n"
      "v_mfma_f32_16x16x32_bf16 v[152:155], v[172:175], v[48:51], v[120:123]\n"
      "s_getpc_b64 vcc\n"
      ".Lg_ct:\n"
      "s_add_u32 vcc_lo, vcc_lo, .Lg_p0-.Lg_ct\n"
      "s_addc_u32 vcc_hi, vcc_hi, 0\n"
      "s_lshl_b32 s30, s38, 1\n"
      "v_lshl_add_u32 v60, v0, 3, s30\n"
      "v_min_u32 v60, .Lg_last-.Lg_p0-4, v60\n"
      "global_load_dword v60, v60, vcc\n"
      "s_mul_i32 s21, s21, 1664525\n"
      "s_add_u32 s21, s21, 1013904223\n"
      "v_mov_b32 v15, s21\n"
      "buffer_store_dword v15, v12, s[8:11], 0 offen sc1\n"
      "v_not_b32 v52, v15\n"
      "v_not_b32 v53, v15\n"
      "v_not_b32 v54, v15\n"
      "v_not_b32 v55, v15\n"
      "s_add_u32 s30, s37, 0\n"
      "buffer_store_dwordx4 v[52:55], v10, s[8:11], s30 offen sc1\n"
      "v_xor_b32 v15, v15, v16\n"
      "v_mov_b32 v66, 0\n"
      "v_mov_b32 v67, 0\n"
      "v_mov_b32 v68, 0\n"
      "v_mov_b32 v69, 0\n"
      "v_mov_b32 v70, v64\n"
      "v_mov_b32 v71, v65\n"
      "v_sub_f32 v156, v140, v76\n"
      "v_sub_f32 v157, v144, v80\n"
      "v_sub_f32 v158, v148, v84\n"
      "v_sub_f32 v159, v152, v88\n"
      "v_sub_f32 v160, v140, v92\n"
      "v_sub_f32 v161, v144, v96\n"
      "v_sub_f32 v162, v148, v100\n"
      "v_sub_f32 v163, v152, v104\n"
      "v_sub_f32 v164, v140, v108\n"
      "v_sub_f32 v165, v144, v112\n"
      "v_sub_f32 v166, v148, v116\n"
      "v_sub_f32 v167, v152, v120\n"
      "v_mul_f32 v140, 0xbae38e39, v140\n"
      "v_mul_f32 v141, 0xbae38e39, v141\n"
      "v_mul_f32 v142, 0xbae38e39, v142\n"
      "v_mul_f32 v143, 0xbae38e39, v143\n"
      "v_mul_f32 v144, 0xbae38e39, v144\n"
      "v_mul_f32 v145, 0xbae38e39, v145\n"
      "v_mul_f32 v146, 0xbae38e39, v146\n"
      "v_mul_f32 v147, 0xbae38e39, v147\n"
      "v_mul_f32 v148, 0xbae38e39, v148\n"
      "v_mul_f32 v149, 0xbae38e39, v149\n"
      "v_mul_f32 v150, 0xbae38e39, v150\n"
      "v_mul_f32 v151, 0xbae38e39, v151\n"
      "v_mul_f32 v152, 0xbae38e39, v152\n"
      "v_mul_f32 v153, 0xbae38e39, v153\n"
      "v_mul_f32 v154, 0xbae38e39, v154\n"
      "v_mul_f32 v155, 0xbae38e39, v155\n"
      "v_mfma_f32_16x16x32_bf16 v[72:75], v[64:67], a[0:3], v[140:143]\n"
      "v_mfma_f32_16x16x32_bf16 v[76:79], v[68:71], a[0:3], v[140:143]\n"
      "v_mfma_f32_16x16x32_bf16 v[80:83], v[64:67], a[72:75], v[144:147]\n"
      "v_mfma_f32_16x16x32_bf16 v[84:87], v[68:71], a[72:75], v[144:147]\n"
      "v_mfma_f32_16x16x32_bf16 v[88:91], v[64:67], a[144:147], v[148:151]\n"
      "s_nop 4\n"
      "v_cvt_pk_bf16_f32 v72, v72, v73\n"
      "v_cvt_pk_bf16_f32 v73, v74, v75\n"
      "v_cvt_pk_bf16_f32 v74, v76, v77\n"
      "v_mfma_f32_16x16x32_bf16 v[92:95], v[68:71], a[144:147], v[148:151]\n"
      "v_cvt_pk_bf16_f32 v75, v78, v79\n"
      "v_accvgpr_write_b32 a0, v72\n"
      "v_accvgpr_write_b32 a1, v73\n"
      "v_accvgpr_write_b32 a2, v74\n"
      "v_accvgpr_write_b32 a3, v75\n"
      "v_mfma_f32_16x16x32_bf16 v[96:99], v[64:67], a[216:219], v[152:155]\n"
      "v_cvt_pk_bf16_f32 v80, v80, v81\n"
      "v_cvt_pk_bf16_f32 v81, v82, v83\n"
      "v_cvt_pk_bf16_f32 v82, v84, v85\n"
      "v_mfma_f32_16x16x32_bf16 v[100:103], v[68:71], a[216:219], v[152:155]\n"
      "v_cvt_pk_bf16_f32 v83, v86, v87\n"
      "v_accvgpr_write_b32 a72, v80\n"
      "v_accvgpr_write_b32 a73, v81\n"
      "v_mfma_f32_16x16x32_bf16 v[124:127], v[172:175], a[0:3], 0\n"
      "v_accvgpr_write_b32 a74, v82\n"
      "v_accvgpr_write_b32 a75, v83\n"
      "v_mfma_f32_16x16x32_bf16 v[72:75], v[64:67], a[4:7], v[140:143]\n"
      "v_cvt_pk_bf16_f32 v88, v88, v89\n"
      "v_cvt_pk_bf16_f32 v89, v90, v91\n"
      "v_cvt_pk_bf16_f32 v90, v92, v93\n"
      "v_mfma_f32_16x16x32_bf16 v[76:79], v[68:71], a[4:7], v[140:143]\n"
      "v_cvt_pk_bf16_f32 v91, v94, v95\n"
      "v_accvgpr_write_b32 a144, v88\n"
      "v_accvgpr_write_b32 a145, v89\n"
      "v_mfma_f32_16x16x32_bf16 v[128:131], v[172:175], a[72:75], 0\n"
      "v_accvgpr_write_b32 a146, v90\n"
      "v_accvgpr_write_b32 a147, v91\n"
      "v_mfma_f32_16x16x32_bf16 v[80:83], v[64:67], a[76:79], v[144:147]\n"
      "v_cvt_pk_bf16_f32 v96, v96, v97\n"
      "v_cvt_pk_bf16_f32 v97, v98, v99\n"
      "v_cvt_pk_bf16_f32 v98, v100, v101\n"
      "v_mfma_f32_16x16x32_bf16 v[84:87], v[68:71], a[76:79], v[144:147]\n"
      "v_cvt_pk_bf16_f32 v99, v102, v103\n"
      "v_accvgpr_write_b32 a216, v96\n"
      "v_accvgpr_write_b32 a217, v97\n"
      "v_mfma_f32_16x16x32_bf16 v[132:135], v[172:175], a[144:147], 0\n"
      "v_accvgpr_write_b32 a218, v98\n"
      "v_accvgpr_write_b32 a219, v99\n"
      "v_mfma_f32_16x16x32_bf16 v[88:91], v[64:67], a[148:151], v[148:151]\n"
      "v_cvt_pk_bf16_f32 v72, v72, v73\n"
      "v_cvt_pk_bf16_f32 v73, v74, v75\n"
      "v_cvt_pk_bf16_f32 v74, v76, v77\n"
      "v_mfma_f32_16x16x32_bf16 v[92:95], v[68:71], a[148:151], v[148:151]\n"
      "v_cvt_pk_bf16_f32 v75, v78, v79\n"
      "v_accvgpr_write_b32 a4, v72\n"
      "v_accvgpr_write_b32 a5, v73\n"
      "v_mfma_f32_16x16x32_bf16 v[136:139], v[172:175], a[216:219], 0\n"
      "v_accvgpr_write_b32 a6, v74\n"
      "v_accvgpr_write_b32 a7, v75\n"
      "v_mfma_f32_16x16x32_bf16 v[96:99], v[64:67], a[220:223], v[152:155]\n"
      "v_cvt_pk_bf16_f32 v80, v80, v81\n"
      "v_cvt_pk_bf16_f32 v81, v82, v83\n"
      "v_cvt_pk_bf16_f32 v82, v84, v85\n"
      "v_mfma_f32_16x16x32_bf16 v[100:103], v[68:71], a[220:223], v[152:155]\n"
      "v_cvt_pk_bf16_f32 v83, v86, v87\n"
      "v_accvgpr_write_b32 a76, v80\n"
      "v_accvgpr_write_b32 a77, v81\n"
      "v_mfma_f32_16x16x32_bf16 v[124:127], v[172:175], a[4:7], v[124:127]\n"
      "v_accvgpr_write_b32 a78, v82\n"
      "v_accvgpr_write_b32 a79, v83\n"
      "v_mfma_f32_16x16x32_bf16 v[72:75], v[64:67], a[8:11], v[140:143]\n"
      "v_cvt_pk_bf16_f32 v88, v88, v89\n"
      "v_cvt_pk_bf16_f32 v89, v90, v91\n"
      "v_cvt_pk_bf16_f32 v90, v92, v93\n"
      "v_mfma_f32_16x16x32_bf16 v[76:79], v[68:71], a[8:11], v[140:143]\n"
      "v_cvt_pk_bf16_f32 v91, v94, v95\n"
      "v_accvgpr_write_b32 a148, v88\n"
      "v_accvgpr_write_b32 a149, v89\n"
      "v_mfma_f32_16x16x32_bf16 v[128:131], v[172:175], a[76:79], v[128:131]\n"
      "v_accvgpr_write_b32 a150, v90\n"
      "v_accvgpr_write_b32 a151, v91\n"
      "v_mfma_f32_16x16x32_bf16 v[80:83], v[64:67], a[80:83], v[144:147]\n"
      "v_cvt_pk_bf16_f32 v96, v96, v97\n"
      "v_cvt_pk_bf16_f32 v97, v98, v99\n"
      "v_cvt_pk_bf16_f32 v98, v100, v101\n"
      "v_mfma_f32_16x16x32_bf16 v[84:87], v[68:71], a[80:83], v[144:147]\n"
      "v_cvt_pk_bf16_f32 v99, v102, v103\n"
      "v_accvgpr_write_b32 a220, v96\n"
      "v_accvgpr_write_b32 a221, v97\n"
      "v_mfma_f32_16x16x32_bf16 v[132:135], v[172:175], a[148:151], v[132:135]\n"
      "v_accvgpr_write_b32 a222, v98\n"
      "v_accvgpr_write_b32 a223, v99\n"
      "v_mfma_f32_16x16x32_bf16 v[88:91], v[64:67], a[152:155], v[148:151]\n"
      "v_cvt_pk_bf16_f32 v72, v72, v73\n"
      "v_cvt_pk_bf16_f32 v73, v74, v75\n"
      "v_cvt_pk_bf16_f32 v74, v76, v77\n"
      "v_mfma_f32_16x16x32_bf16 v[92:95], v[68:71], a[152:155], v[148:151]\n"
      "v_cvt_pk_bf16_f32 v75, v78, v79\n"
      "v_accvgpr_write_b32 a8, v72\n"
      "v_accvgpr_write_b32 a9, v73\n"
      "v_mfma_f32_16x16x32_bf16 v[136:139], v[172:175], a[220:223], v[136:139]\n"
      "v_accvgpr_write_b32 a10, v74\n"
      "v_accvgpr_write_b32 a11, v75\n"
      "v_mfma_f32_16x16x32_bf16 v[96:99], v[64:67], a[224:227], v[152:155]\n"
      "v_cvt_pk_bf16_f32 v80, v80, v81\n"
      "v_cvt_pk_bf16_f32 v81, v82, v83\n"
      "v_cvt_pk_bf16_f32 v82, v84, v85\n"
      "v_mfma_f32_16x16x32_bf16 v[100:103], v[68:71], a[224:227], v[152:155]\n"
      "v_cvt_pk_bf16_f32 v83, v86, v87\n"
      "v_accvgpr_write_b32 a80, v80\n"
      "v_accvgpr_write_b32 a81, v81\n"
      "v_mfma_f32_16x16x32_bf16 v[124:127], v[172:175], a[8:11], v[124:127]\n"
      "v_accvgpr_write_b32 a82, v82\n"
      "v_accvgpr_write_b32 a83, v83\n"
      "v_mfma_f32_16x16x32_bf16 v[72:75], v[64:67], a[12:15], v[140:143]\n"
      "v_cvt_pk_bf16_f32 v88, v88, v89\n"
      "v_cvt_pk_bf16_f32 v89, v90, v91\n"
      "v_cvt_pk_bf16_f32 v90, v92, v93\n"
      "v_mfma_f32_16x16x32_bf16 v[76:79], v[68:71], a[12:15], v[140:143]\n"
      "v_cvt_pk_bf16_f32 v91, v94, v95\n"
      "v_accvgpr_write_b32 a152, v88\n"
      "v_accvgpr_write_b32 a153, v89\n"
      "v_mfma_f32_16x16x32_bf16 v[128:131], v[172:175], a[80:83], v[128:131]\n"
      "v_accvgpr_write_b32 a154, v90\n"
      "v_accvgpr_write_b32 a155, v91\n"
      "v_mfma_f32_16x16x32_bf16 v[80:83], v[64:67], a[84:87], v[144:147]\n"
      "v_cvt_pk_bf16_f32 v96, v96, v97\n"
      "v_cvt_pk_bf16_f32 v97, v98, v99\n"
      "v_cvt_pk_bf16_f32 v98, v100, v101\n"
      "v_mfma_f32_16x16x32_bf16 v[84:87], v[68:71], a[84:87], v[144:147]\n"
      "v_cvt_pk_bf16_f32 v99, v102, v103\n"
      "v_accvgpr_write_b32 a224, v96\n"
      "v_accvgpr_write_b32 a225, v97\n"
      "v_mfma_f32_16x16x32_bf16 v[132:135], v[172:175], a[152:155], v[132:135]\n"
      "v_accvgpr_write_b32 a226, v98\n"
      "v_accvgpr_write_b32 a227, v99\n"
      "v_mfma_f32_16x16x32_bf16 v[88:91], v[64:67], a[156:159], v[148:151]\n"
      "v_cvt_pk_bf16_f32 v72, v72, v73\n"
      "v_cvt_pk_bf16_f32 v73, v74, v75\n"
      "v_cvt_pk_bf16_f32 v74, v76, v77\n"
      "v_mfma_f32_16x16x32_bf16 v[92:95], v[68:71], a[156:159], v[148:151]\n"
      "v_cvt_pk_bf16_f32 v75, v78, v79\n"
      "v_accvgpr_write_b32 a12, v72\n"
      "v_accvgpr_write_b32 a13, v73\n"
      "v_mfma_f32_16x16x32_bf16 v[136:139], v[172:175], a[224:227], v[136:139]\n"
      "v_accvgpr_write_b32 a14, v74\n"
      "v_accvgpr_write_b32 a15, v75\n"
      "v_mfma_f32_16x16x32_bf16 v[96:99], v[64:67], a[228:231], v[152:155]\n"
      "v_cvt_pk_bf16_f32 v80, v80, v81\n"
      "v_cvt_pk_bf16_f32 v81, v82, v83\n"
      "v_cvt_pk_bf16_f32 v82, v84, v85\n"
      "v_mfma_f32_16x16x32_bf16 v[100:103], v[68:71], a[228:231], v[152:155]\n"
      "v_cvt_pk_bf16_f32 v83, v86, v87\n"
      "v_accvgpr_write_b32 a84, v80\n"
      "v_accvgpr_write_b32 a85, v81\n"
      "v_mfma_f32_16x16x32_bf16 v[124:127], v[172:175], a[12:15], v[124:127]\n"
      "v_accvgpr_write_b32 a86, v82\n"
      "v_accvgpr_write_b32 a87, v83\n"
      "v_mfma_f32_16x16x32_bf16 v[72:75], v[64:67], a[16:19], v[140:143]\n"
      "v_cvt_pk_bf16_f32 v88, v88, v89\n"
      "v_cvt_pk_bf16_f32 v89, v90, v91\n"
      "v_cvt_pk_bf16_f32 v90, v92, v93\n"
      "v_mfma_f32_16x16x32_bf16 v[76:79], v[68:71], a[16:19], v[140:143]\n"
      "v_cvt_pk_bf16_f32 v91, v94, v95\n"
      "v_accvgpr_write_b32 a156, v88\n"
      "v_accvgpr_write_b32 a157, v89\n"
      "v_mfma_f32_16x16x32_bf16 v[128:131], v[172:175], a[84:87], v[128:131]\n"
      "v_accvgpr_write_b32 a158, v90\n"
      "v_accvgpr_write_b32 a159, v91\n"
      "v_mfma_f32_16x16x32_bf16 v[80:83], v[64:67], a[88:91], v[144:147]\n"
      "v_cvt_pk_bf16_f32 v96, v96, v97\n"
      "v_cvt_pk_bf16_f32 v97, v98, v99\n"
      "v_cvt_pk_bf16_f32 v98, v100, v101\n"
      "v_mfma_f32_16x16x32_bf16 v[84:87], v[68:71], a[88:91], v[144:147]\n"
      "v_cvt_pk_bf16_f32 v99, v102, v103\n"
      "v_accvgpr_write_b32 a228, v96\n"
      "v_accvgpr_write_b32 a229, v97\n"
      "v_mfma_f32_16x16x32_bf16 v[132:135], v[172:175], a[156:159], v[132:135]\n"
      "v_accvgpr_write_b32 a230, v98\n"
      "v_accvgpr_write_b32 a231, v99\n"
      "v_mfma_f32_16x16x32_bf16 v[88:91], v[64:67], a[160:163], v[148:151]\n"
      "v_cvt_pk_bf16_f32 v72, v72, v73\n"
      "v_cvt_pk_bf16_f32 v73, v74, v75\n"
      "v_cvt_pk_bf16_f32 v74, v76, v77\n"
      "v_mfma_f32_16x16x32_bf16 v[92:95], v[68:71], a[160:163], v[148:151]\n"
      "v_cvt_pk_bf16_f32 v75, v78, v79\n"
      "v_accvgpr_write_b32 a16, v72\n"
      "v_accvgpr_write_b32 a17, v73\n"
      "v_mfma_f32_16x16x32_bf16 v[136:139], v[172:175], a[228:231], v[136:139]\n"
      "v_accvgpr_write_b32 a18, v74\n"
      "v_accvgpr_write_b32 a19, v75\n"
      "v_mfma_f32_16x16x32_bf16 v[96:99], v[64:67], a[232:235], v[152:155]\n"
      "v_cvt_pk_bf16_f32 v80, v80, v81\n"
      "v_cvt_pk_bf16_f32 v81, v82, v83\n"
      "v_cvt_pk_bf16_f32 v82, v84, v85\n"
      "v_mfma_f32_16x16x32_bf16 v[100:103], v[68:71], a[232:235], v[152:155]\n"
      "v_cvt_pk_bf16_f32 v83, v86, v87\n"
      "v_accvgpr_write_b32 a88, v80\n"
      "v_accvgpr_write_b32 a89, v81\n"
      "v_mfma_f32_16x16x32_bf16 v[124:127], v[172:175], a[16:19], v[124:127]\n"
      "v_accvgpr_write_b32 a90, v82\n"
      "v_accvgpr_write_b32 a91, v83\n"
      "v_mfma_f32_16x16x32_bf16 v[72:75], v[64:67], a[20:23], v[140:143]\n"
      "v_cvt_pk_bf16_f32 v88, v88, v89\n"
      "v_cvt_pk_bf16_f32 v89, v90, v91\n"
      "v_cvt_pk_bf16_f32 v90, v92, v93\n"
      "v_mfma_f32_16x16x32_bf16 v[76:79], v[68:71], a[20:23], v[140:143]\n"
      "v_cvt_pk_bf16_f32 v91, v94, v95\n"
      "v_accvgpr_write_b32 a160, v88\n"
      "v_accvgpr_write_b32 a161, v89\n"
      "v_mfma_f32_16x16x32_bf16 v[128:131], v[172:175], a[88:91], v[128:131]\n"
      "v_accvgpr_write_b32 a162, v90\n"
      "v_accvgpr_write_b32 a163, v91\n"
      "v_mfma_f32_16x16x32_bf16 v[80:83], v[64:67], a[92:95], v[144:147]\n"
      "v_cvt_pk_bf16_f32 v96, v96, v97\n"
      "v_cvt_pk_bf16_f32 v97, v98, v99\n"
      "v_cvt_pk_bf16_f32 v98, v100, v101\n"
      "v_mfma_f32_16x16x32_bf16 v[84:87], v[68:71], a[92:95], v[144:147]\n"
      "v_cvt_pk_bf16_f32 v99, v102, v103\n"
      "v_accvgpr_write_b32 a232, v96\n"
      "v_accvgpr_write_b32 a233, v97\n"
      "v_mfma_f32_16x16x32_bf16 v[132:135], v[172:175], a[160:163], v[132:135]\n"
      "v_accvgpr_write_b32 a234, v98\n"
      "v_accvgpr_write_b32 a235, v99\n"
      "v_mfma_f32_16x16x32_bf16 v[88:91], v[64:67], a[164:167], v[148:151]\n"
      "v_cvt_pk_bf16_f32 v72, v72, v73\n"
      "v_cvt_pk_bf16_f32 v73, v74, v75\n"
      "v_cvt_pk_bf16_f32 v74, v76, v77\n"
      "v_mfma_f32_16x16x32_bf16 v[92:95], v[68:71], a[164:167], v[148:151]\n"
      "v_cvt_pk_bf16_f32 v75, v78, v79\n"
      "v_accvgpr_write_b32 a20, v72\n"
      "v_accvgpr_write_b32 a21, v73\n"
      "v_mfma_f32_16x16x32_bf16 v[136:139], v[172:175], a[232:235], v[136:139]\n"
      "v_accvgpr_write_b32 a22, v74\n"
      "v_accvgpr_write_b32 a23, v75\n"
      "v_mfma_f32_16x16x32_bf16 v[96:99], v[64:67], a[236:239], v[152:155]\n"
      "v_cvt_pk_bf16_f32 v80, v80, v81\n"
      "v_cvt_pk_bf16_f32 v81, v82, v83\n"
      "v_cvt_pk_bf16_f32 v82, v84, v85\n"
      "v_mfma_f32_16x16x32_bf16 v[100:103], v[68:71], a[236:239], v[152:155]\n"
      "v_cvt_pk_bf16_f32 v83, v86, v87\n"
      "v_accvgpr_write_b32 a92, v80\n"
      "v_accvgpr_write_b32 a93, v81\n"
      "v_mfma_f32_16x16x32_bf16 v[124:127], v[172:175], a[20:23], v[124:127]\n"
      "v_accvgpr_write_b32 a94, v82\n"
      "v_accvgpr_write_b32 a95, v83\n"
      "v_mfma_f32_16x16x32_bf16 v[72:75], v[64:67], a[24:27], v[140:143]\n"
      "v_cvt_pk_bf16_f32 v88, v88, v89\n"
      "v_cvt_pk_bf16_f32 v89, v90, v91\n"
      "v_cvt_pk_bf16_f32 v90, v92, v93\n"
      "v_mfma_f32_16x16x32_bf16 v[76:79], v[68:71], a[24:27], v[140:143]\n"
      "v_cvt_pk_bf16_f32 v91, v94, v95\n"
      "v_accvgpr_write_b32 a164, v88\n"
      "v_accvgpr_write_b32 a165, v89\n"
      "v_mfma_f32_16x16x32_bf16 v[128:131], v[172:175], a[92:95], v[128:131]\n"
      "v_accvgpr_write_b32 a166, v90\n"
      "v_accvgpr_write_b32 a167, v91\n"
      "v_mfma_f32_16x16x32_bf16 v[80:83], v[64:67], a[96:99], v[144:147]\n"
      "v_cvt_pk_bf16_f32 v96, v96, v97\n"
      "v_cvt_pk_bf16_f32 v97, v98, v99\n"
      "v_cvt_pk_bf16_f32 v98, v100, v101\n"
      "v_mfma_f32_16x16x32_bf16 v[84:87], v[68:71], a[96:99], v[144:147]\n"
      "v_cvt_pk_bf16_f32 v99, v102, v103\n"
      "v_accvgpr_write_b32 a236, v96\n"
      "v_accvgpr_write_b32 a237, v97\n"
      "v_mfma_f32_16x16x32_bf16 v[132:135], v[172:175], a[164:167], v[132:135]\n"
      "v_accvgpr_write_b32 a238, v98\n"
      "v_accvgpr_write_b32 a239, v99\n"
      "v_mfma_f32_16x16x32_bf16 v[88:91], v[64:67], a[168:171], v[148:151]\n"
      "v_cvt_pk_bf16_f32 v72, v72, v73\n"
      "v_cvt_pk_bf16_f32 v73, v74, v75\n"
      "v_cvt_pk_bf16_f32 v74, v76, v77\n"
      "v_mfma_f32_16x16x32_bf16 v[92:95], v[68:71], a[168:171], v[148:151]\n"
      "v_cvt_pk_bf16_f32 v75, v78, v79\n"
      "v_accvgpr_write_b32 a24, v72\n"
      "v_accvgpr_write_b32 a25, v73\n"
      "v_mfma_f32_16x16x32_bf16 v[136:139], v[172:175], a[236:239], v[136:139]\n"
      "v_accvgpr_write_b32 a26, v74\n"
      "v_accvgpr_write_b32 a27, v75\n"
      "v_mfma_f32_16x16x32_bf16 v[96:99], v[64:67], a[240:243], v[152:155]\n"
      "v_cvt_pk_bf16_f32 v80, v80, v81\n"
      "v_cvt_pk_bf16_f32 v81, v82, v83\n"
      "v_cvt_pk_bf16_f32 v82, v84, v85\n"
      "v_mfma_f32_16x16x32_bf16 v[100:103], v[68:71], a[240:243], v[152:155]\n"
      "v_cvt_pk_bf16_f32 v83, v86, v87\n"
      "v_accvgpr_write_b32 a96, v80\n"
      "v_accvgpr_write_b32 a97, v81\n"
      "v_mfma_f32_16x16x32_bf16 v[124:127], v[172:175], a[24:27], v[124:127]\n"
      "v_accvgpr_write_b32 a98, v82\n"
      "v_accvgpr_write_b32 a99, v83\n"
      "v_mfma_f32_16x16x32_bf16 v[72:75], v[64:67], a[28:31], v[140:143]\n"
      "v_cvt_pk_bf16_f32 v88, v88, v89\n"
      "v_cvt_pk_bf16_f32 v89, v90, v91\n"
      "v_cvt_pk_bf16_f32 v90, v92, v93\n"
      "v_mfma_f32_16x16x32_bf16 v[76:79], v[68:71], a[28:31], v[140:143]\n"
      "v_cvt_pk_bf16_f32 v91, v94, v95\n"
      "v_accvgpr_write_b32 a168, v88\n"
      "v_accvgpr_write_b32 a169, v89\n"
      "v_mfma_f32_16x16x32_bf16 v[128:131], v[172:175], a[96:99], v[128:131]\n"
      "v_accvgpr_write_b32 a170, v90\n"
      "v_accvgpr_write_b32 a171, v91\n"
      "v_mfma_f32_16x16x32_bf16 v[80:83], v[64:67], a[100:103], v[144:147]\n"
      "v_cvt_pk_bf16_f32 v96, v96, v97\n"
      "v_cvt_pk_bf16_f32 v97, v98, v99\n"
      "v_cvt_pk_bf16_f32 v98, v100, v101\n"
      "v_mfma_f32_16x16x32_bf16 v[84:87], v[68:71], a[100:103], v[144:147]\n"
      "v_cvt_pk_bf16_f32 v99, v102, v103\n"
      "v_accvgpr_write_b32 a240, v96\n"
      "v_accvgpr_write_b32 a241, v97\n"
      "v_mfma_f32_16x16x32_bf16 v[132:135], v[172:175], a[168:171], v[132:135]\n"
      "v_accvgpr_write_b32 a242, v98\n"
      "v_accvgpr_write_b32 a243, v99\n"
      "v_mfma_f32_16x16x32_bf16 v[88:91], v[64:67], a[172:175], v[148:151]\n"
      "v_cvt_pk_bf16_f32 v72, v72, v73\n"
      "v_cvt_pk_bf16_f32 v73, v74, v75\n"
      "v_cvt_pk_bf16_f32 v74, v76, v77\n"
      "v_mfma_f32_16x16x32_bf16 v[92:95], v[68:71], a[172:175], v[148:151]\n"
      "v_cvt_pk_bf16_f32 v75, v78, v79\n"
      "v_accvgpr_write_b32 a28, v72\n"
      "v_accvgpr_write_b32 a29, v73\n"
      "v_mfma_f32_16x16x32_bf16 v[136:139], v[172:175], a[240:243], v[136:139]\n"
      "v_accvgpr_write_b32 a30, v74\n"
      "v_accvgpr_write_b32 a31, v75\n"
      "v_mfma_f32_16x16x32_bf16 v[96:99], v[64:67], a[244:247], v[152:155]\n"
      "v_cvt_pk_bf16_f32 v80, v80, v81\n"
      "v_cvt_pk_bf16_f32 v81, v82, v83\n"
      "v_cvt_pk_bf16_f32 v82, v84, v85\n"
      "v_mfma_f32_16x16x32_bf16 v[100:103], v[68:71], a[244:247], v[152:155]\n"
      "v_cvt_pk_bf16_f32 v83, v86, v87\n"
      "v_accvgpr_write_b32 a100, v80\n"
      "v_accvgpr_write_b32 a101, v81\n"
      "v_mfma_f32_16x16x32_bf16 v[124:127], v[172:175], a[28:31], v[124:127]\n"
      "v_accvgpr_write_b32 a102, v82\n"
      "v_accvgpr_write_b32 a103, v83\n"
      "v_mfma_f32_16x16x32_bf16 v[72:75], v[64:67], a[32:35], v[140:143]\n"
      "v_cvt_pk_bf16_f32 v88, v88, v89\n"
      "v_cvt_pk_bf16_f32 v89, v90, v91\n"
      "v_cvt_pk_bf16_f32 v90, v92, v93\n"
      "v_mfma_f32_16x16x32_bf16 v[76:79], v[68:71], a[32:35], v[140:143]\n"
      "v_cvt_pk_bf16_f32 v91, v94, v95\n"
      "v_accvgpr_write_b32 a172, v88\n"
      "v_accvgpr_write_b32 a173, v89\n"
      "v_mfma_f32_16x16x32_bf16 v[128:131], v[172:175], a[100:103], v[128:131]\n"
      "v_accvgpr_write_b32 a174, v90\n"
      "v_accvgpr_write_b32 a175, v91\n"
      "v_mfma_f32_16x16x32_bf16 v[80:83], v[64:67], a[104:107], v[144:147]\n"
      "v_cvt_pk_bf16_f32 v96, v96, v97\n"
      "v_cvt_pk_bf16_f32 v97, v98, v99\n"
      "v_cvt_pk_bf16_f32 v98, v100, v101\n"
      "v_mfma_f32_16x16x32_bf16 v[84:87], v[68:71], a[104:107], v[144:147]\n"
      "v_cvt_pk_bf16_f32 v99, v102, v103\n"
      "v_accvgpr_write_b32 a244, v96\n"
      "v_accvgpr_write_b32 a245, v97\n"
      "v_mfma_f32_16x16x32_bf16 v[132:135], v[172:175], a[172:175], v[132:135]\n"
      "v_accvgpr_write_b32 a246, v98\n"
      "v_accvgpr_write_b32 a247, v99\n"
      "v_mfma_f32_16x16x32_bf16 v[88:91], v[64:67], a[176:179], v[148:151]\n"
      "v_cvt_pk_bf16_f32 v72, v72, v73\n"
      "v_cvt_pk_bf16_f32 v73, v74, v75\n"
      "v_cvt_pk_bf16_f32 v74, v76, v77\n"
      "v_mfma_f32_16x16x32_bf16 v[92:95], v[68:71], a[176:179], v[148:151]\n"
      "v_cvt_pk_bf16_f32 v75, v78, v79\n"
      "v_accvgpr_write_b32 a32, v72\n"
      "v_accvgpr_write_b32 a33, v73\n"
      "v_mfma_f32_16x16x32_bf16 v[136:139], v[172:175], a[244:247], v[136:139]\n"
      "v_accvgpr_write_b32 a34, v74\n"
      "v_accvgpr_write_b32 a35, v75\n"
      "v_mfma_f32_16x16x32_bf16 v[96:99], v[64:67], a[248:251], v[152:155]\n"
      "v_cvt_pk_bf16_f32 v80, v80, v81\n"
      "v_cvt_pk_bf16_f32 v81, v82, v83\n"
      "v_cvt_pk_bf16_f32 v82, v84, v85\n"
      "v_mfma_f32_16x16x32_bf16 v[100:103], v[68:71], a[248:251], v[152:155]\n"
      "v_cvt_pk_bf16_f32 v83, v86, v87\n"
      "v_accvgpr_write_b32 a104, v80\n"
      "v_accvgpr_write_b32 a105, v81\n"
      "v_mfma_f32_16x16x32_bf16 v[124:127], v[172:175], a[32:35], v[124:127]\n"
      "v_accvgpr_write_b32 a106, v82\n"
      "v_accvgpr_write_b32 a107, v83\n"
      "v_mfma_f32_16x16x32_bf16 v[72:75], v[64:67], a[36:39], v[140:143]\n"
      "v_cvt_pk_bf16_f32 v88, v88, v89\n"
      "v_cvt_pk_bf16_f32 v89, v90, v91\n"
      "v_cvt_pk_bf16_f32 v90, v92, v93\n"
      "v_mfma_f32_16x16x32_bf16 v[76:79], v[68:71], a[36:39], v[140:143]\n"
      "v_cvt_pk_bf16_f32 v91, v94, v95\n"
      "v_accvgpr_write_b32 a176, v88\n"
      "v_accvgpr_write_b32 a177, v89\n"
      "v_mfma_f32_16x16x32_bf16 v[128:131], v[172:175], a[104:107], v[128:131]\n"
      "v_accvgpr_write_b32 a178, v90\n"
      "v_accvgpr_write_b32 a179, v91\n"
      "v_mfma_f32_16x16x32_bf16 v[80:83], v[64:67], a[108:111], v[144:147]\n"
      "v_cvt_pk_bf16_f32 v96, v96, v97\n"
      "v_cvt_pk_bf16_f32 v97, v98, v99\n"
      "v_cvt_pk_bf16_f32 v98, v100, v101\n"
      "v_mfma_f32_16x16x32_bf16 v[84:87], v[68:71], a[108:111], v[144:147]\n"
      "v_cvt_pk_bf16_f32 v99, v102, v103\n"
      "v_accvgpr_write_b32 a248, v96\n"
      "v_accvgpr_write_b32 a249, v97\n"
      "v_mfma_f32_16x16x32_bf16 v[132:135], v[172:175], a[176:179], v[132:135]\n"
      "v_accvgpr_write_b32 a250, v98\n"
      "v_accvgpr_write_b32 a251, v99\n"
      "v_mfma_f32_16x16x32_bf16 v[88:91], v[64:67], a[180:183], v[148:151]\n"
      "v_cvt_pk_bf16_f32 v72, v72, v73\n"
      "v_cvt_pk_bf16_f32 v73, v74, v75\n"
      "v_cvt_pk_bf16_f32 v74, v76, v77\n"
      "v_mfma_f32_16x16x32_bf16 v[92:95], v[68:71], a[180:183], v[148:151]\n"
      "v_cvt_pk_bf16_f32 v75, v78, v79\n"
      "v_accvgpr_write_b32 a36, v72\n"
      "v_accvgpr_write_b32 a37, v73\n"
      "v_mfma_f32_16x16x32_bf16 v[136:139], v[172:175], a[248:251], v[136:139]\n"
      "v_accvgpr_write_b32 a38, v74\n"
      "v_accvgpr_write_b32 a39, v75\n"
      "v_mfma_f32_16x16x32_bf16 v[96:99], v[64:67], a[252:255], v[152:155]\n"
      "v_cvt_pk_bf16_f32 v80, v80, v81\n"
      "v_cvt_pk_bf16_f32 v81, v82, v83\n"
      "v_cvt_pk_bf16_f32 v82, v84, v85\n"
      "v_mfma_f32_16x16x32_bf16 v[100:103], v[68:71], a[252:255], v[152:155]\n"
      "v_cvt_pk_bf16_f32 v83, v86, v87\n"
      "v_accvgpr_write_b32 a108, v80\n"
      "v_accvgpr_write_b32 a109, v81\n"
      "v_mfma_f32_16x16x32_bf16 v[124:127], v[172:175], a[36:39], v[124:127]\n"
      "v_accvgpr_write_b32 a110, v82\n"
      "v_accvgpr_write_b32 a111, v83\n"
      "s_nop 1\n"
      "v_mfma_f32_16x16x32_bf16 v[128:131], v[172:175], a[108:111], v[128:131]\n"
      "v_cvt_pk_bf16_f32 v88, v88, v89\n"
      "v_cvt_pk_bf16_f32 v89, v90, v91\n"
      "v_cvt_pk_bf16_f32 v90, v92, v93\n"
      "v_cvt_pk_bf16_f32 v91, v94, v95\n"
      "v_accvgpr_write_b32 a180, v88\n"
      "v_accvgpr_write_b32 a181, v89\n"
      "v_accvgpr_write_b32 a182, v90\n"
      "v_accvgpr_write_b32 a183, v91\n"
      "s_nop 1\n"
      "v_mfma_f32_16x16x32_bf16 v[132:135], v[172:175], a[180:183], v[132:135]\n"
      "v_cvt_pk_bf16_f32 v96, v96, v97\n"
      "v_cvt_pk_bf16_f32 v97, v98, v99\n"
      "v_cvt_pk_bf16_f32 v98, v100, v101\n"
      "v_cvt_pk_bf16_f32 v99, v102, v103\n"
      "v_accvgpr_write_b32 a252, v96\n"
      "v_accvgpr_write_b32 a253, v97\n"
      "v_accvgpr_write_b32 a254, v98\n"
      "v_accvgpr_write_b32 a255, v99\n"
      "s_nop 1\n"
      "v_mfma_f32_16x16x32_bf16 v[136:139], v[172:175], a[252:255], v[136:139]\n"
      "v_add_f32 v168, v124, v156\n"
      "v_add_f32 v169, v128, v157\n"
      "v_add_f32 v170, v132, v158\n"
      "s_nop 5\n"
      "v_add_f32 v171, v136, v159\n"
      "v_mul_f32 v140, 0xbb800000, v168\n"
      "v_mul_f32 v141, 0xbb800000, v168\n"
      "v_mul_f32 v142, 0xbb800000, v168\n"
      "v_mul_f32 v143, 0xbb800000, v168\n"
      "v_mul_f32 v144, 0xbb800000, v169\n"
      "v_mul_f32 v145, 0xbb800000, v169\n"
      "v_mul_f32 v146, 0xbb800000, v169\n"
      "v_mul_f32 v147, 0xbb800000, v169\n"
      "v_mul_f32 v148, 0xbb800000, v170\n"
      "v_mul_f32 v149, 0xbb800000, v170\n"
      "v_mul_f32 v150, 0xbb800000, v170\n"
      "v_mul_f32 v151, 0xbb800000, v170\n"
      "v_mul_f32 v152, 0xbb800000, v171\n"
      "v_mul_f32 v153, 0xbb800000, v171\n"
      "v_mul_f32 v154, 0xbb800000, v171\n"
      "v_mul_f32 v155, 0xbb800000, v171\n"
      "v_mfma_f32_16x16x32_bf16 v[72:75], v[64:67], a[40:43], v[140:143]\n"
      "v_mfma_f32_16x16x32_bf16 v[76:79], v[68:71], a[40:43], v[140:143]\n"
      "v_mfma_f32_16x16x32_bf16 v[80:83], v[64:67], a[112:115], v[144:147]\n"
      "v_mfma_f32_16x16x32_bf16 v[84:87], v[68:71], a[112:115], v[144:147]\n"
      "v_mfma_f32_16x16x32_bf16 v[88:91], v[64:67], a[184:187], v[148:151]\n"
      "s_nop 4\n"
      "v_cvt_pk_bf16_f32 v72, v72, v73\n"
      "v_cvt_pk_bf16_f32 v73, v74, v75\n"
      "v_cvt_pk_bf16_f32 v74, v76, v77\n"
      "v_mfma_f32_16x16x32_bf16 v[92:95], v[68:71], a[184:187], v[148:151]\n"
      "v_cvt_pk_bf16_f32 v75, v78, v79\n"
      "v_accvgpr_write_b32 a40, v72\n"
      "v_accvgpr_write_b32 a41, v73\n"
      "v_accvgpr_write_b32 a42, v74\n"
      "v_accvgpr_write_b32 a43, v75\n"
      "v_mfma_f32_16x16x32_bf16 v[96:99], v[64:67], v[20:23], v[152:155]\n"
      "v_cvt_pk_bf16_f32 v80, v80, v81\n"
      "v_cvt_pk_bf16_f32 v81, v82, v83\n"
      "v_cvt_pk_bf16_f32 v82, v84, v85\n"
      "v_mfma_f32_16x16x32_bf16 v[100:103], v[68:71], v[20:23], v[152:155]\n"
      "v_cvt_pk_bf16_f32 v83, v86, v87\n"
      "v_accvgpr_write_b32 a112, v80\n"
      "v_accvgpr_write_b32 a113, v81\n"
      "v_mfma_f32_16x16x32_bf16 v[124:127], v[172:175], a[40:43], v[124:127]\n"
      "v_accvgpr_write_b32 a114, v82\n"
      "v_accvgpr_write_b32 a115, v83\n"
      "v_mfma_f32_16x16x32_bf16 v[72:75], v[64:67], a[44:47], v[140:143]\n"
      "v_cvt_pk_bf16_f32 v88, v88, v89\n"
      "v_cvt_pk_bf16_f32 v89, v90, v91\n"
      "v_cvt_pk_bf16_f32 v90, v92, v93\n"
      "v_mfma_f32_16x16x32_bf16 v[76:79], v[68:71], a[44:47], v[140:143]\n"
      "v_cvt_pk_bf16_f32 v91, v94, v95\n"
      "v_accvgpr_write_b32 a184, v88\n"
      "v_accvgpr_write_b32 a185, v89\n"
      "v_mfma_f32_16x16x32_bf16 v[128:131], v[172:175], a[112:115], v[128:131]\n"
      "v_accvgpr_write_b32 a186, v90\n"
      "v_accvgpr_write_b32 a187, v91\n"
      "v_mfma_f32_16x16x32_bf16 v[80:83], v[64:67], a[116:119], v[144:147]\n"
      "v_cvt_pk_bf16_f32 v20, v96, v97\n"
      "v_cvt_pk_bf16_f32 v21, v98, v99\n"
      "v_cvt_pk_bf16_f32 v22, v100, v101\n"
      "v_mfma_f32_16x16x32_bf16 v[84:87], v[68:71], a[116:119], v[144:147]\n"
      "v_cvt_pk_bf16_f32 v23, v102, v103\n"
      "v_mfma_f32_16x16x32_bf16 v[132:135], v[172:175], a[184:187], v[132:135]\n"
      "v_mfma_f32_16x16x32_bf16 v[88:91], v[64:67], a[188:191], v[148:151]\n"
      "v_cvt_pk_bf16_f32 v72, v72, v73\n"
      "v_cvt_pk_bf16_f32 v73, v74, v75\n"
      "v_cvt_pk_bf16_f32 v74, v76, v77\n"
      "v_mfma_f32_16x16x32_bf16 v[92:95], v[68:71], a[188:191], v[148:151]\n"
      "v_cvt_pk_bf16_f32 v75, v78, v79\n"
      "v_accvgpr_write_b32 a44, v72\n"
      "v_accvgpr_write_b32 a45, v73\n"
      "v_mfma_f32_16x16x32_bf16 v[136:139], v[172:175], v[20:23], v[136:139]\n"
      "v_accvgpr_write_b32 a46, v74\n"
      "v_accvgpr_write_b32 a47, v75\n"
      "v_mfma_f32_16x16x32_bf16 v[96:99], v[64:67], v[24:27], v[152:155]\n"
      "v_cvt_pk_bf16_f32 v80, v80, v81\n"
      "v_cvt_pk_bf16_f32 v81, v82, v83\n"
      "v_cvt_pk_bf16_f32 v82, v84, v85\n"
      "v_mfma_f32_16x16x32_bf16 v[100:103], v[68:71], v[24:27], v[152:155]\n"
      "v_cvt_pk_bf16_f32 v83, v86, v87\n"
      "v_accvgpr_write_b32 a116, v80\n"
      "v_accvgpr_write_b32 a117, v81\n"
      "v_mfma_f32_16x16x32_bf16 v[124:127], v[172:175], a[44:47], v[124:127]\n"
      "v_accvgpr_write_b32 a118, v82\n"
      "v_accvgpr_write_b32 a119, v83\n"
      "v_mfma_f32_16x16x32_bf16 v[72:75], v[64:67], a[48:51], v[140:143]\n"
      "v_cvt_pk_bf16_f32 v88, v88, v89\n"
      "v_cvt_pk_bf16_f32 v89, v90, v91\n"
      "v_cvt_pk_bf16_f32 v90, v92, v93\n"
      "v_mfma_f32_16x16x32_bf16 v[76:79], v[68:71], a[48:51], v[140:143]\n"
      "v_cvt_pk_bf16_f32 v91, v94, v95\n"
      "v_accvgpr_write_b32 a188, v88\n"
      "v_accvgpr_write_b32 a189, v89\n"
      "v_mfma_f32_16x16x32_bf16 v[128:131], v[172:175], a[116:119], v[128:131]\n"
      "v_accvgpr_write_b32 a190, v90\n"
      "v_accvgpr_write_b32 a191, v91\n"
      "v_mfma_f32_16x16x32_bf16 v[80:83], v[64:67], a[120:123], v[144:147]\n"
      "v_cvt_pk_bf16_f32 v24, v96, v97\n"
      "v_cvt_pk_bf16_f32 v25, v98, v99\n"
      "v_cvt_pk_bf16_f32 v26, v100, v101\n"
      "v_mfma_f32_16x16x32_bf16 v[84:87], v[68:71], a[120:123], v[144:147]\n"
      "v_cvt_pk_bf16_f32 v27, v102, v103\n"
      "v_mfma_f32_16x16x32_bf16 v[132:135], v[172:175], a[188:191], v[132:135]\n"
      "v_mfma_f32_16x16x32_bf16 v[88:91], v[64:67], a[192:195], v[148:151]\n"
      "v_cvt_pk_bf16_f32 v72, v72, v73\n"
      "v_cvt_pk_bf16_f32 v73, v74, v75\n"
      "v_cvt_pk_bf16_f32 v74, v76, v77\n"
      "v_mfma_f32_16x16x32_bf16 v[92:95], v[68:71], a[192:195], v[148:151]\n"
      "v_cvt_pk_bf16_f32 v75, v78, v79\n"
      "v_accvgpr_write_b32 a48, v72\n"
      "v_accvgpr_write_b32 a49, v73\n"
      "v_mfma_f32_16x16x32_bf16 v[136:139], v[172:175], v[24:27], v[136:139]\n"
      "v_accvgpr_write_b32 a50, v74\n"
      "v_accvgpr_write_b32 a51, v75\n"
      "v_mfma_f32_16x16x32_bf16 v[96:99], v[64:67], v[28:31], v[152:155]\n"
      "v_cvt_pk_bf16_f32 v80, v80, v81\n"
      "v_cvt_pk_bf16_f32 v81, v82, v83\n"
      "v_cvt_pk_bf16_f32 v82, v84, v85\n"
      "v_mfma_f32_16x16x32_bf16 v[100:103], v[68:71], v[28:31], v[152:155]\n"
      "v_cvt_pk_bf16_f32 v83, v86, v87\n"
      "v_accvgpr_write_b32 a120, v80\n"
      "v_accvgpr_write_b32 a121, v81\n"
      "v_mfma_f32_16x16x32_bf16 v[124:127], v[172:175], a[48:51], v[124:127]\n"
      "v_accvgpr_write_b32 a122, v82\n"
      "v_accvgpr_write_b32 a123, v83\n"
      "v_mfma_f32_16x16x32_bf16 v[72:75], v[64:67], a[52:55], v[140:143]\n"
      "v_cvt_pk_bf16_f32 v88, v88, v89\n"
      "v_cvt_pk_bf16_f32 v89, v90, v91\n"
      "v_cvt_pk_bf16_f32 v90, v92, v93\n"
      "v_mfma_f32_16x16x32_bf16 v[76:79], v[68:71], a[52:55], v[140:143]\n"
      "v_cvt_pk_bf16_f32 v91, v94, v95\n"
      "v_accvgpr_write_b32 a192, v88\n"
      "v_accvgpr_write_b32 a193, v89\n"
      "v_mfma_f32_16x16x32_bf16 v[128:131], v[172:175], a[120:123], v[128:131]\n"
      "v_accvgpr_write_b32 a194, v90\n"
      "v_accvgpr_write_b32 a195, v91\n"
      "v_mfma_f32_16x16x32_bf16 v[80:83], v[64:67], a[124:127], v[144:147]\n"
      "v_cvt_pk_bf16_f32 v28, v96, v97\n"
      "v_cvt_pk_bf16_f32 v29, v98, v99\n"
      "v_cvt_pk_bf16_f32 v30, v100, v101\n"
      "v_mfma_f32_16x16x32_bf16 v[84:87], v[68:71], a[124:127], v[144:147]\n"
      "v_cvt_pk_bf16_f32 v31, v102, v103\n"
      "v_mfma_f32_16x16x32_bf16 v[132:135], v[172:175], a[192:195], v[132:135]\n"
      "v_mfma_f32_16x16x32_bf16 v[88:91], v[64:67], a[196:199], v[148:151]\n"
      "v_cvt_pk_bf16_f32 v72, v72, v73\n"
      "v_cvt_pk_bf16_f32 v73, v74, v75\n"
      "v_cvt_pk_bf16_f32 v74, v76, v77\n"
      "v_mfma_f32_16x16x32_bf16 v[92:95], v[68:71], a[196:199], v[148:151]\n"
      "v_cvt_pk_bf16_f32 v75, v78, v79\n"
      "v_accvgpr_write_b32 a52, v72\n"
      "v_accvgpr_write_b32 a53, v73\n"
      "v_mfma_f32_16x16x32_bf16 v[136:139], v[172:175], v[28:31], v[136:139]\n"
      "v_accvgpr_write_b32 a54, v74\n"
      "v_accvgpr_write_b32 a55, v75\n"
      "v_mfma_f32_16x16x32_bf16 v[96:99], v[64:67], v[32:35], v[152:155]\n"
      "v_cvt_pk_bf16_f32 v80, v80, v81\n"
      "v_cvt_pk_bf16_f32 v81, v82, v83\n"
      "v_cvt_pk_bf16_f32 v82, v84, v85\n"
      "v_mfma_f32_16x16x32_bf16 v[100:103], v[68:71], v[32:35], v[152:155]\n"
      "v_cvt_pk_bf16_f32 v83, v86, v87\n"
      "v_accvgpr_write_b32 a124, v80\n"
      "v_accvgpr_write_b32 a125, v81\n"
      "v_mfma_f32_16x16x32_bf16 v[124:127], v[172:175], a[52:55], v[124:127]\n"
      "v_accvgpr_write_b32 a126, v82\n"
      "v_accvgpr_write_b32 a127, v83\n"
      "v_mfma_f32_16x16x32_bf16 v[72:75], v[64:67], a[56:59], v[140:143]\n"
      "v_cvt_pk_bf16_f32 v88, v88, v89\n"
      "v_cvt_pk_bf16_f32 v89, v90, v91\n"
      "v_cvt_pk_bf16_f32 v90, v92, v93\n"
      "v_mfma_f32_16x16x32_bf16 v[76:79], v[68:71], a[56:59], v[140:143]\n"
      "v_cvt_pk_bf16_f32 v91, v94, v95\n"
      "v_accvgpr_write_b32 a196, v88\n"
      "v_accvgpr_write_b32 a197, v89\n"
      "v_mfma_f32_16x16x32_bf16 v[128:131], v[172:175], a[124:127], v[128:131]\n"
      "v_accvgpr_write_b32 a198, v90\n"
      "v_accvgpr_write_b32 a199, v91\n"
      "v_mfma_f32_16x16x32_bf16 v[80:83], v[64:67], a[128:131], v[144:147]\n"
      "v_cvt_pk_bf16_f32 v32, v96, v97\n"
      "v_cvt_pk_bf16_f32 v33, v98, v99\n"
      "v_cvt_pk_bf16_f32 v34, v100, v101\n"
      "v_mfma_f32_16x16x32_bf16 v[84:87], v[68:71], a[128:131], v[144:147]\n"
      "v_cvt_pk_bf16_f32 v35, v102, v103\n"
      "v_mfma_f32_16x16x32_bf16 v[132:135], v[172:175], a[196:199], v[132:135]\n"
      "v_mfma_f32_16x16x32_bf16 v[88:91], v[64:67], a[200:203], v[148:151]\n"
      "v_cvt_pk_bf16_f32 v72, v72, v73\n"
      "v_cvt_pk_bf16_f32 v73, v74, v75\n"
      "v_cvt_pk_bf16_f32 v74, v76, v77\n"
      "v_mfma_f32_16x16x32_bf16 v[92:95], v[68:71], a[200:203], v[148:151]\n"
      "v_cvt_pk_bf16_f32 v75, v78, v79\n"
      "v_accvgpr_write_b32 a56, v72\n"
      "v_accvgpr_write_b32 a57, v73\n"
      "v_mfma_f32_16x16x32_bf16 v[136:139], v[172:175], v[32:35], v[136:139]\n"
      "v_accvgpr_write_b32 a58, v74\n"
      "v_accvgpr_write_b32 a59, v75\n"
      "v_mfma_f32_16x16x32_bf16 v[96:99], v[64:67], v[36:39], v[152:155]\n"
      "v_cvt_pk_bf16_f32 v80, v80, v81\n"
      "v_cvt_pk_bf16_f32 v81, v82, v83\n"
      "v_cvt_pk_bf16_f32 v82, v84, v85\n"
      "v_mfma_f32_16x16x32_bf16 v[100:103], v[68:71], v[36:39], v[152:155]\n"
      "v_cvt_pk_bf16_f32 v83, v86, v87\n"
      "v_accvgpr_write_b32 a128, v80\n"
      "v_accvgpr_write_b32 a129, v81\n"
      "v_mfma_f32_16x16x32_bf16 v[124:127], v[172:175], a[56:59], v[124:127]\n"
      "v_accvgpr_write_b32 a130, v82\n"
      "v_accvgpr_write_b32 a131, v83\n"
      "s_nop 1\n"
      "v_mfma_f32_16x16x32_bf16 v[128:131], v[172:175], a[128:131], v[128:131]\n"
      "v_cvt_pk_bf16_f32 v88, v88, v89\n"
      "v_cvt_pk_bf16_f32 v89, v90, v91\n"
      "v_cvt_pk_bf16_f32 v90, v92, v93\n"
      "v_cvt_pk_bf16_f32 v91, v94, v95\n"
      "v_accvgpr_write_b32 a200, v88\n"
      "v_accvgpr_write_b32 a201, v89\n"
      "v_accvgpr_write_b32 a202, v90\n"
      "v_accvgpr_write_b32 a203, v91\n"
      "s_nop 1\n"
      "v_mfma_f32_16x16x32_bf16 v[132:135], v[172:175], a[200:203], v[132:135]\n"
      "v_cvt_pk_bf16_f32 v36, v96, v97\n"
      "v_cvt_pk_bf16_f32 v37, v98, v99\n"
      "v_cvt_pk_bf16_f32 v38, v100, v101\n"
      "v_cvt_pk_bf16_f32 v39, v102, v103\n"
      "s_nop 1\n"
      "v_mfma_f32_16x16x32_bf16 v[136:139], v[172:175], v[36:39], v[136:139]\n"
      "v_add_f32 v168, v124, v160\n"
      "v_add_f32 v169, v128, v161\n"
      "v_add_f32 v170, v132, v162\n"
      "s_nop 5\n"
      "v_add_f32 v171, v136, v163\n"
      "v_mul_f32 v140, 0xbc2aaaab, v168\n"
      "v_mul_f32 v141, 0xbc2aaaab, v168\n"
      "v_mul_f32 v142, 0xbc2aaaab, v168\n"
      "v_mul_f32 v143, 0xbc2aaaab, v168\n"
      "v_mul_f32 v144, 0xbc2aaaab, v169\n"
      "v_mul_f32 v145, 0xbc2aaaab, v169\n"
      "v_mul_f32 v146, 0xbc2aaaab, v169\n"
      "v_mul_f32 v147, 0xbc2aaaab, v169\n"
      "v_mul_f32 v148, 0xbc2aaaab, v170\n"
      "v_mul_f32 v149, 0xbc2aaaab, v170\n"
      "v_mul_f32 v150, 0xbc2aaaab, v170\n"
      "v_mul_f32 v151, 0xbc2aaaab, v170\n"
      "v_mul_f32 v152, 0xbc2aaaab, v171\n"
      "v_mul_f32 v153, 0xbc2aaaab, v171\n"
      "v_mul_f32 v154, 0xbc2aaaab, v171\n"
      "v_mul_f32 v155, 0xbc2aaaab, v171\n"
      "v_mfma_f32_16x16x32_bf16 v[72:75], v[64:67], a[60:63], v[140:143]\n"
      "v_mfma_f32_16x16x32_bf16 v[76:79], v[68:71], a[60:63], v[140:143]\n"
      "v_mfma_f32_16x16x32_bf16 v[80:83], v[64:67], a[132:135], v[144:147]\n"
      "v_mfma_f32_16x16x32_bf16 v[84:87], v[68:71], a[132:135], v[144:147]\n"
      "v_mfma_f32_16x16x32_bf16 v[88:91], v[64:67], a[204:207], v[148:151]\n"
      "s_nop 4\n"
      "v_cvt_pk_bf16_f32 v72, v72, v73\n"
      "v_cvt_pk_bf16_f32 v73, v74, v75\n"
      "v_cvt_pk_bf16_f32 v74, v76, v77\n"
      "v_mfma_f32_16x16x32_bf16 v[92:95], v[68:71], a[204:207], v[148:151]\n"
      "v_cvt_pk_bf16_f32 v75, v78, v79\n"
      "v_accvgpr_write_b32 a60, v72\n"
      "v_accvgpr_write_b32 a61, v73\n"
      "v_accvgpr_write_b32 a62, v74\n"
      "v_accvgpr_write_b32 a63, v75\n"
      "v_mfma_f32_16x16x32_bf16 v[96:99], v[64:67], v[40:43], v[152:155]\n"
      "v_cvt_pk_bf16_f32 v80, v80, v81\n"
      "v_cvt_pk_bf16_f32 v81, v82, v83\n"
      "v_cvt_pk_bf16_f32 v82, v84, v85\n"
      "v_mfma_f32_16x16x32_bf16 v[100:103], v[68:71], v[40:43], v[152:155]\n"
      "v_cvt_pk_bf16_f32 v83, v86, v87\n"
      "v_accvgpr_write_b32 a132, v80\n"
      "v_accvgpr_write_b32 a133, v81\n"
      "v_mfma_f32_16x16x32_bf16 v[124:127], v[172:175], a[60:63], v[124:127]\n"
      "v_accvgpr_write_b32 a134, v82\n"
      "v_accvgpr_write_b32 a135, v83\n"
      "v_mfma_f32_16x16x32_bf16 v[72:75], v[64:67], a[64:67], v[140:143]\n"
      "v_cvt_pk_bf16_f32 v88, v88, v89\n"
      "v_cvt_pk_bf16_f32 v89, v90, v91\n"
      "v_cvt_pk_bf16_f32 v90, v92, v93\n"
      "v_mfma_f32_16x16x32_bf16 v[76:79], v[68:71], a[64:67], v[140:143]\n"
      "v_cvt_pk_bf16_f32 v91, v94, v95\n"
      "v_accvgpr_write_b32 a204, v88\n"
      "v_accvgpr_write_b32 a205, v89\n"
      "v_mfma_f32_16x16x32_bf16 v[128:131], v[172:175], a[132:135], v[128:131]\n"
      "v_accvgpr_write_b32 a206, v90\n"
      "v_accvgpr_write_b32 a207, v91\n"
      "v_mfma_f32_16x16x32_bf16 v[80:83], v[64:67], a[136:139], v[144:147]\n"
      "v_cvt_pk_bf16_f32 v40, v96, v97\n"
      "v_cvt_pk_bf16_f32 v41, v98, v99\n"
      "v_cvt_pk_bf16_f32 v42, v100, v101\n"
      "v_mfma_f32_16x16x32_bf16 v[84:87], v[68:71], a[136:139], v[144:147]\n"
      "v_cvt_pk_bf16_f32 v43, v102, v103\n"
      "v_mfma_f32_16x16x32_bf16 v[132:135], v[172:175], a[204:207], v[132:135]\n"
      "v_mfma_f32_16x16x32_bf16 v[88:91], v[64:67], a[208:211], v[148:151]\n"
      "v_cvt_pk_bf16_f32 v72, v72, v73\n"
      "v_cvt_pk_bf16_f32 v73, v74, v75\n"
      "v_cvt_pk_bf16_f32 v74, v76, v77\n"
      "v_mfma_f32_16x16x32_bf16 v[92:95], v[68:71], a[208:211], v[148:151]\n"
      "v_cvt_pk_bf16_f32 v75, v78, v79\n"
      "v_accvgpr_write_b32 a64, v72\n"
      "v_accvgpr_write_b32 a65, v73\n"
      "v_mfma_f32_16x16x32_bf16 v[136:139], v[172:175], v[40:43], v[136:139]\n"
      "v_accvgpr_write_b32 a66, v74\n"
      "v_accvgpr_write_b32 a67, v75\n"
      "v_mfma_f32_16x16x32_bf16 v[96:99], v[64:67], v[44:47], v[152:155]\n"
      "v_cvt_pk_bf16_f32 v80, v80, v81\n"
      "v_cvt_pk_bf16_f32 v81, v82, v83\n"
      "v_cvt_pk_bf16_f32 v82, v84, v85\n"
      "v_mfma_f32_16x16x32_bf16 v[100:103], v[68:71], v[44:47], v[152:155]\n"
      "v_cvt_pk_bf16_f32 v83, v86, v87\n"
      "v_accvgpr_write_b32 a136, v80\n"
      "v_accvgpr_write_b32 a137, v81\n"
      "v_mfma_f32_16x16x32_bf16 v[124:127], v[172:175], a[64:67], v[124:127]\n"
      "v_accvgpr_write_b32 a138, v82\n"
      "v_accvgpr_write_b32 a139, v83\n"
      "s_nop 1\n"
      "v_mfma_f32_16x16x32_bf16 v[128:131], v[172:175], a[136:139], v[128:131]\n"
      "v_cvt_pk_bf16_f32 v88, v88, v89\n"
      "v_cvt_pk_bf16_f32 v89, v90, v91\n"
      "v_cvt_pk_bf16_f32 v90, v92, v93\n"
      "v_cvt_pk_bf16_f32 v91, v94, v95\n"
      "v_accvgpr_write_b32 a208, v88\n"
      "v_accvgpr_write_b32 a209, v89\n"
      "v_accvgpr_write_b32 a210, v90\n"
      "v_accvgpr_write_b32 a211, v91\n"
      "s_nop 1\n"
      "v_mfma_f32_16x16x32_bf16 v[132:135], v[172:175], a[208:211], v[132:135]\n"
      "v_cvt_pk_bf16_f32 v44, v96, v97\n"
      "v_cvt_pk_bf16_f32 v45, v98, v99\n"
      "v_cvt_pk_bf16_f32 v46, v100, v101\n"
      "v_cvt_pk_bf16_f32 v47, v102, v103\n"
      "s_nop 1\n"
      "v_mfma_f32_16x16x32_bf16 v[136:139], v[172:175], v[44:47], v[136:139]\n"
      "v_add_f32 v168, v124, v164\n"
      "v_add_f32 v169, v128, v165\n"
      "v_add_f32 v170, v132, v166\n"
      "s_nop 5\n"
      "v_add_f32 v171, v136, v167\n"
      "v_mul_f32 v140, 0xbd000000, v168\n"
      "v_mul_f32 v141, 0xbd000000, v168\n"
      "v_mul_f32 v142, 0xbd000000, v168\n"
      "v_mul_f32 v143, 0xbd000000, v168\n"
      "v_mul_f32 v144, 0xbd000000, v169\n"
      "v_mul_f32 v145, 0xbd000000, v169\n"
      "v_mul_f32 v146, 0xbd000000, v169\n"
      "v_mul_f32 v147, 0xbd000000, v169\n"
      "v_mul_f32 v148, 0xbd000000, v170\n"
      "v_mul_f32 v149, 0xbd000000, v170\n"
      "v_mul_f32 v150, 0xbd000000, v170\n"
      "v_mul_f32 v151, 0xbd000000, v170\n"
      "v_mul_f32 v152, 0xbd000000, v171\n"
      "v_mul_f32 v153, 0xbd000000, v171\n"
      "v_mul_f32 v154, 0xbd000000, v171\n"
      "v_mul_f32 v155, 0xbd000000, v171\n"
      "v_mfma_f32_16x16x32_bf16 v[72:75], v[64:67], a[68:71], v[140:143]\n"
      "v_mfma_f32_16x16x32_bf16 v[76:79], v[68:71], a[68:71], v[140:143]\n"
      "v_mfma_f32_16x16x32_bf16 v[80:83], v[64:67], a[140:143], v[144:147]\n"
      "v_mfma_f32_16x16x32_bf16 v[84:87], v[68:71], a[140:143], v[144:147]\n"
      "v_mfma_f32_16x16x32_bf16 v[88:91], v[64:67], a[212:215], v[148:151]\n"
      "s_nop 4\n"
      "v_cvt_pk_bf16_f32 v72, v72, v73\n"
      "v_cvt_pk_bf16_f32 v73, v74, v75\n"
      "v_cvt_pk_bf16_f32 v74, v76, v77\n"
      "v_mfma_f32_16x16x32_bf16 v[92:95], v[68:71], a[212:215], v[148:151]\n"
      "v_cvt_pk_bf16_f32 v75, v78, v79\n"
      "v_accvgpr_write_b32 a68, v72\n"
      "v_accvgpr_write_b32 a69, v73\n"
      "v_accvgpr_write_b32 a70, v74\n"
      "v_accvgpr_write_b32 a71, v75\n"
      "v_mfma_f32_16x16x32_bf16 v[96:99], v[64:67], v[48:51], v[152:155]\n"
      "v_cvt_pk_bf16_f32 v80, v80, v81\n"
      "v_cvt_pk_bf16_f32 v81, v82, v83\n"
      "v_cvt_pk_bf16_f32 v82, v84, v85\n"
      "v_mfma_f32_16x16x32_bf16 v[100:103], v[68:71], v[48:51], v[152:155]\n"
      "v_cvt_pk_bf16_f32 v83, v86, v87\n"
      "v_accvgpr_write_b32 a140, v80\n"
      "v_accvgpr_write_b32 a141, v81\n"
      "v_accvgpr_write_b32 a142, v82\n"
      "v_accvgpr_write_b32 a143, v83\n"
      "v_cvt_pk_bf16_f32 v88, v88, v89\n"
      "v_cvt_pk_bf16_f32 v89, v90, v91\n"
      "v_cvt_pk_bf16_f32 v90, v92, v93\n"
      "v_cvt_pk_bf16_f32 v91, v94, v95\n"
      "v_accvgpr_write_b32 a212, v88\n"
      "v_accvgpr_write_b32 a213, v89\n"
      "v_accvgpr_write_b32 a214, v90\n"
      "v_accvgpr_write_b32 a215, v91\n"
      "v_cvt_pk_bf16_f32 v48, v96, v97\n"
      "v_cvt_pk_bf16_f32 v49, v98, v99\n"
      "v_cvt_pk_bf16_f32 v50, v100, v101\n"
      "v_cvt_pk_bf16_f32 v51, v102, v103\n"
      "v_add_u32 v17, 40960, v0\n"
      "s_barrier\n"
      "v_mov_b32 v11, 0\n"
      "ds_write_b32 v13, v11\n"
      "s_waitcnt lgkmcnt(0)\n"
      "s_barrier\n"
      "s_add_u32 s6, s36, 1548288\n"
      "s_add_u32 s36, s36, 4294942720\n"
      "s_add_u32 s42, s37, 4294635520\n"
      "s_add_u32 s37, s37, 4294635520\n"
      "s_add_u32 s36, s36, 24576\n"
      "s_min_u32 s36, s36, s6\n"
      "buffer_load_dword v18, v2, s[8:11], s36 offen sc1\n"
      "s_mov_b32 s40, s36\n"
      "s_add_u32 m0, s38, 0\n"
      "s_waitcnt vmcnt(0)\n"
      "v_cmp_ne_u32 vcc, s21, v18\n"
      "s_cbranch_vccnz .Lsp_g0_1\n"
      ".Lbk_g0_1:\n"
      "buffer_load_dwordx4 v0, s[8:11], s40 offen offset:0 sc1 lds\n"
      "s_add_u32 s36, s36, 24576\n"
      "buffer_load_dwordx4 v0, s[8:11], s40 offen offset:1024 sc1 lds\n"
      "buffer_load_dwordx4 v0, s[8:11], s40 offen offset:2048 sc1 lds\n"
      "buffer_load_dwordx4 v0, s[8:11], s40 offen offset:3072 sc1 lds\n"
      "s_add_u32 m0, s39, 0\n"
      "s_min_u32 s36, s36, s6\n"
      "buffer_load_dwordx4 v1, s[8:11], s40 offen offset:0 sc1 lds\n"
      "buffer_load_dword v18, v2, s[8:11], s36 offen sc1\n"
      "v_add_u32 v11, 1, v11\n"
      "s_mov_b32 s40, s36\n"
      "s_add_u32 m0, s38, 20480\n"
      "s_waitcnt vmcnt(1)\n"
      "ds_write_b32 v13, v11\n"
      "s_waitcnt vmcnt(0)\n"
      "v_cmp_ne_u32 vcc, s21, v18\n"
      "s_cbranch_vccnz .Lsp_g0_2\n"
      ".Lbk_g0_2:\n"
      "buffer_load_dwordx4 v0, s[8:11], s40 offen offset:0 sc1 lds\n"
      "ds_read_b32 v12, v14\n"
      "s_add_u32 s36, s36, 24576\n"
      "buffer_load_dwordx4 v0, s[8:11], s40 offen offset:1024 sc1 lds\n"
      "s_waitcnt lgkmcnt(0)\n"
      "v_cmp_gt_i32 vcc, v11, v12\n"
      "buffer_load_dwordx4 v0, s[8:11], s40 offen offset:2048 sc1 lds\n"
      "s_cbranch_vccnz .Lsp_gy0_3\n"
      ".Lbk_gy0_3:\n"
      "ds_read2st64_b64 v[168:171], v4 offset0:0 offset1:1\n"
      "ds_read_b128 v[52:55], v0 offset:0\n"
      "buffer_load_dwordx4 v0, s[8:11], s40 offen offset:3072 sc1 lds\n"
      "s_add_u32 m0, s39, 20480\n"
      "ds_read_b128 v[64:67], v0 offset:3072\n"
      "s_min_u32 s36, s36, s6\n"
      "ds_read_b128 v[56:59], v0 offset:1024\n"
      "buffer_load_dwordx4 v1, s[8:11], s40 offen offset:0 sc1 lds\n"
      "ds_read_b128 v[68:71], v0 offset:4096\n"
      "ds_read_b128 v[76:79], v0 offset:6144\n"
      "ds_read_b128 v[72:75], v0 offset:5120\n"
      "buffer_load_dword v18, v2, s[8:11], s36 offen sc1\n"
      "ds_read_b128 v[60:63], v0 offset:2048\n"
      "buffer_load_dword v16, v13, s[8:11], s31 offen\n"
      "ds_read_b128 v[84:87], v0 offset:8192\n"
      "ds_read_b128 v[80:83], v0 offset:7168\n"
      "ds_read_b128 v[96:99], v0 offset:11264\n"
      "s_mov_b32 s20, 15\n"
      "s_waitcnt lgkmcnt(0)\n"
      "s_nop 4\n"
      "s_branch .Lg_loop\n"
      ".p2align 12\n"
      ".Lg_p0:\n"
      "s_branch .Lg_pr\n"
      ".p2align 5\n"
      ".Lg_loop:\n"
      "v_mfma_f32_16x16x32_bf16 v[124:127], a[0:3], v[52:55], 0\n"
      "ds_read_b128 v[92:95], v0 offset:10240\n"
      "v_add_f32 v169, v169, v171\n"
      "v_mfma_f32_16x16x32_bf16 v[124:127], a[4:7], v[56:59], v[124:127]\n"
      "s_waitcnt lgkmcnt(4)\n"
      "ds_read_b128 v[88:91], v0 offset:9216\n"
      "v_mul_f32 v161, v133, v161\n"
      "v_mfma_f32_16x16x32_bf16 v[124:127], a[8:11], v[60:63], v[124:127]\n"
      "ds_read_b128 v[112:115], v0 offset:15360\n"
      "v_mul_f32 v163, v135, v163\n"
      "v_mfma_f32_16x16x32_bf16 v[124:127], a[12:15], v[64:67], v[124:127]\n"
      "ds_read_b128 v[100:103], v0 offset:12288\n"
      "v_mul_f32 v139, v139, v163\n"
      "v_mfma_f32_16x16x32_bf16 v[124:127], a[16:19], v[68:71], v[124:127]\n"
      "ds_read_b128 v[104:107], v0 offset:13312\n"
      "v_mul_f32 v131, v131, v159\n"
      "v_mfma_f32_16x16x32_bf16 v[124:127], a[20:23], v[72:75], v[124:127]\n"
      "ds_read_b128 v[108:111], v0 offset:14336\n"
      "v_mfma_f32_16x16x32_bf16 v[124:127], a[24:27], v[76:79], v[124:127]\n"
      "s_waitcnt lgkmcnt(6)\n"
      "ds_read_b128 v[120:123], v0 offset:17408\n"
      "v_sub_f32 v176, -v168, v170\n"
      "v_mfma_f32_16x16x32_bf16 v[124:127], a[28:31], v[80:83], v[124:127]\n"
      "v_rcp_f32 v160, v160\n"
      "v_mfma_f32_16x16x32_bf16 v[124:127], a[32:35], v[84:87], v[124:127]\n"
      "s_waitcnt lgkmcnt(5)\n"
      "ds_read_b128 v[116:119], v0 offset:16384\n"
      "v_fma_f32 v169, -v176, v176, v169\n"
      "v_mfma_f32_16x16x32_bf16 v[124:127], a[36:39], v[88:91], v[124:127]\n"
      "v_mul_f32 v160, v132, v160\n"
      "v_fma_f32 v162, v162, v177, v177\n"
      "v_mfma_f32_16x16x32_bf16 v[124:127], a[40:43], v[92:95], v[124:127]\n"
      "v_mul_f32 v130, v130, v158\n"
      "v_mul_f32 v128, v128, v156\n"
      "v_mfma_f32_16x16x32_bf16 v[124:127], a[44:47], v[96:99], v[124:127]\n"
      "s_waitcnt lgkmcnt(3)\n"
      "v_rcp_f32 v162, v162\n"
      "v_mfma_f32_16x16x32_bf16 v[124:127], a[48:51], v[100:103], v[124:127]\n"
      "v_cvt_pk_bf16_f32 v165, v130, v131\n"
      "v_mul_f32 v162, v134, v162\n"
      "v_mfma_f32_16x16x32_bf16 v[124:127], a[52:55], v[104:107], v[124:127]\n"
      "s_waitcnt lgkmcnt(1)\n"
      "v_mul_f32 v136, v136, v160\n"
      "v_mul_f32 v138, v138, v162\n"
      "v_mfma_f32_16x16x32_bf16 v[124:127], a[56:59], v[108:111], v[124:127]\n"
      "v_mul_f32 v137, v137, v161\n"
      "v_cvt_pk_bf16_f32 v167, v138, v139\n"
      "v_mfma_f32_16x16x32_bf16 v[124:127], a[60:63], v[112:115], v[124:127]\n"
      "s_waitcnt lgkmcnt(0)\n"
      "v_max_f32 v177, 0x3727c5ac, v169\n"
      "v_mfma_f32_16x16x32_bf16 v[124:127], a[64:67], v[116:119], v[124:127]\n"
      "s_add_u32 s37, s37, 165888\n"
      "v_cvt_pk_bf16_f32 v164, v128, v129\n"
      "v_mfma_f32_16x16x32_bf16 v[124:127], a[68:71], v[120:123], v[124:127]\n"
      "v_cvt_pk_bf16_f32 v166, v136, v137\n"
      "v_add_u32 v11, 1, v11\n"
      "v_mfma_f32_16x16x32_bf16 v[132:135], a[144:147], v[52:55], 0\n"
      "buffer_store_dwordx4 v[164:167], v0, s[8:11], s37 offen sc1\n"
      "v_mfma_f32_16x16x32_bf16 v[132:135], a[148:151], v[56:59], v[132:135]\n"
      "v_rsq_f32 v170, v177\n"
      "v_mfma_f32_16x16x32_bf16 v[132:135], a[152:155], v[60:63], v[132:135]\n"
      "v_mul_f32 v178, 0xc0135761, v170\n"
      "v_mul_f32 v170, v170, v170\n"
      "v_mfma_f32_16x16x32_bf16 v[132:135], a[156:159], v[64:67], v[132:135]\n"
      "v_mul_f32 v171, 0x3d372713, v178\n"
      "v_mul_f32 v157, v125, v125\n"
      "v_mfma_f32_16x16x32_bf16 v[132:135], a[160:163], v[68:71], v[132:135]\n"
      "v_mul_f32 v179, v171, v170\n"
      "v_mul_f32 v158, v126, v126\n"
      "v_mfma_f32_16x16x32_bf16 v[132:135], a[164:167], v[72:75], v[132:135]\n"
      "v_fma_f32 v157, v157, v179, v178\n"
      "s_mov_b32 s40, s36\n"
      "v_mfma_f32_16x16x32_bf16 v[132:135], a[168:171], v[76:79], v[132:135]\n"
      "v_mul_f32 v157, v125, v157\n"
      "s_add_u32 m0, s38, 40960\n"
      "v_mfma_f32_16x16x32_bf16 v[132:135], a[172:175], v[80:83], v[132:135]\n"
      "v_exp_f32 v157, v157\n"
      "v_mfma_f32_16x16x32_bf16 v[132:135], a[176:179], v[84:87], v[132:135]\n"
      "v_fma_f32 v158, v158, v179, v178\n"
      "v_fma_f32 v157, v157, v177, v177\n"
      "v_mfma_f32_16x16x32_bf16 v[132:135], a[180:183], v[88:91], v[132:135]\n"
      "v_mfma_f32_16x16x32_bf16 v[132:135], a[184:187], v[92:95], v[132:135]\n"
      "v_mul_f32 v156, v124, v124\n"
      "v_mul_f32 v158, v126, v158\n"
      "v_mfma_f32_16x16x32_bf16 v[132:135], a[188:191], v[96:99], v[132:135]\n"
      "v_mul_f32 v159, v127, v127\n"
      "v_fma_f32 v156, v156, v179, v178\n"
      "v_mfma_f32_16x16x32_bf16 v[132:135], a[192:195], v[100:103], v[132:135]\n"
      "v_fma_f32 v159, v159, v179, v178\n"
      "v_mul_f32 v156, v124, v156\n"
      "v_mfma_f32_16x16x32_bf16 v[132:135], a[196:199], v[104:107], v[132:135]\n"
      "v_exp_f32 v158, v158\n"
      "v_mfma_f32_16x16x32_bf16 v[132:135], a[200:203], v[108:111], v[132:135]\n"
      "v_fma_f32 v158, v158, v177, v177\n"
      "v_mul_f32 v159, v127, v159\n"
      "v_mfma_f32_16x16x32_bf16 v[132:135], a[204:207], v[112:115], v[132:135]\n"
      "v_exp_f32 v156, v156\n"
      "v_mfma_f32_16x16x32_bf16 v[132:135], a[208:211], v[116:119], v[132:135]\n"
      "s_add_u32 s42, s42, 165888\n"
      "v_fma_f32 v156, v156, v177, v177\n"
      "v_mfma_f32_16x16x32_bf16 v[132:135], a[212:215], v[120:123], v[132:135]\n"
      "s_waitcnt vmcnt(3)\n"
      "ds_write_b32 v13, v11\n"
      "v_mfma_f32_16x16x32_bf16 v[128:131], a[72:75], v[52:55], 0\n"
      "v_exp_f32 v159, v159\n"
      "v_mfma_f32_16x16x32_bf16 v[128:131], a[76:79], v[56:59], v[128:131]\n"
      "s_waitcnt vmcnt(2)\n"
      "v_fma_f32 v159, v159, v177, v177\n"
      "v_cmp_ne_u32 vcc, s21, v18\n"
      "v_mfma_f32_16x16x32_bf16 v[128:131], a[80:83], v[60:63], v[128:131]\n"
      "v_mul_f32 v161, v133, v133\n"
      "v_fma_f32 v161, v161, v179, v178\n"
      "v_mfma_f32_16x16x32_bf16 v[128:131], a[84:87], v[64:67], v[128:131]\n"
      "v_mul_f32 v163, v135, v135\n"
      "v_mul_f32 v161, v133, v161\n"
      "v_mfma_f32_16x16x32_bf16 v[128:131], a[88:91], v[68:71], v[128:131]\n"
      "v_mul_f32 v160, v132, v132\n"
      "v_fma_f32 v163, v163, v179, v178\n"
      "v_mfma_f32_16x16x32_bf16 v[128:131], a[92:95], v[72:75], v[128:131]\n"
      "s_cbranch_vccnz .Lsp_g0_4\n"
      ".Lbk_g0_4:\n"
      "v_rcp_f32 v156, v156\n"
      "v_mfma_f32_16x16x32_bf16 v[128:131], a[96:99], v[76:79], v[128:131]\n"
      "buffer_load_dwordx4 v0, s[8:11], s40 offen offset:0 sc1 lds\n"
      "v_mfma_f32_16x16x32_bf16 v[128:131], a[100:103], v[80:83], v[128:131]\n"
      "v_rcp_f32 v159, v159\n"
      "v_mfma_f32_16x16x32_bf16 v[128:131], a[104:107], v[84:87], v[128:131]\n"
      "ds_read_b32 v12, v14\n"
      "v_fma_f32 v160, v160, v179, v178\n"
      "v_mfma_f32_16x16x32_bf16 v[128:131], a[108:111], v[88:91], v[128:131]\n"
      "s_add_u32 s36, s36, 24576\n"
      "v_mul_f32 v162, v134, v134\n"
      "v_mul_f32 v163, v135, v163\n"
      "v_mfma_f32_16x16x32_bf16 v[128:131], a[112:115], v[92:95], v[128:131]\n"
      "v_exp_f32 v163, v163\n"
      "v_mfma_f32_16x16x32_bf16 v[128:131], a[116:119], v[96:99], v[128:131]\n"
      "buffer_load_dwordx4 v0, s[8:11], s40 offen offset:1024 sc1 lds\n"
      "v_mfma_f32_16x16x32_bf16 v[128:131], a[120:123], v[100:103], v[128:131]\n"
      "v_rcp_f32_e64 v158, v158\n"
      "v_mfma_f32_16x16x32_bf16 v[128:131], a[124:127], v[104:107], v[128:131]\n"
      "s_waitcnt lgkmcnt(0)\n"
      "v_mul_f32_e64 v160, v132, v160\n"
      "v_cmp_gt_i32 vcc, v11, v12\n"
      "v_mfma_f32_16x16x32_bf16 v[128:131], a[128:131], v[108:111], v[128:131]\n"
      "v_fma_f32 v162, v162, v179, v178\n"
      "v_mul_f32_e64 v162, v134, v162\n"
      "v_mfma_f32_16x16x32_bf16 v[128:131], a[132:135], v[112:115], v[128:131]\n"
      "buffer_load_dwordx4 v0, s[8:11], s40 offen offset:2048 sc1 lds\n"
      "v_mfma_f32_16x16x32_bf16 v[128:131], a[136:139], v[116:119], v[128:131]\n"
      "v_exp_f32_e64 v162, v162\n"
      "v_mfma_f32_16x16x32_bf16 v[128:131], a[140:143], v[120:123], v[128:131]\n"
      "s_cbranch_vccnz .Lsp_gy0_5\n"
      ".Lbk_gy0_5:\n"
      "ds_read2st64_b64 v[168:171], v4 offset0:40 offset1:41\n"
      "v_mfma_f32_16x16x32_bf16 v[136:139], a[216:219], v[52:55], 0\n"
      "ds_read_b128 v[52:55], v0 offset:20480\n"
      "v_fma_f32 v163, v163, v177, v177\n"
      "v_mfma_f32_16x16x32_bf16 v[136:139], a[220:223], v[56:59], v[136:139]\n"
      "v_rcp_f32 v157, v157\n"
      "v_mfma_f32_16x16x32_bf16 v[136:139], a[224:227], v[60:63], v[136:139]\n"
      "buffer_load_dwordx4 v0, s[8:11], s40 offen offset:3072 sc1 lds\n"
      "v_mfma_f32_16x16x32_bf16 v[136:139], a[228:231], v[64:67], v[136:139]\n"
      "s_add_u32 m0, s39, 40960\n"
      "ds_read_b128 v[64:67], v0 offset:23552\n"
      "v_mul_f32 v158, v126, v158\n"
      "v_mfma_f32_16x16x32_bf16 v[136:139], a[232:235], v[68:71], v[136:139]\n"
      "s_min_u32 s36, s36, s6\n"
      "v_rcp_f32 v163, v163\n"
      "v_mfma_f32_16x16x32_bf16 v[136:139], a[236:239], v[72:75], v[136:139]\n"
      "ds_read_b128 v[56:59], v0 offset:21504\n"
      "v_mul_f32 v157, v125, v157\n"
      "v_mfma_f32_16x16x32_bf16 v[136:139], a[240:243], v[76:79], v[136:139]\n"
      "buffer_load_dwordx4 v1, s[8:11], s40 offen offset:0 sc1 lds\n"
      "v_mfma_f32_16x16x32_bf16 v[136:139], a[244:247], v[80:83], v[136:139]\n"
      "v_exp_f32 v161, v161\n"
      "v_mfma_f32_16x16x32_bf16 v[136:139], a[248:251], v[84:87], v[136:139]\n"
      "ds_read_b128 v[68:71], v0 offset:24576\n"
      "ds_read_b128 v[76:79], v0 offset:26624\n"
      "v_mfma_f32_16x16x32_bf16 v[136:139], a[252:255], v[88:91], v[136:139]\n"
      "v_exp_f32 v160, v160\n"
      "v_mfma_f32_16x16x32_bf16 v[136:139], v[20:23], v[92:95], v[136:139]\n"
      "ds_read_b128 v[72:75], v0 offset:25600\n"
      "v_mul_f32 v129, v129, v157\n"
      "v_mfma_f32_16x16x32_bf16 v[136:139], v[24:27], v[96:99], v[136:139]\n"
      "buffer_load_dword v18, v2, s[8:11], s36 offen sc1\n"
      "v_mfma_f32_16x16x32_bf16 v[136:139], v[28:31], v[100:103], v[136:139]\n"
      "ds_read_b128 v[60:63], v0 offset:22528\n"
      "v_fma_f32 v161, v161, v177, v177\n"
      "v_mfma_f32_16x16x32_bf16 v[136:139], v[32:35], v[104:107], v[136:139]\n"
      "s_waitcnt vmcnt(6) lgkmcnt(4)\n"
      "buffer_store_dword v15, v3, s[8:11], s42 offen sc1\n"
      "v_mfma_f32_16x16x32_bf16 v[136:139], v[36:39], v[108:111], v[136:139]\n"
      "ds_read_b128 v[84:87], v0 offset:28672\n"
      "v_fma_f32 v160, v160, v177, v177\n"
      "v_mfma_f32_16x16x32_bf16 v[136:139], v[40:43], v[112:115], v[136:139]\n"
      "v_rcp_f32 v161, v161\n"
      "v_mfma_f32_16x16x32_bf16 v[136:139], v[44:47], v[116:119], v[136:139]\n"
      "ds_read_b128 v[80:83], v0 offset:27648\n"
      "v_mul_f32 v159, v127, v159\n"
      "v_mfma_f32_16x16x32_bf16 v[136:139], v[48:51], v[120:123], v[136:139]\n"
      "ds_read_b128 v[96:99], v0 offset:31744\n"
      "v_mul_f32 v156, v124, v156\n"
      "v_mfma_f32_16x16x32_bf16 v[124:127], a[0:3], v[52:55], 0\n"
      "ds_read_b128 v[92:95], v0 offset:30720\n"
      "v_add_f32 v169, v169, v171\n"
      "v_mfma_f32_16x16x32_bf16 v[124:127], a[4:7], v[56:59], v[124:127]\n"
      "s_waitcnt lgkmcnt(4)\n"
      "ds_read_b128 v[88:91], v0 offset:29696\n"
      "v_mul_f32 v161, v133, v161\n"
      "v_mfma_f32_16x16x32_bf16 v[124:127], a[8:11], v[60:63], v[124:127]\n"
      "ds_read_b128 v[112:115], v0 offset:35840\n"
      "v_mul_f32 v163, v135, v163\n"
      "v_mfma_f32_16x16x32_bf16 v[124:127], a[12:15], v[64:67], v[124:127]\n"
      "ds_read_b128 v[100:103], v0 offset:32768\n"
      "v_mul_f32 v139, v139, v163\n"
      "v_mfma_f32_16x16x32_bf16 v[124:127], a[16:19], v[68:71], v[124:127]\n"
      "ds_read_b128 v[104:107], v0 offset:33792\n"
      "v_mul_f32 v131, v131, v159\n"
      "v_mfma_f32_16x16x32_bf16 v[124:127], a[20:23], v[72:75], v[124:127]\n"
      "ds_read_b128 v[108:111], v0 offset:34816\n"
      "v_mfma_f32_16x16x32_bf16 v[124:127], a[24:27], v[76:79], v[124:127]\n"
      "s_waitcnt lgkmcnt(6)\n"
      "ds_read_b128 v[120:123], v0 offset:37888\n"
      "v_sub_f32 v176, -v168, v170\n"
      "v_mfma_f32_16x16x32_bf16 v[124:127], a[28:31], v[80:83], v[124:127]\n"
      "v_rcp_f32 v160, v160\n"
      "v_mfma_f32_16x16x32_bf16 v[124:127], a[32:35], v[84:87], v[124:127]\n"
      "s_waitcnt lgkmcnt(5)\n"
      "ds_read_b128 v[116:119], v0 offset:36864\n"
      "v_fma_f32 v169, -v176, v176, v169\n"
      "v_mfma_f32_16x16x32_bf16 v[124:127], a[36:39], v[88:91], v[124:127]\n"
      "v_mul_f32 v160, v132, v160\n"
      "v_fma_f32 v162, v162, v177, v177\n"
      "v_mfma_f32_16x16x32_bf16 v[124:127], a[40:43], v[92:95], v[124:127]\n"
      "v_mul_f32 v130, v130, v158\n"
      "v_mul_f32 v128, v128, v156\n"
      "v_mfma_f32_16x16x32_bf16 v[124:127], a[44:47], v[96:99], v[124:127]\n"
      "s_waitcnt lgkmcnt(3)\n"
      "v_rcp_f32 v162, v162\n"
      "v_mfma_f32_16x16x32_bf16 v[124:127], a[48:51], v[100:103], v[124:127]\n"
      "v_cvt_pk_bf16_f32 v165, v130, v131\n"
      "v_mul_f32 v162, v134, v162\n"
      "v_mfma_f32_16x16x32_bf16 v[124:127], a[52:55], v[104:107], v[124:127]\n"
      "s_waitcnt lgkmcnt(1)\n"
      "v_mul_f32 v136, v136, v160\n"
      "v_mul_f32 v138, v138, v162\n"
      "v_mfma_f32_16x16x32_bf16 v[124:127], a[56:59], v[108:111], v[124:127]\n"
      "v_mul_f32 v137, v137, v161\n"
      "v_cvt_pk_bf16_f32 v167, v138, v139\n"
      "v_mfma_f32_16x16x32_bf16 v[124:127], a[60:63], v[112:115], v[124:127]\n"
      "s_waitcnt lgkmcnt(0)\n"
      "v_max_f32 v177, 0x3727c5ac, v169\n"
      "v_mfma_f32_16x16x32_bf16 v[124:127], a[64:67], v[116:119], v[124:127]\n"
      "s_add_u32 s37, s37, 165888\n"
      "v_cvt_pk_bf16_f32 v164, v128, v129\n"
      "v_mfma_f32_16x16x32_bf16 v[124:127], a[68:71], v[120:123], v[124:127]\n"
      "v_cvt_pk_bf16_f32 v166, v136, v137\n"
      "v_add_u32 v11, 1, v11\n"
      "v_mfma_f32_16x16x32_bf16 v[132:135], a[144:147], v[52:55], 0\n"
      "buffer_store_dwordx4 v[164:167], v0, s[8:11], s37 offen sc1\n"
      "v_mfma_f32_16x16x32_bf16 v[132:135], a[148:151], v[56:59], v[132:135]\n"
      "v_rsq_f32 v170, v177\n"
      "v_mfma_f32_16x16x32_bf16 v[132:135], a[152:155], v[60:63], v[132:135]\n"
      "v_mul_f32 v178, 0xc0135761, v170\n"
      "v_mul_f32 v170, v170, v170\n"
      "v_mfma_f32_16x16x32_bf16 v[132:135], a[156:159], v[64:67], v[132:135]\n"
      "v_mul_f32 v171, 0x3d372713, v178\n"
      "v_mul_f32 v157, v125, v125\n"
      "v_mfma_f32_16x16x32_bf16 v[132:135], a[160:163], v[68:71], v[132:135]\n"
      "v_mul_f32 v179, v171, v170\n"
      "v_mul_f32 v158, v126, v126\n"
      "v_mfma_f32_16x16x32_bf16 v[132:135], a[164:167], v[72:75], v[132:135]\n"
      "v_fma_f32 v157, v157, v179, v178\n"
      "s_mov_b32 s40, s36\n"
      "v_mfma_f32_16x16x32_bf16 v[132:135], a[168:171], v[76:79], v[132:135]\n"
      "v_mul_f32 v157, v125, v157\n"
      "s_add_u32 m0, s38, 61440\n"
      "v_mfma_f32_16x16x32_bf16 v[132:135], a[172:175], v[80:83], v[132:135]\n"
      "v_exp_f32 v157, v157\n"
      "v_mfma_f32_16x16x32_bf16 v[132:135], a[176:179], v[84:87], v[132:135]\n"
      "v_fma_f32 v158, v158, v179, v178\n"
      "v_fma_f32 v157, v157, v177, v177\n"
      "v_mfma_f32_16x16x32_bf16 v[132:135], a[180:183], v[88:91], v[132:135]\n"
      "v_mfma_f32_16x16x32_bf16 v[132:135], a[184:187], v[92:95], v[132:135]\n"
      "v_mul_f32 v156, v124, v124\n"
      "v_mul_f32 v158, v126, v158\n"
      "v_mfma_f32_16x16x32_bf16 v[132:135], a[188:191], v[96:99], v[132:135]\n"
      "v_mul_f32 v159, v127, v127\n"
      "v_fma_f32 v156, v156, v179, v178\n"
      "v_mfma_f32_16x16x32_bf16 v[132:135], a[192:195], v[100:103], v[132:135]\n"
      "v_fma_f32 v159, v159, v179, v178\n"
      "v_mul_f32 v156, v124, v156\n"
      "v_mfma_f32_16x16x32_bf16 v[132:135], a[196:199], v[104:107], v[132:135]\n"
      "v_exp_f32 v158, v158\n"
      "v_mfma_f32_16x16x32_bf16 v[132:135], a[200:203], v[108:111], v[132:135]\n"
      "v_fma_f32 v158, v158, v177, v177\n"
      "v_mul_f32 v159, v127, v159\n"
      "v_mfma_f32_16x16x32_bf16 v[132:135], a[204:207], v[112:115], v[132:135]\n"
      "v_exp_f32 v156, v156\n"
      "v_mfma_f32_16x16x32_bf16 v[132:135], a[208:211], v[116:119], v[132:135]\n"
      "s_add_u32 s42, s42, 165888\n"
      "v_fma_f32 v156, v156, v177, v177\n"
      "v_mfma_f32_16x16x32_bf16 v[132:135], a[212:215], v[120:123], v[132:135]\n"
      "s_waitcnt vmcnt(3)\n"
      "ds_write_b32 v13, v11\n"
      "v_mfma_f32_16x16x32_bf16 v[128:131], a[72:75], v[52:55], 0\n"
      "v_exp_f32 v159, v159\n"
      "v_mfma_f32_16x16x32_bf16 v[128:131], a[76:79], v[56:59], v[128:131]\n"
      "s_waitcnt vmcnt(2)\n"
      "v_fma_f32 v159, v159, v177, v177\n"
      "v_cmp_ne_u32 vcc, s21, v18\n"
      "v_mfma_f32_16x16x32_bf16 v[128:131], a[80:83], v[60:63], v[128:131]\n"
      "v_mul_f32 v161, v133, v133\n"
      "v_fma_f32 v161, v161, v179, v178\n"
      "v_mfma_f32_16x16x32_bf16 v[128:131], a[84:87], v[64:67], v[128:131]\n"
      "v_mul_f32 v163, v135, v135\n"
      "v_mul_f32 v161, v133, v161\n"
      "v_mfma_f32_16x16x32_bf16 v[128:131], a[88:91], v[68:71], v[128:131]\n"
      "v_mul_f32 v160, v132, v132\n"
      "v_fma_f32 v163, v163, v179, v178\n"
      "v_mfma_f32_16x16x32_bf16 v[128:131], a[92:95], v[72:75], v[128:131]\n"
      "s_cbranch_vccnz .Lsp_g0_6\n"
      ".Lbk_g0_6:\n"
      "v_rcp_f32 v156, v156\n"
      "v_mfma_f32_16x16x32_bf16 v[128:131], a[96:99], v[76:79], v[128:131]\n"
      "buffer_load_dwordx4 v0, s[8:11], s40 offen offset:0 sc1 lds\n"
      "v_mfma_f32_16x16x32_bf16 v[128:131], a[100:103], v[80:83], v[128:131]\n"
      "v_rcp_f32 v159, v159\n"
      "v_mfma_f32_16x16x32_bf16 v[128:131], a[104:107], v[84:87], v[128:131]\n"
      "ds_read_b32 v12, v14\n"
      "v_fma_f32 v160, v160, v179, v178\n"
      "v_mfma_f32_16x16x32_bf16 v[128:131], a[108:111], v[88:91], v[128:131]\n"
      "s_add_u32 s36, s36, 24576\n"
      "v_mul_f32 v162, v134, v134\n"
      "v_mul_f32 v163, v135, v163\n"
      "v_mfma_f32_16x16x32_bf16 v[128:131], a[112:115], v[92:95], v[128:131]\n"
      "v_exp_f32 v163, v163\n"
      "v_mfma_f32_16x16x32_bf16 v[128:131], a[116:119], v[96:99], v[128:131]\n"
      "buffer_load_dwordx4 v0, s[8:11], s40 offen offset:1024 sc1 lds\n"
      "v_mfma_f32_16x16x32_bf16 v[128:131], a[120:123], v[100:103], v[128:131]\n"
      "v_rcp_f32 v158, v158\n"
      "v_mfma_f32_16x16x32_bf16 v[128:131], a[124:127], v[104:107], v[128:131]\n"
      "s_waitcnt lgkmcnt(0)\n"
      "v_mul_f32 v160, v132, v160\n"
      "v_cmp_gt_i32 vcc, v11, v12\n"
      "v_mfma_f32_16x16x32_bf16 v[128:131], a[128:131], v[108:111], v[128:131]\n"
      "v_fma_f32 v162, v162, v179, v178\n"
      "v_mul_f32 v162, v134, v162\n"
      "v_mfma_f32_16x16x32_bf16 v[128:131], a[132:135], v[112:115], v[128:131]\n"
      "buffer_load_dwordx4 v0, s[8:11], s40 offen offset:2048 sc1 lds\n"
      "v_mfma_f32_16x16x32_bf16 v[128:131], a[136:139], v[116:119], v[128:131]\n"
      "v_exp_f32 v162, v162\n"
      "v_mfma_f32_16x16x32_bf16 v[128:131], a[140:143], v[120:123], v[128:131]\n"
      "s_cbranch_vccnz .Lsp_gy0_7\n"
      ".Lbk_gy0_7:\n"
      "ds_read2st64_b64 v[168:171], v4 offset0:80 offset1:81\n"
      "v_mfma_f32_16x16x32_bf16 v[136:139], a[216:219], v[52:55], 0\n"
      "ds_read_b128 v[52:55], v17 offset:0\n"
      "v_fma_f32 v163, v163, v177, v177\n"
      "v_mfma_f32_16x16x32_bf16 v[136:139], a[220:223], v[56:59], v[136:139]\n"
      "v_rcp_f32 v157, v157\n"
      "v_mfma_f32_16x16x32_bf16 v[136:139], a[224:227], v[60:63], v[136:139]\n"
      "buffer_load_dwordx4 v0, s[8:11], s40 offen offset:3072 sc1 lds\n"
      "v_mfma_f32_16x16x32_bf16 v[136:139], a[228:231], v[64:67], v[136:139]\n"
      "s_add_u32 m0, s39, 61440\n"
      "ds_read_b128 v[64:67], v17 offset:3072\n"
      "v_mul_f32 v158, v126, v158\n"
      "v_mfma_f32_16x16x32_bf16 v[136:139], a[232:235], v[68:71], v[136:139]\n"
      "s_min_u32 s36, s36, s6\n"
      "v_rcp_f32 v163, v163\n"
      "v_mfma_f32_16x16x32_bf16 v[136:139], a[236:239], v[72:75], v[136:139]\n"
      "ds_read_b128 v[56:59], v17 offset:1024\n"
      "v_mul_f32 v157, v125, v157\n"
      "v_mfma_f32_16x16x32_bf16 v[136:139], a[240:243], v[76:79], v[136:139]\n"
      "buffer_load_dwordx4 v1, s[8:11], s40 offen offset:0 sc1 lds\n"
      "v_mfma_f32_16x16x32_bf16 v[136:139], a[244:247], v[80:83], v[136:139]\n"
      "v_exp_f32 v161, v161\n"
      "v_mfma_f32_16x16x32_bf16 v[136:139], a[248:251], v[84:87], v[136:139]\n"
      "ds_read_b128 v[68:71], v17 offset:4096\n"
      "ds_read_b128 v[76:79], v17 offset:6144\n"
      "v_mfma_f32_16x16x32_bf16 v[136:139], a[252:255], v[88:91], v[136:139]\n"
      "v_exp_f32 v160, v160\n"
      "v_mfma_f32_16x16x32_bf16 v[136:139], v[20:23], v[92:95], v[136:139]\n"
      "ds_read_b128 v[72:75], v17 offset:5120\n"
      "v_mul_f32 v129, v129, v157\n"
      "v_mfma_f32_16x16x32_bf16 v[136:139], v[24:27], v[96:99], v[136:139]\n"
      "buffer_load_dword v18, v2, s[8:11], s36 offen sc1\n"
      "v_mfma_f32_16x16x32_bf16 v[136:139], v[28:31], v[100:103], v[136:139]\n"
      "ds_read_b128 v[60:63], v17 offset:2048\n"
      "v_fma_f32 v161, v161, v177, v177\n"
      "v_mfma_f32_16x16x32_bf16 v[136:139], v[32:35], v[104:107], v[136:139]\n"
      "s_waitcnt vmcnt(6) lgkmcnt(4)\n"
      "buffer_store_dword v15, v3, s[8:11], s42 offen sc1\n"
      "v_mfma_f32_16x16x32_bf16 v[136:139], v[36:39], v[108:111], v[136:139]\n"
      "ds_read_b128 v[84:87], v17 offset:8192\n"
      "v_fma_f32 v160, v160, v177, v177\n"
      "v_mfma_f32_16x16x32_bf16 v[136:139], v[40:43], v[112:115], v[136:139]\n"
      "v_rcp_f32 v161, v161\n"
      "v_mfma_f32_16x16x32_bf16 v[136:139], v[44:47], v[116:119], v[136:139]\n"
      "ds_read_b128 v[80:83], v17 offset:7168\n"
      "v_mul_f32 v159, v127, v159\n"
      "v_mfma_f32_16x16x32_bf16 v[136:139], v[48:51], v[120:123], v[136:139]\n"
      "ds_read_b128 v[96:99], v17 offset:11264\n"
      "v_mul_f32 v156, v124, v156\n"
      "v_mfma_f32_16x16x32_bf16 v[124:127], a[0:3], v[52:55], 0\n"
      "ds_read_b128 v[92:95], v17 offset:10240\n"
      "v_add_f32 v169, v169, v171\n"
      "v_mfma_f32_16x16x32_bf16 v[124:127], a[4:7], v[56:59], v[124:127]\n"
      "s_waitcnt lgkmcnt(4)\n"
      "ds_read_b128 v[88:91], v17 offset:9216\n"
      "v_mul_f32 v161, v133, v161\n"
      "v_mfma_f32_16x16x32_bf16 v[124:127], a[8:11], v[60:63], v[124:127]\n"
      "ds_read_b128 v[112:115], v17 offset:15360\n"
      "v_mul_f32 v163, v135, v163\n"
      "v_mfma_f32_16x16x32_bf16 v[124:127], a[12:15], v[64:67], v[124:127]\n"
      "ds_read_b128 v[100:103], v17 offset:12288\n"
      "v_mul_f32 v139, v139, v163\n"
      "v_mfma_f32_16x16x32_bf16 v[124:127], a[16:19], v[68:71], v[124:127]\n"
      "ds_read_b128 v[104:107], v17 offset:13312\n"
      "v_mul_f32 v131, v131, v159\n"
      "v_mfma_f32_16x16x32_bf16 v[124:127], a[20:23], v[72:75], v[124:127]\n"
      "ds_read_b128 v[108:111], v17 offset:14336\n"
      "v_mfma_f32_16x16x32_bf16 v[124:127], a[24:27], v[76:79], v[124:127]\n"
      "s_waitcnt lgkmcnt(6)\n"
      "ds_read_b128 v[120:123], v17 offset:17408\n"
      "v_sub_f32 v176, -v168, v170\n"
      "v_mfma_f32_16x16x32_bf16 v[124:127], a[28:31], v[80:83], v[124:127]\n"
      "v_rcp_f32 v160, v160\n"
      "v_mfma_f32_16x16x32_bf16 v[124:127], a[32:35], v[84:87], v[124:127]\n"
      "s_waitcnt lgkmcnt(5)\n"
      "ds_read_b128 v[116:119], v17 offset:16384\n"
      "v_fma_f32 v169, -v176, v176, v169\n"
      "v_mfma_f32_16x16x32_bf16 v[124:127], a[36:39], v[88:91], v[124:127]\n"
      "v_mul_f32 v160, v132, v160\n"
      "v_fma_f32 v162, v162, v177, v177\n"
      "v_mfma_f32_16x16x32_bf16 v[124:127], a[40:43], v[92:95], v[124:127]\n"
      "v_mul_f32 v130, v130, v158\n"
      "v_mul_f32 v128, v128, v156\n"
      "v_mfma_f32_16x16x32_bf16 v[124:127], a[44:47], v[96:99], v[124:127]\n"
      "s_waitcnt lgkmcnt(3)\n"
      "v_rcp_f32 v162, v162\n"
      "v_mfma_f32_16x16x32_bf16 v[124:127], a[48:51], v[100:103], v[124:127]\n"
      "v_cvt_pk_bf16_f32 v165, v130, v131\n"
      "v_mul_f32 v162, v134, v162\n"
      "v_mfma_f32_16x16x32_bf16 v[124:127], a[52:55], v[104:107], v[124:127]\n"
      "s_waitcnt lgkmcnt(1)\n"
      "v_mul_f32 v136, v136, v160\n"
      "v_mul_f32 v138, v138, v162\n"
      "v_mfma_f32_16x16x32_bf16 v[124:127], a[56:59], v[108:111], v[124:127]\n"
      "v_mul_f32 v137, v137, v161\n"
      "v_cvt_pk_bf16_f32 v167, v138, v139\n"
      "v_mfma_f32_16x16x32_bf16 v[124:127], a[60:63], v[112:115], v[124:127]\n"
      "s_waitcnt lgkmcnt(0)\n"
      "v_max_f32 v177, 0x3727c5ac, v169\n"
      "v_mfma_f32_16x16x32_bf16 v[124:127], a[64:67], v[116:119], v[124:127]\n"
      "s_add_u32 s37, s37, 165888\n"
      "v_cvt_pk_bf16_f32 v164, v128, v129\n"
      "v_mfma_f32_16x16x32_bf16 v[124:127], a[68:71], v[120:123], v[124:127]\n"
      "v_cvt_pk_bf16_f32 v166, v136, v137\n"
      "v_add_u32 v11, 1, v11\n"
      "v_mfma_f32_16x16x32_bf16 v[132:135], a[144:147], v[52:55], 0\n"
      "buffer_store_dwordx4 v[164:167], v0, s[8:11], s37 offen sc1\n"
      "v_mfma_f32_16x16x32_bf16 v[132:135], a[148:151], v[56:59], v[132:135]\n"
      "v_rsq_f32 v170, v177\n"
      "v_mfma_f32_16x16x32_bf16 v[132:135], a[152:155], v[60:63], v[132:135]\n"
      "v_mul_f32 v178, 0xc0135761, v170\n"
      "v_mul_f32 v170, v170, v170\n"
      "v_mfma_f32_16x16x32_bf16 v[132:135], a[156:159], v[64:67], v[132:135]\n"
      "v_mul_f32 v171, 0x3d372713, v178\n"
      "v_mul_f32 v157, v125, v125\n"
      "v_mfma_f32_16x16x32_bf16 v[132:135], a[160:163], v[68:71], v[132:135]\n"
      "v_mul_f32 v179, v171, v170\n"
      "v_mul_f32 v158, v126, v126\n"
      "v_mfma_f32_16x16x32_bf16 v[132:135], a[164:167], v[72:75], v[132:135]\n"
      "v_fma_f32 v157, v157, v179, v178\n"
      "s_mov_b32 s40, s36\n"
      "v_mfma_f32_16x16x32_bf16 v[132:135], a[168:171], v[76:79], v[132:135]\n"
      "v_mul_f32 v157, v125, v157\n"
      "s_add_u32 m0, s38, 0\n"
      "v_mfma_f32_16x16x32_bf16 v[132:135], a[172:175], v[80:83], v[132:135]\n"
      "v_exp_f32 v157, v157\n"
      "v_mfma_f32_16x16x32_bf16 v[132:135], a[176:179], v[84:87], v[132:135]\n"
      "v_fma_f32 v158, v158, v179, v178\n"
      "v_fma_f32 v157, v157, v177, v177\n"
      "v_mfma_f32_16x16x32_bf16 v[132:135], a[180:183], v[88:91], v[132:135]\n"
      "v_mfma_f32_16x16x32_bf16 v[132:135], a[184:187], v[92:95], v[132:135]\n"
      "v_mul_f32 v156, v124, v124\n"
      "v_mul_f32 v158, v126, v158\n"
      "v_mfma_f32_16x16x32_bf16 v[132:135], a[188:191], v[96:99], v[132:135]\n"
      "v_mul_f32 v159, v127, v127\n"
      "v_fma_f32 v156, v156, v179, v178\n"
      "v_mfma_f32_16x16x32_bf16 v[132:135], a[192:195], v[100:103], v[132:135]\n"
      "v_fma_f32 v159, v159, v179, v178\n"
      "v_mul_f32 v156, v124, v156\n"
      "v_mfma_f32_16x16x32_bf16 v[132:135], a[196:199], v[104:107], v[132:135]\n"
      "v_exp_f32 v158, v158\n"
      "v_mfma_f32_16x16x32_bf16 v[132:135], a[200:203], v[108:111], v[132:135]\n"
      "v_fma_f32 v158, v158, v177, v177\n"
      "v_mul_f32 v159, v127, v159\n"
      "v_mfma_f32_16x16x32_bf16 v[132:135], a[204:207], v[112:115], v[132:135]\n"
      "v_exp_f32 v156, v156\n"
      "v_mfma_f32_16x16x32_bf16 v[132:135], a[208:211], v[116:119], v[132:135]\n"
      "s_add_u32 s42, s42, 165888\n"
      "v_fma_f32 v156, v156, v177, v177\n"
      "v_mfma_f32_16x16x32_bf16 v[132:135], a[212:215], v[120:123], v[132:135]\n"
      "s_waitcnt vmcnt(3)\n"
      "ds_write_b32 v13, v11\n"
      "v_mfma_f32_16x16x32_bf16 v[128:131], a[72:75], v[52:55], 0\n"
      "v_exp_f32 v159, v159\n"
      "v_mfma_f32_16x16x32_bf16 v[128:131], a[76:79], v[56:59], v[128:131]\n"
      "s_waitcnt vmcnt(2)\n"
      "v_fma_f32 v159, v159, v177, v177\n"
      "v_cmp_ne_u32 vcc, s21, v18\n"
      "v_mfma_f32_16x16x32_bf16 v[128:131], a[80:83], v[60:63], v[128:131]\n"
      "v_mul_f32 v161, v133, v133\n"
      "v_fma_f32 v161, v161, v179, v178\n"
      "v_mfma_f32_16x16x32_bf16 v[128:131], a[84:87], v[64:67], v[128:131]\n"
      "v_mul_f32 v163, v135, v135\n"
      "v_mul_f32 v161, v133, v161\n"
      "v_mfma_f32_16x16x32_bf16 v[128:131], a[88:91], v[68:71], v[128:131]\n"
      "v_mul_f32 v160, v132, v132\n"
      "v_fma_f32 v163, v163, v179, v178\n"
      "v_mfma_f32_16x16x32_bf16 v[128:131], a[92:95], v[72:75], v[128:131]\n"
      "s_cbranch_vccnz .Lsp_g0_8\n"
      ".Lbk_g0_8:\n"
      "v_rcp_f32 v156, v156\n"
      "v_mfma_f32_16x16x32_bf16 v[128:131], a[96:99], v[76:79], v[128:131]\n"
      "buffer_load_dwordx4 v0, s[8:11], s40 offen offset:0 sc1 lds\n"
      "v_mfma_f32_16x16x32_bf16 v[128:131], a[100:103], v[80:83], v[128:131]\n"
      "v_rcp_f32 v159, v159\n"
      "v_mfma_f32_16x16x32_bf16 v[128:131], a[104:107], v[84:87], v[128:131]\n"
      "ds_read_b32 v12, v14\n"
      "v_fma_f32 v160, v160, v179, v178\n"
      "v_mfma_f32_16x16x32_bf16 v[128:131], a[108:111], v[88:91], v[128:131]\n"
      "s_add_u32 s36, s36, 24576\n"
      "v_mul_f32 v162, v134, v134\n"
      "v_mul_f32 v163, v135, v163\n"
      "v_mfma_f32_16x16x32_bf16 v[128:131], a[112:115], v[92:95], v[128:131]\n"
      "v_exp_f32 v163, v163\n"
      "v_mfma_f32_16x16x32_bf16 v[128:131], a[116:119], v[96:99], v[128:131]\n"
      "buffer_load_dwordx4 v0, s[8:11], s40 offen offset:1024 sc1 lds\n"
      "v_mfma_f32_16x16x32_bf16 v[128:131], a[120:123], v[100:103], v[128:131]\n"
      "v_rcp_f32 v158, v158\n"
      "v_mfma_f32_16x16x32_bf16 v[128:131], a[124:127], v[104:107], v[128:131]\n"
      "s_waitcnt lgkmcnt(0)\n"
      "v_mul_f32_e64 v160, v132, v160\n"
      "v_cmp_gt_i32 vcc, v11, v12\n"
      "v_mfma_f32_16x16x32_bf16 v[128:131], a[128:131], v[108:111], v[128:131]\n"
      "v_fma_f32 v162, v162, v179, v178\n"
      "v_mul_f32_e64 v162, v134, v162\n"
      "v_mfma_f32_16x16x32_bf16 v[128:131], a[132:135], v[112:115], v[128:131]\n"
      "buffer_load_dwordx4 v0, s[8:11], s40 offen offset:2048 sc1 lds\n"
      "v_mfma_f32_16x16x32_bf16 v[128:131], a[136:139], v[116:119], v[128:131]\n"
      "v_exp_f32_e64 v162, v162\n"
      "v_mfma_f32_16x16x32_bf16 v[128:131], a[140:143], v[120:123], v[128:131]\n"
      "s_cbranch_vccnz .Lsp_gy0_9\n"
      ".Lbk_gy0_9:\n"
      "ds_read2st64_b64 v[168:171], v4 offset0:120 offset1:121\n"
      "v_mfma_f32_16x16x32_bf16 v[136:139], a[216:219], v[52:55], 0\n"
      "ds_read_b128 v[52:55], v17 offset:20480\n"
      "v_fma_f32 v163, v163, v177, v177\n"
      "v_mfma_f32_16x16x32_bf16 v[136:139], a[220:223], v[56:59], v[136:139]\n"
      "v_rcp_f32 v157, v157\n"
      "v_mfma_f32_16x16x32_bf16 v[136:139], a[224:227], v[60:63], v[136:139]\n"
      "buffer_load_dwordx4 v0, s[8:11], s40 offen offset:3072 sc1 lds\n"
      "v_mfma_f32_16x16x32_bf16 v[136:139], a[228:231], v[64:67], v[136:139]\n"
      "s_add_u32 m0, s39, 0\n"
      "ds_read_b128 v[64:67], v17 offset:23552\n"
      "v_mul_f32 v158, v126, v158\n"
      "v_mfma_f32_16x16x32_bf16 v[136:139], a[232:235], v[68:71], v[136:139]\n"
      "s_min_u32 s36, s36, s6\n"
      "v_rcp_f32 v163, v163\n"
      "v_mfma_f32_16x16x32_bf16 v[136:139], a[236:239], v[72:75], v[136:139]\n"
      "ds_read_b128 v[56:59], v17 offset:21504\n"
      "v_mul_f32 v157, v125, v157\n"
      "v_mfma_f32_16x16x32_bf16 v[136:139], a[240:243], v[76:79], v[136:139]\n"
      "buffer_load_dwordx4 v1, s[8:11], s40 offen offset:0 sc1 lds\n"
      "v_mfma_f32_16x16x32_bf16 v[136:139], a[244:247], v[80:83], v[136:139]\n"
      "v_exp_f32 v161, v161\n"
      "v_mfma_f32_16x16x32_bf16 v[136:139], a[248:251], v[84:87], v[136:139]\n"
      "ds_read_b128 v[68:71], v17 offset:24576\n"
      "ds_read_b128 v[76:79], v17 offset:26624\n"
      "v_mfma_f32_16x16x32_bf16 v[136:139], a[252:255], v[88:91], v[136:139]\n"
      "v_exp_f32 v160, v160\n"
      "v_mfma_f32_16x16x32_bf16 v[136:139], v[20:23], v[92:95], v[136:139]\n"
      "ds_read_b128 v[72:75], v17 offset:25600\n"
      "v_mul_f32 v129, v129, v157\n"
      "v_mfma_f32_16x16x32_bf16 v[136:139], v[24:27], v[96:99], v[136:139]\n"
      "buffer_load_dword v18, v2, s[8:11], s36 offen sc1\n"
      "v_mfma_f32_16x16x32_bf16 v[136:139], v[28:31], v[100:103], v[136:139]\n"
      "ds_read_b128 v[60:63], v17 offset:22528\n"
      "v_fma_f32 v161, v161, v177, v177\n"
      "v_mfma_f32_16x16x32_bf16 v[136:139], v[32:35], v[104:107], v[136:139]\n"
      "s_waitcnt vmcnt(6) lgkmcnt(4)\n"
      "buffer_store_dword v15, v3, s[8:11], s42 offen sc1\n"
      "v_mfma_f32_16x16x32_bf16 v[136:139], v[36:39], v[108:111], v[136:139]\n"
      "ds_read_b128 v[84:87], v17 offset:28672\n"
      "v_fma_f32 v160, v160, v177, v177\n"
      "v_mfma_f32_16x16x32_bf16 v[136:139], v[40:43], v[112:115], v[136:139]\n"
      "v_rcp_f32 v161, v161\n"
      "v_mfma_f32_16x16x32_bf16 v[136:139], v[44:47], v[116:119], v[136:139]\n"
      "ds_read_b128 v[80:83], v17 offset:27648\n"
      "v_mul_f32 v159, v127, v159\n"
      "v_mfma_f32_16x16x32_bf16 v[136:139], v[48:51], v[120:123], v[136:139]\n"
      "ds_read_b128 v[96:99], v17 offset:31744\n"
      "v_mul_f32 v156, v124, v156\n"
      "v_mfma_f32_16x16x32_bf16 v[124:127], a[0:3], v[52:55], 0\n"
      "ds_read_b128 v[92:95], v17 offset:30720\n"
      "v_add_f32 v169, v169, v171\n"
      "v_mfma_f32_16x16x32_bf16 v[124:127], a[4:7], v[56:59], v[124:127]\n"
      "s_waitcnt lgkmcnt(4)\n"
      "ds_read_b128 v[88:91], v17 offset:29696\n"
      "v_mul_f32 v161, v133, v161\n"
      "v_mfma_f32_16x16x32_bf16 v[124:127], a[8:11], v[60:63], v[124:127]\n"
      "ds_read_b128 v[112:115], v17 offset:35840\n"
      "v_mul_f32 v163, v135, v163\n"
      "v_mfma_f32_16x16x32_bf16 v[124:127], a[12:15], v[64:67], v[124:127]\n"
      ".Lg_p1:\n"
      "s_cbranch_execz .Lg_pr\n"
      "ds_read_b128 v[100:103], v17 offset:32768\n"
      "v_mul_f32 v139, v139, v163\n"
      "v_mfma_f32_16x16x32_bf16 v[124:127], a[16:19], v[68:71], v[124:127]\n"
      "ds_read_b128 v[104:107], v17 offset:33792\n"
      "v_mul_f32 v131, v131, v159\n"
      "v_mfma_f32_16x16x32_bf16 v[124:127], a[20:23], v[72:75], v[124:127]\n"
      "ds_read_b128 v[108:111], v17 offset:34816\n"
      "v_mfma_f32_16x16x32_bf16 v[124:127], a[24:27], v[76:79], v[124:127]\n"
      "s_waitcnt lgkmcnt(6)\n"
      "ds_read_b128 v[120:123], v17 offset:37888\n"
      "v_sub_f32 v176, -v168, v170\n"
      "v_mfma_f32_16x16x32_bf16 v[124:127], a[28:31], v[80:83], v[124:127]\n"
      "v_rcp_f32 v160, v160\n"
      "v_mfma_f32_16x16x32_bf16 v[124:127], a[32:35], v[84:87], v[124:127]\n"
      "s_waitcnt lgkmcnt(5)\n"
      "ds_read_b128 v[116:119], v17 offset:36864\n"
      "v_fma_f32 v169, -v176, v176, v169\n"
      "v_mfma_f32_16x16x32_bf16 v[124:127], a[36:39], v[88:91], v[124:127]\n"
      "v_mul_f32 v160, v132, v160\n"
      "v_fma_f32 v162, v162, v177, v177\n"
      "v_mfma_f32_16x16x32_bf16 v[124:127], a[40:43], v[92:95], v[124:127]\n"
      "v_mul_f32 v130, v130, v158\n"
      "v_mul_f32 v128, v128, v156\n"
      "v_mfma_f32_16x16x32_bf16 v[124:127], a[44:47], v[96:99], v[124:127]\n"
      "s_waitcnt lgkmcnt(3)\n"
      "v_rcp_f32 v162, v162\n"
      "v_mfma_f32_16x16x32_bf16 v[124:127], a[48:51], v[100:103], v[124:127]\n"
      "v_cvt_pk_bf16_f32 v165, v130, v131\n"
      "v_mul_f32 v162, v134, v162\n"
      "v_mfma_f32_16x16x32_bf16 v[124:127], a[52:55], v[104:107], v[124:127]\n"
      "s_waitcnt lgkmcnt(1)\n"
      "v_mul_f32 v136, v136, v160\n"
      "v_mul_f32 v138, v138, v162\n"
      "v_mfma_f32_16x16x32_bf16 v[124:127], a[56:59], v[108:111], v[124:127]\n"
      "v_mul_f32 v137, v137, v161\n"
      "v_cvt_pk_bf16_f32 v167, v138, v139\n"
      "v_mfma_f32_16x16x32_bf16 v[124:127], a[60:63], v[112:115], v[124:127]\n"
      "s_waitcnt lgkmcnt(0)\n"
      "v_max_f32 v177, 0x3727c5ac, v169\n"
      "v_mfma_f32_16x16x32_bf16 v[124:127], a[64:67], v[116:119], v[124:127]\n"
      "s_add_u32 s37, s37, 165888\n"
      "v_cvt_pk_bf16_f32 v164, v128, v129\n"
      "v_mfma_f32_16x16x32_bf16 v[124:127], a[68:71], v[120:123], v[124:127]\n"
      "v_cvt_pk_bf16_f32 v166, v136, v137\n"
      "v_add_u32 v11, 1, v11\n"
      "v_mfma_f32_16x16x32_bf16 v[132:135], a[144:147], v[52:55], 0\n"
      "buffer_store_dwordx4 v[164:167], v0, s[8:11], s37 offen sc1\n"
      "v_mfma_f32_16x16x32_bf16 v[132:135], a[148:151], v[56:59], v[132:135]\n"
      "v_rsq_f32 v170, v177\n"
      "v_mfma_f32_16x16x32_bf16 v[132:135], a[152:155], v[60:63], v[132:135]\n"
      "v_mul_f32 v178, 0xc0135761, v170\n"
      "v_mul_f32 v170, v170, v170\n"
      "v_mfma_f32_16x16x32_bf16 v[132:135], a[156:159], v[64:67], v[132:135]\n"
      "v_mul_f32 v171, 0x3d372713, v178\n"
      "v_mul_f32 v157, v125, v125\n"
      "v_mfma_f32_16x16x32_bf16 v[132:135], a[160:163], v[68:71], v[132:135]\n"
      "v_mul_f32 v179, v171, v170\n"
      "v_mul_f32 v158, v126, v126\n"
      "v_mfma_f32_16x16x32_bf16 v[132:135], a[164:167], v[72:75], v[132:135]\n"
      "v_fma_f32 v157, v157, v179, v178\n"
      "s_mov_b32 s40, s36\n"
      "v_mfma_f32_16x16x32_bf16 v[132:135], a[168:171], v[76:79], v[132:135]\n"
      "v_mul_f32 v157, v125, v157\n"
      "s_add_u32 m0, s38, 20480\n"
      "v_mfma_f32_16x16x32_bf16 v[132:135], a[172:175], v[80:83], v[132:135]\n"
      "v_exp_f32 v157, v157\n"
      "v_mfma_f32_16x16x32_bf16 v[132:135], a[176:179], v[84:87], v[132:135]\n"
      "v_fma_f32 v158, v158, v179, v178\n"
      "v_fma_f32 v157, v157, v177, v177\n"
      "v_mfma_f32_16x16x32_bf16 v[132:135], a[180:183], v[88:91], v[132:135]\n"
      "v_mfma_f32_16x16x32_bf16 v[132:135], a[184:187], v[92:95], v[132:135]\n"
      "v_mul_f32 v156, v124, v124\n"
      "v_mul_f32 v158, v126, v158\n"
      "v_mfma_f32_16x16x32_bf16 v[132:135], a[188:191], v[96:99], v[132:135]\n"
      "v_mul_f32 v159, v127, v127\n"
      "v_fma_f32 v156, v156, v179, v178\n"
      "v_mfma_f32_16x16x32_bf16 v[132:135], a[192:195], v[100:103], v[132:135]\n"
      "v_fma_f32 v159, v159, v179, v178\n"
      "v_mul_f32 v156, v124, v156\n"
      "v_mfma_f32_16x16x32_bf16 v[132:135], a[196:199], v[104:107], v[132:135]\n"
      "v_exp_f32 v158, v158\n"
      "v_mfma_f32_16x16x32_bf16 v[132:135], a[200:203], v[108:111], v[132:135]\n"
      "v_fma_f32 v158, v158, v177, v177\n"
      "v_mul_f32 v159, v127, v159\n"
      "v_mfma_f32_16x16x32_bf16 v[132:135], a[204:207], v[112:115], v[132:135]\n"
      "v_exp_f32 v156, v156\n"
      "v_mfma_f32_16x16x32_bf16 v[132:135], a[208:211], v[116:119], v[132:135]\n"
      "s_add_u32 s42, s42, 165888\n"
      "v_fma_f32 v156, v156, v177, v177\n"
      "v_mfma_f32_16x16x32_bf16 v[132:135], a[212:215], v[120:123], v[132:135]\n"
      "s_waitcnt vmcnt(3)\n"
      "ds_write_b32 v13, v11\n"
      "v_mfma_f32_16x16x32_bf16 v[128:131], a[72:75], v[52:55], 0\n"
      "v_exp_f32 v159, v159\n"
      "v_mfma_f32_16x16x32_bf16 v[128:131], a[76:79], v[56:59], v[128:131]\n"
      "s_waitcnt vmcnt(2)\n"
      "v_fma_f32 v159, v159, v177, v177\n"
      "v_cmp_ne_u32 vcc, s21, v18\n"
      "v_mfma_f32_16x16x32_bf16 v[128:131], a[80:83], v[60:63], v[128:131]\n"
      "v_mul_f32 v161, v133, v133\n"
      "v_fma_f32 v161, v161, v179, v178\n"
      "v_mfma_f32_16x16x32_bf16 v[128:131], a[84:87], v[64:67], v[128:131]\n"
      "v_mul_f32 v163, v135, v135\n"
      "v_mul_f32 v161, v133, v161\n"
      "v_mfma_f32_16x16x32_bf16 v[128:131], a[88:91], v[68:71], v[128:131]\n"
      "v_mul_f32 v160, v132, v132\n"
      "v_fma_f32 v163, v163, v179, v178\n"
      "v_mfma_f32_16x16x32_bf16 v[128:131], a[92:95], v[72:75], v[128:131]\n"
      "s_cbranch_vccnz .Lsp_g0_10\n"
      ".Lbk_g0_10:\n"
      "v_rcp_f32 v156, v156\n"
      "v_mfma_f32_16x16x32_bf16 v[128:131], a[96:99], v[76:79], v[128:131]\n"
      "buffer_load_dwordx4 v0, s[8:11], s40 offen offset:0 sc1 lds\n"
      "v_mfma_f32_16x16x32_bf16 v[128:131], a[100:103], v[80:83], v[128:131]\n"
      "v_rcp_f32 v159, v159\n"
      "v_mfma_f32_16x16x32_bf16 v[128:131], a[104:107], v[84:87], v[128:131]\n"
      "ds_read_b32 v12, v14\n"
      "v_fma_f32 v160, v160, v179, v178\n"
      "v_mfma_f32_16x16x32_bf16 v[128:131], a[108:111], v[88:91], v[128:131]\n"
      "s_add_u32 s36, s36, 24576\n"
      "v_mul_f32 v162, v134, v134\n"
      "v_mul_f32 v163, v135, v163\n"
      "v_mfma_f32_16x16x32_bf16 v[128:131], a[112:115], v[92:95], v[128:131]\n"
      "v_exp_f32 v163, v163\n"
      "v_mfma_f32_16x16x32_bf16 v[128:131], a[116:119], v[96:99], v[128:131]\n"
      "buffer_load_dwordx4 v0, s[8:11], s40 offen offset:1024 sc1 lds\n"
      "v_mfma_f32_16x16x32_bf16 v[128:131], a[120:123], v[100:103], v[128:131]\n"
      "v_rcp_f32 v158, v158\n"
      "v_mfma_f32_16x16x32_bf16 v[128:131], a[124:127], v[104:107], v[128:131]\n"
      "s_waitcnt lgkmcnt(0)\n"
      "v_mul_f32 v160, v132, v160\n"
      "v_cmp_gt_i32 vcc, v11, v12\n"
      "v_mfma_f32_16x16x32_bf16 v[128:131], a[128:131], v[108:111], v[128:131]\n"
      "v_fma_f32 v162, v162, v179, v178\n"
      "v_mul_f32 v162, v134, v162\n"
      "v_mfma_f32_16x16x32_bf16 v[128:131], a[132:135], v[112:115], v[128:131]\n"
      "buffer_load_dwordx4 v0, s[8:11], s40 offen offset:2048 sc1 lds\n"
      "v_mfma_f32_16x16x32_bf16 v[128:131], a[136:139], v[116:119], v[128:131]\n"
      "v_exp_f32 v162, v162\n"
      "v_mfma_f32_16x16x32_bf16 v[128:131], a[140:143], v[120:123], v[128:131]\n"
      "s_cbranch_vccnz .Lsp_gy0_11\n"
      ".Lbk_gy0_11:\n"
      "ds_read2st64_b64 v[168:171], v4 offset0:0 offset1:1\n"
      "v_mfma_f32_16x16x32_bf16 v[136:139], a[216:219], v[52:55], 0\n"
      "ds_read_b128 v[52:55], v0 offset:0\n"
      "v_fma_f32 v163, v163, v177, v177\n"
      "v_mfma_f32_16x16x32_bf16 v[136:139], a[220:223], v[56:59], v[136:139]\n"
      "v_rcp_f32 v157, v157\n"
      "v_mfma_f32_16x16x32_bf16 v[136:139], a[224:227], v[60:63], v[136:139]\n"
      "buffer_load_dwordx4 v0, s[8:11], s40 offen offset:3072 sc1 lds\n"
      "v_mfma_f32_16x16x32_bf16 v[136:139], a[228:231], v[64:67], v[136:139]\n"
      "s_add_u32 m0, s39, 20480\n"
      "ds_read_b128 v[64:67], v0 offset:3072\n"
      "v_mul_f32 v158, v126, v158\n"
      "v_mfma_f32_16x16x32_bf16 v[136:139], a[232:235], v[68:71], v[136:139]\n"
      "s_min_u32 s36, s36, s6\n"
      "v_rcp_f32 v163, v163\n"
      "v_mfma_f32_16x16x32_bf16 v[136:139], a[236:239], v[72:75], v[136:139]\n"
      "ds_read_b128 v[56:59], v0 offset:1024\n"
      "v_mul_f32 v157, v125, v157\n"
      "v_mfma_f32_16x16x32_bf16 v[136:139], a[240:243], v[76:79], v[136:139]\n"
      "buffer_load_dwordx4 v1, s[8:11], s40 offen offset:0 sc1 lds\n"
      "v_mfma_f32_16x16x32_bf16 v[136:139], a[244:247], v[80:83], v[136:139]\n"
      "v_exp_f32 v161, v161\n"
      "v_mfma_f32_16x16x32_bf16 v[136:139], a[248:251], v[84:87], v[136:139]\n"
      "ds_read_b128 v[68:71], v0 offset:4096\n"
      "ds_read_b128 v[76:79], v0 offset:6144\n"
      "v_mfma_f32_16x16x32_bf16 v[136:139], a[252:255], v[88:91], v[136:139]\n"
      "v_exp_f32 v160, v160\n"
      "v_mfma_f32_16x16x32_bf16 v[136:139], v[20:23], v[92:95], v[136:139]\n"
      "ds_read_b128 v[72:75], v0 offset:5120\n"
      "v_mul_f32 v129, v129, v157\n"
      "v_mfma_f32_16x16x32_bf16 v[136:139], v[24:27], v[96:99], v[136:139]\n"
      "buffer_load_dword v18, v2, s[8:11], s36 offen sc1\n"
      "v_mfma_f32_16x16x32_bf16 v[136:139], v[28:31], v[100:103], v[136:139]\n"
      "ds_read_b128 v[60:63], v0 offset:2048\n"
      "v_fma_f32 v161, v161, v177, v177\n"
      "v_mfma_f32_16x16x32_bf16 v[136:139], v[32:35], v[104:107], v[136:139]\n"
      "s_waitcnt vmcnt(6) lgkmcnt(4)\n"
      "buffer_store_dword v15, v3, s[8:11], s42 offen sc1\n"
      "v_mfma_f32_16x16x32_bf16 v[136:139], v[36:39], v[108:111], v[136:139]\n"
      "ds_read_b128 v[84:87], v0 offset:8192\n"
      "v_fma_f32 v160, v160, v177, v177\n"
      "v_mfma_f32_16x16x32_bf16 v[136:139], v[40:43], v[112:115], v[136:139]\n"
      "v_rcp_f32 v161, v161\n"
      "v_mfma_f32_16x16x32_bf16 v[136:139], v[44:47], v[116:119], v[136:139]\n"
      "ds_read_b128 v[80:83], v0 offset:7168\n"
      "v_mul_f32 v159, v127, v159\n"
      "v_mfma_f32_16x16x32_bf16 v[136:139], v[48:51], v[120:123], v[136:139]\n"
      "ds_read_b128 v[96:99], v0 offset:11264\n"
      "v_mul_f32 v156, v124, v156\n"
      "s_sub_u32 s20, s20, 1\n"
      "s_cbranch_scc0 .Lg_loop\n"
      ".Lg_tail:\n"
      "v_mul_f32 v161, v133, v161\n"
      "v_mul_f32 v163, v135, v163\n"
      "s_nop 4\n"
      "v_mul_f32 v139, v139, v163\n"
      "v_mul_f32 v131, v131, v159\n"
      "v_rcp_f32 v160, v160\n"
      "s_nop 0\n"
      "v_mul_f32 v160, v132, v160\n"
      "v_fma_f32 v162, v162, v177, v177\n"
      "v_mul_f32 v130, v130, v158\n"
      "v_mul_f32 v128, v128, v156\n"
      "v_rcp_f32 v162, v162\n"
      "v_cvt_pk_bf16_f32 v165, v130, v131\n"
      "v_mul_f32 v162, v134, v162\n"
      "v_mul_f32 v136, v136, v160\n"
      "v_mul_f32 v138, v138, v162\n"
      "v_mul_f32 v137, v137, v161\n"
      "v_cvt_pk_bf16_f32 v167, v138, v139\n"
      "s_add_u32 s37, s37, 165888\n"
      "v_cvt_pk_bf16_f32 v164, v128, v129\n"
      "v_cvt_pk_bf16_f32 v166, v136, v137\n"
      "buffer_store_dwordx4 v[164:167], v0, s[8:11], s37 offen sc1\n"
      "s_add_u32 s42, s42, 165888\n"
      "s_waitcnt vmcnt(0)\n"
      "buffer_store_dword v15, v3, s[8:11], s42 offen sc1\n"
      "s_add_u32 s37, s37, 165888\n"
      "s_add_u32 s42, s42, 165888\n"
      "s_endpgm\n"
      ".Lsp_g0_1:\n"
      "s_mov_b32 s22, s23\n"
      ".Lsl_g0_1:\n"
      "buffer_load_dword v18, v2, s[8:11], s40 offen sc1\n"
      "s_waitcnt vmcnt(0)\n"
      "v_cmp_ne_u32 vcc, s21, v18\n"
      "s_cbranch_vccz .Lbk_g0_1\n"
      "s_sleep 4\n"
      "s_sub_u32 s22, s22, 1\n"
      "s_cbranch_scc0 .Lsl_g0_1\n"
      "s_mov_b32 s23, 0\n"
      "s_branch .Lbk_g0_1\n"
      ".Lsp_g0_2:\n"
      "s_mov_b32 s22, s23\n"
      ".Lsl_g0_2:\n"
      "buffer_load_dword v18, v2, s[8:11], s40 offen sc1\n"
      "s_waitcnt vmcnt(0)\n"
      "v_cmp_ne_u32 vcc, s21, v18\n"
      "s_cbranch_vccz .Lbk_g0_2\n"
      "s_sleep 4\n"
      "s_sub_u32 s22, s22, 1\n"
      "s_cbranch_scc0 .Lsl_g0_2\n"
      "s_mov_b32 s23, 0\n"
      "s_branch .Lbk_g0_2\n"
      ".Lsp_gy0_3:\n"
      "s_lshl_b32 s22, s23, 4\n"
      ".Lsl_gy0_3:\n"
      "ds_read_b32 v12, v14\n"
      "s_waitcnt lgkmcnt(0)\n"
      "v_cmp_gt_i32 vcc, v11, v12\n"
      "s_cbranch_vccz .Lbk_gy0_3\n"
      "s_sub_u32 s22, s22, 1\n"
      "s_cbranch_scc0 .Lsl_gy0_3\n"
      "s_mov_b32 s23, 0\n"
      "s_branch .Lbk_gy0_3\n"
      ".Lsp_g0_4:\n"
      "s_mov_b32 s22, s23\n"
      "buffer_load_dword v18, v2, s[8:11], s40 offen sc1\n"
      "s_waitcnt vmcnt(0)\n"
      "v_cmp_ne_u32 vcc, s21, v18\n"
      "s_add_u32 s30, s42, 0\n"
      "buffer_store_dword v15, v3, s[8:11], s30 offen sc1\n"
      "s_cbranch_vccz .Lbk_g0_4\n"
      ".Lsl_g0_4:\n"
      "buffer_load_dword v18, v2, s[8:11], s40 offen sc1\n"
      "s_waitcnt vmcnt(0)\n"
      "v_cmp_ne_u32 vcc, s21, v18\n"
      "s_cbranch_vccz .Lbk_g0_4\n"
      "s_sleep 4\n"
      "s_sub_u32 s22, s22, 1\n"
      "s_cbranch_scc0 .Lsl_g0_4\n"
      "s_mov_b32 s23, 0\n"
      "s_branch .Lbk_g0_4\n"
      ".Lsp_gy0_5:\n"
      "s_lshl_b32 s22, s23, 4\n"
      ".Lsl_gy0_5:\n"
      "ds_read_b32 v12, v14\n"
      "s_waitcnt lgkmcnt(0)\n"
      "v_cmp_gt_i32 vcc, v11, v12\n"
      "s_cbranch_vccz .Lbk_gy0_5\n"
      "s_sub_u32 s22, s22, 1\n"
      "s_cbranch_scc0 .Lsl_gy0_5\n"
      "s_mov_b32 s23, 0\n"
      "s_branch .Lbk_gy0_5\n"
      ".Lsp_g0_6:\n"
      "s_mov_b32 s22, s23\n"
      "buffer_load_dword v18, v2, s[8:11], s40 offen sc1\n"
      "s_waitcnt vmcnt(0)\n"
      "v_cmp_ne_u32 vcc, s21, v18\n"
      "s_add_u32 s30, s42, 0\n"
      "buffer_store_dword v15, v3, s[8:11], s30 offen sc1\n"
      "s_cbranch_vccz .Lbk_g0_6\n"
      ".Lsl_g0_6:\n"
      "buffer_load_dword v18, v2, s[8:11], s40 offen sc1\n"
      "s_waitcnt vmcnt(0)\n"
      "v_cmp_ne_u32 vcc, s21, v18\n"
      "s_cbranch_vccz .Lbk_g0_6\n"
      "s_sleep 4\n"
      "s_sub_u32 s22, s22, 1\n"
      "s_cbranch_scc0 .Lsl_g0_6\n"
      "s_mov_b32 s23, 0\n"
      "s_branch .Lbk_g0_6\n"
      ".Lsp_gy0_7:\n"
      "s_lshl_b32 s22, s23, 4\n"
      ".Lsl_gy0_7:\n"
      "ds_read_b32 v12, v14\n"
      "s_waitcnt lgkmcnt(0)\n"
      "v_cmp_gt_i32 vcc, v11, v12\n"
      "s_cbranch_vccz .Lbk_gy0_7\n"
      "s_sub_u32 s22, s22, 1\n"
      "s_cbranch_scc0 .Lsl_gy0_7\n"
      "s_mov_b32 s23, 0\n"
      "s_branch .Lbk_gy0_7\n"
      ".Lsp_g0_8:\n"
      "s_mov_b32 s22, s23\n"
      "buffer_load_dword v18, v2, s[8:11], s40 offen sc1\n"
      "s_waitcnt vmcnt(0)\n"
      "v_cmp_ne_u32 vcc, s21, v18\n"
      "s_add_u32 s30, s42, 0\n"
      "buffer_store_dword v15, v3, s[8:11], s30 offen sc1\n"
      "s_cbranch_vccz .Lbk_g0_8\n"
      ".Lsl_g0_8:\n"
      "buffer_load_dword v18, v2, s[8:11], s40 offen sc1\n"
      "s_waitcnt vmcnt(0)\n"
      "v_cmp_ne_u32 vcc, s21, v18\n"
      "s_cbranch_vccz .Lbk_g0_8\n"
      "s_sleep 4\n"
      "s_sub_u32 s22, s22, 1\n"
      "s_cbranch_scc0 .Lsl_g0_8\n"
      "s_mov_b32 s23, 0\n"
      "s_branch .Lbk_g0_8\n"
      ".Lsp_gy0_9:\n"
      "s_lshl_b32 s22, s23, 4\n"
      ".Lsl_gy0_9:\n"
      "ds_read_b32 v12, v14\n"
      "s_waitcnt lgkmcnt(0)\n"
      "v_cmp_gt_i32 vcc, v11, v12\n"
      "s_cbranch_vccz .Lbk_gy0_9\n"
      "s_sub_u32 s22, s22, 1\n"
      "s_cbranch_scc0 .Lsl_gy0_9\n"
      "s_mov_b32 s23, 0\n"
      "s_branch .Lbk_gy0_9\n"
      ".Lsp_g0_10:\n"
      "s_mov_b32 s22, s23\n"
      "buffer_load_dword v18, v2, s[8:11], s40 offen sc1\n"
      "s_waitcnt vmcnt(0)\n"
      "v_cmp_ne_u32 vcc, s21, v18\n"
      "s_add_u32 s30, s42, 0\n"
      "buffer_store_dword v15, v3, s[8:11], s30 offen sc1\n"
      "s_cbranch_vccz .Lbk_g0_10\n"
      ".Lsl_g0_10:\n"
      "buffer_load_dword v18, v2, s[8:11], s40 offen sc1\n"
      "s_waitcnt vmcnt(0)\n"
      "v_cmp_ne_u32 vcc, s21, v18\n"
      "s_cbranch_vccz .Lbk_g0_10\n"
      "s_sleep 4\n"
      "s_sub_u32 s22, s22, 1\n"
      "s_cbranch_scc0 .Lsl_g0_10\n"
      "s_mov_b32 s23, 0\n"
      "s_branch .Lbk_g0_10\n"
      ".Lsp_gy0_11:\n"
      "s_lshl_b32 s22, s23, 4\n"
      ".Lsl_gy0_11:\n"
      "ds_read_b32 v12, v14\n"
      "s_waitcnt lgkmcnt(0)\n"
      "v_cmp_gt_i32 vcc, v11, v12\n"
      "s_cbranch_vccz .Lbk_gy0_11\n"
      "s_sub_u32 s22, s22, 1\n"
      "s_cbranch_scc0 .Lsl_gy0_11\n"
      "s_mov_b32 s23, 0\n"
      "s_branch .Lbk_gy0_11\n"
      ".Lg_last:\n"
      :
      : "{s[8:11]}"(rsWS), "{s[12:15]}"(rsA), "{s[92:95]}"(rsC), "{v0}"(v_l16), "{v1}"(v_l5), "{v2}"(v_fa), "{v3}"(v_ra), "{v4}"(v_st), "{v5}"(v_at), "{v6}"(v_vo0), "{v7}"(v_vo1), "{v8}"(v_vo2), "{v9}"(v_vo3), "{v10}"(v_pw), "{v12}"(v_al), "{v13}"(v_yw), "{v14}"(v_yr), "{v16}"(v_hi), "{v54}"(v_tw[0]), "{v55}"(v_tw[1]), "{v64}"(v_id[0]), "{v65}"(v_id[1]), "{s36}"(s_sx), "{s37}"(s_sh), "{s38}"(s_m4), "{s39}"(s_m5)
      : "v255", "a255", "s99", "vcc", "scc", "memory");

  }
}

extern "C" void kernel_launch(void* const* d_in, const int* in_sizes, int n_in, void* d_out, int out_size,
                              void* d_ws, size_t ws_size, hipStream_t stream) {
  char* ws = (char*)d_ws + ((256 - ((size_t)d_ws & 255)) & 255);
  (void)hipFuncSetAttribute((const void*)k_all, hipFuncAttributeMaxDynamicSharedMemorySize, LDS_BYTES);
  k_all<<<256, 256, LDS_BYTES, stream>>>((const u16*)d_in[0], (const u16*)d_in[1], (const u16*)d_in[2], (const u16*)d_in[4],
                                         (const u16*)d_in[5], (const u16*)d_in[6], ws, (u16*)d_out);
}
```

```cpp
#include <hip/hip_runtime.h>
#include <stdint.h>

typedef unsigned short u16;
typedef __attribute__((ext_vector_type(4))) unsigned u32x4;
constexpr unsigned WS_DBG = 65536u;
constexpr unsigned WS_XIN = 1310720u;
constexpr unsigned WS_OB = 9175040u;
constexpr unsigned WS_X1 = 17039360u;
constexpr unsigned WS_H = 26034176u;
constexpr unsigned WS_PS = 78589952u;
constexpr unsigned WS_END = 109740032u;
constexpr unsigned KSK = 0u;
constexpr unsigned ENT = 1u;
constexpr unsigned ROT = 0u;
constexpr unsigned FLIP = 0u;
constexpr unsigned PW = 1664u;
constexpr unsigned REC = 24576u;
constexpr unsigned HREC = 165888u;
constexpr unsigned PREC = 99840u;
constexpr unsigned NREC = 80u;
constexpr unsigned OOB = 0x40000000u, NEG = 0xff61b1e6u;
constexpr int LDS_BYTES = 160 * 1024;

__device__ __forceinline__ u32x4 srd(const void* p, unsigned bytes) {
  const unsigned long long a = (unsigned long long)p;
  return u32x4{(unsigned)a, (unsigned)(a >> 32) & 0xffffu, bytes, 0x00020000u};
}

extern "C" __global__ __launch_bounds__(256, 1) __attribute__((amdgpu_waves_per_eu(1, 1))) void k_all(
    const u16* X, const u16* Wqkv, const u16* Wo, const u16* Wg, const u16* Wu, const u16* Wd, char* ws, u16* out) {
  const unsigned b = blockIdx.x, xcd = b & 7, slot = b >> 3;
  const unsigned w = __builtin_amdgcn_readfirstlane(threadIdx.x >> 6), lane = threadIdx.x & 63;
  const bool fast = ((xcd ^ FLIP) & 1) == 0;
  auto qvo = [&](unsigned i, unsigned hg) {
    const unsigned c = 64 * i + lane, r = c * 2185 >> 16, cc = c - 30 * r, sc = cc * 57 >> 9;
    const unsigned o = (8 * w + r) * 3456 + sc * (1152 - 144) + 144 * hg + cc * 16 - 1024 * i;
    return o | (c < 240 && cc < 27 ? 0u : OOB);
  };
  if (ENT) {
    constexpr unsigned NL = 41, LR = 9;
    const unsigned wi = slot * 4 + w, n0 = wi * NL + lane, n = n0 < 576 * LR ? n0 : 0, r = n / LR, c = n - LR * r;
    const unsigned lx = lane - NL, m = (xcd * 128 + wi) * 4 + lx, t = m / 144;
    unsigned oc = (wi * 8 + (lane & 7)) * 128;
    asm volatile("s_getpc_b64 vcc\n.Lent_pc:\n\ts_sub_u32 vcc_lo, vcc_lo, .Lent_pc-k_all\n\ts_subb_u32 vcc_hi, vcc_hi, 0\n\ts_mov_b32 m0, %1\n\t"
                 "v_min_u32 %0, .Lw_last-k_all, %0\n\tglobal_load_lds_dword %0, vcc" : "+v"(oc) : "s"(162816 + w * 256) : "vcc", "scc", "memory");
    unsigned oq = r * 3456 + c / (LR / 3) * 1152 + ((xcd & 6) + c % (LR / 3)) * 128, ox = (32 * (t & 3) + 31 - (t >> 2)) * 18432 + (m - 144 * t) * 128;
    asm volatile("" : "+v"(oq), "+v"(ox));
    const bool isq = lane < NL && fast;
    oq = isq ? oq : lx < 4 ? ox : 0;
    asm volatile("" : "+v"(oq));
    __builtin_amdgcn_sched_barrier(0);
    const char* pa = (const char*)(isq ? Wqkv : X) + oq;
    asm volatile("s_mov_b32 m0, %1\n\ts_nop 0\n\tglobal_load_lds_dword %0, off" ::"v"(pa), "s"(162816 + w * 256) : "memory");
    __builtin_amdgcn_sched_barrier(0);
  }
  const unsigned i16 = lane & 15, g = lane >> 4, q = i16 >> 2, p = lane & 3;
  const u32x4 rsWS = srd(ws, WS_END);
  const unsigned v_l16 = lane * 16, e = xcd >> 1;
  const unsigned half = (i16 + 16 * (g >> 1)) * 16 + (g & 1) * 8;
  const unsigned line = lane < 32 ? lane * 4 : OOB, v_al = w ? OOB : line + 128 * (1 + b);
  const unsigned up = lane >> 5, l5 = (lane & 31) * 4, v_hi = 0u - up;
  const unsigned l8 = lane >> 3, l7 = (lane & 7) * 16;
#define COMMON(ID, YB) const unsigned v_dbg = WS_DBG + (((ID) + part * 4 + gr) * 4 + w) * 1024 + lane * 4, v_yw = (YB) + w * 4, v_yr = (YB) + p * 4; (void)v_dbg
  if (__builtin_expect(fast && slot < 8, 1)) {
    const unsigned gr = slot & 3, part = 2 * e + (slot >> 2);
    COMMON(0, 61440);
    const unsigned hg = part, h = 4 * hg + w, par = i16 & 1;
    const u32x4 rsA = srd(Wqkv, 576 * 1728 * 2);
    const unsigned v_l5 = w < 3 || lane < 32 ? v_l16 + 16384 - w * 3072 : OOB;
    const unsigned v_fa = 20480 - w * 4096 + (lane < 48 ? lane >> 2 : 11) * 128 + p * 4, v_ra = up * 8 * REC + l5 + 20480 + h * 128;
    const unsigned v_st = 18432 + i16 * 32 + q * 8, v_tw = 61568 + w * 128 + i16 * 8, v_tr = 61568 + w * 128 + g * 32;
    const unsigned v_om = h * 512 + half, v_oe = g ? OOB : 16384 + h * 128 + i16 * 8;
    const unsigned v_pw = 20480 + h * 128 + l8 * REC + l7;
    unsigned v_vo[4], v_bc[4], v_bp[4], v_mk[4], v_sel[4] = {0, 0, 0, 0}, v_ci[4];
#pragma unroll
    for (unsigned i = 0; i < 4; ++i) {
      const unsigned kk = 4 * g + i, ka = 4 * g + 2 * (i & 1) + par;
      v_bc[i] = (i & 1) == par && kk <= i16 ? 0u : NEG;
      v_bp[i] = (i & 1) == par && kk >= i16 + 2 ? 0u : NEG;
      v_mk[i] = (i < 2 ? ka > i16 : ka <= i16) ? 0xffffu << (16 * par) : 0u;
      v_ci[i] = p == 2 ? 0x3f800000u : 0u;
    }
    v_sel[3] = lane == 16 ? 0x3f80u : lane == 17 ? 0x3f800000u : 0u;
#pragma unroll
    for (unsigned i = 0; i < 4; ++i) v_vo[i] = qvo(i, hg);
    const unsigned rb = 65536 + g * 4224 + q * 480;
    const unsigned v_at = rb + 36 * w + 4 * (w & 1) + 8 * p, v_ax = rb + (p < 2 ? p : 2) * 144 + 72 * (w >> 1) + 32;
    const unsigned v_pm = (16 * g + 4 * (i16 >> 1 & 3) + 2 * (w & 1) + par) * 4, v_ms = i16 < 6 ? ~0u : 0u;
    const unsigned s_sx = WS_XIN + (gr * NREC + KSK) * REC + w * 4096, s_ev = s_sx + (32 - KSK) * REC, s_del = 0u - (gr ? NREC + 32 : 32) * REC;
    const unsigned s_kil = gr ? 0x7f7fffffu : 0xff7fffffu, s_so = WS_OB + (gr * NREC + KSK) * REC;
    const unsigned s_m4 = w * 4096, s_m5 = 16384 + w * 1024, s_ms = w * 4224, s_up = part + gr + w;
    asm volatile(
      "s_buffer_load_dword s21, s[8:11], 0\n"
      "s_mov_b32 s23, 65536\n"
      "s_mov_b32 s31, 0x40000000\n"
      "s_mov_b32 s51, 0\n"
      "s_add_u32 m0, s43, 65536\n"
      "s_nop 0\n"
      "buffer_load_dwordx4 v48, s[12:15], s51 offen offset:0 lds\n"
      "buffer_load_dwordx4 v49, s[12:15], s51 offen offset:1024 lds\n"
      "buffer_load_dwordx4 v50, s[12:15], s51 offen offset:2048 lds\n"
      "buffer_load_dwordx4 v51, s[12:15], s51 offen offset:3072 lds\n"
      "s_add_u32 s51, s51, 110592\n"
      "s_add_u32 m0, s43, 82432\n"
      "s_nop 0\n"
      "buffer_load_dwordx4 v48, s[12:15], s51 offen offset:0 lds\n"
      "buffer_load_dwordx4 v49, s[12:15], s51 offen offset:1024 lds\n"
      "buffer_load_dwordx4 v50, s[12:15], s51 offen offset:2048 lds\n"
      "buffer_load_dwordx4 v51, s[12:15], s51 offen offset:3072 lds\n"
      "s_add_u32 s51, s51, 110592\n"
      "s_add_u32 m0, s43, 99328\n"
      "s_nop 0\n"
      "buffer_load_dwordx4 v48, s[12:15], s51 offen offset:0 lds\n"
      "buffer_load_dwordx4 v49, s[12:15], s51 offen offset:1024 lds\n"
      "buffer_load_dwordx4 v50, s[12:15], s51 offen offset:2048 lds\n"
      "buffer_load_dwordx4 v51, s[12:15], s51 offen offset:3072 lds\n"
      "s_add_u32 s51, s51, 110592\n"
      "s_add_u32 s6, s36, 1572864\n"
      "s_add_u32 s36, s36, 4294942720\n"
      "s_add_u32 s48, s37, 811008\n"
      "s_mov_b32 s53, 0x7f7fffff\n"
      "v_mov_b32 v179, 0\n"
      "s_mov_b32 s54, 0xffff\n"
      "s_mov_b32 s55, 0\n"
      "v_mov_b32 v9, 0\n"
      "ds_write_b32 v11, v9\n"
      "v_mov_b32 v56, 0x3f803f80\n"
      "v_mov_b32 v57, 0x3f803f80\n"
      "v_mov_b32 v58, 0x3f803f80\n"
      "v_mov_b32 v59, 0x3f803f80\n"
      "s_mov_b64 exec, 0\n"
      "s_lshr_b32 s22, s41, 12\n"
      ".Lq_pl:\n"
      "s_cmp_eq_u32 s22, 0\n"
      "s_cbranch_scc1 .Lq_p0\n"
      "s_cmp_eq_u32 s22, 1\n"
      "s_cbranch_scc1 .Lq_p1\n"
      "s_cmp_eq_u32 s22, 3\n"
      "s_cbranch_scc1 .Lq_px\n"
      "s_branch .Lq_pe\n"
      ".Lq_pr:\n"
      "s_add_u32 s22, s22, 4\n"
      "s_branch .Lq_pl\n"
      ".Lq_pe:\n"
      "s_mov_b64 exec, -1\n"
      "s_waitcnt vmcnt(8) lgkmcnt(0)\n"
      "s_barrier\n"
      "ds_read_b64_tr_b16 v[60:61], v53 offset:0\n"
      "ds_read_b64_tr_b16 v[62:63], v53 offset:1920\n"
      "ds_read_b64_tr_b16 a[0:1], v52 offset:144\n"
      "ds_read_b64_tr_b16 a[2:3], v52 offset:2064\n"
      "ds_read_b64_tr_b16 a[72:73], v52 offset:288\n"
      "ds_read_b64_tr_b16 a[74:75], v52 offset:2208\n"
      "ds_read_b64_tr_b16 a[144:145], v52 offset:0\n"
      "ds_read_b64_tr_b16 a[146:147], v52 offset:1920\n"
      "s_add_u32 m0, s43, 116224\n"
      "s_nop 0\n"
      "buffer_load_dwordx4 v48, s[12:15], s51 offen offset:0 lds\n"
      "buffer_load_dwordx4 v49, s[12:15], s51 offen offset:1024 lds\n"
      "buffer_load_dwordx4 v50, s[12:15], s51 offen offset:2048 lds\n"
      "buffer_load_dwordx4 v51, s[12:15], s51 offen offset:3072 lds\n"
      "s_add_u32 s51, s51, 110592\n"
      "s_waitcnt vmcnt(8) lgkmcnt(0)\n"
      "s_barrier\n"
      "ds_read_b64_tr_b16 v[68:69], v53 offset:16896\n"
      "ds_read_b64_tr_b16 v[70:71], v53 offset:18816\n"
      "ds_read_b64_tr_b16 a[4:5], v52 offset:17040\n"
      "ds_read_b64_tr_b16 a[6:7], v52 offset:18960\n"
      "ds_read_b64_tr_b16 a[76:77], v52 offset:17184\n"
      "ds_read_b64_tr_b16 a[78:79], v52 offset:19104\n"
      "ds_read_b64_tr_b16 a[148:149], v52 offset:16896\n"
      "ds_read_b64_tr_b16 a[150:151], v52 offset:18816\n"
      "ds_bpermute_b32 v64, v54, v60\n"
      "ds_bpermute_b32 v65, v54, v61\n"
      "ds_bpermute_b32 v66, v54, v62\n"
      "ds_bpermute_b32 v67, v54, v63\n"
      "v_mfma_f32_16x16x32_bf16 v[136:139], a[0:3], v[56:59], 0\n"
      "v_mfma_f32_16x16x32_bf16 v[144:147], v[56:59], a[72:75], 0\n"
      "v_mfma_f32_16x16x32_bf16 v[148:151], a[144:147], v[56:59], 0\n"
      "s_add_u32 m0, s43, 65536\n"
      "s_nop 0\n"
      "buffer_load_dwordx4 v48, s[12:15], s51 offen offset:0 lds\n"
      "buffer_load_dwordx4 v49, s[12:15], s51 offen offset:1024 lds\n"
      "buffer_load_dwordx4 v50, s[12:15], s51 offen offset:2048 lds\n"
      "buffer_load_dwordx4 v51, s[12:15], s51 offen offset:3072 lds\n"
      "s_add_u32 s51, s51, 110592\n"
      "s_waitcnt vmcnt(8) lgkmcnt(0)\n"
      "s_barrier\n"
      "ds_read_b64_tr_b16 v[60:61], v53 offset:33792\n"
      "ds_read_b64_tr_b16 v[62:63], v53 offset:35712\n"
      "ds_read_b64_tr_b16 a[8:9], v52 offset:33936\n"
      "ds_read_b64_tr_b16 a[10:11], v52 offset:35856\n"
      "ds_read_b64_tr_b16 a[80:81], v52 offset:34080\n"
      "ds_read_b64_tr_b16 a[82:83], v52 offset:36000\n"
      "ds_read_b64_tr_b16 a[152:153], v52 offset:33792\n"
      "ds_read_b64_tr_b16 a[154:155], v52 offset:35712\n"
      "ds_bpermute_b32 v72, v54, v68\n"
      "ds_bpermute_b32 v73, v54, v69\n"
      "ds_bpermute_b32 v74, v54, v70\n"
      "ds_bpermute_b32 v75, v54, v71\n"
      "v_mfma_f32_16x16x32_bf16 v[136:139], a[4:7], v[56:59], v[136:139]\n"
      "v_mfma_f32_16x16x32_bf16 v[144:147], v[56:59], a[76:79], v[144:147]\n"
      "v_mfma_f32_16x16x32_bf16 v[148:151], a[148:151], v[56:59], v[148:151]\n"
      "s_add_u32 m0, s43, 82432\n"
      "s_nop 0\n"
      "buffer_load_dwordx4 v48, s[12:15], s51 offen offset:0 lds\n"
      "buffer_load_dwordx4 v49, s[12:15], s51 offen offset:1024 lds\n"
      "buffer_load_dwordx4 v50, s[12:15], s51 offen offset:2048 lds\n"
      "buffer_load_dwordx4 v51, s[12:15], s51 offen offset:3072 lds\n"
      "s_add_u32 s51, s51, 110592\n"
      "v_and_b32 v64, v55, v64\n"
      "v_and_b32 v65, v55, v65\n"
      "v_and_b32 v66, v55, v66\n"
      "v_and_b32 v67, v55, v67\n"
      "v_accvgpr_write_b32 a216, v64\n"
      "v_accvgpr_write_b32 a217, v65\n"
      "v_accvgpr_write_b32 a218, v66\n"
      "v_accvgpr_write_b32 a219, v67\n"
      "v_mfma_f32_16x16x32_bf16 v[140:143], v[64:67], v[56:59], 0\n"
      "s_waitcnt vmcnt(8) lgkmcnt(0)\n"
      "s_barrier\n"
      "ds_read_b64_tr_b16 v[68:69], v53 offset:50688\n"
      "ds_read_b64_tr_b16 v[70:71], v53 offset:52608\n"
      "ds_read_b64_tr_b16 a[12:13], v52 offset:50832\n"
      "ds_read_b64_tr_b16 a[14:15], v52 offset:52752\n"
      "ds_read_b64_tr_b16 a[84:85], v52 offset:50976\n"
      "ds_read_b64_tr_b16 a[86:87], v52 offset:52896\n"
      "ds_read_b64_tr_b16 a[156:157], v52 offset:50688\n"
      "ds_read_b64_tr_b16 a[158:159], v52 offset:52608\n"
      "ds_bpermute_b32 v64, v54, v60\n"
      "ds_bpermute_b32 v65, v54, v61\n"
      "ds_bpermute_b32 v66, v54, v62\n"
      "ds_bpermute_b32 v67, v54, v63\n"
      "v_mfma_f32_16x16x32_bf16 v[136:139], a[8:11], v[56:59], v[136:139]\n"
      "v_mfma_f32_16x16x32_bf16 v[144:147], v[56:59], a[80:83], v[144:147]\n"
      "v_mfma_f32_16x16x32_bf16 v[148:151], a[152:155], v[56:59], v[148:151]\n"
      "s_add_u32 m0, s43, 99328\n"
      "s_nop 0\n"
      "buffer_load_dwordx4 v48, s[12:15], s51 offen offset:0 lds\n"
      "buffer_load_dwordx4 v49, s[12:15], s51 offen offset:1024 lds\n"
      "buffer_load_dwordx4 v50, s[12:15], s51 offen offset:2048 lds\n"
      "buffer_load_dwordx4 v51, s[12:15], s51 offen offset:3072 lds\n"
      "s_add_u32 s51, s51, 110592\n"
      "v_and_b32 v72, v55, v72\n"
      "v_and_b32 v73, v55, v73\n"
      "v_and_b32 v74, v55, v74\n"
      "v_and_b32 v75, v55, v75\n"
      "v_accvgpr_write_b32 a220, v72\n"
      "v_accvgpr_write_b32 a221, v73\n"
      "v_accvgpr_write_b32 a222, v74\n"
      "v_accvgpr_write_b32 a223, v75\n"
      "v_mfma_f32_16x16x32_bf16 v[140:143], v[72:75], v[56:59], v[140:143]\n"
      "s_waitcnt vmcnt(8) lgkmcnt(0)\n"
      "s_barrier\n"
      "ds_read_b64_tr_b16 v[60:61], v53 offset:0\n"
      "ds_read_b64_tr_b16 v[62:63], v53 offset:1920\n"
      "ds_read_b64_tr_b16 a[16:17], v52 offset:144\n"
      "ds_read_b64_tr_b16 a[18:19], v52 offset:2064\n"
      "ds_read_b64_tr_b16 a[88:89], v52 offset:288\n"
      "ds_read_b64_tr_b16 a[90:91], v52 offset:2208\n"
      "ds_read_b64_tr_b16 a[160:161], v52 offset:0\n"
      "ds_read_b64_tr_b16 a[162:163], v52 offset:1920\n"
      "ds_bpermute_b32 v72, v54, v68\n"
      "ds_bpermute_b32 v73, v54, v69\n"
      "ds_bpermute_b32 v74, v54, v70\n"
      "ds_bpermute_b32 v75, v54, v71\n"
      "v_mfma_f32_16x16x32_bf16 v[136:139], a[12:15], v[56:59], v[136:139]\n"
      "v_mfma_f32_16x16x32_bf16 v[144:147], v[56:59], a[84:87], v[144:147]\n"
      "v_mfma_f32_16x16x32_bf16 v[148:151], a[156:159], v[56:59], v[148:151]\n"
      "s_add_u32 m0, s43, 116224\n"
      "s_nop 0\n"
      "buffer_load_dwordx4 v48, s[12:15], s51 offen offset:0 lds\n"
      "buffer_load_dwordx4 v49, s[12:15], s51 offen offset:1024 lds\n"
      "buffer_load_dwordx4 v50, s[12:15], s51 offen offset:2048 lds\n"
      "buffer_load_dwordx4 v51, s[12:15], s51 offen offset:3072 lds\n"
      "s_add_u32 s51, s51, 110592\n"
      "v_and_b32 v64, v55, v64\n"
      "v_and_b32 v65, v55, v65\n"
      "v_and_b32 v66, v55, v66\n"
      "v_and_b32 v67, v55, v67\n"
      "v_accvgpr_write_b32 a224, v64\n"
      "v_accvgpr_write_b32 a225, v65\n"
      "v_accvgpr_write_b32 a226, v66\n"
      "v_accvgpr_write_b32 a227, v67\n"
      "v_mfma_f32_16x16x32_bf16 v[140:143], v[64:67], v[56:59], v[140:143]\n"
      "s_waitcnt vmcnt(8) lgkmcnt(0)\n"
      "s_barrier\n"
      "ds_read_b64_tr_b16 v[68:69], v53 offset:16896\n"
      "ds_read_b64_tr_b16 v[70:71], v53 offset:18816\n"
      "ds_read_b64_tr_b16 a[20:21], v52 offset:17040\n"
      "ds_read_b64_tr_b16 a[22:23], v52 offset:18960\n"
      "ds_read_b64_tr_b16 a[92:93], v52 offset:17184\n"
      "ds_read_b64_tr_b16 a[94:95], v52 offset:19104\n"
      "ds_read_b64_tr_b16 a[164:165], v52 offset:16896\n"
      "ds_read_b64_tr_b16 a[166:167], v52 offset:18816\n"
      "ds_bpermute_b32 v64, v54, v60\n"
      "ds_bpermute_b32 v65, v54, v61\n"
      "ds_bpermute_b32 v66, v54, v62\n"
      "ds_bpermute_b32 v67, v54, v63\n"
      "v_mfma_f32_16x16x32_bf16 v[136:139], a[16:19], v[56:59], v[136:139]\n"
      "v_mfma_f32_16x16x32_bf16 v[144:147], v[56:59], a[88:91], v[144:147]\n"
      "v_mfma_f32_16x16x32_bf16 v[148:151], a[160:163], v[56:59], v[148:151]\n"
      "s_add_u32 m0, s43, 65536\n"
      "s_nop 0\n"
      "buffer_load_dwordx4 v48, s[12:15], s51 offen offset:0 lds\n"
      "buffer_load_dwordx4 v49, s[12:15], s51 offen offset:1024 lds\n"
      "buffer_load_dwordx4 v50, s[12:15], s51 offen offset:2048 lds\n"
      "buffer_load_dwordx4 v51, s[12:15], s51 offen offset:3072 lds\n"
      "s_add_u32 s51, s51, 110592\n"
      "v_and_b32 v72, v55, v72\n"
      "v_and_b32 v73, v55, v73\n"
      "v_and_b32 v74, v55, v74\n"
      "v_and_b32 v75, v55, v75\n"
      "v_accvgpr_write_b32 a228, v72\n"
      "v_accvgpr_write_b32 a229, v73\n"
      "v_accvgpr_write_b32 a230, v74\n"
      "v_accvgpr_write_b32 a231, v75\n"
      "v_mfma_f32_16x16x32_bf16 v[140:143], v[72:75], v[56:59], v[140:143]\n"
      "s_waitcnt vmcnt(8) lgkmcnt(0)\n"
      "s_barrier\n"
      "ds_read_b64_tr_b16 v[60:61], v53 offset:33792\n"
      "ds_read_b64_tr_b16 v[62:63], v53 offset:35712\n"
      "ds_read_b64_tr_b16 a[24:25], v52 offset:33936\n"
      "ds_read_b64_tr_b16 a[26:27], v52 offset:35856\n"
      "ds_read_b64_tr_b16 a[96:97], v52 offset:34080\n"
      "ds_read_b64_tr_b16 a[98:99], v52 offset:36000\n"
      "ds_read_b64_tr_b16 a[168:169], v52 offset:33792\n"
      "ds_read_b64_tr_b16 a[170:171], v52 offset:35712\n"
      "ds_bpermute_b32 v72, v54, v68\n"
      "ds_bpermute_b32 v73, v54, v69\n"
      "ds_bpermute_b32 v74, v54, v70\n"
      "ds_bpermute_b32 v75, v54, v71\n"
      "v_mfma_f32_16x16x32_bf16 v[136:139], a[20:23], v[56:59], v[136:139]\n"
      "v_mfma_f32_16x16x32_bf16 v[144:147], v[56:59], a[92:95], v[144:147]\n"
      "v_mfma_f32_16x16x32_bf16 v[148:151], a[164:167], v[56:59], v[148:151]\n"
      "s_add_u32 m0, s43, 82432\n"
      "s_nop 0\n"
      "buffer_load_dwordx4 v48, s[12:15], s51 offen offset:0 lds\n"
      "buffer_load_dwordx4 v49, s[12:15], s51 offen offset:1024 lds\n"
      "buffer_load_dwordx4 v50, s[12:15], s51 offen offset:2048 lds\n"
      "buffer_load_dwordx4 v51, s[12:15], s51 offen offset:3072 lds\n"
      "s_add_u32 s51, s51, 110592\n"
      "v_and_b32 v64, v55, v64\n"
      "v_and_b32 v65, v55, v65\n"
      "v_and_b32 v66, v55, v66\n"
      "v_and_b32 v67, v55, v67\n"
      "v_accvgpr_write_b32 a232, v64\n"
      "v_accvgpr_write_b32 a233, v65\n"
      "v_accvgpr_write_b32 a234, v66\n"
      "v_accvgpr_write_b32 a235, v67\n"
      "v_mfma_f32_16x16x32_bf16 v[140:143], v[64:67], v[56:59], v[140:143]\n"
      "s_waitcnt vmcnt(8) lgkmcnt(0)\n"
      "s_barrier\n"
      "ds_read_b64_tr_b16 v[68:69], v53 offset:50688\n"
      "ds_read_b64_tr_b16 v[70:71], v53 offset:52608\n"
      "ds_read_b64_tr_b16 a[28:29], v52 offset:50832\n"
      "ds_read_b64_tr_b16 a[30:31], v52 offset:52752\n"
      "ds_read_b64_tr_b16 a[100:101], v52 offset:50976\n"
      "ds_read_b64_tr_b16 a[102:103], v52 offset:52896\n"
      "ds_read_b64_tr_b16 a[172:173], v52 offset:50688\n"
      "ds_read_b64_tr_b16 a[174:175], v52 offset:52608\n"
      "ds_bpermute_b32 v64, v54, v60\n"
      "ds_bpermute_b32 v65, v54, v61\n"
      "ds_bpermute_b32 v66, v54, v62\n"
      "ds_bpermute_b32 v67, v54, v63\n"
      "v_mfma_f32_16x16x32_bf16 v[136:139], a[24:27], v[56:59], v[136:139]\n"
      "v_mfma_f32_16x16x32_bf16 v[144:147], v[56:59], a[96:99], v[144:147]\n"
      "v_mfma_f32_16x16x32_bf16 v[148:151], a[168:171], v[56:59], v[148:151]\n"
      "s_add_u32 m0, s43, 99328\n"
      "s_nop 0\n"
      "buffer_load_dwordx4 v48, s[12:15], s51 offen offset:0 lds\n"
      "buffer_load_dwordx4 v49, s[12:15], s51 offen offset:1024 lds\n"
      "buffer_load_dwordx4 v50, s[12:15], s51 offen offset:2048 lds\n"
      "buffer_load_dwordx4 v51, s[12:15], s51 offen offset:3072 lds\n"
      "s_add_u32 s51, s51, 110592\n"
      "v_and_b32 v72, v55, v72\n"
      "v_and_b32 v73, v55, v73\n"
      "v_and_b32 v74, v55, v74\n"
      "v_and_b32 v75, v55, v75\n"
      "v_accvgpr_write_b32 a236, v72\n"
      "v_accvgpr_write_b32 a237, v73\n"
      "v_accvgpr_write_b32 a238, v74\n"
      "v_accvgpr_write_b32 a239, v75\n"
      "v_mfma_f32_16x16x32_bf16 v[140:143], v[72:75], v[56:59], v[140:143]\n"
      "s_waitcnt vmcnt(8) lgkmcnt(0)\n"
      "s_barrier\n"
      "ds_read_b64_tr_b16 v[60:61], v53 offset:0\n"
      "ds_read_b64_tr_b16 v[62:63], v53 offset:1920\n"
      "ds_read_b64_tr_b16 a[32:33], v52 offset:144\n"
      "ds_read_b64_tr_b16 a[34:35], v52 offset:2064\n"
      "ds_read_b64_tr_b16 a[104:105], v52 offset:288\n"
      "ds_read_b64_tr_b16 a[106:107], v52 offset:2208\n"
      "ds_read_b64_tr_b16 a[176:177], v52 offset:0\n"
      "ds_read_b64_tr_b16 a[178:179], v52 offset:1920\n"
      "ds_bpermute_b32 v72, v54, v68\n"
      "ds_bpermute_b32 v73, v54, v69\n"
      "ds_bpermute_b32 v74, v54, v70\n"
      ".Lq_px:\n"
      "s_cbranch_execz .Lq_pr\n"
      "ds_bpermute_b32 v75, v54, v71\n"
      "v_mfma_f32_16x16x32_bf16 v[136:139], a[28:31], v[56:59], v[136:139]\n"
      "v_mfma_f32_16x16x32_bf16 v[144:147], v[56:59], a[100:103], v[144:147]\n"
      "v_mfma_f32_16x16x32_bf16 v[148:151], a[172:175], v[56:59], v[148:151]\n"
      "s_add_u32 m0, s43, 116224\n"
      "s_nop 0\n"
      "buffer_load_dwordx4 v48, s[12:15], s51 offen offset:0 lds\n"
      "buffer_load_dwordx4 v49, s[12:15], s51 offen offset:1024 lds\n"
      "buffer_load_dwordx4 v50, s[12:15], s51 offen offset:2048 lds\n"
      "buffer_load_dwordx4 v51, s[12:15], s51 offen offset:3072 lds\n"
      "s_add_u32 s51, s51, 110592\n"
      "v_and_b32 v64, v55, v64\n"
      "v_and_b32 v65, v55, v65\n"
      "v_and_b32 v66, v55, v66\n"
      "v_and_b32 v67, v55, v67\n"
      "v_accvgpr_write_b32 a240, v64\n"
      "v_accvgpr_write_b32 a241, v65\n"
      "v_accvgpr_write_b32 a242, v66\n"
      "v_accvgpr_write_b32 a243, v67\n"
      "v_mfma_f32_16x16x32_bf16 v[140:143], v[64:67], v[56:59], v[140:143]\n"
      "s_waitcnt vmcnt(8) lgkmcnt(0)\n"
      "s_barrier\n"
      "ds_read_b64_tr_b16 v[68:69], v53 offset:16896\n"
      "ds_read_b64_tr_b16 v[70:71], v53 offset:18816\n"
      "ds_read_b64_tr_b16 a[36:37], v52 offset:17040\n"
      "ds_read_b64_tr_b16 a[38:39], v52 offset:18960\n"
      "ds_read_b64_tr_b16 a[108:109], v52 offset:17184\n"
      "ds_read_b64_tr_b16 a[110:111], v52 offset:19104\n"
      "ds_read_b64_tr_b16 a[180:181], v52 offset:16896\n"
      "ds_read_b64_tr_b16 a[182:183], v52 offset:18816\n"
      "ds_bpermute_b32 v64, v54, v60\n"
      "ds_bpermute_b32 v65, v54, v61\n"
      "ds_bpermute_b32 v66, v54, v62\n"
      "ds_bpermute_b32 v67, v54, v63\n"
      "v_mfma_f32_16x16x32_bf16 v[136:139], a[32:35], v[56:59], v[136:139]\n"
      "v_mfma_f32_16x16x32_bf16 v[144:147], v[56:59], a[104:107], v[144:147]\n"
      "v_mfma_f32_16x16x32_bf16 v[148:151], a[176:179], v[56:59], v[148:151]\n"
      "s_add_u32 m0, s43, 65536\n"
      "s_nop 0\n"
      "buffer_load_dwordx4 v48, s[12:15], s51 offen offset:0 lds\n"
      "buffer_load_dwordx4 v49, s[12:15], s51 offen offset:1024 lds\n"
      "buffer_load_dwordx4 v50, s[12:15], s51 offen offset:2048 lds\n"
      "buffer_load_dwordx4 v51, s[12:15], s51 offen offset:3072 lds\n"
      "s_add_u32 s51, s51, 110592\n"
      "v_and_b32 v72, v55, v72\n"
      "v_and_b32 v73, v55, v73\n"
      "v_and_b32 v74, v55, v74\n"
      "v_and_b32 v75, v55, v75\n"
      "v_accvgpr_write_b32 a244, v72\n"
      "v_accvgpr_write_b32 a245, v73\n"
      "v_accvgpr_write_b32 a246, v74\n"
      "v_accvgpr_write_b32 a247, v75\n"
      "v_mfma_f32_16x16x32_bf16 v[140:143], v[72:75], v[56:59], v[140:143]\n"
      "s_waitcnt vmcnt(8) lgkmcnt(0)\n"
      "s_barrier\n"
      "ds_read_b64_tr_b16 v[60:61], v53 offset:33792\n"
      "ds_read_b64_tr_b16 v[62:63], v53 offset:35712\n"
      "ds_read_b64_tr_b16 a[40:41], v52 offset:33936\n"
      "ds_read_b64_tr_b16 a[42:43], v52 offset:35856\n"
      "ds_read_b64_tr_b16 a[112:113], v52 offset:34080\n"
      "ds_read_b64_tr_b16 a[114:115], v52 offset:36000\n"
      "ds_read_b64_tr_b16 a[184:185], v52 offset:33792\n"
      "ds_read_b64_tr_b16 a[186:187], v52 offset:35712\n"
      "ds_bpermute_b32 v72, v54, v68\n"
      "ds_bpermute_b32 v73, v54, v69\n"
      "ds_bpermute_b32 v74, v54, v70\n"
      "ds_bpermute_b32 v75, v54, v71\n"
      "v_mfma_f32_16x16x32_bf16 v[136:139], a[36:39], v[56:59], v[136:139]\n"
      "v_mfma_f32_16x16x32_bf16 v[144:147], v[56:59], a[108:111], v[144:147]\n"
      "v_mfma_f32_16x16x32_bf16 v[148:151], a[180:183], v[56:59], v[148:151]\n"
      "s_add_u32 m0, s43, 82432\n"
      "s_nop 0\n"
      "buffer_load_dwordx4 v48, s[12:15], s51 offen offset:0 lds\n"
      "buffer_load_dwordx4 v49, s[12:15], s51 offen offset:1024 lds\n"
      "buffer_load_dwordx4 v50, s[12:15], s51 offen offset:2048 lds\n"
      "buffer_load_dwordx4 v51, s[12:15], s51 offen offset:3072 lds\n"
      "s_add_u32 s51, s51, 110592\n"
      "v_and_b32 v64, v55, v64\n"
      "v_and_b32 v65, v55, v65\n"
      "v_and_b32 v66, v55, v66\n"
      "v_and_b32 v67, v55, v67\n"
      "v_accvgpr_write_b32 a248, v64\n"
      "v_accvgpr_write_b32 a249, v65\n"
      "v_accvgpr_write_b32 a250, v66\n"
      "v_accvgpr_write_b32 a251, v67\n"
      "v_mfma_f32_16x16x32_bf16 v[140:143], v[64:67], v[56:59], v[140:143]\n"
      "s_waitcnt vmcnt(8) lgkmcnt(0)\n"
      "s_barrier\n"
      "ds_read_b64_tr_b16 v[68:69], v53 offset:50688\n"
      "ds_read_b64_tr_b16 v[70:71], v53 offset:52608\n"
      "ds_read_b64_tr_b16 a[44:45], v52 offset:50832\n"
      "ds_read_b64_tr_b16 a[46:47], v52 offset:52752\n"
      "ds_read_b64_tr_b16 a[116:117], v52 offset:50976\n"
      "ds_read_b64_tr_b16 a[118:119], v52 offset:52896\n"
      "ds_read_b64_tr_b16 a[188:189], v52 offset:50688\n"
      "ds_read_b64_tr_b16 a[190:191], v52 offset:52608\n"
      "ds_bpermute_b32 v64, v54, v60\n"
      "ds_bpermute_b32 v65, v54, v61\n"
      "ds_bpermute_b32 v66, v54, v62\n"
      "ds_bpermute_b32 v67, v54, v63\n"
      "v_mfma_f32_16x16x32_bf16 v[136:139], a[40:43], v[56:59], v[136:139]\n"
      "v_mfma_f32_16x16x32_bf16 v[144:147], v[56:59], a[112:115], v[144:147]\n"
      "v_mfma_f32_16x16x32_bf16 v[148:151], a[184:187], v[56:59], v[148:151]\n"
      "s_add_u32 m0, s43, 99328\n"
      "s_nop 0\n"
      "buffer_load_dwordx4 v48, s[12:15], s51 offen offset:0 lds\n"
      "buffer_load_dwordx4 v49, s[12:15], s51 offen offset:1024 lds\n"
      "buffer_load_dwordx4 v50, s[12:15], s51 offen offset:2048 lds\n"
      "buffer_load_dwordx4 v51, s[12:15], s51 offen offset:3072 lds\n"
      "s_add_u32 s51, s51, 110592\n"
      "v_and_b32 v72, v55, v72\n"
      "v_and_b32 v73, v55, v73\n"
      "v_and_b32 v74, v55, v74\n"
      "v_and_b32 v75, v55, v75\n"
      "v_accvgpr_write_b32 a252, v72\n"
      "v_accvgpr_write_b32 a253, v73\n"
      "v_accvgpr_write_b32 a254, v74\n"
      "v_accvgpr_write_b32 a255, v75\n"
      "v_mfma_f32_16x16x32_bf16 v[140:143], v[72:75], v[56:59], v[140:143]\n"
      "s_waitcnt vmcnt(8) lgkmcnt(0)\n"
      "s_barrier\n"
      "ds_read_b64_tr_b16 v[60:61], v53 offset:0\n"
      "ds_read_b64_tr_b16 v[62:63], v53 offset:1920\n"
      "ds_read_b64_tr_b16 a[48:49], v52 offset:144\n"
      "ds_read_b64_tr_b16 a[50:51], v52 offset:2064\n"
      "ds_read_b64_tr_b16 a[120:121], v52 offset:288\n"
      "ds_read_b64_tr_b16 a[122:123], v52 offset:2208\n"
      "ds_read_b64_tr_b16 a[192:193], v52 offset:0\n"
      "ds_read_b64_tr_b16 a[194:195], v52 offset:1920\n"
      "ds_bpermute_b32 v72, v54, v68\n"
      "ds_bpermute_b32 v73, v54, v69\n"
      "ds_bpermute_b32 v74, v54, v70\n"
      "ds_bpermute_b32 v75, v54, v71\n"
      "v_mfma_f32_16x16x32_bf16 v[136:139], a[44:47], v[56:59], v[136:139]\n"
      "v_mfma_f32_16x16x32_bf16 v[144:147], v[56:59], a[116:119], v[144:147]\n"
      "v_mfma_f32_16x16x32_bf16 v[148:151], a[188:191], v[56:59], v[148:151]\n"
      "s_add_u32 m0, s43, 116224\n"
      "s_nop 0\n"
      "buffer_load_dwordx4 v48, s[12:15], s51 offen offset:0 lds\n"
      "buffer_load_dwordx4 v49, s[12:15], s51 offen offset:1024 lds\n"
      "buffer_load_dwordx4 v50, s[12:15], s51 offen offset:2048 lds\n"
      "buffer_load_dwordx4 v51, s[12:15], s51 offen offset:3072 lds\n"
      "s_add_u32 s51, s51, 110592\n"
      "v_and_b32 v64, v55, v64\n"
      "v_and_b32 v65, v55, v65\n"
      "v_and_b32 v66, v55, v66\n"
      "v_and_b32 v67, v55, v67\n"
      "v_mov_b32 v16, v64\n"
      "v_mov_b32 v17, v65\n"
      "v_mov_b32 v18, v66\n"
      "v_mov_b32 v19, v67\n"
      "v_mfma_f32_16x16x32_bf16 v[140:143], v[64:67], v[56:59], v[140:143]\n"
      "s_add_u32 s30, s36, 24576\n"
      "buffer_load_dword v204, v2, s[8:11], s30 offen sc1\n"
      "s_add_u32 s30, s36, 49152\n"
      "buffer_load_dword v205, v2, s[8:11], s30 offen sc1\n"
      "s_waitcnt vmcnt(10) lgkmcnt(0)\n"
      "s_barrier\n"
      "ds_read_b64_tr_b16 v[68:69], v53 offset:16896\n"
      "ds_read_b64_tr_b16 v[70:71], v53 offset:18816\n"
      "ds_read_b64_tr_b16 a[52:53], v52 offset:17040\n"
      "ds_read_b64_tr_b16 a[54:55], v52 offset:18960\n"
      "ds_read_b64_tr_b16 a[124:125], v52 offset:17184\n"
      "ds_read_b64_tr_b16 a[126:127], v52 offset:19104\n"
      "ds_read_b64_tr_b16 a[196:197], v52 offset:16896\n"
      "ds_read_b64_tr_b16 a[198:199], v52 offset:18816\n"
      "ds_bpermute_b32 v64, v54, v60\n"
      "ds_bpermute_b32 v65, v54, v61\n"
      "ds_bpermute_b32 v66, v54, v62\n"
      "ds_bpermute_b32 v67, v54, v63\n"
      "v_mfma_f32_16x16x32_bf16 v[136:139], a[48:51], v[56:59], v[136:139]\n"
      "v_mfma_f32_16x16x32_bf16 v[144:147], v[56:59], a[120:123], v[144:147]\n"
      "v_mfma_f32_16x16x32_bf16 v[148:151], a[192:195], v[56:59], v[148:151]\n"
      "s_add_u32 m0, s43, 65536\n"
      "s_nop 0\n"
      "buffer_load_dwordx4 v48, s[12:15], s51 offen offset:0 lds\n"
      "buffer_load_dwordx4 v49, s[12:15], s51 offen offset:1024 lds\n"
      "buffer_load_dwordx4 v50, s[12:15], s51 offen offset:2048 lds\n"
      "buffer_load_dwordx4 v51, s[12:15], s51 offen offset:3072 lds\n"
      "s_add_u32 s51, s51, 110592\n"
      "v_and_b32 v72, v55, v72\n"
      "v_and_b32 v73, v55, v73\n"
      "v_and_b32 v74, v55, v74\n"
      "v_and_b32 v75, v55, v75\n"
      "v_mov_b32 v20, v72\n"
      "v_mov_b32 v21, v73\n"
      "v_mov_b32 v22, v74\n"
      "v_mov_b32 v23, v75\n"
      "v_mfma_f32_16x16x32_bf16 v[140:143], v[72:75], v[56:59], v[140:143]\n"
      "s_waitcnt vmcnt(10) lgkmcnt(0)\n"
      "s_barrier\n"
      "ds_read_b64_tr_b16 v[60:61], v53 offset:33792\n"
      "ds_read_b64_tr_b16 v[62:63], v53 offset:35712\n"
      "ds_read_b64_tr_b16 a[56:57], v52 offset:33936\n"
      "ds_read_b64_tr_b16 a[58:59], v52 offset:35856\n"
      "ds_read_b64_tr_b16 a[128:129], v52 offset:34080\n"
      "ds_read_b64_tr_b16 a[130:131], v52 offset:36000\n"
      "ds_read_b64_tr_b16 a[200:201], v52 offset:33792\n"
      "ds_read_b64_tr_b16 a[202:203], v52 offset:35712\n"
      "ds_bpermute_b32 v72, v54, v68\n"
      "ds_bpermute_b32 v73, v54, v69\n"
      "ds_bpermute_b32 v74, v54, v70\n"
      "ds_bpermute_b32 v75, v54, v71\n"
      "v_mfma_f32_16x16x32_bf16 v[136:139], a[52:55], v[56:59], v[136:139]\n"
      "v_mfma_f32_16x16x32_bf16 v[144:147], v[56:59], a[124:127], v[144:147]\n"
      "v_mfma_f32_16x16x32_bf16 v[148:151], a[196:199], v[56:59], v[148:151]\n"
      "s_add_u32 m0, s43, 82432\n"
      "s_nop 0\n"
      "buffer_load_dwordx4 v48, s[12:15], s51 offen offset:0 lds\n"
      "buffer_load_dwordx4 v49, s[12:15], s51 offen offset:1024 lds\n"
      "buffer_load_dwordx4 v50, s[12:15], s51 offen offset:2048 lds\n"
      "buffer_load_dwordx4 v51, s[12:15], s51 offen offset:3072 lds\n"
      "s_add_u32 s51, s51, 110592\n"
      "v_and_b32 v64, v55, v64\n"
      "v_and_b32 v65, v55, v65\n"
      "v_and_b32 v66, v55, v66\n"
      "v_and_b32 v67, v55, v67\n"
      "v_mov_b32 v24, v64\n"
      "v_mov_b32 v25, v65\n"
      "v_mov_b32 v26, v66\n"
      "v_mov_b32 v27, v67\n"
      "v_mfma_f32_16x16x32_bf16 v[140:143], v[64:67], v[56:59], v[140:143]\n"
      "s_mul_i32 s21, s21, 1664525\n"
      "s_add_u32 s21, s21, 1013904223\n"
      "v_mov_b32 v13, s21\n"
      "buffer_store_dword v13, v10, s[8:11], 0 offen sc1\n"
      "v_not_b32 v200, v13\n"
      "v_not_b32 v201, v13\n"
      "v_not_b32 v202, v13\n"
      "v_not_b32 v203, v13\n"
      "s_add_u32 s30, s40, 0\n"
      "buffer_store_dwordx4 v[200:203], v76, s[8:11], s30 offen sc1\n"
      "v_xor_b32 v13, v13, v14\n"
      "s_add_u32 s52, s40, 4294893568\n"
      "s_add_u32 s40, s40, 4294918144\n"
      "s_add_u32 s36, s36, 24576\n"
      "s_mov_b32 s45, s36\n"
      "s_min_u32 s45, s45, s6\n"
      "s_waitcnt vmcnt(11)\n"
      "v_cmp_ne_u32 vcc, s21, v204\n"
      "s_add_u32 m0, s41, 0\n"
      "s_add_u32 s36, s36, 24576\n"
      "s_mov_b32 s46, s45\n"
      "s_cbranch_vccnz .Lsp_q0_1\n"
      ".Lbk_q0_1:\n"
      "buffer_load_dwordx4 v0, s[8:11], s46 offen offset:1024 sc1 lds\n"
      "buffer_load_dwordx4 v0, s[8:11], s46 offen offset:0 sc1 lds\n"
      "buffer_load_dwordx4 v0, s[8:11], s46 offen offset:2048 sc1 lds\n"
      "s_mov_b32 s45, s36\n"
      "s_min_u32 s45, s45, s6\n"
      "buffer_load_dwordx4 v0, s[8:11], s46 offen offset:3072 sc1 lds\n"
      "s_add_u32 m0, s42, 0\n"
      "s_nop 0\n"
      "buffer_load_dwordx4 v1, s[8:11], s46 offen offset:0 sc1 lds\n"
      "s_waitcnt lgkmcnt(0)\n"
      "s_barrier\n"
      "ds_read_b64_tr_b16 v[68:69], v53 offset:50688\n"
      "ds_read_b64_tr_b16 v[70:71], v53 offset:52608\n"
      "ds_read_b64_tr_b16 a[60:61], v52 offset:50832\n"
      "ds_read_b64_tr_b16 a[62:63], v52 offset:52752\n"
      "ds_read_b64_tr_b16 a[132:133], v52 offset:50976\n"
      "ds_read_b64_tr_b16 a[134:135], v52 offset:52896\n"
      "ds_read_b64_tr_b16 a[204:205], v52 offset:50688\n"
      "ds_read_b64_tr_b16 a[206:207], v52 offset:52608\n"
      "ds_bpermute_b32 v64, v54, v60\n"
      "ds_bpermute_b32 v65, v54, v61\n"
      "ds_bpermute_b32 v66, v54, v62\n"
      "ds_bpermute_b32 v67, v54, v63\n"
      "v_mfma_f32_16x16x32_bf16 v[136:139], a[56:59], v[56:59], v[136:139]\n"
      "v_mfma_f32_16x16x32_bf16 v[144:147], v[56:59], a[128:131], v[144:147]\n"
      "v_mfma_f32_16x16x32_bf16 v[148:151], a[200:203], v[56:59], v[148:151]\n"
      "v_and_b32 v72, v55, v72\n"
      "v_and_b32 v73, v55, v73\n"
      "v_and_b32 v74, v55, v74\n"
      "v_and_b32 v75, v55, v75\n"
      "v_mov_b32 v28, v72\n"
      "v_mov_b32 v29, v73\n"
      "v_mov_b32 v30, v74\n"
      "v_mov_b32 v31, v75\n"
      "v_mfma_f32_16x16x32_bf16 v[140:143], v[72:75], v[56:59], v[140:143]\n"
      "s_waitcnt vmcnt(11) lgkmcnt(0)\n"
      "s_barrier\n"
      "ds_read_b64_tr_b16 v[60:61], v53 offset:0\n"
      "ds_read_b64_tr_b16 v[62:63], v53 offset:1920\n"
      "ds_read_b64_tr_b16 a[64:65], v52 offset:144\n"
      "ds_read_b64_tr_b16 a[66:67], v52 offset:2064\n"
      "ds_read_b64_tr_b16 a[136:137], v52 offset:288\n"
      "ds_read_b64_tr_b16 a[138:139], v52 offset:2208\n"
      "ds_read_b64_tr_b16 a[208:209], v52 offset:0\n"
      "ds_read_b64_tr_b16 a[210:211], v52 offset:1920\n"
      "ds_bpermute_b32 v72, v54, v68\n"
      "ds_bpermute_b32 v73, v54, v69\n"
      "ds_bpermute_b32 v74, v54, v70\n"
      "ds_bpermute_b32 v75, v54, v71\n"
      "v_mfma_f32_16x16x32_bf16 v[136:139], a[60:63], v[56:59], v[136:139]\n"
      "v_mfma_f32_16x16x32_bf16 v[144:147], v[56:59], a[132:135], v[144:147]\n"
      "v_mfma_f32_16x16x32_bf16 v[148:151], a[204:207], v[56:59], v[148:151]\n"
      "v_and_b32 v64, v55, v64\n"
      "v_and_b32 v65, v55, v65\n"
      "v_and_b32 v66, v55, v66\n"
      "v_and_b32 v67, v55, v67\n"
      "v_mov_b32 v32, v64\n"
      "v_mov_b32 v33, v65\n"
      "v_mov_b32 v34, v66\n"
      "v_mov_b32 v35, v67\n"
      "v_mfma_f32_16x16x32_bf16 v[140:143], v[64:67], v[56:59], v[140:143]\n"
      "s_waitcnt vmcnt(7) lgkmcnt(0)\n"
      "s_barrier\n"
      "ds_read_b64_tr_b16 v[68:69], v53 offset:16896\n"
      "ds_read_b64_tr_b16 v[70:71], v53 offset:18816\n"
      "ds_read_b64_tr_b16 a[68:69], v52 offset:17040\n"
      "ds_read_b64_tr_b16 a[70:71], v52 offset:18960\n"
      "ds_read_b64_tr_b16 a[140:141], v52 offset:17184\n"
      "ds_read_b64_tr_b16 a[142:143], v52 offset:19104\n"
      "ds_read_b64_tr_b16 a[212:213], v52 offset:16896\n"
      "ds_read_b64_tr_b16 a[214:215], v52 offset:18816\n"
      "ds_bpermute_b32 v64, v54, v60\n"
      "ds_bpermute_b32 v65, v54, v61\n"
      "ds_bpermute_b32 v66, v54, v62\n"
      "ds_bpermute_b32 v67, v54, v63\n"
      "v_mfma_f32_16x16x32_bf16 v[136:139], a[64:67], v[56:59], v[136:139]\n"
      "v_mfma_f32_16x16x32_bf16 v[144:147], v[56:59], a[136:139], v[144:147]\n"
      "v_mfma_f32_16x16x32_bf16 v[148:151], a[208:211], v[56:59], v[148:151]\n"
      "v_and_b32 v72, v55, v72\n"
      "v_and_b32 v73, v55, v73\n"
      "v_and_b32 v74, v55, v74\n"
      "v_and_b32 v75, v55, v75\n"
      "v_mov_b32 v36, v72\n"
      "v_mov_b32 v37, v73\n"
      "v_mov_b32 v38, v74\n"
      "v_mov_b32 v39, v75\n"
      "v_mfma_f32_16x16x32_bf16 v[140:143], v[72:75], v[56:59], v[140:143]\n"
      "s_waitcnt lgkmcnt(0)\n"
      "ds_bpermute_b32 v72, v54, v68\n"
      "ds_bpermute_b32 v73, v54, v69\n"
      "ds_bpermute_b32 v74, v54, v70\n"
      "ds_bpermute_b32 v75, v54, v71\n"
      "v_mfma_f32_16x16x32_bf16 v[136:139], a[68:71], v[56:59], v[136:139]\n"
      "v_mfma_f32_16x16x32_bf16 v[144:147], v[56:59], a[140:143], v[144:147]\n"
      "v_mfma_f32_16x16x32_bf16 v[148:151], a[212:215], v[56:59], v[148:151]\n"
      "v_and_b32 v64, v55, v64\n"
      "v_and_b32 v65, v55, v65\n"
      "v_and_b32 v66, v55, v66\n"
      "v_and_b32 v67, v55, v67\n"
      "v_mov_b32 v40, v64\n"
      "v_mov_b32 v41, v65\n"
      "v_mov_b32 v42, v66\n"
      "v_mov_b32 v43, v67\n"
      "v_mfma_f32_16x16x32_bf16 v[140:143], v[64:67], v[56:59], v[140:143]\n"
      "s_waitcnt lgkmcnt(0)\n"
      "v_and_b32 v72, v55, v72\n"
      "v_and_b32 v73, v55, v73\n"
      "v_and_b32 v74, v55, v74\n"
      "v_and_b32 v75, v55, v75\n"
      "v_mov_b32 v44, v72\n"
      "v_mov_b32 v45, v73\n"
      "v_mov_b32 v46, v74\n"
      "v_mov_b32 v47, v75\n"
      "v_mfma_f32_16x16x32_bf16 v[140:143], v[72:75], v[56:59], v[140:143]\n"
      "s_add_u32 s36, s36, 24576\n"
      "s_cmp_eq_u32 s36, s37\n"
      "s_cselect_b32 s47, s38, 0\n"
      "s_cselect_b32 s37, s48, s37\n"
      "s_cselect_b32 s49, s39, s53\n"
      "s_add_u32 m0, s41, 20480\n"
      "s_mov_b32 s46, s45\n"
      "s_add_u32 s45, s36, s47\n"
      "v_add_u32 v9, 1, v9\n"
      "v_cmp_ne_u32 vcc, s21, v205\n"
      "s_min_u32 s45, s45, s6\n"
      "s_cbranch_vccnz .Lsp_q1_2\n"
      ".Lbk_q1_2:\n"
      "buffer_load_dwordx4 v0, s[8:11], s46 offen offset:1024 sc1 lds\n"
      "s_waitcnt vmcnt(1)\n"
      "ds_write_b32 v11, v9\n"
      "buffer_load_dwordx4 v0, s[8:11], s46 offen offset:0 sc1 lds\n"
      "buffer_load_dwordx4 v0, s[8:11], s46 offen offset:3072 sc1 lds\n"
      "buffer_load_dword v15, v2, s[8:11], s45 offen sc1\n"
      "ds_read_b32 v10, v12\n"
      "buffer_load_dwordx4 v0, s[8:11], s46 offen offset:2048 sc1 lds\n"
      "s_waitcnt lgkmcnt(0)\n"
      "v_cmp_gt_i32 vcc, v9, v10\n"
      "s_add_u32 m0, s42, 20480\n"
      "s_nop 0\n"
      "buffer_load_dwordx4 v1, s[8:11], s46 offen offset:0 sc1 lds\n"
      "s_cbranch_vccnz .Lsp_qy0_3\n"
      ".Lbk_qy0_3:\n"
      "ds_read_b128 v[52:55], v0 offset:1024\n"
      "ds_read_b128 v[48:51], v0 offset:0\n"
      "ds_read_b128 v[56:59], v0 offset:2048\n"
      "ds_read_b128 v[68:71], v0 offset:5120\n"
      "ds_read_b128 v[64:67], v0 offset:4096\n"
      "ds_read_b128 v[60:63], v0 offset:3072\n"
      "buffer_load_dword v14, v11, s[8:11], s31 offen\n"
      "ds_read2st64_b64 v[212:215], v4 offset0:0 offset1:1\n"
      "ds_read_b128 v[72:75], v0 offset:6144\n"
      "ds_read_b128 v[76:79], v0 offset:7168\n"
      "ds_read_b128 v[88:91], v0 offset:10240\n"
      "ds_read_b128 v[108:111], v0 offset:15360\n"
      "s_mov_b32 s20, 21\n"
      "s_waitcnt lgkmcnt(0)\n"
      "s_nop 4\n"
      "s_branch .Lq_loop\n"
      ".p2align 12\n"
      ".Lq_p0:\n"
      "s_branch .Lq_pr\n"
      ".p2align 5\n"
      ".Lq_loop:\n"
      "v_mfma_f32_16x16x32_bf16 v[120:123], a[0:3], v[48:51], 0\n"
      "ds_read_b128 v[80:83], v0 offset:8192\n"
      "ds_read_b128 v[84:87], v0 offset:9216\n"
      "v_mfma_f32_16x16x32_bf16 v[120:123], a[4:7], v[52:55], v[120:123]\n"
      "ds_read_b128 v[96:99], v0 offset:12288\n"
      "v_mul_f32 v232, 0x3eae1a93, v219\n"
      "v_mfma_f32_16x16x32_bf16 v[120:123], a[8:11], v[56:59], v[120:123]\n"
      "s_waitcnt lgkmcnt(7)\n"
      "ds_read_b128 v[104:107], v0 offset:14336\n"
      "v_fma_f32 v135, -v218, v151, v135\n"
      "v_mfma_f32_16x16x32_bf16 v[120:123], a[12:15], v[60:63], v[120:123]\n"
      "v_fma_f32 v133, -v218, v149, v133\n"
      "v_fma_f32 v134, -v218, v150, v134\n"
      "v_mfma_f32_16x16x32_bf16 v[120:123], a[16:19], v[64:67], v[120:123]\n"
      "ds_read_b128 v[92:95], v0 offset:11264\n"
      "v_fma_f32 v132, -v218, v148, v132\n"
      "v_mfma_f32_16x16x32_bf16 v[120:123], a[20:23], v[68:71], v[120:123]\n"
      "s_waitcnt lgkmcnt(5)\n"
      "ds_read_b64 v[216:217], v4 offset:1024\n"
      "v_cvt_pk_bf16_f32 v176, v132, v133\n"
      "v_mfma_f32_16x16x32_bf16 v[120:123], a[24:27], v[72:75], v[120:123]\n"
      "ds_read_b128 v[100:103], v0 offset:13312\n"
      "v_cvt_pk_bf16_f32 v177, v134, v135\n"
      "v_mfma_f32_16x16x32_bf16 v[120:123], a[28:31], v[76:79], v[120:123]\n"
      "s_waitcnt lgkmcnt(5)\n"
      "v_rcp_f32 v210, v210\n"
      "v_mfma_f32_16x16x32_bf16 v[120:123], a[32:35], v[80:83], v[120:123]\n"
      "v_mul_f32 v204, v204, v210\n"
      "v_add_f32 v212, v212, v214\n"
      "v_mfma_f32_16x16x32_bf16 v[188:191], v[172:175], v[176:179], v[152:155]\n"
      "ds_read_b128 v[112:115], v0 offset:16384\n"
      "v_mfma_f32_16x16x32_bf16 v[120:123], a[36:39], v[84:87], v[120:123]\n"
      "ds_read_b128 v[116:119], v0 offset:17408\n"
      "v_mul_f32 v208, v208, v210\n"
      "v_mfma_f32_16x16x32_bf16 v[120:123], a[40:43], v[88:91], v[120:123]\n"
      "s_waitcnt lgkmcnt(4)\n"
      "s_add_u32 s36, s36, 24576\n"
      "v_mul_f32 v207, v207, v210\n"
      "v_mfma_f32_16x16x32_bf16 v[120:123], a[44:47], v[92:95], v[120:123]\n"
      "v_mul_f32 v209, v209, v210\n"
      "v_cvt_pk_bf16_f32 v230, v208, v209\n"
      "v_mfma_f32_16x16x32_bf16 v[120:123], a[48:51], v[96:99], v[120:123]\n"
      "s_waitcnt lgkmcnt(2)\n"
      "buffer_store_dwordx2 v[230:231], v8, s[8:11], s40 offen sc1\n"
      "v_mfma_f32_16x16x32_bf16 v[120:123], a[52:55], v[100:103], v[120:123]\n"
      "v_mul_f32 v206, v206, v210\n"
      "v_add_f32 v218, v212, v216\n"
      "v_mfma_f32_16x16x32_bf16 v[120:123], a[56:59], v[104:107], v[120:123]\n"
      "s_mov_b32 s46, s45\n"
      "v_add_f32 v213, v213, v215\n"
      "v_mfma_f32_16x16x32_bf16 v[120:123], a[60:63], v[108:111], v[120:123]\n"
      "s_waitcnt lgkmcnt(1)\n"
      "v_cvt_pk_bf16_f32 v229, v206, v207\n"
      "v_mfma_f32_16x16x32_bf16 v[120:123], a[64:67], v[112:115], v[120:123]\n"
      "s_waitcnt lgkmcnt(0)\n"
      "v_mul_f32 v205, v205, v210\n"
      "v_cvt_pk_bf16_f32 v228, v204, v205\n"
      "v_mfma_f32_16x16x32_bf16 v[120:123], a[68:71], v[116:119], v[120:123]\n"
      "buffer_store_dwordx2 v[228:229], v7, s[8:11], s40 offen sc1\n"
      "v_mfma_f32_16x16x32_bf16 v[124:127], a[216:219], v[48:51], 0\n"
      "v_add_f32 v213, v213, v217\n"
      "v_fma_f32 v213, -v218, v218, v213\n"
      "v_mfma_f32_16x16x32_bf16 v[124:127], a[220:223], v[52:55], v[124:127]\n"
      "v_max_f32 v213, 0x3727c5ac, v213\n"
      "s_mov_b32 s45, s36\n"
      "v_mfma_f32_16x16x32_bf16 v[124:127], a[224:227], v[56:59], v[124:127]\n"
      "v_mov_b32 v187, v185\n"
      "v_mfma_f32_16x16x32_bf16 v[124:127], a[228:231], v[60:63], v[124:127]\n"
      "v_rsq_f32 v219, v213\n"
      "v_mfma_f32_16x16x32_bf16 v[124:127], a[232:235], v[64:67], v[124:127]\n"
      "s_add_u32 m0, s41, 40960\n"
      "ds_write_b64 v5, v[218:219] offset:0\n"
      "v_mfma_f32_16x16x32_bf16 v[124:127], a[236:239], v[68:71], v[124:127]\n"
      "s_waitcnt vmcnt(5)\n"
      "v_mov_b32 v186, v184\n"
      "v_cmp_ne_u32 vcc, s21, v15\n"
      "v_mfma_f32_16x16x32_bf16 v[124:127], a[240:243], v[72:75], v[124:127]\n"
      "s_min_u32 s45, s45, s6\n"
      "v_mfma_f32_16x16x32_bf16 v[124:127], a[244:247], v[76:79], v[124:127]\n"
      "v_cndmask_b32 v233, v219, 1.0, s[54:55]\n"
      "v_add_u32 v9, 1, v9\n"
      "v_mfma_f32_16x16x32_bf16 v[124:127], a[248:251], v[80:83], v[124:127]\n"
      "v_fma_f32 v123, -v218, v139, v123\n"
      "v_fma_f32 v122, -v218, v138, v122\n"
      "v_mfma_f32_16x16x32_bf16 v[124:127], a[252:255], v[84:87], v[124:127]\n"
      "v_fma_f32 v120, -v218, v136, v120\n"
      "v_fma_f32 v121, -v218, v137, v121\n"
      "v_mfma_f32_16x16x32_bf16 v[124:127], v[16:19], v[88:91], v[124:127]\n"
      "ds_read_b128 v[224:227], v6 offset:16\n"
      "v_mul_f32 v121, v219, v121\n"
      "v_mfma_f32_16x16x32_bf16 v[124:127], v[20:23], v[92:95], v[124:127]\n"
      "v_mul_f32 v120, v219, v120\n"
      "v_mfma_f32_16x16x32_bf16 v[124:127], v[24:27], v[96:99], v[124:127]\n"
      "s_cbranch_vccnz .Lsp_q2_4\n"
      ".Lbk_q2_4:\n"
      "v_mfma_f32_16x16x32_bf16 v[124:127], v[28:31], v[100:103], v[124:127]\n"
      "buffer_load_dword v15, v2, s[8:11], s45 offen sc1\n"
      "v_mfma_f32_16x16x32_bf16 v[124:127], v[32:35], v[104:107], v[124:127]\n"
      "v_mul_f32 v123, v219, v123\n"
      "v_mul_f32 v122, v219, v122\n"
      "v_mfma_f32_16x16x32_bf16 v[124:127], v[36:39], v[108:111], v[124:127]\n"
      "s_waitcnt vmcnt(4) lgkmcnt(1)\n"
      "ds_write_b32 v11, v9\n"
      "v_mfma_f32_16x16x32_bf16 v[124:127], v[40:43], v[112:115], v[124:127]\n"
      "v_mov_b32 v182, v180\n"
      "v_mfma_f32_16x16x32_bf16 v[124:127], v[44:47], v[116:119], v[124:127]\n"
      "v_cvt_pk_bf16_f32 v173, v122, v123\n"
      "v_mfma_f32_16x16x32_bf16 v[128:131], v[48:51], a[72:75], 0\n"
      "buffer_load_dwordx4 v0, s[8:11], s46 offen offset:1024 sc1 lds\n"
      "v_mfma_f32_16x16x32_bf16 v[128:131], v[52:55], a[76:79], v[128:131]\n"
      "ds_read_b128 v[220:223], v6 offset:0\n"
      "v_cvt_pk_bf16_f32 v172, v120, v121\n"
      "v_mfma_f32_16x16x32_bf16 v[128:131], v[56:59], a[80:83], v[128:131]\n"
      "v_fma_f32 v126, -v218, v142, v126\n"
      "v_fma_f32 v124, -v218, v140, v124\n"
      "v_mfma_f32_16x16x32_bf16 v[128:131], v[60:63], a[84:87], v[128:131]\n"
      "s_add_u32 s52, s52, 24576\n"
      "v_fma_f32 v125, -v218, v141, v125\n"
      "v_mul_f32 v124, v233, v124\n"
      "v_mfma_f32_16x16x32_bf16 v[128:131], v[64:67], a[88:91], v[128:131]\n"
      "v_mul_f32 v126, v219, v126\n"
      "v_mul_f32 v125, v233, v125\n"
      "v_mfma_f32_16x16x32_bf16 v[128:131], v[68:71], a[92:95], v[128:131]\n"
      "v_fma_f32 v127, -v218, v143, v127\n"
      "v_cvt_pk_bf16_f32 v175, v124, v125\n"
      "v_mfma_f32_16x16x32_bf16 v[128:131], v[72:75], a[96:99], v[128:131]\n"
      "v_mul_f32 v127, v219, v127\n"
      "v_cvt_pk_bf16_f32 v174, v126, v127\n"
      "v_mfma_f32_16x16x32_bf16 v[128:131], v[76:79], a[100:103], v[128:131]\n"
      "buffer_load_dwordx4 v0, s[8:11], s46 offen offset:2048 sc1 lds\n"
      "v_mfma_f32_16x16x32_bf16 v[192:195], v[172:175], v[176:179], v[156:159]\n"
      "ds_read_b32 v10, v12\n"
      "v_mfma_f32_16x16x32_bf16 v[196:199], v[172:175], v[164:167], v[168:171]\n"
      "v_mov_b32 v183, v181\n"
      "v_mov_b32 v178, v175\n"
      "v_mfma_f32_16x16x32_bf16 v[128:131], v[80:83], a[104:107], v[128:131]\n"
      "buffer_load_dwordx4 v0, s[8:11], s46 offen offset:0 sc1 lds\n"
      "v_mfma_f32_16x16x32_bf16 v[128:131], v[84:87], a[108:111], v[128:131]\n"
      "v_max_f32 v235, v194, v195\n"
      "v_max_f32 v234, v192, v193\n"
      "v_mfma_f32_16x16x32_bf16 v[128:131], v[88:91], a[112:115], v[128:131]\n"
      "v_max3_f32 v234, v188, v189, v234\n"
      "v_max3_f32 v235, v190, v191, v235\n"
      "v_mfma_f32_16x16x32_bf16 v[128:131], v[92:95], a[116:119], v[128:131]\n"
      "v_max_f32 v236, v234, v235\n"
      "v_mov_b32 v237, v236\n"
      "v_mfma_f32_16x16x32_bf16 v[128:131], v[96:99], a[120:123], v[128:131]\n"
      "v_permlane16_swap_b32 v236, v237\n"
      "v_mfma_f32_16x16x32_bf16 v[128:131], v[100:103], a[124:127], v[128:131]\n"
      "v_max_f32 v236, v236, v237\n"
      "v_mov_b32 v237, v236\n"
      "v_mfma_f32_16x16x32_bf16 v[128:131], v[104:107], a[128:131], v[128:131]\n"
      "s_waitcnt lgkmcnt(0)\n"
      "v_permlane32_swap_b32 v236, v237\n"
      "v_cmp_gt_i32 vcc, v9, v10\n"
      "v_mfma_f32_16x16x32_bf16 v[128:131], v[108:111], a[132:135], v[128:131]\n"
      "buffer_load_dwordx4 v0, s[8:11], s46 offen offset:3072 sc1 lds\n"
      "v_mfma_f32_16x16x32_bf16 v[128:131], v[112:115], a[136:139], v[128:131]\n"
      "s_add_u32 m0, s42, 40960\n"
      "v_max_f32 v236, v236, v237\n"
      "v_mul_f32 v236, v236, v232\n"
      "v_mfma_f32_16x16x32_bf16 v[128:131], v[116:119], a[140:143], v[128:131]\n"
      "s_add_u32 s40, s40, 24576\n"
      "v_fma_f32 v234, v234, v232, -v236\n"
      "v_fma_f32 v235, v235, v232, -v236\n"
      "v_mfma_f32_16x16x32_bf16 v[132:135], a[144:147], v[48:51], 0\n"
      "v_exp_f32 v234, v234\n"
      "v_mfma_f32_16x16x32_bf16 v[132:135], a[148:151], v[52:55], v[132:135]\n"
      "s_cbranch_vccnz .Lsp_qy1_5\n"
      ".Lbk_qy1_5:\n"
      "ds_read_b128 v[48:51], v0 offset:20480\n"
      "v_mfma_f32_16x16x32_bf16 v[132:135], a[152:155], v[56:59], v[132:135]\n"
      "v_exp_f32 v235, v235\n"
      "v_mfma_f32_16x16x32_bf16 v[132:135], a[156:159], v[60:63], v[132:135]\n"
      "ds_read_b128 v[56:59], v0 offset:22528\n"
      "v_fma_f32 v130, -v224, v147, v130\n"
      "v_mfma_f32_16x16x32_bf16 v[132:135], a[160:163], v[64:67], v[132:135]\n"
      "ds_read_b64 v[216:217], v4 offset:21504\n"
      "v_fma_f32 v129, -v222, v146, v129\n"
      "v_mfma_f32_16x16x32_bf16 v[132:135], a[164:167], v[68:71], v[132:135]\n"
      "buffer_load_dwordx4 v1, s[8:11], s46 offen offset:0 sc1 lds\n"
      "v_mfma_f32_16x16x32_bf16 v[132:135], a[168:171], v[72:75], v[132:135]\n"
      "v_cvt_pk_bf16_f32 v234, v234, v234\n"
      "v_fma_f32 v131, -v226, v144, v131\n"
      "v_mfma_f32_16x16x32_bf16 v[132:135], a[172:175], v[76:79], v[132:135]\n"
      "ds_read_b128 v[60:63], v0 offset:23552\n"
      "v_and_b32 v200, v160, v234\n"
      "v_mfma_f32_16x16x32_bf16 v[132:135], a[176:179], v[80:83], v[132:135]\n"
      "v_cvt_pk_bf16_f32 v235, v235, v235\n"
      "v_mul_f32 v129, v223, v129\n"
      "v_mfma_f32_16x16x32_bf16 v[132:135], a[180:183], v[84:87], v[132:135]\n"
      "ds_read_b128 v[52:55], v0 offset:21504\n"
      "v_mul_f32 v131, v227, v131\n"
      "v_mfma_f32_16x16x32_bf16 v[132:135], a[184:187], v[88:91], v[132:135]\n"
      "ds_read_b128 v[88:91], v0 offset:30720\n"
      "v_mul_f32 v130, v225, v130\n"
      "v_cvt_pk_bf16_f32 v185, v198, v199\n"
      "v_mfma_f32_16x16x32_bf16 v[132:135], a[188:191], v[92:95], v[132:135]\n"
      "v_cvt_pk_bf16_f32 v184, v196, v197\n"
      "v_and_b32 v201, v161, v235\n"
      "v_fma_f32 v128, -v220, v145, v128\n"
      "v_mfma_f32_16x16x32_bf16 v[132:135], a[192:195], v[96:99], v[132:135]\n"
      "ds_read_b128 v[68:71], v0 offset:25600\n"
      "v_and_b32 v203, v163, v235\n"
      "v_mfma_f32_16x16x32_bf16 v[132:135], a[196:199], v[100:103], v[132:135]\n"
      "v_mul_f32 v128, v221, v128\n"
      "v_and_b32 v202, v162, v234\n"
      "v_mfma_f32_16x16x32_bf16 v[132:135], a[200:203], v[104:107], v[132:135]\n"
      "v_cvt_pk_bf16_f32 v180, v128, v129\n"
      "v_cvt_pk_bf16_f32 v181, v130, v131\n"
      "v_mfma_f32_16x16x32_bf16 v[208:211], v[184:187], v[200:203], 0\n"
      "ds_read_b128 v[64:67], v0 offset:24576\n"
      "ds_read_b128 v[72:75], v0 offset:26624\n"
      "v_mfma_f32_16x16x32_bf16 v[204:207], v[180:183], v[200:203], 0\n"
      "s_waitcnt vmcnt(6) lgkmcnt(4)\n"
      "buffer_store_dword v13, v3, s[8:11], s52 offen sc1\n"
      "v_mfma_f32_16x16x32_bf16 v[132:135], a[204:207], v[108:111], v[132:135]\n"
      "ds_read2st64_b64 v[212:215], v4 offset0:40 offset1:41\n"
      "v_mfma_f32_16x16x32_bf16 v[132:135], a[208:211], v[112:115], v[132:135]\n"
      "v_rcp_f32 v210, v210\n"
      "v_mfma_f32_16x16x32_bf16 v[132:135], a[212:215], v[116:119], v[132:135]\n"
      "ds_read_b128 v[76:79], v0 offset:27648\n"
      "v_mul_f32 v209, v209, v210\n"
      "v_mfma_f32_16x16x32_bf16 v[120:123], a[0:3], v[48:51], 0\n"
      "ds_read_b128 v[80:83], v0 offset:28672\n"
      "ds_read_b128 v[100:103], v0 offset:33792\n"
      "v_mfma_f32_16x16x32_bf16 v[120:123], a[4:7], v[52:55], v[120:123]\n"
      "ds_read_b128 v[92:95], v0 offset:31744\n"
      "v_mul_f32 v208, v208, v210\n"
      "v_mfma_f32_16x16x32_bf16 v[120:123], a[8:11], v[56:59], v[120:123]\n"
      "ds_read_b128 v[84:87], v0 offset:29696\n"
      "v_fma_f32 v134, -v218, v150, v134\n"
      "v_mfma_f32_16x16x32_bf16 v[120:123], a[12:15], v[60:63], v[120:123]\n"
      "s_waitcnt lgkmcnt(6)\n"
      "ds_read_b128 v[104:107], v0 offset:34816\n"
      "v_fma_f32 v132, -v218, v148, v132\n"
      "v_mfma_f32_16x16x32_bf16 v[120:123], a[16:19], v[64:67], v[120:123]\n"
      "s_mov_b32 s50, s49\n"
      "ds_read_b128 v[96:99], v0 offset:32768\n"
      "v_mfma_f32_16x16x32_bf16 v[120:123], a[20:23], v[68:71], v[120:123]\n"
      "ds_read_b128 v[108:111], v0 offset:35840\n"
      "v_fma_f32 v133, -v218, v149, v133\n"
      "v_mfma_f32_16x16x32_bf16 v[120:123], a[24:27], v[72:75], v[120:123]\n"
      "s_waitcnt lgkmcnt(7)\n"
      "v_fma_f32 v135, -v218, v151, v135\n"
      "v_cvt_pk_bf16_f32 v177, v134, v135\n"
      "v_mfma_f32_16x16x32_bf16 v[120:123], a[28:31], v[76:79], v[120:123]\n"
      "s_waitcnt lgkmcnt(5)\n"
      "ds_read_b128 v[112:115], v0 offset:36864\n"
      "v_cvt_pk_bf16_f32 v176, v132, v133\n"
      "v_mfma_f32_16x16x32_bf16 v[120:123], a[32:35], v[80:83], v[120:123]\n"
      "v_mov_b32 v187, v185\n"
      "v_mul_f32 v232, 0x3eae1a93, v219\n"
      "v_mfma_f32_16x16x32_bf16 v[188:191], v[172:175], v[176:179], v[152:155]\n"
      "s_waitcnt lgkmcnt(4)\n"
      "ds_read_b128 v[116:119], v0 offset:37888\n"
      "v_mul_f32 v207, v207, v210\n"
      "v_mfma_f32_16x16x32_bf16 v[120:123], a[36:39], v[84:87], v[120:123]\n"
      "v_mul_f32 v204, v204, v210\n"
      "v_mfma_f32_16x16x32_bf16 v[120:123], a[40:43], v[88:91], v[120:123]\n"
      "v_cvt_pk_bf16_f32 v230, v208, v209\n"
      "v_add_f32 v213, v213, v215\n"
      "v_mfma_f32_16x16x32_bf16 v[120:123], a[44:47], v[92:95], v[120:123]\n"
      "s_waitcnt lgkmcnt(2)\n"
      "v_mul_f32 v205, v205, v210\n"
      "v_mfma_f32_16x16x32_bf16 v[120:123], a[48:51], v[96:99], v[120:123]\n"
      "buffer_store_dwordx2 v[230:231], v8, s[8:11], s40 offen sc1\n"
      "v_mfma_f32_16x16x32_bf16 v[120:123], a[52:55], v[100:103], v[120:123]\n"
      "v_add_f32 v212, v212, v214\n"
      "v_mfma_f32_16x16x32_bf16 v[120:123], a[56:59], v[104:107], v[120:123]\n"
      "v_cvt_pk_bf16_f32 v228, v204, v205\n"
      "v_mul_f32 v206, v206, v210\n"
      "v_mfma_f32_16x16x32_bf16 v[120:123], a[60:63], v[108:111], v[120:123]\n"
      "s_waitcnt vmcnt(7) lgkmcnt(0)\n"
      "v_cvt_pk_bf16_f32 v229, v206, v207\n"
      "v_cmp_ne_u32 vcc, s21, v15\n"
      "v_mfma_f32_16x16x32_bf16 v[120:123], a[64:67], v[112:115], v[120:123]\n"
      "s_add_u32 m0, s41, 0\n"
      "v_mov_b32 v182, v180\n"
      "v_mfma_f32_16x16x32_bf16 v[120:123], a[68:71], v[116:119], v[120:123]\n"
      "buffer_store_dwordx2 v[228:229], v7, s[8:11], s40 offen sc1\n"
      "v_mfma_f32_16x16x32_bf16 v[124:127], a[216:219], v[48:51], 0\n"
      "v_add_f32 v218, v212, v216\n"
      "v_add_f32_e64 v213, v213, v217\n"
      "v_mfma_f32_16x16x32_bf16 v[124:127], a[220:223], v[52:55], v[124:127]\n"
      "s_add_u32 s36, s36, 24576\n"
      "v_fma_f32 v213, -v218, v218, v213\n"
      "v_mfma_f32_16x16x32_bf16 v[124:127], a[224:227], v[56:59], v[124:127]\n"
      "s_mov_b32 s46, s45\n"
      "s_cbranch_vccnz .Lsp_q0_6\n"
      ".Lbk_q0_6:\n"
      "v_mfma_f32_16x16x32_bf16 v[124:127], a[228:231], v[60:63], v[124:127]\n"
      "buffer_load_dwordx4 v0, s[8:11], s46 offen offset:1024 sc1 lds\n"
      "v_mfma_f32_16x16x32_bf16 v[124:127], a[232:235], v[64:67], v[124:127]\n"
      "v_max_f32 v213, 0x3727c5ac, v213\n"
      "v_fma_f32 v120, -v218, v136, v120\n"
      "v_mfma_f32_16x16x32_bf16 v[124:127], a[236:239], v[68:71], v[124:127]\n"
      "v_mov_b32 v186, v184\n"
      "v_mfma_f32_16x16x32_bf16 v[124:127], a[240:243], v[72:75], v[124:127]\n"
      "v_add_u32 v9, 1, v9\n"
      "v_mfma_f32_16x16x32_bf16 v[124:127], a[244:247], v[76:79], v[124:127]\n"
      "s_add_u32 s40, s40, 24576\n"
      "v_fma_f32 v123, -v218, v139, v123\n"
      "v_mfma_f32_16x16x32_bf16 v[124:127], a[248:251], v[80:83], v[124:127]\n"
      "v_rsq_f32 v219, v213\n"
      "v_mfma_f32_16x16x32_bf16 v[124:127], a[252:255], v[84:87], v[124:127]\n"
      "v_mfma_f32_16x16x32_bf16 v[124:127], v[16:19], v[88:91], v[124:127]\n"
      "v_fma_f32 v121, -v218, v137, v121\n"
      "v_fma_f32 v122, -v218, v138, v122\n"
      "v_mfma_f32_16x16x32_bf16 v[124:127], v[20:23], v[92:95], v[124:127]\n"
      "buffer_load_dwordx4 v0, s[8:11], s46 offen offset:0 sc1 lds\n"
      "v_mfma_f32_16x16x32_bf16 v[124:127], v[24:27], v[96:99], v[124:127]\n"
      "v_mul_f32 v120, v219, v120\n"
      "v_mul_f32 v122, v219, v122\n"
      "v_mfma_f32_16x16x32_bf16 v[124:127], v[28:31], v[100:103], v[124:127]\n"
      "ds_write_b64 v5, v[218:219] offset:512\n"
      "v_mfma_f32_16x16x32_bf16 v[124:127], v[32:35], v[104:107], v[124:127]\n"
      "v_mul_f32 v121, v219, v121\n"
      "v_mul_f32 v123, v219, v123\n"
      "v_mfma_f32_16x16x32_bf16 v[124:127], v[36:39], v[108:111], v[124:127]\n"
      "buffer_load_dwordx4 v0, s[8:11], s46 offen offset:2048 sc1 lds\n"
      "v_mfma_f32_16x16x32_bf16 v[124:127], v[40:43], v[112:115], v[124:127]\n"
      "s_mov_b32 s45, s36\n"
      "ds_read_b128 v[224:227], v6 offset:528\n"
      "v_mfma_f32_16x16x32_bf16 v[124:127], v[44:47], v[116:119], v[124:127]\n"
      "s_min_u32 s45, s45, s6\n"
      "v_cndmask_b32 v233, v219, 1.0, s[54:55]\n"
      "v_mfma_f32_16x16x32_bf16 v[128:131], v[48:51], a[72:75], 0\n"
      "s_add_u32 s52, s52, 24576\n"
      "v_cvt_pk_bf16_f32 v172, v120, v121\n"
      "v_cvt_pk_bf16_f32 v173, v122, v123\n"
      "v_mfma_f32_16x16x32_bf16 v[128:131], v[52:55], a[76:79], v[128:131]\n"
      "buffer_load_dwordx4 v0, s[8:11], s46 offen offset:3072 sc1 lds\n"
      "v_mfma_f32_16x16x32_bf16 v[128:131], v[56:59], a[80:83], v[128:131]\n"
      "v_fma_f32 v126, -v218, v142, v126\n"
      "v_fma_f32 v124, -v218, v140, v124\n"
      "v_mul_f32 v126, v219, v126\n"
      "v_mfma_f32_16x16x32_bf16 v[128:131], v[60:63], a[84:87], v[128:131]\n"
      "v_fma_f32 v125, -v218, v141, v125\n"
      "v_mul_f32 v125, v233, v125\n"
      "v_mfma_f32_16x16x32_bf16 v[128:131], v[64:67], a[88:91], v[128:131]\n"
      "s_waitcnt vmcnt(7) lgkmcnt(1)\n"
      "ds_write_b32 v11, v9\n"
      "v_mfma_f32_16x16x32_bf16 v[128:131], v[68:71], a[92:95], v[128:131]\n"
      "v_fma_f32 v127, -v218, v143, v127\n"
      "v_mul_f32 v124, v233, v124\n"
      "v_mul_f32 v127, v219, v127\n"
      "v_mfma_f32_16x16x32_bf16 v[128:131], v[72:75], a[96:99], v[128:131]\n"
      "v_cvt_pk_bf16_f32 v174, v126, v127\n"
      "v_cvt_pk_bf16_f32 v175, v124, v125\n"
      "v_mfma_f32_16x16x32_bf16 v[128:131], v[76:79], a[100:103], v[128:131]\n"
      "buffer_load_dword v15, v2, s[8:11], s45 offen sc1\n"
      "v_mfma_f32_16x16x32_bf16 v[192:195], v[172:175], v[176:179], v[156:159]\n"
      "v_mov_b32 v183, v181\n"
      "v_mfma_f32_16x16x32_bf16 v[196:199], v[172:175], v[164:167], v[168:171]\n"
      "ds_read_b128 v[220:223], v6 offset:512\n"
      "s_add_u32 m0, s42, 0\n"
      "v_mfma_f32_16x16x32_bf16 v[128:131], v[80:83], a[104:107], v[128:131]\n"
      "buffer_load_dwordx4 v1, s[8:11], s46 offen offset:0 sc1 lds\n"
      "v_mfma_f32_16x16x32_bf16 v[128:131], v[84:87], a[108:111], v[128:131]\n"
      "v_max_f32 v235, v194, v195\n"
      "v_max3_f32 v235, v190, v191, v235\n"
      "v_mfma_f32_16x16x32_bf16 v[128:131], v[88:91], a[112:115], v[128:131]\n"
      "v_max_f32 v234, v192, v193\n"
      "v_max3_f32 v234, v188, v189, v234\n"
      "v_mfma_f32_16x16x32_bf16 v[128:131], v[92:95], a[116:119], v[128:131]\n"
      "ds_read_b32 v10, v12\n"
      "v_max_f32 v236, v234, v235\n"
      "v_mfma_f32_16x16x32_bf16 v[128:131], v[96:99], a[120:123], v[128:131]\n"
      "v_mov_b32 v237, v236\n"
      "v_cvt_pk_bf16_f32 v185, v198, v199\n"
      "v_mfma_f32_16x16x32_bf16 v[128:131], v[100:103], a[124:127], v[128:131]\n"
      "v_permlane16_swap_b32 v236, v237\n"
      "v_mfma_f32_16x16x32_bf16 v[128:131], v[104:107], a[128:131], v[128:131]\n"
      "v_max_f32 v236, v236, v237\n"
      "v_mov_b32 v237, v236\n"
      "v_mfma_f32_16x16x32_bf16 v[128:131], v[108:111], a[132:135], v[128:131]\n"
      "v_permlane32_swap_b32 v236, v237\n"
      "v_mfma_f32_16x16x32_bf16 v[128:131], v[112:115], a[136:139], v[128:131]\n"
      "s_waitcnt lgkmcnt(0)\n"
      "v_cmp_gt_i32 vcc, v9, v10\n"
      "v_max_f32 v236, v236, v237\n"
      "v_mfma_f32_16x16x32_bf16 v[128:131], v[116:119], a[140:143], v[128:131]\n"
      "v_mul_f32_e64 v236, v236, v232\n"
      "v_fma_f32 v235, v235, v232, -v236\n"
      "v_mfma_f32_16x16x32_bf16 v[132:135], a[144:147], v[48:51], 0\n"
      "v_exp_f32_e64 v235, v235\n"
      "v_mfma_f32_16x16x32_bf16 v[132:135], a[148:151], v[52:55], v[132:135]\n"
      "s_cbranch_vccnz .Lsp_qy2_7\n"
      ".Lbk_qy2_7:\n"
      "v_cvt_pk_bf16_f32 v235, v235, v235\n"
      "v_mfma_f32_16x16x32_bf16 v[132:135], a[152:155], v[56:59], v[132:135]\n"
      "v_fma_f32 v130, -v224, v147, v130\n"
      "v_mul_f32 v130, v225, v130\n"
      "v_mfma_f32_16x16x32_bf16 v[132:135], a[156:159], v[60:63], v[132:135]\n"
      "v_fma_f32 v129, -v222, v146, v129\n"
      "v_cvt_pk_bf16_f32 v184, v196, v197\n"
      "v_mfma_f32_16x16x32_bf16 v[132:135], a[160:163], v[64:67], v[132:135]\n"
      "v_fma_f32 v234, v234, v232, -v236\n"
      "v_fma_f32 v131, -v226, v144, v131\n"
      "v_mfma_f32_16x16x32_bf16 v[132:135], a[164:167], v[68:71], v[132:135]\n"
      "ds_read_b128 v[52:55], v0 offset:41984\n"
      "ds_read_b128 v[64:67], v0 offset:45056\n"
      "v_mfma_f32_16x16x32_bf16 v[132:135], a[168:171], v[72:75], v[132:135]\n"
      "ds_read_b128 v[56:59], v0 offset:43008\n"
      "v_mul_f32 v129, v223, v129\n"
      "v_mfma_f32_16x16x32_bf16 v[132:135], a[172:175], v[76:79], v[132:135]\n"
      "v_exp_f32 v234, v234\n"
      "v_mfma_f32_16x16x32_bf16 v[132:135], a[176:179], v[80:83], v[132:135]\n"
      "v_fma_f32 v128, -v220, v145, v128\n"
      "v_and_b32 v203, v163, v235\n"
      "v_mfma_f32_16x16x32_bf16 v[132:135], a[180:183], v[84:87], v[132:135]\n"
      "v_cvt_pk_bf16_f32 v234, v234, v234\n"
      "v_and_b32 v201, v161, v235\n"
      "v_mfma_f32_16x16x32_bf16 v[132:135], a[184:187], v[88:91], v[132:135]\n"
      "ds_read_b128 v[48:51], v0 offset:40960\n"
      "v_and_b32 v202, v162, v234\n"
      "v_mfma_f32_16x16x32_bf16 v[132:135], a[188:191], v[92:95], v[132:135]\n"
      "ds_read_b128 v[60:63], v0 offset:44032\n"
      "v_mul_f32 v128, v221, v128\n"
      "v_mfma_f32_16x16x32_bf16 v[132:135], a[192:195], v[96:99], v[132:135]\n"
      "ds_read_b128 v[76:79], v0 offset:48128\n"
      "v_cvt_pk_bf16_f32 v180, v128, v129\n"
      "v_mfma_f32_16x16x32_bf16 v[132:135], a[196:199], v[100:103], v[132:135]\n"
      "ds_read_b128 v[100:103], v0 offset:54272\n"
      "v_and_b32 v200, v160, v234\n"
      "v_mfma_f32_16x16x32_bf16 v[132:135], a[200:203], v[104:107], v[132:135]\n"
      "v_mul_f32 v131, v227, v131\n"
      "v_cvt_pk_bf16_f32 v181, v130, v131\n"
      "v_mfma_f32_16x16x32_bf16 v[208:211], v[184:187], v[200:203], 0\n"
      "s_waitcnt vmcnt(6) lgkmcnt(4)\n"
      "buffer_store_dword v13, v3, s[8:11], s52 offen sc1\n"
      "v_mfma_f32_16x16x32_bf16 v[204:207], v[180:183], v[200:203], 0\n"
      "ds_read_b128 v[84:87], v0 offset:50176\n"
      "ds_read_b128 v[72:75], v0 offset:47104\n"
      "v_mfma_f32_16x16x32_bf16 v[132:135], a[204:207], v[108:111], v[132:135]\n"
      "ds_read_b128 v[68:71], v0 offset:46080\n"
      "v_mov_b32 v178, v175\n"
      "v_mfma_f32_16x16x32_bf16 v[132:135], a[208:211], v[112:115], v[132:135]\n"
      "v_rcp_f32 v210, v210\n"
      "v_mfma_f32_16x16x32_bf16 v[132:135], a[212:215], v[116:119], v[132:135]\n"
      "s_waitcnt lgkmcnt(4)\n"
      "ds_read_b128 v[92:95], v0 offset:52224\n"
      "v_mul_f32 v205, v205, v210\n"
      "v_mfma_f32_16x16x32_bf16 v[120:123], a[0:3], v[48:51], 0\n"
      "s_add_u32 s36, s36, 24576\n"
      "ds_read_b128 v[80:83], v0 offset:49152\n"
      "v_mov_b32 v182, v180\n"
      "v_mfma_f32_16x16x32_bf16 v[120:123], a[4:7], v[52:55], v[120:123]\n"
      "ds_read_b128 v[96:99], v0 offset:53248\n"
      "v_mfma_f32_16x16x32_bf16 v[120:123], a[8:11], v[56:59], v[120:123]\n"
      "ds_read_b128 v[88:91], v0 offset:51200\n"
      "v_fma_f32 v132, -v218, v148, v132\n"
      "v_mfma_f32_16x16x32_bf16 v[120:123], a[12:15], v[60:63], v[120:123]\n"
      "s_cmp_eq_u32 s36, s37\n"
      "v_fma_f32 v133, -v218, v149, v133\n"
      "v_cvt_pk_bf16_f32 v176, v132, v133\n"
      "v_mfma_f32_16x16x32_bf16 v[120:123], a[16:19], v[64:67], v[120:123]\n"
      "s_waitcnt lgkmcnt(4)\n"
      "ds_read2st64_b64 v[212:215], v4 offset0:80 offset1:81\n"
      "s_cselect_b32 s47, s38, 0\n"
      "v_mfma_f32_16x16x32_bf16 v[120:123], a[20:23], v[68:71], v[120:123]\n"
      "s_cselect_b32 s37, s48, s37\n"
      "ds_read_b128 v[112:115], v0 offset:57344\n"
      "v_fma_f32 v135, -v218, v151, v135\n"
      "v_mfma_f32_16x16x32_bf16 v[120:123], a[24:27], v[72:75], v[120:123]\n"
      "s_cselect_b32 s49, s39, s53\n"
      "v_fma_f32 v134, -v218, v150, v134\n"
      "v_mov_b32 v183, v181\n"
      "v_mfma_f32_16x16x32_bf16 v[120:123], a[28:31], v[76:79], v[120:123]\n"
      "s_waitcnt lgkmcnt(4)\n"
      "ds_read_b128 v[104:107], v0 offset:55296\n"
      "v_cvt_pk_bf16_f32 v177, v134, v135\n"
      "v_mfma_f32_16x16x32_bf16 v[120:123], a[32:35], v[80:83], v[120:123]\n"
      "ds_read_b128 v[108:111], v0 offset:56320\n"
      "v_mul_f32 v206, v206, v210\n"
      "v_mfma_f32_16x16x32_bf16 v[188:191], v[172:175], v[176:179], v[152:155]\n"
      "ds_read_b64 v[216:217], v4 offset:41984\n"
      "v_mul_f32 v207, v207, v210\n"
      "v_mfma_f32_16x16x32_bf16 v[120:123], a[36:39], v[84:87], v[120:123]\n"
      "s_waitcnt lgkmcnt(5)\n"
      "s_add_u32 m0, s41, 20480\n"
      "ds_read_b128 v[116:119], v0 offset:58368\n"
      "v_mfma_f32_16x16x32_bf16 v[120:123], a[40:43], v[88:91], v[120:123]\n"
      "s_mov_b32 s46, s45\n"
      "s_add_u32 s45, s36, s47\n"
      "v_mul_f32 v209, v209, v210\n"
      "v_mfma_f32_16x16x32_bf16 v[120:123], a[44:47], v[92:95], v[120:123]\n"
      "v_cvt_pk_bf16_f32 v229, v206, v207\n"
      "v_mfma_f32_16x16x32_bf16 v[120:123], a[48:51], v[96:99], v[120:123]\n"
      "s_waitcnt lgkmcnt(4)\n"
      "v_add_f32 v213, v213, v215\n"
      "v_mfma_f32_16x16x32_bf16 v[120:123], a[52:55], v[100:103], v[120:123]\n"
      "s_waitcnt lgkmcnt(3)\n"
      "v_mul_f32 v232, 0x3eae1a93, v219\n"
      "v_mul_f32 v204, v204, v210\n"
      "v_mfma_f32_16x16x32_bf16 v[120:123], a[56:59], v[104:107], v[120:123]\n"
      "s_waitcnt lgkmcnt(2)\n"
      "v_mul_f32 v208, v208, v210\n"
      "v_cvt_pk_bf16_f32 v230, v208, v209\n"
      "v_mfma_f32_16x16x32_bf16 v[120:123], a[60:63], v[108:111], v[120:123]\n"
      "v_mfma_f32_16x16x32_bf16 v[120:123], a[64:67], v[112:115], v[120:123]\n"
      "s_waitcnt lgkmcnt(0)\n"
      "buffer_store_dwordx2 v[230:231], v8, s[8:11], s40 offen sc1\n"
      "v_mfma_f32_16x16x32_bf16 v[120:123], a[68:71], v[116:119], v[120:123]\n"
      "v_cvt_pk_bf16_f32 v228, v204, v205\n"
      "v_mfma_f32_16x16x32_bf16 v[124:127], a[216:219], v[48:51], 0\n"
      "v_add_f32 v212, v212, v214\n"
      "v_add_f32 v213, v213, v217\n"
      "v_mfma_f32_16x16x32_bf16 v[124:127], a[220:223], v[52:55], v[124:127]\n"
      "v_add_f32 v218, v212, v216\n"
      "v_fma_f32 v213, -v218, v218, v213\n"
      "v_mfma_f32_16x16x32_bf16 v[124:127], a[224:227], v[56:59], v[124:127]\n"
      "buffer_store_dwordx2 v[228:229], v7, s[8:11], s40 offen sc1\n"
      "v_mfma_f32_16x16x32_bf16 v[124:127], a[228:231], v[60:63], v[124:127]\n"
      "s_add_u32 s52, s52, 24576\n"
      "v_fma_f32 v123, -v218, v139, v123\n"
      "v_add_u32 v9, 1, v9\n"
      "v_mfma_f32_16x16x32_bf16 v[124:127], a[232:235], v[64:67], v[124:127]\n"
      "v_fma_f32 v122, -v218, v138, v122\n"
      "v_mfma_f32_16x16x32_bf16 v[124:127], a[236:239], v[68:71], v[124:127]\n"
      "s_waitcnt vmcnt(4)\n"
      "v_max_f32 v213, 0x3727c5ac, v213\n"
      "v_cmp_ne_u32 vcc, s21, v15\n"
      "v_mfma_f32_16x16x32_bf16 v[124:127], a[240:243], v[72:75], v[124:127]\n"
      "v_rsq_f32 v219, v213\n"
      "v_mfma_f32_16x16x32_bf16 v[124:127], a[244:247], v[76:79], v[124:127]\n"
      "v_mov_b32 v186, v184\n"
      "v_fma_f32 v120, -v218, v136, v120\n"
      "v_mfma_f32_16x16x32_bf16 v[124:127], a[248:251], v[80:83], v[124:127]\n"
      "s_min_u32 s45, s45, s6\n"
      "ds_write_b64 v5, v[218:219] offset:1024\n"
      "v_mfma_f32_16x16x32_bf16 v[124:127], a[252:255], v[84:87], v[124:127]\n"
      "s_cbranch_vccnz .Lsp_q1_8\n"
      ".Lbk_q1_8:\n"
      "v_mfma_f32_16x16x32_bf16 v[124:127], v[16:19], v[88:91], v[124:127]\n"
      "buffer_load_dwordx4 v0, s[8:11], s46 offen offset:1024 sc1 lds\n"
      "v_mfma_f32_16x16x32_bf16 v[124:127], v[20:23], v[92:95], v[124:127]\n"
      "v_cndmask_b32 v233, v219, 1.0, s[54:55]\n"
      "v_mul_f32 v123, v219, v123\n"
      "v_mfma_f32_16x16x32_bf16 v[124:127], v[24:27], v[96:99], v[124:127]\n"
      "ds_read_b128 v[220:223], v6 offset:1024\n"
      "v_mul_f32 v120, v219, v120\n"
      "v_mfma_f32_16x16x32_bf16 v[124:127], v[28:31], v[100:103], v[124:127]\n"
      "v_mul_f32 v122, v219, v122\n"
      "v_fma_f32 v121, -v218, v137, v121\n"
      "v_mfma_f32_16x16x32_bf16 v[124:127], v[32:35], v[104:107], v[124:127]\n"
      "s_waitcnt vmcnt(4)\n"
      "ds_write_b32 v11, v9\n"
      "v_mfma_f32_16x16x32_bf16 v[124:127], v[36:39], v[108:111], v[124:127]\n"
      "buffer_load_dwordx4 v0, s[8:11], s46 offen offset:0 sc1 lds\n"
      "v_mfma_f32_16x16x32_bf16 v[124:127], v[40:43], v[112:115], v[124:127]\n"
      "v_mfma_f32_16x16x32_bf16 v[124:127], v[44:47], v[116:119], v[124:127]\n"
      "v_cvt_pk_bf16_f32 v173, v122, v123\n"
      "v_mfma_f32_16x16x32_bf16 v[128:131], v[48:51], a[72:75], 0\n"
      "v_mul_f32 v121, v219, v121\n"
      "v_cvt_pk_bf16_f32 v172, v120, v121\n"
      "v_mfma_f32_16x16x32_bf16 v[128:131], v[52:55], a[76:79], v[128:131]\n"
      "buffer_load_dwordx4 v0, s[8:11], s46 offen offset:3072 sc1 lds\n"
      "v_mfma_f32_16x16x32_bf16 v[128:131], v[56:59], a[80:83], v[128:131]\n"
      "v_fma_f32 v125, -v218, v141, v125\n"
      "v_fma_f32 v124, -v218, v140, v124\n"
      "v_mfma_f32_16x16x32_bf16 v[128:131], v[60:63], a[84:87], v[128:131]\n"
      "v_fma_f32 v127, -v218, v143, v127\n"
      "v_mul_f32 v125, v233, v125\n"
      "v_mfma_f32_16x16x32_bf16 v[128:131], v[64:67], a[88:91], v[128:131]\n"
      "v_mul_f32 v124, v233, v124\n"
      "v_mul_f32 v127, v219, v127\n"
      "v_mfma_f32_16x16x32_bf16 v[128:131], v[68:71], a[92:95], v[128:131]\n"
      "v_fma_f32 v126, -v218, v142, v126\n"
      "v_mul_f32 v126, v219, v126\n"
      "v_mfma_f32_16x16x32_bf16 v[128:131], v[72:75], a[96:99], v[128:131]\n"
      "v_cvt_pk_bf16_f32 v175, v124, v125\n"
      "v_cvt_pk_bf16_f32 v174, v126, v127\n"
      "v_mfma_f32_16x16x32_bf16 v[128:131], v[76:79], a[100:103], v[128:131]\n"
      "buffer_load_dword v15, v2, s[8:11], s45 offen sc1\n"
      "v_mfma_f32_16x16x32_bf16 v[192:195], v[172:175], v[176:179], v[156:159]\n"
      "ds_read_b128 v[224:227], v6 offset:1040\n"
      "v_mov_b32 v187, v185\n"
      "v_mfma_f32_16x16x32_bf16 v[196:199], v[172:175], v[164:167], v[168:171]\n"
      "ds_read_b32 v10, v12\n"
      "v_mfma_f32_16x16x32_bf16 v[128:131], v[80:83], a[104:107], v[128:131]\n"
      "buffer_load_dwordx4 v0, s[8:11], s46 offen offset:2048 sc1 lds\n"
      "v_mfma_f32_16x16x32_bf16 v[128:131], v[84:87], a[108:111], v[128:131]\n"
      "v_med3_f32 v234, v192, v193, s50\n"
      "v_med3_f32 v235, v194, v195, s50\n"
      "v_mfma_f32_16x16x32_bf16 v[128:131], v[88:91], a[112:115], v[128:131]\n"
      "v_cvt_pk_bf16_f32 v185, v198, v199\n"
      "v_max3_f32 v234, v188, v189, v234\n"
      "v_mfma_f32_16x16x32_bf16 v[128:131], v[92:95], a[116:119], v[128:131]\n"
      "v_max3_f32 v235, v190, v191, v235\n"
      "v_max_f32 v236, v234, v235\n"
      "v_mfma_f32_16x16x32_bf16 v[128:131], v[96:99], a[120:123], v[128:131]\n"
      "s_waitcnt lgkmcnt(0)\n"
      "v_mov_b32 v237, v236\n"
      "v_cmp_gt_i32 vcc, v9, v10\n"
      "v_mfma_f32_16x16x32_bf16 v[128:131], v[100:103], a[124:127], v[128:131]\n"
      "s_add_u32 m0, s42, 20480\n"
      "v_permlane16_swap_b32 v236, v237\n"
      "v_mfma_f32_16x16x32_bf16 v[128:131], v[104:107], a[128:131], v[128:131]\n"
      "v_max_f32 v236, v236, v237\n"
      "v_mov_b32 v237, v236\n"
      "v_mfma_f32_16x16x32_bf16 v[128:131], v[108:111], a[132:135], v[128:131]\n"
      "buffer_load_dwordx4 v1, s[8:11], s46 offen offset:0 sc1 lds\n"
      "v_mfma_f32_16x16x32_bf16 v[128:131], v[112:115], a[136:139], v[128:131]\n"
      ".Lq_p1:\n"
      "s_cbranch_execz .Lq_pr\n"
      "v_permlane32_swap_b32 v236, v237\n"
      "v_mfma_f32_16x16x32_bf16 v[128:131], v[116:119], a[140:143], v[128:131]\n"
      "v_max_f32 v236, v236, v237\n"
      "v_mul_f32_e64 v236, v236, v232\n"
      "v_mfma_f32_16x16x32_bf16 v[132:135], a[144:147], v[48:51], 0\n"
      "s_cbranch_vccnz .Lsp_qy0_9\n"
      ".Lbk_qy0_9:\n"
      "v_fma_f32 v234, v234, v232, -v236\n"
      "v_mfma_f32_16x16x32_bf16 v[132:135], a[148:151], v[52:55], v[132:135]\n"
      "ds_read_b128 v[52:55], v0 offset:1024\n"
      "ds_read_b128 v[48:51], v0 offset:0\n"
      "v_mfma_f32_16x16x32_bf16 v[132:135], a[152:155], v[56:59], v[132:135]\n"
      "v_fma_f32 v131, -v226, v144, v131\n"
      "v_fma_f32 v235, v235, v232, -v236\n"
      "v_mfma_f32_16x16x32_bf16 v[132:135], a[156:159], v[60:63], v[132:135]\n"
      "ds_read_b128 v[56:59], v0 offset:2048\n"
      "v_fma_f32 v128, -v220, v145, v128\n"
      "v_mfma_f32_16x16x32_bf16 v[132:135], a[160:163], v[64:67], v[132:135]\n"
      "v_exp_f32 v234, v234\n"
      "v_mfma_f32_16x16x32_bf16 v[132:135], a[164:167], v[68:71], v[132:135]\n"
      "v_exp_f32 v235, v235\n"
      "v_mfma_f32_16x16x32_bf16 v[132:135], a[168:171], v[72:75], v[132:135]\n"
      "v_cvt_pk_bf16_f32 v234, v234, v234\n"
      "v_fma_f32 v129, -v222, v146, v129\n"
      "v_mfma_f32_16x16x32_bf16 v[132:135], a[172:175], v[76:79], v[132:135]\n"
      "v_mul_f32 v129, v223, v129\n"
      "v_fma_f32 v130, -v224, v147, v130\n"
      "v_mfma_f32_16x16x32_bf16 v[132:135], a[176:179], v[80:83], v[132:135]\n"
      "v_cvt_pk_bf16_f32 v235, v235, v235\n"
      "v_and_b32 v200, v160, v234\n"
      "v_mfma_f32_16x16x32_bf16 v[132:135], a[180:183], v[84:87], v[132:135]\n"
      "v_cvt_pk_bf16_f32 v184, v196, v197\n"
      "v_and_b32 v201, v161, v235\n"
      "v_mfma_f32_16x16x32_bf16 v[132:135], a[184:187], v[88:91], v[132:135]\n"
      "ds_read_b128 v[68:71], v0 offset:5120\n"
      "v_mul_f32 v130, v225, v130\n"
      "v_mfma_f32_16x16x32_bf16 v[132:135], a[188:191], v[92:95], v[132:135]\n"
      "v_and_b32 v203, v163, v235\n"
      "v_mul_f32_e64 v131, v227, v131\n"
      "v_mfma_f32_16x16x32_bf16 v[132:135], a[192:195], v[96:99], v[132:135]\n"
      "ds_read_b128 v[64:67], v0 offset:4096\n"
      "v_mul_f32_e64 v128, v221, v128\n"
      "v_mfma_f32_16x16x32_bf16 v[132:135], a[196:199], v[100:103], v[132:135]\n"
      "v_and_b32_e64 v202, v162, v234\n"
      "v_cvt_pk_bf16_f32 v181, v130, v131\n"
      "v_mfma_f32_16x16x32_bf16 v[132:135], a[200:203], v[104:107], v[132:135]\n"
      "s_add_u32 s40, s40, 24576\n"
      "ds_read_b128 v[60:63], v0 offset:3072\n"
      "v_cvt_pk_bf16_f32 v180, v128, v129\n"
      "v_mfma_f32_16x16x32_bf16 v[208:211], v[184:187], v[200:203], 0\n"
      "s_waitcnt vmcnt(6) lgkmcnt(3)\n"
      "buffer_store_dword v13, v3, s[8:11], s52 offen sc1\n"
      "ds_read2st64_b64 v[212:215], v4 offset0:0 offset1:1\n"
      "v_mfma_f32_16x16x32_bf16 v[204:207], v[180:183], v[200:203], 0\n"
      "ds_read_b128 v[72:75], v0 offset:6144\n"
      "v_mfma_f32_16x16x32_bf16 v[132:135], a[204:207], v[108:111], v[132:135]\n"
      "ds_read_b128 v[76:79], v0 offset:7168\n"
      "v_mov_b32_e64 v178, v175\n"
      "v_mfma_f32_16x16x32_bf16 v[132:135], a[208:211], v[112:115], v[132:135]\n"
      "ds_read_b128 v[88:91], v0 offset:10240\n"
      "ds_read_b128 v[108:111], v0 offset:15360\n"
      "v_mfma_f32_16x16x32_bf16 v[132:135], a[212:215], v[116:119], v[132:135]\n"
      "s_sub_u32 s20, s20, 1\n"
      "s_cbranch_scc0 .Lq_loop\n"
      ".Lq_tail:\n"
      "v_mul_f32 v232, 0x3eae1a93, v219\n"
      "s_nop 7\n"
      "v_fma_f32 v135, -v218, v151, v135\n"
      "v_fma_f32 v133, -v218, v149, v133\n"
      "v_fma_f32 v134, -v218, v150, v134\n"
      "v_fma_f32 v132, -v218, v148, v132\n"
      "v_cvt_pk_bf16_f32 v176, v132, v133\n"
      "v_cvt_pk_bf16_f32 v177, v134, v135\n"
      "v_rcp_f32 v210, v210\n"
      "s_nop 0\n"
      "v_mul_f32 v204, v204, v210\n"
      "v_mfma_f32_16x16x32_bf16 v[188:191], v[172:175], v[176:179], v[152:155]\n"
      "v_mul_f32 v208, v208, v210\n"
      "v_mul_f32 v207, v207, v210\n"
      "v_mul_f32 v209, v209, v210\n"
      "v_cvt_pk_bf16_f32 v230, v208, v209\n"
      "buffer_store_dwordx2 v[230:231], v8, s[8:11], s40 offen sc1\n"
      "v_mul_f32 v206, v206, v210\n"
      "v_cvt_pk_bf16_f32 v229, v206, v207\n"
      "v_mul_f32 v205, v205, v210\n"
      "v_cvt_pk_bf16_f32 v228, v204, v205\n"
      "buffer_store_dwordx2 v[228:229], v7, s[8:11], s40 offen sc1\n"
      "v_mov_b32 v187, v185\n"
      "v_mov_b32 v186, v184\n"
      "v_mov_b32 v182, v180\n"
      "s_add_u32 s52, s52, 24576\n"
      "v_mov_b32 v183, v181\n"
      "s_add_u32 s40, s40, 24576\n"
      "s_waitcnt vmcnt(0)\n"
      "buffer_store_dword v13, v3, s[8:11], s52 offen sc1\n"
      "s_add_u32 s52, s52, 24576\n"
      "s_cmp_lg_u32 s44, 0\n"
      "s_cbranch_scc1 .Lq_done\n"
      "v_lshlrev_b32 v48, 3, v0\n"
      "s_mov_b32 s22, s23\n"
      ".Lq_up:\n"
      "s_mov_b32 s30, 128\n"
      "buffer_load_dword v49, v48, s[8:11], s30 offen sc1\n"
      "s_mov_b32 s30, 8320\n"
      "buffer_load_dword v50, v48, s[8:11], s30 offen sc1\n"
      "s_mov_b32 s30, 16512\n"
      "buffer_load_dword v51, v48, s[8:11], s30 offen sc1\n"
      "s_mov_b32 s30, 24704\n"
      "buffer_load_dword v52, v48, s[8:11], s30 offen sc1\n"
      "s_waitcnt vmcnt(0)\n"
      "v_xor_b32 v49, s21, v49\n"
      "v_xor_b32 v50, s21, v50\n"
      "v_xor_b32 v51, s21, v51\n"
      "v_xor_b32 v52, s21, v52\n"
      "v_or3_b32 v49, v49, v50, v51\n"
      "v_or_b32 v49, v49, v52\n"
      "v_cmp_ne_u32 vcc, 0, v49\n"
      "s_cbranch_vccz .Lq_ok\n"
      "s_sleep 4\n"
      "s_sub_u32 s22, s22, 1\n"
      "s_cbranch_scc0 .Lq_up\n"
      ".Lq_ok:\n"
      "v_lshrrev_b32 v48, 2, v0\n"
      "v_and_b32 v48, 0x7c, v48\n"
      "v_mov_b32 v49, s21\n"
      "buffer_store_dword v49, v48, s[8:11], 0 offen sc1\n"
      ".Lq_done:\n"
      "s_endpgm\n"
      ".Lsp_q0_1:\n"
      "s_mov_b32 s22, s23\n"
      ".Lsl_q0_1:\n"
      "buffer_load_dword v15, v2, s[8:11], s46 offen sc1\n"
      "s_waitcnt vmcnt(0)\n"
      "v_cmp_ne_u32 vcc, s21, v15\n"
      "s_cbranch_vccz .Lbk_q0_1\n"
      "s_sleep 4\n"
      "s_sub_u32 s22, s22, 1\n"
      "s_cbranch_scc0 .Lsl_q0_1\n"
      "s_mov_b32 s23, 0\n"
      "s_branch .Lbk_q0_1\n"
      ".Lsp_q1_2:\n"
      "s_mov_b32 s22, s23\n"
      ".Lsl_q1_2:\n"
      "buffer_load_dword v15, v2, s[8:11], s46 offen sc1\n"
      "s_waitcnt vmcnt(0)\n"
      "v_cmp_ne_u32 vcc, s21, v15\n"
      "s_cbranch_vccz .Lbk_q1_2\n"
      "s_sleep 4\n"
      "s_sub_u32 s22, s22, 1\n"
      "s_cbranch_scc0 .Lsl_q1_2\n"
      "s_mov_b32 s23, 0\n"
      "s_branch .Lbk_q1_2\n"
      ".Lsp_qy0_3:\n"
      "s_lshl_b32 s22, s23, 4\n"
      ".Lsl_qy0_3:\n"
      "ds_read_b32 v10, v12\n"
      "s_waitcnt lgkmcnt(0)\n"
      "v_cmp_gt_i32 vcc, v9, v10\n"
      "s_cbranch_vccz .Lbk_qy0_3\n"
      "s_sub_u32 s22, s22, 1\n"
      "s_cbranch_scc0 .Lsl_qy0_3\n"
      "s_mov_b32 s23, 0\n"
      "s_branch .Lbk_qy0_3\n"
      ".Lsp_q2_4:\n"
      "s_mov_b32 s22, s23\n"
      "buffer_load_dword v15, v2, s[8:11], s46 offen sc1\n"
      "s_waitcnt vmcnt(0)\n"
      "v_cmp_ne_u32 vcc, s21, v15\n"
      "s_add_u32 s30, s52, 24576\n"
      "buffer_store_dword v13, v3, s[8:11], s30 offen sc1\n"
      "s_cbranch_vccz .Lbk_q2_4\n"
      ".Lsl_q2_4:\n"
      "buffer_load_dword v15, v2, s[8:11], s46 offen sc1\n"
      "s_waitcnt vmcnt(0)\n"
      "v_cmp_ne_u32 vcc, s21, v15\n"
      "s_cbranch_vccz .Lbk_q2_4\n"
      "s_sleep 4\n"
      "s_sub_u32 s22, s22, 1\n"
      "s_cbranch_scc0 .Lsl_q2_4\n"
      "s_mov_b32 s23, 0\n"
      "s_branch .Lbk_q2_4\n"
      ".Lsp_qy1_5:\n"
      "s_lshl_b32 s22, s23, 4\n"
      ".Lsl_qy1_5:\n"
      "ds_read_b32 v10, v12\n"
      "s_waitcnt lgkmcnt(0)\n"
      "v_cmp_gt_i32 vcc, v9, v10\n"
      "s_cbranch_vccz .Lbk_qy1_5\n"
      "s_sub_u32 s22, s22, 1\n"
      "s_cbranch_scc0 .Lsl_qy1_5\n"
      "s_mov_b32 s23, 0\n"
      "s_branch .Lbk_qy1_5\n"
      ".Lsp_q0_6:\n"
      "s_mov_b32 s22, s23\n"
      "buffer_load_dword v15, v2, s[8:11], s46 offen sc1\n"
      "s_waitcnt vmcnt(0)\n"
      "v_cmp_ne_u32 vcc, s21, v15\n"
      "s_add_u32 s30, s52, 24576\n"
      "buffer_store_dword v13, v3, s[8:11], s30 offen sc1\n"
      "s_cbranch_vccz .Lbk_q0_6\n"
      ".Lsl_q0_6:\n"
      "buffer_load_dword v15, v2, s[8:11], s46 offen sc1\n"
      "s_waitcnt vmcnt(0)\n"
      "v_cmp_ne_u32 vcc, s21, v15\n"
      "s_cbranch_vccz .Lbk_q0_6\n"
      "s_sleep 4\n"
      "s_sub_u32 s22, s22, 1\n"
      "s_cbranch_scc0 .Lsl_q0_6\n"
      "s_mov_b32 s23, 0\n"
      "s_branch .Lbk_q0_6\n"
      ".Lsp_qy2_7:\n"
      "s_lshl_b32 s22, s23, 4\n"
      ".Lsl_qy2_7:\n"
      "ds_read_b32 v10, v12\n"
      "s_waitcnt lgkmcnt(0)\n"
      "v_cmp_gt_i32 vcc, v9, v10\n"
      "s_cbranch_vccz .Lbk_qy2_7\n"
      "s_sub_u32 s22, s22, 1\n"
      "s_cbranch_scc0 .Lsl_qy2_7\n"
      "s_mov_b32 s23, 0\n"
      "s_branch .Lbk_qy2_7\n"
      ".Lsp_q1_8:\n"
      "s_mov_b32 s22, s23\n"
      "buffer_load_dword v15, v2, s[8:11], s46 offen sc1\n"
      "s_waitcnt vmcnt(0)\n"
      "v_cmp_ne_u32 vcc, s21, v15\n"
      "s_add_u32 s30, s52, 0\n"
      "buffer_store_dword v13, v3, s[8:11], s30 offen sc1\n"
      "s_cbranch_vccz .Lbk_q1_8\n"
      ".Lsl_q1_8:\n"
      "buffer_load_dword v15, v2, s[8:11], s46 offen sc1\n"
      "s_waitcnt vmcnt(0)\n"
      "v_cmp_ne_u32 vcc, s21, v15\n"
      "s_cbranch_vccz .Lbk_q1_8\n"
      "s_sleep 4\n"
      "s_sub_u32 s22, s22, 1\n"
      "s_cbranch_scc0 .Lsl_q1_8\n"
      "s_mov_b32 s23, 0\n"
      "s_branch .Lbk_q1_8\n"
      ".Lsp_qy0_9:\n"
      "s_lshl_b32 s22, s23, 4\n"
      ".Lsl_qy0_9:\n"
      "ds_read_b32 v10, v12\n"
      "s_waitcnt lgkmcnt(0)\n"
      "v_cmp_gt_i32 vcc, v9, v10\n"
      "s_cbranch_vccz .Lbk_qy0_9\n"
      "s_sub_u32 s22, s22, 1\n"
      "s_cbranch_scc0 .Lsl_qy0_9\n"
      "s_mov_b32 s23, 0\n"
      "s_branch .Lbk_qy0_9\n"
      ".Lq_last:\n"
      :
      : "{s[8:11]}"(rsWS), "{s[12:15]}"(rsA), "{v0}"(v_l16), "{v1}"(v_l5), "{v2}"(v_fa), "{v3}"(v_ra), "{v4}"(v_st), "{v5}"(v_tw), "{v6}"(v_tr), "{v7}"(v_om), "{v8}"(v_oe), "{v10}"(v_al), "{v11}"(v_yw), "{v12}"(v_yr), "{v14}"(v_hi), "{v152}"(v_bc[0]), "{v153}"(v_bc[1]), "{v154}"(v_bc[2]), "{v155}"(v_bc[3]), "{v156}"(v_bp[0]), "{v157}"(v_bp[1]), "{v158}"(v_bp[2]), "{v159}"(v_bp[3]), "{v160}"(v_mk[0]), "{v161}"(v_mk[1]), "{v162}"(v_mk[2]), "{v163}"(v_mk[3]), "{v164}"(v_sel[0]), "{v165}"(v_sel[1]), "{v166}"(v_sel[2]), "{v167}"(v_sel[3]), "{v168}"(v_ci[0]), "{v169}"(v_ci[1]), "{v170}"(v_ci[2]), "{v171}"(v_ci[3]), "{v48}"(v_vo[0]), "{v49}"(v_vo[1]), "{v50}"(v_vo[2]), "{v51}"(v_vo[3]), "{v52}"(v_at), "{v53}"(v_ax), "{v54}"(v_pm), "{v55}"(v_ms), "{v76}"(v_pw), "{s36}"(s_sx), "{s37}"(s_ev), "{s38}"(s_del), "{s39}"(s_kil), "{s40}"(s_so), "{s41}"(s_m4), "{s42}"(s_m5), "{s43}"(s_ms), "{s44}"(s_up)
      : "v255", "a255", "s99", "vcc", "scc", "memory");

  } else if (fast && slot < 10) {
    const unsigned gr = e, part = slot - 8;
    COMMON(32, 81920);
    const unsigned c = part, o = w & 1;
    const u32x4 rsA = srd(Wo, 576 * 576 * 2);
    const unsigned v_l5 = v_l16 + 16384 - w * 3072, e0 = 16384 + o * 2048 + g * 512 + i16 * 8, e1 = e0 + 2048 - o * 4096;
    const unsigned v_ex[8] = {e0, e1, e0 + 20480, e1 + 20480, e0 + 40960, e1 + 40960, e0 + 61440, e1 + 61440}, v_l2 = v_l16 + 40960;
    const unsigned v_fa = 20480 - w * 4096 + (8 * w + (lane >> 3)) * 128 + (lane & 7) * 4, v_ra = up * 8 * REC + l5 + 19456 + (4 * c + w) * 128;
    const unsigned v_xp = (9 * c + 2 * w) * 1024 + v_l16, v_xs = (9 * c + 8) * 1024 + (w >> 1) * 512 + half;
    const unsigned v_rp = v_xp + WS_XIN, v_rs = v_xs + WS_XIN;
    const unsigned v_ew = 82944 + (w >> 1) * 1024 + lane * 4;
    const unsigned v_sw = 86016 + i16 * 144 + (4 * w + g) * 8, v_sr = 86016 + (8 * (w >> 1) + l8) * 144 + l7;
    const unsigned v_ss = 18432 + c * 512 + (w >> 1) * 256 + l8 * 32 + p * 8;
    const unsigned v_pw = 19456 + (4 * c + w) * 128 + l8 * REC + l7;
    unsigned v_vo[9], v_vp[9];
#pragma unroll
    for (unsigned j = 0; j < 9; ++j) {
      const unsigned ch = 64 * j + lane, r = ch / 36, x = 576 * c + ch % 36 * 16 - 1024 * (j & 3);
      v_vo[j] = (20 * o + r) * 1152 + x;
      v_vp[j] = (36 * (4 * o + (r >> 2)) + 16 + (r & 3)) * 1152 + x;
    }
    const unsigned ro = (8 * g + q) * 576;
    const unsigned v_t0 = ro + o * 18432 + 128 * w + 16 * p, v_t1 = v_t0 + 18432 - 2 * o * 18432, v_ts = ro + o * 18432 + 512 + 32 * (w >> 1) + 8 * p;
    const unsigned s_sx = WS_OB + (gr * NREC + KSK) * REC + w * 4096, s_ev = s_sx + (31 - KSK) * REC, s_sy = WS_X1 + (gr * NREC + KSK) * REC;
    const unsigned s_m4 = w * 4096, s_m5 = 16384 + w * 1024, s_m9 = w * 9216, s_so = (w >> 1) * 8 * 41472;
    const unsigned s_s8 = s_so, s_wg = o, s_rb = WS_OB + w * 4096;
    asm volatile(
      "s_buffer_load_dword s21, s[8:11], 0\n"
      "s_mov_b32 s23, 65536\n"
      "s_mov_b32 s31, 0x40000000\n"
      "s_add_u32 m0, s41, 0\n"
      "s_nop 0\n"
      "buffer_load_dwordx4 v100, s[12:15], s42 offen offset:0 lds\n"
      "buffer_load_dwordx4 v101, s[12:15], s42 offen offset:1024 lds\n"
      "buffer_load_dwordx4 v102, s[12:15], s42 offen offset:2048 lds\n"
      "buffer_load_dwordx4 v103, s[12:15], s42 offen offset:3072 lds\n"
      "s_add_u32 m0, s41, 4096\n"
      "s_nop 0\n"
      "buffer_load_dwordx4 v104, s[12:15], s42 offen offset:0 lds\n"
      "buffer_load_dwordx4 v105, s[12:15], s42 offen offset:1024 lds\n"
      "buffer_load_dwordx4 v106, s[12:15], s42 offen offset:2048 lds\n"
      "buffer_load_dwordx4 v107, s[12:15], s42 offen offset:3072 lds\n"
      "s_add_u32 m0, s41, 8192\n"
      "s_nop 0\n"
      "buffer_load_dwordx4 v108, s[12:15], s42 offen offset:0 lds\n"
      "s_add_u32 s42, s42, 41472\n"
      "s_add_u32 m0, s41, 36864\n"
      "s_nop 0\n"
      "buffer_load_dwordx4 v100, s[12:15], s42 offen offset:0 lds\n"
      "buffer_load_dwordx4 v101, s[12:15], s42 offen offset:1024 lds\n"
      "buffer_load_dwordx4 v102, s[12:15], s42 offen offset:2048 lds\n"
      "buffer_load_dwordx4 v103, s[12:15], s42 offen offset:3072 lds\n"
      "s_add_u32 m0, s41, 40960\n"
      "s_nop 0\n"
      "buffer_load_dwordx4 v104, s[12:15], s42 offen offset:0 lds\n"
      "buffer_load_dwordx4 v105, s[12:15], s42 offen offset:1024 lds\n"
      "buffer_load_dwordx4 v106, s[12:15], s42 offen offset:2048 lds\n"
      "buffer_load_dwordx4 v107, s[12:15], s42 offen offset:3072 lds\n"
      "s_add_u32 m0, s41, 45056\n"
      "s_nop 0\n"
      "buffer_load_dwordx4 v108, s[12:15], s42 offen offset:0 lds\n"
      "s_add_u32 s42, s42, 41472\n"
      "s_add_u32 m0, s41, 73728\n"
      "s_nop 0\n"
      "buffer_load_dwordx4 v100, s[12:15], s42 offen offset:0 lds\n"
      "buffer_load_dwordx4 v101, s[12:15], s42 offen offset:1024 lds\n"
      "buffer_load_dwordx4 v102, s[12:15], s42 offen offset:2048 lds\n"
      "buffer_load_dwordx4 v103, s[12:15], s42 offen offset:3072 lds\n"
      "s_add_u32 m0, s41, 77824\n"
      "s_nop 0\n"
      "buffer_load_dwordx4 v104, s[12:15], s42 offen offset:0 lds\n"
      "buffer_load_dwordx4 v105, s[12:15], s42 offen offset:1024 lds\n"
      "buffer_load_dwordx4 v106, s[12:15], s42 offen offset:2048 lds\n"
      "buffer_load_dwordx4 v107, s[12:15], s42 offen offset:3072 lds\n"
      "s_add_u32 m0, s41, 81920\n"
      "s_nop 0\n"
      "buffer_load_dwordx4 v108, s[12:15], s42 offen offset:0 lds\n"
      "s_add_u32 s42, s42, 41472\n"
      "s_waitcnt vmcnt(18)\n"
      "s_barrier\n"
      "ds_read_b64_tr_b16 v[64:65], v120 offset:0\n"
      "ds_read_b64_tr_b16 v[66:67], v120 offset:2304\n"
      "ds_read_b64_tr_b16 a[0:1], v118 offset:0\n"
      "ds_read_b64_tr_b16 a[2:3], v118 offset:2304\n"
      "ds_read_b64_tr_b16 a[72:73], v118 offset:8\n"
      "ds_read_b64_tr_b16 a[74:75], v118 offset:2312\n"
      "ds_read_b64_tr_b16 a[144:145], v118 offset:64\n"
      "ds_read_b64_tr_b16 a[146:147], v118 offset:2368\n"
      "ds_read_b64_tr_b16 a[216:217], v118 offset:72\n"
      "ds_read_b64_tr_b16 a[218:219], v118 offset:2376\n"
      "ds_read_b64_tr_b16 a[36:37], v119 offset:0\n"
      "ds_read_b64_tr_b16 a[38:39], v119 offset:2304\n"
      "ds_read_b64_tr_b16 a[108:109], v119 offset:8\n"
      "ds_read_b64_tr_b16 a[110:111], v119 offset:2312\n"
      "ds_read_b64_tr_b16 a[180:181], v119 offset:64\n"
      "ds_read_b64_tr_b16 a[182:183], v119 offset:2368\n"
      "ds_read_b64_tr_b16 a[252:253], v119 offset:72\n"
      "ds_read_b64_tr_b16 a[254:255], v119 offset:2376\n"
      "v_add_u32 v118, 36864, v118\n"
      "v_add_u32 v119, 36864, v119\n"
      "v_add_u32 v120, 36864, v120\n"
      "s_add_u32 m0, s41, 110592\n"
      "s_nop 0\n"
      "buffer_load_dwordx4 v100, s[12:15], s42 offen offset:0 lds\n"
      "buffer_load_dwordx4 v101, s[12:15], s42 offen offset:1024 lds\n"
      "buffer_load_dwordx4 v102, s[12:15], s42 offen offset:2048 lds\n"
      "buffer_load_dwordx4 v103, s[12:15], s42 offen offset:3072 lds\n"
      "s_add_u32 m0, s41, 114688\n"
      "s_nop 0\n"
      "buffer_load_dwordx4 v104, s[12:15], s42 offen offset:0 lds\n"
      "buffer_load_dwordx4 v105, s[12:15], s42 offen offset:1024 lds\n"
      "buffer_load_dwordx4 v106, s[12:15], s42 offen offset:2048 lds\n"
      "buffer_load_dwordx4 v107, s[12:15], s42 offen offset:3072 lds\n"
      "s_add_u32 m0, s41, 118784\n"
      "s_nop 0\n"
      "buffer_load_dwordx4 v108, s[12:15], s42 offen offset:0 lds\n"
      "s_add_u32 s42, s42, 41472\n"
      "s_waitcnt vmcnt(18) lgkmcnt(0)\n"
      "s_barrier\n"
      "ds_read_b64_tr_b16 v[68:69], v120 offset:0\n"
      "ds_read_b64_tr_b16 v[70:71], v120 offset:2304\n"
      "ds_read_b64_tr_b16 a[4:5], v118 offset:0\n"
      "ds_read_b64_tr_b16 a[6:7], v118 offset:2304\n"
      "ds_read_b64_tr_b16 a[76:77], v118 offset:8\n"
      "ds_read_b64_tr_b16 a[78:79], v118 offset:2312\n"
      "ds_read_b64_tr_b16 a[148:149], v118 offset:64\n"
      "ds_read_b64_tr_b16 a[150:151], v118 offset:2368\n"
      "ds_read_b64_tr_b16 a[220:221], v118 offset:72\n"
      "ds_read_b64_tr_b16 a[222:223], v118 offset:2376\n"
      "ds_read_b64_tr_b16 a[40:41], v119 offset:0\n"
      "ds_read_b64_tr_b16 a[42:43], v119 offset:2304\n"
      "ds_read_b64_tr_b16 a[112:113], v119 offset:8\n"
      "ds_read_b64_tr_b16 a[114:115], v119 offset:2312\n"
      "ds_read_b64_tr_b16 a[184:185], v119 offset:64\n"
      "ds_read_b64_tr_b16 a[186:187], v119 offset:2368\n"
      "ds_read_b64_tr_b16 v[32:33], v119 offset:72\n"
      "ds_read_b64_tr_b16 v[34:35], v119 offset:2376\n"
      "v_add_u32 v118, 36864, v118\n"
      "v_add_u32 v119, 36864, v119\n"
      "v_add_u32 v120, 36864, v120\n"
      "s_add_u32 m0, s41, 0\n"
      "s_nop 0\n"
      "buffer_load_dwordx4 v100, s[12:15], s42 offen offset:0 lds\n"
      "buffer_load_dwordx4 v101, s[12:15], s42 offen offset:1024 lds\n"
      "buffer_load_dwordx4 v102, s[12:15], s42 offen offset:2048 lds\n"
      "buffer_load_dwordx4 v103, s[12:15], s42 offen offset:3072 lds\n"
      "s_add_u32 m0, s41, 4096\n"
      "s_nop 0\n"
      "buffer_load_dwordx4 v104, s[12:15], s42 offen offset:0 lds\n"
      "buffer_load_dwordx4 v105, s[12:15], s42 offen offset:1024 lds\n"
      "buffer_load_dwordx4 v106, s[12:15], s42 offen offset:2048 lds\n"
      "buffer_load_dwordx4 v107, s[12:15], s42 offen offset:3072 lds\n"
      "s_add_u32 m0, s41, 8192\n"
      "s_nop 0\n"
      "buffer_load_dwordx4 v108, s[12:15], s42 offen offset:0 lds\n"
      "s_add_u32 s42, s42, 41472\n"
      "s_waitcnt vmcnt(18) lgkmcnt(0)\n"
      "s_barrier\n"
      "ds_read_b64_tr_b16 v[72:73], v120 offset:0\n"
      "ds_read_b64_tr_b16 v[74:75], v120 offset:2304\n"
      "ds_read_b64_tr_b16 a[8:9], v118 offset:0\n"
      "ds_read_b64_tr_b16 a[10:11], v118 offset:2304\n"
      "ds_read_b64_tr_b16 a[80:81], v118 offset:8\n"
      "ds_read_b64_tr_b16 a[82:83], v118 offset:2312\n"
      "ds_read_b64_tr_b16 a[152:153], v118 offset:64\n"
      "ds_read_b64_tr_b16 a[154:155], v118 offset:2368\n"
      "ds_read_b64_tr_b16 a[224:225], v118 offset:72\n"
      "ds_read_b64_tr_b16 a[226:227], v118 offset:2376\n"
      "ds_read_b64_tr_b16 a[44:45], v119 offset:0\n"
      "ds_read_b64_tr_b16 a[46:47], v119 offset:2304\n"
      "ds_read_b64_tr_b16 a[116:117], v119 offset:8\n"
      "ds_read_b64_tr_b16 a[118:119], v119 offset:2312\n"
      "ds_read_b64_tr_b16 a[188:189], v119 offset:64\n"
      "ds_read_b64_tr_b16 a[190:191], v119 offset:2368\n"
      "ds_read_b64_tr_b16 v[36:37], v119 offset:72\n"
      "ds_read_b64_tr_b16 v[38:39], v119 offset:2376\n"
      "v_add_u32 v118, 36864, v118\n"
      "v_add_u32 v119, 36864, v119\n"
      "v_add_u32 v120, 36864, v120\n"
      "s_add_u32 m0, s41, 36864\n"
      "s_nop 0\n"
      "buffer_load_dwordx4 v100, s[12:15], s42 offen offset:0 lds\n"
      "buffer_load_dwordx4 v101, s[12:15], s42 offen offset:1024 lds\n"
      "buffer_load_dwordx4 v102, s[12:15], s42 offen offset:2048 lds\n"
      "buffer_load_dwordx4 v103, s[12:15], s42 offen offset:3072 lds\n"
      "s_add_u32 m0, s41, 40960\n"
      "s_nop 0\n"
      "buffer_load_dwordx4 v104, s[12:15], s42 offen offset:0 lds\n"
      "buffer_load_dwordx4 v105, s[12:15], s42 offen offset:1024 lds\n"
      "buffer_load_dwordx4 v106, s[12:15], s42 offen offset:2048 lds\n"
      "buffer_load_dwordx4 v107, s[12:15], s42 offen offset:3072 lds\n"
      "s_add_u32 m0, s41, 45056\n"
      "s_nop 0\n"
      "buffer_load_dwordx4 v108, s[12:15], s42 offen offset:0 lds\n"
      "s_add_u32 s42, s42, 41472\n"
      "s_waitcnt vmcnt(18) lgkmcnt(0)\n"
      "s_barrier\n"
      "ds_read_b64_tr_b16 v[76:77], v120 offset:0\n"
      "ds_read_b64_tr_b16 v[78:79], v120 offset:2304\n"
      "ds_read_b64_tr_b16 a[12:13], v118 offset:0\n"
      "ds_read_b64_tr_b16 a[14:15], v118 offset:2304\n"
      "ds_read_b64_tr_b16 a[84:85], v118 offset:8\n"
      "ds_read_b64_tr_b16 a[86:87], v118 offset:2312\n"
      "ds_read_b64_tr_b16 a[156:157], v118 offset:64\n"
      "ds_read_b64_tr_b16 a[158:159], v118 offset:2368\n"
      "ds_read_b64_tr_b16 a[228:229], v118 offset:72\n"
      "ds_read_b64_tr_b16 a[230:231], v118 offset:2376\n"
      "ds_read_b64_tr_b16 a[48:49], v119 offset:0\n"
      "ds_read_b64_tr_b16 a[50:51], v119 offset:2304\n"
      "ds_read_b64_tr_b16 a[120:121], v119 offset:8\n"
      "ds_read_b64_tr_b16 a[122:123], v119 offset:2312\n"
      "ds_read_b64_tr_b16 a[192:193], v119 offset:64\n"
      "ds_read_b64_tr_b16 a[194:195], v119 offset:2368\n"
      "ds_read_b64_tr_b16 v[40:41], v119 offset:72\n"
      "ds_read_b64_tr_b16 v[42:43], v119 offset:2376\n"
      "v_add_u32 v118, 4294856704, v118\n"
      "v_add_u32 v119, 4294856704, v119\n"
      "v_add_u32 v120, 4294856704, v120\n"
      "s_add_u32 m0, s41, 73728\n"
      "s_nop 0\n"
      "buffer_load_dwordx4 v100, s[12:15], s42 offen offset:0 lds\n"
      "buffer_load_dwordx4 v101, s[12:15], s42 offen offset:1024 lds\n"
      "buffer_load_dwordx4 v102, s[12:15], s42 offen offset:2048 lds\n"
      "buffer_load_dwordx4 v103, s[12:15], s42 offen offset:3072 lds\n"
      "s_add_u32 m0, s41, 77824\n"
      "s_nop 0\n"
      "buffer_load_dwordx4 v104, s[12:15], s42 offen offset:0 lds\n"
      "buffer_load_dwordx4 v105, s[12:15], s42 offen offset:1024 lds\n"
      "buffer_load_dwordx4 v106, s[12:15], s42 offen offset:2048 lds\n"
      "buffer_load_dwordx4 v107, s[12:15], s42 offen offset:3072 lds\n"
      "s_add_u32 m0, s41, 81920\n"
      "s_nop 0\n"
      "buffer_load_dwordx4 v108, s[12:15], s42 offen offset:0 lds\n"
      "s_add_u32 s42, s42, 41472\n"
      "s_waitcnt vmcnt(18) lgkmcnt(0)\n"
      "s_barrier\n"
      "ds_read_b64_tr_b16 v[80:81], v120 offset:0\n"
      "ds_read_b64_tr_b16 v[82:83], v120 offset:2304\n"
      "ds_read_b64_tr_b16 a[16:17], v118 offset:0\n"
      "ds_read_b64_tr_b16 a[18:19], v118 offset:2304\n"
      "ds_read_b64_tr_b16 a[88:89], v118 offset:8\n"
      "ds_read_b64_tr_b16 a[90:91], v118 offset:2312\n"
      "ds_read_b64_tr_b16 a[160:161], v118 offset:64\n"
      "ds_read_b64_tr_b16 a[162:163], v118 offset:2368\n"
      "ds_read_b64_tr_b16 a[232:233], v118 offset:72\n"
      "ds_read_b64_tr_b16 a[234:235], v118 offset:2376\n"
      "ds_read_b64_tr_b16 a[52:53], v119 offset:0\n"
      "ds_read_b64_tr_b16 a[54:55], v119 offset:2304\n"
      "ds_read_b64_tr_b16 a[124:125], v119 offset:8\n"
      "ds_read_b64_tr_b16 a[126:127], v119 offset:2312\n"
      "ds_read_b64_tr_b16 a[196:197], v119 offset:64\n"
      "ds_read_b64_tr_b16 a[198:199], v119 offset:2368\n"
      "ds_read_b64_tr_b16 v[44:45], v119 offset:72\n"
      "ds_read_b64_tr_b16 v[46:47], v119 offset:2376\n"
      "v_add_u32 v118, 36864, v118\n"
      "v_add_u32 v119, 36864, v119\n"
      "v_add_u32 v120, 36864, v120\n"
      "s_add_u32 m0, s41, 110592\n"
      "s_nop 0\n"
      "buffer_load_dwordx4 v100, s[12:15], s42 offen offset:0 lds\n"
      "buffer_load_dwordx4 v101, s[12:15], s42 offen offset:1024 lds\n"
      "buffer_load_dwordx4 v102, s[12:15], s42 offen offset:2048 lds\n"
      "buffer_load_dwordx4 v103, s[12:15], s42 offen offset:3072 lds\n"
      "s_add_u32 m0, s41, 114688\n"
      "s_nop 0\n"
      "buffer_load_dwordx4 v104, s[12:15], s42 offen offset:0 lds\n"
      "buffer_load_dwordx4 v105, s[12:15], s42 offen offset:1024 lds\n"
      "buffer_load_dwordx4 v106, s[12:15], s42 offen offset:2048 lds\n"
      "buffer_load_dwordx4 v107, s[12:15], s42 offen offset:3072 lds\n"
      "s_add_u32 m0, s41, 118784\n"
      "s_nop 0\n"
      "buffer_load_dwordx4 v108, s[12:15], s42 offen offset:0 lds\n"
      "s_add_u32 s42, s42, 41472\n"
      "s_waitcnt vmcnt(18) lgkmcnt(0)\n"
      "s_barrier\n"
      "ds_read_b64_tr_b16 v[84:85], v120 offset:0\n"
      "ds_read_b64_tr_b16 v[86:87], v120 offset:2304\n"
      "ds_read_b64_tr_b16 a[20:21], v118 offset:0\n"
      "ds_read_b64_tr_b16 a[22:23], v118 offset:2304\n"
      "ds_read_b64_tr_b16 a[92:93], v118 offset:8\n"
      "ds_read_b64_tr_b16 a[94:95], v118 offset:2312\n"
      "ds_read_b64_tr_b16 a[164:165], v118 offset:64\n"
      "ds_read_b64_tr_b16 a[166:167], v118 offset:2368\n"
      "ds_read_b64_tr_b16 a[236:237], v118 offset:72\n"
      "ds_read_b64_tr_b16 a[238:239], v118 offset:2376\n"
      "ds_read_b64_tr_b16 a[56:57], v119 offset:0\n"
      "ds_read_b64_tr_b16 a[58:59], v119 offset:2304\n"
      "ds_read_b64_tr_b16 a[128:129], v119 offset:8\n"
      "ds_read_b64_tr_b16 a[130:131], v119 offset:2312\n"
      "ds_read_b64_tr_b16 a[200:201], v119 offset:64\n"
      "ds_read_b64_tr_b16 a[202:203], v119 offset:2368\n"
      "ds_read_b64_tr_b16 v[48:49], v119 offset:72\n"
      "ds_read_b64_tr_b16 v[50:51], v119 offset:2376\n"
      "v_add_u32 v118, 36864, v118\n"
      "v_add_u32 v119, 36864, v119\n"
      "v_add_u32 v120, 36864, v120\n"
      "s_add_u32 m0, s41, 0\n"
      "s_nop 0\n"
      "buffer_load_dwordx4 v109, s[12:15], s43 offen offset:0 lds\n"
      "buffer_load_dwordx4 v110, s[12:15], s43 offen offset:1024 lds\n"
      "buffer_load_dwordx4 v111, s[12:15], s43 offen offset:2048 lds\n"
      "buffer_load_dwordx4 v112, s[12:15], s43 offen offset:3072 lds\n"
      "s_add_u32 m0, s41, 4096\n"
      "s_nop 0\n"
      "buffer_load_dwordx4 v113, s[12:15], s43 offen offset:0 lds\n"
      "buffer_load_dwordx4 v114, s[12:15], s43 offen offset:1024 lds\n"
      "buffer_load_dwordx4 v115, s[12:15], s43 offen offset:2048 lds\n"
      "buffer_load_dwordx4 v116, s[12:15], s43 offen offset:3072 lds\n"
      "s_add_u32 m0, s41, 8192\n"
      "s_nop 0\n"
      "buffer_load_dwordx4 v117, s[12:15], s43 offen offset:0 lds\n"
      "s_add_u32 s42, s42, 41472\n"
      "s_waitcnt vmcnt(18) lgkmcnt(0)\n"
      "s_barrier\n"
      "ds_read_b64_tr_b16 v[88:89], v120 offset:0\n"
      "ds_read_b64_tr_b16 v[90:91], v120 offset:2304\n"
      "ds_read_b64_tr_b16 a[24:25], v118 offset:0\n"
      "ds_read_b64_tr_b16 a[26:27], v118 offset:2304\n"
      "ds_read_b64_tr_b16 a[96:97], v118 offset:8\n"
      "ds_read_b64_tr_b16 a[98:99], v118 offset:2312\n"
      "ds_read_b64_tr_b16 a[168:169], v118 offset:64\n"
      "ds_read_b64_tr_b16 a[170:171], v118 offset:2368\n"
      "ds_read_b64_tr_b16 a[240:241], v118 offset:72\n"
      "ds_read_b64_tr_b16 a[242:243], v118 offset:2376\n"
      "ds_read_b64_tr_b16 a[60:61], v119 offset:0\n"
      "ds_read_b64_tr_b16 a[62:63], v119 offset:2304\n"
      "ds_read_b64_tr_b16 a[132:133], v119 offset:8\n"
      "ds_read_b64_tr_b16 a[134:135], v119 offset:2312\n"
      "ds_read_b64_tr_b16 a[204:205], v119 offset:64\n"
      "ds_read_b64_tr_b16 a[206:207], v119 offset:2368\n"
      "ds_read_b64_tr_b16 v[52:53], v119 offset:72\n"
      "ds_read_b64_tr_b16 v[54:55], v119 offset:2376\n"
      "v_add_u32 v118, 36864, v118\n"
      "v_add_u32 v119, 36864, v119\n"
      "v_add_u32 v120, 36864, v120\n"
      "s_waitcnt vmcnt(9) lgkmcnt(0)\n"
      "s_barrier\n"
      "ds_read_b64_tr_b16 v[92:93], v120 offset:0\n"
      "ds_read_b64_tr_b16 v[94:95], v120 offset:2304\n"
      "ds_read_b64_tr_b16 a[28:29], v118 offset:0\n"
      "ds_read_b64_tr_b16 a[30:31], v118 offset:2304\n"
      "ds_read_b64_tr_b16 a[100:101], v118 offset:8\n"
      "ds_read_b64_tr_b16 a[102:103], v118 offset:2312\n"
      "ds_read_b64_tr_b16 a[172:173], v118 offset:64\n"
      "ds_read_b64_tr_b16 a[174:175], v118 offset:2368\n"
      "ds_read_b64_tr_b16 a[244:245], v118 offset:72\n"
      "ds_read_b64_tr_b16 a[246:247], v118 offset:2376\n"
      "ds_read_b64_tr_b16 a[64:65], v119 offset:0\n"
      "ds_read_b64_tr_b16 a[66:67], v119 offset:2304\n"
      "ds_read_b64_tr_b16 a[136:137], v119 offset:8\n"
      "ds_read_b64_tr_b16 a[138:139], v119 offset:2312\n"
      "ds_read_b64_tr_b16 a[208:209], v119 offset:64\n"
      "ds_read_b64_tr_b16 a[210:211], v119 offset:2368\n"
      "ds_read_b64_tr_b16 v[56:57], v119 offset:72\n"
      "ds_read_b64_tr_b16 v[58:59], v119 offset:2376\n"
      "v_add_u32 v118, 4294856704, v118\n"
      "v_add_u32 v119, 4294856704, v119\n"
      "v_add_u32 v120, 4294856704, v120\n"
      "s_waitcnt vmcnt(0) lgkmcnt(0)\n"
      "s_barrier\n"
      "ds_read_b64_tr_b16 v[96:97], v120 offset:0\n"
      "ds_read_b64_tr_b16 v[98:99], v120 offset:2304\n"
      "ds_read_b64_tr_b16 a[32:33], v118 offset:0\n"
      "ds_read_b64_tr_b16 a[34:35], v118 offset:2304\n"
      "ds_read_b64_tr_b16 a[104:105], v118 offset:8\n"
      "ds_read_b64_tr_b16 a[106:107], v118 offset:2312\n"
      "ds_read_b64_tr_b16 a[176:177], v118 offset:64\n"
      "ds_read_b64_tr_b16 a[178:179], v118 offset:2368\n"
      "ds_read_b64_tr_b16 a[248:249], v118 offset:72\n"
      "ds_read_b64_tr_b16 a[250:251], v118 offset:2376\n"
      "ds_read_b64_tr_b16 a[68:69], v119 offset:0\n"
      "ds_read_b64_tr_b16 a[70:71], v119 offset:2304\n"
      "ds_read_b64_tr_b16 a[140:141], v119 offset:8\n"
      "ds_read_b64_tr_b16 a[142:143], v119 offset:2312\n"
      "ds_read_b64_tr_b16 a[212:213], v119 offset:64\n"
      "ds_read_b64_tr_b16 a[214:215], v119 offset:2368\n"
      "ds_read_b64_tr_b16 v[60:61], v119 offset:72\n"
      "ds_read_b64_tr_b16 v[62:63], v119 offset:2376\n"
      "v_add_u32 v118, 36864, v118\n"
      "v_add_u32 v119, 36864, v119\n"
      "v_add_u32 v120, 36864, v120\n"
      "s_waitcnt lgkmcnt(0)\n"
      "s_getpc_b64 vcc\n"
      ".Lw_ct:\n"
      "s_add_u32 vcc_lo, vcc_lo, .Lw_p0-.Lw_ct\n"
      "s_addc_u32 vcc_hi, vcc_hi, 0\n"
      "s_lshl_b32 s30, s39, 1\n"
      "v_lshl_add_u32 v130, v0, 3, s30\n"
      "v_min_u32 v130, .Lw_last-.Lw_p0-4, v130\n"
      "global_load_dword v130, v130, vcc\n"
      "s_mul_i32 s21, s21, 1664525\n"
      "s_add_u32 s21, s21, 1013904223\n"
      "v_mov_b32 v25, s21\n"
      "buffer_store_dword v25, v22, s[8:11], 0 offen sc1\n"
      "v_not_b32 v124, v25\n"
      "v_not_b32 v125, v25\n"
      "v_not_b32 v126, v25\n"
      "v_not_b32 v127, v25\n"
      "s_add_u32 s30, s38, 0\n"
      "buffer_store_dwordx4 v[124:127], v121, s[8:11], s30 offen sc1\n"
      "v_xor_b32 v25, v25, v26\n"
      "v_mov_b32 v27, 0x3ae38e39\n"
      "s_barrier\n"
      "v_mov_b32 v21, 0\n"
      "ds_write_b32 v23, v21\n"
      "s_waitcnt lgkmcnt(0)\n"
      "s_barrier\n"
      "s_add_u32 s6, s36, 1572864\n"
      "s_add_u32 s36, s36, 4294942720\n"
      "s_mov_b32 s51, 24576\n"
      "s_waitcnt vmcnt(0) lgkmcnt(0)\n"
      "s_cmp_lg_u32 s44, 0\n"
      "s_cbranch_scc1 .Lv_start\n"
      "s_mov_b64 exec, 0\n"
      "s_lshr_b32 s22, s39, 13\n"
      ".Lw_pl:\n"
      "s_cmp_eq_u32 s22, 0\n"
      "s_cbranch_scc1 .Lw_p0\n"
      "s_cmp_eq_u32 s22, 1\n"
      "s_cbranch_scc1 .Lw_p1\n"
      "s_branch .Lw_pe\n"
      ".Lw_pr:\n"
      "s_add_u32 s22, s22, 2\n"
      "s_branch .Lw_pl\n"
      ".Lw_pe:\n"
      "s_mov_b64 exec, -1\n"
      "s_add_u32 s50, s38, 4294893568\n"
      "s_add_u32 s49, s38, 4294893568\n"
      "s_add_u32 s38, s38, 4294942720\n"
      "s_cmp_eq_u32 s36, s37\n"
      "s_cselect_b32 s48, 49152, s51\n"
      "s_add_u32 s36, s36, s48\n"
      "s_min_u32 s36, s36, s6\n"
      "buffer_load_dword v28, v11, s[8:11], s36 offen sc1\n"
      "s_mov_b32 s46, s36\n"
      "s_add_u32 s36, s36, 24576\n"
      "s_min_u32 s36, s36, s6\n"
      "s_waitcnt vmcnt(0)\n"
      "v_cmp_ne_u32 vcc, s21, v28\n"
      "s_add_u32 m0, s39, 0\n"
      "s_cbranch_vccnz .Lsp_w0_1\n"
      ".Lbk_w0_1:\n"
      "buffer_load_dword v28, v11, s[8:11], s36 offen sc1\n"
      "buffer_load_dwordx4 v0, s[8:11], s46 offen offset:0 sc1 lds\n"
      "buffer_load_dwordx4 v0, s[8:11], s46 offen offset:1024 sc1 lds\n"
      "buffer_load_dwordx4 v0, s[8:11], s46 offen offset:3072 sc1 lds\n"
      "buffer_load_dwordx4 v0, s[8:11], s46 offen offset:2048 sc1 lds\n"
      "s_add_u32 m0, s40, 0\n"
      "s_nop 0\n"
      "buffer_load_dwordx4 v1, s[8:11], s46 offen offset:0 sc1 lds\n"
      "s_sub_u32 s47, s46, s45\n"
      "buffer_load_dwordx2 v[200:201], v14, s[8:11], s47 offen sc1\n"
      "buffer_load_dwordx4 v[192:195], v13, s[8:11], s47 offen sc1\n"
      "buffer_load_dword v26, v23, s[8:11], s31 offen\n"
      "s_add_u32 m0, s39, 20480\n"
      "s_mov_b32 s46, s36\n"
      "s_waitcnt vmcnt(8)\n"
      "v_cmp_ne_u32 vcc, s21, v28\n"
      "buffer_load_dword v26, v23, s[8:11], s31 offen\n"
      "s_cmp_eq_u32 s36, s37\n"
      "s_cselect_b32 s48, 49152, s51\n"
      "s_add_u32 s36, s36, s48\n"
      "s_cbranch_vccnz .Lsp_w1_2\n"
      ".Lbk_w1_2:\n"
      "s_min_u32 s36, s36, s6\n"
      "buffer_load_dword v28, v11, s[8:11], s36 offen sc1\n"
      "buffer_load_dwordx4 v0, s[8:11], s46 offen offset:0 sc1 lds\n"
      "buffer_load_dwordx4 v0, s[8:11], s46 offen offset:1024 sc1 lds\n"
      "buffer_load_dwordx4 v0, s[8:11], s46 offen offset:3072 sc1 lds\n"
      "v_add_u32 v21, 1, v21\n"
      "s_waitcnt vmcnt(8)\n"
      "ds_write_b32 v23, v21\n"
      "buffer_load_dwordx4 v0, s[8:11], s46 offen offset:2048 sc1 lds\n"
      "s_add_u32 m0, s40, 20480\n"
      "buffer_load_dwordx4 v[196:199], v13, s[8:11], s47 offen offset:1024 sc1\n"
      "buffer_load_dwordx4 v1, s[8:11], s46 offen offset:0 sc1 lds\n"
      "ds_read_b32 v22, v24\n"
      "s_waitcnt lgkmcnt(0)\n"
      "v_cmp_gt_i32 vcc, v21, v22\n"
      "s_waitcnt vmcnt(10)\n"
      "v_and_b32 v189, 0xffff0000, v200\n"
      "v_lshlrev_b32 v188, 16, v200\n"
      "s_cbranch_vccnz .Lsp_wy0_3\n"
      ".Lbk_wy0_3:\n"
      "ds_read_b128 v[104:107], v0 offset:1024\n"
      "v_lshlrev_b32 v190, 16, v201\n"
      "ds_read_b128 v[100:103], v0 offset:0\n"
      "ds_read_b128 v[120:123], v0 offset:5120\n"
      "s_waitcnt vmcnt(9)\n"
      "v_lshlrev_b32 v178, 16, v195\n"
      "ds_read_b128 v[112:115], v0 offset:3072\n"
      "ds_read_b128 v[116:119], v0 offset:4096\n"
      "v_lshlrev_b32 v174, 16, v193\n"
      "ds_read_b128 v[124:127], v0 offset:6144\n"
      "s_sub_u32 s47, s46, s45\n"
      "ds_read_b128 v[108:111], v0 offset:2048\n"
      "v_and_b32 v191, 0xffff0000, v201\n"
      "v_and_b32 v177, 0xffff0000, v194\n"
      "ds_read_b128 v[136:139], v0 offset:8192\n"
      "v_lshlrev_b32 v176, 16, v194\n"
      "ds_read_b128 v[128:131], v0 offset:7168\n"
      "v_lshlrev_b32 v172, 16, v192\n"
      "v_and_b32 v175, 0xffff0000, v193\n"
      "buffer_load_dwordx2 v[200:201], v14, s[8:11], s47 offen sc1\n"
      "ds_read2_b32 v[132:133], v2 offset0:0 offset1:32\n"
      "v_and_b32 v173, 0xffff0000, v192\n"
      "ds_read2_b32 v[134:135], v2 offset0:64 offset1:96\n"
      "s_mov_b32 s20, 15\n"
      "s_waitcnt lgkmcnt(0)\n"
      "s_nop 4\n"
      "s_branch .Lw_loop\n"
      ".p2align 12\n"
      ".Lw_p0:\n"
      "s_branch .Lw_pr\n"
      ".p2align 5\n"
      ".Lw_loop:\n"
      "v_mfma_f32_16x16x32_bf16 v[188:191], v[64:67], v[100:103], v[188:191]\n"
      "ds_read_b128 v[140:143], v0 offset:9216\n"
      "v_and_b32 v179, 0xffff0000, v195\n"
      "v_mfma_f32_16x16x32_bf16 v[188:191], v[68:71], v[104:107], v[188:191]\n"
      "buffer_load_dwordx4 v[192:195], v13, s[8:11], s47 offen sc1\n"
      "v_mfma_f32_16x16x32_bf16 v[188:191], v[72:75], v[108:111], v[188:191]\n"
      "v_add_f32 v218, v218, v182\n"
      "v_mfma_f32_16x16x32_bf16 v[188:191], v[76:79], v[112:115], v[188:191]\n"
      "ds_read_b128 v[156:159], v0 offset:13312\n"
      "v_fmac_f32 v219, v184, v184\n"
      "v_mfma_f32_16x16x32_bf16 v[188:191], v[80:83], v[116:119], v[188:191]\n"
      "ds_read_b128 v[164:167], v0 offset:15360\n"
      "v_cvt_pk_bf16_f32 v213, v182, v183\n"
      "v_mfma_f32_16x16x32_bf16 v[188:191], v[84:87], v[120:123], v[188:191]\n"
      "ds_read_b128 v[144:147], v0 offset:10240\n"
      "v_mfma_f32_16x16x32_bf16 v[188:191], v[88:91], v[124:127], v[188:191]\n"
      "s_waitcnt lgkmcnt(4)\n"
      "v_cvt_pk_bf16_f32 v215, v186, v187\n"
      "v_add_f32 v218, v218, v183\n"
      "v_mfma_f32_16x16x32_bf16 v[188:191], v[92:95], v[128:131], v[188:191]\n"
      "buffer_store_dwordx4 v[208:211], v15, s[8:11], s38 offen sc1\n"
      "v_mfma_f32_16x16x32_bf16 v[188:191], v[96:99], v[132:135], v[188:191]\n"
      "v_cvt_pk_bf16_f32 v212, v180, v181\n"
      "v_add_f32 v218, v218, v184\n"
      "v_mfma_f32_16x16x32_bf16 v[172:175], a[0:3], v[100:103], v[172:175]\n"
      "s_add_u32 s50, s50, 24576\n"
      "v_cvt_pk_bf16_f32 v214, v184, v185\n"
      "v_add_f32 v218, v218, v185\n"
      "v_mfma_f32_16x16x32_bf16 v[172:175], a[4:7], v[104:107], v[172:175]\n"
      "s_mov_b32 s46, s36\n"
      "v_add_f32 v218, v218, v186\n"
      "v_mfma_f32_16x16x32_bf16 v[172:175], a[8:11], v[108:111], v[172:175]\n"
      "ds_read_b128 v[148:151], v0 offset:11264\n"
      "v_add_f32 v218, v218, v187\n"
      "v_mfma_f32_16x16x32_bf16 v[172:175], a[12:15], v[112:115], v[172:175]\n"
      "v_fmac_f32 v219, v185, v185\n"
      "v_add_u32 v21, 1, v21\n"
      "v_mfma_f32_16x16x32_bf16 v[172:175], a[16:19], v[116:119], v[172:175]\n"
      "s_add_u32 s36, s36, 24576\n"
      "ds_read_b128 v[152:155], v0 offset:12288\n"
      "v_fmac_f32 v219, v186, v186\n"
      "v_mfma_f32_16x16x32_bf16 v[172:175], a[20:23], v[120:123], v[172:175]\n"
      "buffer_store_dwordx2 v[216:217], v16, s[8:11], s38 offen sc1\n"
      "v_mfma_f32_16x16x32_bf16 v[172:175], a[24:27], v[124:127], v[172:175]\n"
      "ds_read_b128 v[160:163], v0 offset:14336\n"
      "v_mfma_f32_16x16x32_bf16 v[172:175], a[28:31], v[128:131], v[172:175]\n"
      "ds_read2_b32 v[170:171], v3 offset0:64 offset1:96\n"
      "v_mfma_f32_16x16x32_bf16 v[172:175], a[32:35], v[132:135], v[172:175]\n"
      "ds_read2_b32 v[168:169], v3 offset0:0 offset1:32\n"
      "s_min_u32 s36, s36, s6\n"
      "v_mfma_f32_16x16x32_bf16 v[172:175], a[36:39], v[136:139], v[172:175]\n"
      "s_waitcnt lgkmcnt(4)\n"
      "v_mfma_f32_16x16x32_bf16 v[172:175], a[40:43], v[140:143], v[172:175]\n"
      "buffer_store_dwordx4 v[212:215], v15, s[8:11], s38 offen offset:1024 sc1\n"
      "v_mfma_f32_16x16x32_bf16 v[172:175], a[44:47], v[144:147], v[172:175]\n"
      "ds_write_b32 v18, v218\n"
      "v_mfma_f32_16x16x32_bf16 v[172:175], a[48:51], v[148:151], v[172:175]\n"
      "s_waitcnt vmcnt(11) lgkmcnt(4)\n"
      "v_fmac_f32_e64 v219, v187, v187\n"
      "v_cmp_ne_u32 vcc, s21, v28\n"
      "v_mfma_f32_16x16x32_bf16 v[172:175], a[52:55], v[152:155], v[172:175]\n"
      "ds_write_b32 v18, v219 offset:4\n"
      "v_mfma_f32_16x16x32_bf16 v[172:175], a[56:59], v[156:159], v[172:175]\n"
      "s_waitcnt lgkmcnt(3)\n"
      "s_add_u32 m0, s39, 40960\n"
      "v_mfma_f32_16x16x32_bf16 v[172:175], a[60:63], v[160:163], v[172:175]\n"
      "ds_write_b32 v23, v21\n"
      "v_mfma_f32_16x16x32_bf16 v[172:175], a[64:67], v[164:167], v[172:175]\n"
      "s_waitcnt lgkmcnt(3)\n"
      "s_cbranch_vccnz .Lsp_w0_4\n"
      ".Lbk_w0_4:\n"
      "v_mfma_f32_16x16x32_bf16 v[172:175], a[68:71], v[168:171], v[172:175]\n"
      "v_mfma_f32_16x16x32_bf16 v[176:179], a[72:75], v[100:103], v[176:179]\n"
      "buffer_load_dword v28, v11, s[8:11], s36 offen sc1\n"
      "v_mfma_f32_16x16x32_bf16 v[176:179], a[76:79], v[104:107], v[176:179]\n"
      "s_waitcnt vmcnt(7) lgkmcnt(2)\n"
      "v_lshlrev_b32 v186, 16, v199\n"
      "v_lshlrev_b32 v184, 16, v198\n"
      "v_mfma_f32_16x16x32_bf16 v[176:179], a[80:83], v[108:111], v[176:179]\n"
      "v_lshlrev_b32 v180, 16, v196\n"
      "v_and_b32 v183, 0xffff0000, v197\n"
      "v_mfma_f32_16x16x32_bf16 v[176:179], a[84:87], v[112:115], v[176:179]\n"
      "buffer_store_dword v25, v12, s[8:11], s50 offen sc1\n"
      "v_mfma_f32_16x16x32_bf16 v[176:179], a[88:91], v[116:119], v[176:179]\n"
      "v_lshlrev_b32 v182, 16, v197\n"
      "v_and_b32 v181, 0xffff0000, v196\n"
      "v_mfma_f32_16x16x32_bf16 v[176:179], a[92:95], v[120:123], v[176:179]\n"
      "v_and_b32 v187, 0xffff0000, v199\n"
      "v_and_b32 v185, 0xffff0000, v198\n"
      "v_mfma_f32_16x16x32_bf16 v[176:179], a[96:99], v[124:127], v[176:179]\n"
      "buffer_load_dwordx4 v[196:199], v13, s[8:11], s47 offen offset:1024 sc1\n"
      "v_mfma_f32_16x16x32_bf16 v[176:179], a[100:103], v[128:131], v[176:179]\n"
      "ds_read_b32 v22, v24\n"
      "v_mul_f32 v219, v172, v172\n"
      "v_mfma_f32_16x16x32_bf16 v[176:179], a[104:107], v[132:135], v[176:179]\n"
      "v_mfma_f32_16x16x32_bf16 v[176:179], a[108:111], v[136:139], v[176:179]\n"
      "v_mfma_f32_16x16x32_bf16 v[176:179], a[112:115], v[140:143], v[176:179]\n"
      "buffer_load_dwordx4 v0, s[8:11], s46 offen offset:0 sc1 lds\n"
      "v_mfma_f32_16x16x32_bf16 v[176:179], a[116:119], v[144:147], v[176:179]\n"
      "v_add_f32 v218, v172, v173\n"
      "v_fmac_f32 v219, v173, v173\n"
      "v_mfma_f32_16x16x32_bf16 v[176:179], a[120:123], v[148:151], v[176:179]\n"
      "s_waitcnt lgkmcnt(0)\n"
      "v_cmp_gt_i32 vcc, v21, v22\n"
      "v_mfma_f32_16x16x32_bf16 v[176:179], a[124:127], v[152:155], v[176:179]\n"
      "v_mfma_f32_16x16x32_bf16 v[176:179], a[128:131], v[156:159], v[176:179]\n"
      "buffer_load_dwordx4 v0, s[8:11], s46 offen offset:1024 sc1 lds\n"
      "v_mfma_f32_16x16x32_bf16 v[176:179], a[132:135], v[160:163], v[176:179]\n"
      "s_cbranch_vccnz .Lsp_wz0_5\n"
      ".Lbk_wz0_5:\n"
      "ds_read2st64_b32 v[204:205], v17 offset1:1\n"
      "v_mfma_f32_16x16x32_bf16 v[176:179], a[136:139], v[164:167], v[176:179]\n"
      "ds_read2st64_b32 v[206:207], v17 offset0:2 offset1:3\n"
      "v_add_u32 v21, 1, v21\n"
      "v_mfma_f32_16x16x32_bf16 v[176:179], a[140:143], v[168:171], v[176:179]\n"
      "s_waitcnt vmcnt(10)\n"
      "ds_write_b32 v23, v21\n"
      "v_mfma_f32_16x16x32_bf16 v[180:183], a[144:147], v[100:103], v[180:183]\n"
      "buffer_load_dwordx4 v0, s[8:11], s46 offen offset:3072 sc1 lds\n"
      "v_mfma_f32_16x16x32_bf16 v[180:183], a[148:151], v[104:107], v[180:183]\n"
      "v_add_f32 v218, v218, v174\n"
      "v_fmac_f32 v219, v174, v174\n"
      "v_mfma_f32_16x16x32_bf16 v[180:183], a[152:155], v[108:111], v[180:183]\n"
      "v_fmac_f32 v219, v175, v175\n"
      "v_fmac_f32 v219, v176, v176\n"
      "v_mfma_f32_16x16x32_bf16 v[180:183], a[156:159], v[112:115], v[180:183]\n"
      "v_add_f32 v218, v218, v175\n"
      "v_add_f32 v218, v218, v176\n"
      "v_mfma_f32_16x16x32_bf16 v[180:183], a[160:163], v[116:119], v[180:183]\n"
      "buffer_load_dwordx4 v0, s[8:11], s46 offen offset:2048 sc1 lds\n"
      "v_mfma_f32_16x16x32_bf16 v[180:183], a[164:167], v[120:123], v[180:183]\n"
      "s_add_u32 m0, s40, 40960\n"
      "v_add_f32 v218, v218, v177\n"
      "v_cvt_pk_bf16_f32 v209, v174, v175\n"
      "v_mfma_f32_16x16x32_bf16 v[180:183], a[168:171], v[124:127], v[180:183]\n"
      "s_waitcnt lgkmcnt(0)\n"
      "v_add_f32 v188, v188, v204\n"
      "v_add_f32 v190, v190, v206\n"
      "v_mfma_f32_16x16x32_bf16 v[180:183], a[172:175], v[128:131], v[180:183]\n"
      "v_cvt_pk_bf16_f32 v208, v172, v173\n"
      "v_fmac_f32 v219, v177, v177\n"
      "v_mfma_f32_16x16x32_bf16 v[180:183], a[176:179], v[132:135], v[180:183]\n"
      "buffer_load_dwordx4 v1, s[8:11], s46 offen offset:0 sc1 lds\n"
      "v_mfma_f32_16x16x32_bf16 v[180:183], a[180:183], v[136:139], v[180:183]\n"
      "s_add_u32 s38, s38, 24576\n"
      "v_cvt_pk_bf16_f32 v210, v176, v177\n"
      "v_mfma_f32_16x16x32_bf16 v[180:183], a[184:187], v[140:143], v[180:183]\n"
      "ds_read_b32 v22, v24\n"
      "v_add_f32 v218, v218, v178\n"
      "v_mfma_f32_16x16x32_bf16 v[180:183], a[188:191], v[144:147], v[180:183]\n"
      "v_add_f32 v218, v218, v179\n"
      "v_add_f32 v218, v218, v188\n"
      "v_mfma_f32_16x16x32_bf16 v[180:183], a[192:195], v[148:151], v[180:183]\n"
      "s_add_u32 s50, s50, 24576\n"
      "v_fmac_f32 v219, v178, v178\n"
      "v_fmac_f32 v219, v179, v179\n"
      "v_mfma_f32_16x16x32_bf16 v[180:183], a[196:199], v[152:155], v[180:183]\n"
      "v_add_f32 v191, v191, v207\n"
      "v_cvt_pk_bf16_f32 v211, v178, v179\n"
      "v_mfma_f32_16x16x32_bf16 v[180:183], a[200:203], v[156:159], v[180:183]\n"
      "v_fmac_f32 v219, v188, v188\n"
      "v_add_f32 v189, v189, v205\n"
      "v_mfma_f32_16x16x32_bf16 v[180:183], a[204:207], v[160:163], v[180:183]\n"
      "s_waitcnt lgkmcnt(0)\n"
      "v_fmac_f32 v219, v189, v189\n"
      "v_cmp_gt_i32 vcc, v21, v22\n"
      "v_mfma_f32_16x16x32_bf16 v[180:183], a[208:211], v[164:167], v[180:183]\n"
      "v_fmac_f32 v219, v190, v190\n"
      "v_add_f32 v218, v218, v189\n"
      "v_mfma_f32_16x16x32_bf16 v[180:183], a[212:215], v[168:171], v[180:183]\n"
      "s_waitcnt vmcnt(11)\n"
      "v_lshlrev_b32 v172, 16, v192\n"
      "v_mfma_f32_16x16x32_bf16 v[184:187], a[216:219], v[100:103], v[184:187]\n"
      "v_and_b32 v175, 0xffff0000, v193\n"
      "v_and_b32 v177, 0xffff0000, v194\n"
      "v_mfma_f32_16x16x32_bf16 v[184:187], a[220:223], v[104:107], v[184:187]\n"
      "s_cbranch_vccnz .Lsp_wy1_6\n"
      ".Lbk_wy1_6:\n"
      "v_add_f32 v218, v218, v190\n"
      "v_mfma_f32_16x16x32_bf16 v[184:187], a[224:227], v[108:111], v[184:187]\n"
      "ds_read_b128 v[104:107], v0 offset:21504\n"
      "v_cvt_pk_bf16_f32 v216, v188, v189\n"
      "v_mfma_f32_16x16x32_bf16 v[184:187], a[228:231], v[112:115], v[184:187]\n"
      "v_add_f32 v218, v218, v191\n"
      "v_add_f32 v218, v218, v180\n"
      "v_mfma_f32_16x16x32_bf16 v[184:187], a[232:235], v[116:119], v[184:187]\n"
      "ds_read_b128 v[116:119], v0 offset:24576\n"
      "v_cvt_pk_bf16_f32 v217, v190, v191\n"
      "v_mfma_f32_16x16x32_bf16 v[184:187], a[236:239], v[120:123], v[184:187]\n"
      "ds_read_b128 v[108:111], v0 offset:22528\n"
      "v_lshlrev_b32 v188, 16, v200\n"
      "v_mfma_f32_16x16x32_bf16 v[184:187], a[240:243], v[124:127], v[184:187]\n"
      "ds_read_b128 v[100:103], v0 offset:20480\n"
      "v_fmac_f32 v219, v191, v191\n"
      "v_mfma_f32_16x16x32_bf16 v[184:187], a[244:247], v[128:131], v[184:187]\n"
      "v_lshlrev_b32 v190, 16, v201\n"
      "v_and_b32 v191, 0xffff0000, v201\n"
      "v_mfma_f32_16x16x32_bf16 v[184:187], a[248:251], v[132:135], v[184:187]\n"
      "ds_read2_b32 v[134:135], v4 offset0:64 offset1:96\n"
      "v_lshlrev_b32 v174, 16, v193\n"
      "v_mfma_f32_16x16x32_bf16 v[184:187], a[252:255], v[136:139], v[184:187]\n"
      "s_sub_u32 s47, s46, s45\n"
      "v_and_b32 v189, 0xffff0000, v200\n"
      "v_fmac_f32 v219, v180, v180\n"
      "v_mfma_f32_16x16x32_bf16 v[184:187], v[32:35], v[140:143], v[184:187]\n"
      "buffer_store_dwordx4 v[208:211], v15, s[8:11], s38 offen sc1\n"
      "v_mfma_f32_16x16x32_bf16 v[184:187], v[36:39], v[144:147], v[184:187]\n"
      "ds_read_b128 v[136:139], v0 offset:28672\n"
      "v_fmac_f32 v219, v181, v181\n"
      "v_mfma_f32_16x16x32_bf16 v[184:187], v[40:43], v[148:151], v[184:187]\n"
      "ds_read2_b32 v[132:133], v4 offset0:0 offset1:32\n"
      "v_lshlrev_b32 v178, 16, v195\n"
      "v_mfma_f32_16x16x32_bf16 v[184:187], v[44:47], v[152:155], v[184:187]\n"
      "ds_read_b128 v[112:115], v0 offset:23552\n"
      "v_add_f32 v218, v218, v181\n"
      "v_mfma_f32_16x16x32_bf16 v[184:187], v[48:51], v[156:159], v[184:187]\n"
      "ds_read_b128 v[120:123], v0 offset:25600\n"
      "v_and_b32 v173, 0xffff0000, v192\n"
      "v_mfma_f32_16x16x32_bf16 v[184:187], v[52:55], v[160:163], v[184:187]\n"
      "ds_read_b128 v[124:127], v0 offset:26624\n"
      "v_lshlrev_b32 v176, 16, v194\n"
      "v_mfma_f32_16x16x32_bf16 v[184:187], v[56:59], v[164:167], v[184:187]\n"
      "buffer_load_dwordx2 v[200:201], v14, s[8:11], s47 offen sc1\n"
      "v_mfma_f32_16x16x32_bf16 v[184:187], v[60:63], v[168:171], v[184:187]\n"
      "s_waitcnt lgkmcnt(5)\n"
      "ds_read_b128 v[128:131], v0 offset:27648\n"
      "v_add_f32 v218, v218, v182\n"
      "v_mfma_f32_16x16x32_bf16 v[188:191], v[64:67], v[100:103], v[188:191]\n"
      "ds_read_b128 v[140:143], v0 offset:29696\n"
      "v_cvt_pk_bf16_f32 v213, v182, v183\n"
      "v_mfma_f32_16x16x32_bf16 v[188:191], v[68:71], v[104:107], v[188:191]\n"
      "ds_read_b128 v[152:155], v0 offset:32768\n"
      "v_mfma_f32_16x16x32_bf16 v[188:191], v[72:75], v[108:111], v[188:191]\n"
      "s_waitcnt lgkmcnt(5)\n"
      "buffer_store_dwordx2 v[216:217], v16, s[8:11], s38 offen sc1\n"
      "v_mfma_f32_16x16x32_bf16 v[188:191], v[76:79], v[112:115], v[188:191]\n"
      "ds_read_b128 v[144:147], v0 offset:30720\n"
      "v_cvt_pk_bf16_f32 v212, v180, v181\n"
      "v_mfma_f32_16x16x32_bf16 v[188:191], v[80:83], v[116:119], v[188:191]\n"
      "s_waitcnt lgkmcnt(4)\n"
      "ds_read_b128 v[164:167], v0 offset:35840\n"
      "v_add_f32 v218, v218, v183\n"
      "v_mfma_f32_16x16x32_bf16 v[188:191], v[84:87], v[120:123], v[188:191]\n"
      "v_fmac_f32 v219, v182, v182\n"
      "v_add_f32 v218, v218, v184\n"
      "v_mfma_f32_16x16x32_bf16 v[188:191], v[88:91], v[124:127], v[188:191]\n"
      "s_waitcnt lgkmcnt(4)\n"
      "ds_read_b128 v[148:151], v0 offset:31744\n"
      "v_add_f32 v218, v218, v185\n"
      "v_mfma_f32_16x16x32_bf16 v[188:191], v[92:95], v[128:131], v[188:191]\n"
      "ds_read_b128 v[160:163], v0 offset:34816\n"
      "v_and_b32 v179, 0xffff0000, v195\n"
      "v_mfma_f32_16x16x32_bf16 v[188:191], v[96:99], v[132:135], v[188:191]\n"
      "buffer_load_dwordx4 v[192:195], v13, s[8:11], s47 offen sc1\n"
      "v_mfma_f32_16x16x32_bf16 v[172:175], a[0:3], v[100:103], v[172:175]\n"
      "v_cvt_pk_bf16_f32 v215, v186, v187\n"
      "v_fmac_f32 v219, v183, v183\n"
      "v_mfma_f32_16x16x32_bf16 v[172:175], a[4:7], v[104:107], v[172:175]\n"
      "v_cvt_pk_bf16_f32 v214, v184, v185\n"
      "v_mfma_f32_16x16x32_bf16 v[172:175], a[8:11], v[108:111], v[172:175]\n"
      "ds_read2_b32 v[168:169], v5 offset0:0 offset1:32\n"
      "v_mfma_f32_16x16x32_bf16 v[172:175], a[12:15], v[112:115], v[172:175]\n"
      "ds_read_b128 v[156:159], v0 offset:33792\n"
      "v_mfma_f32_16x16x32_bf16 v[172:175], a[16:19], v[116:119], v[172:175]\n"
      "buffer_store_dwordx4 v[212:215], v15, s[8:11], s38 offen offset:1024 sc1\n"
      "v_mfma_f32_16x16x32_bf16 v[172:175], a[20:23], v[120:123], v[172:175]\n"
      "v_fmac_f32 v219, v184, v184\n"
      "v_fmac_f32 v219, v185, v185\n"
      "v_mfma_f32_16x16x32_bf16 v[172:175], a[24:27], v[124:127], v[172:175]\n"
      "ds_read2_b32 v[170:171], v5 offset0:64 offset1:96\n"
      "v_add_f32 v218, v218, v186\n"
      "v_mfma_f32_16x16x32_bf16 v[172:175], a[28:31], v[128:131], v[172:175]\n"
      "v_fmac_f32 v219, v186, v186\n"
      "v_add_f32 v218, v218, v187\n"
      "v_mfma_f32_16x16x32_bf16 v[172:175], a[32:35], v[132:135], v[172:175]\n"
      "ds_write_b32 v18, v218\n"
      "v_mfma_f32_16x16x32_bf16 v[172:175], a[36:39], v[136:139], v[172:175]\n"
      "s_waitcnt lgkmcnt(3)\n"
      "s_add_u32 m0, s39, 61440\n"
      "v_add_u32 v21, 1, v21\n"
      "v_mfma_f32_16x16x32_bf16 v[172:175], a[40:43], v[140:143], v[172:175]\n"
      "s_mov_b32 s46, s36\n"
      "v_mfma_f32_16x16x32_bf16 v[172:175], a[44:47], v[144:147], v[172:175]\n"
      "v_fmac_f32 v219, v187, v187\n"
      "v_mfma_f32_16x16x32_bf16 v[172:175], a[48:51], v[148:151], v[172:175]\n"
      "ds_write_b32 v18, v219 offset:4\n"
      "v_mfma_f32_16x16x32_bf16 v[172:175], a[52:55], v[152:155], v[172:175]\n"
      "s_waitcnt vmcnt(10) lgkmcnt(2)\n"
      "v_and_b32 v181, 0xffff0000, v196\n"
      "v_mfma_f32_16x16x32_bf16 v[172:175], a[56:59], v[156:159], v[172:175]\n"
      "ds_write_b32 v23, v21\n"
      "v_mfma_f32_16x16x32_bf16 v[172:175], a[60:63], v[160:163], v[172:175]\n"
      "v_lshlrev_b32 v186, 16, v199\n"
      "v_cmp_ne_u32 vcc, s21, v28\n"
      "v_mfma_f32_16x16x32_bf16 v[172:175], a[64:67], v[164:167], v[172:175]\n"
      "buffer_store_dword v25, v12, s[8:11], s50 offen sc1\n"
      "v_mfma_f32_16x16x32_bf16 v[172:175], a[68:71], v[168:171], v[172:175]\n"
      "s_cmp_eq_u32 s36, s37\n"
      "s_cselect_b32 s48, 49152, s51\n"
      "s_add_u32 s36, s36, s48\n"
      "v_mfma_f32_16x16x32_bf16 v[176:179], a[72:75], v[100:103], v[176:179]\n"
      "s_cbranch_vccnz .Lsp_w1_7\n"
      ".Lbk_w1_7:\n"
      "s_min_u32 s36, s36, s6\n"
      "v_mfma_f32_16x16x32_bf16 v[176:179], a[76:79], v[104:107], v[176:179]\n"
      "buffer_load_dword v28, v11, s[8:11], s36 offen sc1\n"
      "v_mfma_f32_16x16x32_bf16 v[176:179], a[80:83], v[108:111], v[176:179]\n"
      "v_and_b32 v183, 0xffff0000, v197\n"
      "v_mul_f32 v219, v172, v172\n"
      "v_mfma_f32_16x16x32_bf16 v[176:179], a[84:87], v[112:115], v[176:179]\n"
      "buffer_load_dwordx4 v0, s[8:11], s46 offen offset:0 sc1 lds\n"
      "v_mfma_f32_16x16x32_bf16 v[176:179], a[88:91], v[116:119], v[176:179]\n"
      "v_mfma_f32_16x16x32_bf16 v[176:179], a[92:95], v[120:123], v[176:179]\n"
      "v_fmac_f32 v219, v173, v173\n"
      "v_mfma_f32_16x16x32_bf16 v[176:179], a[96:99], v[124:127], v[176:179]\n"
      "v_lshlrev_b32 v184, 16, v198\n"
      "v_and_b32 v185, 0xffff0000, v198\n"
      "v_mfma_f32_16x16x32_bf16 v[176:179], a[100:103], v[128:131], v[176:179]\n"
      "ds_read_b32 v22, v24\n"
      "v_fmac_f32 v219, v174, v174\n"
      "v_mfma_f32_16x16x32_bf16 v[176:179], a[104:107], v[132:135], v[176:179]\n"
      "buffer_load_dwordx4 v0, s[8:11], s46 offen offset:1024 sc1 lds\n"
      "v_mfma_f32_16x16x32_bf16 v[176:179], a[108:111], v[136:139], v[176:179]\n"
      "v_lshlrev_b32 v182, 16, v197\n"
      "v_add_f32 v218, v172, v173\n"
      "v_mfma_f32_16x16x32_bf16 v[176:179], a[112:115], v[140:143], v[176:179]\n"
      "v_add_f32 v218, v218, v174\n"
      "v_fmac_f32 v219, v175, v175\n"
      "v_mfma_f32_16x16x32_bf16 v[176:179], a[116:119], v[144:147], v[176:179]\n"
      "v_cvt_pk_bf16_f32 v208, v172, v173\n"
      "v_mfma_f32_16x16x32_bf16 v[176:179], a[120:123], v[148:151], v[176:179]\n"
      "s_waitcnt lgkmcnt(0)\n"
      "v_and_b32 v187, 0xffff0000, v199\n"
      "v_cmp_gt_i32 vcc, v21, v22\n"
      "v_mfma_f32_16x16x32_bf16 v[176:179], a[124:127], v[152:155], v[176:179]\n"
      "buffer_load_dwordx4 v0, s[8:11], s46 offen offset:3072 sc1 lds\n"
      "v_mfma_f32_16x16x32_bf16 v[176:179], a[128:131], v[156:159], v[176:179]\n"
      "v_lshlrev_b32 v180, 16, v196\n"
      "v_add_f32 v218, v218, v175\n"
      "v_mfma_f32_16x16x32_bf16 v[176:179], a[132:135], v[160:163], v[176:179]\n"
      "s_cbranch_vccnz .Lsp_wz1_8\n"
      ".Lbk_wz1_8:\n"
      "ds_read2st64_b32 v[204:205], v17 offset1:1\n"
      "v_mfma_f32_16x16x32_bf16 v[176:179], a[136:139], v[164:167], v[176:179]\n"
      "ds_read2st64_b32 v[206:207], v17 offset0:2 offset1:3\n"
      "v_add_u32 v21, 1, v21\n"
      "v_mfma_f32_16x16x32_bf16 v[176:179], a[140:143], v[168:171], v[176:179]\n"
      "s_waitcnt vmcnt(10)\n"
      "ds_write_b32 v23, v21\n"
      "v_mfma_f32_16x16x32_bf16 v[180:183], a[144:147], v[100:103], v[180:183]\n"
      "buffer_load_dwordx4 v0, s[8:11], s46 offen offset:2048 sc1 lds\n"
      "v_mfma_f32_16x16x32_bf16 v[180:183], a[148:151], v[104:107], v[180:183]\n"
      "v_mfma_f32_16x16x32_bf16 v[180:183], a[152:155], v[108:111], v[180:183]\n"
      "s_add_u32 m0, s40, 61440\n"
      "v_add_f32 v218, v218, v176\n"
      "v_mfma_f32_16x16x32_bf16 v[180:183], a[156:159], v[112:115], v[180:183]\n"
      "v_fmac_f32 v219, v176, v176\n"
      "v_cvt_pk_bf16_f32 v209, v174, v175\n"
      "v_mfma_f32_16x16x32_bf16 v[180:183], a[160:163], v[116:119], v[180:183]\n"
      "buffer_load_dwordx4 v[196:199], v13, s[8:11], s47 offen offset:1024 sc1\n"
      "v_mfma_f32_16x16x32_bf16 v[180:183], a[164:167], v[120:123], v[180:183]\n"
      "s_waitcnt lgkmcnt(1)\n"
      "v_cvt_pk_bf16_f32 v211, v178, v179\n"
      "v_add_f32 v190, v190, v206\n"
      "v_mfma_f32_16x16x32_bf16 v[180:183], a[168:171], v[124:127], v[180:183]\n"
      "v_add_f32 v218, v218, v177\n"
      "v_add_f32 v218, v218, v178\n"
      "v_mfma_f32_16x16x32_bf16 v[180:183], a[172:175], v[128:131], v[180:183]\n"
      "v_add_f32 v218, v218, v179\n"
      "v_add_f32 v189, v189, v205\n"
      "v_mfma_f32_16x16x32_bf16 v[180:183], a[176:179], v[132:135], v[180:183]\n"
      "buffer_load_dwordx4 v1, s[8:11], s46 offen offset:0 sc1 lds\n"
      "v_mfma_f32_16x16x32_bf16 v[180:183], a[180:183], v[136:139], v[180:183]\n"
      "v_add_f32 v188, v188, v204\n"
      "v_fmac_f32 v219, v177, v177\n"
      "v_mfma_f32_16x16x32_bf16 v[180:183], a[184:187], v[140:143], v[180:183]\n"
      "ds_read_b32 v22, v24\n"
      "v_fmac_f32 v219, v178, v178\n"
      "v_mfma_f32_16x16x32_bf16 v[180:183], a[188:191], v[144:147], v[180:183]\n"
      "v_cvt_pk_bf16_f32 v210, v176, v177\n"
      "v_fmac_f32 v219, v179, v179\n"
      "v_mfma_f32_16x16x32_bf16 v[180:183], a[192:195], v[148:151], v[180:183]\n"
      "v_add_f32 v191, v191, v207\n"
      "v_fmac_f32 v219, v188, v188\n"
      "v_mfma_f32_16x16x32_bf16 v[180:183], a[196:199], v[152:155], v[180:183]\n"
      "v_add_f32 v218, v218, v188\n"
      "v_mfma_f32_16x16x32_bf16 v[180:183], a[200:203], v[156:159], v[180:183]\n"
      "v_add_f32 v218, v218, v189\n"
      "v_add_f32 v218, v218, v190\n"
      "v_mfma_f32_16x16x32_bf16 v[180:183], a[204:207], v[160:163], v[180:183]\n"
      "s_waitcnt lgkmcnt(0)\n"
      "v_fmac_f32 v219, v189, v189\n"
      "v_cmp_gt_i32 vcc, v21, v22\n"
      "v_mfma_f32_16x16x32_bf16 v[180:183], a[208:211], v[164:167], v[180:183]\n"
      "v_cvt_pk_bf16_f32 v216, v188, v189\n"
      "v_fmac_f32 v219, v190, v190\n"
      "v_mfma_f32_16x16x32_bf16 v[180:183], a[212:215], v[168:171], v[180:183]\n"
      "s_waitcnt vmcnt(11)\n"
      "v_and_b32 v189, 0xffff0000, v200\n"
      "v_fmac_f32 v219, v191, v191\n"
      "v_mfma_f32_16x16x32_bf16 v[184:187], a[216:219], v[100:103], v[184:187]\n"
      "s_add_u32 s38, s38, 24576\n"
      "v_cvt_pk_bf16_f32 v217, v190, v191\n"
      "v_lshlrev_b32_e64 v188, 16, v200\n"
      "v_mfma_f32_16x16x32_bf16 v[184:187], a[220:223], v[104:107], v[184:187]\n"
      "s_cbranch_vccnz .Lsp_wy0_9\n"
      ".Lbk_wy0_9:\n"
      "v_mfma_f32_16x16x32_bf16 v[184:187], a[224:227], v[108:111], v[184:187]\n"
      "ds_read_b128 v[104:107], v10 offset:1024\n"
      "v_add_f32 v218, v218, v191\n"
      "v_mfma_f32_16x16x32_bf16 v[184:187], a[228:231], v[112:115], v[184:187]\n"
      "v_fmac_f32 v219, v180, v180\n"
      "v_lshlrev_b32 v190, 16, v201\n"
      "v_mfma_f32_16x16x32_bf16 v[184:187], a[232:235], v[116:119], v[184:187]\n"
      "ds_read_b128 v[100:103], v10 offset:0\n"
      "v_add_f32 v218, v218, v180\n"
      "v_mfma_f32_16x16x32_bf16 v[184:187], a[236:239], v[120:123], v[184:187]\n"
      "s_waitcnt vmcnt(9)\n"
      "ds_read_b128 v[120:123], v10 offset:5120\n"
      "v_lshlrev_b32 v178, 16, v195\n"
      "v_mfma_f32_16x16x32_bf16 v[184:187], a[240:243], v[124:127], v[184:187]\n"
      "ds_read_b128 v[112:115], v10 offset:3072\n"
      "v_fmac_f32 v219, v181, v181\n"
      "v_mfma_f32_16x16x32_bf16 v[184:187], a[244:247], v[128:131], v[184:187]\n"
      "ds_read_b128 v[116:119], v10 offset:4096\n"
      "v_lshlrev_b32 v174, 16, v193\n"
      "v_mfma_f32_16x16x32_bf16 v[184:187], a[248:251], v[132:135], v[184:187]\n"
      "ds_read_b128 v[124:127], v10 offset:6144\n"
      "v_fmac_f32 v219, v182, v182\n"
      "v_mfma_f32_16x16x32_bf16 v[184:187], a[252:255], v[136:139], v[184:187]\n"
      "s_sub_u32 s47, s46, s45\n"
      "ds_read_b128 v[108:111], v10 offset:2048\n"
      "v_mfma_f32_16x16x32_bf16 v[184:187], v[32:35], v[140:143], v[184:187]\n"
      "v_and_b32 v191, 0xffff0000, v201\n"
      "v_and_b32 v177, 0xffff0000, v194\n"
      "v_mfma_f32_16x16x32_bf16 v[184:187], v[36:39], v[144:147], v[184:187]\n"
      "ds_read_b128 v[136:139], v10 offset:8192\n"
      "v_lshlrev_b32 v176, 16, v194\n"
      "v_mfma_f32_16x16x32_bf16 v[184:187], v[40:43], v[148:151], v[184:187]\n"
      "ds_read_b128 v[128:131], v10 offset:7168\n"
      "v_lshlrev_b32 v172, 16, v192\n"
      "v_mfma_f32_16x16x32_bf16 v[184:187], v[44:47], v[152:155], v[184:187]\n"
      "v_fmac_f32 v219, v183, v183\n"
      "v_and_b32 v175, 0xffff0000, v193\n"
      "v_mfma_f32_16x16x32_bf16 v[184:187], v[48:51], v[156:159], v[184:187]\n"
      "buffer_load_dwordx2 v[200:201], v14, s[8:11], s47 offen sc1\n"
      "v_mfma_f32_16x16x32_bf16 v[184:187], v[52:55], v[160:163], v[184:187]\n"
      "ds_read2_b32 v[132:133], v6 offset0:0 offset1:32\n"
      "v_and_b32 v173, 0xffff0000, v192\n"
      "v_mfma_f32_16x16x32_bf16 v[184:187], v[56:59], v[164:167], v[184:187]\n"
      "s_waitcnt lgkmcnt(3)\n"
      "ds_read2_b32 v[134:135], v6 offset0:64 offset1:96\n"
      "v_add_f32 v218, v218, v181\n"
      "v_mfma_f32_16x16x32_bf16 v[184:187], v[60:63], v[168:171], v[184:187]\n"
      "v_mfma_f32_16x16x32_bf16 v[188:191], v[64:67], v[100:103], v[188:191]\n"
      "ds_read_b128 v[140:143], v10 offset:9216\n"
      "v_and_b32 v179, 0xffff0000, v195\n"
      "v_mfma_f32_16x16x32_bf16 v[188:191], v[68:71], v[104:107], v[188:191]\n"
      "buffer_load_dwordx4 v[192:195], v13, s[8:11], s47 offen sc1\n"
      "v_mfma_f32_16x16x32_bf16 v[188:191], v[72:75], v[108:111], v[188:191]\n"
      "v_add_f32 v218, v218, v182\n"
      "v_mfma_f32_16x16x32_bf16 v[188:191], v[76:79], v[112:115], v[188:191]\n"
      "ds_read_b128 v[156:159], v10 offset:13312\n"
      "v_fmac_f32 v219, v184, v184\n"
      "v_mfma_f32_16x16x32_bf16 v[188:191], v[80:83], v[116:119], v[188:191]\n"
      "ds_read_b128 v[164:167], v10 offset:15360\n"
      "v_cvt_pk_bf16_f32 v213, v182, v183\n"
      "v_mfma_f32_16x16x32_bf16 v[188:191], v[84:87], v[120:123], v[188:191]\n"
      "ds_read_b128 v[144:147], v10 offset:10240\n"
      "v_mfma_f32_16x16x32_bf16 v[188:191], v[88:91], v[124:127], v[188:191]\n"
      "s_waitcnt lgkmcnt(4)\n"
      "v_cvt_pk_bf16_f32 v215, v186, v187\n"
      "v_add_f32 v218, v218, v183\n"
      "v_mfma_f32_16x16x32_bf16 v[188:191], v[92:95], v[128:131], v[188:191]\n"
      "buffer_store_dwordx4 v[208:211], v15, s[8:11], s38 offen sc1\n"
      "v_mfma_f32_16x16x32_bf16 v[188:191], v[96:99], v[132:135], v[188:191]\n"
      "v_cvt_pk_bf16_f32 v212, v180, v181\n"
      "v_add_f32 v218, v218, v184\n"
      "v_mfma_f32_16x16x32_bf16 v[172:175], a[0:3], v[100:103], v[172:175]\n"
      "s_add_u32 s50, s50, 24576\n"
      "v_cvt_pk_bf16_f32 v214, v184, v185\n"
      "v_add_f32 v218, v218, v185\n"
      "v_mfma_f32_16x16x32_bf16 v[172:175], a[4:7], v[104:107], v[172:175]\n"
      "s_mov_b32 s46, s36\n"
      "v_add_f32 v218, v218, v186\n"
      "v_mfma_f32_16x16x32_bf16 v[172:175], a[8:11], v[108:111], v[172:175]\n"
      "ds_read_b128 v[148:151], v10 offset:11264\n"
      "v_add_f32 v218, v218, v187\n"
      "v_mfma_f32_16x16x32_bf16 v[172:175], a[12:15], v[112:115], v[172:175]\n"
      "v_fmac_f32 v219, v185, v185\n"
      "v_add_u32 v21, 1, v21\n"
      "v_mfma_f32_16x16x32_bf16 v[172:175], a[16:19], v[116:119], v[172:175]\n"
      "s_add_u32 s36, s36, 24576\n"
      "ds_read_b128 v[152:155], v10 offset:12288\n"
      "v_fmac_f32_e64 v219, v186, v186\n"
      "v_mfma_f32_16x16x32_bf16 v[172:175], a[20:23], v[120:123], v[172:175]\n"
      "buffer_store_dwordx2 v[216:217], v16, s[8:11], s38 offen sc1\n"
      "v_mfma_f32_16x16x32_bf16 v[172:175], a[24:27], v[124:127], v[172:175]\n"
      "ds_read_b128 v[160:163], v10 offset:14336\n"
      "v_mfma_f32_16x16x32_bf16 v[172:175], a[28:31], v[128:131], v[172:175]\n"
      "ds_read2_b32 v[170:171], v7 offset0:64 offset1:96\n"
      "v_mfma_f32_16x16x32_bf16 v[172:175], a[32:35], v[132:135], v[172:175]\n"
      "ds_read2_b32 v[168:169], v7 offset0:0 offset1:32\n"
      "s_min_u32 s36, s36, s6\n"
      "v_mfma_f32_16x16x32_bf16 v[172:175], a[36:39], v[136:139], v[172:175]\n"
      "s_waitcnt lgkmcnt(4)\n"
      "v_mfma_f32_16x16x32_bf16 v[172:175], a[40:43], v[140:143], v[172:175]\n"
      "buffer_store_dwordx4 v[212:215], v15, s[8:11], s38 offen offset:1024 sc1\n"
      "v_mfma_f32_16x16x32_bf16 v[172:175], a[44:47], v[144:147], v[172:175]\n"
      "ds_write_b32 v18, v218\n"
      "v_mfma_f32_16x16x32_bf16 v[172:175], a[48:51], v[148:151], v[172:175]\n"
      "s_waitcnt vmcnt(11) lgkmcnt(4)\n"
      "v_fmac_f32_e64 v219, v187, v187\n"
      "v_cmp_ne_u32 vcc, s21, v28\n"
      "v_mfma_f32_16x16x32_bf16 v[172:175], a[52:55], v[152:155], v[172:175]\n"
      "ds_write_b32 v18, v219 offset:4\n"
      "v_mfma_f32_16x16x32_bf16 v[172:175], a[56:59], v[156:159], v[172:175]\n"
      "s_waitcnt lgkmcnt(3)\n"
      "s_add_u32 m0, s39, 0\n"
      "v_mfma_f32_16x16x32_bf16 v[172:175], a[60:63], v[160:163], v[172:175]\n"
      "ds_write_b32 v23, v21\n"
      "v_mfma_f32_16x16x32_bf16 v[172:175], a[64:67], v[164:167], v[172:175]\n"
      "s_waitcnt lgkmcnt(3)\n"
      "s_cbranch_vccnz .Lsp_w0_10\n"
      ".Lbk_w0_10:\n"
      "v_mfma_f32_16x16x32_bf16 v[172:175], a[68:71], v[168:171], v[172:175]\n"
      "v_mfma_f32_16x16x32_bf16 v[176:179], a[72:75], v[100:103], v[176:179]\n"
      "buffer_load_dword v28, v11, s[8:11], s36 offen sc1\n"
      "v_mfma_f32_16x16x32_bf16 v[176:179], a[76:79], v[104:107], v[176:179]\n"
      "s_waitcnt vmcnt(7) lgkmcnt(2)\n"
      "v_lshlrev_b32 v186, 16, v199\n"
      "v_lshlrev_b32 v184, 16, v198\n"
      "v_mfma_f32_16x16x32_bf16 v[176:179], a[80:83], v[108:111], v[176:179]\n"
      "v_lshlrev_b32 v180, 16, v196\n"
      "v_and_b32 v183, 0xffff0000, v197\n"
      "v_mfma_f32_16x16x32_bf16 v[176:179], a[84:87], v[112:115], v[176:179]\n"
      "buffer_store_dword v25, v12, s[8:11], s50 offen sc1\n"
      "v_mfma_f32_16x16x32_bf16 v[176:179], a[88:91], v[116:119], v[176:179]\n"
      "v_lshlrev_b32 v182, 16, v197\n"
      "v_and_b32 v181, 0xffff0000, v196\n"
      "v_mfma_f32_16x16x32_bf16 v[176:179], a[92:95], v[120:123], v[176:179]\n"
      "v_and_b32 v187, 0xffff0000, v199\n"
      "v_and_b32 v185, 0xffff0000, v198\n"
      "v_mfma_f32_16x16x32_bf16 v[176:179], a[96:99], v[124:127], v[176:179]\n"
      "buffer_load_dwordx4 v[196:199], v13, s[8:11], s47 offen offset:1024 sc1\n"
      "v_mfma_f32_16x16x32_bf16 v[176:179], a[100:103], v[128:131], v[176:179]\n"
      "ds_read_b32 v22, v24\n"
      "v_mul_f32 v219, v172, v172\n"
      "v_mfma_f32_16x16x32_bf16 v[176:179], a[104:107], v[132:135], v[176:179]\n"
      "v_mfma_f32_16x16x32_bf16 v[176:179], a[108:111], v[136:139], v[176:179]\n"
      "v_mfma_f32_16x16x32_bf16 v[176:179], a[112:115], v[140:143], v[176:179]\n"
      "buffer_load_dwordx4 v0, s[8:11], s46 offen offset:0 sc1 lds\n"
      "v_mfma_f32_16x16x32_bf16 v[176:179], a[116:119], v[144:147], v[176:179]\n"
      "v_add_f32 v218, v172, v173\n"
      "v_fmac_f32 v219, v173, v173\n"
      "v_mfma_f32_16x16x32_bf16 v[176:179], a[120:123], v[148:151], v[176:179]\n"
      "s_waitcnt lgkmcnt(0)\n"
      "v_cmp_gt_i32 vcc, v21, v22\n"
      "v_mfma_f32_16x16x32_bf16 v[176:179], a[124:127], v[152:155], v[176:179]\n"
      "v_mfma_f32_16x16x32_bf16 v[176:179], a[128:131], v[156:159], v[176:179]\n"
      "buffer_load_dwordx4 v0, s[8:11], s46 offen offset:1024 sc1 lds\n"
      "v_mfma_f32_16x16x32_bf16 v[176:179], a[132:135], v[160:163], v[176:179]\n"
      "s_cbranch_vccnz .Lsp_wz0_11\n"
      ".Lbk_wz0_11:\n"
      "ds_read2st64_b32 v[204:205], v17 offset1:1\n"
      "v_mfma_f32_16x16x32_bf16 v[176:179], a[136:139], v[164:167], v[176:179]\n"
      "ds_read2st64_b32 v[206:207], v17 offset0:2 offset1:3\n"
      "v_add_u32 v21, 1, v21\n"
      "v_mfma_f32_16x16x32_bf16 v[176:179], a[140:143], v[168:171], v[176:179]\n"
      "s_waitcnt vmcnt(10)\n"
      "ds_write_b32 v23, v21\n"
      "v_mfma_f32_16x16x32_bf16 v[180:183], a[144:147], v[100:103], v[180:183]\n"
      "buffer_load_dwordx4 v0, s[8:11], s46 offen offset:3072 sc1 lds\n"
      "v_mfma_f32_16x16x32_bf16 v[180:183], a[148:151], v[104:107], v[180:183]\n"
      "v_add_f32 v218, v218, v174\n"
      "v_fmac_f32 v219, v174, v174\n"
      "v_mfma_f32_16x16x32_bf16 v[180:183], a[152:155], v[108:111], v[180:183]\n"
      "v_fmac_f32 v219, v175, v175\n"
      "v_fmac_f32 v219, v176, v176\n"
      "v_mfma_f32_16x16x32_bf16 v[180:183], a[156:159], v[112:115], v[180:183]\n"
      "v_add_f32 v218, v218, v175\n"
      "v_add_f32 v218, v218, v176\n"
      "v_mfma_f32_16x16x32_bf16 v[180:183], a[160:163], v[116:119], v[180:183]\n"
      "buffer_load_dwordx4 v0, s[8:11], s46 offen offset:2048 sc1 lds\n"
      "v_mfma_f32_16x16x32_bf16 v[180:183], a[164:167], v[120:123], v[180:183]\n"
      "s_add_u32 m0, s40, 0\n"
      "v_add_f32 v218, v218, v177\n"
      "v_cvt_pk_bf16_f32 v209, v174, v175\n"
      "v_mfma_f32_16x16x32_bf16 v[180:183], a[168:171], v[124:127], v[180:183]\n"
      "s_waitcnt lgkmcnt(0)\n"
      "v_add_f32 v188, v188, v204\n"
      "v_add_f32 v190, v190, v206\n"
      "v_mfma_f32_16x16x32_bf16 v[180:183], a[172:175], v[128:131], v[180:183]\n"
      "v_cvt_pk_bf16_f32 v208, v172, v173\n"
      "v_fmac_f32 v219, v177, v177\n"
      "v_mfma_f32_16x16x32_bf16 v[180:183], a[176:179], v[132:135], v[180:183]\n"
      "buffer_load_dwordx4 v1, s[8:11], s46 offen offset:0 sc1 lds\n"
      "v_mfma_f32_16x16x32_bf16 v[180:183], a[180:183], v[136:139], v[180:183]\n"
      "s_add_u32 s38, s38, 24576\n"
      "v_cvt_pk_bf16_f32 v210, v176, v177\n"
      "v_mfma_f32_16x16x32_bf16 v[180:183], a[184:187], v[140:143], v[180:183]\n"
      "ds_read_b32 v22, v24\n"
      "v_add_f32 v218, v218, v178\n"
      "v_mfma_f32_16x16x32_bf16 v[180:183], a[188:191], v[144:147], v[180:183]\n"
      "v_add_f32 v218, v218, v179\n"
      "v_add_f32 v218, v218, v188\n"
      "v_mfma_f32_16x16x32_bf16 v[180:183], a[192:195], v[148:151], v[180:183]\n"
      "s_add_u32 s50, s50, 24576\n"
      "v_fmac_f32 v219, v178, v178\n"
      "v_fmac_f32 v219, v179, v179\n"
      "v_mfma_f32_16x16x32_bf16 v[180:183], a[196:199], v[152:155], v[180:183]\n"
      "v_add_f32 v191, v191, v207\n"
      "v_cvt_pk_bf16_f32 v211, v178, v179\n"
      "v_mfma_f32_16x16x32_bf16 v[180:183], a[200:203], v[156:159], v[180:183]\n"
      "v_fmac_f32 v219, v188, v188\n"
      "v_add_f32 v189, v189, v205\n"
      "v_mfma_f32_16x16x32_bf16 v[180:183], a[204:207], v[160:163], v[180:183]\n"
      "s_waitcnt lgkmcnt(0)\n"
      "v_fmac_f32 v219, v189, v189\n"
      "v_cmp_gt_i32 vcc, v21, v22\n"
      "v_mfma_f32_16x16x32_bf16 v[180:183], a[208:211], v[164:167], v[180:183]\n"
      ".Lw_p1:\n"
      "s_cbranch_execz .Lw_pr\n"
      "v_fmac_f32 v219, v190, v190\n"
      "v_add_f32 v218, v218, v189\n"
      "v_mfma_f32_16x16x32_bf16 v[180:183], a[212:215], v[168:171], v[180:183]\n"
      "s_waitcnt vmcnt(11)\n"
      "v_lshlrev_b32 v172, 16, v192\n"
      "v_mfma_f32_16x16x32_bf16 v[184:187], a[216:219], v[100:103], v[184:187]\n"
      "v_and_b32 v175, 0xffff0000, v193\n"
      "v_and_b32 v177, 0xffff0000, v194\n"
      "v_mfma_f32_16x16x32_bf16 v[184:187], a[220:223], v[104:107], v[184:187]\n"
      "s_cbranch_vccnz .Lsp_wy1_12\n"
      ".Lbk_wy1_12:\n"
      "v_add_f32 v218, v218, v190\n"
      "v_mfma_f32_16x16x32_bf16 v[184:187], a[224:227], v[108:111], v[184:187]\n"
      "ds_read_b128 v[104:107], v10 offset:21504\n"
      "v_cvt_pk_bf16_f32 v216, v188, v189\n"
      "v_mfma_f32_16x16x32_bf16 v[184:187], a[228:231], v[112:115], v[184:187]\n"
      "v_add_f32 v218, v218, v191\n"
      "v_add_f32 v218, v218, v180\n"
      "v_mfma_f32_16x16x32_bf16 v[184:187], a[232:235], v[116:119], v[184:187]\n"
      "ds_read_b128 v[116:119], v10 offset:24576\n"
      "v_cvt_pk_bf16_f32 v217, v190, v191\n"
      "v_mfma_f32_16x16x32_bf16 v[184:187], a[236:239], v[120:123], v[184:187]\n"
      "ds_read_b128 v[108:111], v10 offset:22528\n"
      "v_lshlrev_b32 v188, 16, v200\n"
      "v_mfma_f32_16x16x32_bf16 v[184:187], a[240:243], v[124:127], v[184:187]\n"
      "ds_read_b128 v[100:103], v10 offset:20480\n"
      "v_fmac_f32 v219, v191, v191\n"
      "v_mfma_f32_16x16x32_bf16 v[184:187], a[244:247], v[128:131], v[184:187]\n"
      "v_lshlrev_b32 v190, 16, v201\n"
      "v_and_b32 v191, 0xffff0000, v201\n"
      "v_mfma_f32_16x16x32_bf16 v[184:187], a[248:251], v[132:135], v[184:187]\n"
      "ds_read2_b32 v[134:135], v8 offset0:64 offset1:96\n"
      "v_lshlrev_b32 v174, 16, v193\n"
      "v_mfma_f32_16x16x32_bf16 v[184:187], a[252:255], v[136:139], v[184:187]\n"
      "s_sub_u32 s47, s46, s45\n"
      "v_and_b32 v189, 0xffff0000, v200\n"
      "v_fmac_f32 v219, v180, v180\n"
      "v_mfma_f32_16x16x32_bf16 v[184:187], v[32:35], v[140:143], v[184:187]\n"
      "buffer_store_dwordx4 v[208:211], v15, s[8:11], s38 offen sc1\n"
      "v_mfma_f32_16x16x32_bf16 v[184:187], v[36:39], v[144:147], v[184:187]\n"
      "ds_read_b128 v[136:139], v10 offset:28672\n"
      "v_fmac_f32 v219, v181, v181\n"
      "v_mfma_f32_16x16x32_bf16 v[184:187], v[40:43], v[148:151], v[184:187]\n"
      "ds_read2_b32 v[132:133], v8 offset0:0 offset1:32\n"
      "v_lshlrev_b32 v178, 16, v195\n"
      "v_mfma_f32_16x16x32_bf16 v[184:187], v[44:47], v[152:155], v[184:187]\n"
      "ds_read_b128 v[112:115], v10 offset:23552\n"
      "v_add_f32 v218, v218, v181\n"
      "v_mfma_f32_16x16x32_bf16 v[184:187], v[48:51], v[156:159], v[184:187]\n"
      "ds_read_b128 v[120:123], v10 offset:25600\n"
      "v_and_b32 v173, 0xffff0000, v192\n"
      "v_mfma_f32_16x16x32_bf16 v[184:187], v[52:55], v[160:163], v[184:187]\n"
      "ds_read_b128 v[124:127], v10 offset:26624\n"
      "v_lshlrev_b32 v176, 16, v194\n"
      "v_mfma_f32_16x16x32_bf16 v[184:187], v[56:59], v[164:167], v[184:187]\n"
      "buffer_load_dwordx2 v[200:201], v14, s[8:11], s47 offen sc1\n"
      "v_mfma_f32_16x16x32_bf16 v[184:187], v[60:63], v[168:171], v[184:187]\n"
      "s_waitcnt lgkmcnt(5)\n"
      "ds_read_b128 v[128:131], v10 offset:27648\n"
      "v_add_f32 v218, v218, v182\n"
      "v_mfma_f32_16x16x32_bf16 v[188:191], v[64:67], v[100:103], v[188:191]\n"
      "ds_read_b128 v[140:143], v10 offset:29696\n"
      "v_cvt_pk_bf16_f32 v213, v182, v183\n"
      "v_mfma_f32_16x16x32_bf16 v[188:191], v[68:71], v[104:107], v[188:191]\n"
      "ds_read_b128 v[152:155], v10 offset:32768\n"
      "v_mfma_f32_16x16x32_bf16 v[188:191], v[72:75], v[108:111], v[188:191]\n"
      "s_waitcnt lgkmcnt(5)\n"
      "buffer_store_dwordx2 v[216:217], v16, s[8:11], s38 offen sc1\n"
      "v_mfma_f32_16x16x32_bf16 v[188:191], v[76:79], v[112:115], v[188:191]\n"
      "ds_read_b128 v[144:147], v10 offset:30720\n"
      "v_cvt_pk_bf16_f32 v212, v180, v181\n"
      "v_mfma_f32_16x16x32_bf16 v[188:191], v[80:83], v[116:119], v[188:191]\n"
      "s_waitcnt lgkmcnt(4)\n"
      "ds_read_b128 v[164:167], v10 offset:35840\n"
      "v_add_f32 v218, v218, v183\n"
      "v_mfma_f32_16x16x32_bf16 v[188:191], v[84:87], v[120:123], v[188:191]\n"
      "v_fmac_f32 v219, v182, v182\n"
      "v_add_f32 v218, v218, v184\n"
      "v_mfma_f32_16x16x32_bf16 v[188:191], v[88:91], v[124:127], v[188:191]\n"
      "s_waitcnt lgkmcnt(4)\n"
      "ds_read_b128 v[148:151], v10 offset:31744\n"
      "v_add_f32 v218, v218, v185\n"
      "v_mfma_f32_16x16x32_bf16 v[188:191], v[92:95], v[128:131], v[188:191]\n"
      "ds_read_b128 v[160:163], v10 offset:34816\n"
      "v_and_b32 v179, 0xffff0000, v195\n"
      "v_mfma_f32_16x16x32_bf16 v[188:191], v[96:99], v[132:135], v[188:191]\n"
      "buffer_load_dwordx4 v[192:195], v13, s[8:11], s47 offen sc1\n"
      "v_mfma_f32_16x16x32_bf16 v[172:175], a[0:3], v[100:103], v[172:175]\n"
      "v_cvt_pk_bf16_f32 v215, v186, v187\n"
      "v_fmac_f32 v219, v183, v183\n"
      "v_mfma_f32_16x16x32_bf16 v[172:175], a[4:7], v[104:107], v[172:175]\n"
      "v_cvt_pk_bf16_f32 v214, v184, v185\n"
      "v_mfma_f32_16x16x32_bf16 v[172:175], a[8:11], v[108:111], v[172:175]\n"
      "ds_read2_b32 v[168:169], v9 offset0:0 offset1:32\n"
      "v_mfma_f32_16x16x32_bf16 v[172:175], a[12:15], v[112:115], v[172:175]\n"
      "ds_read_b128 v[156:159], v10 offset:33792\n"
      "v_mfma_f32_16x16x32_bf16 v[172:175], a[16:19], v[116:119], v[172:175]\n"
      "buffer_store_dwordx4 v[212:215], v15, s[8:11], s38 offen offset:1024 sc1\n"
      "v_mfma_f32_16x16x32_bf16 v[172:175], a[20:23], v[120:123], v[172:175]\n"
      "v_fmac_f32 v219, v184, v184\n"
      "v_fmac_f32 v219, v185, v185\n"
      "v_mfma_f32_16x16x32_bf16 v[172:175], a[24:27], v[124:127], v[172:175]\n"
      "ds_read2_b32 v[170:171], v9 offset0:64 offset1:96\n"
      "v_add_f32 v218, v218, v186\n"
      "v_mfma_f32_16x16x32_bf16 v[172:175], a[28:31], v[128:131], v[172:175]\n"
      "v_fmac_f32 v219, v186, v186\n"
      "v_add_f32 v218, v218, v187\n"
      "v_mfma_f32_16x16x32_bf16 v[172:175], a[32:35], v[132:135], v[172:175]\n"
      "ds_write_b32 v18, v218\n"
      "v_mfma_f32_16x16x32_bf16 v[172:175], a[36:39], v[136:139], v[172:175]\n"
      "s_waitcnt lgkmcnt(3)\n"
      "s_add_u32 m0, s39, 20480\n"
      "v_add_u32 v21, 1, v21\n"
      "v_mfma_f32_16x16x32_bf16 v[172:175], a[40:43], v[140:143], v[172:175]\n"
      "s_mov_b32 s46, s36\n"
      "v_mfma_f32_16x16x32_bf16 v[172:175], a[44:47], v[144:147], v[172:175]\n"
      "v_fmac_f32 v219, v187, v187\n"
      "v_mfma_f32_16x16x32_bf16 v[172:175], a[48:51], v[148:151], v[172:175]\n"
      "ds_write_b32 v18, v219 offset:4\n"
      "v_mfma_f32_16x16x32_bf16 v[172:175], a[52:55], v[152:155], v[172:175]\n"
      "s_waitcnt vmcnt(10) lgkmcnt(2)\n"
      "v_and_b32 v181, 0xffff0000, v196\n"
      "v_mfma_f32_16x16x32_bf16 v[172:175], a[56:59], v[156:159], v[172:175]\n"
      "ds_write_b32 v23, v21\n"
      "v_mfma_f32_16x16x32_bf16 v[172:175], a[60:63], v[160:163], v[172:175]\n"
      "v_lshlrev_b32 v186, 16, v199\n"
      "v_cmp_ne_u32 vcc, s21, v28\n"
      "v_mfma_f32_16x16x32_bf16 v[172:175], a[64:67], v[164:167], v[172:175]\n"
      "buffer_store_dword v25, v12, s[8:11], s50 offen sc1\n"
      "v_mfma_f32_16x16x32_bf16 v[172:175], a[68:71], v[168:171], v[172:175]\n"
      "s_cmp_eq_u32 s36, s37\n"
      "s_cselect_b32 s48, 49152, s51\n"
      "s_add_u32 s36, s36, s48\n"
      "v_mfma_f32_16x16x32_bf16 v[176:179], a[72:75], v[100:103], v[176:179]\n"
      "s_cbranch_vccnz .Lsp_w1_13\n"
      ".Lbk_w1_13:\n"
      "s_min_u32 s36, s36, s6\n"
      "v_mfma_f32_16x16x32_bf16 v[176:179], a[76:79], v[104:107], v[176:179]\n"
      "buffer_load_dword v28, v11, s[8:11], s36 offen sc1\n"
      "v_mfma_f32_16x16x32_bf16 v[176:179], a[80:83], v[108:111], v[176:179]\n"
      "v_and_b32 v183, 0xffff0000, v197\n"
      "v_mul_f32 v219, v172, v172\n"
      "v_mfma_f32_16x16x32_bf16 v[176:179], a[84:87], v[112:115], v[176:179]\n"
      "buffer_load_dwordx4 v0, s[8:11], s46 offen offset:0 sc1 lds\n"
      "v_mfma_f32_16x16x32_bf16 v[176:179], a[88:91], v[116:119], v[176:179]\n"
      "v_mfma_f32_16x16x32_bf16 v[176:179], a[92:95], v[120:123], v[176:179]\n"
      "v_fmac_f32 v219, v173, v173\n"
      "v_mfma_f32_16x16x32_bf16 v[176:179], a[96:99], v[124:127], v[176:179]\n"
      "v_lshlrev_b32 v184, 16, v198\n"
      "v_and_b32 v185, 0xffff0000, v198\n"
      "v_mfma_f32_16x16x32_bf16 v[176:179], a[100:103], v[128:131], v[176:179]\n"
      "ds_read_b32 v22, v24\n"
      "v_fmac_f32 v219, v174, v174\n"
      "v_mfma_f32_16x16x32_bf16 v[176:179], a[104:107], v[132:135], v[176:179]\n"
      "buffer_load_dwordx4 v0, s[8:11], s46 offen offset:1024 sc1 lds\n"
      "v_mfma_f32_16x16x32_bf16 v[176:179], a[108:111], v[136:139], v[176:179]\n"
      "v_lshlrev_b32 v182, 16, v197\n"
      "v_add_f32 v218, v172, v173\n"
      "v_mfma_f32_16x16x32_bf16 v[176:179], a[112:115], v[140:143], v[176:179]\n"
      "v_add_f32 v218, v218, v174\n"
      "v_fmac_f32 v219, v175, v175\n"
      "v_mfma_f32_16x16x32_bf16 v[176:179], a[116:119], v[144:147], v[176:179]\n"
      "v_cvt_pk_bf16_f32 v208, v172, v173\n"
      "v_mfma_f32_16x16x32_bf16 v[176:179], a[120:123], v[148:151], v[176:179]\n"
      "s_waitcnt lgkmcnt(0)\n"
      "v_and_b32 v187, 0xffff0000, v199\n"
      "v_cmp_gt_i32 vcc, v21, v22\n"
      "v_mfma_f32_16x16x32_bf16 v[176:179], a[124:127], v[152:155], v[176:179]\n"
      "buffer_load_dwordx4 v0, s[8:11], s46 offen offset:3072 sc1 lds\n"
      "v_mfma_f32_16x16x32_bf16 v[176:179], a[128:131], v[156:159], v[176:179]\n"
      "v_lshlrev_b32 v180, 16, v196\n"
      "v_add_f32 v218, v218, v175\n"
      "v_mfma_f32_16x16x32_bf16 v[176:179], a[132:135], v[160:163], v[176:179]\n"
      "s_cbranch_vccnz .Lsp_wz1_14\n"
      ".Lbk_wz1_14:\n"
      "ds_read2st64_b32 v[204:205], v17 offset1:1\n"
      "v_mfma_f32_16x16x32_bf16 v[176:179], a[136:139], v[164:167], v[176:179]\n"
      "ds_read2st64_b32 v[206:207], v17 offset0:2 offset1:3\n"
      "v_add_u32 v21, 1, v21\n"
      "v_mfma_f32_16x16x32_bf16 v[176:179], a[140:143], v[168:171], v[176:179]\n"
      "s_waitcnt vmcnt(10)\n"
      "ds_write_b32 v23, v21\n"
      "v_mfma_f32_16x16x32_bf16 v[180:183], a[144:147], v[100:103], v[180:183]\n"
      "buffer_load_dwordx4 v0, s[8:11], s46 offen offset:2048 sc1 lds\n"
      "v_mfma_f32_16x16x32_bf16 v[180:183], a[148:151], v[104:107], v[180:183]\n"
      "v_mfma_f32_16x16x32_bf16 v[180:183], a[152:155], v[108:111], v[180:183]\n"
      "s_add_u32 m0, s40, 20480\n"
      "v_add_f32 v218, v218, v176\n"
      "v_mfma_f32_16x16x32_bf16 v[180:183], a[156:159], v[112:115], v[180:183]\n"
      "v_fmac_f32 v219, v176, v176\n"
      "v_cvt_pk_bf16_f32 v209, v174, v175\n"
      "v_mfma_f32_16x16x32_bf16 v[180:183], a[160:163], v[116:119], v[180:183]\n"
      "buffer_load_dwordx4 v[196:199], v13, s[8:11], s47 offen offset:1024 sc1\n"
      "v_mfma_f32_16x16x32_bf16 v[180:183], a[164:167], v[120:123], v[180:183]\n"
      "s_waitcnt lgkmcnt(1)\n"
      "v_cvt_pk_bf16_f32 v211, v178, v179\n"
      "v_add_f32 v190, v190, v206\n"
      "v_mfma_f32_16x16x32_bf16 v[180:183], a[168:171], v[124:127], v[180:183]\n"
      "v_add_f32 v218, v218, v177\n"
      "v_add_f32 v218, v218, v178\n"
      "v_mfma_f32_16x16x32_bf16 v[180:183], a[172:175], v[128:131], v[180:183]\n"
      "v_add_f32 v218, v218, v179\n"
      "v_add_f32 v189, v189, v205\n"
      "v_mfma_f32_16x16x32_bf16 v[180:183], a[176:179], v[132:135], v[180:183]\n"
      "buffer_load_dwordx4 v1, s[8:11], s46 offen offset:0 sc1 lds\n"
      "v_mfma_f32_16x16x32_bf16 v[180:183], a[180:183], v[136:139], v[180:183]\n"
      "v_add_f32 v188, v188, v204\n"
      "v_fmac_f32 v219, v177, v177\n"
      "v_mfma_f32_16x16x32_bf16 v[180:183], a[184:187], v[140:143], v[180:183]\n"
      "ds_read_b32 v22, v24\n"
      "v_fmac_f32 v219, v178, v178\n"
      "v_mfma_f32_16x16x32_bf16 v[180:183], a[188:191], v[144:147], v[180:183]\n"
      "v_cvt_pk_bf16_f32 v210, v176, v177\n"
      "v_fmac_f32 v219, v179, v179\n"
      "v_mfma_f32_16x16x32_bf16 v[180:183], a[192:195], v[148:151], v[180:183]\n"
      "v_add_f32 v191, v191, v207\n"
      "v_fmac_f32 v219, v188, v188\n"
      "v_mfma_f32_16x16x32_bf16 v[180:183], a[196:199], v[152:155], v[180:183]\n"
      "v_add_f32 v218, v218, v188\n"
      "v_mfma_f32_16x16x32_bf16 v[180:183], a[200:203], v[156:159], v[180:183]\n"
      "v_add_f32 v218, v218, v189\n"
      "v_add_f32 v218, v218, v190\n"
      "v_mfma_f32_16x16x32_bf16 v[180:183], a[204:207], v[160:163], v[180:183]\n"
      "s_waitcnt lgkmcnt(0)\n"
      "v_fmac_f32 v219, v189, v189\n"
      "v_cmp_gt_i32 vcc, v21, v22\n"
      "v_mfma_f32_16x16x32_bf16 v[180:183], a[208:211], v[164:167], v[180:183]\n"
      "v_cvt_pk_bf16_f32 v216, v188, v189\n"
      "v_fmac_f32 v219, v190, v190\n"
      "v_mfma_f32_16x16x32_bf16 v[180:183], a[212:215], v[168:171], v[180:183]\n"
      "s_waitcnt vmcnt(11)\n"
      "v_and_b32 v189, 0xffff0000, v200\n"
      "v_fmac_f32 v219, v191, v191\n"
      "v_mfma_f32_16x16x32_bf16 v[184:187], a[216:219], v[100:103], v[184:187]\n"
      "s_add_u32 s38, s38, 24576\n"
      "v_cvt_pk_bf16_f32 v217, v190, v191\n"
      "v_lshlrev_b32_e64 v188, 16, v200\n"
      "v_mfma_f32_16x16x32_bf16 v[184:187], a[220:223], v[104:107], v[184:187]\n"
      "s_cbranch_vccnz .Lsp_wy0_15\n"
      ".Lbk_wy0_15:\n"
      "v_mfma_f32_16x16x32_bf16 v[184:187], a[224:227], v[108:111], v[184:187]\n"
      "ds_read_b128 v[104:107], v0 offset:1024\n"
      "v_add_f32 v218, v218, v191\n"
      "v_mfma_f32_16x16x32_bf16 v[184:187], a[228:231], v[112:115], v[184:187]\n"
      "v_fmac_f32 v219, v180, v180\n"
      "v_lshlrev_b32 v190, 16, v201\n"
      "v_mfma_f32_16x16x32_bf16 v[184:187], a[232:235], v[116:119], v[184:187]\n"
      "ds_read_b128 v[100:103], v0 offset:0\n"
      "v_add_f32 v218, v218, v180\n"
      "v_mfma_f32_16x16x32_bf16 v[184:187], a[236:239], v[120:123], v[184:187]\n"
      "s_waitcnt vmcnt(9)\n"
      "ds_read_b128 v[120:123], v0 offset:5120\n"
      "v_lshlrev_b32 v178, 16, v195\n"
      "v_mfma_f32_16x16x32_bf16 v[184:187], a[240:243], v[124:127], v[184:187]\n"
      "ds_read_b128 v[112:115], v0 offset:3072\n"
      "v_fmac_f32 v219, v181, v181\n"
      "v_mfma_f32_16x16x32_bf16 v[184:187], a[244:247], v[128:131], v[184:187]\n"
      "ds_read_b128 v[116:119], v0 offset:4096\n"
      "v_lshlrev_b32 v174, 16, v193\n"
      "v_mfma_f32_16x16x32_bf16 v[184:187], a[248:251], v[132:135], v[184:187]\n"
      "ds_read_b128 v[124:127], v0 offset:6144\n"
      "v_fmac_f32 v219, v182, v182\n"
      "v_mfma_f32_16x16x32_bf16 v[184:187], a[252:255], v[136:139], v[184:187]\n"
      "s_sub_u32 s47, s46, s45\n"
      "ds_read_b128 v[108:111], v0 offset:2048\n"
      "v_mfma_f32_16x16x32_bf16 v[184:187], v[32:35], v[140:143], v[184:187]\n"
      "v_and_b32 v191, 0xffff0000, v201\n"
      "v_and_b32 v177, 0xffff0000, v194\n"
      "v_mfma_f32_16x16x32_bf16 v[184:187], v[36:39], v[144:147], v[184:187]\n"
      "ds_read_b128 v[136:139], v0 offset:8192\n"
      "v_lshlrev_b32 v176, 16, v194\n"
      "v_mfma_f32_16x16x32_bf16 v[184:187], v[40:43], v[148:151], v[184:187]\n"
      "ds_read_b128 v[128:131], v0 offset:7168\n"
      "v_lshlrev_b32 v172, 16, v192\n"
      "v_mfma_f32_16x16x32_bf16 v[184:187], v[44:47], v[152:155], v[184:187]\n"
      "v_fmac_f32 v219, v183, v183\n"
      "v_and_b32 v175, 0xffff0000, v193\n"
      "v_mfma_f32_16x16x32_bf16 v[184:187], v[48:51], v[156:159], v[184:187]\n"
      "buffer_load_dwordx2 v[200:201], v14, s[8:11], s47 offen sc1\n"
      "v_mfma_f32_16x16x32_bf16 v[184:187], v[52:55], v[160:163], v[184:187]\n"
      "ds_read2_b32 v[132:133], v2 offset0:0 offset1:32\n"
      "v_and_b32 v173, 0xffff0000, v192\n"
      "v_mfma_f32_16x16x32_bf16 v[184:187], v[56:59], v[164:167], v[184:187]\n"
      "s_waitcnt lgkmcnt(3)\n"
      "ds_read2_b32 v[134:135], v2 offset0:64 offset1:96\n"
      "v_add_f32 v218, v218, v181\n"
      "v_mfma_f32_16x16x32_bf16 v[184:187], v[60:63], v[168:171], v[184:187]\n"
      "s_sub_u32 s20, s20, 1\n"
      "s_cbranch_scc0 .Lw_loop\n"
      ".Lw_tail:\n"
      "v_add_f32 v218, v218, v182\n"
      "s_nop 7\n"
      "v_fmac_f32 v219, v184, v184\n"
      "v_cvt_pk_bf16_f32 v213, v182, v183\n"
      "v_cvt_pk_bf16_f32 v215, v186, v187\n"
      "v_add_f32 v218, v218, v183\n"
      "buffer_store_dwordx4 v[208:211], v15, s[8:11], s38 offen sc1\n"
      "v_cvt_pk_bf16_f32 v212, v180, v181\n"
      "v_add_f32 v218, v218, v184\n"
      "s_add_u32 s50, s50, 24576\n"
      "v_cvt_pk_bf16_f32 v214, v184, v185\n"
      "v_add_f32 v218, v218, v185\n"
      "v_add_f32 v218, v218, v186\n"
      "v_add_f32 v218, v218, v187\n"
      "v_fmac_f32 v219, v185, v185\n"
      "v_fmac_f32 v219, v186, v186\n"
      "buffer_store_dwordx2 v[216:217], v16, s[8:11], s38 offen sc1\n"
      "buffer_store_dwordx4 v[212:215], v15, s[8:11], s38 offen offset:1024 sc1\n"
      "ds_write_b32 v18, v218\n"
      "v_fmac_f32 v219, v187, v187\n"
      "ds_write_b32 v18, v219 offset:4\n"
      "s_waitcnt lgkmcnt(0)\n"
      "s_barrier\n"
      "s_waitcnt vmcnt(12)\n"
      "buffer_store_dword v25, v12, s[8:11], s50 offen sc1\n"
      "s_add_u32 s38, s38, 24576\n"
      "s_add_u32 s50, s50, 24576\n"
      "s_waitcnt vmcnt(1)\n"
      "buffer_store_dword v25, v12, s[8:11], s50 offen sc1\n"
      "s_add_u32 s50, s50, 24576\n"
      "s_endpgm\n"
      ".Lv_start:\n"
      "s_mov_b64 exec, 0\n"
      "s_lshr_b32 s22, s39, 13\n"
      ".Lv_pl:\n"
      "s_cmp_eq_u32 s22, 0\n"
      "s_cbranch_scc1 .Lv_p0\n"
      "s_cmp_eq_u32 s22, 1\n"
      "s_cbranch_scc1 .Lv_p1\n"
      "s_branch .Lv_pe\n"
      ".Lv_pr:\n"
      "s_add_u32 s22, s22, 2\n"
      "s_branch .Lv_pl\n"
      ".Lv_pe:\n"
      "s_mov_b64 exec, -1\n"
      "s_add_u32 s50, s38, 4294893568\n"
      "s_add_u32 s49, s38, 4294918144\n"
      "s_add_u32 s38, s38, 4294942720\n"
      "s_cmp_eq_u32 s36, s37\n"
      "s_cselect_b32 s48, 49152, s51\n"
      "s_add_u32 s36, s36, s48\n"
      "s_min_u32 s36, s36, s6\n"
      "buffer_load_dword v28, v11, s[8:11], s36 offen sc1\n"
      "s_mov_b32 s46, s36\n"
      "s_add_u32 s36, s36, 24576\n"
      "s_min_u32 s36, s36, s6\n"
      "s_add_u32 m0, s39, 0\n"
      "s_waitcnt vmcnt(0)\n"
      "v_cmp_ne_u32 vcc, s21, v28\n"
      "s_cbranch_vccnz .Lsp_v0_16\n"
      ".Lbk_v0_16:\n"
      "buffer_load_dword v28, v11, s[8:11], s36 offen sc1\n"
      "buffer_load_dwordx4 v0, s[8:11], s46 offen offset:3072 sc1 lds\n"
      "buffer_load_dwordx4 v0, s[8:11], s46 offen offset:0 sc1 lds\n"
      "buffer_load_dwordx4 v0, s[8:11], s46 offen offset:1024 sc1 lds\n"
      "buffer_load_dwordx4 v0, s[8:11], s46 offen offset:2048 sc1 lds\n"
      "s_add_u32 m0, s40, 0\n"
      "s_nop 0\n"
      "buffer_load_dwordx4 v1, s[8:11], s46 offen offset:0 sc1 lds\n"
      "s_sub_u32 s47, s46, s45\n"
      "buffer_load_dwordx4 v[192:195], v13, s[8:11], s47 offen sc1\n"
      "s_mov_b32 s46, s36\n"
      "s_cmp_eq_u32 s36, s37\n"
      "s_cselect_b32 s48, 49152, s51\n"
      "buffer_load_dword v26, v23, s[8:11], s31 offen\n"
      "s_add_u32 s36, s36, s48\n"
      "s_waitcnt vmcnt(7)\n"
      "v_cmp_ne_u32 vcc, s21, v28\n"
      "s_min_u32 s36, s36, s6\n"
      "s_cbranch_vccnz .Lsp_v1_17\n"
      ".Lbk_v1_17:\n"
      "buffer_load_dword v28, v11, s[8:11], s36 offen sc1\n"
      "s_add_u32 m0, s39, 20480\n"
      "s_nop 0\n"
      "buffer_load_dwordx4 v0, s[8:11], s46 offen offset:0 sc1 lds\n"
      "buffer_load_dwordx4 v0, s[8:11], s46 offen offset:1024 sc1 lds\n"
      "buffer_load_dwordx4 v0, s[8:11], s46 offen offset:2048 sc1 lds\n"
      "buffer_load_dwordx4 v[196:199], v13, s[8:11], s47 offen offset:1024 sc1\n"
      "v_add_u32 v21, 1, v21\n"
      "s_waitcnt vmcnt(7)\n"
      "ds_write_b32 v23, v21\n"
      "buffer_load_dwordx4 v0, s[8:11], s46 offen offset:3072 sc1 lds\n"
      "s_add_u32 m0, s40, 20480\n"
      "s_nop 0\n"
      "buffer_load_dwordx4 v1, s[8:11], s46 offen offset:0 sc1 lds\n"
      "ds_read_b32 v22, v24\n"
      "buffer_load_dword v26, v23, s[8:11], s31 offen\n"
      "s_waitcnt lgkmcnt(0)\n"
      "v_cmp_gt_i32 vcc, v21, v22\n"
      "buffer_load_dword v26, v23, s[8:11], s31 offen\n"
      "s_cbranch_vccnz .Lsp_vy0_18\n"
      ".Lbk_vy0_18:\n"
      "s_sub_u32 s47, s46, s45\n"
      "s_waitcnt vmcnt(10)\n"
      "v_lshlrev_b32 v174, 16, v193\n"
      "v_and_b32 v179, 0xffff0000, v195\n"
      "ds_read_b128 v[100:103], v0 offset:8192\n"
      "ds_read_b128 v[108:111], v0 offset:10240\n"
      "v_and_b32 v173, 0xffff0000, v192\n"
      "ds_read_b128 v[120:123], v0 offset:13312\n"
      "ds_read_b128 v[128:131], v0 offset:15360\n"
      "v_lshlrev_b32 v178, 16, v195\n"
      "ds_read_b128 v[104:107], v0 offset:9216\n"
      "v_lshlrev_b32 v172, 16, v192\n"
      "buffer_load_dword v26, v23, s[8:11], s31 offen\n"
      "s_mov_b32 s46, s36\n"
      "ds_read_b128 v[112:115], v0 offset:11264\n"
      "ds_read2_b32 v[134:135], v2 offset0:64 offset1:96\n"
      "ds_read2_b32 v[132:133], v2 offset0:0 offset1:32\n"
      "s_add_u32 s36, s36, 24576\n"
      "ds_read_b128 v[116:119], v0 offset:12288\n"
      "ds_read_b128 v[124:127], v0 offset:14336\n"
      "ds_read_b128 v[160:163], v0 offset:6144\n"
      "v_and_b32 v177, 0xffff0000, v194\n"
      "s_mov_b32 s20, 15\n"
      "s_waitcnt lgkmcnt(0)\n"
      "s_nop 4\n"
      "s_branch .Lv_loop\n"
      ".p2align 12\n"
      ".Lv_p0:\n"
      "s_branch .Lv_pr\n"
      ".p2align 5\n"
      ".Lv_loop:\n"
      "v_mfma_f32_16x16x32_bf16 v[188:191], v[64:67], v[100:103], 0\n"
      "ds_read_b128 v[140:143], v0 offset:1024\n"
      "v_and_b32 v175, 0xffff0000, v193\n"
      "v_mfma_f32_16x16x32_bf16 v[188:191], v[68:71], v[104:107], v[188:191]\n"
      "ds_read2_b32 v[168:169], v3 offset0:0 offset1:32\n"
      "v_lshlrev_b32 v176, 16, v194\n"
      "v_mfma_f32_16x16x32_bf16 v[188:191], v[72:75], v[108:111], v[188:191]\n"
      "s_waitcnt lgkmcnt(5)\n"
      "ds_read_b128 v[164:167], v0 offset:7168\n"
      "v_add_f32 v218, v218, v184\n"
      "v_mfma_f32_16x16x32_bf16 v[188:191], v[76:79], v[112:115], v[188:191]\n"
      "s_waitcnt lgkmcnt(5)\n"
      "ds_read_b128 v[144:147], v0 offset:2048\n"
      "v_add_f32 v218, v218, v185\n"
      "v_mfma_f32_16x16x32_bf16 v[188:191], v[80:83], v[116:119], v[188:191]\n"
      "buffer_load_dwordx4 v[192:195], v13, s[8:11], s47 offen sc1\n"
      "v_mfma_f32_16x16x32_bf16 v[188:191], v[84:87], v[120:123], v[188:191]\n"
      "s_waitcnt lgkmcnt(4)\n"
      "ds_read2_b32 v[170:171], v3 offset0:64 offset1:96\n"
      "v_fmac_f32 v219, v183, v183\n"
      "v_mfma_f32_16x16x32_bf16 v[188:191], v[88:91], v[124:127], v[188:191]\n"
      "s_min_u32 s36, s36, s6\n"
      "ds_read_b128 v[136:139], v0 offset:0\n"
      "v_fmac_f32 v219, v184, v184\n"
      "v_mfma_f32_16x16x32_bf16 v[188:191], v[92:95], v[128:131], v[188:191]\n"
      "ds_read_b128 v[148:151], v0 offset:3072\n"
      "v_mfma_f32_16x16x32_bf16 v[188:191], v[96:99], v[132:135], v[188:191]\n"
      "v_cvt_pk_bf16_f32 v215, v186, v187\n"
      "v_add_f32 v218, v218, v186\n"
      "v_mfma_f32_16x16x32_bf16 v[172:175], a[0:3], v[100:103], v[172:175]\n"
      "ds_read_b128 v[152:155], v0 offset:4096\n"
      "v_add_f32 v218, v218, v187\n"
      "v_mfma_f32_16x16x32_bf16 v[172:175], a[4:7], v[104:107], v[172:175]\n"
      "ds_read_b128 v[156:159], v0 offset:5120\n"
      "v_fmac_f32 v219, v185, v185\n"
      "v_mfma_f32_16x16x32_bf16 v[172:175], a[8:11], v[108:111], v[172:175]\n"
      "s_add_u32 m0, s39, 40960\n"
      "v_cvt_pk_bf16_f32 v214, v184, v185\n"
      "v_mfma_f32_16x16x32_bf16 v[172:175], a[12:15], v[112:115], v[172:175]\n"
      "ds_write_b32 v18, v218\n"
      "v_mfma_f32_16x16x32_bf16 v[172:175], a[16:19], v[116:119], v[172:175]\n"
      "s_waitcnt vmcnt(10) lgkmcnt(4)\n"
      "v_fmac_f32_e64 v219, v186, v186\n"
      "v_cmp_ne_u32 vcc, s21, v28\n"
      "v_mfma_f32_16x16x32_bf16 v[172:175], a[20:23], v[120:123], v[172:175]\n"
      "ds_write_b32 v17, v190 offset:512\n"
      "v_mfma_f32_16x16x32_bf16 v[172:175], a[24:27], v[124:127], v[172:175]\n"
      "buffer_store_dwordx4 v[212:215], v15, s[8:11], s38 offen offset:1024 sc1\n"
      "v_mfma_f32_16x16x32_bf16 v[172:175], a[28:31], v[128:131], v[172:175]\n"
      "ds_write_b32 v17, v188 offset:0\n"
      "v_mfma_f32_16x16x32_bf16 v[172:175], a[32:35], v[132:135], v[172:175]\n"
      "v_fmac_f32_e64 v219, v187, v187\n"
      "v_mfma_f32_16x16x32_bf16 v[172:175], a[36:39], v[136:139], v[172:175]\n"
      "ds_write_b32 v17, v191 offset:768\n"
      "v_mfma_f32_16x16x32_bf16 v[172:175], a[40:43], v[140:143], v[172:175]\n"
      "s_cbranch_vccnz .Lsp_v0_19\n"
      ".Lbk_v0_19:\n"
      "v_add_u32 v21, 1, v21\n"
      "v_mfma_f32_16x16x32_bf16 v[172:175], a[44:47], v[144:147], v[172:175]\n"
      "s_waitcnt lgkmcnt(3)\n"
      "ds_write_b32 v18, v219 offset:4\n"
      "v_mfma_f32_16x16x32_bf16 v[172:175], a[48:51], v[148:151], v[172:175]\n"
      "buffer_load_dword v28, v11, s[8:11], s36 offen sc1\n"
      "v_mfma_f32_16x16x32_bf16 v[172:175], a[52:55], v[152:155], v[172:175]\n"
      "ds_write_b32 v17, v189 offset:256\n"
      "v_mfma_f32_16x16x32_bf16 v[172:175], a[56:59], v[156:159], v[172:175]\n"
      "s_waitcnt vmcnt(8) lgkmcnt(3)\n"
      "v_lshlrev_b32 v186, 16, v199\n"
      "v_and_b32 v185, 0xffff0000, v198\n"
      "v_mfma_f32_16x16x32_bf16 v[172:175], a[60:63], v[160:163], v[172:175]\n"
      "ds_write_b32 v23, v21\n"
      "v_mfma_f32_16x16x32_bf16 v[172:175], a[64:67], v[164:167], v[172:175]\n"
      "v_and_b32 v181, 0xffff0000, v196\n"
      "v_and_b32 v187, 0xffff0000, v199\n"
      "v_mfma_f32_16x16x32_bf16 v[172:175], a[68:71], v[168:171], v[172:175]\n"
      "buffer_load_dwordx4 v0, s[8:11], s46 offen offset:3072 sc1 lds\n"
      "v_mfma_f32_16x16x32_bf16 v[176:179], a[72:75], v[100:103], v[176:179]\n"
      "v_lshlrev_b32 v182, 16, v197\n"
      "s_add_u32 s38, s38, 24576\n"
      "v_mfma_f32_16x16x32_bf16 v[176:179], a[76:79], v[104:107], v[176:179]\n"
      "v_lshlrev_b32 v184, 16, v198\n"
      "v_mfma_f32_16x16x32_bf16 v[176:179], a[80:83], v[108:111], v[176:179]\n"
      "v_cvt_pk_bf16_f32 v208, v172, v173\n"
      "v_mfma_f32_16x16x32_bf16 v[176:179], a[84:87], v[112:115], v[176:179]\n"
      "v_mul_f32 v219, v172, v172\n"
      "v_mfma_f32_16x16x32_bf16 v[176:179], a[88:91], v[116:119], v[176:179]\n"
      "v_lshlrev_b32 v180, 16, v196\n"
      "v_and_b32 v183, 0xffff0000, v197\n"
      "v_mfma_f32_16x16x32_bf16 v[176:179], a[92:95], v[120:123], v[176:179]\n"
      "v_fmac_f32 v219, v173, v173\n"
      "v_mfma_f32_16x16x32_bf16 v[176:179], a[96:99], v[124:127], v[176:179]\n"
      "buffer_load_dwordx4 v[196:199], v13, s[8:11], s47 offen offset:1024 sc1\n"
      "v_mfma_f32_16x16x32_bf16 v[176:179], a[100:103], v[128:131], v[176:179]\n"
      "ds_read_b32 v22, v24\n"
      "v_mfma_f32_16x16x32_bf16 v[176:179], a[104:107], v[132:135], v[176:179]\n"
      "v_fmac_f32 v219, v174, v174\n"
      "v_mfma_f32_16x16x32_bf16 v[176:179], a[108:111], v[136:139], v[176:179]\n"
      "v_fmac_f32 v219, v175, v175\n"
      "v_mfma_f32_16x16x32_bf16 v[176:179], a[112:115], v[140:143], v[176:179]\n"
      "buffer_load_dwordx4 v0, s[8:11], s46 offen offset:0 sc1 lds\n"
      "v_mfma_f32_16x16x32_bf16 v[176:179], a[116:119], v[144:147], v[176:179]\n"
      "v_mfma_f32_16x16x32_bf16 v[176:179], a[120:123], v[148:151], v[176:179]\n"
      "s_waitcnt lgkmcnt(0)\n"
      "v_cmp_gt_i32 vcc, v21, v22\n"
      "v_add_f32_e64 v218, v172, v173\n"
      "v_mfma_f32_16x16x32_bf16 v[176:179], a[124:127], v[152:155], v[176:179]\n"
      "v_add_f32_e64 v218, v218, v174\n"
      "v_add_f32_e64 v218, v218, v175\n"
      "v_mfma_f32_16x16x32_bf16 v[176:179], a[128:131], v[156:159], v[176:179]\n"
      "buffer_load_dwordx4 v0, s[8:11], s46 offen offset:1024 sc1 lds\n"
      "v_mfma_f32_16x16x32_bf16 v[176:179], a[132:135], v[160:163], v[176:179]\n"
      "s_cbranch_vccnz .Lsp_vz0_20\n"
      ".Lbk_vz0_20:\n"
      "ds_read_b128 v[204:207], v19\n"
      "v_mfma_f32_16x16x32_bf16 v[176:179], a[136:139], v[164:167], v[176:179]\n"
      "v_cvt_pk_bf16_f32 v209, v174, v175\n"
      "v_add_u32 v21, 1, v21\n"
      "v_mfma_f32_16x16x32_bf16 v[176:179], a[140:143], v[168:171], v[176:179]\n"
      "s_waitcnt vmcnt(10)\n"
      "ds_write_b32 v23, v21\n"
      "v_mfma_f32_16x16x32_bf16 v[180:183], a[144:147], v[100:103], v[180:183]\n"
      "v_mfma_f32_16x16x32_bf16 v[180:183], a[148:151], v[104:107], v[180:183]\n"
      "buffer_load_dwordx4 v0, s[8:11], s46 offen offset:2048 sc1 lds\n"
      "v_mfma_f32_16x16x32_bf16 v[180:183], a[152:155], v[108:111], v[180:183]\n"
      "s_add_u32 s49, s49, 24576\n"
      "s_add_u32 m0, s40, 40960\n"
      "v_mfma_f32_16x16x32_bf16 v[180:183], a[156:159], v[112:115], v[180:183]\n"
      "v_mfma_f32_16x16x32_bf16 v[180:183], a[160:163], v[116:119], v[180:183]\n"
      "s_waitcnt lgkmcnt(1)\n"
      "v_add_f32 v204, v204, v206\n"
      "v_cvt_pk_bf16_f32 v210, v176, v177\n"
      "v_mfma_f32_16x16x32_bf16 v[180:183], a[164:167], v[120:123], v[180:183]\n"
      "buffer_load_dwordx4 v1, s[8:11], s46 offen offset:0 sc1 lds\n"
      "v_mfma_f32_16x16x32_bf16 v[180:183], a[168:171], v[124:127], v[180:183]\n"
      "v_add_f32 v205, v205, v207\n"
      "v_add_f32_dpp v204, v204, v204 quad_perm:[1,0,3,2] row_mask:0xf bank_mask:0xf\n"
      "v_mfma_f32_16x16x32_bf16 v[180:183], a[172:175], v[128:131], v[180:183]\n"
      "v_fmac_f32 v219, v176, v176\n"
      "v_mfma_f32_16x16x32_bf16 v[180:183], a[176:179], v[132:135], v[180:183]\n"
      "v_add_f32_dpp v204, v204, v204 quad_perm:[2,3,0,1] row_mask:0xf bank_mask:0xf\n"
      "v_cvt_pk_bf16_f32 v211, v178, v179\n"
      "v_mfma_f32_16x16x32_bf16 v[180:183], a[180:183], v[136:139], v[180:183]\n"
      "buffer_store_dwordx4 v[208:211], v15, s[8:11], s38 offen sc1\n"
      "v_mfma_f32_16x16x32_bf16 v[180:183], a[184:187], v[140:143], v[180:183]\n"
      "ds_read_b32 v22, v24\n"
      "v_add_f32_dpp v205, v205, v205 quad_perm:[1,0,3,2] row_mask:0xf bank_mask:0xf\n"
      "v_mfma_f32_16x16x32_bf16 v[180:183], a[188:191], v[144:147], v[180:183]\n"
      "s_add_u32 s50, s50, 24576\n"
      "v_add_f32_dpp v205, v205, v205 quad_perm:[2,3,0,1] row_mask:0xf bank_mask:0xf\n"
      "v_fmac_f32 v219, v177, v177\n"
      "v_mfma_f32_16x16x32_bf16 v[180:183], a[192:195], v[148:151], v[180:183]\n"
      "v_add_f32_dpp v205, v205, v205 row_half_mirror row_mask:0xf bank_mask:0xf\n"
      "v_fmac_f32 v219, v178, v178\n"
      "v_mfma_f32_16x16x32_bf16 v[180:183], a[196:199], v[152:155], v[180:183]\n"
      "v_add_f32_dpp v204, v204, v204 row_half_mirror row_mask:0xf bank_mask:0xf\n"
      "v_mfma_f32_16x16x32_bf16 v[180:183], a[200:203], v[156:159], v[180:183]\n"
      "v_mul_f32 v204, v27, v204\n"
      "v_fmaak_f32 v205, v27, v205, 0x36a7c5ac\n"
      "v_mfma_f32_16x16x32_bf16 v[180:183], a[204:207], v[160:163], v[180:183]\n"
      "s_waitcnt vmcnt(12) lgkmcnt(1)\n"
      "buffer_store_dword v25, v12, s[8:11], s50 offen sc1\n"
      "v_mfma_f32_16x16x32_bf16 v[180:183], a[208:211], v[164:167], v[180:183]\n"
      "s_waitcnt lgkmcnt(0)\n"
      "v_cmp_gt_i32 vcc, v21, v22\n"
      "v_add_f32 v218, v218, v176\n"
      "v_mfma_f32_16x16x32_bf16 v[180:183], a[212:215], v[168:171], v[180:183]\n"
      "v_add_f32 v218, v218, v177\n"
      "v_add_f32_e64 v218, v218, v178\n"
      "v_mfma_f32_16x16x32_bf16 v[184:187], a[216:219], v[100:103], v[184:187]\n"
      "buffer_store_dwordx2 v[204:205], v20, s[8:11], s49 offen sc1\n"
      "v_mfma_f32_16x16x32_bf16 v[184:187], a[220:223], v[104:107], v[184:187]\n"
      "s_sub_u32 s47, s46, s45\n"
      "s_cbranch_vccnz .Lsp_vy1_21\n"
      ".Lbk_vy1_21:\n"
      "v_mfma_f32_16x16x32_bf16 v[184:187], a[224:227], v[108:111], v[184:187]\n"
      "s_waitcnt vmcnt(11)\n"
      "ds_read_b128 v[104:107], v0 offset:29696\n"
      "v_and_b32 v177, 0xffff0000, v194\n"
      "v_mfma_f32_16x16x32_bf16 v[184:187], a[228:231], v[112:115], v[184:187]\n"
      "v_fmac_f32 v219, v179, v179\n"
      "v_mfma_f32_16x16x32_bf16 v[184:187], a[232:235], v[116:119], v[184:187]\n"
      "v_fmac_f32 v219, v180, v180\n"
      "v_add_f32 v218, v218, v179\n"
      "v_mfma_f32_16x16x32_bf16 v[184:187], a[236:239], v[120:123], v[184:187]\n"
      "v_cvt_pk_bf16_f32 v212, v180, v181\n"
      "v_and_b32 v179, 0xffff0000, v195\n"
      "v_mfma_f32_16x16x32_bf16 v[184:187], a[240:243], v[124:127], v[184:187]\n"
      "ds_read_b128 v[120:123], v0 offset:33792\n"
      "v_lshlrev_b32 v174, 16, v193\n"
      "v_mfma_f32_16x16x32_bf16 v[184:187], a[244:247], v[128:131], v[184:187]\n"
      "ds_read_b128 v[112:115], v0 offset:31744\n"
      "v_add_f32 v218, v218, v180\n"
      "v_mfma_f32_16x16x32_bf16 v[184:187], a[248:251], v[132:135], v[184:187]\n"
      "ds_read_b128 v[100:103], v0 offset:28672\n"
      "v_add_f32 v218, v218, v181\n"
      "v_mfma_f32_16x16x32_bf16 v[184:187], a[252:255], v[136:139], v[184:187]\n"
      "ds_read_b128 v[124:127], v0 offset:34816\n"
      "v_mfma_f32_16x16x32_bf16 v[184:187], v[32:35], v[140:143], v[184:187]\n"
      "ds_read_b128 v[108:111], v0 offset:30720\n"
      "v_and_b32 v175, 0xffff0000, v193\n"
      "v_mfma_f32_16x16x32_bf16 v[184:187], v[36:39], v[144:147], v[184:187]\n"
      "ds_read_b128 v[116:119], v0 offset:32768\n"
      "v_fmac_f32 v219, v181, v181\n"
      "v_mfma_f32_16x16x32_bf16 v[184:187], v[40:43], v[148:151], v[184:187]\n"
      "v_add_f32 v218, v218, v182\n"
      "v_mfma_f32_16x16x32_bf16 v[184:187], v[44:47], v[152:155], v[184:187]\n"
      "ds_read2_b32 v[132:133], v4 offset0:0 offset1:32\n"
      "v_and_b32 v173, 0xffff0000, v192\n"
      "v_mfma_f32_16x16x32_bf16 v[184:187], v[48:51], v[156:159], v[184:187]\n"
      "ds_read_b128 v[128:131], v0 offset:35840\n"
      "v_lshlrev_b32 v178, 16, v195\n"
      "v_mfma_f32_16x16x32_bf16 v[184:187], v[52:55], v[160:163], v[184:187]\n"
      "ds_read_b128 v[156:159], v0 offset:25600\n"
      "v_lshlrev_b32 v172, 16, v192\n"
      "v_mfma_f32_16x16x32_bf16 v[184:187], v[56:59], v[164:167], v[184:187]\n"
      "ds_read_b128 v[144:147], v0 offset:22528\n"
      "v_lshlrev_b32 v176, 16, v194\n"
      "v_mfma_f32_16x16x32_bf16 v[184:187], v[60:63], v[168:171], v[184:187]\n"
      "s_waitcnt lgkmcnt(5)\n"
      "buffer_load_dwordx4 v[192:195], v13, s[8:11], s47 offen sc1\n"
      "v_mfma_f32_16x16x32_bf16 v[188:191], v[64:67], v[100:103], 0\n"
      "ds_read2_b32 v[134:135], v4 offset0:64 offset1:96\n"
      "v_fmac_f32 v219, v182, v182\n"
      "v_mfma_f32_16x16x32_bf16 v[188:191], v[68:71], v[104:107], v[188:191]\n"
      "ds_read_b128 v[148:151], v0 offset:23552\n"
      "v_add_f32 v218, v218, v183\n"
      "v_mfma_f32_16x16x32_bf16 v[188:191], v[72:75], v[108:111], v[188:191]\n"
      "s_mov_b32 s46, s36\n"
      "v_cvt_pk_bf16_f32 v213, v182, v183\n"
      "v_cvt_pk_bf16_f32 v215, v186, v187\n"
      "v_mfma_f32_16x16x32_bf16 v[188:191], v[76:79], v[112:115], v[188:191]\n"
      "s_waitcnt lgkmcnt(4)\n"
      "ds_read_b128 v[160:163], v0 offset:26624\n"
      "v_fmac_f32 v219, v183, v183\n"
      "v_mfma_f32_16x16x32_bf16 v[188:191], v[80:83], v[116:119], v[188:191]\n"
      "ds_read_b128 v[140:143], v0 offset:21504\n"
      "v_fmac_f32 v219, v184, v184\n"
      "v_mfma_f32_16x16x32_bf16 v[188:191], v[84:87], v[120:123], v[188:191]\n"
      "ds_read2_b32 v[168:169], v5 offset0:0 offset1:32\n"
      "v_add_f32 v218, v218, v184\n"
      "v_mfma_f32_16x16x32_bf16 v[188:191], v[88:91], v[124:127], v[188:191]\n"
      "ds_read_b128 v[152:155], v0 offset:24576\n"
      "v_fmac_f32 v219, v185, v185\n"
      "v_mfma_f32_16x16x32_bf16 v[188:191], v[92:95], v[128:131], v[188:191]\n"
      "s_waitcnt lgkmcnt(5)\n"
      "ds_read_b128 v[136:139], v0 offset:20480\n"
      "v_add_f32 v218, v218, v185\n"
      "v_mfma_f32_16x16x32_bf16 v[188:191], v[96:99], v[132:135], v[188:191]\n"
      "s_cmp_eq_u32 s36, s37\n"
      "s_cselect_b32 s48, 49152, s51\n"
      "ds_read_b128 v[164:167], v0 offset:27648\n"
      "v_mfma_f32_16x16x32_bf16 v[172:175], a[0:3], v[100:103], v[172:175]\n"
      "ds_read2_b32 v[170:171], v5 offset0:64 offset1:96\n"
      "v_cvt_pk_bf16_f32 v214, v184, v185\n"
      "v_mfma_f32_16x16x32_bf16 v[172:175], a[4:7], v[104:107], v[172:175]\n"
      "v_fmac_f32 v219, v186, v186\n"
      "v_add_f32 v218, v218, v186\n"
      "v_mfma_f32_16x16x32_bf16 v[172:175], a[8:11], v[108:111], v[172:175]\n"
      "ds_write_b32 v17, v189 offset:256\n"
      "v_mfma_f32_16x16x32_bf16 v[172:175], a[12:15], v[112:115], v[172:175]\n"
      "v_add_f32 v218, v218, v187\n"
      "v_add_u32 v21, 1, v21\n"
      "v_mfma_f32_16x16x32_bf16 v[172:175], a[16:19], v[116:119], v[172:175]\n"
      "buffer_store_dwordx4 v[212:215], v15, s[8:11], s38 offen offset:1024 sc1\n"
      "v_mfma_f32_16x16x32_bf16 v[172:175], a[20:23], v[120:123], v[172:175]\n"
      "ds_write_b32 v17, v190 offset:512\n"
      "v_mfma_f32_16x16x32_bf16 v[172:175], a[24:27], v[124:127], v[172:175]\n"
      "v_fmac_f32 v219, v187, v187\n"
      "v_mfma_f32_16x16x32_bf16 v[172:175], a[28:31], v[128:131], v[172:175]\n"
      "s_add_u32 s36, s36, s48\n"
      "ds_write_b32 v18, v218\n"
      "v_mfma_f32_16x16x32_bf16 v[172:175], a[32:35], v[132:135], v[172:175]\n"
      "s_waitcnt vmcnt(11) lgkmcnt(3)\n"
      "v_cmp_ne_u32 vcc, s21, v28\n"
      "v_mfma_f32_16x16x32_bf16 v[172:175], a[36:39], v[136:139], v[172:175]\n"
      "ds_write_b32 v17, v188 offset:0\n"
      "v_mfma_f32_16x16x32_bf16 v[172:175], a[40:43], v[140:143], v[172:175]\n"
      "s_waitcnt vmcnt(9) lgkmcnt(3)\n"
      "s_min_u32 s36, s36, s6\n"
      "v_lshlrev_b32 v182, 16, v197\n"
      "v_mfma_f32_16x16x32_bf16 v[172:175], a[44:47], v[144:147], v[172:175]\n"
      "ds_write_b32 v17, v191 offset:768\n"
      "v_mfma_f32_16x16x32_bf16 v[172:175], a[48:51], v[148:151], v[172:175]\n"
      "s_cbranch_vccnz .Lsp_v1_22\n"
      ".Lbk_v1_22:\n"
      "v_and_b32 v181, 0xffff0000, v196\n"
      "v_mfma_f32_16x16x32_bf16 v[172:175], a[52:55], v[152:155], v[172:175]\n"
      "ds_write_b32 v18, v219 offset:4\n"
      "v_mfma_f32_16x16x32_bf16 v[172:175], a[56:59], v[156:159], v[172:175]\n"
      "buffer_load_dword v28, v11, s[8:11], s36 offen sc1\n"
      "v_mfma_f32_16x16x32_bf16 v[172:175], a[60:63], v[160:163], v[172:175]\n"
      "s_add_u32 m0, s39, 61440\n"
      "ds_write_b32 v23, v21\n"
      "v_mfma_f32_16x16x32_bf16 v[172:175], a[64:67], v[164:167], v[172:175]\n"
      "v_and_b32 v187, 0xffff0000, v199\n"
      "v_and_b32 v183, 0xffff0000, v197\n"
      "v_mfma_f32_16x16x32_bf16 v[172:175], a[68:71], v[168:171], v[172:175]\n"
      "buffer_load_dwordx4 v0, s[8:11], s46 offen offset:0 sc1 lds\n"
      "v_mfma_f32_16x16x32_bf16 v[176:179], a[72:75], v[100:103], v[176:179]\n"
      "s_add_u32 s49, s49, 24576\n"
      "v_mfma_f32_16x16x32_bf16 v[176:179], a[76:79], v[104:107], v[176:179]\n"
      "v_mfma_f32_16x16x32_bf16 v[176:179], a[80:83], v[108:111], v[176:179]\n"
      "v_cvt_pk_bf16_f32 v209, v174, v175\n"
      "v_mfma_f32_16x16x32_bf16 v[176:179], a[84:87], v[112:115], v[176:179]\n"
      "buffer_load_dwordx4 v0, s[8:11], s46 offen offset:1024 sc1 lds\n"
      "v_mfma_f32_16x16x32_bf16 v[176:179], a[88:91], v[116:119], v[176:179]\n"
      "v_add_f32 v218, v172, v173\n"
      "v_mfma_f32_16x16x32_bf16 v[176:179], a[92:95], v[120:123], v[176:179]\n"
      "v_add_f32 v218, v218, v174\n"
      "v_lshlrev_b32 v180, 16, v196\n"
      "v_mfma_f32_16x16x32_bf16 v[176:179], a[96:99], v[124:127], v[176:179]\n"
      "ds_read_b32 v22, v24\n"
      "v_lshlrev_b32 v186, 16, v199\n"
      "v_mfma_f32_16x16x32_bf16 v[176:179], a[100:103], v[128:131], v[176:179]\n"
      "buffer_load_dwordx4 v0, s[8:11], s46 offen offset:2048 sc1 lds\n"
      "v_mfma_f32_16x16x32_bf16 v[176:179], a[104:107], v[132:135], v[176:179]\n"
      "v_mul_f32 v219, v172, v172\n"
      "v_fmac_f32 v219, v173, v173\n"
      "v_mfma_f32_16x16x32_bf16 v[176:179], a[108:111], v[136:139], v[176:179]\n"
      "v_lshlrev_b32 v184, 16, v198\n"
      "v_mfma_f32_16x16x32_bf16 v[176:179], a[112:115], v[140:143], v[176:179]\n"
      "v_fmac_f32_e64 v219, v174, v174\n"
      "v_mfma_f32_16x16x32_bf16 v[176:179], a[116:119], v[144:147], v[176:179]\n"
      "s_waitcnt lgkmcnt(0)\n"
      "v_cmp_gt_i32 vcc, v21, v22\n"
      "v_add_f32_e64 v218, v218, v175\n"
      "v_mfma_f32_16x16x32_bf16 v[176:179], a[120:123], v[148:151], v[176:179]\n"
      "v_mfma_f32_16x16x32_bf16 v[176:179], a[124:127], v[152:155], v[176:179]\n"
      "v_and_b32 v185, 0xffff0000, v198\n"
      "v_cvt_pk_bf16_f32 v208, v172, v173\n"
      "v_mfma_f32_16x16x32_bf16 v[176:179], a[128:131], v[156:159], v[176:179]\n"
      "buffer_load_dwordx4 v[196:199], v13, s[8:11], s47 offen offset:1024 sc1\n"
      "v_mfma_f32_16x16x32_bf16 v[176:179], a[132:135], v[160:163], v[176:179]\n"
      "s_cbranch_vccnz .Lsp_vz1_23\n"
      ".Lbk_vz1_23:\n"
      "ds_read_b128 v[204:207], v19\n"
      "v_mfma_f32_16x16x32_bf16 v[176:179], a[136:139], v[164:167], v[176:179]\n"
      "v_add_u32 v21, 1, v21\n"
      "v_mfma_f32_16x16x32_bf16 v[176:179], a[140:143], v[168:171], v[176:179]\n"
      "s_waitcnt vmcnt(10)\n"
      "ds_write_b32 v23, v21\n"
      "v_mfma_f32_16x16x32_bf16 v[180:183], a[144:147], v[100:103], v[180:183]\n"
      "v_fmac_f32 v219, v175, v175\n"
      "v_mfma_f32_16x16x32_bf16 v[180:183], a[148:151], v[104:107], v[180:183]\n"
      "buffer_load_dwordx4 v0, s[8:11], s46 offen offset:3072 sc1 lds\n"
      "v_mfma_f32_16x16x32_bf16 v[180:183], a[152:155], v[108:111], v[180:183]\n"
      "v_cvt_pk_bf16_f32 v210, v176, v177\n"
      "v_fmac_f32 v219, v176, v176\n"
      "v_mfma_f32_16x16x32_bf16 v[180:183], a[156:159], v[112:115], v[180:183]\n"
      "s_add_u32 m0, s40, 61440\n"
      "v_fmac_f32 v219, v177, v177\n"
      "v_mfma_f32_16x16x32_bf16 v[180:183], a[160:163], v[116:119], v[180:183]\n"
      "s_waitcnt lgkmcnt(1)\n"
      "v_add_f32 v204, v204, v206\n"
      "v_add_f32 v205, v205, v207\n"
      "v_mfma_f32_16x16x32_bf16 v[180:183], a[164:167], v[120:123], v[180:183]\n"
      "v_add_f32_dpp v204, v204, v204 quad_perm:[1,0,3,2] row_mask:0xf bank_mask:0xf\n"
      "v_fmac_f32 v219, v178, v178\n"
      "v_mfma_f32_16x16x32_bf16 v[180:183], a[168:171], v[124:127], v[180:183]\n"
      "buffer_load_dwordx4 v1, s[8:11], s46 offen offset:0 sc1 lds\n"
      "v_mfma_f32_16x16x32_bf16 v[180:183], a[172:175], v[128:131], v[180:183]\n"
      "v_add_f32_dpp v205, v205, v205 quad_perm:[1,0,3,2] row_mask:0xf bank_mask:0xf\n"
      "v_add_f32_dpp v204, v204, v204 quad_perm:[2,3,0,1] row_mask:0xf bank_mask:0xf\n"
      "v_mfma_f32_16x16x32_bf16 v[180:183], a[176:179], v[132:135], v[180:183]\n"
      "v_add_f32_dpp v204, v204, v204 row_half_mirror row_mask:0xf bank_mask:0xf\n"
      "v_cvt_pk_bf16_f32 v211, v178, v179\n"
      "v_mfma_f32_16x16x32_bf16 v[180:183], a[180:183], v[136:139], v[180:183]\n"
      "s_add_u32 s38, s38, 24576\n"
      "v_mul_f32 v204, v27, v204\n"
      "v_add_f32_dpp v205, v205, v205 quad_perm:[2,3,0,1] row_mask:0xf bank_mask:0xf\n"
      "v_mfma_f32_16x16x32_bf16 v[180:183], a[184:187], v[140:143], v[180:183]\n"
      "ds_read_b32 v22, v24\n"
      "v_add_f32 v218, v218, v176\n"
      "v_mfma_f32_16x16x32_bf16 v[180:183], a[188:191], v[144:147], v[180:183]\n"
      "v_add_f32_dpp v205, v205, v205 row_half_mirror row_mask:0xf bank_mask:0xf\n"
      "v_fmaak_f32 v205, v27, v205, 0x36a7c5ac\n"
      "v_mfma_f32_16x16x32_bf16 v[180:183], a[192:195], v[148:151], v[180:183]\n"
      "buffer_store_dwordx2 v[204:205], v20, s[8:11], s49 offen sc1\n"
      "v_mfma_f32_16x16x32_bf16 v[180:183], a[196:199], v[152:155], v[180:183]\n"
      "s_add_u32 s50, s50, 24576\n"
      "v_add_f32 v218, v218, v177\n"
      "v_mfma_f32_16x16x32_bf16 v[180:183], a[200:203], v[156:159], v[180:183]\n"
      "v_add_f32 v218, v218, v178\n"
      "v_mfma_f32_16x16x32_bf16 v[180:183], a[204:207], v[160:163], v[180:183]\n"
      "s_waitcnt lgkmcnt(0)\n"
      "v_add_f32 v218, v218, v179\n"
      "v_cmp_gt_i32 vcc, v21, v22\n"
      "v_mfma_f32_16x16x32_bf16 v[180:183], a[208:211], v[164:167], v[180:183]\n"
      "s_waitcnt vmcnt(10)\n"
      "buffer_store_dword v25, v12, s[8:11], s50 offen sc1\n"
      "v_mfma_f32_16x16x32_bf16 v[180:183], a[212:215], v[168:171], v[180:183]\n"
      "v_fmac_f32 v219, v179, v179\n"
      "v_mfma_f32_16x16x32_bf16 v[184:187], a[216:219], v[100:103], v[184:187]\n"
      "s_cbranch_vccnz .Lsp_vy0_24\n"
      ".Lbk_vy0_24:\n"
      "s_sub_u32 s47, s46, s45\n"
      "v_mfma_f32_16x16x32_bf16 v[184:187], a[220:223], v[104:107], v[184:187]\n"
      "s_waitcnt vmcnt(10)\n"
      "v_lshlrev_b32 v174, 16, v193\n"
      "v_and_b32 v179, 0xffff0000, v195\n"
      "v_mfma_f32_16x16x32_bf16 v[184:187], a[224:227], v[108:111], v[184:187]\n"
      "ds_read_b128 v[100:103], v10 offset:8192\n"
      "v_cvt_pk_bf16_f32 v213, v182, v183\n"
      "v_mfma_f32_16x16x32_bf16 v[184:187], a[228:231], v[112:115], v[184:187]\n"
      "ds_read_b128 v[108:111], v10 offset:10240\n"
      "v_and_b32 v173, 0xffff0000, v192\n"
      "v_mfma_f32_16x16x32_bf16 v[184:187], a[232:235], v[116:119], v[184:187]\n"
      "v_fmac_f32 v219, v180, v180\n"
      "v_mfma_f32_16x16x32_bf16 v[184:187], a[236:239], v[120:123], v[184:187]\n"
      "ds_read_b128 v[120:123], v10 offset:13312\n"
      "v_add_f32 v218, v218, v180\n"
      "v_mfma_f32_16x16x32_bf16 v[184:187], a[240:243], v[124:127], v[184:187]\n"
      "v_add_f32 v218, v218, v181\n"
      "v_add_f32 v218, v218, v182\n"
      "v_mfma_f32_16x16x32_bf16 v[184:187], a[244:247], v[128:131], v[184:187]\n"
      "ds_read_b128 v[128:131], v10 offset:15360\n"
      "v_lshlrev_b32 v178, 16, v195\n"
      "v_mfma_f32_16x16x32_bf16 v[184:187], a[248:251], v[132:135], v[184:187]\n"
      "v_mfma_f32_16x16x32_bf16 v[184:187], a[252:255], v[136:139], v[184:187]\n"
      "ds_read_b128 v[104:107], v10 offset:9216\n"
      "v_lshlrev_b32 v172, 16, v192\n"
      "v_mfma_f32_16x16x32_bf16 v[184:187], v[32:35], v[140:143], v[184:187]\n"
      "buffer_store_dwordx4 v[208:211], v15, s[8:11], s38 offen sc1\n"
      "v_mfma_f32_16x16x32_bf16 v[184:187], v[36:39], v[144:147], v[184:187]\n"
      "s_mov_b32 s46, s36\n"
      "ds_read_b128 v[112:115], v10 offset:11264\n"
      "v_fmac_f32 v219, v181, v181\n"
      "v_mfma_f32_16x16x32_bf16 v[184:187], v[40:43], v[148:151], v[184:187]\n"
      "ds_read2_b32 v[134:135], v6 offset0:64 offset1:96\n"
      "v_cvt_pk_bf16_f32 v212, v180, v181\n"
      "v_mfma_f32_16x16x32_bf16 v[184:187], v[44:47], v[152:155], v[184:187]\n"
      "ds_read2_b32 v[132:133], v6 offset0:0 offset1:32\n"
      "s_add_u32 s36, s36, 24576\n"
      "v_mfma_f32_16x16x32_bf16 v[184:187], v[48:51], v[156:159], v[184:187]\n"
      "ds_read_b128 v[116:119], v10 offset:12288\n"
      "v_fmac_f32 v219, v182, v182\n"
      "v_mfma_f32_16x16x32_bf16 v[184:187], v[52:55], v[160:163], v[184:187]\n"
      "ds_read_b128 v[124:127], v10 offset:14336\n"
      "v_add_f32 v218, v218, v183\n"
      "v_mfma_f32_16x16x32_bf16 v[184:187], v[56:59], v[164:167], v[184:187]\n"
      "s_waitcnt lgkmcnt(5)\n"
      "ds_read_b128 v[160:163], v10 offset:6144\n"
      "v_and_b32 v177, 0xffff0000, v194\n"
      "v_mfma_f32_16x16x32_bf16 v[184:187], v[60:63], v[168:171], v[184:187]\n"
      "v_mfma_f32_16x16x32_bf16 v[188:191], v[64:67], v[100:103], 0\n"
      "ds_read_b128 v[140:143], v10 offset:1024\n"
      "v_and_b32 v175, 0xffff0000, v193\n"
      "v_mfma_f32_16x16x32_bf16 v[188:191], v[68:71], v[104:107], v[188:191]\n"
      "ds_read2_b32 v[168:169], v7 offset0:0 offset1:32\n"
      "v_lshlrev_b32 v176, 16, v194\n"
      "v_mfma_f32_16x16x32_bf16 v[188:191], v[72:75], v[108:111], v[188:191]\n"
      "s_waitcnt lgkmcnt(5)\n"
      "ds_read_b128 v[164:167], v10 offset:7168\n"
      "v_add_f32 v218, v218, v184\n"
      "v_mfma_f32_16x16x32_bf16 v[188:191], v[76:79], v[112:115], v[188:191]\n"
      "s_waitcnt lgkmcnt(5)\n"
      "ds_read_b128 v[144:147], v10 offset:2048\n"
      "v_add_f32 v218, v218, v185\n"
      "v_mfma_f32_16x16x32_bf16 v[188:191], v[80:83], v[116:119], v[188:191]\n"
      "buffer_load_dwordx4 v[192:195], v13, s[8:11], s47 offen sc1\n"
      "v_mfma_f32_16x16x32_bf16 v[188:191], v[84:87], v[120:123], v[188:191]\n"
      "s_waitcnt lgkmcnt(4)\n"
      "ds_read2_b32 v[170:171], v7 offset0:64 offset1:96\n"
      "v_fmac_f32 v219, v183, v183\n"
      "v_mfma_f32_16x16x32_bf16 v[188:191], v[88:91], v[124:127], v[188:191]\n"
      "s_min_u32 s36, s36, s6\n"
      "ds_read_b128 v[136:139], v10 offset:0\n"
      "v_fmac_f32 v219, v184, v184\n"
      "v_mfma_f32_16x16x32_bf16 v[188:191], v[92:95], v[128:131], v[188:191]\n"
      "ds_read_b128 v[148:151], v10 offset:3072\n"
      "v_mfma_f32_16x16x32_bf16 v[188:191], v[96:99], v[132:135], v[188:191]\n"
      "v_cvt_pk_bf16_f32 v215, v186, v187\n"
      "v_add_f32 v218, v218, v186\n"
      "v_mfma_f32_16x16x32_bf16 v[172:175], a[0:3], v[100:103], v[172:175]\n"
      "ds_read_b128 v[152:155], v10 offset:4096\n"
      "v_add_f32 v218, v218, v187\n"
      "v_mfma_f32_16x16x32_bf16 v[172:175], a[4:7], v[104:107], v[172:175]\n"
      "ds_read_b128 v[156:159], v10 offset:5120\n"
      "v_fmac_f32_e64 v219, v185, v185\n"
      "v_mfma_f32_16x16x32_bf16 v[172:175], a[8:11], v[108:111], v[172:175]\n"
      "s_add_u32 m0, s39, 0\n"
      "v_cvt_pk_bf16_f32 v214, v184, v185\n"
      "v_mfma_f32_16x16x32_bf16 v[172:175], a[12:15], v[112:115], v[172:175]\n"
      "ds_write_b32 v18, v218\n"
      "v_mfma_f32_16x16x32_bf16 v[172:175], a[16:19], v[116:119], v[172:175]\n"
      "s_waitcnt vmcnt(10) lgkmcnt(4)\n"
      "v_fmac_f32_e64 v219, v186, v186\n"
      "v_cmp_ne_u32 vcc, s21, v28\n"
      "v_mfma_f32_16x16x32_bf16 v[172:175], a[20:23], v[120:123], v[172:175]\n"
      "ds_write_b32 v17, v190 offset:512\n"
      "v_mfma_f32_16x16x32_bf16 v[172:175], a[24:27], v[124:127], v[172:175]\n"
      "buffer_store_dwordx4 v[212:215], v15, s[8:11], s38 offen offset:1024 sc1\n"
      "v_mfma_f32_16x16x32_bf16 v[172:175], a[28:31], v[128:131], v[172:175]\n"
      "ds_write_b32 v17, v188 offset:0\n"
      "v_mfma_f32_16x16x32_bf16 v[172:175], a[32:35], v[132:135], v[172:175]\n"
      "v_fmac_f32_e64 v219, v187, v187\n"
      "v_mfma_f32_16x16x32_bf16 v[172:175], a[36:39], v[136:139], v[172:175]\n"
      "ds_write_b32 v17, v191 offset:768\n"
      "v_mfma_f32_16x16x32_bf16 v[172:175], a[40:43], v[140:143], v[172:175]\n"
      "s_cbranch_vccnz .Lsp_v0_25\n"
      ".Lbk_v0_25:\n"
      "v_add_u32 v21, 1, v21\n"
      "v_mfma_f32_16x16x32_bf16 v[172:175], a[44:47], v[144:147], v[172:175]\n"
      "s_waitcnt lgkmcnt(3)\n"
      "ds_write_b32 v18, v219 offset:4\n"
      "v_mfma_f32_16x16x32_bf16 v[172:175], a[48:51], v[148:151], v[172:175]\n"
      "buffer_load_dword v28, v11, s[8:11], s36 offen sc1\n"
      "v_mfma_f32_16x16x32_bf16 v[172:175], a[52:55], v[152:155], v[172:175]\n"
      "ds_write_b32 v17, v189 offset:256\n"
      "v_mfma_f32_16x16x32_bf16 v[172:175], a[56:59], v[156:159], v[172:175]\n"
      "s_waitcnt vmcnt(8) lgkmcnt(3)\n"
      "v_lshlrev_b32 v186, 16, v199\n"
      "v_and_b32 v185, 0xffff0000, v198\n"
      "v_mfma_f32_16x16x32_bf16 v[172:175], a[60:63], v[160:163], v[172:175]\n"
      "ds_write_b32 v23, v21\n"
      "v_mfma_f32_16x16x32_bf16 v[172:175], a[64:67], v[164:167], v[172:175]\n"
      "v_and_b32 v181, 0xffff0000, v196\n"
      "v_and_b32 v187, 0xffff0000, v199\n"
      "v_mfma_f32_16x16x32_bf16 v[172:175], a[68:71], v[168:171], v[172:175]\n"
      "buffer_load_dwordx4 v0, s[8:11], s46 offen offset:3072 sc1 lds\n"
      "v_mfma_f32_16x16x32_bf16 v[176:179], a[72:75], v[100:103], v[176:179]\n"
      "v_lshlrev_b32 v182, 16, v197\n"
      "s_add_u32 s38, s38, 24576\n"
      "v_mfma_f32_16x16x32_bf16 v[176:179], a[76:79], v[104:107], v[176:179]\n"
      "v_lshlrev_b32 v184, 16, v198\n"
      "v_mfma_f32_16x16x32_bf16 v[176:179], a[80:83], v[108:111], v[176:179]\n"
      "v_cvt_pk_bf16_f32 v208, v172, v173\n"
      "v_mfma_f32_16x16x32_bf16 v[176:179], a[84:87], v[112:115], v[176:179]\n"
      "v_mul_f32 v219, v172, v172\n"
      "v_mfma_f32_16x16x32_bf16 v[176:179], a[88:91], v[116:119], v[176:179]\n"
      "v_lshlrev_b32 v180, 16, v196\n"
      "v_and_b32 v183, 0xffff0000, v197\n"
      "v_mfma_f32_16x16x32_bf16 v[176:179], a[92:95], v[120:123], v[176:179]\n"
      "v_fmac_f32 v219, v173, v173\n"
      "v_mfma_f32_16x16x32_bf16 v[176:179], a[96:99], v[124:127], v[176:179]\n"
      "buffer_load_dwordx4 v[196:199], v13, s[8:11], s47 offen offset:1024 sc1\n"
      "v_mfma_f32_16x16x32_bf16 v[176:179], a[100:103], v[128:131], v[176:179]\n"
      "ds_read_b32 v22, v24\n"
      "v_mfma_f32_16x16x32_bf16 v[176:179], a[104:107], v[132:135], v[176:179]\n"
      "v_fmac_f32 v219, v174, v174\n"
      "v_mfma_f32_16x16x32_bf16 v[176:179], a[108:111], v[136:139], v[176:179]\n"
      "v_fmac_f32 v219, v175, v175\n"
      "v_mfma_f32_16x16x32_bf16 v[176:179], a[112:115], v[140:143], v[176:179]\n"
      "buffer_load_dwordx4 v0, s[8:11], s46 offen offset:0 sc1 lds\n"
      "v_mfma_f32_16x16x32_bf16 v[176:179], a[116:119], v[144:147], v[176:179]\n"
      "v_mfma_f32_16x16x32_bf16 v[176:179], a[120:123], v[148:151], v[176:179]\n"
      "s_waitcnt lgkmcnt(0)\n"
      "v_cmp_gt_i32 vcc, v21, v22\n"
      "v_add_f32_e64 v218, v172, v173\n"
      "v_mfma_f32_16x16x32_bf16 v[176:179], a[124:127], v[152:155], v[176:179]\n"
      "v_add_f32_e64 v218, v218, v174\n"
      "v_add_f32_e64 v218, v218, v175\n"
      "v_mfma_f32_16x16x32_bf16 v[176:179], a[128:131], v[156:159], v[176:179]\n"
      "buffer_load_dwordx4 v0, s[8:11], s46 offen offset:1024 sc1 lds\n"
      "v_mfma_f32_16x16x32_bf16 v[176:179], a[132:135], v[160:163], v[176:179]\n"
      "s_cbranch_vccnz .Lsp_vz0_26\n"
      ".Lbk_vz0_26:\n"
      "ds_read_b128 v[204:207], v19\n"
      "v_mfma_f32_16x16x32_bf16 v[176:179], a[136:139], v[164:167], v[176:179]\n"
      "v_cvt_pk_bf16_f32 v209, v174, v175\n"
      "v_add_u32 v21, 1, v21\n"
      "v_mfma_f32_16x16x32_bf16 v[176:179], a[140:143], v[168:171], v[176:179]\n"
      "s_waitcnt vmcnt(10)\n"
      "ds_write_b32 v23, v21\n"
      "v_mfma_f32_16x16x32_bf16 v[180:183], a[144:147], v[100:103], v[180:183]\n"
      "v_mfma_f32_16x16x32_bf16 v[180:183], a[148:151], v[104:107], v[180:183]\n"
      "buffer_load_dwordx4 v0, s[8:11], s46 offen offset:2048 sc1 lds\n"
      "v_mfma_f32_16x16x32_bf16 v[180:183], a[152:155], v[108:111], v[180:183]\n"
      "s_add_u32 s49, s49, 24576\n"
      "s_add_u32 m0, s40, 0\n"
      "v_mfma_f32_16x16x32_bf16 v[180:183], a[156:159], v[112:115], v[180:183]\n"
      "v_mfma_f32_16x16x32_bf16 v[180:183], a[160:163], v[116:119], v[180:183]\n"
      "s_waitcnt lgkmcnt(1)\n"
      "v_add_f32 v204, v204, v206\n"
      "v_cvt_pk_bf16_f32 v210, v176, v177\n"
      "v_mfma_f32_16x16x32_bf16 v[180:183], a[164:167], v[120:123], v[180:183]\n"
      "buffer_load_dwordx4 v1, s[8:11], s46 offen offset:0 sc1 lds\n"
      "v_mfma_f32_16x16x32_bf16 v[180:183], a[168:171], v[124:127], v[180:183]\n"
      "v_add_f32 v205, v205, v207\n"
      "v_add_f32_dpp v204, v204, v204 quad_perm:[1,0,3,2] row_mask:0xf bank_mask:0xf\n"
      "v_mfma_f32_16x16x32_bf16 v[180:183], a[172:175], v[128:131], v[180:183]\n"
      "v_fmac_f32 v219, v176, v176\n"
      "v_mfma_f32_16x16x32_bf16 v[180:183], a[176:179], v[132:135], v[180:183]\n"
      "v_add_f32_dpp v204, v204, v204 quad_perm:[2,3,0,1] row_mask:0xf bank_mask:0xf\n"
      "v_cvt_pk_bf16_f32 v211, v178, v179\n"
      "v_mfma_f32_16x16x32_bf16 v[180:183], a[180:183], v[136:139], v[180:183]\n"
      "buffer_store_dwordx4 v[208:211], v15, s[8:11], s38 offen sc1\n"
      "v_mfma_f32_16x16x32_bf16 v[180:183], a[184:187], v[140:143], v[180:183]\n"
      "ds_read_b32 v22, v24\n"
      "v_add_f32_dpp v205, v205, v205 quad_perm:[1,0,3,2] row_mask:0xf bank_mask:0xf\n"
      "v_mfma_f32_16x16x32_bf16 v[180:183], a[188:191], v[144:147], v[180:183]\n"
      "s_add_u32 s50, s50, 24576\n"
      "v_add_f32_dpp v205, v205, v205 quad_perm:[2,3,0,1] row_mask:0xf bank_mask:0xf\n"
      "v_fmac_f32 v219, v177, v177\n"
      "v_mfma_f32_16x16x32_bf16 v[180:183], a[192:195], v[148:151], v[180:183]\n"
      "v_add_f32_dpp v205, v205, v205 row_half_mirror row_mask:0xf bank_mask:0xf\n"
      "v_fmac_f32 v219, v178, v178\n"
      "v_mfma_f32_16x16x32_bf16 v[180:183], a[196:199], v[152:155], v[180:183]\n"
      ".Lv_p1:\n"
      "s_cbranch_execz .Lv_pr\n"
      "v_add_f32_dpp v204, v204, v204 row_half_mirror row_mask:0xf bank_mask:0xf\n"
      "v_mfma_f32_16x16x32_bf16 v[180:183], a[200:203], v[156:159], v[180:183]\n"
      "v_mul_f32 v204, v27, v204\n"
      "v_fmaak_f32 v205, v27, v205, 0x36a7c5ac\n"
      "v_mfma_f32_16x16x32_bf16 v[180:183], a[204:207], v[160:163], v[180:183]\n"
      "s_waitcnt vmcnt(12) lgkmcnt(1)\n"
      "buffer_store_dword v25, v12, s[8:11], s50 offen sc1\n"
      "v_mfma_f32_16x16x32_bf16 v[180:183], a[208:211], v[164:167], v[180:183]\n"
      "s_waitcnt lgkmcnt(0)\n"
      "v_cmp_gt_i32 vcc, v21, v22\n"
      "v_add_f32 v218, v218, v176\n"
      "v_mfma_f32_16x16x32_bf16 v[180:183], a[212:215], v[168:171], v[180:183]\n"
      "v_add_f32 v218, v218, v177\n"
      "v_add_f32_e64 v218, v218, v178\n"
      "v_mfma_f32_16x16x32_bf16 v[184:187], a[216:219], v[100:103], v[184:187]\n"
      "buffer_store_dwordx2 v[204:205], v20, s[8:11], s49 offen sc1\n"
      "v_mfma_f32_16x16x32_bf16 v[184:187], a[220:223], v[104:107], v[184:187]\n"
      "s_sub_u32 s47, s46, s45\n"
      "s_cbranch_vccnz .Lsp_vy1_27\n"
      ".Lbk_vy1_27:\n"
      "v_mfma_f32_16x16x32_bf16 v[184:187], a[224:227], v[108:111], v[184:187]\n"
      "s_waitcnt vmcnt(11)\n"
      "ds_read_b128 v[104:107], v10 offset:29696\n"
      "v_and_b32 v177, 0xffff0000, v194\n"
      "v_mfma_f32_16x16x32_bf16 v[184:187], a[228:231], v[112:115], v[184:187]\n"
      "v_fmac_f32 v219, v179, v179\n"
      "v_mfma_f32_16x16x32_bf16 v[184:187], a[232:235], v[116:119], v[184:187]\n"
      "v_fmac_f32 v219, v180, v180\n"
      "v_add_f32 v218, v218, v179\n"
      "v_mfma_f32_16x16x32_bf16 v[184:187], a[236:239], v[120:123], v[184:187]\n"
      "v_cvt_pk_bf16_f32 v212, v180, v181\n"
      "v_and_b32 v179, 0xffff0000, v195\n"
      "v_mfma_f32_16x16x32_bf16 v[184:187], a[240:243], v[124:127], v[184:187]\n"
      "ds_read_b128 v[120:123], v10 offset:33792\n"
      "v_lshlrev_b32 v174, 16, v193\n"
      "v_mfma_f32_16x16x32_bf16 v[184:187], a[244:247], v[128:131], v[184:187]\n"
      "ds_read_b128 v[112:115], v10 offset:31744\n"
      "v_add_f32 v218, v218, v180\n"
      "v_mfma_f32_16x16x32_bf16 v[184:187], a[248:251], v[132:135], v[184:187]\n"
      "ds_read_b128 v[100:103], v10 offset:28672\n"
      "v_add_f32 v218, v218, v181\n"
      "v_mfma_f32_16x16x32_bf16 v[184:187], a[252:255], v[136:139], v[184:187]\n"
      "ds_read_b128 v[124:127], v10 offset:34816\n"
      "v_mfma_f32_16x16x32_bf16 v[184:187], v[32:35], v[140:143], v[184:187]\n"
      "ds_read_b128 v[108:111], v10 offset:30720\n"
      "v_and_b32 v175, 0xffff0000, v193\n"
      "v_mfma_f32_16x16x32_bf16 v[184:187], v[36:39], v[144:147], v[184:187]\n"
      "ds_read_b128 v[116:119], v10 offset:32768\n"
      "v_fmac_f32 v219, v181, v181\n"
      "v_mfma_f32_16x16x32_bf16 v[184:187], v[40:43], v[148:151], v[184:187]\n"
      "v_add_f32 v218, v218, v182\n"
      "v_mfma_f32_16x16x32_bf16 v[184:187], v[44:47], v[152:155], v[184:187]\n"
      "ds_read2_b32 v[132:133], v8 offset0:0 offset1:32\n"
      "v_and_b32 v173, 0xffff0000, v192\n"
      "v_mfma_f32_16x16x32_bf16 v[184:187], v[48:51], v[156:159], v[184:187]\n"
      "ds_read_b128 v[128:131], v10 offset:35840\n"
      "v_lshlrev_b32 v178, 16, v195\n"
      "v_mfma_f32_16x16x32_bf16 v[184:187], v[52:55], v[160:163], v[184:187]\n"
      "ds_read_b128 v[156:159], v10 offset:25600\n"
      "v_lshlrev_b32 v172, 16, v192\n"
      "v_mfma_f32_16x16x32_bf16 v[184:187], v[56:59], v[164:167], v[184:187]\n"
      "ds_read_b128 v[144:147], v10 offset:22528\n"
      "v_lshlrev_b32 v176, 16, v194\n"
      "v_mfma_f32_16x16x32_bf16 v[184:187], v[60:63], v[168:171], v[184:187]\n"
      "s_waitcnt lgkmcnt(5)\n"
      "buffer_load_dwordx4 v[192:195], v13, s[8:11], s47 offen sc1\n"
      "v_mfma_f32_16x16x32_bf16 v[188:191], v[64:67], v[100:103], 0\n"
      "ds_read2_b32 v[134:135], v8 offset0:64 offset1:96\n"
      "v_fmac_f32 v219, v182, v182\n"
      "v_mfma_f32_16x16x32_bf16 v[188:191], v[68:71], v[104:107], v[188:191]\n"
      "ds_read_b128 v[148:151], v10 offset:23552\n"
      "v_add_f32 v218, v218, v183\n"
      "v_mfma_f32_16x16x32_bf16 v[188:191], v[72:75], v[108:111], v[188:191]\n"
      "s_mov_b32 s46, s36\n"
      "v_cvt_pk_bf16_f32 v213, v182, v183\n"
      "v_cvt_pk_bf16_f32 v215, v186, v187\n"
      "v_mfma_f32_16x16x32_bf16 v[188:191], v[76:79], v[112:115], v[188:191]\n"
      "s_waitcnt lgkmcnt(4)\n"
      "ds_read_b128 v[160:163], v10 offset:26624\n"
      "v_fmac_f32 v219, v183, v183\n"
      "v_mfma_f32_16x16x32_bf16 v[188:191], v[80:83], v[116:119], v[188:191]\n"
      "ds_read_b128 v[140:143], v10 offset:21504\n"
      "v_fmac_f32 v219, v184, v184\n"
      "v_mfma_f32_16x16x32_bf16 v[188:191], v[84:87], v[120:123], v[188:191]\n"
      "ds_read2_b32 v[168:169], v9 offset0:0 offset1:32\n"
      "v_add_f32 v218, v218, v184\n"
      "v_mfma_f32_16x16x32_bf16 v[188:191], v[88:91], v[124:127], v[188:191]\n"
      "ds_read_b128 v[152:155], v10 offset:24576\n"
      "v_fmac_f32 v219, v185, v185\n"
      "v_mfma_f32_16x16x32_bf16 v[188:191], v[92:95], v[128:131], v[188:191]\n"
      "s_waitcnt lgkmcnt(5)\n"
      "ds_read_b128 v[136:139], v10 offset:20480\n"
      "v_add_f32 v218, v218, v185\n"
      "v_mfma_f32_16x16x32_bf16 v[188:191], v[96:99], v[132:135], v[188:191]\n"
      "s_cmp_eq_u32 s36, s37\n"
      "s_cselect_b32 s48, 49152, s51\n"
      "ds_read_b128 v[164:167], v10 offset:27648\n"
      "v_mfma_f32_16x16x32_bf16 v[172:175], a[0:3], v[100:103], v[172:175]\n"
      "ds_read2_b32 v[170:171], v9 offset0:64 offset1:96\n"
      "v_cvt_pk_bf16_f32 v214, v184, v185\n"
      "v_mfma_f32_16x16x32_bf16 v[172:175], a[4:7], v[104:107], v[172:175]\n"
      "v_fmac_f32 v219, v186, v186\n"
      "v_add_f32 v218, v218, v186\n"
      "v_mfma_f32_16x16x32_bf16 v[172:175], a[8:11], v[108:111], v[172:175]\n"
      "ds_write_b32 v17, v189 offset:256\n"
      "v_mfma_f32_16x16x32_bf16 v[172:175], a[12:15], v[112:115], v[172:175]\n"
      "v_add_f32 v218, v218, v187\n"
      "v_add_u32 v21, 1, v21\n"
      "v_mfma_f32_16x16x32_bf16 v[172:175], a[16:19], v[116:119], v[172:175]\n"
      "buffer_store_dwordx4 v[212:215], v15, s[8:11], s38 offen offset:1024 sc1\n"
      "v_mfma_f32_16x16x32_bf16 v[172:175], a[20:23], v[120:123], v[172:175]\n"
      "ds_write_b32 v17, v190 offset:512\n"
      "v_mfma_f32_16x16x32_bf16 v[172:175], a[24:27], v[124:127], v[172:175]\n"
      "v_fmac_f32 v219, v187, v187\n"
      "v_mfma_f32_16x16x32_bf16 v[172:175], a[28:31], v[128:131], v[172:175]\n"
      "s_add_u32 s36, s36, s48\n"
      "ds_write_b32 v18, v218\n"
      "v_mfma_f32_16x16x32_bf16 v[172:175], a[32:35], v[132:135], v[172:175]\n"
      "s_waitcnt vmcnt(11) lgkmcnt(3)\n"
      "v_cmp_ne_u32 vcc, s21, v28\n"
      "v_mfma_f32_16x16x32_bf16 v[172:175], a[36:39], v[136:139], v[172:175]\n"
      "ds_write_b32 v17, v188 offset:0\n"
      "v_mfma_f32_16x16x32_bf16 v[172:175], a[40:43], v[140:143], v[172:175]\n"
      "s_waitcnt vmcnt(9) lgkmcnt(3)\n"
      "s_min_u32 s36, s36, s6\n"
      "v_lshlrev_b32 v182, 16, v197\n"
      "v_mfma_f32_16x16x32_bf16 v[172:175], a[44:47], v[144:147], v[172:175]\n"
      "ds_write_b32 v17, v191 offset:768\n"
      "v_mfma_f32_16x16x32_bf16 v[172:175], a[48:51], v[148:151], v[172:175]\n"
      "s_cbranch_vccnz .Lsp_v1_28\n"
      ".Lbk_v1_28:\n"
      "v_and_b32 v181, 0xffff0000, v196\n"
      "v_mfma_f32_16x16x32_bf16 v[172:175], a[52:55], v[152:155], v[172:175]\n"
      "ds_write_b32 v18, v219 offset:4\n"
      "v_mfma_f32_16x16x32_bf16 v[172:175], a[56:59], v[156:159], v[172:175]\n"
      "buffer_load_dword v28, v11, s[8:11], s36 offen sc1\n"
      "v_mfma_f32_16x16x32_bf16 v[172:175], a[60:63], v[160:163], v[172:175]\n"
      "s_add_u32 m0, s39, 20480\n"
      "ds_write_b32 v23, v21\n"
      "v_mfma_f32_16x16x32_bf16 v[172:175], a[64:67], v[164:167], v[172:175]\n"
      "v_and_b32 v187, 0xffff0000, v199\n"
      "v_and_b32 v183, 0xffff0000, v197\n"
      "v_mfma_f32_16x16x32_bf16 v[172:175], a[68:71], v[168:171], v[172:175]\n"
      "buffer_load_dwordx4 v0, s[8:11], s46 offen offset:0 sc1 lds\n"
      "v_mfma_f32_16x16x32_bf16 v[176:179], a[72:75], v[100:103], v[176:179]\n"
      "s_add_u32 s49, s49, 24576\n"
      "v_mfma_f32_16x16x32_bf16 v[176:179], a[76:79], v[104:107], v[176:179]\n"
      "v_mfma_f32_16x16x32_bf16 v[176:179], a[80:83], v[108:111], v[176:179]\n"
      "v_cvt_pk_bf16_f32 v209, v174, v175\n"
      "v_mfma_f32_16x16x32_bf16 v[176:179], a[84:87], v[112:115], v[176:179]\n"
      "buffer_load_dwordx4 v0, s[8:11], s46 offen offset:1024 sc1 lds\n"
      "v_mfma_f32_16x16x32_bf16 v[176:179], a[88:91], v[116:119], v[176:179]\n"
      "v_add_f32 v218, v172, v173\n"
      "v_mfma_f32_16x16x32_bf16 v[176:179], a[92:95], v[120:123], v[176:179]\n"
      "v_add_f32 v218, v218, v174\n"
      "v_lshlrev_b32 v180, 16, v196\n"
      "v_mfma_f32_16x16x32_bf16 v[176:179], a[96:99], v[124:127], v[176:179]\n"
      "ds_read_b32 v22, v24\n"
      "v_lshlrev_b32 v186, 16, v199\n"
      "v_mfma_f32_16x16x32_bf16 v[176:179], a[100:103], v[128:131], v[176:179]\n"
      "buffer_load_dwordx4 v0, s[8:11], s46 offen offset:2048 sc1 lds\n"
      "v_mfma_f32_16x16x32_bf16 v[176:179], a[104:107], v[132:135], v[176:179]\n"
      "v_mul_f32 v219, v172, v172\n"
      "v_fmac_f32 v219, v173, v173\n"
      "v_mfma_f32_16x16x32_bf16 v[176:179], a[108:111], v[136:139], v[176:179]\n"
      "v_lshlrev_b32 v184, 16, v198\n"
      "v_mfma_f32_16x16x32_bf16 v[176:179], a[112:115], v[140:143], v[176:179]\n"
      "v_fmac_f32_e64 v219, v174, v174\n"
      "v_mfma_f32_16x16x32_bf16 v[176:179], a[116:119], v[144:147], v[176:179]\n"
      "s_waitcnt lgkmcnt(0)\n"
      "v_cmp_gt_i32 vcc, v21, v22\n"
      "v_add_f32_e64 v218, v218, v175\n"
      "v_mfma_f32_16x16x32_bf16 v[176:179], a[120:123], v[148:151], v[176:179]\n"
      "v_mfma_f32_16x16x32_bf16 v[176:179], a[124:127], v[152:155], v[176:179]\n"
      "v_and_b32 v185, 0xffff0000, v198\n"
      "v_cvt_pk_bf16_f32 v208, v172, v173\n"
      "v_mfma_f32_16x16x32_bf16 v[176:179], a[128:131], v[156:159], v[176:179]\n"
      "buffer_load_dwordx4 v[196:199], v13, s[8:11], s47 offen offset:1024 sc1\n"
      "v_mfma_f32_16x16x32_bf16 v[176:179], a[132:135], v[160:163], v[176:179]\n"
      "s_cbranch_vccnz .Lsp_vz1_29\n"
      ".Lbk_vz1_29:\n"
      "ds_read_b128 v[204:207], v19\n"
      "v_mfma_f32_16x16x32_bf16 v[176:179], a[136:139], v[164:167], v[176:179]\n"
      "v_add_u32 v21, 1, v21\n"
      "v_mfma_f32_16x16x32_bf16 v[176:179], a[140:143], v[168:171], v[176:179]\n"
      "s_waitcnt vmcnt(10)\n"
      "ds_write_b32 v23, v21\n"
      "v_mfma_f32_16x16x32_bf16 v[180:183], a[144:147], v[100:103], v[180:183]\n"
      "v_fmac_f32 v219, v175, v175\n"
      "v_mfma_f32_16x16x32_bf16 v[180:183], a[148:151], v[104:107], v[180:183]\n"
      "buffer_load_dwordx4 v0, s[8:11], s46 offen offset:3072 sc1 lds\n"
      "v_mfma_f32_16x16x32_bf16 v[180:183], a[152:155], v[108:111], v[180:183]\n"
      "v_cvt_pk_bf16_f32 v210, v176, v177\n"
      "v_fmac_f32 v219, v176, v176\n"
      "v_mfma_f32_16x16x32_bf16 v[180:183], a[156:159], v[112:115], v[180:183]\n"
      "s_add_u32 m0, s40, 20480\n"
      "v_fmac_f32 v219, v177, v177\n"
      "v_mfma_f32_16x16x32_bf16 v[180:183], a[160:163], v[116:119], v[180:183]\n"
      "s_waitcnt lgkmcnt(1)\n"
      "v_add_f32 v204, v204, v206\n"
      "v_add_f32 v205, v205, v207\n"
      "v_mfma_f32_16x16x32_bf16 v[180:183], a[164:167], v[120:123], v[180:183]\n"
      "v_add_f32_dpp v204, v204, v204 quad_perm:[1,0,3,2] row_mask:0xf bank_mask:0xf\n"
      "v_fmac_f32 v219, v178, v178\n"
      "v_mfma_f32_16x16x32_bf16 v[180:183], a[168:171], v[124:127], v[180:183]\n"
      "buffer_load_dwordx4 v1, s[8:11], s46 offen offset:0 sc1 lds\n"
      "v_mfma_f32_16x16x32_bf16 v[180:183], a[172:175], v[128:131], v[180:183]\n"
      "v_add_f32_dpp v205, v205, v205 quad_perm:[1,0,3,2] row_mask:0xf bank_mask:0xf\n"
      "v_add_f32_dpp v204, v204, v204 quad_perm:[2,3,0,1] row_mask:0xf bank_mask:0xf\n"
      "v_mfma_f32_16x16x32_bf16 v[180:183], a[176:179], v[132:135], v[180:183]\n"
      "v_add_f32_dpp v204, v204, v204 row_half_mirror row_mask:0xf bank_mask:0xf\n"
      "v_cvt_pk_bf16_f32 v211, v178, v179\n"
      "v_mfma_f32_16x16x32_bf16 v[180:183], a[180:183], v[136:139], v[180:183]\n"
      "s_add_u32 s38, s38, 24576\n"
      "v_mul_f32 v204, v27, v204\n"
      "v_add_f32_dpp v205, v205, v205 quad_perm:[2,3,0,1] row_mask:0xf bank_mask:0xf\n"
      "v_mfma_f32_16x16x32_bf16 v[180:183], a[184:187], v[140:143], v[180:183]\n"
      "ds_read_b32 v22, v24\n"
      "v_add_f32 v218, v218, v176\n"
      "v_mfma_f32_16x16x32_bf16 v[180:183], a[188:191], v[144:147], v[180:183]\n"
      "v_add_f32_dpp v205, v205, v205 row_half_mirror row_mask:0xf bank_mask:0xf\n"
      "v_fmaak_f32 v205, v27, v205, 0x36a7c5ac\n"
      "v_mfma_f32_16x16x32_bf16 v[180:183], a[192:195], v[148:151], v[180:183]\n"
      "buffer_store_dwordx2 v[204:205], v20, s[8:11], s49 offen sc1\n"
      "v_mfma_f32_16x16x32_bf16 v[180:183], a[196:199], v[152:155], v[180:183]\n"
      "s_add_u32 s50, s50, 24576\n"
      "v_add_f32 v218, v218, v177\n"
      "v_mfma_f32_16x16x32_bf16 v[180:183], a[200:203], v[156:159], v[180:183]\n"
      "v_add_f32 v218, v218, v178\n"
      "v_mfma_f32_16x16x32_bf16 v[180:183], a[204:207], v[160:163], v[180:183]\n"
      "s_waitcnt lgkmcnt(0)\n"
      "v_add_f32 v218, v218, v179\n"
      "v_cmp_gt_i32 vcc, v21, v22\n"
      "v_mfma_f32_16x16x32_bf16 v[180:183], a[208:211], v[164:167], v[180:183]\n"
      "s_waitcnt vmcnt(10)\n"
      "buffer_store_dword v25, v12, s[8:11], s50 offen sc1\n"
      "v_mfma_f32_16x16x32_bf16 v[180:183], a[212:215], v[168:171], v[180:183]\n"
      "v_fmac_f32 v219, v179, v179\n"
      "v_mfma_f32_16x16x32_bf16 v[184:187], a[216:219], v[100:103], v[184:187]\n"
      "s_cbranch_vccnz .Lsp_vy0_30\n"
      ".Lbk_vy0_30:\n"
      "s_sub_u32 s47, s46, s45\n"
      "v_mfma_f32_16x16x32_bf16 v[184:187], a[220:223], v[104:107], v[184:187]\n"
      "s_waitcnt vmcnt(10)\n"
      "v_lshlrev_b32 v174, 16, v193\n"
      "v_and_b32 v179, 0xffff0000, v195\n"
      "v_mfma_f32_16x16x32_bf16 v[184:187], a[224:227], v[108:111], v[184:187]\n"
      "ds_read_b128 v[100:103], v0 offset:8192\n"
      "v_cvt_pk_bf16_f32 v213, v182, v183\n"
      "v_mfma_f32_16x16x32_bf16 v[184:187], a[228:231], v[112:115], v[184:187]\n"
      "ds_read_b128 v[108:111], v0 offset:10240\n"
      "v_and_b32 v173, 0xffff0000, v192\n"
      "v_mfma_f32_16x16x32_bf16 v[184:187], a[232:235], v[116:119], v[184:187]\n"
      "v_fmac_f32 v219, v180, v180\n"
      "v_mfma_f32_16x16x32_bf16 v[184:187], a[236:239], v[120:123], v[184:187]\n"
      "ds_read_b128 v[120:123], v0 offset:13312\n"
      "v_add_f32 v218, v218, v180\n"
      "v_mfma_f32_16x16x32_bf16 v[184:187], a[240:243], v[124:127], v[184:187]\n"
      "v_add_f32 v218, v218, v181\n"
      "v_add_f32 v218, v218, v182\n"
      "v_mfma_f32_16x16x32_bf16 v[184:187], a[244:247], v[128:131], v[184:187]\n"
      "ds_read_b128 v[128:131], v0 offset:15360\n"
      "v_lshlrev_b32 v178, 16, v195\n"
      "v_mfma_f32_16x16x32_bf16 v[184:187], a[248:251], v[132:135], v[184:187]\n"
      "v_mfma_f32_16x16x32_bf16 v[184:187], a[252:255], v[136:139], v[184:187]\n"
      "ds_read_b128 v[104:107], v0 offset:9216\n"
      "v_lshlrev_b32 v172, 16, v192\n"
      "v_mfma_f32_16x16x32_bf16 v[184:187], v[32:35], v[140:143], v[184:187]\n"
      "buffer_store_dwordx4 v[208:211], v15, s[8:11], s38 offen sc1\n"
      "v_mfma_f32_16x16x32_bf16 v[184:187], v[36:39], v[144:147], v[184:187]\n"
      "s_mov_b32 s46, s36\n"
      "ds_read_b128 v[112:115], v0 offset:11264\n"
      "v_fmac_f32 v219, v181, v181\n"
      "v_mfma_f32_16x16x32_bf16 v[184:187], v[40:43], v[148:151], v[184:187]\n"
      "ds_read2_b32 v[134:135], v2 offset0:64 offset1:96\n"
      "v_cvt_pk_bf16_f32 v212, v180, v181\n"
      "v_mfma_f32_16x16x32_bf16 v[184:187], v[44:47], v[152:155], v[184:187]\n"
      "ds_read2_b32 v[132:133], v2 offset0:0 offset1:32\n"
      "s_add_u32 s36, s36, 24576\n"
      "v_mfma_f32_16x16x32_bf16 v[184:187], v[48:51], v[156:159], v[184:187]\n"
      "ds_read_b128 v[116:119], v0 offset:12288\n"
      "v_fmac_f32 v219, v182, v182\n"
      "v_mfma_f32_16x16x32_bf16 v[184:187], v[52:55], v[160:163], v[184:187]\n"
      "ds_read_b128 v[124:127], v0 offset:14336\n"
      "v_add_f32 v218, v218, v183\n"
      "v_mfma_f32_16x16x32_bf16 v[184:187], v[56:59], v[164:167], v[184:187]\n"
      "s_waitcnt lgkmcnt(5)\n"
      "ds_read_b128 v[160:163], v0 offset:6144\n"
      "v_and_b32 v177, 0xffff0000, v194\n"
      "v_mfma_f32_16x16x32_bf16 v[184:187], v[60:63], v[168:171], v[184:187]\n"
      "s_sub_u32 s20, s20, 1\n"
      "s_cbranch_scc0 .Lv_loop\n"
      ".Lv_tail:\n"
      "s_nop 8\n"
      "v_add_f32 v218, v218, v184\n"
      "v_add_f32 v218, v218, v185\n"
      "v_fmac_f32 v219, v183, v183\n"
      "v_fmac_f32 v219, v184, v184\n"
      "v_cvt_pk_bf16_f32 v215, v186, v187\n"
      "v_add_f32 v218, v218, v186\n"
      "v_add_f32 v218, v218, v187\n"
      "v_fmac_f32 v219, v185, v185\n"
      "v_cvt_pk_bf16_f32 v214, v184, v185\n"
      "ds_write_b32 v18, v218\n"
      "v_fmac_f32 v219, v186, v186\n"
      "buffer_store_dwordx4 v[212:215], v15, s[8:11], s38 offen offset:1024 sc1\n"
      "v_fmac_f32 v219, v187, v187\n"
      "ds_write_b32 v18, v219 offset:4\n"
      "s_waitcnt lgkmcnt(0)\n"
      "s_barrier\n"
      "s_add_u32 s38, s38, 24576\n"
      "ds_read_b128 v[204:207], v19\n"
      "s_add_u32 s49, s49, 24576\n"
      "s_waitcnt lgkmcnt(0)\n"
      "v_add_f32 v204, v204, v206\n"
      "v_add_f32 v205, v205, v207\n"
      "s_nop 0\n"
      "v_add_f32_dpp v204, v204, v204 quad_perm:[1,0,3,2] row_mask:0xf bank_mask:0xf\n"
      "s_nop 1\n"
      "v_add_f32_dpp v204, v204, v204 quad_perm:[2,3,0,1] row_mask:0xf bank_mask:0xf\n"
      "v_add_f32_dpp v205, v205, v205 quad_perm:[1,0,3,2] row_mask:0xf bank_mask:0xf\n"
      "s_add_u32 s50, s50, 24576\n"
      "s_nop 0\n"
      "v_add_f32_dpp v205, v205, v205 quad_perm:[2,3,0,1] row_mask:0xf bank_mask:0xf\n"
      "s_nop 1\n"
      "v_add_f32_dpp v205, v205, v205 row_half_mirror row_mask:0xf bank_mask:0xf\n"
      "v_add_f32_dpp v204, v204, v204 row_half_mirror row_mask:0xf bank_mask:0xf\n"
      "v_mul_f32 v204, v27, v204\n"
      "v_fmaak_f32 v205, v27, v205, 0x36a7c5ac\n"
      "s_waitcnt vmcnt(3)\n"
      "buffer_store_dword v25, v12, s[8:11], s50 offen sc1\n"
      "buffer_store_dwordx2 v[204:205], v20, s[8:11], s49 offen sc1\n"
      "s_add_u32 s49, s49, 24576\n"
      "s_add_u32 s50, s50, 24576\n"
      "s_waitcnt vmcnt(0)\n"
      "buffer_store_dword v25, v12, s[8:11], s50 offen sc1\n"
      "s_add_u32 s50, s50, 24576\n"
      "s_endpgm\n"
      ".Lsp_w0_1:\n"
      "s_mov_b32 s22, s23\n"
      ".Lsl_w0_1:\n"
      "buffer_load_dword v28, v11, s[8:11], s46 offen sc1\n"
      "s_waitcnt vmcnt(0)\n"
      "v_cmp_ne_u32 vcc, s21, v28\n"
      "s_cbranch_vccz .Lbk_w0_1\n"
      "s_sleep 4\n"
      "s_sub_u32 s22, s22, 1\n"
      "s_cbranch_scc0 .Lsl_w0_1\n"
      "s_mov_b32 s23, 0\n"
      "s_branch .Lbk_w0_1\n"
      ".Lsp_w1_2:\n"
      "s_mov_b32 s22, s23\n"
      ".Lsl_w1_2:\n"
      "buffer_load_dword v28, v11, s[8:11], s46 offen sc1\n"
      "s_waitcnt vmcnt(0)\n"
      "v_cmp_ne_u32 vcc, s21, v28\n"
      "s_cbranch_vccz .Lbk_w1_2\n"
      "s_sleep 4\n"
      "s_sub_u32 s22, s22, 1\n"
      "s_cbranch_scc0 .Lsl_w1_2\n"
      "s_mov_b32 s23, 0\n"
      "s_branch .Lbk_w1_2\n"
      ".Lsp_wy0_3:\n"
      "s_lshl_b32 s22, s23, 4\n"
      ".Lsl_wy0_3:\n"
      "ds_read_b32 v22, v24\n"
      "s_waitcnt lgkmcnt(0)\n"
      "v_cmp_gt_i32 vcc, v21, v22\n"
      "s_cbranch_vccz .Lbk_wy0_3\n"
      "s_sub_u32 s22, s22, 1\n"
      "s_cbranch_scc0 .Lsl_wy0_3\n"
      "s_mov_b32 s23, 0\n"
      "s_branch .Lbk_wy0_3\n"
      ".Lsp_w0_4:\n"
      "s_mov_b32 s22, s23\n"
      "buffer_load_dword v28, v11, s[8:11], s46 offen sc1\n"
      "s_waitcnt vmcnt(0)\n"
      "v_cmp_ne_u32 vcc, s21, v28\n"
      "s_add_u32 s30, s50, 0\n"
      "buffer_store_dword v25, v12, s[8:11], s30 offen sc1\n"
      "s_add_u32 s30, s50, 24576\n"
      "buffer_store_dword v25, v12, s[8:11], s30 offen sc1\n"
      "s_cbranch_vccz .Lbk_w0_4\n"
      ".Lsl_w0_4:\n"
      "buffer_load_dword v28, v11, s[8:11], s46 offen sc1\n"
      "s_waitcnt vmcnt(0)\n"
      "v_cmp_ne_u32 vcc, s21, v28\n"
      "s_cbranch_vccz .Lbk_w0_4\n"
      "s_sleep 4\n"
      "s_sub_u32 s22, s22, 1\n"
      "s_cbranch_scc0 .Lsl_w0_4\n"
      "s_mov_b32 s23, 0\n"
      "s_branch .Lbk_w0_4\n"
      ".Lsp_wz0_5:\n"
      "s_lshl_b32 s22, s23, 4\n"
      ".Lsl_wz0_5:\n"
      "ds_read_b32 v22, v24\n"
      "s_waitcnt lgkmcnt(0)\n"
      "v_cmp_gt_i32 vcc, v21, v22\n"
      "s_cbranch_vccz .Lbk_wz0_5\n"
      "s_sub_u32 s22, s22, 1\n"
      "s_cbranch_scc0 .Lsl_wz0_5\n"
      "s_mov_b32 s23, 0\n"
      "s_branch .Lbk_wz0_5\n"
      ".Lsp_wy1_6:\n"
      "s_lshl_b32 s22, s23, 4\n"
      ".Lsl_wy1_6:\n"
      "ds_read_b32 v22, v24\n"
      "s_waitcnt lgkmcnt(0)\n"
      "v_cmp_gt_i32 vcc, v21, v22\n"
      "s_cbranch_vccz .Lbk_wy1_6\n"
      "s_sub_u32 s22, s22, 1\n"
      "s_cbranch_scc0 .Lsl_wy1_6\n"
      "s_mov_b32 s23, 0\n"
      "s_branch .Lbk_wy1_6\n"
      ".Lsp_w1_7:\n"
      "s_mov_b32 s22, s23\n"
      "buffer_load_dword v28, v11, s[8:11], s46 offen sc1\n"
      "s_waitcnt vmcnt(0)\n"
      "v_cmp_ne_u32 vcc, s21, v28\n"
      "s_add_u32 s30, s50, 24576\n"
      "buffer_store_dword v25, v12, s[8:11], s30 offen sc1\n"
      "s_cbranch_vccz .Lbk_w1_7\n"
      ".Lsl_w1_7:\n"
      "buffer_load_dword v28, v11, s[8:11], s46 offen sc1\n"
      "s_waitcnt vmcnt(0)\n"
      "v_cmp_ne_u32 vcc, s21, v28\n"
      "s_cbranch_vccz .Lbk_w1_7\n"
      "s_sleep 4\n"
      "s_sub_u32 s22, s22, 1\n"
      "s_cbranch_scc0 .Lsl_w1_7\n"
      "s_mov_b32 s23, 0\n"
      "s_branch .Lbk_w1_7\n"
      ".Lsp_wz1_8:\n"
      "s_lshl_b32 s22, s23, 4\n"
      ".Lsl_wz1_8:\n"
      "ds_read_b32 v22, v24\n"
      "s_waitcnt lgkmcnt(0)\n"
      "v_cmp_gt_i32 vcc, v21, v22\n"
      "s_cbranch_vccz .Lbk_wz1_8\n"
      "s_sub_u32 s22, s22, 1\n"
      "s_cbranch_scc0 .Lsl_wz1_8\n"
      "s_mov_b32 s23, 0\n"
      "s_branch .Lbk_wz1_8\n"
      ".Lsp_wy0_9:\n"
      "s_lshl_b32 s22, s23, 4\n"
      ".Lsl_wy0_9:\n"
      "ds_read_b32 v22, v24\n"
      "s_waitcnt lgkmcnt(0)\n"
      "v_cmp_gt_i32 vcc, v21, v22\n"
      "s_cbranch_vccz .Lbk_wy0_9\n"
      "s_sub_u32 s22, s22, 1\n"
      "s_cbranch_scc0 .Lsl_wy0_9\n"
      "s_mov_b32 s23, 0\n"
      "s_branch .Lbk_wy0_9\n"
      ".Lsp_w0_10:\n"
      "s_mov_b32 s22, s23\n"
      "buffer_load_dword v28, v11, s[8:11], s46 offen sc1\n"
      "s_waitcnt vmcnt(0)\n"
      "v_cmp_ne_u32 vcc, s21, v28\n"
      "s_add_u32 s30, s50, 0\n"
      "buffer_store_dword v25, v12, s[8:11], s30 offen sc1\n"
      "s_add_u32 s30, s50, 24576\n"
      "buffer_store_dword v25, v12, s[8:11], s30 offen sc1\n"
      "s_cbranch_vccz .Lbk_w0_10\n"
      ".Lsl_w0_10:\n"
      "buffer_load_dword v28, v11, s[8:11], s46 offen sc1\n"
      "s_waitcnt vmcnt(0)\n"
      "v_cmp_ne_u32 vcc, s21, v28\n"
      "s_cbranch_vccz .Lbk_w0_10\n"
      "s_sleep 4\n"
      "s_sub_u32 s22, s22, 1\n"
      "s_cbranch_scc0 .Lsl_w0_10\n"
      "s_mov_b32 s23, 0\n"
      "s_branch .Lbk_w0_10\n"
      ".Lsp_wz0_11:\n"
      "s_lshl_b32 s22, s23, 4\n"
      ".Lsl_wz0_11:\n"
      "ds_read_b32 v22, v24\n"
      "s_waitcnt lgkmcnt(0)\n"
      "v_cmp_gt_i32 vcc, v21, v22\n"
      "s_cbranch_vccz .Lbk_wz0_11\n"
      "s_sub_u32 s22, s22, 1\n"
      "s_cbranch_scc0 .Lsl_wz0_11\n"
      "s_mov_b32 s23, 0\n"
      "s_branch .Lbk_wz0_11\n"
      ".Lsp_wy1_12:\n"
      "s_lshl_b32 s22, s23, 4\n"
      ".Lsl_wy1_12:\n"
      "ds_read_b32 v22, v24\n"
      "s_waitcnt lgkmcnt(0)\n"
      "v_cmp_gt_i32 vcc, v21, v22\n"
      "s_cbranch_vccz .Lbk_wy1_12\n"
      "s_sub_u32 s22, s22, 1\n"
      "s_cbranch_scc0 .Lsl_wy1_12\n"
      "s_mov_b32 s23, 0\n"
      "s_branch .Lbk_wy1_12\n"
      ".Lsp_w1_13:\n"
      "s_mov_b32 s22, s23\n"
      "buffer_load_dword v28, v11, s[8:11], s46 offen sc1\n"
      "s_waitcnt vmcnt(0)\n"
      "v_cmp_ne_u32 vcc, s21, v28\n"
      "s_add_u32 s30, s50, 24576\n"
      "buffer_store_dword v25, v12, s[8:11], s30 offen sc1\n"
      "s_cbranch_vccz .Lbk_w1_13\n"
      ".Lsl_w1_13:\n"
      "buffer_load_dword v28, v11, s[8:11], s46 offen sc1\n"
      "s_waitcnt vmcnt(0)\n"
      "v_cmp_ne_u32 vcc, s21, v28\n"
      "s_cbranch_vccz .Lbk_w1_13\n"
      "s_sleep 4\n"
      "s_sub_u32 s22, s22, 1\n"
      "s_cbranch_scc0 .Lsl_w1_13\n"
      "s_mov_b32 s23, 0\n"
      "s_branch .Lbk_w1_13\n"
      ".Lsp_wz1_14:\n"
      "s_lshl_b32 s22, s23, 4\n"
      ".Lsl_wz1_14:\n"
      "ds_read_b32 v22, v24\n"
      "s_waitcnt lgkmcnt(0)\n"
      "v_cmp_gt_i32 vcc, v21, v22\n"
      "s_cbranch_vccz .Lbk_wz1_14\n"
      "s_sub_u32 s22, s22, 1\n"
      "s_cbranch_scc0 .Lsl_wz1_14\n"
      "s_mov_b32 s23, 0\n"
      "s_branch .Lbk_wz1_14\n"
      ".Lsp_wy0_15:\n"
      "s_lshl_b32 s22, s23, 4\n"
      ".Lsl_wy0_15:\n"
      "ds_read_b32 v22, v24\n"
      "s_waitcnt lgkmcnt(0)\n"
      "v_cmp_gt_i32 vcc, v21, v22\n"
      "s_cbranch_vccz .Lbk_wy0_15\n"
      "s_sub_u32 s22, s22, 1\n"
      "s_cbranch_scc0 .Lsl_wy0_15\n"
      "s_mov_b32 s23, 0\n"
      "s_branch .Lbk_wy0_15\n"
      ".Lsp_v0_16:\n"
      "s_mov_b32 s22, s23\n"
      ".Lsl_v0_16:\n"
      "buffer_load_dword v28, v11, s[8:11], s46 offen sc1\n"
      "s_waitcnt vmcnt(0)\n"
      "v_cmp_ne_u32 vcc, s21, v28\n"
      "s_cbranch_vccz .Lbk_v0_16\n"
      "s_sleep 4\n"
      "s_sub_u32 s22, s22, 1\n"
      "s_cbranch_scc0 .Lsl_v0_16\n"
      "s_mov_b32 s23, 0\n"
      "s_branch .Lbk_v0_16\n"
      ".Lsp_v1_17:\n"
      "s_mov_b32 s22, s23\n"
      ".Lsl_v1_17:\n"
      "buffer_load_dword v28, v11, s[8:11], s46 offen sc1\n"
      "s_waitcnt vmcnt(0)\n"
      "v_cmp_ne_u32 vcc, s21, v28\n"
      "s_cbranch_vccz .Lbk_v1_17\n"
      "s_sleep 4\n"
      "s_sub_u32 s22, s22, 1\n"
      "s_cbranch_scc0 .Lsl_v1_17\n"
      "s_mov_b32 s23, 0\n"
      "s_branch .Lbk_v1_17\n"
      ".Lsp_vy0_18:\n"
      "s_lshl_b32 s22, s23, 4\n"
      ".Lsl_vy0_18:\n"
      "ds_read_b32 v22, v24\n"
      "s_waitcnt lgkmcnt(0)\n"
      "v_cmp_gt_i32 vcc, v21, v22\n"
      "s_cbranch_vccz .Lbk_vy0_18\n"
      "s_sub_u32 s22, s22, 1\n"
      "s_cbranch_scc0 .Lsl_vy0_18\n"
      "s_mov_b32 s23, 0\n"
      "s_branch .Lbk_vy0_18\n"
      ".Lsp_v0_19:\n"
      "s_mov_b32 s22, s23\n"
      "buffer_load_dword v28, v11, s[8:11], s46 offen sc1\n"
      "s_waitcnt vmcnt(0)\n"
      "v_cmp_ne_u32 vcc, s21, v28\n"
      "s_add_u32 s30, s50, 24576\n"
      "buffer_store_dword v25, v12, s[8:11], s30 offen sc1\n"
      "s_cbranch_vccz .Lbk_v0_19\n"
      ".Lsl_v0_19:\n"
      "buffer_load_dword v28, v11, s[8:11], s46 offen sc1\n"
      "s_waitcnt vmcnt(0)\n"
      "v_cmp_ne_u32 vcc, s21, v28\n"
      "s_cbranch_vccz .Lbk_v0_19\n"
      "s_sleep 4\n"
      "s_sub_u32 s22, s22, 1\n"
      "s_cbranch_scc0 .Lsl_v0_19\n"
      "s_mov_b32 s23, 0\n"
      "s_branch .Lbk_v0_19\n"
      ".Lsp_vz0_20:\n"
      "s_lshl_b32 s22, s23, 4\n"
      ".Lsl_vz0_20:\n"
      "ds_read_b32 v22, v24\n"
      "s_waitcnt lgkmcnt(0)\n"
      "v_cmp_gt_i32 vcc, v21, v22\n"
      "s_cbranch_vccz .Lbk_vz0_20\n"
      "s_sub_u32 s22, s22, 1\n"
      "s_cbranch_scc0 .Lsl_vz0_20\n"
      "s_mov_b32 s23, 0\n"
      "s_branch .Lbk_vz0_20\n"
      ".Lsp_vy1_21:\n"
      "s_lshl_b32 s22, s23, 4\n"
      ".Lsl_vy1_21:\n"
      "ds_read_b32 v22, v24\n"
      "s_waitcnt lgkmcnt(0)\n"
      "v_cmp_gt_i32 vcc, v21, v22\n"
      "s_cbranch_vccz .Lbk_vy1_21\n"
      "s_sub_u32 s22, s22, 1\n"
      "s_cbranch_scc0 .Lsl_vy1_21\n"
      "s_mov_b32 s23, 0\n"
      "s_branch .Lbk_vy1_21\n"
      ".Lsp_v1_22:\n"
      "s_mov_b32 s22, s23\n"
      "buffer_load_dword v28, v11, s[8:11], s46 offen sc1\n"
      "s_waitcnt vmcnt(0)\n"
      "v_cmp_ne_u32 vcc, s21, v28\n"
      "s_add_u32 s30, s50, 24576\n"
      "buffer_store_dword v25, v12, s[8:11], s30 offen sc1\n"
      "s_cbranch_vccz .Lbk_v1_22\n"
      ".Lsl_v1_22:\n"
      "buffer_load_dword v28, v11, s[8:11], s46 offen sc1\n"
      "s_waitcnt vmcnt(0)\n"
      "v_cmp_ne_u32 vcc, s21, v28\n"
      "s_cbranch_vccz .Lbk_v1_22\n"
      "s_sleep 4\n"
      "s_sub_u32 s22, s22, 1\n"
      "s_cbranch_scc0 .Lsl_v1_22\n"
      "s_mov_b32 s23, 0\n"
      "s_branch .Lbk_v1_22\n"
      ".Lsp_vz1_23:\n"
      "s_lshl_b32 s22, s23, 4\n"
      ".Lsl_vz1_23:\n"
      "ds_read_b32 v22, v24\n"
      "s_waitcnt lgkmcnt(0)\n"
      "v_cmp_gt_i32 vcc, v21, v22\n"
      "s_cbranch_vccz .Lbk_vz1_23\n"
      "s_sub_u32 s22, s22, 1\n"
      "s_cbranch_scc0 .Lsl_vz1_23\n"
      "s_mov_b32 s23, 0\n"
      "s_branch .Lbk_vz1_23\n"
      ".Lsp_vy0_24:\n"
      "s_lshl_b32 s22, s23, 4\n"
      ".Lsl_vy0_24:\n"
      "ds_read_b32 v22, v24\n"
      "s_waitcnt lgkmcnt(0)\n"
      "v_cmp_gt_i32 vcc, v21, v22\n"
      "s_cbranch_vccz .Lbk_vy0_24\n"
      "s_sub_u32 s22, s22, 1\n"
      "s_cbranch_scc0 .Lsl_vy0_24\n"
      "s_mov_b32 s23, 0\n"
      "s_branch .Lbk_vy0_24\n"
      ".Lsp_v0_25:\n"
      "s_mov_b32 s22, s23\n"
      "buffer_load_dword v28, v11, s[8:11], s46 offen sc1\n"
      "s_waitcnt vmcnt(0)\n"
      "v_cmp_ne_u32 vcc, s21, v28\n"
      "s_add_u32 s30, s50, 24576\n"
      "buffer_store_dword v25, v12, s[8:11], s30 offen sc1\n"
      "s_cbranch_vccz .Lbk_v0_25\n"
      ".Lsl_v0_25:\n"
      "buffer_load_dword v28, v11, s[8:11], s46 offen sc1\n"
      "s_waitcnt vmcnt(0)\n"
      "v_cmp_ne_u32 vcc, s21, v28\n"
      "s_cbranch_vccz .Lbk_v0_25\n"
      "s_sleep 4\n"
      "s_sub_u32 s22, s22, 1\n"
      "s_cbranch_scc0 .Lsl_v0_25\n"
      "s_mov_b32 s23, 0\n"
      "s_branch .Lbk_v0_25\n"
      ".Lsp_vz0_26:\n"
      "s_lshl_b32 s22, s23, 4\n"
      ".Lsl_vz0_26:\n"
      "ds_read_b32 v22, v24\n"
      "s_waitcnt lgkmcnt(0)\n"
      "v_cmp_gt_i32 vcc, v21, v22\n"
      "s_cbranch_vccz .Lbk_vz0_26\n"
      "s_sub_u32 s22, s22, 1\n"
      "s_cbranch_scc0 .Lsl_vz0_26\n"
      "s_mov_b32 s23, 0\n"
      "s_branch .Lbk_vz0_26\n"
      ".Lsp_vy1_27:\n"
      "s_lshl_b32 s22, s23, 4\n"
      ".Lsl_vy1_27:\n"
      "ds_read_b32 v22, v24\n"
      "s_waitcnt lgkmcnt(0)\n"
      "v_cmp_gt_i32 vcc, v21, v22\n"
      "s_cbranch_vccz .Lbk_vy1_27\n"
      "s_sub_u32 s22, s22, 1\n"
      "s_cbranch_scc0 .Lsl_vy1_27\n"
      "s_mov_b32 s23, 0\n"
      "s_branch .Lbk_vy1_27\n"
      ".Lsp_v1_28:\n"
      "s_mov_b32 s22, s23\n"
      "buffer_load_dword v28, v11, s[8:11], s46 offen sc1\n"
      "s_waitcnt vmcnt(0)\n"
      "v_cmp_ne_u32 vcc, s21, v28\n"
      "s_add_u32 s30, s50, 24576\n"
      "buffer_store_dword v25, v12, s[8:11], s30 offen sc1\n"
      "s_cbranch_vccz .Lbk_v1_28\n"
      ".Lsl_v1_28:\n"
      "buffer_load_dword v28, v11, s[8:11], s46 offen sc1\n"
      "s_waitcnt vmcnt(0)\n"
      "v_cmp_ne_u32 vcc, s21, v28\n"
      "s_cbranch_vccz .Lbk_v1_28\n"
      "s_sleep 4\n"
      "s_sub_u32 s22, s22, 1\n"
      "s_cbranch_scc0 .Lsl_v1_28\n"
      "s_mov_b32 s23, 0\n"
      "s_branch .Lbk_v1_28\n"
      ".Lsp_vz1_29:\n"
      "s_lshl_b32 s22, s23, 4\n"
      ".Lsl_vz1_29:\n"
      "ds_read_b32 v22, v24\n"
      "s_waitcnt lgkmcnt(0)\n"
      "v_cmp_gt_i32 vcc, v21, v22\n"
      "s_cbranch_vccz .Lbk_vz1_29\n"
      "s_sub_u32 s22, s22, 1\n"
      "s_cbranch_scc0 .Lsl_vz1_29\n"
      "s_mov_b32 s23, 0\n"
      "s_branch .Lbk_vz1_29\n"
      ".Lsp_vy0_30:\n"
      "s_lshl_b32 s22, s23, 4\n"
      ".Lsl_vy0_30:\n"
      "ds_read_b32 v22, v24\n"
      "s_waitcnt lgkmcnt(0)\n"
      "v_cmp_gt_i32 vcc, v21, v22\n"
      "s_cbranch_vccz .Lbk_vy0_30\n"
      "s_sub_u32 s22, s22, 1\n"
      "s_cbranch_scc0 .Lsl_vy0_30\n"
      "s_mov_b32 s23, 0\n"
      "s_branch .Lbk_vy0_30\n"
      ".Lw_last:\n"
      :
      : "{s[8:11]}"(rsWS), "{s[12:15]}"(rsA), "{v0}"(v_l16), "{v1}"(v_l5), "{v2}"(v_ex[0]), "{v3}"(v_ex[1]), "{v4}"(v_ex[2]), "{v5}"(v_ex[3]), "{v6}"(v_ex[4]), "{v7}"(v_ex[5]), "{v8}"(v_ex[6]), "{v9}"(v_ex[7]), "{v10}"(v_l2), "{v11}"(v_fa), "{v12}"(v_ra), "{v13}"(v_rp), "{v14}"(v_rs), "{v15}"(v_xp), "{v16}"(v_xs), "{v17}"(v_ew), "{v18}"(v_sw), "{v19}"(v_sr), "{v20}"(v_ss), "{v22}"(v_al), "{v23}"(v_yw), "{v24}"(v_yr), "{v26}"(v_hi), "{v100}"(v_vo[0]), "{v101}"(v_vo[1]), "{v102}"(v_vo[2]), "{v103}"(v_vo[3]), "{v104}"(v_vo[4]), "{v105}"(v_vo[5]), "{v106}"(v_vo[6]), "{v107}"(v_vo[7]), "{v108}"(v_vo[8]), "{v109}"(v_vp[0]), "{v110}"(v_vp[1]), "{v111}"(v_vp[2]), "{v112}"(v_vp[3]), "{v113}"(v_vp[4]), "{v114}"(v_vp[5]), "{v115}"(v_vp[6]), "{v116}"(v_vp[7]), "{v117}"(v_vp[8]), "{v118}"(v_t0), "{v119}"(v_t1), "{v120}"(v_ts), "{v121}"(v_pw), "{s36}"(s_sx), "{s37}"(s_ev), "{s38}"(s_sy), "{s39}"(s_m4), "{s40}"(s_m5), "{s41}"(s_m9), "{s42}"(s_so), "{s43}"(s_s8), "{s44}"(s_wg), "{s45}"(s_rb)
      : "v255", "a255", "s99", "vcc", "scc", "memory");

  } else if (fast && slot < 28) {
    const unsigned td = (slot - 10) / 3, gr = td < 4 ? td : (e + 5 - td) & 3, part = (td < 4 ? e : td) * 3 + (slot - 10) % 3;
    COMMON(40, 149760);
    const unsigned ks = part / 3, n3 = part % 3;
    const u32x4 rsA = srd(Wd, 4608 * 576 * 2), rsB = srd(out, 2048 * 576 * 2), rsC = srd(X, 2048 * 576 * 2);
    const unsigned v_l16b = v_l16 + 4096, v_l8 = 1024 + lane * 8, f0 = 24 * ks + 6 * w;
    const unsigned v_fa = 147456 - f0 * 1024 + (f0 + (l8 < 5 ? l8 : 5)) * 128 + l7 / 4, v_ga = (l8 < 4 ? l8 : 4) * 4 * PW + 1536 + l7 / 4;
    const unsigned v_ra = up * 6 * PREC + l5 + 1536, v_rf = up * 33 * REC + l5 + 20480 + (4 * n3 + w) * 128;
    const unsigned v_pw = l8 < 5 ? (l8 + 7) * PREC + l8 * 4 * PW + 1536 + l7 : OOB, v_pu = ks && l8 + ks < 5 ? OOB : v_pw - 6 * PREC;
    const unsigned pf = 3 * (w >> 1) + 2 * (w & 1), sf = 3 * (w >> 1) + 1;
    const unsigned v_at = (8 * g + q) * 384 + 64 * pf + 16 * p, v_as = (8 * g + q) * 384 + 64 * sf + 32 * (w & 1) + 8 * p;
    auto vo = [&](unsigned i) { const unsigned c = 64 * i + lane; return (8 * w + c / 24) * 1152 + 384 * n3 + c % 24 * 16 - 1024 * i; };
    const unsigned v_vo0 = vo(0), v_vo1 = vo(1), v_vo2 = vo(2);
    const unsigned v_xp = (6 * n3 + pf) * 1024 + v_l16, v_xs = (6 * n3 + sf) * 1024 + (w & 1) * 512 + half;
    const unsigned v_op = i16 * 1152 + 384 * n3 + 64 * pf + 16 * g, v_os = i16 * 1152 + 384 * n3 + 64 * sf + 32 * (w & 1) + 8 * g;
    const unsigned v_sw = 147456 + i16 * 136 + (4 * w + g) * 8, v_sr = 147456 + (4 * w + g) * 136 + i16 * 8;
    const unsigned v_ss = 18432 + n3 * 512 + w * 128 + g * 32 + p * 8;
    const unsigned s_ph = ks ? 6 - ks : 0, s_p0 = 0u - s_ph, r0 = gr * NREC + s_p0;
    const unsigned s_sh = WS_H + r0 * HREC + f0 * 1024, s_ps = WS_PS + r0 * PREC + (n3 * 20 + w) * PW;
    const unsigned s_x1 = WS_X1 + r0 * REC, s_xr = WS_XIN + (r0 + 33) * REC, s_or = (32 * gr + 63 - s_p0) * 18432;
    const unsigned s_k0 = ROT * 6 * gr;
    const unsigned s_m3 = w * 3072, s_m6 = w * 6144, s_so = ks * 768 * 1152;
    const unsigned s_i0 = (32 * gr + 31 - ks) * 18432, s_i5 = ks < 2 ? s_i0 - 30 * 18432 : OOB;
    const unsigned s_j0 = WS_XIN + (gr * NREC + ks) * REC, s_j5 = ks < 2 ? s_j0 + 30 * REC : OOB, s_he = s_sh + (63 + s_ph) * HREC;
    auto pe = [&](unsigned i) { return ks == 3 - i ? OOB : s_ps + (63 - i + s_ph) * PREC + ((s_ph + 8 - i) % 6) * 4 * PW; };
    const unsigned s_pe[3] = {pe(0), pe(1), pe(2)};
    asm volatile(
      "s_buffer_load_dword s21, s[8:11], 0\n"
      "s_mov_b32 s23, 65536\n"
      "s_mov_b32 s31, 0x40000000\n"
      "buffer_load_dwordx4 v[56:59], v8, s[92:95], s46 offen\n"
      "buffer_load_dwordx2 v[60:61], v9, s[92:95], s46 offen\n"
      "s_waitcnt lgkmcnt(0)\n"
      "s_mul_i32 s21, s21, 1664525\n"
      "s_add_u32 s21, s21, 1013904223\n"
      "v_mov_b32 v18, s21\n"
      "v_mov_b32 v21, v18\n"
      "v_not_b32 v224, v18\n"
      "v_not_b32 v225, v18\n"
      "v_not_b32 v226, v18\n"
      "v_not_b32 v227, v18\n"
      "v_xor_b32 v18, v18, v19\n"
      "s_waitcnt vmcnt(0)\n"
      "buffer_store_dwordx4 v[56:59], v6, s[8:11], s48 offen sc1\n"
      "buffer_store_dwordx2 v[60:61], v7, s[8:11], s48 offen sc1\n"
      "v_lshlrev_b32 v164, 16, v56\n"
      "v_and_b32 v165, 0xffff0000, v56\n"
      "v_lshlrev_b32 v166, 16, v57\n"
      "v_and_b32 v167, 0xffff0000, v57\n"
      "v_lshlrev_b32 v168, 16, v58\n"
      "v_and_b32 v169, 0xffff0000, v58\n"
      "v_lshlrev_b32 v170, 16, v59\n"
      "v_and_b32 v171, 0xffff0000, v59\n"
      "v_lshlrev_b32 v172, 16, v60\n"
      "v_and_b32 v173, 0xffff0000, v60\n"
      "v_lshlrev_b32 v174, 16, v61\n"
      "v_and_b32 v175, 0xffff0000, v61\n"
      "v_add_f32 v62, v164, v165\n"
      "v_mul_f32 v63, v164, v164\n"
      "v_fmac_f32 v63, v165, v165\n"
      "v_add_f32 v62, v62, v166\n"
      "v_fmac_f32 v63, v166, v166\n"
      "v_add_f32 v62, v62, v167\n"
      "v_fmac_f32 v63, v167, v167\n"
      "v_add_f32 v62, v62, v168\n"
      "v_fmac_f32 v63, v168, v168\n"
      "v_add_f32 v62, v62, v169\n"
      "v_fmac_f32 v63, v169, v169\n"
      "v_add_f32 v62, v62, v170\n"
      "v_fmac_f32 v63, v170, v170\n"
      "v_add_f32 v62, v62, v171\n"
      "v_fmac_f32 v63, v171, v171\n"
      "v_add_f32 v62, v62, v172\n"
      "v_fmac_f32 v63, v172, v172\n"
      "v_add_f32 v62, v62, v173\n"
      "v_fmac_f32 v63, v173, v173\n"
      "v_add_f32 v62, v62, v174\n"
      "v_fmac_f32 v63, v174, v174\n"
      "v_add_f32 v62, v62, v175\n"
      "v_fmac_f32 v63, v175, v175\n"
      "ds_write_b64 v10, v[62:63] offset:0\n"
      "s_waitcnt lgkmcnt(0)\n"
      "s_barrier\n"
      "ds_read_b64 v[152:153], v11 offset:0\n"
      "s_waitcnt lgkmcnt(0)\n"
      "v_add_f32_dpp v152, v152, v152 row_ror:8 row_mask:0xf bank_mask:0xf\n"
      "v_add_f32_dpp v153, v153, v153 row_ror:8 row_mask:0xf bank_mask:0xf\n"
      "s_nop 0\n"
      "v_add_f32_dpp v152, v152, v152 row_ror:4 row_mask:0xf bank_mask:0xf\n"
      "v_add_f32_dpp v153, v153, v153 row_ror:4 row_mask:0xf bank_mask:0xf\n"
      "s_nop 0\n"
      "v_add_f32_dpp v152, v152, v152 row_ror:2 row_mask:0xf bank_mask:0xf\n"
      "v_add_f32_dpp v153, v153, v153 row_ror:2 row_mask:0xf bank_mask:0xf\n"
      "s_nop 0\n"
      "v_add_f32_dpp v152, v152, v152 row_ror:1 row_mask:0xf bank_mask:0xf\n"
      "v_add_f32_dpp v153, v153, v153 row_ror:1 row_mask:0xf bank_mask:0xf\n"
      "v_mul_f32 v152, 0x3ae38e39, v152\n"
      "v_add_f32 v153, 0x3afba882, v153\n"
      "v_mul_f32 v153, 0x3ae38e39, v153\n"
      "buffer_store_dwordx2 v[152:153], v12, s[8:11], s48 offen sc1\n"
      "s_add_u32 s30, s46, 4294856704\n"
      "buffer_load_dwordx4 v[64:67], v8, s[92:95], s30 offen\n"
      "buffer_load_dwordx2 v[68:69], v9, s[92:95], s30 offen\n"
      "s_add_u32 s30, s46, 4294746112\n"
      "buffer_load_dwordx4 v[72:75], v8, s[92:95], s30 offen\n"
      "buffer_load_dwordx2 v[76:77], v9, s[92:95], s30 offen\n"
      "s_waitcnt vmcnt(4)\n"
      "buffer_store_dword v18, v5, s[8:11], s48 offen sc1\n"
      "s_add_u32 s30, s46, 4294635520\n"
      "buffer_load_dwordx4 v[80:83], v8, s[92:95], s30 offen\n"
      "buffer_load_dwordx2 v[84:85], v9, s[92:95], s30 offen\n"
      "s_add_u32 s30, s46, 4294524928\n"
      "buffer_load_dwordx4 v[88:91], v8, s[92:95], s30 offen\n"
      "buffer_load_dwordx2 v[92:93], v9, s[92:95], s30 offen\n"
      "buffer_load_dwordx4 v[96:99], v8, s[92:95], s47 offen\n"
      "buffer_load_dwordx2 v[100:101], v9, s[92:95], s47 offen\n"
      "buffer_store_dword v18, v15, s[8:11], 0 offen sc1\n"
      "s_cmp_eq_u32 s45, 0\n"
      "s_cselect_b32 s30, 0x40000000, s37\n"
      "buffer_store_dword v21, v3, s[8:11], s30 offen sc1\n"
      "s_waitcnt vmcnt(11)\n"
      "s_add_u32 s30, s48, 147456\n"
      "buffer_store_dwordx4 v[64:67], v6, s[8:11], s30 offen sc1\n"
      "buffer_store_dwordx2 v[68:69], v7, s[8:11], s30 offen sc1\n"
      "v_lshlrev_b32 v164, 16, v64\n"
      "v_and_b32 v165, 0xffff0000, v64\n"
      "v_lshlrev_b32 v166, 16, v65\n"
      "v_and_b32 v167, 0xffff0000, v65\n"
      "v_lshlrev_b32 v168, 16, v66\n"
      "v_and_b32 v169, 0xffff0000, v66\n"
      "v_lshlrev_b32 v170, 16, v67\n"
      "v_and_b32 v171, 0xffff0000, v67\n"
      "v_lshlrev_b32 v172, 16, v68\n"
      "v_and_b32 v173, 0xffff0000, v68\n"
      "v_lshlrev_b32 v174, 16, v69\n"
      "v_and_b32 v175, 0xffff0000, v69\n"
      "v_add_f32 v70, v164, v165\n"
      "v_mul_f32 v71, v164, v164\n"
      "v_fmac_f32 v71, v165, v165\n"
      "v_add_f32 v70, v70, v166\n"
      "v_fmac_f32 v71, v166, v166\n"
      "v_add_f32 v70, v70, v167\n"
      "v_fmac_f32 v71, v167, v167\n"
      "v_add_f32 v70, v70, v168\n"
      "v_fmac_f32 v71, v168, v168\n"
      "v_add_f32 v70, v70, v169\n"
      "v_fmac_f32 v71, v169, v169\n"
      "v_add_f32 v70, v70, v170\n"
      "v_fmac_f32 v71, v170, v170\n"
      "v_add_f32 v70, v70, v171\n"
      "v_fmac_f32 v71, v171, v171\n"
      "v_add_f32 v70, v70, v172\n"
      "v_fmac_f32 v71, v172, v172\n"
      "v_add_f32 v70, v70, v173\n"
      "v_fmac_f32 v71, v173, v173\n"
      "v_add_f32 v70, v70, v174\n"
      "v_fmac_f32 v71, v174, v174\n"
      "v_add_f32 v70, v70, v175\n"
      "v_fmac_f32 v71, v175, v175\n"
      "ds_write_b64 v10, v[70:71] offset:2176\n"
      "s_waitcnt vmcnt(11)\n"
      "s_add_u32 s30, s48, 294912\n"
      "buffer_store_dwordx4 v[72:75], v6, s[8:11], s30 offen sc1\n"
      "buffer_store_dwordx2 v[76:77], v7, s[8:11], s30 offen sc1\n"
      "v_lshlrev_b32 v164, 16, v72\n"
      "v_and_b32 v165, 0xffff0000, v72\n"
      "v_lshlrev_b32 v166, 16, v73\n"
      "v_and_b32 v167, 0xffff0000, v73\n"
      "v_lshlrev_b32 v168, 16, v74\n"
      "v_and_b32 v169, 0xffff0000, v74\n"
      "v_lshlrev_b32 v170, 16, v75\n"
      "v_and_b32 v171, 0xffff0000, v75\n"
      "v_lshlrev_b32 v172, 16, v76\n"
      "v_and_b32 v173, 0xffff0000, v76\n"
      "v_lshlrev_b32 v174, 16, v77\n"
      "v_and_b32 v175, 0xffff0000, v77\n"
      "v_add_f32 v78, v164, v165\n"
      "v_mul_f32 v79, v164, v164\n"
      "v_fmac_f32 v79, v165, v165\n"
      "v_add_f32 v78, v78, v166\n"
      "v_fmac_f32 v79, v166, v166\n"
      "v_add_f32 v78, v78, v167\n"
      "v_fmac_f32 v79, v167, v167\n"
      "v_add_f32 v78, v78, v168\n"
      "v_fmac_f32 v79, v168, v168\n"
      "v_add_f32 v78, v78, v169\n"
      "v_fmac_f32 v79, v169, v169\n"
      "v_add_f32 v78, v78, v170\n"
      "v_fmac_f32 v79, v170, v170\n"
      "v_add_f32 v78, v78, v171\n"
      "v_fmac_f32 v79, v171, v171\n"
      "v_add_f32 v78, v78, v172\n"
      "v_fmac_f32 v79, v172, v172\n"
      "v_add_f32 v78, v78, v173\n"
      "v_fmac_f32 v79, v173, v173\n"
      "v_add_f32 v78, v78, v174\n"
      "v_fmac_f32 v79, v174, v174\n"
      "v_add_f32 v78, v78, v175\n"
      "v_fmac_f32 v79, v175, v175\n"
      "ds_write_b64 v10, v[78:79] offset:4352\n"
      "s_waitcnt vmcnt(10)\n"
      "s_add_u32 s30, s48, 442368\n"
      "buffer_store_dwordx4 v[80:83], v6, s[8:11], s30 offen sc1\n"
      "buffer_store_dwordx2 v[84:85], v7, s[8:11], s30 offen sc1\n"
      "v_lshlrev_b32 v164, 16, v80\n"
      "v_and_b32 v165, 0xffff0000, v80\n"
      "v_lshlrev_b32 v166, 16, v81\n"
      "v_and_b32 v167, 0xffff0000, v81\n"
      "v_lshlrev_b32 v168, 16, v82\n"
      "v_and_b32 v169, 0xffff0000, v82\n"
      "v_lshlrev_b32 v170, 16, v83\n"
      "v_and_b32 v171, 0xffff0000, v83\n"
      "v_lshlrev_b32 v172, 16, v84\n"
      "v_and_b32 v173, 0xffff0000, v84\n"
      "v_lshlrev_b32 v174, 16, v85\n"
      "v_and_b32 v175, 0xffff0000, v85\n"
      "v_add_f32 v86, v164, v165\n"
      "v_mul_f32 v87, v164, v164\n"
      "v_fmac_f32 v87, v165, v165\n"
      "v_add_f32 v86, v86, v166\n"
      "v_fmac_f32 v87, v166, v166\n"
      "v_add_f32 v86, v86, v167\n"
      "v_fmac_f32 v87, v167, v167\n"
      "v_add_f32 v86, v86, v168\n"
      "v_fmac_f32 v87, v168, v168\n"
      "v_add_f32 v86, v86, v169\n"
      "v_fmac_f32 v87, v169, v169\n"
      "v_add_f32 v86, v86, v170\n"
      "v_fmac_f32 v87, v170, v170\n"
      "v_add_f32 v86, v86, v171\n"
      "v_fmac_f32 v87, v171, v171\n"
      "v_add_f32 v86, v86, v172\n"
      "v_fmac_f32 v87, v172, v172\n"
      "v_add_f32 v86, v86, v173\n"
      "v_fmac_f32 v87, v173, v173\n"
      "v_add_f32 v86, v86, v174\n"
      "v_fmac_f32 v87, v174, v174\n"
      "v_add_f32 v86, v86, v175\n"
      "v_fmac_f32 v87, v175, v175\n"
      "ds_write_b64 v10, v[86:87] offset:6528\n"
      "s_waitcnt vmcnt(10)\n"
      "s_add_u32 s30, s48, 589824\n"
      "buffer_store_dwordx4 v[88:91], v6, s[8:11], s30 offen sc1\n"
      "buffer_store_dwordx2 v[92:93], v7, s[8:11], s30 offen sc1\n"
      "v_lshlrev_b32 v164, 16, v88\n"
      "v_and_b32 v165, 0xffff0000, v88\n"
      "v_lshlrev_b32 v166, 16, v89\n"
      "v_and_b32 v167, 0xffff0000, v89\n"
      "v_lshlrev_b32 v168, 16, v90\n"
      "v_and_b32 v169, 0xffff0000, v90\n"
      "v_lshlrev_b32 v170, 16, v91\n"
      "v_and_b32 v171, 0xffff0000, v91\n"
      "v_lshlrev_b32 v172, 16, v92\n"
      "v_and_b32 v173, 0xffff0000, v92\n"
      "v_lshlrev_b32 v174, 16, v93\n"
      "v_and_b32 v175, 0xffff0000, v93\n"
      "v_add_f32 v94, v164, v165\n"
      "v_mul_f32 v95, v164, v164\n"
      "v_fmac_f32 v95, v165, v165\n"
      "v_add_f32 v94, v94, v166\n"
      "v_fmac_f32 v95, v166, v166\n"
      "v_add_f32 v94, v94, v167\n"
      "v_fmac_f32 v95, v167, v167\n"
      "v_add_f32 v94, v94, v168\n"
      "v_fmac_f32 v95, v168, v168\n"
      "v_add_f32 v94, v94, v169\n"
      "v_fmac_f32 v95, v169, v169\n"
      "v_add_f32 v94, v94, v170\n"
      "v_fmac_f32 v95, v170, v170\n"
      "v_add_f32 v94, v94, v171\n"
      "v_fmac_f32 v95, v171, v171\n"
      "v_add_f32 v94, v94, v172\n"
      "v_fmac_f32 v95, v172, v172\n"
      "v_add_f32 v94, v94, v173\n"
      "v_fmac_f32 v95, v173, v173\n"
      "v_add_f32 v94, v94, v174\n"
      "v_fmac_f32 v95, v174, v174\n"
      "v_add_f32 v94, v94, v175\n"
      "v_fmac_f32 v95, v175, v175\n"
      "ds_write_b64 v10, v[94:95] offset:8704\n"
      "s_waitcnt vmcnt(10)\n"
      "buffer_store_dwordx4 v[96:99], v6, s[8:11], s49 offen sc1\n"
      "buffer_store_dwordx2 v[100:101], v7, s[8:11], s49 offen sc1\n"
      "v_lshlrev_b32 v164, 16, v96\n"
      "v_and_b32 v165, 0xffff0000, v96\n"
      "v_lshlrev_b32 v166, 16, v97\n"
      "v_and_b32 v167, 0xffff0000, v97\n"
      "v_lshlrev_b32 v168, 16, v98\n"
      "v_and_b32 v169, 0xffff0000, v98\n"
      "v_lshlrev_b32 v170, 16, v99\n"
      "v_and_b32 v171, 0xffff0000, v99\n"
      "v_lshlrev_b32 v172, 16, v100\n"
      "v_and_b32 v173, 0xffff0000, v100\n"
      "v_lshlrev_b32 v174, 16, v101\n"
      "v_and_b32 v175, 0xffff0000, v101\n"
      "v_add_f32 v102, v164, v165\n"
      "v_mul_f32 v103, v164, v164\n"
      "v_fmac_f32 v103, v165, v165\n"
      "v_add_f32 v102, v102, v166\n"
      "v_fmac_f32 v103, v166, v166\n"
      "v_add_f32 v102, v102, v167\n"
      "v_fmac_f32 v103, v167, v167\n"
      "v_add_f32 v102, v102, v168\n"
      "v_fmac_f32 v103, v168, v168\n"
      "v_add_f32 v102, v102, v169\n"
      "v_fmac_f32 v103, v169, v169\n"
      "v_add_f32 v102, v102, v170\n"
      "v_fmac_f32 v103, v170, v170\n"
      "v_add_f32 v102, v102, v171\n"
      "v_fmac_f32 v103, v171, v171\n"
      "v_add_f32 v102, v102, v172\n"
      "v_fmac_f32 v103, v172, v172\n"
      "v_add_f32 v102, v102, v173\n"
      "v_fmac_f32 v103, v173, v173\n"
      "v_add_f32 v102, v102, v174\n"
      "v_fmac_f32 v103, v174, v174\n"
      "v_add_f32 v102, v102, v175\n"
      "v_fmac_f32 v103, v175, v175\n"
      "ds_write_b64 v10, v[102:103] offset:10880\n"
      "s_waitcnt lgkmcnt(0)\n"
      "s_barrier\n"
      "ds_read_b64 v[154:155], v11 offset:2176\n"
      "ds_read_b64 v[156:157], v11 offset:4352\n"
      "ds_read_b64 v[158:159], v11 offset:6528\n"
      "ds_read_b64 v[160:161], v11 offset:8704\n"
      "ds_read_b64 v[162:163], v11 offset:10880\n"
      "s_waitcnt lgkmcnt(0)\n"
      "v_add_f32_dpp v154, v154, v154 row_ror:8 row_mask:0xf bank_mask:0xf\n"
      "v_add_f32_dpp v155, v155, v155 row_ror:8 row_mask:0xf bank_mask:0xf\n"
      "v_add_f32_dpp v156, v156, v156 row_ror:8 row_mask:0xf bank_mask:0xf\n"
      "v_add_f32_dpp v157, v157, v157 row_ror:8 row_mask:0xf bank_mask:0xf\n"
      "v_add_f32_dpp v158, v158, v158 row_ror:8 row_mask:0xf bank_mask:0xf\n"
      "v_add_f32_dpp v159, v159, v159 row_ror:8 row_mask:0xf bank_mask:0xf\n"
      "v_add_f32_dpp v160, v160, v160 row_ror:8 row_mask:0xf bank_mask:0xf\n"
      "v_add_f32_dpp v161, v161, v161 row_ror:8 row_mask:0xf bank_mask:0xf\n"
      "v_add_f32_dpp v162, v162, v162 row_ror:8 row_mask:0xf bank_mask:0xf\n"
      "v_add_f32_dpp v163, v163, v163 row_ror:8 row_mask:0xf bank_mask:0xf\n"
      "v_add_f32_dpp v154, v154, v154 row_ror:4 row_mask:0xf bank_mask:0xf\n"
      "v_add_f32_dpp v155, v155, v155 row_ror:4 row_mask:0xf bank_mask:0xf\n"
      "v_add_f32_dpp v156, v156, v156 row_ror:4 row_mask:0xf bank_mask:0xf\n"
      "v_add_f32_dpp v157, v157, v157 row_ror:4 row_mask:0xf bank_mask:0xf\n"
      "v_add_f32_dpp v158, v158, v158 row_ror:4 row_mask:0xf bank_mask:0xf\n"
      "v_add_f32_dpp v159, v159, v159 row_ror:4 row_mask:0xf bank_mask:0xf\n"
      "v_add_f32_dpp v160, v160, v160 row_ror:4 row_mask:0xf bank_mask:0xf\n"
      "v_add_f32_dpp v161, v161, v161 row_ror:4 row_mask:0xf bank_mask:0xf\n"
      "v_add_f32_dpp v162, v162, v162 row_ror:4 row_mask:0xf bank_mask:0xf\n"
      "v_add_f32_dpp v163, v163, v163 row_ror:4 row_mask:0xf bank_mask:0xf\n"
      "v_add_f32_dpp v154, v154, v154 row_ror:2 row_mask:0xf bank_mask:0xf\n"
      "v_add_f32_dpp v155, v155, v155 row_ror:2 row_mask:0xf bank_mask:0xf\n"
      "v_add_f32_dpp v156, v156, v156 row_ror:2 row_mask:0xf bank_mask:0xf\n"
      "v_add_f32_dpp v157, v157, v157 row_ror:2 row_mask:0xf bank_mask:0xf\n"
      "v_add_f32_dpp v158, v158, v158 row_ror:2 row_mask:0xf bank_mask:0xf\n"
      "v_add_f32_dpp v159, v159, v159 row_ror:2 row_mask:0xf bank_mask:0xf\n"
      "v_add_f32_dpp v160, v160, v160 row_ror:2 row_mask:0xf bank_mask:0xf\n"
      "v_add_f32_dpp v161, v161, v161 row_ror:2 row_mask:0xf bank_mask:0xf\n"
      "v_add_f32_dpp v162, v162, v162 row_ror:2 row_mask:0xf bank_mask:0xf\n"
      "v_add_f32_dpp v163, v163, v163 row_ror:2 row_mask:0xf bank_mask:0xf\n"
      "v_add_f32_dpp v154, v154, v154 row_ror:1 row_mask:0xf bank_mask:0xf\n"
      "v_add_f32_dpp v155, v155, v155 row_ror:1 row_mask:0xf bank_mask:0xf\n"
      "v_add_f32_dpp v156, v156, v156 row_ror:1 row_mask:0xf bank_mask:0xf\n"
      "v_add_f32_dpp v157, v157, v157 row_ror:1 row_mask:0xf bank_mask:0xf\n"
      "v_add_f32_dpp v158, v158, v158 row_ror:1 row_mask:0xf bank_mask:0xf\n"
      "v_add_f32_dpp v159, v159, v159 row_ror:1 row_mask:0xf bank_mask:0xf\n"
      "v_add_f32_dpp v160, v160, v160 row_ror:1 row_mask:0xf bank_mask:0xf\n"
      "v_add_f32_dpp v161, v161, v161 row_ror:1 row_mask:0xf bank_mask:0xf\n"
      "v_add_f32_dpp v162, v162, v162 row_ror:1 row_mask:0xf bank_mask:0xf\n"
      "v_add_f32_dpp v163, v163, v163 row_ror:1 row_mask:0xf bank_mask:0xf\n"
      "v_mul_f32 v154, 0x3ae38e39, v154\n"
      "v_add_f32 v155, 0x3afba882, v155\n"
      "v_mul_f32 v155, 0x3ae38e39, v155\n"
      "v_mul_f32 v156, 0x3ae38e39, v156\n"
      "v_add_f32 v157, 0x3afba882, v157\n"
      "v_mul_f32 v157, 0x3ae38e39, v157\n"
      "v_mul_f32 v158, 0x3ae38e39, v158\n"
      "v_add_f32 v159, 0x3afba882, v159\n"
      "v_mul_f32 v159, 0x3ae38e39, v159\n"
      "v_mul_f32 v160, 0x3ae38e39, v160\n"
      "v_add_f32 v161, 0x3afba882, v161\n"
      "v_mul_f32 v161, 0x3ae38e39, v161\n"
      "v_mul_f32 v162, 0x3ae38e39, v162\n"
      "v_add_f32 v163, 0x3afba882, v163\n"
      "v_mul_f32 v163, 0x3ae38e39, v163\n"
      "s_add_u32 s30, s48, 147456\n"
      "buffer_store_dwordx2 v[154:155], v12, s[8:11], s30 offen sc1\n"
      "s_add_u32 s30, s48, 294912\n"
      "buffer_store_dwordx2 v[156:157], v12, s[8:11], s30 offen sc1\n"
      "s_add_u32 s30, s48, 442368\n"
      "buffer_store_dwordx2 v[158:159], v12, s[8:11], s30 offen sc1\n"
      "s_add_u32 s30, s48, 589824\n"
      "buffer_store_dwordx2 v[160:161], v12, s[8:11], s30 offen sc1\n"
      "buffer_store_dwordx2 v[162:163], v12, s[8:11], s49 offen sc1\n"
      "s_waitcnt vmcnt(0)\n"
      "s_add_u32 s30, s48, 147456\n"
      "buffer_store_dword v18, v5, s[8:11], s30 offen sc1\n"
      "s_add_u32 s30, s48, 294912\n"
      "buffer_store_dword v18, v5, s[8:11], s30 offen sc1\n"
      "s_add_u32 s30, s48, 442368\n"
      "buffer_store_dword v18, v5, s[8:11], s30 offen sc1\n"
      "s_add_u32 s30, s48, 589824\n"
      "buffer_store_dword v18, v5, s[8:11], s30 offen sc1\n"
      "buffer_store_dword v18, v5, s[8:11], s49 offen sc1\n"
      "v_and_b32 v20, 0x40000000, v19\n"
      "v_or_b32 v5, v20, v5\n"
      "s_add_u32 s30, s37, 0\n"
      "buffer_store_dwordx4 v[224:227], v183, s[8:11], s30 offen sc1\n"
      "s_add_u32 s30, s37, 0\n"
      "buffer_store_dwordx4 v[224:227], v181, s[8:11], s30 offen sc1\n"
      "v_mov_b32 v216, -1\n"
      "v_mov_b32 v217, -1\n"
      "v_mov_b32 v218, -1\n"
      "v_mov_b32 v219, -1\n"
      "buffer_store_dwordx4 v[216:219], v0, s[8:11], s51 offen sc1\n"
      "buffer_store_dwordx2 v[216:217], v13, s[8:11], s51 offen sc1\n"
      "buffer_store_dwordx4 v[216:219], v0, s[8:11], s52 offen sc1\n"
      "buffer_store_dwordx2 v[216:217], v13, s[8:11], s52 offen sc1\n"
      "buffer_store_dwordx4 v[216:219], v0, s[8:11], s53 offen sc1\n"
      "buffer_store_dwordx2 v[216:217], v13, s[8:11], s53 offen sc1\n"
      "s_sleep 32\n"
      "s_waitcnt vmcnt(0) lgkmcnt(0)\n"
      "s_mov_b32 s64, s63\n"
      "s_mov_b32 s65, 6\n"
      ".Ld_r0:\n"
      "s_mul_i32 s66, s64, 36864\n"
      "s_add_u32 s66, s66, s44\n"
      "s_mul_i32 s30, s64, 8192\n"
      "s_lshr_b32 s30, s30, 16\n"
      "s_mul_i32 s30, s30, 8\n"
      "s_sub_u32 s30, s64, s30\n"
      "s_mul_i32 s30, s30, 12288\n"
      "s_add_u32 s30, s30, 0\n"
      "s_add_u32 m0, s42, s30\n"
      "s_add_u32 s64, s64, 1\n"
      "buffer_load_dwordx4 v178, s[12:15], s66 offen offset:0 lds\n"
      "buffer_load_dwordx4 v179, s[12:15], s66 offen offset:1024 lds\n"
      "buffer_load_dwordx4 v180, s[12:15], s66 offen offset:2048 lds\n"
      "s_cmp_eq_u32 s64, 24\n"
      "s_cselect_b32 s64, 0, s64\n"
      "s_sub_u32 s65, s65, 1\n"
      "s_cbranch_scc0 .Ld_r0\n"
      "v_add_u32 v20, 49152, v176\n"
      "v_add_u32 v21, 49152, v177\n"
      "v_add_u32 v22, 98304, v176\n"
      "v_add_u32 v23, 98304, v177\n"
      "s_mov_b32 s65, 23\n"
      "s_cmp_eq_u32 s63, 6\n"
      "s_cbranch_scc1 .Ld_rs6\n"
      "s_cmp_eq_u32 s63, 12\n"
      "s_cbranch_scc1 .Ld_rs12\n"
      "s_cmp_eq_u32 s63, 18\n"
      "s_cbranch_scc1 .Ld_rs18\n"
      ".Ld_rs0:\n"
      "s_add_u32 s66, s44, 258048\n"
      "s_cmp_ge_u32 s65, 7\n"
      "s_cselect_b32 s66, s66, s31\n"
      "s_add_u32 m0, s42, 86016\n"
      "s_waitcnt vmcnt(18) lgkmcnt(0)\n"
      "s_barrier\n"
      "ds_read_b64_tr_b16 a[0:1], v176 offset:0\n"
      "ds_read_b64_tr_b16 a[2:3], v176 offset:1536\n"
      "ds_read_b64_tr_b16 a[96:97], v176 offset:8\n"
      "ds_read_b64_tr_b16 a[98:99], v176 offset:1544\n"
      "ds_read_b64_tr_b16 a[192:193], v177 offset:0\n"
      "ds_read_b64_tr_b16 a[194:195], v177 offset:1536\n"
      "buffer_load_dwordx4 v178, s[12:15], s66 offen offset:0 lds\n"
      "buffer_load_dwordx4 v179, s[12:15], s66 offen offset:1024 lds\n"
      "buffer_load_dwordx4 v180, s[12:15], s66 offen offset:2048 lds\n"
      "s_sub_u32 s65, s65, 1\n"
      ".Ld_rs1:\n"
      "s_add_u32 s66, s44, 294912\n"
      "s_cmp_ge_u32 s65, 7\n"
      "s_cselect_b32 s66, s66, s31\n"
      "s_add_u32 m0, s42, 0\n"
      "s_waitcnt vmcnt(18) lgkmcnt(0)\n"
      "s_barrier\n"
      "ds_read_b64_tr_b16 a[4:5], v176 offset:12288\n"
      "ds_read_b64_tr_b16 a[6:7], v176 offset:13824\n"
      "ds_read_b64_tr_b16 a[100:101], v176 offset:12296\n"
      "ds_read_b64_tr_b16 a[102:103], v176 offset:13832\n"
      "ds_read_b64_tr_b16 a[196:197], v177 offset:12288\n"
      "ds_read_b64_tr_b16 a[198:199], v177 offset:13824\n"
      "buffer_load_dwordx4 v178, s[12:15], s66 offen offset:0 lds\n"
      "buffer_load_dwordx4 v179, s[12:15], s66 offen offset:1024 lds\n"
      "buffer_load_dwordx4 v180, s[12:15], s66 offen offset:2048 lds\n"
      "s_sub_u32 s65, s65, 1\n"
      ".Ld_rs2:\n"
      "s_add_u32 s66, s44, 331776\n"
      "s_cmp_ge_u32 s65, 7\n"
      "s_cselect_b32 s66, s66, s31\n"
      "s_add_u32 m0, s42, 12288\n"
      "s_waitcnt vmcnt(18) lgkmcnt(0)\n"
      "s_barrier\n"
      "ds_read_b64_tr_b16 a[8:9], v176 offset:24576\n"
      "ds_read_b64_tr_b16 a[10:11], v176 offset:26112\n"
      "ds_read_b64_tr_b16 a[104:105], v176 offset:24584\n"
      "ds_read_b64_tr_b16 a[106:107], v176 offset:26120\n"
      "ds_read_b64_tr_b16 a[200:201], v177 offset:24576\n"
      "ds_read_b64_tr_b16 a[202:203], v177 offset:26112\n"
      "buffer_load_dwordx4 v178, s[12:15], s66 offen offset:0 lds\n"
      "buffer_load_dwordx4 v179, s[12:15], s66 offen offset:1024 lds\n"
      "buffer_load_dwordx4 v180, s[12:15], s66 offen offset:2048 lds\n"
      "s_sub_u32 s65, s65, 1\n"
      ".Ld_rs3:\n"
      "s_add_u32 s66, s44, 368640\n"
      "s_cmp_ge_u32 s65, 7\n"
      "s_cselect_b32 s66, s66, s31\n"
      "s_add_u32 m0, s42, 24576\n"
      "s_waitcnt vmcnt(18) lgkmcnt(0)\n"
      "s_barrier\n"
      "ds_read_b64_tr_b16 a[12:13], v176 offset:36864\n"
      "ds_read_b64_tr_b16 a[14:15], v176 offset:38400\n"
      "ds_read_b64_tr_b16 a[108:109], v176 offset:36872\n"
      "ds_read_b64_tr_b16 a[110:111], v176 offset:38408\n"
      "ds_read_b64_tr_b16 a[204:205], v177 offset:36864\n"
      "ds_read_b64_tr_b16 a[206:207], v177 offset:38400\n"
      "buffer_load_dwordx4 v178, s[12:15], s66 offen offset:0 lds\n"
      "buffer_load_dwordx4 v179, s[12:15], s66 offen offset:1024 lds\n"
      "buffer_load_dwordx4 v180, s[12:15], s66 offen offset:2048 lds\n"
      "s_sub_u32 s65, s65, 1\n"
      ".Ld_rs4:\n"
      "s_add_u32 s66, s44, 405504\n"
      "s_cmp_ge_u32 s65, 7\n"
      "s_cselect_b32 s66, s66, s31\n"
      "s_add_u32 m0, s42, 36864\n"
      "s_waitcnt vmcnt(18) lgkmcnt(0)\n"
      "s_barrier\n"
      "ds_read_b64_tr_b16 a[16:17], v20 offset:0\n"
      "ds_read_b64_tr_b16 a[18:19], v20 offset:1536\n"
      "ds_read_b64_tr_b16 a[112:113], v20 offset:8\n"
      "ds_read_b64_tr_b16 a[114:115], v20 offset:1544\n"
      "ds_read_b64_tr_b16 a[208:209], v21 offset:0\n"
      "ds_read_b64_tr_b16 a[210:211], v21 offset:1536\n"
      "buffer_load_dwordx4 v178, s[12:15], s66 offen offset:0 lds\n"
      "buffer_load_dwordx4 v179, s[12:15], s66 offen offset:1024 lds\n"
      "buffer_load_dwordx4 v180, s[12:15], s66 offen offset:2048 lds\n"
      "s_sub_u32 s65, s65, 1\n"
      ".Ld_rs5:\n"
      "s_add_u32 s66, s44, 442368\n"
      "s_cmp_ge_u32 s65, 7\n"
      "s_cselect_b32 s66, s66, s31\n"
      "s_add_u32 m0, s42, 49152\n"
      "s_waitcnt vmcnt(18) lgkmcnt(0)\n"
      "s_barrier\n"
      "ds_read_b64_tr_b16 a[20:21], v20 offset:12288\n"
      "ds_read_b64_tr_b16 a[22:23], v20 offset:13824\n"
      "ds_read_b64_tr_b16 a[116:117], v20 offset:12296\n"
      "ds_read_b64_tr_b16 a[118:119], v20 offset:13832\n"
      "ds_read_b64_tr_b16 a[212:213], v21 offset:12288\n"
      "ds_read_b64_tr_b16 a[214:215], v21 offset:13824\n"
      "buffer_load_dwordx4 v178, s[12:15], s66 offen offset:0 lds\n"
      "buffer_load_dwordx4 v179, s[12:15], s66 offen offset:1024 lds\n"
      "buffer_load_dwordx4 v180, s[12:15], s66 offen offset:2048 lds\n"
      "s_sub_u32 s65, s65, 1\n"
      "s_cbranch_scc1 .Ld_re\n"
      ".Ld_rs6:\n"
      "s_add_u32 s66, s44, 479232\n"
      "s_cmp_ge_u32 s65, 7\n"
      "s_cselect_b32 s66, s66, s31\n"
      "s_add_u32 m0, s42, 61440\n"
      "s_waitcnt vmcnt(18) lgkmcnt(0)\n"
      "s_barrier\n"
      "ds_read_b64_tr_b16 a[24:25], v20 offset:24576\n"
      "ds_read_b64_tr_b16 a[26:27], v20 offset:26112\n"
      "ds_read_b64_tr_b16 a[120:121], v20 offset:24584\n"
      "ds_read_b64_tr_b16 a[122:123], v20 offset:26120\n"
      "ds_read_b64_tr_b16 a[216:217], v21 offset:24576\n"
      "ds_read_b64_tr_b16 a[218:219], v21 offset:26112\n"
      "buffer_load_dwordx4 v178, s[12:15], s66 offen offset:0 lds\n"
      "buffer_load_dwordx4 v179, s[12:15], s66 offen offset:1024 lds\n"
      "buffer_load_dwordx4 v180, s[12:15], s66 offen offset:2048 lds\n"
      "s_sub_u32 s65, s65, 1\n"
      ".Ld_rs7:\n"
      "s_add_u32 s66, s44, 516096\n"
      "s_cmp_ge_u32 s65, 7\n"
      "s_cselect_b32 s66, s66, s31\n"
      "s_add_u32 m0, s42, 73728\n"
      "s_waitcnt vmcnt(18) lgkmcnt(0)\n"
      "s_barrier\n"
      "ds_read_b64_tr_b16 a[28:29], v20 offset:36864\n"
      "ds_read_b64_tr_b16 a[30:31], v20 offset:38400\n"
      "ds_read_b64_tr_b16 a[124:125], v20 offset:36872\n"
      "ds_read_b64_tr_b16 a[126:127], v20 offset:38408\n"
      "ds_read_b64_tr_b16 a[220:221], v21 offset:36864\n"
      "ds_read_b64_tr_b16 a[222:223], v21 offset:38400\n"
      "buffer_load_dwordx4 v178, s[12:15], s66 offen offset:0 lds\n"
      "buffer_load_dwordx4 v179, s[12:15], s66 offen offset:1024 lds\n"
      "buffer_load_dwordx4 v180, s[12:15], s66 offen offset:2048 lds\n"
      "s_sub_u32 s65, s65, 1\n"
      ".Ld_rs8:\n"
      "s_add_u32 s66, s44, 552960\n"
      "s_cmp_ge_u32 s65, 7\n"
      "s_cselect_b32 s66, s66, s31\n"
      "s_add_u32 m0, s42, 86016\n"
      "s_waitcnt vmcnt(18) lgkmcnt(0)\n"
      "s_barrier\n"
      "ds_read_b64_tr_b16 a[32:33], v176 offset:0\n"
      "ds_read_b64_tr_b16 a[34:35], v176 offset:1536\n"
      "ds_read_b64_tr_b16 a[128:129], v176 offset:8\n"
      "ds_read_b64_tr_b16 a[130:131], v176 offset:1544\n"
      "ds_read_b64_tr_b16 a[224:225], v177 offset:0\n"
      "ds_read_b64_tr_b16 a[226:227], v177 offset:1536\n"
      "buffer_load_dwordx4 v178, s[12:15], s66 offen offset:0 lds\n"
      "buffer_load_dwordx4 v179, s[12:15], s66 offen offset:1024 lds\n"
      "buffer_load_dwordx4 v180, s[12:15], s66 offen offset:2048 lds\n"
      "s_sub_u32 s65, s65, 1\n"
      ".Ld_rs9:\n"
      "s_add_u32 s66, s44, 589824\n"
      "s_cmp_ge_u32 s65, 7\n"
      "s_cselect_b32 s66, s66, s31\n"
      "s_add_u32 m0, s42, 0\n"
      "s_waitcnt vmcnt(18) lgkmcnt(0)\n"
      "s_barrier\n"
      "ds_read_b64_tr_b16 a[36:37], v176 offset:12288\n"
      "ds_read_b64_tr_b16 a[38:39], v176 offset:13824\n"
      "ds_read_b64_tr_b16 a[132:133], v176 offset:12296\n"
      "ds_read_b64_tr_b16 a[134:135], v176 offset:13832\n"
      "ds_read_b64_tr_b16 a[228:229], v177 offset:12288\n"
      "ds_read_b64_tr_b16 a[230:231], v177 offset:13824\n"
      "buffer_load_dwordx4 v178, s[12:15], s66 offen offset:0 lds\n"
      "buffer_load_dwordx4 v179, s[12:15], s66 offen offset:1024 lds\n"
      "buffer_load_dwordx4 v180, s[12:15], s66 offen offset:2048 lds\n"
      "s_sub_u32 s65, s65, 1\n"
      ".Ld_rs10:\n"
      "s_add_u32 s66, s44, 626688\n"
      "s_cmp_ge_u32 s65, 7\n"
      "s_cselect_b32 s66, s66, s31\n"
      "s_add_u32 m0, s42, 12288\n"
      "s_waitcnt vmcnt(18) lgkmcnt(0)\n"
      "s_barrier\n"
      "ds_read_b64_tr_b16 a[40:41], v176 offset:24576\n"
      "ds_read_b64_tr_b16 a[42:43], v176 offset:26112\n"
      "ds_read_b64_tr_b16 a[136:137], v176 offset:24584\n"
      "ds_read_b64_tr_b16 a[138:139], v176 offset:26120\n"
      "ds_read_b64_tr_b16 a[232:233], v177 offset:24576\n"
      "ds_read_b64_tr_b16 a[234:235], v177 offset:26112\n"
      "buffer_load_dwordx4 v178, s[12:15], s66 offen offset:0 lds\n"
      "buffer_load_dwordx4 v179, s[12:15], s66 offen offset:1024 lds\n"
      "buffer_load_dwordx4 v180, s[12:15], s66 offen offset:2048 lds\n"
      "s_sub_u32 s65, s65, 1\n"
      ".Ld_rs11:\n"
      "s_add_u32 s66, s44, 663552\n"
      "s_cmp_ge_u32 s65, 7\n"
      "s_cselect_b32 s66, s66, s31\n"
      "s_add_u32 m0, s42, 24576\n"
      "s_waitcnt vmcnt(18) lgkmcnt(0)\n"
      "s_barrier\n"
      "ds_read_b64_tr_b16 a[44:45], v176 offset:36864\n"
      "ds_read_b64_tr_b16 a[46:47], v176 offset:38400\n"
      "ds_read_b64_tr_b16 a[140:141], v176 offset:36872\n"
      "ds_read_b64_tr_b16 a[142:143], v176 offset:38408\n"
      "ds_read_b64_tr_b16 a[236:237], v177 offset:36864\n"
      "ds_read_b64_tr_b16 a[238:239], v177 offset:38400\n"
      "buffer_load_dwordx4 v178, s[12:15], s66 offen offset:0 lds\n"
      "buffer_load_dwordx4 v179, s[12:15], s66 offen offset:1024 lds\n"
      "buffer_load_dwordx4 v180, s[12:15], s66 offen offset:2048 lds\n"
      "s_sub_u32 s65, s65, 1\n"
      "s_cbranch_scc1 .Ld_re\n"
      ".Ld_rs12:\n"
      "s_add_u32 s66, s44, 700416\n"
      "s_cmp_ge_u32 s65, 7\n"
      "s_cselect_b32 s66, s66, s31\n"
      "s_add_u32 m0, s42, 36864\n"
      "s_waitcnt vmcnt(18) lgkmcnt(0)\n"
      "s_barrier\n"
      "ds_read_b64_tr_b16 a[48:49], v20 offset:0\n"
      "ds_read_b64_tr_b16 a[50:51], v20 offset:1536\n"
      "ds_read_b64_tr_b16 a[144:145], v20 offset:8\n"
      "ds_read_b64_tr_b16 a[146:147], v20 offset:1544\n"
      "ds_read_b64_tr_b16 a[240:241], v21 offset:0\n"
      "ds_read_b64_tr_b16 a[242:243], v21 offset:1536\n"
      "buffer_load_dwordx4 v178, s[12:15], s66 offen offset:0 lds\n"
      "buffer_load_dwordx4 v179, s[12:15], s66 offen offset:1024 lds\n"
      "buffer_load_dwordx4 v180, s[12:15], s66 offen offset:2048 lds\n"
      "s_sub_u32 s65, s65, 1\n"
      ".Ld_rs13:\n"
      "s_add_u32 s66, s44, 737280\n"
      "s_cmp_ge_u32 s65, 7\n"
      "s_cselect_b32 s66, s66, s31\n"
      "s_add_u32 m0, s42, 49152\n"
      "s_waitcnt vmcnt(18) lgkmcnt(0)\n"
      "s_barrier\n"
      "ds_read_b64_tr_b16 a[52:53], v20 offset:12288\n"
      "ds_read_b64_tr_b16 a[54:55], v20 offset:13824\n"
      "ds_read_b64_tr_b16 a[148:149], v20 offset:12296\n"
      "ds_read_b64_tr_b16 a[150:151], v20 offset:13832\n"
      "ds_read_b64_tr_b16 a[244:245], v21 offset:12288\n"
      "ds_read_b64_tr_b16 a[246:247], v21 offset:13824\n"
      "buffer_load_dwordx4 v178, s[12:15], s66 offen offset:0 lds\n"
      "buffer_load_dwordx4 v179, s[12:15], s66 offen offset:1024 lds\n"
      "buffer_load_dwordx4 v180, s[12:15], s66 offen offset:2048 lds\n"
      "s_sub_u32 s65, s65, 1\n"
      ".Ld_rs14:\n"
      "s_add_u32 s66, s44, 774144\n"
      "s_cmp_ge_u32 s65, 7\n"
      "s_cselect_b32 s66, s66, s31\n"
      "s_add_u32 m0, s42, 61440\n"
      "s_waitcnt vmcnt(18) lgkmcnt(0)\n"
      "s_barrier\n"
      "ds_read_b64_tr_b16 a[56:57], v20 offset:24576\n"
      "ds_read_b64_tr_b16 a[58:59], v20 offset:26112\n"
      "ds_read_b64_tr_b16 a[152:153], v20 offset:24584\n"
      "ds_read_b64_tr_b16 a[154:155], v20 offset:26120\n"
      "ds_read_b64_tr_b16 a[248:249], v21 offset:24576\n"
      "ds_read_b64_tr_b16 a[250:251], v21 offset:26112\n"
      "buffer_load_dwordx4 v178, s[12:15], s66 offen offset:0 lds\n"
      "buffer_load_dwordx4 v179, s[12:15], s66 offen offset:1024 lds\n"
      "buffer_load_dwordx4 v180, s[12:15], s66 offen offset:2048 lds\n"
      "s_sub_u32 s65, s65, 1\n"
      ".Ld_rs15:\n"
      "s_add_u32 s66, s44, 811008\n"
      "s_cmp_ge_u32 s65, 7\n"
      "s_cselect_b32 s66, s66, s31\n"
      "s_add_u32 m0, s42, 73728\n"
      "s_waitcnt vmcnt(18) lgkmcnt(0)\n"
      "s_barrier\n"
      "ds_read_b64_tr_b16 a[60:61], v20 offset:36864\n"
      "ds_read_b64_tr_b16 a[62:63], v20 offset:38400\n"
      "ds_read_b64_tr_b16 a[156:157], v20 offset:36872\n"
      "ds_read_b64_tr_b16 a[158:159], v20 offset:38408\n"
      "ds_read_b64_tr_b16 a[252:253], v21 offset:36864\n"
      "ds_read_b64_tr_b16 a[254:255], v21 offset:38400\n"
      "buffer_load_dwordx4 v178, s[12:15], s66 offen offset:0 lds\n"
      "buffer_load_dwordx4 v179, s[12:15], s66 offen offset:1024 lds\n"
      "buffer_load_dwordx4 v180, s[12:15], s66 offen offset:2048 lds\n"
      "s_sub_u32 s65, s65, 1\n"
      ".Ld_rs16:\n"
      "s_add_u32 s66, s44, 847872\n"
      "s_cmp_ge_u32 s65, 7\n"
      "s_cselect_b32 s66, s66, s31\n"
      "s_add_u32 m0, s42, 86016\n"
      "s_waitcnt vmcnt(18) lgkmcnt(0)\n"
      "s_barrier\n"
      "ds_read_b64_tr_b16 a[64:65], v176 offset:0\n"
      "ds_read_b64_tr_b16 a[66:67], v176 offset:1536\n"
      "ds_read_b64_tr_b16 a[160:161], v176 offset:8\n"
      "ds_read_b64_tr_b16 a[162:163], v176 offset:1544\n"
      "ds_read_b64_tr_b16 v[24:25], v177 offset:0\n"
      "ds_read_b64_tr_b16 v[26:27], v177 offset:1536\n"
      "buffer_load_dwordx4 v178, s[12:15], s66 offen offset:0 lds\n"
      "buffer_load_dwordx4 v179, s[12:15], s66 offen offset:1024 lds\n"
      "buffer_load_dwordx4 v180, s[12:15], s66 offen offset:2048 lds\n"
      "s_sub_u32 s65, s65, 1\n"
      ".Ld_rs17:\n"
      "s_add_u32 s66, s44, 0\n"
      "s_cmp_ge_u32 s65, 7\n"
      "s_cselect_b32 s66, s66, s31\n"
      "s_add_u32 m0, s42, 0\n"
      "s_waitcnt vmcnt(18) lgkmcnt(0)\n"
      "s_barrier\n"
      "ds_read_b64_tr_b16 a[68:69], v176 offset:12288\n"
      "ds_read_b64_tr_b16 a[70:71], v176 offset:13824\n"
      "ds_read_b64_tr_b16 a[164:165], v176 offset:12296\n"
      "ds_read_b64_tr_b16 a[166:167], v176 offset:13832\n"
      "ds_read_b64_tr_b16 v[28:29], v177 offset:12288\n"
      "ds_read_b64_tr_b16 v[30:31], v177 offset:13824\n"
      "buffer_load_dwordx4 v178, s[12:15], s66 offen offset:0 lds\n"
      "buffer_load_dwordx4 v179, s[12:15], s66 offen offset:1024 lds\n"
      "buffer_load_dwordx4 v180, s[12:15], s66 offen offset:2048 lds\n"
      "s_sub_u32 s65, s65, 1\n"
      "s_cbranch_scc1 .Ld_re\n"
      ".Ld_rs18:\n"
      "s_add_u32 s66, s44, 36864\n"
      "s_cmp_ge_u32 s65, 7\n"
      "s_cselect_b32 s66, s66, s31\n"
      "s_add_u32 m0, s42, 12288\n"
      "s_waitcnt vmcnt(18) lgkmcnt(0)\n"
      "s_barrier\n"
      "ds_read_b64_tr_b16 a[72:73], v176 offset:24576\n"
      "ds_read_b64_tr_b16 a[74:75], v176 offset:26112\n"
      "ds_read_b64_tr_b16 a[168:169], v176 offset:24584\n"
      "ds_read_b64_tr_b16 a[170:171], v176 offset:26120\n"
      "ds_read_b64_tr_b16 v[32:33], v177 offset:24576\n"
      "ds_read_b64_tr_b16 v[34:35], v177 offset:26112\n"
      "buffer_load_dwordx4 v178, s[12:15], s66 offen offset:0 lds\n"
      "buffer_load_dwordx4 v179, s[12:15], s66 offen offset:1024 lds\n"
      "buffer_load_dwordx4 v180, s[12:15], s66 offen offset:2048 lds\n"
      "s_sub_u32 s65, s65, 1\n"
      ".Ld_rs19:\n"
      "s_add_u32 s66, s44, 73728\n"
      "s_cmp_ge_u32 s65, 7\n"
      "s_cselect_b32 s66, s66, s31\n"
      "s_add_u32 m0, s42, 24576\n"
      "s_waitcnt vmcnt(18) lgkmcnt(0)\n"
      "s_barrier\n"
      "ds_read_b64_tr_b16 a[76:77], v176 offset:36864\n"
      "ds_read_b64_tr_b16 a[78:79], v176 offset:38400\n"
      "ds_read_b64_tr_b16 a[172:173], v176 offset:36872\n"
      "ds_read_b64_tr_b16 a[174:175], v176 offset:38408\n"
      "ds_read_b64_tr_b16 v[36:37], v177 offset:36864\n"
      "ds_read_b64_tr_b16 v[38:39], v177 offset:38400\n"
      "buffer_load_dwordx4 v178, s[12:15], s66 offen offset:0 lds\n"
      "buffer_load_dwordx4 v179, s[12:15], s66 offen offset:1024 lds\n"
      "buffer_load_dwordx4 v180, s[12:15], s66 offen offset:2048 lds\n"
      "s_sub_u32 s65, s65, 1\n"
      ".Ld_rs20:\n"
      "s_add_u32 s66, s44, 110592\n"
      "s_cmp_ge_u32 s65, 7\n"
      "s_cselect_b32 s66, s66, s31\n"
      "s_add_u32 m0, s42, 36864\n"
      "s_waitcnt vmcnt(18) lgkmcnt(0)\n"
      "s_barrier\n"
      "ds_read_b64_tr_b16 a[80:81], v20 offset:0\n"
      "ds_read_b64_tr_b16 a[82:83], v20 offset:1536\n"
      "ds_read_b64_tr_b16 a[176:177], v20 offset:8\n"
      "ds_read_b64_tr_b16 a[178:179], v20 offset:1544\n"
      "ds_read_b64_tr_b16 v[40:41], v21 offset:0\n"
      "ds_read_b64_tr_b16 v[42:43], v21 offset:1536\n"
      "buffer_load_dwordx4 v178, s[12:15], s66 offen offset:0 lds\n"
      "buffer_load_dwordx4 v179, s[12:15], s66 offen offset:1024 lds\n"
      "buffer_load_dwordx4 v180, s[12:15], s66 offen offset:2048 lds\n"
      "s_sub_u32 s65, s65, 1\n"
      ".Ld_rs21:\n"
      "s_add_u32 s66, s44, 147456\n"
      "s_cmp_ge_u32 s65, 7\n"
      "s_cselect_b32 s66, s66, s31\n"
      "s_add_u32 m0, s42, 49152\n"
      "s_waitcnt vmcnt(18) lgkmcnt(0)\n"
      "s_barrier\n"
      "ds_read_b64_tr_b16 a[84:85], v20 offset:12288\n"
      "ds_read_b64_tr_b16 a[86:87], v20 offset:13824\n"
      "ds_read_b64_tr_b16 a[180:181], v20 offset:12296\n"
      "ds_read_b64_tr_b16 a[182:183], v20 offset:13832\n"
      "ds_read_b64_tr_b16 v[44:45], v21 offset:12288\n"
      "ds_read_b64_tr_b16 v[46:47], v21 offset:13824\n"
      "buffer_load_dwordx4 v178, s[12:15], s66 offen offset:0 lds\n"
      "buffer_load_dwordx4 v179, s[12:15], s66 offen offset:1024 lds\n"
      "buffer_load_dwordx4 v180, s[12:15], s66 offen offset:2048 lds\n"
      "s_sub_u32 s65, s65, 1\n"
      ".Ld_rs22:\n"
      "s_add_u32 s66, s44, 184320\n"
      "s_cmp_ge_u32 s65, 7\n"
      "s_cselect_b32 s66, s66, s31\n"
      "s_add_u32 m0, s42, 61440\n"
      "s_waitcnt vmcnt(18) lgkmcnt(0)\n"
      "s_barrier\n"
      "ds_read_b64_tr_b16 a[88:89], v20 offset:24576\n"
      "ds_read_b64_tr_b16 a[90:91], v20 offset:26112\n"
      "ds_read_b64_tr_b16 a[184:185], v20 offset:24584\n"
      "ds_read_b64_tr_b16 a[186:187], v20 offset:26120\n"
      "ds_read_b64_tr_b16 v[48:49], v21 offset:24576\n"
      "ds_read_b64_tr_b16 v[50:51], v21 offset:26112\n"
      "buffer_load_dwordx4 v178, s[12:15], s66 offen offset:0 lds\n"
      "buffer_load_dwordx4 v179, s[12:15], s66 offen offset:1024 lds\n"
      "buffer_load_dwordx4 v180, s[12:15], s66 offen offset:2048 lds\n"
      "s_sub_u32 s65, s65, 1\n"
      ".Ld_rs23:\n"
      "s_add_u32 s66, s44, 221184\n"
      "s_cmp_ge_u32 s65, 7\n"
      "s_cselect_b32 s66, s66, s31\n"
      "s_add_u32 m0, s42, 73728\n"
      "s_waitcnt vmcnt(18) lgkmcnt(0)\n"
      "s_barrier\n"
      "ds_read_b64_tr_b16 a[92:93], v20 offset:36864\n"
      "ds_read_b64_tr_b16 a[94:95], v20 offset:38400\n"
      "ds_read_b64_tr_b16 a[188:189], v20 offset:36872\n"
      "ds_read_b64_tr_b16 a[190:191], v20 offset:38408\n"
      "ds_read_b64_tr_b16 v[52:53], v21 offset:36864\n"
      "ds_read_b64_tr_b16 v[54:55], v21 offset:38400\n"
      "buffer_load_dwordx4 v178, s[12:15], s66 offen offset:0 lds\n"
      "buffer_load_dwordx4 v179, s[12:15], s66 offen offset:1024 lds\n"
      "buffer_load_dwordx4 v180, s[12:15], s66 offen offset:2048 lds\n"
      "s_sub_u32 s65, s65, 1\n"
      "s_cbranch_scc0 .Ld_rs0\n"
      ".Ld_re:\n"
      "s_waitcnt vmcnt(0) lgkmcnt(0)\n"
      "v_add_u32 v22, 49152, v0\n"
      "v_add_u32 v23, 98304, v0\n"
      "s_lshr_b32 s61, s42, 10\n"
      "s_mul_i32 s61, s61, 1366\n"
      "s_and_b32 s61, s61, 0x7000\n"
      "s_getpc_b64 vcc\n"
      ".Ld_ct:\n"
      "s_add_u32 vcc_lo, vcc_lo, .Ld_p0-.Ld_ct\n"
      "s_addc_u32 vcc_hi, vcc_hi, 0\n"
      "s_lshl_b32 s30, s61, 1\n"
      "v_lshl_add_u32 v56, v0, 3, s30\n"
      "v_min_u32 v56, .Ld_last-.Ld_p0-4, v56\n"
      "global_load_dword v56, v56, vcc\n"
      "s_waitcnt vmcnt(0) lgkmcnt(0)\n"
      "s_mov_b64 exec, 0\n"
      "s_lshr_b32 s22, s61, 12\n"
      ".Ld_pl:\n"
      "s_cmp_eq_u32 s22, 0\n"
      "s_cbranch_scc1 .Ld_p0\n"
      "s_cmp_eq_u32 s22, 1\n"
      "s_cbranch_scc1 .Ld_p1\n"
      "s_branch .Ld_pe\n"
      ".Ld_pr:\n"
      "s_add_u32 s22, s22, 4\n"
      "s_branch .Ld_pl\n"
      ".Ld_pe:\n"
      "s_mov_b64 exec, -1\n"
      "s_barrier\n"
      "v_mov_b32 v14, 0\n"
      "ds_write_b32 v16, v14\n"
      "s_waitcnt lgkmcnt(0)\n"
      "s_barrier\n"
      "s_mov_b32 s6, s50\n"
      "s_add_u32 s36, s36, 4294801408\n"
      "s_add_u32 s56, s37, 0\n"
      "s_add_u32 s55, s37, 4294787584\n"
      "s_add_u32 s37, s37, 4294894080\n"
      "s_mov_b32 s59, 0x40000000\n"
      "s_mov_b32 s60, 0x40000000\n"
      "s_mov_b32 s62, 63\n"
      "s_cmp_eq_u32 s45, 1\n"
      "s_cbranch_scc1 .Ld_in1\n"
      "s_cmp_eq_u32 s45, 2\n"
      "s_cbranch_scc1 .Ld_in2\n"
      "s_cmp_eq_u32 s45, 3\n"
      "s_cbranch_scc1 .Ld_in3\n"
      "s_cmp_eq_u32 s45, 4\n"
      "s_cbranch_scc1 .Ld_in4\n"
      "s_cmp_eq_u32 s45, 5\n"
      "s_cbranch_scc1 .Ld_in5\n"
      "s_add_u32 s36, s36, 165888\n"
      "s_min_u32 s36, s36, s6\n"
      "buffer_load_dword v20, v2, s[8:11], s36 offen sc1\n"
      "s_add_u32 m0, s43, 0\n"
      "s_mov_b32 s54, s36\n"
      "s_add_u32 s36, s36, 165888\n"
      "s_min_u32 s36, s36, s6\n"
      "s_waitcnt vmcnt(0)\n"
      "v_cmp_ne_u32 vcc, s21, v20\n"
      "s_cbranch_vccnz .Lsp_d0_1\n"
      ".Lbk_d0_1:\n"
      "buffer_load_dwordx4 v0, s[8:11], s54 offen offset:0 sc1 lds\n"
      "buffer_load_dwordx4 v0, s[8:11], s54 offen offset:1024 sc1 lds\n"
      "buffer_load_dword v20, v2, s[8:11], s36 offen sc1\n"
      "buffer_load_dwordx4 v0, s[8:11], s54 offen offset:2048 sc1 lds\n"
      "buffer_load_dwordx4 v0, s[8:11], s54 offen offset:3072 sc1 lds\n"
      "s_add_u32 m0, s43, 4096\n"
      "s_nop 0\n"
      "buffer_load_dwordx4 v1, s[8:11], s54 offen offset:1024 sc1 lds\n"
      "buffer_load_dwordx4 v1, s[8:11], s54 offen offset:0 sc1 lds\n"
      "s_mov_b32 s54, s36\n"
      "s_add_u32 m0, s43, 24576\n"
      "v_add_u32 v14, 1, v14\n"
      "s_add_u32 s36, s36, 165888\n"
      "buffer_load_dwordx4 v[216:219], v6, s[8:11], s38 offen sc1\n"
      "s_waitcnt vmcnt(5)\n"
      "v_cmp_ne_u32 vcc, s21, v20\n"
      "buffer_load_dwordx2 v[220:221], v7, s[8:11], s38 offen sc1\n"
      "s_cbranch_vccnz .Lsp_d1_2\n"
      ".Lbk_d1_2:\n"
      "buffer_load_dwordx4 v0, s[8:11], s54 offen offset:0 sc1 lds\n"
      "buffer_load_dwordx4 v0, s[8:11], s54 offen offset:1024 sc1 lds\n"
      "s_waitcnt vmcnt(4)\n"
      "ds_write_b32 v16, v14\n"
      "buffer_load_dwordx4 v0, s[8:11], s54 offen offset:2048 sc1 lds\n"
      "s_min_u32 s36, s36, s6\n"
      "buffer_load_dword v20, v2, s[8:11], s36 offen sc1\n"
      "buffer_load_dwordx4 v0, s[8:11], s54 offen offset:3072 sc1 lds\n"
      "ds_read_b32 v15, v17\n"
      "s_add_u32 m0, s43, 28672\n"
      "s_nop 0\n"
      "buffer_load_dwordx4 v1, s[8:11], s54 offen offset:0 sc1 lds\n"
      "s_waitcnt lgkmcnt(0)\n"
      "v_cmp_gt_i32 vcc, v14, v15\n"
      "buffer_load_dwordx4 v1, s[8:11], s54 offen offset:1024 sc1 lds\n"
      "s_cbranch_vccnz .Lsp_dy0_3\n"
      ".Lbk_dy0_3:\n"
      "ds_read_b128 v[56:59], v0 offset:0\n"
      "ds_read_b128 v[60:63], v0 offset:1024\n"
      "ds_read_b128 v[84:87], v0 offset:7168\n"
      "ds_read_b128 v[72:75], v0 offset:4096\n"
      "ds_read_b128 v[88:91], v0 offset:8192\n"
      "ds_read_b128 v[68:71], v0 offset:3072\n"
      "ds_read_b128 v[104:107], v0 offset:12288\n"
      "ds_read_b128 v[96:99], v0 offset:10240\n"
      "ds_read_b128 v[80:83], v0 offset:6144\n"
      "ds_read_b128 v[64:67], v0 offset:2048\n"
      "ds_read_b128 v[92:95], v0 offset:9216\n"
      "ds_read_b128 v[76:79], v0 offset:5120\n"
      "ds_read_b128 v[132:135], v0 offset:19456\n"
      "ds_read_b128 v[100:103], v0 offset:11264\n"
      "s_waitcnt vmcnt(0) lgkmcnt(0)\n"
      "s_nop 4\n"
      "s_branch .Ld_t0\n"
      ".p2align 12\n"
      ".Ld_p0:\n"
      "s_branch .Ld_pr\n"
      ".p2align 5\n"
      ".Ld_t0:\n"
      "v_mfma_f32_16x16x32_bf16 v[152:155], a[0:3], v[56:59], 0\n"
      "ds_read_b128 v[108:111], v0 offset:13312\n"
      "ds_read_b128 v[120:123], v0 offset:16384\n"
      "v_mfma_f32_16x16x32_bf16 v[152:155], a[4:7], v[60:63], v[152:155]\n"
      "s_waitcnt lgkmcnt(6)\n"
      "ds_read_b128 v[116:119], v0 offset:15360\n"
      "v_add_f32 v236, v236, v168\n"
      "v_mfma_f32_16x16x32_bf16 v[152:155], a[8:11], v[64:67], v[152:155]\n"
      "v_cvt_pk_bf16_f32 v228, v160, v161\n"
      "v_cvt_pk_bf16_f32 v229, v162, v163\n"
      "v_mfma_f32_16x16x32_bf16 v[152:155], a[12:15], v[68:71], v[152:155]\n"
      "buffer_store_dwordx2 v[228:229], v13, s[8:11], s37 offen sc1\n"
      "v_mfma_f32_16x16x32_bf16 v[152:155], a[16:19], v[72:75], v[152:155]\n"
      "s_waitcnt lgkmcnt(4)\n"
      "v_cvt_pk_bf16_f32 v178, v168, v169\n"
      "v_add_f32 v175, v175, v213\n"
      "v_mfma_f32_16x16x32_bf16 v[152:155], a[20:23], v[76:79], v[152:155]\n"
      "ds_read_b128 v[112:115], v0 offset:14336\n"
      "v_add_f32 v171, v171, v211\n"
      "v_mfma_f32_16x16x32_bf16 v[152:155], a[24:27], v[80:83], v[152:155]\n"
      "ds_read_b128 v[124:127], v0 offset:17408\n"
      "v_add_f32 v236, v236, v169\n"
      "v_mfma_f32_16x16x32_bf16 v[152:155], a[28:31], v[84:87], v[152:155]\n"
      "v_add_f32 v173, v173, v204\n"
      "v_cvt_pk_bf16_f32 v179, v170, v171\n"
      "v_mfma_f32_16x16x32_bf16 v[152:155], a[32:35], v[88:91], v[152:155]\n"
      "ds_read_b128 v[128:131], v0 offset:18432\n"
      "v_add_f32 v173, v173, v212\n"
      "v_mfma_f32_16x16x32_bf16 v[152:155], a[36:39], v[92:95], v[152:155]\n"
      "buffer_store_dwordx4 v[176:179], v6, s[8:11], s59 offen sc1\n"
      "v_mfma_f32_16x16x32_bf16 v[152:155], a[40:43], v[96:99], v[152:155]\n"
      "s_waitcnt lgkmcnt(3)\n"
      "ds_read_b128 v[148:151], v0 offset:23552\n"
      "v_cvt_pk_bf16_f32 v180, v172, v173\n"
      "v_mfma_f32_16x16x32_bf16 v[152:155], a[44:47], v[100:103], v[152:155]\n"
      "ds_read_b128 v[144:147], v0 offset:22528\n"
      "v_fmac_f32 v237, v168, v168\n"
      "v_mfma_f32_16x16x32_bf16 v[152:155], a[48:51], v[104:107], v[152:155]\n"
      "s_add_u32 m0, s43, 49152\n"
      "ds_read_b128 v[136:139], v0 offset:20480\n"
      "v_fmac_f32 v237, v169, v169\n"
      "v_mfma_f32_16x16x32_bf16 v[152:155], a[52:55], v[108:111], v[152:155]\n"
      "s_waitcnt lgkmcnt(4)\n"
      "s_mov_b32 s54, s36\n"
      "ds_read_b128 v[140:143], v0 offset:21504\n"
      "v_mfma_f32_16x16x32_bf16 v[152:155], a[56:59], v[112:115], v[152:155]\n"
      "v_cvt_pk_bf16_f32 v181, v174, v175\n"
      "v_fmac_f32 v237, v170, v170\n"
      "v_mfma_f32_16x16x32_bf16 v[152:155], a[60:63], v[116:119], v[152:155]\n"
      "buffer_store_dwordx2 v[180:181], v7, s[8:11], s59 offen sc1\n"
      "v_mfma_f32_16x16x32_bf16 v[152:155], a[64:67], v[120:123], v[152:155]\n"
      "v_add_u32 v14, 1, v14\n"
      "v_mfma_f32_16x16x32_bf16 v[152:155], a[68:71], v[124:127], v[152:155]\n"
      "s_waitcnt lgkmcnt(3)\n"
      "v_add_f32 v236, v236, v170\n"
      "v_fmac_f32 v237, v171, v171\n"
      "v_mfma_f32_16x16x32_bf16 v[152:155], a[72:75], v[128:131], v[152:155]\n"
      "v_fmac_f32 v237, v172, v172\n"
      "v_fmac_f32 v237, v173, v173\n"
      "v_mfma_f32_16x16x32_bf16 v[152:155], a[76:79], v[132:135], v[152:155]\n"
      "s_waitcnt lgkmcnt(1)\n"
      "buffer_store_dwordx2 v[180:181], v9, s[16:19], s60 offen\n"
      "v_mfma_f32_16x16x32_bf16 v[152:155], a[80:83], v[136:139], v[152:155]\n"
      "s_waitcnt lgkmcnt(0)\n"
      "v_add_f32 v236, v236, v171\n"
      "v_fmac_f32 v237, v174, v174\n"
      "v_mfma_f32_16x16x32_bf16 v[152:155], a[84:87], v[140:143], v[152:155]\n"
      "v_add_f32 v236, v236, v172\n"
      "v_fmac_f32 v237, v175, v175\n"
      "v_mfma_f32_16x16x32_bf16 v[152:155], a[88:91], v[144:147], v[152:155]\n"
      "v_add_f32 v236, v236, v173\n"
      "v_add_f32 v236, v236, v174\n"
      "v_mfma_f32_16x16x32_bf16 v[152:155], a[92:95], v[148:151], v[152:155]\n"
      "buffer_store_dwordx4 v[176:179], v8, s[16:19], s60 offen\n"
      "v_mfma_f32_16x16x32_bf16 v[156:159], a[96:99], v[56:59], 0\n"
      "s_waitcnt vmcnt(15)\n"
      "v_add_f32 v236, v236, v175\n"
      "v_lshlrev_b32 v166, 16, v217\n"
      "v_mfma_f32_16x16x32_bf16 v[156:159], a[100:103], v[60:63], v[156:159]\n"
      "s_waitcnt vmcnt(10)\n"
      "v_cmp_ne_u32 vcc, s21, v20\n"
      "v_mfma_f32_16x16x32_bf16 v[156:159], a[104:107], v[64:67], v[156:159]\n"
      "ds_write_b64 v10, v[236:237]\n"
      "v_mfma_f32_16x16x32_bf16 v[156:159], a[108:111], v[68:71], v[156:159]\n"
      "s_add_u32 s36, s36, 165888\n"
      "v_mfma_f32_16x16x32_bf16 v[156:159], a[112:115], v[72:75], v[156:159]\n"
      "s_min_u32 s36, s36, s6\n"
      "s_cbranch_vccnz .Lsp_d2_4\n"
      ".Lbk_d2_4:\n"
      "v_mfma_f32_16x16x32_bf16 v[156:159], a[116:119], v[76:79], v[156:159]\n"
      "s_waitcnt vmcnt(7)\n"
      "ds_write_b32 v16, v14\n"
      "v_mfma_f32_16x16x32_bf16 v[156:159], a[120:123], v[80:83], v[156:159]\n"
      "buffer_load_dword v20, v2, s[8:11], s36 offen sc1\n"
      "v_mfma_f32_16x16x32_bf16 v[156:159], a[124:127], v[84:87], v[156:159]\n"
      "v_and_b32 v167, 0xffff0000, v217\n"
      "v_lshlrev_b32 v164, 16, v216\n"
      "v_mfma_f32_16x16x32_bf16 v[156:159], a[128:131], v[88:91], v[156:159]\n"
      "v_mfma_f32_16x16x32_bf16 v[156:159], a[132:135], v[92:95], v[156:159]\n"
      "buffer_load_dwordx4 v0, s[8:11], s54 offen offset:3072 sc1 lds\n"
      "v_mfma_f32_16x16x32_bf16 v[156:159], a[136:139], v[96:99], v[156:159]\n"
      "s_add_u32 s55, s55, 106496\n"
      "v_mfma_f32_16x16x32_bf16 v[156:159], a[140:143], v[100:103], v[156:159]\n"
      "v_mfma_f32_16x16x32_bf16 v[156:159], a[144:147], v[104:107], v[156:159]\n"
      "s_add_u32 s37, s37, 173056\n"
      "v_and_b32 v165, 0xffff0000, v216\n"
      "v_mfma_f32_16x16x32_bf16 v[156:159], a[148:151], v[108:111], v[156:159]\n"
      "buffer_load_dwordx4 v0, s[8:11], s54 offen offset:1024 sc1 lds\n"
      "v_mfma_f32_16x16x32_bf16 v[156:159], a[152:155], v[112:115], v[156:159]\n"
      "ds_read_b32 v15, v17\n"
      "v_mfma_f32_16x16x32_bf16 v[156:159], a[156:159], v[116:119], v[156:159]\n"
      "v_mfma_f32_16x16x32_bf16 v[156:159], a[160:163], v[120:123], v[156:159]\n"
      "v_lshlrev_b32 v170, 16, v219\n"
      "v_mfma_f32_16x16x32_bf16 v[156:159], a[164:167], v[124:127], v[156:159]\n"
      "buffer_load_dwordx4 v0, s[8:11], s54 offen offset:2048 sc1 lds\n"
      "v_mfma_f32_16x16x32_bf16 v[156:159], a[168:171], v[128:131], v[156:159]\n"
      "v_mfma_f32_16x16x32_bf16 v[156:159], a[172:175], v[132:135], v[156:159]\n"
      "s_waitcnt lgkmcnt(0)\n"
      "v_cmp_gt_i32 vcc, v14, v15\n"
      "v_mfma_f32_16x16x32_bf16 v[156:159], a[176:179], v[136:139], v[156:159]\n"
      "v_add_f32 v165, v165, v153\n"
      "v_mfma_f32_16x16x32_bf16 v[156:159], a[180:183], v[140:143], v[156:159]\n"
      "buffer_load_dwordx4 v0, s[8:11], s54 offen offset:0 sc1 lds\n"
      "v_mfma_f32_16x16x32_bf16 v[156:159], a[184:187], v[144:147], v[156:159]\n"
      "v_add_f32 v164, v164, v152\n"
      "s_add_u32 m0, s43, 53248\n"
      "v_mfma_f32_16x16x32_bf16 v[156:159], a[188:191], v[148:151], v[156:159]\n"
      "s_cbranch_vccnz .Lsp_dy1_5\n"
      ".Lbk_dy1_5:\n"
      "ds_read_b64 v[238:239], v11\n"
      "v_mfma_f32_16x16x32_bf16 v[160:163], a[192:195], v[56:59], 0\n"
      "v_lshlrev_b32 v168, 16, v218\n"
      "v_mfma_f32_16x16x32_bf16 v[160:163], a[196:199], v[60:63], v[160:163]\n"
      "buffer_load_dwordx4 v1, s[8:11], s54 offen offset:0 sc1 lds\n"
      "v_mfma_f32_16x16x32_bf16 v[160:163], a[200:203], v[64:67], v[160:163]\n"
      "v_mfma_f32_16x16x32_bf16 v[160:163], a[204:207], v[68:71], v[160:163]\n"
      "ds_read_b128 v[56:59], v0 offset:24576\n"
      "v_mfma_f32_16x16x32_bf16 v[160:163], a[208:211], v[72:75], v[160:163]\n"
      "ds_read_b128 v[64:67], v0 offset:26624\n"
      "v_mfma_f32_16x16x32_bf16 v[160:163], a[212:215], v[76:79], v[160:163]\n"
      "buffer_load_dwordx4 v1, s[8:11], s54 offen offset:1024 sc1 lds\n"
      "v_mfma_f32_16x16x32_bf16 v[160:163], a[216:219], v[80:83], v[160:163]\n"
      "s_waitcnt lgkmcnt(2)\n"
      "ds_read_b128 v[68:71], v0 offset:27648\n"
      "v_add_f32_dpp v238, v238, v238 row_ror:8 row_mask:0xf bank_mask:0xf\n"
      "v_mfma_f32_16x16x32_bf16 v[160:163], a[220:223], v[84:87], v[160:163]\n"
      "ds_read_b128 v[80:83], v0 offset:30720\n"
      "v_add_f32_dpp v238, v238, v238 row_ror:4 row_mask:0xf bank_mask:0xf\n"
      "v_mfma_f32_16x16x32_bf16 v[160:163], a[224:227], v[88:91], v[160:163]\n"
      "ds_read_b128 v[60:63], v0 offset:25600\n"
      "v_add_f32_dpp v238, v238, v238 row_ror:2 row_mask:0xf bank_mask:0xf\n"
      "v_mfma_f32_16x16x32_bf16 v[160:163], a[228:231], v[92:95], v[160:163]\n"
      "ds_read_b128 v[92:95], v0 offset:33792\n"
      "v_add_f32_dpp v239, v239, v239 row_ror:8 row_mask:0xf bank_mask:0xf\n"
      "v_mfma_f32_16x16x32_bf16 v[160:163], a[232:235], v[96:99], v[160:163]\n"
      "ds_read_b128 v[84:87], v0 offset:31744\n"
      "v_add_f32_dpp v239, v239, v239 row_ror:4 row_mask:0xf bank_mask:0xf\n"
      "v_mfma_f32_16x16x32_bf16 v[160:163], a[236:239], v[100:103], v[160:163]\n"
      "s_waitcnt vmcnt(11) lgkmcnt(5)\n"
      "buffer_store_dword v18, v4, s[8:11], s55 offen sc1\n"
      "v_mfma_f32_16x16x32_bf16 v[160:163], a[240:243], v[104:107], v[160:163]\n"
      "ds_read_b128 v[72:75], v0 offset:28672\n"
      "v_add_f32_dpp v239, v239, v239 row_ror:2 row_mask:0xf bank_mask:0xf\n"
      "v_mfma_f32_16x16x32_bf16 v[160:163], a[244:247], v[108:111], v[160:163]\n"
      "ds_read_b128 v[76:79], v0 offset:29696\n"
      "v_add_f32_dpp v239, v239, v239 row_ror:1 row_mask:0xf bank_mask:0xf\n"
      "v_mfma_f32_16x16x32_bf16 v[160:163], a[248:251], v[112:115], v[160:163]\n"
      "v_add_f32_dpp v238, v238, v238 row_ror:1 row_mask:0xf bank_mask:0xf\n"
      "v_add_f32 v168, v168, v156\n"
      "v_mfma_f32_16x16x32_bf16 v[160:163], a[252:255], v[116:119], v[160:163]\n"
      "ds_read_b128 v[100:103], v0 offset:35840\n"
      "v_mul_f32 v238, 0x3ae38e39, v238\n"
      "v_mfma_f32_16x16x32_bf16 v[160:163], v[24:27], v[120:123], v[160:163]\n"
      "v_and_b32 v169, 0xffff0000, v218\n"
      "v_mfma_f32_16x16x32_bf16 v[160:163], v[28:31], v[124:127], v[160:163]\n"
      "ds_read_b128 v[96:99], v0 offset:34816\n"
      "v_and_b32 v175, 0xffff0000, v221\n"
      "v_mfma_f32_16x16x32_bf16 v[160:163], v[32:35], v[128:131], v[160:163]\n"
      "ds_read_b128 v[116:119], v0 offset:39936\n"
      "v_lshlrev_b32 v174, 16, v221\n"
      "v_mfma_f32_16x16x32_bf16 v[160:163], v[36:39], v[132:135], v[160:163]\n"
      "ds_read_b128 v[120:123], v0 offset:40960\n"
      "v_add_f32 v167, v167, v155\n"
      "v_mfma_f32_16x16x32_bf16 v[160:163], v[40:43], v[136:139], v[160:163]\n"
      "ds_read_b128 v[108:111], v0 offset:37888\n"
      "v_and_b32 v171, 0xffff0000, v219\n"
      "v_mfma_f32_16x16x32_bf16 v[160:163], v[44:47], v[140:143], v[160:163]\n"
      "ds_read_b128 v[88:91], v0 offset:32768\n"
      "v_add_f32 v166, v166, v154\n"
      "v_mfma_f32_16x16x32_bf16 v[160:163], v[48:51], v[144:147], v[160:163]\n"
      "v_add_f32 v239, 0x3afba882, v239\n"
      "v_mul_f32 v239, 0x3ae38e39, v239\n"
      "v_mfma_f32_16x16x32_bf16 v[160:163], v[52:55], v[148:151], v[160:163]\n"
      "s_sub_u32 s62, s62, 1\n"
      "s_cbranch_scc1 .Ld_out0\n"
      ".Ld_t1:\n"
      "v_mfma_f32_16x16x32_bf16 v[152:155], a[0:3], v[56:59], 0\n"
      "s_waitcnt lgkmcnt(4)\n"
      "buffer_store_dwordx2 v[238:239], v12, s[8:11], s59 offen sc1\n"
      "v_mfma_f32_16x16x32_bf16 v[152:155], a[4:7], v[60:63], v[152:155]\n"
      "ds_read_b128 v[104:107], v0 offset:36864\n"
      "v_mfma_f32_16x16x32_bf16 v[152:155], a[8:11], v[64:67], v[152:155]\n"
      "ds_read_b128 v[140:143], v0 offset:46080\n"
      "v_mfma_f32_16x16x32_bf16 v[152:155], a[12:15], v[68:71], v[152:155]\n"
      "ds_read_b128 v[112:115], v0 offset:38912\n"
      "v_mfma_f32_16x16x32_bf16 v[152:155], a[16:19], v[72:75], v[152:155]\n"
      "ds_read_b128 v[124:127], v0 offset:41984\n"
      "v_mfma_f32_16x16x32_bf16 v[152:155], a[20:23], v[76:79], v[152:155]\n"
      "ds_read_b128 v[132:135], v0 offset:44032\n"
      "v_mfma_f32_16x16x32_bf16 v[152:155], a[24:27], v[80:83], v[152:155]\n"
      "s_mov_b32 s54, s36\n"
      "v_add_f32 v174, v174, v162\n"
      "s_add_u32 s36, s36, 165888\n"
      "v_mfma_f32_16x16x32_bf16 v[152:155], a[28:31], v[84:87], v[152:155]\n"
      "s_waitcnt lgkmcnt(5)\n"
      "ds_read_b128 v[148:151], v0 offset:48128\n"
      "v_mfma_f32_16x16x32_bf16 v[152:155], a[32:35], v[88:91], v[152:155]\n"
      "ds_read_b128 v[128:131], v0 offset:43008\n"
      "s_add_u32 m0, s43, 73728\n"
      "v_mfma_f32_16x16x32_bf16 v[152:155], a[36:39], v[92:95], v[152:155]\n"
      "ds_read_b128 v[136:139], v0 offset:45056\n"
      "v_mfma_f32_16x16x32_bf16 v[152:155], a[40:43], v[96:99], v[152:155]\n"
      "v_mfma_f32_16x16x32_bf16 v[152:155], a[44:47], v[100:103], v[152:155]\n"
      "s_waitcnt lgkmcnt(4)\n"
      "v_add_f32 v170, v170, v158\n"
      "v_mfma_f32_16x16x32_bf16 v[152:155], a[48:51], v[104:107], v[152:155]\n"
      "v_mfma_f32_16x16x32_bf16 v[152:155], a[52:55], v[108:111], v[152:155]\n"
      "s_waitcnt vmcnt(8) lgkmcnt(3)\n"
      "v_lshlrev_b32 v172, 16, v220\n"
      "v_cmp_ne_u32 vcc, s21, v20\n"
      "v_mfma_f32_16x16x32_bf16 v[152:155], a[56:59], v[112:115], v[152:155]\n"
      "ds_read_b128 v[144:147], v0 offset:47104\n"
      "s_add_u32 s38, s38, 147456\n"
      "v_mfma_f32_16x16x32_bf16 v[152:155], a[60:63], v[116:119], v[152:155]\n"
      "s_min_u32 s36, s36, s6\n"
      "v_add_f32 v171, v171, v159\n"
      "v_mfma_f32_16x16x32_bf16 v[152:155], a[64:67], v[120:123], v[152:155]\n"
      "v_mfma_f32_16x16x32_bf16 v[152:155], a[68:71], v[124:127], v[152:155]\n"
      "s_waitcnt lgkmcnt(1)\n"
      "v_mfma_f32_16x16x32_bf16 v[152:155], a[72:75], v[128:131], v[152:155]\n"
      "v_add_f32 v169, v169, v157\n"
      "v_add_f32 v172, v172, v160\n"
      "v_mfma_f32_16x16x32_bf16 v[152:155], a[76:79], v[132:135], v[152:155]\n"
      "v_mfma_f32_16x16x32_bf16 v[152:155], a[80:83], v[136:139], v[152:155]\n"
      "s_cbranch_vccnz .Lsp_d3_6\n"
      ".Lbk_d3_6:\n"
      "v_mfma_f32_16x16x32_bf16 v[152:155], a[84:87], v[140:143], v[152:155]\n"
      "s_waitcnt lgkmcnt(0)\n"
      "v_mfma_f32_16x16x32_bf16 v[152:155], a[88:91], v[144:147], v[152:155]\n"
      "v_mfma_f32_16x16x32_bf16 v[152:155], a[92:95], v[148:151], v[152:155]\n"
      "buffer_load_dwordx4 v0, s[8:11], s54 offen offset:2048 sc1 lds\n"
      "v_mfma_f32_16x16x32_bf16 v[156:159], a[96:99], v[56:59], 0\n"
      "v_mfma_f32_16x16x32_bf16 v[156:159], a[100:103], v[60:63], v[156:159]\n"
      "v_add_u32 v14, 1, v14\n"
      "v_mfma_f32_16x16x32_bf16 v[156:159], a[104:107], v[64:67], v[156:159]\n"
      "v_cvt_pk_bf16_f32 v225, v154, v155\n"
      "v_mfma_f32_16x16x32_bf16 v[156:159], a[108:111], v[68:71], v[156:159]\n"
      "v_add_f32 v175, v175, v163\n"
      "v_mfma_f32_16x16x32_bf16 v[156:159], a[112:115], v[72:75], v[156:159]\n"
      "buffer_load_dwordx4 v0, s[8:11], s54 offen offset:1024 sc1 lds\n"
      "v_mfma_f32_16x16x32_bf16 v[156:159], a[116:119], v[76:79], v[156:159]\n"
      "v_mfma_f32_16x16x32_bf16 v[156:159], a[120:123], v[80:83], v[156:159]\n"
      "v_mfma_f32_16x16x32_bf16 v[156:159], a[124:127], v[84:87], v[156:159]\n"
      "v_and_b32 v173, 0xffff0000, v220\n"
      "v_mfma_f32_16x16x32_bf16 v[156:159], a[128:131], v[88:91], v[156:159]\n"
      "buffer_load_dword v20, v2, s[8:11], s36 offen sc1\n"
      "v_mfma_f32_16x16x32_bf16 v[156:159], a[132:135], v[92:95], v[156:159]\n"
      "s_waitcnt vmcnt(5)\n"
      "ds_write_b32 v16, v14\n"
      "v_mfma_f32_16x16x32_bf16 v[156:159], a[136:139], v[96:99], v[156:159]\n"
      "buffer_load_dwordx4 v0, s[8:11], s54 offen offset:0 sc1 lds\n"
      "v_mfma_f32_16x16x32_bf16 v[156:159], a[140:143], v[100:103], v[156:159]\n"
      "v_add_f32 v173, v173, v161\n"
      "v_mfma_f32_16x16x32_bf16 v[156:159], a[144:147], v[104:107], v[156:159]\n"
      "v_mfma_f32_16x16x32_bf16 v[156:159], a[148:151], v[108:111], v[156:159]\n"
      "v_mfma_f32_16x16x32_bf16 v[156:159], a[152:155], v[112:115], v[156:159]\n"
      "v_mfma_f32_16x16x32_bf16 v[156:159], a[156:159], v[116:119], v[156:159]\n"
      "buffer_load_dwordx4 v0, s[8:11], s54 offen offset:3072 sc1 lds\n"
      "v_mfma_f32_16x16x32_bf16 v[156:159], a[160:163], v[120:123], v[156:159]\n"
      "s_add_u32 m0, s43, 77824\n"
      "v_mfma_f32_16x16x32_bf16 v[156:159], a[164:167], v[124:127], v[156:159]\n"
      "v_mfma_f32_16x16x32_bf16 v[156:159], a[168:171], v[128:131], v[156:159]\n"
      "ds_read_b32 v15, v17\n"
      "v_mfma_f32_16x16x32_bf16 v[156:159], a[172:175], v[132:135], v[156:159]\n"
      "s_add_u32 s55, s55, 173056\n"
      "v_mfma_f32_16x16x32_bf16 v[156:159], a[176:179], v[136:139], v[156:159]\n"
      "buffer_load_dwordx4 v1, s[8:11], s54 offen offset:0 sc1 lds\n"
      "v_mfma_f32_16x16x32_bf16 v[156:159], a[180:183], v[140:143], v[156:159]\n"
      "v_mfma_f32_16x16x32_bf16 v[156:159], a[184:187], v[144:147], v[156:159]\n"
      "v_cvt_pk_bf16_f32 v224, v152, v153\n"
      "v_mfma_f32_16x16x32_bf16 v[156:159], a[188:191], v[148:151], v[156:159]\n"
      "s_waitcnt lgkmcnt(0)\n"
      "v_cmp_gt_i32 vcc, v14, v15\n"
      "v_mfma_f32_16x16x32_bf16 v[160:163], a[192:195], v[56:59], 0\n"
      "buffer_load_dwordx4 v1, s[8:11], s54 offen offset:1024 sc1 lds\n"
      "v_mfma_f32_16x16x32_bf16 v[160:163], a[196:199], v[60:63], v[160:163]\n"
      "v_mfma_f32_16x16x32_bf16 v[160:163], a[200:203], v[64:67], v[160:163]\n"
      "s_cbranch_vccnz .Lsp_dy2_7\n"
      ".Lbk_dy2_7:\n"
      "v_mfma_f32_16x16x32_bf16 v[160:163], a[204:207], v[68:71], v[160:163]\n"
      "ds_read_b128 v[68:71], v22 offset:3072\n"
      "v_mfma_f32_16x16x32_bf16 v[160:163], a[208:211], v[72:75], v[160:163]\n"
      "ds_read_b128 v[60:63], v22 offset:1024\n"
      "v_mfma_f32_16x16x32_bf16 v[160:163], a[212:215], v[76:79], v[160:163]\n"
      "s_waitcnt vmcnt(7)\n"
      "buffer_store_dword v18, v5, s[8:11], s59 offen sc1\n"
      "v_mfma_f32_16x16x32_bf16 v[160:163], a[216:219], v[80:83], v[160:163]\n"
      "v_cvt_pk_bf16_f32 v227, v158, v159\n"
      "v_mfma_f32_16x16x32_bf16 v[160:163], a[220:223], v[84:87], v[160:163]\n"
      "ds_read_b128 v[80:83], v22 offset:6144\n"
      "v_cvt_pk_bf16_f32 v226, v156, v157\n"
      "v_mfma_f32_16x16x32_bf16 v[160:163], a[224:227], v[88:91], v[160:163]\n"
      "ds_read_b128 v[72:75], v22 offset:4096\n"
      "v_mfma_f32_16x16x32_bf16 v[160:163], a[228:231], v[92:95], v[160:163]\n"
      "ds_read_b128 v[76:79], v22 offset:5120\n"
      "v_mfma_f32_16x16x32_bf16 v[160:163], a[232:235], v[96:99], v[160:163]\n"
      "ds_read_b128 v[96:99], v22 offset:10240\n"
      "v_mfma_f32_16x16x32_bf16 v[160:163], a[236:239], v[100:103], v[160:163]\n"
      "v_mfma_f32_16x16x32_bf16 v[160:163], a[240:243], v[104:107], v[160:163]\n"
      "buffer_store_dwordx4 v[224:227], v0, s[8:11], s37 offen sc1\n"
      "v_mfma_f32_16x16x32_bf16 v[160:163], a[244:247], v[108:111], v[160:163]\n"
      "v_mfma_f32_16x16x32_bf16 v[160:163], a[248:251], v[112:115], v[160:163]\n"
      "ds_read_b128 v[84:87], v22 offset:7168\n"
      "v_mfma_f32_16x16x32_bf16 v[160:163], a[252:255], v[116:119], v[160:163]\n"
      "v_mfma_f32_16x16x32_bf16 v[160:163], v[24:27], v[120:123], v[160:163]\n"
      "ds_read_b128 v[56:59], v22 offset:0\n"
      "v_mfma_f32_16x16x32_bf16 v[160:163], v[28:31], v[124:127], v[160:163]\n"
      "ds_read_b128 v[92:95], v22 offset:9216\n"
      "s_mov_b32 s54, s36\n"
      "v_mfma_f32_16x16x32_bf16 v[160:163], v[32:35], v[128:131], v[160:163]\n"
      "ds_read_b128 v[64:67], v22 offset:2048\n"
      "v_mfma_f32_16x16x32_bf16 v[160:163], v[36:39], v[132:135], v[160:163]\n"
      "ds_read_b128 v[100:103], v22 offset:11264\n"
      "v_mfma_f32_16x16x32_bf16 v[160:163], v[40:43], v[136:139], v[160:163]\n"
      "ds_read_b128 v[132:135], v22 offset:19456\n"
      "v_mfma_f32_16x16x32_bf16 v[160:163], v[44:47], v[140:143], v[160:163]\n"
      "v_mfma_f32_16x16x32_bf16 v[160:163], v[48:51], v[144:147], v[160:163]\n"
      "s_waitcnt lgkmcnt(4)\n"
      "ds_read_b128 v[112:115], v22 offset:14336\n"
      "s_add_u32 m0, s43, 98304\n"
      "v_mfma_f32_16x16x32_bf16 v[160:163], v[52:55], v[148:151], v[160:163]\n"
      "s_sub_u32 s62, s62, 1\n"
      "s_cbranch_scc1 .Ld_out1\n"
      ".Ld_t2:\n"
      "v_mfma_f32_16x16x32_bf16 v[152:155], a[0:3], v[56:59], 0\n"
      "ds_read_b128 v[88:91], v22 offset:8192\n"
      "v_mfma_f32_16x16x32_bf16 v[152:155], a[4:7], v[60:63], v[152:155]\n"
      "s_waitcnt lgkmcnt(4)\n"
      "ds_read_b128 v[120:123], v22 offset:16384\n"
      "v_mfma_f32_16x16x32_bf16 v[152:155], a[8:11], v[64:67], v[152:155]\n"
      "s_add_u32 s36, s36, 165888\n"
      "ds_read_b128 v[104:107], v22 offset:12288\n"
      "v_cvt_pk_bf16_f32 v229, v162, v163\n"
      "v_mfma_f32_16x16x32_bf16 v[152:155], a[12:15], v[68:71], v[152:155]\n"
      "ds_read_b128 v[148:151], v22 offset:23552\n"
      "v_cvt_pk_bf16_f32 v228, v160, v161\n"
      "v_mfma_f32_16x16x32_bf16 v[152:155], a[16:19], v[72:75], v[152:155]\n"
      "buffer_store_dwordx2 v[228:229], v13, s[8:11], s37 offen sc1\n"
      "v_mfma_f32_16x16x32_bf16 v[152:155], a[20:23], v[76:79], v[152:155]\n"
      "ds_read_b128 v[108:111], v22 offset:13312\n"
      "v_mfma_f32_16x16x32_bf16 v[152:155], a[24:27], v[80:83], v[152:155]\n"
      "ds_read_b128 v[116:119], v22 offset:15360\n"
      "v_mfma_f32_16x16x32_bf16 v[152:155], a[28:31], v[84:87], v[152:155]\n"
      "s_waitcnt lgkmcnt(5)\n"
      "ds_read_b128 v[124:127], v22 offset:17408\n"
      "v_mfma_f32_16x16x32_bf16 v[152:155], a[32:35], v[88:91], v[152:155]\n"
      "ds_read_b128 v[140:143], v22 offset:21504\n"
      "v_mfma_f32_16x16x32_bf16 v[152:155], a[36:39], v[92:95], v[152:155]\n"
      "ds_read_b128 v[128:131], v22 offset:18432\n"
      "v_mfma_f32_16x16x32_bf16 v[152:155], a[40:43], v[96:99], v[152:155]\n"
      "v_mfma_f32_16x16x32_bf16 v[152:155], a[44:47], v[100:103], v[152:155]\n"
      "s_waitcnt lgkmcnt(5)\n"
      "ds_read_b128 v[136:139], v22 offset:20480\n"
      "v_mfma_f32_16x16x32_bf16 v[152:155], a[48:51], v[104:107], v[152:155]\n"
      "s_waitcnt lgkmcnt(5)\n"
      "v_mfma_f32_16x16x32_bf16 v[152:155], a[52:55], v[108:111], v[152:155]\n"
      "v_mfma_f32_16x16x32_bf16 v[152:155], a[56:59], v[112:115], v[152:155]\n"
      "s_waitcnt lgkmcnt(3)\n"
      "ds_read_b128 v[144:147], v22 offset:22528\n"
      "v_mfma_f32_16x16x32_bf16 v[152:155], a[60:63], v[116:119], v[152:155]\n"
      "v_add_u32 v14, 1, v14\n"
      "v_mfma_f32_16x16x32_bf16 v[152:155], a[64:67], v[120:123], v[152:155]\n"
      "s_waitcnt vmcnt(7) lgkmcnt(2)\n"
      "v_cmp_ne_u32 vcc, s21, v20\n"
      "v_mfma_f32_16x16x32_bf16 v[152:155], a[68:71], v[124:127], v[152:155]\n"
      "v_mfma_f32_16x16x32_bf16 v[152:155], a[72:75], v[128:131], v[152:155]\n"
      "v_mfma_f32_16x16x32_bf16 v[152:155], a[76:79], v[132:135], v[152:155]\n"
      "s_waitcnt lgkmcnt(1)\n"
      "v_mfma_f32_16x16x32_bf16 v[152:155], a[80:83], v[136:139], v[152:155]\n"
      "v_mfma_f32_16x16x32_bf16 v[152:155], a[84:87], v[140:143], v[152:155]\n"
      "s_waitcnt lgkmcnt(0)\n"
      "s_cbranch_vccnz .Lsp_d4_8\n"
      ".Lbk_d4_8:\n"
      "v_mfma_f32_16x16x32_bf16 v[152:155], a[88:91], v[144:147], v[152:155]\n"
      "v_mfma_f32_16x16x32_bf16 v[152:155], a[92:95], v[148:151], v[152:155]\n"
      "s_min_u32 s36, s36, s6\n"
      "v_mfma_f32_16x16x32_bf16 v[156:159], a[96:99], v[56:59], 0\n"
      "buffer_load_dwordx4 v0, s[8:11], s54 offen offset:0 sc1 lds\n"
      "v_mfma_f32_16x16x32_bf16 v[156:159], a[100:103], v[60:63], v[156:159]\n"
      "v_mfma_f32_16x16x32_bf16 v[156:159], a[104:107], v[64:67], v[156:159]\n"
      "v_mfma_f32_16x16x32_bf16 v[156:159], a[108:111], v[68:71], v[156:159]\n"
      "v_mfma_f32_16x16x32_bf16 v[156:159], a[112:115], v[72:75], v[156:159]\n"
      "buffer_load_dwordx4 v0, s[8:11], s54 offen offset:2048 sc1 lds\n"
      "v_mfma_f32_16x16x32_bf16 v[156:159], a[116:119], v[76:79], v[156:159]\n"
      "s_waitcnt vmcnt(5)\n"
      "ds_write_b32 v16, v14\n"
      "v_mfma_f32_16x16x32_bf16 v[156:159], a[120:123], v[80:83], v[156:159]\n"
      "v_mfma_f32_16x16x32_bf16 v[156:159], a[124:127], v[84:87], v[156:159]\n"
      "v_mfma_f32_16x16x32_bf16 v[156:159], a[128:131], v[88:91], v[156:159]\n"
      "v_mfma_f32_16x16x32_bf16 v[156:159], a[132:135], v[92:95], v[156:159]\n"
      "buffer_load_dword v20, v2, s[8:11], s36 offen sc1\n"
      "v_mfma_f32_16x16x32_bf16 v[156:159], a[136:139], v[96:99], v[156:159]\n"
      "v_mfma_f32_16x16x32_bf16 v[156:159], a[140:143], v[100:103], v[156:159]\n"
      "v_cvt_pk_bf16_f32 v225, v154, v155\n"
      "v_mfma_f32_16x16x32_bf16 v[156:159], a[144:147], v[104:107], v[156:159]\n"
      "buffer_load_dwordx4 v0, s[8:11], s54 offen offset:3072 sc1 lds\n"
      "v_mfma_f32_16x16x32_bf16 v[156:159], a[148:151], v[108:111], v[156:159]\n"
      "v_mfma_f32_16x16x32_bf16 v[156:159], a[152:155], v[112:115], v[156:159]\n"
      "v_mfma_f32_16x16x32_bf16 v[156:159], a[156:159], v[116:119], v[156:159]\n"
      "ds_read_b32 v15, v17\n"
      "v_mfma_f32_16x16x32_bf16 v[156:159], a[160:163], v[120:123], v[156:159]\n"
      "buffer_load_dwordx4 v0, s[8:11], s54 offen offset:1024 sc1 lds\n"
      "v_mfma_f32_16x16x32_bf16 v[156:159], a[164:167], v[124:127], v[156:159]\n"
      "s_add_u32 m0, s43, 102400\n"
      "v_mfma_f32_16x16x32_bf16 v[156:159], a[168:171], v[128:131], v[156:159]\n"
      "v_mfma_f32_16x16x32_bf16 v[156:159], a[172:175], v[132:135], v[156:159]\n"
      "v_mfma_f32_16x16x32_bf16 v[156:159], a[176:179], v[136:139], v[156:159]\n"
      "buffer_load_dwordx4 v1, s[8:11], s54 offen offset:1024 sc1 lds\n"
      "v_mfma_f32_16x16x32_bf16 v[156:159], a[180:183], v[140:143], v[156:159]\n"
      "v_mfma_f32_16x16x32_bf16 v[156:159], a[184:187], v[144:147], v[156:159]\n"
      "s_waitcnt lgkmcnt(0)\n"
      "v_cmp_gt_i32 vcc, v14, v15\n"
      "v_mfma_f32_16x16x32_bf16 v[156:159], a[188:191], v[148:151], v[156:159]\n"
      "v_mfma_f32_16x16x32_bf16 v[160:163], a[192:195], v[56:59], 0\n"
      "buffer_load_dwordx4 v1, s[8:11], s54 offen offset:0 sc1 lds\n"
      "v_mfma_f32_16x16x32_bf16 v[160:163], a[196:199], v[60:63], v[160:163]\n"
      "s_cbranch_vccnz .Lsp_dy3_9\n"
      ".Lbk_dy3_9:\n"
      "ds_read_b128 v[60:63], v22 offset:25600\n"
      "v_mfma_f32_16x16x32_bf16 v[160:163], a[200:203], v[64:67], v[160:163]\n"
      "v_mfma_f32_16x16x32_bf16 v[160:163], a[204:207], v[68:71], v[160:163]\n"
      "v_cvt_pk_bf16_f32 v227, v158, v159\n"
      "v_mfma_f32_16x16x32_bf16 v[160:163], a[208:211], v[72:75], v[160:163]\n"
      "ds_read_b128 v[64:67], v22 offset:26624\n"
      "v_mfma_f32_16x16x32_bf16 v[160:163], a[212:215], v[76:79], v[160:163]\n"
      "s_mov_b32 s54, s36\n"
      "ds_read_b128 v[72:75], v22 offset:28672\n"
      "v_mfma_f32_16x16x32_bf16 v[160:163], a[216:219], v[80:83], v[160:163]\n"
      "ds_read_b128 v[68:71], v22 offset:27648\n"
      "v_mfma_f32_16x16x32_bf16 v[160:163], a[220:223], v[84:87], v[160:163]\n"
      "v_cvt_pk_bf16_f32 v224, v152, v153\n"
      "v_mfma_f32_16x16x32_bf16 v[160:163], a[224:227], v[88:91], v[160:163]\n"
      "s_waitcnt vmcnt(7) lgkmcnt(3)\n"
      "buffer_store_dword v18, v4, s[8:11], s55 offen sc1\n"
      "v_mfma_f32_16x16x32_bf16 v[160:163], a[228:231], v[92:95], v[160:163]\n"
      "ds_read_b128 v[92:95], v22 offset:33792\n"
      "v_mfma_f32_16x16x32_bf16 v[160:163], a[232:235], v[96:99], v[160:163]\n"
      "ds_read_b128 v[56:59], v22 offset:24576\n"
      "v_mfma_f32_16x16x32_bf16 v[160:163], a[236:239], v[100:103], v[160:163]\n"
      "buffer_load_dword v21, v3, s[8:11], s56 offen sc1\n"
      "v_mfma_f32_16x16x32_bf16 v[160:163], a[240:243], v[104:107], v[160:163]\n"
      "ds_read_b128 v[100:103], v22 offset:35840\n"
      "v_mfma_f32_16x16x32_bf16 v[160:163], a[244:247], v[108:111], v[160:163]\n"
      "ds_read_b128 v[80:83], v22 offset:30720\n"
      "v_mfma_f32_16x16x32_bf16 v[160:163], a[248:251], v[112:115], v[160:163]\n"
      "ds_read_b128 v[112:115], v22 offset:38912\n"
      "v_mfma_f32_16x16x32_bf16 v[160:163], a[252:255], v[116:119], v[160:163]\n"
      "s_add_u32 s37, s37, 106496\n"
      "ds_read_b128 v[88:91], v22 offset:32768\n"
      "v_mfma_f32_16x16x32_bf16 v[160:163], v[24:27], v[120:123], v[160:163]\n"
      "ds_read_b128 v[96:99], v22 offset:34816\n"
      "v_cvt_pk_bf16_f32 v226, v156, v157\n"
      "v_mfma_f32_16x16x32_bf16 v[160:163], v[28:31], v[124:127], v[160:163]\n"
      "ds_read_b128 v[76:79], v22 offset:29696\n"
      "v_mfma_f32_16x16x32_bf16 v[160:163], v[32:35], v[128:131], v[160:163]\n"
      "v_mfma_f32_16x16x32_bf16 v[160:163], v[36:39], v[132:135], v[160:163]\n"
      "buffer_store_dwordx4 v[224:227], v0, s[8:11], s37 offen sc1\n"
      "v_mfma_f32_16x16x32_bf16 v[160:163], v[40:43], v[136:139], v[160:163]\n"
      "ds_read_b128 v[116:119], v22 offset:39936\n"
      "v_mfma_f32_16x16x32_bf16 v[160:163], v[44:47], v[140:143], v[160:163]\n"
      "ds_read_b128 v[84:87], v22 offset:31744\n"
      "v_mfma_f32_16x16x32_bf16 v[160:163], v[48:51], v[144:147], v[160:163]\n"
      "s_waitcnt lgkmcnt(4)\n"
      "ds_read_b128 v[104:107], v22 offset:36864\n"
      "v_mfma_f32_16x16x32_bf16 v[160:163], v[52:55], v[148:151], v[160:163]\n"
      "s_sub_u32 s62, s62, 1\n"
      "s_cbranch_scc1 .Ld_out2\n"
      ".Ld_t3:\n"
      "v_mfma_f32_16x16x32_bf16 v[152:155], a[0:3], v[56:59], 0\n"
      "ds_read_b128 v[120:123], v22 offset:40960\n"
      "v_mfma_f32_16x16x32_bf16 v[152:155], a[4:7], v[60:63], v[152:155]\n"
      "ds_read_b128 v[108:111], v22 offset:37888\n"
      "v_mfma_f32_16x16x32_bf16 v[152:155], a[8:11], v[64:67], v[152:155]\n"
      "ds_read_b128 v[132:135], v22 offset:44032\n"
      "v_mfma_f32_16x16x32_bf16 v[152:155], a[12:15], v[68:71], v[152:155]\n"
      "ds_read_b128 v[136:139], v22 offset:45056\n"
      "v_cvt_pk_bf16_f32 v229, v162, v163\n"
      "v_mfma_f32_16x16x32_bf16 v[152:155], a[16:19], v[72:75], v[152:155]\n"
      "s_waitcnt lgkmcnt(5)\n"
      "v_mfma_f32_16x16x32_bf16 v[152:155], a[20:23], v[76:79], v[152:155]\n"
      "ds_read_b128 v[124:127], v22 offset:41984\n"
      "v_mfma_f32_16x16x32_bf16 v[152:155], a[24:27], v[80:83], v[152:155]\n"
      "s_add_u32 m0, s43, 122880\n"
      "v_cvt_pk_bf16_f32 v228, v160, v161\n"
      "v_mfma_f32_16x16x32_bf16 v[152:155], a[28:31], v[84:87], v[152:155]\n"
      "buffer_store_dwordx2 v[228:229], v13, s[8:11], s37 offen sc1\n"
      "v_mfma_f32_16x16x32_bf16 v[152:155], a[32:35], v[88:91], v[152:155]\n"
      "v_mfma_f32_16x16x32_bf16 v[152:155], a[36:39], v[92:95], v[152:155]\n"
      "ds_read_b128 v[128:131], v22 offset:43008\n"
      "v_mfma_f32_16x16x32_bf16 v[152:155], a[40:43], v[96:99], v[152:155]\n"
      "ds_read_b128 v[144:147], v22 offset:47104\n"
      "v_mfma_f32_16x16x32_bf16 v[152:155], a[44:47], v[100:103], v[152:155]\n"
      "s_waitcnt lgkmcnt(3)\n"
      "ds_read_b128 v[148:151], v22 offset:48128\n"
      "v_mfma_f32_16x16x32_bf16 v[152:155], a[48:51], v[104:107], v[152:155]\n"
      "ds_read_b128 v[140:143], v22 offset:46080\n"
      "v_mfma_f32_16x16x32_bf16 v[152:155], a[52:55], v[108:111], v[152:155]\n"
      "v_mfma_f32_16x16x32_bf16 v[152:155], a[56:59], v[112:115], v[152:155]\n"
      "s_waitcnt vmcnt(8) lgkmcnt(4)\n"
      "v_cmp_ne_u32 vcc, s21, v20\n"
      "v_mfma_f32_16x16x32_bf16 v[152:155], a[60:63], v[116:119], v[152:155]\n"
      "v_mfma_f32_16x16x32_bf16 v[152:155], a[64:67], v[120:123], v[152:155]\n"
      "s_add_u32 s36, s36, 165888\n"
      "s_add_u32 s55, s55, 106496\n"
      "v_mfma_f32_16x16x32_bf16 v[152:155], a[68:71], v[124:127], v[152:155]\n"
      "s_waitcnt lgkmcnt(2)\n"
      "v_mfma_f32_16x16x32_bf16 v[152:155], a[72:75], v[128:131], v[152:155]\n"
      "s_cbranch_vccnz .Lsp_d5_10\n"
      ".Lbk_d5_10:\n"
      "v_mfma_f32_16x16x32_bf16 v[152:155], a[76:79], v[132:135], v[152:155]\n"
      "buffer_load_dwordx4 v0, s[8:11], s54 offen offset:1024 sc1 lds\n"
      "v_mfma_f32_16x16x32_bf16 v[152:155], a[80:83], v[136:139], v[152:155]\n"
      "s_waitcnt lgkmcnt(0)\n"
      "v_mfma_f32_16x16x32_bf16 v[152:155], a[84:87], v[140:143], v[152:155]\n"
      "v_add_u32 v14, 1, v14\n"
      "v_mfma_f32_16x16x32_bf16 v[152:155], a[88:91], v[144:147], v[152:155]\n"
      "v_mfma_f32_16x16x32_bf16 v[152:155], a[92:95], v[148:151], v[152:155]\n"
      "buffer_load_dwordx4 v0, s[8:11], s54 offen offset:0 sc1 lds\n"
      "v_mfma_f32_16x16x32_bf16 v[156:159], a[96:99], v[56:59], 0\n"
      "v_mfma_f32_16x16x32_bf16 v[156:159], a[100:103], v[60:63], v[156:159]\n"
      "v_mfma_f32_16x16x32_bf16 v[156:159], a[104:107], v[64:67], v[156:159]\n"
      "v_mfma_f32_16x16x32_bf16 v[156:159], a[108:111], v[68:71], v[156:159]\n"
      "buffer_load_dwordx4 v0, s[8:11], s54 offen offset:2048 sc1 lds\n"
      "v_mfma_f32_16x16x32_bf16 v[156:159], a[112:115], v[72:75], v[156:159]\n"
      "s_waitcnt vmcnt(7)\n"
      "ds_write_b32 v16, v14\n"
      "v_mfma_f32_16x16x32_bf16 v[156:159], a[116:119], v[76:79], v[156:159]\n"
      "v_mfma_f32_16x16x32_bf16 v[156:159], a[120:123], v[80:83], v[156:159]\n"
      "v_mfma_f32_16x16x32_bf16 v[156:159], a[124:127], v[84:87], v[156:159]\n"
      "buffer_load_dwordx4 v0, s[8:11], s54 offen offset:3072 sc1 lds\n"
      "v_mfma_f32_16x16x32_bf16 v[156:159], a[128:131], v[88:91], v[156:159]\n"
      "v_mfma_f32_16x16x32_bf16 v[156:159], a[132:135], v[92:95], v[156:159]\n"
      "v_cvt_pk_bf16_f32 v224, v152, v153\n"
      "v_mfma_f32_16x16x32_bf16 v[156:159], a[136:139], v[96:99], v[156:159]\n"
      "v_mfma_f32_16x16x32_bf16 v[156:159], a[140:143], v[100:103], v[156:159]\n"
      "s_min_u32 s36, s36, s6\n"
      "s_add_u32 m0, s43, 126976\n"
      "v_mfma_f32_16x16x32_bf16 v[156:159], a[144:147], v[104:107], v[156:159]\n"
      "buffer_load_dwordx4 v1, s[8:11], s54 offen offset:1024 sc1 lds\n"
      "v_mfma_f32_16x16x32_bf16 v[156:159], a[148:151], v[108:111], v[156:159]\n"
      "ds_read_b32 v15, v17\n"
      "v_mfma_f32_16x16x32_bf16 v[156:159], a[152:155], v[112:115], v[156:159]\n"
      "v_mfma_f32_16x16x32_bf16 v[156:159], a[156:159], v[116:119], v[156:159]\n"
      "v_mfma_f32_16x16x32_bf16 v[156:159], a[160:163], v[120:123], v[156:159]\n"
      "buffer_load_dword v20, v2, s[8:11], s36 offen sc1\n"
      "v_mfma_f32_16x16x32_bf16 v[156:159], a[164:167], v[124:127], v[156:159]\n"
      "v_mfma_f32_16x16x32_bf16 v[156:159], a[168:171], v[128:131], v[156:159]\n"
      "s_waitcnt lgkmcnt(0)\n"
      "v_cmp_gt_i32 vcc, v14, v15\n"
      "v_mfma_f32_16x16x32_bf16 v[156:159], a[172:175], v[132:135], v[156:159]\n"
      "buffer_load_dwordx4 v1, s[8:11], s54 offen offset:0 sc1 lds\n"
      "v_mfma_f32_16x16x32_bf16 v[156:159], a[176:179], v[136:139], v[156:159]\n"
      "s_add_u32 s58, s56, 6656\n"
      "v_mfma_f32_16x16x32_bf16 v[156:159], a[180:183], v[140:143], v[156:159]\n"
      "s_add_u32 s57, s56, 13312\n"
      "v_mfma_f32_16x16x32_bf16 v[156:159], a[184:187], v[144:147], v[156:159]\n"
      "s_waitcnt vmcnt(9)\n"
      "s_cbranch_vccnz .Lsp_dy4_11\n"
      ".Lbk_dy4_11:\n"
      "v_cmp_ne_u32 vcc, s21, v21\n"
      "v_mfma_f32_16x16x32_bf16 v[156:159], a[188:191], v[148:151], v[156:159]\n"
      ".Ld_p1:\n"
      "s_cbranch_execz .Ld_pr\n"
      "v_mfma_f32_16x16x32_bf16 v[160:163], a[192:195], v[56:59], 0\n"
      "v_mfma_f32_16x16x32_bf16 v[160:163], a[196:199], v[60:63], v[160:163]\n"
      "s_cbranch_vccnz .Lsp_df_12\n"
      ".Lbk_df_12:\n"
      "buffer_load_dwordx4 v[176:179], v0, s[8:11], s56 offen sc1\n"
      "v_mfma_f32_16x16x32_bf16 v[160:163], a[200:203], v[64:67], v[160:163]\n"
      "ds_read_b128 v[60:63], v23 offset:1024\n"
      "v_mfma_f32_16x16x32_bf16 v[160:163], a[204:207], v[68:71], v[160:163]\n"
      "ds_read_b128 v[64:67], v23 offset:2048\n"
      "v_mfma_f32_16x16x32_bf16 v[160:163], a[208:211], v[72:75], v[160:163]\n"
      "buffer_load_dwordx2 v[180:181], v13, s[8:11], s56 offen sc1\n"
      "v_mfma_f32_16x16x32_bf16 v[160:163], a[212:215], v[76:79], v[160:163]\n"
      "ds_read_b128 v[72:75], v23 offset:4096\n"
      "v_mfma_f32_16x16x32_bf16 v[160:163], a[216:219], v[80:83], v[160:163]\n"
      "ds_read_b128 v[68:71], v23 offset:3072\n"
      "s_add_u32 s61, s41, 4294967264\n"
      "v_mfma_f32_16x16x32_bf16 v[160:163], a[220:223], v[84:87], v[160:163]\n"
      "ds_read_b128 v[80:83], v23 offset:6144\n"
      "v_mfma_f32_16x16x32_bf16 v[160:163], a[224:227], v[88:91], v[160:163]\n"
      "v_cvt_pk_bf16_f32 v225, v154, v155\n"
      "v_mfma_f32_16x16x32_bf16 v[160:163], a[228:231], v[92:95], v[160:163]\n"
      "buffer_load_dwordx4 v[184:187], v0, s[8:11], s58 offen sc1\n"
      "v_mfma_f32_16x16x32_bf16 v[160:163], a[232:235], v[96:99], v[160:163]\n"
      "ds_read_b128 v[84:87], v23 offset:7168\n"
      "v_cvt_pk_bf16_f32 v226, v156, v157\n"
      "v_mfma_f32_16x16x32_bf16 v[160:163], a[236:239], v[100:103], v[160:163]\n"
      "ds_read_b128 v[76:79], v23 offset:5120\n"
      "v_mfma_f32_16x16x32_bf16 v[160:163], a[240:243], v[104:107], v[160:163]\n"
      "s_add_u32 s37, s37, 106496\n"
      "ds_read_b128 v[56:59], v23 offset:0\n"
      "v_mfma_f32_16x16x32_bf16 v[160:163], a[244:247], v[108:111], v[160:163]\n"
      "s_waitcnt vmcnt(10) lgkmcnt(4)\n"
      "buffer_store_dword v18, v4, s[8:11], s55 offen sc1\n"
      "v_mfma_f32_16x16x32_bf16 v[160:163], a[248:251], v[112:115], v[160:163]\n"
      "ds_read_b128 v[88:91], v23 offset:8192\n"
      "v_mfma_f32_16x16x32_bf16 v[160:163], a[252:255], v[116:119], v[160:163]\n"
      "ds_read_b128 v[96:99], v23 offset:10240\n"
      "v_mfma_f32_16x16x32_bf16 v[160:163], v[24:27], v[120:123], v[160:163]\n"
      "buffer_load_dwordx4 v[192:195], v0, s[8:11], s57 offen sc1\n"
      "v_mfma_f32_16x16x32_bf16 v[160:163], v[28:31], v[124:127], v[160:163]\n"
      "v_mfma_f32_16x16x32_bf16 v[160:163], v[32:35], v[128:131], v[160:163]\n"
      "buffer_load_dwordx2 v[188:189], v13, s[8:11], s58 offen sc1\n"
      "v_mfma_f32_16x16x32_bf16 v[160:163], v[36:39], v[132:135], v[160:163]\n"
      "ds_read_b128 v[100:103], v23 offset:11264\n"
      "v_mfma_f32_16x16x32_bf16 v[160:163], v[40:43], v[136:139], v[160:163]\n"
      "s_add_u32 s58, s56, 19968\n"
      "ds_read_b128 v[132:135], v23 offset:19456\n"
      "v_mfma_f32_16x16x32_bf16 v[160:163], v[44:47], v[140:143], v[160:163]\n"
      "ds_read_b128 v[104:107], v23 offset:12288\n"
      "v_mfma_f32_16x16x32_bf16 v[160:163], v[48:51], v[144:147], v[160:163]\n"
      "s_waitcnt lgkmcnt(3)\n"
      "buffer_load_dwordx2 v[196:197], v13, s[8:11], s57 offen sc1\n"
      "v_mfma_f32_16x16x32_bf16 v[160:163], v[52:55], v[148:151], v[160:163]\n"
      "s_sub_u32 s62, s62, 1\n"
      "s_cbranch_scc1 .Ld_out3\n"
      ".Ld_t4:\n"
      "v_mfma_f32_16x16x32_bf16 v[152:155], a[0:3], v[56:59], 0\n"
      "ds_read_b128 v[92:95], v23 offset:9216\n"
      "v_mfma_f32_16x16x32_bf16 v[152:155], a[4:7], v[60:63], v[152:155]\n"
      "ds_read_b128 v[108:111], v23 offset:13312\n"
      "s_add_u32 m0, s43, 0\n"
      "v_mfma_f32_16x16x32_bf16 v[152:155], a[8:11], v[64:67], v[152:155]\n"
      "ds_read_b128 v[128:131], v23 offset:18432\n"
      "v_cvt_pk_bf16_f32 v228, v160, v161\n"
      "v_mfma_f32_16x16x32_bf16 v[152:155], a[12:15], v[68:71], v[152:155]\n"
      "buffer_load_dwordx4 v[200:203], v0, s[8:11], s58 offen sc1\n"
      "v_mfma_f32_16x16x32_bf16 v[152:155], a[16:19], v[72:75], v[152:155]\n"
      "ds_read_b128 v[116:119], v23 offset:15360\n"
      "v_cvt_pk_bf16_f32 v227, v158, v159\n"
      "v_mfma_f32_16x16x32_bf16 v[152:155], a[20:23], v[76:79], v[152:155]\n"
      "ds_read_b128 v[112:115], v23 offset:14336\n"
      "v_mfma_f32_16x16x32_bf16 v[152:155], a[24:27], v[80:83], v[152:155]\n"
      "s_mov_b32 s54, s36\n"
      "ds_read_b128 v[120:123], v23 offset:16384\n"
      "v_mfma_f32_16x16x32_bf16 v[152:155], a[28:31], v[84:87], v[152:155]\n"
      "buffer_store_dwordx4 v[224:227], v0, s[8:11], s37 offen sc1\n"
      "v_mfma_f32_16x16x32_bf16 v[152:155], a[32:35], v[88:91], v[152:155]\n"
      "s_waitcnt lgkmcnt(4)\n"
      "ds_read_b128 v[140:143], v23 offset:21504\n"
      "v_mfma_f32_16x16x32_bf16 v[152:155], a[36:39], v[92:95], v[152:155]\n"
      "ds_read_b128 v[124:127], v23 offset:17408\n"
      "v_mfma_f32_16x16x32_bf16 v[152:155], a[40:43], v[96:99], v[152:155]\n"
      "ds_read_b128 v[136:139], v23 offset:20480\n"
      "v_mfma_f32_16x16x32_bf16 v[152:155], a[44:47], v[100:103], v[152:155]\n"
      "v_cvt_pk_bf16_f32 v229, v162, v163\n"
      "v_mfma_f32_16x16x32_bf16 v[152:155], a[48:51], v[104:107], v[152:155]\n"
      "buffer_store_dwordx2 v[228:229], v13, s[8:11], s37 offen sc1\n"
      "v_mfma_f32_16x16x32_bf16 v[152:155], a[52:55], v[108:111], v[152:155]\n"
      "s_waitcnt lgkmcnt(3)\n"
      "ds_read_b128 v[148:151], v23 offset:23552\n"
      "v_add_u32_e64 v14, 1, v14\n"
      "v_mfma_f32_16x16x32_bf16 v[152:155], a[56:59], v[112:115], v[152:155]\n"
      "s_add_u32 s36, s36, 165888\n"
      "s_add_u32 s37, s37, 106496\n"
      "ds_read_b128 v[144:147], v23 offset:22528\n"
      "v_mfma_f32_16x16x32_bf16 v[152:155], a[60:63], v[116:119], v[152:155]\n"
      "v_mfma_f32_16x16x32_bf16 v[152:155], a[64:67], v[120:123], v[152:155]\n"
      "s_waitcnt lgkmcnt(3)\n"
      "s_min_u32 s36, s36, s6\n"
      "v_mfma_f32_16x16x32_bf16 v[152:155], a[68:71], v[124:127], v[152:155]\n"
      "buffer_load_dwordx2 v[204:205], v13, s[8:11], s58 offen sc1\n"
      "v_mfma_f32_16x16x32_bf16 v[152:155], a[72:75], v[128:131], v[152:155]\n"
      "s_cmp_lt_u32 s41, 32\n"
      "s_cselect_b32 s59, s39, 0x40000000\n"
      "v_mfma_f32_16x16x32_bf16 v[152:155], a[76:79], v[132:135], v[152:155]\n"
      "s_waitcnt lgkmcnt(2)\n"
      "s_add_u32 s57, s56, 26624\n"
      "v_mfma_f32_16x16x32_bf16 v[152:155], a[80:83], v[136:139], v[152:155]\n"
      "s_waitcnt vmcnt(12) lgkmcnt(1)\n"
      "s_add_u32 s56, s56, 599040\n"
      "v_cmp_ne_u32 vcc, s21, v20\n"
      "v_mfma_f32_16x16x32_bf16 v[152:155], a[84:87], v[140:143], v[152:155]\n"
      "s_waitcnt lgkmcnt(0)\n"
      "buffer_load_dwordx2 v[212:213], v13, s[8:11], s57 offen sc1\n"
      "v_mfma_f32_16x16x32_bf16 v[152:155], a[88:91], v[144:147], v[152:155]\n"
      "v_mfma_f32_16x16x32_bf16 v[152:155], a[92:95], v[148:151], v[152:155]\n"
      "s_add_u32 s39, s39, 147456\n"
      "v_mfma_f32_16x16x32_bf16 v[156:159], a[96:99], v[56:59], 0\n"
      "s_waitcnt vmcnt(12)\n"
      "ds_write_b32 v16, v14\n"
      "v_mfma_f32_16x16x32_bf16 v[156:159], a[100:103], v[60:63], v[156:159]\n"
      "buffer_load_dwordx4 v[208:211], v0, s[8:11], s57 offen sc1\n"
      "v_mfma_f32_16x16x32_bf16 v[156:159], a[104:107], v[64:67], v[156:159]\n"
      "s_cmp_lt_u32 s61, 32\n"
      "s_cselect_b32 s60, s40, 0x40000000\n"
      "v_mfma_f32_16x16x32_bf16 v[156:159], a[108:111], v[68:71], v[156:159]\n"
      "s_cbranch_vccnz .Lsp_d0_13\n"
      ".Lbk_d0_13:\n"
      "v_cvt_pk_bf16_f32 v224, v152, v153\n"
      "v_mfma_f32_16x16x32_bf16 v[156:159], a[112:115], v[72:75], v[156:159]\n"
      "v_cvt_pk_bf16_f32 v225, v154, v155\n"
      "v_mfma_f32_16x16x32_bf16 v[156:159], a[116:119], v[76:79], v[156:159]\n"
      "buffer_load_dwordx4 v0, s[8:11], s54 offen offset:0 sc1 lds\n"
      "v_mfma_f32_16x16x32_bf16 v[156:159], a[120:123], v[80:83], v[156:159]\n"
      "s_waitcnt vmcnt(13)\n"
      "v_lshlrev_b32 v234, 16, v178\n"
      "v_lshlrev_b32 v233, 16, v177\n"
      "v_mfma_f32_16x16x32_bf16 v[156:159], a[124:127], v[84:87], v[156:159]\n"
      "v_lshlrev_b32 v232, 16, v176\n"
      "v_add_f32 v166, v166, v233\n"
      "v_mfma_f32_16x16x32_bf16 v[156:159], a[128:131], v[88:91], v[156:159]\n"
      "s_add_u32 s55, s55, 106496\n"
      "v_add_f32 v164, v164, v232\n"
      "v_mfma_f32_16x16x32_bf16 v[156:159], a[132:135], v[92:95], v[156:159]\n"
      "buffer_load_dwordx4 v0, s[8:11], s54 offen offset:1024 sc1 lds\n"
      "v_mfma_f32_16x16x32_bf16 v[156:159], a[136:139], v[96:99], v[156:159]\n"
      "s_waitcnt vmcnt(13) lgkmcnt(0)\n"
      "v_lshlrev_b32 v233, 16, v181\n"
      "v_lshlrev_b32 v235, 16, v179\n"
      "v_mfma_f32_16x16x32_bf16 v[156:159], a[140:143], v[100:103], v[156:159]\n"
      "v_and_b32 v176, 0xffff0000, v176\n"
      "v_lshlrev_b32 v232, 16, v180\n"
      "v_mfma_f32_16x16x32_bf16 v[156:159], a[144:147], v[104:107], v[156:159]\n"
      "v_add_f32 v172, v172, v232\n"
      "v_and_b32 v181, 0xffff0000, v181\n"
      "v_mfma_f32_16x16x32_bf16 v[156:159], a[148:151], v[108:111], v[156:159]\n"
      "v_add_f32 v168, v168, v234\n"
      "v_add_f32 v174, v174, v233\n"
      "v_mfma_f32_16x16x32_bf16 v[156:159], a[152:155], v[112:115], v[156:159]\n"
      "buffer_load_dword v20, v2, s[8:11], s36 offen sc1\n"
      "v_mfma_f32_16x16x32_bf16 v[156:159], a[156:159], v[116:119], v[156:159]\n"
      "ds_read_b32 v15, v17\n"
      "v_add_f32 v165, v165, v176\n"
      "v_mfma_f32_16x16x32_bf16 v[156:159], a[160:163], v[120:123], v[156:159]\n"
      "buffer_load_dwordx4 v0, s[8:11], s54 offen offset:2048 sc1 lds\n"
      "v_mfma_f32_16x16x32_bf16 v[156:159], a[164:167], v[124:127], v[156:159]\n"
      "s_waitcnt vmcnt(14)\n"
      "v_lshlrev_b32 v232, 16, v184\n"
      "v_and_b32 v177, 0xffff0000, v177\n"
      "v_mfma_f32_16x16x32_bf16 v[156:159], a[168:171], v[128:131], v[156:159]\n"
      "s_add_u32 s40, s40, 4294856704\n"
      "v_add_f32 v167, v167, v177\n"
      "v_and_b32 v180, 0xffff0000, v180\n"
      "v_mfma_f32_16x16x32_bf16 v[156:159], a[172:175], v[132:135], v[156:159]\n"
      "v_add_f32 v164, v164, v232\n"
      "v_lshlrev_b32 v233, 16, v185\n"
      "v_mfma_f32_16x16x32_bf16 v[156:159], a[176:179], v[136:139], v[156:159]\n"
      "buffer_load_dwordx4 v0, s[8:11], s54 offen offset:3072 sc1 lds\n"
      "v_mfma_f32_16x16x32_bf16 v[156:159], a[180:183], v[140:143], v[156:159]\n"
      "s_waitcnt lgkmcnt(0)\n"
      "v_add_f32 v166, v166, v233\n"
      "v_cmp_gt_i32 vcc, v14, v15\n"
      "v_mfma_f32_16x16x32_bf16 v[156:159], a[184:187], v[144:147], v[156:159]\n"
      "s_waitcnt vmcnt(12)\n"
      "v_lshlrev_b32 v232, 16, v188\n"
      "v_add_f32 v172, v172, v232\n"
      "v_mfma_f32_16x16x32_bf16 v[156:159], a[188:191], v[148:151], v[156:159]\n"
      "v_and_b32 v185, 0xffff0000, v185\n"
      "v_lshlrev_b32 v232, 16, v192\n"
      "v_mfma_f32_16x16x32_bf16 v[160:163], a[192:195], v[56:59], 0\n"
      "s_add_u32 m0, s43, 4096\n"
      "v_lshlrev_b32 v234, 16, v186\n"
      "v_add_f32 v164, v164, v232\n"
      "v_mfma_f32_16x16x32_bf16 v[160:163], a[196:199], v[60:63], v[160:163]\n"
      "buffer_load_dwordx4 v1, s[8:11], s54 offen offset:1024 sc1 lds\n"
      "v_mfma_f32_16x16x32_bf16 v[160:163], a[200:203], v[64:67], v[160:163]\n"
      "s_cbranch_vccnz .Lsp_dy5_14\n"
      ".Lbk_dy5_14:\n"
      "v_cvt_pk_bf16_f32 v227, v158, v159\n"
      "v_mfma_f32_16x16x32_bf16 v[160:163], a[204:207], v[68:71], v[160:163]\n"
      "ds_read_b128 v[56:59], v23 offset:24576\n"
      "v_lshlrev_b32 v233, 16, v189\n"
      "v_mfma_f32_16x16x32_bf16 v[160:163], a[208:211], v[72:75], v[160:163]\n"
      "s_waitcnt vmcnt(12)\n"
      "ds_read_b128 v[64:67], v23 offset:26624\n"
      "v_lshlrev_b32 v232, 16, v196\n"
      "v_mfma_f32_16x16x32_bf16 v[160:163], a[212:215], v[76:79], v[160:163]\n"
      "buffer_load_dwordx4 v1, s[8:11], s54 offen offset:0 sc1 lds\n"
      "v_mfma_f32_16x16x32_bf16 v[160:163], a[216:219], v[80:83], v[160:163]\n"
      "ds_read_b128 v[76:79], v23 offset:29696\n"
      "v_add_f32 v174, v174, v233\n"
      "v_mfma_f32_16x16x32_bf16 v[160:163], a[220:223], v[84:87], v[160:163]\n"
      "v_and_b32 v184, 0xffff0000, v184\n"
      "v_add_f32 v172, v172, v232\n"
      "v_mfma_f32_16x16x32_bf16 v[160:163], a[224:227], v[88:91], v[160:163]\n"
      "v_add_f32 v170, v170, v235\n"
      "v_cvt_pk_bf16_f32 v226, v156, v157\n"
      "v_mfma_f32_16x16x32_bf16 v[160:163], a[228:231], v[92:95], v[160:163]\n"
      "s_add_u32 s41, s41, 6\n"
      "ds_read_b128 v[88:91], v23 offset:32768\n"
      "v_add_f32 v165, v165, v184\n"
      "v_mfma_f32_16x16x32_bf16 v[160:163], a[232:235], v[96:99], v[160:163]\n"
      "ds_read_b128 v[68:71], v23 offset:27648\n"
      "v_mfma_f32_16x16x32_bf16 v[160:163], a[236:239], v[100:103], v[160:163]\n"
      "ds_read_b128 v[96:99], v23 offset:34816\n"
      "v_add_f32 v168, v168, v234\n"
      "v_mfma_f32_16x16x32_bf16 v[160:163], a[240:243], v[104:107], v[160:163]\n"
      "ds_read_b128 v[80:83], v23 offset:30720\n"
      "v_lshlrev_b32 v233, 16, v193\n"
      "v_mfma_f32_16x16x32_bf16 v[160:163], a[244:247], v[108:111], v[160:163]\n"
      "ds_read_b128 v[72:75], v23 offset:28672\n"
      "v_and_b32 v178, 0xffff0000, v178\n"
      "v_mfma_f32_16x16x32_bf16 v[160:163], a[248:251], v[112:115], v[160:163]\n"
      "s_waitcnt vmcnt(12) lgkmcnt(5)\n"
      "s_mov_b32 s54, s36\n"
      "ds_read_b128 v[112:115], v23 offset:38912\n"
      "v_lshlrev_b32 v232, 16, v200\n"
      "v_mfma_f32_16x16x32_bf16 v[160:163], a[252:255], v[116:119], v[160:163]\n"
      "v_add_f32 v164, v164, v232\n"
      "v_and_b32 v186, 0xffff0000, v186\n"
      "v_mfma_f32_16x16x32_bf16 v[160:163], v[24:27], v[120:123], v[160:163]\n"
      "s_waitcnt vmcnt(10) lgkmcnt(5)\n"
      "buffer_store_dword v18, v4, s[8:11], s55 offen sc1\n"
      "v_mfma_f32_16x16x32_bf16 v[160:163], v[28:31], v[124:127], v[160:163]\n"
      "ds_read_b128 v[60:63], v23 offset:25600\n"
      "v_add_f32 v169, v169, v178\n"
      "v_mfma_f32_16x16x32_bf16 v[160:163], v[32:35], v[128:131], v[160:163]\n"
      "buffer_store_dwordx4 v[224:227], v0, s[8:11], s37 offen sc1\n"
      "v_mfma_f32_16x16x32_bf16 v[160:163], v[36:39], v[132:135], v[160:163]\n"
      "ds_read_b128 v[104:107], v23 offset:36864\n"
      "v_and_b32 v200, 0xffff0000, v200\n"
      "v_mfma_f32_16x16x32_bf16 v[160:163], v[40:43], v[136:139], v[160:163]\n"
      "ds_read_b128 v[84:87], v23 offset:31744\n"
      "v_add_f32 v173, v173, v180\n"
      "v_mfma_f32_16x16x32_bf16 v[160:163], v[44:47], v[140:143], v[160:163]\n"
      "ds_read_b128 v[140:143], v23 offset:46080\n"
      "v_add_f32 v166, v166, v233\n"
      "v_mfma_f32_16x16x32_bf16 v[160:163], v[48:51], v[144:147], v[160:163]\n"
      "ds_read_b128 v[100:103], v23 offset:35840\n"
      "v_lshlrev_b32_e64 v234, 16, v194\n"
      "v_mfma_f32_16x16x32_bf16 v[160:163], v[52:55], v[148:151], v[160:163]\n"
      "s_sub_u32 s62, s62, 1\n"
      "s_cbranch_scc1 .Ld_out4\n"
      ".Ld_t5:\n"
      "v_mfma_f32_16x16x32_bf16 v[152:155], a[0:3], v[56:59], 0\n"
      "s_waitcnt lgkmcnt(4)\n"
      "ds_read_b128 v[92:95], v23 offset:33792\n"
      "ds_read_b128 v[116:119], v23 offset:39936\n"
      "v_mfma_f32_16x16x32_bf16 v[152:155], a[4:7], v[60:63], v[152:155]\n"
      "ds_read_b128 v[108:111], v23 offset:37888\n"
      "v_and_b32 v192, 0xffff0000, v192\n"
      "v_mfma_f32_16x16x32_bf16 v[152:155], a[8:11], v[64:67], v[152:155]\n"
      "ds_read_b128 v[128:131], v23 offset:43008\n"
      "v_cvt_pk_bf16_f32 v229, v162, v163\n"
      "v_mfma_f32_16x16x32_bf16 v[152:155], a[12:15], v[68:71], v[152:155]\n"
      "ds_read_b128 v[136:139], v23 offset:45056\n"
      "v_add_f32 v175, v175, v181\n"
      "v_mfma_f32_16x16x32_bf16 v[152:155], a[16:19], v[72:75], v[152:155]\n"
      "v_cvt_pk_bf16_f32 v228, v160, v161\n"
      "v_and_b32 v189, 0xffff0000, v189\n"
      "v_mfma_f32_16x16x32_bf16 v[152:155], a[20:23], v[76:79], v[152:155]\n"
      "s_waitcnt vmcnt(11) lgkmcnt(5)\n"
      "ds_read_b128 v[144:147], v23 offset:47104\n"
      "v_lshlrev_b32 v232, 16, v204\n"
      "v_mfma_f32_16x16x32_bf16 v[152:155], a[24:27], v[80:83], v[152:155]\n"
      "buffer_store_dwordx2 v[228:229], v13, s[8:11], s37 offen sc1\n"
      "v_mfma_f32_16x16x32_bf16 v[152:155], a[28:31], v[84:87], v[152:155]\n"
      "v_lshlrev_b32 v235, 16, v187\n"
      "v_add_f32 v172, v172, v232\n"
      "v_mfma_f32_16x16x32_bf16 v[152:155], a[32:35], v[88:91], v[152:155]\n"
      "s_waitcnt vmcnt(10) lgkmcnt(3)\n"
      "ds_read_b128 v[120:123], v23 offset:40960\n"
      "v_lshlrev_b32 v232, 16, v208\n"
      "v_mfma_f32_16x16x32_bf16 v[152:155], a[36:39], v[92:95], v[152:155]\n"
      "ds_read_b128 v[124:127], v23 offset:41984\n"
      "v_add_f32 v170, v170, v235\n"
      "v_mfma_f32_16x16x32_bf16 v[152:155], a[40:43], v[96:99], v[152:155]\n"
      "s_add_u32 m0, s43, 24576\n"
      "v_and_b32 v194, 0xffff0000, v194\n"
      "v_and_b32 v208, 0xffff0000, v208\n"
      "v_mfma_f32_16x16x32_bf16 v[152:155], a[44:47], v[100:103], v[152:155]\n"
      "ds_read_b128 v[132:135], v23 offset:44032\n"
      "v_add_f32 v167, v167, v185\n"
      "v_mfma_f32_16x16x32_bf16 v[152:155], a[48:51], v[104:107], v[152:155]\n"
      "v_lshlrev_b32 v233, 16, v197\n"
      "v_add_u32 v14, 1, v14\n"
      "v_mfma_f32_16x16x32_bf16 v[152:155], a[52:55], v[108:111], v[152:155]\n"
      "s_add_u32 s36, s36, 165888\n"
      "v_lshlrev_b32 v235, 16, v195\n"
      "v_add_f32 v168, v168, v234\n"
      "v_mfma_f32_16x16x32_bf16 v[152:155], a[56:59], v[112:115], v[152:155]\n"
      "buffer_load_dwordx4 v[216:219], v6, s[8:11], s38 offen sc1\n"
      "v_mfma_f32_16x16x32_bf16 v[152:155], a[60:63], v[116:119], v[152:155]\n"
      "s_waitcnt lgkmcnt(2)\n"
      "ds_read_b128 v[148:151], v23 offset:48128\n"
      "v_add_f32 v164, v164, v232\n"
      "v_mfma_f32_16x16x32_bf16 v[152:155], a[64:67], v[120:123], v[152:155]\n"
      "s_waitcnt lgkmcnt(2)\n"
      "v_add_f32 v165, v165, v192\n"
      "v_lshlrev_b32 v234, 16, v202\n"
      "v_mfma_f32_16x16x32_bf16 v[152:155], a[68:71], v[124:127], v[152:155]\n"
      "v_add_f32 v174, v174, v233\n"
      "v_lshlrev_b32 v232, 16, v212\n"
      "v_mfma_f32_16x16x32_bf16 v[152:155], a[72:75], v[128:131], v[152:155]\n"
      "s_waitcnt lgkmcnt(1)\n"
      "v_and_b32 v179, 0xffff0000, v179\n"
      "v_add_f32 v169, v169, v186\n"
      "v_mfma_f32_16x16x32_bf16 v[152:155], a[76:79], v[132:135], v[152:155]\n"
      "s_waitcnt vmcnt(8)\n"
      "v_lshlrev_b32 v233, 16, v201\n"
      "v_cmp_ne_u32 vcc, s21, v20\n"
      "v_mfma_f32_16x16x32_bf16 v[152:155], a[80:83], v[136:139], v[152:155]\n"
      "buffer_load_dwordx2 v[220:221], v7, s[8:11], s38 offen sc1\n"
      "v_mfma_f32_16x16x32_bf16 v[152:155], a[84:87], v[140:143], v[152:155]\n"
      "v_add_f32 v166, v166, v233\n"
      "v_and_b32 v193, 0xffff0000, v193\n"
      "v_mfma_f32_16x16x32_bf16 v[152:155], a[88:91], v[144:147], v[152:155]\n"
      "s_waitcnt lgkmcnt(0)\n"
      "v_and_b32 v197, 0xffff0000, v197\n"
      "v_lshlrev_b32 v233, 16, v205\n"
      "v_mfma_f32_16x16x32_bf16 v[152:155], a[92:95], v[148:151], v[152:155]\n"
      "v_add_f32 v170, v170, v235\n"
      "v_add_f32 v174, v174, v233\n"
      "v_mfma_f32_16x16x32_bf16 v[156:159], a[96:99], v[56:59], 0\n"
      "s_cbranch_vccnz .Lsp_d1_15\n"
      ".Lbk_d1_15:\n"
      "v_lshlrev_b32 v235, 16, v203\n"
      "v_mfma_f32_16x16x32_bf16 v[156:159], a[100:103], v[60:63], v[156:159]\n"
      "buffer_load_dwordx4 v0, s[8:11], s54 offen offset:0 sc1 lds\n"
      "v_mfma_f32_16x16x32_bf16 v[156:159], a[104:107], v[64:67], v[156:159]\n"
      "v_and_b32 v187, 0xffff0000, v187\n"
      "v_lshlrev_b32 v233, 16, v209\n"
      "v_mfma_f32_16x16x32_bf16 v[156:159], a[108:111], v[68:71], v[156:159]\n"
      "v_add_f32 v170, v170, v235\n"
      "v_add_f32 v166, v166, v233\n"
      "v_mfma_f32_16x16x32_bf16 v[156:159], a[112:115], v[72:75], v[156:159]\n"
      "v_and_b32 v201, 0xffff0000, v201\n"
      "v_add_f32 v165, v165, v200\n"
      "v_mfma_f32_16x16x32_bf16 v[156:159], a[116:119], v[76:79], v[156:159]\n"
      "v_and_b32 v188, 0xffff0000, v188\n"
      "v_add_f32 v165, v165, v208\n"
      "v_mfma_f32_16x16x32_bf16 v[156:159], a[120:123], v[80:83], v[156:159]\n"
      "buffer_load_dwordx4 v0, s[8:11], s54 offen offset:1024 sc1 lds\n"
      "v_mfma_f32_16x16x32_bf16 v[156:159], a[124:127], v[84:87], v[156:159]\n"
      "v_add_f32 v168, v168, v234\n"
      "v_mul_f32 v237, v164, v164\n"
      "v_mfma_f32_16x16x32_bf16 v[156:159], a[128:131], v[88:91], v[156:159]\n"
      "v_add_f32 v167, v167, v193\n"
      "v_lshlrev_b32 v233, 16, v213\n"
      "v_mfma_f32_16x16x32_bf16 v[156:159], a[132:135], v[92:95], v[156:159]\n"
      "s_waitcnt vmcnt(7)\n"
      "ds_write_b32 v16, v14\n"
      "v_mfma_f32_16x16x32_bf16 v[156:159], a[136:139], v[96:99], v[156:159]\n"
      "buffer_load_dwordx4 v0, s[8:11], s54 offen offset:2048 sc1 lds\n"
      "v_mfma_f32_16x16x32_bf16 v[156:159], a[140:143], v[100:103], v[156:159]\n"
      "v_add_f32 v171, v171, v179\n"
      "v_add_f32 v167, v167, v201\n"
      "v_mfma_f32_16x16x32_bf16 v[156:159], a[144:147], v[104:107], v[156:159]\n"
      "v_lshlrev_b32 v234, 16, v210\n"
      "v_and_b32 v213, 0xffff0000, v213\n"
      "v_mfma_f32_16x16x32_bf16 v[156:159], a[148:151], v[108:111], v[156:159]\n"
      "s_min_u32 s36, s36, s6\n"
      "v_add_f32 v173, v173, v188\n"
      "v_and_b32 v209, 0xffff0000, v209\n"
      "v_mfma_f32_16x16x32_bf16 v[156:159], a[152:155], v[112:115], v[156:159]\n"
      "buffer_load_dword v20, v2, s[8:11], s36 offen sc1\n"
      "v_mfma_f32_16x16x32_bf16 v[156:159], a[156:159], v[116:119], v[156:159]\n"
      "v_add_f32 v236, v164, v165\n"
      "v_add_f32 v175, v175, v189\n"
      "v_mfma_f32_16x16x32_bf16 v[156:159], a[160:163], v[120:123], v[156:159]\n"
      "buffer_load_dwordx4 v0, s[8:11], s54 offen offset:3072 sc1 lds\n"
      "v_mfma_f32_16x16x32_bf16 v[156:159], a[164:167], v[124:127], v[156:159]\n"
      "v_add_f32 v167, v167, v209\n"
      "v_and_b32 v203, 0xffff0000, v203\n"
      "v_mfma_f32_16x16x32_bf16 v[156:159], a[168:171], v[128:131], v[156:159]\n"
      "ds_read_b32 v15, v17\n"
      "v_fmac_f32 v237, v165, v165\n"
      "v_mfma_f32_16x16x32_bf16 v[156:159], a[172:175], v[132:135], v[156:159]\n"
      "s_add_u32 m0, s43, 28672\n"
      "v_add_f32 v175, v175, v197\n"
      "v_and_b32 v204, 0xffff0000, v204\n"
      "v_mfma_f32_16x16x32_bf16 v[156:159], a[176:179], v[136:139], v[156:159]\n"
      "v_and_b32 v195, 0xffff0000, v195\n"
      "v_add_f32 v172, v172, v232\n"
      "v_mfma_f32_16x16x32_bf16 v[156:159], a[180:183], v[140:143], v[156:159]\n"
      "buffer_load_dwordx4 v1, s[8:11], s54 offen offset:0 sc1 lds\n"
      "v_mfma_f32_16x16x32_bf16 v[156:159], a[184:187], v[144:147], v[156:159]\n"
      "v_fmac_f32 v237, v166, v166\n"
      "v_cvt_pk_bf16_f32 v225, v154, v155\n"
      "v_mfma_f32_16x16x32_bf16 v[156:159], a[188:191], v[148:151], v[156:159]\n"
      "s_waitcnt lgkmcnt(0)\n"
      "v_add_f32 v169, v169, v194\n"
      "v_cmp_gt_i32 vcc, v14, v15\n"
      "v_mfma_f32_16x16x32_bf16 v[160:163], a[192:195], v[56:59], 0\n"
      "v_and_b32 v196, 0xffff0000, v196\n"
      "v_and_b32 v212, 0xffff0000, v212\n"
      "v_mfma_f32_16x16x32_bf16 v[160:163], a[196:199], v[60:63], v[160:163]\n"
      "buffer_load_dwordx4 v1, s[8:11], s54 offen offset:1024 sc1 lds\n"
      "v_mfma_f32_16x16x32_bf16 v[160:163], a[200:203], v[64:67], v[160:163]\n"
      "s_cbranch_vccnz .Lsp_dy0_16\n"
      ".Lbk_dy0_16:\n"
      "v_mfma_f32_16x16x32_bf16 v[160:163], a[204:207], v[68:71], v[160:163]\n"
      "ds_read_b128 v[56:59], v0 offset:0\n"
      "v_cvt_pk_bf16_f32 v177, v166, v167\n"
      "v_mfma_f32_16x16x32_bf16 v[160:163], a[208:211], v[72:75], v[160:163]\n"
      "v_add_f32 v171, v171, v187\n"
      "v_and_b32 v205, 0xffff0000, v205\n"
      "v_mfma_f32_16x16x32_bf16 v[160:163], a[212:215], v[76:79], v[160:163]\n"
      "ds_read_b128 v[60:63], v0 offset:1024\n"
      "v_cvt_pk_bf16_f32 v224, v152, v153\n"
      "v_mfma_f32_16x16x32_bf16 v[160:163], a[216:219], v[80:83], v[160:163]\n"
      "s_add_u32 s55, s55, 106496\n"
      "v_and_b32 v202, 0xffff0000, v202\n"
      "v_add_f32 v169, v169, v202\n"
      "v_mfma_f32_16x16x32_bf16 v[160:163], a[220:223], v[84:87], v[160:163]\n"
      "ds_read_b128 v[84:87], v0 offset:7168\n"
      "v_add_f32 v236, v236, v166\n"
      "v_mfma_f32_16x16x32_bf16 v[160:163], a[224:227], v[88:91], v[160:163]\n"
      "ds_read_b128 v[72:75], v0 offset:4096\n"
      "v_and_b32 v210, 0xffff0000, v210\n"
      "v_mfma_f32_16x16x32_bf16 v[160:163], a[228:231], v[92:95], v[160:163]\n"
      "v_add_f32 v173, v173, v196\n"
      "v_lshlrev_b32 v235, 16, v211\n"
      "v_mfma_f32_16x16x32_bf16 v[160:163], a[232:235], v[96:99], v[160:163]\n"
      "v_add_f32 v170, v170, v235\n"
      "v_add_f32 v171, v171, v195\n"
      "v_mfma_f32_16x16x32_bf16 v[160:163], a[236:239], v[100:103], v[160:163]\n"
      "s_add_u32 s37, s37, 106496\n"
      "ds_read_b128 v[88:91], v0 offset:8192\n"
      "v_cvt_pk_bf16_f32 v227, v158, v159\n"
      "v_mfma_f32_16x16x32_bf16 v[160:163], a[240:243], v[104:107], v[160:163]\n"
      "s_waitcnt vmcnt(9) lgkmcnt(3)\n"
      "buffer_store_dword v18, v4, s[8:11], s55 offen sc1\n"
      "v_mfma_f32_16x16x32_bf16 v[160:163], a[244:247], v[108:111], v[160:163]\n"
      "ds_read_b128 v[68:71], v0 offset:3072\n"
      "v_add_f32 v174, v174, v233\n"
      "v_mfma_f32_16x16x32_bf16 v[160:163], a[248:251], v[112:115], v[160:163]\n"
      "ds_read_b128 v[104:107], v0 offset:12288\n"
      "v_add_f32 v169, v169, v210\n"
      "v_mfma_f32_16x16x32_bf16 v[160:163], a[252:255], v[116:119], v[160:163]\n"
      "ds_read_b128 v[96:99], v0 offset:10240\n"
      "v_fmac_f32 v237, v167, v167\n"
      "v_mfma_f32_16x16x32_bf16 v[160:163], v[24:27], v[120:123], v[160:163]\n"
      "ds_read_b128 v[80:83], v0 offset:6144\n"
      "v_add_f32 v175, v175, v205\n"
      "v_mfma_f32_16x16x32_bf16 v[160:163], v[28:31], v[124:127], v[160:163]\n"
      "ds_read_b128 v[64:67], v0 offset:2048\n"
      "v_add_f32 v168, v168, v234\n"
      "v_mfma_f32_16x16x32_bf16 v[160:163], v[32:35], v[128:131], v[160:163]\n"
      "v_add_f32 v236, v236, v167\n"
      "v_cvt_pk_bf16_f32 v226, v156, v157\n"
      "v_mfma_f32_16x16x32_bf16 v[160:163], v[36:39], v[132:135], v[160:163]\n"
      "ds_read_b128 v[92:95], v0 offset:9216\n"
      "v_cvt_pk_bf16_f32 v176, v164, v165\n"
      "v_mfma_f32_16x16x32_bf16 v[160:163], v[40:43], v[136:139], v[160:163]\n"
      "buffer_store_dwordx4 v[224:227], v0, s[8:11], s37 offen sc1\n"
      "ds_read_b128 v[76:79], v0 offset:5120\n"
      "v_mfma_f32_16x16x32_bf16 v[160:163], v[44:47], v[140:143], v[160:163]\n"
      "ds_read_b128 v[132:135], v0 offset:19456\n"
      "v_and_b32 v211, 0xffff0000, v211\n"
      "v_mfma_f32_16x16x32_bf16 v[160:163], v[48:51], v[144:147], v[160:163]\n"
      "ds_read_b128 v[100:103], v0 offset:11264\n"
      "v_add_f32 v171, v171, v203\n"
      "v_mfma_f32_16x16x32_bf16 v[160:163], v[52:55], v[148:151], v[160:163]\n"
      "s_sub_u32 s62, s62, 1\n"
      "s_cbranch_scc0 .Ld_t0\n"
      ".Ld_out5:\n"
      "s_nop 8\n"
      "v_cvt_pk_bf16_f32 v228, v160, v161\n"
      "v_cvt_pk_bf16_f32 v229, v162, v163\n"
      "buffer_store_dwordx2 v[228:229], v13, s[8:11], s37 offen sc1\n"
      "v_cvt_pk_bf16_f32 v178, v168, v169\n"
      "v_add_f32 v175, v175, v213\n"
      "v_add_f32 v171, v171, v211\n"
      "v_add_f32 v173, v173, v204\n"
      "v_cvt_pk_bf16_f32 v179, v170, v171\n"
      "v_add_f32 v173, v173, v212\n"
      "v_cvt_pk_bf16_f32 v180, v172, v173\n"
      "v_cvt_pk_bf16_f32 v181, v174, v175\n"
      "buffer_store_dwordx2 v[180:181], v9, s[16:19], s60 offen\n"
      "buffer_store_dwordx4 v[176:179], v8, s[16:19], s60 offen\n"
      "s_add_u32 s55, s55, 106496\n"
      "s_add_u32 s37, s37, 173056\n"
      "s_waitcnt vmcnt(2)\n"
      "buffer_store_dword v18, v4, s[8:11], s55 offen sc1\n"
      "s_add_u32 s55, s55, 173056\n"
      "s_branch .Ld_end\n"
      ".Ld_out0:\n"
      "s_nop 8\n"
      "v_add_f32 v174, v174, v162\n"
      "v_add_f32 v170, v170, v158\n"
      "v_lshlrev_b32 v172, 16, v220\n"
      "s_add_u32 s38, s38, 147456\n"
      "v_add_f32 v171, v171, v159\n"
      "v_add_f32 v169, v169, v157\n"
      "v_add_f32 v172, v172, v160\n"
      "v_add_f32 v175, v175, v163\n"
      "v_and_b32 v173, 0xffff0000, v220\n"
      "v_add_f32 v173, v173, v161\n"
      "s_add_u32 s55, s55, 173056\n"
      "s_add_u32 s57, s56, 6656\n"
      "s_add_u32 s58, s56, 13312\n"
      "s_add_u32 s62, s56, 19968\n"
      "s_add_u32 s36, s56, 26624\n"
      "s_mov_b32 s54, 0\n"
      "s_mov_b32 s22, s23\n"
      ".Ld_a0:\n"
      "buffer_load_dwordx4 v[176:179], v0, s[8:11], s56 offen sc1\n"
      "buffer_load_dwordx4 v[184:187], v0, s[8:11], s57 offen sc1\n"
      "buffer_load_dwordx4 v[192:195], v0, s[8:11], s58 offen sc1\n"
      "buffer_load_dwordx4 v[200:203], v0, s[8:11], s62 offen sc1\n"
      "buffer_load_dwordx4 v[208:211], v0, s[8:11], s36 offen sc1\n"
      "buffer_load_dword v21, v3, s[8:11], s56 offen sc1\n"
      "buffer_load_dwordx2 v[180:181], v13, s[8:11], s56 offen sc1\n"
      "buffer_load_dwordx2 v[188:189], v13, s[8:11], s57 offen sc1\n"
      "buffer_load_dwordx2 v[196:197], v13, s[8:11], s58 offen sc1\n"
      "buffer_load_dwordx2 v[204:205], v13, s[8:11], s62 offen sc1\n"
      "buffer_load_dwordx2 v[212:213], v13, s[8:11], s36 offen sc1\n"
      "s_waitcnt vmcnt(6)\n"
      "s_cmp_lg_u32 s54, 0\n"
      "s_cbranch_scc1 .Ld_b0\n"
      "v_max_u32 v232, v176, v177\n"
      "v_max3_u32 v232, v232, v178, v179\n"
      "v_max3_u32 v232, v232, v184, v185\n"
      "v_max3_u32 v232, v232, v186, v187\n"
      "v_max3_u32 v232, v232, v192, v193\n"
      "v_max3_u32 v232, v232, v194, v195\n"
      "v_max3_u32 v232, v232, v200, v201\n"
      "v_max3_u32 v232, v232, v202, v203\n"
      "v_max3_u32 v232, v232, v208, v209\n"
      "v_max3_u32 v232, v232, v210, v211\n"
      "v_cmp_eq_u32 vcc, -1, v232\n"
      "s_cbranch_vccz .Ld_b0\n"
      "s_waitcnt vmcnt(0)\n"
      "v_cmp_ne_u32 vcc, s21, v21\n"
      "s_nop 1\n"
      "s_cmp_eq_u64 vcc, 0\n"
      "s_cselect_b32 s54, 1, 0\n"
      "s_sub_u32 s22, s22, 1\n"
      "s_cbranch_scc0 .Ld_a0\n"
      ".Ld_b0:\n"
      "s_add_u32 s61, s41, 4294967264\n"
      "s_cmp_lt_u32 s41, 32\n"
      "s_cselect_b32 s59, s39, 0x40000000\n"
      "s_add_u32 s39, s39, 147456\n"
      "s_cmp_lt_u32 s61, 32\n"
      "s_cselect_b32 s60, s40, 0x40000000\n"
      "s_add_u32 s40, s40, 4294856704\n"
      "s_add_u32 s41, s41, 6\n"
      "v_lshlrev_b32 v232, 16, v176\n"
      "v_lshlrev_b32 v233, 16, v177\n"
      "v_lshlrev_b32 v234, 16, v178\n"
      "v_lshlrev_b32 v235, 16, v179\n"
      "v_and_b32 v176, 0xffff0000, v176\n"
      "v_and_b32 v177, 0xffff0000, v177\n"
      "v_and_b32 v178, 0xffff0000, v178\n"
      "v_and_b32 v179, 0xffff0000, v179\n"
      "v_add_f32 v164, v164, v232\n"
      "v_add_f32 v166, v166, v233\n"
      "v_add_f32 v168, v168, v234\n"
      "v_add_f32 v170, v170, v235\n"
      "v_add_f32 v165, v165, v176\n"
      "v_add_f32 v167, v167, v177\n"
      "v_add_f32 v169, v169, v178\n"
      "v_add_f32 v171, v171, v179\n"
      "v_lshlrev_b32 v232, 16, v184\n"
      "v_lshlrev_b32 v233, 16, v185\n"
      "v_lshlrev_b32 v234, 16, v186\n"
      "v_lshlrev_b32 v235, 16, v187\n"
      "v_and_b32 v184, 0xffff0000, v184\n"
      "v_and_b32 v185, 0xffff0000, v185\n"
      "v_and_b32 v186, 0xffff0000, v186\n"
      "v_and_b32 v187, 0xffff0000, v187\n"
      "v_add_f32 v164, v164, v232\n"
      "v_add_f32 v166, v166, v233\n"
      "v_add_f32 v168, v168, v234\n"
      "v_add_f32 v170, v170, v235\n"
      "v_add_f32 v165, v165, v184\n"
      "v_add_f32 v167, v167, v185\n"
      "v_add_f32 v169, v169, v186\n"
      "v_add_f32 v171, v171, v187\n"
      "v_lshlrev_b32 v232, 16, v192\n"
      "v_lshlrev_b32 v233, 16, v193\n"
      "v_lshlrev_b32 v234, 16, v194\n"
      "v_lshlrev_b32 v235, 16, v195\n"
      "v_and_b32 v192, 0xffff0000, v192\n"
      "v_and_b32 v193, 0xffff0000, v193\n"
      "v_and_b32 v194, 0xffff0000, v194\n"
      "v_and_b32 v195, 0xffff0000, v195\n"
      "v_add_f32 v164, v164, v232\n"
      "v_add_f32 v166, v166, v233\n"
      "v_add_f32 v168, v168, v234\n"
      "v_add_f32 v170, v170, v235\n"
      "v_add_f32 v165, v165, v192\n"
      "v_add_f32 v167, v167, v193\n"
      "v_add_f32 v169, v169, v194\n"
      "v_add_f32 v171, v171, v195\n"
      "v_lshlrev_b32 v232, 16, v200\n"
      "v_lshlrev_b32 v233, 16, v201\n"
      "v_lshlrev_b32 v234, 16, v202\n"
      "v_lshlrev_b32 v235, 16, v203\n"
      "v_and_b32 v200, 0xffff0000, v200\n"
      "v_and_b32 v201, 0xffff0000, v201\n"
      "v_and_b32 v202, 0xffff0000, v202\n"
      "v_and_b32 v203, 0xffff0000, v203\n"
      "v_add_f32 v164, v164, v232\n"
      "v_add_f32 v166, v166, v233\n"
      "v_add_f32 v168, v168, v234\n"
      "v_add_f32 v170, v170, v235\n"
      "v_add_f32 v165, v165, v200\n"
      "v_add_f32 v167, v167, v201\n"
      "v_add_f32 v169, v169, v202\n"
      "v_add_f32 v171, v171, v203\n"
      "v_lshlrev_b32 v232, 16, v208\n"
      "v_lshlrev_b32 v233, 16, v209\n"
      "v_lshlrev_b32 v234, 16, v210\n"
      "v_lshlrev_b32 v235, 16, v211\n"
      "v_and_b32 v208, 0xffff0000, v208\n"
      "v_and_b32 v209, 0xffff0000, v209\n"
      "v_and_b32 v210, 0xffff0000, v210\n"
      "v_and_b32 v211, 0xffff0000, v211\n"
      "v_add_f32 v164, v164, v232\n"
      "v_add_f32 v166, v166, v233\n"
      "v_add_f32 v168, v168, v234\n"
      "v_add_f32 v170, v170, v235\n"
      "v_add_f32 v165, v165, v208\n"
      "v_add_f32 v167, v167, v209\n"
      "v_add_f32 v169, v169, v210\n"
      "v_add_f32 v171, v171, v211\n"
      "v_cvt_pk_bf16_f32 v176, v164, v165\n"
      "v_cvt_pk_bf16_f32 v177, v166, v167\n"
      "v_cvt_pk_bf16_f32 v178, v168, v169\n"
      "v_cvt_pk_bf16_f32 v179, v170, v171\n"
      "buffer_store_dwordx4 v[176:179], v8, s[16:19], s60 offen\n"
      "s_mov_b32 s22, s23\n"
      "s_waitcnt vmcnt(1)\n"
      ".Ld_c0:\n"
      "s_cmp_lg_u32 s54, 0\n"
      "s_cbranch_scc1 .Ld_d0\n"
      "v_max_u32 v232, v180, v181\n"
      "v_max3_u32 v232, v232, v188, v189\n"
      "v_max3_u32 v232, v232, v196, v197\n"
      "v_max3_u32 v232, v232, v204, v205\n"
      "v_max3_u32 v232, v232, v212, v213\n"
      "v_cmp_eq_u32 vcc, -1, v232\n"
      "s_cbranch_vccz .Ld_d0\n"
      "v_cmp_ne_u32 vcc, s21, v21\n"
      "s_nop 1\n"
      "s_cmp_eq_u64 vcc, 0\n"
      "s_cselect_b32 s54, 1, 0\n"
      "buffer_load_dword v21, v3, s[8:11], s56 offen sc1\n"
      "buffer_load_dwordx2 v[180:181], v13, s[8:11], s56 offen sc1\n"
      "buffer_load_dwordx2 v[188:189], v13, s[8:11], s57 offen sc1\n"
      "buffer_load_dwordx2 v[196:197], v13, s[8:11], s58 offen sc1\n"
      "buffer_load_dwordx2 v[204:205], v13, s[8:11], s62 offen sc1\n"
      "buffer_load_dwordx2 v[212:213], v13, s[8:11], s36 offen sc1\n"
      "s_waitcnt vmcnt(0)\n"
      "s_sub_u32 s22, s22, 1\n"
      "s_cbranch_scc0 .Ld_c0\n"
      ".Ld_d0:\n"
      "v_lshlrev_b32 v232, 16, v180\n"
      "v_lshlrev_b32 v233, 16, v181\n"
      "v_and_b32 v180, 0xffff0000, v180\n"
      "v_and_b32 v181, 0xffff0000, v181\n"
      "v_add_f32 v172, v172, v232\n"
      "v_add_f32 v174, v174, v233\n"
      "v_add_f32 v173, v173, v180\n"
      "v_add_f32 v175, v175, v181\n"
      "v_lshlrev_b32 v232, 16, v188\n"
      "v_lshlrev_b32 v233, 16, v189\n"
      "v_and_b32 v188, 0xffff0000, v188\n"
      "v_and_b32 v189, 0xffff0000, v189\n"
      "v_add_f32 v172, v172, v232\n"
      "v_add_f32 v174, v174, v233\n"
      "v_add_f32 v173, v173, v188\n"
      "v_add_f32 v175, v175, v189\n"
      "v_lshlrev_b32 v232, 16, v196\n"
      "v_lshlrev_b32 v233, 16, v197\n"
      "v_and_b32 v196, 0xffff0000, v196\n"
      "v_and_b32 v197, 0xffff0000, v197\n"
      "v_add_f32 v172, v172, v232\n"
      "v_add_f32 v174, v174, v233\n"
      "v_add_f32 v173, v173, v196\n"
      "v_add_f32 v175, v175, v197\n"
      "v_lshlrev_b32 v232, 16, v204\n"
      "v_lshlrev_b32 v233, 16, v205\n"
      "v_and_b32 v204, 0xffff0000, v204\n"
      "v_and_b32 v205, 0xffff0000, v205\n"
      "v_add_f32 v172, v172, v232\n"
      "v_add_f32 v174, v174, v233\n"
      "v_add_f32 v173, v173, v204\n"
      "v_add_f32 v175, v175, v205\n"
      "v_lshlrev_b32 v232, 16, v212\n"
      "v_lshlrev_b32 v233, 16, v213\n"
      "v_and_b32 v212, 0xffff0000, v212\n"
      "v_and_b32 v213, 0xffff0000, v213\n"
      "v_add_f32 v172, v172, v232\n"
      "v_add_f32 v174, v174, v233\n"
      "v_add_f32 v173, v173, v212\n"
      "v_add_f32 v175, v175, v213\n"
      "v_cvt_pk_bf16_f32 v180, v172, v173\n"
      "v_cvt_pk_bf16_f32 v181, v174, v175\n"
      "buffer_store_dwordx2 v[180:181], v9, s[16:19], s60 offen\n"
      "s_branch .Ld_end\n"
      ".Ld_out1:\n"
      "s_nop 8\n"
      "v_cvt_pk_bf16_f32 v229, v162, v163\n"
      "v_cvt_pk_bf16_f32 v228, v160, v161\n"
      "buffer_store_dwordx2 v[228:229], v13, s[8:11], s37 offen sc1\n"
      "s_add_u32 s57, s56, 6656\n"
      "s_add_u32 s58, s56, 13312\n"
      "s_add_u32 s62, s56, 19968\n"
      "s_add_u32 s36, s56, 26624\n"
      "s_mov_b32 s54, 0\n"
      "s_mov_b32 s22, s23\n"
      ".Ld_a1:\n"
      "buffer_load_dwordx4 v[176:179], v0, s[8:11], s56 offen sc1\n"
      "buffer_load_dwordx4 v[184:187], v0, s[8:11], s57 offen sc1\n"
      "buffer_load_dwordx4 v[192:195], v0, s[8:11], s58 offen sc1\n"
      "buffer_load_dwordx4 v[200:203], v0, s[8:11], s62 offen sc1\n"
      "buffer_load_dwordx4 v[208:211], v0, s[8:11], s36 offen sc1\n"
      "buffer_load_dword v21, v3, s[8:11], s56 offen sc1\n"
      "buffer_load_dwordx2 v[180:181], v13, s[8:11], s56 offen sc1\n"
      "buffer_load_dwordx2 v[188:189], v13, s[8:11], s57 offen sc1\n"
      "buffer_load_dwordx2 v[196:197], v13, s[8:11], s58 offen sc1\n"
      "buffer_load_dwordx2 v[204:205], v13, s[8:11], s62 offen sc1\n"
      "buffer_load_dwordx2 v[212:213], v13, s[8:11], s36 offen sc1\n"
      "s_waitcnt vmcnt(6)\n"
      "s_cmp_lg_u32 s54, 0\n"
      "s_cbranch_scc1 .Ld_b1\n"
      "v_max_u32 v232, v176, v177\n"
      "v_max3_u32 v232, v232, v178, v179\n"
      "v_max3_u32 v232, v232, v184, v185\n"
      "v_max3_u32 v232, v232, v186, v187\n"
      "v_max3_u32 v232, v232, v192, v193\n"
      "v_max3_u32 v232, v232, v194, v195\n"
      "v_max3_u32 v232, v232, v200, v201\n"
      "v_max3_u32 v232, v232, v202, v203\n"
      "v_max3_u32 v232, v232, v208, v209\n"
      "v_max3_u32 v232, v232, v210, v211\n"
      "v_cmp_eq_u32 vcc, -1, v232\n"
      "s_cbranch_vccz .Ld_b1\n"
      "s_waitcnt vmcnt(0)\n"
      "v_cmp_ne_u32 vcc, s21, v21\n"
      "s_nop 1\n"
      "s_cmp_eq_u64 vcc, 0\n"
      "s_cselect_b32 s54, 1, 0\n"
      "s_sub_u32 s22, s22, 1\n"
      "s_cbranch_scc0 .Ld_a1\n"
      ".Ld_b1:\n"
      "buffer_store_dword v18, v4, s[8:11], s55 offen sc1\n"
      "s_add_u32 s37, s37, 106496\n"
      "s_add_u32 s55, s55, 106496\n"
      "s_add_u32 s61, s41, 4294967264\n"
      "s_cmp_lt_u32 s41, 32\n"
      "s_cselect_b32 s59, s39, 0x40000000\n"
      "s_add_u32 s39, s39, 147456\n"
      "s_cmp_lt_u32 s61, 32\n"
      "s_cselect_b32 s60, s40, 0x40000000\n"
      "s_add_u32 s40, s40, 4294856704\n"
      "s_add_u32 s41, s41, 6\n"
      "v_lshlrev_b32 v232, 16, v176\n"
      "v_lshlrev_b32 v233, 16, v177\n"
      "v_lshlrev_b32 v234, 16, v178\n"
      "v_lshlrev_b32 v235, 16, v179\n"
      "v_and_b32 v176, 0xffff0000, v176\n"
      "v_and_b32 v177, 0xffff0000, v177\n"
      "v_and_b32 v178, 0xffff0000, v178\n"
      "v_and_b32 v179, 0xffff0000, v179\n"
      "v_add_f32 v164, v164, v232\n"
      "v_add_f32 v166, v166, v233\n"
      "v_add_f32 v168, v168, v234\n"
      "v_add_f32 v170, v170, v235\n"
      "v_add_f32 v165, v165, v176\n"
      "v_add_f32 v167, v167, v177\n"
      "v_add_f32 v169, v169, v178\n"
      "v_add_f32 v171, v171, v179\n"
      "v_lshlrev_b32 v232, 16, v184\n"
      "v_lshlrev_b32 v233, 16, v185\n"
      "v_lshlrev_b32 v234, 16, v186\n"
      "v_lshlrev_b32 v235, 16, v187\n"
      "v_and_b32 v184, 0xffff0000, v184\n"
      "v_and_b32 v185, 0xffff0000, v185\n"
      "v_and_b32 v186, 0xffff0000, v186\n"
      "v_and_b32 v187, 0xffff0000, v187\n"
      "v_add_f32 v164, v164, v232\n"
      "v_add_f32 v166, v166, v233\n"
      "v_add_f32 v168, v168, v234\n"
      "v_add_f32 v170, v170, v235\n"
      "v_add_f32 v165, v165, v184\n"
      "v_add_f32 v167, v167, v185\n"
      "v_add_f32 v169, v169, v186\n"
      "v_add_f32 v171, v171, v187\n"
      "v_lshlrev_b32 v232, 16, v192\n"
      "v_lshlrev_b32 v233, 16, v193\n"
      "v_lshlrev_b32 v234, 16, v194\n"
      "v_lshlrev_b32 v235, 16, v195\n"
      "v_and_b32 v192, 0xffff0000, v192\n"
      "v_and_b32 v193, 0xffff0000, v193\n"
      "v_and_b32 v194, 0xffff0000, v194\n"
      "v_and_b32 v195, 0xffff0000, v195\n"
      "v_add_f32 v164, v164, v232\n"
      "v_add_f32 v166, v166, v233\n"
      "v_add_f32 v168, v168, v234\n"
      "v_add_f32 v170, v170, v235\n"
      "v_add_f32 v165, v165, v192\n"
      "v_add_f32 v167, v167, v193\n"
      "v_add_f32 v169, v169, v194\n"
      "v_add_f32 v171, v171, v195\n"
      "v_lshlrev_b32 v232, 16, v200\n"
      "v_lshlrev_b32 v233, 16, v201\n"
      "v_lshlrev_b32 v234, 16, v202\n"
      "v_lshlrev_b32 v235, 16, v203\n"
      "v_and_b32 v200, 0xffff0000, v200\n"
      "v_and_b32 v201, 0xffff0000, v201\n"
      "v_and_b32 v202, 0xffff0000, v202\n"
      "v_and_b32 v203, 0xffff0000, v203\n"
      "v_add_f32 v164, v164, v232\n"
      "v_add_f32 v166, v166, v233\n"
      "v_add_f32 v168, v168, v234\n"
      "v_add_f32 v170, v170, v235\n"
      "v_add_f32 v165, v165, v200\n"
      "v_add_f32 v167, v167, v201\n"
      "v_add_f32 v169, v169, v202\n"
      "v_add_f32 v171, v171, v203\n"
      "v_lshlrev_b32 v232, 16, v208\n"
      "v_lshlrev_b32 v233, 16, v209\n"
      "v_lshlrev_b32 v234, 16, v210\n"
      "v_lshlrev_b32 v235, 16, v211\n"
      "v_and_b32 v208, 0xffff0000, v208\n"
      "v_and_b32 v209, 0xffff0000, v209\n"
      "v_and_b32 v210, 0xffff0000, v210\n"
      "v_and_b32 v211, 0xffff0000, v211\n"
      "v_add_f32 v164, v164, v232\n"
      "v_add_f32 v166, v166, v233\n"
      "v_add_f32 v168, v168, v234\n"
      "v_add_f32 v170, v170, v235\n"
      "v_add_f32 v165, v165, v208\n"
      "v_add_f32 v167, v167, v209\n"
      "v_add_f32 v169, v169, v210\n"
      "v_add_f32 v171, v171, v211\n"
      "v_cvt_pk_bf16_f32 v176, v164, v165\n"
      "v_cvt_pk_bf16_f32 v177, v166, v167\n"
      "v_cvt_pk_bf16_f32 v178, v168, v169\n"
      "v_cvt_pk_bf16_f32 v179, v170, v171\n"
      "buffer_store_dwordx4 v[176:179], v8, s[16:19], s60 offen\n"
      "s_mov_b32 s22, s23\n"
      "s_waitcnt vmcnt(2)\n"
      ".Ld_c1:\n"
      "s_cmp_lg_u32 s54, 0\n"
      "s_cbranch_scc1 .Ld_d1\n"
      "v_max_u32 v232, v180, v181\n"
      "v_max3_u32 v232, v232, v188, v189\n"
      "v_max3_u32 v232, v232, v196, v197\n"
      "v_max3_u32 v232, v232, v204, v205\n"
      "v_max3_u32 v232, v232, v212, v213\n"
      "v_cmp_eq_u32 vcc, -1, v232\n"
      "s_cbranch_vccz .Ld_d1\n"
      "v_cmp_ne_u32 vcc, s21, v21\n"
      "s_nop 1\n"
      "s_cmp_eq_u64 vcc, 0\n"
      "s_cselect_b32 s54, 1, 0\n"
      "buffer_load_dword v21, v3, s[8:11], s56 offen sc1\n"
      "buffer_load_dwordx2 v[180:181], v13, s[8:11], s56 offen sc1\n"
      "buffer_load_dwordx2 v[188:189], v13, s[8:11], s57 offen sc1\n"
      "buffer_load_dwordx2 v[196:197], v13, s[8:11], s58 offen sc1\n"
      "buffer_load_dwordx2 v[204:205], v13, s[8:11], s62 offen sc1\n"
      "buffer_load_dwordx2 v[212:213], v13, s[8:11], s36 offen sc1\n"
      "s_waitcnt vmcnt(0)\n"
      "s_sub_u32 s22, s22, 1\n"
      "s_cbranch_scc0 .Ld_c1\n"
      ".Ld_d1:\n"
      "v_lshlrev_b32 v232, 16, v180\n"
      "v_lshlrev_b32 v233, 16, v181\n"
      "v_and_b32 v180, 0xffff0000, v180\n"
      "v_and_b32 v181, 0xffff0000, v181\n"
      "v_add_f32 v172, v172, v232\n"
      "v_add_f32 v174, v174, v233\n"
      "v_add_f32 v173, v173, v180\n"
      "v_add_f32 v175, v175, v181\n"
      "v_lshlrev_b32 v232, 16, v188\n"
      "v_lshlrev_b32 v233, 16, v189\n"
      "v_and_b32 v188, 0xffff0000, v188\n"
      "v_and_b32 v189, 0xffff0000, v189\n"
      "v_add_f32 v172, v172, v232\n"
      "v_add_f32 v174, v174, v233\n"
      "v_add_f32 v173, v173, v188\n"
      "v_add_f32 v175, v175, v189\n"
      "v_lshlrev_b32 v232, 16, v196\n"
      "v_lshlrev_b32 v233, 16, v197\n"
      "v_and_b32 v196, 0xffff0000, v196\n"
      "v_and_b32 v197, 0xffff0000, v197\n"
      "v_add_f32 v172, v172, v232\n"
      "v_add_f32 v174, v174, v233\n"
      "v_add_f32 v173, v173, v196\n"
      "v_add_f32 v175, v175, v197\n"
      "v_lshlrev_b32 v232, 16, v204\n"
      "v_lshlrev_b32 v233, 16, v205\n"
      "v_and_b32 v204, 0xffff0000, v204\n"
      "v_and_b32 v205, 0xffff0000, v205\n"
      "v_add_f32 v172, v172, v232\n"
      "v_add_f32 v174, v174, v233\n"
      "v_add_f32 v173, v173, v204\n"
      "v_add_f32 v175, v175, v205\n"
      "v_lshlrev_b32 v232, 16, v212\n"
      "v_lshlrev_b32 v233, 16, v213\n"
      "v_and_b32 v212, 0xffff0000, v212\n"
      "v_and_b32 v213, 0xffff0000, v213\n"
      "v_add_f32 v172, v172, v232\n"
      "v_add_f32 v174, v174, v233\n"
      "v_add_f32 v173, v173, v212\n"
      "v_add_f32 v175, v175, v213\n"
      "v_cvt_pk_bf16_f32 v180, v172, v173\n"
      "v_cvt_pk_bf16_f32 v181, v174, v175\n"
      "buffer_store_dwordx2 v[180:181], v9, s[16:19], s60 offen\n"
      "s_branch .Ld_end\n"
      ".Ld_out2:\n"
      "s_nop 8\n"
      "v_cvt_pk_bf16_f32 v229, v162, v163\n"
      "v_cvt_pk_bf16_f32 v228, v160, v161\n"
      "buffer_store_dwordx2 v[228:229], v13, s[8:11], s37 offen sc1\n"
      "s_add_u32 s57, s56, 6656\n"
      "s_add_u32 s58, s56, 13312\n"
      "s_add_u32 s62, s56, 19968\n"
      "s_add_u32 s36, s56, 26624\n"
      "s_mov_b32 s54, 0\n"
      "s_mov_b32 s22, s23\n"
      ".Ld_a2:\n"
      "buffer_load_dwordx4 v[176:179], v0, s[8:11], s56 offen sc1\n"
      "buffer_load_dwordx4 v[184:187], v0, s[8:11], s57 offen sc1\n"
      "buffer_load_dwordx4 v[192:195], v0, s[8:11], s58 offen sc1\n"
      "buffer_load_dwordx4 v[200:203], v0, s[8:11], s62 offen sc1\n"
      "buffer_load_dwordx4 v[208:211], v0, s[8:11], s36 offen sc1\n"
      "buffer_load_dword v21, v3, s[8:11], s56 offen sc1\n"
      "buffer_load_dwordx2 v[180:181], v13, s[8:11], s56 offen sc1\n"
      "buffer_load_dwordx2 v[188:189], v13, s[8:11], s57 offen sc1\n"
      "buffer_load_dwordx2 v[196:197], v13, s[8:11], s58 offen sc1\n"
      "buffer_load_dwordx2 v[204:205], v13, s[8:11], s62 offen sc1\n"
      "buffer_load_dwordx2 v[212:213], v13, s[8:11], s36 offen sc1\n"
      "s_waitcnt vmcnt(6)\n"
      "s_cmp_lg_u32 s54, 0\n"
      "s_cbranch_scc1 .Ld_b2\n"
      "v_max_u32 v232, v176, v177\n"
      "v_max3_u32 v232, v232, v178, v179\n"
      "v_max3_u32 v232, v232, v184, v185\n"
      "v_max3_u32 v232, v232, v186, v187\n"
      "v_max3_u32 v232, v232, v192, v193\n"
      "v_max3_u32 v232, v232, v194, v195\n"
      "v_max3_u32 v232, v232, v200, v201\n"
      "v_max3_u32 v232, v232, v202, v203\n"
      "v_max3_u32 v232, v232, v208, v209\n"
      "v_max3_u32 v232, v232, v210, v211\n"
      "v_cmp_eq_u32 vcc, -1, v232\n"
      "s_cbranch_vccz .Ld_b2\n"
      "s_waitcnt vmcnt(0)\n"
      "v_cmp_ne_u32 vcc, s21, v21\n"
      "s_nop 1\n"
      "s_cmp_eq_u64 vcc, 0\n"
      "s_cselect_b32 s54, 1, 0\n"
      "s_sub_u32 s22, s22, 1\n"
      "s_cbranch_scc0 .Ld_a2\n"
      ".Ld_b2:\n"
      "s_add_u32 s55, s55, 106496\n"
      "s_add_u32 s61, s41, 4294967264\n"
      "s_add_u32 s37, s37, 106496\n"
      "buffer_store_dword v18, v4, s[8:11], s55 offen sc1\n"
      "s_cmp_lt_u32 s41, 32\n"
      "s_cselect_b32 s59, s39, 0x40000000\n"
      "s_add_u32 s39, s39, 147456\n"
      "s_cmp_lt_u32 s61, 32\n"
      "s_cselect_b32 s60, s40, 0x40000000\n"
      "s_add_u32 s55, s55, 106496\n"
      "s_add_u32 s40, s40, 4294856704\n"
      "s_add_u32 s41, s41, 6\n"
      "v_lshlrev_b32 v232, 16, v176\n"
      "v_lshlrev_b32 v233, 16, v177\n"
      "v_lshlrev_b32 v234, 16, v178\n"
      "v_lshlrev_b32 v235, 16, v179\n"
      "v_and_b32 v176, 0xffff0000, v176\n"
      "v_and_b32 v177, 0xffff0000, v177\n"
      "v_and_b32 v178, 0xffff0000, v178\n"
      "v_and_b32 v179, 0xffff0000, v179\n"
      "v_add_f32 v164, v164, v232\n"
      "v_add_f32 v166, v166, v233\n"
      "v_add_f32 v168, v168, v234\n"
      "v_add_f32 v170, v170, v235\n"
      "v_add_f32 v165, v165, v176\n"
      "v_add_f32 v167, v167, v177\n"
      "v_add_f32 v169, v169, v178\n"
      "v_add_f32 v171, v171, v179\n"
      "v_lshlrev_b32 v232, 16, v184\n"
      "v_lshlrev_b32 v233, 16, v185\n"
      "v_lshlrev_b32 v234, 16, v186\n"
      "v_lshlrev_b32 v235, 16, v187\n"
      "v_and_b32 v184, 0xffff0000, v184\n"
      "v_and_b32 v185, 0xffff0000, v185\n"
      "v_and_b32 v186, 0xffff0000, v186\n"
      "v_and_b32 v187, 0xffff0000, v187\n"
      "v_add_f32 v164, v164, v232\n"
      "v_add_f32 v166, v166, v233\n"
      "v_add_f32 v168, v168, v234\n"
      "v_add_f32 v170, v170, v235\n"
      "v_add_f32 v165, v165, v184\n"
      "v_add_f32 v167, v167, v185\n"
      "v_add_f32 v169, v169, v186\n"
      "v_add_f32 v171, v171, v187\n"
      "v_lshlrev_b32 v232, 16, v192\n"
      "v_lshlrev_b32 v233, 16, v193\n"
      "v_lshlrev_b32 v234, 16, v194\n"
      "v_lshlrev_b32 v235, 16, v195\n"
      "v_and_b32 v192, 0xffff0000, v192\n"
      "v_and_b32 v193, 0xffff0000, v193\n"
      "v_and_b32 v194, 0xffff0000, v194\n"
      "v_and_b32 v195, 0xffff0000, v195\n"
      "v_add_f32 v164, v164, v232\n"
      "v_add_f32 v166, v166, v233\n"
      "v_add_f32 v168, v168, v234\n"
      "v_add_f32 v170, v170, v235\n"
      "v_add_f32 v165, v165, v192\n"
      "v_add_f32 v167, v167, v193\n"
      "v_add_f32 v169, v169, v194\n"
      "v_add_f32 v171, v171, v195\n"
      "v_lshlrev_b32 v232, 16, v200\n"
      "v_lshlrev_b32 v233, 16, v201\n"
      "v_lshlrev_b32 v234, 16, v202\n"
      "v_lshlrev_b32 v235, 16, v203\n"
      "v_and_b32 v200, 0xffff0000, v200\n"
      "v_and_b32 v201, 0xffff0000, v201\n"
      "v_and_b32 v202, 0xffff0000, v202\n"
      "v_and_b32 v203, 0xffff0000, v203\n"
      "v_add_f32 v164, v164, v232\n"
      "v_add_f32 v166, v166, v233\n"
      "v_add_f32 v168, v168, v234\n"
      "v_add_f32 v170, v170, v235\n"
      "v_add_f32 v165, v165, v200\n"
      "v_add_f32 v167, v167, v201\n"
      "v_add_f32 v169, v169, v202\n"
      "v_add_f32 v171, v171, v203\n"
      "v_lshlrev_b32 v232, 16, v208\n"
      "v_lshlrev_b32 v233, 16, v209\n"
      "v_lshlrev_b32 v234, 16, v210\n"
      "v_lshlrev_b32 v235, 16, v211\n"
      "v_and_b32 v208, 0xffff0000, v208\n"
      "v_and_b32 v209, 0xffff0000, v209\n"
      "v_and_b32 v210, 0xffff0000, v210\n"
      "v_and_b32 v211, 0xffff0000, v211\n"
      "v_add_f32 v164, v164, v232\n"
      "v_add_f32 v166, v166, v233\n"
      "v_add_f32 v168, v168, v234\n"
      "v_add_f32 v170, v170, v235\n"
      "v_add_f32 v165, v165, v208\n"
      "v_add_f32 v167, v167, v209\n"
      "v_add_f32 v169, v169, v210\n"
      "v_add_f32 v171, v171, v211\n"
      "v_cvt_pk_bf16_f32 v176, v164, v165\n"
      "v_cvt_pk_bf16_f32 v177, v166, v167\n"
      "v_cvt_pk_bf16_f32 v178, v168, v169\n"
      "v_cvt_pk_bf16_f32 v179, v170, v171\n"
      "buffer_store_dwordx4 v[176:179], v8, s[16:19], s60 offen\n"
      "s_mov_b32 s22, s23\n"
      "s_waitcnt vmcnt(2)\n"
      ".Ld_c2:\n"
      "s_cmp_lg_u32 s54, 0\n"
      "s_cbranch_scc1 .Ld_d2\n"
      "v_max_u32 v232, v180, v181\n"
      "v_max3_u32 v232, v232, v188, v189\n"
      "v_max3_u32 v232, v232, v196, v197\n"
      "v_max3_u32 v232, v232, v204, v205\n"
      "v_max3_u32 v232, v232, v212, v213\n"
      "v_cmp_eq_u32 vcc, -1, v232\n"
      "s_cbranch_vccz .Ld_d2\n"
      "v_cmp_ne_u32 vcc, s21, v21\n"
      "s_nop 1\n"
      "s_cmp_eq_u64 vcc, 0\n"
      "s_cselect_b32 s54, 1, 0\n"
      "buffer_load_dword v21, v3, s[8:11], s56 offen sc1\n"
      "buffer_load_dwordx2 v[180:181], v13, s[8:11], s56 offen sc1\n"
      "buffer_load_dwordx2 v[188:189], v13, s[8:11], s57 offen sc1\n"
      "buffer_load_dwordx2 v[196:197], v13, s[8:11], s58 offen sc1\n"
      "buffer_load_dwordx2 v[204:205], v13, s[8:11], s62 offen sc1\n"
      "buffer_load_dwordx2 v[212:213], v13, s[8:11], s36 offen sc1\n"
      "s_waitcnt vmcnt(0)\n"
      "s_sub_u32 s22, s22, 1\n"
      "s_cbranch_scc0 .Ld_c2\n"
      ".Ld_d2:\n"
      "v_lshlrev_b32 v232, 16, v180\n"
      "v_lshlrev_b32 v233, 16, v181\n"
      "v_and_b32 v180, 0xffff0000, v180\n"
      "v_and_b32 v181, 0xffff0000, v181\n"
      "v_add_f32 v172, v172, v232\n"
      "v_add_f32 v174, v174, v233\n"
      "v_add_f32 v173, v173, v180\n"
      "v_add_f32 v175, v175, v181\n"
      "v_lshlrev_b32 v232, 16, v188\n"
      "v_lshlrev_b32 v233, 16, v189\n"
      "v_and_b32 v188, 0xffff0000, v188\n"
      "v_and_b32 v189, 0xffff0000, v189\n"
      "v_add_f32 v172, v172, v232\n"
      "v_add_f32 v174, v174, v233\n"
      "v_add_f32 v173, v173, v188\n"
      "v_add_f32 v175, v175, v189\n"
      "v_lshlrev_b32 v232, 16, v196\n"
      "v_lshlrev_b32 v233, 16, v197\n"
      "v_and_b32 v196, 0xffff0000, v196\n"
      "v_and_b32 v197, 0xffff0000, v197\n"
      "v_add_f32 v172, v172, v232\n"
      "v_add_f32 v174, v174, v233\n"
      "v_add_f32 v173, v173, v196\n"
      "v_add_f32 v175, v175, v197\n"
      "v_lshlrev_b32 v232, 16, v204\n"
      "v_lshlrev_b32 v233, 16, v205\n"
      "v_and_b32 v204, 0xffff0000, v204\n"
      "v_and_b32 v205, 0xffff0000, v205\n"
      "v_add_f32 v172, v172, v232\n"
      "v_add_f32 v174, v174, v233\n"
      "v_add_f32 v173, v173, v204\n"
      "v_add_f32 v175, v175, v205\n"
      "v_lshlrev_b32 v232, 16, v212\n"
      "v_lshlrev_b32 v233, 16, v213\n"
      "v_and_b32 v212, 0xffff0000, v212\n"
      "v_and_b32 v213, 0xffff0000, v213\n"
      "v_add_f32 v172, v172, v232\n"
      "v_add_f32 v174, v174, v233\n"
      "v_add_f32 v173, v173, v212\n"
      "v_add_f32 v175, v175, v213\n"
      "v_cvt_pk_bf16_f32 v180, v172, v173\n"
      "v_cvt_pk_bf16_f32 v181, v174, v175\n"
      "buffer_store_dwordx2 v[180:181], v9, s[16:19], s60 offen\n"
      "s_branch .Ld_end\n"
      ".Ld_out3:\n"
      "s_nop 8\n"
      "v_cvt_pk_bf16_f32 v228, v160, v161\n"
      "buffer_load_dwordx4 v[200:203], v0, s[8:11], s58 offen sc1\n"
      "v_cvt_pk_bf16_f32 v227, v158, v159\n"
      "buffer_store_dwordx4 v[224:227], v0, s[8:11], s37 offen sc1\n"
      "v_cvt_pk_bf16_f32 v229, v162, v163\n"
      "buffer_store_dwordx2 v[228:229], v13, s[8:11], s37 offen sc1\n"
      "s_add_u32 s37, s37, 106496\n"
      "buffer_load_dwordx2 v[204:205], v13, s[8:11], s58 offen sc1\n"
      "s_cmp_lt_u32 s41, 32\n"
      "s_cselect_b32 s59, s39, 0x40000000\n"
      "s_add_u32 s57, s56, 26624\n"
      "s_add_u32 s56, s56, 599040\n"
      "buffer_load_dwordx2 v[212:213], v13, s[8:11], s57 offen sc1\n"
      "s_add_u32 s39, s39, 147456\n"
      "buffer_load_dwordx4 v[208:211], v0, s[8:11], s57 offen sc1\n"
      "s_cmp_lt_u32 s61, 32\n"
      "s_cselect_b32 s60, s40, 0x40000000\n"
      "s_waitcnt vmcnt(12)\n"
      "v_lshlrev_b32 v234, 16, v178\n"
      "v_lshlrev_b32 v233, 16, v177\n"
      "v_lshlrev_b32 v232, 16, v176\n"
      "v_add_f32 v166, v166, v233\n"
      "s_add_u32 s55, s55, 106496\n"
      "v_add_f32 v164, v164, v232\n"
      "s_waitcnt vmcnt(11)\n"
      "v_lshlrev_b32 v233, 16, v181\n"
      "v_lshlrev_b32 v235, 16, v179\n"
      "v_and_b32 v176, 0xffff0000, v176\n"
      "v_lshlrev_b32 v232, 16, v180\n"
      "v_add_f32 v172, v172, v232\n"
      "v_and_b32 v181, 0xffff0000, v181\n"
      "v_add_f32 v168, v168, v234\n"
      "v_add_f32 v174, v174, v233\n"
      "v_add_f32 v165, v165, v176\n"
      "s_waitcnt vmcnt(10)\n"
      "v_lshlrev_b32 v232, 16, v184\n"
      "v_and_b32 v177, 0xffff0000, v177\n"
      "s_add_u32 s40, s40, 4294856704\n"
      "v_add_f32 v167, v167, v177\n"
      "v_and_b32 v180, 0xffff0000, v180\n"
      "v_add_f32 v164, v164, v232\n"
      "v_lshlrev_b32 v233, 16, v185\n"
      "v_add_f32 v166, v166, v233\n"
      "s_waitcnt vmcnt(7)\n"
      "v_lshlrev_b32 v232, 16, v188\n"
      "v_add_f32 v172, v172, v232\n"
      "v_and_b32 v185, 0xffff0000, v185\n"
      "v_lshlrev_b32 v232, 16, v192\n"
      "v_lshlrev_b32 v234, 16, v186\n"
      "v_add_f32 v164, v164, v232\n"
      "v_lshlrev_b32 v233, 16, v189\n"
      "s_waitcnt vmcnt(6)\n"
      "v_lshlrev_b32 v232, 16, v196\n"
      "v_add_f32 v174, v174, v233\n"
      "v_and_b32 v184, 0xffff0000, v184\n"
      "v_add_f32 v172, v172, v232\n"
      "v_add_f32 v170, v170, v235\n"
      "s_add_u32 s41, s41, 6\n"
      "v_add_f32 v165, v165, v184\n"
      "v_add_f32 v168, v168, v234\n"
      "v_lshlrev_b32 v233, 16, v193\n"
      "v_and_b32 v178, 0xffff0000, v178\n"
      "s_waitcnt vmcnt(5)\n"
      "v_lshlrev_b32 v232, 16, v200\n"
      "v_add_f32 v164, v164, v232\n"
      "v_and_b32 v186, 0xffff0000, v186\n"
      "s_waitcnt vmcnt(3)\n"
      "buffer_store_dword v18, v4, s[8:11], s55 offen sc1\n"
      "v_add_f32 v169, v169, v178\n"
      "v_and_b32 v200, 0xffff0000, v200\n"
      "v_add_f32 v173, v173, v180\n"
      "v_add_f32 v166, v166, v233\n"
      "v_lshlrev_b32 v234, 16, v194\n"
      "v_and_b32 v192, 0xffff0000, v192\n"
      "v_add_f32 v175, v175, v181\n"
      "v_and_b32 v189, 0xffff0000, v189\n"
      "s_waitcnt vmcnt(3)\n"
      "v_lshlrev_b32 v232, 16, v204\n"
      "v_lshlrev_b32 v235, 16, v187\n"
      "v_add_f32 v172, v172, v232\n"
      "s_waitcnt vmcnt(1)\n"
      "v_lshlrev_b32 v232, 16, v208\n"
      "v_add_f32 v170, v170, v235\n"
      "v_and_b32 v194, 0xffff0000, v194\n"
      "v_and_b32 v208, 0xffff0000, v208\n"
      "v_add_f32 v167, v167, v185\n"
      "v_lshlrev_b32 v233, 16, v197\n"
      "v_lshlrev_b32 v235, 16, v195\n"
      "v_add_f32 v168, v168, v234\n"
      "v_add_f32 v164, v164, v232\n"
      "v_add_f32 v165, v165, v192\n"
      "v_lshlrev_b32 v234, 16, v202\n"
      "v_add_f32 v174, v174, v233\n"
      "v_lshlrev_b32 v232, 16, v212\n"
      "v_and_b32 v179, 0xffff0000, v179\n"
      "v_add_f32 v169, v169, v186\n"
      "v_lshlrev_b32 v233, 16, v201\n"
      "v_add_f32 v166, v166, v233\n"
      "v_and_b32 v193, 0xffff0000, v193\n"
      "v_and_b32 v197, 0xffff0000, v197\n"
      "v_lshlrev_b32 v233, 16, v205\n"
      "v_add_f32 v170, v170, v235\n"
      "v_add_f32 v174, v174, v233\n"
      "v_lshlrev_b32 v235, 16, v203\n"
      "v_and_b32 v187, 0xffff0000, v187\n"
      "v_lshlrev_b32 v233, 16, v209\n"
      "v_add_f32 v170, v170, v235\n"
      "v_add_f32 v166, v166, v233\n"
      "v_and_b32 v201, 0xffff0000, v201\n"
      "v_add_f32 v165, v165, v200\n"
      "v_and_b32 v188, 0xffff0000, v188\n"
      "v_add_f32 v165, v165, v208\n"
      "v_add_f32 v168, v168, v234\n"
      "v_add_f32 v167, v167, v193\n"
      "v_lshlrev_b32 v233, 16, v213\n"
      "v_add_f32 v171, v171, v179\n"
      "v_add_f32 v167, v167, v201\n"
      "v_lshlrev_b32 v234, 16, v210\n"
      "v_and_b32 v213, 0xffff0000, v213\n"
      "v_add_f32 v173, v173, v188\n"
      "v_and_b32 v209, 0xffff0000, v209\n"
      "v_add_f32 v175, v175, v189\n"
      "v_add_f32 v167, v167, v209\n"
      "v_and_b32 v203, 0xffff0000, v203\n"
      "v_add_f32 v175, v175, v197\n"
      "v_and_b32 v204, 0xffff0000, v204\n"
      "v_and_b32 v195, 0xffff0000, v195\n"
      "v_add_f32 v172, v172, v232\n"
      "v_add_f32 v169, v169, v194\n"
      "v_and_b32 v196, 0xffff0000, v196\n"
      "v_and_b32 v212, 0xffff0000, v212\n"
      "v_cvt_pk_bf16_f32 v177, v166, v167\n"
      "v_add_f32 v171, v171, v187\n"
      "v_and_b32 v205, 0xffff0000, v205\n"
      "s_add_u32 s55, s55, 106496\n"
      "v_and_b32 v202, 0xffff0000, v202\n"
      "v_add_f32 v169, v169, v202\n"
      "v_and_b32 v210, 0xffff0000, v210\n"
      "v_add_f32 v173, v173, v196\n"
      "v_lshlrev_b32 v235, 16, v211\n"
      "v_add_f32 v170, v170, v235\n"
      "v_add_f32 v171, v171, v195\n"
      "v_add_f32 v174, v174, v233\n"
      "v_add_f32 v169, v169, v210\n"
      "v_add_f32 v175, v175, v205\n"
      "v_add_f32 v168, v168, v234\n"
      "v_cvt_pk_bf16_f32 v176, v164, v165\n"
      "v_and_b32 v211, 0xffff0000, v211\n"
      "v_add_f32 v171, v171, v203\n"
      "v_cvt_pk_bf16_f32 v178, v168, v169\n"
      "v_add_f32 v175, v175, v213\n"
      "v_add_f32 v171, v171, v211\n"
      "v_add_f32 v173, v173, v204\n"
      "v_cvt_pk_bf16_f32 v179, v170, v171\n"
      "v_add_f32 v173, v173, v212\n"
      "v_cvt_pk_bf16_f32 v180, v172, v173\n"
      "v_cvt_pk_bf16_f32 v181, v174, v175\n"
      "buffer_store_dwordx2 v[180:181], v9, s[16:19], s60 offen\n"
      "buffer_store_dwordx4 v[176:179], v8, s[16:19], s60 offen\n"
      "s_branch .Ld_end\n"
      ".Ld_out4:\n"
      "v_and_b32 v192, 0xffff0000, v192\n"
      "s_nop 7\n"
      "v_cvt_pk_bf16_f32 v229, v162, v163\n"
      "v_add_f32 v175, v175, v181\n"
      "v_cvt_pk_bf16_f32 v228, v160, v161\n"
      "v_and_b32 v189, 0xffff0000, v189\n"
      "s_waitcnt vmcnt(11)\n"
      "v_lshlrev_b32 v232, 16, v204\n"
      "buffer_store_dwordx2 v[228:229], v13, s[8:11], s37 offen sc1\n"
      "v_lshlrev_b32 v235, 16, v187\n"
      "v_add_f32 v172, v172, v232\n"
      "s_waitcnt vmcnt(10)\n"
      "v_lshlrev_b32 v232, 16, v208\n"
      "v_add_f32 v170, v170, v235\n"
      "v_and_b32 v194, 0xffff0000, v194\n"
      "v_and_b32 v208, 0xffff0000, v208\n"
      "v_add_f32 v167, v167, v185\n"
      "v_lshlrev_b32 v233, 16, v197\n"
      "v_lshlrev_b32 v235, 16, v195\n"
      "v_add_f32 v168, v168, v234\n"
      "v_add_f32 v164, v164, v232\n"
      "v_add_f32 v165, v165, v192\n"
      "v_lshlrev_b32 v234, 16, v202\n"
      "v_add_f32 v174, v174, v233\n"
      "v_lshlrev_b32 v232, 16, v212\n"
      "v_and_b32 v179, 0xffff0000, v179\n"
      "v_add_f32 v169, v169, v186\n"
      "v_lshlrev_b32 v233, 16, v201\n"
      "v_add_f32 v166, v166, v233\n"
      "v_and_b32 v193, 0xffff0000, v193\n"
      "v_and_b32 v197, 0xffff0000, v197\n"
      "v_lshlrev_b32 v233, 16, v205\n"
      "v_add_f32 v170, v170, v235\n"
      "v_add_f32 v174, v174, v233\n"
      "v_lshlrev_b32 v235, 16, v203\n"
      "v_and_b32 v187, 0xffff0000, v187\n"
      "v_lshlrev_b32 v233, 16, v209\n"
      "v_add_f32 v170, v170, v235\n"
      "v_add_f32 v166, v166, v233\n"
      "v_and_b32 v201, 0xffff0000, v201\n"
      "v_add_f32 v165, v165, v200\n"
      "v_and_b32 v188, 0xffff0000, v188\n"
      "v_add_f32 v165, v165, v208\n"
      "v_add_f32 v168, v168, v234\n"
      "v_add_f32 v167, v167, v193\n"
      "v_lshlrev_b32 v233, 16, v213\n"
      "v_add_f32 v171, v171, v179\n"
      "v_add_f32 v167, v167, v201\n"
      "v_lshlrev_b32 v234, 16, v210\n"
      "v_and_b32 v213, 0xffff0000, v213\n"
      "v_add_f32 v173, v173, v188\n"
      "v_and_b32 v209, 0xffff0000, v209\n"
      "v_add_f32 v175, v175, v189\n"
      "v_add_f32 v167, v167, v209\n"
      "v_and_b32 v203, 0xffff0000, v203\n"
      "v_add_f32 v175, v175, v197\n"
      "v_and_b32 v204, 0xffff0000, v204\n"
      "v_and_b32 v195, 0xffff0000, v195\n"
      "v_add_f32 v172, v172, v232\n"
      "v_add_f32 v169, v169, v194\n"
      "v_and_b32 v196, 0xffff0000, v196\n"
      "v_and_b32 v212, 0xffff0000, v212\n"
      "v_cvt_pk_bf16_f32 v177, v166, v167\n"
      "v_add_f32 v171, v171, v187\n"
      "v_and_b32 v205, 0xffff0000, v205\n"
      "s_add_u32 s55, s55, 106496\n"
      "v_and_b32 v202, 0xffff0000, v202\n"
      "v_add_f32 v169, v169, v202\n"
      "v_and_b32 v210, 0xffff0000, v210\n"
      "v_add_f32 v173, v173, v196\n"
      "v_lshlrev_b32 v235, 16, v211\n"
      "v_add_f32 v170, v170, v235\n"
      "v_add_f32 v171, v171, v195\n"
      "s_add_u32 s37, s37, 106496\n"
      "s_waitcnt vmcnt(0)\n"
      "buffer_store_dword v18, v4, s[8:11], s55 offen sc1\n"
      "v_add_f32 v174, v174, v233\n"
      "v_add_f32 v169, v169, v210\n"
      "v_add_f32 v175, v175, v205\n"
      "v_add_f32 v168, v168, v234\n"
      "v_cvt_pk_bf16_f32 v176, v164, v165\n"
      "v_and_b32 v211, 0xffff0000, v211\n"
      "v_add_f32 v171, v171, v203\n"
      "v_cvt_pk_bf16_f32 v178, v168, v169\n"
      "v_add_f32 v175, v175, v213\n"
      "v_add_f32 v171, v171, v211\n"
      "v_add_f32 v173, v173, v204\n"
      "v_cvt_pk_bf16_f32 v179, v170, v171\n"
      "v_add_f32 v173, v173, v212\n"
      "v_cvt_pk_bf16_f32 v180, v172, v173\n"
      "v_cvt_pk_bf16_f32 v181, v174, v175\n"
      "buffer_store_dwordx2 v[180:181], v9, s[16:19], s60 offen\n"
      "buffer_store_dwordx4 v[176:179], v8, s[16:19], s60 offen\n"
      "s_add_u32 s55, s55, 106496\n"
      "s_branch .Ld_end\n"
      ".Ld_in1:\n"
      "s_add_u32 s36, s36, 165888\n"
      "s_min_u32 s36, s36, s6\n"
      "s_mov_b32 s54, s36\n"
      "s_add_u32 s36, s36, 165888\n"
      "s_min_u32 s36, s36, s6\n"
      "buffer_load_dword v20, v2, s[8:11], s36 offen sc1\n"
      "s_mov_b32 s54, s36\n"
      "s_add_u32 m0, s43, 24576\n"
      "s_add_u32 s36, s36, 165888\n"
      "s_waitcnt vmcnt(0)\n"
      "v_cmp_ne_u32 vcc, s21, v20\n"
      "s_cbranch_vccnz .Lsp_d1_17\n"
      ".Lbk_d1_17:\n"
      "buffer_load_dwordx4 v0, s[8:11], s54 offen offset:0 sc1 lds\n"
      "buffer_load_dwordx4 v0, s[8:11], s54 offen offset:1024 sc1 lds\n"
      "buffer_load_dwordx4 v0, s[8:11], s54 offen offset:2048 sc1 lds\n"
      "s_min_u32 s36, s36, s6\n"
      "buffer_load_dword v20, v2, s[8:11], s36 offen sc1\n"
      "buffer_load_dwordx4 v0, s[8:11], s54 offen offset:3072 sc1 lds\n"
      "s_add_u32 m0, s43, 28672\n"
      "s_nop 0\n"
      "buffer_load_dwordx4 v1, s[8:11], s54 offen offset:0 sc1 lds\n"
      "buffer_load_dwordx4 v1, s[8:11], s54 offen offset:1024 sc1 lds\n"
      "s_add_u32 m0, s43, 49152\n"
      "s_mov_b32 s54, s36\n"
      "v_add_u32 v14, 1, v14\n"
      "s_waitcnt vmcnt(3)\n"
      "v_cmp_ne_u32 vcc, s21, v20\n"
      "s_add_u32 s36, s36, 165888\n"
      "s_min_u32 s36, s36, s6\n"
      "s_cbranch_vccnz .Lsp_d2_18\n"
      ".Lbk_d2_18:\n"
      "s_waitcnt vmcnt(0)\n"
      "ds_write_b32 v16, v14\n"
      "buffer_load_dword v20, v2, s[8:11], s36 offen sc1\n"
      "buffer_load_dwordx4 v0, s[8:11], s54 offen offset:3072 sc1 lds\n"
      "s_add_u32 s55, s55, 106496\n"
      "s_add_u32 s37, s37, 173056\n"
      "buffer_load_dwordx4 v0, s[8:11], s54 offen offset:1024 sc1 lds\n"
      "ds_read_b32 v15, v17\n"
      "buffer_load_dwordx4 v0, s[8:11], s54 offen offset:2048 sc1 lds\n"
      "s_waitcnt lgkmcnt(0)\n"
      "v_cmp_gt_i32 vcc, v14, v15\n"
      "buffer_load_dwordx4 v0, s[8:11], s54 offen offset:0 sc1 lds\n"
      "s_add_u32 m0, s43, 53248\n"
      "s_cbranch_vccnz .Lsp_dy1_19\n"
      ".Lbk_dy1_19:\n"
      "buffer_load_dwordx4 v1, s[8:11], s54 offen offset:0 sc1 lds\n"
      "ds_read_b128 v[56:59], v0 offset:24576\n"
      "ds_read_b128 v[64:67], v0 offset:26624\n"
      "buffer_load_dwordx4 v1, s[8:11], s54 offen offset:1024 sc1 lds\n"
      "ds_read_b128 v[68:71], v0 offset:27648\n"
      "ds_read_b128 v[80:83], v0 offset:30720\n"
      "ds_read_b128 v[60:63], v0 offset:25600\n"
      "ds_read_b128 v[92:95], v0 offset:33792\n"
      "ds_read_b128 v[84:87], v0 offset:31744\n"
      "ds_read_b128 v[72:75], v0 offset:28672\n"
      "ds_read_b128 v[76:79], v0 offset:29696\n"
      "ds_read_b128 v[100:103], v0 offset:35840\n"
      "ds_read_b128 v[96:99], v0 offset:34816\n"
      "ds_read_b128 v[116:119], v0 offset:39936\n"
      "ds_read_b128 v[120:123], v0 offset:40960\n"
      "ds_read_b128 v[108:111], v0 offset:37888\n"
      "ds_read_b128 v[88:91], v0 offset:32768\n"
      "s_waitcnt vmcnt(0) lgkmcnt(0)\n"
      "s_nop 4\n"
      "s_branch .Ld_t1\n"
      ".Ld_in2:\n"
      "s_add_u32 s36, s36, 165888\n"
      "s_min_u32 s36, s36, s6\n"
      "s_mov_b32 s54, s36\n"
      "s_add_u32 s36, s36, 165888\n"
      "s_min_u32 s36, s36, s6\n"
      "s_mov_b32 s54, s36\n"
      "s_add_u32 s36, s36, 165888\n"
      "s_min_u32 s36, s36, s6\n"
      "buffer_load_dword v20, v2, s[8:11], s36 offen sc1\n"
      "s_add_u32 m0, s43, 49152\n"
      "s_mov_b32 s54, s36\n"
      "s_waitcnt vmcnt(0)\n"
      "v_cmp_ne_u32 vcc, s21, v20\n"
      "s_add_u32 s36, s36, 165888\n"
      "s_min_u32 s36, s36, s6\n"
      "s_cbranch_vccnz .Lsp_d2_20\n"
      ".Lbk_d2_20:\n"
      "buffer_load_dword v20, v2, s[8:11], s36 offen sc1\n"
      "buffer_load_dwordx4 v0, s[8:11], s54 offen offset:3072 sc1 lds\n"
      "s_add_u32 s55, s55, 106496\n"
      "s_add_u32 s37, s37, 173056\n"
      "buffer_load_dwordx4 v0, s[8:11], s54 offen offset:1024 sc1 lds\n"
      "buffer_load_dwordx4 v0, s[8:11], s54 offen offset:2048 sc1 lds\n"
      "buffer_load_dwordx4 v0, s[8:11], s54 offen offset:0 sc1 lds\n"
      "s_add_u32 m0, s43, 53248\n"
      "s_nop 0\n"
      "buffer_load_dwordx4 v1, s[8:11], s54 offen offset:0 sc1 lds\n"
      "buffer_load_dwordx4 v1, s[8:11], s54 offen offset:1024 sc1 lds\n"
      "s_mov_b32 s54, s36\n"
      "s_add_u32 s36, s36, 165888\n"
      "s_add_u32 m0, s43, 73728\n"
      "s_waitcnt vmcnt(6)\n"
      "v_cmp_ne_u32 vcc, s21, v20\n"
      "s_add_u32 s38, s38, 147456\n"
      "s_min_u32 s36, s36, s6\n"
      "s_cbranch_vccnz .Lsp_d3_21\n"
      ".Lbk_d3_21:\n"
      "buffer_load_dwordx4 v0, s[8:11], s54 offen offset:2048 sc1 lds\n"
      "v_add_u32 v14, 1, v14\n"
      "buffer_load_dwordx4 v0, s[8:11], s54 offen offset:1024 sc1 lds\n"
      "buffer_load_dword v20, v2, s[8:11], s36 offen sc1\n"
      "s_waitcnt vmcnt(3)\n"
      "ds_write_b32 v16, v14\n"
      "buffer_load_dwordx4 v0, s[8:11], s54 offen offset:0 sc1 lds\n"
      "buffer_load_dwordx4 v0, s[8:11], s54 offen offset:3072 sc1 lds\n"
      "s_add_u32 m0, s43, 77824\n"
      "ds_read_b32 v15, v17\n"
      "s_add_u32 s55, s55, 173056\n"
      "buffer_load_dwordx4 v1, s[8:11], s54 offen offset:0 sc1 lds\n"
      "s_waitcnt lgkmcnt(0)\n"
      "v_cmp_gt_i32 vcc, v14, v15\n"
      "buffer_load_dwordx4 v1, s[8:11], s54 offen offset:1024 sc1 lds\n"
      "s_cbranch_vccnz .Lsp_dy2_22\n"
      ".Lbk_dy2_22:\n"
      "ds_read_b128 v[68:71], v22 offset:3072\n"
      "ds_read_b128 v[60:63], v22 offset:1024\n"
      "ds_read_b128 v[80:83], v22 offset:6144\n"
      "ds_read_b128 v[72:75], v22 offset:4096\n"
      "ds_read_b128 v[76:79], v22 offset:5120\n"
      "ds_read_b128 v[96:99], v22 offset:10240\n"
      "ds_read_b128 v[84:87], v22 offset:7168\n"
      "ds_read_b128 v[56:59], v22 offset:0\n"
      "ds_read_b128 v[92:95], v22 offset:9216\n"
      "s_mov_b32 s54, s36\n"
      "ds_read_b128 v[64:67], v22 offset:2048\n"
      "ds_read_b128 v[100:103], v22 offset:11264\n"
      "ds_read_b128 v[132:135], v22 offset:19456\n"
      "ds_read_b128 v[112:115], v22 offset:14336\n"
      "s_add_u32 m0, s43, 98304\n"
      "s_waitcnt vmcnt(0) lgkmcnt(0)\n"
      "s_nop 4\n"
      "s_branch .Ld_t2\n"
      ".Ld_in3:\n"
      "s_add_u32 s36, s36, 165888\n"
      "s_min_u32 s36, s36, s6\n"
      "s_mov_b32 s54, s36\n"
      "s_add_u32 s36, s36, 165888\n"
      "s_min_u32 s36, s36, s6\n"
      "s_mov_b32 s54, s36\n"
      "s_add_u32 s36, s36, 165888\n"
      "s_min_u32 s36, s36, s6\n"
      "s_mov_b32 s54, s36\n"
      "s_add_u32 s36, s36, 165888\n"
      "s_min_u32 s36, s36, s6\n"
      "buffer_load_dword v20, v2, s[8:11], s36 offen sc1\n"
      "s_add_u32 s55, s55, 106496\n"
      "s_add_u32 s37, s37, 173056\n"
      "s_mov_b32 s54, s36\n"
      "s_add_u32 s36, s36, 165888\n"
      "s_add_u32 m0, s43, 73728\n"
      "s_waitcnt vmcnt(0)\n"
      "v_cmp_ne_u32 vcc, s21, v20\n"
      "s_add_u32 s38, s38, 147456\n"
      "s_min_u32 s36, s36, s6\n"
      "s_cbranch_vccnz .Lsp_d3_23\n"
      ".Lbk_d3_23:\n"
      "buffer_load_dwordx4 v0, s[8:11], s54 offen offset:2048 sc1 lds\n"
      "buffer_load_dwordx4 v0, s[8:11], s54 offen offset:1024 sc1 lds\n"
      "buffer_load_dword v20, v2, s[8:11], s36 offen sc1\n"
      "buffer_load_dwordx4 v0, s[8:11], s54 offen offset:0 sc1 lds\n"
      "buffer_load_dwordx4 v0, s[8:11], s54 offen offset:3072 sc1 lds\n"
      "s_add_u32 m0, s43, 77824\n"
      "s_add_u32 s55, s55, 173056\n"
      "buffer_load_dwordx4 v1, s[8:11], s54 offen offset:0 sc1 lds\n"
      "buffer_load_dwordx4 v1, s[8:11], s54 offen offset:1024 sc1 lds\n"
      "s_mov_b32 s54, s36\n"
      "s_add_u32 m0, s43, 98304\n"
      "s_add_u32 s36, s36, 165888\n"
      "v_add_u32 v14, 1, v14\n"
      "s_waitcnt vmcnt(4)\n"
      "v_cmp_ne_u32 vcc, s21, v20\n"
      "s_cbranch_vccnz .Lsp_d4_24\n"
      ".Lbk_d4_24:\n"
      "s_min_u32 s36, s36, s6\n"
      "buffer_load_dwordx4 v0, s[8:11], s54 offen offset:0 sc1 lds\n"
      "buffer_load_dwordx4 v0, s[8:11], s54 offen offset:2048 sc1 lds\n"
      "s_waitcnt vmcnt(2)\n"
      "ds_write_b32 v16, v14\n"
      "buffer_load_dword v20, v2, s[8:11], s36 offen sc1\n"
      "buffer_load_dwordx4 v0, s[8:11], s54 offen offset:3072 sc1 lds\n"
      "ds_read_b32 v15, v17\n"
      "buffer_load_dwordx4 v0, s[8:11], s54 offen offset:1024 sc1 lds\n"
      "s_add_u32 m0, s43, 102400\n"
      "s_nop 0\n"
      "buffer_load_dwordx4 v1, s[8:11], s54 offen offset:1024 sc1 lds\n"
      "s_waitcnt lgkmcnt(0)\n"
      "v_cmp_gt_i32 vcc, v14, v15\n"
      "buffer_load_dwordx4 v1, s[8:11], s54 offen offset:0 sc1 lds\n"
      "s_cbranch_vccnz .Lsp_dy3_25\n"
      ".Lbk_dy3_25:\n"
      "ds_read_b128 v[60:63], v22 offset:25600\n"
      "ds_read_b128 v[64:67], v22 offset:26624\n"
      "s_mov_b32 s54, s36\n"
      "ds_read_b128 v[72:75], v22 offset:28672\n"
      "ds_read_b128 v[68:71], v22 offset:27648\n"
      "ds_read_b128 v[92:95], v22 offset:33792\n"
      "ds_read_b128 v[56:59], v22 offset:24576\n"
      "ds_read_b128 v[100:103], v22 offset:35840\n"
      "ds_read_b128 v[80:83], v22 offset:30720\n"
      "ds_read_b128 v[112:115], v22 offset:38912\n"
      "s_add_u32 s37, s37, 106496\n"
      "ds_read_b128 v[88:91], v22 offset:32768\n"
      "ds_read_b128 v[96:99], v22 offset:34816\n"
      "ds_read_b128 v[76:79], v22 offset:29696\n"
      "ds_read_b128 v[116:119], v22 offset:39936\n"
      "ds_read_b128 v[84:87], v22 offset:31744\n"
      "ds_read_b128 v[104:107], v22 offset:36864\n"
      "s_waitcnt vmcnt(0) lgkmcnt(0)\n"
      "s_nop 4\n"
      "s_branch .Ld_t3\n"
      ".Ld_in4:\n"
      "s_add_u32 s36, s36, 165888\n"
      "s_min_u32 s36, s36, s6\n"
      "s_mov_b32 s54, s36\n"
      "s_add_u32 s36, s36, 165888\n"
      "s_min_u32 s36, s36, s6\n"
      "s_mov_b32 s54, s36\n"
      "s_add_u32 s36, s36, 165888\n"
      "s_min_u32 s36, s36, s6\n"
      "s_mov_b32 s54, s36\n"
      "s_add_u32 s36, s36, 165888\n"
      "s_min_u32 s36, s36, s6\n"
      "s_add_u32 s55, s55, 106496\n"
      "s_add_u32 s37, s37, 173056\n"
      "s_mov_b32 s54, s36\n"
      "s_add_u32 s36, s36, 165888\n"
      "s_add_u32 s38, s38, 147456\n"
      "s_min_u32 s36, s36, s6\n"
      "buffer_load_dword v20, v2, s[8:11], s36 offen sc1\n"
      "s_add_u32 s55, s55, 173056\n"
      "s_mov_b32 s54, s36\n"
      "s_add_u32 m0, s43, 98304\n"
      "s_add_u32 s36, s36, 165888\n"
      "s_waitcnt vmcnt(0)\n"
      "v_cmp_ne_u32 vcc, s21, v20\n"
      "s_cbranch_vccnz .Lsp_d4_26\n"
      ".Lbk_d4_26:\n"
      "s_min_u32 s36, s36, s6\n"
      "buffer_load_dwordx4 v0, s[8:11], s54 offen offset:0 sc1 lds\n"
      "buffer_load_dwordx4 v0, s[8:11], s54 offen offset:2048 sc1 lds\n"
      "buffer_load_dword v20, v2, s[8:11], s36 offen sc1\n"
      "buffer_load_dwordx4 v0, s[8:11], s54 offen offset:3072 sc1 lds\n"
      "buffer_load_dwordx4 v0, s[8:11], s54 offen offset:1024 sc1 lds\n"
      "s_add_u32 m0, s43, 102400\n"
      "s_nop 0\n"
      "buffer_load_dwordx4 v1, s[8:11], s54 offen offset:1024 sc1 lds\n"
      "buffer_load_dwordx4 v1, s[8:11], s54 offen offset:0 sc1 lds\n"
      "s_mov_b32 s54, s36\n"
      "s_add_u32 s37, s37, 106496\n"
      "s_add_u32 m0, s43, 122880\n"
      "s_waitcnt vmcnt(4)\n"
      "v_cmp_ne_u32 vcc, s21, v20\n"
      "s_add_u32 s36, s36, 165888\n"
      "s_add_u32 s55, s55, 106496\n"
      "s_cbranch_vccnz .Lsp_d5_27\n"
      ".Lbk_d5_27:\n"
      "buffer_load_dwordx4 v0, s[8:11], s54 offen offset:1024 sc1 lds\n"
      "v_add_u32 v14, 1, v14\n"
      "buffer_load_dwordx4 v0, s[8:11], s54 offen offset:0 sc1 lds\n"
      "buffer_load_dwordx4 v0, s[8:11], s54 offen offset:2048 sc1 lds\n"
      "s_waitcnt vmcnt(3)\n"
      "ds_write_b32 v16, v14\n"
      "buffer_load_dwordx4 v0, s[8:11], s54 offen offset:3072 sc1 lds\n"
      "s_min_u32 s36, s36, s6\n"
      "s_add_u32 m0, s43, 126976\n"
      "s_nop 0\n"
      "buffer_load_dwordx4 v1, s[8:11], s54 offen offset:1024 sc1 lds\n"
      "ds_read_b32 v15, v17\n"
      "buffer_load_dword v20, v2, s[8:11], s36 offen sc1\n"
      "s_waitcnt lgkmcnt(0)\n"
      "v_cmp_gt_i32 vcc, v14, v15\n"
      "buffer_load_dwordx4 v1, s[8:11], s54 offen offset:0 sc1 lds\n"
      "s_cbranch_vccnz .Lsp_dy4_28\n"
      ".Lbk_dy4_28:\n"
      "ds_read_b128 v[60:63], v23 offset:1024\n"
      "ds_read_b128 v[64:67], v23 offset:2048\n"
      "ds_read_b128 v[72:75], v23 offset:4096\n"
      "ds_read_b128 v[68:71], v23 offset:3072\n"
      "s_add_u32 s61, s41, 4294967264\n"
      "ds_read_b128 v[80:83], v23 offset:6144\n"
      "ds_read_b128 v[84:87], v23 offset:7168\n"
      "ds_read_b128 v[76:79], v23 offset:5120\n"
      "s_add_u32 s37, s37, 106496\n"
      "ds_read_b128 v[56:59], v23 offset:0\n"
      "ds_read_b128 v[88:91], v23 offset:8192\n"
      "ds_read_b128 v[96:99], v23 offset:10240\n"
      "ds_read_b128 v[100:103], v23 offset:11264\n"
      "ds_read_b128 v[132:135], v23 offset:19456\n"
      "ds_read_b128 v[104:107], v23 offset:12288\n"
      "s_waitcnt vmcnt(0) lgkmcnt(0)\n"
      "s_nop 4\n"
      "s_branch .Ld_t4\n"
      ".Ld_in5:\n"
      "s_add_u32 s36, s36, 165888\n"
      "s_min_u32 s36, s36, s6\n"
      "s_mov_b32 s54, s36\n"
      "s_add_u32 s36, s36, 165888\n"
      "s_min_u32 s36, s36, s6\n"
      "s_mov_b32 s54, s36\n"
      "s_add_u32 s36, s36, 165888\n"
      "s_min_u32 s36, s36, s6\n"
      "s_mov_b32 s54, s36\n"
      "s_add_u32 s36, s36, 165888\n"
      "s_min_u32 s36, s36, s6\n"
      "s_add_u32 s55, s55, 106496\n"
      "s_add_u32 s37, s37, 173056\n"
      "s_mov_b32 s54, s36\n"
      "s_add_u32 s36, s36, 165888\n"
      "s_add_u32 s38, s38, 147456\n"
      "s_min_u32 s36, s36, s6\n"
      "s_add_u32 s55, s55, 173056\n"
      "s_mov_b32 s54, s36\n"
      "s_add_u32 s36, s36, 165888\n"
      "s_min_u32 s36, s36, s6\n"
      "buffer_load_dword v20, v2, s[8:11], s36 offen sc1\n"
      "s_mov_b32 s54, s36\n"
      "s_add_u32 s37, s37, 106496\n"
      "s_add_u32 m0, s43, 122880\n"
      "s_waitcnt vmcnt(0)\n"
      "v_cmp_ne_u32 vcc, s21, v20\n"
      "s_add_u32 s36, s36, 165888\n"
      "s_add_u32 s55, s55, 106496\n"
      "s_cbranch_vccnz .Lsp_d5_29\n"
      ".Lbk_d5_29:\n"
      "buffer_load_dwordx4 v0, s[8:11], s54 offen offset:1024 sc1 lds\n"
      "buffer_load_dwordx4 v0, s[8:11], s54 offen offset:0 sc1 lds\n"
      "buffer_load_dwordx4 v0, s[8:11], s54 offen offset:2048 sc1 lds\n"
      "buffer_load_dwordx4 v0, s[8:11], s54 offen offset:3072 sc1 lds\n"
      "s_min_u32 s36, s36, s6\n"
      "s_add_u32 m0, s43, 126976\n"
      "s_nop 0\n"
      "buffer_load_dwordx4 v1, s[8:11], s54 offen offset:1024 sc1 lds\n"
      "buffer_load_dword v20, v2, s[8:11], s36 offen sc1\n"
      "buffer_load_dwordx4 v1, s[8:11], s54 offen offset:0 sc1 lds\n"
      "s_add_u32 s61, s41, 4294967264\n"
      "s_add_u32 s37, s37, 106496\n"
      "s_add_u32 m0, s43, 0\n"
      "s_mov_b32 s54, s36\n"
      "v_add_u32 v14, 1, v14\n"
      "s_add_u32 s36, s36, 165888\n"
      "s_add_u32 s37, s37, 106496\n"
      "s_min_u32 s36, s36, s6\n"
      "s_cmp_lt_u32 s41, 32\n"
      "s_cselect_b32 s59, s39, 0x40000000\n"
      "s_add_u32 s56, s56, 599040\n"
      "s_waitcnt vmcnt(1)\n"
      "v_cmp_ne_u32 vcc, s21, v20\n"
      "s_add_u32 s39, s39, 147456\n"
      "s_waitcnt vmcnt(0)\n"
      "ds_write_b32 v16, v14\n"
      "s_cmp_lt_u32 s61, 32\n"
      "s_cselect_b32 s60, s40, 0x40000000\n"
      "s_cbranch_vccnz .Lsp_d0_30\n"
      ".Lbk_d0_30:\n"
      "buffer_load_dwordx4 v0, s[8:11], s54 offen offset:0 sc1 lds\n"
      "s_add_u32 s55, s55, 106496\n"
      "buffer_load_dwordx4 v0, s[8:11], s54 offen offset:1024 sc1 lds\n"
      "buffer_load_dword v20, v2, s[8:11], s36 offen sc1\n"
      "ds_read_b32 v15, v17\n"
      "buffer_load_dwordx4 v0, s[8:11], s54 offen offset:2048 sc1 lds\n"
      "s_add_u32 s40, s40, 4294856704\n"
      "buffer_load_dwordx4 v0, s[8:11], s54 offen offset:3072 sc1 lds\n"
      "s_waitcnt lgkmcnt(0)\n"
      "v_cmp_gt_i32 vcc, v14, v15\n"
      "s_add_u32 m0, s43, 4096\n"
      "s_nop 0\n"
      "buffer_load_dwordx4 v1, s[8:11], s54 offen offset:1024 sc1 lds\n"
      "s_cbranch_vccnz .Lsp_dy5_31\n"
      ".Lbk_dy5_31:\n"
      "ds_read_b128 v[56:59], v23 offset:24576\n"
      "ds_read_b128 v[64:67], v23 offset:26624\n"
      "buffer_load_dwordx4 v1, s[8:11], s54 offen offset:0 sc1 lds\n"
      "ds_read_b128 v[76:79], v23 offset:29696\n"
      "s_add_u32 s41, s41, 6\n"
      "ds_read_b128 v[88:91], v23 offset:32768\n"
      "ds_read_b128 v[68:71], v23 offset:27648\n"
      "ds_read_b128 v[96:99], v23 offset:34816\n"
      "ds_read_b128 v[80:83], v23 offset:30720\n"
      "ds_read_b128 v[72:75], v23 offset:28672\n"
      "s_mov_b32 s54, s36\n"
      "ds_read_b128 v[112:115], v23 offset:38912\n"
      "ds_read_b128 v[60:63], v23 offset:25600\n"
      "ds_read_b128 v[104:107], v23 offset:36864\n"
      "ds_read_b128 v[84:87], v23 offset:31744\n"
      "ds_read_b128 v[140:143], v23 offset:46080\n"
      "ds_read_b128 v[100:103], v23 offset:35840\n"
      "s_waitcnt vmcnt(0) lgkmcnt(0)\n"
      "s_nop 4\n"
      "s_branch .Ld_t5\n"
      ".Ld_end:\n"
      "s_endpgm\n"
      ".Lsp_d0_1:\n"
      "s_mov_b32 s22, s23\n"
      ".Lsl_d0_1:\n"
      "buffer_load_dword v20, v2, s[8:11], s54 offen sc1\n"
      "s_waitcnt vmcnt(0)\n"
      "v_cmp_ne_u32 vcc, s21, v20\n"
      "s_cbranch_vccz .Lbk_d0_1\n"
      "s_sleep 4\n"
      "s_sub_u32 s22, s22, 1\n"
      "s_cbranch_scc0 .Lsl_d0_1\n"
      "s_mov_b32 s23, 0\n"
      "s_branch .Lbk_d0_1\n"
      ".Lsp_d1_2:\n"
      "s_mov_b32 s22, s23\n"
      ".Lsl_d1_2:\n"
      "buffer_load_dword v20, v2, s[8:11], s54 offen sc1\n"
      "s_waitcnt vmcnt(0)\n"
      "v_cmp_ne_u32 vcc, s21, v20\n"
      "s_cbranch_vccz .Lbk_d1_2\n"
      "s_sleep 4\n"
      "s_sub_u32 s22, s22, 1\n"
      "s_cbranch_scc0 .Lsl_d1_2\n"
      "s_mov_b32 s23, 0\n"
      "s_branch .Lbk_d1_2\n"
      ".Lsp_dy0_3:\n"
      "s_lshl_b32 s22, s23, 4\n"
      ".Lsl_dy0_3:\n"
      "ds_read_b32 v15, v17\n"
      "s_waitcnt lgkmcnt(0)\n"
      "v_cmp_gt_i32 vcc, v14, v15\n"
      "s_cbranch_vccz .Lbk_dy0_3\n"
      "s_sub_u32 s22, s22, 1\n"
      "s_cbranch_scc0 .Lsl_dy0_3\n"
      "s_mov_b32 s23, 0\n"
      "s_branch .Lbk_dy0_3\n"
      ".Lsp_d2_4:\n"
      "s_mov_b32 s22, s23\n"
      "buffer_load_dword v20, v2, s[8:11], s54 offen sc1\n"
      "s_waitcnt vmcnt(0)\n"
      "v_cmp_ne_u32 vcc, s21, v20\n"
      "s_add_u32 s30, s55, 106496\n"
      "buffer_store_dword v18, v4, s[8:11], s30 offen sc1\n"
      "s_cbranch_vccz .Lbk_d2_4\n"
      ".Lsl_d2_4:\n"
      "buffer_load_dword v20, v2, s[8:11], s54 offen sc1\n"
      "s_waitcnt vmcnt(0)\n"
      "v_cmp_ne_u32 vcc, s21, v20\n"
      "s_cbranch_vccz .Lbk_d2_4\n"
      "s_sleep 4\n"
      "s_sub_u32 s22, s22, 1\n"
      "s_cbranch_scc0 .Lsl_d2_4\n"
      "s_mov_b32 s23, 0\n"
      "s_branch .Lbk_d2_4\n"
      ".Lsp_dy1_5:\n"
      "s_lshl_b32 s22, s23, 4\n"
      ".Lsl_dy1_5:\n"
      "ds_read_b32 v15, v17\n"
      "s_waitcnt lgkmcnt(0)\n"
      "v_cmp_gt_i32 vcc, v14, v15\n"
      "s_cbranch_vccz .Lbk_dy1_5\n"
      "s_sub_u32 s22, s22, 1\n"
      "s_cbranch_scc0 .Lsl_dy1_5\n"
      "s_mov_b32 s23, 0\n"
      "s_branch .Lbk_dy1_5\n"
      ".Lsp_d3_6:\n"
      "s_mov_b32 s22, s23\n"
      "buffer_load_dword v20, v2, s[8:11], s54 offen sc1\n"
      "s_waitcnt vmcnt(0)\n"
      "v_cmp_ne_u32 vcc, s21, v20\n"
      "buffer_store_dword v18, v5, s[8:11], s59 offen sc1\n"
      "s_cbranch_vccz .Lbk_d3_6\n"
      ".Lsl_d3_6:\n"
      "buffer_load_dword v20, v2, s[8:11], s54 offen sc1\n"
      "s_waitcnt vmcnt(0)\n"
      "v_cmp_ne_u32 vcc, s21, v20\n"
      "s_cbranch_vccz .Lbk_d3_6\n"
      "s_sleep 4\n"
      "s_sub_u32 s22, s22, 1\n"
      "s_cbranch_scc0 .Lsl_d3_6\n"
      "s_mov_b32 s23, 0\n"
      "s_branch .Lbk_d3_6\n"
      ".Lsp_dy2_7:\n"
      "s_lshl_b32 s22, s23, 4\n"
      ".Lsl_dy2_7:\n"
      "ds_read_b32 v15, v17\n"
      "s_waitcnt lgkmcnt(0)\n"
      "v_cmp_gt_i32 vcc, v14, v15\n"
      "s_cbranch_vccz .Lbk_dy2_7\n"
      "s_sub_u32 s22, s22, 1\n"
      "s_cbranch_scc0 .Lsl_dy2_7\n"
      "s_mov_b32 s23, 0\n"
      "s_branch .Lbk_dy2_7\n"
      ".Lsp_d4_8:\n"
      "s_mov_b32 s22, s23\n"
      "buffer_load_dword v20, v2, s[8:11], s54 offen sc1\n"
      "s_waitcnt vmcnt(0)\n"
      "v_cmp_ne_u32 vcc, s21, v20\n"
      "s_add_u32 s30, s55, 0\n"
      "buffer_store_dword v18, v4, s[8:11], s30 offen sc1\n"
      "s_cbranch_vccz .Lbk_d4_8\n"
      ".Lsl_d4_8:\n"
      "buffer_load_dword v20, v2, s[8:11], s54 offen sc1\n"
      "s_waitcnt vmcnt(0)\n"
      "v_cmp_ne_u32 vcc, s21, v20\n"
      "s_cbranch_vccz .Lbk_d4_8\n"
      "s_sleep 4\n"
      "s_sub_u32 s22, s22, 1\n"
      "s_cbranch_scc0 .Lsl_d4_8\n"
      "s_mov_b32 s23, 0\n"
      "s_branch .Lbk_d4_8\n"
      ".Lsp_dy3_9:\n"
      "s_lshl_b32 s22, s23, 4\n"
      ".Lsl_dy3_9:\n"
      "ds_read_b32 v15, v17\n"
      "s_waitcnt lgkmcnt(0)\n"
      "v_cmp_gt_i32 vcc, v14, v15\n"
      "s_cbranch_vccz .Lbk_dy3_9\n"
      "s_sub_u32 s22, s22, 1\n"
      "s_cbranch_scc0 .Lsl_dy3_9\n"
      "s_mov_b32 s23, 0\n"
      "s_branch .Lbk_dy3_9\n"
      ".Lsp_d5_10:\n"
      "s_mov_b32 s22, s23\n"
      "buffer_load_dword v20, v2, s[8:11], s54 offen sc1\n"
      "s_waitcnt vmcnt(0)\n"
      "v_cmp_ne_u32 vcc, s21, v20\n"
      "s_add_u32 s30, s55, 0\n"
      "buffer_store_dword v18, v4, s[8:11], s30 offen sc1\n"
      "s_cbranch_vccz .Lbk_d5_10\n"
      ".Lsl_d5_10:\n"
      "buffer_load_dword v20, v2, s[8:11], s54 offen sc1\n"
      "s_waitcnt vmcnt(0)\n"
      "v_cmp_ne_u32 vcc, s21, v20\n"
      "s_cbranch_vccz .Lbk_d5_10\n"
      "s_sleep 4\n"
      "s_sub_u32 s22, s22, 1\n"
      "s_cbranch_scc0 .Lsl_d5_10\n"
      "s_mov_b32 s23, 0\n"
      "s_branch .Lbk_d5_10\n"
      ".Lsp_dy4_11:\n"
      "s_lshl_b32 s22, s23, 4\n"
      ".Lsl_dy4_11:\n"
      "ds_read_b32 v15, v17\n"
      "s_waitcnt lgkmcnt(0)\n"
      "v_cmp_gt_i32 vcc, v14, v15\n"
      "s_cbranch_vccz .Lbk_dy4_11\n"
      "s_sub_u32 s22, s22, 1\n"
      "s_cbranch_scc0 .Lsl_dy4_11\n"
      "s_mov_b32 s23, 0\n"
      "s_branch .Lbk_dy4_11\n"
      ".Lsp_df_12:\n"
      "s_mov_b32 s22, s23\n"
      "buffer_load_dword v21, v3, s[8:11], s56 offen sc1\n"
      "s_waitcnt vmcnt(0)\n"
      "v_cmp_ne_u32 vcc, s21, v21\n"
      "s_add_u32 s30, s55, 0\n"
      "buffer_store_dword v18, v4, s[8:11], s30 offen sc1\n"
      "s_cbranch_vccz .Lbk_df_12\n"
      ".Lsl_df_12:\n"
      "buffer_load_dword v21, v3, s[8:11], s56 offen sc1\n"
      "s_waitcnt vmcnt(0)\n"
      "v_cmp_ne_u32 vcc, s21, v21\n"
      "s_cbranch_vccz .Lbk_df_12\n"
      "s_sleep 4\n"
      "s_sub_u32 s22, s22, 1\n"
      "s_cbranch_scc0 .Lsl_df_12\n"
      "s_mov_b32 s23, 0\n"
      "s_branch .Lbk_df_12\n"
      ".Lsp_d0_13:\n"
      "s_mov_b32 s22, s23\n"
      "buffer_load_dword v20, v2, s[8:11], s54 offen sc1\n"
      "s_waitcnt vmcnt(0)\n"
      "v_cmp_ne_u32 vcc, s21, v20\n"
      "s_add_u32 s30, s55, 106496\n"
      "buffer_store_dword v18, v4, s[8:11], s30 offen sc1\n"
      "s_cbranch_vccz .Lbk_d0_13\n"
      ".Lsl_d0_13:\n"
      "buffer_load_dword v20, v2, s[8:11], s54 offen sc1\n"
      "s_waitcnt vmcnt(0)\n"
      "v_cmp_ne_u32 vcc, s21, v20\n"
      "s_cbranch_vccz .Lbk_d0_13\n"
      "s_sleep 4\n"
      "s_sub_u32 s22, s22, 1\n"
      "s_cbranch_scc0 .Lsl_d0_13\n"
      "s_mov_b32 s23, 0\n"
      "s_branch .Lbk_d0_13\n"
      ".Lsp_dy5_14:\n"
      "s_lshl_b32 s22, s23, 4\n"
      ".Lsl_dy5_14:\n"
      "ds_read_b32 v15, v17\n"
      "s_waitcnt lgkmcnt(0)\n"
      "v_cmp_gt_i32 vcc, v14, v15\n"
      "s_cbranch_vccz .Lbk_dy5_14\n"
      "s_sub_u32 s22, s22, 1\n"
      "s_cbranch_scc0 .Lsl_dy5_14\n"
      "s_mov_b32 s23, 0\n"
      "s_branch .Lbk_dy5_14\n"
      ".Lsp_d1_15:\n"
      "s_mov_b32 s22, s23\n"
      "buffer_load_dword v20, v2, s[8:11], s54 offen sc1\n"
      "s_waitcnt vmcnt(0)\n"
      "v_cmp_ne_u32 vcc, s21, v20\n"
      "s_add_u32 s30, s55, 106496\n"
      "buffer_store_dword v18, v4, s[8:11], s30 offen sc1\n"
      "s_cbranch_vccz .Lbk_d1_15\n"
      ".Lsl_d1_15:\n"
      "buffer_load_dword v20, v2, s[8:11], s54 offen sc1\n"
      "s_waitcnt vmcnt(0)\n"
      "v_cmp_ne_u32 vcc, s21, v20\n"
      "s_cbranch_vccz .Lbk_d1_15\n"
      "s_sleep 4\n"
      "s_sub_u32 s22, s22, 1\n"
      "s_cbranch_scc0 .Lsl_d1_15\n"
      "s_mov_b32 s23, 0\n"
      "s_branch .Lbk_d1_15\n"
      ".Lsp_dy0_16:\n"
      "s_lshl_b32 s22, s23, 4\n"
      ".Lsl_dy0_16:\n"
      "ds_read_b32 v15, v17\n"
      "s_waitcnt lgkmcnt(0)\n"
      "v_cmp_gt_i32 vcc, v14, v15\n"
      "s_cbranch_vccz .Lbk_dy0_16\n"
      "s_sub_u32 s22, s22, 1\n"
      "s_cbranch_scc0 .Lsl_dy0_16\n"
      "s_mov_b32 s23, 0\n"
      "s_branch .Lbk_dy0_16\n"
      ".Lsp_d1_17:\n"
      "s_mov_b32 s22, s23\n"
      ".Lsl_d1_17:\n"
      "buffer_load_dword v20, v2, s[8:11], s54 offen sc1\n"
      "s_waitcnt vmcnt(0)\n"
      "v_cmp_ne_u32 vcc, s21, v20\n"
      "s_cbranch_vccz .Lbk_d1_17\n"
      "s_sleep 4\n"
      "s_sub_u32 s22, s22, 1\n"
      "s_cbranch_scc0 .Lsl_d1_17\n"
      "s_mov_b32 s23, 0\n"
      "s_branch .Lbk_d1_17\n"
      ".Lsp_d2_18:\n"
      "s_mov_b32 s22, s23\n"
      ".Lsl_d2_18:\n"
      "buffer_load_dword v20, v2, s[8:11], s54 offen sc1\n"
      "s_waitcnt vmcnt(0)\n"
      "v_cmp_ne_u32 vcc, s21, v20\n"
      "s_cbranch_vccz .Lbk_d2_18\n"
      "s_sleep 4\n"
      "s_sub_u32 s22, s22, 1\n"
      "s_cbranch_scc0 .Lsl_d2_18\n"
      "s_mov_b32 s23, 0\n"
      "s_branch .Lbk_d2_18\n"
      ".Lsp_dy1_19:\n"
      "s_lshl_b32 s22, s23, 4\n"
      ".Lsl_dy1_19:\n"
      "ds_read_b32 v15, v17\n"
      "s_waitcnt lgkmcnt(0)\n"
      "v_cmp_gt_i32 vcc, v14, v15\n"
      "s_cbranch_vccz .Lbk_dy1_19\n"
      "s_sub_u32 s22, s22, 1\n"
      "s_cbranch_scc0 .Lsl_dy1_19\n"
      "s_mov_b32 s23, 0\n"
      "s_branch .Lbk_dy1_19\n"
      ".Lsp_d2_20:\n"
      "s_mov_b32 s22, s23\n"
      ".Lsl_d2_20:\n"
      "buffer_load_dword v20, v2, s[8:11], s54 offen sc1\n"
      "s_waitcnt vmcnt(0)\n"
      "v_cmp_ne_u32 vcc, s21, v20\n"
      "s_cbranch_vccz .Lbk_d2_20\n"
      "s_sleep 4\n"
      "s_sub_u32 s22, s22, 1\n"
      "s_cbranch_scc0 .Lsl_d2_20\n"
      "s_mov_b32 s23, 0\n"
      "s_branch .Lbk_d2_20\n"
      ".Lsp_d3_21:\n"
      "s_mov_b32 s22, s23\n"
      ".Lsl_d3_21:\n"
      "buffer_load_dword v20, v2, s[8:11], s54 offen sc1\n"
      "s_waitcnt vmcnt(0)\n"
      "v_cmp_ne_u32 vcc, s21, v20\n"
      "s_cbranch_vccz .Lbk_d3_21\n"
      "s_sleep 4\n"
      "s_sub_u32 s22, s22, 1\n"
      "s_cbranch_scc0 .Lsl_d3_21\n"
      "s_mov_b32 s23, 0\n"
      "s_branch .Lbk_d3_21\n"
      ".Lsp_dy2_22:\n"
      "s_lshl_b32 s22, s23, 4\n"
      ".Lsl_dy2_22:\n"
      "ds_read_b32 v15, v17\n"
      "s_waitcnt lgkmcnt(0)\n"
      "v_cmp_gt_i32 vcc, v14, v15\n"
      "s_cbranch_vccz .Lbk_dy2_22\n"
      "s_sub_u32 s22, s22, 1\n"
      "s_cbranch_scc0 .Lsl_dy2_22\n"
      "s_mov_b32 s23, 0\n"
      "s_branch .Lbk_dy2_22\n"
      ".Lsp_d3_23:\n"
      "s_mov_b32 s22, s23\n"
      ".Lsl_d3_23:\n"
      "buffer_load_dword v20, v2, s[8:11], s54 offen sc1\n"
      "s_waitcnt vmcnt(0)\n"
      "v_cmp_ne_u32 vcc, s21, v20\n"
      "s_cbranch_vccz .Lbk_d3_23\n"
      "s_sleep 4\n"
      "s_sub_u32 s22, s22, 1\n"
      "s_cbranch_scc0 .Lsl_d3_23\n"
      "s_mov_b32 s23, 0\n"
      "s_branch .Lbk_d3_23\n"
      ".Lsp_d4_24:\n"
      "s_mov_b32 s22, s23\n"
      ".Lsl_d4_24:\n"
      "buffer_load_dword v20, v2, s[8:11], s54 offen sc1\n"
      "s_waitcnt vmcnt(0)\n"
      "v_cmp_ne_u32 vcc, s21, v20\n"
      "s_cbranch_vccz .Lbk_d4_24\n"
      "s_sleep 4\n"
      "s_sub_u32 s22, s22, 1\n"
      "s_cbranch_scc0 .Lsl_d4_24\n"
      "s_mov_b32 s23, 0\n"
      "s_branch .Lbk_d4_24\n"
      ".Lsp_dy3_25:\n"
      "s_lshl_b32 s22, s23, 4\n"
      ".Lsl_dy3_25:\n"
      "ds_read_b32 v15, v17\n"
      "s_waitcnt lgkmcnt(0)\n"
      "v_cmp_gt_i32 vcc, v14, v15\n"
      "s_cbranch_vccz .Lbk_dy3_25\n"
      "s_sub_u32 s22, s22, 1\n"
      "s_cbranch_scc0 .Lsl_dy3_25\n"
      "s_mov_b32 s23, 0\n"
      "s_branch .Lbk_dy3_25\n"
      ".Lsp_d4_26:\n"
      "s_mov_b32 s22, s23\n"
      ".Lsl_d4_26:\n"
      "buffer_load_dword v20, v2, s[8:11], s54 offen sc1\n"
      "s_waitcnt vmcnt(0)\n"
      "v_cmp_ne_u32 vcc, s21, v20\n"
      "s_cbranch_vccz .Lbk_d4_26\n"
      "s_sleep 4\n"
      "s_sub_u32 s22, s22, 1\n"
      "s_cbranch_scc0 .Lsl_d4_26\n"
      "s_mov_b32 s23, 0\n"
      "s_branch .Lbk_d4_26\n"
      ".Lsp_d5_27:\n"
      "s_mov_b32 s22, s23\n"
      ".Lsl_d5_27:\n"
      "buffer_load_dword v20, v2, s[8:11], s54 offen sc1\n"
      "s_waitcnt vmcnt(0)\n"
      "v_cmp_ne_u32 vcc, s21, v20\n"
      "s_cbranch_vccz .Lbk_d5_27\n"
      "s_sleep 4\n"
      "s_sub_u32 s22, s22, 1\n"
      "s_cbranch_scc0 .Lsl_d5_27\n"
      "s_mov_b32 s23, 0\n"
      "s_branch .Lbk_d5_27\n"
      ".Lsp_dy4_28:\n"
      "s_lshl_b32 s22, s23, 4\n"
      ".Lsl_dy4_28:\n"
      "ds_read_b32 v15, v17\n"
      "s_waitcnt lgkmcnt(0)\n"
      "v_cmp_gt_i32 vcc, v14, v15\n"
      "s_cbranch_vccz .Lbk_dy4_28\n"
      "s_sub_u32 s22, s22, 1\n"
      "s_cbranch_scc0 .Lsl_dy4_28\n"
      "s_mov_b32 s23, 0\n"
      "s_branch .Lbk_dy4_28\n"
      ".Lsp_d5_29:\n"
      "s_mov_b32 s22, s23\n"
      ".Lsl_d5_29:\n"
      "buffer_load_dword v20, v2, s[8:11], s54 offen sc1\n"
      "s_waitcnt vmcnt(0)\n"
      "v_cmp_ne_u32 vcc, s21, v20\n"
      "s_cbranch_vccz .Lbk_d5_29\n"
      "s_sleep 4\n"
      "s_sub_u32 s22, s22, 1\n"
      "s_cbranch_scc0 .Lsl_d5_29\n"
      "s_mov_b32 s23, 0\n"
      "s_branch .Lbk_d5_29\n"
      ".Lsp_d0_30:\n"
      "s_mov_b32 s22, s23\n"
      ".Lsl_d0_30:\n"
      "buffer_load_dword v20, v2, s[8:11], s54 offen sc1\n"
      "s_waitcnt vmcnt(0)\n"
      "v_cmp_ne_u32 vcc, s21, v20\n"
      "s_cbranch_vccz .Lbk_d0_30\n"
      "s_sleep 4\n"
      "s_sub_u32 s22, s22, 1\n"
      "s_cbranch_scc0 .Lsl_d0_30\n"
      "s_mov_b32 s23, 0\n"
      "s_branch .Lbk_d0_30\n"
      ".Lsp_dy5_31:\n"
      "s_lshl_b32 s22, s23, 4\n"
      ".Lsl_dy5_31:\n"
      "ds_read_b32 v15, v17\n"
      "s_waitcnt lgkmcnt(0)\n"
      "v_cmp_gt_i32 vcc, v14, v15\n"
      "s_cbranch_vccz .Lbk_dy5_31\n"
      "s_sub_u32 s22, s22, 1\n"
      "s_cbranch_scc0 .Lsl_dy5_31\n"
      "s_mov_b32 s23, 0\n"
      "s_branch .Lbk_dy5_31\n"
      ".Ld_last:\n"
      :
      : "{s[8:11]}"(rsWS), "{s[12:15]}"(rsA), "{s[16:19]}"(rsB), "{s[92:95]}"(rsC), "{v0}"(v_l16), "{v1}"(v_l16b), "{v2}"(v_fa), "{v3}"(v_ga), "{v4}"(v_ra), "{v5}"(v_rf), "{v6}"(v_xp), "{v7}"(v_xs), "{v8}"(v_op), "{v9}"(v_os), "{v10}"(v_sw), "{v11}"(v_sr), "{v12}"(v_ss), "{v13}"(v_l8), "{v15}"(v_al), "{v16}"(v_yw), "{v17}"(v_yr), "{v19}"(v_hi), "{v176}"(v_at), "{v177}"(v_as), "{v178}"(v_vo0), "{v179}"(v_vo1), "{v180}"(v_vo2), "{v181}"(v_pw), "{v183}"(v_pu), "{s36}"(s_sh), "{s37}"(s_ps), "{s38}"(s_x1), "{s39}"(s_xr), "{s40}"(s_or), "{s41}"(s_p0), "{s42}"(s_m3), "{s43}"(s_m6), "{s44}"(s_so), "{s45}"(s_ph), "{s46}"(s_i0), "{s47}"(s_i5), "{s48}"(s_j0), "{s49}"(s_j5), "{s50}"(s_he), "{s51}"(s_pe[0]), "{s52}"(s_pe[1]), "{s53}"(s_pe[2]), "{s63}"(s_k0)
      : "v255", "a255", "s99", "vcc", "scc", "memory");

  } else {
    const unsigned e1 = (e - 1) & 3, gr = !fast ? slot & 3 : slot < 31 ? (e + slot - 27) & 3 : e1, part = !fast ? 8 * e + (slot >> 2) : slot < 31 ? 32 + e : 32 + e1;
    COMMON(112, 81920);
    const unsigned j = part, f = 4 * j + w;
    const u32x4 rsA = srd(w >> 1 ? Wu : Wg, 576 * 4608 * 2), rsC = srd(Wo, 576 * 576 * 2);
    const unsigned tw0 = !fast && slot < 4 ? ((e * 16 + slot * 4 + w) * 81 + lane) * 128 : OOB, v_tw[2] = {tw0, lane < 17 ? tw0 + 8192 : OOB};
    const unsigned v_l5 = w < 3 ? v_l16 + 16384 - w * 3072 : OOB;
    const unsigned v_fa = 19456 - w * 4096 + (lane >> 3) * 128 + (lane & 7) * 4, v_ra = up * 8 * HREC + l5 + 147456 - f * 1024 + f * 128;
    const unsigned one = q == g ? 0x3f80u << (16 * (p & 1)) : 0u, v_id[2] = {p < 2 ? one : 0u, p < 2 ? 0u : one};
    const unsigned v_st = 18432 + i16 * 32 + q * 8, v_pw = 147456 - f * 1024 + f * 128 + l8 * HREC + l7;
    const unsigned v_at = 65536 + (8 * g + q) * 256 + ((4 * w + p) ^ (4 * q)) * 16;
    const unsigned v_vo0 = (16 * (w & 1) + g) * 9216 + 256 * j + (i16 ^ (4 * g)) * 16;
    const unsigned v_vo1 = v_vo0 + 4 * 9216 - 1024, v_vo2 = v_vo1 + 4 * 9216 - 1024, v_vo3 = v_vo2 + 4 * 9216 - 1024;
    const unsigned s_sx = WS_X1 + gr * NREC * REC + w * 4096, s_sh = WS_H + gr * NREC * HREC + f * 1024;
    const unsigned s_m4 = w * 4096, s_m5 = 16384 + w * 1024;
    asm volatile(
      "s_buffer_load_dword s21, s[8:11], 0\n"
      "s_mov_b32 s23, 65536\n"
      "s_mov_b32 s31, 0x40000000\n"
      "s_mov_b32 s41, 0\n"
      "s_sleep 16\n"
      "buffer_load_dword v58, v54, s[92:95], s41 offen\n"
      "buffer_load_dword v59, v55, s[92:95], s41 offen\n"
      "s_waitcnt vmcnt(0)\n"
      "s_add_u32 m0, s38, 65536\n"
      "s_nop 0\n"
      "buffer_load_dwordx4 v6, s[12:15], s41 offen offset:0 lds\n"
      "buffer_load_dwordx4 v7, s[12:15], s41 offen offset:1024 lds\n"
      "buffer_load_dwordx4 v8, s[12:15], s41 offen offset:2048 lds\n"
      "buffer_load_dwordx4 v9, s[12:15], s41 offen offset:3072 lds\n"
      "s_add_u32 s41, s41, 294912\n"
      "s_add_u32 m0, s38, 81920\n"
      "s_nop 0\n"
      "buffer_load_dwordx4 v6, s[12:15], s41 offen offset:0 lds\n"
      "buffer_load_dwordx4 v7, s[12:15], s41 offen offset:1024 lds\n"
      "buffer_load_dwordx4 v8, s[12:15], s41 offen offset:2048 lds\n"
      "buffer_load_dwordx4 v9, s[12:15], s41 offen offset:3072 lds\n"
      "s_add_u32 s41, s41, 294912\n"
      "s_add_u32 m0, s38, 98304\n"
      "s_nop 0\n"
      "buffer_load_dwordx4 v6, s[12:15], s41 offen offset:0 lds\n"
      "buffer_load_dwordx4 v7, s[12:15], s41 offen offset:1024 lds\n"
      "buffer_load_dwordx4 v8, s[12:15], s41 offen offset:2048 lds\n"
      "buffer_load_dwordx4 v9, s[12:15], s41 offen offset:3072 lds\n"
      "s_add_u32 s41, s41, 294912\n"
      "s_add_u32 m0, s38, 114688\n"
      "s_nop 0\n"
      "buffer_load_dwordx4 v6, s[12:15], s41 offen offset:0 lds\n"
      "buffer_load_dwordx4 v7, s[12:15], s41 offen offset:1024 lds\n"
      "buffer_load_dwordx4 v8, s[12:15], s41 offen offset:2048 lds\n"
      "buffer_load_dwordx4 v9, s[12:15], s41 offen offset:3072 lds\n"
      "s_add_u32 s41, s41, 294912\n"
      "s_add_u32 m0, s38, 131072\n"
      "s_nop 0\n"
      "buffer_load_dwordx4 v6, s[12:15], s41 offen offset:0 lds\n"
      "buffer_load_dwordx4 v7, s[12:15], s41 offen offset:1024 lds\n"
      "buffer_load_dwordx4 v8, s[12:15], s41 offen offset:2048 lds\n"
      "buffer_load_dwordx4 v9, s[12:15], s41 offen offset:3072 lds\n"
      "s_add_u32 s41, s41, 294912\n"
      "v_mov_b32 v172, 0x3f803f80\n"
      "v_mov_b32 v173, 0x3f803f80\n"
      "v_mov_b32 v174, 0x3f803f80\n"
      "v_mov_b32 v175, 0x3f803f80\n"
      "v_add_u32 v19, 49152, v5\n"
      "s_mov_b64 exec, 0\n"
      "s_lshr_b32 s22, s38, 12\n"
      ".Lg_pl:\n"
      "s_cmp_eq_u32 s22, 0\n"
      "s_cbranch_scc1 .Lg_p0\n"
      "s_cmp_eq_u32 s22, 1\n"
      "s_cbranch_scc1 .Lg_p1\n"
      "s_branch .Lg_pe\n"
      ".Lg_pr:\n"
      "s_add_u32 s22, s22, 4\n"
      "s_branch .Lg_pl\n"
      ".Lg_pe:\n"
      "s_mov_b64 exec, -1\n"
      "s_waitcnt vmcnt(16)\n"
      "s_barrier\n"
      "ds_read_b64_tr_b16 a[0:1], v5 offset:0\n"
      "ds_read_b64_tr_b16 a[2:3], v5 offset:1024\n"
      "ds_read_b64_tr_b16 a[72:73], v5 offset:8192\n"
      "ds_read_b64_tr_b16 a[74:75], v5 offset:9216\n"
      "ds_read_b64_tr_b16 a[144:145], v5 offset:8\n"
      "ds_read_b64_tr_b16 a[146:147], v5 offset:1032\n"
      "ds_read_b64_tr_b16 a[216:217], v5 offset:8200\n"
      "ds_read_b64_tr_b16 a[218:219], v5 offset:9224\n"
      "s_add_u32 m0, s38, 147456\n"
      "s_nop 0\n"
      "buffer_load_dwordx4 v6, s[12:15], s41 offen offset:0 lds\n"
      "buffer_load_dwordx4 v7, s[12:15], s41 offen offset:1024 lds\n"
      "buffer_load_dwordx4 v8, s[12:15], s41 offen offset:2048 lds\n"
      "buffer_load_dwordx4 v9, s[12:15], s41 offen offset:3072 lds\n"
      "s_add_u32 s41, s41, 294912\n"
      "s_waitcnt vmcnt(16) lgkmcnt(0)\n"
      "s_barrier\n"
      "ds_read_b64_tr_b16 a[4:5], v5 offset:16384\n"
      "ds_read_b64_tr_b16 a[6:7], v5 offset:17408\n"
      "ds_read_b64_tr_b16 a[76:77], v5 offset:24576\n"
      "ds_read_b64_tr_b16 a[78:79], v5 offset:25600\n"
      "ds_read_b64_tr_b16 a[148:149], v5 offset:16392\n"
      "ds_read_b64_tr_b16 a[150:151], v5 offset:17416\n"
      "ds_read_b64_tr_b16 a[220:221], v5 offset:24584\n"
      "ds_read_b64_tr_b16 a[222:223], v5 offset:25608\n"
      "v_mfma_f32_16x16x32_bf16 v[76:79], v[172:175], a[0:3], 0\n"
      "v_mfma_f32_16x16x32_bf16 v[80:83], v[172:175], a[72:75], 0\n"
      "v_mfma_f32_16x16x32_bf16 v[84:87], v[172:175], a[144:147], 0\n"
      "v_mfma_f32_16x16x32_bf16 v[88:91], v[172:175], a[216:219], 0\n"
      "s_add_u32 m0, s38, 65536\n"
      "s_nop 0\n"
      "buffer_load_dwordx4 v6, s[12:15], s41 offen offset:0 lds\n"
      "buffer_load_dwordx4 v7, s[12:15], s41 offen offset:1024 lds\n"
      "buffer_load_dwordx4 v8, s[12:15], s41 offen offset:2048 lds\n"
      "buffer_load_dwordx4 v9, s[12:15], s41 offen offset:3072 lds\n"
      "s_add_u32 s41, s41, 294912\n"
      "s_waitcnt vmcnt(16) lgkmcnt(0)\n"
      "s_barrier\n"
      "ds_read_b64_tr_b16 a[8:9], v5 offset:32768\n"
      "ds_read_b64_tr_b16 a[10:11], v5 offset:33792\n"
      "ds_read_b64_tr_b16 a[80:81], v5 offset:40960\n"
      "ds_read_b64_tr_b16 a[82:83], v5 offset:41984\n"
      "ds_read_b64_tr_b16 a[152:153], v5 offset:32776\n"
      "ds_read_b64_tr_b16 a[154:155], v5 offset:33800\n"
      "ds_read_b64_tr_b16 a[224:225], v5 offset:40968\n"
      "ds_read_b64_tr_b16 a[226:227], v5 offset:41992\n"
      "v_mfma_f32_16x16x32_bf16 v[76:79], v[172:175], a[4:7], v[76:79]\n"
      "v_mfma_f32_16x16x32_bf16 v[80:83], v[172:175], a[76:79], v[80:83]\n"
      "v_mfma_f32_16x16x32_bf16 v[84:87], v[172:175], a[148:151], v[84:87]\n"
      "v_mfma_f32_16x16x32_bf16 v[88:91], v[172:175], a[220:223], v[88:91]\n"
      "s_add_u32 m0, s38, 81920\n"
      "s_nop 0\n"
      "buffer_load_dwordx4 v6, s[12:15], s41 offen offset:0 lds\n"
      "buffer_load_dwordx4 v7, s[12:15], s41 offen offset:1024 lds\n"
      "buffer_load_dwordx4 v8, s[12:15], s41 offen offset:2048 lds\n"
      "buffer_load_dwordx4 v9, s[12:15], s41 offen offset:3072 lds\n"
      "s_add_u32 s41, s41, 294912\n"
      "s_waitcnt vmcnt(16) lgkmcnt(0)\n"
      "s_barrier\n"
      "ds_read_b64_tr_b16 a[12:13], v19 offset:0\n"
      "ds_read_b64_tr_b16 a[14:15], v19 offset:1024\n"
      "ds_read_b64_tr_b16 a[84:85], v19 offset:8192\n"
      "ds_read_b64_tr_b16 a[86:87], v19 offset:9216\n"
      "ds_read_b64_tr_b16 a[156:157], v19 offset:8\n"
      "ds_read_b64_tr_b16 a[158:159], v19 offset:1032\n"
      "ds_read_b64_tr_b16 a[228:229], v19 offset:8200\n"
      "ds_read_b64_tr_b16 a[230:231], v19 offset:9224\n"
      "v_mfma_f32_16x16x32_bf16 v[76:79], v[172:175], a[8:11], v[76:79]\n"
      "v_mfma_f32_16x16x32_bf16 v[80:83], v[172:175], a[80:83], v[80:83]\n"
      "v_mfma_f32_16x16x32_bf16 v[84:87], v[172:175], a[152:155], v[84:87]\n"
      "v_mfma_f32_16x16x32_bf16 v[88:91], v[172:175], a[224:227], v[88:91]\n"
      "s_add_u32 m0, s38, 98304\n"
      "s_nop 0\n"
      "buffer_load_dwordx4 v6, s[12:15], s41 offen offset:0 lds\n"
      "buffer_load_dwordx4 v7, s[12:15], s41 offen offset:1024 lds\n"
      "buffer_load_dwordx4 v8, s[12:15], s41 offen offset:2048 lds\n"
      "buffer_load_dwordx4 v9, s[12:15], s41 offen offset:3072 lds\n"
      "s_add_u32 s41, s41, 294912\n"
      "s_waitcnt vmcnt(16) lgkmcnt(0)\n"
      "s_barrier\n"
      "ds_read_b64_tr_b16 a[16:17], v19 offset:16384\n"
      "ds_read_b64_tr_b16 a[18:19], v19 offset:17408\n"
      "ds_read_b64_tr_b16 a[88:89], v19 offset:24576\n"
      "ds_read_b64_tr_b16 a[90:91], v19 offset:25600\n"
      "ds_read_b64_tr_b16 a[160:161], v19 offset:16392\n"
      "ds_read_b64_tr_b16 a[162:163], v19 offset:17416\n"
      "ds_read_b64_tr_b16 a[232:233], v19 offset:24584\n"
      "ds_read_b64_tr_b16 a[234:235], v19 offset:25608\n"
      "v_mfma_f32_16x16x32_bf16 v[76:79], v[172:175], a[12:15], v[76:79]\n"
      "v_mfma_f32_16x16x32_bf16 v[80:83], v[172:175], a[84:87], v[80:83]\n"
      "v_mfma_f32_16x16x32_bf16 v[84:87], v[172:175], a[156:159], v[84:87]\n"
      "v_mfma_f32_16x16x32_bf16 v[88:91], v[172:175], a[228:231], v[88:91]\n"
      "s_add_u32 m0, s38, 114688\n"
      "s_nop 0\n"
      "buffer_load_dwordx4 v6, s[12:15], s41 offen offset:0 lds\n"
      "buffer_load_dwordx4 v7, s[12:15], s41 offen offset:1024 lds\n"
      "buffer_load_dwordx4 v8, s[12:15], s41 offen offset:2048 lds\n"
      "buffer_load_dwordx4 v9, s[12:15], s41 offen offset:3072 lds\n"
      "s_add_u32 s41, s41, 294912\n"
      "s_waitcnt vmcnt(16) lgkmcnt(0)\n"
      "s_barrier\n"
      "ds_read_b64_tr_b16 a[20:21], v19 offset:32768\n"
      "ds_read_b64_tr_b16 a[22:23], v19 offset:33792\n"
      "ds_read_b64_tr_b16 a[92:93], v19 offset:40960\n"
      "ds_read_b64_tr_b16 a[94:95], v19 offset:41984\n"
      "ds_read_b64_tr_b16 a[164:165], v19 offset:32776\n"
      "ds_read_b64_tr_b16 a[166:167], v19 offset:33800\n"
      "ds_read_b64_tr_b16 a[236:237], v19 offset:40968\n"
      "ds_read_b64_tr_b16 a[238:239], v19 offset:41992\n"
      "v_mfma_f32_16x16x32_bf16 v[76:79], v[172:175], a[16:19], v[76:79]\n"
      "v_mfma_f32_16x16x32_bf16 v[80:83], v[172:175], a[88:91], v[80:83]\n"
      "v_mfma_f32_16x16x32_bf16 v[84:87], v[172:175], a[160:163], v[84:87]\n"
      "v_mfma_f32_16x16x32_bf16 v[88:91], v[172:175], a[232:235], v[88:91]\n"
      "s_add_u32 m0, s38, 131072\n"
      "s_nop 0\n"
      "buffer_load_dwordx4 v6, s[12:15], s41 offen offset:0 lds\n"
      "buffer_load_dwordx4 v7, s[12:15], s41 offen offset:1024 lds\n"
      "buffer_load_dwordx4 v8, s[12:15], s41 offen offset:2048 lds\n"
      "buffer_load_dwordx4 v9, s[12:15], s41 offen offset:3072 lds\n"
      "s_add_u32 s41, s41, 294912\n"
      "s_waitcnt vmcnt(16) lgkmcnt(0)\n"
      "s_barrier\n"
      "ds_read_b64_tr_b16 a[24:25], v5 offset:0\n"
      "ds_read_b64_tr_b16 a[26:27], v5 offset:1024\n"
      "ds_read_b64_tr_b16 a[96:97], v5 offset:8192\n"
      "ds_read_b64_tr_b16 a[98:99], v5 offset:9216\n"
      "ds_read_b64_tr_b16 a[168:169], v5 offset:8\n"
      "ds_read_b64_tr_b16 a[170:171], v5 offset:1032\n"
      "ds_read_b64_tr_b16 a[240:241], v5 offset:8200\n"
      "ds_read_b64_tr_b16 a[242:243], v5 offset:9224\n"
      "v_mfma_f32_16x16x32_bf16 v[76:79], v[172:175], a[20:23], v[76:79]\n"
      "v_mfma_f32_16x16x32_bf16 v[80:83], v[172:175], a[92:95], v[80:83]\n"
      "v_mfma_f32_16x16x32_bf16 v[84:87], v[172:175], a[164:167], v[84:87]\n"
      "v_mfma_f32_16x16x32_bf16 v[88:91], v[172:175], a[236:239], v[88:91]\n"
      "s_add_u32 m0, s38, 147456\n"
      "s_nop 0\n"
      "buffer_load_dwordx4 v6, s[12:15], s41 offen offset:0 lds\n"
      "buffer_load_dwordx4 v7, s[12:15], s41 offen offset:1024 lds\n"
      "buffer_load_dwordx4 v8, s[12:15], s41 offen offset:2048 lds\n"
      "buffer_load_dwordx4 v9, s[12:15], s41 offen offset:3072 lds\n"
      "s_add_u32 s41, s41, 294912\n"
      "s_waitcnt vmcnt(16) lgkmcnt(0)\n"
      "s_barrier\n"
      "ds_read_b64_tr_b16 a[28:29], v5 offset:16384\n"
      "ds_read_b64_tr_b16 a[30:31], v5 offset:17408\n"
      "ds_read_b64_tr_b16 a[100:101], v5 offset:24576\n"
      "ds_read_b64_tr_b16 a[102:103], v5 offset:25600\n"
      "ds_read_b64_tr_b16 a[172:173], v5 offset:16392\n"
      "ds_read_b64_tr_b16 a[174:175], v5 offset:17416\n"
      "ds_read_b64_tr_b16 a[244:245], v5 offset:24584\n"
      "ds_read_b64_tr_b16 a[246:247], v5 offset:25608\n"
      "v_mfma_f32_16x16x32_bf16 v[76:79], v[172:175], a[24:27], v[76:79]\n"
      "v_mfma_f32_16x16x32_bf16 v[80:83], v[172:175], a[96:99], v[80:83]\n"
      "v_mfma_f32_16x16x32_bf16 v[84:87], v[172:175], a[168:171], v[84:87]\n"
      "v_mfma_f32_16x16x32_bf16 v[88:91], v[172:175], a[240:243], v[88:91]\n"
      "s_add_u32 m0, s38, 65536\n"
      "s_nop 0\n"
      "buffer_load_dwordx4 v6, s[12:15], s41 offen offset:0 lds\n"
      "buffer_load_dwordx4 v7, s[12:15], s41 offen offset:1024 lds\n"
      "buffer_load_dwordx4 v8, s[12:15], s41 offen offset:2048 lds\n"
      "buffer_load_dwordx4 v9, s[12:15], s41 offen offset:3072 lds\n"
      "s_add_u32 s41, s41, 294912\n"
      "s_waitcnt vmcnt(16) lgkmcnt(0)\n"
      "s_barrier\n"
      "ds_read_b64_tr_b16 a[32:33], v5 offset:32768\n"
      "ds_read_b64_tr_b16 a[34:35], v5 offset:33792\n"
      "ds_read_b64_tr_b16 a[104:105], v5 offset:40960\n"
      "ds_read_b64_tr_b16 a[106:107], v5 offset:41984\n"
      "ds_read_b64_tr_b16 a[176:177], v5 offset:32776\n"
      "ds_read_b64_tr_b16 a[178:179], v5 offset:33800\n"
      "ds_read_b64_tr_b16 a[248:249], v5 offset:40968\n"
      "ds_read_b64_tr_b16 a[250:251], v5 offset:41992\n"
      "v_mfma_f32_16x16x32_bf16 v[76:79], v[172:175], a[28:31], v[76:79]\n"
      "v_mfma_f32_16x16x32_bf16 v[80:83], v[172:175], a[100:103], v[80:83]\n"
      "v_mfma_f32_16x16x32_bf16 v[84:87], v[172:175], a[172:175], v[84:87]\n"
      "v_mfma_f32_16x16x32_bf16 v[88:91], v[172:175], a[244:247], v[88:91]\n"
      "s_add_u32 m0, s38, 81920\n"
      "s_nop 0\n"
      "buffer_load_dwordx4 v6, s[12:15], s41 offen offset:0 lds\n"
      "buffer_load_dwordx4 v7, s[12:15], s41 offen offset:1024 lds\n"
      "buffer_load_dwordx4 v8, s[12:15], s41 offen offset:2048 lds\n"
      "buffer_load_dwordx4 v9, s[12:15], s41 offen offset:3072 lds\n"
      "s_add_u32 s41, s41, 294912\n"
      "s_waitcnt vmcnt(16) lgkmcnt(0)\n"
      "s_barrier\n"
      "ds_read_b64_tr_b16 a[36:37], v19 offset:0\n"
      "ds_read_b64_tr_b16 a[38:39], v19 offset:1024\n"
      "ds_read_b64_tr_b16 a[108:109], v19 offset:8192\n"
      "ds_read_b64_tr_b16 a[110:111], v19 offset:9216\n"
      "ds_read_b64_tr_b16 a[180:181], v19 offset:8\n"
      "ds_read_b64_tr_b16 a[182:183], v19 offset:1032\n"
      "ds_read_b64_tr_b16 a[252:253], v19 offset:8200\n"
      "ds_read_b64_tr_b16 a[254:255], v19 offset:9224\n"
      "v_mfma_f32_16x16x32_bf16 v[76:79], v[172:175], a[32:35], v[76:79]\n"
      "v_mfma_f32_16x16x32_bf16 v[80:83], v[172:175], a[104:107], v[80:83]\n"
      "v_mfma_f32_16x16x32_bf16 v[84:87], v[172:175], a[176:179], v[84:87]\n"
      "v_mfma_f32_16x16x32_bf16 v[88:91], v[172:175], a[248:251], v[88:91]\n"
      "s_add_u32 m0, s38, 98304\n"
      "s_nop 0\n"
      "buffer_load_dwordx4 v6, s[12:15], s41 offen offset:0 lds\n"
      "buffer_load_dwordx4 v7, s[12:15], s41 offen offset:1024 lds\n"
      "buffer_load_dwordx4 v8, s[12:15], s41 offen offset:2048 lds\n"
      "buffer_load_dwordx4 v9, s[12:15], s41 offen offset:3072 lds\n"
      "s_add_u32 s41, s41, 294912\n"
      "s_waitcnt vmcnt(16) lgkmcnt(0)\n"
      "s_barrier\n"
      "ds_read_b64_tr_b16 a[40:41], v19 offset:16384\n"
      "ds_read_b64_tr_b16 a[42:43], v19 offset:17408\n"
      "ds_read_b64_tr_b16 a[112:113], v19 offset:24576\n"
      "ds_read_b64_tr_b16 a[114:115], v19 offset:25600\n"
      "ds_read_b64_tr_b16 a[184:185], v19 offset:16392\n"
      "ds_read_b64_tr_b16 a[186:187], v19 offset:17416\n"
      "ds_read_b64_tr_b16 v[20:21], v19 offset:24584\n"
      "ds_read_b64_tr_b16 v[22:23], v19 offset:25608\n"
      "v_mfma_f32_16x16x32_bf16 v[76:79], v[172:175], a[36:39], v[76:79]\n"
      "v_mfma_f32_16x16x32_bf16 v[80:83], v[172:175], a[108:111], v[80:83]\n"
      "v_mfma_f32_16x16x32_bf16 v[84:87], v[172:175], a[180:183], v[84:87]\n"
      "v_mfma_f32_16x16x32_bf16 v[88:91], v[172:175], a[252:255], v[88:91]\n"
      "s_add_u32 m0, s38, 114688\n"
      "s_nop 0\n"
      "buffer_load_dwordx4 v6, s[12:15], s41 offen offset:0 lds\n"
      "buffer_load_dwordx4 v7, s[12:15], s41 offen offset:1024 lds\n"
      "buffer_load_dwordx4 v8, s[12:15], s41 offen offset:2048 lds\n"
      "buffer_load_dwordx4 v9, s[12:15], s41 offen offset:3072 lds\n"
      "s_add_u32 s41, s41, 294912\n"
      "s_waitcnt vmcnt(16) lgkmcnt(0)\n"
      "s_barrier\n"
      "ds_read_b64_tr_b16 a[44:45], v19 offset:32768\n"
      "ds_read_b64_tr_b16 a[46:47], v19 offset:33792\n"
      "ds_read_b64_tr_b16 a[116:117], v19 offset:40960\n"
      "ds_read_b64_tr_b16 a[118:119], v19 offset:41984\n"
      "ds_read_b64_tr_b16 a[188:189], v19 offset:32776\n"
      "ds_read_b64_tr_b16 a[190:191], v19 offset:33800\n"
      "ds_read_b64_tr_b16 v[24:25], v19 offset:40968\n"
      "ds_read_b64_tr_b16 v[26:27], v19 offset:41992\n"
      "v_mfma_f32_16x16x32_bf16 v[92:95], v[172:175], a[40:43], v[76:79]\n"
      "v_mfma_f32_16x16x32_bf16 v[96:99], v[172:175], a[112:115], v[80:83]\n"
      "v_mfma_f32_16x16x32_bf16 v[100:103], v[172:175], a[184:187], v[84:87]\n"
      "v_mfma_f32_16x16x32_bf16 v[104:107], v[172:175], v[20:23], v[88:91]\n"
      "s_add_u32 m0, s38, 131072\n"
      "s_nop 0\n"
      "buffer_load_dwordx4 v6, s[12:15], s41 offen offset:0 lds\n"
      "buffer_load_dwordx4 v7, s[12:15], s41 offen offset:1024 lds\n"
      "buffer_load_dwordx4 v8, s[12:15], s41 offen offset:2048 lds\n"
      "buffer_load_dwordx4 v9, s[12:15], s41 offen offset:3072 lds\n"
      "s_add_u32 s41, s41, 294912\n"
      "s_waitcnt vmcnt(16) lgkmcnt(0)\n"
      "s_barrier\n"
      "ds_read_b64_tr_b16 a[48:49], v5 offset:0\n"
      "ds_read_b64_tr_b16 a[50:51], v5 offset:1024\n"
      "ds_read_b64_tr_b16 a[120:121], v5 offset:8192\n"
      "ds_read_b64_tr_b16 a[122:123], v5 offset:9216\n"
      "ds_read_b64_tr_b16 a[192:193], v5 offset:8\n"
      "ds_read_b64_tr_b16 a[194:195], v5 offset:1032\n"
      "ds_read_b64_tr_b16 v[28:29], v5 offset:8200\n"
      "ds_read_b64_tr_b16 v[30:31], v5 offset:9224\n"
      "v_mfma_f32_16x16x32_bf16 v[92:95], v[172:175], a[44:47], v[92:95]\n"
      "v_mfma_f32_16x16x32_bf16 v[96:99], v[172:175], a[116:119], v[96:99]\n"
      "v_mfma_f32_16x16x32_bf16 v[100:103], v[172:175], a[188:191], v[100:103]\n"
      "v_mfma_f32_16x16x32_bf16 v[104:107], v[172:175], v[24:27], v[104:107]\n"
      "s_add_u32 m0, s38, 147456\n"
      "s_nop 0\n"
      "buffer_load_dwordx4 v6, s[12:15], s41 offen offset:0 lds\n"
      "buffer_load_dwordx4 v7, s[12:15], s41 offen offset:1024 lds\n"
      "buffer_load_dwordx4 v8, s[12:15], s41 offen offset:2048 lds\n"
      "buffer_load_dwordx4 v9, s[12:15], s41 offen offset:3072 lds\n"
      "s_add_u32 s41, s41, 294912\n"
      "s_waitcnt vmcnt(16) lgkmcnt(0)\n"
      "s_barrier\n"
      "ds_read_b64_tr_b16 a[52:53], v5 offset:16384\n"
      "ds_read_b64_tr_b16 a[54:55], v5 offset:17408\n"
      "ds_read_b64_tr_b16 a[124:125], v5 offset:24576\n"
      "ds_read_b64_tr_b16 a[126:127], v5 offset:25600\n"
      "ds_read_b64_tr_b16 a[196:197], v5 offset:16392\n"
      "ds_read_b64_tr_b16 a[198:199], v5 offset:17416\n"
      "ds_read_b64_tr_b16 v[32:33], v5 offset:24584\n"
      "ds_read_b64_tr_b16 v[34:35], v5 offset:25608\n"
      "v_mfma_f32_16x16x32_bf16 v[92:95], v[172:175], a[48:51], v[92:95]\n"
      "v_mfma_f32_16x16x32_bf16 v[96:99], v[172:175], a[120:123], v[96:99]\n"
      "v_mfma_f32_16x16x32_bf16 v[100:103], v[172:175], a[192:195], v[100:103]\n"
      "v_mfma_f32_16x16x32_bf16 v[104:107], v[172:175], v[28:31], v[104:107]\n"
      "s_waitcnt vmcnt(12) lgkmcnt(0)\n"
      "s_barrier\n"
      "ds_read_b64_tr_b16 a[56:57], v5 offset:32768\n"
      "ds_read_b64_tr_b16 a[58:59], v5 offset:33792\n"
      "ds_read_b64_tr_b16 a[128:129], v5 offset:40960\n"
      "ds_read_b64_tr_b16 a[130:131], v5 offset:41984\n"
      "ds_read_b64_tr_b16 a[200:201], v5 offset:32776\n"
      "ds_read_b64_tr_b16 a[202:203], v5 offset:33800\n"
      "ds_read_b64_tr_b16 v[36:37], v5 offset:40968\n"
      "ds_read_b64_tr_b16 v[38:39], v5 offset:41992\n"
      "v_mfma_f32_16x16x32_bf16 v[92:95], v[172:175], a[52:55], v[92:95]\n"
      "v_mfma_f32_16x16x32_bf16 v[96:99], v[172:175], a[124:127], v[96:99]\n"
      "v_mfma_f32_16x16x32_bf16 v[100:103], v[172:175], a[196:199], v[100:103]\n"
      "v_mfma_f32_16x16x32_bf16 v[104:107], v[172:175], v[32:35], v[104:107]\n"
      "s_waitcnt vmcnt(8) lgkmcnt(0)\n"
      "s_barrier\n"
      "ds_read_b64_tr_b16 a[60:61], v19 offset:0\n"
      "ds_read_b64_tr_b16 a[62:63], v19 offset:1024\n"
      "ds_read_b64_tr_b16 a[132:133], v19 offset:8192\n"
      "ds_read_b64_tr_b16 a[134:135], v19 offset:9216\n"
      "ds_read_b64_tr_b16 a[204:205], v19 offset:8\n"
      "ds_read_b64_tr_b16 a[206:207], v19 offset:1032\n"
      "ds_read_b64_tr_b16 v[40:41], v19 offset:8200\n"
      "ds_read_b64_tr_b16 v[42:43], v19 offset:9224\n"
      "v_mfma_f32_16x16x32_bf16 v[92:95], v[172:175], a[56:59], v[92:95]\n"
      "v_mfma_f32_16x16x32_bf16 v[96:99], v[172:175], a[128:131], v[96:99]\n"
      "v_mfma_f32_16x16x32_bf16 v[100:103], v[172:175], a[200:203], v[100:103]\n"
      "v_mfma_f32_16x16x32_bf16 v[104:107], v[172:175], v[36:39], v[104:107]\n"
      "s_waitcnt vmcnt(4) lgkmcnt(0)\n"
      "s_barrier\n"
      "ds_read_b64_tr_b16 a[64:65], v19 offset:16384\n"
      "ds_read_b64_tr_b16 a[66:67], v19 offset:17408\n"
      "ds_read_b64_tr_b16 a[136:137], v19 offset:24576\n"
      "ds_read_b64_tr_b16 a[138:139], v19 offset:25600\n"
      "ds_read_b64_tr_b16 a[208:209], v19 offset:16392\n"
      "ds_read_b64_tr_b16 a[210:211], v19 offset:17416\n"
      "ds_read_b64_tr_b16 v[44:45], v19 offset:24584\n"
      "ds_read_b64_tr_b16 v[46:47], v19 offset:25608\n"
      "v_mfma_f32_16x16x32_bf16 v[108:111], v[172:175], a[60:63], v[92:95]\n"
      "v_mfma_f32_16x16x32_bf16 v[112:115], v[172:175], a[132:135], v[96:99]\n"
      "v_mfma_f32_16x16x32_bf16 v[116:119], v[172:175], a[204:207], v[100:103]\n"
      "v_mfma_f32_16x16x32_bf16 v[120:123], v[172:175], v[40:43], v[104:107]\n"
      "s_waitcnt vmcnt(0) lgkmcnt(0)\n"
      "s_barrier\n"
      "ds_read_b64_tr_b16 a[68:69], v19 offset:32768\n"
      "ds_read_b64_tr_b16 a[70:71], v19 offset:33792\n"
      "ds_read_b64_tr_b16 a[140:141], v19 offset:40960\n"
      "ds_read_b64_tr_b16 a[142:143], v19 offset:41984\n"
      "ds_read_b64_tr_b16 a[212:213], v19 offset:32776\n"
      "ds_read_b64_tr_b16 a[214:215], v19 offset:33800\n"
      "ds_read_b64_tr_b16 v[48:49], v19 offset:40968\n"
      "ds_read_b64_tr_b16 v[50:51], v19 offset:41992\n"
      "v_mfma_f32_16x16x32_bf16 v[108:111], v[172:175], a[64:67], v[108:111]\n"
      "v_mfma_f32_16x16x32_bf16 v[112:115], v[172:175], a[136:139], v[112:115]\n"
      "v_mfma_f32_16x16x32_bf16 v[116:119], v[172:175], a[208:211], v[116:119]\n"
      "v_mfma_f32_16x16x32_bf16 v[120:123], v[172:175], v[44:47], v[120:123]\n"
      "s_waitcnt lgkmcnt(0)\n"
      "v_mfma_f32_16x16x32_bf16 v[140:143], v[172:175], a[68:71], v[108:111]\n"
      "v_mfma_f32_16x16x32_bf16 v[144:147], v[172:175], a[140:143], v[112:115]\n"
      "v_mfma_f32_16x16x32_bf16 v[148:151], v[172:175], a[212:215], v[116:119]\n"
      "v_mfma_f32_16x16x32_bf16 v[152:155], v[172:175], v[48:51], v[120:123]\n"
      "s_getpc_b64 vcc\n"
      ".Lg_ct:\n"
      "s_add_u32 vcc_lo, vcc_lo, .Lg_p0-.Lg_ct\n"
      "s_addc_u32 vcc_hi, vcc_hi, 0\n"
      "s_lshl_b32 s30, s38, 1\n"
      "v_lshl_add_u32 v60, v0, 3, s30\n"
      "v_min_u32 v60, .Lg_last-.Lg_p0-4, v60\n"
      "global_load_dword v60, v60, vcc\n"
      "s_mul_i32 s21, s21, 1664525\n"
      "s_add_u32 s21, s21, 1013904223\n"
      "v_mov_b32 v15, s21\n"
      "buffer_store_dword v15, v12, s[8:11], 0 offen sc1\n"
      "v_not_b32 v52, v15\n"
      "v_not_b32 v53, v15\n"
      "v_not_b32 v54, v15\n"
      "v_not_b32 v55, v15\n"
      "s_add_u32 s30, s37, 0\n"
      "buffer_store_dwordx4 v[52:55], v10, s[8:11], s30 offen sc1\n"
      "v_xor_b32 v15, v15, v16\n"
      "v_mov_b32 v66, 0\n"
      "v_mov_b32 v67, 0\n"
      "v_mov_b32 v68, 0\n"
      "v_mov_b32 v69, 0\n"
      "v_mov_b32 v70, v64\n"
      "v_mov_b32 v71, v65\n"
      "v_sub_f32 v156, v140, v76\n"
      "v_sub_f32 v157, v144, v80\n"
      "v_sub_f32 v158, v148, v84\n"
      "v_sub_f32 v159, v152, v88\n"
      "v_sub_f32 v160, v140, v92\n"
      "v_sub_f32 v161, v144, v96\n"
      "v_sub_f32 v162, v148, v100\n"
      "v_sub_f32 v163, v152, v104\n"
      "v_sub_f32 v164, v140, v108\n"
      "v_sub_f32 v165, v144, v112\n"
      "v_sub_f32 v166, v148, v116\n"
      "v_sub_f32 v167, v152, v120\n"
      "v_mul_f32 v140, 0xbae38e39, v140\n"
      "v_mul_f32 v141, 0xbae38e39, v141\n"
      "v_mul_f32 v142, 0xbae38e39, v142\n"
      "v_mul_f32 v143, 0xbae38e39, v143\n"
      "v_mul_f32 v144, 0xbae38e39, v144\n"
      "v_mul_f32 v145, 0xbae38e39, v145\n"
      "v_mul_f32 v146, 0xbae38e39, v146\n"
      "v_mul_f32 v147, 0xbae38e39, v147\n"
      "v_mul_f32 v148, 0xbae38e39, v148\n"
      "v_mul_f32 v149, 0xbae38e39, v149\n"
      "v_mul_f32 v150, 0xbae38e39, v150\n"
      "v_mul_f32 v151, 0xbae38e39, v151\n"
      "v_mul_f32 v152, 0xbae38e39, v152\n"
      "v_mul_f32 v153, 0xbae38e39, v153\n"
      "v_mul_f32 v154, 0xbae38e39, v154\n"
      "v_mul_f32 v155, 0xbae38e39, v155\n"
      "v_mfma_f32_16x16x32_bf16 v[72:75], v[64:67], a[0:3], v[140:143]\n"
      "v_mfma_f32_16x16x32_bf16 v[76:79], v[68:71], a[0:3], v[140:143]\n"
      "v_mfma_f32_16x16x32_bf16 v[80:83], v[64:67], a[72:75], v[144:147]\n"
      "v_mfma_f32_16x16x32_bf16 v[84:87], v[68:71], a[72:75], v[144:147]\n"
      "v_mfma_f32_16x16x32_bf16 v[88:91], v[64:67], a[144:147], v[148:151]\n"
      "s_nop 4\n"
      "v_cvt_pk_bf16_f32 v72, v72, v73\n"
      "v_cvt_pk_bf16_f32 v73, v74, v75\n"
      "v_cvt_pk_bf16_f32 v74, v76, v77\n"
      "v_mfma_f32_16x16x32_bf16 v[92:95], v[68:71], a[144:147], v[148:151]\n"
      "v_cvt_pk_bf16_f32 v75, v78, v79\n"
      "v_accvgpr_write_b32 a0, v72\n"
      "v_accvgpr_write_b32 a1, v73\n"
      "v_accvgpr_write_b32 a2, v74\n"
      "v_accvgpr_write_b32 a3, v75\n"
      "v_mfma_f32_16x16x32_bf16 v[96:99], v[64:67], a[216:219], v[152:155]\n"
      "v_cvt_pk_bf16_f32 v80, v80, v81\n"
      "v_cvt_pk_bf16_f32 v81, v82, v83\n"
      "v_cvt_pk_bf16_f32 v82, v84, v85\n"
      "v_mfma_f32_16x16x32_bf16 v[100:103], v[68:71], a[216:219], v[152:155]\n"
      "v_cvt_pk_bf16_f32 v83, v86, v87\n"
      "v_accvgpr_write_b32 a72, v80\n"
      "v_accvgpr_write_b32 a73, v81\n"
      "v_mfma_f32_16x16x32_bf16 v[124:127], v[172:175], a[0:3], 0\n"
      "v_accvgpr_write_b32 a74, v82\n"
      "v_accvgpr_write_b32 a75, v83\n"
      "v_mfma_f32_16x16x32_bf16 v[72:75], v[64:67], a[4:7], v[140:143]\n"
      "v_cvt_pk_bf16_f32 v88, v88, v89\n"
      "v_cvt_pk_bf16_f32 v89, v90, v91\n"
      "v_cvt_pk_bf16_f32 v90, v92, v93\n"
      "v_mfma_f32_16x16x32_bf16 v[76:79], v[68:71], a[4:7], v[140:143]\n"
      "v_cvt_pk_bf16_f32 v91, v94, v95\n"
      "v_accvgpr_write_b32 a144, v88\n"
      "v_accvgpr_write_b32 a145, v89\n"
      "v_mfma_f32_16x16x32_bf16 v[128:131], v[172:175], a[72:75], 0\n"
      "v_accvgpr_write_b32 a146, v90\n"
      "v_accvgpr_write_b32 a147, v91\n"
      "v_mfma_f32_16x16x32_bf16 v[80:83], v[64:67], a[76:79], v[144:147]\n"
      "v_cvt_pk_bf16_f32 v96, v96, v97\n"
      "v_cvt_pk_bf16_f32 v97, v98, v99\n"
      "v_cvt_pk_bf16_f32 v98, v100, v101\n"
      "v_mfma_f32_16x16x32_bf16 v[84:87], v[68:71], a[76:79], v[144:147]\n"
      "v_cvt_pk_bf16_f32 v99, v102, v103\n"
      "v_accvgpr_write_b32 a216, v96\n"
      "v_accvgpr_write_b32 a217, v97\n"
      "v_mfma_f32_16x16x32_bf16 v[132:135], v[172:175], a[144:147], 0\n"
      "v_accvgpr_write_b32 a218, v98\n"
      "v_accvgpr_write_b32 a219, v99\n"
      "v_mfma_f32_16x16x32_bf16 v[88:91], v[64:67], a[148:151], v[148:151]\n"
      "v_cvt_pk_bf16_f32 v72, v72, v73\n"
      "v_cvt_pk_bf16_f32 v73, v74, v75\n"
      "v_cvt_pk_bf16_f32 v74, v76, v77\n"
      "v_mfma_f32_16x16x32_bf16 v[92:95], v[68:71], a[148:151], v[148:151]\n"
      "v_cvt_pk_bf16_f32 v75, v78, v79\n"
      "v_accvgpr_write_b32 a4, v72\n"
      "v_accvgpr_write_b32 a5, v73\n"
      "v_mfma_f32_16x16x32_bf16 v[136:139], v[172:175], a[216:219], 0\n"
      "v_accvgpr_write_b32 a6, v74\n"
      "v_accvgpr_write_b32 a7, v75\n"
      "v_mfma_f32_16x16x32_bf16 v[96:99], v[64:67], a[220:223], v[152:155]\n"
      "v_cvt_pk_bf16_f32 v80, v80, v81\n"
      "v_cvt_pk_bf16_f32 v81, v82, v83\n"
      "v_cvt_pk_bf16_f32 v82, v84, v85\n"
      "v_mfma_f32_16x16x32_bf16 v[100:103], v[68:71], a[220:223], v[152:155]\n"
      "v_cvt_pk_bf16_f32 v83, v86, v87\n"
      "v_accvgpr_write_b32 a76, v80\n"
      "v_accvgpr_write_b32 a77, v81\n"
      "v_mfma_f32_16x16x32_bf16 v[124:127], v[172:175], a[4:7], v[124:127]\n"
      "v_accvgpr_write_b32 a78, v82\n"
      "v_accvgpr_write_b32 a79, v83\n"
      "v_mfma_f32_16x16x32_bf16 v[72:75], v[64:67], a[8:11], v[140:143]\n"
      "v_cvt_pk_bf16_f32 v88, v88, v89\n"
      "v_cvt_pk_bf16_f32 v89, v90, v91\n"
      "v_cvt_pk_bf16_f32 v90, v92, v93\n"
      "v_mfma_f32_16x16x32_bf16 v[76:79], v[68:71], a[8:11], v[140:143]\n"
      "v_cvt_pk_bf16_f32 v91, v94, v95\n"
      "v_accvgpr_write_b32 a148, v88\n"
      "v_accvgpr_write_b32 a149, v89\n"
      "v_mfma_f32_16x16x32_bf16 v[128:131], v[172:175], a[76:79], v[128:131]\n"
      "v_accvgpr_write_b32 a150, v90\n"
      "v_accvgpr_write_b32 a151, v91\n"
      "v_mfma_f32_16x16x32_bf16 v[80:83], v[64:67], a[80:83], v[144:147]\n"
      "v_cvt_pk_bf16_f32 v96, v96, v97\n"
      "v_cvt_pk_bf16_f32 v97, v98, v99\n"
      "v_cvt_pk_bf16_f32 v98, v100, v101\n"
      "v_mfma_f32_16x16x32_bf16 v[84:87], v[68:71], a[80:83], v[144:147]\n"
      "v_cvt_pk_bf16_f32 v99, v102, v103\n"
      "v_accvgpr_write_b32 a220, v96\n"
      "v_accvgpr_write_b32 a221, v97\n"
      "v_mfma_f32_16x16x32_bf16 v[132:135], v[172:175], a[148:151], v[132:135]\n"
      "v_accvgpr_write_b32 a222, v98\n"
      "v_accvgpr_write_b32 a223, v99\n"
      "v_mfma_f32_16x16x32_bf16 v[88:91], v[64:67], a[152:155], v[148:151]\n"
      "v_cvt_pk_bf16_f32 v72, v72, v73\n"
      "v_cvt_pk_bf16_f32 v73, v74, v75\n"
      "v_cvt_pk_bf16_f32 v74, v76, v77\n"
      "v_mfma_f32_16x16x32_bf16 v[92:95], v[68:71], a[152:155], v[148:151]\n"
      "v_cvt_pk_bf16_f32 v75, v78, v79\n"
      "v_accvgpr_write_b32 a8, v72\n"
      "v_accvgpr_write_b32 a9, v73\n"
      "v_mfma_f32_16x16x32_bf16 v[136:139], v[172:175], a[220:223], v[136:139]\n"
      "v_accvgpr_write_b32 a10, v74\n"
      "v_accvgpr_write_b32 a11, v75\n"
      "v_mfma_f32_16x16x32_bf16 v[96:99], v[64:67], a[224:227], v[152:155]\n"
      "v_cvt_pk_bf16_f32 v80, v80, v81\n"
      "v_cvt_pk_bf16_f32 v81, v82, v83\n"
      "v_cvt_pk_bf16_f32 v82, v84, v85\n"
      "v_mfma_f32_16x16x32_bf16 v[100:103], v[68:71], a[224:227], v[152:155]\n"
      "v_cvt_pk_bf16_f32 v83, v86, v87\n"
      "v_accvgpr_write_b32 a80, v80\n"
      "v_accvgpr_write_b32 a81, v81\n"
      "v_mfma_f32_16x16x32_bf16 v[124:127], v[172:175], a[8:11], v[124:127]\n"
      "v_accvgpr_write_b32 a82, v82\n"
      "v_accvgpr_write_b32 a83, v83\n"
      "v_mfma_f32_16x16x32_bf16 v[72:75], v[64:67], a[12:15], v[140:143]\n"
      "v_cvt_pk_bf16_f32 v88, v88, v89\n"
      "v_cvt_pk_bf16_f32 v89, v90, v91\n"
      "v_cvt_pk_bf16_f32 v90, v92, v93\n"
      "v_mfma_f32_16x16x32_bf16 v[76:79], v[68:71], a[12:15], v[140:143]\n"
      "v_cvt_pk_bf16_f32 v91, v94, v95\n"
      "v_accvgpr_write_b32 a152, v88\n"
      "v_accvgpr_write_b32 a153, v89\n"
      "v_mfma_f32_16x16x32_bf16 v[128:131], v[172:175], a[80:83], v[128:131]\n"
      "v_accvgpr_write_b32 a154, v90\n"
      "v_accvgpr_write_b32 a155, v91\n"
      "v_mfma_f32_16x16x32_bf16 v[80:83], v[64:67], a[84:87], v[144:147]\n"
      "v_cvt_pk_bf16_f32 v96, v96, v97\n"
      "v_cvt_pk_bf16_f32 v97, v98, v99\n"
      "v_cvt_pk_bf16_f32 v98, v100, v101\n"
      "v_mfma_f32_16x16x32_bf16 v[84:87], v[68:71], a[84:87], v[144:147]\n"
      "v_cvt_pk_bf16_f32 v99, v102, v103\n"
      "v_accvgpr_write_b32 a224, v96\n"
      "v_accvgpr_write_b32 a225, v97\n"
      "v_mfma_f32_16x16x32_bf16 v[132:135], v[172:175], a[152:155], v[132:135]\n"
      "v_accvgpr_write_b32 a226, v98\n"
      "v_accvgpr_write_b32 a227, v99\n"
      "v_mfma_f32_16x16x32_bf16 v[88:91], v[64:67], a[156:159], v[148:151]\n"
      "v_cvt_pk_bf16_f32 v72, v72, v73\n"
      "v_cvt_pk_bf16_f32 v73, v74, v75\n"
      "v_cvt_pk_bf16_f32 v74, v76, v77\n"
      "v_mfma_f32_16x16x32_bf16 v[92:95], v[68:71], a[156:159], v[148:151]\n"
      "v_cvt_pk_bf16_f32 v75, v78, v79\n"
      "v_accvgpr_write_b32 a12, v72\n"
      "v_accvgpr_write_b32 a13, v73\n"
      "v_mfma_f32_16x16x32_bf16 v[136:139], v[172:175], a[224:227], v[136:139]\n"
      "v_accvgpr_write_b32 a14, v74\n"
      "v_accvgpr_write_b32 a15, v75\n"
      "v_mfma_f32_16x16x32_bf16 v[96:99], v[64:67], a[228:231], v[152:155]\n"
      "v_cvt_pk_bf16_f32 v80, v80, v81\n"
      "v_cvt_pk_bf16_f32 v81, v82, v83\n"
      "v_cvt_pk_bf16_f32 v82, v84, v85\n"
      "v_mfma_f32_16x16x32_bf16 v[100:103], v[68:71], a[228:231], v[152:155]\n"
      "v_cvt_pk_bf16_f32 v83, v86, v87\n"
      "v_accvgpr_write_b32 a84, v80\n"
      "v_accvgpr_write_b32 a85, v81\n"
      "v_mfma_f32_16x16x32_bf16 v[124:127], v[172:175], a[12:15], v[124:127]\n"
      "v_accvgpr_write_b32 a86, v82\n"
      "v_accvgpr_write_b32 a87, v83\n"
      "v_mfma_f32_16x16x32_bf16 v[72:75], v[64:67], a[16:19], v[140:143]\n"
      "v_cvt_pk_bf16_f32 v88, v88, v89\n"
      "v_cvt_pk_bf16_f32 v89, v90, v91\n"
      "v_cvt_pk_bf16_f32 v90, v92, v93\n"
      "v_mfma_f32_16x16x32_bf16 v[76:79], v[68:71], a[16:19], v[140:143]\n"
      "v_cvt_pk_bf16_f32 v91, v94, v95\n"
      "v_accvgpr_write_b32 a156, v88\n"
      "v_accvgpr_write_b32 a157, v89\n"
      "v_mfma_f32_16x16x32_bf16 v[128:131], v[172:175], a[84:87], v[128:131]\n"
      "v_accvgpr_write_b32 a158, v90\n"
      "v_accvgpr_write_b32 a159, v91\n"
      "v_mfma_f32_16x16x32_bf16 v[80:83], v[64:67], a[88:91], v[144:147]\n"
      "v_cvt_pk_bf16_f32 v96, v96, v97\n"
      "v_cvt_pk_bf16_f32 v97, v98, v99\n"
      "v_cvt_pk_bf16_f32 v98, v100, v101\n"
      "v_mfma_f32_16x16x32_bf16 v[84:87], v[68:71], a[88:91], v[144:147]\n"
      "v_cvt_pk_bf16_f32 v99, v102, v103\n"
      "v_accvgpr_write_b32 a228, v96\n"
      "v_accvgpr_write_b32 a229, v97\n"
      "v_mfma_f32_16x16x32_bf16 v[132:135], v[172:175], a[156:159], v[132:135]\n"
      "v_accvgpr_write_b32 a230, v98\n"
      "v_accvgpr_write_b32 a231, v99\n"
      "v_mfma_f32_16x16x32_bf16 v[88:91], v[64:67], a[160:163], v[148:151]\n"
      "v_cvt_pk_bf16_f32 v72, v72, v73\n"
      "v_cvt_pk_bf16_f32 v73, v74, v75\n"
      "v_cvt_pk_bf16_f32 v74, v76, v77\n"
      "v_mfma_f32_16x16x32_bf16 v[92:95], v[68:71], a[160:163], v[148:151]\n"
      "v_cvt_pk_bf16_f32 v75, v78, v79\n"
      "v_accvgpr_write_b32 a16, v72\n"
      "v_accvgpr_write_b32 a17, v73\n"
      "v_mfma_f32_16x16x32_bf16 v[136:139], v[172:175], a[228:231], v[136:139]\n"
      "v_accvgpr_write_b32 a18, v74\n"
      "v_accvgpr_write_b32 a19, v75\n"
      "v_mfma_f32_16x16x32_bf16 v[96:99], v[64:67], a[232:235], v[152:155]\n"
      "v_cvt_pk_bf16_f32 v80, v80, v81\n"
      "v_cvt_pk_bf16_f32 v81, v82, v83\n"
      "v_cvt_pk_bf16_f32 v82, v84, v85\n"
      "v_mfma_f32_16x16x32_bf16 v[100:103], v[68:71], a[232:235], v[152:155]\n"
      "v_cvt_pk_bf16_f32 v83, v86, v87\n"
      "v_accvgpr_write_b32 a88, v80\n"
      "v_accvgpr_write_b32 a89, v81\n"
      "v_mfma_f32_16x16x32_bf16 v[124:127], v[172:175], a[16:19], v[124:127]\n"
      "v_accvgpr_write_b32 a90, v82\n"
      "v_accvgpr_write_b32 a91, v83\n"
      "v_mfma_f32_16x16x32_bf16 v[72:75], v[64:67], a[20:23], v[140:143]\n"
      "v_cvt_pk_bf16_f32 v88, v88, v89\n"
      "v_cvt_pk_bf16_f32 v89, v90, v91\n"
      "v_cvt_pk_bf16_f32 v90, v92, v93\n"
      "v_mfma_f32_16x16x32_bf16 v[76:79], v[68:71], a[20:23], v[140:143]\n"
      "v_cvt_pk_bf16_f32 v91, v94, v95\n"
      "v_accvgpr_write_b32 a160, v88\n"
      "v_accvgpr_write_b32 a161, v89\n"
      "v_mfma_f32_16x16x32_bf16 v[128:131], v[172:175], a[88:91], v[128:131]\n"
      "v_accvgpr_write_b32 a162, v90\n"
      "v_accvgpr_write_b32 a163, v91\n"
      "v_mfma_f32_16x16x32_bf16 v[80:83], v[64:67], a[92:95], v[144:147]\n"
      "v_cvt_pk_bf16_f32 v96, v96, v97\n"
      "v_cvt_pk_bf16_f32 v97, v98, v99\n"
      "v_cvt_pk_bf16_f32 v98, v100, v101\n"
      "v_mfma_f32_16x16x32_bf16 v[84:87], v[68:71], a[92:95], v[144:147]\n"
      "v_cvt_pk_bf16_f32 v99, v102, v103\n"
      "v_accvgpr_write_b32 a232, v96\n"
      "v_accvgpr_write_b32 a233, v97\n"
      "v_mfma_f32_16x16x32_bf16 v[132:135], v[172:175], a[160:163], v[132:135]\n"
      "v_accvgpr_write_b32 a234, v98\n"
      "v_accvgpr_write_b32 a235, v99\n"
      "v_mfma_f32_16x16x32_bf16 v[88:91], v[64:67], a[164:167], v[148:151]\n"
      "v_cvt_pk_bf16_f32 v72, v72, v73\n"
      "v_cvt_pk_bf16_f32 v73, v74, v75\n"
      "v_cvt_pk_bf16_f32 v74, v76, v77\n"
      "v_mfma_f32_16x16x32_bf16 v[92:95], v[68:71], a[164:167], v[148:151]\n"
      "v_cvt_pk_bf16_f32 v75, v78, v79\n"
      "v_accvgpr_write_b32 a20, v72\n"
      "v_accvgpr_write_b32 a21, v73\n"
      "v_mfma_f32_16x16x32_bf16 v[136:139], v[172:175], a[232:235], v[136:139]\n"
      "v_accvgpr_write_b32 a22, v74\n"
      "v_accvgpr_write_b32 a23, v75\n"
      "v_mfma_f32_16x16x32_bf16 v[96:99], v[64:67], a[236:239], v[152:155]\n"
      "v_cvt_pk_bf16_f32 v80, v80, v81\n"
      "v_cvt_pk_bf16_f32 v81, v82, v83\n"
      "v_cvt_pk_bf16_f32 v82, v84, v85\n"
      "v_mfma_f32_16x16x32_bf16 v[100:103], v[68:71], a[236:239], v[152:155]\n"
      "v_cvt_pk_bf16_f32 v83, v86, v87\n"
      "v_accvgpr_write_b32 a92, v80\n"
      "v_accvgpr_write_b32 a93, v81\n"
      "v_mfma_f32_16x16x32_bf16 v[124:127], v[172:175], a[20:23], v[124:127]\n"
      "v_accvgpr_write_b32 a94, v82\n"
      "v_accvgpr_write_b32 a95, v83\n"
      "v_mfma_f32_16x16x32_bf16 v[72:75], v[64:67], a[24:27], v[140:143]\n"
      "v_cvt_pk_bf16_f32 v88, v88, v89\n"
      "v_cvt_pk_bf16_f32 v89, v90, v91\n"
      "v_cvt_pk_bf16_f32 v90, v92, v93\n"
      "v_mfma_f32_16x16x32_bf16 v[76:79], v[68:71], a[24:27], v[140:143]\n"
      "v_cvt_pk_bf16_f32 v91, v94, v95\n"
      "v_accvgpr_write_b32 a164, v88\n"
      "v_accvgpr_write_b32 a165, v89\n"
      "v_mfma_f32_16x16x32_bf16 v[128:131], v[172:175], a[92:95], v[128:131]\n"
      "v_accvgpr_write_b32 a166, v90\n"
      "v_accvgpr_write_b32 a167, v91\n"
      "v_mfma_f32_16x16x32_bf16 v[80:83], v[64:67], a[96:99], v[144:147]\n"
      "v_cvt_pk_bf16_f32 v96, v96, v97\n"
      "v_cvt_pk_bf16_f32 v97, v98, v99\n"
      "v_cvt_pk_bf16_f32 v98, v100, v101\n"
      "v_mfma_f32_16x16x32_bf16 v[84:87], v[68:71], a[96:99], v[144:147]\n"
      "v_cvt_pk_bf16_f32 v99, v102, v103\n"
      "v_accvgpr_write_b32 a236, v96\n"
      "v_accvgpr_write_b32 a237, v97\n"
      "v_mfma_f32_16x16x32_bf16 v[132:135], v[172:175], a[164:167], v[132:135]\n"
      "v_accvgpr_write_b32 a238, v98\n"
      "v_accvgpr_write_b32 a239, v99\n"
      "v_mfma_f32_16x16x32_bf16 v[88:91], v[64:67], a[168:171], v[148:151]\n"
      "v_cvt_pk_bf16_f32 v72, v72, v73\n"
      "v_cvt_pk_bf16_f32 v73, v74, v75\n"
      "v_cvt_pk_bf16_f32 v74, v76, v77\n"
      "v_mfma_f32_16x16x32_bf16 v[92:95], v[68:71], a[168:171], v[148:151]\n"
      "v_cvt_pk_bf16_f32 v75, v78, v79\n"
      "v_accvgpr_write_b32 a24, v72\n"
      "v_accvgpr_write_b32 a25, v73\n"
      "v_mfma_f32_16x16x32_bf16 v[136:139], v[172:175], a[236:239], v[136:139]\n"
      "v_accvgpr_write_b32 a26, v74\n"
      "v_accvgpr_write_b32 a27, v75\n"
      "v_mfma_f32_16x16x32_bf16 v[96:99], v[64:67], a[240:243], v[152:155]\n"
      "v_cvt_pk_bf16_f32 v80, v80, v81\n"
      "v_cvt_pk_bf16_f32 v81, v82, v83\n"
      "v_cvt_pk_bf16_f32 v82, v84, v85\n"
      "v_mfma_f32_16x16x32_bf16 v[100:103], v[68:71], a[240:243], v[152:155]\n"
      "v_cvt_pk_bf16_f32 v83, v86, v87\n"
      "v_accvgpr_write_b32 a96, v80\n"
      "v_accvgpr_write_b32 a97, v81\n"
      "v_mfma_f32_16x16x32_bf16 v[124:127], v[172:175], a[24:27], v[124:127]\n"
      "v_accvgpr_write_b32 a98, v82\n"
      "v_accvgpr_write_b32 a99, v83\n"
      "v_mfma_f32_16x16x32_bf16 v[72:75], v[64:67], a[28:31], v[140:143]\n"
      "v_cvt_pk_bf16_f32 v88, v88, v89\n"
      "v_cvt_pk_bf16_f32 v89, v90, v91\n"
      "v_cvt_pk_bf16_f32 v90, v92, v93\n"
      "v_mfma_f32_16x16x32_bf16 v[76:79], v[68:71], a[28:31], v[140:143]\n"
      "v_cvt_pk_bf16_f32 v91, v94, v95\n"
      "v_accvgpr_write_b32 a168, v88\n"
      "v_accvgpr_write_b32 a169, v89\n"
      "v_mfma_f32_16x16x32_bf16 v[128:131], v[172:175], a[96:99], v[128:131]\n"
      "v_accvgpr_write_b32 a170, v90\n"
      "v_accvgpr_write_b32 a171, v91\n"
      "v_mfma_f32_16x16x32_bf16 v[80:83], v[64:67], a[100:103], v[144:147]\n"
      "v_cvt_pk_bf16_f32 v96, v96, v97\n"
      "v_cvt_pk_bf16_f32 v97, v98, v99\n"
      "v_cvt_pk_bf16_f32 v98, v100, v101\n"
      "v_mfma_f32_16x16x32_bf16 v[84:87], v[68:71], a[100:103], v[144:147]\n"
      "v_cvt_pk_bf16_f32 v99, v102, v103\n"
      "v_accvgpr_write_b32 a240, v96\n"
      "v_accvgpr_write_b32 a241, v97\n"
      "v_mfma_f32_16x16x32_bf16 v[132:135], v[172:175], a[168:171], v[132:135]\n"
      "v_accvgpr_write_b32 a242, v98\n"
      "v_accvgpr_write_b32 a243, v99\n"
      "v_mfma_f32_16x16x32_bf16 v[88:91], v[64:67], a[172:175], v[148:151]\n"
      "v_cvt_pk_bf16_f32 v72, v72, v73\n"
      "v_cvt_pk_bf16_f32 v73, v74, v75\n"
      "v_cvt_pk_bf16_f32 v74, v76, v77\n"
      "v_mfma_f32_16x16x32_bf16 v[92:95], v[68:71], a[172:175], v[148:151]\n"
      "v_cvt_pk_bf16_f32 v75, v78, v79\n"
      "v_accvgpr_write_b32 a28, v72\n"
      "v_accvgpr_write_b32 a29, v73\n"
      "v_mfma_f32_16x16x32_bf16 v[136:139], v[172:175], a[240:243], v[136:139]\n"
      "v_accvgpr_write_b32 a30, v74\n"
      "v_accvgpr_write_b32 a31, v75\n"
      "v_mfma_f32_16x16x32_bf16 v[96:99], v[64:67], a[244:247], v[152:155]\n"
      "v_cvt_pk_bf16_f32 v80, v80, v81\n"
      "v_cvt_pk_bf16_f32 v81, v82, v83\n"
      "v_cvt_pk_bf16_f32 v82, v84, v85\n"
      "v_mfma_f32_16x16x32_bf16 v[100:103], v[68:71], a[244:247], v[152:155]\n"
      "v_cvt_pk_bf16_f32 v83, v86, v87\n"
      "v_accvgpr_write_b32 a100, v80\n"
      "v_accvgpr_write_b32 a101, v81\n"
      "v_mfma_f32_16x16x32_bf16 v[124:127], v[172:175], a[28:31], v[124:127]\n"
      "v_accvgpr_write_b32 a102, v82\n"
      "v_accvgpr_write_b32 a103, v83\n"
      "v_mfma_f32_16x16x32_bf16 v[72:75], v[64:67], a[32:35], v[140:143]\n"
      "v_cvt_pk_bf16_f32 v88, v88, v89\n"
      "v_cvt_pk_bf16_f32 v89, v90, v91\n"
      "v_cvt_pk_bf16_f32 v90, v92, v93\n"
      "v_mfma_f32_16x16x32_bf16 v[76:79], v[68:71], a[32:35], v[140:143]\n"
      "v_cvt_pk_bf16_f32 v91, v94, v95\n"
      "v_accvgpr_write_b32 a172, v88\n"
      "v_accvgpr_write_b32 a173, v89\n"
      "v_mfma_f32_16x16x32_bf16 v[128:131], v[172:175], a[100:103], v[128:131]\n"
      "v_accvgpr_write_b32 a174, v90\n"
      "v_accvgpr_write_b32 a175, v91\n"
      "v_mfma_f32_16x16x32_bf16 v[80:83], v[64:67], a[104:107], v[144:147]\n"
      "v_cvt_pk_bf16_f32 v96, v96, v97\n"
      "v_cvt_pk_bf16_f32 v97, v98, v99\n"
      "v_cvt_pk_bf16_f32 v98, v100, v101\n"
      "v_mfma_f32_16x16x32_bf16 v[84:87], v[68:71], a[104:107], v[144:147]\n"
      "v_cvt_pk_bf16_f32 v99, v102, v103\n"
      "v_accvgpr_write_b32 a244, v96\n"
      "v_accvgpr_write_b32 a245, v97\n"
      "v_mfma_f32_16x16x32_bf16 v[132:135], v[172:175], a[172:175], v[132:135]\n"
      "v_accvgpr_write_b32 a246, v98\n"
      "v_accvgpr_write_b32 a247, v99\n"
      "v_mfma_f32_16x16x32_bf16 v[88:91], v[64:67], a[176:179], v[148:151]\n"
      "v_cvt_pk_bf16_f32 v72, v72, v73\n"
      "v_cvt_pk_bf16_f32 v73, v74, v75\n"
      "v_cvt_pk_bf16_f32 v74, v76, v77\n"
      "v_mfma_f32_16x16x32_bf16 v[92:95], v[68:71], a[176:179], v[148:151]\n"
      "v_cvt_pk_bf16_f32 v75, v78, v79\n"
      "v_accvgpr_write_b32 a32, v72\n"
      "v_accvgpr_write_b32 a33, v73\n"
      "v_mfma_f32_16x16x32_bf16 v[136:139], v[172:175], a[244:247], v[136:139]\n"
      "v_accvgpr_write_b32 a34, v74\n"
      "v_accvgpr_write_b32 a35, v75\n"
      "v_mfma_f32_16x16x32_bf16 v[96:99], v[64:67], a[248:251], v[152:155]\n"
      "v_cvt_pk_bf16_f32 v80, v80, v81\n"
      "v_cvt_pk_bf16_f32 v81, v82, v83\n"
      "v_cvt_pk_bf16_f32 v82, v84, v85\n"
      "v_mfma_f32_16x16x32_bf16 v[100:103], v[68:71], a[248:251], v[152:155]\n"
      "v_cvt_pk_bf16_f32 v83, v86, v87\n"
      "v_accvgpr_write_b32 a104, v80\n"
      "v_accvgpr_write_b32 a105, v81\n"
      "v_mfma_f32_16x16x32_bf16 v[124:127], v[172:175], a[32:35], v[124:127]\n"
      "v_accvgpr_write_b32 a106, v82\n"
      "v_accvgpr_write_b32 a107, v83\n"
      "v_mfma_f32_16x16x32_bf16 v[72:75], v[64:67], a[36:39], v[140:143]\n"
      "v_cvt_pk_bf16_f32 v88, v88, v89\n"
      "v_cvt_pk_bf16_f32 v89, v90, v91\n"
      "v_cvt_pk_bf16_f32 v90, v92, v93\n"
      "v_mfma_f32_16x16x32_bf16 v[76:79], v[68:71], a[36:39], v[140:143]\n"
      "v_cvt_pk_bf16_f32 v91, v94, v95\n"
      "v_accvgpr_write_b32 a176, v88\n"
      "v_accvgpr_write_b32 a177, v89\n"
      "v_mfma_f32_16x16x32_bf16 v[128:131], v[172:175], a[104:107], v[128:131]\n"
      "v_accvgpr_write_b32 a178, v90\n"
      "v_accvgpr_write_b32 a179, v91\n"
      "v_mfma_f32_16x16x32_bf16 v[80:83], v[64:67], a[108:111], v[144:147]\n"
      "v_cvt_pk_bf16_f32 v96, v96, v97\n"
      "v_cvt_pk_bf16_f32 v97, v98, v99\n"
      "v_cvt_pk_bf16_f32 v98, v100, v101\n"
      "v_mfma_f32_16x16x32_bf16 v[84:87], v[68:71], a[108:111], v[144:147]\n"
      "v_cvt_pk_bf16_f32 v99, v102, v103\n"
      "v_accvgpr_write_b32 a248, v96\n"
      "v_accvgpr_write_b32 a249, v97\n"
      "v_mfma_f32_16x16x32_bf16 v[132:135], v[172:175], a[176:179], v[132:135]\n"
      "v_accvgpr_write_b32 a250, v98\n"
      "v_accvgpr_write_b32 a251, v99\n"
      "v_mfma_f32_16x16x32_bf16 v[88:91], v[64:67], a[180:183], v[148:151]\n"
      "v_cvt_pk_bf16_f32 v72, v72, v73\n"
      "v_cvt_pk_bf16_f32 v73, v74, v75\n"
      "v_cvt_pk_bf16_f32 v74, v76, v77\n"
      "v_mfma_f32_16x16x32_bf16 v[92:95], v[68:71], a[180:183], v[148:151]\n"
      "v_cvt_pk_bf16_f32 v75, v78, v79\n"
      "v_accvgpr_write_b32 a36, v72\n"
      "v_accvgpr_write_b32 a37, v73\n"
      "v_mfma_f32_16x16x32_bf16 v[136:139], v[172:175], a[248:251], v[136:139]\n"
      "v_accvgpr_write_b32 a38, v74\n"
      "v_accvgpr_write_b32 a39, v75\n"
      "v_mfma_f32_16x16x32_bf16 v[96:99], v[64:67], a[252:255], v[152:155]\n"
      "v_cvt_pk_bf16_f32 v80, v80, v81\n"
      "v_cvt_pk_bf16_f32 v81, v82, v83\n"
      "v_cvt_pk_bf16_f32 v82, v84, v85\n"
      "v_mfma_f32_16x16x32_bf16 v[100:103], v[68:71], a[252:255], v[152:155]\n"
      "v_cvt_pk_bf16_f32 v83, v86, v87\n"
      "v_accvgpr_write_b32 a108, v80\n"
      "v_accvgpr_write_b32 a109, v81\n"
      "v_mfma_f32_16x16x32_bf16 v[124:127], v[172:175], a[36:39], v[124:127]\n"
      "v_accvgpr_write_b32 a110, v82\n"
      "v_accvgpr_write_b32 a111, v83\n"
      "s_nop 1\n"
      "v_mfma_f32_16x16x32_bf16 v[128:131], v[172:175], a[108:111], v[128:131]\n"
      "v_cvt_pk_bf16_f32 v88, v88, v89\n"
      "v_cvt_pk_bf16_f32 v89, v90, v91\n"
      "v_cvt_pk_bf16_f32 v90, v92, v93\n"
      "v_cvt_pk_bf16_f32 v91, v94, v95\n"
      "v_accvgpr_write_b32 a180, v88\n"
      "v_accvgpr_write_b32 a181, v89\n"
      "v_accvgpr_write_b32 a182, v90\n"
      "v_accvgpr_write_b32 a183, v91\n"
      "s_nop 1\n"
      "v_mfma_f32_16x16x32_bf16 v[132:135], v[172:175], a[180:183], v[132:135]\n"
      "v_cvt_pk_bf16_f32 v96, v96, v97\n"
      "v_cvt_pk_bf16_f32 v97, v98, v99\n"
      "v_cvt_pk_bf16_f32 v98, v100, v101\n"
      "v_cvt_pk_bf16_f32 v99, v102, v103\n"
      "v_accvgpr_write_b32 a252, v96\n"
      "v_accvgpr_write_b32 a253, v97\n"
      "v_accvgpr_write_b32 a254, v98\n"
      "v_accvgpr_write_b32 a255, v99\n"
      "s_nop 1\n"
      "v_mfma_f32_16x16x32_bf16 v[136:139], v[172:175], a[252:255], v[136:139]\n"
      "v_add_f32 v168, v124, v156\n"
      "v_add_f32 v169, v128, v157\n"
      "v_add_f32 v170, v132, v158\n"
      "s_nop 5\n"
      "v_add_f32 v171, v136, v159\n"
      "v_mul_f32 v140, 0xbb800000, v168\n"
      "v_mul_f32 v141, 0xbb800000, v168\n"
      "v_mul_f32 v142, 0xbb800000, v168\n"
      "v_mul_f32 v143, 0xbb800000, v168\n"
      "v_mul_f32 v144, 0xbb800000, v169\n"
      "v_mul_f32 v145, 0xbb800000, v169\n"
      "v_mul_f32 v146, 0xbb800000, v169\n"
      "v_mul_f32 v147, 0xbb800000, v169\n"
      "v_mul_f32 v148, 0xbb800000, v170\n"
      "v_mul_f32 v149, 0xbb800000, v170\n"
      "v_mul_f32 v150, 0xbb800000, v170\n"
      "v_mul_f32 v151, 0xbb800000, v170\n"
      "v_mul_f32 v152, 0xbb800000, v171\n"
      "v_mul_f32 v153, 0xbb800000, v171\n"
      "v_mul_f32 v154, 0xbb800000, v171\n"
      "v_mul_f32 v155, 0xbb800000, v171\n"
      "v_mfma_f32_16x16x32_bf16 v[72:75], v[64:67], a[40:43], v[140:143]\n"
      "v_mfma_f32_16x16x32_bf16 v[76:79], v[68:71], a[40:43], v[140:143]\n"
      "v_mfma_f32_16x16x32_bf16 v[80:83], v[64:67], a[112:115], v[144:147]\n"
      "v_mfma_f32_16x16x32_bf16 v[84:87], v[68:71], a[112:115], v[144:147]\n"
      "v_mfma_f32_16x16x32_bf16 v[88:91], v[64:67], a[184:187], v[148:151]\n"
      "s_nop 4\n"
      "v_cvt_pk_bf16_f32 v72, v72, v73\n"
      "v_cvt_pk_bf16_f32 v73, v74, v75\n"
      "v_cvt_pk_bf16_f32 v74, v76, v77\n"
      "v_mfma_f32_16x16x32_bf16 v[92:95], v[68:71], a[184:187], v[148:151]\n"
      "v_cvt_pk_bf16_f32 v75, v78, v79\n"
      "v_accvgpr_write_b32 a40, v72\n"
      "v_accvgpr_write_b32 a41, v73\n"
      "v_accvgpr_write_b32 a42, v74\n"
      "v_accvgpr_write_b32 a43, v75\n"
      "v_mfma_f32_16x16x32_bf16 v[96:99], v[64:67], v[20:23], v[152:155]\n"
      "v_cvt_pk_bf16_f32 v80, v80, v81\n"
      "v_cvt_pk_bf16_f32 v81, v82, v83\n"
      "v_cvt_pk_bf16_f32 v82, v84, v85\n"
      "v_mfma_f32_16x16x32_bf16 v[100:103], v[68:71], v[20:23], v[152:155]\n"
      "v_cvt_pk_bf16_f32 v83, v86, v87\n"
      "v_accvgpr_write_b32 a112, v80\n"
      "v_accvgpr_write_b32 a113, v81\n"
      "v_mfma_f32_16x16x32_bf16 v[124:127], v[172:175], a[40:43], v[124:127]\n"
      "v_accvgpr_write_b32 a114, v82\n"
      "v_accvgpr_write_b32 a115, v83\n"
      "v_mfma_f32_16x16x32_bf16 v[72:75], v[64:67], a[44:47], v[140:143]\n"
      "v_cvt_pk_bf16_f32 v88, v88, v89\n"
      "v_cvt_pk_bf16_f32 v89, v90, v91\n"
      "v_cvt_pk_bf16_f32 v90, v92, v93\n"
      "v_mfma_f32_16x16x32_bf16 v[76:79], v[68:71], a[44:47], v[140:143]\n"
      "v_cvt_pk_bf16_f32 v91, v94, v95\n"
      "v_accvgpr_write_b32 a184, v88\n"
      "v_accvgpr_write_b32 a185, v89\n"
      "v_mfma_f32_16x16x32_bf16 v[128:131], v[172:175], a[112:115], v[128:131]\n"
      "v_accvgpr_write_b32 a186, v90\n"
      "v_accvgpr_write_b32 a187, v91\n"
      "v_mfma_f32_16x16x32_bf16 v[80:83], v[64:67], a[116:119], v[144:147]\n"
      "v_cvt_pk_bf16_f32 v20, v96, v97\n"
      "v_cvt_pk_bf16_f32 v21, v98, v99\n"
      "v_cvt_pk_bf16_f32 v22, v100, v101\n"
      "v_mfma_f32_16x16x32_bf16 v[84:87], v[68:71], a[116:119], v[144:147]\n"
      "v_cvt_pk_bf16_f32 v23, v102, v103\n"
      "v_mfma_f32_16x16x32_bf16 v[132:135], v[172:175], a[184:187], v[132:135]\n"
      "v_mfma_f32_16x16x32_bf16 v[88:91], v[64:67], a[188:191], v[148:151]\n"
      "v_cvt_pk_bf16_f32 v72, v72, v73\n"
      "v_cvt_pk_bf16_f32 v73, v74, v75\n"
      "v_cvt_pk_bf16_f32 v74, v76, v77\n"
      "v_mfma_f32_16x16x32_bf16 v[92:95], v[68:71], a[188:191], v[148:151]\n"
      "v_cvt_pk_bf16_f32 v75, v78, v79\n"
      "v_accvgpr_write_b32 a44, v72\n"
      "v_accvgpr_write_b32 a45, v73\n"
      "v_mfma_f32_16x16x32_bf16 v[136:139], v[172:175], v[20:23], v[136:139]\n"
      "v_accvgpr_write_b32 a46, v74\n"
      "v_accvgpr_write_b32 a47, v75\n"
      "v_mfma_f32_16x16x32_bf16 v[96:99], v[64:67], v[24:27], v[152:155]\n"
      "v_cvt_pk_bf16_f32 v80, v80, v81\n"
      "v_cvt_pk_bf16_f32 v81, v82, v83\n"
      "v_cvt_pk_bf16_f32 v82, v84, v85\n"
      "v_mfma_f32_16x16x32_bf16 v[100:103], v[68:71], v[24:27], v[152:155]\n"
      "v_cvt_pk_bf16_f32 v83, v86, v87\n"
      "v_accvgpr_write_b32 a116, v80\n"
      "v_accvgpr_write_b32 a117, v81\n"
      "v_mfma_f32_16x16x32_bf16 v[124:127], v[172:175], a[44:47], v[124:127]\n"
      "v_accvgpr_write_b32 a118, v82\n"
      "v_accvgpr_write_b32 a119, v83\n"
      "v_mfma_f32_16x16x32_bf16 v[72:75], v[64:67], a[48:51], v[140:143]\n"
      "v_cvt_pk_bf16_f32 v88, v88, v89\n"
      "v_cvt_pk_bf16_f32 v89, v90, v91\n"
      "v_cvt_pk_bf16_f32 v90, v92, v93\n"
      "v_mfma_f32_16x16x32_bf16 v[76:79], v[68:71], a[48:51], v[140:143]\n"
      "v_cvt_pk_bf16_f32 v91, v94, v95\n"
      "v_accvgpr_write_b32 a188, v88\n"
      "v_accvgpr_write_b32 a189, v89\n"
      "v_mfma_f32_16x16x32_bf16 v[128:131], v[172:175], a[116:119], v[128:131]\n"
      "v_accvgpr_write_b32 a190, v90\n"
      "v_accvgpr_write_b32 a191, v91\n"
      "v_mfma_f32_16x16x32_bf16 v[80:83], v[64:67], a[120:123], v[144:147]\n"
      "v_cvt_pk_bf16_f32 v24, v96, v97\n"
      "v_cvt_pk_bf16_f32 v25, v98, v99\n"
      "v_cvt_pk_bf16_f32 v26, v100, v101\n"
      "v_mfma_f32_16x16x32_bf16 v[84:87], v[68:71], a[120:123], v[144:147]\n"
      "v_cvt_pk_bf16_f32 v27, v102, v103\n"
      "v_mfma_f32_16x16x32_bf16 v[132:135], v[172:175], a[188:191], v[132:135]\n"
      "v_mfma_f32_16x16x32_bf16 v[88:91], v[64:67], a[192:195], v[148:151]\n"
      "v_cvt_pk_bf16_f32 v72, v72, v73\n"
      "v_cvt_pk_bf16_f32 v73, v74, v75\n"
      "v_cvt_pk_bf16_f32 v74, v76, v77\n"
      "v_mfma_f32_16x16x32_bf16 v[92:95], v[68:71], a[192:195], v[148:151]\n"
      "v_cvt_pk_bf16_f32 v75, v78, v79\n"
      "v_accvgpr_write_b32 a48, v72\n"
      "v_accvgpr_write_b32 a49, v73\n"
      "v_mfma_f32_16x16x32_bf16 v[136:139], v[172:175], v[24:27], v[136:139]\n"
      "v_accvgpr_write_b32 a50, v74\n"
      "v_accvgpr_write_b32 a51, v75\n"
      "v_mfma_f32_16x16x32_bf16 v[96:99], v[64:67], v[28:31], v[152:155]\n"
      "v_cvt_pk_bf16_f32 v80, v80, v81\n"
      "v_cvt_pk_bf16_f32 v81, v82, v83\n"
      "v_cvt_pk_bf16_f32 v82, v84, v85\n"
      "v_mfma_f32_16x16x32_bf16 v[100:103], v[68:71], v[28:31], v[152:155]\n"
      "v_cvt_pk_bf16_f32 v83, v86, v87\n"
      "v_accvgpr_write_b32 a120, v80\n"
      "v_accvgpr_write_b32 a121, v81\n"
      "v_mfma_f32_16x16x32_bf16 v[124:127], v[172:175], a[48:51], v[124:127]\n"
      "v_accvgpr_write_b32 a122, v82\n"
      "v_accvgpr_write_b32 a123, v83\n"
      "v_mfma_f32_16x16x32_bf16 v[72:75], v[64:67], a[52:55], v[140:143]\n"
      "v_cvt_pk_bf16_f32 v88, v88, v89\n"
      "v_cvt_pk_bf16_f32 v89, v90, v91\n"
      "v_cvt_pk_bf16_f32 v90, v92, v93\n"
      "v_mfma_f32_16x16x32_bf16 v[76:79], v[68:71], a[52:55], v[140:143]\n"
      "v_cvt_pk_bf16_f32 v91, v94, v95\n"
      "v_accvgpr_write_b32 a192, v88\n"
      "v_accvgpr_write_b32 a193, v89\n"
      "v_mfma_f32_16x16x32_bf16 v[128:131], v[172:175], a[120:123], v[128:131]\n"
      "v_accvgpr_write_b32 a194, v90\n"
      "v_accvgpr_write_b32 a195, v91\n"
      "v_mfma_f32_16x16x32_bf16 v[80:83], v[64:67], a[124:127], v[144:147]\n"
      "v_cvt_pk_bf16_f32 v28, v96, v97\n"
      "v_cvt_pk_bf16_f32 v29, v98, v99\n"
      "v_cvt_pk_bf16_f32 v30, v100, v101\n"
      "v_mfma_f32_16x16x32_bf16 v[84:87], v[68:71], a[124:127], v[144:147]\n"
      "v_cvt_pk_bf16_f32 v31, v102, v103\n"
      "v_mfma_f32_16x16x32_bf16 v[132:135], v[172:175], a[192:195], v[132:135]\n"
      "v_mfma_f32_16x16x32_bf16 v[88:91], v[64:67], a[196:199], v[148:151]\n"
      "v_cvt_pk_bf16_f32 v72, v72, v73\n"
      "v_cvt_pk_bf16_f32 v73, v74, v75\n"
      "v_cvt_pk_bf16_f32 v74, v76, v77\n"
      "v_mfma_f32_16x16x32_bf16 v[92:95], v[68:71], a[196:199], v[148:151]\n"
      "v_cvt_pk_bf16_f32 v75, v78, v79\n"
      "v_accvgpr_write_b32 a52, v72\n"
      "v_accvgpr_write_b32 a53, v73\n"
      "v_mfma_f32_16x16x32_bf16 v[136:139], v[172:175], v[28:31], v[136:139]\n"
      "v_accvgpr_write_b32 a54, v74\n"
      "v_accvgpr_write_b32 a55, v75\n"
      "v_mfma_f32_16x16x32_bf16 v[96:99], v[64:67], v[32:35], v[152:155]\n"
      "v_cvt_pk_bf16_f32 v80, v80, v81\n"
      "v_cvt_pk_bf16_f32 v81, v82, v83\n"
      "v_cvt_pk_bf16_f32 v82, v84, v85\n"
      "v_mfma_f32_16x16x32_bf16 v[100:103], v[68:71], v[32:35], v[152:155]\n"
      "v_cvt_pk_bf16_f32 v83, v86, v87\n"
      "v_accvgpr_write_b32 a124, v80\n"
      "v_accvgpr_write_b32 a125, v81\n"
      "v_mfma_f32_16x16x32_bf16 v[124:127], v[172:175], a[52:55], v[124:127]\n"
      "v_accvgpr_write_b32 a126, v82\n"
      "v_accvgpr_write_b32 a127, v83\n"
      "v_mfma_f32_16x16x32_bf16 v[72:75], v[64:67], a[56:59], v[140:143]\n"
      "v_cvt_pk_bf16_f32 v88, v88, v89\n"
      "v_cvt_pk_bf16_f32 v89, v90, v91\n"
      "v_cvt_pk_bf16_f32 v90, v92, v93\n"
      "v_mfma_f32_16x16x32_bf16 v[76:79], v[68:71], a[56:59], v[140:143]\n"
      "v_cvt_pk_bf16_f32 v91, v94, v95\n"
      "v_accvgpr_write_b32 a196, v88\n"
      "v_accvgpr_write_b32 a197, v89\n"
      "v_mfma_f32_16x16x32_bf16 v[128:131], v[172:175], a[124:127], v[128:131]\n"
      "v_accvgpr_write_b32 a198, v90\n"
      "v_accvgpr_write_b32 a199, v91\n"
      "v_mfma_f32_16x16x32_bf16 v[80:83], v[64:67], a[128:131], v[144:147]\n"
      "v_cvt_pk_bf16_f32 v32, v96, v97\n"
      "v_cvt_pk_bf16_f32 v33, v98, v99\n"
      "v_cvt_pk_bf16_f32 v34, v100, v101\n"
      "v_mfma_f32_16x16x32_bf16 v[84:87], v[68:71], a[128:131], v[144:147]\n"
      "v_cvt_pk_bf16_f32 v35, v102, v103\n"
      "v_mfma_f32_16x16x32_bf16 v[132:135], v[172:175], a[196:199], v[132:135]\n"
      "v_mfma_f32_16x16x32_bf16 v[88:91], v[64:67], a[200:203], v[148:151]\n"
      "v_cvt_pk_bf16_f32 v72, v72, v73\n"
      "v_cvt_pk_bf16_f32 v73, v74, v75\n"
      "v_cvt_pk_bf16_f32 v74, v76, v77\n"
      "v_mfma_f32_16x16x32_bf16 v[92:95], v[68:71], a[200:203], v[148:151]\n"
      "v_cvt_pk_bf16_f32 v75, v78, v79\n"
      "v_accvgpr_write_b32 a56, v72\n"
      "v_accvgpr_write_b32 a57, v73\n"
      "v_mfma_f32_16x16x32_bf16 v[136:139], v[172:175], v[32:35], v[136:139]\n"
      "v_accvgpr_write_b32 a58, v74\n"
      "v_accvgpr_write_b32 a59, v75\n"
      "v_mfma_f32_16x16x32_bf16 v[96:99], v[64:67], v[36:39], v[152:155]\n"
      "v_cvt_pk_bf16_f32 v80, v80, v81\n"
      "v_cvt_pk_bf16_f32 v81, v82, v83\n"
      "v_cvt_pk_bf16_f32 v82, v84, v85\n"
      "v_mfma_f32_16x16x32_bf16 v[100:103], v[68:71], v[36:39], v[152:155]\n"
      "v_cvt_pk_bf16_f32 v83, v86, v87\n"
      "v_accvgpr_write_b32 a128, v80\n"
      "v_accvgpr_write_b32 a129, v81\n"
      "v_mfma_f32_16x16x32_bf16 v[124:127], v[172:175], a[56:59], v[124:127]\n"
      "v_accvgpr_write_b32 a130, v82\n"
      "v_accvgpr_write_b32 a131, v83\n"
      "s_nop 1\n"
      "v_mfma_f32_16x16x32_bf16 v[128:131], v[172:175], a[128:131], v[128:131]\n"
      "v_cvt_pk_bf16_f32 v88, v88, v89\n"
      "v_cvt_pk_bf16_f32 v89, v90, v91\n"
      "v_cvt_pk_bf16_f32 v90, v92, v93\n"
      "v_cvt_pk_bf16_f32 v91, v94, v95\n"
      "v_accvgpr_write_b32 a200, v88\n"
      "v_accvgpr_write_b32 a201, v89\n"
      "v_accvgpr_write_b32 a202, v90\n"
      "v_accvgpr_write_b32 a203, v91\n"
      "s_nop 1\n"
      "v_mfma_f32_16x16x32_bf16 v[132:135], v[172:175], a[200:203], v[132:135]\n"
      "v_cvt_pk_bf16_f32 v36, v96, v97\n"
      "v_cvt_pk_bf16_f32 v37, v98, v99\n"
      "v_cvt_pk_bf16_f32 v38, v100, v101\n"
      "v_cvt_pk_bf16_f32 v39, v102, v103\n"
      "s_nop 1\n"
      "v_mfma_f32_16x16x32_bf16 v[136:139], v[172:175], v[36:39], v[136:139]\n"
      "v_add_f32 v168, v124, v160\n"
      "v_add_f32 v169, v128, v161\n"
      "v_add_f32 v170, v132, v162\n"
      "s_nop 5\n"
      "v_add_f32 v171, v136, v163\n"
      "v_mul_f32 v140, 0xbc2aaaab, v168\n"
      "v_mul_f32 v141, 0xbc2aaaab, v168\n"
      "v_mul_f32 v142, 0xbc2aaaab, v168\n"
      "v_mul_f32 v143, 0xbc2aaaab, v168\n"
      "v_mul_f32 v144, 0xbc2aaaab, v169\n"
      "v_mul_f32 v145, 0xbc2aaaab, v169\n"
      "v_mul_f32 v146, 0xbc2aaaab, v169\n"
      "v_mul_f32 v147, 0xbc2aaaab, v169\n"
      "v_mul_f32 v148, 0xbc2aaaab, v170\n"
      "v_mul_f32 v149, 0xbc2aaaab, v170\n"
      "v_mul_f32 v150, 0xbc2aaaab, v170\n"
      "v_mul_f32 v151, 0xbc2aaaab, v170\n"
      "v_mul_f32 v152, 0xbc2aaaab, v171\n"
      "v_mul_f32 v153, 0xbc2aaaab, v171\n"
      "v_mul_f32 v154, 0xbc2aaaab, v171\n"
      "v_mul_f32 v155, 0xbc2aaaab, v171\n"
      "v_mfma_f32_16x16x32_bf16 v[72:75], v[64:67], a[60:63], v[140:143]\n"
      "v_mfma_f32_16x16x32_bf16 v[76:79], v[68:71], a[60:63], v[140:143]\n"
      "v_mfma_f32_16x16x32_bf16 v[80:83], v[64:67], a[132:135], v[144:147]\n"
      "v_mfma_f32_16x16x32_bf16 v[84:87], v[68:71], a[132:135], v[144:147]\n"
      "v_mfma_f32_16x16x32_bf16 v[88:91], v[64:67], a[204:207], v[148:151]\n"
      "s_nop 4\n"
      "v_cvt_pk_bf16_f32 v72, v72, v73\n"
      "v_cvt_pk_bf16_f32 v73, v74, v75\n"
      "v_cvt_pk_bf16_f32 v74, v76, v77\n"
      "v_mfma_f32_16x16x32_bf16 v[92:95], v[68:71], a[204:207], v[148:151]\n"
      "v_cvt_pk_bf16_f32 v75, v78, v79\n"
      "v_accvgpr_write_b32 a60, v72\n"
      "v_accvgpr_write_b32 a61, v73\n"
      "v_accvgpr_write_b32 a62, v74\n"
      "v_accvgpr_write_b32 a63, v75\n"
      "v_mfma_f32_16x16x32_bf16 v[96:99], v[64:67], v[40:43], v[152:155]\n"
      "v_cvt_pk_bf16_f32 v80, v80, v81\n"
      "v_cvt_pk_bf16_f32 v81, v82, v83\n"
      "v_cvt_pk_bf16_f32 v82, v84, v85\n"
      "v_mfma_f32_16x16x32_bf16 v[100:103], v[68:71], v[40:43], v[152:155]\n"
      "v_cvt_pk_bf16_f32 v83, v86, v87\n"
      "v_accvgpr_write_b32 a132, v80\n"
      "v_accvgpr_write_b32 a133, v81\n"
      "v_mfma_f32_16x16x32_bf16 v[124:127], v[172:175], a[60:63], v[124:127]\n"
      "v_accvgpr_write_b32 a134, v82\n"
      "v_accvgpr_write_b32 a135, v83\n"
      "v_mfma_f32_16x16x32_bf16 v[72:75], v[64:67], a[64:67], v[140:143]\n"
      "v_cvt_pk_bf16_f32 v88, v88, v89\n"
      "v_cvt_pk_bf16_f32 v89, v90, v91\n"
      "v_cvt_pk_bf16_f32 v90, v92, v93\n"
      "v_mfma_f32_16x16x32_bf16 v[76:79], v[68:71], a[64:67], v[140:143]\n"
      "v_cvt_pk_bf16_f32 v91, v94, v95\n"
      "v_accvgpr_write_b32 a204, v88\n"
      "v_accvgpr_write_b32 a205, v89\n"
      "v_mfma_f32_16x16x32_bf16 v[128:131], v[172:175], a[132:135], v[128:131]\n"
      "v_accvgpr_write_b32 a206, v90\n"
      "v_accvgpr_write_b32 a207, v91\n"
      "v_mfma_f32_16x16x32_bf16 v[80:83], v[64:67], a[136:139], v[144:147]\n"
      "v_cvt_pk_bf16_f32 v40, v96, v97\n"
      "v_cvt_pk_bf16_f32 v41, v98, v99\n"
      "v_cvt_pk_bf16_f32 v42, v100, v101\n"
      "v_mfma_f32_16x16x32_bf16 v[84:87], v[68:71], a[136:139], v[144:147]\n"
      "v_cvt_pk_bf16_f32 v43, v102, v103\n"
      "v_mfma_f32_16x16x32_bf16 v[132:135], v[172:175], a[204:207], v[132:135]\n"
      "v_mfma_f32_16x16x32_bf16 v[88:91], v[64:67], a[208:211], v[148:151]\n"
      "v_cvt_pk_bf16_f32 v72, v72, v73\n"
      "v_cvt_pk_bf16_f32 v73, v74, v75\n"
      "v_cvt_pk_bf16_f32 v74, v76, v77\n"
      "v_mfma_f32_16x16x32_bf16 v[92:95], v[68:71], a[208:211], v[148:151]\n"
      "v_cvt_pk_bf16_f32 v75, v78, v79\n"
      "v_accvgpr_write_b32 a64, v72\n"
      "v_accvgpr_write_b32 a65, v73\n"
      "v_mfma_f32_16x16x32_bf16 v[136:139], v[172:175], v[40:43], v[136:139]\n"
      "v_accvgpr_write_b32 a66, v74\n"
      "v_accvgpr_write_b32 a67, v75\n"
      "v_mfma_f32_16x16x32_bf16 v[96:99], v[64:67], v[44:47], v[152:155]\n"
      "v_cvt_pk_bf16_f32 v80, v80, v81\n"
      "v_cvt_pk_bf16_f32 v81, v82, v83\n"
      "v_cvt_pk_bf16_f32 v82, v84, v85\n"
      "v_mfma_f32_16x16x32_bf16 v[100:103], v[68:71], v[44:47], v[152:155]\n"
      "v_cvt_pk_bf16_f32 v83, v86, v87\n"
      "v_accvgpr_write_b32 a136, v80\n"
      "v_accvgpr_write_b32 a137, v81\n"
      "v_mfma_f32_16x16x32_bf16 v[124:127], v[172:175], a[64:67], v[124:127]\n"
      "v_accvgpr_write_b32 a138, v82\n"
      "v_accvgpr_write_b32 a139, v83\n"
      "s_nop 1\n"
      "v_mfma_f32_16x16x32_bf16 v[128:131], v[172:175], a[136:139], v[128:131]\n"
      "v_cvt_pk_bf16_f32 v88, v88, v89\n"
      "v_cvt_pk_bf16_f32 v89, v90, v91\n"
      "v_cvt_pk_bf16_f32 v90, v92, v93\n"
      "v_cvt_pk_bf16_f32 v91, v94, v95\n"
      "v_accvgpr_write_b32 a208, v88\n"
      "v_accvgpr_write_b32 a209, v89\n"
      "v_accvgpr_write_b32 a210, v90\n"
      "v_accvgpr_write_b32 a211, v91\n"
      "s_nop 1\n"
      "v_mfma_f32_16x16x32_bf16 v[132:135], v[172:175], a[208:211], v[132:135]\n"
      "v_cvt_pk_bf16_f32 v44, v96, v97\n"
      "v_cvt_pk_bf16_f32 v45, v98, v99\n"
      "v_cvt_pk_bf16_f32 v46, v100, v101\n"
      "v_cvt_pk_bf16_f32 v47, v102, v103\n"
      "s_nop 1\n"
      "v_mfma_f32_16x16x32_bf16 v[136:139], v[172:175], v[44:47], v[136:139]\n"
      "v_add_f32 v168, v124, v164\n"
      "v_add_f32 v169, v128, v165\n"
      "v_add_f32 v170, v132, v166\n"
      "s_nop 5\n"
      "v_add_f32 v171, v136, v167\n"
      "v_mul_f32 v140, 0xbd000000, v168\n"
      "v_mul_f32 v141, 0xbd000000, v168\n"
      "v_mul_f32 v142, 0xbd000000, v168\n"
      "v_mul_f32 v143, 0xbd000000, v168\n"
      "v_mul_f32 v144, 0xbd000000, v169\n"
      "v_mul_f32 v145, 0xbd000000, v169\n"
      "v_mul_f32 v146, 0xbd000000, v169\n"
      "v_mul_f32 v147, 0xbd000000, v169\n"
      "v_mul_f32 v148, 0xbd000000, v170\n"
      "v_mul_f32 v149, 0xbd000000, v170\n"
      "v_mul_f32 v150, 0xbd000000, v170\n"
      "v_mul_f32 v151, 0xbd000000, v170\n"
      "v_mul_f32 v152, 0xbd000000, v171\n"
      "v_mul_f32 v153, 0xbd000000, v171\n"
      "v_mul_f32 v154, 0xbd000000, v171\n"
      "v_mul_f32 v155, 0xbd000000, v171\n"
      "v_mfma_f32_16x16x32_bf16 v[72:75], v[64:67], a[68:71], v[140:143]\n"
      "v_mfma_f32_16x16x32_bf16 v[76:79], v[68:71], a[68:71], v[140:143]\n"
      "v_mfma_f32_16x16x32_bf16 v[80:83], v[64:67], a[140:143], v[144:147]\n"
      "v_mfma_f32_16x16x32_bf16 v[84:87], v[68:71], a[140:143], v[144:147]\n"
      "v_mfma_f32_16x16x32_bf16 v[88:91], v[64:67], a[212:215], v[148:151]\n"
      "s_nop 4\n"
      "v_cvt_pk_bf16_f32 v72, v72, v73\n"
      "v_cvt_pk_bf16_f32 v73, v74, v75\n"
      "v_cvt_pk_bf16_f32 v74, v76, v77\n"
      "v_mfma_f32_16x16x32_bf16 v[92:95], v[68:71], a[212:215], v[148:151]\n"
      "v_cvt_pk_bf16_f32 v75, v78, v79\n"
      "v_accvgpr_write_b32 a68, v72\n"
      "v_accvgpr_write_b32 a69, v73\n"
      "v_accvgpr_write_b32 a70, v74\n"
      "v_accvgpr_write_b32 a71, v75\n"
      "v_mfma_f32_16x16x32_bf16 v[96:99], v[64:67], v[48:51], v[152:155]\n"
      "v_cvt_pk_bf16_f32 v80, v80, v81\n"
      "v_cvt_pk_bf16_f32 v81, v82, v83\n"
      "v_cvt_pk_bf16_f32 v82, v84, v85\n"
      "v_mfma_f32_16x16x32_bf16 v[100:103], v[68:71], v[48:51], v[152:155]\n"
      "v_cvt_pk_bf16_f32 v83, v86, v87\n"
      "v_accvgpr_write_b32 a140, v80\n"
      "v_accvgpr_write_b32 a141, v81\n"
      "v_accvgpr_write_b32 a142, v82\n"
      "v_accvgpr_write_b32 a143, v83\n"
      "v_cvt_pk_bf16_f32 v88, v88, v89\n"
      "v_cvt_pk_bf16_f32 v89, v90, v91\n"
      "v_cvt_pk_bf16_f32 v90, v92, v93\n"
      "v_cvt_pk_bf16_f32 v91, v94, v95\n"
      "v_accvgpr_write_b32 a212, v88\n"
      "v_accvgpr_write_b32 a213, v89\n"
      "v_accvgpr_write_b32 a214, v90\n"
      "v_accvgpr_write_b32 a215, v91\n"
      "v_cvt_pk_bf16_f32 v48, v96, v97\n"
      "v_cvt_pk_bf16_f32 v49, v98, v99\n"
      "v_cvt_pk_bf16_f32 v50, v100, v101\n"
      "v_cvt_pk_bf16_f32 v51, v102, v103\n"
      "v_add_u32 v17, 40960, v0\n"
      "s_barrier\n"
      "v_mov_b32 v11, 0\n"
      "ds_write_b32 v13, v11\n"
      "s_waitcnt lgkmcnt(0)\n"
      "s_barrier\n"
      "s_add_u32 s6, s36, 1548288\n"
      "s_add_u32 s36, s36, 4294942720\n"
      "s_add_u32 s42, s37, 4294635520\n"
      "s_add_u32 s37, s37, 4294635520\n"
      "s_add_u32 s36, s36, 24576\n"
      "s_min_u32 s36, s36, s6\n"
      "buffer_load_dword v18, v2, s[8:11], s36 offen sc1\n"
      "s_mov_b32 s40, s36\n"
      "s_add_u32 m0, s38, 0\n"
      "s_waitcnt vmcnt(0)\n"
      "v_cmp_ne_u32 vcc, s21, v18\n"
      "s_cbranch_vccnz .Lsp_g0_1\n"
      ".Lbk_g0_1:\n"
      "buffer_load_dwordx4 v0, s[8:11], s40 offen offset:0 sc1 lds\n"
      "s_add_u32 s36, s36, 24576\n"
      "buffer_load_dwordx4 v0, s[8:11], s40 offen offset:1024 sc1 lds\n"
      "buffer_load_dwordx4 v0, s[8:11], s40 offen offset:2048 sc1 lds\n"
      "buffer_load_dwordx4 v0, s[8:11], s40 offen offset:3072 sc1 lds\n"
      "s_add_u32 m0, s39, 0\n"
      "s_min_u32 s36, s36, s6\n"
      "buffer_load_dwordx4 v1, s[8:11], s40 offen offset:0 sc1 lds\n"
      "buffer_load_dword v18, v2, s[8:11], s36 offen sc1\n"
      "v_add_u32 v11, 1, v11\n"
      "s_mov_b32 s40, s36\n"
      "s_add_u32 m0, s38, 20480\n"
      "s_waitcnt vmcnt(1)\n"
      "ds_write_b32 v13, v11\n"
      "s_waitcnt vmcnt(0)\n"
      "v_cmp_ne_u32 vcc, s21, v18\n"
      "s_cbranch_vccnz .Lsp_g0_2\n"
      ".Lbk_g0_2:\n"
      "buffer_load_dwordx4 v0, s[8:11], s40 offen offset:0 sc1 lds\n"
      "ds_read_b32 v12, v14\n"
      "s_add_u32 s36, s36, 24576\n"
      "buffer_load_dwordx4 v0, s[8:11], s40 offen offset:1024 sc1 lds\n"
      "s_waitcnt lgkmcnt(0)\n"
      "v_cmp_gt_i32 vcc, v11, v12\n"
      "buffer_load_dwordx4 v0, s[8:11], s40 offen offset:2048 sc1 lds\n"
      "s_cbranch_vccnz .Lsp_gy0_3\n"
      ".Lbk_gy0_3:\n"
      "ds_read2st64_b64 v[168:171], v4 offset0:0 offset1:1\n"
      "ds_read_b128 v[52:55], v0 offset:0\n"
      "buffer_load_dwordx4 v0, s[8:11], s40 offen offset:3072 sc1 lds\n"
      "s_add_u32 m0, s39, 20480\n"
      "ds_read_b128 v[64:67], v0 offset:3072\n"
      "s_min_u32 s36, s36, s6\n"
      "ds_read_b128 v[56:59], v0 offset:1024\n"
      "buffer_load_dwordx4 v1, s[8:11], s40 offen offset:0 sc1 lds\n"
      "ds_read_b128 v[68:71], v0 offset:4096\n"
      "ds_read_b128 v[76:79], v0 offset:6144\n"
      "ds_read_b128 v[72:75], v0 offset:5120\n"
      "buffer_load_dword v18, v2, s[8:11], s36 offen sc1\n"
      "ds_read_b128 v[60:63], v0 offset:2048\n"
      "buffer_load_dword v16, v13, s[8:11], s31 offen\n"
      "ds_read_b128 v[84:87], v0 offset:8192\n"
      "ds_read_b128 v[80:83], v0 offset:7168\n"
      "ds_read_b128 v[96:99], v0 offset:11264\n"
      "s_mov_b32 s20, 15\n"
      "s_waitcnt lgkmcnt(0)\n"
      "s_nop 4\n"
      "s_branch .Lg_loop\n"
      ".p2align 12\n"
      ".Lg_p0:\n"
      "s_branch .Lg_pr\n"
      ".p2align 5\n"
      ".Lg_loop:\n"
      "v_mfma_f32_16x16x32_bf16 v[124:127], a[0:3], v[52:55], 0\n"
      "ds_read_b128 v[92:95], v0 offset:10240\n"
      "v_add_f32 v169, v169, v171\n"
      "v_mfma_f32_16x16x32_bf16 v[124:127], a[4:7], v[56:59], v[124:127]\n"
      "s_waitcnt lgkmcnt(4)\n"
      "ds_read_b128 v[88:91], v0 offset:9216\n"
      "v_mul_f32 v161, v133, v161\n"
      "v_mfma_f32_16x16x32_bf16 v[124:127], a[8:11], v[60:63], v[124:127]\n"
      "ds_read_b128 v[112:115], v0 offset:15360\n"
      "v_mul_f32 v163, v135, v163\n"
      "v_mfma_f32_16x16x32_bf16 v[124:127], a[12:15], v[64:67], v[124:127]\n"
      "ds_read_b128 v[100:103], v0 offset:12288\n"
      "v_mul_f32 v139, v139, v163\n"
      "v_mfma_f32_16x16x32_bf16 v[124:127], a[16:19], v[68:71], v[124:127]\n"
      "ds_read_b128 v[104:107], v0 offset:13312\n"
      "v_mul_f32 v131, v131, v159\n"
      "v_mfma_f32_16x16x32_bf16 v[124:127], a[20:23], v[72:75], v[124:127]\n"
      "ds_read_b128 v[108:111], v0 offset:14336\n"
      "v_mfma_f32_16x16x32_bf16 v[124:127], a[24:27], v[76:79], v[124:127]\n"
      "s_waitcnt lgkmcnt(6)\n"
      "ds_read_b128 v[120:123], v0 offset:17408\n"
      "v_sub_f32 v176, -v168, v170\n"
      "v_mfma_f32_16x16x32_bf16 v[124:127], a[28:31], v[80:83], v[124:127]\n"
      "v_rcp_f32 v160, v160\n"
      "v_mfma_f32_16x16x32_bf16 v[124:127], a[32:35], v[84:87], v[124:127]\n"
      "s_waitcnt lgkmcnt(5)\n"
      "ds_read_b128 v[116:119], v0 offset:16384\n"
      "v_fma_f32 v169, -v176, v176, v169\n"
      "v_mfma_f32_16x16x32_bf16 v[124:127], a[36:39], v[88:91], v[124:127]\n"
      "v_mul_f32 v160, v132, v160\n"
      "v_fma_f32 v162, v162, v177, v177\n"
      "v_mfma_f32_16x16x32_bf16 v[124:127], a[40:43], v[92:95], v[124:127]\n"
      "v_mul_f32 v130, v130, v158\n"
      "v_mul_f32 v128, v128, v156\n"
      "v_mfma_f32_16x16x32_bf16 v[124:127], a[44:47], v[96:99], v[124:127]\n"
      "s_waitcnt lgkmcnt(3)\n"
      "v_rcp_f32 v162, v162\n"
      "v_mfma_f32_16x16x32_bf16 v[124:127], a[48:51], v[100:103], v[124:127]\n"
      "v_cvt_pk_bf16_f32 v165, v130, v131\n"
      "v_mul_f32 v162, v134, v162\n"
      "v_mfma_f32_16x16x32_bf16 v[124:127], a[52:55], v[104:107], v[124:127]\n"
      "s_waitcnt lgkmcnt(1)\n"
      "v_mul_f32 v136, v136, v160\n"
      "v_mul_f32 v138, v138, v162\n"
      "v_mfma_f32_16x16x32_bf16 v[124:127], a[56:59], v[108:111], v[124:127]\n"
      "v_mul_f32 v137, v137, v161\n"
      "v_cvt_pk_bf16_f32 v167, v138, v139\n"
      "v_mfma_f32_16x16x32_bf16 v[124:127], a[60:63], v[112:115], v[124:127]\n"
      "s_waitcnt lgkmcnt(0)\n"
      "v_max_f32 v177, 0x3727c5ac, v169\n"
      "v_mfma_f32_16x16x32_bf16 v[124:127], a[64:67], v[116:119], v[124:127]\n"
      "s_add_u32 s37, s37, 165888\n"
      "v_cvt_pk_bf16_f32 v164, v128, v129\n"
      "v_mfma_f32_16x16x32_bf16 v[124:127], a[68:71], v[120:123], v[124:127]\n"
      "v_cvt_pk_bf16_f32 v166, v136, v137\n"
      "v_add_u32 v11, 1, v11\n"
      "v_mfma_f32_16x16x32_bf16 v[132:135], a[144:147], v[52:55], 0\n"
      "buffer_store_dwordx4 v[164:167], v0, s[8:11], s37 offen sc1\n"
      "v_mfma_f32_16x16x32_bf16 v[132:135], a[148:151], v[56:59], v[132:135]\n"
      "v_rsq_f32 v170, v177\n"
      "v_mfma_f32_16x16x32_bf16 v[132:135], a[152:155], v[60:63], v[132:135]\n"
      "v_mul_f32 v178, 0xc0135761, v170\n"
      "v_mul_f32 v170, v170, v170\n"
      "v_mfma_f32_16x16x32_bf16 v[132:135], a[156:159], v[64:67], v[132:135]\n"
      "v_mul_f32 v171, 0x3d372713, v178\n"
      "v_mul_f32 v157, v125, v125\n"
      "v_mfma_f32_16x16x32_bf16 v[132:135], a[160:163], v[68:71], v[132:135]\n"
      "v_mul_f32 v179, v171, v170\n"
      "v_mul_f32 v158, v126, v126\n"
      "v_mfma_f32_16x16x32_bf16 v[132:135], a[164:167], v[72:75], v[132:135]\n"
      "v_fma_f32 v157, v157, v179, v178\n"
      "s_mov_b32 s40, s36\n"
      "v_mfma_f32_16x16x32_bf16 v[132:135], a[168:171], v[76:79], v[132:135]\n"
      "v_mul_f32 v157, v125, v157\n"
      "s_add_u32 m0, s38, 40960\n"
      "v_mfma_f32_16x16x32_bf16 v[132:135], a[172:175], v[80:83], v[132:135]\n"
      "v_exp_f32 v157, v157\n"
      "v_mfma_f32_16x16x32_bf16 v[132:135], a[176:179], v[84:87], v[132:135]\n"
      "v_fma_f32 v158, v158, v179, v178\n"
      "v_fma_f32 v157, v157, v177, v177\n"
      "v_mfma_f32_16x16x32_bf16 v[132:135], a[180:183], v[88:91], v[132:135]\n"
      "v_mfma_f32_16x16x32_bf16 v[132:135], a[184:187], v[92:95], v[132:135]\n"
      "v_mul_f32 v156, v124, v124\n"
      "v_mul_f32 v158, v126, v158\n"
      "v_mfma_f32_16x16x32_bf16 v[132:135], a[188:191], v[96:99], v[132:135]\n"
      "v_mul_f32 v159, v127, v127\n"
      "v_fma_f32 v156, v156, v179, v178\n"
      "v_mfma_f32_16x16x32_bf16 v[132:135], a[192:195], v[100:103], v[132:135]\n"
      "v_fma_f32 v159, v159, v179, v178\n"
      "v_mul_f32 v156, v124, v156\n"
      "v_mfma_f32_16x16x32_bf16 v[132:135], a[196:199], v[104:107], v[132:135]\n"
      "v_exp_f32 v158, v158\n"
      "v_mfma_f32_16x16x32_bf16 v[132:135], a[200:203], v[108:111], v[132:135]\n"
      "v_fma_f32 v158, v158, v177, v177\n"
      "v_mul_f32 v159, v127, v159\n"
      "v_mfma_f32_16x16x32_bf16 v[132:135], a[204:207], v[112:115], v[132:135]\n"
      "v_exp_f32 v156, v156\n"
      "v_mfma_f32_16x16x32_bf16 v[132:135], a[208:211], v[116:119], v[132:135]\n"
      "s_add_u32 s42, s42, 165888\n"
      "v_fma_f32 v156, v156, v177, v177\n"
      "v_mfma_f32_16x16x32_bf16 v[132:135], a[212:215], v[120:123], v[132:135]\n"
      "s_waitcnt vmcnt(3)\n"
      "ds_write_b32 v13, v11\n"
      "v_mfma_f32_16x16x32_bf16 v[128:131], a[72:75], v[52:55], 0\n"
      "v_exp_f32 v159, v159\n"
      "v_mfma_f32_16x16x32_bf16 v[128:131], a[76:79], v[56:59], v[128:131]\n"
      "s_waitcnt vmcnt(2)\n"
      "v_fma_f32 v159, v159, v177, v177\n"
      "v_cmp_ne_u32 vcc, s21, v18\n"
      "v_mfma_f32_16x16x32_bf16 v[128:131], a[80:83], v[60:63], v[128:131]\n"
      "v_mul_f32 v161, v133, v133\n"
      "v_fma_f32 v161, v161, v179, v178\n"
      "v_mfma_f32_16x16x32_bf16 v[128:131], a[84:87], v[64:67], v[128:131]\n"
      "v_mul_f32 v163, v135, v135\n"
      "v_mul_f32 v161, v133, v161\n"
      "v_mfma_f32_16x16x32_bf16 v[128:131], a[88:91], v[68:71], v[128:131]\n"
      "v_mul_f32 v160, v132, v132\n"
      "v_fma_f32 v163, v163, v179, v178\n"
      "v_mfma_f32_16x16x32_bf16 v[128:131], a[92:95], v[72:75], v[128:131]\n"
      "s_cbranch_vccnz .Lsp_g0_4\n"
      ".Lbk_g0_4:\n"
      "v_rcp_f32 v156, v156\n"
      "v_mfma_f32_16x16x32_bf16 v[128:131], a[96:99], v[76:79], v[128:131]\n"
      "buffer_load_dwordx4 v0, s[8:11], s40 offen offset:0 sc1 lds\n"
      "v_mfma_f32_16x16x32_bf16 v[128:131], a[100:103], v[80:83], v[128:131]\n"
      "v_rcp_f32 v159, v159\n"
      "v_mfma_f32_16x16x32_bf16 v[128:131], a[104:107], v[84:87], v[128:131]\n"
      "ds_read_b32 v12, v14\n"
      "v_fma_f32 v160, v160, v179, v178\n"
      "v_mfma_f32_16x16x32_bf16 v[128:131], a[108:111], v[88:91], v[128:131]\n"
      "s_add_u32 s36, s36, 24576\n"
      "v_mul_f32 v162, v134, v134\n"
      "v_mul_f32 v163, v135, v163\n"
      "v_mfma_f32_16x16x32_bf16 v[128:131], a[112:115], v[92:95], v[128:131]\n"
      "v_exp_f32 v163, v163\n"
      "v_mfma_f32_16x16x32_bf16 v[128:131], a[116:119], v[96:99], v[128:131]\n"
      "buffer_load_dwordx4 v0, s[8:11], s40 offen offset:1024 sc1 lds\n"
      "v_mfma_f32_16x16x32_bf16 v[128:131], a[120:123], v[100:103], v[128:131]\n"
      "v_rcp_f32_e64 v158, v158\n"
      "v_mfma_f32_16x16x32_bf16 v[128:131], a[124:127], v[104:107], v[128:131]\n"
      "s_waitcnt lgkmcnt(0)\n"
      "v_mul_f32_e64 v160, v132, v160\n"
      "v_cmp_gt_i32 vcc, v11, v12\n"
      "v_mfma_f32_16x16x32_bf16 v[128:131], a[128:131], v[108:111], v[128:131]\n"
      "v_fma_f32 v162, v162, v179, v178\n"
      "v_mul_f32_e64 v162, v134, v162\n"
      "v_mfma_f32_16x16x32_bf16 v[128:131], a[132:135], v[112:115], v[128:131]\n"
      "buffer_load_dwordx4 v0, s[8:11], s40 offen offset:2048 sc1 lds\n"
      "v_mfma_f32_16x16x32_bf16 v[128:131], a[136:139], v[116:119], v[128:131]\n"
      "v_exp_f32_e64 v162, v162\n"
      "v_mfma_f32_16x16x32_bf16 v[128:131], a[140:143], v[120:123], v[128:131]\n"
      "s_cbranch_vccnz .Lsp_gy0_5\n"
      ".Lbk_gy0_5:\n"
      "ds_read2st64_b64 v[168:171], v4 offset0:40 offset1:41\n"
      "v_mfma_f32_16x16x32_bf16 v[136:139], a[216:219], v[52:55], 0\n"
      "ds_read_b128 v[52:55], v0 offset:20480\n"
      "v_fma_f32 v163, v163, v177, v177\n"
      "v_mfma_f32_16x16x32_bf16 v[136:139], a[220:223], v[56:59], v[136:139]\n"
      "v_rcp_f32 v157, v157\n"
      "v_mfma_f32_16x16x32_bf16 v[136:139], a[224:227], v[60:63], v[136:139]\n"
      "buffer_load_dwordx4 v0, s[8:11], s40 offen offset:3072 sc1 lds\n"
      "v_mfma_f32_16x16x32_bf16 v[136:139], a[228:231], v[64:67], v[136:139]\n"
      "s_add_u32 m0, s39, 40960\n"
      "ds_read_b128 v[64:67], v0 offset:23552\n"
      "v_mul_f32 v158, v126, v158\n"
      "v_mfma_f32_16x16x32_bf16 v[136:139], a[232:235], v[68:71], v[136:139]\n"
      "s_min_u32 s36, s36, s6\n"
      "v_rcp_f32 v163, v163\n"
      "v_mfma_f32_16x16x32_bf16 v[136:139], a[236:239], v[72:75], v[136:139]\n"
      "ds_read_b128 v[56:59], v0 offset:21504\n"
      "v_mul_f32 v157, v125, v157\n"
      "v_mfma_f32_16x16x32_bf16 v[136:139], a[240:243], v[76:79], v[136:139]\n"
      "buffer_load_dwordx4 v1, s[8:11], s40 offen offset:0 sc1 lds\n"
      "v_mfma_f32_16x16x32_bf16 v[136:139], a[244:247], v[80:83], v[136:139]\n"
      "v_exp_f32 v161, v161\n"
      "v_mfma_f32_16x16x32_bf16 v[136:139], a[248:251], v[84:87], v[136:139]\n"
      "ds_read_b128 v[68:71], v0 offset:24576\n"
      "ds_read_b128 v[76:79], v0 offset:26624\n"
      "v_mfma_f32_16x16x32_bf16 v[136:139], a[252:255], v[88:91], v[136:139]\n"
      "v_exp_f32 v160, v160\n"
      "v_mfma_f32_16x16x32_bf16 v[136:139], v[20:23], v[92:95], v[136:139]\n"
      "ds_read_b128 v[72:75], v0 offset:25600\n"
      "v_mul_f32 v129, v129, v157\n"
      "v_mfma_f32_16x16x32_bf16 v[136:139], v[24:27], v[96:99], v[136:139]\n"
      "buffer_load_dword v18, v2, s[8:11], s36 offen sc1\n"
      "v_mfma_f32_16x16x32_bf16 v[136:139], v[28:31], v[100:103], v[136:139]\n"
      "ds_read_b128 v[60:63], v0 offset:22528\n"
      "v_fma_f32 v161, v161, v177, v177\n"
      "v_mfma_f32_16x16x32_bf16 v[136:139], v[32:35], v[104:107], v[136:139]\n"
      "s_waitcnt vmcnt(6) lgkmcnt(4)\n"
      "buffer_store_dword v15, v3, s[8:11], s42 offen sc1\n"
      "v_mfma_f32_16x16x32_bf16 v[136:139], v[36:39], v[108:111], v[136:139]\n"
      "ds_read_b128 v[84:87], v0 offset:28672\n"
      "v_fma_f32 v160, v160, v177, v177\n"
      "v_mfma_f32_16x16x32_bf16 v[136:139], v[40:43], v[112:115], v[136:139]\n"
      "v_rcp_f32 v161, v161\n"
      "v_mfma_f32_16x16x32_bf16 v[136:139], v[44:47], v[116:119], v[136:139]\n"
      "ds_read_b128 v[80:83], v0 offset:27648\n"
      "v_mul_f32 v159, v127, v159\n"
      "v_mfma_f32_16x16x32_bf16 v[136:139], v[48:51], v[120:123], v[136:139]\n"
      "ds_read_b128 v[96:99], v0 offset:31744\n"
      "v_mul_f32 v156, v124, v156\n"
      "v_mfma_f32_16x16x32_bf16 v[124:127], a[0:3], v[52:55], 0\n"
      "ds_read_b128 v[92:95], v0 offset:30720\n"
      "v_add_f32 v169, v169, v171\n"
      "v_mfma_f32_16x16x32_bf16 v[124:127], a[4:7], v[56:59], v[124:127]\n"
      "s_waitcnt lgkmcnt(4)\n"
      "ds_read_b128 v[88:91], v0 offset:29696\n"
      "v_mul_f32 v161, v133, v161\n"
      "v_mfma_f32_16x16x32_bf16 v[124:127], a[8:11], v[60:63], v[124:127]\n"
      "ds_read_b128 v[112:115], v0 offset:35840\n"
      "v_mul_f32 v163, v135, v163\n"
      "v_mfma_f32_16x16x32_bf16 v[124:127], a[12:15], v[64:67], v[124:127]\n"
      "ds_read_b128 v[100:103], v0 offset:32768\n"
      "v_mul_f32 v139, v139, v163\n"
      "v_mfma_f32_16x16x32_bf16 v[124:127], a[16:19], v[68:71], v[124:127]\n"
      "ds_read_b128 v[104:107], v0 offset:33792\n"
      "v_mul_f32 v131, v131, v159\n"
      "v_mfma_f32_16x16x32_bf16 v[124:127], a[20:23], v[72:75], v[124:127]\n"
      "ds_read_b128 v[108:111], v0 offset:34816\n"
      "v_mfma_f32_16x16x32_bf16 v[124:127], a[24:27], v[76:79], v[124:127]\n"
      "s_waitcnt lgkmcnt(6)\n"
      "ds_read_b128 v[120:123], v0 offset:37888\n"
      "v_sub_f32 v176, -v168, v170\n"
      "v_mfma_f32_16x16x32_bf16 v[124:127], a[28:31], v[80:83], v[124:127]\n"
      "v_rcp_f32 v160, v160\n"
      "v_mfma_f32_16x16x32_bf16 v[124:127], a[32:35], v[84:87], v[124:127]\n"
      "s_waitcnt lgkmcnt(5)\n"
      "ds_read_b128 v[116:119], v0 offset:36864\n"
      "v_fma_f32 v169, -v176, v176, v169\n"
      "v_mfma_f32_16x16x32_bf16 v[124:127], a[36:39], v[88:91], v[124:127]\n"
      "v_mul_f32 v160, v132, v160\n"
      "v_fma_f32 v162, v162, v177, v177\n"
      "v_mfma_f32_16x16x32_bf16 v[124:127], a[40:43], v[92:95], v[124:127]\n"
      "v_mul_f32 v130, v130, v158\n"
      "v_mul_f32 v128, v128, v156\n"
      "v_mfma_f32_16x16x32_bf16 v[124:127], a[44:47], v[96:99], v[124:127]\n"
      "s_waitcnt lgkmcnt(3)\n"
      "v_rcp_f32 v162, v162\n"
      "v_mfma_f32_16x16x32_bf16 v[124:127], a[48:51], v[100:103], v[124:127]\n"
      "v_cvt_pk_bf16_f32 v165, v130, v131\n"
      "v_mul_f32 v162, v134, v162\n"
      "v_mfma_f32_16x16x32_bf16 v[124:127], a[52:55], v[104:107], v[124:127]\n"
      "s_waitcnt lgkmcnt(1)\n"
      "v_mul_f32 v136, v136, v160\n"
      "v_mul_f32 v138, v138, v162\n"
      "v_mfma_f32_16x16x32_bf16 v[124:127], a[56:59], v[108:111], v[124:127]\n"
      "v_mul_f32 v137, v137, v161\n"
      "v_cvt_pk_bf16_f32 v167, v138, v139\n"
      "v_mfma_f32_16x16x32_bf16 v[124:127], a[60:63], v[112:115], v[124:127]\n"
      "s_waitcnt lgkmcnt(0)\n"
      "v_max_f32 v177, 0x3727c5ac, v169\n"
      "v_mfma_f32_16x16x32_bf16 v[124:127], a[64:67], v[116:119], v[124:127]\n"
      "s_add_u32 s37, s37, 165888\n"
      "v_cvt_pk_bf16_f32 v164, v128, v129\n"
      "v_mfma_f32_16x16x32_bf16 v[124:127], a[68:71], v[120:123], v[124:127]\n"
      "v_cvt_pk_bf16_f32 v166, v136, v137\n"
      "v_add_u32 v11, 1, v11\n"
      "v_mfma_f32_16x16x32_bf16 v[132:135], a[144:147], v[52:55], 0\n"
      "buffer_store_dwordx4 v[164:167], v0, s[8:11], s37 offen sc1\n"
      "v_mfma_f32_16x16x32_bf16 v[132:135], a[148:151], v[56:59], v[132:135]\n"
      "v_rsq_f32 v170, v177\n"
      "v_mfma_f32_16x16x32_bf16 v[132:135], a[152:155], v[60:63], v[132:135]\n"
      "v_mul_f32 v178, 0xc0135761, v170\n"
      "v_mul_f32 v170, v170, v170\n"
      "v_mfma_f32_16x16x32_bf16 v[132:135], a[156:159], v[64:67], v[132:135]\n"
      "v_mul_f32 v171, 0x3d372713, v178\n"
      "v_mul_f32 v157, v125, v125\n"
      "v_mfma_f32_16x16x32_bf16 v[132:135], a[160:163], v[68:71], v[132:135]\n"
      "v_mul_f32 v179, v171, v170\n"
      "v_mul_f32 v158, v126, v126\n"
      "v_mfma_f32_16x16x32_bf16 v[132:135], a[164:167], v[72:75], v[132:135]\n"
      "v_fma_f32 v157, v157, v179, v178\n"
      "s_mov_b32 s40, s36\n"
      "v_mfma_f32_16x16x32_bf16 v[132:135], a[168:171], v[76:79], v[132:135]\n"
      "v_mul_f32 v157, v125, v157\n"
      "s_add_u32 m0, s38, 61440\n"
      "v_mfma_f32_16x16x32_bf16 v[132:135], a[172:175], v[80:83], v[132:135]\n"
      "v_exp_f32 v157, v157\n"
      "v_mfma_f32_16x16x32_bf16 v[132:135], a[176:179], v[84:87], v[132:135]\n"
      "v_fma_f32 v158, v158, v179, v178\n"
      "v_fma_f32 v157, v157, v177, v177\n"
      "v_mfma_f32_16x16x32_bf16 v[132:135], a[180:183], v[88:91], v[132:135]\n"
      "v_mfma_f32_16x16x32_bf16 v[132:135], a[184:187], v[92:95], v[132:135]\n"
      "v_mul_f32 v156, v124, v124\n"
      "v_mul_f32 v158, v126, v158\n"
      "v_mfma_f32_16x16x32_bf16 v[132:135], a[188:191], v[96:99], v[132:135]\n"
      "v_mul_f32 v159, v127, v127\n"
      "v_fma_f32 v156, v156, v179, v178\n"
      "v_mfma_f32_16x16x32_bf16 v[132:135], a[192:195], v[100:103], v[132:135]\n"
      "v_fma_f32 v159, v159, v179, v178\n"
      "v_mul_f32 v156, v124, v156\n"
      "v_mfma_f32_16x16x32_bf16 v[132:135], a[196:199], v[104:107], v[132:135]\n"
      "v_exp_f32 v158, v158\n"
      "v_mfma_f32_16x16x32_bf16 v[132:135], a[200:203], v[108:111], v[132:135]\n"
      "v_fma_f32 v158, v158, v177, v177\n"
      "v_mul_f32 v159, v127, v159\n"
      "v_mfma_f32_16x16x32_bf16 v[132:135], a[204:207], v[112:115], v[132:135]\n"
      "v_exp_f32 v156, v156\n"
      "v_mfma_f32_16x16x32_bf16 v[132:135], a[208:211], v[116:119], v[132:135]\n"
      "s_add_u32 s42, s42, 165888\n"
      "v_fma_f32 v156, v156, v177, v177\n"
      "v_mfma_f32_16x16x32_bf16 v[132:135], a[212:215], v[120:123], v[132:135]\n"
      "s_waitcnt vmcnt(3)\n"
      "ds_write_b32 v13, v11\n"
      "v_mfma_f32_16x16x32_bf16 v[128:131], a[72:75], v[52:55], 0\n"
      "v_exp_f32 v159, v159\n"
      "v_mfma_f32_16x16x32_bf16 v[128:131], a[76:79], v[56:59], v[128:131]\n"
      "s_waitcnt vmcnt(2)\n"
      "v_fma_f32 v159, v159, v177, v177\n"
      "v_cmp_ne_u32 vcc, s21, v18\n"
      "v_mfma_f32_16x16x32_bf16 v[128:131], a[80:83], v[60:63], v[128:131]\n"
      "v_mul_f32 v161, v133, v133\n"
      "v_fma_f32 v161, v161, v179, v178\n"
      "v_mfma_f32_16x16x32_bf16 v[128:131], a[84:87], v[64:67], v[128:131]\n"
      "v_mul_f32 v163, v135, v135\n"
      "v_mul_f32 v161, v133, v161\n"
      "v_mfma_f32_16x16x32_bf16 v[128:131], a[88:91], v[68:71], v[128:131]\n"
      "v_mul_f32 v160, v132, v132\n"
      "v_fma_f32 v163, v163, v179, v178\n"
      "v_mfma_f32_16x16x32_bf16 v[128:131], a[92:95], v[72:75], v[128:131]\n"
      "s_cbranch_vccnz .Lsp_g0_6\n"
      ".Lbk_g0_6:\n"
      "v_rcp_f32 v156, v156\n"
      "v_mfma_f32_16x16x32_bf16 v[128:131], a[96:99], v[76:79], v[128:131]\n"
      "buffer_load_dwordx4 v0, s[8:11], s40 offen offset:0 sc1 lds\n"
      "v_mfma_f32_16x16x32_bf16 v[128:131], a[100:103], v[80:83], v[128:131]\n"
      "v_rcp_f32 v159, v159\n"
      "v_mfma_f32_16x16x32_bf16 v[128:131], a[104:107], v[84:87], v[128:131]\n"
      "ds_read_b32 v12, v14\n"
      "v_fma_f32 v160, v160, v179, v178\n"
      "v_mfma_f32_16x16x32_bf16 v[128:131], a[108:111], v[88:91], v[128:131]\n"
      "s_add_u32 s36, s36, 24576\n"
      "v_mul_f32 v162, v134, v134\n"
      "v_mul_f32 v163, v135, v163\n"
      "v_mfma_f32_16x16x32_bf16 v[128:131], a[112:115], v[92:95], v[128:131]\n"
      "v_exp_f32 v163, v163\n"
      "v_mfma_f32_16x16x32_bf16 v[128:131], a[116:119], v[96:99], v[128:131]\n"
      "buffer_load_dwordx4 v0, s[8:11], s40 offen offset:1024 sc1 lds\n"
      "v_mfma_f32_16x16x32_bf16 v[128:131], a[120:123], v[100:103], v[128:131]\n"
      "v_rcp_f32 v158, v158\n"
      "v_mfma_f32_16x16x32_bf16 v[128:131], a[124:127], v[104:107], v[128:131]\n"
      "s_waitcnt lgkmcnt(0)\n"
      "v_mul_f32 v160, v132, v160\n"
      "v_cmp_gt_i32 vcc, v11, v12\n"
      "v_mfma_f32_16x16x32_bf16 v[128:131], a[128:131], v[108:111], v[128:131]\n"
      "v_fma_f32 v162, v162, v179, v178\n"
      "v_mul_f32 v162, v134, v162\n"
      "v_mfma_f32_16x16x32_bf16 v[128:131], a[132:135], v[112:115], v[128:131]\n"
      "buffer_load_dwordx4 v0, s[8:11], s40 offen offset:2048 sc1 lds\n"
      "v_mfma_f32_16x16x32_bf16 v[128:131], a[136:139], v[116:119], v[128:131]\n"
      "v_exp_f32 v162, v162\n"
      "v_mfma_f32_16x16x32_bf16 v[128:131], a[140:143], v[120:123], v[128:131]\n"
      "s_cbranch_vccnz .Lsp_gy0_7\n"
      ".Lbk_gy0_7:\n"
      "ds_read2st64_b64 v[168:171], v4 offset0:80 offset1:81\n"
      "v_mfma_f32_16x16x32_bf16 v[136:139], a[216:219], v[52:55], 0\n"
      "ds_read_b128 v[52:55], v17 offset:0\n"
      "v_fma_f32 v163, v163, v177, v177\n"
      "v_mfma_f32_16x16x32_bf16 v[136:139], a[220:223], v[56:59], v[136:139]\n"
      "v_rcp_f32 v157, v157\n"
      "v_mfma_f32_16x16x32_bf16 v[136:139], a[224:227], v[60:63], v[136:139]\n"
      "buffer_load_dwordx4 v0, s[8:11], s40 offen offset:3072 sc1 lds\n"
      "v_mfma_f32_16x16x32_bf16 v[136:139], a[228:231], v[64:67], v[136:139]\n"
      "s_add_u32 m0, s39, 61440\n"
      "ds_read_b128 v[64:67], v17 offset:3072\n"
      "v_mul_f32 v158, v126, v158\n"
      "v_mfma_f32_16x16x32_bf16 v[136:139], a[232:235], v[68:71], v[136:139]\n"
      "s_min_u32 s36, s36, s6\n"
      "v_rcp_f32 v163, v163\n"
      "v_mfma_f32_16x16x32_bf16 v[136:139], a[236:239], v[72:75], v[136:139]\n"
      "ds_read_b128 v[56:59], v17 offset:1024\n"
      "v_mul_f32 v157, v125, v157\n"
      "v_mfma_f32_16x16x32_bf16 v[136:139], a[240:243], v[76:79], v[136:139]\n"
      "buffer_load_dwordx4 v1, s[8:11], s40 offen offset:0 sc1 lds\n"
      "v_mfma_f32_16x16x32_bf16 v[136:139], a[244:247], v[80:83], v[136:139]\n"
      "v_exp_f32 v161, v161\n"
      "v_mfma_f32_16x16x32_bf16 v[136:139], a[248:251], v[84:87], v[136:139]\n"
      "ds_read_b128 v[68:71], v17 offset:4096\n"
      "ds_read_b128 v[76:79], v17 offset:6144\n"
      "v_mfma_f32_16x16x32_bf16 v[136:139], a[252:255], v[88:91], v[136:139]\n"
      "v_exp_f32 v160, v160\n"
      "v_mfma_f32_16x16x32_bf16 v[136:139], v[20:23], v[92:95], v[136:139]\n"
      "ds_read_b128 v[72:75], v17 offset:5120\n"
      "v_mul_f32 v129, v129, v157\n"
      "v_mfma_f32_16x16x32_bf16 v[136:139], v[24:27], v[96:99], v[136:139]\n"
      "buffer_load_dword v18, v2, s[8:11], s36 offen sc1\n"
      "v_mfma_f32_16x16x32_bf16 v[136:139], v[28:31], v[100:103], v[136:139]\n"
      "ds_read_b128 v[60:63], v17 offset:2048\n"
      "v_fma_f32 v161, v161, v177, v177\n"
      "v_mfma_f32_16x16x32_bf16 v[136:139], v[32:35], v[104:107], v[136:139]\n"
      "s_waitcnt vmcnt(6) lgkmcnt(4)\n"
      "buffer_store_dword v15, v3, s[8:11], s42 offen sc1\n"
      "v_mfma_f32_16x16x32_bf16 v[136:139], v[36:39], v[108:111], v[136:139]\n"
      "ds_read_b128 v[84:87], v17 offset:8192\n"
      "v_fma_f32 v160, v160, v177, v177\n"
      "v_mfma_f32_16x16x32_bf16 v[136:139], v[40:43], v[112:115], v[136:139]\n"
      "v_rcp_f32 v161, v161\n"
      "v_mfma_f32_16x16x32_bf16 v[136:139], v[44:47], v[116:119], v[136:139]\n"
      "ds_read_b128 v[80:83], v17 offset:7168\n"
      "v_mul_f32 v159, v127, v159\n"
      "v_mfma_f32_16x16x32_bf16 v[136:139], v[48:51], v[120:123], v[136:139]\n"
      "ds_read_b128 v[96:99], v17 offset:11264\n"
      "v_mul_f32 v156, v124, v156\n"
      "v_mfma_f32_16x16x32_bf16 v[124:127], a[0:3], v[52:55], 0\n"
      "ds_read_b128 v[92:95], v17 offset:10240\n"
      "v_add_f32 v169, v169, v171\n"
      "v_mfma_f32_16x16x32_bf16 v[124:127], a[4:7], v[56:59], v[124:127]\n"
      "s_waitcnt lgkmcnt(4)\n"
      "ds_read_b128 v[88:91], v17 offset:9216\n"
      "v_mul_f32 v161, v133, v161\n"
      "v_mfma_f32_16x16x32_bf16 v[124:127], a[8:11], v[60:63], v[124:127]\n"
      "ds_read_b128 v[112:115], v17 offset:15360\n"
      "v_mul_f32 v163, v135, v163\n"
      "v_mfma_f32_16x16x32_bf16 v[124:127], a[12:15], v[64:67], v[124:127]\n"
      "ds_read_b128 v[100:103], v17 offset:12288\n"
      "v_mul_f32 v139, v139, v163\n"
      "v_mfma_f32_16x16x32_bf16 v[124:127], a[16:19], v[68:71], v[124:127]\n"
      "ds_read_b128 v[104:107], v17 offset:13312\n"
      "v_mul_f32 v131, v131, v159\n"
      "v_mfma_f32_16x16x32_bf16 v[124:127], a[20:23], v[72:75], v[124:127]\n"
      "ds_read_b128 v[108:111], v17 offset:14336\n"
      "v_mfma_f32_16x16x32_bf16 v[124:127], a[24:27], v[76:79], v[124:127]\n"
      "s_waitcnt lgkmcnt(6)\n"
      "ds_read_b128 v[120:123], v17 offset:17408\n"
      "v_sub_f32 v176, -v168, v170\n"
      "v_mfma_f32_16x16x32_bf16 v[124:127], a[28:31], v[80:83], v[124:127]\n"
      "v_rcp_f32 v160, v160\n"
      "v_mfma_f32_16x16x32_bf16 v[124:127], a[32:35], v[84:87], v[124:127]\n"
      "s_waitcnt lgkmcnt(5)\n"
      "ds_read_b128 v[116:119], v17 offset:16384\n"
      "v_fma_f32 v169, -v176, v176, v169\n"
      "v_mfma_f32_16x16x32_bf16 v[124:127], a[36:39], v[88:91], v[124:127]\n"
      "v_mul_f32 v160, v132, v160\n"
      "v_fma_f32 v162, v162, v177, v177\n"
      "v_mfma_f32_16x16x32_bf16 v[124:127], a[40:43], v[92:95], v[124:127]\n"
      "v_mul_f32 v130, v130, v158\n"
      "v_mul_f32 v128, v128, v156\n"
      "v_mfma_f32_16x16x32_bf16 v[124:127], a[44:47], v[96:99], v[124:127]\n"
      "s_waitcnt lgkmcnt(3)\n"
      "v_rcp_f32 v162, v162\n"
      "v_mfma_f32_16x16x32_bf16 v[124:127], a[48:51], v[100:103], v[124:127]\n"
      "v_cvt_pk_bf16_f32 v165, v130, v131\n"
      "v_mul_f32 v162, v134, v162\n"
      "v_mfma_f32_16x16x32_bf16 v[124:127], a[52:55], v[104:107], v[124:127]\n"
      "s_waitcnt lgkmcnt(1)\n"
      "v_mul_f32 v136, v136, v160\n"
      "v_mul_f32 v138, v138, v162\n"
      "v_mfma_f32_16x16x32_bf16 v[124:127], a[56:59], v[108:111], v[124:127]\n"
      "v_mul_f32 v137, v137, v161\n"
      "v_cvt_pk_bf16_f32 v167, v138, v139\n"
      "v_mfma_f32_16x16x32_bf16 v[124:127], a[60:63], v[112:115], v[124:127]\n"
      "s_waitcnt lgkmcnt(0)\n"
      "v_max_f32 v177, 0x3727c5ac, v169\n"
      "v_mfma_f32_16x16x32_bf16 v[124:127], a[64:67], v[116:119], v[124:127]\n"
      "s_add_u32 s37, s37, 165888\n"
      "v_cvt_pk_bf16_f32 v164, v128, v129\n"
      "v_mfma_f32_16x16x32_bf16 v[124:127], a[68:71], v[120:123], v[124:127]\n"
      "v_cvt_pk_bf16_f32 v166, v136, v137\n"
      "v_add_u32 v11, 1, v11\n"
      "v_mfma_f32_16x16x32_bf16 v[132:135], a[144:147], v[52:55], 0\n"
      "buffer_store_dwordx4 v[164:167], v0, s[8:11], s37 offen sc1\n"
      "v_mfma_f32_16x16x32_bf16 v[132:135], a[148:151], v[56:59], v[132:135]\n"
      "v_rsq_f32 v170, v177\n"
      "v_mfma_f32_16x16x32_bf16 v[132:135], a[152:155], v[60:63], v[132:135]\n"
      "v_mul_f32 v178, 0xc0135761, v170\n"
      "v_mul_f32 v170, v170, v170\n"
      "v_mfma_f32_16x16x32_bf16 v[132:135], a[156:159], v[64:67], v[132:135]\n"
      "v_mul_f32 v171, 0x3d372713, v178\n"
      "v_mul_f32 v157, v125, v125\n"
      "v_mfma_f32_16x16x32_bf16 v[132:135], a[160:163], v[68:71], v[132:135]\n"
      "v_mul_f32 v179, v171, v170\n"
      "v_mul_f32 v158, v126, v126\n"
      "v_mfma_f32_16x16x32_bf16 v[132:135], a[164:167], v[72:75], v[132:135]\n"
      "v_fma_f32 v157, v157, v179, v178\n"
      "s_mov_b32 s40, s36\n"
      "v_mfma_f32_16x16x32_bf16 v[132:135], a[168:171], v[76:79], v[132:135]\n"
      "v_mul_f32 v157, v125, v157\n"
      "s_add_u32 m0, s38, 0\n"
      "v_mfma_f32_16x16x32_bf16 v[132:135], a[172:175], v[80:83], v[132:135]\n"
      "v_exp_f32 v157, v157\n"
      "v_mfma_f32_16x16x32_bf16 v[132:135], a[176:179], v[84:87], v[132:135]\n"
      "v_fma_f32 v158, v158, v179, v178\n"
      "v_fma_f32 v157, v157, v177, v177\n"
      "v_mfma_f32_16x16x32_bf16 v[132:135], a[180:183], v[88:91], v[132:135]\n"
      "v_mfma_f32_16x16x32_bf16 v[132:135], a[184:187], v[92:95], v[132:135]\n"
      "v_mul_f32 v156, v124, v124\n"
      "v_mul_f32 v158, v126, v158\n"
      "v_mfma_f32_16x16x32_bf16 v[132:135], a[188:191], v[96:99], v[132:135]\n"
      "v_mul_f32 v159, v127, v127\n"
      "v_fma_f32 v156, v156, v179, v178\n"
      "v_mfma_f32_16x16x32_bf16 v[132:135], a[192:195], v[100:103], v[132:135]\n"
      "v_fma_f32 v159, v159, v179, v178\n"
      "v_mul_f32 v156, v124, v156\n"
      "v_mfma_f32_16x16x32_bf16 v[132:135], a[196:199], v[104:107], v[132:135]\n"
      "v_exp_f32 v158, v158\n"
      "v_mfma_f32_16x16x32_bf16 v[132:135], a[200:203], v[108:111], v[132:135]\n"
      "v_fma_f32 v158, v158, v177, v177\n"
      "v_mul_f32 v159, v127, v159\n"
      "v_mfma_f32_16x16x32_bf16 v[132:135], a[204:207], v[112:115], v[132:135]\n"
      "v_exp_f32 v156, v156\n"
      "v_mfma_f32_16x16x32_bf16 v[132:135], a[208:211], v[116:119], v[132:135]\n"
      "s_add_u32 s42, s42, 165888\n"
      "v_fma_f32 v156, v156, v177, v177\n"
      "v_mfma_f32_16x16x32_bf16 v[132:135], a[212:215], v[120:123], v[132:135]\n"
      "s_waitcnt vmcnt(3)\n"
      "ds_write_b32 v13, v11\n"
      "v_mfma_f32_16x16x32_bf16 v[128:131], a[72:75], v[52:55], 0\n"
      "v_exp_f32 v159, v159\n"
      "v_mfma_f32_16x16x32_bf16 v[128:131], a[76:79], v[56:59], v[128:131]\n"
      "s_waitcnt vmcnt(2)\n"
      "v_fma_f32 v159, v159, v177, v177\n"
      "v_cmp_ne_u32 vcc, s21, v18\n"
      "v_mfma_f32_16x16x32_bf16 v[128:131], a[80:83], v[60:63], v[128:131]\n"
      "v_mul_f32 v161, v133, v133\n"
      "v_fma_f32 v161, v161, v179, v178\n"
      "v_mfma_f32_16x16x32_bf16 v[128:131], a[84:87], v[64:67], v[128:131]\n"
      "v_mul_f32 v163, v135, v135\n"
      "v_mul_f32 v161, v133, v161\n"
      "v_mfma_f32_16x16x32_bf16 v[128:131], a[88:91], v[68:71], v[128:131]\n"
      "v_mul_f32 v160, v132, v132\n"
      "v_fma_f32 v163, v163, v179, v178\n"
      "v_mfma_f32_16x16x32_bf16 v[128:131], a[92:95], v[72:75], v[128:131]\n"
      "s_cbranch_vccnz .Lsp_g0_8\n"
      ".Lbk_g0_8:\n"
      "v_rcp_f32 v156, v156\n"
      "v_mfma_f32_16x16x32_bf16 v[128:131], a[96:99], v[76:79], v[128:131]\n"
      "buffer_load_dwordx4 v0, s[8:11], s40 offen offset:0 sc1 lds\n"
      "v_mfma_f32_16x16x32_bf16 v[128:131], a[100:103], v[80:83], v[128:131]\n"
      "v_rcp_f32 v159, v159\n"
      "v_mfma_f32_16x16x32_bf16 v[128:131], a[104:107], v[84:87], v[128:131]\n"
      "ds_read_b32 v12, v14\n"
      "v_fma_f32 v160, v160, v179, v178\n"
      "v_mfma_f32_16x16x32_bf16 v[128:131], a[108:111], v[88:91], v[128:131]\n"
      "s_add_u32 s36, s36, 24576\n"
      "v_mul_f32 v162, v134, v134\n"
      "v_mul_f32 v163, v135, v163\n"
      "v_mfma_f32_16x16x32_bf16 v[128:131], a[112:115], v[92:95], v[128:131]\n"
      "v_exp_f32 v163, v163\n"
      "v_mfma_f32_16x16x32_bf16 v[128:131], a[116:119], v[96:99], v[128:131]\n"
      "buffer_load_dwordx4 v0, s[8:11], s40 offen offset:1024 sc1 lds\n"
      "v_mfma_f32_16x16x32_bf16 v[128:131], a[120:123], v[100:103], v[128:131]\n"
      "v_rcp_f32 v158, v158\n"
      "v_mfma_f32_16x16x32_bf16 v[128:131], a[124:127], v[104:107], v[128:131]\n"
      "s_waitcnt lgkmcnt(0)\n"
      "v_mul_f32_e64 v160, v132, v160\n"
      "v_cmp_gt_i32 vcc, v11, v12\n"
      "v_mfma_f32_16x16x32_bf16 v[128:131], a[128:131], v[108:111], v[128:131]\n"
      "v_fma_f32 v162, v162, v179, v178\n"
      "v_mul_f32_e64 v162, v134, v162\n"
      "v_mfma_f32_16x16x32_bf16 v[128:131], a[132:135], v[112:115], v[128:131]\n"
      "buffer_load_dwordx4 v0, s[8:11], s40 offen offset:2048 sc1 lds\n"
      "v_mfma_f32_16x16x32_bf16 v[128:131], a[136:139], v[116:119], v[128:131]\n"
      "v_exp_f32_e64 v162, v162\n"
      "v_mfma_f32_16x16x32_bf16 v[128:131], a[140:143], v[120:123], v[128:131]\n"
      "s_cbranch_vccnz .Lsp_gy0_9\n"
      ".Lbk_gy0_9:\n"
      "ds_read2st64_b64 v[168:171], v4 offset0:120 offset1:121\n"
      "v_mfma_f32_16x16x32_bf16 v[136:139], a[216:219], v[52:55], 0\n"
      "ds_read_b128 v[52:55], v17 offset:20480\n"
      "v_fma_f32 v163, v163, v177, v177\n"
      "v_mfma_f32_16x16x32_bf16 v[136:139], a[220:223], v[56:59], v[136:139]\n"
      "v_rcp_f32 v157, v157\n"
      "v_mfma_f32_16x16x32_bf16 v[136:139], a[224:227], v[60:63], v[136:139]\n"
      "buffer_load_dwordx4 v0, s[8:11], s40 offen offset:3072 sc1 lds\n"
      "v_mfma_f32_16x16x32_bf16 v[136:139], a[228:231], v[64:67], v[136:139]\n"
      "s_add_u32 m0, s39, 0\n"
      "ds_read_b128 v[64:67], v17 offset:23552\n"
      "v_mul_f32 v158, v126, v158\n"
      "v_mfma_f32_16x16x32_bf16 v[136:139], a[232:235], v[68:71], v[136:139]\n"
      "s_min_u32 s36, s36, s6\n"
      "v_rcp_f32 v163, v163\n"
      "v_mfma_f32_16x16x32_bf16 v[136:139], a[236:239], v[72:75], v[136:139]\n"
      "ds_read_b128 v[56:59], v17 offset:21504\n"
      "v_mul_f32 v157, v125, v157\n"
      "v_mfma_f32_16x16x32_bf16 v[136:139], a[240:243], v[76:79], v[136:139]\n"
      "buffer_load_dwordx4 v1, s[8:11], s40 offen offset:0 sc1 lds\n"
      "v_mfma_f32_16x16x32_bf16 v[136:139], a[244:247], v[80:83], v[136:139]\n"
      "v_exp_f32 v161, v161\n"
      "v_mfma_f32_16x16x32_bf16 v[136:139], a[248:251], v[84:87], v[136:139]\n"
      "ds_read_b128 v[68:71], v17 offset:24576\n"
      "ds_read_b128 v[76:79], v17 offset:26624\n"
      "v_mfma_f32_16x16x32_bf16 v[136:139], a[252:255], v[88:91], v[136:139]\n"
      "v_exp_f32 v160, v160\n"
      "v_mfma_f32_16x16x32_bf16 v[136:139], v[20:23], v[92:95], v[136:139]\n"
      "ds_read_b128 v[72:75], v17 offset:25600\n"
      "v_mul_f32 v129, v129, v157\n"
      "v_mfma_f32_16x16x32_bf16 v[136:139], v[24:27], v[96:99], v[136:139]\n"
      "buffer_load_dword v18, v2, s[8:11], s36 offen sc1\n"
      "v_mfma_f32_16x16x32_bf16 v[136:139], v[28:31], v[100:103], v[136:139]\n"
      "ds_read_b128 v[60:63], v17 offset:22528\n"
      "v_fma_f32 v161, v161, v177, v177\n"
      "v_mfma_f32_16x16x32_bf16 v[136:139], v[32:35], v[104:107], v[136:139]\n"
      "s_waitcnt vmcnt(6) lgkmcnt(4)\n"
      "buffer_store_dword v15, v3, s[8:11], s42 offen sc1\n"
      "v_mfma_f32_16x16x32_bf16 v[136:139], v[36:39], v[108:111], v[136:139]\n"
      "ds_read_b128 v[84:87], v17 offset:28672\n"
      "v_fma_f32 v160, v160, v177, v177\n"
      "v_mfma_f32_16x16x32_bf16 v[136:139], v[40:43], v[112:115], v[136:139]\n"
      "v_rcp_f32 v161, v161\n"
      "v_mfma_f32_16x16x32_bf16 v[136:139], v[44:47], v[116:119], v[136:139]\n"
      "ds_read_b128 v[80:83], v17 offset:27648\n"
      "v_mul_f32 v159, v127, v159\n"
      "v_mfma_f32_16x16x32_bf16 v[136:139], v[48:51], v[120:123], v[136:139]\n"
      "ds_read_b128 v[96:99], v17 offset:31744\n"
      "v_mul_f32 v156, v124, v156\n"
      "v_mfma_f32_16x16x32_bf16 v[124:127], a[0:3], v[52:55], 0\n"
      "ds_read_b128 v[92:95], v17 offset:30720\n"
      "v_add_f32 v169, v169, v171\n"
      "v_mfma_f32_16x16x32_bf16 v[124:127], a[4:7], v[56:59], v[124:127]\n"
      "s_waitcnt lgkmcnt(4)\n"
      "ds_read_b128 v[88:91], v17 offset:29696\n"
      "v_mul_f32 v161, v133, v161\n"
      "v_mfma_f32_16x16x32_bf16 v[124:127], a[8:11], v[60:63], v[124:127]\n"
      "ds_read_b128 v[112:115], v17 offset:35840\n"
      "v_mul_f32 v163, v135, v163\n"
      "v_mfma_f32_16x16x32_bf16 v[124:127], a[12:15], v[64:67], v[124:127]\n"
      ".Lg_p1:\n"
      "s_cbranch_execz .Lg_pr\n"
      "ds_read_b128 v[100:103], v17 offset:32768\n"
      "v_mul_f32 v139, v139, v163\n"
      "v_mfma_f32_16x16x32_bf16 v[124:127], a[16:19], v[68:71], v[124:127]\n"
      "ds_read_b128 v[104:107], v17 offset:33792\n"
      "v_mul_f32 v131, v131, v159\n"
      "v_mfma_f32_16x16x32_bf16 v[124:127], a[20:23], v[72:75], v[124:127]\n"
      "ds_read_b128 v[108:111], v17 offset:34816\n"
      "v_mfma_f32_16x16x32_bf16 v[124:127], a[24:27], v[76:79], v[124:127]\n"
      "s_waitcnt lgkmcnt(6)\n"
      "ds_read_b128 v[120:123], v17 offset:37888\n"
      "v_sub_f32 v176, -v168, v170\n"
      "v_mfma_f32_16x16x32_bf16 v[124:127], a[28:31], v[80:83], v[124:127]\n"
      "v_rcp_f32 v160, v160\n"
      "v_mfma_f32_16x16x32_bf16 v[124:127], a[32:35], v[84:87], v[124:127]\n"
      "s_waitcnt lgkmcnt(5)\n"
      "ds_read_b128 v[116:119], v17 offset:36864\n"
      "v_fma_f32 v169, -v176, v176, v169\n"
      "v_mfma_f32_16x16x32_bf16 v[124:127], a[36:39], v[88:91], v[124:127]\n"
      "v_mul_f32 v160, v132, v160\n"
      "v_fma_f32 v162, v162, v177, v177\n"
      "v_mfma_f32_16x16x32_bf16 v[124:127], a[40:43], v[92:95], v[124:127]\n"
      "v_mul_f32 v130, v130, v158\n"
      "v_mul_f32 v128, v128, v156\n"
      "v_mfma_f32_16x16x32_bf16 v[124:127], a[44:47], v[96:99], v[124:127]\n"
      "s_waitcnt lgkmcnt(3)\n"
      "v_rcp_f32 v162, v162\n"
      "v_mfma_f32_16x16x32_bf16 v[124:127], a[48:51], v[100:103], v[124:127]\n"
      "v_cvt_pk_bf16_f32 v165, v130, v131\n"
      "v_mul_f32 v162, v134, v162\n"
      "v_mfma_f32_16x16x32_bf16 v[124:127], a[52:55], v[104:107], v[124:127]\n"
      "s_waitcnt lgkmcnt(1)\n"
      "v_mul_f32 v136, v136, v160\n"
      "v_mul_f32 v138, v138, v162\n"
      "v_mfma_f32_16x16x32_bf16 v[124:127], a[56:59], v[108:111], v[124:127]\n"
      "v_mul_f32 v137, v137, v161\n"
      "v_cvt_pk_bf16_f32 v167, v138, v139\n"
      "v_mfma_f32_16x16x32_bf16 v[124:127], a[60:63], v[112:115], v[124:127]\n"
      "s_waitcnt lgkmcnt(0)\n"
      "v_max_f32 v177, 0x3727c5ac, v169\n"
      "v_mfma_f32_16x16x32_bf16 v[124:127], a[64:67], v[116:119], v[124:127]\n"
      "s_add_u32 s37, s37, 165888\n"
      "v_cvt_pk_bf16_f32 v164, v128, v129\n"
      "v_mfma_f32_16x16x32_bf16 v[124:127], a[68:71], v[120:123], v[124:127]\n"
      "v_cvt_pk_bf16_f32 v166, v136, v137\n"
      "v_add_u32 v11, 1, v11\n"
      "v_mfma_f32_16x16x32_bf16 v[132:135], a[144:147], v[52:55], 0\n"
      "buffer_store_dwordx4 v[164:167], v0, s[8:11], s37 offen sc1\n"
      "v_mfma_f32_16x16x32_bf16 v[132:135], a[148:151], v[56:59], v[132:135]\n"
      "v_rsq_f32 v170, v177\n"
      "v_mfma_f32_16x16x32_bf16 v[132:135], a[152:155], v[60:63], v[132:135]\n"
      "v_mul_f32 v178, 0xc0135761, v170\n"
      "v_mul_f32 v170, v170, v170\n"
      "v_mfma_f32_16x16x32_bf16 v[132:135], a[156:159], v[64:67], v[132:135]\n"
      "v_mul_f32 v171, 0x3d372713, v178\n"
      "v_mul_f32 v157, v125, v125\n"
      "v_mfma_f32_16x16x32_bf16 v[132:135], a[160:163], v[68:71], v[132:135]\n"
      "v_mul_f32 v179, v171, v170\n"
      "v_mul_f32 v158, v126, v126\n"
      "v_mfma_f32_16x16x32_bf16 v[132:135], a[164:167], v[72:75], v[132:135]\n"
      "v_fma_f32 v157, v157, v179, v178\n"
      "s_mov_b32 s40, s36\n"
      "v_mfma_f32_16x16x32_bf16 v[132:135], a[168:171], v[76:79], v[132:135]\n"
      "v_mul_f32 v157, v125, v157\n"
      "s_add_u32 m0, s38, 20480\n"
      "v_mfma_f32_16x16x32_bf16 v[132:135], a[172:175], v[80:83], v[132:135]\n"
      "v_exp_f32 v157, v157\n"
      "v_mfma_f32_16x16x32_bf16 v[132:135], a[176:179], v[84:87], v[132:135]\n"
      "v_fma_f32 v158, v158, v179, v178\n"
      "v_fma_f32 v157, v157, v177, v177\n"
      "v_mfma_f32_16x16x32_bf16 v[132:135], a[180:183], v[88:91], v[132:135]\n"
      "v_mfma_f32_16x16x32_bf16 v[132:135], a[184:187], v[92:95], v[132:135]\n"
      "v_mul_f32 v156, v124, v124\n"
      "v_mul_f32 v158, v126, v158\n"
      "v_mfma_f32_16x16x32_bf16 v[132:135], a[188:191], v[96:99], v[132:135]\n"
      "v_mul_f32 v159, v127, v127\n"
      "v_fma_f32 v156, v156, v179, v178\n"
      "v_mfma_f32_16x16x32_bf16 v[132:135], a[192:195], v[100:103], v[132:135]\n"
      "v_fma_f32 v159, v159, v179, v178\n"
      "v_mul_f32 v156, v124, v156\n"
      "v_mfma_f32_16x16x32_bf16 v[132:135], a[196:199], v[104:107], v[132:135]\n"
      "v_exp_f32 v158, v158\n"
      "v_mfma_f32_16x16x32_bf16 v[132:135], a[200:203], v[108:111], v[132:135]\n"
      "v_fma_f32 v158, v158, v177, v177\n"
      "v_mul_f32 v159, v127, v159\n"
      "v_mfma_f32_16x16x32_bf16 v[132:135], a[204:207], v[112:115], v[132:135]\n"
      "v_exp_f32 v156, v156\n"
      "v_mfma_f32_16x16x32_bf16 v[132:135], a[208:211], v[116:119], v[132:135]\n"
      "s_add_u32 s42, s42, 165888\n"
      "v_fma_f32 v156, v156, v177, v177\n"
      "v_mfma_f32_16x16x32_bf16 v[132:135], a[212:215], v[120:123], v[132:135]\n"
      "s_waitcnt vmcnt(3)\n"
      "ds_write_b32 v13, v11\n"
      "v_mfma_f32_16x16x32_bf16 v[128:131], a[72:75], v[52:55], 0\n"
      "v_exp_f32 v159, v159\n"
      "v_mfma_f32_16x16x32_bf16 v[128:131], a[76:79], v[56:59], v[128:131]\n"
      "s_waitcnt vmcnt(2)\n"
      "v_fma_f32 v159, v159, v177, v177\n"
      "v_cmp_ne_u32 vcc, s21, v18\n"
      "v_mfma_f32_16x16x32_bf16 v[128:131], a[80:83], v[60:63], v[128:131]\n"
      "v_mul_f32 v161, v133, v133\n"
      "v_fma_f32 v161, v161, v179, v178\n"
      "v_mfma_f32_16x16x32_bf16 v[128:131], a[84:87], v[64:67], v[128:131]\n"
      "v_mul_f32 v163, v135, v135\n"
      "v_mul_f32 v161, v133, v161\n"
      "v_mfma_f32_16x16x32_bf16 v[128:131], a[88:91], v[68:71], v[128:131]\n"
      "v_mul_f32 v160, v132, v132\n"
      "v_fma_f32 v163, v163, v179, v178\n"
      "v_mfma_f32_16x16x32_bf16 v[128:131], a[92:95], v[72:75], v[128:131]\n"
      "s_cbranch_vccnz .Lsp_g0_10\n"
      ".Lbk_g0_10:\n"
      "v_rcp_f32 v156, v156\n"
      "v_mfma_f32_16x16x32_bf16 v[128:131], a[96:99], v[76:79], v[128:131]\n"
      "buffer_load_dwordx4 v0, s[8:11], s40 offen offset:0 sc1 lds\n"
      "v_mfma_f32_16x16x32_bf16 v[128:131], a[100:103], v[80:83], v[128:131]\n"
      "v_rcp_f32 v159, v159\n"
      "v_mfma_f32_16x16x32_bf16 v[128:131], a[104:107], v[84:87], v[128:131]\n"
      "ds_read_b32 v12, v14\n"
      "v_fma_f32 v160, v160, v179, v178\n"
      "v_mfma_f32_16x16x32_bf16 v[128:131], a[108:111], v[88:91], v[128:131]\n"
      "s_add_u32 s36, s36, 24576\n"
      "v_mul_f32 v162, v134, v134\n"
      "v_mul_f32 v163, v135, v163\n"
      "v_mfma_f32_16x16x32_bf16 v[128:131], a[112:115], v[92:95], v[128:131]\n"
      "v_exp_f32 v163, v163\n"
      "v_mfma_f32_16x16x32_bf16 v[128:131], a[116:119], v[96:99], v[128:131]\n"
      "buffer_load_dwordx4 v0, s[8:11], s40 offen offset:1024 sc1 lds\n"
      "v_mfma_f32_16x16x32_bf16 v[128:131], a[120:123], v[100:103], v[128:131]\n"
      "v_rcp_f32 v158, v158\n"
      "v_mfma_f32_16x16x32_bf16 v[128:131], a[124:127], v[104:107], v[128:131]\n"
      "s_waitcnt lgkmcnt(0)\n"
      "v_mul_f32 v160, v132, v160\n"
      "v_cmp_gt_i32 vcc, v11, v12\n"
      "v_mfma_f32_16x16x32_bf16 v[128:131], a[128:131], v[108:111], v[128:131]\n"
      "v_fma_f32 v162, v162, v179, v178\n"
      "v_mul_f32 v162, v134, v162\n"
      "v_mfma_f32_16x16x32_bf16 v[128:131], a[132:135], v[112:115], v[128:131]\n"
      "buffer_load_dwordx4 v0, s[8:11], s40 offen offset:2048 sc1 lds\n"
      "v_mfma_f32_16x16x32_bf16 v[128:131], a[136:139], v[116:119], v[128:131]\n"
      "v_exp_f32 v162, v162\n"
      "v_mfma_f32_16x16x32_bf16 v[128:131], a[140:143], v[120:123], v[128:131]\n"
      "s_cbranch_vccnz .Lsp_gy0_11\n"
      ".Lbk_gy0_11:\n"
      "ds_read2st64_b64 v[168:171], v4 offset0:0 offset1:1\n"
      "v_mfma_f32_16x16x32_bf16 v[136:139], a[216:219], v[52:55], 0\n"
      "ds_read_b128 v[52:55], v0 offset:0\n"
      "v_fma_f32 v163, v163, v177, v177\n"
      "v_mfma_f32_16x16x32_bf16 v[136:139], a[220:223], v[56:59], v[136:139]\n"
      "v_rcp_f32 v157, v157\n"
      "v_mfma_f32_16x16x32_bf16 v[136:139], a[224:227], v[60:63], v[136:139]\n"
      "buffer_load_dwordx4 v0, s[8:11], s40 offen offset:3072 sc1 lds\n"
      "v_mfma_f32_16x16x32_bf16 v[136:139], a[228:231], v[64:67], v[136:139]\n"
      "s_add_u32 m0, s39, 20480\n"
      "ds_read_b128 v[64:67], v0 offset:3072\n"
      "v_mul_f32 v158, v126, v158\n"
      "v_mfma_f32_16x16x32_bf16 v[136:139], a[232:235], v[68:71], v[136:139]\n"
      "s_min_u32 s36, s36, s6\n"
      "v_rcp_f32 v163, v163\n"
      "v_mfma_f32_16x16x32_bf16 v[136:139], a[236:239], v[72:75], v[136:139]\n"
      "ds_read_b128 v[56:59], v0 offset:1024\n"
      "v_mul_f32 v157, v125, v157\n"
      "v_mfma_f32_16x16x32_bf16 v[136:139], a[240:243], v[76:79], v[136:139]\n"
      "buffer_load_dwordx4 v1, s[8:11], s40 offen offset:0 sc1 lds\n"
      "v_mfma_f32_16x16x32_bf16 v[136:139], a[244:247], v[80:83], v[136:139]\n"
      "v_exp_f32 v161, v161\n"
      "v_mfma_f32_16x16x32_bf16 v[136:139], a[248:251], v[84:87], v[136:139]\n"
      "ds_read_b128 v[68:71], v0 offset:4096\n"
      "ds_read_b128 v[76:79], v0 offset:6144\n"
      "v_mfma_f32_16x16x32_bf16 v[136:139], a[252:255], v[88:91], v[136:139]\n"
      "v_exp_f32 v160, v160\n"
      "v_mfma_f32_16x16x32_bf16 v[136:139], v[20:23], v[92:95], v[136:139]\n"
      "ds_read_b128 v[72:75], v0 offset:5120\n"
      "v_mul_f32 v129, v129, v157\n"
      "v_mfma_f32_16x16x32_bf16 v[136:139], v[24:27], v[96:99], v[136:139]\n"
      "buffer_load_dword v18, v2, s[8:11], s36 offen sc1\n"
      "v_mfma_f32_16x16x32_bf16 v[136:139], v[28:31], v[100:103], v[136:139]\n"
      "ds_read_b128 v[60:63], v0 offset:2048\n"
      "v_fma_f32 v161, v161, v177, v177\n"
      "v_mfma_f32_16x16x32_bf16 v[136:139], v[32:35], v[104:107], v[136:139]\n"
      "s_waitcnt vmcnt(6) lgkmcnt(4)\n"
      "buffer_store_dword v15, v3, s[8:11], s42 offen sc1\n"
      "v_mfma_f32_16x16x32_bf16 v[136:139], v[36:39], v[108:111], v[136:139]\n"
      "ds_read_b128 v[84:87], v0 offset:8192\n"
      "v_fma_f32 v160, v160, v177, v177\n"
      "v_mfma_f32_16x16x32_bf16 v[136:139], v[40:43], v[112:115], v[136:139]\n"
      "v_rcp_f32 v161, v161\n"
      "v_mfma_f32_16x16x32_bf16 v[136:139], v[44:47], v[116:119], v[136:139]\n"
      "ds_read_b128 v[80:83], v0 offset:7168\n"
      "v_mul_f32 v159, v127, v159\n"
      "v_mfma_f32_16x16x32_bf16 v[136:139], v[48:51], v[120:123], v[136:139]\n"
      "ds_read_b128 v[96:99], v0 offset:11264\n"
      "v_mul_f32 v156, v124, v156\n"
      "s_sub_u32 s20, s20, 1\n"
      "s_cbranch_scc0 .Lg_loop\n"
      ".Lg_tail:\n"
      "v_mul_f32 v161, v133, v161\n"
      "v_mul_f32 v163, v135, v163\n"
      "s_nop 4\n"
      "v_mul_f32 v139, v139, v163\n"
      "v_mul_f32 v131, v131, v159\n"
      "v_rcp_f32 v160, v160\n"
      "s_nop 0\n"
      "v_mul_f32 v160, v132, v160\n"
      "v_fma_f32 v162, v162, v177, v177\n"
      "v_mul_f32 v130, v130, v158\n"
      "v_mul_f32 v128, v128, v156\n"
      "v_rcp_f32 v162, v162\n"
      "v_cvt_pk_bf16_f32 v165, v130, v131\n"
      "v_mul_f32 v162, v134, v162\n"
      "v_mul_f32 v136, v136, v160\n"
      "v_mul_f32 v138, v138, v162\n"
      "v_mul_f32 v137, v137, v161\n"
      "v_cvt_pk_bf16_f32 v167, v138, v139\n"
      "s_add_u32 s37, s37, 165888\n"
      "v_cvt_pk_bf16_f32 v164, v128, v129\n"
      "v_cvt_pk_bf16_f32 v166, v136, v137\n"
      "buffer_store_dwordx4 v[164:167], v0, s[8:11], s37 offen sc1\n"
      "s_add_u32 s42, s42, 165888\n"
      "s_waitcnt vmcnt(0)\n"
      "buffer_store_dword v15, v3, s[8:11], s42 offen sc1\n"
      "s_add_u32 s37, s37, 165888\n"
      "s_add_u32 s42, s42, 165888\n"
      "s_endpgm\n"
      ".Lsp_g0_1:\n"
      "s_mov_b32 s22, s23\n"
      ".Lsl_g0_1:\n"
      "buffer_load_dword v18, v2, s[8:11], s40 offen sc1\n"
      "s_waitcnt vmcnt(0)\n"
      "v_cmp_ne_u32 vcc, s21, v18\n"
      "s_cbranch_vccz .Lbk_g0_1\n"
      "s_sleep 4\n"
      "s_sub_u32 s22, s22, 1\n"
      "s_cbranch_scc0 .Lsl_g0_1\n"
      "s_mov_b32 s23, 0\n"
      "s_branch .Lbk_g0_1\n"
      ".Lsp_g0_2:\n"
      "s_mov_b32 s22, s23\n"
      ".Lsl_g0_2:\n"
      "buffer_load_dword v18, v2, s[8:11], s40 offen sc1\n"
      "s_waitcnt vmcnt(0)\n"
      "v_cmp_ne_u32 vcc, s21, v18\n"
      "s_cbranch_vccz .Lbk_g0_2\n"
      "s_sleep 4\n"
      "s_sub_u32 s22, s22, 1\n"
      "s_cbranch_scc0 .Lsl_g0_2\n"
      "s_mov_b32 s23, 0\n"
      "s_branch .Lbk_g0_2\n"
      ".Lsp_gy0_3:\n"
      "s_lshl_b32 s22, s23, 4\n"
      ".Lsl_gy0_3:\n"
      "ds_read_b32 v12, v14\n"
      "s_waitcnt lgkmcnt(0)\n"
      "v_cmp_gt_i32 vcc, v11, v12\n"
      "s_cbranch_vccz .Lbk_gy0_3\n"
      "s_sub_u32 s22, s22, 1\n"
      "s_cbranch_scc0 .Lsl_gy0_3\n"
      "s_mov_b32 s23, 0\n"
      "s_branch .Lbk_gy0_3\n"
      ".Lsp_g0_4:\n"
      "s_mov_b32 s22, s23\n"
      "buffer_load_dword v18, v2, s[8:11], s40 offen sc1\n"
      "s_waitcnt vmcnt(0)\n"
      "v_cmp_ne_u32 vcc, s21, v18\n"
      "s_add_u32 s30, s42, 0\n"
      "buffer_store_dword v15, v3, s[8:11], s30 offen sc1\n"
      "s_cbranch_vccz .Lbk_g0_4\n"
      ".Lsl_g0_4:\n"
      "buffer_load_dword v18, v2, s[8:11], s40 offen sc1\n"
      "s_waitcnt vmcnt(0)\n"
      "v_cmp_ne_u32 vcc, s21, v18\n"
      "s_cbranch_vccz .Lbk_g0_4\n"
      "s_sleep 4\n"
      "s_sub_u32 s22, s22, 1\n"
      "s_cbranch_scc0 .Lsl_g0_4\n"
      "s_mov_b32 s23, 0\n"
      "s_branch .Lbk_g0_4\n"
      ".Lsp_gy0_5:\n"
      "s_lshl_b32 s22, s23, 4\n"
      ".Lsl_gy0_5:\n"
      "ds_read_b32 v12, v14\n"
      "s_waitcnt lgkmcnt(0)\n"
      "v_cmp_gt_i32 vcc, v11, v12\n"
      "s_cbranch_vccz .Lbk_gy0_5\n"
      "s_sub_u32 s22, s22, 1\n"
      "s_cbranch_scc0 .Lsl_gy0_5\n"
      "s_mov_b32 s23, 0\n"
      "s_branch .Lbk_gy0_5\n"
      ".Lsp_g0_6:\n"
      "s_mov_b32 s22, s23\n"
      "buffer_load_dword v18, v2, s[8:11], s40 offen sc1\n"
      "s_waitcnt vmcnt(0)\n"
      "v_cmp_ne_u32 vcc, s21, v18\n"
      "s_add_u32 s30, s42, 0\n"
      "buffer_store_dword v15, v3, s[8:11], s30 offen sc1\n"
      "s_cbranch_vccz .Lbk_g0_6\n"
      ".Lsl_g0_6:\n"
      "buffer_load_dword v18, v2, s[8:11], s40 offen sc1\n"
      "s_waitcnt vmcnt(0)\n"
      "v_cmp_ne_u32 vcc, s21, v18\n"
      "s_cbranch_vccz .Lbk_g0_6\n"
      "s_sleep 4\n"
      "s_sub_u32 s22, s22, 1\n"
      "s_cbranch_scc0 .Lsl_g0_6\n"
      "s_mov_b32 s23, 0\n"
      "s_branch .Lbk_g0_6\n"
      ".Lsp_gy0_7:\n"
      "s_lshl_b32 s22, s23, 4\n"
      ".Lsl_gy0_7:\n"
      "ds_read_b32 v12, v14\n"
      "s_waitcnt lgkmcnt(0)\n"
      "v_cmp_gt_i32 vcc, v11, v12\n"
      "s_cbranch_vccz .Lbk_gy0_7\n"
      "s_sub_u32 s22, s22, 1\n"
      "s_cbranch_scc0 .Lsl_gy0_7\n"
      "s_mov_b32 s23, 0\n"
      "s_branch .Lbk_gy0_7\n"
      ".Lsp_g0_8:\n"
      "s_mov_b32 s22, s23\n"
      "buffer_load_dword v18, v2, s[8:11], s40 offen sc1\n"
      "s_waitcnt vmcnt(0)\n"
      "v_cmp_ne_u32 vcc, s21, v18\n"
      "s_add_u32 s30, s42, 0\n"
      "buffer_store_dword v15, v3, s[8:11], s30 offen sc1\n"
      "s_cbranch_vccz .Lbk_g0_8\n"
      ".Lsl_g0_8:\n"
      "buffer_load_dword v18, v2, s[8:11], s40 offen sc1\n"
      "s_waitcnt vmcnt(0)\n"
      "v_cmp_ne_u32 vcc, s21, v18\n"
      "s_cbranch_vccz .Lbk_g0_8\n"
      "s_sleep 4\n"
      "s_sub_u32 s22, s22, 1\n"
      "s_cbranch_scc0 .Lsl_g0_8\n"
      "s_mov_b32 s23, 0\n"
      "s_branch .Lbk_g0_8\n"
      ".Lsp_gy0_9:\n"
      "s_lshl_b32 s22, s23, 4\n"
      ".Lsl_gy0_9:\n"
      "ds_read_b32 v12, v14\n"
      "s_waitcnt lgkmcnt(0)\n"
      "v_cmp_gt_i32 vcc, v11, v12\n"
      "s_cbranch_vccz .Lbk_gy0_9\n"
      "s_sub_u32 s22, s22, 1\n"
      "s_cbranch_scc0 .Lsl_gy0_9\n"
      "s_mov_b32 s23, 0\n"
      "s_branch .Lbk_gy0_9\n"
      ".Lsp_g0_10:\n"
      "s_mov_b32 s22, s23\n"
      "buffer_load_dword v18, v2, s[8:11], s40 offen sc1\n"
      "s_waitcnt vmcnt(0)\n"
      "v_cmp_ne_u32 vcc, s21, v18\n"
      "s_add_u32 s30, s42, 0\n"
      "buffer_store_dword v15, v3, s[8:11], s30 offen sc1\n"
      "s_cbranch_vccz .Lbk_g0_10\n"
      ".Lsl_g0_10:\n"
      "buffer_load_dword v18, v2, s[8:11], s40 offen sc1\n"
      "s_waitcnt vmcnt(0)\n"
      "v_cmp_ne_u32 vcc, s21, v18\n"
      "s_cbranch_vccz .Lbk_g0_10\n"
      "s_sleep 4\n"
      "s_sub_u32 s22, s22, 1\n"
      "s_cbranch_scc0 .Lsl_g0_10\n"
      "s_mov_b32 s23, 0\n"
      "s_branch .Lbk_g0_10\n"
      ".Lsp_gy0_11:\n"
      "s_lshl_b32 s22, s23, 4\n"
      ".Lsl_gy0_11:\n"
      "ds_read_b32 v12, v14\n"
      "s_waitcnt lgkmcnt(0)\n"
      "v_cmp_gt_i32 vcc, v11, v12\n"
      "s_cbranch_vccz .Lbk_gy0_11\n"
      "s_sub_u32 s22, s22, 1\n"
      "s_cbranch_scc0 .Lsl_gy0_11\n"
      "s_mov_b32 s23, 0\n"
      "s_branch .Lbk_gy0_11\n"
      ".Lg_last:\n"
      :
      : "{s[8:11]}"(rsWS), "{s[12:15]}"(rsA), "{s[92:95]}"(rsC), "{v0}"(v_l16), "{v1}"(v_l5), "{v2}"(v_fa), "{v3}"(v_ra), "{v4}"(v_st), "{v5}"(v_at), "{v6}"(v_vo0), "{v7}"(v_vo1), "{v8}"(v_vo2), "{v9}"(v_vo3), "{v10}"(v_pw), "{v12}"(v_al), "{v13}"(v_yw), "{v14}"(v_yr), "{v16}"(v_hi), "{v54}"(v_tw[0]), "{v55}"(v_tw[1]), "{v64}"(v_id[0]), "{v65}"(v_id[1]), "{s36}"(s_sx), "{s37}"(s_sh), "{s38}"(s_m4), "{s39}"(s_m5)
      : "v255", "a255", "s99", "vcc", "scc", "memory");

  }
}

extern "C" void kernel_launch(void* const* d_in, const int* in_sizes, int n_in, void* d_out, int out_size,
                              void* d_ws, size_t ws_size, hipStream_t stream) {
  char* ws = (char*)d_ws + ((256 - ((size_t)d_ws & 255)) & 255);
  (void)hipFuncSetAttribute((const void*)k_all, hipFuncAttributeMaxDynamicSharedMemorySize, LDS_BYTES);
  k_all<<<256, 256, LDS_BYTES, stream>>>((const u16*)d_in[0], (const u16*)d_in[1], (const u16*)d_in[2], (const u16*)d_in[4],
                                         (const u16*)d_in[5], (const u16*)d_in[6], ws, (u16*)d_out);
}
```

```cpp
#include <hip/hip_runtime.h>
#include <stdint.h>

typedef unsigned short u16;
typedef __attribute__((ext_vector_type(4))) unsigned u32x4;
constexpr unsigned WS_DBG = 65536u;
constexpr unsigned WS_XIN = 1310720u;
constexpr unsigned WS_OB = 9175040u;
constexpr unsigned WS_X1 = 17039360u;
constexpr unsigned WS_H = 26034176u;
constexpr unsigned WS_PS = 78589952u;
constexpr unsigned WS_END = 109740032u;
constexpr unsigned KSK = 0u;
constexpr unsigned ENT = 1u;
constexpr unsigned ROT = 0u;
constexpr unsigned FLIP = 0u;
constexpr unsigned PW = 1664u;
constexpr unsigned REC = 24576u;
constexpr unsigned HREC = 165888u;
constexpr unsigned PREC = 99840u;
constexpr unsigned NREC = 80u;
constexpr unsigned OOB = 0x40000000u, NEG = 0xff61b1e6u;
constexpr int LDS_BYTES = 160 * 1024;

__device__ __forceinline__ u32x4 srd(const void* p, unsigned bytes) {
  const unsigned long long a = (unsigned long long)p;
  return u32x4{(unsigned)a, (unsigned)(a >> 32) & 0xffffu, bytes, 0x00020000u};
}

extern "C" __global__ __launch_bounds__(256, 1) __attribute__((amdgpu_waves_per_eu(1, 1))) void k_all(
    const u16* X, const u16* Wqkv, const u16* Wo, const u16* Wg, const u16* Wu, const u16* Wd, char* ws, u16* out) {
  const unsigned b = blockIdx.x, xcd = b & 7, slot = b >> 3;
  const unsigned w = __builtin_amdgcn_readfirstlane(threadIdx.x >> 6), lane = threadIdx.x & 63;
  const bool fast = ((xcd ^ FLIP) & 1) == 0;
  auto qvo = [&](unsigned i, unsigned hg) {
    const unsigned c = 64 * i + lane, r = c * 2185 >> 16, cc = c - 30 * r, sc = cc * 57 >> 9;
    const unsigned o = (8 * w + r) * 3456 + sc * (1152 - 144) + 144 * hg + cc * 16 - 1024 * i;
    return o | (c < 240 && cc < 27 ? 0u : OOB);
  };
  if (ENT) {
    constexpr unsigned NL = 41, LR = 9;
    const unsigned wi = slot * 4 + w, n0 = wi * NL + lane, n = n0 < 576 * LR ? n0 : 0, r = n / LR, c = n - LR * r;
    const unsigned lx = lane - NL, m = (xcd * 128 + wi) * 4 + lx, t = m / 144;
    unsigned oc = (wi * 8 + (lane & 7)) * 128;
    asm volatile("s_getpc_b64 vcc\n.Lent_pc:\n\ts_sub_u32 vcc_lo, vcc_lo, .Lent_pc-k_all\n\ts_subb_u32 vcc_hi, vcc_hi, 0\n\ts_mov_b32 m0, %1\n\t"
                 "v_min_u32 %0, .Lw_last-k_all, %0\n\tglobal_load_lds_dword %0, vcc" : "+v"(oc) : "s"(162816 + w * 256) : "vcc", "scc", "memory");
    unsigned oq = r * 3456 + c / (LR / 3) * 1152 + ((xcd & 6) + c % (LR / 3)) * 128, ox = (32 * (t & 3) + 31 - (t >> 2)) * 18432 + (m - 144 * t) * 128;
    asm volatile("" : "+v"(oq), "+v"(ox));
    const bool isq = lane < NL && fast;
    oq = isq ? oq : lx < 4 ? ox : 0;
    asm volatile("" : "+v"(oq));
    __builtin_amdgcn_sched_barrier(0);
    const char* pa = (const char*)(isq ? Wqkv : X) + oq;
    asm volatile("s_mov_b32 m0, %1\n\ts_nop 0\n\tglobal_load_lds_dword %0, off" ::"v"(pa), "s"(162816 + w * 256) : "memory");
    __builtin_amdgcn_sched_barrier(0);
  }
  const unsigned i16 = lane & 15, g = lane >> 4, q = i16 >> 2, p = lane & 3;
  const u32x4 rsWS = srd(ws, WS_END);
  const unsigned v_l16 = lane * 16, e = xcd >> 1;
  const unsigned half = (i16 + 16 * (g >> 1)) * 16 + (g & 1) * 8;
  const unsigned line = lane < 32 ? lane * 4 : OOB, v_al = w ? OOB : line + 128 * (1 + b);
  const unsigned up = lane >> 5, l5 = (lane & 31) * 4, v_hi = 0u - up;
  const unsigned l8 = lane >> 3, l7 = (lane & 7) * 16;
#define COMMON(ID, YB) const unsigned v_dbg = WS_DBG + (((ID) + part * 4 + gr) * 4 + w) * 1024 + lane * 4, v_yw = (YB) + w * 4, v_yr = (YB) + p * 4; (void)v_dbg
  if (__builtin_expect(fast && slot < 8, 1)) {
    const unsigned gr = slot & 3, part = 2 * e + (slot >> 2);
    COMMON(0, 61440);
    const unsigned hg = part, h = 4 * hg + w, par = i16 & 1;
    const u32x4 rsA = srd(Wqkv, 576 * 1728 * 2);
    const unsigned v_l5 = w < 3 || lane < 32 ? v_l16 + 16384 - w * 3072 : OOB;
    const unsigned v_fa = 20480 - w * 4096 + (lane < 48 ? lane >> 2 : 11) * 128 + p * 4, v_ra = up * 8 * REC + l5 + 20480 + h * 128;
    const unsigned v_st = 18432 + i16 * 32 + q * 8, v_tw = 61568 + w * 128 + i16 * 8, v_tr = 61568 + w * 128 + g * 32;
    const unsigned v_om = h * 512 + half, v_oe = g ? OOB : 16384 + h * 128 + i16 * 8;
    const unsigned v_pw = 20480 + h * 128 + l8 * REC + l7;
    unsigned v_vo[4], v_bc[4], v_bp[4], v_mk[4], v_sel[4] = {0, 0, 0, 0}, v_ci[4];
#pragma unroll
    for (unsigned i = 0; i < 4; ++i) {
      const unsigned kk = 4 * g + i, ka = 4 * g + 2 * (i & 1) + par;
      v_bc[i] = (i & 1) == par && kk <= i16 ? 0u : NEG;
      v_bp[i] = (i & 1) == par && kk >= i16 + 2 ? 0u : NEG;
      v_mk[i] = (i < 2 ? ka > i16 : ka <= i16) ? 0xffffu << (16 * par) : 0u;
      v_ci[i] = p == 2 ? 0x3f800000u : 0u;
    }
    v_sel[3] = lane == 16 ? 0x3f80u : lane == 17 ? 0x3f800000u : 0u;
#pragma unroll
    for (unsigned i = 0; i < 4; ++i) v_vo[i] = qvo(i, hg);
    const unsigned rb = 65536 + g * 4224 + q * 480;
    const unsigned v_at = rb + 36 * w + 4 * (w & 1) + 8 * p, v_ax = rb + (p < 2 ? p : 2) * 144 + 72 * (w >> 1) + 32;
    const unsigned v_pm = (16 * g + 4 * (i16 >> 1 & 3) + 2 * (w & 1) + par) * 4, v_ms = i16 < 6 ? ~0u : 0u;
    const unsigned s_sx = WS_XIN + (gr * NREC + KSK) * REC + w * 4096, s_ev = s_sx + (32 - KSK) * REC, s_del = 0u - (gr ? NREC + 32 : 32) * REC;
    const unsigned s_kil = gr ? 0x7f7fffffu : 0xff7fffffu, s_so = WS_OB + (gr * NREC + KSK) * REC;
    const unsigned s_m4 = w * 4096, s_m5 = 16384 + w * 1024, s_ms = w * 4224, s_up = part + gr + w;
    asm volatile(
      "s_buffer_load_dword s21, s[8:11], 0\n"
      "s_mov_b32 s23, 65536\n"
      "s_mov_b32 s31, 0x40000000\n"
      "s_mov_b32 s51, 0\n"
      "s_add_u32 m0, s43, 65536\n"
      "s_nop 0\n"
      "buffer_load_dwordx4 v48, s[12:15], s51 offen offset:0 lds\n"
      "buffer_load_dwordx4 v49, s[12:15], s51 offen offset:1024 lds\n"
      "buffer_load_dwordx4 v50, s[12:15], s51 offen offset:2048 lds\n"
      "buffer_load_dwordx4 v51, s[12:15], s51 offen offset:3072 lds\n"
      "s_add_u32 s51, s51, 110592\n"
      "s_add_u32 m0, s43, 82432\n"
      "s_nop 0\n"
      "buffer_load_dwordx4 v48, s[12:15], s51 offen offset:0 lds\n"
      "buffer_load_dwordx4 v49, s[12:15], s51 offen offset:1024 lds\n"
      "buffer_load_dwordx4 v50, s[12:15], s51 offen offset:2048 lds\n"
      "buffer_load_dwordx4 v51, s[12:15], s51 offen offset:3072 lds\n"
      "s_add_u32 s51, s51, 110592\n"
      "s_add_u32 m0, s43, 99328\n"
      "s_nop 0\n"
      "buffer_load_dwordx4 v48, s[12:15], s51 offen offset:0 lds\n"
      "buffer_load_dwordx4 v49, s[12:15], s51 offen offset:1024 lds\n"
      "buffer_load_dwordx4 v50, s[12:15], s51 offen offset:2048 lds\n"
      "buffer_load_dwordx4 v51, s[12:15], s51 offen offset:3072 lds\n"
      "s_add_u32 s51, s51, 110592\n"
      "s_add_u32 s6, s36, 1572864\n"
      "s_add_u32 s36, s36, 4294942720\n"
      "s_add_u32 s48, s37, 811008\n"
      "s_mov_b32 s53, 0x7f7fffff\n"
      "v_mov_b32 v179, 0\n"
      "s_mov_b32 s54, 0xffff\n"
      "s_mov_b32 s55, 0\n"
      "v_mov_b32 v9, 0\n"
      "ds_write_b32 v11, v9\n"
      "v_mov_b32 v56, 0x3f803f80\n"
      "v_mov_b32 v57, 0x3f803f80\n"
      "v_mov_b32 v58, 0x3f803f80\n"
      "v_mov_b32 v59, 0x3f803f80\n"
      "s_mov_b64 exec, 0\n"
      "s_lshr_b32 s22, s41, 12\n"
      ".Lq_pl:\n"
      "s_cmp_eq_u32 s22, 0\n"
      "s_cbranch_scc1 .Lq_p0\n"
      "s_cmp_eq_u32 s22, 1\n"
      "s_cbranch_scc1 .Lq_p1\n"
      "s_cmp_eq_u32 s22, 3\n"
      "s_cbranch_scc1 .Lq_px\n"
      "s_branch .Lq_pe\n"
      ".Lq_pr:\n"
      "s_add_u32 s22, s22, 4\n"
      "s_branch .Lq_pl\n"
      ".Lq_pe:\n"
      "s_mov_b64 exec, -1\n"
      "s_waitcnt vmcnt(8) lgkmcnt(0)\n"
      "s_barrier\n"
      "ds_read_b64_tr_b16 v[60:61], v53 offset:0\n"
      "ds_read_b64_tr_b16 v[62:63], v53 offset:1920\n"
      "ds_read_b64_tr_b16 a[0:1], v52 offset:144\n"
      "ds_read_b64_tr_b16 a[2:3], v52 offset:2064\n"
      "ds_read_b64_tr_b16 a[72:73], v52 offset:288\n"
      "ds_read_b64_tr_b16 a[74:75], v52 offset:2208\n"
      "ds_read_b64_tr_b16 a[144:145], v52 offset:0\n"
      "ds_read_b64_tr_b16 a[146:147], v52 offset:1920\n"
      "s_add_u32 m0, s43, 116224\n"
      "s_nop 0\n"
      "buffer_load_dwordx4 v48, s[12:15], s51 offen offset:0 lds\n"
      "buffer_load_dwordx4 v49, s[12:15], s51 offen offset:1024 lds\n"
      "buffer_load_dwordx4 v50, s[12:15], s51 offen offset:2048 lds\n"
      "buffer_load_dwordx4 v51, s[12:15], s51 offen offset:3072 lds\n"
      "s_add_u32 s51, s51, 110592\n"
      "s_waitcnt vmcnt(8) lgkmcnt(0)\n"
      "s_barrier\n"
      "ds_read_b64_tr_b16 v[68:69], v53 offset:16896\n"
      "ds_read_b64_tr_b16 v[70:71], v53 offset:18816\n"
      "ds_read_b64_tr_b16 a[4:5], v52 offset:17040\n"
      "ds_read_b64_tr_b16 a[6:7], v52 offset:18960\n"
      "ds_read_b64_tr_b16 a[76:77], v52 offset:17184\n"
      "ds_read_b64_tr_b16 a[78:79], v52 offset:19104\n"
      "ds_read_b64_tr_b16 a[148:149], v52 offset:16896\n"
      "ds_read_b64_tr_b16 a[150:151], v52 offset:18816\n"
      "ds_bpermute_b32 v64, v54, v60\n"
      "ds_bpermute_b32 v65, v54, v61\n"
      "ds_bpermute_b32 v66, v54, v62\n"
      "ds_bpermute_b32 v67, v54, v63\n"
      "v_mfma_f32_16x16x32_bf16 v[136:139], a[0:3], v[56:59], 0\n"
      "v_mfma_f32_16x16x32_bf16 v[144:147], v[56:59], a[72:75], 0\n"
      "v_mfma_f32_16x16x32_bf16 v[148:151], a[144:147], v[56:59], 0\n"
      "s_add_u32 m0, s43, 65536\n"
      "s_nop 0\n"
      "buffer_load_dwordx4 v48, s[12:15], s51 offen offset:0 lds\n"
      "buffer_load_dwordx4 v49, s[12:15], s51 offen offset:1024 lds\n"
      "buffer_load_dwordx4 v50, s[12:15], s51 offen offset:2048 lds\n"
      "buffer_load_dwordx4 v51, s[12:15], s51 offen offset:3072 lds\n"
      "s_add_u32 s51, s51, 110592\n"
      "s_waitcnt vmcnt(8) lgkmcnt(0)\n"
      "s_barrier\n"
      "ds_read_b64_tr_b16 v[60:61], v53 offset:33792\n"
      "ds_read_b64_tr_b16 v[62:63], v53 offset:35712\n"
      "ds_read_b64_tr_b16 a[8:9], v52 offset:33936\n"
      "ds_read_b64_tr_b16 a[10:11], v52 offset:35856\n"
      "ds_read_b64_tr_b16 a[80:81], v52 offset:34080\n"
      "ds_read_b64_tr_b16 a[82:83], v52 offset:36000\n"
      "ds_read_b64_tr_b16 a[152:153], v52 offset:33792\n"
      "ds_read_b64_tr_b16 a[154:155], v52 offset:35712\n"
      "ds_bpermute_b32 v72, v54, v68\n"
      "ds_bpermute_b32 v73, v54, v69\n"
      "ds_bpermute_b32 v74, v54, v70\n"
      "ds_bpermute_b32 v75, v54, v71\n"
      "v_mfma_f32_16x16x32_bf16 v[136:139], a[4:7], v[56:59], v[136:139]\n"
      "v_mfma_f32_16x16x32_bf16 v[144:147], v[56:59], a[76:79], v[144:147]\n"
      "v_mfma_f32_16x16x32_bf16 v[148:151], a[148:151], v[56:59], v[148:151]\n"
      "s_add_u32 m0, s43, 82432\n"
      "s_nop 0\n"
      "buffer_load_dwordx4 v48, s[12:15], s51 offen offset:0 lds\n"
      "buffer_load_dwordx4 v49, s[12:15], s51 offen offset:1024 lds\n"
      "buffer_load_dwordx4 v50, s[12:15], s51 offen offset:2048 lds\n"
      "buffer_load_dwordx4 v51, s[12:15], s51 offen offset:3072 lds\n"
      "s_add_u32 s51, s51, 110592\n"
      "v_and_b32 v64, v55, v64\n"
      "v_and_b32 v65, v55, v65\n"
      "v_and_b32 v66, v55, v66\n"
      "v_and_b32 v67, v55, v67\n"
      "v_accvgpr_write_b32 a216, v64\n"
      "v_accvgpr_write_b32 a217, v65\n"
      "v_accvgpr_write_b32 a218, v66\n"
      "v_accvgpr_write_b32 a219, v67\n"
      "v_mfma_f32_16x16x32_bf16 v[140:143], v[64:67], v[56:59], 0\n"
      "s_waitcnt vmcnt(8) lgkmcnt(0)\n"
      "s_barrier\n"
      "ds_read_b64_tr_b16 v[68:69], v53 offset:50688\n"
      "ds_read_b64_tr_b16 v[70:71], v53 offset:52608\n"
      "ds_read_b64_tr_b16 a[12:13], v52 offset:50832\n"
      "ds_read_b64_tr_b16 a[14:15], v52 offset:52752\n"
      "ds_read_b64_tr_b16 a[84:85], v52 offset:50976\n"
      "ds_read_b64_tr_b16 a[86:87], v52 offset:52896\n"
      "ds_read_b64_tr_b16 a[156:157], v52 offset:50688\n"
      "ds_read_b64_tr_b16 a[158:159], v52 offset:52608\n"
      "ds_bpermute_b32 v64, v54, v60\n"
      "ds_bpermute_b32 v65, v54, v61\n"
      "ds_bpermute_b32 v66, v54, v62\n"
      "ds_bpermute_b32 v67, v54, v63\n"
      "v_mfma_f32_16x16x32_bf16 v[136:139], a[8:11], v[56:59], v[136:139]\n"
      "v_mfma_f32_16x16x32_bf16 v[144:147], v[56:59], a[80:83], v[144:147]\n"
      "v_mfma_f32_16x16x32_bf16 v[148:151], a[152:155], v[56:59], v[148:151]\n"
      "s_add_u32 m0, s43, 99328\n"
      "s_nop 0\n"
      "buffer_load_dwordx4 v48, s[12:15], s51 offen offset:0 lds\n"
      "buffer_load_dwordx4 v49, s[12:15], s51 offen offset:1024 lds\n"
      "buffer_load_dwordx4 v50, s[12:15], s51 offen offset:2048 lds\n"
      "buffer_load_dwordx4 v51, s[12:15], s51 offen offset:3072 lds\n"
      "s_add_u32 s51, s51, 110592\n"
      "v_and_b32 v72, v55, v72\n"
      "v_and_b32 v73, v55, v73\n"
      "v_and_b32 v74, v55, v74\n"
      "v_and_b32 v75, v55, v75\n"
      "v_accvgpr_write_b32 a220, v72\n"
      "v_accvgpr_write_b32 a221, v73\n"
      "v_accvgpr_write_b32 a222, v74\n"
      "v_accvgpr_write_b32 a223, v75\n"
      "v_mfma_f32_16x16x32_bf16 v[140:143], v[72:75], v[56:59], v[140:143]\n"
      "s_waitcnt vmcnt(8) lgkmcnt(0)\n"
      "s_barrier\n"
      "ds_read_b64_tr_b16 v[60:61], v53 offset:0\n"
      "ds_read_b64_tr_b16 v[62:63], v53 offset:1920\n"
      "ds_read_b64_tr_b16 a[16:17], v52 offset:144\n"
      "ds_read_b64_tr_b16 a[18:19], v52 offset:2064\n"
      "ds_read_b64_tr_b16 a[88:89], v52 offset:288\n"
      "ds_read_b64_tr_b16 a[90:91], v52 offset:2208\n"
      "ds_read_b64_tr_b16 a[160:161], v52 offset:0\n"
      "ds_read_b64_tr_b16 a[162:163], v52 offset:1920\n"
      "ds_bpermute_b32 v72, v54, v68\n"
      "ds_bpermute_b32 v73, v54, v69\n"
      "ds_bpermute_b32 v74, v54, v70\n"
      "ds_bpermute_b32 v75, v54, v71\n"
      "v_mfma_f32_16x16x32_bf16 v[136:139], a[12:15], v[56:59], v[136:139]\n"
      "v_mfma_f32_16x16x32_bf16 v[144:147], v[56:59], a[84:87], v[144:147]\n"
      "v_mfma_f32_16x16x32_bf16 v[148:151], a[156:159], v[56:59], v[148:151]\n"
      "s_add_u32 m0, s43, 116224\n"
      "s_nop 0\n"
      "buffer_load_dwordx4 v48, s[12:15], s51 offen offset:0 lds\n"
      "buffer_load_dwordx4 v49, s[12:15], s51 offen offset:1024 lds\n"
      "buffer_load_dwordx4 v50, s[12:15], s51 offen offset:2048 lds\n"
      "buffer_load_dwordx4 v51, s[12:15], s51 offen offset:3072 lds\n"
      "s_add_u32 s51, s51, 110592\n"
      "v_and_b32 v64, v55, v64\n"
      "v_and_b32 v65, v55, v65\n"
      "v_and_b32 v66, v55, v66\n"
      "v_and_b32 v67, v55, v67\n"
      "v_accvgpr_write_b32 a224, v64\n"
      "v_accvgpr_write_b32 a225, v65\n"
      "v_accvgpr_write_b32 a226, v66\n"
      "v_accvgpr_write_b32 a227, v67\n"
      "v_mfma_f32_16x16x32_bf16 v[140:143], v[64:67], v[56:59], v[140:143]\n"
      "s_waitcnt vmcnt(8) lgkmcnt(0)\n"
      "s_barrier\n"
      "ds_read_b64_tr_b16 v[68:69], v53 offset:16896\n"
      "ds_read_b64_tr_b16 v[70:71], v53 offset:18816\n"
      "ds_read_b64_tr_b16 a[20:21], v52 offset:17040\n"
      "ds_read_b64_tr_b16 a[22:23], v52 offset:18960\n"
      "ds_read_b64_tr_b16 a[92:93], v52 offset:17184\n"
      "ds_read_b64_tr_b16 a[94:95], v52 offset:19104\n"
      "ds_read_b64_tr_b16 a[164:165], v52 offset:16896\n"
      "ds_read_b64_tr_b16 a[166:167], v52 offset:18816\n"
      "ds_bpermute_b32 v64, v54, v60\n"
      "ds_bpermute_b32 v65, v54, v61\n"
      "ds_bpermute_b32 v66, v54, v62\n"
      "ds_bpermute_b32 v67, v54, v63\n"
      "v_mfma_f32_16x16x32_bf16 v[136:139], a[16:19], v[56:59], v[136:139]\n"
      "v_mfma_f32_16x16x32_bf16 v[144:147], v[56:59], a[88:91], v[144:147]\n"
      "v_mfma_f32_16x16x32_bf16 v[148:151], a[160:163], v[56:59], v[148:151]\n"
      "s_add_u32 m0, s43, 65536\n"
      "s_nop 0\n"
      "buffer_load_dwordx4 v48, s[12:15], s51 offen offset:0 lds\n"
      "buffer_load_dwordx4 v49, s[12:15], s51 offen offset:1024 lds\n"
      "buffer_load_dwordx4 v50, s[12:15], s51 offen offset:2048 lds\n"
      "buffer_load_dwordx4 v51, s[12:15], s51 offen offset:3072 lds\n"
      "s_add_u32 s51, s51, 110592\n"
      "v_and_b32 v72, v55, v72\n"
      "v_and_b32 v73, v55, v73\n"
      "v_and_b32 v74, v55, v74\n"
      "v_and_b32 v75, v55, v75\n"
      "v_accvgpr_write_b32 a228, v72\n"
      "v_accvgpr_write_b32 a229, v73\n"
      "v_accvgpr_write_b32 a230, v74\n"
      "v_accvgpr_write_b32 a231, v75\n"
      "v_mfma_f32_16x16x32_bf16 v[140:143], v[72:75], v[56:59], v[140:143]\n"
      "s_waitcnt vmcnt(8) lgkmcnt(0)\n"
      "s_barrier\n"
      "ds_read_b64_tr_b16 v[60:61], v53 offset:33792\n"
      "ds_read_b64_tr_b16 v[62:63], v53 offset:35712\n"
      "ds_read_b64_tr_b16 a[24:25], v52 offset:33936\n"
      "ds_read_b64_tr_b16 a[26:27], v52 offset:35856\n"
      "ds_read_b64_tr_b16 a[96:97], v52 offset:34080\n"
      "ds_read_b64_tr_b16 a[98:99], v52 offset:36000\n"
      "ds_read_b64_tr_b16 a[168:169], v52 offset:33792\n"
      "ds_read_b64_tr_b16 a[170:171], v52 offset:35712\n"
      "ds_bpermute_b32 v72, v54, v68\n"
      "ds_bpermute_b32 v73, v54, v69\n"
      "ds_bpermute_b32 v74, v54, v70\n"
      "ds_bpermute_b32 v75, v54, v71\n"
      "v_mfma_f32_16x16x32_bf16 v[136:139], a[20:23], v[56:59], v[136:139]\n"
      "v_mfma_f32_16x16x32_bf16 v[144:147], v[56:59], a[92:95], v[144:147]\n"
      "v_mfma_f32_16x16x32_bf16 v[148:151], a[164:167], v[56:59], v[148:151]\n"
      "s_add_u32 m0, s43, 82432\n"
      "s_nop 0\n"
      "buffer_load_dwordx4 v48, s[12:15], s51 offen offset:0 lds\n"
      "buffer_load_dwordx4 v49, s[12:15], s51 offen offset:1024 lds\n"
      "buffer_load_dwordx4 v50, s[12:15], s51 offen offset:2048 lds\n"
      "buffer_load_dwordx4 v51, s[12:15], s51 offen offset:3072 lds\n"
      "s_add_u32 s51, s51, 110592\n"
      "v_and_b32 v64, v55, v64\n"
      "v_and_b32 v65, v55, v65\n"
      "v_and_b32 v66, v55, v66\n"
      "v_and_b32 v67, v55, v67\n"
      "v_accvgpr_write_b32 a232, v64\n"
      "v_accvgpr_write_b32 a233, v65\n"
      "v_accvgpr_write_b32 a234, v66\n"
      "v_accvgpr_write_b32 a235, v67\n"
      "v_mfma_f32_16x16x32_bf16 v[140:143], v[64:67], v[56:59], v[140:143]\n"
      "s_waitcnt vmcnt(8) lgkmcnt(0)\n"
      "s_barrier\n"
      "ds_read_b64_tr_b16 v[68:69], v53 offset:50688\n"
      "ds_read_b64_tr_b16 v[70:71], v53 offset:52608\n"
      "ds_read_b64_tr_b16 a[28:29], v52 offset:50832\n"
      "ds_read_b64_tr_b16 a[30:31], v52 offset:52752\n"
      "ds_read_b64_tr_b16 a[100:101], v52 offset:50976\n"
      "ds_read_b64_tr_b16 a[102:103], v52 offset:52896\n"
      "ds_read_b64_tr_b16 a[172:173], v52 offset:50688\n"
      "ds_read_b64_tr_b16 a[174:175], v52 offset:52608\n"
      "ds_bpermute_b32 v64, v54, v60\n"
      "ds_bpermute_b32 v65, v54, v61\n"
      "ds_bpermute_b32 v66, v54, v62\n"
      "ds_bpermute_b32 v67, v54, v63\n"
      "v_mfma_f32_16x16x32_bf16 v[136:139], a[24:27], v[56:59], v[136:139]\n"
      "v_mfma_f32_16x16x32_bf16 v[144:147], v[56:59], a[96:99], v[144:147]\n"
      "v_mfma_f32_16x16x32_bf16 v[148:151], a[168:171], v[56:59], v[148:151]\n"
      "s_add_u32 m0, s43, 99328\n"
      "s_nop 0\n"
      "buffer_load_dwordx4 v48, s[12:15], s51 offen offset:0 lds\n"
      "buffer_load_dwordx4 v49, s[12:15], s51 offen offset:1024 lds\n"
      "buffer_load_dwordx4 v50, s[12:15], s51 offen offset:2048 lds\n"
      "buffer_load_dwordx4 v51, s[12:15], s51 offen offset:3072 lds\n"
      "s_add_u32 s51, s51, 110592\n"
      "v_and_b32 v72, v55, v72\n"
      "v_and_b32 v73, v55, v73\n"
      "v_and_b32 v74, v55, v74\n"
      "v_and_b32 v75, v55, v75\n"
      "v_accvgpr_write_b32 a236, v72\n"
      "v_accvgpr_write_b32 a237, v73\n"
      "v_accvgpr_write_b32 a238, v74\n"
      "v_accvgpr_write_b32 a239, v75\n"
      "v_mfma_f32_16x16x32_bf16 v[140:143], v[72:75], v[56:59], v[140:143]\n"
      "s_waitcnt vmcnt(8) lgkmcnt(0)\n"
      "s_barrier\n"
      "ds_read_b64_tr_b16 v[60:61], v53 offset:0\n"
      "ds_read_b64_tr_b16 v[62:63], v53 offset:1920\n"
      "ds_read_b64_tr_b16 a[32:33], v52 offset:144\n"
      "ds_read_b64_tr_b16 a[34:35], v52 offset:2064\n"
      "ds_read_b64_tr_b16 a[104:105], v52 offset:288\n"
      "ds_read_b64_tr_b16 a[106:107], v52 offset:2208\n"
      "ds_read_b64_tr_b16 a[176:177], v52 offset:0\n"
      "ds_read_b64_tr_b16 a[178:179], v52 offset:1920\n"
      "ds_bpermute_b32 v72, v54, v68\n"
      "ds_bpermute_b32 v73, v54, v69\n"
      "ds_bpermute_b32 v74, v54, v70\n"
      ".Lq_px:\n"
      "s_cbranch_execz .Lq_pr\n"
      "ds_bpermute_b32 v75, v54, v71\n"
      "v_mfma_f32_16x16x32_bf16 v[136:139], a[28:31], v[56:59], v[136:139]\n"
      "v_mfma_f32_16x16x32_bf16 v[144:147], v[56:59], a[100:103], v[144:147]\n"
      "v_mfma_f32_16x16x32_bf16 v[148:151], a[172:175], v[56:59], v[148:151]\n"
      "s_add_u32 m0, s43, 116224\n"
      "s_nop 0\n"
      "buffer_load_dwordx4 v48, s[12:15], s51 offen offset:0 lds\n"
      "buffer_load_dwordx4 v49, s[12:15], s51 offen offset:1024 lds\n"
      "buffer_load_dwordx4 v50, s[12:15], s51 offen offset:2048 lds\n"
      "buffer_load_dwordx4 v51, s[12:15], s51 offen offset:3072 lds\n"
      "s_add_u32 s51, s51, 110592\n"
      "v_and_b32 v64, v55, v64\n"
      "v_and_b32 v65, v55, v65\n"
      "v_and_b32 v66, v55, v66\n"
      "v_and_b32 v67, v55, v67\n"
      "v_accvgpr_write_b32 a240, v64\n"
      "v_accvgpr_write_b32 a241, v65\n"
      "v_accvgpr_write_b32 a242, v66\n"
      "v_accvgpr_write_b32 a243, v67\n"
      "v_mfma_f32_16x16x32_bf16 v[140:143], v[64:67], v[56:59], v[140:143]\n"
      "s_waitcnt vmcnt(8) lgkmcnt(0)\n"
      "s_barrier\n"
      "ds_read_b64_tr_b16 v[68:69], v53 offset:16896\n"
      "ds_read_b64_tr_b16 v[70:71], v53 offset:18816\n"
      "ds_read_b64_tr_b16 a[36:37], v52 offset:17040\n"
      "ds_read_b64_tr_b16 a[38:39], v52 offset:18960\n"
      "ds_read_b64_tr_b16 a[108:109], v52 offset:17184\n"
      "ds_read_b64_tr_b16 a[110:111], v52 offset:19104\n"
      "ds_read_b64_tr_b16 a[180:181], v52 offset:16896\n"
      "ds_read_b64_tr_b16 a[182:183], v52 offset:18816\n"
      "ds_bpermute_b32 v64, v54, v60\n"
      "ds_bpermute_b32 v65, v54, v61\n"
      "ds_bpermute_b32 v66, v54, v62\n"
      "ds_bpermute_b32 v67, v54, v63\n"
      "v_mfma_f32_16x16x32_bf16 v[136:139], a[32:35], v[56:59], v[136:139]\n"
      "v_mfma_f32_16x16x32_bf16 v[144:147], v[56:59], a[104:107], v[144:147]\n"
      "v_mfma_f32_16x16x32_bf16 v[148:151], a[176:179], v[56:59], v[148:151]\n"
      "s_add_u32 m0, s43, 65536\n"
      "s_nop 0\n"
      "buffer_load_dwordx4 v48, s[12:15], s51 offen offset:0 lds\n"
      "buffer_load_dwordx4 v49, s[12:15], s51 offen offset:1024 lds\n"
      "buffer_load_dwordx4 v50, s[12:15], s51 offen offset:2048 lds\n"
      "buffer_load_dwordx4 v51, s[12:15], s51 offen offset:3072 lds\n"
      "s_add_u32 s51, s51, 110592\n"
      "v_and_b32 v72, v55, v72\n"
      "v_and_b32 v73, v55, v73\n"
      "v_and_b32 v74, v55, v74\n"
      "v_and_b32 v75, v55, v75\n"
      "v_accvgpr_write_b32 a244, v72\n"
      "v_accvgpr_write_b32 a245, v73\n"
      "v_accvgpr_write_b32 a246, v74\n"
      "v_accvgpr_write_b32 a247, v75\n"
      "v_mfma_f32_16x16x32_bf16 v[140:143], v[72:75], v[56:59], v[140:143]\n"
      "s_waitcnt vmcnt(8) lgkmcnt(0)\n"
      "s_barrier\n"
      "ds_read_b64_tr_b16 v[60:61], v53 offset:33792\n"
      "ds_read_b64_tr_b16 v[62:63], v53 offset:35712\n"
      "ds_read_b64_tr_b16 a[40:41], v52 offset:33936\n"
      "ds_read_b64_tr_b16 a[42:43], v52 offset:35856\n"
      "ds_read_b64_tr_b16 a[112:113], v52 offset:34080\n"
      "ds_read_b64_tr_b16 a[114:115], v52 offset:36000\n"
      "ds_read_b64_tr_b16 a[184:185], v52 offset:33792\n"
      "ds_read_b64_tr_b16 a[186:187], v52 offset:35712\n"
      "ds_bpermute_b32 v72, v54, v68\n"
      "ds_bpermute_b32 v73, v54, v69\n"
      "ds_bpermute_b32 v74, v54, v70\n"
      "ds_bpermute_b32 v75, v54, v71\n"
      "v_mfma_f32_16x16x32_bf16 v[136:139], a[36:39], v[56:59], v[136:139]\n"
      "v_mfma_f32_16x16x32_bf16 v[144:147], v[56:59], a[108:111], v[144:147]\n"
      "v_mfma_f32_16x16x32_bf16 v[148:151], a[180:183], v[56:59], v[148:151]\n"
      "s_add_u32 m0, s43, 82432\n"
      "s_nop 0\n"
      "buffer_load_dwordx4 v48, s[12:15], s51 offen offset:0 lds\n"
      "buffer_load_dwordx4 v49, s[12:15], s51 offen offset:1024 lds\n"
      "buffer_load_dwordx4 v50, s[12:15], s51 offen offset:2048 lds\n"
      "buffer_load_dwordx4 v51, s[12:15], s51 offen offset:3072 lds\n"
      "s_add_u32 s51, s51, 110592\n"
      "v_and_b32 v64, v55, v64\n"
      "v_and_b32 v65, v55, v65\n"
      "v_and_b32 v66, v55, v66\n"
      "v_and_b32 v67, v55, v67\n"
      "v_accvgpr_write_b32 a248, v64\n"
      "v_accvgpr_write_b32 a249, v65\n"
      "v_accvgpr_write_b32 a250, v66\n"
      "v_accvgpr_write_b32 a251, v67\n"
      "v_mfma_f32_16x16x32_bf16 v[140:143], v[64:67], v[56:59], v[140:143]\n"
      "s_waitcnt vmcnt(8) lgkmcnt(0)\n"
      "s_barrier\n"
      "ds_read_b64_tr_b16 v[68:69], v53 offset:50688\n"
      "ds_read_b64_tr_b16 v[70:71], v53 offset:52608\n"
      "ds_read_b64_tr_b16 a[44:45], v52 offset:50832\n"
      "ds_read_b64_tr_b16 a[46:47], v52 offset:52752\n"
      "ds_read_b64_tr_b16 a[116:117], v52 offset:50976\n"
      "ds_read_b64_tr_b16 a[118:119], v52 offset:52896\n"
      "ds_read_b64_tr_b16 a[188:189], v52 offset:50688\n"
      "ds_read_b64_tr_b16 a[190:191], v52 offset:52608\n"
      "ds_bpermute_b32 v64, v54, v60\n"
      "ds_bpermute_b32 v65, v54, v61\n"
      "ds_bpermute_b32 v66, v54, v62\n"
      "ds_bpermute_b32 v67, v54, v63\n"
      "v_mfma_f32_16x16x32_bf16 v[136:139], a[40:43], v[56:59], v[136:139]\n"
      "v_mfma_f32_16x16x32_bf16 v[144:147], v[56:59], a[112:115], v[144:147]\n"
      "v_mfma_f32_16x16x32_bf16 v[148:151], a[184:187], v[56:59], v[148:151]\n"
      "s_add_u32 m0, s43, 99328\n"
      "s_nop 0\n"
      "buffer_load_dwordx4 v48, s[12:15], s51 offen offset:0 lds\n"
      "buffer_load_dwordx4 v49, s[12:15], s51 offen offset:1024 lds\n"
      "buffer_load_dwordx4 v50, s[12:15], s51 offen offset:2048 lds\n"
      "buffer_load_dwordx4 v51, s[12:15], s51 offen offset:3072 lds\n"
      "s_add_u32 s51, s51, 110592\n"
      "v_and_b32 v72, v55, v72\n"
      "v_and_b32 v73, v55, v73\n"
      "v_and_b32 v74, v55, v74\n"
      "v_and_b32 v75, v55, v75\n"
      "v_accvgpr_write_b32 a252, v72\n"
      "v_accvgpr_write_b32 a253, v73\n"
      "v_accvgpr_write_b32 a254, v74\n"
      "v_accvgpr_write_b32 a255, v75\n"
      "v_mfma_f32_16x16x32_bf16 v[140:143], v[72:75], v[56:59], v[140:143]\n"
      "s_waitcnt vmcnt(8) lgkmcnt(0)\n"
      "s_barrier\n"
      "ds_read_b64_tr_b16 v[60:61], v53 offset:0\n"
      "ds_read_b64_tr_b16 v[62:63], v53 offset:1920\n"
      "ds_read_b64_tr_b16 a[48:49], v52 offset:144\n"
      "ds_read_b64_tr_b16 a[50:51], v52 offset:2064\n"
      "ds_read_b64_tr_b16 a[120:121], v52 offset:288\n"
      "ds_read_b64_tr_b16 a[122:123], v52 offset:2208\n"
      "ds_read_b64_tr_b16 a[192:193], v52 offset:0\n"
      "ds_read_b64_tr_b16 a[194:195], v52 offset:1920\n"
      "ds_bpermute_b32 v72, v54, v68\n"
      "ds_bpermute_b32 v73, v54, v69\n"
      "ds_bpermute_b32 v74, v54, v70\n"
      "ds_bpermute_b32 v75, v54, v71\n"
      "v_mfma_f32_16x16x32_bf16 v[136:139], a[44:47], v[56:59], v[136:139]\n"
      "v_mfma_f32_16x16x32_bf16 v[144:147], v[56:59], a[116:119], v[144:147]\n"
      "v_mfma_f32_16x16x32_bf16 v[148:151], a[188:191], v[56:59], v[148:151]\n"
      "s_add_u32 m0, s43, 116224\n"
      "s_nop 0\n"
      "buffer_load_dwordx4 v48, s[12:15], s51 offen offset:0 lds\n"
      "buffer_load_dwordx4 v49, s[12:15], s51 offen offset:1024 lds\n"
      "buffer_load_dwordx4 v50, s[12:15], s51 offen offset:2048 lds\n"
      "buffer_load_dwordx4 v51, s[12:15], s51 offen offset:3072 lds\n"
      "s_add_u32 s51, s51, 110592\n"
      "v_and_b32 v64, v55, v64\n"
      "v_and_b32 v65, v55, v65\n"
      "v_and_b32 v66, v55, v66\n"
      "v_and_b32 v67, v55, v67\n"
      "v_mov_b32 v16, v64\n"
      "v_mov_b32 v17, v65\n"
      "v_mov_b32 v18, v66\n"
      "v_mov_b32 v19, v67\n"
      "v_mfma_f32_16x16x32_bf16 v[140:143], v[64:67], v[56:59], v[140:143]\n"
      "s_waitcnt vmcnt(8) lgkmcnt(0)\n"
      "s_barrier\n"
      "ds_read_b64_tr_b16 v[68:69], v53 offset:16896\n"
      "ds_read_b64_tr_b16 v[70:71], v53 offset:18816\n"
      "ds_read_b64_tr_b16 a[52:53], v52 offset:17040\n"
      "ds_read_b64_tr_b16 a[54:55], v52 offset:18960\n"
      "ds_read_b64_tr_b16 a[124:125], v52 offset:17184\n"
      "ds_read_b64_tr_b16 a[126:127], v52 offset:19104\n"
      "ds_read_b64_tr_b16 a[196:197], v52 offset:16896\n"
      "ds_read_b64_tr_b16 a[198:199], v52 offset:18816\n"
      "ds_bpermute_b32 v64, v54, v60\n"
      "ds_bpermute_b32 v65, v54, v61\n"
      "ds_bpermute_b32 v66, v54, v62\n"
      "ds_bpermute_b32 v67, v54, v63\n"
      "v_mfma_f32_16x16x32_bf16 v[136:139], a[48:51], v[56:59], v[136:139]\n"
      "v_mfma_f32_16x16x32_bf16 v[144:147], v[56:59], a[120:123], v[144:147]\n"
      "v_mfma_f32_16x16x32_bf16 v[148:151], a[192:195], v[56:59], v[148:151]\n"
      "s_add_u32 m0, s43, 65536\n"
      "s_nop 0\n"
      "buffer_load_dwordx4 v48, s[12:15], s51 offen offset:0 lds\n"
      "buffer_load_dwordx4 v49, s[12:15], s51 offen offset:1024 lds\n"
      "buffer_load_dwordx4 v50, s[12:15], s51 offen offset:2048 lds\n"
      "buffer_load_dwordx4 v51, s[12:15], s51 offen offset:3072 lds\n"
      "s_add_u32 s51, s51, 110592\n"
      "v_and_b32 v72, v55, v72\n"
      "v_and_b32 v73, v55, v73\n"
      "v_and_b32 v74, v55, v74\n"
      "v_and_b32 v75, v55, v75\n"
      "v_mov_b32 v20, v72\n"
      "v_mov_b32 v21, v73\n"
      "v_mov_b32 v22, v74\n"
      "v_mov_b32 v23, v75\n"
      "v_mfma_f32_16x16x32_bf16 v[140:143], v[72:75], v[56:59], v[140:143]\n"
      "s_add_u32 s30, s36, 24576\n"
      "buffer_load_dword v204, v2, s[8:11], s30 offen sc1\n"
      "s_add_u32 s30, s36, 49152\n"
      "buffer_load_dword v205, v2, s[8:11], s30 offen sc1\n"
      "s_waitcnt vmcnt(10) lgkmcnt(0)\n"
      "s_barrier\n"
      "ds_read_b64_tr_b16 v[60:61], v53 offset:33792\n"
      "ds_read_b64_tr_b16 v[62:63], v53 offset:35712\n"
      "ds_read_b64_tr_b16 a[56:57], v52 offset:33936\n"
      "ds_read_b64_tr_b16 a[58:59], v52 offset:35856\n"
      "ds_read_b64_tr_b16 a[128:129], v52 offset:34080\n"
      "ds_read_b64_tr_b16 a[130:131], v52 offset:36000\n"
      "ds_read_b64_tr_b16 a[200:201], v52 offset:33792\n"
      "ds_read_b64_tr_b16 a[202:203], v52 offset:35712\n"
      "ds_bpermute_b32 v72, v54, v68\n"
      "ds_bpermute_b32 v73, v54, v69\n"
      "ds_bpermute_b32 v74, v54, v70\n"
      "ds_bpermute_b32 v75, v54, v71\n"
      "v_mfma_f32_16x16x32_bf16 v[136:139], a[52:55], v[56:59], v[136:139]\n"
      "v_mfma_f32_16x16x32_bf16 v[144:147], v[56:59], a[124:127], v[144:147]\n"
      "v_mfma_f32_16x16x32_bf16 v[148:151], a[196:199], v[56:59], v[148:151]\n"
      "s_add_u32 m0, s43, 82432\n"
      "s_nop 0\n"
      "buffer_load_dwordx4 v48, s[12:15], s51 offen offset:0 lds\n"
      "buffer_load_dwordx4 v49, s[12:15], s51 offen offset:1024 lds\n"
      "buffer_load_dwordx4 v50, s[12:15], s51 offen offset:2048 lds\n"
      "buffer_load_dwordx4 v51, s[12:15], s51 offen offset:3072 lds\n"
      "s_add_u32 s51, s51, 110592\n"
      "v_and_b32 v64, v55, v64\n"
      "v_and_b32 v65, v55, v65\n"
      "v_and_b32 v66, v55, v66\n"
      "v_and_b32 v67, v55, v67\n"
      "v_mov_b32 v24, v64\n"
      "v_mov_b32 v25, v65\n"
      "v_mov_b32 v26, v66\n"
      "v_mov_b32 v27, v67\n"
      "v_mfma_f32_16x16x32_bf16 v[140:143], v[64:67], v[56:59], v[140:143]\n"
      "s_waitcnt vmcnt(10) lgkmcnt(0)\n"
      "s_barrier\n"
      "ds_read_b64_tr_b16 v[68:69], v53 offset:50688\n"
      "ds_read_b64_tr_b16 v[70:71], v53 offset:52608\n"
      "ds_read_b64_tr_b16 a[60:61], v52 offset:50832\n"
      "ds_read_b64_tr_b16 a[62:63], v52 offset:52752\n"
      "ds_read_b64_tr_b16 a[132:133], v52 offset:50976\n"
      "ds_read_b64_tr_b16 a[134:135], v52 offset:52896\n"
      "ds_read_b64_tr_b16 a[204:205], v52 offset:50688\n"
      "ds_read_b64_tr_b16 a[206:207], v52 offset:52608\n"
      "ds_bpermute_b32 v64, v54, v60\n"
      "ds_bpermute_b32 v65, v54, v61\n"
      "ds_bpermute_b32 v66, v54, v62\n"
      "ds_bpermute_b32 v67, v54, v63\n"
      "v_mfma_f32_16x16x32_bf16 v[136:139], a[56:59], v[56:59], v[136:139]\n"
      "v_mfma_f32_16x16x32_bf16 v[144:147], v[56:59], a[128:131], v[144:147]\n"
      "v_mfma_f32_16x16x32_bf16 v[148:151], a[200:203], v[56:59], v[148:151]\n"
      "v_and_b32 v72, v55, v72\n"
      "v_and_b32 v73, v55, v73\n"
      "v_and_b32 v74, v55, v74\n"
      "v_and_b32 v75, v55, v75\n"
      "v_mov_b32 v28, v72\n"
      "v_mov_b32 v29, v73\n"
      "v_mov_b32 v30, v74\n"
      "v_mov_b32 v31, v75\n"
      "v_mfma_f32_16x16x32_bf16 v[140:143], v[72:75], v[56:59], v[140:143]\n"
      "s_mul_i32 s21, s21, 1664525\n"
      "s_add_u32 s21, s21, 1013904223\n"
      "v_mov_b32 v13, s21\n"
      "buffer_store_dword v13, v10, s[8:11], 0 offen sc1\n"
      "v_not_b32 v200, v13\n"
      "v_not_b32 v201, v13\n"
      "v_not_b32 v202, v13\n"
      "v_not_b32 v203, v13\n"
      "s_add_u32 s30, s40, 0\n"
      "buffer_store_dwordx4 v[200:203], v76, s[8:11], s30 offen sc1\n"
      "v_xor_b32 v13, v13, v14\n"
      "s_add_u32 s52, s40, 4294893568\n"
      "s_add_u32 s40, s40, 4294918144\n"
      "s_add_u32 s36, s36, 24576\n"
      "s_mov_b32 s45, s36\n"
      "s_min_u32 s45, s45, s6\n"
      "s_waitcnt vmcnt(7)\n"
      "v_cmp_ne_u32 vcc, s21, v204\n"
      "s_add_u32 m0, s41, 0\n"
      "s_add_u32 s36, s36, 24576\n"
      "s_mov_b32 s46, s45\n"
      "s_cbranch_vccnz .Lsp_q0_1\n"
      ".Lbk_q0_1:\n"
      "buffer_load_dwordx4 v0, s[8:11], s46 offen offset:1024 sc1 lds\n"
      "buffer_load_dwordx4 v0, s[8:11], s46 offen offset:0 sc1 lds\n"
      "buffer_load_dwordx4 v0, s[8:11], s46 offen offset:2048 sc1 lds\n"
      "s_mov_b32 s45, s36\n"
      "s_min_u32 s45, s45, s6\n"
      "buffer_load_dwordx4 v0, s[8:11], s46 offen offset:3072 sc1 lds\n"
      "s_add_u32 m0, s42, 0\n"
      "s_nop 0\n"
      "buffer_load_dwordx4 v1, s[8:11], s46 offen offset:0 sc1 lds\n"
      "s_waitcnt lgkmcnt(0)\n"
      "s_barrier\n"
      "ds_read_b64_tr_b16 v[60:61], v53 offset:0\n"
      "ds_read_b64_tr_b16 v[62:63], v53 offset:1920\n"
      "ds_read_b64_tr_b16 a[64:65], v52 offset:144\n"
      "ds_read_b64_tr_b16 a[66:67], v52 offset:2064\n"
      "ds_read_b64_tr_b16 a[136:137], v52 offset:288\n"
      "ds_read_b64_tr_b16 a[138:139], v52 offset:2208\n"
      "ds_read_b64_tr_b16 a[208:209], v52 offset:0\n"
      "ds_read_b64_tr_b16 a[210:211], v52 offset:1920\n"
      "ds_bpermute_b32 v72, v54, v68\n"
      "ds_bpermute_b32 v73, v54, v69\n"
      "ds_bpermute_b32 v74, v54, v70\n"
      "ds_bpermute_b32 v75, v54, v71\n"
      "v_mfma_f32_16x16x32_bf16 v[136:139], a[60:63], v[56:59], v[136:139]\n"
      "v_mfma_f32_16x16x32_bf16 v[144:147], v[56:59], a[132:135], v[144:147]\n"
      "v_mfma_f32_16x16x32_bf16 v[148:151], a[204:207], v[56:59], v[148:151]\n"
      "v_and_b32 v64, v55, v64\n"
      "v_and_b32 v65, v55, v65\n"
      "v_and_b32 v66, v55, v66\n"
      "v_and_b32 v67, v55, v67\n"
      "v_mov_b32 v32, v64\n"
      "v_mov_b32 v33, v65\n"
      "v_mov_b32 v34, v66\n"
      "v_mov_b32 v35, v67\n"
      "v_mfma_f32_16x16x32_bf16 v[140:143], v[64:67], v[56:59], v[140:143]\n"
      "s_waitcnt vmcnt(7) lgkmcnt(0)\n"
      "s_barrier\n"
      "ds_read_b64_tr_b16 v[68:69], v53 offset:16896\n"
      "ds_read_b64_tr_b16 v[70:71], v53 offset:18816\n"
      "ds_read_b64_tr_b16 a[68:69], v52 offset:17040\n"
      "ds_read_b64_tr_b16 a[70:71], v52 offset:18960\n"
      "ds_read_b64_tr_b16 a[140:141], v52 offset:17184\n"
      "ds_read_b64_tr_b16 a[142:143], v52 offset:19104\n"
      "ds_read_b64_tr_b16 a[212:213], v52 offset:16896\n"
      "ds_read_b64_tr_b16 a[214:215], v52 offset:18816\n"
      "ds_bpermute_b32 v64, v54, v60\n"
      "ds_bpermute_b32 v65, v54, v61\n"
      "ds_bpermute_b32 v66, v54, v62\n"
      "ds_bpermute_b32 v67, v54, v63\n"
      "v_mfma_f32_16x16x32_bf16 v[136:139], a[64:67], v[56:59], v[136:139]\n"
      "v_mfma_f32_16x16x32_bf16 v[144:147], v[56:59], a[136:139], v[144:147]\n"
      "v_mfma_f32_16x16x32_bf16 v[148:151], a[208:211], v[56:59], v[148:151]\n"
      "v_and_b32 v72, v55, v72\n"
      "v_and_b32 v73, v55, v73\n"
      "v_and_b32 v74, v55, v74\n"
      "v_and_b32 v75, v55, v75\n"
      "v_mov_b32 v36, v72\n"
      "v_mov_b32 v37, v73\n"
      "v_mov_b32 v38, v74\n"
      "v_mov_b32 v39, v75\n"
      "v_mfma_f32_16x16x32_bf16 v[140:143], v[72:75], v[56:59], v[140:143]\n"
      "s_waitcnt lgkmcnt(0)\n"
      "ds_bpermute_b32 v72, v54, v68\n"
      "ds_bpermute_b32 v73, v54, v69\n"
      "ds_bpermute_b32 v74, v54, v70\n"
      "ds_bpermute_b32 v75, v54, v71\n"
      "v_mfma_f32_16x16x32_bf16 v[136:139], a[68:71], v[56:59], v[136:139]\n"
      "v_mfma_f32_16x16x32_bf16 v[144:147], v[56:59], a[140:143], v[144:147]\n"
      "v_mfma_f32_16x16x32_bf16 v[148:151], a[212:215], v[56:59], v[148:151]\n"
      "v_and_b32 v64, v55, v64\n"
      "v_and_b32 v65, v55, v65\n"
      "v_and_b32 v66, v55, v66\n"
      "v_and_b32 v67, v55, v67\n"
      "v_mov_b32 v40, v64\n"
      "v_mov_b32 v41, v65\n"
      "v_mov_b32 v42, v66\n"
      "v_mov_b32 v43, v67\n"
      "v_mfma_f32_16x16x32_bf16 v[140:143], v[64:67], v[56:59], v[140:143]\n"
      "s_waitcnt lgkmcnt(0)\n"
      "v_and_b32 v72, v55, v72\n"
      "v_and_b32 v73, v55, v73\n"
      "v_and_b32 v74, v55, v74\n"
      "v_and_b32 v75, v55, v75\n"
      "v_mov_b32 v44, v72\n"
      "v_mov_b32 v45, v73\n"
      "v_mov_b32 v46, v74\n"
      "v_mov_b32 v47, v75\n"
      "v_mfma_f32_16x16x32_bf16 v[140:143], v[72:75], v[56:59], v[140:143]\n"
      "s_add_u32 s36, s36, 24576\n"
      "s_cmp_eq_u32 s36, s37\n"
      "s_cselect_b32 s47, s38, 0\n"
      "s_cselect_b32 s37, s48, s37\n"
      "s_cselect_b32 s49, s39, s53\n"
      "s_add_u32 m0, s41, 20480\n"
      "s_mov_b32 s46, s45\n"
      "s_add_u32 s45, s36, s47\n"
      "v_add_u32 v9, 1, v9\n"
      "v_cmp_ne_u32 vcc, s21, v205\n"
      "s_min_u32 s45, s45, s6\n"
      "s_cbranch_vccnz .Lsp_q1_2\n"
      ".Lbk_q1_2:\n"
      "buffer_load_dwordx4 v0, s[8:11], s46 offen offset:1024 sc1 lds\n"
      "s_waitcnt vmcnt(1)\n"
      "ds_write_b32 v11, v9\n"
      "buffer_load_dwordx4 v0, s[8:11], s46 offen offset:0 sc1 lds\n"
      "buffer_load_dwordx4 v0, s[8:11], s46 offen offset:3072 sc1 lds\n"
      "buffer_load_dword v15, v2, s[8:11], s45 offen sc1\n"
      "ds_read_b32 v10, v12\n"
      "buffer_load_dwordx4 v0, s[8:11], s46 offen offset:2048 sc1 lds\n"
      "s_waitcnt lgkmcnt(0)\n"
      "v_cmp_gt_i32 vcc, v9, v10\n"
      "s_add_u32 m0, s42, 20480\n"
      "s_nop 0\n"
      "buffer_load_dwordx4 v1, s[8:11], s46 offen offset:0 sc1 lds\n"
      "s_cbranch_vccnz .Lsp_qy0_3\n"
      ".Lbk_qy0_3:\n"
      "ds_read_b128 v[52:55], v0 offset:1024\n"
      "ds_read_b128 v[48:51], v0 offset:0\n"
      "ds_read_b128 v[56:59], v0 offset:2048\n"
      "ds_read_b128 v[68:71], v0 offset:5120\n"
      "ds_read_b128 v[64:67], v0 offset:4096\n"
      "ds_read_b128 v[60:63], v0 offset:3072\n"
      "buffer_load_dword v14, v11, s[8:11], s31 offen\n"
      "ds_read2st64_b64 v[212:215], v4 offset0:0 offset1:1\n"
      "ds_read_b128 v[72:75], v0 offset:6144\n"
      "ds_read_b128 v[76:79], v0 offset:7168\n"
      "ds_read_b128 v[88:91], v0 offset:10240\n"
      "ds_read_b128 v[108:111], v0 offset:15360\n"
      "s_mov_b32 s20, 21\n"
      "s_waitcnt lgkmcnt(0)\n"
      "s_nop 4\n"
      "s_branch .Lq_loop\n"
      ".p2align 12\n"
      ".Lq_p0:\n"
      "s_branch .Lq_pr\n"
      ".p2align 5\n"
      ".Lq_loop:\n"
      "v_mfma_f32_16x16x32_bf16 v[120:123], a[0:3], v[48:51], 0\n"
      "ds_read_b128 v[80:83], v0 offset:8192\n"
      "ds_read_b128 v[84:87], v0 offset:9216\n"
      "v_mfma_f32_16x16x32_bf16 v[120:123], a[4:7], v[52:55], v[120:123]\n"
      "ds_read_b128 v[96:99], v0 offset:12288\n"
      "v_mul_f32 v232, 0x3eae1a93, v219\n"
      "v_mfma_f32_16x16x32_bf16 v[120:123], a[8:11], v[56:59], v[120:123]\n"
      "s_waitcnt lgkmcnt(7)\n"
      "ds_read_b128 v[104:107], v0 offset:14336\n"
      "v_fma_f32 v135, -v218, v151, v135\n"
      "v_mfma_f32_16x16x32_bf16 v[120:123], a[12:15], v[60:63], v[120:123]\n"
      "v_fma_f32 v133, -v218, v149, v133\n"
      "v_fma_f32 v134, -v218, v150, v134\n"
      "v_mfma_f32_16x16x32_bf16 v[120:123], a[16:19], v[64:67], v[120:123]\n"
      "ds_read_b128 v[92:95], v0 offset:11264\n"
      "v_fma_f32 v132, -v218, v148, v132\n"
      "v_mfma_f32_16x16x32_bf16 v[120:123], a[20:23], v[68:71], v[120:123]\n"
      "s_waitcnt lgkmcnt(5)\n"
      "ds_read_b64 v[216:217], v4 offset:1024\n"
      "v_cvt_pk_bf16_f32 v176, v132, v133\n"
      "v_mfma_f32_16x16x32_bf16 v[120:123], a[24:27], v[72:75], v[120:123]\n"
      "ds_read_b128 v[100:103], v0 offset:13312\n"
      "v_cvt_pk_bf16_f32 v177, v134, v135\n"
      "v_mfma_f32_16x16x32_bf16 v[120:123], a[28:31], v[76:79], v[120:123]\n"
      "s_waitcnt lgkmcnt(5)\n"
      "v_rcp_f32 v210, v210\n"
      "v_mfma_f32_16x16x32_bf16 v[120:123], a[32:35], v[80:83], v[120:123]\n"
      "v_mul_f32 v204, v204, v210\n"
      "v_add_f32 v212, v212, v214\n"
      "v_mfma_f32_16x16x32_bf16 v[188:191], v[172:175], v[176:179], v[152:155]\n"
      "ds_read_b128 v[112:115], v0 offset:16384\n"
      "v_mfma_f32_16x16x32_bf16 v[120:123], a[36:39], v[84:87], v[120:123]\n"
      "ds_read_b128 v[116:119], v0 offset:17408\n"
      "v_mul_f32 v208, v208, v210\n"
      "v_mfma_f32_16x16x32_bf16 v[120:123], a[40:43], v[88:91], v[120:123]\n"
      "s_waitcnt lgkmcnt(4)\n"
      "s_add_u32 s36, s36, 24576\n"
      "v_mul_f32 v207, v207, v210\n"
      "v_mfma_f32_16x16x32_bf16 v[120:123], a[44:47], v[92:95], v[120:123]\n"
      "v_mul_f32 v209, v209, v210\n"
      "v_cvt_pk_bf16_f32 v230, v208, v209\n"
      "v_mfma_f32_16x16x32_bf16 v[120:123], a[48:51], v[96:99], v[120:123]\n"
      "s_waitcnt lgkmcnt(2)\n"
      "buffer_store_dwordx2 v[230:231], v8, s[8:11], s40 offen sc1\n"
      "v_mfma_f32_16x16x32_bf16 v[120:123], a[52:55], v[100:103], v[120:123]\n"
      "v_mul_f32 v206, v206, v210\n"
      "v_add_f32 v218, v212, v216\n"
      "v_mfma_f32_16x16x32_bf16 v[120:123], a[56:59], v[104:107], v[120:123]\n"
      "s_mov_b32 s46, s45\n"
      "v_add_f32 v213, v213, v215\n"
      "v_mfma_f32_16x16x32_bf16 v[120:123], a[60:63], v[108:111], v[120:123]\n"
      "s_waitcnt lgkmcnt(1)\n"
      "v_cvt_pk_bf16_f32 v229, v206, v207\n"
      "v_mfma_f32_16x16x32_bf16 v[120:123], a[64:67], v[112:115], v[120:123]\n"
      "s_waitcnt lgkmcnt(0)\n"
      "v_mul_f32 v205, v205, v210\n"
      "v_cvt_pk_bf16_f32 v228, v204, v205\n"
      "v_mfma_f32_16x16x32_bf16 v[120:123], a[68:71], v[116:119], v[120:123]\n"
      "buffer_store_dwordx2 v[228:229], v7, s[8:11], s40 offen sc1\n"
      "v_mfma_f32_16x16x32_bf16 v[124:127], a[216:219], v[48:51], 0\n"
      "v_add_f32 v213, v213, v217\n"
      "v_fma_f32 v213, -v218, v218, v213\n"
      "v_mfma_f32_16x16x32_bf16 v[124:127], a[220:223], v[52:55], v[124:127]\n"
      "v_max_f32 v213, 0x3727c5ac, v213\n"
      "s_mov_b32 s45, s36\n"
      "v_mfma_f32_16x16x32_bf16 v[124:127], a[224:227], v[56:59], v[124:127]\n"
      "v_mov_b32 v187, v185\n"
      "v_mfma_f32_16x16x32_bf16 v[124:127], a[228:231], v[60:63], v[124:127]\n"
      "v_rsq_f32 v219, v213\n"
      "v_mfma_f32_16x16x32_bf16 v[124:127], a[232:235], v[64:67], v[124:127]\n"
      "s_add_u32 m0, s41, 40960\n"
      "ds_write_b64 v5, v[218:219] offset:0\n"
      "v_mfma_f32_16x16x32_bf16 v[124:127], a[236:239], v[68:71], v[124:127]\n"
      "s_waitcnt vmcnt(5)\n"
      "v_mov_b32 v186, v184\n"
      "v_cmp_ne_u32 vcc, s21, v15\n"
      "v_mfma_f32_16x16x32_bf16 v[124:127], a[240:243], v[72:75], v[124:127]\n"
      "s_min_u32 s45, s45, s6\n"
      "v_mfma_f32_16x16x32_bf16 v[124:127], a[244:247], v[76:79], v[124:127]\n"
      "v_cndmask_b32 v233, v219, 1.0, s[54:55]\n"
      "v_add_u32 v9, 1, v9\n"
      "v_mfma_f32_16x16x32_bf16 v[124:127], a[248:251], v[80:83], v[124:127]\n"
      "v_fma_f32 v123, -v218, v139, v123\n"
      "v_fma_f32 v122, -v218, v138, v122\n"
      "v_mfma_f32_16x16x32_bf16 v[124:127], a[252:255], v[84:87], v[124:127]\n"
      "v_fma_f32 v120, -v218, v136, v120\n"
      "v_fma_f32 v121, -v218, v137, v121\n"
      "v_mfma_f32_16x16x32_bf16 v[124:127], v[16:19], v[88:91], v[124:127]\n"
      "ds_read_b128 v[224:227], v6 offset:16\n"
      "v_mul_f32 v121, v219, v121\n"
      "v_mfma_f32_16x16x32_bf16 v[124:127], v[20:23], v[92:95], v[124:127]\n"
      "v_mul_f32 v120, v219, v120\n"
      "v_mfma_f32_16x16x32_bf16 v[124:127], v[24:27], v[96:99], v[124:127]\n"
      "s_cbranch_vccnz .Lsp_q2_4\n"
      ".Lbk_q2_4:\n"
      "v_mfma_f32_16x16x32_bf16 v[124:127], v[28:31], v[100:103], v[124:127]\n"
      "buffer_load_dword v15, v2, s[8:11], s45 offen sc1\n"
      "v_mfma_f32_16x16x32_bf16 v[124:127], v[32:35], v[104:107], v[124:127]\n"
      "v_mul_f32 v123, v219, v123\n"
      "v_mul_f32 v122, v219, v122\n"
      "v_mfma_f32_16x16x32_bf16 v[124:127], v[36:39], v[108:111], v[124:127]\n"
      "s_waitcnt vmcnt(4) lgkmcnt(1)\n"
      "ds_write_b32 v11, v9\n"
      "v_mfma_f32_16x16x32_bf16 v[124:127], v[40:43], v[112:115], v[124:127]\n"
      "v_mov_b32 v182, v180\n"
      "v_mfma_f32_16x16x32_bf16 v[124:127], v[44:47], v[116:119], v[124:127]\n"
      "v_cvt_pk_bf16_f32 v173, v122, v123\n"
      "v_mfma_f32_16x16x32_bf16 v[128:131], v[48:51], a[72:75], 0\n"
      "buffer_load_dwordx4 v0, s[8:11], s46 offen offset:1024 sc1 lds\n"
      "v_mfma_f32_16x16x32_bf16 v[128:131], v[52:55], a[76:79], v[128:131]\n"
      "ds_read_b128 v[220:223], v6 offset:0\n"
      "v_cvt_pk_bf16_f32 v172, v120, v121\n"
      "v_mfma_f32_16x16x32_bf16 v[128:131], v[56:59], a[80:83], v[128:131]\n"
      "v_fma_f32 v126, -v218, v142, v126\n"
      "v_fma_f32 v124, -v218, v140, v124\n"
      "v_mfma_f32_16x16x32_bf16 v[128:131], v[60:63], a[84:87], v[128:131]\n"
      "s_add_u32 s52, s52, 24576\n"
      "v_fma_f32 v125, -v218, v141, v125\n"
      "v_mul_f32 v124, v233, v124\n"
      "v_mfma_f32_16x16x32_bf16 v[128:131], v[64:67], a[88:91], v[128:131]\n"
      "v_mul_f32 v126, v219, v126\n"
      "v_mul_f32 v125, v233, v125\n"
      "v_mfma_f32_16x16x32_bf16 v[128:131], v[68:71], a[92:95], v[128:131]\n"
      "v_fma_f32 v127, -v218, v143, v127\n"
      "v_cvt_pk_bf16_f32 v175, v124, v125\n"
      "v_mfma_f32_16x16x32_bf16 v[128:131], v[72:75], a[96:99], v[128:131]\n"
      "v_mul_f32 v127, v219, v127\n"
      "v_cvt_pk_bf16_f32 v174, v126, v127\n"
      "v_mfma_f32_16x16x32_bf16 v[128:131], v[76:79], a[100:103], v[128:131]\n"
      "buffer_load_dwordx4 v0, s[8:11], s46 offen offset:2048 sc1 lds\n"
      "v_mfma_f32_16x16x32_bf16 v[192:195], v[172:175], v[176:179], v[156:159]\n"
      "ds_read_b32 v10, v12\n"
      "v_mfma_f32_16x16x32_bf16 v[196:199], v[172:175], v[164:167], v[168:171]\n"
      "v_mov_b32 v183, v181\n"
      "v_mov_b32 v178, v175\n"
      "v_mfma_f32_16x16x32_bf16 v[128:131], v[80:83], a[104:107], v[128:131]\n"
      "buffer_load_dwordx4 v0, s[8:11], s46 offen offset:0 sc1 lds\n"
      "v_mfma_f32_16x16x32_bf16 v[128:131], v[84:87], a[108:111], v[128:131]\n"
      "v_max_f32 v235, v194, v195\n"
      "v_max_f32 v234, v192, v193\n"
      "v_mfma_f32_16x16x32_bf16 v[128:131], v[88:91], a[112:115], v[128:131]\n"
      "v_max3_f32 v234, v188, v189, v234\n"
      "v_max3_f32 v235, v190, v191, v235\n"
      "v_mfma_f32_16x16x32_bf16 v[128:131], v[92:95], a[116:119], v[128:131]\n"
      "v_max_f32 v236, v234, v235\n"
      "v_mov_b32 v237, v236\n"
      "v_mfma_f32_16x16x32_bf16 v[128:131], v[96:99], a[120:123], v[128:131]\n"
      "v_permlane16_swap_b32 v236, v237\n"
      "v_mfma_f32_16x16x32_bf16 v[128:131], v[100:103], a[124:127], v[128:131]\n"
      "v_max_f32 v236, v236, v237\n"
      "v_mov_b32 v237, v236\n"
      "v_mfma_f32_16x16x32_bf16 v[128:131], v[104:107], a[128:131], v[128:131]\n"
      "s_waitcnt lgkmcnt(0)\n"
      "v_permlane32_swap_b32 v236, v237\n"
      "v_cmp_gt_i32 vcc, v9, v10\n"
      "v_mfma_f32_16x16x32_bf16 v[128:131], v[108:111], a[132:135], v[128:131]\n"
      "buffer_load_dwordx4 v0, s[8:11], s46 offen offset:3072 sc1 lds\n"
      "v_mfma_f32_16x16x32_bf16 v[128:131], v[112:115], a[136:139], v[128:131]\n"
      "s_add_u32 m0, s42, 40960\n"
      "v_max_f32 v236, v236, v237\n"
      "v_mul_f32 v236, v236, v232\n"
      "v_mfma_f32_16x16x32_bf16 v[128:131], v[116:119], a[140:143], v[128:131]\n"
      "s_add_u32 s40, s40, 24576\n"
      "v_fma_f32 v234, v234, v232, -v236\n"
      "v_fma_f32 v235, v235, v232, -v236\n"
      "v_mfma_f32_16x16x32_bf16 v[132:135], a[144:147], v[48:51], 0\n"
      "v_exp_f32 v234, v234\n"
      "v_mfma_f32_16x16x32_bf16 v[132:135], a[148:151], v[52:55], v[132:135]\n"
      "s_cbranch_vccnz .Lsp_qy1_5\n"
      ".Lbk_qy1_5:\n"
      "ds_read_b128 v[48:51], v0 offset:20480\n"
      "v_mfma_f32_16x16x32_bf16 v[132:135], a[152:155], v[56:59], v[132:135]\n"
      "v_exp_f32 v235, v235\n"
      "v_mfma_f32_16x16x32_bf16 v[132:135], a[156:159], v[60:63], v[132:135]\n"
      "ds_read_b128 v[56:59], v0 offset:22528\n"
      "v_fma_f32 v130, -v224, v147, v130\n"
      "v_mfma_f32_16x16x32_bf16 v[132:135], a[160:163], v[64:67], v[132:135]\n"
      "ds_read_b64 v[216:217], v4 offset:21504\n"
      "v_fma_f32 v129, -v222, v146, v129\n"
      "v_mfma_f32_16x16x32_bf16 v[132:135], a[164:167], v[68:71], v[132:135]\n"
      "buffer_load_dwordx4 v1, s[8:11], s46 offen offset:0 sc1 lds\n"
      "v_mfma_f32_16x16x32_bf16 v[132:135], a[168:171], v[72:75], v[132:135]\n"
      "v_cvt_pk_bf16_f32 v234, v234, v234\n"
      "v_fma_f32 v131, -v226, v144, v131\n"
      "v_mfma_f32_16x16x32_bf16 v[132:135], a[172:175], v[76:79], v[132:135]\n"
      "ds_read_b128 v[60:63], v0 offset:23552\n"
      "v_and_b32 v200, v160, v234\n"
      "v_mfma_f32_16x16x32_bf16 v[132:135], a[176:179], v[80:83], v[132:135]\n"
      "v_cvt_pk_bf16_f32 v235, v235, v235\n"
      "v_mul_f32 v129, v223, v129\n"
      "v_mfma_f32_16x16x32_bf16 v[132:135], a[180:183], v[84:87], v[132:135]\n"
      "ds_read_b128 v[52:55], v0 offset:21504\n"
      "v_mul_f32 v131, v227, v131\n"
      "v_mfma_f32_16x16x32_bf16 v[132:135], a[184:187], v[88:91], v[132:135]\n"
      "ds_read_b128 v[88:91], v0 offset:30720\n"
      "v_mul_f32 v130, v225, v130\n"
      "v_cvt_pk_bf16_f32 v185, v198, v199\n"
      "v_mfma_f32_16x16x32_bf16 v[132:135], a[188:191], v[92:95], v[132:135]\n"
      "v_cvt_pk_bf16_f32 v184, v196, v197\n"
      "v_and_b32 v201, v161, v235\n"
      "v_fma_f32 v128, -v220, v145, v128\n"
      "v_mfma_f32_16x16x32_bf16 v[132:135], a[192:195], v[96:99], v[132:135]\n"
      "ds_read_b128 v[68:71], v0 offset:25600\n"
      "v_and_b32 v203, v163, v235\n"
      "v_mfma_f32_16x16x32_bf16 v[132:135], a[196:199], v[100:103], v[132:135]\n"
      "v_mul_f32 v128, v221, v128\n"
      "v_and_b32 v202, v162, v234\n"
      "v_mfma_f32_16x16x32_bf16 v[132:135], a[200:203], v[104:107], v[132:135]\n"
      "v_cvt_pk_bf16_f32 v180, v128, v129\n"
      "v_cvt_pk_bf16_f32 v181, v130, v131\n"
      "v_mfma_f32_16x16x32_bf16 v[208:211], v[184:187], v[200:203], 0\n"
      "ds_read_b128 v[64:67], v0 offset:24576\n"
      "ds_read_b128 v[72:75], v0 offset:26624\n"
      "v_mfma_f32_16x16x32_bf16 v[204:207], v[180:183], v[200:203], 0\n"
      "s_waitcnt vmcnt(6) lgkmcnt(4)\n"
      "buffer_store_dword v13, v3, s[8:11], s52 offen sc1\n"
      "v_mfma_f32_16x16x32_bf16 v[132:135], a[204:207], v[108:111], v[132:135]\n"
      "ds_read2st64_b64 v[212:215], v4 offset0:40 offset1:41\n"
      "v_mfma_f32_16x16x32_bf16 v[132:135], a[208:211], v[112:115], v[132:135]\n"
      "v_rcp_f32 v210, v210\n"
      "v_mfma_f32_16x16x32_bf16 v[132:135], a[212:215], v[116:119], v[132:135]\n"
      "ds_read_b128 v[76:79], v0 offset:27648\n"
      "v_mul_f32 v209, v209, v210\n"
      "v_mfma_f32_16x16x32_bf16 v[120:123], a[0:3], v[48:51], 0\n"
      "ds_read_b128 v[80:83], v0 offset:28672\n"
      "ds_read_b128 v[100:103], v0 offset:33792\n"
      "v_mfma_f32_16x16x32_bf16 v[120:123], a[4:7], v[52:55], v[120:123]\n"
      "ds_read_b128 v[92:95], v0 offset:31744\n"
      "v_mul_f32 v208, v208, v210\n"
      "v_mfma_f32_16x16x32_bf16 v[120:123], a[8:11], v[56:59], v[120:123]\n"
      "ds_read_b128 v[84:87], v0 offset:29696\n"
      "v_fma_f32 v134, -v218, v150, v134\n"
      "v_mfma_f32_16x16x32_bf16 v[120:123], a[12:15], v[60:63], v[120:123]\n"
      "s_waitcnt lgkmcnt(6)\n"
      "ds_read_b128 v[104:107], v0 offset:34816\n"
      "v_fma_f32 v132, -v218, v148, v132\n"
      "v_mfma_f32_16x16x32_bf16 v[120:123], a[16:19], v[64:67], v[120:123]\n"
      "s_mov_b32 s50, s49\n"
      "ds_read_b128 v[96:99], v0 offset:32768\n"
      "v_mfma_f32_16x16x32_bf16 v[120:123], a[20:23], v[68:71], v[120:123]\n"
      "ds_read_b128 v[108:111], v0 offset:35840\n"
      "v_fma_f32 v133, -v218, v149, v133\n"
      "v_mfma_f32_16x16x32_bf16 v[120:123], a[24:27], v[72:75], v[120:123]\n"
      "s_waitcnt lgkmcnt(7)\n"
      "v_fma_f32 v135, -v218, v151, v135\n"
      "v_cvt_pk_bf16_f32 v177, v134, v135\n"
      "v_mfma_f32_16x16x32_bf16 v[120:123], a[28:31], v[76:79], v[120:123]\n"
      "s_waitcnt lgkmcnt(5)\n"
      "ds_read_b128 v[112:115], v0 offset:36864\n"
      "v_cvt_pk_bf16_f32 v176, v132, v133\n"
      "v_mfma_f32_16x16x32_bf16 v[120:123], a[32:35], v[80:83], v[120:123]\n"
      "v_mov_b32 v187, v185\n"
      "v_mul_f32 v232, 0x3eae1a93, v219\n"
      "v_mfma_f32_16x16x32_bf16 v[188:191], v[172:175], v[176:179], v[152:155]\n"
      "s_waitcnt lgkmcnt(4)\n"
      "ds_read_b128 v[116:119], v0 offset:37888\n"
      "v_mul_f32 v207, v207, v210\n"
      "v_mfma_f32_16x16x32_bf16 v[120:123], a[36:39], v[84:87], v[120:123]\n"
      "v_mul_f32 v204, v204, v210\n"
      "v_mfma_f32_16x16x32_bf16 v[120:123], a[40:43], v[88:91], v[120:123]\n"
      "v_cvt_pk_bf16_f32 v230, v208, v209\n"
      "v_add_f32 v213, v213, v215\n"
      "v_mfma_f32_16x16x32_bf16 v[120:123], a[44:47], v[92:95], v[120:123]\n"
      "s_waitcnt lgkmcnt(2)\n"
      "v_mul_f32 v205, v205, v210\n"
      "v_mfma_f32_16x16x32_bf16 v[120:123], a[48:51], v[96:99], v[120:123]\n"
      "buffer_store_dwordx2 v[230:231], v8, s[8:11], s40 offen sc1\n"
      "v_mfma_f32_16x16x32_bf16 v[120:123], a[52:55], v[100:103], v[120:123]\n"
      "v_add_f32 v212, v212, v214\n"
      "v_mfma_f32_16x16x32_bf16 v[120:123], a[56:59], v[104:107], v[120:123]\n"
      "v_cvt_pk_bf16_f32 v228, v204, v205\n"
      "v_mul_f32 v206, v206, v210\n"
      "v_mfma_f32_16x16x32_bf16 v[120:123], a[60:63], v[108:111], v[120:123]\n"
      "s_waitcnt vmcnt(7) lgkmcnt(0)\n"
      "v_cvt_pk_bf16_f32 v229, v206, v207\n"
      "v_cmp_ne_u32 vcc, s21, v15\n"
      "v_mfma_f32_16x16x32_bf16 v[120:123], a[64:67], v[112:115], v[120:123]\n"
      "s_add_u32 m0, s41, 0\n"
      "v_mov_b32 v182, v180\n"
      "v_mfma_f32_16x16x32_bf16 v[120:123], a[68:71], v[116:119], v[120:123]\n"
      "buffer_store_dwordx2 v[228:229], v7, s[8:11], s40 offen sc1\n"
      "v_mfma_f32_16x16x32_bf16 v[124:127], a[216:219], v[48:51], 0\n"
      "v_add_f32 v218, v212, v216\n"
      "v_add_f32_e64 v213, v213, v217\n"
      "v_mfma_f32_16x16x32_bf16 v[124:127], a[220:223], v[52:55], v[124:127]\n"
      "s_add_u32 s36, s36, 24576\n"
      "v_fma_f32 v213, -v218, v218, v213\n"
      "v_mfma_f32_16x16x32_bf16 v[124:127], a[224:227], v[56:59], v[124:127]\n"
      "s_mov_b32 s46, s45\n"
      "s_cbranch_vccnz .Lsp_q0_6\n"
      ".Lbk_q0_6:\n"
      "v_mfma_f32_16x16x32_bf16 v[124:127], a[228:231], v[60:63], v[124:127]\n"
      "buffer_load_dwordx4 v0, s[8:11], s46 offen offset:1024 sc1 lds\n"
      "v_mfma_f32_16x16x32_bf16 v[124:127], a[232:235], v[64:67], v[124:127]\n"
      "v_max_f32 v213, 0x3727c5ac, v213\n"
      "v_fma_f32 v120, -v218, v136, v120\n"
      "v_mfma_f32_16x16x32_bf16 v[124:127], a[236:239], v[68:71], v[124:127]\n"
      "v_mov_b32 v186, v184\n"
      "v_mfma_f32_16x16x32_bf16 v[124:127], a[240:243], v[72:75], v[124:127]\n"
      "v_add_u32 v9, 1, v9\n"
      "v_mfma_f32_16x16x32_bf16 v[124:127], a[244:247], v[76:79], v[124:127]\n"
      "s_add_u32 s40, s40, 24576\n"
      "v_fma_f32 v123, -v218, v139, v123\n"
      "v_mfma_f32_16x16x32_bf16 v[124:127], a[248:251], v[80:83], v[124:127]\n"
      "v_rsq_f32 v219, v213\n"
      "v_mfma_f32_16x16x32_bf16 v[124:127], a[252:255], v[84:87], v[124:127]\n"
      "v_mfma_f32_16x16x32_bf16 v[124:127], v[16:19], v[88:91], v[124:127]\n"
      "v_fma_f32 v121, -v218, v137, v121\n"
      "v_fma_f32 v122, -v218, v138, v122\n"
      "v_mfma_f32_16x16x32_bf16 v[124:127], v[20:23], v[92:95], v[124:127]\n"
      "buffer_load_dwordx4 v0, s[8:11], s46 offen offset:0 sc1 lds\n"
      "v_mfma_f32_16x16x32_bf16 v[124:127], v[24:27], v[96:99], v[124:127]\n"
      "v_mul_f32 v120, v219, v120\n"
      "v_mul_f32 v122, v219, v122\n"
      "v_mfma_f32_16x16x32_bf16 v[124:127], v[28:31], v[100:103], v[124:127]\n"
      "ds_write_b64 v5, v[218:219] offset:512\n"
      "v_mfma_f32_16x16x32_bf16 v[124:127], v[32:35], v[104:107], v[124:127]\n"
      "v_mul_f32 v121, v219, v121\n"
      "v_mul_f32 v123, v219, v123\n"
      "v_mfma_f32_16x16x32_bf16 v[124:127], v[36:39], v[108:111], v[124:127]\n"
      "buffer_load_dwordx4 v0, s[8:11], s46 offen offset:2048 sc1 lds\n"
      "v_mfma_f32_16x16x32_bf16 v[124:127], v[40:43], v[112:115], v[124:127]\n"
      "s_mov_b32 s45, s36\n"
      "ds_read_b128 v[224:227], v6 offset:528\n"
      "v_mfma_f32_16x16x32_bf16 v[124:127], v[44:47], v[116:119], v[124:127]\n"
      "s_min_u32 s45, s45, s6\n"
      "v_cndmask_b32 v233, v219, 1.0, s[54:55]\n"
      "v_mfma_f32_16x16x32_bf16 v[128:131], v[48:51], a[72:75], 0\n"
      "s_add_u32 s52, s52, 24576\n"
      "v_cvt_pk_bf16_f32 v172, v120, v121\n"
      "v_cvt_pk_bf16_f32 v173, v122, v123\n"
      "v_mfma_f32_16x16x32_bf16 v[128:131], v[52:55], a[76:79], v[128:131]\n"
      "buffer_load_dwordx4 v0, s[8:11], s46 offen offset:3072 sc1 lds\n"
      "v_mfma_f32_16x16x32_bf16 v[128:131], v[56:59], a[80:83], v[128:131]\n"
      "v_fma_f32 v126, -v218, v142, v126\n"
      "v_fma_f32 v124, -v218, v140, v124\n"
      "v_mul_f32 v126, v219, v126\n"
      "v_mfma_f32_16x16x32_bf16 v[128:131], v[60:63], a[84:87], v[128:131]\n"
      "v_fma_f32 v125, -v218, v141, v125\n"
      "v_mul_f32 v125, v233, v125\n"
      "v_mfma_f32_16x16x32_bf16 v[128:131], v[64:67], a[88:91], v[128:131]\n"
      "s_waitcnt vmcnt(7) lgkmcnt(1)\n"
      "ds_write_b32 v11, v9\n"
      "v_mfma_f32_16x16x32_bf16 v[128:131], v[68:71], a[92:95], v[128:131]\n"
      "v_fma_f32 v127, -v218, v143, v127\n"
      "v_mul_f32 v124, v233, v124\n"
      "v_mul_f32 v127, v219, v127\n"
      "v_mfma_f32_16x16x32_bf16 v[128:131], v[72:75], a[96:99], v[128:131]\n"
      "v_cvt_pk_bf16_f32 v174, v126, v127\n"
      "v_cvt_pk_bf16_f32 v175, v124, v125\n"
      "v_mfma_f32_16x16x32_bf16 v[128:131], v[76:79], a[100:103], v[128:131]\n"
      "buffer_load_dword v15, v2, s[8:11], s45 offen sc1\n"
      "v_mfma_f32_16x16x32_bf16 v[192:195], v[172:175], v[176:179], v[156:159]\n"
      "v_mov_b32 v183, v181\n"
      "v_mfma_f32_16x16x32_bf16 v[196:199], v[172:175], v[164:167], v[168:171]\n"
      "ds_read_b128 v[220:223], v6 offset:512\n"
      "s_add_u32 m0, s42, 0\n"
      "v_mfma_f32_16x16x32_bf16 v[128:131], v[80:83], a[104:107], v[128:131]\n"
      "buffer_load_dwordx4 v1, s[8:11], s46 offen offset:0 sc1 lds\n"
      "v_mfma_f32_16x16x32_bf16 v[128:131], v[84:87], a[108:111], v[128:131]\n"
      "v_max_f32 v235, v194, v195\n"
      "v_max3_f32 v235, v190, v191, v235\n"
      "v_mfma_f32_16x16x32_bf16 v[128:131], v[88:91], a[112:115], v[128:131]\n"
      "v_max_f32 v234, v192, v193\n"
      "v_max3_f32 v234, v188, v189, v234\n"
      "v_mfma_f32_16x16x32_bf16 v[128:131], v[92:95], a[116:119], v[128:131]\n"
      "ds_read_b32 v10, v12\n"
      "v_max_f32 v236, v234, v235\n"
      "v_mfma_f32_16x16x32_bf16 v[128:131], v[96:99], a[120:123], v[128:131]\n"
      "v_mov_b32 v237, v236\n"
      "v_cvt_pk_bf16_f32 v185, v198, v199\n"
      "v_mfma_f32_16x16x32_bf16 v[128:131], v[100:103], a[124:127], v[128:131]\n"
      "v_permlane16_swap_b32 v236, v237\n"
      "v_mfma_f32_16x16x32_bf16 v[128:131], v[104:107], a[128:131], v[128:131]\n"
      "v_max_f32 v236, v236, v237\n"
      "v_mov_b32 v237, v236\n"
      "v_mfma_f32_16x16x32_bf16 v[128:131], v[108:111], a[132:135], v[128:131]\n"
      "v_permlane32_swap_b32 v236, v237\n"
      "v_mfma_f32_16x16x32_bf16 v[128:131], v[112:115], a[136:139], v[128:131]\n"
      "s_waitcnt lgkmcnt(0)\n"
      "v_cmp_gt_i32 vcc, v9, v10\n"
      "v_max_f32 v236, v236, v237\n"
      "v_mfma_f32_16x16x32_bf16 v[128:131], v[116:119], a[140:143], v[128:131]\n"
      "v_mul_f32_e64 v236, v236, v232\n"
      "v_fma_f32 v235, v235, v232, -v236\n"
      "v_mfma_f32_16x16x32_bf16 v[132:135], a[144:147], v[48:51], 0\n"
      "v_exp_f32_e64 v235, v235\n"
      "v_mfma_f32_16x16x32_bf16 v[132:135], a[148:151], v[52:55], v[132:135]\n"
      "s_cbranch_vccnz .Lsp_qy2_7\n"
      ".Lbk_qy2_7:\n"
      "v_cvt_pk_bf16_f32 v235, v235, v235\n"
      "v_mfma_f32_16x16x32_bf16 v[132:135], a[152:155], v[56:59], v[132:135]\n"
      "v_fma_f32 v130, -v224, v147, v130\n"
      "v_mul_f32 v130, v225, v130\n"
      "v_mfma_f32_16x16x32_bf16 v[132:135], a[156:159], v[60:63], v[132:135]\n"
      "v_fma_f32 v129, -v222, v146, v129\n"
      "v_cvt_pk_bf16_f32 v184, v196, v197\n"
      "v_mfma_f32_16x16x32_bf16 v[132:135], a[160:163], v[64:67], v[132:135]\n"
      "v_fma_f32 v234, v234, v232, -v236\n"
      "v_fma_f32 v131, -v226, v144, v131\n"
      "v_mfma_f32_16x16x32_bf16 v[132:135], a[164:167], v[68:71], v[132:135]\n"
      "ds_read_b128 v[52:55], v0 offset:41984\n"
      "ds_read_b128 v[64:67], v0 offset:45056\n"
      "v_mfma_f32_16x16x32_bf16 v[132:135], a[168:171], v[72:75], v[132:135]\n"
      "ds_read_b128 v[56:59], v0 offset:43008\n"
      "v_mul_f32 v129, v223, v129\n"
      "v_mfma_f32_16x16x32_bf16 v[132:135], a[172:175], v[76:79], v[132:135]\n"
      "v_exp_f32 v234, v234\n"
      "v_mfma_f32_16x16x32_bf16 v[132:135], a[176:179], v[80:83], v[132:135]\n"
      "v_fma_f32 v128, -v220, v145, v128\n"
      "v_and_b32 v203, v163, v235\n"
      "v_mfma_f32_16x16x32_bf16 v[132:135], a[180:183], v[84:87], v[132:135]\n"
      "v_cvt_pk_bf16_f32 v234, v234, v234\n"
      "v_and_b32 v201, v161, v235\n"
      "v_mfma_f32_16x16x32_bf16 v[132:135], a[184:187], v[88:91], v[132:135]\n"
      "ds_read_b128 v[48:51], v0 offset:40960\n"
      "v_and_b32 v202, v162, v234\n"
      "v_mfma_f32_16x16x32_bf16 v[132:135], a[188:191], v[92:95], v[132:135]\n"
      "ds_read_b128 v[60:63], v0 offset:44032\n"
      "v_mul_f32 v128, v221, v128\n"
      "v_mfma_f32_16x16x32_bf16 v[132:135], a[192:195], v[96:99], v[132:135]\n"
      "ds_read_b128 v[76:79], v0 offset:48128\n"
      "v_cvt_pk_bf16_f32 v180, v128, v129\n"
      "v_mfma_f32_16x16x32_bf16 v[132:135], a[196:199], v[100:103], v[132:135]\n"
      "ds_read_b128 v[100:103], v0 offset:54272\n"
      "v_and_b32 v200, v160, v234\n"
      "v_mfma_f32_16x16x32_bf16 v[132:135], a[200:203], v[104:107], v[132:135]\n"
      "v_mul_f32 v131, v227, v131\n"
      "v_cvt_pk_bf16_f32 v181, v130, v131\n"
      "v_mfma_f32_16x16x32_bf16 v[208:211], v[184:187], v[200:203], 0\n"
      "s_waitcnt vmcnt(6) lgkmcnt(4)\n"
      "buffer_store_dword v13, v3, s[8:11], s52 offen sc1\n"
      "v_mfma_f32_16x16x32_bf16 v[204:207], v[180:183], v[200:203], 0\n"
      "ds_read_b128 v[84:87], v0 offset:50176\n"
      "ds_read_b128 v[72:75], v0 offset:47104\n"
      "v_mfma_f32_16x16x32_bf16 v[132:135], a[204:207], v[108:111], v[132:135]\n"
      "ds_read_b128 v[68:71], v0 offset:46080\n"
      "v_mov_b32 v178, v175\n"
      "v_mfma_f32_16x16x32_bf16 v[132:135], a[208:211], v[112:115], v[132:135]\n"
      "v_rcp_f32 v210, v210\n"
      "v_mfma_f32_16x16x32_bf16 v[132:135], a[212:215], v[116:119], v[132:135]\n"
      "s_waitcnt lgkmcnt(4)\n"
      "ds_read_b128 v[92:95], v0 offset:52224\n"
      "v_mul_f32 v205, v205, v210\n"
      "v_mfma_f32_16x16x32_bf16 v[120:123], a[0:3], v[48:51], 0\n"
      "s_add_u32 s36, s36, 24576\n"
      "ds_read_b128 v[80:83], v0 offset:49152\n"
      "v_mov_b32 v182, v180\n"
      "v_mfma_f32_16x16x32_bf16 v[120:123], a[4:7], v[52:55], v[120:123]\n"
      "ds_read_b128 v[96:99], v0 offset:53248\n"
      "v_mfma_f32_16x16x32_bf16 v[120:123], a[8:11], v[56:59], v[120:123]\n"
      "ds_read_b128 v[88:91], v0 offset:51200\n"
      "v_fma_f32 v132, -v218, v148, v132\n"
      "v_mfma_f32_16x16x32_bf16 v[120:123], a[12:15], v[60:63], v[120:123]\n"
      "s_cmp_eq_u32 s36, s37\n"
      "v_fma_f32 v133, -v218, v149, v133\n"
      "v_cvt_pk_bf16_f32 v176, v132, v133\n"
      "v_mfma_f32_16x16x32_bf16 v[120:123], a[16:19], v[64:67], v[120:123]\n"
      "s_waitcnt lgkmcnt(4)\n"
      "ds_read2st64_b64 v[212:215], v4 offset0:80 offset1:81\n"
      "s_cselect_b32 s47, s38, 0\n"
      "v_mfma_f32_16x16x32_bf16 v[120:123], a[20:23], v[68:71], v[120:123]\n"
      "s_cselect_b32 s37, s48, s37\n"
      "ds_read_b128 v[112:115], v0 offset:57344\n"
      "v_fma_f32 v135, -v218, v151, v135\n"
      "v_mfma_f32_16x16x32_bf16 v[120:123], a[24:27], v[72:75], v[120:123]\n"
      "s_cselect_b32 s49, s39, s53\n"
      "v_fma_f32 v134, -v218, v150, v134\n"
      "v_mov_b32 v183, v181\n"
      "v_mfma_f32_16x16x32_bf16 v[120:123], a[28:31], v[76:79], v[120:123]\n"
      "s_waitcnt lgkmcnt(4)\n"
      "ds_read_b128 v[104:107], v0 offset:55296\n"
      "v_cvt_pk_bf16_f32 v177, v134, v135\n"
      "v_mfma_f32_16x16x32_bf16 v[120:123], a[32:35], v[80:83], v[120:123]\n"
      "ds_read_b128 v[108:111], v0 offset:56320\n"
      "v_mul_f32 v206, v206, v210\n"
      "v_mfma_f32_16x16x32_bf16 v[188:191], v[172:175], v[176:179], v[152:155]\n"
      "ds_read_b64 v[216:217], v4 offset:41984\n"
      "v_mul_f32 v207, v207, v210\n"
      "v_mfma_f32_16x16x32_bf16 v[120:123], a[36:39], v[84:87], v[120:123]\n"
      "s_waitcnt lgkmcnt(5)\n"
      "s_add_u32 m0, s41, 20480\n"
      "ds_read_b128 v[116:119], v0 offset:58368\n"
      "v_mfma_f32_16x16x32_bf16 v[120:123], a[40:43], v[88:91], v[120:123]\n"
      "s_mov_b32 s46, s45\n"
      "s_add_u32 s45, s36, s47\n"
      "v_mul_f32 v209, v209, v210\n"
      "v_mfma_f32_16x16x32_bf16 v[120:123], a[44:47], v[92:95], v[120:123]\n"
      "v_cvt_pk_bf16_f32 v229, v206, v207\n"
      "v_mfma_f32_16x16x32_bf16 v[120:123], a[48:51], v[96:99], v[120:123]\n"
      "s_waitcnt lgkmcnt(4)\n"
      "v_add_f32 v213, v213, v215\n"
      "v_mfma_f32_16x16x32_bf16 v[120:123], a[52:55], v[100:103], v[120:123]\n"
      "s_waitcnt lgkmcnt(3)\n"
      "v_mul_f32 v232, 0x3eae1a93, v219\n"
      "v_mul_f32 v204, v204, v210\n"
      "v_mfma_f32_16x16x32_bf16 v[120:123], a[56:59], v[104:107], v[120:123]\n"
      "s_waitcnt lgkmcnt(2)\n"
      "v_mul_f32 v208, v208, v210\n"
      "v_cvt_pk_bf16_f32 v230, v208, v209\n"
      "v_mfma_f32_16x16x32_bf16 v[120:123], a[60:63], v[108:111], v[120:123]\n"
      "v_mfma_f32_16x16x32_bf16 v[120:123], a[64:67], v[112:115], v[120:123]\n"
      "s_waitcnt lgkmcnt(0)\n"
      "buffer_store_dwordx2 v[230:231], v8, s[8:11], s40 offen sc1\n"
      "v_mfma_f32_16x16x32_bf16 v[120:123], a[68:71], v[116:119], v[120:123]\n"
      "v_cvt_pk_bf16_f32 v228, v204, v205\n"
      "v_mfma_f32_16x16x32_bf16 v[124:127], a[216:219], v[48:51], 0\n"
      "v_add_f32 v212, v212, v214\n"
      "v_add_f32 v213, v213, v217\n"
      "v_mfma_f32_16x16x32_bf16 v[124:127], a[220:223], v[52:55], v[124:127]\n"
      "v_add_f32 v218, v212, v216\n"
      "v_fma_f32 v213, -v218, v218, v213\n"
      "v_mfma_f32_16x16x32_bf16 v[124:127], a[224:227], v[56:59], v[124:127]\n"
      "buffer_store_dwordx2 v[228:229], v7, s[8:11], s40 offen sc1\n"
      "v_mfma_f32_16x16x32_bf16 v[124:127], a[228:231], v[60:63], v[124:127]\n"
      "s_add_u32 s52, s52, 24576\n"
      "v_fma_f32 v123, -v218, v139, v123\n"
      "v_add_u32 v9, 1, v9\n"
      "v_mfma_f32_16x16x32_bf16 v[124:127], a[232:235], v[64:67], v[124:127]\n"
      "v_fma_f32 v122, -v218, v138, v122\n"
      "v_mfma_f32_16x16x32_bf16 v[124:127], a[236:239], v[68:71], v[124:127]\n"
      "s_waitcnt vmcnt(4)\n"
      "v_max_f32 v213, 0x3727c5ac, v213\n"
      "v_cmp_ne_u32 vcc, s21, v15\n"
      "v_mfma_f32_16x16x32_bf16 v[124:127], a[240:243], v[72:75], v[124:127]\n"
      "v_rsq_f32 v219, v213\n"
      "v_mfma_f32_16x16x32_bf16 v[124:127], a[244:247], v[76:79], v[124:127]\n"
      "v_mov_b32 v186, v184\n"
      "v_fma_f32 v120, -v218, v136, v120\n"
      "v_mfma_f32_16x16x32_bf16 v[124:127], a[248:251], v[80:83], v[124:127]\n"
      "s_min_u32 s45, s45, s6\n"
      "ds_write_b64 v5, v[218:219] offset:1024\n"
      "v_mfma_f32_16x16x32_bf16 v[124:127], a[252:255], v[84:87], v[124:127]\n"
      "s_cbranch_vccnz .Lsp_q1_8\n"
      ".Lbk_q1_8:\n"
      "v_mfma_f32_16x16x32_bf16 v[124:127], v[16:19], v[88:91], v[124:127]\n"
      "buffer_load_dwordx4 v0, s[8:11], s46 offen offset:1024 sc1 lds\n"
      "v_mfma_f32_16x16x32_bf16 v[124:127], v[20:23], v[92:95], v[124:127]\n"
      "v_cndmask_b32 v233, v219, 1.0, s[54:55]\n"
      "v_mul_f32 v123, v219, v123\n"
      "v_mfma_f32_16x16x32_bf16 v[124:127], v[24:27], v[96:99], v[124:127]\n"
      "ds_read_b128 v[220:223], v6 offset:1024\n"
      "v_mul_f32 v120, v219, v120\n"
      "v_mfma_f32_16x16x32_bf16 v[124:127], v[28:31], v[100:103], v[124:127]\n"
      "v_mul_f32 v122, v219, v122\n"
      "v_fma_f32 v121, -v218, v137, v121\n"
      "v_mfma_f32_16x16x32_bf16 v[124:127], v[32:35], v[104:107], v[124:127]\n"
      "s_waitcnt vmcnt(4)\n"
      "ds_write_b32 v11, v9\n"
      "v_mfma_f32_16x16x32_bf16 v[124:127], v[36:39], v[108:111], v[124:127]\n"
      "buffer_load_dwordx4 v0, s[8:11], s46 offen offset:0 sc1 lds\n"
      "v_mfma_f32_16x16x32_bf16 v[124:127], v[40:43], v[112:115], v[124:127]\n"
      "v_mfma_f32_16x16x32_bf16 v[124:127], v[44:47], v[116:119], v[124:127]\n"
      "v_cvt_pk_bf16_f32 v173, v122, v123\n"
      "v_mfma_f32_16x16x32_bf16 v[128:131], v[48:51], a[72:75], 0\n"
      "v_mul_f32 v121, v219, v121\n"
      "v_cvt_pk_bf16_f32 v172, v120, v121\n"
      "v_mfma_f32_16x16x32_bf16 v[128:131], v[52:55], a[76:79], v[128:131]\n"
      "buffer_load_dwordx4 v0, s[8:11], s46 offen offset:3072 sc1 lds\n"
      "v_mfma_f32_16x16x32_bf16 v[128:131], v[56:59], a[80:83], v[128:131]\n"
      "v_fma_f32 v125, -v218, v141, v125\n"
      "v_fma_f32 v124, -v218, v140, v124\n"
      "v_mfma_f32_16x16x32_bf16 v[128:131], v[60:63], a[84:87], v[128:131]\n"
      "v_fma_f32 v127, -v218, v143, v127\n"
      "v_mul_f32 v125, v233, v125\n"
      "v_mfma_f32_16x16x32_bf16 v[128:131], v[64:67], a[88:91], v[128:131]\n"
      "v_mul_f32 v124, v233, v124\n"
      "v_mul_f32 v127, v219, v127\n"
      "v_mfma_f32_16x16x32_bf16 v[128:131], v[68:71], a[92:95], v[128:131]\n"
      "v_fma_f32 v126, -v218, v142, v126\n"
      "v_mul_f32 v126, v219, v126\n"
      "v_mfma_f32_16x16x32_bf16 v[128:131], v[72:75], a[96:99], v[128:131]\n"
      "v_cvt_pk_bf16_f32 v175, v124, v125\n"
      "v_cvt_pk_bf16_f32 v174, v126, v127\n"
      "v_mfma_f32_16x16x32_bf16 v[128:131], v[76:79], a[100:103], v[128:131]\n"
      "buffer_load_dword v15, v2, s[8:11], s45 offen sc1\n"
      "v_mfma_f32_16x16x32_bf16 v[192:195], v[172:175], v[176:179], v[156:159]\n"
      "ds_read_b128 v[224:227], v6 offset:1040\n"
      "v_mov_b32 v187, v185\n"
      "v_mfma_f32_16x16x32_bf16 v[196:199], v[172:175], v[164:167], v[168:171]\n"
      "ds_read_b32 v10, v12\n"
      "v_mfma_f32_16x16x32_bf16 v[128:131], v[80:83], a[104:107], v[128:131]\n"
      "buffer_load_dwordx4 v0, s[8:11], s46 offen offset:2048 sc1 lds\n"
      "v_mfma_f32_16x16x32_bf16 v[128:131], v[84:87], a[108:111], v[128:131]\n"
      "v_med3_f32 v234, v192, v193, s50\n"
      "v_med3_f32 v235, v194, v195, s50\n"
      "v_mfma_f32_16x16x32_bf16 v[128:131], v[88:91], a[112:115], v[128:131]\n"
      "v_cvt_pk_bf16_f32 v185, v198, v199\n"
      "v_max3_f32 v234, v188, v189, v234\n"
      "v_mfma_f32_16x16x32_bf16 v[128:131], v[92:95], a[116:119], v[128:131]\n"
      "v_max3_f32 v235, v190, v191, v235\n"
      "v_max_f32 v236, v234, v235\n"
      "v_mfma_f32_16x16x32_bf16 v[128:131], v[96:99], a[120:123], v[128:131]\n"
      "s_waitcnt lgkmcnt(0)\n"
      "v_mov_b32 v237, v236\n"
      "v_cmp_gt_i32 vcc, v9, v10\n"
      "v_mfma_f32_16x16x32_bf16 v[128:131], v[100:103], a[124:127], v[128:131]\n"
      "s_add_u32 m0, s42, 20480\n"
      "v_permlane16_swap_b32 v236, v237\n"
      "v_mfma_f32_16x16x32_bf16 v[128:131], v[104:107], a[128:131], v[128:131]\n"
      "v_max_f32 v236, v236, v237\n"
      "v_mov_b32 v237, v236\n"
      "v_mfma_f32_16x16x32_bf16 v[128:131], v[108:111], a[132:135], v[128:131]\n"
      "buffer_load_dwordx4 v1, s[8:11], s46 offen offset:0 sc1 lds\n"
      "v_mfma_f32_16x16x32_bf16 v[128:131], v[112:115], a[136:139], v[128:131]\n"
      ".Lq_p1:\n"
      "s_cbranch_execz .Lq_pr\n"
      "v_permlane32_swap_b32 v236, v237\n"
      "v_mfma_f32_16x16x32_bf16 v[128:131], v[116:119], a[140:143], v[128:131]\n"
      "v_max_f32 v236, v236, v237\n"
      "v_mul_f32_e64 v236, v236, v232\n"
      "v_mfma_f32_16x16x32_bf16 v[132:135], a[144:147], v[48:51], 0\n"
      "s_cbranch_vccnz .Lsp_qy0_9\n"
      ".Lbk_qy0_9:\n"
      "v_fma_f32 v234, v234, v232, -v236\n"
      "v_mfma_f32_16x16x32_bf16 v[132:135], a[148:151], v[52:55], v[132:135]\n"
      "ds_read_b128 v[52:55], v0 offset:1024\n"
      "ds_read_b128 v[48:51], v0 offset:0\n"
      "v_mfma_f32_16x16x32_bf16 v[132:135], a[152:155], v[56:59], v[132:135]\n"
      "v_fma_f32 v131, -v226, v144, v131\n"
      "v_fma_f32 v235, v235, v232, -v236\n"
      "v_mfma_f32_16x16x32_bf16 v[132:135], a[156:159], v[60:63], v[132:135]\n"
      "ds_read_b128 v[56:59], v0 offset:2048\n"
      "v_fma_f32 v128, -v220, v145, v128\n"
      "v_mfma_f32_16x16x32_bf16 v[132:135], a[160:163], v[64:67], v[132:135]\n"
      "v_exp_f32 v234, v234\n"
      "v_mfma_f32_16x16x32_bf16 v[132:135], a[164:167], v[68:71], v[132:135]\n"
      "v_exp_f32 v235, v235\n"
      "v_mfma_f32_16x16x32_bf16 v[132:135], a[168:171], v[72:75], v[132:135]\n"
      "v_cvt_pk_bf16_f32 v234, v234, v234\n"
      "v_fma_f32 v129, -v222, v146, v129\n"
      "v_mfma_f32_16x16x32_bf16 v[132:135], a[172:175], v[76:79], v[132:135]\n"
      "v_mul_f32 v129, v223, v129\n"
      "v_fma_f32 v130, -v224, v147, v130\n"
      "v_mfma_f32_16x16x32_bf16 v[132:135], a[176:179], v[80:83], v[132:135]\n"
      "v_cvt_pk_bf16_f32 v235, v235, v235\n"
      "v_and_b32 v200, v160, v234\n"
      "v_mfma_f32_16x16x32_bf16 v[132:135], a[180:183], v[84:87], v[132:135]\n"
      "v_cvt_pk_bf16_f32 v184, v196, v197\n"
      "v_and_b32 v201, v161, v235\n"
      "v_mfma_f32_16x16x32_bf16 v[132:135], a[184:187], v[88:91], v[132:135]\n"
      "ds_read_b128 v[68:71], v0 offset:5120\n"
      "v_mul_f32 v130, v225, v130\n"
      "v_mfma_f32_16x16x32_bf16 v[132:135], a[188:191], v[92:95], v[132:135]\n"
      "v_and_b32 v203, v163, v235\n"
      "v_mul_f32_e64 v131, v227, v131\n"
      "v_mfma_f32_16x16x32_bf16 v[132:135], a[192:195], v[96:99], v[132:135]\n"
      "ds_read_b128 v[64:67], v0 offset:4096\n"
      "v_mul_f32_e64 v128, v221, v128\n"
      "v_mfma_f32_16x16x32_bf16 v[132:135], a[196:199], v[100:103], v[132:135]\n"
      "v_and_b32_e64 v202, v162, v234\n"
      "v_cvt_pk_bf16_f32 v181, v130, v131\n"
      "v_mfma_f32_16x16x32_bf16 v[132:135], a[200:203], v[104:107], v[132:135]\n"
      "s_add_u32 s40, s40, 24576\n"
      "ds_read_b128 v[60:63], v0 offset:3072\n"
      "v_cvt_pk_bf16_f32 v180, v128, v129\n"
      "v_mfma_f32_16x16x32_bf16 v[208:211], v[184:187], v[200:203], 0\n"
      "s_waitcnt vmcnt(6) lgkmcnt(3)\n"
      "buffer_store_dword v13, v3, s[8:11], s52 offen sc1\n"
      "ds_read2st64_b64 v[212:215], v4 offset0:0 offset1:1\n"
      "v_mfma_f32_16x16x32_bf16 v[204:207], v[180:183], v[200:203], 0\n"
      "ds_read_b128 v[72:75], v0 offset:6144\n"
      "v_mfma_f32_16x16x32_bf16 v[132:135], a[204:207], v[108:111], v[132:135]\n"
      "ds_read_b128 v[76:79], v0 offset:7168\n"
      "v_mov_b32_e64 v178, v175\n"
      "v_mfma_f32_16x16x32_bf16 v[132:135], a[208:211], v[112:115], v[132:135]\n"
      "ds_read_b128 v[88:91], v0 offset:10240\n"
      "ds_read_b128 v[108:111], v0 offset:15360\n"
      "v_mfma_f32_16x16x32_bf16 v[132:135], a[212:215], v[116:119], v[132:135]\n"
      "s_sub_u32 s20, s20, 1\n"
      "s_cbranch_scc0 .Lq_loop\n"
      ".Lq_tail:\n"
      "v_mul_f32 v232, 0x3eae1a93, v219\n"
      "s_nop 7\n"
      "v_fma_f32 v135, -v218, v151, v135\n"
      "v_fma_f32 v133, -v218, v149, v133\n"
      "v_fma_f32 v134, -v218, v150, v134\n"
      "v_fma_f32 v132, -v218, v148, v132\n"
      "v_cvt_pk_bf16_f32 v176, v132, v133\n"
      "v_cvt_pk_bf16_f32 v177, v134, v135\n"
      "v_rcp_f32 v210, v210\n"
      "s_nop 0\n"
      "v_mul_f32 v204, v204, v210\n"
      "v_mfma_f32_16x16x32_bf16 v[188:191], v[172:175], v[176:179], v[152:155]\n"
      "v_mul_f32 v208, v208, v210\n"
      "v_mul_f32 v207, v207, v210\n"
      "v_mul_f32 v209, v209, v210\n"
      "v_cvt_pk_bf16_f32 v230, v208, v209\n"
      "buffer_store_dwordx2 v[230:231], v8, s[8:11], s40 offen sc1\n"
      "v_mul_f32 v206, v206, v210\n"
      "v_cvt_pk_bf16_f32 v229, v206, v207\n"
      "v_mul_f32 v205, v205, v210\n"
      "v_cvt_pk_bf16_f32 v228, v204, v205\n"
      "buffer_store_dwordx2 v[228:229], v7, s[8:11], s40 offen sc1\n"
      "v_mov_b32 v187, v185\n"
      "v_mov_b32 v186, v184\n"
      "v_mov_b32 v182, v180\n"
      "s_add_u32 s52, s52, 24576\n"
      "v_mov_b32 v183, v181\n"
      "s_add_u32 s40, s40, 24576\n"
      "s_waitcnt vmcnt(0)\n"
      "buffer_store_dword v13, v3, s[8:11], s52 offen sc1\n"
      "s_add_u32 s52, s52, 24576\n"
      "s_cmp_lg_u32 s44, 0\n"
      "s_cbranch_scc1 .Lq_done\n"
      "v_lshlrev_b32 v48, 3, v0\n"
      "s_mov_b32 s22, s23\n"
      ".Lq_up:\n"
      "s_mov_b32 s30, 128\n"
      "buffer_load_dword v49, v48, s[8:11], s30 offen sc1\n"
      "s_mov_b32 s30, 8320\n"
      "buffer_load_dword v50, v48, s[8:11], s30 offen sc1\n"
      "s_mov_b32 s30, 16512\n"
      "buffer_load_dword v51, v48, s[8:11], s30 offen sc1\n"
      "s_mov_b32 s30, 24704\n"
      "buffer_load_dword v52, v48, s[8:11], s30 offen sc1\n"
      "s_waitcnt vmcnt(0)\n"
      "v_xor_b32 v49, s21, v49\n"
      "v_xor_b32 v50, s21, v50\n"
      "v_xor_b32 v51, s21, v51\n"
      "v_xor_b32 v52, s21, v52\n"
      "v_or3_b32 v49, v49, v50, v51\n"
      "v_or_b32 v49, v49, v52\n"
      "v_cmp_ne_u32 vcc, 0, v49\n"
      "s_cbranch_vccz .Lq_ok\n"
      "s_sleep 4\n"
      "s_sub_u32 s22, s22, 1\n"
      "s_cbranch_scc0 .Lq_up\n"
      ".Lq_ok:\n"
      "v_lshrrev_b32 v48, 2, v0\n"
      "v_and_b32 v48, 0x7c, v48\n"
      "v_mov_b32 v49, s21\n"
      "buffer_store_dword v49, v48, s[8:11], 0 offen sc1\n"
      ".Lq_done:\n"
      "s_endpgm\n"
      ".Lsp_q0_1:\n"
      "s_mov_b32 s22, s23\n"
      ".Lsl_q0_1:\n"
      "buffer_load_dword v15, v2, s[8:11], s46 offen sc1\n"
      "s_waitcnt vmcnt(0)\n"
      "v_cmp_ne_u32 vcc, s21, v15\n"
      "s_cbranch_vccz .Lbk_q0_1\n"
      "s_sleep 4\n"
      "s_sub_u32 s22, s22, 1\n"
      "s_cbranch_scc0 .Lsl_q0_1\n"
      "s_mov_b32 s23, 0\n"
      "s_branch .Lbk_q0_1\n"
      ".Lsp_q1_2:\n"
      "s_mov_b32 s22, s23\n"
      ".Lsl_q1_2:\n"
      "buffer_load_dword v15, v2, s[8:11], s46 offen sc1\n"
      "s_waitcnt vmcnt(0)\n"
      "v_cmp_ne_u32 vcc, s21, v15\n"
      "s_cbranch_vccz .Lbk_q1_2\n"
      "s_sleep 4\n"
      "s_sub_u32 s22, s22, 1\n"
      "s_cbranch_scc0 .Lsl_q1_2\n"
      "s_mov_b32 s23, 0\n"
      "s_branch .Lbk_q1_2\n"
      ".Lsp_qy0_3:\n"
      "s_lshl_b32 s22, s23, 4\n"
      ".Lsl_qy0_3:\n"
      "ds_read_b32 v10, v12\n"
      "s_waitcnt lgkmcnt(0)\n"
      "v_cmp_gt_i32 vcc, v9, v10\n"
      "s_cbranch_vccz .Lbk_qy0_3\n"
      "s_sub_u32 s22, s22, 1\n"
      "s_cbranch_scc0 .Lsl_qy0_3\n"
      "s_mov_b32 s23, 0\n"
      "s_branch .Lbk_qy0_3\n"
      ".Lsp_q2_4:\n"
      "s_mov_b32 s22, s23\n"
      ".Lsl_q2_4:\n"
      "buffer_load_dword v15, v2, s[8:11], s46 offen sc1\n"
      "s_waitcnt vmcnt(0)\n"
      "v_cmp_ne_u32 vcc, s21, v15\n"
      "s_cbranch_vccz .Lbk_q2_4\n"
      "s_sleep 4\n"
      "s_sub_u32 s22, s22, 1\n"
      "s_cbranch_scc0 .Lsl_q2_4\n"
      "s_mov_b32 s23, 0\n"
      "s_branch .Lbk_q2_4\n"
      ".Lsp_qy1_5:\n"
      "s_lshl_b32 s22, s23, 4\n"
      ".Lsl_qy1_5:\n"
      "ds_read_b32 v10, v12\n"
      "s_waitcnt lgkmcnt(0)\n"
      "v_cmp_gt_i32 vcc, v9, v10\n"
      "s_cbranch_vccz .Lbk_qy1_5\n"
      "s_sub_u32 s22, s22, 1\n"
      "s_cbranch_scc0 .Lsl_qy1_5\n"
      "s_mov_b32 s23, 0\n"
      "s_branch .Lbk_qy1_5\n"
      ".Lsp_q0_6:\n"
      "s_mov_b32 s22, s23\n"
      ".Lsl_q0_6:\n"
      "buffer_load_dword v15, v2, s[8:11], s46 offen sc1\n"
      "s_waitcnt vmcnt(0)\n"
      "v_cmp_ne_u32 vcc, s21, v15\n"
      "s_cbranch_vccz .Lbk_q0_6\n"
      "s_sleep 4\n"
      "s_sub_u32 s22, s22, 1\n"
      "s_cbranch_scc0 .Lsl_q0_6\n"
      "s_mov_b32 s23, 0\n"
      "s_branch .Lbk_q0_6\n"
      ".Lsp_qy2_7:\n"
      "s_lshl_b32 s22, s23, 4\n"
      ".Lsl_qy2_7:\n"
      "ds_read_b32 v10, v12\n"
      "s_waitcnt lgkmcnt(0)\n"
      "v_cmp_gt_i32 vcc, v9, v10\n"
      "s_cbranch_vccz .Lbk_qy2_7\n"
      "s_sub_u32 s22, s22, 1\n"
      "s_cbranch_scc0 .Lsl_qy2_7\n"
      "s_mov_b32 s23, 0\n"
      "s_branch .Lbk_qy2_7\n"
      ".Lsp_q1_8:\n"
      "s_mov_b32 s22, s23\n"
      ".Lsl_q1_8:\n"
      "buffer_load_dword v15, v2, s[8:11], s46 offen sc1\n"
      "s_waitcnt vmcnt(0)\n"
      "v_cmp_ne_u32 vcc, s21, v15\n"
      "s_cbranch_vccz .Lbk_q1_8\n"
      "s_sleep 4\n"
      "s_sub_u32 s22, s22, 1\n"
      "s_cbranch_scc0 .Lsl_q1_8\n"
      "s_mov_b32 s23, 0\n"
      "s_branch .Lbk_q1_8\n"
      ".Lsp_qy0_9:\n"
      "s_lshl_b32 s22, s23, 4\n"
      ".Lsl_qy0_9:\n"
      "ds_read_b32 v10, v12\n"
      "s_waitcnt lgkmcnt(0)\n"
      "v_cmp_gt_i32 vcc, v9, v10\n"
      "s_cbranch_vccz .Lbk_qy0_9\n"
      "s_sub_u32 s22, s22, 1\n"
      "s_cbranch_scc0 .Lsl_qy0_9\n"
      "s_mov_b32 s23, 0\n"
      "s_branch .Lbk_qy0_9\n"
      ".Lq_last:\n"
      :
      : "{s[8:11]}"(rsWS), "{s[12:15]}"(rsA), "{v0}"(v_l16), "{v1}"(v_l5), "{v2}"(v_fa), "{v3}"(v_ra), "{v4}"(v_st), "{v5}"(v_tw), "{v6}"(v_tr), "{v7}"(v_om), "{v8}"(v_oe), "{v10}"(v_al), "{v11}"(v_yw), "{v12}"(v_yr), "{v14}"(v_hi), "{v152}"(v_bc[0]), "{v153}"(v_bc[1]), "{v154}"(v_bc[2]), "{v155}"(v_bc[3]), "{v156}"(v_bp[0]), "{v157}"(v_bp[1]), "{v158}"(v_bp[2]), "{v159}"(v_bp[3]), "{v160}"(v_mk[0]), "{v161}"(v_mk[1]), "{v162}"(v_mk[2]), "{v163}"(v_mk[3]), "{v164}"(v_sel[0]), "{v165}"(v_sel[1]), "{v166}"(v_sel[2]), "{v167}"(v_sel[3]), "{v168}"(v_ci[0]), "{v169}"(v_ci[1]), "{v170}"(v_ci[2]), "{v171}"(v_ci[3]), "{v48}"(v_vo[0]), "{v49}"(v_vo[1]), "{v50}"(v_vo[2]), "{v51}"(v_vo[3]), "{v52}"(v_at), "{v53}"(v_ax), "{v54}"(v_pm), "{v55}"(v_ms), "{v76}"(v_pw), "{s36}"(s_sx), "{s37}"(s_ev), "{s38}"(s_del), "{s39}"(s_kil), "{s40}"(s_so), "{s41}"(s_m4), "{s42}"(s_m5), "{s43}"(s_ms), "{s44}"(s_up)
      : "v255", "a255", "s99", "vcc", "scc", "memory");

  } else if (fast && slot < 10) {
    const unsigned gr = e, part = slot - 8;
    COMMON(32, 81920);
    const unsigned c = part, o = w & 1;
    const u32x4 rsA = srd(Wo, 576 * 576 * 2);
    const unsigned v_l5 = v_l16 + 16384 - w * 3072, e0 = 16384 + o * 2048 + g * 512 + i16 * 8, e1 = e0 + 2048 - o * 4096;
    const unsigned v_ex[8] = {e0, e1, e0 + 20480, e1 + 20480, e0 + 40960, e1 + 40960, e0 + 61440, e1 + 61440}, v_l2 = v_l16 + 40960;
    const unsigned v_fa = 20480 - w * 4096 + (8 * w + (lane >> 3)) * 128 + (lane & 7) * 4, v_ra = up * 8 * REC + l5 + 19456 + (4 * c + w) * 128;
    const unsigned v_xp = (9 * c + 2 * w) * 1024 + v_l16, v_xs = (9 * c + 8) * 1024 + (w >> 1) * 512 + half;
    const unsigned v_rp = v_xp + WS_XIN, v_rs = v_xs + WS_XIN;
    const unsigned v_ew = 82944 + (w >> 1) * 1024 + lane * 4;
    const unsigned v_sw = 86016 + i16 * 144 + (4 * w + g) * 8, v_sr = 86016 + (8 * (w >> 1) + l8) * 144 + l7;
    const unsigned v_ss = 18432 + c * 512 + (w >> 1) * 256 + l8 * 32 + p * 8;
    const unsigned v_pw = 19456 + (4 * c + w) * 128 + l8 * REC + l7;
    unsigned v_vo[9], v_vp[9];
#pragma unroll
    for (unsigned j = 0; j < 9; ++j) {
      const unsigned ch = 64 * j + lane, r = ch / 36, x = 576 * c + ch % 36 * 16 - 1024 * (j & 3);
      v_vo[j] = (20 * o + r) * 1152 + x;
      v_vp[j] = (36 * (4 * o + (r >> 2)) + 16 + (r & 3)) * 1152 + x;
    }
    const unsigned ro = (8 * g + q) * 576;
    const unsigned v_t0 = ro + o * 18432 + 128 * w + 16 * p, v_t1 = v_t0 + 18432 - 2 * o * 18432, v_ts = ro + o * 18432 + 512 + 32 * (w >> 1) + 8 * p;
    const unsigned s_sx = WS_OB + (gr * NREC + KSK) * REC + w * 4096, s_ev = s_sx + (31 - KSK) * REC, s_sy = WS_X1 + (gr * NREC + KSK) * REC;
    const unsigned s_m4 = w * 4096, s_m5 = 16384 + w * 1024, s_m9 = w * 9216, s_so = (w >> 1) * 8 * 41472;
    const unsigned s_s8 = s_so, s_wg = o, s_rb = WS_OB + w * 4096;
    asm volatile(
      "s_buffer_load_dword s21, s[8:11], 0\n"
      "s_mov_b32 s23, 65536\n"
      "s_mov_b32 s31, 0x40000000\n"
      "s_add_u32 m0, s41, 0\n"
      "s_nop 0\n"
      "buffer_load_dwordx4 v100, s[12:15], s42 offen offset:0 lds\n"
      "buffer_load_dwordx4 v101, s[12:15], s42 offen offset:1024 lds\n"
      "buffer_load_dwordx4 v102, s[12:15], s42 offen offset:2048 lds\n"
      "buffer_load_dwordx4 v103, s[12:15], s42 offen offset:3072 lds\n"
      "s_add_u32 m0, s41, 4096\n"
      "s_nop 0\n"
      "buffer_load_dwordx4 v104, s[12:15], s42 offen offset:0 lds\n"
      "buffer_load_dwordx4 v105, s[12:15], s42 offen offset:1024 lds\n"
      "buffer_load_dwordx4 v106, s[12:15], s42 offen offset:2048 lds\n"
      "buffer_load_dwordx4 v107, s[12:15], s42 offen offset:3072 lds\n"
      "s_add_u32 m0, s41, 8192\n"
      "s_nop 0\n"
      "buffer_load_dwordx4 v108, s[12:15], s42 offen offset:0 lds\n"
      "s_add_u32 s42, s42, 41472\n"
      "s_add_u32 m0, s41, 36864\n"
      "s_nop 0\n"
      "buffer_load_dwordx4 v100, s[12:15], s42 offen offset:0 lds\n"
      "buffer_load_dwordx4 v101, s[12:15], s42 offen offset:1024 lds\n"
      "buffer_load_dwordx4 v102, s[12:15], s42 offen offset:2048 lds\n"
      "buffer_load_dwordx4 v103, s[12:15], s42 offen offset:3072 lds\n"
      "s_add_u32 m0, s41, 40960\n"
      "s_nop 0\n"
      "buffer_load_dwordx4 v104, s[12:15], s42 offen offset:0 lds\n"
      "buffer_load_dwordx4 v105, s[12:15], s42 offen offset:1024 lds\n"
      "buffer_load_dwordx4 v106, s[12:15], s42 offen offset:2048 lds\n"
      "buffer_load_dwordx4 v107, s[12:15], s42 offen offset:3072 lds\n"
      "s_add_u32 m0, s41, 45056\n"
      "s_nop 0\n"
      "buffer_load_dwordx4 v108, s[12:15], s42 offen offset:0 lds\n"
      "s_add_u32 s42, s42, 41472\n"
      "s_add_u32 m0, s41, 73728\n"
      "s_nop 0\n"
      "buffer_load_dwordx4 v100, s[12:15], s42 offen offset:0 lds\n"
      "buffer_load_dwordx4 v101, s[12:15], s42 offen offset:1024 lds\n"
      "buffer_load_dwordx4 v102, s[12:15], s42 offen offset:2048 lds\n"
      "buffer_load_dwordx4 v103, s[12:15], s42 offen offset:3072 lds\n"
      "s_add_u32 m0, s41, 77824\n"
      "s_nop 0\n"
      "buffer_load_dwordx4 v104, s[12:15], s42 offen offset:0 lds\n"
      "buffer_load_dwordx4 v105, s[12:15], s42 offen offset:1024 lds\n"
      "buffer_load_dwordx4 v106, s[12:15], s42 offen offset:2048 lds\n"
      "buffer_load_dwordx4 v107, s[12:15], s42 offen offset:3072 lds\n"
      "s_add_u32 m0, s41, 81920\n"
      "s_nop 0\n"
      "buffer_load_dwordx4 v108, s[12:15], s42 offen offset:0 lds\n"
      "s_add_u32 s42, s42, 41472\n"
      "s_waitcnt vmcnt(18)\n"
      "s_barrier\n"
      "ds_read_b64_tr_b16 v[64:65], v120 offset:0\n"
      "ds_read_b64_tr_b16 v[66:67], v120 offset:2304\n"
      "ds_read_b64_tr_b16 a[0:1], v118 offset:0\n"
      "ds_read_b64_tr_b16 a[2:3], v118 offset:2304\n"
      "ds_read_b64_tr_b16 a[72:73], v118 offset:8\n"
      "ds_read_b64_tr_b16 a[74:75], v118 offset:2312\n"
      "ds_read_b64_tr_b16 a[144:145], v118 offset:64\n"
      "ds_read_b64_tr_b16 a[146:147], v118 offset:2368\n"
      "ds_read_b64_tr_b16 a[216:217], v118 offset:72\n"
      "ds_read_b64_tr_b16 a[218:219], v118 offset:2376\n"
      "ds_read_b64_tr_b16 a[36:37], v119 offset:0\n"
      "ds_read_b64_tr_b16 a[38:39], v119 offset:2304\n"
      "ds_read_b64_tr_b16 a[108:109], v119 offset:8\n"
      "ds_read_b64_tr_b16 a[110:111], v119 offset:2312\n"
      "ds_read_b64_tr_b16 a[180:181], v119 offset:64\n"
      "ds_read_b64_tr_b16 a[182:183], v119 offset:2368\n"
      "ds_read_b64_tr_b16 a[252:253], v119 offset:72\n"
      "ds_read_b64_tr_b16 a[254:255], v119 offset:2376\n"
      "v_add_u32 v118, 36864, v118\n"
      "v_add_u32 v119, 36864, v119\n"
      "v_add_u32 v120, 36864, v120\n"
      "s_add_u32 m0, s41, 110592\n"
      "s_nop 0\n"
      "buffer_load_dwordx4 v100, s[12:15], s42 offen offset:0 lds\n"
      "buffer_load_dwordx4 v101, s[12:15], s42 offen offset:1024 lds\n"
      "buffer_load_dwordx4 v102, s[12:15], s42 offen offset:2048 lds\n"
      "buffer_load_dwordx4 v103, s[12:15], s42 offen offset:3072 lds\n"
      "s_add_u32 m0, s41, 114688\n"
      "s_nop 0\n"
      "buffer_load_dwordx4 v104, s[12:15], s42 offen offset:0 lds\n"
      "buffer_load_dwordx4 v105, s[12:15], s42 offen offset:1024 lds\n"
      "buffer_load_dwordx4 v106, s[12:15], s42 offen offset:2048 lds\n"
      "buffer_load_dwordx4 v107, s[12:15], s42 offen offset:3072 lds\n"
      "s_add_u32 m0, s41, 118784\n"
      "s_nop 0\n"
      "buffer_load_dwordx4 v108, s[12:15], s42 offen offset:0 lds\n"
      "s_add_u32 s42, s42, 41472\n"
      "s_waitcnt vmcnt(18) lgkmcnt(0)\n"
      "s_barrier\n"
      "ds_read_b64_tr_b16 v[68:69], v120 offset:0\n"
      "ds_read_b64_tr_b16 v[70:71], v120 offset:2304\n"
      "ds_read_b64_tr_b16 a[4:5], v118 offset:0\n"
      "ds_read_b64_tr_b16 a[6:7], v118 offset:2304\n"
      "ds_read_b64_tr_b16 a[76:77], v118 offset:8\n"
      "ds_read_b64_tr_b16 a[78:79], v118 offset:2312\n"
      "ds_read_b64_tr_b16 a[148:149], v118 offset:64\n"
      "ds_read_b64_tr_b16 a[150:151], v118 offset:2368\n"
      "ds_read_b64_tr_b16 a[220:221], v118 offset:72\n"
      "ds_read_b64_tr_b16 a[222:223], v118 offset:2376\n"
      "ds_read_b64_tr_b16 a[40:41], v119 offset:0\n"
      "ds_read_b64_tr_b16 a[42:43], v119 offset:2304\n"
      "ds_read_b64_tr_b16 a[112:113], v119 offset:8\n"
      "ds_read_b64_tr_b16 a[114:115], v119 offset:2312\n"
      "ds_read_b64_tr_b16 a[184:185], v119 offset:64\n"
      "ds_read_b64_tr_b16 a[186:187], v119 offset:2368\n"
      "ds_read_b64_tr_b16 v[32:33], v119 offset:72\n"
      "ds_read_b64_tr_b16 v[34:35], v119 offset:2376\n"
      "v_add_u32 v118, 36864, v118\n"
      "v_add_u32 v119, 36864, v119\n"
      "v_add_u32 v120, 36864, v120\n"
      "s_add_u32 m0, s41, 0\n"
      "s_nop 0\n"
      "buffer_load_dwordx4 v100, s[12:15], s42 offen offset:0 lds\n"
      "buffer_load_dwordx4 v101, s[12:15], s42 offen offset:1024 lds\n"
      "buffer_load_dwordx4 v102, s[12:15], s42 offen offset:2048 lds\n"
      "buffer_load_dwordx4 v103, s[12:15], s42 offen offset:3072 lds\n"
      "s_add_u32 m0, s41, 4096\n"
      "s_nop 0\n"
      "buffer_load_dwordx4 v104, s[12:15], s42 offen offset:0 lds\n"
      "buffer_load_dwordx4 v105, s[12:15], s42 offen offset:1024 lds\n"
      "buffer_load_dwordx4 v106, s[12:15], s42 offen offset:2048 lds\n"
      "buffer_load_dwordx4 v107, s[12:15], s42 offen offset:3072 lds\n"
      "s_add_u32 m0, s41, 8192\n"
      "s_nop 0\n"
      "buffer_load_dwordx4 v108, s[12:15], s42 offen offset:0 lds\n"
      "s_add_u32 s42, s42, 41472\n"
      "s_waitcnt vmcnt(18) lgkmcnt(0)\n"
      "s_barrier\n"
      "ds_read_b64_tr_b16 v[72:73], v120 offset:0\n"
      "ds_read_b64_tr_b16 v[74:75], v120 offset:2304\n"
      "ds_read_b64_tr_b16 a[8:9], v118 offset:0\n"
      "ds_read_b64_tr_b16 a[10:11], v118 offset:2304\n"
      "ds_read_b64_tr_b16 a[80:81], v118 offset:8\n"
      "ds_read_b64_tr_b16 a[82:83], v118 offset:2312\n"
      "ds_read_b64_tr_b16 a[152:153], v118 offset:64\n"
      "ds_read_b64_tr_b16 a[154:155], v118 offset:2368\n"
      "ds_read_b64_tr_b16 a[224:225], v118 offset:72\n"
      "ds_read_b64_tr_b16 a[226:227], v118 offset:2376\n"
      "ds_read_b64_tr_b16 a[44:45], v119 offset:0\n"
      "ds_read_b64_tr_b16 a[46:47], v119 offset:2304\n"
      "ds_read_b64_tr_b16 a[116:117], v119 offset:8\n"
      "ds_read_b64_tr_b16 a[118:119], v119 offset:2312\n"
      "ds_read_b64_tr_b16 a[188:189], v119 offset:64\n"
      "ds_read_b64_tr_b16 a[190:191], v119 offset:2368\n"
      "ds_read_b64_tr_b16 v[36:37], v119 offset:72\n"
      "ds_read_b64_tr_b16 v[38:39], v119 offset:2376\n"
      "v_add_u32 v118, 36864, v118\n"
      "v_add_u32 v119, 36864, v119\n"
      "v_add_u32 v120, 36864, v120\n"
      "s_add_u32 m0, s41, 36864\n"
      "s_nop 0\n"
      "buffer_load_dwordx4 v100, s[12:15], s42 offen offset:0 lds\n"
      "buffer_load_dwordx4 v101, s[12:15], s42 offen offset:1024 lds\n"
      "buffer_load_dwordx4 v102, s[12:15], s42 offen offset:2048 lds\n"
      "buffer_load_dwordx4 v103, s[12:15], s42 offen offset:3072 lds\n"
      "s_add_u32 m0, s41, 40960\n"
      "s_nop 0\n"
      "buffer_load_dwordx4 v104, s[12:15], s42 offen offset:0 lds\n"
      "buffer_load_dwordx4 v105, s[12:15], s42 offen offset:1024 lds\n"
      "buffer_load_dwordx4 v106, s[12:15], s42 offen offset:2048 lds\n"
      "buffer_load_dwordx4 v107, s[12:15], s42 offen offset:3072 lds\n"
      "s_add_u32 m0, s41, 45056\n"
      "s_nop 0\n"
      "buffer_load_dwordx4 v108, s[12:15], s42 offen offset:0 lds\n"
      "s_add_u32 s42, s42, 41472\n"
      "s_waitcnt vmcnt(18) lgkmcnt(0)\n"
      "s_barrier\n"
      "ds_read_b64_tr_b16 v[76:77], v120 offset:0\n"
      "ds_read_b64_tr_b16 v[78:79], v120 offset:2304\n"
      "ds_read_b64_tr_b16 a[12:13], v118 offset:0\n"
      "ds_read_b64_tr_b16 a[14:15], v118 offset:2304\n"
      "ds_read_b64_tr_b16 a[84:85], v118 offset:8\n"
      "ds_read_b64_tr_b16 a[86:87], v118 offset:2312\n"
      "ds_read_b64_tr_b16 a[156:157], v118 offset:64\n"
      "ds_read_b64_tr_b16 a[158:159], v118 offset:2368\n"
      "ds_read_b64_tr_b16 a[228:229], v118 offset:72\n"
      "ds_read_b64_tr_b16 a[230:231], v118 offset:2376\n"
      "ds_read_b64_tr_b16 a[48:49], v119 offset:0\n"
      "ds_read_b64_tr_b16 a[50:51], v119 offset:2304\n"
      "ds_read_b64_tr_b16 a[120:121], v119 offset:8\n"
      "ds_read_b64_tr_b16 a[122:123], v119 offset:2312\n"
      "ds_read_b64_tr_b16 a[192:193], v119 offset:64\n"
      "ds_read_b64_tr_b16 a[194:195], v119 offset:2368\n"
      "ds_read_b64_tr_b16 v[40:41], v119 offset:72\n"
      "ds_read_b64_tr_b16 v[42:43], v119 offset:2376\n"
      "v_add_u32 v118, 4294856704, v118\n"
      "v_add_u32 v119, 4294856704, v119\n"
      "v_add_u32 v120, 4294856704, v120\n"
      "s_add_u32 m0, s41, 73728\n"
      "s_nop 0\n"
      "buffer_load_dwordx4 v100, s[12:15], s42 offen offset:0 lds\n"
      "buffer_load_dwordx4 v101, s[12:15], s42 offen offset:1024 lds\n"
      "buffer_load_dwordx4 v102, s[12:15], s42 offen offset:2048 lds\n"
      "buffer_load_dwordx4 v103, s[12:15], s42 offen offset:3072 lds\n"
      "s_add_u32 m0, s41, 77824\n"
      "s_nop 0\n"
      "buffer_load_dwordx4 v104, s[12:15], s42 offen offset:0 lds\n"
      "buffer_load_dwordx4 v105, s[12:15], s42 offen offset:1024 lds\n"
      "buffer_load_dwordx4 v106, s[12:15], s42 offen offset:2048 lds\n"
      "buffer_load_dwordx4 v107, s[12:15], s42 offen offset:3072 lds\n"
      "s_add_u32 m0, s41, 81920\n"
      "s_nop 0\n"
      "buffer_load_dwordx4 v108, s[12:15], s42 offen offset:0 lds\n"
      "s_add_u32 s42, s42, 41472\n"
      "s_waitcnt vmcnt(18) lgkmcnt(0)\n"
      "s_barrier\n"
      "ds_read_b64_tr_b16 v[80:81], v120 offset:0\n"
      "ds_read_b64_tr_b16 v[82:83], v120 offset:2304\n"
      "ds_read_b64_tr_b16 a[16:17], v118 offset:0\n"
      "ds_read_b64_tr_b16 a[18:19], v118 offset:2304\n"
      "ds_read_b64_tr_b16 a[88:89], v118 offset:8\n"
      "ds_read_b64_tr_b16 a[90:91], v118 offset:2312\n"
      "ds_read_b64_tr_b16 a[160:161], v118 offset:64\n"
      "ds_read_b64_tr_b16 a[162:163], v118 offset:2368\n"
      "ds_read_b64_tr_b16 a[232:233], v118 offset:72\n"
      "ds_read_b64_tr_b16 a[234:235], v118 offset:2376\n"
      "ds_read_b64_tr_b16 a[52:53], v119 offset:0\n"
      "ds_read_b64_tr_b16 a[54:55], v119 offset:2304\n"
      "ds_read_b64_tr_b16 a[124:125], v119 offset:8\n"
      "ds_read_b64_tr_b16 a[126:127], v119 offset:2312\n"
      "ds_read_b64_tr_b16 a[196:197], v119 offset:64\n"
      "ds_read_b64_tr_b16 a[198:199], v119 offset:2368\n"
      "ds_read_b64_tr_b16 v[44:45], v119 offset:72\n"
      "ds_read_b64_tr_b16 v[46:47], v119 offset:2376\n"
      "v_add_u32 v118, 36864, v118\n"
      "v_add_u32 v119, 36864, v119\n"
      "v_add_u32 v120, 36864, v120\n"
      "s_add_u32 m0, s41, 110592\n"
      "s_nop 0\n"
      "buffer_load_dwordx4 v100, s[12:15], s42 offen offset:0 lds\n"
      "buffer_load_dwordx4 v101, s[12:15], s42 offen offset:1024 lds\n"
      "buffer_load_dwordx4 v102, s[12:15], s42 offen offset:2048 lds\n"
      "buffer_load_dwordx4 v103, s[12:15], s42 offen offset:3072 lds\n"
      "s_add_u32 m0, s41, 114688\n"
      "s_nop 0\n"
      "buffer_load_dwordx4 v104, s[12:15], s42 offen offset:0 lds\n"
      "buffer_load_dwordx4 v105, s[12:15], s42 offen offset:1024 lds\n"
      "buffer_load_dwordx4 v106, s[12:15], s42 offen offset:2048 lds\n"
      "buffer_load_dwordx4 v107, s[12:15], s42 offen offset:3072 lds\n"
      "s_add_u32 m0, s41, 118784\n"
      "s_nop 0\n"
      "buffer_load_dwordx4 v108, s[12:15], s42 offen offset:0 lds\n"
      "s_add_u32 s42, s42, 41472\n"
      "s_waitcnt vmcnt(18) lgkmcnt(0)\n"
      "s_barrier\n"
      "ds_read_b64_tr_b16 v[84:85], v120 offset:0\n"
      "ds_read_b64_tr_b16 v[86:87], v120 offset:2304\n"
      "ds_read_b64_tr_b16 a[20:21], v118 offset:0\n"
      "ds_read_b64_tr_b16 a[22:23], v118 offset:2304\n"
      "ds_read_b64_tr_b16 a[92:93], v118 offset:8\n"
      "ds_read_b64_tr_b16 a[94:95], v118 offset:2312\n"
      "ds_read_b64_tr_b16 a[164:165], v118 offset:64\n"
      "ds_read_b64_tr_b16 a[166:167], v118 offset:2368\n"
      "ds_read_b64_tr_b16 a[236:237], v118 offset:72\n"
      "ds_read_b64_tr_b16 a[238:239], v118 offset:2376\n"
      "ds_read_b64_tr_b16 a[56:57], v119 offset:0\n"
      "ds_read_b64_tr_b16 a[58:59], v119 offset:2304\n"
      "ds_read_b64_tr_b16 a[128:129], v119 offset:8\n"
      "ds_read_b64_tr_b16 a[130:131], v119 offset:2312\n"
      "ds_read_b64_tr_b16 a[200:201], v119 offset:64\n"
      "ds_read_b64_tr_b16 a[202:203], v119 offset:2368\n"
      "ds_read_b64_tr_b16 v[48:49], v119 offset:72\n"
      "ds_read_b64_tr_b16 v[50:51], v119 offset:2376\n"
      "v_add_u32 v118, 36864, v118\n"
      "v_add_u32 v119, 36864, v119\n"
      "v_add_u32 v120, 36864, v120\n"
      "s_add_u32 m0, s41, 0\n"
      "s_nop 0\n"
      "buffer_load_dwordx4 v109, s[12:15], s43 offen offset:0 lds\n"
      "buffer_load_dwordx4 v110, s[12:15], s43 offen offset:1024 lds\n"
      "buffer_load_dwordx4 v111, s[12:15], s43 offen offset:2048 lds\n"
      "buffer_load_dwordx4 v112, s[12:15], s43 offen offset:3072 lds\n"
      "s_add_u32 m0, s41, 4096\n"
      "s_nop 0\n"
      "buffer_load_dwordx4 v113, s[12:15], s43 offen offset:0 lds\n"
      "buffer_load_dwordx4 v114, s[12:15], s43 offen offset:1024 lds\n"
      "buffer_load_dwordx4 v115, s[12:15], s43 offen offset:2048 lds\n"
      "buffer_load_dwordx4 v116, s[12:15], s43 offen offset:3072 lds\n"
      "s_add_u32 m0, s41, 8192\n"
      "s_nop 0\n"
      "buffer_load_dwordx4 v117, s[12:15], s43 offen offset:0 lds\n"
      "s_add_u32 s42, s42, 41472\n"
      "s_waitcnt vmcnt(18) lgkmcnt(0)\n"
      "s_barrier\n"
      "ds_read_b64_tr_b16 v[88:89], v120 offset:0\n"
      "ds_read_b64_tr_b16 v[90:91], v120 offset:2304\n"
      "ds_read_b64_tr_b16 a[24:25], v118 offset:0\n"
      "ds_read_b64_tr_b16 a[26:27], v118 offset:2304\n"
      "ds_read_b64_tr_b16 a[96:97], v118 offset:8\n"
      "ds_read_b64_tr_b16 a[98:99], v118 offset:2312\n"
      "ds_read_b64_tr_b16 a[168:169], v118 offset:64\n"
      "ds_read_b64_tr_b16 a[170:171], v118 offset:2368\n"
      "ds_read_b64_tr_b16 a[240:241], v118 offset:72\n"
      "ds_read_b64_tr_b16 a[242:243], v118 offset:2376\n"
      "ds_read_b64_tr_b16 a[60:61], v119 offset:0\n"
      "ds_read_b64_tr_b16 a[62:63], v119 offset:2304\n"
      "ds_read_b64_tr_b16 a[132:133], v119 offset:8\n"
      "ds_read_b64_tr_b16 a[134:135], v119 offset:2312\n"
      "ds_read_b64_tr_b16 a[204:205], v119 offset:64\n"
      "ds_read_b64_tr_b16 a[206:207], v119 offset:2368\n"
      "ds_read_b64_tr_b16 v[52:53], v119 offset:72\n"
      "ds_read_b64_tr_b16 v[54:55], v119 offset:2376\n"
      "v_add_u32 v118, 36864, v118\n"
      "v_add_u32 v119, 36864, v119\n"
      "v_add_u32 v120, 36864, v120\n"
      "s_waitcnt vmcnt(9) lgkmcnt(0)\n"
      "s_barrier\n"
      "ds_read_b64_tr_b16 v[92:93], v120 offset:0\n"
      "ds_read_b64_tr_b16 v[94:95], v120 offset:2304\n"
      "ds_read_b64_tr_b16 a[28:29], v118 offset:0\n"
      "ds_read_b64_tr_b16 a[30:31], v118 offset:2304\n"
      "ds_read_b64_tr_b16 a[100:101], v118 offset:8\n"
      "ds_read_b64_tr_b16 a[102:103], v118 offset:2312\n"
      "ds_read_b64_tr_b16 a[172:173], v118 offset:64\n"
      "ds_read_b64_tr_b16 a[174:175], v118 offset:2368\n"
      "ds_read_b64_tr_b16 a[244:245], v118 offset:72\n"
      "ds_read_b64_tr_b16 a[246:247], v118 offset:2376\n"
      "ds_read_b64_tr_b16 a[64:65], v119 offset:0\n"
      "ds_read_b64_tr_b16 a[66:67], v119 offset:2304\n"
      "ds_read_b64_tr_b16 a[136:137], v119 offset:8\n"
      "ds_read_b64_tr_b16 a[138:139], v119 offset:2312\n"
      "ds_read_b64_tr_b16 a[208:209], v119 offset:64\n"
      "ds_read_b64_tr_b16 a[210:211], v119 offset:2368\n"
      "ds_read_b64_tr_b16 v[56:57], v119 offset:72\n"
      "ds_read_b64_tr_b16 v[58:59], v119 offset:2376\n"
      "v_add_u32 v118, 4294856704, v118\n"
      "v_add_u32 v119, 4294856704, v119\n"
      "v_add_u32 v120, 4294856704, v120\n"
      "s_waitcnt vmcnt(0) lgkmcnt(0)\n"
      "s_barrier\n"
      "ds_read_b64_tr_b16 v[96:97], v120 offset:0\n"
      "ds_read_b64_tr_b16 v[98:99], v120 offset:2304\n"
      "ds_read_b64_tr_b16 a[32:33], v118 offset:0\n"
      "ds_read_b64_tr_b16 a[34:35], v118 offset:2304\n"
      "ds_read_b64_tr_b16 a[104:105], v118 offset:8\n"
      "ds_read_b64_tr_b16 a[106:107], v118 offset:2312\n"
      "ds_read_b64_tr_b16 a[176:177], v118 offset:64\n"
      "ds_read_b64_tr_b16 a[178:179], v118 offset:2368\n"
      "ds_read_b64_tr_b16 a[248:249], v118 offset:72\n"
      "ds_read_b64_tr_b16 a[250:251], v118 offset:2376\n"
      "ds_read_b64_tr_b16 a[68:69], v119 offset:0\n"
      "ds_read_b64_tr_b16 a[70:71], v119 offset:2304\n"
      "ds_read_b64_tr_b16 a[140:141], v119 offset:8\n"
      "ds_read_b64_tr_b16 a[142:143], v119 offset:2312\n"
      "ds_read_b64_tr_b16 a[212:213], v119 offset:64\n"
      "ds_read_b64_tr_b16 a[214:215], v119 offset:2368\n"
      "ds_read_b64_tr_b16 v[60:61], v119 offset:72\n"
      "ds_read_b64_tr_b16 v[62:63], v119 offset:2376\n"
      "v_add_u32 v118, 36864, v118\n"
      "v_add_u32 v119, 36864, v119\n"
      "v_add_u32 v120, 36864, v120\n"
      "s_waitcnt lgkmcnt(0)\n"
      "s_getpc_b64 vcc\n"
      ".Lw_ct:\n"
      "s_add_u32 vcc_lo, vcc_lo, .Lw_p0-.Lw_ct\n"
      "s_addc_u32 vcc_hi, vcc_hi, 0\n"
      "s_lshl_b32 s30, s39, 1\n"
      "v_lshl_add_u32 v130, v0, 3, s30\n"
      "v_min_u32 v130, .Lw_last-.Lw_p0-4, v130\n"
      "global_load_dword v130, v130, vcc\n"
      "s_mul_i32 s21, s21, 1664525\n"
      "s_add_u32 s21, s21, 1013904223\n"
      "v_mov_b32 v25, s21\n"
      "buffer_store_dword v25, v22, s[8:11], 0 offen sc1\n"
      "v_not_b32 v124, v25\n"
      "v_not_b32 v125, v25\n"
      "v_not_b32 v126, v25\n"
      "v_not_b32 v127, v25\n"
      "s_add_u32 s30, s38, 0\n"
      "buffer_store_dwordx4 v[124:127], v121, s[8:11], s30 offen sc1\n"
      "v_xor_b32 v25, v25, v26\n"
      "v_mov_b32 v27, 0x3ae38e39\n"
      "s_barrier\n"
      "v_mov_b32 v21, 0\n"
      "ds_write_b32 v23, v21\n"
      "s_waitcnt lgkmcnt(0)\n"
      "s_barrier\n"
      "s_add_u32 s6, s36, 1572864\n"
      "s_add_u32 s36, s36, 4294942720\n"
      "s_mov_b32 s51, 24576\n"
      "s_waitcnt vmcnt(0) lgkmcnt(0)\n"
      "s_cmp_lg_u32 s44, 0\n"
      "s_cbranch_scc1 .Lv_start\n"
      "s_mov_b64 exec, 0\n"
      "s_lshr_b32 s22, s39, 13\n"
      ".Lw_pl:\n"
      "s_cmp_eq_u32 s22, 0\n"
      "s_cbranch_scc1 .Lw_p0\n"
      "s_cmp_eq_u32 s22, 1\n"
      "s_cbranch_scc1 .Lw_p1\n"
      "s_branch .Lw_pe\n"
      ".Lw_pr:\n"
      "s_add_u32 s22, s22, 2\n"
      "s_branch .Lw_pl\n"
      ".Lw_pe:\n"
      "s_mov_b64 exec, -1\n"
      "s_add_u32 s50, s38, 4294893568\n"
      "s_add_u32 s49, s38, 4294893568\n"
      "s_add_u32 s38, s38, 4294942720\n"
      "s_cmp_eq_u32 s36, s37\n"
      "s_cselect_b32 s48, 49152, s51\n"
      "s_add_u32 s36, s36, s48\n"
      "s_min_u32 s36, s36, s6\n"
      "buffer_load_dword v28, v11, s[8:11], s36 offen sc1\n"
      "s_mov_b32 s46, s36\n"
      "s_add_u32 s36, s36, 24576\n"
      "s_min_u32 s36, s36, s6\n"
      "s_waitcnt vmcnt(0)\n"
      "v_cmp_ne_u32 vcc, s21, v28\n"
      "s_add_u32 m0, s39, 0\n"
      "s_cbranch_vccnz .Lsp_w0_1\n"
      ".Lbk_w0_1:\n"
      "buffer_load_dword v28, v11, s[8:11], s36 offen sc1\n"
      "buffer_load_dwordx4 v0, s[8:11], s46 offen offset:0 sc1 lds\n"
      "buffer_load_dwordx4 v0, s[8:11], s46 offen offset:1024 sc1 lds\n"
      "buffer_load_dwordx4 v0, s[8:11], s46 offen offset:3072 sc1 lds\n"
      "buffer_load_dwordx4 v0, s[8:11], s46 offen offset:2048 sc1 lds\n"
      "s_add_u32 m0, s40, 0\n"
      "s_nop 0\n"
      "buffer_load_dwordx4 v1, s[8:11], s46 offen offset:0 sc1 lds\n"
      "s_sub_u32 s47, s46, s45\n"
      "buffer_load_dwordx2 v[200:201], v14, s[8:11], s47 offen sc1\n"
      "buffer_load_dwordx4 v[192:195], v13, s[8:11], s47 offen sc1\n"
      "buffer_load_dword v26, v23, s[8:11], s31 offen\n"
      "s_add_u32 m0, s39, 20480\n"
      "s_mov_b32 s46, s36\n"
      "s_waitcnt vmcnt(8)\n"
      "v_cmp_ne_u32 vcc, s21, v28\n"
      "buffer_load_dword v26, v23, s[8:11], s31 offen\n"
      "s_cmp_eq_u32 s36, s37\n"
      "s_cselect_b32 s48, 49152, s51\n"
      "s_add_u32 s36, s36, s48\n"
      "s_cbranch_vccnz .Lsp_w1_2\n"
      ".Lbk_w1_2:\n"
      "s_min_u32 s36, s36, s6\n"
      "buffer_load_dword v28, v11, s[8:11], s36 offen sc1\n"
      "buffer_load_dwordx4 v0, s[8:11], s46 offen offset:0 sc1 lds\n"
      "buffer_load_dwordx4 v0, s[8:11], s46 offen offset:1024 sc1 lds\n"
      "buffer_load_dwordx4 v0, s[8:11], s46 offen offset:3072 sc1 lds\n"
      "v_add_u32 v21, 1, v21\n"
      "s_waitcnt vmcnt(8)\n"
      "ds_write_b32 v23, v21\n"
      "buffer_load_dwordx4 v0, s[8:11], s46 offen offset:2048 sc1 lds\n"
      "s_add_u32 m0, s40, 20480\n"
      "buffer_load_dwordx4 v[196:199], v13, s[8:11], s47 offen offset:1024 sc1\n"
      "buffer_load_dwordx4 v1, s[8:11], s46 offen offset:0 sc1 lds\n"
      "ds_read_b32 v22, v24\n"
      "s_waitcnt lgkmcnt(0)\n"
      "v_cmp_gt_i32 vcc, v21, v22\n"
      "s_waitcnt vmcnt(10)\n"
      "v_and_b32 v189, 0xffff0000, v200\n"
      "v_lshlrev_b32 v188, 16, v200\n"
      "s_cbranch_vccnz .Lsp_wy0_3\n"
      ".Lbk_wy0_3:\n"
      "ds_read_b128 v[104:107], v0 offset:1024\n"
      "v_lshlrev_b32 v190, 16, v201\n"
      "ds_read_b128 v[100:103], v0 offset:0\n"
      "ds_read_b128 v[120:123], v0 offset:5120\n"
      "s_waitcnt vmcnt(9)\n"
      "v_lshlrev_b32 v178, 16, v195\n"
      "ds_read_b128 v[112:115], v0 offset:3072\n"
      "ds_read_b128 v[116:119], v0 offset:4096\n"
      "v_lshlrev_b32 v174, 16, v193\n"
      "ds_read_b128 v[124:127], v0 offset:6144\n"
      "s_sub_u32 s47, s46, s45\n"
      "ds_read_b128 v[108:111], v0 offset:2048\n"
      "v_and_b32 v191, 0xffff0000, v201\n"
      "v_and_b32 v177, 0xffff0000, v194\n"
      "ds_read_b128 v[136:139], v0 offset:8192\n"
      "v_lshlrev_b32 v176, 16, v194\n"
      "ds_read_b128 v[128:131], v0 offset:7168\n"
      "v_lshlrev_b32 v172, 16, v192\n"
      "v_and_b32 v175, 0xffff0000, v193\n"
      "buffer_load_dwordx2 v[200:201], v14, s[8:11], s47 offen sc1\n"
      "ds_read2_b32 v[132:133], v2 offset0:0 offset1:32\n"
      "v_and_b32 v173, 0xffff0000, v192\n"
      "ds_read2_b32 v[134:135], v2 offset0:64 offset1:96\n"
      "s_mov_b32 s20, 15\n"
      "s_waitcnt lgkmcnt(0)\n"
      "s_nop 4\n"
      "s_branch .Lw_loop\n"
      ".p2align 12\n"
      ".Lw_p0:\n"
      "s_branch .Lw_pr\n"
      ".p2align 5\n"
      ".Lw_loop:\n"
      "v_mfma_f32_16x16x32_bf16 v[188:191], v[64:67], v[100:103], v[188:191]\n"
      "ds_read_b128 v[140:143], v0 offset:9216\n"
      "v_and_b32 v179, 0xffff0000, v195\n"
      "v_mfma_f32_16x16x32_bf16 v[188:191], v[68:71], v[104:107], v[188:191]\n"
      "buffer_load_dwordx4 v[192:195], v13, s[8:11], s47 offen sc1\n"
      "v_mfma_f32_16x16x32_bf16 v[188:191], v[72:75], v[108:111], v[188:191]\n"
      "v_add_f32 v218, v218, v182\n"
      "v_mfma_f32_16x16x32_bf16 v[188:191], v[76:79], v[112:115], v[188:191]\n"
      "ds_read_b128 v[156:159], v0 offset:13312\n"
      "v_fmac_f32 v219, v184, v184\n"
      "v_mfma_f32_16x16x32_bf16 v[188:191], v[80:83], v[116:119], v[188:191]\n"
      "ds_read_b128 v[164:167], v0 offset:15360\n"
      "v_cvt_pk_bf16_f32 v213, v182, v183\n"
      "v_mfma_f32_16x16x32_bf16 v[188:191], v[84:87], v[120:123], v[188:191]\n"
      "ds_read_b128 v[144:147], v0 offset:10240\n"
      "v_mfma_f32_16x16x32_bf16 v[188:191], v[88:91], v[124:127], v[188:191]\n"
      "s_waitcnt lgkmcnt(4)\n"
      "v_cvt_pk_bf16_f32 v215, v186, v187\n"
      "v_add_f32 v218, v218, v183\n"
      "v_mfma_f32_16x16x32_bf16 v[188:191], v[92:95], v[128:131], v[188:191]\n"
      "buffer_store_dwordx4 v[208:211], v15, s[8:11], s38 offen sc1\n"
      "v_mfma_f32_16x16x32_bf16 v[188:191], v[96:99], v[132:135], v[188:191]\n"
      "v_cvt_pk_bf16_f32 v212, v180, v181\n"
      "v_add_f32 v218, v218, v184\n"
      "v_mfma_f32_16x16x32_bf16 v[172:175], a[0:3], v[100:103], v[172:175]\n"
      "s_add_u32 s50, s50, 24576\n"
      "v_cvt_pk_bf16_f32 v214, v184, v185\n"
      "v_add_f32 v218, v218, v185\n"
      "v_mfma_f32_16x16x32_bf16 v[172:175], a[4:7], v[104:107], v[172:175]\n"
      "s_mov_b32 s46, s36\n"
      "v_add_f32 v218, v218, v186\n"
      "v_mfma_f32_16x16x32_bf16 v[172:175], a[8:11], v[108:111], v[172:175]\n"
      "ds_read_b128 v[148:151], v0 offset:11264\n"
      "v_add_f32 v218, v218, v187\n"
      "v_mfma_f32_16x16x32_bf16 v[172:175], a[12:15], v[112:115], v[172:175]\n"
      "v_fmac_f32 v219, v185, v185\n"
      "v_add_u32 v21, 1, v21\n"
      "v_mfma_f32_16x16x32_bf16 v[172:175], a[16:19], v[116:119], v[172:175]\n"
      "s_add_u32 s36, s36, 24576\n"
      "ds_read_b128 v[152:155], v0 offset:12288\n"
      "v_fmac_f32 v219, v186, v186\n"
      "v_mfma_f32_16x16x32_bf16 v[172:175], a[20:23], v[120:123], v[172:175]\n"
      "buffer_store_dwordx2 v[216:217], v16, s[8:11], s38 offen sc1\n"
      "v_mfma_f32_16x16x32_bf16 v[172:175], a[24:27], v[124:127], v[172:175]\n"
      "ds_read_b128 v[160:163], v0 offset:14336\n"
      "v_mfma_f32_16x16x32_bf16 v[172:175], a[28:31], v[128:131], v[172:175]\n"
      "ds_read2_b32 v[170:171], v3 offset0:64 offset1:96\n"
      "v_mfma_f32_16x16x32_bf16 v[172:175], a[32:35], v[132:135], v[172:175]\n"
      "ds_read2_b32 v[168:169], v3 offset0:0 offset1:32\n"
      "s_min_u32 s36, s36, s6\n"
      "v_mfma_f32_16x16x32_bf16 v[172:175], a[36:39], v[136:139], v[172:175]\n"
      "s_waitcnt lgkmcnt(4)\n"
      "v_mfma_f32_16x16x32_bf16 v[172:175], a[40:43], v[140:143], v[172:175]\n"
      "buffer_store_dwordx4 v[212:215], v15, s[8:11], s38 offen offset:1024 sc1\n"
      "v_mfma_f32_16x16x32_bf16 v[172:175], a[44:47], v[144:147], v[172:175]\n"
      "ds_write_b32 v18, v218\n"
      "v_mfma_f32_16x16x32_bf16 v[172:175], a[48:51], v[148:151], v[172:175]\n"
      "s_waitcnt vmcnt(11) lgkmcnt(4)\n"
      "v_fmac_f32_e64 v219, v187, v187\n"
      "v_cmp_ne_u32 vcc, s21, v28\n"
      "v_mfma_f32_16x16x32_bf16 v[172:175], a[52:55], v[152:155], v[172:175]\n"
      "ds_write_b32 v18, v219 offset:4\n"
      "v_mfma_f32_16x16x32_bf16 v[172:175], a[56:59], v[156:159], v[172:175]\n"
      "s_waitcnt lgkmcnt(3)\n"
      "s_add_u32 m0, s39, 40960\n"
      "v_mfma_f32_16x16x32_bf16 v[172:175], a[60:63], v[160:163], v[172:175]\n"
      "ds_write_b32 v23, v21\n"
      "v_mfma_f32_16x16x32_bf16 v[172:175], a[64:67], v[164:167], v[172:175]\n"
      "s_waitcnt lgkmcnt(3)\n"
      "s_cbranch_vccnz .Lsp_w0_4\n"
      ".Lbk_w0_4:\n"
      "v_mfma_f32_16x16x32_bf16 v[172:175], a[68:71], v[168:171], v[172:175]\n"
      "v_mfma_f32_16x16x32_bf16 v[176:179], a[72:75], v[100:103], v[176:179]\n"
      "buffer_load_dword v28, v11, s[8:11], s36 offen sc1\n"
      "v_mfma_f32_16x16x32_bf16 v[176:179], a[76:79], v[104:107], v[176:179]\n"
      "s_waitcnt vmcnt(7) lgkmcnt(2)\n"
      "v_lshlrev_b32 v186, 16, v199\n"
      "v_lshlrev_b32 v184, 16, v198\n"
      "v_mfma_f32_16x16x32_bf16 v[176:179], a[80:83], v[108:111], v[176:179]\n"
      "v_lshlrev_b32 v180, 16, v196\n"
      "v_and_b32 v183, 0xffff0000, v197\n"
      "v_mfma_f32_16x16x32_bf16 v[176:179], a[84:87], v[112:115], v[176:179]\n"
      "buffer_store_dword v25, v12, s[8:11], s50 offen sc1\n"
      "v_mfma_f32_16x16x32_bf16 v[176:179], a[88:91], v[116:119], v[176:179]\n"
      "v_lshlrev_b32 v182, 16, v197\n"
      "v_and_b32 v181, 0xffff0000, v196\n"
      "v_mfma_f32_16x16x32_bf16 v[176:179], a[92:95], v[120:123], v[176:179]\n"
      "v_and_b32 v187, 0xffff0000, v199\n"
      "v_and_b32 v185, 0xffff0000, v198\n"
      "v_mfma_f32_16x16x32_bf16 v[176:179], a[96:99], v[124:127], v[176:179]\n"
      "buffer_load_dwordx4 v[196:199], v13, s[8:11], s47 offen offset:1024 sc1\n"
      "v_mfma_f32_16x16x32_bf16 v[176:179], a[100:103], v[128:131], v[176:179]\n"
      "ds_read_b32 v22, v24\n"
      "v_mul_f32 v219, v172, v172\n"
      "v_mfma_f32_16x16x32_bf16 v[176:179], a[104:107], v[132:135], v[176:179]\n"
      "v_mfma_f32_16x16x32_bf16 v[176:179], a[108:111], v[136:139], v[176:179]\n"
      "v_mfma_f32_16x16x32_bf16 v[176:179], a[112:115], v[140:143], v[176:179]\n"
      "buffer_load_dwordx4 v0, s[8:11], s46 offen offset:0 sc1 lds\n"
      "v_mfma_f32_16x16x32_bf16 v[176:179], a[116:119], v[144:147], v[176:179]\n"
      "v_add_f32 v218, v172, v173\n"
      "v_fmac_f32 v219, v173, v173\n"
      "v_mfma_f32_16x16x32_bf16 v[176:179], a[120:123], v[148:151], v[176:179]\n"
      "s_waitcnt lgkmcnt(0)\n"
      "v_cmp_gt_i32 vcc, v21, v22\n"
      "v_mfma_f32_16x16x32_bf16 v[176:179], a[124:127], v[152:155], v[176:179]\n"
      "v_mfma_f32_16x16x32_bf16 v[176:179], a[128:131], v[156:159], v[176:179]\n"
      "buffer_load_dwordx4 v0, s[8:11], s46 offen offset:1024 sc1 lds\n"
      "v_mfma_f32_16x16x32_bf16 v[176:179], a[132:135], v[160:163], v[176:179]\n"
      "s_cbranch_vccnz .Lsp_wz0_5\n"
      ".Lbk_wz0_5:\n"
      "ds_read2st64_b32 v[204:205], v17 offset1:1\n"
      "v_mfma_f32_16x16x32_bf16 v[176:179], a[136:139], v[164:167], v[176:179]\n"
      "ds_read2st64_b32 v[206:207], v17 offset0:2 offset1:3\n"
      "v_add_u32 v21, 1, v21\n"
      "v_mfma_f32_16x16x32_bf16 v[176:179], a[140:143], v[168:171], v[176:179]\n"
      "s_waitcnt vmcnt(10)\n"
      "ds_write_b32 v23, v21\n"
      "v_mfma_f32_16x16x32_bf16 v[180:183], a[144:147], v[100:103], v[180:183]\n"
      "buffer_load_dwordx4 v0, s[8:11], s46 offen offset:3072 sc1 lds\n"
      "v_mfma_f32_16x16x32_bf16 v[180:183], a[148:151], v[104:107], v[180:183]\n"
      "v_add_f32 v218, v218, v174\n"
      "v_fmac_f32 v219, v174, v174\n"
      "v_mfma_f32_16x16x32_bf16 v[180:183], a[152:155], v[108:111], v[180:183]\n"
      "v_fmac_f32 v219, v175, v175\n"
      "v_fmac_f32 v219, v176, v176\n"
      "v_mfma_f32_16x16x32_bf16 v[180:183], a[156:159], v[112:115], v[180:183]\n"
      "v_add_f32 v218, v218, v175\n"
      "v_add_f32 v218, v218, v176\n"
      "v_mfma_f32_16x16x32_bf16 v[180:183], a[160:163], v[116:119], v[180:183]\n"
      "buffer_load_dwordx4 v0, s[8:11], s46 offen offset:2048 sc1 lds\n"
      "v_mfma_f32_16x16x32_bf16 v[180:183], a[164:167], v[120:123], v[180:183]\n"
      "s_add_u32 m0, s40, 40960\n"
      "v_add_f32 v218, v218, v177\n"
      "v_cvt_pk_bf16_f32 v209, v174, v175\n"
      "v_mfma_f32_16x16x32_bf16 v[180:183], a[168:171], v[124:127], v[180:183]\n"
      "s_waitcnt lgkmcnt(0)\n"
      "v_add_f32 v188, v188, v204\n"
      "v_add_f32 v190, v190, v206\n"
      "v_mfma_f32_16x16x32_bf16 v[180:183], a[172:175], v[128:131], v[180:183]\n"
      "v_cvt_pk_bf16_f32 v208, v172, v173\n"
      "v_fmac_f32 v219, v177, v177\n"
      "v_mfma_f32_16x16x32_bf16 v[180:183], a[176:179], v[132:135], v[180:183]\n"
      "buffer_load_dwordx4 v1, s[8:11], s46 offen offset:0 sc1 lds\n"
      "v_mfma_f32_16x16x32_bf16 v[180:183], a[180:183], v[136:139], v[180:183]\n"
      "s_add_u32 s38, s38, 24576\n"
      "v_cvt_pk_bf16_f32 v210, v176, v177\n"
      "v_mfma_f32_16x16x32_bf16 v[180:183], a[184:187], v[140:143], v[180:183]\n"
      "ds_read_b32 v22, v24\n"
      "v_add_f32 v218, v218, v178\n"
      "v_mfma_f32_16x16x32_bf16 v[180:183], a[188:191], v[144:147], v[180:183]\n"
      "v_add_f32 v218, v218, v179\n"
      "v_add_f32 v218, v218, v188\n"
      "v_mfma_f32_16x16x32_bf16 v[180:183], a[192:195], v[148:151], v[180:183]\n"
      "s_add_u32 s50, s50, 24576\n"
      "v_fmac_f32 v219, v178, v178\n"
      "v_fmac_f32 v219, v179, v179\n"
      "v_mfma_f32_16x16x32_bf16 v[180:183], a[196:199], v[152:155], v[180:183]\n"
      "v_add_f32 v191, v191, v207\n"
      "v_cvt_pk_bf16_f32 v211, v178, v179\n"
      "v_mfma_f32_16x16x32_bf16 v[180:183], a[200:203], v[156:159], v[180:183]\n"
      "v_fmac_f32 v219, v188, v188\n"
      "v_add_f32 v189, v189, v205\n"
      "v_mfma_f32_16x16x32_bf16 v[180:183], a[204:207], v[160:163], v[180:183]\n"
      "s_waitcnt lgkmcnt(0)\n"
      "v_fmac_f32 v219, v189, v189\n"
      "v_cmp_gt_i32 vcc, v21, v22\n"
      "v_mfma_f32_16x16x32_bf16 v[180:183], a[208:211], v[164:167], v[180:183]\n"
      "v_fmac_f32 v219, v190, v190\n"
      "v_add_f32 v218, v218, v189\n"
      "v_mfma_f32_16x16x32_bf16 v[180:183], a[212:215], v[168:171], v[180:183]\n"
      "s_waitcnt vmcnt(11)\n"
      "v_lshlrev_b32 v172, 16, v192\n"
      "v_mfma_f32_16x16x32_bf16 v[184:187], a[216:219], v[100:103], v[184:187]\n"
      "v_and_b32 v175, 0xffff0000, v193\n"
      "v_and_b32 v177, 0xffff0000, v194\n"
      "v_mfma_f32_16x16x32_bf16 v[184:187], a[220:223], v[104:107], v[184:187]\n"
      "s_cbranch_vccnz .Lsp_wy1_6\n"
      ".Lbk_wy1_6:\n"
      "v_add_f32 v218, v218, v190\n"
      "v_mfma_f32_16x16x32_bf16 v[184:187], a[224:227], v[108:111], v[184:187]\n"
      "ds_read_b128 v[104:107], v0 offset:21504\n"
      "v_cvt_pk_bf16_f32 v216, v188, v189\n"
      "v_mfma_f32_16x16x32_bf16 v[184:187], a[228:231], v[112:115], v[184:187]\n"
      "v_add_f32 v218, v218, v191\n"
      "v_add_f32 v218, v218, v180\n"
      "v_mfma_f32_16x16x32_bf16 v[184:187], a[232:235], v[116:119], v[184:187]\n"
      "ds_read_b128 v[116:119], v0 offset:24576\n"
      "v_cvt_pk_bf16_f32 v217, v190, v191\n"
      "v_mfma_f32_16x16x32_bf16 v[184:187], a[236:239], v[120:123], v[184:187]\n"
      "ds_read_b128 v[108:111], v0 offset:22528\n"
      "v_lshlrev_b32 v188, 16, v200\n"
      "v_mfma_f32_16x16x32_bf16 v[184:187], a[240:243], v[124:127], v[184:187]\n"
      "ds_read_b128 v[100:103], v0 offset:20480\n"
      "v_fmac_f32 v219, v191, v191\n"
      "v_mfma_f32_16x16x32_bf16 v[184:187], a[244:247], v[128:131], v[184:187]\n"
      "v_lshlrev_b32 v190, 16, v201\n"
      "v_and_b32 v191, 0xffff0000, v201\n"
      "v_mfma_f32_16x16x32_bf16 v[184:187], a[248:251], v[132:135], v[184:187]\n"
      "ds_read2_b32 v[134:135], v4 offset0:64 offset1:96\n"
      "v_lshlrev_b32 v174, 16, v193\n"
      "v_mfma_f32_16x16x32_bf16 v[184:187], a[252:255], v[136:139], v[184:187]\n"
      "s_sub_u32 s47, s46, s45\n"
      "v_and_b32 v189, 0xffff0000, v200\n"
      "v_fmac_f32 v219, v180, v180\n"
      "v_mfma_f32_16x16x32_bf16 v[184:187], v[32:35], v[140:143], v[184:187]\n"
      "buffer_store_dwordx4 v[208:211], v15, s[8:11], s38 offen sc1\n"
      "v_mfma_f32_16x16x32_bf16 v[184:187], v[36:39], v[144:147], v[184:187]\n"
      "ds_read_b128 v[136:139], v0 offset:28672\n"
      "v_fmac_f32 v219, v181, v181\n"
      "v_mfma_f32_16x16x32_bf16 v[184:187], v[40:43], v[148:151], v[184:187]\n"
      "ds_read2_b32 v[132:133], v4 offset0:0 offset1:32\n"
      "v_lshlrev_b32 v178, 16, v195\n"
      "v_mfma_f32_16x16x32_bf16 v[184:187], v[44:47], v[152:155], v[184:187]\n"
      "ds_read_b128 v[112:115], v0 offset:23552\n"
      "v_add_f32 v218, v218, v181\n"
      "v_mfma_f32_16x16x32_bf16 v[184:187], v[48:51], v[156:159], v[184:187]\n"
      "ds_read_b128 v[120:123], v0 offset:25600\n"
      "v_and_b32 v173, 0xffff0000, v192\n"
      "v_mfma_f32_16x16x32_bf16 v[184:187], v[52:55], v[160:163], v[184:187]\n"
      "ds_read_b128 v[124:127], v0 offset:26624\n"
      "v_lshlrev_b32 v176, 16, v194\n"
      "v_mfma_f32_16x16x32_bf16 v[184:187], v[56:59], v[164:167], v[184:187]\n"
      "buffer_load_dwordx2 v[200:201], v14, s[8:11], s47 offen sc1\n"
      "v_mfma_f32_16x16x32_bf16 v[184:187], v[60:63], v[168:171], v[184:187]\n"
      "s_waitcnt lgkmcnt(5)\n"
      "ds_read_b128 v[128:131], v0 offset:27648\n"
      "v_add_f32 v218, v218, v182\n"
      "v_mfma_f32_16x16x32_bf16 v[188:191], v[64:67], v[100:103], v[188:191]\n"
      "ds_read_b128 v[140:143], v0 offset:29696\n"
      "v_cvt_pk_bf16_f32 v213, v182, v183\n"
      "v_mfma_f32_16x16x32_bf16 v[188:191], v[68:71], v[104:107], v[188:191]\n"
      "ds_read_b128 v[152:155], v0 offset:32768\n"
      "v_mfma_f32_16x16x32_bf16 v[188:191], v[72:75], v[108:111], v[188:191]\n"
      "s_waitcnt lgkmcnt(5)\n"
      "buffer_store_dwordx2 v[216:217], v16, s[8:11], s38 offen sc1\n"
      "v_mfma_f32_16x16x32_bf16 v[188:191], v[76:79], v[112:115], v[188:191]\n"
      "ds_read_b128 v[144:147], v0 offset:30720\n"
      "v_cvt_pk_bf16_f32 v212, v180, v181\n"
      "v_mfma_f32_16x16x32_bf16 v[188:191], v[80:83], v[116:119], v[188:191]\n"
      "s_waitcnt lgkmcnt(4)\n"
      "ds_read_b128 v[164:167], v0 offset:35840\n"
      "v_add_f32 v218, v218, v183\n"
      "v_mfma_f32_16x16x32_bf16 v[188:191], v[84:87], v[120:123], v[188:191]\n"
      "v_fmac_f32 v219, v182, v182\n"
      "v_add_f32 v218, v218, v184\n"
      "v_mfma_f32_16x16x32_bf16 v[188:191], v[88:91], v[124:127], v[188:191]\n"
      "s_waitcnt lgkmcnt(4)\n"
      "ds_read_b128 v[148:151], v0 offset:31744\n"
      "v_add_f32 v218, v218, v185\n"
      "v_mfma_f32_16x16x32_bf16 v[188:191], v[92:95], v[128:131], v[188:191]\n"
      "ds_read_b128 v[160:163], v0 offset:34816\n"
      "v_and_b32 v179, 0xffff0000, v195\n"
      "v_mfma_f32_16x16x32_bf16 v[188:191], v[96:99], v[132:135], v[188:191]\n"
      "buffer_load_dwordx4 v[192:195], v13, s[8:11], s47 offen sc1\n"
      "v_mfma_f32_16x16x32_bf16 v[172:175], a[0:3], v[100:103], v[172:175]\n"
      "v_cvt_pk_bf16_f32 v215, v186, v187\n"
      "v_fmac_f32 v219, v183, v183\n"
      "v_mfma_f32_16x16x32_bf16 v[172:175], a[4:7], v[104:107], v[172:175]\n"
      "v_cvt_pk_bf16_f32 v214, v184, v185\n"
      "v_mfma_f32_16x16x32_bf16 v[172:175], a[8:11], v[108:111], v[172:175]\n"
      "ds_read2_b32 v[168:169], v5 offset0:0 offset1:32\n"
      "v_mfma_f32_16x16x32_bf16 v[172:175], a[12:15], v[112:115], v[172:175]\n"
      "ds_read_b128 v[156:159], v0 offset:33792\n"
      "v_mfma_f32_16x16x32_bf16 v[172:175], a[16:19], v[116:119], v[172:175]\n"
      "buffer_store_dwordx4 v[212:215], v15, s[8:11], s38 offen offset:1024 sc1\n"
      "v_mfma_f32_16x16x32_bf16 v[172:175], a[20:23], v[120:123], v[172:175]\n"
      "v_fmac_f32 v219, v184, v184\n"
      "v_fmac_f32 v219, v185, v185\n"
      "v_mfma_f32_16x16x32_bf16 v[172:175], a[24:27], v[124:127], v[172:175]\n"
      "ds_read2_b32 v[170:171], v5 offset0:64 offset1:96\n"
      "v_add_f32 v218, v218, v186\n"
      "v_mfma_f32_16x16x32_bf16 v[172:175], a[28:31], v[128:131], v[172:175]\n"
      "v_fmac_f32 v219, v186, v186\n"
      "v_add_f32 v218, v218, v187\n"
      "v_mfma_f32_16x16x32_bf16 v[172:175], a[32:35], v[132:135], v[172:175]\n"
      "ds_write_b32 v18, v218\n"
      "v_mfma_f32_16x16x32_bf16 v[172:175], a[36:39], v[136:139], v[172:175]\n"
      "s_waitcnt lgkmcnt(3)\n"
      "s_add_u32 m0, s39, 61440\n"
      "v_add_u32 v21, 1, v21\n"
      "v_mfma_f32_16x16x32_bf16 v[172:175], a[40:43], v[140:143], v[172:175]\n"
      "s_mov_b32 s46, s36\n"
      "v_mfma_f32_16x16x32_bf16 v[172:175], a[44:47], v[144:147], v[172:175]\n"
      "v_fmac_f32 v219, v187, v187\n"
      "v_mfma_f32_16x16x32_bf16 v[172:175], a[48:51], v[148:151], v[172:175]\n"
      "ds_write_b32 v18, v219 offset:4\n"
      "v_mfma_f32_16x16x32_bf16 v[172:175], a[52:55], v[152:155], v[172:175]\n"
      "s_waitcnt vmcnt(10) lgkmcnt(2)\n"
      "v_and_b32 v181, 0xffff0000, v196\n"
      "v_mfma_f32_16x16x32_bf16 v[172:175], a[56:59], v[156:159], v[172:175]\n"
      "ds_write_b32 v23, v21\n"
      "v_mfma_f32_16x16x32_bf16 v[172:175], a[60:63], v[160:163], v[172:175]\n"
      "v_lshlrev_b32 v186, 16, v199\n"
      "v_cmp_ne_u32 vcc, s21, v28\n"
      "v_mfma_f32_16x16x32_bf16 v[172:175], a[64:67], v[164:167], v[172:175]\n"
      "buffer_store_dword v25, v12, s[8:11], s50 offen sc1\n"
      "v_mfma_f32_16x16x32_bf16 v[172:175], a[68:71], v[168:171], v[172:175]\n"
      "s_cmp_eq_u32 s36, s37\n"
      "s_cselect_b32 s48, 49152, s51\n"
      "s_add_u32 s36, s36, s48\n"
      "v_mfma_f32_16x16x32_bf16 v[176:179], a[72:75], v[100:103], v[176:179]\n"
      "s_cbranch_vccnz .Lsp_w1_7\n"
      ".Lbk_w1_7:\n"
      "s_min_u32 s36, s36, s6\n"
      "v_mfma_f32_16x16x32_bf16 v[176:179], a[76:79], v[104:107], v[176:179]\n"
      "buffer_load_dword v28, v11, s[8:11], s36 offen sc1\n"
      "v_mfma_f32_16x16x32_bf16 v[176:179], a[80:83], v[108:111], v[176:179]\n"
      "v_and_b32 v183, 0xffff0000, v197\n"
      "v_mul_f32 v219, v172, v172\n"
      "v_mfma_f32_16x16x32_bf16 v[176:179], a[84:87], v[112:115], v[176:179]\n"
      "buffer_load_dwordx4 v0, s[8:11], s46 offen offset:0 sc1 lds\n"
      "v_mfma_f32_16x16x32_bf16 v[176:179], a[88:91], v[116:119], v[176:179]\n"
      "v_mfma_f32_16x16x32_bf16 v[176:179], a[92:95], v[120:123], v[176:179]\n"
      "v_fmac_f32 v219, v173, v173\n"
      "v_mfma_f32_16x16x32_bf16 v[176:179], a[96:99], v[124:127], v[176:179]\n"
      "v_lshlrev_b32 v184, 16, v198\n"
      "v_and_b32 v185, 0xffff0000, v198\n"
      "v_mfma_f32_16x16x32_bf16 v[176:179], a[100:103], v[128:131], v[176:179]\n"
      "ds_read_b32 v22, v24\n"
      "v_fmac_f32 v219, v174, v174\n"
      "v_mfma_f32_16x16x32_bf16 v[176:179], a[104:107], v[132:135], v[176:179]\n"
      "buffer_load_dwordx4 v0, s[8:11], s46 offen offset:1024 sc1 lds\n"
      "v_mfma_f32_16x16x32_bf16 v[176:179], a[108:111], v[136:139], v[176:179]\n"
      "v_lshlrev_b32 v182, 16, v197\n"
      "v_add_f32 v218, v172, v173\n"
      "v_mfma_f32_16x16x32_bf16 v[176:179], a[112:115], v[140:143], v[176:179]\n"
      "v_add_f32 v218, v218, v174\n"
      "v_fmac_f32 v219, v175, v175\n"
      "v_mfma_f32_16x16x32_bf16 v[176:179], a[116:119], v[144:147], v[176:179]\n"
      "v_cvt_pk_bf16_f32 v208, v172, v173\n"
      "v_mfma_f32_16x16x32_bf16 v[176:179], a[120:123], v[148:151], v[176:179]\n"
      "s_waitcnt lgkmcnt(0)\n"
      "v_and_b32 v187, 0xffff0000, v199\n"
      "v_cmp_gt_i32 vcc, v21, v22\n"
      "v_mfma_f32_16x16x32_bf16 v[176:179], a[124:127], v[152:155], v[176:179]\n"
      "buffer_load_dwordx4 v0, s[8:11], s46 offen offset:3072 sc1 lds\n"
      "v_mfma_f32_16x16x32_bf16 v[176:179], a[128:131], v[156:159], v[176:179]\n"
      "v_lshlrev_b32 v180, 16, v196\n"
      "v_add_f32 v218, v218, v175\n"
      "v_mfma_f32_16x16x32_bf16 v[176:179], a[132:135], v[160:163], v[176:179]\n"
      "s_cbranch_vccnz .Lsp_wz1_8\n"
      ".Lbk_wz1_8:\n"
      "ds_read2st64_b32 v[204:205], v17 offset1:1\n"
      "v_mfma_f32_16x16x32_bf16 v[176:179], a[136:139], v[164:167], v[176:179]\n"
      "ds_read2st64_b32 v[206:207], v17 offset0:2 offset1:3\n"
      "v_add_u32 v21, 1, v21\n"
      "v_mfma_f32_16x16x32_bf16 v[176:179], a[140:143], v[168:171], v[176:179]\n"
      "s_waitcnt vmcnt(10)\n"
      "ds_write_b32 v23, v21\n"
      "v_mfma_f32_16x16x32_bf16 v[180:183], a[144:147], v[100:103], v[180:183]\n"
      "buffer_load_dwordx4 v0, s[8:11], s46 offen offset:2048 sc1 lds\n"
      "v_mfma_f32_16x16x32_bf16 v[180:183], a[148:151], v[104:107], v[180:183]\n"
      "v_mfma_f32_16x16x32_bf16 v[180:183], a[152:155], v[108:111], v[180:183]\n"
      "s_add_u32 m0, s40, 61440\n"
      "v_add_f32 v218, v218, v176\n"
      "v_mfma_f32_16x16x32_bf16 v[180:183], a[156:159], v[112:115], v[180:183]\n"
      "v_fmac_f32 v219, v176, v176\n"
      "v_cvt_pk_bf16_f32 v209, v174, v175\n"
      "v_mfma_f32_16x16x32_bf16 v[180:183], a[160:163], v[116:119], v[180:183]\n"
      "buffer_load_dwordx4 v[196:199], v13, s[8:11], s47 offen offset:1024 sc1\n"
      "v_mfma_f32_16x16x32_bf16 v[180:183], a[164:167], v[120:123], v[180:183]\n"
      "s_waitcnt lgkmcnt(1)\n"
      "v_cvt_pk_bf16_f32 v211, v178, v179\n"
      "v_add_f32 v190, v190, v206\n"
      "v_mfma_f32_16x16x32_bf16 v[180:183], a[168:171], v[124:127], v[180:183]\n"
      "v_add_f32 v218, v218, v177\n"
      "v_add_f32 v218, v218, v178\n"
      "v_mfma_f32_16x16x32_bf16 v[180:183], a[172:175], v[128:131], v[180:183]\n"
      "v_add_f32 v218, v218, v179\n"
      "v_add_f32 v189, v189, v205\n"
      "v_mfma_f32_16x16x32_bf16 v[180:183], a[176:179], v[132:135], v[180:183]\n"
      "buffer_load_dwordx4 v1, s[8:11], s46 offen offset:0 sc1 lds\n"
      "v_mfma_f32_16x16x32_bf16 v[180:183], a[180:183], v[136:139], v[180:183]\n"
      "v_add_f32 v188, v188, v204\n"
      "v_fmac_f32 v219, v177, v177\n"
      "v_mfma_f32_16x16x32_bf16 v[180:183], a[184:187], v[140:143], v[180:183]\n"
      "ds_read_b32 v22, v24\n"
      "v_fmac_f32 v219, v178, v178\n"
      "v_mfma_f32_16x16x32_bf16 v[180:183], a[188:191], v[144:147], v[180:183]\n"
      "v_cvt_pk_bf16_f32 v210, v176, v177\n"
      "v_fmac_f32 v219, v179, v179\n"
      "v_mfma_f32_16x16x32_bf16 v[180:183], a[192:195], v[148:151], v[180:183]\n"
      "v_add_f32 v191, v191, v207\n"
      "v_fmac_f32 v219, v188, v188\n"
      "v_mfma_f32_16x16x32_bf16 v[180:183], a[196:199], v[152:155], v[180:183]\n"
      "v_add_f32 v218, v218, v188\n"
      "v_mfma_f32_16x16x32_bf16 v[180:183], a[200:203], v[156:159], v[180:183]\n"
      "v_add_f32 v218, v218, v189\n"
      "v_add_f32 v218, v218, v190\n"
      "v_mfma_f32_16x16x32_bf16 v[180:183], a[204:207], v[160:163], v[180:183]\n"
      "s_waitcnt lgkmcnt(0)\n"
      "v_fmac_f32 v219, v189, v189\n"
      "v_cmp_gt_i32 vcc, v21, v22\n"
      "v_mfma_f32_16x16x32_bf16 v[180:183], a[208:211], v[164:167], v[180:183]\n"
      "v_cvt_pk_bf16_f32 v216, v188, v189\n"
      "v_fmac_f32 v219, v190, v190\n"
      "v_mfma_f32_16x16x32_bf16 v[180:183], a[212:215], v[168:171], v[180:183]\n"
      "s_waitcnt vmcnt(11)\n"
      "v_and_b32 v189, 0xffff0000, v200\n"
      "v_fmac_f32 v219, v191, v191\n"
      "v_mfma_f32_16x16x32_bf16 v[184:187], a[216:219], v[100:103], v[184:187]\n"
      "s_add_u32 s38, s38, 24576\n"
      "v_cvt_pk_bf16_f32 v217, v190, v191\n"
      "v_lshlrev_b32_e64 v188, 16, v200\n"
      "v_mfma_f32_16x16x32_bf16 v[184:187], a[220:223], v[104:107], v[184:187]\n"
      "s_cbranch_vccnz .Lsp_wy0_9\n"
      ".Lbk_wy0_9:\n"
      "v_mfma_f32_16x16x32_bf16 v[184:187], a[224:227], v[108:111], v[184:187]\n"
      "ds_read_b128 v[104:107], v10 offset:1024\n"
      "v_add_f32 v218, v218, v191\n"
      "v_mfma_f32_16x16x32_bf16 v[184:187], a[228:231], v[112:115], v[184:187]\n"
      "v_fmac_f32 v219, v180, v180\n"
      "v_lshlrev_b32 v190, 16, v201\n"
      "v_mfma_f32_16x16x32_bf16 v[184:187], a[232:235], v[116:119], v[184:187]\n"
      "ds_read_b128 v[100:103], v10 offset:0\n"
      "v_add_f32 v218, v218, v180\n"
      "v_mfma_f32_16x16x32_bf16 v[184:187], a[236:239], v[120:123], v[184:187]\n"
      "s_waitcnt vmcnt(9)\n"
      "ds_read_b128 v[120:123], v10 offset:5120\n"
      "v_lshlrev_b32 v178, 16, v195\n"
      "v_mfma_f32_16x16x32_bf16 v[184:187], a[240:243], v[124:127], v[184:187]\n"
      "ds_read_b128 v[112:115], v10 offset:3072\n"
      "v_fmac_f32 v219, v181, v181\n"
      "v_mfma_f32_16x16x32_bf16 v[184:187], a[244:247], v[128:131], v[184:187]\n"
      "ds_read_b128 v[116:119], v10 offset:4096\n"
      "v_lshlrev_b32 v174, 16, v193\n"
      "v_mfma_f32_16x16x32_bf16 v[184:187], a[248:251], v[132:135], v[184:187]\n"
      "ds_read_b128 v[124:127], v10 offset:6144\n"
      "v_fmac_f32 v219, v182, v182\n"
      "v_mfma_f32_16x16x32_bf16 v[184:187], a[252:255], v[136:139], v[184:187]\n"
      "s_sub_u32 s47, s46, s45\n"
      "ds_read_b128 v[108:111], v10 offset:2048\n"
      "v_mfma_f32_16x16x32_bf16 v[184:187], v[32:35], v[140:143], v[184:187]\n"
      "v_and_b32 v191, 0xffff0000, v201\n"
      "v_and_b32 v177, 0xffff0000, v194\n"
      "v_mfma_f32_16x16x32_bf16 v[184:187], v[36:39], v[144:147], v[184:187]\n"
      "ds_read_b128 v[136:139], v10 offset:8192\n"
      "v_lshlrev_b32 v176, 16, v194\n"
      "v_mfma_f32_16x16x32_bf16 v[184:187], v[40:43], v[148:151], v[184:187]\n"
      "ds_read_b128 v[128:131], v10 offset:7168\n"
      "v_lshlrev_b32 v172, 16, v192\n"
      "v_mfma_f32_16x16x32_bf16 v[184:187], v[44:47], v[152:155], v[184:187]\n"
      "v_fmac_f32 v219, v183, v183\n"
      "v_and_b32 v175, 0xffff0000, v193\n"
      "v_mfma_f32_16x16x32_bf16 v[184:187], v[48:51], v[156:159], v[184:187]\n"
      "buffer_load_dwordx2 v[200:201], v14, s[8:11], s47 offen sc1\n"
      "v_mfma_f32_16x16x32_bf16 v[184:187], v[52:55], v[160:163], v[184:187]\n"
      "ds_read2_b32 v[132:133], v6 offset0:0 offset1:32\n"
      "v_and_b32 v173, 0xffff0000, v192\n"
      "v_mfma_f32_16x16x32_bf16 v[184:187], v[56:59], v[164:167], v[184:187]\n"
      "s_waitcnt lgkmcnt(3)\n"
      "ds_read2_b32 v[134:135], v6 offset0:64 offset1:96\n"
      "v_add_f32 v218, v218, v181\n"
      "v_mfma_f32_16x16x32_bf16 v[184:187], v[60:63], v[168:171], v[184:187]\n"
      "v_mfma_f32_16x16x32_bf16 v[188:191], v[64:67], v[100:103], v[188:191]\n"
      "ds_read_b128 v[140:143], v10 offset:9216\n"
      "v_and_b32 v179, 0xffff0000, v195\n"
      "v_mfma_f32_16x16x32_bf16 v[188:191], v[68:71], v[104:107], v[188:191]\n"
      "buffer_load_dwordx4 v[192:195], v13, s[8:11], s47 offen sc1\n"
      "v_mfma_f32_16x16x32_bf16 v[188:191], v[72:75], v[108:111], v[188:191]\n"
      "v_add_f32 v218, v218, v182\n"
      "v_mfma_f32_16x16x32_bf16 v[188:191], v[76:79], v[112:115], v[188:191]\n"
      "ds_read_b128 v[156:159], v10 offset:13312\n"
      "v_fmac_f32 v219, v184, v184\n"
      "v_mfma_f32_16x16x32_bf16 v[188:191], v[80:83], v[116:119], v[188:191]\n"
      "ds_read_b128 v[164:167], v10 offset:15360\n"
      "v_cvt_pk_bf16_f32 v213, v182, v183\n"
      "v_mfma_f32_16x16x32_bf16 v[188:191], v[84:87], v[120:123], v[188:191]\n"
      "ds_read_b128 v[144:147], v10 offset:10240\n"
      "v_mfma_f32_16x16x32_bf16 v[188:191], v[88:91], v[124:127], v[188:191]\n"
      "s_waitcnt lgkmcnt(4)\n"
      "v_cvt_pk_bf16_f32 v215, v186, v187\n"
      "v_add_f32 v218, v218, v183\n"
      "v_mfma_f32_16x16x32_bf16 v[188:191], v[92:95], v[128:131], v[188:191]\n"
      "buffer_store_dwordx4 v[208:211], v15, s[8:11], s38 offen sc1\n"
      "v_mfma_f32_16x16x32_bf16 v[188:191], v[96:99], v[132:135], v[188:191]\n"
      "v_cvt_pk_bf16_f32 v212, v180, v181\n"
      "v_add_f32 v218, v218, v184\n"
      "v_mfma_f32_16x16x32_bf16 v[172:175], a[0:3], v[100:103], v[172:175]\n"
      "s_add_u32 s50, s50, 24576\n"
      "v_cvt_pk_bf16_f32 v214, v184, v185\n"
      "v_add_f32 v218, v218, v185\n"
      "v_mfma_f32_16x16x32_bf16 v[172:175], a[4:7], v[104:107], v[172:175]\n"
      "s_mov_b32 s46, s36\n"
      "v_add_f32 v218, v218, v186\n"
      "v_mfma_f32_16x16x32_bf16 v[172:175], a[8:11], v[108:111], v[172:175]\n"
      "ds_read_b128 v[148:151], v10 offset:11264\n"
      "v_add_f32 v218, v218, v187\n"
      "v_mfma_f32_16x16x32_bf16 v[172:175], a[12:15], v[112:115], v[172:175]\n"
      "v_fmac_f32 v219, v185, v185\n"
      "v_add_u32 v21, 1, v21\n"
      "v_mfma_f32_16x16x32_bf16 v[172:175], a[16:19], v[116:119], v[172:175]\n"
      "s_add_u32 s36, s36, 24576\n"
      "ds_read_b128 v[152:155], v10 offset:12288\n"
      "v_fmac_f32_e64 v219, v186, v186\n"
      "v_mfma_f32_16x16x32_bf16 v[172:175], a[20:23], v[120:123], v[172:175]\n"
      "buffer_store_dwordx2 v[216:217], v16, s[8:11], s38 offen sc1\n"
      "v_mfma_f32_16x16x32_bf16 v[172:175], a[24:27], v[124:127], v[172:175]\n"
      "ds_read_b128 v[160:163], v10 offset:14336\n"
      "v_mfma_f32_16x16x32_bf16 v[172:175], a[28:31], v[128:131], v[172:175]\n"
      "ds_read2_b32 v[170:171], v7 offset0:64 offset1:96\n"
      "v_mfma_f32_16x16x32_bf16 v[172:175], a[32:35], v[132:135], v[172:175]\n"
      "ds_read2_b32 v[168:169], v7 offset0:0 offset1:32\n"
      "s_min_u32 s36, s36, s6\n"
      "v_mfma_f32_16x16x32_bf16 v[172:175], a[36:39], v[136:139], v[172:175]\n"
      "s_waitcnt lgkmcnt(4)\n"
      "v_mfma_f32_16x16x32_bf16 v[172:175], a[40:43], v[140:143], v[172:175]\n"
      "buffer_store_dwordx4 v[212:215], v15, s[8:11], s38 offen offset:1024 sc1\n"
      "v_mfma_f32_16x16x32_bf16 v[172:175], a[44:47], v[144:147], v[172:175]\n"
      "ds_write_b32 v18, v218\n"
      "v_mfma_f32_16x16x32_bf16 v[172:175], a[48:51], v[148:151], v[172:175]\n"
      "s_waitcnt vmcnt(11) lgkmcnt(4)\n"
      "v_fmac_f32_e64 v219, v187, v187\n"
      "v_cmp_ne_u32 vcc, s21, v28\n"
      "v_mfma_f32_16x16x32_bf16 v[172:175], a[52:55], v[152:155], v[172:175]\n"
      "ds_write_b32 v18, v219 offset:4\n"
      "v_mfma_f32_16x16x32_bf16 v[172:175], a[56:59], v[156:159], v[172:175]\n"
      "s_waitcnt lgkmcnt(3)\n"
      "s_add_u32 m0, s39, 0\n"
      "v_mfma_f32_16x16x32_bf16 v[172:175], a[60:63], v[160:163], v[172:175]\n"
      "ds_write_b32 v23, v21\n"
      "v_mfma_f32_16x16x32_bf16 v[172:175], a[64:67], v[164:167], v[172:175]\n"
      "s_waitcnt lgkmcnt(3)\n"
      "s_cbranch_vccnz .Lsp_w0_10\n"
      ".Lbk_w0_10:\n"
      "v_mfma_f32_16x16x32_bf16 v[172:175], a[68:71], v[168:171], v[172:175]\n"
      "v_mfma_f32_16x16x32_bf16 v[176:179], a[72:75], v[100:103], v[176:179]\n"
      "buffer_load_dword v28, v11, s[8:11], s36 offen sc1\n"
      "v_mfma_f32_16x16x32_bf16 v[176:179], a[76:79], v[104:107], v[176:179]\n"
      "s_waitcnt vmcnt(7) lgkmcnt(2)\n"
      "v_lshlrev_b32 v186, 16, v199\n"
      "v_lshlrev_b32 v184, 16, v198\n"
      "v_mfma_f32_16x16x32_bf16 v[176:179], a[80:83], v[108:111], v[176:179]\n"
      "v_lshlrev_b32 v180, 16, v196\n"
      "v_and_b32 v183, 0xffff0000, v197\n"
      "v_mfma_f32_16x16x32_bf16 v[176:179], a[84:87], v[112:115], v[176:179]\n"
      "buffer_store_dword v25, v12, s[8:11], s50 offen sc1\n"
      "v_mfma_f32_16x16x32_bf16 v[176:179], a[88:91], v[116:119], v[176:179]\n"
      "v_lshlrev_b32 v182, 16, v197\n"
      "v_and_b32 v181, 0xffff0000, v196\n"
      "v_mfma_f32_16x16x32_bf16 v[176:179], a[92:95], v[120:123], v[176:179]\n"
      "v_and_b32 v187, 0xffff0000, v199\n"
      "v_and_b32 v185, 0xffff0000, v198\n"
      "v_mfma_f32_16x16x32_bf16 v[176:179], a[96:99], v[124:127], v[176:179]\n"
      "buffer_load_dwordx4 v[196:199], v13, s[8:11], s47 offen offset:1024 sc1\n"
      "v_mfma_f32_16x16x32_bf16 v[176:179], a[100:103], v[128:131], v[176:179]\n"
      "ds_read_b32 v22, v24\n"
      "v_mul_f32 v219, v172, v172\n"
      "v_mfma_f32_16x16x32_bf16 v[176:179], a[104:107], v[132:135], v[176:179]\n"
      "v_mfma_f32_16x16x32_bf16 v[176:179], a[108:111], v[136:139], v[176:179]\n"
      "v_mfma_f32_16x16x32_bf16 v[176:179], a[112:115], v[140:143], v[176:179]\n"
      "buffer_load_dwordx4 v0, s[8:11], s46 offen offset:0 sc1 lds\n"
      "v_mfma_f32_16x16x32_bf16 v[176:179], a[116:119], v[144:147], v[176:179]\n"
      "v_add_f32 v218, v172, v173\n"
      "v_fmac_f32 v219, v173, v173\n"
      "v_mfma_f32_16x16x32_bf16 v[176:179], a[120:123], v[148:151], v[176:179]\n"
      "s_waitcnt lgkmcnt(0)\n"
      "v_cmp_gt_i32 vcc, v21, v22\n"
      "v_mfma_f32_16x16x32_bf16 v[176:179], a[124:127], v[152:155], v[176:179]\n"
      "v_mfma_f32_16x16x32_bf16 v[176:179], a[128:131], v[156:159], v[176:179]\n"
      "buffer_load_dwordx4 v0, s[8:11], s46 offen offset:1024 sc1 lds\n"
      "v_mfma_f32_16x16x32_bf16 v[176:179], a[132:135], v[160:163], v[176:179]\n"
      "s_cbranch_vccnz .Lsp_wz0_11\n"
      ".Lbk_wz0_11:\n"
      "ds_read2st64_b32 v[204:205], v17 offset1:1\n"
      "v_mfma_f32_16x16x32_bf16 v[176:179], a[136:139], v[164:167], v[176:179]\n"
      "ds_read2st64_b32 v[206:207], v17 offset0:2 offset1:3\n"
      "v_add_u32 v21, 1, v21\n"
      "v_mfma_f32_16x16x32_bf16 v[176:179], a[140:143], v[168:171], v[176:179]\n"
      "s_waitcnt vmcnt(10)\n"
      "ds_write_b32 v23, v21\n"
      "v_mfma_f32_16x16x32_bf16 v[180:183], a[144:147], v[100:103], v[180:183]\n"
      "buffer_load_dwordx4 v0, s[8:11], s46 offen offset:3072 sc1 lds\n"
      "v_mfma_f32_16x16x32_bf16 v[180:183], a[148:151], v[104:107], v[180:183]\n"
      "v_add_f32 v218, v218, v174\n"
      "v_fmac_f32 v219, v174, v174\n"
      "v_mfma_f32_16x16x32_bf16 v[180:183], a[152:155], v[108:111], v[180:183]\n"
      "v_fmac_f32 v219, v175, v175\n"
      "v_fmac_f32 v219, v176, v176\n"
      "v_mfma_f32_16x16x32_bf16 v[180:183], a[156:159], v[112:115], v[180:183]\n"
      "v_add_f32 v218, v218, v175\n"
      "v_add_f32 v218, v218, v176\n"
      "v_mfma_f32_16x16x32_bf16 v[180:183], a[160:163], v[116:119], v[180:183]\n"
      "buffer_load_dwordx4 v0, s[8:11], s46 offen offset:2048 sc1 lds\n"
      "v_mfma_f32_16x16x32_bf16 v[180:183], a[164:167], v[120:123], v[180:183]\n"
      "s_add_u32 m0, s40, 0\n"
      "v_add_f32 v218, v218, v177\n"
      "v_cvt_pk_bf16_f32 v209, v174, v175\n"
      "v_mfma_f32_16x16x32_bf16 v[180:183], a[168:171], v[124:127], v[180:183]\n"
      "s_waitcnt lgkmcnt(0)\n"
      "v_add_f32 v188, v188, v204\n"
      "v_add_f32 v190, v190, v206\n"
      "v_mfma_f32_16x16x32_bf16 v[180:183], a[172:175], v[128:131], v[180:183]\n"
      "v_cvt_pk_bf16_f32 v208, v172, v173\n"
      "v_fmac_f32 v219, v177, v177\n"
      "v_mfma_f32_16x16x32_bf16 v[180:183], a[176:179], v[132:135], v[180:183]\n"
      "buffer_load_dwordx4 v1, s[8:11], s46 offen offset:0 sc1 lds\n"
      "v_mfma_f32_16x16x32_bf16 v[180:183], a[180:183], v[136:139], v[180:183]\n"
      "s_add_u32 s38, s38, 24576\n"
      "v_cvt_pk_bf16_f32 v210, v176, v177\n"
      "v_mfma_f32_16x16x32_bf16 v[180:183], a[184:187], v[140:143], v[180:183]\n"
      "ds_read_b32 v22, v24\n"
      "v_add_f32 v218, v218, v178\n"
      "v_mfma_f32_16x16x32_bf16 v[180:183], a[188:191], v[144:147], v[180:183]\n"
      "v_add_f32 v218, v218, v179\n"
      "v_add_f32 v218, v218, v188\n"
      "v_mfma_f32_16x16x32_bf16 v[180:183], a[192:195], v[148:151], v[180:183]\n"
      "s_add_u32 s50, s50, 24576\n"
      "v_fmac_f32 v219, v178, v178\n"
      "v_fmac_f32 v219, v179, v179\n"
      "v_mfma_f32_16x16x32_bf16 v[180:183], a[196:199], v[152:155], v[180:183]\n"
      "v_add_f32 v191, v191, v207\n"
      "v_cvt_pk_bf16_f32 v211, v178, v179\n"
      "v_mfma_f32_16x16x32_bf16 v[180:183], a[200:203], v[156:159], v[180:183]\n"
      "v_fmac_f32 v219, v188, v188\n"
      "v_add_f32 v189, v189, v205\n"
      "v_mfma_f32_16x16x32_bf16 v[180:183], a[204:207], v[160:163], v[180:183]\n"
      "s_waitcnt lgkmcnt(0)\n"
      "v_fmac_f32 v219, v189, v189\n"
      "v_cmp_gt_i32 vcc, v21, v22\n"
      "v_mfma_f32_16x16x32_bf16 v[180:183], a[208:211], v[164:167], v[180:183]\n"
      ".Lw_p1:\n"
      "s_cbranch_execz .Lw_pr\n"
      "v_fmac_f32 v219, v190, v190\n"
      "v_add_f32 v218, v218, v189\n"
      "v_mfma_f32_16x16x32_bf16 v[180:183], a[212:215], v[168:171], v[180:183]\n"
      "s_waitcnt vmcnt(11)\n"
      "v_lshlrev_b32 v172, 16, v192\n"
      "v_mfma_f32_16x16x32_bf16 v[184:187], a[216:219], v[100:103], v[184:187]\n"
      "v_and_b32 v175, 0xffff0000, v193\n"
      "v_and_b32 v177, 0xffff0000, v194\n"
      "v_mfma_f32_16x16x32_bf16 v[184:187], a[220:223], v[104:107], v[184:187]\n"
      "s_cbranch_vccnz .Lsp_wy1_12\n"
      ".Lbk_wy1_12:\n"
      "v_add_f32 v218, v218, v190\n"
      "v_mfma_f32_16x16x32_bf16 v[184:187], a[224:227], v[108:111], v[184:187]\n"
      "ds_read_b128 v[104:107], v10 offset:21504\n"
      "v_cvt_pk_bf16_f32 v216, v188, v189\n"
      "v_mfma_f32_16x16x32_bf16 v[184:187], a[228:231], v[112:115], v[184:187]\n"
      "v_add_f32 v218, v218, v191\n"
      "v_add_f32 v218, v218, v180\n"
      "v_mfma_f32_16x16x32_bf16 v[184:187], a[232:235], v[116:119], v[184:187]\n"
      "ds_read_b128 v[116:119], v10 offset:24576\n"
      "v_cvt_pk_bf16_f32 v217, v190, v191\n"
      "v_mfma_f32_16x16x32_bf16 v[184:187], a[236:239], v[120:123], v[184:187]\n"
      "ds_read_b128 v[108:111], v10 offset:22528\n"
      "v_lshlrev_b32 v188, 16, v200\n"
      "v_mfma_f32_16x16x32_bf16 v[184:187], a[240:243], v[124:127], v[184:187]\n"
      "ds_read_b128 v[100:103], v10 offset:20480\n"
      "v_fmac_f32 v219, v191, v191\n"
      "v_mfma_f32_16x16x32_bf16 v[184:187], a[244:247], v[128:131], v[184:187]\n"
      "v_lshlrev_b32 v190, 16, v201\n"
      "v_and_b32 v191, 0xffff0000, v201\n"
      "v_mfma_f32_16x16x32_bf16 v[184:187], a[248:251], v[132:135], v[184:187]\n"
      "ds_read2_b32 v[134:135], v8 offset0:64 offset1:96\n"
      "v_lshlrev_b32 v174, 16, v193\n"
      "v_mfma_f32_16x16x32_bf16 v[184:187], a[252:255], v[136:139], v[184:187]\n"
      "s_sub_u32 s47, s46, s45\n"
      "v_and_b32 v189, 0xffff0000, v200\n"
      "v_fmac_f32 v219, v180, v180\n"
      "v_mfma_f32_16x16x32_bf16 v[184:187], v[32:35], v[140:143], v[184:187]\n"
      "buffer_store_dwordx4 v[208:211], v15, s[8:11], s38 offen sc1\n"
      "v_mfma_f32_16x16x32_bf16 v[184:187], v[36:39], v[144:147], v[184:187]\n"
      "ds_read_b128 v[136:139], v10 offset:28672\n"
      "v_fmac_f32 v219, v181, v181\n"
      "v_mfma_f32_16x16x32_bf16 v[184:187], v[40:43], v[148:151], v[184:187]\n"
      "ds_read2_b32 v[132:133], v8 offset0:0 offset1:32\n"
      "v_lshlrev_b32 v178, 16, v195\n"
      "v_mfma_f32_16x16x32_bf16 v[184:187], v[44:47], v[152:155], v[184:187]\n"
      "ds_read_b128 v[112:115], v10 offset:23552\n"
      "v_add_f32 v218, v218, v181\n"
      "v_mfma_f32_16x16x32_bf16 v[184:187], v[48:51], v[156:159], v[184:187]\n"
      "ds_read_b128 v[120:123], v10 offset:25600\n"
      "v_and_b32 v173, 0xffff0000, v192\n"
      "v_mfma_f32_16x16x32_bf16 v[184:187], v[52:55], v[160:163], v[184:187]\n"
      "ds_read_b128 v[124:127], v10 offset:26624\n"
      "v_lshlrev_b32 v176, 16, v194\n"
      "v_mfma_f32_16x16x32_bf16 v[184:187], v[56:59], v[164:167], v[184:187]\n"
      "buffer_load_dwordx2 v[200:201], v14, s[8:11], s47 offen sc1\n"
      "v_mfma_f32_16x16x32_bf16 v[184:187], v[60:63], v[168:171], v[184:187]\n"
      "s_waitcnt lgkmcnt(5)\n"
      "ds_read_b128 v[128:131], v10 offset:27648\n"
      "v_add_f32 v218, v218, v182\n"
      "v_mfma_f32_16x16x32_bf16 v[188:191], v[64:67], v[100:103], v[188:191]\n"
      "ds_read_b128 v[140:143], v10 offset:29696\n"
      "v_cvt_pk_bf16_f32 v213, v182, v183\n"
      "v_mfma_f32_16x16x32_bf16 v[188:191], v[68:71], v[104:107], v[188:191]\n"
      "ds_read_b128 v[152:155], v10 offset:32768\n"
      "v_mfma_f32_16x16x32_bf16 v[188:191], v[72:75], v[108:111], v[188:191]\n"
      "s_waitcnt lgkmcnt(5)\n"
      "buffer_store_dwordx2 v[216:217], v16, s[8:11], s38 offen sc1\n"
      "v_mfma_f32_16x16x32_bf16 v[188:191], v[76:79], v[112:115], v[188:191]\n"
      "ds_read_b128 v[144:147], v10 offset:30720\n"
      "v_cvt_pk_bf16_f32 v212, v180, v181\n"
      "v_mfma_f32_16x16x32_bf16 v[188:191], v[80:83], v[116:119], v[188:191]\n"
      "s_waitcnt lgkmcnt(4)\n"
      "ds_read_b128 v[164:167], v10 offset:35840\n"
      "v_add_f32 v218, v218, v183\n"
      "v_mfma_f32_16x16x32_bf16 v[188:191], v[84:87], v[120:123], v[188:191]\n"
      "v_fmac_f32 v219, v182, v182\n"
      "v_add_f32 v218, v218, v184\n"
      "v_mfma_f32_16x16x32_bf16 v[188:191], v[88:91], v[124:127], v[188:191]\n"
      "s_waitcnt lgkmcnt(4)\n"
      "ds_read_b128 v[148:151], v10 offset:31744\n"
      "v_add_f32 v218, v218, v185\n"
      "v_mfma_f32_16x16x32_bf16 v[188:191], v[92:95], v[128:131], v[188:191]\n"
      "ds_read_b128 v[160:163], v10 offset:34816\n"
      "v_and_b32 v179, 0xffff0000, v195\n"
      "v_mfma_f32_16x16x32_bf16 v[188:191], v[96:99], v[132:135], v[188:191]\n"
      "buffer_load_dwordx4 v[192:195], v13, s[8:11], s47 offen sc1\n"
      "v_mfma_f32_16x16x32_bf16 v[172:175], a[0:3], v[100:103], v[172:175]\n"
      "v_cvt_pk_bf16_f32 v215, v186, v187\n"
      "v_fmac_f32 v219, v183, v183\n"
      "v_mfma_f32_16x16x32_bf16 v[172:175], a[4:7], v[104:107], v[172:175]\n"
      "v_cvt_pk_bf16_f32 v214, v184, v185\n"
      "v_mfma_f32_16x16x32_bf16 v[172:175], a[8:11], v[108:111], v[172:175]\n"
      "ds_read2_b32 v[168:169], v9 offset0:0 offset1:32\n"
      "v_mfma_f32_16x16x32_bf16 v[172:175], a[12:15], v[112:115], v[172:175]\n"
      "ds_read_b128 v[156:159], v10 offset:33792\n"
      "v_mfma_f32_16x16x32_bf16 v[172:175], a[16:19], v[116:119], v[172:175]\n"
      "buffer_store_dwordx4 v[212:215], v15, s[8:11], s38 offen offset:1024 sc1\n"
      "v_mfma_f32_16x16x32_bf16 v[172:175], a[20:23], v[120:123], v[172:175]\n"
      "v_fmac_f32 v219, v184, v184\n"
      "v_fmac_f32 v219, v185, v185\n"
      "v_mfma_f32_16x16x32_bf16 v[172:175], a[24:27], v[124:127], v[172:175]\n"
      "ds_read2_b32 v[170:171], v9 offset0:64 offset1:96\n"
      "v_add_f32 v218, v218, v186\n"
      "v_mfma_f32_16x16x32_bf16 v[172:175], a[28:31], v[128:131], v[172:175]\n"
      "v_fmac_f32 v219, v186, v186\n"
      "v_add_f32 v218, v218, v187\n"
      "v_mfma_f32_16x16x32_bf16 v[172:175], a[32:35], v[132:135], v[172:175]\n"
      "ds_write_b32 v18, v218\n"
      "v_mfma_f32_16x16x32_bf16 v[172:175], a[36:39], v[136:139], v[172:175]\n"
      "s_waitcnt lgkmcnt(3)\n"
      "s_add_u32 m0, s39, 20480\n"
      "v_add_u32 v21, 1, v21\n"
      "v_mfma_f32_16x16x32_bf16 v[172:175], a[40:43], v[140:143], v[172:175]\n"
      "s_mov_b32 s46, s36\n"
      "v_mfma_f32_16x16x32_bf16 v[172:175], a[44:47], v[144:147], v[172:175]\n"
      "v_fmac_f32 v219, v187, v187\n"
      "v_mfma_f32_16x16x32_bf16 v[172:175], a[48:51], v[148:151], v[172:175]\n"
      "ds_write_b32 v18, v219 offset:4\n"
      "v_mfma_f32_16x16x32_bf16 v[172:175], a[52:55], v[152:155], v[172:175]\n"
      "s_waitcnt vmcnt(10) lgkmcnt(2)\n"
      "v_and_b32 v181, 0xffff0000, v196\n"
      "v_mfma_f32_16x16x32_bf16 v[172:175], a[56:59], v[156:159], v[172:175]\n"
      "ds_write_b32 v23, v21\n"
      "v_mfma_f32_16x16x32_bf16 v[172:175], a[60:63], v[160:163], v[172:175]\n"
      "v_lshlrev_b32 v186, 16, v199\n"
      "v_cmp_ne_u32 vcc, s21, v28\n"
      "v_mfma_f32_16x16x32_bf16 v[172:175], a[64:67], v[164:167], v[172:175]\n"
      "buffer_store_dword v25, v12, s[8:11], s50 offen sc1\n"
      "v_mfma_f32_16x16x32_bf16 v[172:175], a[68:71], v[168:171], v[172:175]\n"
      "s_cmp_eq_u32 s36, s37\n"
      "s_cselect_b32 s48, 49152, s51\n"
      "s_add_u32 s36, s36, s48\n"
      "v_mfma_f32_16x16x32_bf16 v[176:179], a[72:75], v[100:103], v[176:179]\n"
      "s_cbranch_vccnz .Lsp_w1_13\n"
      ".Lbk_w1_13:\n"
      "s_min_u32 s36, s36, s6\n"
      "v_mfma_f32_16x16x32_bf16 v[176:179], a[76:79], v[104:107], v[176:179]\n"
      "buffer_load_dword v28, v11, s[8:11], s36 offen sc1\n"
      "v_mfma_f32_16x16x32_bf16 v[176:179], a[80:83], v[108:111], v[176:179]\n"
      "v_and_b32 v183, 0xffff0000, v197\n"
      "v_mul_f32 v219, v172, v172\n"
      "v_mfma_f32_16x16x32_bf16 v[176:179], a[84:87], v[112:115], v[176:179]\n"
      "buffer_load_dwordx4 v0, s[8:11], s46 offen offset:0 sc1 lds\n"
      "v_mfma_f32_16x16x32_bf16 v[176:179], a[88:91], v[116:119], v[176:179]\n"
      "v_mfma_f32_16x16x32_bf16 v[176:179], a[92:95], v[120:123], v[176:179]\n"
      "v_fmac_f32 v219, v173, v173\n"
      "v_mfma_f32_16x16x32_bf16 v[176:179], a[96:99], v[124:127], v[176:179]\n"
      "v_lshlrev_b32 v184, 16, v198\n"
      "v_and_b32 v185, 0xffff0000, v198\n"
      "v_mfma_f32_16x16x32_bf16 v[176:179], a[100:103], v[128:131], v[176:179]\n"
      "ds_read_b32 v22, v24\n"
      "v_fmac_f32 v219, v174, v174\n"
      "v_mfma_f32_16x16x32_bf16 v[176:179], a[104:107], v[132:135], v[176:179]\n"
      "buffer_load_dwordx4 v0, s[8:11], s46 offen offset:1024 sc1 lds\n"
      "v_mfma_f32_16x16x32_bf16 v[176:179], a[108:111], v[136:139], v[176:179]\n"
      "v_lshlrev_b32 v182, 16, v197\n"
      "v_add_f32 v218, v172, v173\n"
      "v_mfma_f32_16x16x32_bf16 v[176:179], a[112:115], v[140:143], v[176:179]\n"
      "v_add_f32 v218, v218, v174\n"
      "v_fmac_f32 v219, v175, v175\n"
      "v_mfma_f32_16x16x32_bf16 v[176:179], a[116:119], v[144:147], v[176:179]\n"
      "v_cvt_pk_bf16_f32 v208, v172, v173\n"
      "v_mfma_f32_16x16x32_bf16 v[176:179], a[120:123], v[148:151], v[176:179]\n"
      "s_waitcnt lgkmcnt(0)\n"
      "v_and_b32 v187, 0xffff0000, v199\n"
      "v_cmp_gt_i32 vcc, v21, v22\n"
      "v_mfma_f32_16x16x32_bf16 v[176:179], a[124:127], v[152:155], v[176:179]\n"
      "buffer_load_dwordx4 v0, s[8:11], s46 offen offset:3072 sc1 lds\n"
      "v_mfma_f32_16x16x32_bf16 v[176:179], a[128:131], v[156:159], v[176:179]\n"
      "v_lshlrev_b32 v180, 16, v196\n"
      "v_add_f32 v218, v218, v175\n"
      "v_mfma_f32_16x16x32_bf16 v[176:179], a[132:135], v[160:163], v[176:179]\n"
      "s_cbranch_vccnz .Lsp_wz1_14\n"
      ".Lbk_wz1_14:\n"
      "ds_read2st64_b32 v[204:205], v17 offset1:1\n"
      "v_mfma_f32_16x16x32_bf16 v[176:179], a[136:139], v[164:167], v[176:179]\n"
      "ds_read2st64_b32 v[206:207], v17 offset0:2 offset1:3\n"
      "v_add_u32 v21, 1, v21\n"
      "v_mfma_f32_16x16x32_bf16 v[176:179], a[140:143], v[168:171], v[176:179]\n"
      "s_waitcnt vmcnt(10)\n"
      "ds_write_b32 v23, v21\n"
      "v_mfma_f32_16x16x32_bf16 v[180:183], a[144:147], v[100:103], v[180:183]\n"
      "buffer_load_dwordx4 v0, s[8:11], s46 offen offset:2048 sc1 lds\n"
      "v_mfma_f32_16x16x32_bf16 v[180:183], a[148:151], v[104:107], v[180:183]\n"
      "v_mfma_f32_16x16x32_bf16 v[180:183], a[152:155], v[108:111], v[180:183]\n"
      "s_add_u32 m0, s40, 20480\n"
      "v_add_f32 v218, v218, v176\n"
      "v_mfma_f32_16x16x32_bf16 v[180:183], a[156:159], v[112:115], v[180:183]\n"
      "v_fmac_f32 v219, v176, v176\n"
      "v_cvt_pk_bf16_f32 v209, v174, v175\n"
      "v_mfma_f32_16x16x32_bf16 v[180:183], a[160:163], v[116:119], v[180:183]\n"
      "buffer_load_dwordx4 v[196:199], v13, s[8:11], s47 offen offset:1024 sc1\n"
      "v_mfma_f32_16x16x32_bf16 v[180:183], a[164:167], v[120:123], v[180:183]\n"
      "s_waitcnt lgkmcnt(1)\n"
      "v_cvt_pk_bf16_f32 v211, v178, v179\n"
      "v_add_f32 v190, v190, v206\n"
      "v_mfma_f32_16x16x32_bf16 v[180:183], a[168:171], v[124:127], v[180:183]\n"
      "v_add_f32 v218, v218, v177\n"
      "v_add_f32 v218, v218, v178\n"
      "v_mfma_f32_16x16x32_bf16 v[180:183], a[172:175], v[128:131], v[180:183]\n"
      "v_add_f32 v218, v218, v179\n"
      "v_add_f32 v189, v189, v205\n"
      "v_mfma_f32_16x16x32_bf16 v[180:183], a[176:179], v[132:135], v[180:183]\n"
      "buffer_load_dwordx4 v1, s[8:11], s46 offen offset:0 sc1 lds\n"
      "v_mfma_f32_16x16x32_bf16 v[180:183], a[180:183], v[136:139], v[180:183]\n"
      "v_add_f32 v188, v188, v204\n"
      "v_fmac_f32 v219, v177, v177\n"
      "v_mfma_f32_16x16x32_bf16 v[180:183], a[184:187], v[140:143], v[180:183]\n"
      "ds_read_b32 v22, v24\n"
      "v_fmac_f32 v219, v178, v178\n"
      "v_mfma_f32_16x16x32_bf16 v[180:183], a[188:191], v[144:147], v[180:183]\n"
      "v_cvt_pk_bf16_f32 v210, v176, v177\n"
      "v_fmac_f32 v219, v179, v179\n"
      "v_mfma_f32_16x16x32_bf16 v[180:183], a[192:195], v[148:151], v[180:183]\n"
      "v_add_f32 v191, v191, v207\n"
      "v_fmac_f32 v219, v188, v188\n"
      "v_mfma_f32_16x16x32_bf16 v[180:183], a[196:199], v[152:155], v[180:183]\n"
      "v_add_f32 v218, v218, v188\n"
      "v_mfma_f32_16x16x32_bf16 v[180:183], a[200:203], v[156:159], v[180:183]\n"
      "v_add_f32 v218, v218, v189\n"
      "v_add_f32 v218, v218, v190\n"
      "v_mfma_f32_16x16x32_bf16 v[180:183], a[204:207], v[160:163], v[180:183]\n"
      "s_waitcnt lgkmcnt(0)\n"
      "v_fmac_f32 v219, v189, v189\n"
      "v_cmp_gt_i32 vcc, v21, v22\n"
      "v_mfma_f32_16x16x32_bf16 v[180:183], a[208:211], v[164:167], v[180:183]\n"
      "v_cvt_pk_bf16_f32 v216, v188, v189\n"
      "v_fmac_f32 v219, v190, v190\n"
      "v_mfma_f32_16x16x32_bf16 v[180:183], a[212:215], v[168:171], v[180:183]\n"
      "s_waitcnt vmcnt(11)\n"
      "v_and_b32 v189, 0xffff0000, v200\n"
      "v_fmac_f32 v219, v191, v191\n"
      "v_mfma_f32_16x16x32_bf16 v[184:187], a[216:219], v[100:103], v[184:187]\n"
      "s_add_u32 s38, s38, 24576\n"
      "v_cvt_pk_bf16_f32 v217, v190, v191\n"
      "v_lshlrev_b32_e64 v188, 16, v200\n"
      "v_mfma_f32_16x16x32_bf16 v[184:187], a[220:223], v[104:107], v[184:187]\n"
      "s_cbranch_vccnz .Lsp_wy0_15\n"
      ".Lbk_wy0_15:\n"
      "v_mfma_f32_16x16x32_bf16 v[184:187], a[224:227], v[108:111], v[184:187]\n"
      "ds_read_b128 v[104:107], v0 offset:1024\n"
      "v_add_f32 v218, v218, v191\n"
      "v_mfma_f32_16x16x32_bf16 v[184:187], a[228:231], v[112:115], v[184:187]\n"
      "v_fmac_f32 v219, v180, v180\n"
      "v_lshlrev_b32 v190, 16, v201\n"
      "v_mfma_f32_16x16x32_bf16 v[184:187], a[232:235], v[116:119], v[184:187]\n"
      "ds_read_b128 v[100:103], v0 offset:0\n"
      "v_add_f32 v218, v218, v180\n"
      "v_mfma_f32_16x16x32_bf16 v[184:187], a[236:239], v[120:123], v[184:187]\n"
      "s_waitcnt vmcnt(9)\n"
      "ds_read_b128 v[120:123], v0 offset:5120\n"
      "v_lshlrev_b32 v178, 16, v195\n"
      "v_mfma_f32_16x16x32_bf16 v[184:187], a[240:243], v[124:127], v[184:187]\n"
      "ds_read_b128 v[112:115], v0 offset:3072\n"
      "v_fmac_f32 v219, v181, v181\n"
      "v_mfma_f32_16x16x32_bf16 v[184:187], a[244:247], v[128:131], v[184:187]\n"
      "ds_read_b128 v[116:119], v0 offset:4096\n"
      "v_lshlrev_b32 v174, 16, v193\n"
      "v_mfma_f32_16x16x32_bf16 v[184:187], a[248:251], v[132:135], v[184:187]\n"
      "ds_read_b128 v[124:127], v0 offset:6144\n"
      "v_fmac_f32 v219, v182, v182\n"
      "v_mfma_f32_16x16x32_bf16 v[184:187], a[252:255], v[136:139], v[184:187]\n"
      "s_sub_u32 s47, s46, s45\n"
      "ds_read_b128 v[108:111], v0 offset:2048\n"
      "v_mfma_f32_16x16x32_bf16 v[184:187], v[32:35], v[140:143], v[184:187]\n"
      "v_and_b32 v191, 0xffff0000, v201\n"
      "v_and_b32 v177, 0xffff0000, v194\n"
      "v_mfma_f32_16x16x32_bf16 v[184:187], v[36:39], v[144:147], v[184:187]\n"
      "ds_read_b128 v[136:139], v0 offset:8192\n"
      "v_lshlrev_b32 v176, 16, v194\n"
      "v_mfma_f32_16x16x32_bf16 v[184:187], v[40:43], v[148:151], v[184:187]\n"
      "ds_read_b128 v[128:131], v0 offset:7168\n"
      "v_lshlrev_b32 v172, 16, v192\n"
      "v_mfma_f32_16x16x32_bf16 v[184:187], v[44:47], v[152:155], v[184:187]\n"
      "v_fmac_f32 v219, v183, v183\n"
      "v_and_b32 v175, 0xffff0000, v193\n"
      "v_mfma_f32_16x16x32_bf16 v[184:187], v[48:51], v[156:159], v[184:187]\n"
      "buffer_load_dwordx2 v[200:201], v14, s[8:11], s47 offen sc1\n"
      "v_mfma_f32_16x16x32_bf16 v[184:187], v[52:55], v[160:163], v[184:187]\n"
      "ds_read2_b32 v[132:133], v2 offset0:0 offset1:32\n"
      "v_and_b32 v173, 0xffff0000, v192\n"
      "v_mfma_f32_16x16x32_bf16 v[184:187], v[56:59], v[164:167], v[184:187]\n"
      "s_waitcnt lgkmcnt(3)\n"
      "ds_read2_b32 v[134:135], v2 offset0:64 offset1:96\n"
      "v_add_f32 v218, v218, v181\n"
      "v_mfma_f32_16x16x32_bf16 v[184:187], v[60:63], v[168:171], v[184:187]\n"
      "s_sub_u32 s20, s20, 1\n"
      "s_cbranch_scc0 .Lw_loop\n"
      ".Lw_tail:\n"
      "v_add_f32 v218, v218, v182\n"
      "s_nop 7\n"
      "v_fmac_f32 v219, v184, v184\n"
      "v_cvt_pk_bf16_f32 v213, v182, v183\n"
      "v_cvt_pk_bf16_f32 v215, v186, v187\n"
      "v_add_f32 v218, v218, v183\n"
      "buffer_store_dwordx4 v[208:211], v15, s[8:11], s38 offen sc1\n"
      "v_cvt_pk_bf16_f32 v212, v180, v181\n"
      "v_add_f32 v218, v218, v184\n"
      "s_add_u32 s50, s50, 24576\n"
      "v_cvt_pk_bf16_f32 v214, v184, v185\n"
      "v_add_f32 v218, v218, v185\n"
      "v_add_f32 v218, v218, v186\n"
      "v_add_f32 v218, v218, v187\n"
      "v_fmac_f32 v219, v185, v185\n"
      "v_fmac_f32 v219, v186, v186\n"
      "buffer_store_dwordx2 v[216:217], v16, s[8:11], s38 offen sc1\n"
      "buffer_store_dwordx4 v[212:215], v15, s[8:11], s38 offen offset:1024 sc1\n"
      "ds_write_b32 v18, v218\n"
      "v_fmac_f32 v219, v187, v187\n"
      "ds_write_b32 v18, v219 offset:4\n"
      "s_waitcnt lgkmcnt(0)\n"
      "s_barrier\n"
      "s_waitcnt vmcnt(12)\n"
      "buffer_store_dword v25, v12, s[8:11], s50 offen sc1\n"
      "s_add_u32 s38, s38, 24576\n"
      "s_add_u32 s50, s50, 24576\n"
      "s_waitcnt vmcnt(1)\n"
      "buffer_store_dword v25, v12, s[8:11], s50 offen sc1\n"
      "s_add_u32 s50, s50, 24576\n"
      "s_endpgm\n"
      ".Lv_start:\n"
      "s_mov_b64 exec, 0\n"
      "s_lshr_b32 s22, s39, 13\n"
      ".Lv_pl:\n"
      "s_cmp_eq_u32 s22, 0\n"
      "s_cbranch_scc1 .Lv_p0\n"
      "s_cmp_eq_u32 s22, 1\n"
      "s_cbranch_scc1 .Lv_p1\n"
      "s_branch .Lv_pe\n"
      ".Lv_pr:\n"
      "s_add_u32 s22, s22, 2\n"
      "s_branch .Lv_pl\n"
      ".Lv_pe:\n"
      "s_mov_b64 exec, -1\n"
      "s_add_u32 s50, s38, 4294893568\n"
      "s_add_u32 s49, s38, 4294918144\n"
      "s_add_u32 s38, s38, 4294942720\n"
      "s_cmp_eq_u32 s36, s37\n"
      "s_cselect_b32 s48, 49152, s51\n"
      "s_add_u32 s36, s36, s48\n"
      "s_min_u32 s36, s36, s6\n"
      "buffer_load_dword v28, v11, s[8:11], s36 offen sc1\n"
      "s_mov_b32 s46, s36\n"
      "s_add_u32 s36, s36, 24576\n"
      "s_min_u32 s36, s36, s6\n"
      "s_add_u32 m0, s39, 0\n"
      "s_waitcnt vmcnt(0)\n"
      "v_cmp_ne_u32 vcc, s21, v28\n"
      "s_cbranch_vccnz .Lsp_v0_16\n"
      ".Lbk_v0_16:\n"
      "buffer_load_dword v28, v11, s[8:11], s36 offen sc1\n"
      "buffer_load_dwordx4 v0, s[8:11], s46 offen offset:3072 sc1 lds\n"
      "buffer_load_dwordx4 v0, s[8:11], s46 offen offset:0 sc1 lds\n"
      "buffer_load_dwordx4 v0, s[8:11], s46 offen offset:1024 sc1 lds\n"
      "buffer_load_dwordx4 v0, s[8:11], s46 offen offset:2048 sc1 lds\n"
      "s_add_u32 m0, s40, 0\n"
      "s_nop 0\n"
      "buffer_load_dwordx4 v1, s[8:11], s46 offen offset:0 sc1 lds\n"
      "s_sub_u32 s47, s46, s45\n"
      "buffer_load_dwordx4 v[192:195], v13, s[8:11], s47 offen sc1\n"
      "s_mov_b32 s46, s36\n"
      "s_cmp_eq_u32 s36, s37\n"
      "s_cselect_b32 s48, 49152, s51\n"
      "buffer_load_dword v26, v23, s[8:11], s31 offen\n"
      "s_add_u32 s36, s36, s48\n"
      "s_waitcnt vmcnt(7)\n"
      "v_cmp_ne_u32 vcc, s21, v28\n"
      "s_min_u32 s36, s36, s6\n"
      "s_cbranch_vccnz .Lsp_v1_17\n"
      ".Lbk_v1_17:\n"
      "buffer_load_dword v28, v11, s[8:11], s36 offen sc1\n"
      "s_add_u32 m0, s39, 20480\n"
      "s_nop 0\n"
      "buffer_load_dwordx4 v0, s[8:11], s46 offen offset:0 sc1 lds\n"
      "buffer_load_dwordx4 v0, s[8:11], s46 offen offset:1024 sc1 lds\n"
      "buffer_load_dwordx4 v0, s[8:11], s46 offen offset:2048 sc1 lds\n"
      "buffer_load_dwordx4 v[196:199], v13, s[8:11], s47 offen offset:1024 sc1\n"
      "v_add_u32 v21, 1, v21\n"
      "s_waitcnt vmcnt(7)\n"
      "ds_write_b32 v23, v21\n"
      "buffer_load_dwordx4 v0, s[8:11], s46 offen offset:3072 sc1 lds\n"
      "s_add_u32 m0, s40, 20480\n"
      "s_nop 0\n"
      "buffer_load_dwordx4 v1, s[8:11], s46 offen offset:0 sc1 lds\n"
      "ds_read_b32 v22, v24\n"
      "buffer_load_dword v26, v23, s[8:11], s31 offen\n"
      "s_waitcnt lgkmcnt(0)\n"
      "v_cmp_gt_i32 vcc, v21, v22\n"
      "buffer_load_dword v26, v23, s[8:11], s31 offen\n"
      "s_cbranch_vccnz .Lsp_vy0_18\n"
      ".Lbk_vy0_18:\n"
      "s_sub_u32 s47, s46, s45\n"
      "s_waitcnt vmcnt(10)\n"
      "v_lshlrev_b32 v174, 16, v193\n"
      "v_and_b32 v179, 0xffff0000, v195\n"
      "ds_read_b128 v[100:103], v0 offset:8192\n"
      "ds_read_b128 v[108:111], v0 offset:10240\n"
      "v_and_b32 v173, 0xffff0000, v192\n"
      "ds_read_b128 v[120:123], v0 offset:13312\n"
      "ds_read_b128 v[128:131], v0 offset:15360\n"
      "v_lshlrev_b32 v178, 16, v195\n"
      "ds_read_b128 v[104:107], v0 offset:9216\n"
      "v_lshlrev_b32 v172, 16, v192\n"
      "buffer_load_dword v26, v23, s[8:11], s31 offen\n"
      "s_mov_b32 s46, s36\n"
      "ds_read_b128 v[112:115], v0 offset:11264\n"
      "ds_read2_b32 v[134:135], v2 offset0:64 offset1:96\n"
      "ds_read2_b32 v[132:133], v2 offset0:0 offset1:32\n"
      "s_add_u32 s36, s36, 24576\n"
      "ds_read_b128 v[116:119], v0 offset:12288\n"
      "ds_read_b128 v[124:127], v0 offset:14336\n"
      "ds_read_b128 v[160:163], v0 offset:6144\n"
      "v_and_b32 v177, 0xffff0000, v194\n"
      "s_mov_b32 s20, 15\n"
      "s_waitcnt lgkmcnt(0)\n"
      "s_nop 4\n"
      "s_branch .Lv_loop\n"
      ".p2align 12\n"
      ".Lv_p0:\n"
      "s_branch .Lv_pr\n"
      ".p2align 5\n"
      ".Lv_loop:\n"
      "v_mfma_f32_16x16x32_bf16 v[188:191], v[64:67], v[100:103], 0\n"
      "ds_read_b128 v[140:143], v0 offset:1024\n"
      "v_and_b32 v175, 0xffff0000, v193\n"
      "v_mfma_f32_16x16x32_bf16 v[188:191], v[68:71], v[104:107], v[188:191]\n"
      "ds_read2_b32 v[168:169], v3 offset0:0 offset1:32\n"
      "v_lshlrev_b32 v176, 16, v194\n"
      "v_mfma_f32_16x16x32_bf16 v[188:191], v[72:75], v[108:111], v[188:191]\n"
      "s_waitcnt lgkmcnt(5)\n"
      "ds_read_b128 v[164:167], v0 offset:7168\n"
      "v_add_f32 v218, v218, v184\n"
      "v_mfma_f32_16x16x32_bf16 v[188:191], v[76:79], v[112:115], v[188:191]\n"
      "s_waitcnt lgkmcnt(5)\n"
      "ds_read_b128 v[144:147], v0 offset:2048\n"
      "v_add_f32 v218, v218, v185\n"
      "v_mfma_f32_16x16x32_bf16 v[188:191], v[80:83], v[116:119], v[188:191]\n"
      "buffer_load_dwordx4 v[192:195], v13, s[8:11], s47 offen sc1\n"
      "v_mfma_f32_16x16x32_bf16 v[188:191], v[84:87], v[120:123], v[188:191]\n"
      "s_waitcnt lgkmcnt(4)\n"
      "ds_read2_b32 v[170:171], v3 offset0:64 offset1:96\n"
      "v_fmac_f32 v219, v183, v183\n"
      "v_mfma_f32_16x16x32_bf16 v[188:191], v[88:91], v[124:127], v[188:191]\n"
      "s_min_u32 s36, s36, s6\n"
      "ds_read_b128 v[136:139], v0 offset:0\n"
      "v_fmac_f32 v219, v184, v184\n"
      "v_mfma_f32_16x16x32_bf16 v[188:191], v[92:95], v[128:131], v[188:191]\n"
      "ds_read_b128 v[148:151], v0 offset:3072\n"
      "v_mfma_f32_16x16x32_bf16 v[188:191], v[96:99], v[132:135], v[188:191]\n"
      "v_cvt_pk_bf16_f32 v215, v186, v187\n"
      "v_add_f32 v218, v218, v186\n"
      "v_mfma_f32_16x16x32_bf16 v[172:175], a[0:3], v[100:103], v[172:175]\n"
      "ds_read_b128 v[152:155], v0 offset:4096\n"
      "v_add_f32 v218, v218, v187\n"
      "v_mfma_f32_16x16x32_bf16 v[172:175], a[4:7], v[104:107], v[172:175]\n"
      "ds_read_b128 v[156:159], v0 offset:5120\n"
      "v_fmac_f32 v219, v185, v185\n"
      "v_mfma_f32_16x16x32_bf16 v[172:175], a[8:11], v[108:111], v[172:175]\n"
      "s_add_u32 m0, s39, 40960\n"
      "v_cvt_pk_bf16_f32 v214, v184, v185\n"
      "v_mfma_f32_16x16x32_bf16 v[172:175], a[12:15], v[112:115], v[172:175]\n"
      "ds_write_b32 v18, v218\n"
      "v_mfma_f32_16x16x32_bf16 v[172:175], a[16:19], v[116:119], v[172:175]\n"
      "s_waitcnt vmcnt(10) lgkmcnt(4)\n"
      "v_fmac_f32_e64 v219, v186, v186\n"
      "v_cmp_ne_u32 vcc, s21, v28\n"
      "v_mfma_f32_16x16x32_bf16 v[172:175], a[20:23], v[120:123], v[172:175]\n"
      "ds_write_b32 v17, v190 offset:512\n"
      "v_mfma_f32_16x16x32_bf16 v[172:175], a[24:27], v[124:127], v[172:175]\n"
      "buffer_store_dwordx4 v[212:215], v15, s[8:11], s38 offen offset:1024 sc1\n"
      "v_mfma_f32_16x16x32_bf16 v[172:175], a[28:31], v[128:131], v[172:175]\n"
      "ds_write_b32 v17, v188 offset:0\n"
      "v_mfma_f32_16x16x32_bf16 v[172:175], a[32:35], v[132:135], v[172:175]\n"
      "v_fmac_f32_e64 v219, v187, v187\n"
      "v_mfma_f32_16x16x32_bf16 v[172:175], a[36:39], v[136:139], v[172:175]\n"
      "ds_write_b32 v17, v191 offset:768\n"
      "v_mfma_f32_16x16x32_bf16 v[172:175], a[40:43], v[140:143], v[172:175]\n"
      "s_cbranch_vccnz .Lsp_v0_19\n"
      ".Lbk_v0_19:\n"
      "v_add_u32 v21, 1, v21\n"
      "v_mfma_f32_16x16x32_bf16 v[172:175], a[44:47], v[144:147], v[172:175]\n"
      "s_waitcnt lgkmcnt(3)\n"
      "ds_write_b32 v18, v219 offset:4\n"
      "v_mfma_f32_16x16x32_bf16 v[172:175], a[48:51], v[148:151], v[172:175]\n"
      "buffer_load_dword v28, v11, s[8:11], s36 offen sc1\n"
      "v_mfma_f32_16x16x32_bf16 v[172:175], a[52:55], v[152:155], v[172:175]\n"
      "ds_write_b32 v17, v189 offset:256\n"
      "v_mfma_f32_16x16x32_bf16 v[172:175], a[56:59], v[156:159], v[172:175]\n"
      "s_waitcnt vmcnt(8) lgkmcnt(3)\n"
      "v_lshlrev_b32 v186, 16, v199\n"
      "v_and_b32 v185, 0xffff0000, v198\n"
      "v_mfma_f32_16x16x32_bf16 v[172:175], a[60:63], v[160:163], v[172:175]\n"
      "ds_write_b32 v23, v21\n"
      "v_mfma_f32_16x16x32_bf16 v[172:175], a[64:67], v[164:167], v[172:175]\n"
      "v_and_b32 v181, 0xffff0000, v196\n"
      "v_and_b32 v187, 0xffff0000, v199\n"
      "v_mfma_f32_16x16x32_bf16 v[172:175], a[68:71], v[168:171], v[172:175]\n"
      "buffer_load_dwordx4 v0, s[8:11], s46 offen offset:3072 sc1 lds\n"
      "v_mfma_f32_16x16x32_bf16 v[176:179], a[72:75], v[100:103], v[176:179]\n"
      "v_lshlrev_b32 v182, 16, v197\n"
      "s_add_u32 s38, s38, 24576\n"
      "v_mfma_f32_16x16x32_bf16 v[176:179], a[76:79], v[104:107], v[176:179]\n"
      "v_lshlrev_b32 v184, 16, v198\n"
      "v_mfma_f32_16x16x32_bf16 v[176:179], a[80:83], v[108:111], v[176:179]\n"
      "v_cvt_pk_bf16_f32 v208, v172, v173\n"
      "v_mfma_f32_16x16x32_bf16 v[176:179], a[84:87], v[112:115], v[176:179]\n"
      "v_mul_f32 v219, v172, v172\n"
      "v_mfma_f32_16x16x32_bf16 v[176:179], a[88:91], v[116:119], v[176:179]\n"
      "v_lshlrev_b32 v180, 16, v196\n"
      "v_and_b32 v183, 0xffff0000, v197\n"
      "v_mfma_f32_16x16x32_bf16 v[176:179], a[92:95], v[120:123], v[176:179]\n"
      "v_fmac_f32 v219, v173, v173\n"
      "v_mfma_f32_16x16x32_bf16 v[176:179], a[96:99], v[124:127], v[176:179]\n"
      "buffer_load_dwordx4 v[196:199], v13, s[8:11], s47 offen offset:1024 sc1\n"
      "v_mfma_f32_16x16x32_bf16 v[176:179], a[100:103], v[128:131], v[176:179]\n"
      "ds_read_b32 v22, v24\n"
      "v_mfma_f32_16x16x32_bf16 v[176:179], a[104:107], v[132:135], v[176:179]\n"
      "v_fmac_f32 v219, v174, v174\n"
      "v_mfma_f32_16x16x32_bf16 v[176:179], a[108:111], v[136:139], v[176:179]\n"
      "v_fmac_f32 v219, v175, v175\n"
      "v_mfma_f32_16x16x32_bf16 v[176:179], a[112:115], v[140:143], v[176:179]\n"
      "buffer_load_dwordx4 v0, s[8:11], s46 offen offset:0 sc1 lds\n"
      "v_mfma_f32_16x16x32_bf16 v[176:179], a[116:119], v[144:147], v[176:179]\n"
      "v_mfma_f32_16x16x32_bf16 v[176:179], a[120:123], v[148:151], v[176:179]\n"
      "s_waitcnt lgkmcnt(0)\n"
      "v_cmp_gt_i32 vcc, v21, v22\n"
      "v_add_f32_e64 v218, v172, v173\n"
      "v_mfma_f32_16x16x32_bf16 v[176:179], a[124:127], v[152:155], v[176:179]\n"
      "v_add_f32_e64 v218, v218, v174\n"
      "v_add_f32_e64 v218, v218, v175\n"
      "v_mfma_f32_16x16x32_bf16 v[176:179], a[128:131], v[156:159], v[176:179]\n"
      "buffer_load_dwordx4 v0, s[8:11], s46 offen offset:1024 sc1 lds\n"
      "v_mfma_f32_16x16x32_bf16 v[176:179], a[132:135], v[160:163], v[176:179]\n"
      "s_cbranch_vccnz .Lsp_vz0_20\n"
      ".Lbk_vz0_20:\n"
      "ds_read_b128 v[204:207], v19\n"
      "v_mfma_f32_16x16x32_bf16 v[176:179], a[136:139], v[164:167], v[176:179]\n"
      "v_cvt_pk_bf16_f32 v209, v174, v175\n"
      "v_add_u32 v21, 1, v21\n"
      "v_mfma_f32_16x16x32_bf16 v[176:179], a[140:143], v[168:171], v[176:179]\n"
      "s_waitcnt vmcnt(10)\n"
      "ds_write_b32 v23, v21\n"
      "v_mfma_f32_16x16x32_bf16 v[180:183], a[144:147], v[100:103], v[180:183]\n"
      "v_mfma_f32_16x16x32_bf16 v[180:183], a[148:151], v[104:107], v[180:183]\n"
      "buffer_load_dwordx4 v0, s[8:11], s46 offen offset:2048 sc1 lds\n"
      "v_mfma_f32_16x16x32_bf16 v[180:183], a[152:155], v[108:111], v[180:183]\n"
      "s_add_u32 s49, s49, 24576\n"
      "s_add_u32 m0, s40, 40960\n"
      "v_mfma_f32_16x16x32_bf16 v[180:183], a[156:159], v[112:115], v[180:183]\n"
      "v_mfma_f32_16x16x32_bf16 v[180:183], a[160:163], v[116:119], v[180:183]\n"
      "s_waitcnt lgkmcnt(1)\n"
      "v_add_f32 v204, v204, v206\n"
      "v_cvt_pk_bf16_f32 v210, v176, v177\n"
      "v_mfma_f32_16x16x32_bf16 v[180:183], a[164:167], v[120:123], v[180:183]\n"
      "buffer_load_dwordx4 v1, s[8:11], s46 offen offset:0 sc1 lds\n"
      "v_mfma_f32_16x16x32_bf16 v[180:183], a[168:171], v[124:127], v[180:183]\n"
      "v_add_f32 v205, v205, v207\n"
      "v_add_f32_dpp v204, v204, v204 quad_perm:[1,0,3,2] row_mask:0xf bank_mask:0xf\n"
      "v_mfma_f32_16x16x32_bf16 v[180:183], a[172:175], v[128:131], v[180:183]\n"
      "v_fmac_f32 v219, v176, v176\n"
      "v_mfma_f32_16x16x32_bf16 v[180:183], a[176:179], v[132:135], v[180:183]\n"
      "v_add_f32_dpp v204, v204, v204 quad_perm:[2,3,0,1] row_mask:0xf bank_mask:0xf\n"
      "v_cvt_pk_bf16_f32 v211, v178, v179\n"
      "v_mfma_f32_16x16x32_bf16 v[180:183], a[180:183], v[136:139], v[180:183]\n"
      "buffer_store_dwordx4 v[208:211], v15, s[8:11], s38 offen sc1\n"
      "v_mfma_f32_16x16x32_bf16 v[180:183], a[184:187], v[140:143], v[180:183]\n"
      "ds_read_b32 v22, v24\n"
      "v_add_f32_dpp v205, v205, v205 quad_perm:[1,0,3,2] row_mask:0xf bank_mask:0xf\n"
      "v_mfma_f32_16x16x32_bf16 v[180:183], a[188:191], v[144:147], v[180:183]\n"
      "s_add_u32 s50, s50, 24576\n"
      "v_add_f32_dpp v205, v205, v205 quad_perm:[2,3,0,1] row_mask:0xf bank_mask:0xf\n"
      "v_fmac_f32 v219, v177, v177\n"
      "v_mfma_f32_16x16x32_bf16 v[180:183], a[192:195], v[148:151], v[180:183]\n"
      "v_add_f32_dpp v205, v205, v205 row_half_mirror row_mask:0xf bank_mask:0xf\n"
      "v_fmac_f32 v219, v178, v178\n"
      "v_mfma_f32_16x16x32_bf16 v[180:183], a[196:199], v[152:155], v[180:183]\n"
      "v_add_f32_dpp v204, v204, v204 row_half_mirror row_mask:0xf bank_mask:0xf\n"
      "v_mfma_f32_16x16x32_bf16 v[180:183], a[200:203], v[156:159], v[180:183]\n"
      "v_mul_f32 v204, v27, v204\n"
      "v_fmaak_f32 v205, v27, v205, 0x36a7c5ac\n"
      "v_mfma_f32_16x16x32_bf16 v[180:183], a[204:207], v[160:163], v[180:183]\n"
      "s_waitcnt vmcnt(12) lgkmcnt(1)\n"
      "buffer_store_dword v25, v12, s[8:11], s50 offen sc1\n"
      "v_mfma_f32_16x16x32_bf16 v[180:183], a[208:211], v[164:167], v[180:183]\n"
      "s_waitcnt lgkmcnt(0)\n"
      "v_cmp_gt_i32 vcc, v21, v22\n"
      "v_add_f32 v218, v218, v176\n"
      "v_mfma_f32_16x16x32_bf16 v[180:183], a[212:215], v[168:171], v[180:183]\n"
      "v_add_f32 v218, v218, v177\n"
      "v_add_f32_e64 v218, v218, v178\n"
      "v_mfma_f32_16x16x32_bf16 v[184:187], a[216:219], v[100:103], v[184:187]\n"
      "buffer_store_dwordx2 v[204:205], v20, s[8:11], s49 offen sc1\n"
      "v_mfma_f32_16x16x32_bf16 v[184:187], a[220:223], v[104:107], v[184:187]\n"
      "s_sub_u32 s47, s46, s45\n"
      "s_cbranch_vccnz .Lsp_vy1_21\n"
      ".Lbk_vy1_21:\n"
      "v_mfma_f32_16x16x32_bf16 v[184:187], a[224:227], v[108:111], v[184:187]\n"
      "s_waitcnt vmcnt(11)\n"
      "ds_read_b128 v[104:107], v0 offset:29696\n"
      "v_and_b32 v177, 0xffff0000, v194\n"
      "v_mfma_f32_16x16x32_bf16 v[184:187], a[228:231], v[112:115], v[184:187]\n"
      "v_fmac_f32 v219, v179, v179\n"
      "v_mfma_f32_16x16x32_bf16 v[184:187], a[232:235], v[116:119], v[184:187]\n"
      "v_fmac_f32 v219, v180, v180\n"
      "v_add_f32 v218, v218, v179\n"
      "v_mfma_f32_16x16x32_bf16 v[184:187], a[236:239], v[120:123], v[184:187]\n"
      "v_cvt_pk_bf16_f32 v212, v180, v181\n"
      "v_and_b32 v179, 0xffff0000, v195\n"
      "v_mfma_f32_16x16x32_bf16 v[184:187], a[240:243], v[124:127], v[184:187]\n"
      "ds_read_b128 v[120:123], v0 offset:33792\n"
      "v_lshlrev_b32 v174, 16, v193\n"
      "v_mfma_f32_16x16x32_bf16 v[184:187], a[244:247], v[128:131], v[184:187]\n"
      "ds_read_b128 v[112:115], v0 offset:31744\n"
      "v_add_f32 v218, v218, v180\n"
      "v_mfma_f32_16x16x32_bf16 v[184:187], a[248:251], v[132:135], v[184:187]\n"
      "ds_read_b128 v[100:103], v0 offset:28672\n"
      "v_add_f32 v218, v218, v181\n"
      "v_mfma_f32_16x16x32_bf16 v[184:187], a[252:255], v[136:139], v[184:187]\n"
      "ds_read_b128 v[124:127], v0 offset:34816\n"
      "v_mfma_f32_16x16x32_bf16 v[184:187], v[32:35], v[140:143], v[184:187]\n"
      "ds_read_b128 v[108:111], v0 offset:30720\n"
      "v_and_b32 v175, 0xffff0000, v193\n"
      "v_mfma_f32_16x16x32_bf16 v[184:187], v[36:39], v[144:147], v[184:187]\n"
      "ds_read_b128 v[116:119], v0 offset:32768\n"
      "v_fmac_f32 v219, v181, v181\n"
      "v_mfma_f32_16x16x32_bf16 v[184:187], v[40:43], v[148:151], v[184:187]\n"
      "v_add_f32 v218, v218, v182\n"
      "v_mfma_f32_16x16x32_bf16 v[184:187], v[44:47], v[152:155], v[184:187]\n"
      "ds_read2_b32 v[132:133], v4 offset0:0 offset1:32\n"
      "v_and_b32 v173, 0xffff0000, v192\n"
      "v_mfma_f32_16x16x32_bf16 v[184:187], v[48:51], v[156:159], v[184:187]\n"
      "ds_read_b128 v[128:131], v0 offset:35840\n"
      "v_lshlrev_b32 v178, 16, v195\n"
      "v_mfma_f32_16x16x32_bf16 v[184:187], v[52:55], v[160:163], v[184:187]\n"
      "ds_read_b128 v[156:159], v0 offset:25600\n"
      "v_lshlrev_b32 v172, 16, v192\n"
      "v_mfma_f32_16x16x32_bf16 v[184:187], v[56:59], v[164:167], v[184:187]\n"
      "ds_read_b128 v[144:147], v0 offset:22528\n"
      "v_lshlrev_b32 v176, 16, v194\n"
      "v_mfma_f32_16x16x32_bf16 v[184:187], v[60:63], v[168:171], v[184:187]\n"
      "s_waitcnt lgkmcnt(5)\n"
      "buffer_load_dwordx4 v[192:195], v13, s[8:11], s47 offen sc1\n"
      "v_mfma_f32_16x16x32_bf16 v[188:191], v[64:67], v[100:103], 0\n"
      "ds_read2_b32 v[134:135], v4 offset0:64 offset1:96\n"
      "v_fmac_f32 v219, v182, v182\n"
      "v_mfma_f32_16x16x32_bf16 v[188:191], v[68:71], v[104:107], v[188:191]\n"
      "ds_read_b128 v[148:151], v0 offset:23552\n"
      "v_add_f32 v218, v218, v183\n"
      "v_mfma_f32_16x16x32_bf16 v[188:191], v[72:75], v[108:111], v[188:191]\n"
      "s_mov_b32 s46, s36\n"
      "v_cvt_pk_bf16_f32 v213, v182, v183\n"
      "v_cvt_pk_bf16_f32 v215, v186, v187\n"
      "v_mfma_f32_16x16x32_bf16 v[188:191], v[76:79], v[112:115], v[188:191]\n"
      "s_waitcnt lgkmcnt(4)\n"
      "ds_read_b128 v[160:163], v0 offset:26624\n"
      "v_fmac_f32 v219, v183, v183\n"
      "v_mfma_f32_16x16x32_bf16 v[188:191], v[80:83], v[116:119], v[188:191]\n"
      "ds_read_b128 v[140:143], v0 offset:21504\n"
      "v_fmac_f32 v219, v184, v184\n"
      "v_mfma_f32_16x16x32_bf16 v[188:191], v[84:87], v[120:123], v[188:191]\n"
      "ds_read2_b32 v[168:169], v5 offset0:0 offset1:32\n"
      "v_add_f32 v218, v218, v184\n"
      "v_mfma_f32_16x16x32_bf16 v[188:191], v[88:91], v[124:127], v[188:191]\n"
      "ds_read_b128 v[152:155], v0 offset:24576\n"
      "v_fmac_f32 v219, v185, v185\n"
      "v_mfma_f32_16x16x32_bf16 v[188:191], v[92:95], v[128:131], v[188:191]\n"
      "s_waitcnt lgkmcnt(5)\n"
      "ds_read_b128 v[136:139], v0 offset:20480\n"
      "v_add_f32 v218, v218, v185\n"
      "v_mfma_f32_16x16x32_bf16 v[188:191], v[96:99], v[132:135], v[188:191]\n"
      "s_cmp_eq_u32 s36, s37\n"
      "s_cselect_b32 s48, 49152, s51\n"
      "ds_read_b128 v[164:167], v0 offset:27648\n"
      "v_mfma_f32_16x16x32_bf16 v[172:175], a[0:3], v[100:103], v[172:175]\n"
      "ds_read2_b32 v[170:171], v5 offset0:64 offset1:96\n"
      "v_cvt_pk_bf16_f32 v214, v184, v185\n"
      "v_mfma_f32_16x16x32_bf16 v[172:175], a[4:7], v[104:107], v[172:175]\n"
      "v_fmac_f32 v219, v186, v186\n"
      "v_add_f32 v218, v218, v186\n"
      "v_mfma_f32_16x16x32_bf16 v[172:175], a[8:11], v[108:111], v[172:175]\n"
      "ds_write_b32 v17, v189 offset:256\n"
      "v_mfma_f32_16x16x32_bf16 v[172:175], a[12:15], v[112:115], v[172:175]\n"
      "v_add_f32 v218, v218, v187\n"
      "v_add_u32 v21, 1, v21\n"
      "v_mfma_f32_16x16x32_bf16 v[172:175], a[16:19], v[116:119], v[172:175]\n"
      "buffer_store_dwordx4 v[212:215], v15, s[8:11], s38 offen offset:1024 sc1\n"
      "v_mfma_f32_16x16x32_bf16 v[172:175], a[20:23], v[120:123], v[172:175]\n"
      "ds_write_b32 v17, v190 offset:512\n"
      "v_mfma_f32_16x16x32_bf16 v[172:175], a[24:27], v[124:127], v[172:175]\n"
      "v_fmac_f32 v219, v187, v187\n"
      "v_mfma_f32_16x16x32_bf16 v[172:175], a[28:31], v[128:131], v[172:175]\n"
      "s_add_u32 s36, s36, s48\n"
      "ds_write_b32 v18, v218\n"
      "v_mfma_f32_16x16x32_bf16 v[172:175], a[32:35], v[132:135], v[172:175]\n"
      "s_waitcnt vmcnt(11) lgkmcnt(3)\n"
      "v_cmp_ne_u32 vcc, s21, v28\n"
      "v_mfma_f32_16x16x32_bf16 v[172:175], a[36:39], v[136:139], v[172:175]\n"
      "ds_write_b32 v17, v188 offset:0\n"
      "v_mfma_f32_16x16x32_bf16 v[172:175], a[40:43], v[140:143], v[172:175]\n"
      "s_waitcnt vmcnt(9) lgkmcnt(3)\n"
      "s_min_u32 s36, s36, s6\n"
      "v_lshlrev_b32 v182, 16, v197\n"
      "v_mfma_f32_16x16x32_bf16 v[172:175], a[44:47], v[144:147], v[172:175]\n"
      "ds_write_b32 v17, v191 offset:768\n"
      "v_mfma_f32_16x16x32_bf16 v[172:175], a[48:51], v[148:151], v[172:175]\n"
      "s_cbranch_vccnz .Lsp_v1_22\n"
      ".Lbk_v1_22:\n"
      "v_and_b32 v181, 0xffff0000, v196\n"
      "v_mfma_f32_16x16x32_bf16 v[172:175], a[52:55], v[152:155], v[172:175]\n"
      "ds_write_b32 v18, v219 offset:4\n"
      "v_mfma_f32_16x16x32_bf16 v[172:175], a[56:59], v[156:159], v[172:175]\n"
      "buffer_load_dword v28, v11, s[8:11], s36 offen sc1\n"
      "v_mfma_f32_16x16x32_bf16 v[172:175], a[60:63], v[160:163], v[172:175]\n"
      "s_add_u32 m0, s39, 61440\n"
      "ds_write_b32 v23, v21\n"
      "v_mfma_f32_16x16x32_bf16 v[172:175], a[64:67], v[164:167], v[172:175]\n"
      "v_and_b32 v187, 0xffff0000, v199\n"
      "v_and_b32 v183, 0xffff0000, v197\n"
      "v_mfma_f32_16x16x32_bf16 v[172:175], a[68:71], v[168:171], v[172:175]\n"
      "buffer_load_dwordx4 v0, s[8:11], s46 offen offset:0 sc1 lds\n"
      "v_mfma_f32_16x16x32_bf16 v[176:179], a[72:75], v[100:103], v[176:179]\n"
      "s_add_u32 s49, s49, 24576\n"
      "v_mfma_f32_16x16x32_bf16 v[176:179], a[76:79], v[104:107], v[176:179]\n"
      "v_mfma_f32_16x16x32_bf16 v[176:179], a[80:83], v[108:111], v[176:179]\n"
      "v_cvt_pk_bf16_f32 v209, v174, v175\n"
      "v_mfma_f32_16x16x32_bf16 v[176:179], a[84:87], v[112:115], v[176:179]\n"
      "buffer_load_dwordx4 v0, s[8:11], s46 offen offset:1024 sc1 lds\n"
      "v_mfma_f32_16x16x32_bf16 v[176:179], a[88:91], v[116:119], v[176:179]\n"
      "v_add_f32 v218, v172, v173\n"
      "v_mfma_f32_16x16x32_bf16 v[176:179], a[92:95], v[120:123], v[176:179]\n"
      "v_add_f32 v218, v218, v174\n"
      "v_lshlrev_b32 v180, 16, v196\n"
      "v_mfma_f32_16x16x32_bf16 v[176:179], a[96:99], v[124:127], v[176:179]\n"
      "ds_read_b32 v22, v24\n"
      "v_lshlrev_b32 v186, 16, v199\n"
      "v_mfma_f32_16x16x32_bf16 v[176:179], a[100:103], v[128:131], v[176:179]\n"
      "buffer_load_dwordx4 v0, s[8:11], s46 offen offset:2048 sc1 lds\n"
      "v_mfma_f32_16x16x32_bf16 v[176:179], a[104:107], v[132:135], v[176:179]\n"
      "v_mul_f32 v219, v172, v172\n"
      "v_fmac_f32 v219, v173, v173\n"
      "v_mfma_f32_16x16x32_bf16 v[176:179], a[108:111], v[136:139], v[176:179]\n"
      "v_lshlrev_b32 v184, 16, v198\n"
      "v_mfma_f32_16x16x32_bf16 v[176:179], a[112:115], v[140:143], v[176:179]\n"
      "v_fmac_f32_e64 v219, v174, v174\n"
      "v_mfma_f32_16x16x32_bf16 v[176:179], a[116:119], v[144:147], v[176:179]\n"
      "s_waitcnt lgkmcnt(0)\n"
      "v_cmp_gt_i32 vcc, v21, v22\n"
      "v_add_f32_e64 v218, v218, v175\n"
      "v_mfma_f32_16x16x32_bf16 v[176:179], a[120:123], v[148:151], v[176:179]\n"
      "v_mfma_f32_16x16x32_bf16 v[176:179], a[124:127], v[152:155], v[176:179]\n"
      "v_and_b32 v185, 0xffff0000, v198\n"
      "v_cvt_pk_bf16_f32 v208, v172, v173\n"
      "v_mfma_f32_16x16x32_bf16 v[176:179], a[128:131], v[156:159], v[176:179]\n"
      "buffer_load_dwordx4 v[196:199], v13, s[8:11], s47 offen offset:1024 sc1\n"
      "v_mfma_f32_16x16x32_bf16 v[176:179], a[132:135], v[160:163], v[176:179]\n"
      "s_cbranch_vccnz .Lsp_vz1_23\n"
      ".Lbk_vz1_23:\n"
      "ds_read_b128 v[204:207], v19\n"
      "v_mfma_f32_16x16x32_bf16 v[176:179], a[136:139], v[164:167], v[176:179]\n"
      "v_add_u32 v21, 1, v21\n"
      "v_mfma_f32_16x16x32_bf16 v[176:179], a[140:143], v[168:171], v[176:179]\n"
      "s_waitcnt vmcnt(10)\n"
      "ds_write_b32 v23, v21\n"
      "v_mfma_f32_16x16x32_bf16 v[180:183], a[144:147], v[100:103], v[180:183]\n"
      "v_fmac_f32 v219, v175, v175\n"
      "v_mfma_f32_16x16x32_bf16 v[180:183], a[148:151], v[104:107], v[180:183]\n"
      "buffer_load_dwordx4 v0, s[8:11], s46 offen offset:3072 sc1 lds\n"
      "v_mfma_f32_16x16x32_bf16 v[180:183], a[152:155], v[108:111], v[180:183]\n"
      "v_cvt_pk_bf16_f32 v210, v176, v177\n"
      "v_fmac_f32 v219, v176, v176\n"
      "v_mfma_f32_16x16x32_bf16 v[180:183], a[156:159], v[112:115], v[180:183]\n"
      "s_add_u32 m0, s40, 61440\n"
      "v_fmac_f32 v219, v177, v177\n"
      "v_mfma_f32_16x16x32_bf16 v[180:183], a[160:163], v[116:119], v[180:183]\n"
      "s_waitcnt lgkmcnt(1)\n"
      "v_add_f32 v204, v204, v206\n"
      "v_add_f32 v205, v205, v207\n"
      "v_mfma_f32_16x16x32_bf16 v[180:183], a[164:167], v[120:123], v[180:183]\n"
      "v_add_f32_dpp v204, v204, v204 quad_perm:[1,0,3,2] row_mask:0xf bank_mask:0xf\n"
      "v_fmac_f32 v219, v178, v178\n"
      "v_mfma_f32_16x16x32_bf16 v[180:183], a[168:171], v[124:127], v[180:183]\n"
      "buffer_load_dwordx4 v1, s[8:11], s46 offen offset:0 sc1 lds\n"
      "v_mfma_f32_16x16x32_bf16 v[180:183], a[172:175], v[128:131], v[180:183]\n"
      "v_add_f32_dpp v205, v205, v205 quad_perm:[1,0,3,2] row_mask:0xf bank_mask:0xf\n"
      "v_add_f32_dpp v204, v204, v204 quad_perm:[2,3,0,1] row_mask:0xf bank_mask:0xf\n"
      "v_mfma_f32_16x16x32_bf16 v[180:183], a[176:179], v[132:135], v[180:183]\n"
      "v_add_f32_dpp v204, v204, v204 row_half_mirror row_mask:0xf bank_mask:0xf\n"
      "v_cvt_pk_bf16_f32 v211, v178, v179\n"
      "v_mfma_f32_16x16x32_bf16 v[180:183], a[180:183], v[136:139], v[180:183]\n"
      "s_add_u32 s38, s38, 24576\n"
      "v_mul_f32 v204, v27, v204\n"
      "v_add_f32_dpp v205, v205, v205 quad_perm:[2,3,0,1] row_mask:0xf bank_mask:0xf\n"
      "v_mfma_f32_16x16x32_bf16 v[180:183], a[184:187], v[140:143], v[180:183]\n"
      "ds_read_b32 v22, v24\n"
      "v_add_f32 v218, v218, v176\n"
      "v_mfma_f32_16x16x32_bf16 v[180:183], a[188:191], v[144:147], v[180:183]\n"
      "v_add_f32_dpp v205, v205, v205 row_half_mirror row_mask:0xf bank_mask:0xf\n"
      "v_fmaak_f32 v205, v27, v205, 0x36a7c5ac\n"
      "v_mfma_f32_16x16x32_bf16 v[180:183], a[192:195], v[148:151], v[180:183]\n"
      "buffer_store_dwordx2 v[204:205], v20, s[8:11], s49 offen sc1\n"
      "v_mfma_f32_16x16x32_bf16 v[180:183], a[196:199], v[152:155], v[180:183]\n"
      "s_add_u32 s50, s50, 24576\n"
      "v_add_f32 v218, v218, v177\n"
      "v_mfma_f32_16x16x32_bf16 v[180:183], a[200:203], v[156:159], v[180:183]\n"
      "v_add_f32 v218, v218, v178\n"
      "v_mfma_f32_16x16x32_bf16 v[180:183], a[204:207], v[160:163], v[180:183]\n"
      "s_waitcnt lgkmcnt(0)\n"
      "v_add_f32 v218, v218, v179\n"
      "v_cmp_gt_i32 vcc, v21, v22\n"
      "v_mfma_f32_16x16x32_bf16 v[180:183], a[208:211], v[164:167], v[180:183]\n"
      "s_waitcnt vmcnt(10)\n"
      "buffer_store_dword v25, v12, s[8:11], s50 offen sc1\n"
      "v_mfma_f32_16x16x32_bf16 v[180:183], a[212:215], v[168:171], v[180:183]\n"
      "v_fmac_f32 v219, v179, v179\n"
      "v_mfma_f32_16x16x32_bf16 v[184:187], a[216:219], v[100:103], v[184:187]\n"
      "s_cbranch_vccnz .Lsp_vy0_24\n"
      ".Lbk_vy0_24:\n"
      "s_sub_u32 s47, s46, s45\n"
      "v_mfma_f32_16x16x32_bf16 v[184:187], a[220:223], v[104:107], v[184:187]\n"
      "s_waitcnt vmcnt(10)\n"
      "v_lshlrev_b32 v174, 16, v193\n"
      "v_and_b32 v179, 0xffff0000, v195\n"
      "v_mfma_f32_16x16x32_bf16 v[184:187], a[224:227], v[108:111], v[184:187]\n"
      "ds_read_b128 v[100:103], v10 offset:8192\n"
      "v_cvt_pk_bf16_f32 v213, v182, v183\n"
      "v_mfma_f32_16x16x32_bf16 v[184:187], a[228:231], v[112:115], v[184:187]\n"
      "ds_read_b128 v[108:111], v10 offset:10240\n"
      "v_and_b32 v173, 0xffff0000, v192\n"
      "v_mfma_f32_16x16x32_bf16 v[184:187], a[232:235], v[116:119], v[184:187]\n"
      "v_fmac_f32 v219, v180, v180\n"
      "v_mfma_f32_16x16x32_bf16 v[184:187], a[236:239], v[120:123], v[184:187]\n"
      "ds_read_b128 v[120:123], v10 offset:13312\n"
      "v_add_f32 v218, v218, v180\n"
      "v_mfma_f32_16x16x32_bf16 v[184:187], a[240:243], v[124:127], v[184:187]\n"
      "v_add_f32 v218, v218, v181\n"
      "v_add_f32 v218, v218, v182\n"
      "v_mfma_f32_16x16x32_bf16 v[184:187], a[244:247], v[128:131], v[184:187]\n"
      "ds_read_b128 v[128:131], v10 offset:15360\n"
      "v_lshlrev_b32 v178, 16, v195\n"
      "v_mfma_f32_16x16x32_bf16 v[184:187], a[248:251], v[132:135], v[184:187]\n"
      "v_mfma_f32_16x16x32_bf16 v[184:187], a[252:255], v[136:139], v[184:187]\n"
      "ds_read_b128 v[104:107], v10 offset:9216\n"
      "v_lshlrev_b32 v172, 16, v192\n"
      "v_mfma_f32_16x16x32_bf16 v[184:187], v[32:35], v[140:143], v[184:187]\n"
      "buffer_store_dwordx4 v[208:211], v15, s[8:11], s38 offen sc1\n"
      "v_mfma_f32_16x16x32_bf16 v[184:187], v[36:39], v[144:147], v[184:187]\n"
      "s_mov_b32 s46, s36\n"
      "ds_read_b128 v[112:115], v10 offset:11264\n"
      "v_fmac_f32 v219, v181, v181\n"
      "v_mfma_f32_16x16x32_bf16 v[184:187], v[40:43], v[148:151], v[184:187]\n"
      "ds_read2_b32 v[134:135], v6 offset0:64 offset1:96\n"
      "v_cvt_pk_bf16_f32 v212, v180, v181\n"
      "v_mfma_f32_16x16x32_bf16 v[184:187], v[44:47], v[152:155], v[184:187]\n"
      "ds_read2_b32 v[132:133], v6 offset0:0 offset1:32\n"
      "s_add_u32 s36, s36, 24576\n"
      "v_mfma_f32_16x16x32_bf16 v[184:187], v[48:51], v[156:159], v[184:187]\n"
      "ds_read_b128 v[116:119], v10 offset:12288\n"
      "v_fmac_f32 v219, v182, v182\n"
      "v_mfma_f32_16x16x32_bf16 v[184:187], v[52:55], v[160:163], v[184:187]\n"
      "ds_read_b128 v[124:127], v10 offset:14336\n"
      "v_add_f32 v218, v218, v183\n"
      "v_mfma_f32_16x16x32_bf16 v[184:187], v[56:59], v[164:167], v[184:187]\n"
      "s_waitcnt lgkmcnt(5)\n"
      "ds_read_b128 v[160:163], v10 offset:6144\n"
      "v_and_b32 v177, 0xffff0000, v194\n"
      "v_mfma_f32_16x16x32_bf16 v[184:187], v[60:63], v[168:171], v[184:187]\n"
      "v_mfma_f32_16x16x32_bf16 v[188:191], v[64:67], v[100:103], 0\n"
      "ds_read_b128 v[140:143], v10 offset:1024\n"
      "v_and_b32 v175, 0xffff0000, v193\n"
      "v_mfma_f32_16x16x32_bf16 v[188:191], v[68:71], v[104:107], v[188:191]\n"
      "ds_read2_b32 v[168:169], v7 offset0:0 offset1:32\n"
      "v_lshlrev_b32 v176, 16, v194\n"
      "v_mfma_f32_16x16x32_bf16 v[188:191], v[72:75], v[108:111], v[188:191]\n"
      "s_waitcnt lgkmcnt(5)\n"
      "ds_read_b128 v[164:167], v10 offset:7168\n"
      "v_add_f32 v218, v218, v184\n"
      "v_mfma_f32_16x16x32_bf16 v[188:191], v[76:79], v[112:115], v[188:191]\n"
      "s_waitcnt lgkmcnt(5)\n"
      "ds_read_b128 v[144:147], v10 offset:2048\n"
      "v_add_f32 v218, v218, v185\n"
      "v_mfma_f32_16x16x32_bf16 v[188:191], v[80:83], v[116:119], v[188:191]\n"
      "buffer_load_dwordx4 v[192:195], v13, s[8:11], s47 offen sc1\n"
      "v_mfma_f32_16x16x32_bf16 v[188:191], v[84:87], v[120:123], v[188:191]\n"
      "s_waitcnt lgkmcnt(4)\n"
      "ds_read2_b32 v[170:171], v7 offset0:64 offset1:96\n"
      "v_fmac_f32 v219, v183, v183\n"
      "v_mfma_f32_16x16x32_bf16 v[188:191], v[88:91], v[124:127], v[188:191]\n"
      "s_min_u32 s36, s36, s6\n"
      "ds_read_b128 v[136:139], v10 offset:0\n"
      "v_fmac_f32 v219, v184, v184\n"
      "v_mfma_f32_16x16x32_bf16 v[188:191], v[92:95], v[128:131], v[188:191]\n"
      "ds_read_b128 v[148:151], v10 offset:3072\n"
      "v_mfma_f32_16x16x32_bf16 v[188:191], v[96:99], v[132:135], v[188:191]\n"
      "v_cvt_pk_bf16_f32 v215, v186, v187\n"
      "v_add_f32 v218, v218, v186\n"
      "v_mfma_f32_16x16x32_bf16 v[172:175], a[0:3], v[100:103], v[172:175]\n"
      "ds_read_b128 v[152:155], v10 offset:4096\n"
      "v_add_f32 v218, v218, v187\n"
      "v_mfma_f32_16x16x32_bf16 v[172:175], a[4:7], v[104:107], v[172:175]\n"
      "ds_read_b128 v[156:159], v10 offset:5120\n"
      "v_fmac_f32_e64 v219, v185, v185\n"
      "v_mfma_f32_16x16x32_bf16 v[172:175], a[8:11], v[108:111], v[172:175]\n"
      "s_add_u32 m0, s39, 0\n"
      "v_cvt_pk_bf16_f32 v214, v184, v185\n"
      "v_mfma_f32_16x16x32_bf16 v[172:175], a[12:15], v[112:115], v[172:175]\n"
      "ds_write_b32 v18, v218\n"
      "v_mfma_f32_16x16x32_bf16 v[172:175], a[16:19], v[116:119], v[172:175]\n"
      "s_waitcnt vmcnt(10) lgkmcnt(4)\n"
      "v_fmac_f32_e64 v219, v186, v186\n"
      "v_cmp_ne_u32 vcc, s21, v28\n"
      "v_mfma_f32_16x16x32_bf16 v[172:175], a[20:23], v[120:123], v[172:175]\n"
      "ds_write_b32 v17, v190 offset:512\n"
      "v_mfma_f32_16x16x32_bf16 v[172:175], a[24:27], v[124:127], v[172:175]\n"
      "buffer_store_dwordx4 v[212:215], v15, s[8:11], s38 offen offset:1024 sc1\n"
      "v_mfma_f32_16x16x32_bf16 v[172:175], a[28:31], v[128:131], v[172:175]\n"
      "ds_write_b32 v17, v188 offset:0\n"
      "v_mfma_f32_16x16x32_bf16 v[172:175], a[32:35], v[132:135], v[172:175]\n"
      "v_fmac_f32_e64 v219, v187, v187\n"
      "v_mfma_f32_16x16x32_bf16 v[172:175], a[36:39], v[136:139], v[172:175]\n"
      "ds_write_b32 v17, v191 offset:768\n"
      "v_mfma_f32_16x16x32_bf16 v[172:175], a[40:43], v[140:143], v[172:175]\n"
      "s_cbranch_vccnz .Lsp_v0_25\n"
      ".Lbk_v0_25:\n"
      "v_add_u32 v21, 1, v21\n"
      "v_mfma_f32_16x16x32_bf16 v[172:175], a[44:47], v[144:147], v[172:175]\n"
      "s_waitcnt lgkmcnt(3)\n"
      "ds_write_b32 v18, v219 offset:4\n"
      "v_mfma_f32_16x16x32_bf16 v[172:175], a[48:51], v[148:151], v[172:175]\n"
      "buffer_load_dword v28, v11, s[8:11], s36 offen sc1\n"
      "v_mfma_f32_16x16x32_bf16 v[172:175], a[52:55], v[152:155], v[172:175]\n"
      "ds_write_b32 v17, v189 offset:256\n"
      "v_mfma_f32_16x16x32_bf16 v[172:175], a[56:59], v[156:159], v[172:175]\n"
      "s_waitcnt vmcnt(8) lgkmcnt(3)\n"
      "v_lshlrev_b32 v186, 16, v199\n"
      "v_and_b32 v185, 0xffff0000, v198\n"
      "v_mfma_f32_16x16x32_bf16 v[172:175], a[60:63], v[160:163], v[172:175]\n"
      "ds_write_b32 v23, v21\n"
      "v_mfma_f32_16x16x32_bf16 v[172:175], a[64:67], v[164:167], v[172:175]\n"
      "v_and_b32 v181, 0xffff0000, v196\n"
      "v_and_b32 v187, 0xffff0000, v199\n"
      "v_mfma_f32_16x16x32_bf16 v[172:175], a[68:71], v[168:171], v[172:175]\n"
      "buffer_load_dwordx4 v0, s[8:11], s46 offen offset:3072 sc1 lds\n"
      "v_mfma_f32_16x16x32_bf16 v[176:179], a[72:75], v[100:103], v[176:179]\n"
      "v_lshlrev_b32 v182, 16, v197\n"
      "s_add_u32 s38, s38, 24576\n"
      "v_mfma_f32_16x16x32_bf16 v[176:179], a[76:79], v[104:107], v[176:179]\n"
      "v_lshlrev_b32 v184, 16, v198\n"
      "v_mfma_f32_16x16x32_bf16 v[176:179], a[80:83], v[108:111], v[176:179]\n"
      "v_cvt_pk_bf16_f32 v208, v172, v173\n"
      "v_mfma_f32_16x16x32_bf16 v[176:179], a[84:87], v[112:115], v[176:179]\n"
      "v_mul_f32 v219, v172, v172\n"
      "v_mfma_f32_16x16x32_bf16 v[176:179], a[88:91], v[116:119], v[176:179]\n"
      "v_lshlrev_b32 v180, 16, v196\n"
      "v_and_b32 v183, 0xffff0000, v197\n"
      "v_mfma_f32_16x16x32_bf16 v[176:179], a[92:95], v[120:123], v[176:179]\n"
      "v_fmac_f32 v219, v173, v173\n"
      "v_mfma_f32_16x16x32_bf16 v[176:179], a[96:99], v[124:127], v[176:179]\n"
      "buffer_load_dwordx4 v[196:199], v13, s[8:11], s47 offen offset:1024 sc1\n"
      "v_mfma_f32_16x16x32_bf16 v[176:179], a[100:103], v[128:131], v[176:179]\n"
      "ds_read_b32 v22, v24\n"
      "v_mfma_f32_16x16x32_bf16 v[176:179], a[104:107], v[132:135], v[176:179]\n"
      "v_fmac_f32 v219, v174, v174\n"
      "v_mfma_f32_16x16x32_bf16 v[176:179], a[108:111], v[136:139], v[176:179]\n"
      "v_fmac_f32 v219, v175, v175\n"
      "v_mfma_f32_16x16x32_bf16 v[176:179], a[112:115], v[140:143], v[176:179]\n"
      "buffer_load_dwordx4 v0, s[8:11], s46 offen offset:0 sc1 lds\n"
      "v_mfma_f32_16x16x32_bf16 v[176:179], a[116:119], v[144:147], v[176:179]\n"
      "v_mfma_f32_16x16x32_bf16 v[176:179], a[120:123], v[148:151], v[176:179]\n"
      "s_waitcnt lgkmcnt(0)\n"
      "v_cmp_gt_i32 vcc, v21, v22\n"
      "v_add_f32_e64 v218, v172, v173\n"
      "v_mfma_f32_16x16x32_bf16 v[176:179], a[124:127], v[152:155], v[176:179]\n"
      "v_add_f32_e64 v218, v218, v174\n"
      "v_add_f32_e64 v218, v218, v175\n"
      "v_mfma_f32_16x16x32_bf16 v[176:179], a[128:131], v[156:159], v[176:179]\n"
      "buffer_load_dwordx4 v0, s[8:11], s46 offen offset:1024 sc1 lds\n"
      "v_mfma_f32_16x16x32_bf16 v[176:179], a[132:135], v[160:163], v[176:179]\n"
      "s_cbranch_vccnz .Lsp_vz0_26\n"
      ".Lbk_vz0_26:\n"
      "ds_read_b128 v[204:207], v19\n"
      "v_mfma_f32_16x16x32_bf16 v[176:179], a[136:139], v[164:167], v[176:179]\n"
      "v_cvt_pk_bf16_f32 v209, v174, v175\n"
      "v_add_u32 v21, 1, v21\n"
      "v_mfma_f32_16x16x32_bf16 v[176:179], a[140:143], v[168:171], v[176:179]\n"
      "s_waitcnt vmcnt(10)\n"
      "ds_write_b32 v23, v21\n"
      "v_mfma_f32_16x16x32_bf16 v[180:183], a[144:147], v[100:103], v[180:183]\n"
      "v_mfma_f32_16x16x32_bf16 v[180:183], a[148:151], v[104:107], v[180:183]\n"
      "buffer_load_dwordx4 v0, s[8:11], s46 offen offset:2048 sc1 lds\n"
      "v_mfma_f32_16x16x32_bf16 v[180:183], a[152:155], v[108:111], v[180:183]\n"
      "s_add_u32 s49, s49, 24576\n"
      "s_add_u32 m0, s40, 0\n"
      "v_mfma_f32_16x16x32_bf16 v[180:183], a[156:159], v[112:115], v[180:183]\n"
      "v_mfma_f32_16x16x32_bf16 v[180:183], a[160:163], v[116:119], v[180:183]\n"
      "s_waitcnt lgkmcnt(1)\n"
      "v_add_f32 v204, v204, v206\n"
      "v_cvt_pk_bf16_f32 v210, v176, v177\n"
      "v_mfma_f32_16x16x32_bf16 v[180:183], a[164:167], v[120:123], v[180:183]\n"
      "buffer_load_dwordx4 v1, s[8:11], s46 offen offset:0 sc1 lds\n"
      "v_mfma_f32_16x16x32_bf16 v[180:183], a[168:171], v[124:127], v[180:183]\n"
      "v_add_f32 v205, v205, v207\n"
      "v_add_f32_dpp v204, v204, v204 quad_perm:[1,0,3,2] row_mask:0xf bank_mask:0xf\n"
      "v_mfma_f32_16x16x32_bf16 v[180:183], a[172:175], v[128:131], v[180:183]\n"
      "v_fmac_f32 v219, v176, v176\n"
      "v_mfma_f32_16x16x32_bf16 v[180:183], a[176:179], v[132:135], v[180:183]\n"
      "v_add_f32_dpp v204, v204, v204 quad_perm:[2,3,0,1] row_mask:0xf bank_mask:0xf\n"
      "v_cvt_pk_bf16_f32 v211, v178, v179\n"
      "v_mfma_f32_16x16x32_bf16 v[180:183], a[180:183], v[136:139], v[180:183]\n"
      "buffer_store_dwordx4 v[208:211], v15, s[8:11], s38 offen sc1\n"
      "v_mfma_f32_16x16x32_bf16 v[180:183], a[184:187], v[140:143], v[180:183]\n"
      "ds_read_b32 v22, v24\n"
      "v_add_f32_dpp v205, v205, v205 quad_perm:[1,0,3,2] row_mask:0xf bank_mask:0xf\n"
      "v_mfma_f32_16x16x32_bf16 v[180:183], a[188:191], v[144:147], v[180:183]\n"
      "s_add_u32 s50, s50, 24576\n"
      "v_add_f32_dpp v205, v205, v205 quad_perm:[2,3,0,1] row_mask:0xf bank_mask:0xf\n"
      "v_fmac_f32 v219, v177, v177\n"
      "v_mfma_f32_16x16x32_bf16 v[180:183], a[192:195], v[148:151], v[180:183]\n"
      "v_add_f32_dpp v205, v205, v205 row_half_mirror row_mask:0xf bank_mask:0xf\n"
      "v_fmac_f32 v219, v178, v178\n"
      "v_mfma_f32_16x16x32_bf16 v[180:183], a[196:199], v[152:155], v[180:183]\n"
      ".Lv_p1:\n"
      "s_cbranch_execz .Lv_pr\n"
      "v_add_f32_dpp v204, v204, v204 row_half_mirror row_mask:0xf bank_mask:0xf\n"
      "v_mfma_f32_16x16x32_bf16 v[180:183], a[200:203], v[156:159], v[180:183]\n"
      "v_mul_f32 v204, v27, v204\n"
      "v_fmaak_f32 v205, v27, v205, 0x36a7c5ac\n"
      "v_mfma_f32_16x16x32_bf16 v[180:183], a[204:207], v[160:163], v[180:183]\n"
      "s_waitcnt vmcnt(12) lgkmcnt(1)\n"
      "buffer_store_dword v25, v12, s[8:11], s50 offen sc1\n"
      "v_mfma_f32_16x16x32_bf16 v[180:183], a[208:211], v[164:167], v[180:183]\n"
      "s_waitcnt lgkmcnt(0)\n"
      "v_cmp_gt_i32 vcc, v21, v22\n"
      "v_add_f32 v218, v218, v176\n"
      "v_mfma_f32_16x16x32_bf16 v[180:183], a[212:215], v[168:171], v[180:183]\n"
      "v_add_f32 v218, v218, v177\n"
      "v_add_f32_e64 v218, v218, v178\n"
      "v_mfma_f32_16x16x32_bf16 v[184:187], a[216:219], v[100:103], v[184:187]\n"
      "buffer_store_dwordx2 v[204:205], v20, s[8:11], s49 offen sc1\n"
      "v_mfma_f32_16x16x32_bf16 v[184:187], a[220:223], v[104:107], v[184:187]\n"
      "s_sub_u32 s47, s46, s45\n"
      "s_cbranch_vccnz .Lsp_vy1_27\n"
      ".Lbk_vy1_27:\n"
      "v_mfma_f32_16x16x32_bf16 v[184:187], a[224:227], v[108:111], v[184:187]\n"
      "s_waitcnt vmcnt(11)\n"
      "ds_read_b128 v[104:107], v10 offset:29696\n"
      "v_and_b32 v177, 0xffff0000, v194\n"
      "v_mfma_f32_16x16x32_bf16 v[184:187], a[228:231], v[112:115], v[184:187]\n"
      "v_fmac_f32 v219, v179, v179\n"
      "v_mfma_f32_16x16x32_bf16 v[184:187], a[232:235], v[116:119], v[184:187]\n"
      "v_fmac_f32 v219, v180, v180\n"
      "v_add_f32 v218, v218, v179\n"
      "v_mfma_f32_16x16x32_bf16 v[184:187], a[236:239], v[120:123], v[184:187]\n"
      "v_cvt_pk_bf16_f32 v212, v180, v181\n"
      "v_and_b32 v179, 0xffff0000, v195\n"
      "v_mfma_f32_16x16x32_bf16 v[184:187], a[240:243], v[124:127], v[184:187]\n"
      "ds_read_b128 v[120:123], v10 offset:33792\n"
      "v_lshlrev_b32 v174, 16, v193\n"
      "v_mfma_f32_16x16x32_bf16 v[184:187], a[244:247], v[128:131], v[184:187]\n"
      "ds_read_b128 v[112:115], v10 offset:31744\n"
      "v_add_f32 v218, v218, v180\n"
      "v_mfma_f32_16x16x32_bf16 v[184:187], a[248:251], v[132:135], v[184:187]\n"
      "ds_read_b128 v[100:103], v10 offset:28672\n"
      "v_add_f32 v218, v218, v181\n"
      "v_mfma_f32_16x16x32_bf16 v[184:187], a[252:255], v[136:139], v[184:187]\n"
      "ds_read_b128 v[124:127], v10 offset:34816\n"
      "v_mfma_f32_16x16x32_bf16 v[184:187], v[32:35], v[140:143], v[184:187]\n"
      "ds_read_b128 v[108:111], v10 offset:30720\n"
      "v_and_b32 v175, 0xffff0000, v193\n"
      "v_mfma_f32_16x16x32_bf16 v[184:187], v[36:39], v[144:147], v[184:187]\n"
      "ds_read_b128 v[116:119], v10 offset:32768\n"
      "v_fmac_f32 v219, v181, v181\n"
      "v_mfma_f32_16x16x32_bf16 v[184:187], v[40:43], v[148:151], v[184:187]\n"
      "v_add_f32 v218, v218, v182\n"
      "v_mfma_f32_16x16x32_bf16 v[184:187], v[44:47], v[152:155], v[184:187]\n"
      "ds_read2_b32 v[132:133], v8 offset0:0 offset1:32\n"
      "v_and_b32 v173, 0xffff0000, v192\n"
      "v_mfma_f32_16x16x32_bf16 v[184:187], v[48:51], v[156:159], v[184:187]\n"
      "ds_read_b128 v[128:131], v10 offset:35840\n"
      "v_lshlrev_b32 v178, 16, v195\n"
      "v_mfma_f32_16x16x32_bf16 v[184:187], v[52:55], v[160:163], v[184:187]\n"
      "ds_read_b128 v[156:159], v10 offset:25600\n"
      "v_lshlrev_b32 v172, 16, v192\n"
      "v_mfma_f32_16x16x32_bf16 v[184:187], v[56:59], v[164:167], v[184:187]\n"
      "ds_read_b128 v[144:147], v10 offset:22528\n"
      "v_lshlrev_b32 v176, 16, v194\n"
      "v_mfma_f32_16x16x32_bf16 v[184:187], v[60:63], v[168:171], v[184:187]\n"
      "s_waitcnt lgkmcnt(5)\n"
      "buffer_load_dwordx4 v[192:195], v13, s[8:11], s47 offen sc1\n"
      "v_mfma_f32_16x16x32_bf16 v[188:191], v[64:67], v[100:103], 0\n"
      "ds_read2_b32 v[134:135], v8 offset0:64 offset1:96\n"
      "v_fmac_f32 v219, v182, v182\n"
      "v_mfma_f32_16x16x32_bf16 v[188:191], v[68:71], v[104:107], v[188:191]\n"
      "ds_read_b128 v[148:151], v10 offset:23552\n"
      "v_add_f32 v218, v218, v183\n"
      "v_mfma_f32_16x16x32_bf16 v[188:191], v[72:75], v[108:111], v[188:191]\n"
      "s_mov_b32 s46, s36\n"
      "v_cvt_pk_bf16_f32 v213, v182, v183\n"
      "v_cvt_pk_bf16_f32 v215, v186, v187\n"
      "v_mfma_f32_16x16x32_bf16 v[188:191], v[76:79], v[112:115], v[188:191]\n"
      "s_waitcnt lgkmcnt(4)\n"
      "ds_read_b128 v[160:163], v10 offset:26624\n"
      "v_fmac_f32 v219, v183, v183\n"
      "v_mfma_f32_16x16x32_bf16 v[188:191], v[80:83], v[116:119], v[188:191]\n"
      "ds_read_b128 v[140:143], v10 offset:21504\n"
      "v_fmac_f32 v219, v184, v184\n"
      "v_mfma_f32_16x16x32_bf16 v[188:191], v[84:87], v[120:123], v[188:191]\n"
      "ds_read2_b32 v[168:169], v9 offset0:0 offset1:32\n"
      "v_add_f32 v218, v218, v184\n"
      "v_mfma_f32_16x16x32_bf16 v[188:191], v[88:91], v[124:127], v[188:191]\n"
      "ds_read_b128 v[152:155], v10 offset:24576\n"
      "v_fmac_f32 v219, v185, v185\n"
      "v_mfma_f32_16x16x32_bf16 v[188:191], v[92:95], v[128:131], v[188:191]\n"
      "s_waitcnt lgkmcnt(5)\n"
      "ds_read_b128 v[136:139], v10 offset:20480\n"
      "v_add_f32 v218, v218, v185\n"
      "v_mfma_f32_16x16x32_bf16 v[188:191], v[96:99], v[132:135], v[188:191]\n"
      "s_cmp_eq_u32 s36, s37\n"
      "s_cselect_b32 s48, 49152, s51\n"
      "ds_read_b128 v[164:167], v10 offset:27648\n"
      "v_mfma_f32_16x16x32_bf16 v[172:175], a[0:3], v[100:103], v[172:175]\n"
      "ds_read2_b32 v[170:171], v9 offset0:64 offset1:96\n"
      "v_cvt_pk_bf16_f32 v214, v184, v185\n"
      "v_mfma_f32_16x16x32_bf16 v[172:175], a[4:7], v[104:107], v[172:175]\n"
      "v_fmac_f32 v219, v186, v186\n"
      "v_add_f32 v218, v218, v186\n"
      "v_mfma_f32_16x16x32_bf16 v[172:175], a[8:11], v[108:111], v[172:175]\n"
      "ds_write_b32 v17, v189 offset:256\n"
      "v_mfma_f32_16x16x32_bf16 v[172:175], a[12:15], v[112:115], v[172:175]\n"
      "v_add_f32 v218, v218, v187\n"
      "v_add_u32 v21, 1, v21\n"
      "v_mfma_f32_16x16x32_bf16 v[172:175], a[16:19], v[116:119], v[172:175]\n"
      "buffer_store_dwordx4 v[212:215], v15, s[8:11], s38 offen offset:1024 sc1\n"
      "v_mfma_f32_16x16x32_bf16 v[172:175], a[20:23], v[120:123], v[172:175]\n"
      "ds_write_b32 v17, v190 offset:512\n"
      "v_mfma_f32_16x16x32_bf16 v[172:175], a[24:27], v[124:127], v[172:175]\n"
      "v_fmac_f32 v219, v187, v187\n"
      "v_mfma_f32_16x16x32_bf16 v[172:175], a[28:31], v[128:131], v[172:175]\n"
      "s_add_u32 s36, s36, s48\n"
      "ds_write_b32 v18, v218\n"
      "v_mfma_f32_16x16x32_bf16 v[172:175], a[32:35], v[132:135], v[172:175]\n"
      "s_waitcnt vmcnt(11) lgkmcnt(3)\n"
      "v_cmp_ne_u32 vcc, s21, v28\n"
      "v_mfma_f32_16x16x32_bf16 v[172:175], a[36:39], v[136:139], v[172:175]\n"
      "ds_write_b32 v17, v188 offset:0\n"
      "v_mfma_f32_16x16x32_bf16 v[172:175], a[40:43], v[140:143], v[172:175]\n"
      "s_waitcnt vmcnt(9) lgkmcnt(3)\n"
      "s_min_u32 s36, s36, s6\n"
      "v_lshlrev_b32 v182, 16, v197\n"
      "v_mfma_f32_16x16x32_bf16 v[172:175], a[44:47], v[144:147], v[172:175]\n"
      "ds_write_b32 v17, v191 offset:768\n"
      "v_mfma_f32_16x16x32_bf16 v[172:175], a[48:51], v[148:151], v[172:175]\n"
      "s_cbranch_vccnz .Lsp_v1_28\n"
      ".Lbk_v1_28:\n"
      "v_and_b32 v181, 0xffff0000, v196\n"
      "v_mfma_f32_16x16x32_bf16 v[172:175], a[52:55], v[152:155], v[172:175]\n"
      "ds_write_b32 v18, v219 offset:4\n"
      "v_mfma_f32_16x16x32_bf16 v[172:175], a[56:59], v[156:159], v[172:175]\n"
      "buffer_load_dword v28, v11, s[8:11], s36 offen sc1\n"
      "v_mfma_f32_16x16x32_bf16 v[172:175], a[60:63], v[160:163], v[172:175]\n"
      "s_add_u32 m0, s39, 20480\n"
      "ds_write_b32 v23, v21\n"
      "v_mfma_f32_16x16x32_bf16 v[172:175], a[64:67], v[164:167], v[172:175]\n"
      "v_and_b32 v187, 0xffff0000, v199\n"
      "v_and_b32 v183, 0xffff0000, v197\n"
      "v_mfma_f32_16x16x32_bf16 v[172:175], a[68:71], v[168:171], v[172:175]\n"
      "buffer_load_dwordx4 v0, s[8:11], s46 offen offset:0 sc1 lds\n"
      "v_mfma_f32_16x16x32_bf16 v[176:179], a[72:75], v[100:103], v[176:179]\n"
      "s_add_u32 s49, s49, 24576\n"
      "v_mfma_f32_16x16x32_bf16 v[176:179], a[76:79], v[104:107], v[176:179]\n"
      "v_mfma_f32_16x16x32_bf16 v[176:179], a[80:83], v[108:111], v[176:179]\n"
      "v_cvt_pk_bf16_f32 v209, v174, v175\n"
      "v_mfma_f32_16x16x32_bf16 v[176:179], a[84:87], v[112:115], v[176:179]\n"
      "buffer_load_dwordx4 v0, s[8:11], s46 offen offset:1024 sc1 lds\n"
      "v_mfma_f32_16x16x32_bf16 v[176:179], a[88:91], v[116:119], v[176:179]\n"
      "v_add_f32 v218, v172, v173\n"
      "v_mfma_f32_16x16x32_bf16 v[176:179], a[92:95], v[120:123], v[176:179]\n"
      "v_add_f32 v218, v218, v174\n"
      "v_lshlrev_b32 v180, 16, v196\n"
      "v_mfma_f32_16x16x32_bf16 v[176:179], a[96:99], v[124:127], v[176:179]\n"
      "ds_read_b32 v22, v24\n"
      "v_lshlrev_b32 v186, 16, v199\n"
      "v_mfma_f32_16x16x32_bf16 v[176:179], a[100:103], v[128:131], v[176:179]\n"
      "buffer_load_dwordx4 v0, s[8:11], s46 offen offset:2048 sc1 lds\n"
      "v_mfma_f32_16x16x32_bf16 v[176:179], a[104:107], v[132:135], v[176:179]\n"
      "v_mul_f32 v219, v172, v172\n"
      "v_fmac_f32 v219, v173, v173\n"
      "v_mfma_f32_16x16x32_bf16 v[176:179], a[108:111], v[136:139], v[176:179]\n"
      "v_lshlrev_b32 v184, 16, v198\n"
      "v_mfma_f32_16x16x32_bf16 v[176:179], a[112:115], v[140:143], v[176:179]\n"
      "v_fmac_f32_e64 v219, v174, v174\n"
      "v_mfma_f32_16x16x32_bf16 v[176:179], a[116:119], v[144:147], v[176:179]\n"
      "s_waitcnt lgkmcnt(0)\n"
      "v_cmp_gt_i32 vcc, v21, v22\n"
      "v_add_f32_e64 v218, v218, v175\n"
      "v_mfma_f32_16x16x32_bf16 v[176:179], a[120:123], v[148:151], v[176:179]\n"
      "v_mfma_f32_16x16x32_bf16 v[176:179], a[124:127], v[152:155], v[176:179]\n"
      "v_and_b32 v185, 0xffff0000, v198\n"
      "v_cvt_pk_bf16_f32 v208, v172, v173\n"
      "v_mfma_f32_16x16x32_bf16 v[176:179], a[128:131], v[156:159], v[176:179]\n"
      "buffer_load_dwordx4 v[196:199], v13, s[8:11], s47 offen offset:1024 sc1\n"
      "v_mfma_f32_16x16x32_bf16 v[176:179], a[132:135], v[160:163], v[176:179]\n"
      "s_cbranch_vccnz .Lsp_vz1_29\n"
      ".Lbk_vz1_29:\n"
      "ds_read_b128 v[204:207], v19\n"
      "v_mfma_f32_16x16x32_bf16 v[176:179], a[136:139], v[164:167], v[176:179]\n"
      "v_add_u32 v21, 1, v21\n"
      "v_mfma_f32_16x16x32_bf16 v[176:179], a[140:143], v[168:171], v[176:179]\n"
      "s_waitcnt vmcnt(10)\n"
      "ds_write_b32 v23, v21\n"
      "v_mfma_f32_16x16x32_bf16 v[180:183], a[144:147], v[100:103], v[180:183]\n"
      "v_fmac_f32 v219, v175, v175\n"
      "v_mfma_f32_16x16x32_bf16 v[180:183], a[148:151], v[104:107], v[180:183]\n"
      "buffer_load_dwordx4 v0, s[8:11], s46 offen offset:3072 sc1 lds\n"
      "v_mfma_f32_16x16x32_bf16 v[180:183], a[152:155], v[108:111], v[180:183]\n"
      "v_cvt_pk_bf16_f32 v210, v176, v177\n"
      "v_fmac_f32 v219, v176, v176\n"
      "v_mfma_f32_16x16x32_bf16 v[180:183], a[156:159], v[112:115], v[180:183]\n"
      "s_add_u32 m0, s40, 20480\n"
      "v_fmac_f32 v219, v177, v177\n"
      "v_mfma_f32_16x16x32_bf16 v[180:183], a[160:163], v[116:119], v[180:183]\n"
      "s_waitcnt lgkmcnt(1)\n"
      "v_add_f32 v204, v204, v206\n"
      "v_add_f32 v205, v205, v207\n"
      "v_mfma_f32_16x16x32_bf16 v[180:183], a[164:167], v[120:123], v[180:183]\n"
      "v_add_f32_dpp v204, v204, v204 quad_perm:[1,0,3,2] row_mask:0xf bank_mask:0xf\n"
      "v_fmac_f32 v219, v178, v178\n"
      "v_mfma_f32_16x16x32_bf16 v[180:183], a[168:171], v[124:127], v[180:183]\n"
      "buffer_load_dwordx4 v1, s[8:11], s46 offen offset:0 sc1 lds\n"
      "v_mfma_f32_16x16x32_bf16 v[180:183], a[172:175], v[128:131], v[180:183]\n"
      "v_add_f32_dpp v205, v205, v205 quad_perm:[1,0,3,2] row_mask:0xf bank_mask:0xf\n"
      "v_add_f32_dpp v204, v204, v204 quad_perm:[2,3,0,1] row_mask:0xf bank_mask:0xf\n"
      "v_mfma_f32_16x16x32_bf16 v[180:183], a[176:179], v[132:135], v[180:183]\n"
      "v_add_f32_dpp v204, v204, v204 row_half_mirror row_mask:0xf bank_mask:0xf\n"
      "v_cvt_pk_bf16_f32 v211, v178, v179\n"
      "v_mfma_f32_16x16x32_bf16 v[180:183], a[180:183], v[136:139], v[180:183]\n"
      "s_add_u32 s38, s38, 24576\n"
      "v_mul_f32 v204, v27, v204\n"
      "v_add_f32_dpp v205, v205, v205 quad_perm:[2,3,0,1] row_mask:0xf bank_mask:0xf\n"
      "v_mfma_f32_16x16x32_bf16 v[180:183], a[184:187], v[140:143], v[180:183]\n"
      "ds_read_b32 v22, v24\n"
      "v_add_f32 v218, v218, v176\n"
      "v_mfma_f32_16x16x32_bf16 v[180:183], a[188:191], v[144:147], v[180:183]\n"
      "v_add_f32_dpp v205, v205, v205 row_half_mirror row_mask:0xf bank_mask:0xf\n"
      "v_fmaak_f32 v205, v27, v205, 0x36a7c5ac\n"
      "v_mfma_f32_16x16x32_bf16 v[180:183], a[192:195], v[148:151], v[180:183]\n"
      "buffer_store_dwordx2 v[204:205], v20, s[8:11], s49 offen sc1\n"
      "v_mfma_f32_16x16x32_bf16 v[180:183], a[196:199], v[152:155], v[180:183]\n"
      "s_add_u32 s50, s50, 24576\n"
      "v_add_f32 v218, v218, v177\n"
      "v_mfma_f32_16x16x32_bf16 v[180:183], a[200:203], v[156:159], v[180:183]\n"
      "v_add_f32 v218, v218, v178\n"
      "v_mfma_f32_16x16x32_bf16 v[180:183], a[204:207], v[160:163], v[180:183]\n"
      "s_waitcnt lgkmcnt(0)\n"
      "v_add_f32 v218, v218, v179\n"
      "v_cmp_gt_i32 vcc, v21, v22\n"
      "v_mfma_f32_16x16x32_bf16 v[180:183], a[208:211], v[164:167], v[180:183]\n"
      "s_waitcnt vmcnt(10)\n"
      "buffer_store_dword v25, v12, s[8:11], s50 offen sc1\n"
      "v_mfma_f32_16x16x32_bf16 v[180:183], a[212:215], v[168:171], v[180:183]\n"
      "v_fmac_f32 v219, v179, v179\n"
      "v_mfma_f32_16x16x32_bf16 v[184:187], a[216:219], v[100:103], v[184:187]\n"
      "s_cbranch_vccnz .Lsp_vy0_30\n"
      ".Lbk_vy0_30:\n"
      "s_sub_u32 s47, s46, s45\n"
      "v_mfma_f32_16x16x32_bf16 v[184:187], a[220:223], v[104:107], v[184:187]\n"
      "s_waitcnt vmcnt(10)\n"
      "v_lshlrev_b32 v174, 16, v193\n"
      "v_and_b32 v179, 0xffff0000, v195\n"
      "v_mfma_f32_16x16x32_bf16 v[184:187], a[224:227], v[108:111], v[184:187]\n"
      "ds_read_b128 v[100:103], v0 offset:8192\n"
      "v_cvt_pk_bf16_f32 v213, v182, v183\n"
      "v_mfma_f32_16x16x32_bf16 v[184:187], a[228:231], v[112:115], v[184:187]\n"
      "ds_read_b128 v[108:111], v0 offset:10240\n"
      "v_and_b32 v173, 0xffff0000, v192\n"
      "v_mfma_f32_16x16x32_bf16 v[184:187], a[232:235], v[116:119], v[184:187]\n"
      "v_fmac_f32 v219, v180, v180\n"
      "v_mfma_f32_16x16x32_bf16 v[184:187], a[236:239], v[120:123], v[184:187]\n"
      "ds_read_b128 v[120:123], v0 offset:13312\n"
      "v_add_f32 v218, v218, v180\n"
      "v_mfma_f32_16x16x32_bf16 v[184:187], a[240:243], v[124:127], v[184:187]\n"
      "v_add_f32 v218, v218, v181\n"
      "v_add_f32 v218, v218, v182\n"
      "v_mfma_f32_16x16x32_bf16 v[184:187], a[244:247], v[128:131], v[184:187]\n"
      "ds_read_b128 v[128:131], v0 offset:15360\n"
      "v_lshlrev_b32 v178, 16, v195\n"
      "v_mfma_f32_16x16x32_bf16 v[184:187], a[248:251], v[132:135], v[184:187]\n"
      "v_mfma_f32_16x16x32_bf16 v[184:187], a[252:255], v[136:139], v[184:187]\n"
      "ds_read_b128 v[104:107], v0 offset:9216\n"
      "v_lshlrev_b32 v172, 16, v192\n"
      "v_mfma_f32_16x16x32_bf16 v[184:187], v[32:35], v[140:143], v[184:187]\n"
      "buffer_store_dwordx4 v[208:211], v15, s[8:11], s38 offen sc1\n"
      "v_mfma_f32_16x16x32_bf16 v[184:187], v[36:39], v[144:147], v[184:187]\n"
      "s_mov_b32 s46, s36\n"
      "ds_read_b128 v[112:115], v0 offset:11264\n"
      "v_fmac_f32 v219, v181, v181\n"
      "v_mfma_f32_16x16x32_bf16 v[184:187], v[40:43], v[148:151], v[184:187]\n"
      "ds_read2_b32 v[134:135], v2 offset0:64 offset1:96\n"
      "v_cvt_pk_bf16_f32 v212, v180, v181\n"
      "v_mfma_f32_16x16x32_bf16 v[184:187], v[44:47], v[152:155], v[184:187]\n"
      "ds_read2_b32 v[132:133], v2 offset0:0 offset1:32\n"
      "s_add_u32 s36, s36, 24576\n"
      "v_mfma_f32_16x16x32_bf16 v[184:187], v[48:51], v[156:159], v[184:187]\n"
      "ds_read_b128 v[116:119], v0 offset:12288\n"
      "v_fmac_f32 v219, v182, v182\n"
      "v_mfma_f32_16x16x32_bf16 v[184:187], v[52:55], v[160:163], v[184:187]\n"
      "ds_read_b128 v[124:127], v0 offset:14336\n"
      "v_add_f32 v218, v218, v183\n"
      "v_mfma_f32_16x16x32_bf16 v[184:187], v[56:59], v[164:167], v[184:187]\n"
      "s_waitcnt lgkmcnt(5)\n"
      "ds_read_b128 v[160:163], v0 offset:6144\n"
      "v_and_b32 v177, 0xffff0000, v194\n"
      "v_mfma_f32_16x16x32_bf16 v[184:187], v[60:63], v[168:171], v[184:187]\n"
      "s_sub_u32 s20, s20, 1\n"
      "s_cbranch_scc0 .Lv_loop\n"
      ".Lv_tail:\n"
      "s_nop 8\n"
      "v_add_f32 v218, v218, v184\n"
      "v_add_f32 v218, v218, v185\n"
      "v_fmac_f32 v219, v183, v183\n"
      "v_fmac_f32 v219, v184, v184\n"
      "v_cvt_pk_bf16_f32 v215, v186, v187\n"
      "v_add_f32 v218, v218, v186\n"
      "v_add_f32 v218, v218, v187\n"
      "v_fmac_f32 v219, v185, v185\n"
      "v_cvt_pk_bf16_f32 v214, v184, v185\n"
      "ds_write_b32 v18, v218\n"
      "v_fmac_f32 v219, v186, v186\n"
      "buffer_store_dwordx4 v[212:215], v15, s[8:11], s38 offen offset:1024 sc1\n"
      "v_fmac_f32 v219, v187, v187\n"
      "ds_write_b32 v18, v219 offset:4\n"
      "s_waitcnt lgkmcnt(0)\n"
      "s_barrier\n"
      "s_add_u32 s38, s38, 24576\n"
      "ds_read_b128 v[204:207], v19\n"
      "s_add_u32 s49, s49, 24576\n"
      "s_waitcnt lgkmcnt(0)\n"
      "v_add_f32 v204, v204, v206\n"
      "v_add_f32 v205, v205, v207\n"
      "s_nop 0\n"
      "v_add_f32_dpp v204, v204, v204 quad_perm:[1,0,3,2] row_mask:0xf bank_mask:0xf\n"
      "s_nop 1\n"
      "v_add_f32_dpp v204, v204, v204 quad_perm:[2,3,0,1] row_mask:0xf bank_mask:0xf\n"
      "v_add_f32_dpp v205, v205, v205 quad_perm:[1,0,3,2] row_mask:0xf bank_mask:0xf\n"
      "s_add_u32 s50, s50, 24576\n"
      "s_nop 0\n"
      "v_add_f32_dpp v205, v205, v205 quad_perm:[2,3,0,1] row_mask:0xf bank_mask:0xf\n"
      "s_nop 1\n"
      "v_add_f32_dpp v205, v205, v205 row_half_mirror row_mask:0xf bank_mask:0xf\n"
      "v_add_f32_dpp v204, v204, v204 row_half_mirror row_mask:0xf bank_mask:0xf\n"
      "v_mul_f32 v204, v27, v204\n"
      "v_fmaak_f32 v205, v27, v205, 0x36a7c5ac\n"
      "s_waitcnt vmcnt(3)\n"
      "buffer_store_dword v25, v12, s[8:11], s50 offen sc1\n"
      "buffer_store_dwordx2 v[204:205], v20, s[8:11], s49 offen sc1\n"
      "s_add_u32 s49, s49, 24576\n"
      "s_add_u32 s50, s50, 24576\n"
      "s_waitcnt vmcnt(0)\n"
      "buffer_store_dword v25, v12, s[8:11], s50 offen sc1\n"
      "s_add_u32 s50, s50, 24576\n"
      "s_endpgm\n"
      ".Lsp_w0_1:\n"
      "s_mov_b32 s22, s23\n"
      ".Lsl_w0_1:\n"
      "buffer_load_dword v28, v11, s[8:11], s46 offen sc1\n"
      "s_waitcnt vmcnt(0)\n"
      "v_cmp_ne_u32 vcc, s21, v28\n"
      "s_cbranch_vccz .Lbk_w0_1\n"
      "s_sleep 1\n"
      "s_sub_u32 s22, s22, 1\n"
      "s_cbranch_scc0 .Lsl_w0_1\n"
      "s_mov_b32 s23, 0\n"
      "s_branch .Lbk_w0_1\n"
      ".Lsp_w1_2:\n"
      "s_mov_b32 s22, s23\n"
      ".Lsl_w1_2:\n"
      "buffer_load_dword v28, v11, s[8:11], s46 offen sc1\n"
      "s_waitcnt vmcnt(0)\n"
      "v_cmp_ne_u32 vcc, s21, v28\n"
      "s_cbranch_vccz .Lbk_w1_2\n"
      "s_sleep 1\n"
      "s_sub_u32 s22, s22, 1\n"
      "s_cbranch_scc0 .Lsl_w1_2\n"
      "s_mov_b32 s23, 0\n"
      "s_branch .Lbk_w1_2\n"
      ".Lsp_wy0_3:\n"
      "s_lshl_b32 s22, s23, 4\n"
      ".Lsl_wy0_3:\n"
      "ds_read_b32 v22, v24\n"
      "s_waitcnt lgkmcnt(0)\n"
      "v_cmp_gt_i32 vcc, v21, v22\n"
      "s_cbranch_vccz .Lbk_wy0_3\n"
      "s_sub_u32 s22, s22, 1\n"
      "s_cbranch_scc0 .Lsl_wy0_3\n"
      "s_mov_b32 s23, 0\n"
      "s_branch .Lbk_wy0_3\n"
      ".Lsp_w0_4:\n"
      "s_mov_b32 s22, s23\n"
      ".Lsl_w0_4:\n"
      "buffer_load_dword v28, v11, s[8:11], s46 offen sc1\n"
      "s_waitcnt vmcnt(0)\n"
      "v_cmp_ne_u32 vcc, s21, v28\n"
      "s_cbranch_vccz .Lbk_w0_4\n"
      "s_sleep 1\n"
      "s_sub_u32 s22, s22, 1\n"
      "s_cbranch_scc0 .Lsl_w0_4\n"
      "s_mov_b32 s23, 0\n"
      "s_branch .Lbk_w0_4\n"
      ".Lsp_wz0_5:\n"
      "s_lshl_b32 s22, s23, 4\n"
      ".Lsl_wz0_5:\n"
      "ds_read_b32 v22, v24\n"
      "s_waitcnt lgkmcnt(0)\n"
      "v_cmp_gt_i32 vcc, v21, v22\n"
      "s_cbranch_vccz .Lbk_wz0_5\n"
      "s_sub_u32 s22, s22, 1\n"
      "s_cbranch_scc0 .Lsl_wz0_5\n"
      "s_mov_b32 s23, 0\n"
      "s_branch .Lbk_wz0_5\n"
      ".Lsp_wy1_6:\n"
      "s_lshl_b32 s22, s23, 4\n"
      ".Lsl_wy1_6:\n"
      "ds_read_b32 v22, v24\n"
      "s_waitcnt lgkmcnt(0)\n"
      "v_cmp_gt_i32 vcc, v21, v22\n"
      "s_cbranch_vccz .Lbk_wy1_6\n"
      "s_sub_u32 s22, s22, 1\n"
      "s_cbranch_scc0 .Lsl_wy1_6\n"
      "s_mov_b32 s23, 0\n"
      "s_branch .Lbk_wy1_6\n"
      ".Lsp_w1_7:\n"
      "s_mov_b32 s22, s23\n"
      ".Lsl_w1_7:\n"
      "buffer_load_dword v28, v11, s[8:11], s46 offen sc1\n"
      "s_waitcnt vmcnt(0)\n"
      "v_cmp_ne_u32 vcc, s21, v28\n"
      "s_cbranch_vccz .Lbk_w1_7\n"
      "s_sleep 1\n"
      "s_sub_u32 s22, s22, 1\n"
      "s_cbranch_scc0 .Lsl_w1_7\n"
      "s_mov_b32 s23, 0\n"
      "s_branch .Lbk_w1_7\n"
      ".Lsp_wz1_8:\n"
      "s_lshl_b32 s22, s23, 4\n"
      ".Lsl_wz1_8:\n"
      "ds_read_b32 v22, v24\n"
      "s_waitcnt lgkmcnt(0)\n"
      "v_cmp_gt_i32 vcc, v21, v22\n"
      "s_cbranch_vccz .Lbk_wz1_8\n"
      "s_sub_u32 s22, s22, 1\n"
      "s_cbranch_scc0 .Lsl_wz1_8\n"
      "s_mov_b32 s23, 0\n"
      "s_branch .Lbk_wz1_8\n"
      ".Lsp_wy0_9:\n"
      "s_lshl_b32 s22, s23, 4\n"
      ".Lsl_wy0_9:\n"
      "ds_read_b32 v22, v24\n"
      "s_waitcnt lgkmcnt(0)\n"
      "v_cmp_gt_i32 vcc, v21, v22\n"
      "s_cbranch_vccz .Lbk_wy0_9\n"
      "s_sub_u32 s22, s22, 1\n"
      "s_cbranch_scc0 .Lsl_wy0_9\n"
      "s_mov_b32 s23, 0\n"
      "s_branch .Lbk_wy0_9\n"
      ".Lsp_w0_10:\n"
      "s_mov_b32 s22, s23\n"
      ".Lsl_w0_10:\n"
      "buffer_load_dword v28, v11, s[8:11], s46 offen sc1\n"
      "s_waitcnt vmcnt(0)\n"
      "v_cmp_ne_u32 vcc, s21, v28\n"
      "s_cbranch_vccz .Lbk_w0_10\n"
      "s_sleep 1\n"
      "s_sub_u32 s22, s22, 1\n"
      "s_cbranch_scc0 .Lsl_w0_10\n"
      "s_mov_b32 s23, 0\n"
      "s_branch .Lbk_w0_10\n"
      ".Lsp_wz0_11:\n"
      "s_lshl_b32 s22, s23, 4\n"
      ".Lsl_wz0_11:\n"
      "ds_read_b32 v22, v24\n"
      "s_waitcnt lgkmcnt(0)\n"
      "v_cmp_gt_i32 vcc, v21, v22\n"
      "s_cbranch_vccz .Lbk_wz0_11\n"
      "s_sub_u32 s22, s22, 1\n"
      "s_cbranch_scc0 .Lsl_wz0_11\n"
      "s_mov_b32 s23, 0\n"
      "s_branch .Lbk_wz0_11\n"
      ".Lsp_wy1_12:\n"
      "s_lshl_b32 s22, s23, 4\n"
      ".Lsl_wy1_12:\n"
      "ds_read_b32 v22, v24\n"
      "s_waitcnt lgkmcnt(0)\n"
      "v_cmp_gt_i32 vcc, v21, v22\n"
      "s_cbranch_vccz .Lbk_wy1_12\n"
      "s_sub_u32 s22, s22, 1\n"
      "s_cbranch_scc0 .Lsl_wy1_12\n"
      "s_mov_b32 s23, 0\n"
      "s_branch .Lbk_wy1_12\n"
      ".Lsp_w1_13:\n"
      "s_mov_b32 s22, s23\n"
      ".Lsl_w1_13:\n"
      "buffer_load_dword v28, v11, s[8:11], s46 offen sc1\n"
      "s_waitcnt vmcnt(0)\n"
      "v_cmp_ne_u32 vcc, s21, v28\n"
      "s_cbranch_vccz .Lbk_w1_13\n"
      "s_sleep 1\n"
      "s_sub_u32 s22, s22, 1\n"
      "s_cbranch_scc0 .Lsl_w1_13\n"
      "s_mov_b32 s23, 0\n"
      "s_branch .Lbk_w1_13\n"
      ".Lsp_wz1_14:\n"
      "s_lshl_b32 s22, s23, 4\n"
      ".Lsl_wz1_14:\n"
      "ds_read_b32 v22, v24\n"
      "s_waitcnt lgkmcnt(0)\n"
      "v_cmp_gt_i32 vcc, v21, v22\n"
      "s_cbranch_vccz .Lbk_wz1_14\n"
      "s_sub_u32 s22, s22, 1\n"
      "s_cbranch_scc0 .Lsl_wz1_14\n"
      "s_mov_b32 s23, 0\n"
      "s_branch .Lbk_wz1_14\n"
      ".Lsp_wy0_15:\n"
      "s_lshl_b32 s22, s23, 4\n"
      ".Lsl_wy0_15:\n"
      "ds_read_b32 v22, v24\n"
      "s_waitcnt lgkmcnt(0)\n"
      "v_cmp_gt_i32 vcc, v21, v22\n"
      "s_cbranch_vccz .Lbk_wy0_15\n"
      "s_sub_u32 s22, s22, 1\n"
      "s_cbranch_scc0 .Lsl_wy0_15\n"
      "s_mov_b32 s23, 0\n"
      "s_branch .Lbk_wy0_15\n"
      ".Lsp_v0_16:\n"
      "s_mov_b32 s22, s23\n"
      ".Lsl_v0_16:\n"
      "buffer_load_dword v28, v11, s[8:11], s46 offen sc1\n"
      "s_waitcnt vmcnt(0)\n"
      "v_cmp_ne_u32 vcc, s21, v28\n"
      "s_cbranch_vccz .Lbk_v0_16\n"
      "s_sleep 1\n"
      "s_sub_u32 s22, s22, 1\n"
      "s_cbranch_scc0 .Lsl_v0_16\n"
      "s_mov_b32 s23, 0\n"
      "s_branch .Lbk_v0_16\n"
      ".Lsp_v1_17:\n"
      "s_mov_b32 s22, s23\n"
      ".Lsl_v1_17:\n"
      "buffer_load_dword v28, v11, s[8:11], s46 offen sc1\n"
      "s_waitcnt vmcnt(0)\n"
      "v_cmp_ne_u32 vcc, s21, v28\n"
      "s_cbranch_vccz .Lbk_v1_17\n"
      "s_sleep 1\n"
      "s_sub_u32 s22, s22, 1\n"
      "s_cbranch_scc0 .Lsl_v1_17\n"
      "s_mov_b32 s23, 0\n"
      "s_branch .Lbk_v1_17\n"
      ".Lsp_vy0_18:\n"
      "s_lshl_b32 s22, s23, 4\n"
      ".Lsl_vy0_18:\n"
      "ds_read_b32 v22, v24\n"
      "s_waitcnt lgkmcnt(0)\n"
      "v_cmp_gt_i32 vcc, v21, v22\n"
      "s_cbranch_vccz .Lbk_vy0_18\n"
      "s_sub_u32 s22, s22, 1\n"
      "s_cbranch_scc0 .Lsl_vy0_18\n"
      "s_mov_b32 s23, 0\n"
      "s_branch .Lbk_vy0_18\n"
      ".Lsp_v0_19:\n"
      "s_mov_b32 s22, s23\n"
      ".Lsl_v0_19:\n"
      "buffer_load_dword v28, v11, s[8:11], s46 offen sc1\n"
      "s_waitcnt vmcnt(0)\n"
      "v_cmp_ne_u32 vcc, s21, v28\n"
      "s_cbranch_vccz .Lbk_v0_19\n"
      "s_sleep 1\n"
      "s_sub_u32 s22, s22, 1\n"
      "s_cbranch_scc0 .Lsl_v0_19\n"
      "s_mov_b32 s23, 0\n"
      "s_branch .Lbk_v0_19\n"
      ".Lsp_vz0_20:\n"
      "s_lshl_b32 s22, s23, 4\n"
      ".Lsl_vz0_20:\n"
      "ds_read_b32 v22, v24\n"
      "s_waitcnt lgkmcnt(0)\n"
      "v_cmp_gt_i32 vcc, v21, v22\n"
      "s_cbranch_vccz .Lbk_vz0_20\n"
      "s_sub_u32 s22, s22, 1\n"
      "s_cbranch_scc0 .Lsl_vz0_20\n"
      "s_mov_b32 s23, 0\n"
      "s_branch .Lbk_vz0_20\n"
      ".Lsp_vy1_21:\n"
      "s_lshl_b32 s22, s23, 4\n"
      ".Lsl_vy1_21:\n"
      "ds_read_b32 v22, v24\n"
      "s_waitcnt lgkmcnt(0)\n"
      "v_cmp_gt_i32 vcc, v21, v22\n"
      "s_cbranch_vccz .Lbk_vy1_21\n"
      "s_sub_u32 s22, s22, 1\n"
      "s_cbranch_scc0 .Lsl_vy1_21\n"
      "s_mov_b32 s23, 0\n"
      "s_branch .Lbk_vy1_21\n"
      ".Lsp_v1_22:\n"
      "s_mov_b32 s22, s23\n"
      ".Lsl_v1_22:\n"
      "buffer_load_dword v28, v11, s[8:11], s46 offen sc1\n"
      "s_waitcnt vmcnt(0)\n"
      "v_cmp_ne_u32 vcc, s21, v28\n"
      "s_cbranch_vccz .Lbk_v1_22\n"
      "s_sleep 1\n"
      "s_sub_u32 s22, s22, 1\n"
      "s_cbranch_scc0 .Lsl_v1_22\n"
      "s_mov_b32 s23, 0\n"
      "s_branch .Lbk_v1_22\n"
      ".Lsp_vz1_23:\n"
      "s_lshl_b32 s22, s23, 4\n"
      ".Lsl_vz1_23:\n"
      "ds_read_b32 v22, v24\n"
      "s_waitcnt lgkmcnt(0)\n"
      "v_cmp_gt_i32 vcc, v21, v22\n"
      "s_cbranch_vccz .Lbk_vz1_23\n"
      "s_sub_u32 s22, s22, 1\n"
      "s_cbranch_scc0 .Lsl_vz1_23\n"
      "s_mov_b32 s23, 0\n"
      "s_branch .Lbk_vz1_23\n"
      ".Lsp_vy0_24:\n"
      "s_lshl_b32 s22, s23, 4\n"
      ".Lsl_vy0_24:\n"
      "ds_read_b32 v22, v24\n"
      "s_waitcnt lgkmcnt(0)\n"
      "v_cmp_gt_i32 vcc, v21, v22\n"
      "s_cbranch_vccz .Lbk_vy0_24\n"
      "s_sub_u32 s22, s22, 1\n"
      "s_cbranch_scc0 .Lsl_vy0_24\n"
      "s_mov_b32 s23, 0\n"
      "s_branch .Lbk_vy0_24\n"
      ".Lsp_v0_25:\n"
      "s_mov_b32 s22, s23\n"
      ".Lsl_v0_25:\n"
      "buffer_load_dword v28, v11, s[8:11], s46 offen sc1\n"
      "s_waitcnt vmcnt(0)\n"
      "v_cmp_ne_u32 vcc, s21, v28\n"
      "s_cbranch_vccz .Lbk_v0_25\n"
      "s_sleep 1\n"
      "s_sub_u32 s22, s22, 1\n"
      "s_cbranch_scc0 .Lsl_v0_25\n"
      "s_mov_b32 s23, 0\n"
      "s_branch .Lbk_v0_25\n"
      ".Lsp_vz0_26:\n"
      "s_lshl_b32 s22, s23, 4\n"
      ".Lsl_vz0_26:\n"
      "ds_read_b32 v22, v24\n"
      "s_waitcnt lgkmcnt(0)\n"
      "v_cmp_gt_i32 vcc, v21, v22\n"
      "s_cbranch_vccz .Lbk_vz0_26\n"
      "s_sub_u32 s22, s22, 1\n"
      "s_cbranch_scc0 .Lsl_vz0_26\n"
      "s_mov_b32 s23, 0\n"
      "s_branch .Lbk_vz0_26\n"
      ".Lsp_vy1_27:\n"
      "s_lshl_b32 s22, s23, 4\n"
      ".Lsl_vy1_27:\n"
      "ds_read_b32 v22, v24\n"
      "s_waitcnt lgkmcnt(0)\n"
      "v_cmp_gt_i32 vcc, v21, v22\n"
      "s_cbranch_vccz .Lbk_vy1_27\n"
      "s_sub_u32 s22, s22, 1\n"
      "s_cbranch_scc0 .Lsl_vy1_27\n"
      "s_mov_b32 s23, 0\n"
      "s_branch .Lbk_vy1_27\n"
      ".Lsp_v1_28:\n"
      "s_mov_b32 s22, s23\n"
      ".Lsl_v1_28:\n"
      "buffer_load_dword v28, v11, s[8:11], s46 offen sc1\n"
      "s_waitcnt vmcnt(0)\n"
      "v_cmp_ne_u32 vcc, s21, v28\n"
      "s_cbranch_vccz .Lbk_v1_28\n"
      "s_sleep 1\n"
      "s_sub_u32 s22, s22, 1\n"
      "s_cbranch_scc0 .Lsl_v1_28\n"
      "s_mov_b32 s23, 0\n"
      "s_branch .Lbk_v1_28\n"
      ".Lsp_vz1_29:\n"
      "s_lshl_b32 s22, s23, 4\n"
      ".Lsl_vz1_29:\n"
      "ds_read_b32 v22, v24\n"
      "s_waitcnt lgkmcnt(0)\n"
      "v_cmp_gt_i32 vcc, v21, v22\n"
      "s_cbranch_vccz .Lbk_vz1_29\n"
      "s_sub_u32 s22, s22, 1\n"
      "s_cbranch_scc0 .Lsl_vz1_29\n"
      "s_mov_b32 s23, 0\n"
      "s_branch .Lbk_vz1_29\n"
      ".Lsp_vy0_30:\n"
      "s_lshl_b32 s22, s23, 4\n"
      ".Lsl_vy0_30:\n"
      "ds_read_b32 v22, v24\n"
      "s_waitcnt lgkmcnt(0)\n"
      "v_cmp_gt_i32 vcc, v21, v22\n"
      "s_cbranch_vccz .Lbk_vy0_30\n"
      "s_sub_u32 s22, s22, 1\n"
      "s_cbranch_scc0 .Lsl_vy0_30\n"
      "s_mov_b32 s23, 0\n"
      "s_branch .Lbk_vy0_30\n"
      ".Lw_last:\n"
      :
      : "{s[8:11]}"(rsWS), "{s[12:15]}"(rsA), "{v0}"(v_l16), "{v1}"(v_l5), "{v2}"(v_ex[0]), "{v3}"(v_ex[1]), "{v4}"(v_ex[2]), "{v5}"(v_ex[3]), "{v6}"(v_ex[4]), "{v7}"(v_ex[5]), "{v8}"(v_ex[6]), "{v9}"(v_ex[7]), "{v10}"(v_l2), "{v11}"(v_fa), "{v12}"(v_ra), "{v13}"(v_rp), "{v14}"(v_rs), "{v15}"(v_xp), "{v16}"(v_xs), "{v17}"(v_ew), "{v18}"(v_sw), "{v19}"(v_sr), "{v20}"(v_ss), "{v22}"(v_al), "{v23}"(v_yw), "{v24}"(v_yr), "{v26}"(v_hi), "{v100}"(v_vo[0]), "{v101}"(v_vo[1]), "{v102}"(v_vo[2]), "{v103}"(v_vo[3]), "{v104}"(v_vo[4]), "{v105}"(v_vo[5]), "{v106}"(v_vo[6]), "{v107}"(v_vo[7]), "{v108}"(v_vo[8]), "{v109}"(v_vp[0]), "{v110}"(v_vp[1]), "{v111}"(v_vp[2]), "{v112}"(v_vp[3]), "{v113}"(v_vp[4]), "{v114}"(v_vp[5]), "{v115}"(v_vp[6]), "{v116}"(v_vp[7]), "{v117}"(v_vp[8]), "{v118}"(v_t0), "{v119}"(v_t1), "{v120}"(v_ts), "{v121}"(v_pw), "{s36}"(s_sx), "{s37}"(s_ev), "{s38}"(s_sy), "{s39}"(s_m4), "{s40}"(s_m5), "{s41}"(s_m9), "{s42}"(s_so), "{s43}"(s_s8), "{s44}"(s_wg), "{s45}"(s_rb)
      : "v255", "a255", "s99", "vcc", "scc", "memory");

  } else if (fast && slot < 28) {
    const unsigned td = (slot - 10) / 3, gr = td < 4 ? td : (e + 5 - td) & 3, part = (td < 4 ? e : td) * 3 + (slot - 10) % 3;
    COMMON(40, 149760);
    const unsigned ks = part / 3, n3 = part % 3;
    const u32x4 rsA = srd(Wd, 4608 * 576 * 2), rsB = srd(out, 2048 * 576 * 2), rsC = srd(X, 2048 * 576 * 2);
    const unsigned v_l16b = v_l16 + 4096, v_l8 = 1024 + lane * 8, f0 = 24 * ks + 6 * w;
    const unsigned v_fa = 147456 - f0 * 1024 + (f0 + (l8 < 5 ? l8 : 5)) * 128 + l7 / 4, v_ga = (l8 < 4 ? l8 : 4) * 4 * PW + 1536 + l7 / 4;
    const unsigned v_ra = up * 6 * PREC + l5 + 1536, v_rf = up * 33 * REC + l5 + 20480 + (4 * n3 + w) * 128;
    const unsigned v_pw = l8 < 5 ? (l8 + 7) * PREC + l8 * 4 * PW + 1536 + l7 : OOB, v_pu = ks && l8 + ks < 5 ? OOB : v_pw - 6 * PREC;
    const unsigned pf = 3 * (w >> 1) + 2 * (w & 1), sf = 3 * (w >> 1) + 1;
    const unsigned v_at = (8 * g + q) * 384 + 64 * pf + 16 * p, v_as = (8 * g + q) * 384 + 64 * sf + 32 * (w & 1) + 8 * p;
    auto vo = [&](unsigned i) { const unsigned c = 64 * i + lane; return (8 * w + c / 24) * 1152 + 384 * n3 + c % 24 * 16 - 1024 * i; };
    const unsigned v_vo0 = vo(0), v_vo1 = vo(1), v_vo2 = vo(2);
    const unsigned v_xp = (6 * n3 + pf) * 1024 + v_l16, v_xs = (6 * n3 + sf) * 1024 + (w & 1) * 512 + half;
    const unsigned v_op = i16 * 1152 + 384 * n3 + 64 * pf + 16 * g, v_os = i16 * 1152 + 384 * n3 + 64 * sf + 32 * (w & 1) + 8 * g;
    const unsigned v_sw = 147456 + i16 * 136 + (4 * w + g) * 8, v_sr = 147456 + (4 * w + g) * 136 + i16 * 8;
    const unsigned v_ss = 18432 + n3 * 512 + w * 128 + g * 32 + p * 8;
    const unsigned s_ph = ks ? 6 - ks : 0, s_p0 = 0u - s_ph, r0 = gr * NREC + s_p0;
    const unsigned s_sh = WS_H + r0 * HREC + f0 * 1024, s_ps = WS_PS + r0 * PREC + (n3 * 20 + w) * PW;
    const unsigned s_x1 = WS_X1 + r0 * REC, s_xr = WS_XIN + (r0 + 33) * REC, s_or = (32 * gr + 63 - s_p0) * 18432;
    const unsigned s_k0 = ROT * 6 * gr;
    const unsigned s_m3 = w * 3072, s_m6 = w * 6144, s_so = ks * 768 * 1152;
    const unsigned s_i0 = (32 * gr + 31 - ks) * 18432, s_i5 = ks < 2 ? s_i0 - 30 * 18432 : OOB;
    const unsigned s_j0 = WS_XIN + (gr * NREC + ks) * REC, s_j5 = ks < 2 ? s_j0 + 30 * REC : OOB, s_he = s_sh + (63 + s_ph) * HREC;
    auto pe = [&](unsigned i) { return ks == 3 - i ? OOB : s_ps + (63 - i + s_ph) * PREC + ((s_ph + 8 - i) % 6) * 4 * PW; };
    const unsigned s_pe[3] = {pe(0), pe(1), pe(2)};
    asm volatile(
      "s_buffer_load_dword s21, s[8:11], 0\n"
      "s_mov_b32 s23, 65536\n"
      "s_mov_b32 s31, 0x40000000\n"
      "buffer_load_dwordx4 v[56:59], v8, s[92:95], s46 offen\n"
      "buffer_load_dwordx2 v[60:61], v9, s[92:95], s46 offen\n"
      "s_waitcnt lgkmcnt(0)\n"
      "s_mul_i32 s21, s21, 1664525\n"
      "s_add_u32 s21, s21, 1013904223\n"
      "v_mov_b32 v18, s21\n"
      "v_mov_b32 v21, v18\n"
      "v_not_b32 v224, v18\n"
      "v_not_b32 v225, v18\n"
      "v_not_b32 v226, v18\n"
      "v_not_b32 v227, v18\n"
      "v_xor_b32 v18, v18, v19\n"
      "s_waitcnt vmcnt(0)\n"
      "buffer_store_dwordx4 v[56:59], v6, s[8:11], s48 offen sc1\n"
      "buffer_store_dwordx2 v[60:61], v7, s[8:11], s48 offen sc1\n"
      "v_lshlrev_b32 v164, 16, v56\n"
      "v_and_b32 v165, 0xffff0000, v56\n"
      "v_lshlrev_b32 v166, 16, v57\n"
      "v_and_b32 v167, 0xffff0000, v57\n"
      "v_lshlrev_b32 v168, 16, v58\n"
      "v_and_b32 v169, 0xffff0000, v58\n"
      "v_lshlrev_b32 v170, 16, v59\n"
      "v_and_b32 v171, 0xffff0000, v59\n"
      "v_lshlrev_b32 v172, 16, v60\n"
      "v_and_b32 v173, 0xffff0000, v60\n"
      "v_lshlrev_b32 v174, 16, v61\n"
      "v_and_b32 v175, 0xffff0000, v61\n"
      "v_add_f32 v62, v164, v165\n"
      "v_mul_f32 v63, v164, v164\n"
      "v_fmac_f32 v63, v165, v165\n"
      "v_add_f32 v62, v62, v166\n"
      "v_fmac_f32 v63, v166, v166\n"
      "v_add_f32 v62, v62, v167\n"
      "v_fmac_f32 v63, v167, v167\n"
      "v_add_f32 v62, v62, v168\n"
      "v_fmac_f32 v63, v168, v168\n"
      "v_add_f32 v62, v62, v169\n"
      "v_fmac_f32 v63, v169, v169\n"
      "v_add_f32 v62, v62, v170\n"
      "v_fmac_f32 v63, v170, v170\n"
      "v_add_f32 v62, v62, v171\n"
      "v_fmac_f32 v63, v171, v171\n"
      "v_add_f32 v62, v62, v172\n"
      "v_fmac_f32 v63, v172, v172\n"
      "v_add_f32 v62, v62, v173\n"
      "v_fmac_f32 v63, v173, v173\n"
      "v_add_f32 v62, v62, v174\n"
      "v_fmac_f32 v63, v174, v174\n"
      "v_add_f32 v62, v62, v175\n"
      "v_fmac_f32 v63, v175, v175\n"
      "ds_write_b64 v10, v[62:63] offset:0\n"
      "s_waitcnt lgkmcnt(0)\n"
      "s_barrier\n"
      "ds_read_b64 v[152:153], v11 offset:0\n"
      "s_waitcnt lgkmcnt(0)\n"
      "v_add_f32_dpp v152, v152, v152 row_ror:8 row_mask:0xf bank_mask:0xf\n"
      "v_add_f32_dpp v153, v153, v153 row_ror:8 row_mask:0xf bank_mask:0xf\n"
      "s_nop 0\n"
      "v_add_f32_dpp v152, v152, v152 row_ror:4 row_mask:0xf bank_mask:0xf\n"
      "v_add_f32_dpp v153, v153, v153 row_ror:4 row_mask:0xf bank_mask:0xf\n"
      "s_nop 0\n"
      "v_add_f32_dpp v152, v152, v152 row_ror:2 row_mask:0xf bank_mask:0xf\n"
      "v_add_f32_dpp v153, v153, v153 row_ror:2 row_mask:0xf bank_mask:0xf\n"
      "s_nop 0\n"
      "v_add_f32_dpp v152, v152, v152 row_ror:1 row_mask:0xf bank_mask:0xf\n"
      "v_add_f32_dpp v153, v153, v153 row_ror:1 row_mask:0xf bank_mask:0xf\n"
      "v_mul_f32 v152, 0x3ae38e39, v152\n"
      "v_add_f32 v153, 0x3afba882, v153\n"
      "v_mul_f32 v153, 0x3ae38e39, v153\n"
      "buffer_store_dwordx2 v[152:153], v12, s[8:11], s48 offen sc1\n"
      "s_add_u32 s30, s46, 4294856704\n"
      "buffer_load_dwordx4 v[64:67], v8, s[92:95], s30 offen\n"
      "buffer_load_dwordx2 v[68:69], v9, s[92:95], s30 offen\n"
      "s_add_u32 s30, s46, 4294746112\n"
      "buffer_load_dwordx4 v[72:75], v8, s[92:95], s30 offen\n"
      "buffer_load_dwordx2 v[76:77], v9, s[92:95], s30 offen\n"
      "s_waitcnt vmcnt(4)\n"
      "buffer_store_dword v18, v5, s[8:11], s48 offen sc1\n"
      "s_add_u32 s30, s46, 4294635520\n"
      "buffer_load_dwordx4 v[80:83], v8, s[92:95], s30 offen\n"
      "buffer_load_dwordx2 v[84:85], v9, s[92:95], s30 offen\n"
      "s_add_u32 s30, s46, 4294524928\n"
      "buffer_load_dwordx4 v[88:91], v8, s[92:95], s30 offen\n"
      "buffer_load_dwordx2 v[92:93], v9, s[92:95], s30 offen\n"
      "buffer_load_dwordx4 v[96:99], v8, s[92:95], s47 offen\n"
      "buffer_load_dwordx2 v[100:101], v9, s[92:95], s47 offen\n"
      "buffer_store_dword v18, v15, s[8:11], 0 offen sc1\n"
      "s_cmp_eq_u32 s45, 0\n"
      "s_cselect_b32 s30, 0x40000000, s37\n"
      "buffer_store_dword v21, v3, s[8:11], s30 offen sc1\n"
      "s_waitcnt vmcnt(11)\n"
      "s_add_u32 s30, s48, 147456\n"
      "buffer_store_dwordx4 v[64:67], v6, s[8:11], s30 offen sc1\n"
      "buffer_store_dwordx2 v[68:69], v7, s[8:11], s30 offen sc1\n"
      "v_lshlrev_b32 v164, 16, v64\n"
      "v_and_b32 v165, 0xffff0000, v64\n"
      "v_lshlrev_b32 v166, 16, v65\n"
      "v_and_b32 v167, 0xffff0000, v65\n"
      "v_lshlrev_b32 v168, 16, v66\n"
      "v_and_b32 v169, 0xffff0000, v66\n"
      "v_lshlrev_b32 v170, 16, v67\n"
      "v_and_b32 v171, 0xffff0000, v67\n"
      "v_lshlrev_b32 v172, 16, v68\n"
      "v_and_b32 v173, 0xffff0000, v68\n"
      "v_lshlrev_b32 v174, 16, v69\n"
      "v_and_b32 v175, 0xffff0000, v69\n"
      "v_add_f32 v70, v164, v165\n"
      "v_mul_f32 v71, v164, v164\n"
      "v_fmac_f32 v71, v165, v165\n"
      "v_add_f32 v70, v70, v166\n"
      "v_fmac_f32 v71, v166, v166\n"
      "v_add_f32 v70, v70, v167\n"
      "v_fmac_f32 v71, v167, v167\n"
      "v_add_f32 v70, v70, v168\n"
      "v_fmac_f32 v71, v168, v168\n"
      "v_add_f32 v70, v70, v169\n"
      "v_fmac_f32 v71, v169, v169\n"
      "v_add_f32 v70, v70, v170\n"
      "v_fmac_f32 v71, v170, v170\n"
      "v_add_f32 v70, v70, v171\n"
      "v_fmac_f32 v71, v171, v171\n"
      "v_add_f32 v70, v70, v172\n"
      "v_fmac_f32 v71, v172, v172\n"
      "v_add_f32 v70, v70, v173\n"
      "v_fmac_f32 v71, v173, v173\n"
      "v_add_f32 v70, v70, v174\n"
      "v_fmac_f32 v71, v174, v174\n"
      "v_add_f32 v70, v70, v175\n"
      "v_fmac_f32 v71, v175, v175\n"
      "ds_write_b64 v10, v[70:71] offset:2176\n"
      "s_waitcnt vmcnt(11)\n"
      "s_add_u32 s30, s48, 294912\n"
      "buffer_store_dwordx4 v[72:75], v6, s[8:11], s30 offen sc1\n"
      "buffer_store_dwordx2 v[76:77], v7, s[8:11], s30 offen sc1\n"
      "v_lshlrev_b32 v164, 16, v72\n"
      "v_and_b32 v165, 0xffff0000, v72\n"
      "v_lshlrev_b32 v166, 16, v73\n"
      "v_and_b32 v167, 0xffff0000, v73\n"
      "v_lshlrev_b32 v168, 16, v74\n"
      "v_and_b32 v169, 0xffff0000, v74\n"
      "v_lshlrev_b32 v170, 16, v75\n"
      "v_and_b32 v171, 0xffff0000, v75\n"
      "v_lshlrev_b32 v172, 16, v76\n"
      "v_and_b32 v173, 0xffff0000, v76\n"
      "v_lshlrev_b32 v174, 16, v77\n"
      "v_and_b32 v175, 0xffff0000, v77\n"
      "v_add_f32 v78, v164, v165\n"
      "v_mul_f32 v79, v164, v164\n"
      "v_fmac_f32 v79, v165, v165\n"
      "v_add_f32 v78, v78, v166\n"
      "v_fmac_f32 v79, v166, v166\n"
      "v_add_f32 v78, v78, v167\n"
      "v_fmac_f32 v79, v167, v167\n"
      "v_add_f32 v78, v78, v168\n"
      "v_fmac_f32 v79, v168, v168\n"
      "v_add_f32 v78, v78, v169\n"
      "v_fmac_f32 v79, v169, v169\n"
      "v_add_f32 v78, v78, v170\n"
      "v_fmac_f32 v79, v170, v170\n"
      "v_add_f32 v78, v78, v171\n"
      "v_fmac_f32 v79, v171, v171\n"
      "v_add_f32 v78, v78, v172\n"
      "v_fmac_f32 v79, v172, v172\n"
      "v_add_f32 v78, v78, v173\n"
      "v_fmac_f32 v79, v173, v173\n"
      "v_add_f32 v78, v78, v174\n"
      "v_fmac_f32 v79, v174, v174\n"
      "v_add_f32 v78, v78, v175\n"
      "v_fmac_f32 v79, v175, v175\n"
      "ds_write_b64 v10, v[78:79] offset:4352\n"
      "s_waitcnt vmcnt(10)\n"
      "s_add_u32 s30, s48, 442368\n"
      "buffer_store_dwordx4 v[80:83], v6, s[8:11], s30 offen sc1\n"
      "buffer_store_dwordx2 v[84:85], v7, s[8:11], s30 offen sc1\n"
      "v_lshlrev_b32 v164, 16, v80\n"
      "v_and_b32 v165, 0xffff0000, v80\n"
      "v_lshlrev_b32 v166, 16, v81\n"
      "v_and_b32 v167, 0xffff0000, v81\n"
      "v_lshlrev_b32 v168, 16, v82\n"
      "v_and_b32 v169, 0xffff0000, v82\n"
      "v_lshlrev_b32 v170, 16, v83\n"
      "v_and_b32 v171, 0xffff0000, v83\n"
      "v_lshlrev_b32 v172, 16, v84\n"
      "v_and_b32 v173, 0xffff0000, v84\n"
      "v_lshlrev_b32 v174, 16, v85\n"
      "v_and_b32 v175, 0xffff0000, v85\n"
      "v_add_f32 v86, v164, v165\n"
      "v_mul_f32 v87, v164, v164\n"
      "v_fmac_f32 v87, v165, v165\n"
      "v_add_f32 v86, v86, v166\n"
      "v_fmac_f32 v87, v166, v166\n"
      "v_add_f32 v86, v86, v167\n"
      "v_fmac_f32 v87, v167, v167\n"
      "v_add_f32 v86, v86, v168\n"
      "v_fmac_f32 v87, v168, v168\n"
      "v_add_f32 v86, v86, v169\n"
      "v_fmac_f32 v87, v169, v169\n"
      "v_add_f32 v86, v86, v170\n"
      "v_fmac_f32 v87, v170, v170\n"
      "v_add_f32 v86, v86, v171\n"
      "v_fmac_f32 v87, v171, v171\n"
      "v_add_f32 v86, v86, v172\n"
      "v_fmac_f32 v87, v172, v172\n"
      "v_add_f32 v86, v86, v173\n"
      "v_fmac_f32 v87, v173, v173\n"
      "v_add_f32 v86, v86, v174\n"
      "v_fmac_f32 v87, v174, v174\n"
      "v_add_f32 v86, v86, v175\n"
      "v_fmac_f32 v87, v175, v175\n"
      "ds_write_b64 v10, v[86:87] offset:6528\n"
      "s_waitcnt vmcnt(10)\n"
      "s_add_u32 s30, s48, 589824\n"
      "buffer_store_dwordx4 v[88:91], v6, s[8:11], s30 offen sc1\n"
      "buffer_store_dwordx2 v[92:93], v7, s[8:11], s30 offen sc1\n"
      "v_lshlrev_b32 v164, 16, v88\n"
      "v_and_b32 v165, 0xffff0000, v88\n"
      "v_lshlrev_b32 v166, 16, v89\n"
      "v_and_b32 v167, 0xffff0000, v89\n"
      "v_lshlrev_b32 v168, 16, v90\n"
      "v_and_b32 v169, 0xffff0000, v90\n"
      "v_lshlrev_b32 v170, 16, v91\n"
      "v_and_b32 v171, 0xffff0000, v91\n"
      "v_lshlrev_b32 v172, 16, v92\n"
      "v_and_b32 v173, 0xffff0000, v92\n"
      "v_lshlrev_b32 v174, 16, v93\n"
      "v_and_b32 v175, 0xffff0000, v93\n"
      "v_add_f32 v94, v164, v165\n"
      "v_mul_f32 v95, v164, v164\n"
      "v_fmac_f32 v95, v165, v165\n"
      "v_add_f32 v94, v94, v166\n"
      "v_fmac_f32 v95, v166, v166\n"
      "v_add_f32 v94, v94, v167\n"
      "v_fmac_f32 v95, v167, v167\n"
      "v_add_f32 v94, v94, v168\n"
      "v_fmac_f32 v95, v168, v168\n"
      "v_add_f32 v94, v94, v169\n"
      "v_fmac_f32 v95, v169, v169\n"
      "v_add_f32 v94, v94, v170\n"
      "v_fmac_f32 v95, v170, v170\n"
      "v_add_f32 v94, v94, v171\n"
      "v_fmac_f32 v95, v171, v171\n"
      "v_add_f32 v94, v94, v172\n"
      "v_fmac_f32 v95, v172, v172\n"
      "v_add_f32 v94, v94, v173\n"
      "v_fmac_f32 v95, v173, v173\n"
      "v_add_f32 v94, v94, v174\n"
      "v_fmac_f32 v95, v174, v174\n"
      "v_add_f32 v94, v94, v175\n"
      "v_fmac_f32 v95, v175, v175\n"
      "ds_write_b64 v10, v[94:95] offset:8704\n"
      "s_waitcnt vmcnt(10)\n"
      "buffer_store_dwordx4 v[96:99], v6, s[8:11], s49 offen sc1\n"
      "buffer_store_dwordx2 v[100:101], v7, s[8:11], s49 offen sc1\n"
      "v_lshlrev_b32 v164, 16, v96\n"
      "v_and_b32 v165, 0xffff0000, v96\n"
      "v_lshlrev_b32 v166, 16, v97\n"
      "v_and_b32 v167, 0xffff0000, v97\n"
      "v_lshlrev_b32 v168, 16, v98\n"
      "v_and_b32 v169, 0xffff0000, v98\n"
      "v_lshlrev_b32 v170, 16, v99\n"
      "v_and_b32 v171, 0xffff0000, v99\n"
      "v_lshlrev_b32 v172, 16, v100\n"
      "v_and_b32 v173, 0xffff0000, v100\n"
      "v_lshlrev_b32 v174, 16, v101\n"
      "v_and_b32 v175, 0xffff0000, v101\n"
      "v_add_f32 v102, v164, v165\n"
      "v_mul_f32 v103, v164, v164\n"
      "v_fmac_f32 v103, v165, v165\n"
      "v_add_f32 v102, v102, v166\n"
      "v_fmac_f32 v103, v166, v166\n"
      "v_add_f32 v102, v102, v167\n"
      "v_fmac_f32 v103, v167, v167\n"
      "v_add_f32 v102, v102, v168\n"
      "v_fmac_f32 v103, v168, v168\n"
      "v_add_f32 v102, v102, v169\n"
      "v_fmac_f32 v103, v169, v169\n"
      "v_add_f32 v102, v102, v170\n"
      "v_fmac_f32 v103, v170, v170\n"
      "v_add_f32 v102, v102, v171\n"
      "v_fmac_f32 v103, v171, v171\n"
      "v_add_f32 v102, v102, v172\n"
      "v_fmac_f32 v103, v172, v172\n"
      "v_add_f32 v102, v102, v173\n"
      "v_fmac_f32 v103, v173, v173\n"
      "v_add_f32 v102, v102, v174\n"
      "v_fmac_f32 v103, v174, v174\n"
      "v_add_f32 v102, v102, v175\n"
      "v_fmac_f32 v103, v175, v175\n"
      "ds_write_b64 v10, v[102:103] offset:10880\n"
      "s_waitcnt lgkmcnt(0)\n"
      "s_barrier\n"
      "ds_read_b64 v[154:155], v11 offset:2176\n"
      "ds_read_b64 v[156:157], v11 offset:4352\n"
      "ds_read_b64 v[158:159], v11 offset:6528\n"
      "ds_read_b64 v[160:161], v11 offset:8704\n"
      "ds_read_b64 v[162:163], v11 offset:10880\n"
      "s_waitcnt lgkmcnt(0)\n"
      "v_add_f32_dpp v154, v154, v154 row_ror:8 row_mask:0xf bank_mask:0xf\n"
      "v_add_f32_dpp v155, v155, v155 row_ror:8 row_mask:0xf bank_mask:0xf\n"
      "v_add_f32_dpp v156, v156, v156 row_ror:8 row_mask:0xf bank_mask:0xf\n"
      "v_add_f32_dpp v157, v157, v157 row_ror:8 row_mask:0xf bank_mask:0xf\n"
      "v_add_f32_dpp v158, v158, v158 row_ror:8 row_mask:0xf bank_mask:0xf\n"
      "v_add_f32_dpp v159, v159, v159 row_ror:8 row_mask:0xf bank_mask:0xf\n"
      "v_add_f32_dpp v160, v160, v160 row_ror:8 row_mask:0xf bank_mask:0xf\n"
      "v_add_f32_dpp v161, v161, v161 row_ror:8 row_mask:0xf bank_mask:0xf\n"
      "v_add_f32_dpp v162, v162, v162 row_ror:8 row_mask:0xf bank_mask:0xf\n"
      "v_add_f32_dpp v163, v163, v163 row_ror:8 row_mask:0xf bank_mask:0xf\n"
      "v_add_f32_dpp v154, v154, v154 row_ror:4 row_mask:0xf bank_mask:0xf\n"
      "v_add_f32_dpp v155, v155, v155 row_ror:4 row_mask:0xf bank_mask:0xf\n"
      "v_add_f32_dpp v156, v156, v156 row_ror:4 row_mask:0xf bank_mask:0xf\n"
      "v_add_f32_dpp v157, v157, v157 row_ror:4 row_mask:0xf bank_mask:0xf\n"
      "v_add_f32_dpp v158, v158, v158 row_ror:4 row_mask:0xf bank_mask:0xf\n"
      "v_add_f32_dpp v159, v159, v159 row_ror:4 row_mask:0xf bank_mask:0xf\n"
      "v_add_f32_dpp v160, v160, v160 row_ror:4 row_mask:0xf bank_mask:0xf\n"
      "v_add_f32_dpp v161, v161, v161 row_ror:4 row_mask:0xf bank_mask:0xf\n"
      "v_add_f32_dpp v162, v162, v162 row_ror:4 row_mask:0xf bank_mask:0xf\n"
      "v_add_f32_dpp v163, v163, v163 row_ror:4 row_mask:0xf bank_mask:0xf\n"
      "v_add_f32_dpp v154, v154, v154 row_ror:2 row_mask:0xf bank_mask:0xf\n"
      "v_add_f32_dpp v155, v155, v155 row_ror:2 row_mask:0xf bank_mask:0xf\n"
      "v_add_f32_dpp v156, v156, v156 row_ror:2 row_mask:0xf bank_mask:0xf\n"
      "v_add_f32_dpp v157, v157, v157 row_ror:2 row_mask:0xf bank_mask:0xf\n"
      "v_add_f32_dpp v158, v158, v158 row_ror:2 row_mask:0xf bank_mask:0xf\n"
      "v_add_f32_dpp v159, v159, v159 row_ror:2 row_mask:0xf bank_mask:0xf\n"
      "v_add_f32_dpp v160, v160, v160 row_ror:2 row_mask:0xf bank_mask:0xf\n"
      "v_add_f32_dpp v161, v161, v161 row_ror:2 row_mask:0xf bank_mask:0xf\n"
      "v_add_f32_dpp v162, v162, v162 row_ror:2 row_mask:0xf bank_mask:0xf\n"
      "v_add_f32_dpp v163, v163, v163 row_ror:2 row_mask:0xf bank_mask:0xf\n"
      "v_add_f32_dpp v154, v154, v154 row_ror:1 row_mask:0xf bank_mask:0xf\n"
      "v_add_f32_dpp v155, v155, v155 row_ror:1 row_mask:0xf bank_mask:0xf\n"
      "v_add_f32_dpp v156, v156, v156 row_ror:1 row_mask:0xf bank_mask:0xf\n"
      "v_add_f32_dpp v157, v157, v157 row_ror:1 row_mask:0xf bank_mask:0xf\n"
      "v_add_f32_dpp v158, v158, v158 row_ror:1 row_mask:0xf bank_mask:0xf\n"
      "v_add_f32_dpp v159, v159, v159 row_ror:1 row_mask:0xf bank_mask:0xf\n"
      "v_add_f32_dpp v160, v160, v160 row_ror:1 row_mask:0xf bank_mask:0xf\n"
      "v_add_f32_dpp v161, v161, v161 row_ror:1 row_mask:0xf bank_mask:0xf\n"
      "v_add_f32_dpp v162, v162, v162 row_ror:1 row_mask:0xf bank_mask:0xf\n"
      "v_add_f32_dpp v163, v163, v163 row_ror:1 row_mask:0xf bank_mask:0xf\n"
      "v_mul_f32 v154, 0x3ae38e39, v154\n"
      "v_add_f32 v155, 0x3afba882, v155\n"
      "v_mul_f32 v155, 0x3ae38e39, v155\n"
      "v_mul_f32 v156, 0x3ae38e39, v156\n"
      "v_add_f32 v157, 0x3afba882, v157\n"
      "v_mul_f32 v157, 0x3ae38e39, v157\n"
      "v_mul_f32 v158, 0x3ae38e39, v158\n"
      "v_add_f32 v159, 0x3afba882, v159\n"
      "v_mul_f32 v159, 0x3ae38e39, v159\n"
      "v_mul_f32 v160, 0x3ae38e39, v160\n"
      "v_add_f32 v161, 0x3afba882, v161\n"
      "v_mul_f32 v161, 0x3ae38e39, v161\n"
      "v_mul_f32 v162, 0x3ae38e39, v162\n"
      "v_add_f32 v163, 0x3afba882, v163\n"
      "v_mul_f32 v163, 0x3ae38e39, v163\n"
      "s_add_u32 s30, s48, 147456\n"
      "buffer_store_dwordx2 v[154:155], v12, s[8:11], s30 offen sc1\n"
      "s_add_u32 s30, s48, 294912\n"
      "buffer_store_dwordx2 v[156:157], v12, s[8:11], s30 offen sc1\n"
      "s_add_u32 s30, s48, 442368\n"
      "buffer_store_dwordx2 v[158:159], v12, s[8:11], s30 offen sc1\n"
      "s_add_u32 s30, s48, 589824\n"
      "buffer_store_dwordx2 v[160:161], v12, s[8:11], s30 offen sc1\n"
      "buffer_store_dwordx2 v[162:163], v12, s[8:11], s49 offen sc1\n"
      "s_waitcnt vmcnt(0)\n"
      "s_add_u32 s30, s48, 147456\n"
      "buffer_store_dword v18, v5, s[8:11], s30 offen sc1\n"
      "s_add_u32 s30, s48, 294912\n"
      "buffer_store_dword v18, v5, s[8:11], s30 offen sc1\n"
      "s_add_u32 s30, s48, 442368\n"
      "buffer_store_dword v18, v5, s[8:11], s30 offen sc1\n"
      "s_add_u32 s30, s48, 589824\n"
      "buffer_store_dword v18, v5, s[8:11], s30 offen sc1\n"
      "buffer_store_dword v18, v5, s[8:11], s49 offen sc1\n"
      "v_and_b32 v20, 0x40000000, v19\n"
      "v_or_b32 v5, v20, v5\n"
      "s_add_u32 s30, s37, 0\n"
      "buffer_store_dwordx4 v[224:227], v183, s[8:11], s30 offen sc1\n"
      "s_add_u32 s30, s37, 0\n"
      "buffer_store_dwordx4 v[224:227], v181, s[8:11], s30 offen sc1\n"
      "v_mov_b32 v216, -1\n"
      "v_mov_b32 v217, -1\n"
      "v_mov_b32 v218, -1\n"
      "v_mov_b32 v219, -1\n"
      "buffer_store_dwordx4 v[216:219], v0, s[8:11], s51 offen sc1\n"
      "buffer_store_dwordx2 v[216:217], v13, s[8:11], s51 offen sc1\n"
      "buffer_store_dwordx4 v[216:219], v0, s[8:11], s52 offen sc1\n"
      "buffer_store_dwordx2 v[216:217], v13, s[8:11], s52 offen sc1\n"
      "buffer_store_dwordx4 v[216:219], v0, s[8:11], s53 offen sc1\n"
      "buffer_store_dwordx2 v[216:217], v13, s[8:11], s53 offen sc1\n"
      "s_sleep 32\n"
      "s_waitcnt vmcnt(0) lgkmcnt(0)\n"
      "s_mov_b32 s64, s63\n"
      "s_mov_b32 s65, 6\n"
      ".Ld_r0:\n"
      "s_mul_i32 s66, s64, 36864\n"
      "s_add_u32 s66, s66, s44\n"
      "s_mul_i32 s30, s64, 8192\n"
      "s_lshr_b32 s30, s30, 16\n"
      "s_mul_i32 s30, s30, 8\n"
      "s_sub_u32 s30, s64, s30\n"
      "s_mul_i32 s30, s30, 12288\n"
      "s_add_u32 s30, s30, 0\n"
      "s_add_u32 m0, s42, s30\n"
      "s_add_u32 s64, s64, 1\n"
      "buffer_load_dwordx4 v178, s[12:15], s66 offen offset:0 lds\n"
      "buffer_load_dwordx4 v179, s[12:15], s66 offen offset:1024 lds\n"
      "buffer_load_dwordx4 v180, s[12:15], s66 offen offset:2048 lds\n"
      "s_cmp_eq_u32 s64, 24\n"
      "s_cselect_b32 s64, 0, s64\n"
      "s_sub_u32 s65, s65, 1\n"
      "s_cbranch_scc0 .Ld_r0\n"
      "v_add_u32 v20, 49152, v176\n"
      "v_add_u32 v21, 49152, v177\n"
      "v_add_u32 v22, 98304, v176\n"
      "v_add_u32 v23, 98304, v177\n"
      "s_mov_b32 s65, 23\n"
      "s_cmp_eq_u32 s63, 6\n"
      "s_cbranch_scc1 .Ld_rs6\n"
      "s_cmp_eq_u32 s63, 12\n"
      "s_cbranch_scc1 .Ld_rs12\n"
      "s_cmp_eq_u32 s63, 18\n"
      "s_cbranch_scc1 .Ld_rs18\n"
      ".Ld_rs0:\n"
      "s_add_u32 s66, s44, 258048\n"
      "s_cmp_ge_u32 s65, 7\n"
      "s_cselect_b32 s66, s66, s31\n"
      "s_add_u32 m0, s42, 86016\n"
      "s_waitcnt vmcnt(18) lgkmcnt(0)\n"
      "s_barrier\n"
      "ds_read_b64_tr_b16 a[0:1], v176 offset:0\n"
      "ds_read_b64_tr_b16 a[2:3], v176 offset:1536\n"
      "ds_read_b64_tr_b16 a[96:97], v176 offset:8\n"
      "ds_read_b64_tr_b16 a[98:99], v176 offset:1544\n"
      "ds_read_b64_tr_b16 a[192:193], v177 offset:0\n"
      "ds_read_b64_tr_b16 a[194:195], v177 offset:1536\n"
      "buffer_load_dwordx4 v178, s[12:15], s66 offen offset:0 lds\n"
      "buffer_load_dwordx4 v179, s[12:15], s66 offen offset:1024 lds\n"
      "buffer_load_dwordx4 v180, s[12:15], s66 offen offset:2048 lds\n"
      "s_sub_u32 s65, s65, 1\n"
      ".Ld_rs1:\n"
      "s_add_u32 s66, s44, 294912\n"
      "s_cmp_ge_u32 s65, 7\n"
      "s_cselect_b32 s66, s66, s31\n"
      "s_add_u32 m0, s42, 0\n"
      "s_waitcnt vmcnt(18) lgkmcnt(0)\n"
      "s_barrier\n"
      "ds_read_b64_tr_b16 a[4:5], v176 offset:12288\n"
      "ds_read_b64_tr_b16 a[6:7], v176 offset:13824\n"
      "ds_read_b64_tr_b16 a[100:101], v176 offset:12296\n"
      "ds_read_b64_tr_b16 a[102:103], v176 offset:13832\n"
      "ds_read_b64_tr_b16 a[196:197], v177 offset:12288\n"
      "ds_read_b64_tr_b16 a[198:199], v177 offset:13824\n"
      "buffer_load_dwordx4 v178, s[12:15], s66 offen offset:0 lds\n"
      "buffer_load_dwordx4 v179, s[12:15], s66 offen offset:1024 lds\n"
      "buffer_load_dwordx4 v180, s[12:15], s66 offen offset:2048 lds\n"
      "s_sub_u32 s65, s65, 1\n"
      ".Ld_rs2:\n"
      "s_add_u32 s66, s44, 331776\n"
      "s_cmp_ge_u32 s65, 7\n"
      "s_cselect_b32 s66, s66, s31\n"
      "s_add_u32 m0, s42, 12288\n"
      "s_waitcnt vmcnt(18) lgkmcnt(0)\n"
      "s_barrier\n"
      "ds_read_b64_tr_b16 a[8:9], v176 offset:24576\n"
      "ds_read_b64_tr_b16 a[10:11], v176 offset:26112\n"
      "ds_read_b64_tr_b16 a[104:105], v176 offset:24584\n"
      "ds_read_b64_tr_b16 a[106:107], v176 offset:26120\n"
      "ds_read_b64_tr_b16 a[200:201], v177 offset:24576\n"
      "ds_read_b64_tr_b16 a[202:203], v177 offset:26112\n"
      "buffer_load_dwordx4 v178, s[12:15], s66 offen offset:0 lds\n"
      "buffer_load_dwordx4 v179, s[12:15], s66 offen offset:1024 lds\n"
      "buffer_load_dwordx4 v180, s[12:15], s66 offen offset:2048 lds\n"
      "s_sub_u32 s65, s65, 1\n"
      ".Ld_rs3:\n"
      "s_add_u32 s66, s44, 368640\n"
      "s_cmp_ge_u32 s65, 7\n"
      "s_cselect_b32 s66, s66, s31\n"
      "s_add_u32 m0, s42, 24576\n"
      "s_waitcnt vmcnt(18) lgkmcnt(0)\n"
      "s_barrier\n"
      "ds_read_b64_tr_b16 a[12:13], v176 offset:36864\n"
      "ds_read_b64_tr_b16 a[14:15], v176 offset:38400\n"
      "ds_read_b64_tr_b16 a[108:109], v176 offset:36872\n"
      "ds_read_b64_tr_b16 a[110:111], v176 offset:38408\n"
      "ds_read_b64_tr_b16 a[204:205], v177 offset:36864\n"
      "ds_read_b64_tr_b16 a[206:207], v177 offset:38400\n"
      "buffer_load_dwordx4 v178, s[12:15], s66 offen offset:0 lds\n"
      "buffer_load_dwordx4 v179, s[12:15], s66 offen offset:1024 lds\n"
      "buffer_load_dwordx4 v180, s[12:15], s66 offen offset:2048 lds\n"
      "s_sub_u32 s65, s65, 1\n"
      ".Ld_rs4:\n"
      "s_add_u32 s66, s44, 405504\n"
      "s_cmp_ge_u32 s65, 7\n"
      "s_cselect_b32 s66, s66, s31\n"
      "s_add_u32 m0, s42, 36864\n"
      "s_waitcnt vmcnt(18) lgkmcnt(0)\n"
      "s_barrier\n"
      "ds_read_b64_tr_b16 a[16:17], v20 offset:0\n"
      "ds_read_b64_tr_b16 a[18:19], v20 offset:1536\n"
      "ds_read_b64_tr_b16 a[112:113], v20 offset:8\n"
      "ds_read_b64_tr_b16 a[114:115], v20 offset:1544\n"
      "ds_read_b64_tr_b16 a[208:209], v21 offset:0\n"
      "ds_read_b64_tr_b16 a[210:211], v21 offset:1536\n"
      "buffer_load_dwordx4 v178, s[12:15], s66 offen offset:0 lds\n"
      "buffer_load_dwordx4 v179, s[12:15], s66 offen offset:1024 lds\n"
      "buffer_load_dwordx4 v180, s[12:15], s66 offen offset:2048 lds\n"
      "s_sub_u32 s65, s65, 1\n"
      ".Ld_rs5:\n"
      "s_add_u32 s66, s44, 442368\n"
      "s_cmp_ge_u32 s65, 7\n"
      "s_cselect_b32 s66, s66, s31\n"
      "s_add_u32 m0, s42, 49152\n"
      "s_waitcnt vmcnt(18) lgkmcnt(0)\n"
      "s_barrier\n"
      "ds_read_b64_tr_b16 a[20:21], v20 offset:12288\n"
      "ds_read_b64_tr_b16 a[22:23], v20 offset:13824\n"
      "ds_read_b64_tr_b16 a[116:117], v20 offset:12296\n"
      "ds_read_b64_tr_b16 a[118:119], v20 offset:13832\n"
      "ds_read_b64_tr_b16 a[212:213], v21 offset:12288\n"
      "ds_read_b64_tr_b16 a[214:215], v21 offset:13824\n"
      "buffer_load_dwordx4 v178, s[12:15], s66 offen offset:0 lds\n"
      "buffer_load_dwordx4 v179, s[12:15], s66 offen offset:1024 lds\n"
      "buffer_load_dwordx4 v180, s[12:15], s66 offen offset:2048 lds\n"
      "s_sub_u32 s65, s65, 1\n"
      "s_cbranch_scc1 .Ld_re\n"
      ".Ld_rs6:\n"
      "s_add_u32 s66, s44, 479232\n"
      "s_cmp_ge_u32 s65, 7\n"
      "s_cselect_b32 s66, s66, s31\n"
      "s_add_u32 m0, s42, 61440\n"
      "s_waitcnt vmcnt(18) lgkmcnt(0)\n"
      "s_barrier\n"
      "ds_read_b64_tr_b16 a[24:25], v20 offset:24576\n"
      "ds_read_b64_tr_b16 a[26:27], v20 offset:26112\n"
      "ds_read_b64_tr_b16 a[120:121], v20 offset:24584\n"
      "ds_read_b64_tr_b16 a[122:123], v20 offset:26120\n"
      "ds_read_b64_tr_b16 a[216:217], v21 offset:24576\n"
      "ds_read_b64_tr_b16 a[218:219], v21 offset:26112\n"
      "buffer_load_dwordx4 v178, s[12:15], s66 offen offset:0 lds\n"
      "buffer_load_dwordx4 v179, s[12:15], s66 offen offset:1024 lds\n"
      "buffer_load_dwordx4 v180, s[12:15], s66 offen offset:2048 lds\n"
      "s_sub_u32 s65, s65, 1\n"
      ".Ld_rs7:\n"
      "s_add_u32 s66, s44, 516096\n"
      "s_cmp_ge_u32 s65, 7\n"
      "s_cselect_b32 s66, s66, s31\n"
      "s_add_u32 m0, s42, 73728\n"
      "s_waitcnt vmcnt(18) lgkmcnt(0)\n"
      "s_barrier\n"
      "ds_read_b64_tr_b16 a[28:29], v20 offset:36864\n"
      "ds_read_b64_tr_b16 a[30:31], v20 offset:38400\n"
      "ds_read_b64_tr_b16 a[124:125], v20 offset:36872\n"
      "ds_read_b64_tr_b16 a[126:127], v20 offset:38408\n"
      "ds_read_b64_tr_b16 a[220:221], v21 offset:36864\n"
      "ds_read_b64_tr_b16 a[222:223], v21 offset:38400\n"
      "buffer_load_dwordx4 v178, s[12:15], s66 offen offset:0 lds\n"
      "buffer_load_dwordx4 v179, s[12:15], s66 offen offset:1024 lds\n"
      "buffer_load_dwordx4 v180, s[12:15], s66 offen offset:2048 lds\n"
      "s_sub_u32 s65, s65, 1\n"
      ".Ld_rs8:\n"
      "s_add_u32 s66, s44, 552960\n"
      "s_cmp_ge_u32 s65, 7\n"
      "s_cselect_b32 s66, s66, s31\n"
      "s_add_u32 m0, s42, 86016\n"
      "s_waitcnt vmcnt(18) lgkmcnt(0)\n"
      "s_barrier\n"
      "ds_read_b64_tr_b16 a[32:33], v176 offset:0\n"
      "ds_read_b64_tr_b16 a[34:35], v176 offset:1536\n"
      "ds_read_b64_tr_b16 a[128:129], v176 offset:8\n"
      "ds_read_b64_tr_b16 a[130:131], v176 offset:1544\n"
      "ds_read_b64_tr_b16 a[224:225], v177 offset:0\n"
      "ds_read_b64_tr_b16 a[226:227], v177 offset:1536\n"
      "buffer_load_dwordx4 v178, s[12:15], s66 offen offset:0 lds\n"
      "buffer_load_dwordx4 v179, s[12:15], s66 offen offset:1024 lds\n"
      "buffer_load_dwordx4 v180, s[12:15], s66 offen offset:2048 lds\n"
      "s_sub_u32 s65, s65, 1\n"
      ".Ld_rs9:\n"
      "s_add_u32 s66, s44, 589824\n"
      "s_cmp_ge_u32 s65, 7\n"
      "s_cselect_b32 s66, s66, s31\n"
      "s_add_u32 m0, s42, 0\n"
      "s_waitcnt vmcnt(18) lgkmcnt(0)\n"
      "s_barrier\n"
      "ds_read_b64_tr_b16 a[36:37], v176 offset:12288\n"
      "ds_read_b64_tr_b16 a[38:39], v176 offset:13824\n"
      "ds_read_b64_tr_b16 a[132:133], v176 offset:12296\n"
      "ds_read_b64_tr_b16 a[134:135], v176 offset:13832\n"
      "ds_read_b64_tr_b16 a[228:229], v177 offset:12288\n"
      "ds_read_b64_tr_b16 a[230:231], v177 offset:13824\n"
      "buffer_load_dwordx4 v178, s[12:15], s66 offen offset:0 lds\n"
      "buffer_load_dwordx4 v179, s[12:15], s66 offen offset:1024 lds\n"
      "buffer_load_dwordx4 v180, s[12:15], s66 offen offset:2048 lds\n"
      "s_sub_u32 s65, s65, 1\n"
      ".Ld_rs10:\n"
      "s_add_u32 s66, s44, 626688\n"
      "s_cmp_ge_u32 s65, 7\n"
      "s_cselect_b32 s66, s66, s31\n"
      "s_add_u32 m0, s42, 12288\n"
      "s_waitcnt vmcnt(18) lgkmcnt(0)\n"
      "s_barrier\n"
      "ds_read_b64_tr_b16 a[40:41], v176 offset:24576\n"
      "ds_read_b64_tr_b16 a[42:43], v176 offset:26112\n"
      "ds_read_b64_tr_b16 a[136:137], v176 offset:24584\n"
      "ds_read_b64_tr_b16 a[138:139], v176 offset:26120\n"
      "ds_read_b64_tr_b16 a[232:233], v177 offset:24576\n"
      "ds_read_b64_tr_b16 a[234:235], v177 offset:26112\n"
      "buffer_load_dwordx4 v178, s[12:15], s66 offen offset:0 lds\n"
      "buffer_load_dwordx4 v179, s[12:15], s66 offen offset:1024 lds\n"
      "buffer_load_dwordx4 v180, s[12:15], s66 offen offset:2048 lds\n"
      "s_sub_u32 s65, s65, 1\n"
      ".Ld_rs11:\n"
      "s_add_u32 s66, s44, 663552\n"
      "s_cmp_ge_u32 s65, 7\n"
      "s_cselect_b32 s66, s66, s31\n"
      "s_add_u32 m0, s42, 24576\n"
      "s_waitcnt vmcnt(18) lgkmcnt(0)\n"
      "s_barrier\n"
      "ds_read_b64_tr_b16 a[44:45], v176 offset:36864\n"
      "ds_read_b64_tr_b16 a[46:47], v176 offset:38400\n"
      "ds_read_b64_tr_b16 a[140:141], v176 offset:36872\n"
      "ds_read_b64_tr_b16 a[142:143], v176 offset:38408\n"
      "ds_read_b64_tr_b16 a[236:237], v177 offset:36864\n"
      "ds_read_b64_tr_b16 a[238:239], v177 offset:38400\n"
      "buffer_load_dwordx4 v178, s[12:15], s66 offen offset:0 lds\n"
      "buffer_load_dwordx4 v179, s[12:15], s66 offen offset:1024 lds\n"
      "buffer_load_dwordx4 v180, s[12:15], s66 offen offset:2048 lds\n"
      "s_sub_u32 s65, s65, 1\n"
      "s_cbranch_scc1 .Ld_re\n"
      ".Ld_rs12:\n"
      "s_add_u32 s66, s44, 700416\n"
      "s_cmp_ge_u32 s65, 7\n"
      "s_cselect_b32 s66, s66, s31\n"
      "s_add_u32 m0, s42, 36864\n"
      "s_waitcnt vmcnt(18) lgkmcnt(0)\n"
      "s_barrier\n"
      "ds_read_b64_tr_b16 a[48:49], v20 offset:0\n"
      "ds_read_b64_tr_b16 a[50:51], v20 offset:1536\n"
      "ds_read_b64_tr_b16 a[144:145], v20 offset:8\n"
      "ds_read_b64_tr_b16 a[146:147], v20 offset:1544\n"
      "ds_read_b64_tr_b16 a[240:241], v21 offset:0\n"
      "ds_read_b64_tr_b16 a[242:243], v21 offset:1536\n"
      "buffer_load_dwordx4 v178, s[12:15], s66 offen offset:0 lds\n"
      "buffer_load_dwordx4 v179, s[12:15], s66 offen offset:1024 lds\n"
      "buffer_load_dwordx4 v180, s[12:15], s66 offen offset:2048 lds\n"
      "s_sub_u32 s65, s65, 1\n"
      ".Ld_rs13:\n"
      "s_add_u32 s66, s44, 737280\n"
      "s_cmp_ge_u32 s65, 7\n"
      "s_cselect_b32 s66, s66, s31\n"
      "s_add_u32 m0, s42, 49152\n"
      "s_waitcnt vmcnt(18) lgkmcnt(0)\n"
      "s_barrier\n"
      "ds_read_b64_tr_b16 a[52:53], v20 offset:12288\n"
      "ds_read_b64_tr_b16 a[54:55], v20 offset:13824\n"
      "ds_read_b64_tr_b16 a[148:149], v20 offset:12296\n"
      "ds_read_b64_tr_b16 a[150:151], v20 offset:13832\n"
      "ds_read_b64_tr_b16 a[244:245], v21 offset:12288\n"
      "ds_read_b64_tr_b16 a[246:247], v21 offset:13824\n"
      "buffer_load_dwordx4 v178, s[12:15], s66 offen offset:0 lds\n"
      "buffer_load_dwordx4 v179, s[12:15], s66 offen offset:1024 lds\n"
      "buffer_load_dwordx4 v180, s[12:15], s66 offen offset:2048 lds\n"
      "s_sub_u32 s65, s65, 1\n"
      ".Ld_rs14:\n"
      "s_add_u32 s66, s44, 774144\n"
      "s_cmp_ge_u32 s65, 7\n"
      "s_cselect_b32 s66, s66, s31\n"
      "s_add_u32 m0, s42, 61440\n"
      "s_waitcnt vmcnt(18) lgkmcnt(0)\n"
      "s_barrier\n"
      "ds_read_b64_tr_b16 a[56:57], v20 offset:24576\n"
      "ds_read_b64_tr_b16 a[58:59], v20 offset:26112\n"
      "ds_read_b64_tr_b16 a[152:153], v20 offset:24584\n"
      "ds_read_b64_tr_b16 a[154:155], v20 offset:26120\n"
      "ds_read_b64_tr_b16 a[248:249], v21 offset:24576\n"
      "ds_read_b64_tr_b16 a[250:251], v21 offset:26112\n"
      "buffer_load_dwordx4 v178, s[12:15], s66 offen offset:0 lds\n"
      "buffer_load_dwordx4 v179, s[12:15], s66 offen offset:1024 lds\n"
      "buffer_load_dwordx4 v180, s[12:15], s66 offen offset:2048 lds\n"
      "s_sub_u32 s65, s65, 1\n"
      ".Ld_rs15:\n"
      "s_add_u32 s66, s44, 811008\n"
      "s_cmp_ge_u32 s65, 7\n"
      "s_cselect_b32 s66, s66, s31\n"
      "s_add_u32 m0, s42, 73728\n"
      "s_waitcnt vmcnt(18) lgkmcnt(0)\n"
      "s_barrier\n"
      "ds_read_b64_tr_b16 a[60:61], v20 offset:36864\n"
      "ds_read_b64_tr_b16 a[62:63], v20 offset:38400\n"
      "ds_read_b64_tr_b16 a[156:157], v20 offset:36872\n"
      "ds_read_b64_tr_b16 a[158:159], v20 offset:38408\n"
      "ds_read_b64_tr_b16 a[252:253], v21 offset:36864\n"
      "ds_read_b64_tr_b16 a[254:255], v21 offset:38400\n"
      "buffer_load_dwordx4 v178, s[12:15], s66 offen offset:0 lds\n"
      "buffer_load_dwordx4 v179, s[12:15], s66 offen offset:1024 lds\n"
      "buffer_load_dwordx4 v180, s[12:15], s66 offen offset:2048 lds\n"
      "s_sub_u32 s65, s65, 1\n"
      ".Ld_rs16:\n"
      "s_add_u32 s66, s44, 847872\n"
      "s_cmp_ge_u32 s65, 7\n"
      "s_cselect_b32 s66, s66, s31\n"
      "s_add_u32 m0, s42, 86016\n"
      "s_waitcnt vmcnt(18) lgkmcnt(0)\n"
      "s_barrier\n"
      "ds_read_b64_tr_b16 a[64:65], v176 offset:0\n"
      "ds_read_b64_tr_b16 a[66:67], v176 offset:1536\n"
      "ds_read_b64_tr_b16 a[160:161], v176 offset:8\n"
      "ds_read_b64_tr_b16 a[162:163], v176 offset:1544\n"
      "ds_read_b64_tr_b16 v[24:25], v177 offset:0\n"
      "ds_read_b64_tr_b16 v[26:27], v177 offset:1536\n"
      "buffer_load_dwordx4 v178, s[12:15], s66 offen offset:0 lds\n"
      "buffer_load_dwordx4 v179, s[12:15], s66 offen offset:1024 lds\n"
      "buffer_load_dwordx4 v180, s[12:15], s66 offen offset:2048 lds\n"
      "s_sub_u32 s65, s65, 1\n"
      ".Ld_rs17:\n"
      "s_add_u32 s66, s44, 0\n"
      "s_cmp_ge_u32 s65, 7\n"
      "s_cselect_b32 s66, s66, s31\n"
      "s_add_u32 m0, s42, 0\n"
      "s_waitcnt vmcnt(18) lgkmcnt(0)\n"
      "s_barrier\n"
      "ds_read_b64_tr_b16 a[68:69], v176 offset:12288\n"
      "ds_read_b64_tr_b16 a[70:71], v176 offset:13824\n"
      "ds_read_b64_tr_b16 a[164:165], v176 offset:12296\n"
      "ds_read_b64_tr_b16 a[166:167], v176 offset:13832\n"
      "ds_read_b64_tr_b16 v[28:29], v177 offset:12288\n"
      "ds_read_b64_tr_b16 v[30:31], v177 offset:13824\n"
      "buffer_load_dwordx4 v178, s[12:15], s66 offen offset:0 lds\n"
      "buffer_load_dwordx4 v179, s[12:15], s66 offen offset:1024 lds\n"
      "buffer_load_dwordx4 v180, s[12:15], s66 offen offset:2048 lds\n"
      "s_sub_u32 s65, s65, 1\n"
      "s_cbranch_scc1 .Ld_re\n"
      ".Ld_rs18:\n"
      "s_add_u32 s66, s44, 36864\n"
      "s_cmp_ge_u32 s65, 7\n"
      "s_cselect_b32 s66, s66, s31\n"
      "s_add_u32 m0, s42, 12288\n"
      "s_waitcnt vmcnt(18) lgkmcnt(0)\n"
      "s_barrier\n"
      "ds_read_b64_tr_b16 a[72:73], v176 offset:24576\n"
      "ds_read_b64_tr_b16 a[74:75], v176 offset:26112\n"
      "ds_read_b64_tr_b16 a[168:169], v176 offset:24584\n"
      "ds_read_b64_tr_b16 a[170:171], v176 offset:26120\n"
      "ds_read_b64_tr_b16 v[32:33], v177 offset:24576\n"
      "ds_read_b64_tr_b16 v[34:35], v177 offset:26112\n"
      "buffer_load_dwordx4 v178, s[12:15], s66 offen offset:0 lds\n"
      "buffer_load_dwordx4 v179, s[12:15], s66 offen offset:1024 lds\n"
      "buffer_load_dwordx4 v180, s[12:15], s66 offen offset:2048 lds\n"
      "s_sub_u32 s65, s65, 1\n"
      ".Ld_rs19:\n"
      "s_add_u32 s66, s44, 73728\n"
      "s_cmp_ge_u32 s65, 7\n"
      "s_cselect_b32 s66, s66, s31\n"
      "s_add_u32 m0, s42, 24576\n"
      "s_waitcnt vmcnt(18) lgkmcnt(0)\n"
      "s_barrier\n"
      "ds_read_b64_tr_b16 a[76:77], v176 offset:36864\n"
      "ds_read_b64_tr_b16 a[78:79], v176 offset:38400\n"
      "ds_read_b64_tr_b16 a[172:173], v176 offset:36872\n"
      "ds_read_b64_tr_b16 a[174:175], v176 offset:38408\n"
      "ds_read_b64_tr_b16 v[36:37], v177 offset:36864\n"
      "ds_read_b64_tr_b16 v[38:39], v177 offset:38400\n"
      "buffer_load_dwordx4 v178, s[12:15], s66 offen offset:0 lds\n"
      "buffer_load_dwordx4 v179, s[12:15], s66 offen offset:1024 lds\n"
      "buffer_load_dwordx4 v180, s[12:15], s66 offen offset:2048 lds\n"
      "s_sub_u32 s65, s65, 1\n"
      ".Ld_rs20:\n"
      "s_add_u32 s66, s44, 110592\n"
      "s_cmp_ge_u32 s65, 7\n"
      "s_cselect_b32 s66, s66, s31\n"
      "s_add_u32 m0, s42, 36864\n"
      "s_waitcnt vmcnt(18) lgkmcnt(0)\n"
      "s_barrier\n"
      "ds_read_b64_tr_b16 a[80:81], v20 offset:0\n"
      "ds_read_b64_tr_b16 a[82:83], v20 offset:1536\n"
      "ds_read_b64_tr_b16 a[176:177], v20 offset:8\n"
      "ds_read_b64_tr_b16 a[178:179], v20 offset:1544\n"
      "ds_read_b64_tr_b16 v[40:41], v21 offset:0\n"
      "ds_read_b64_tr_b16 v[42:43], v21 offset:1536\n"
      "buffer_load_dwordx4 v178, s[12:15], s66 offen offset:0 lds\n"
      "buffer_load_dwordx4 v179, s[12:15], s66 offen offset:1024 lds\n"
      "buffer_load_dwordx4 v180, s[12:15], s66 offen offset:2048 lds\n"
      "s_sub_u32 s65, s65, 1\n"
      ".Ld_rs21:\n"
      "s_add_u32 s66, s44, 147456\n"
      "s_cmp_ge_u32 s65, 7\n"
      "s_cselect_b32 s66, s66, s31\n"
      "s_add_u32 m0, s42, 49152\n"
      "s_waitcnt vmcnt(18) lgkmcnt(0)\n"
      "s_barrier\n"
      "ds_read_b64_tr_b16 a[84:85], v20 offset:12288\n"
      "ds_read_b64_tr_b16 a[86:87], v20 offset:13824\n"
      "ds_read_b64_tr_b16 a[180:181], v20 offset:12296\n"
      "ds_read_b64_tr_b16 a[182:183], v20 offset:13832\n"
      "ds_read_b64_tr_b16 v[44:45], v21 offset:12288\n"
      "ds_read_b64_tr_b16 v[46:47], v21 offset:13824\n"
      "buffer_load_dwordx4 v178, s[12:15], s66 offen offset:0 lds\n"
      "buffer_load_dwordx4 v179, s[12:15], s66 offen offset:1024 lds\n"
      "buffer_load_dwordx4 v180, s[12:15], s66 offen offset:2048 lds\n"
      "s_sub_u32 s65, s65, 1\n"
      ".Ld_rs22:\n"
      "s_add_u32 s66, s44, 184320\n"
      "s_cmp_ge_u32 s65, 7\n"
      "s_cselect_b32 s66, s66, s31\n"
      "s_add_u32 m0, s42, 61440\n"
      "s_waitcnt vmcnt(18) lgkmcnt(0)\n"
      "s_barrier\n"
      "ds_read_b64_tr_b16 a[88:89], v20 offset:24576\n"
      "ds_read_b64_tr_b16 a[90:91], v20 offset:26112\n"
      "ds_read_b64_tr_b16 a[184:185], v20 offset:24584\n"
      "ds_read_b64_tr_b16 a[186:187], v20 offset:26120\n"
      "ds_read_b64_tr_b16 v[48:49], v21 offset:24576\n"
      "ds_read_b64_tr_b16 v[50:51], v21 offset:26112\n"
      "buffer_load_dwordx4 v178, s[12:15], s66 offen offset:0 lds\n"
      "buffer_load_dwordx4 v179, s[12:15], s66 offen offset:1024 lds\n"
      "buffer_load_dwordx4 v180, s[12:15], s66 offen offset:2048 lds\n"
      "s_sub_u32 s65, s65, 1\n"
      ".Ld_rs23:\n"
      "s_add_u32 s66, s44, 221184\n"
      "s_cmp_ge_u32 s65, 7\n"
      "s_cselect_b32 s66, s66, s31\n"
      "s_add_u32 m0, s42, 73728\n"
      "s_waitcnt vmcnt(18) lgkmcnt(0)\n"
      "s_barrier\n"
      "ds_read_b64_tr_b16 a[92:93], v20 offset:36864\n"
      "ds_read_b64_tr_b16 a[94:95], v20 offset:38400\n"
      "ds_read_b64_tr_b16 a[188:189], v20 offset:36872\n"
      "ds_read_b64_tr_b16 a[190:191], v20 offset:38408\n"
      "ds_read_b64_tr_b16 v[52:53], v21 offset:36864\n"
      "ds_read_b64_tr_b16 v[54:55], v21 offset:38400\n"
      "buffer_load_dwordx4 v178, s[12:15], s66 offen offset:0 lds\n"
      "buffer_load_dwordx4 v179, s[12:15], s66 offen offset:1024 lds\n"
      "buffer_load_dwordx4 v180, s[12:15], s66 offen offset:2048 lds\n"
      "s_sub_u32 s65, s65, 1\n"
      "s_cbranch_scc0 .Ld_rs0\n"
      ".Ld_re:\n"
      "s_waitcnt vmcnt(0) lgkmcnt(0)\n"
      "v_add_u32 v22, 49152, v0\n"
      "v_add_u32 v23, 98304, v0\n"
      "s_lshr_b32 s61, s42, 10\n"
      "s_mul_i32 s61, s61, 1366\n"
      "s_and_b32 s61, s61, 0x7000\n"
      "s_getpc_b64 vcc\n"
      ".Ld_ct:\n"
      "s_add_u32 vcc_lo, vcc_lo, .Ld_p0-.Ld_ct\n"
      "s_addc_u32 vcc_hi, vcc_hi, 0\n"
      "s_lshl_b32 s30, s61, 1\n"
      "v_lshl_add_u32 v56, v0, 3, s30\n"
      "v_min_u32 v56, .Ld_last-.Ld_p0-4, v56\n"
      "global_load_dword v56, v56, vcc\n"
      "s_waitcnt vmcnt(0) lgkmcnt(0)\n"
      "s_mov_b64 exec, 0\n"
      "s_lshr_b32 s22, s61, 12\n"
      ".Ld_pl:\n"
      "s_cmp_eq_u32 s22, 0\n"
      "s_cbranch_scc1 .Ld_p0\n"
      "s_cmp_eq_u32 s22, 1\n"
      "s_cbranch_scc1 .Ld_p1\n"
      "s_branch .Ld_pe\n"
      ".Ld_pr:\n"
      "s_add_u32 s22, s22, 4\n"
      "s_branch .Ld_pl\n"
      ".Ld_pe:\n"
      "s_mov_b64 exec, -1\n"
      "s_barrier\n"
      "v_mov_b32 v14, 0\n"
      "ds_write_b32 v16, v14\n"
      "s_waitcnt lgkmcnt(0)\n"
      "s_barrier\n"
      "s_mov_b32 s6, s50\n"
      "s_add_u32 s36, s36, 4294801408\n"
      "s_add_u32 s56, s37, 0\n"
      "s_add_u32 s55, s37, 4294787584\n"
      "s_add_u32 s37, s37, 4294894080\n"
      "s_mov_b32 s59, 0x40000000\n"
      "s_mov_b32 s60, 0x40000000\n"
      "s_mov_b32 s62, 63\n"
      "s_cmp_eq_u32 s45, 1\n"
      "s_cbranch_scc1 .Ld_in1\n"
      "s_cmp_eq_u32 s45, 2\n"
      "s_cbranch_scc1 .Ld_in2\n"
      "s_cmp_eq_u32 s45, 3\n"
      "s_cbranch_scc1 .Ld_in3\n"
      "s_cmp_eq_u32 s45, 4\n"
      "s_cbranch_scc1 .Ld_in4\n"
      "s_cmp_eq_u32 s45, 5\n"
      "s_cbranch_scc1 .Ld_in5\n"
      "s_add_u32 s36, s36, 165888\n"
      "s_min_u32 s36, s36, s6\n"
      "buffer_load_dword v20, v2, s[8:11], s36 offen sc1\n"
      "s_add_u32 m0, s43, 0\n"
      "s_mov_b32 s54, s36\n"
      "s_add_u32 s36, s36, 165888\n"
      "s_min_u32 s36, s36, s6\n"
      "s_waitcnt vmcnt(0)\n"
      "v_cmp_ne_u32 vcc, s21, v20\n"
      "s_cbranch_vccnz .Lsp_d0_1\n"
      ".Lbk_d0_1:\n"
      "buffer_load_dwordx4 v0, s[8:11], s54 offen offset:0 sc1 lds\n"
      "buffer_load_dwordx4 v0, s[8:11], s54 offen offset:1024 sc1 lds\n"
      "buffer_load_dword v20, v2, s[8:11], s36 offen sc1\n"
      "buffer_load_dwordx4 v0, s[8:11], s54 offen offset:2048 sc1 lds\n"
      "buffer_load_dwordx4 v0, s[8:11], s54 offen offset:3072 sc1 lds\n"
      "s_add_u32 m0, s43, 4096\n"
      "s_nop 0\n"
      "buffer_load_dwordx4 v1, s[8:11], s54 offen offset:1024 sc1 lds\n"
      "buffer_load_dwordx4 v1, s[8:11], s54 offen offset:0 sc1 lds\n"
      "s_mov_b32 s54, s36\n"
      "s_add_u32 m0, s43, 24576\n"
      "v_add_u32 v14, 1, v14\n"
      "s_add_u32 s36, s36, 165888\n"
      "buffer_load_dwordx4 v[216:219], v6, s[8:11], s38 offen sc1\n"
      "s_waitcnt vmcnt(5)\n"
      "v_cmp_ne_u32 vcc, s21, v20\n"
      "buffer_load_dwordx2 v[220:221], v7, s[8:11], s38 offen sc1\n"
      "s_cbranch_vccnz .Lsp_d1_2\n"
      ".Lbk_d1_2:\n"
      "buffer_load_dwordx4 v0, s[8:11], s54 offen offset:0 sc1 lds\n"
      "buffer_load_dwordx4 v0, s[8:11], s54 offen offset:1024 sc1 lds\n"
      "s_waitcnt vmcnt(4)\n"
      "ds_write_b32 v16, v14\n"
      "buffer_load_dwordx4 v0, s[8:11], s54 offen offset:2048 sc1 lds\n"
      "s_min_u32 s36, s36, s6\n"
      "buffer_load_dword v20, v2, s[8:11], s36 offen sc1\n"
      "buffer_load_dwordx4 v0, s[8:11], s54 offen offset:3072 sc1 lds\n"
      "ds_read_b32 v15, v17\n"
      "s_add_u32 m0, s43, 28672\n"
      "s_nop 0\n"
      "buffer_load_dwordx4 v1, s[8:11], s54 offen offset:0 sc1 lds\n"
      "s_waitcnt lgkmcnt(0)\n"
      "v_cmp_gt_i32 vcc, v14, v15\n"
      "buffer_load_dwordx4 v1, s[8:11], s54 offen offset:1024 sc1 lds\n"
      "s_cbranch_vccnz .Lsp_dy0_3\n"
      ".Lbk_dy0_3:\n"
      "ds_read_b128 v[56:59], v0 offset:0\n"
      "ds_read_b128 v[60:63], v0 offset:1024\n"
      "ds_read_b128 v[84:87], v0 offset:7168\n"
      "ds_read_b128 v[72:75], v0 offset:4096\n"
      "ds_read_b128 v[88:91], v0 offset:8192\n"
      "ds_read_b128 v[68:71], v0 offset:3072\n"
      "ds_read_b128 v[104:107], v0 offset:12288\n"
      "ds_read_b128 v[96:99], v0 offset:10240\n"
      "ds_read_b128 v[80:83], v0 offset:6144\n"
      "ds_read_b128 v[64:67], v0 offset:2048\n"
      "ds_read_b128 v[92:95], v0 offset:9216\n"
      "ds_read_b128 v[76:79], v0 offset:5120\n"
      "ds_read_b128 v[132:135], v0 offset:19456\n"
      "ds_read_b128 v[100:103], v0 offset:11264\n"
      "s_waitcnt vmcnt(0) lgkmcnt(0)\n"
      "s_nop 4\n"
      "s_branch .Ld_t0\n"
      ".p2align 12\n"
      ".Ld_p0:\n"
      "s_branch .Ld_pr\n"
      ".p2align 5\n"
      ".Ld_t0:\n"
      "v_mfma_f32_16x16x32_bf16 v[152:155], a[0:3], v[56:59], 0\n"
      "ds_read_b128 v[108:111], v0 offset:13312\n"
      "ds_read_b128 v[120:123], v0 offset:16384\n"
      "v_mfma_f32_16x16x32_bf16 v[152:155], a[4:7], v[60:63], v[152:155]\n"
      "s_waitcnt lgkmcnt(6)\n"
      "ds_read_b128 v[116:119], v0 offset:15360\n"
      "v_add_f32 v236, v236, v168\n"
      "v_mfma_f32_16x16x32_bf16 v[152:155], a[8:11], v[64:67], v[152:155]\n"
      "v_cvt_pk_bf16_f32 v228, v160, v161\n"
      "v_cvt_pk_bf16_f32 v229, v162, v163\n"
      "v_mfma_f32_16x16x32_bf16 v[152:155], a[12:15], v[68:71], v[152:155]\n"
      "buffer_store_dwordx2 v[228:229], v13, s[8:11], s37 offen sc1\n"
      "v_mfma_f32_16x16x32_bf16 v[152:155], a[16:19], v[72:75], v[152:155]\n"
      "s_waitcnt lgkmcnt(4)\n"
      "v_cvt_pk_bf16_f32 v178, v168, v169\n"
      "v_add_f32 v175, v175, v213\n"
      "v_mfma_f32_16x16x32_bf16 v[152:155], a[20:23], v[76:79], v[152:155]\n"
      "ds_read_b128 v[112:115], v0 offset:14336\n"
      "v_add_f32 v171, v171, v211\n"
      "v_mfma_f32_16x16x32_bf16 v[152:155], a[24:27], v[80:83], v[152:155]\n"
      "ds_read_b128 v[124:127], v0 offset:17408\n"
      "v_add_f32 v236, v236, v169\n"
      "v_mfma_f32_16x16x32_bf16 v[152:155], a[28:31], v[84:87], v[152:155]\n"
      "v_add_f32 v173, v173, v204\n"
      "v_cvt_pk_bf16_f32 v179, v170, v171\n"
      "v_mfma_f32_16x16x32_bf16 v[152:155], a[32:35], v[88:91], v[152:155]\n"
      "ds_read_b128 v[128:131], v0 offset:18432\n"
      "v_add_f32 v173, v173, v212\n"
      "v_mfma_f32_16x16x32_bf16 v[152:155], a[36:39], v[92:95], v[152:155]\n"
      "buffer_store_dwordx4 v[176:179], v6, s[8:11], s59 offen sc1\n"
      "v_mfma_f32_16x16x32_bf16 v[152:155], a[40:43], v[96:99], v[152:155]\n"
      "s_waitcnt lgkmcnt(3)\n"
      "ds_read_b128 v[148:151], v0 offset:23552\n"
      "v_cvt_pk_bf16_f32 v180, v172, v173\n"
      "v_mfma_f32_16x16x32_bf16 v[152:155], a[44:47], v[100:103], v[152:155]\n"
      "ds_read_b128 v[144:147], v0 offset:22528\n"
      "v_fmac_f32 v237, v168, v168\n"
      "v_mfma_f32_16x16x32_bf16 v[152:155], a[48:51], v[104:107], v[152:155]\n"
      "s_add_u32 m0, s43, 49152\n"
      "ds_read_b128 v[136:139], v0 offset:20480\n"
      "v_fmac_f32 v237, v169, v169\n"
      "v_mfma_f32_16x16x32_bf16 v[152:155], a[52:55], v[108:111], v[152:155]\n"
      "s_waitcnt lgkmcnt(4)\n"
      "s_mov_b32 s54, s36\n"
      "ds_read_b128 v[140:143], v0 offset:21504\n"
      "v_mfma_f32_16x16x32_bf16 v[152:155], a[56:59], v[112:115], v[152:155]\n"
      "v_cvt_pk_bf16_f32 v181, v174, v175\n"
      "v_fmac_f32 v237, v170, v170\n"
      "v_mfma_f32_16x16x32_bf16 v[152:155], a[60:63], v[116:119], v[152:155]\n"
      "buffer_store_dwordx2 v[180:181], v7, s[8:11], s59 offen sc1\n"
      "v_mfma_f32_16x16x32_bf16 v[152:155], a[64:67], v[120:123], v[152:155]\n"
      "v_add_u32 v14, 1, v14\n"
      "v_mfma_f32_16x16x32_bf16 v[152:155], a[68:71], v[124:127], v[152:155]\n"
      "s_waitcnt lgkmcnt(3)\n"
      "v_add_f32 v236, v236, v170\n"
      "v_fmac_f32 v237, v171, v171\n"
      "v_mfma_f32_16x16x32_bf16 v[152:155], a[72:75], v[128:131], v[152:155]\n"
      "v_fmac_f32 v237, v172, v172\n"
      "v_fmac_f32 v237, v173, v173\n"
      "v_mfma_f32_16x16x32_bf16 v[152:155], a[76:79], v[132:135], v[152:155]\n"
      "s_waitcnt lgkmcnt(1)\n"
      "buffer_store_dwordx2 v[180:181], v9, s[16:19], s60 offen\n"
      "v_mfma_f32_16x16x32_bf16 v[152:155], a[80:83], v[136:139], v[152:155]\n"
      "s_waitcnt lgkmcnt(0)\n"
      "v_add_f32 v236, v236, v171\n"
      "v_fmac_f32 v237, v174, v174\n"
      "v_mfma_f32_16x16x32_bf16 v[152:155], a[84:87], v[140:143], v[152:155]\n"
      "v_add_f32 v236, v236, v172\n"
      "v_fmac_f32 v237, v175, v175\n"
      "v_mfma_f32_16x16x32_bf16 v[152:155], a[88:91], v[144:147], v[152:155]\n"
      "v_add_f32 v236, v236, v173\n"
      "v_add_f32 v236, v236, v174\n"
      "v_mfma_f32_16x16x32_bf16 v[152:155], a[92:95], v[148:151], v[152:155]\n"
      "buffer_store_dwordx4 v[176:179], v8, s[16:19], s60 offen\n"
      "v_mfma_f32_16x16x32_bf16 v[156:159], a[96:99], v[56:59], 0\n"
      "s_waitcnt vmcnt(15)\n"
      "v_add_f32 v236, v236, v175\n"
      "v_lshlrev_b32 v166, 16, v217\n"
      "v_mfma_f32_16x16x32_bf16 v[156:159], a[100:103], v[60:63], v[156:159]\n"
      "s_waitcnt vmcnt(10)\n"
      "v_cmp_ne_u32 vcc, s21, v20\n"
      "v_mfma_f32_16x16x32_bf16 v[156:159], a[104:107], v[64:67], v[156:159]\n"
      "ds_write_b64 v10, v[236:237]\n"
      "v_mfma_f32_16x16x32_bf16 v[156:159], a[108:111], v[68:71], v[156:159]\n"
      "s_add_u32 s36, s36, 165888\n"
      "v_mfma_f32_16x16x32_bf16 v[156:159], a[112:115], v[72:75], v[156:159]\n"
      "s_min_u32 s36, s36, s6\n"
      "s_cbranch_vccnz .Lsp_d2_4\n"
      ".Lbk_d2_4:\n"
      "v_mfma_f32_16x16x32_bf16 v[156:159], a[116:119], v[76:79], v[156:159]\n"
      "s_waitcnt vmcnt(7)\n"
      "ds_write_b32 v16, v14\n"
      "v_mfma_f32_16x16x32_bf16 v[156:159], a[120:123], v[80:83], v[156:159]\n"
      "buffer_load_dword v20, v2, s[8:11], s36 offen sc1\n"
      "v_mfma_f32_16x16x32_bf16 v[156:159], a[124:127], v[84:87], v[156:159]\n"
      "v_and_b32 v167, 0xffff0000, v217\n"
      "v_lshlrev_b32 v164, 16, v216\n"
      "v_mfma_f32_16x16x32_bf16 v[156:159], a[128:131], v[88:91], v[156:159]\n"
      "v_mfma_f32_16x16x32_bf16 v[156:159], a[132:135], v[92:95], v[156:159]\n"
      "buffer_load_dwordx4 v0, s[8:11], s54 offen offset:3072 sc1 lds\n"
      "v_mfma_f32_16x16x32_bf16 v[156:159], a[136:139], v[96:99], v[156:159]\n"
      "s_add_u32 s55, s55, 106496\n"
      "v_mfma_f32_16x16x32_bf16 v[156:159], a[140:143], v[100:103], v[156:159]\n"
      "v_mfma_f32_16x16x32_bf16 v[156:159], a[144:147], v[104:107], v[156:159]\n"
      "s_add_u32 s37, s37, 173056\n"
      "v_and_b32 v165, 0xffff0000, v216\n"
      "v_mfma_f32_16x16x32_bf16 v[156:159], a[148:151], v[108:111], v[156:159]\n"
      "buffer_load_dwordx4 v0, s[8:11], s54 offen offset:1024 sc1 lds\n"
      "v_mfma_f32_16x16x32_bf16 v[156:159], a[152:155], v[112:115], v[156:159]\n"
      "ds_read_b32 v15, v17\n"
      "v_mfma_f32_16x16x32_bf16 v[156:159], a[156:159], v[116:119], v[156:159]\n"
      "v_mfma_f32_16x16x32_bf16 v[156:159], a[160:163], v[120:123], v[156:159]\n"
      "v_lshlrev_b32 v170, 16, v219\n"
      "v_mfma_f32_16x16x32_bf16 v[156:159], a[164:167], v[124:127], v[156:159]\n"
      "buffer_load_dwordx4 v0, s[8:11], s54 offen offset:2048 sc1 lds\n"
      "v_mfma_f32_16x16x32_bf16 v[156:159], a[168:171], v[128:131], v[156:159]\n"
      "v_mfma_f32_16x16x32_bf16 v[156:159], a[172:175], v[132:135], v[156:159]\n"
      "s_waitcnt lgkmcnt(0)\n"
      "v_cmp_gt_i32 vcc, v14, v15\n"
      "v_mfma_f32_16x16x32_bf16 v[156:159], a[176:179], v[136:139], v[156:159]\n"
      "v_add_f32 v165, v165, v153\n"
      "v_mfma_f32_16x16x32_bf16 v[156:159], a[180:183], v[140:143], v[156:159]\n"
      "buffer_load_dwordx4 v0, s[8:11], s54 offen offset:0 sc1 lds\n"
      "v_mfma_f32_16x16x32_bf16 v[156:159], a[184:187], v[144:147], v[156:159]\n"
      "v_add_f32 v164, v164, v152\n"
      "s_add_u32 m0, s43, 53248\n"
      "v_mfma_f32_16x16x32_bf16 v[156:159], a[188:191], v[148:151], v[156:159]\n"
      "s_cbranch_vccnz .Lsp_dy1_5\n"
      ".Lbk_dy1_5:\n"
      "ds_read_b64 v[238:239], v11\n"
      "v_mfma_f32_16x16x32_bf16 v[160:163], a[192:195], v[56:59], 0\n"
      "v_lshlrev_b32 v168, 16, v218\n"
      "v_mfma_f32_16x16x32_bf16 v[160:163], a[196:199], v[60:63], v[160:163]\n"
      "buffer_load_dwordx4 v1, s[8:11], s54 offen offset:0 sc1 lds\n"
      "v_mfma_f32_16x16x32_bf16 v[160:163], a[200:203], v[64:67], v[160:163]\n"
      "v_mfma_f32_16x16x32_bf16 v[160:163], a[204:207], v[68:71], v[160:163]\n"
      "ds_read_b128 v[56:59], v0 offset:24576\n"
      "v_mfma_f32_16x16x32_bf16 v[160:163], a[208:211], v[72:75], v[160:163]\n"
      "ds_read_b128 v[64:67], v0 offset:26624\n"
      "v_mfma_f32_16x16x32_bf16 v[160:163], a[212:215], v[76:79], v[160:163]\n"
      "buffer_load_dwordx4 v1, s[8:11], s54 offen offset:1024 sc1 lds\n"
      "v_mfma_f32_16x16x32_bf16 v[160:163], a[216:219], v[80:83], v[160:163]\n"
      "s_waitcnt lgkmcnt(2)\n"
      "ds_read_b128 v[68:71], v0 offset:27648\n"
      "v_add_f32_dpp v238, v238, v238 row_ror:8 row_mask:0xf bank_mask:0xf\n"
      "v_mfma_f32_16x16x32_bf16 v[160:163], a[220:223], v[84:87], v[160:163]\n"
      "ds_read_b128 v[80:83], v0 offset:30720\n"
      "v_add_f32_dpp v238, v238, v238 row_ror:4 row_mask:0xf bank_mask:0xf\n"
      "v_mfma_f32_16x16x32_bf16 v[160:163], a[224:227], v[88:91], v[160:163]\n"
      "ds_read_b128 v[60:63], v0 offset:25600\n"
      "v_add_f32_dpp v238, v238, v238 row_ror:2 row_mask:0xf bank_mask:0xf\n"
      "v_mfma_f32_16x16x32_bf16 v[160:163], a[228:231], v[92:95], v[160:163]\n"
      "ds_read_b128 v[92:95], v0 offset:33792\n"
      "v_add_f32_dpp v239, v239, v239 row_ror:8 row_mask:0xf bank_mask:0xf\n"
      "v_mfma_f32_16x16x32_bf16 v[160:163], a[232:235], v[96:99], v[160:163]\n"
      "ds_read_b128 v[84:87], v0 offset:31744\n"
      "v_add_f32_dpp v239, v239, v239 row_ror:4 row_mask:0xf bank_mask:0xf\n"
      "v_mfma_f32_16x16x32_bf16 v[160:163], a[236:239], v[100:103], v[160:163]\n"
      "s_waitcnt vmcnt(11) lgkmcnt(5)\n"
      "buffer_store_dword v18, v4, s[8:11], s55 offen sc1\n"
      "v_mfma_f32_16x16x32_bf16 v[160:163], a[240:243], v[104:107], v[160:163]\n"
      "ds_read_b128 v[72:75], v0 offset:28672\n"
      "v_add_f32_dpp v239, v239, v239 row_ror:2 row_mask:0xf bank_mask:0xf\n"
      "v_mfma_f32_16x16x32_bf16 v[160:163], a[244:247], v[108:111], v[160:163]\n"
      "ds_read_b128 v[76:79], v0 offset:29696\n"
      "v_add_f32_dpp v239, v239, v239 row_ror:1 row_mask:0xf bank_mask:0xf\n"
      "v_mfma_f32_16x16x32_bf16 v[160:163], a[248:251], v[112:115], v[160:163]\n"
      "v_add_f32_dpp v238, v238, v238 row_ror:1 row_mask:0xf bank_mask:0xf\n"
      "v_add_f32 v168, v168, v156\n"
      "v_mfma_f32_16x16x32_bf16 v[160:163], a[252:255], v[116:119], v[160:163]\n"
      "ds_read_b128 v[100:103], v0 offset:35840\n"
      "v_mul_f32 v238, 0x3ae38e39, v238\n"
      "v_mfma_f32_16x16x32_bf16 v[160:163], v[24:27], v[120:123], v[160:163]\n"
      "v_and_b32 v169, 0xffff0000, v218\n"
      "v_mfma_f32_16x16x32_bf16 v[160:163], v[28:31], v[124:127], v[160:163]\n"
      "ds_read_b128 v[96:99], v0 offset:34816\n"
      "v_and_b32 v175, 0xffff0000, v221\n"
      "v_mfma_f32_16x16x32_bf16 v[160:163], v[32:35], v[128:131], v[160:163]\n"
      "ds_read_b128 v[116:119], v0 offset:39936\n"
      "v_lshlrev_b32 v174, 16, v221\n"
      "v_mfma_f32_16x16x32_bf16 v[160:163], v[36:39], v[132:135], v[160:163]\n"
      "ds_read_b128 v[120:123], v0 offset:40960\n"
      "v_add_f32 v167, v167, v155\n"
      "v_mfma_f32_16x16x32_bf16 v[160:163], v[40:43], v[136:139], v[160:163]\n"
      "ds_read_b128 v[108:111], v0 offset:37888\n"
      "v_and_b32 v171, 0xffff0000, v219\n"
      "v_mfma_f32_16x16x32_bf16 v[160:163], v[44:47], v[140:143], v[160:163]\n"
      "ds_read_b128 v[88:91], v0 offset:32768\n"
      "v_add_f32 v166, v166, v154\n"
      "v_mfma_f32_16x16x32_bf16 v[160:163], v[48:51], v[144:147], v[160:163]\n"
      "v_add_f32 v239, 0x3afba882, v239\n"
      "v_mul_f32 v239, 0x3ae38e39, v239\n"
      "v_mfma_f32_16x16x32_bf16 v[160:163], v[52:55], v[148:151], v[160:163]\n"
      "s_sub_u32 s62, s62, 1\n"
      "s_cbranch_scc1 .Ld_out0\n"
      ".Ld_t1:\n"
      "v_mfma_f32_16x16x32_bf16 v[152:155], a[0:3], v[56:59], 0\n"
      "s_waitcnt lgkmcnt(4)\n"
      "buffer_store_dwordx2 v[238:239], v12, s[8:11], s59 offen sc1\n"
      "v_mfma_f32_16x16x32_bf16 v[152:155], a[4:7], v[60:63], v[152:155]\n"
      "ds_read_b128 v[104:107], v0 offset:36864\n"
      "v_mfma_f32_16x16x32_bf16 v[152:155], a[8:11], v[64:67], v[152:155]\n"
      "ds_read_b128 v[140:143], v0 offset:46080\n"
      "v_mfma_f32_16x16x32_bf16 v[152:155], a[12:15], v[68:71], v[152:155]\n"
      "ds_read_b128 v[112:115], v0 offset:38912\n"
      "v_mfma_f32_16x16x32_bf16 v[152:155], a[16:19], v[72:75], v[152:155]\n"
      "ds_read_b128 v[124:127], v0 offset:41984\n"
      "v_mfma_f32_16x16x32_bf16 v[152:155], a[20:23], v[76:79], v[152:155]\n"
      "ds_read_b128 v[132:135], v0 offset:44032\n"
      "v_mfma_f32_16x16x32_bf16 v[152:155], a[24:27], v[80:83], v[152:155]\n"
      "s_mov_b32 s54, s36\n"
      "v_add_f32 v174, v174, v162\n"
      "s_add_u32 s36, s36, 165888\n"
      "v_mfma_f32_16x16x32_bf16 v[152:155], a[28:31], v[84:87], v[152:155]\n"
      "s_waitcnt lgkmcnt(5)\n"
      "ds_read_b128 v[148:151], v0 offset:48128\n"
      "v_mfma_f32_16x16x32_bf16 v[152:155], a[32:35], v[88:91], v[152:155]\n"
      "ds_read_b128 v[128:131], v0 offset:43008\n"
      "s_add_u32 m0, s43, 73728\n"
      "v_mfma_f32_16x16x32_bf16 v[152:155], a[36:39], v[92:95], v[152:155]\n"
      "ds_read_b128 v[136:139], v0 offset:45056\n"
      "v_mfma_f32_16x16x32_bf16 v[152:155], a[40:43], v[96:99], v[152:155]\n"
      "v_mfma_f32_16x16x32_bf16 v[152:155], a[44:47], v[100:103], v[152:155]\n"
      "s_waitcnt lgkmcnt(4)\n"
      "v_add_f32 v170, v170, v158\n"
      "v_mfma_f32_16x16x32_bf16 v[152:155], a[48:51], v[104:107], v[152:155]\n"
      "v_mfma_f32_16x16x32_bf16 v[152:155], a[52:55], v[108:111], v[152:155]\n"
      "s_waitcnt vmcnt(8) lgkmcnt(3)\n"
      "v_lshlrev_b32 v172, 16, v220\n"
      "v_cmp_ne_u32 vcc, s21, v20\n"
      "v_mfma_f32_16x16x32_bf16 v[152:155], a[56:59], v[112:115], v[152:155]\n"
      "ds_read_b128 v[144:147], v0 offset:47104\n"
      "s_add_u32 s38, s38, 147456\n"
      "v_mfma_f32_16x16x32_bf16 v[152:155], a[60:63], v[116:119], v[152:155]\n"
      "s_min_u32 s36, s36, s6\n"
      "v_add_f32 v171, v171, v159\n"
      "v_mfma_f32_16x16x32_bf16 v[152:155], a[64:67], v[120:123], v[152:155]\n"
      "v_mfma_f32_16x16x32_bf16 v[152:155], a[68:71], v[124:127], v[152:155]\n"
      "s_waitcnt lgkmcnt(1)\n"
      "v_mfma_f32_16x16x32_bf16 v[152:155], a[72:75], v[128:131], v[152:155]\n"
      "v_add_f32 v169, v169, v157\n"
      "v_add_f32 v172, v172, v160\n"
      "v_mfma_f32_16x16x32_bf16 v[152:155], a[76:79], v[132:135], v[152:155]\n"
      "v_mfma_f32_16x16x32_bf16 v[152:155], a[80:83], v[136:139], v[152:155]\n"
      "s_cbranch_vccnz .Lsp_d3_6\n"
      ".Lbk_d3_6:\n"
      "v_mfma_f32_16x16x32_bf16 v[152:155], a[84:87], v[140:143], v[152:155]\n"
      "s_waitcnt lgkmcnt(0)\n"
      "v_mfma_f32_16x16x32_bf16 v[152:155], a[88:91], v[144:147], v[152:155]\n"
      "v_mfma_f32_16x16x32_bf16 v[152:155], a[92:95], v[148:151], v[152:155]\n"
      "buffer_load_dwordx4 v0, s[8:11], s54 offen offset:2048 sc1 lds\n"
      "v_mfma_f32_16x16x32_bf16 v[156:159], a[96:99], v[56:59], 0\n"
      "v_mfma_f32_16x16x32_bf16 v[156:159], a[100:103], v[60:63], v[156:159]\n"
      "v_add_u32 v14, 1, v14\n"
      "v_mfma_f32_16x16x32_bf16 v[156:159], a[104:107], v[64:67], v[156:159]\n"
      "v_cvt_pk_bf16_f32 v225, v154, v155\n"
      "v_mfma_f32_16x16x32_bf16 v[156:159], a[108:111], v[68:71], v[156:159]\n"
      "v_add_f32 v175, v175, v163\n"
      "v_mfma_f32_16x16x32_bf16 v[156:159], a[112:115], v[72:75], v[156:159]\n"
      "buffer_load_dwordx4 v0, s[8:11], s54 offen offset:1024 sc1 lds\n"
      "v_mfma_f32_16x16x32_bf16 v[156:159], a[116:119], v[76:79], v[156:159]\n"
      "v_mfma_f32_16x16x32_bf16 v[156:159], a[120:123], v[80:83], v[156:159]\n"
      "v_mfma_f32_16x16x32_bf16 v[156:159], a[124:127], v[84:87], v[156:159]\n"
      "v_and_b32 v173, 0xffff0000, v220\n"
      "v_mfma_f32_16x16x32_bf16 v[156:159], a[128:131], v[88:91], v[156:159]\n"
      "buffer_load_dword v20, v2, s[8:11], s36 offen sc1\n"
      "v_mfma_f32_16x16x32_bf16 v[156:159], a[132:135], v[92:95], v[156:159]\n"
      "s_waitcnt vmcnt(5)\n"
      "ds_write_b32 v16, v14\n"
      "v_mfma_f32_16x16x32_bf16 v[156:159], a[136:139], v[96:99], v[156:159]\n"
      "buffer_load_dwordx4 v0, s[8:11], s54 offen offset:0 sc1 lds\n"
      "v_mfma_f32_16x16x32_bf16 v[156:159], a[140:143], v[100:103], v[156:159]\n"
      "v_add_f32 v173, v173, v161\n"
      "v_mfma_f32_16x16x32_bf16 v[156:159], a[144:147], v[104:107], v[156:159]\n"
      "v_mfma_f32_16x16x32_bf16 v[156:159], a[148:151], v[108:111], v[156:159]\n"
      "v_mfma_f32_16x16x32_bf16 v[156:159], a[152:155], v[112:115], v[156:159]\n"
      "v_mfma_f32_16x16x32_bf16 v[156:159], a[156:159], v[116:119], v[156:159]\n"
      "buffer_load_dwordx4 v0, s[8:11], s54 offen offset:3072 sc1 lds\n"
      "v_mfma_f32_16x16x32_bf16 v[156:159], a[160:163], v[120:123], v[156:159]\n"
      "s_add_u32 m0, s43, 77824\n"
      "v_mfma_f32_16x16x32_bf16 v[156:159], a[164:167], v[124:127], v[156:159]\n"
      "v_mfma_f32_16x16x32_bf16 v[156:159], a[168:171], v[128:131], v[156:159]\n"
      "ds_read_b32 v15, v17\n"
      "v_mfma_f32_16x16x32_bf16 v[156:159], a[172:175], v[132:135], v[156:159]\n"
      "s_add_u32 s55, s55, 173056\n"
      "v_mfma_f32_16x16x32_bf16 v[156:159], a[176:179], v[136:139], v[156:159]\n"
      "buffer_load_dwordx4 v1, s[8:11], s54 offen offset:0 sc1 lds\n"
      "v_mfma_f32_16x16x32_bf16 v[156:159], a[180:183], v[140:143], v[156:159]\n"
      "v_mfma_f32_16x16x32_bf16 v[156:159], a[184:187], v[144:147], v[156:159]\n"
      "v_cvt_pk_bf16_f32 v224, v152, v153\n"
      "v_mfma_f32_16x16x32_bf16 v[156:159], a[188:191], v[148:151], v[156:159]\n"
      "s_waitcnt lgkmcnt(0)\n"
      "v_cmp_gt_i32 vcc, v14, v15\n"
      "v_mfma_f32_16x16x32_bf16 v[160:163], a[192:195], v[56:59], 0\n"
      "buffer_load_dwordx4 v1, s[8:11], s54 offen offset:1024 sc1 lds\n"
      "v_mfma_f32_16x16x32_bf16 v[160:163], a[196:199], v[60:63], v[160:163]\n"
      "v_mfma_f32_16x16x32_bf16 v[160:163], a[200:203], v[64:67], v[160:163]\n"
      "s_cbranch_vccnz .Lsp_dy2_7\n"
      ".Lbk_dy2_7:\n"
      "v_mfma_f32_16x16x32_bf16 v[160:163], a[204:207], v[68:71], v[160:163]\n"
      "ds_read_b128 v[68:71], v22 offset:3072\n"
      "v_mfma_f32_16x16x32_bf16 v[160:163], a[208:211], v[72:75], v[160:163]\n"
      "ds_read_b128 v[60:63], v22 offset:1024\n"
      "v_mfma_f32_16x16x32_bf16 v[160:163], a[212:215], v[76:79], v[160:163]\n"
      "s_waitcnt vmcnt(7)\n"
      "buffer_store_dword v18, v5, s[8:11], s59 offen sc1\n"
      "v_mfma_f32_16x16x32_bf16 v[160:163], a[216:219], v[80:83], v[160:163]\n"
      "v_cvt_pk_bf16_f32 v227, v158, v159\n"
      "v_mfma_f32_16x16x32_bf16 v[160:163], a[220:223], v[84:87], v[160:163]\n"
      "ds_read_b128 v[80:83], v22 offset:6144\n"
      "v_cvt_pk_bf16_f32 v226, v156, v157\n"
      "v_mfma_f32_16x16x32_bf16 v[160:163], a[224:227], v[88:91], v[160:163]\n"
      "ds_read_b128 v[72:75], v22 offset:4096\n"
      "v_mfma_f32_16x16x32_bf16 v[160:163], a[228:231], v[92:95], v[160:163]\n"
      "ds_read_b128 v[76:79], v22 offset:5120\n"
      "v_mfma_f32_16x16x32_bf16 v[160:163], a[232:235], v[96:99], v[160:163]\n"
      "ds_read_b128 v[96:99], v22 offset:10240\n"
      "v_mfma_f32_16x16x32_bf16 v[160:163], a[236:239], v[100:103], v[160:163]\n"
      "v_mfma_f32_16x16x32_bf16 v[160:163], a[240:243], v[104:107], v[160:163]\n"
      "buffer_store_dwordx4 v[224:227], v0, s[8:11], s37 offen sc1\n"
      "v_mfma_f32_16x16x32_bf16 v[160:163], a[244:247], v[108:111], v[160:163]\n"
      "v_mfma_f32_16x16x32_bf16 v[160:163], a[248:251], v[112:115], v[160:163]\n"
      "ds_read_b128 v[84:87], v22 offset:7168\n"
      "v_mfma_f32_16x16x32_bf16 v[160:163], a[252:255], v[116:119], v[160:163]\n"
      "v_mfma_f32_16x16x32_bf16 v[160:163], v[24:27], v[120:123], v[160:163]\n"
      "ds_read_b128 v[56:59], v22 offset:0\n"
      "v_mfma_f32_16x16x32_bf16 v[160:163], v[28:31], v[124:127], v[160:163]\n"
      "ds_read_b128 v[92:95], v22 offset:9216\n"
      "s_mov_b32 s54, s36\n"
      "v_mfma_f32_16x16x32_bf16 v[160:163], v[32:35], v[128:131], v[160:163]\n"
      "ds_read_b128 v[64:67], v22 offset:2048\n"
      "v_mfma_f32_16x16x32_bf16 v[160:163], v[36:39], v[132:135], v[160:163]\n"
      "ds_read_b128 v[100:103], v22 offset:11264\n"
      "v_mfma_f32_16x16x32_bf16 v[160:163], v[40:43], v[136:139], v[160:163]\n"
      "ds_read_b128 v[132:135], v22 offset:19456\n"
      "v_mfma_f32_16x16x32_bf16 v[160:163], v[44:47], v[140:143], v[160:163]\n"
      "v_mfma_f32_16x16x32_bf16 v[160:163], v[48:51], v[144:147], v[160:163]\n"
      "s_waitcnt lgkmcnt(4)\n"
      "ds_read_b128 v[112:115], v22 offset:14336\n"
      "s_add_u32 m0, s43, 98304\n"
      "v_mfma_f32_16x16x32_bf16 v[160:163], v[52:55], v[148:151], v[160:163]\n"
      "s_sub_u32 s62, s62, 1\n"
      "s_cbranch_scc1 .Ld_out1\n"
      ".Ld_t2:\n"
      "v_mfma_f32_16x16x32_bf16 v[152:155], a[0:3], v[56:59], 0\n"
      "ds_read_b128 v[88:91], v22 offset:8192\n"
      "v_mfma_f32_16x16x32_bf16 v[152:155], a[4:7], v[60:63], v[152:155]\n"
      "s_waitcnt lgkmcnt(4)\n"
      "ds_read_b128 v[120:123], v22 offset:16384\n"
      "v_mfma_f32_16x16x32_bf16 v[152:155], a[8:11], v[64:67], v[152:155]\n"
      "s_add_u32 s36, s36, 165888\n"
      "ds_read_b128 v[104:107], v22 offset:12288\n"
      "v_cvt_pk_bf16_f32 v229, v162, v163\n"
      "v_mfma_f32_16x16x32_bf16 v[152:155], a[12:15], v[68:71], v[152:155]\n"
      "ds_read_b128 v[148:151], v22 offset:23552\n"
      "v_cvt_pk_bf16_f32 v228, v160, v161\n"
      "v_mfma_f32_16x16x32_bf16 v[152:155], a[16:19], v[72:75], v[152:155]\n"
      "buffer_store_dwordx2 v[228:229], v13, s[8:11], s37 offen sc1\n"
      "v_mfma_f32_16x16x32_bf16 v[152:155], a[20:23], v[76:79], v[152:155]\n"
      "ds_read_b128 v[108:111], v22 offset:13312\n"
      "v_mfma_f32_16x16x32_bf16 v[152:155], a[24:27], v[80:83], v[152:155]\n"
      "ds_read_b128 v[116:119], v22 offset:15360\n"
      "v_mfma_f32_16x16x32_bf16 v[152:155], a[28:31], v[84:87], v[152:155]\n"
      "s_waitcnt lgkmcnt(5)\n"
      "ds_read_b128 v[124:127], v22 offset:17408\n"
      "v_mfma_f32_16x16x32_bf16 v[152:155], a[32:35], v[88:91], v[152:155]\n"
      "ds_read_b128 v[140:143], v22 offset:21504\n"
      "v_mfma_f32_16x16x32_bf16 v[152:155], a[36:39], v[92:95], v[152:155]\n"
      "ds_read_b128 v[128:131], v22 offset:18432\n"
      "v_mfma_f32_16x16x32_bf16 v[152:155], a[40:43], v[96:99], v[152:155]\n"
      "v_mfma_f32_16x16x32_bf16 v[152:155], a[44:47], v[100:103], v[152:155]\n"
      "s_waitcnt lgkmcnt(5)\n"
      "ds_read_b128 v[136:139], v22 offset:20480\n"
      "v_mfma_f32_16x16x32_bf16 v[152:155], a[48:51], v[104:107], v[152:155]\n"
      "s_waitcnt lgkmcnt(5)\n"
      "v_mfma_f32_16x16x32_bf16 v[152:155], a[52:55], v[108:111], v[152:155]\n"
      "v_mfma_f32_16x16x32_bf16 v[152:155], a[56:59], v[112:115], v[152:155]\n"
      "s_waitcnt lgkmcnt(3)\n"
      "ds_read_b128 v[144:147], v22 offset:22528\n"
      "v_mfma_f32_16x16x32_bf16 v[152:155], a[60:63], v[116:119], v[152:155]\n"
      "v_add_u32 v14, 1, v14\n"
      "v_mfma_f32_16x16x32_bf16 v[152:155], a[64:67], v[120:123], v[152:155]\n"
      "s_waitcnt vmcnt(7) lgkmcnt(2)\n"
      "v_cmp_ne_u32 vcc, s21, v20\n"
      "v_mfma_f32_16x16x32_bf16 v[152:155], a[68:71], v[124:127], v[152:155]\n"
      "v_mfma_f32_16x16x32_bf16 v[152:155], a[72:75], v[128:131], v[152:155]\n"
      "v_mfma_f32_16x16x32_bf16 v[152:155], a[76:79], v[132:135], v[152:155]\n"
      "s_waitcnt lgkmcnt(1)\n"
      "v_mfma_f32_16x16x32_bf16 v[152:155], a[80:83], v[136:139], v[152:155]\n"
      "v_mfma_f32_16x16x32_bf16 v[152:155], a[84:87], v[140:143], v[152:155]\n"
      "s_waitcnt lgkmcnt(0)\n"
      "s_cbranch_vccnz .Lsp_d4_8\n"
      ".Lbk_d4_8:\n"
      "v_mfma_f32_16x16x32_bf16 v[152:155], a[88:91], v[144:147], v[152:155]\n"
      "v_mfma_f32_16x16x32_bf16 v[152:155], a[92:95], v[148:151], v[152:155]\n"
      "s_min_u32 s36, s36, s6\n"
      "v_mfma_f32_16x16x32_bf16 v[156:159], a[96:99], v[56:59], 0\n"
      "buffer_load_dwordx4 v0, s[8:11], s54 offen offset:0 sc1 lds\n"
      "v_mfma_f32_16x16x32_bf16 v[156:159], a[100:103], v[60:63], v[156:159]\n"
      "v_mfma_f32_16x16x32_bf16 v[156:159], a[104:107], v[64:67], v[156:159]\n"
      "v_mfma_f32_16x16x32_bf16 v[156:159], a[108:111], v[68:71], v[156:159]\n"
      "v_mfma_f32_16x16x32_bf16 v[156:159], a[112:115], v[72:75], v[156:159]\n"
      "buffer_load_dwordx4 v0, s[8:11], s54 offen offset:2048 sc1 lds\n"
      "v_mfma_f32_16x16x32_bf16 v[156:159], a[116:119], v[76:79], v[156:159]\n"
      "s_waitcnt vmcnt(5)\n"
      "ds_write_b32 v16, v14\n"
      "v_mfma_f32_16x16x32_bf16 v[156:159], a[120:123], v[80:83], v[156:159]\n"
      "v_mfma_f32_16x16x32_bf16 v[156:159], a[124:127], v[84:87], v[156:159]\n"
      "v_mfma_f32_16x16x32_bf16 v[156:159], a[128:131], v[88:91], v[156:159]\n"
      "v_mfma_f32_16x16x32_bf16 v[156:159], a[132:135], v[92:95], v[156:159]\n"
      "buffer_load_dword v20, v2, s[8:11], s36 offen sc1\n"
      "v_mfma_f32_16x16x32_bf16 v[156:159], a[136:139], v[96:99], v[156:159]\n"
      "v_mfma_f32_16x16x32_bf16 v[156:159], a[140:143], v[100:103], v[156:159]\n"
      "v_cvt_pk_bf16_f32 v225, v154, v155\n"
      "v_mfma_f32_16x16x32_bf16 v[156:159], a[144:147], v[104:107], v[156:159]\n"
      "buffer_load_dwordx4 v0, s[8:11], s54 offen offset:3072 sc1 lds\n"
      "v_mfma_f32_16x16x32_bf16 v[156:159], a[148:151], v[108:111], v[156:159]\n"
      "v_mfma_f32_16x16x32_bf16 v[156:159], a[152:155], v[112:115], v[156:159]\n"
      "v_mfma_f32_16x16x32_bf16 v[156:159], a[156:159], v[116:119], v[156:159]\n"
      "ds_read_b32 v15, v17\n"
      "v_mfma_f32_16x16x32_bf16 v[156:159], a[160:163], v[120:123], v[156:159]\n"
      "buffer_load_dwordx4 v0, s[8:11], s54 offen offset:1024 sc1 lds\n"
      "v_mfma_f32_16x16x32_bf16 v[156:159], a[164:167], v[124:127], v[156:159]\n"
      "s_add_u32 m0, s43, 102400\n"
      "v_mfma_f32_16x16x32_bf16 v[156:159], a[168:171], v[128:131], v[156:159]\n"
      "v_mfma_f32_16x16x32_bf16 v[156:159], a[172:175], v[132:135], v[156:159]\n"
      "v_mfma_f32_16x16x32_bf16 v[156:159], a[176:179], v[136:139], v[156:159]\n"
      "buffer_load_dwordx4 v1, s[8:11], s54 offen offset:1024 sc1 lds\n"
      "v_mfma_f32_16x16x32_bf16 v[156:159], a[180:183], v[140:143], v[156:159]\n"
      "v_mfma_f32_16x16x32_bf16 v[156:159], a[184:187], v[144:147], v[156:159]\n"
      "s_waitcnt lgkmcnt(0)\n"
      "v_cmp_gt_i32 vcc, v14, v15\n"
      "v_mfma_f32_16x16x32_bf16 v[156:159], a[188:191], v[148:151], v[156:159]\n"
      "v_mfma_f32_16x16x32_bf16 v[160:163], a[192:195], v[56:59], 0\n"
      "buffer_load_dwordx4 v1, s[8:11], s54 offen offset:0 sc1 lds\n"
      "v_mfma_f32_16x16x32_bf16 v[160:163], a[196:199], v[60:63], v[160:163]\n"
      "s_cbranch_vccnz .Lsp_dy3_9\n"
      ".Lbk_dy3_9:\n"
      "ds_read_b128 v[60:63], v22 offset:25600\n"
      "v_mfma_f32_16x16x32_bf16 v[160:163], a[200:203], v[64:67], v[160:163]\n"
      "v_mfma_f32_16x16x32_bf16 v[160:163], a[204:207], v[68:71], v[160:163]\n"
      "v_cvt_pk_bf16_f32 v227, v158, v159\n"
      "v_mfma_f32_16x16x32_bf16 v[160:163], a[208:211], v[72:75], v[160:163]\n"
      "ds_read_b128 v[64:67], v22 offset:26624\n"
      "v_mfma_f32_16x16x32_bf16 v[160:163], a[212:215], v[76:79], v[160:163]\n"
      "s_mov_b32 s54, s36\n"
      "ds_read_b128 v[72:75], v22 offset:28672\n"
      "v_mfma_f32_16x16x32_bf16 v[160:163], a[216:219], v[80:83], v[160:163]\n"
      "ds_read_b128 v[68:71], v22 offset:27648\n"
      "v_mfma_f32_16x16x32_bf16 v[160:163], a[220:223], v[84:87], v[160:163]\n"
      "v_cvt_pk_bf16_f32 v224, v152, v153\n"
      "v_mfma_f32_16x16x32_bf16 v[160:163], a[224:227], v[88:91], v[160:163]\n"
      "s_waitcnt vmcnt(7) lgkmcnt(3)\n"
      "buffer_store_dword v18, v4, s[8:11], s55 offen sc1\n"
      "v_mfma_f32_16x16x32_bf16 v[160:163], a[228:231], v[92:95], v[160:163]\n"
      "ds_read_b128 v[92:95], v22 offset:33792\n"
      "v_mfma_f32_16x16x32_bf16 v[160:163], a[232:235], v[96:99], v[160:163]\n"
      "ds_read_b128 v[56:59], v22 offset:24576\n"
      "v_mfma_f32_16x16x32_bf16 v[160:163], a[236:239], v[100:103], v[160:163]\n"
      "buffer_load_dword v21, v3, s[8:11], s56 offen sc1\n"
      "v_mfma_f32_16x16x32_bf16 v[160:163], a[240:243], v[104:107], v[160:163]\n"
      "ds_read_b128 v[100:103], v22 offset:35840\n"
      "v_mfma_f32_16x16x32_bf16 v[160:163], a[244:247], v[108:111], v[160:163]\n"
      "ds_read_b128 v[80:83], v22 offset:30720\n"
      "v_mfma_f32_16x16x32_bf16 v[160:163], a[248:251], v[112:115], v[160:163]\n"
      "ds_read_b128 v[112:115], v22 offset:38912\n"
      "v_mfma_f32_16x16x32_bf16 v[160:163], a[252:255], v[116:119], v[160:163]\n"
      "s_add_u32 s37, s37, 106496\n"
      "ds_read_b128 v[88:91], v22 offset:32768\n"
      "v_mfma_f32_16x16x32_bf16 v[160:163], v[24:27], v[120:123], v[160:163]\n"
      "ds_read_b128 v[96:99], v22 offset:34816\n"
      "v_cvt_pk_bf16_f32 v226, v156, v157\n"
      "v_mfma_f32_16x16x32_bf16 v[160:163], v[28:31], v[124:127], v[160:163]\n"
      "ds_read_b128 v[76:79], v22 offset:29696\n"
      "v_mfma_f32_16x16x32_bf16 v[160:163], v[32:35], v[128:131], v[160:163]\n"
      "v_mfma_f32_16x16x32_bf16 v[160:163], v[36:39], v[132:135], v[160:163]\n"
      "buffer_store_dwordx4 v[224:227], v0, s[8:11], s37 offen sc1\n"
      "v_mfma_f32_16x16x32_bf16 v[160:163], v[40:43], v[136:139], v[160:163]\n"
      "ds_read_b128 v[116:119], v22 offset:39936\n"
      "v_mfma_f32_16x16x32_bf16 v[160:163], v[44:47], v[140:143], v[160:163]\n"
      "ds_read_b128 v[84:87], v22 offset:31744\n"
      "v_mfma_f32_16x16x32_bf16 v[160:163], v[48:51], v[144:147], v[160:163]\n"
      "s_waitcnt lgkmcnt(4)\n"
      "ds_read_b128 v[104:107], v22 offset:36864\n"
      "v_mfma_f32_16x16x32_bf16 v[160:163], v[52:55], v[148:151], v[160:163]\n"
      "s_sub_u32 s62, s62, 1\n"
      "s_cbranch_scc1 .Ld_out2\n"
      ".Ld_t3:\n"
      "v_mfma_f32_16x16x32_bf16 v[152:155], a[0:3], v[56:59], 0\n"
      "ds_read_b128 v[120:123], v22 offset:40960\n"
      "v_mfma_f32_16x16x32_bf16 v[152:155], a[4:7], v[60:63], v[152:155]\n"
      "ds_read_b128 v[108:111], v22 offset:37888\n"
      "v_mfma_f32_16x16x32_bf16 v[152:155], a[8:11], v[64:67], v[152:155]\n"
      "ds_read_b128 v[132:135], v22 offset:44032\n"
      "v_mfma_f32_16x16x32_bf16 v[152:155], a[12:15], v[68:71], v[152:155]\n"
      "ds_read_b128 v[136:139], v22 offset:45056\n"
      "v_cvt_pk_bf16_f32 v229, v162, v163\n"
      "v_mfma_f32_16x16x32_bf16 v[152:155], a[16:19], v[72:75], v[152:155]\n"
      "s_waitcnt lgkmcnt(5)\n"
      "v_mfma_f32_16x16x32_bf16 v[152:155], a[20:23], v[76:79], v[152:155]\n"
      "ds_read_b128 v[124:127], v22 offset:41984\n"
      "v_mfma_f32_16x16x32_bf16 v[152:155], a[24:27], v[80:83], v[152:155]\n"
      "s_add_u32 m0, s43, 122880\n"
      "v_cvt_pk_bf16_f32 v228, v160, v161\n"
      "v_mfma_f32_16x16x32_bf16 v[152:155], a[28:31], v[84:87], v[152:155]\n"
      "buffer_store_dwordx2 v[228:229], v13, s[8:11], s37 offen sc1\n"
      "v_mfma_f32_16x16x32_bf16 v[152:155], a[32:35], v[88:91], v[152:155]\n"
      "v_mfma_f32_16x16x32_bf16 v[152:155], a[36:39], v[92:95], v[152:155]\n"
      "ds_read_b128 v[128:131], v22 offset:43008\n"
      "v_mfma_f32_16x16x32_bf16 v[152:155], a[40:43], v[96:99], v[152:155]\n"
      "ds_read_b128 v[144:147], v22 offset:47104\n"
      "v_mfma_f32_16x16x32_bf16 v[152:155], a[44:47], v[100:103], v[152:155]\n"
      "s_waitcnt lgkmcnt(3)\n"
      "ds_read_b128 v[148:151], v22 offset:48128\n"
      "v_mfma_f32_16x16x32_bf16 v[152:155], a[48:51], v[104:107], v[152:155]\n"
      "ds_read_b128 v[140:143], v22 offset:46080\n"
      "v_mfma_f32_16x16x32_bf16 v[152:155], a[52:55], v[108:111], v[152:155]\n"
      "v_mfma_f32_16x16x32_bf16 v[152:155], a[56:59], v[112:115], v[152:155]\n"
      "s_waitcnt vmcnt(8) lgkmcnt(4)\n"
      "v_cmp_ne_u32 vcc, s21, v20\n"
      "v_mfma_f32_16x16x32_bf16 v[152:155], a[60:63], v[116:119], v[152:155]\n"
      "v_mfma_f32_16x16x32_bf16 v[152:155], a[64:67], v[120:123], v[152:155]\n"
      "s_add_u32 s36, s36, 165888\n"
      "s_add_u32 s55, s55, 106496\n"
      "v_mfma_f32_16x16x32_bf16 v[152:155], a[68:71], v[124:127], v[152:155]\n"
      "s_waitcnt lgkmcnt(2)\n"
      "v_mfma_f32_16x16x32_bf16 v[152:155], a[72:75], v[128:131], v[152:155]\n"
      "s_cbranch_vccnz .Lsp_d5_10\n"
      ".Lbk_d5_10:\n"
      "v_mfma_f32_16x16x32_bf16 v[152:155], a[76:79], v[132:135], v[152:155]\n"
      "buffer_load_dwordx4 v0, s[8:11], s54 offen offset:1024 sc1 lds\n"
      "v_mfma_f32_16x16x32_bf16 v[152:155], a[80:83], v[136:139], v[152:155]\n"
      "s_waitcnt lgkmcnt(0)\n"
      "v_mfma_f32_16x16x32_bf16 v[152:155], a[84:87], v[140:143], v[152:155]\n"
      "v_add_u32 v14, 1, v14\n"
      "v_mfma_f32_16x16x32_bf16 v[152:155], a[88:91], v[144:147], v[152:155]\n"
      "v_mfma_f32_16x16x32_bf16 v[152:155], a[92:95], v[148:151], v[152:155]\n"
      "buffer_load_dwordx4 v0, s[8:11], s54 offen offset:0 sc1 lds\n"
      "v_mfma_f32_16x16x32_bf16 v[156:159], a[96:99], v[56:59], 0\n"
      "v_mfma_f32_16x16x32_bf16 v[156:159], a[100:103], v[60:63], v[156:159]\n"
      "v_mfma_f32_16x16x32_bf16 v[156:159], a[104:107], v[64:67], v[156:159]\n"
      "v_mfma_f32_16x16x32_bf16 v[156:159], a[108:111], v[68:71], v[156:159]\n"
      "buffer_load_dwordx4 v0, s[8:11], s54 offen offset:2048 sc1 lds\n"
      "v_mfma_f32_16x16x32_bf16 v[156:159], a[112:115], v[72:75], v[156:159]\n"
      "s_waitcnt vmcnt(7)\n"
      "ds_write_b32 v16, v14\n"
      "v_mfma_f32_16x16x32_bf16 v[156:159], a[116:119], v[76:79], v[156:159]\n"
      "v_mfma_f32_16x16x32_bf16 v[156:159], a[120:123], v[80:83], v[156:159]\n"
      "v_mfma_f32_16x16x32_bf16 v[156:159], a[124:127], v[84:87], v[156:159]\n"
      "buffer_load_dwordx4 v0, s[8:11], s54 offen offset:3072 sc1 lds\n"
      "v_mfma_f32_16x16x32_bf16 v[156:159], a[128:131], v[88:91], v[156:159]\n"
      "v_mfma_f32_16x16x32_bf16 v[156:159], a[132:135], v[92:95], v[156:159]\n"
      "v_cvt_pk_bf16_f32 v224, v152, v153\n"
      "v_mfma_f32_16x16x32_bf16 v[156:159], a[136:139], v[96:99], v[156:159]\n"
      "v_mfma_f32_16x16x32_bf16 v[156:159], a[140:143], v[100:103], v[156:159]\n"
      "s_min_u32 s36, s36, s6\n"
      "s_add_u32 m0, s43, 126976\n"
      "v_mfma_f32_16x16x32_bf16 v[156:159], a[144:147], v[104:107], v[156:159]\n"
      "buffer_load_dwordx4 v1, s[8:11], s54 offen offset:1024 sc1 lds\n"
      "v_mfma_f32_16x16x32_bf16 v[156:159], a[148:151], v[108:111], v[156:159]\n"
      "ds_read_b32 v15, v17\n"
      "v_mfma_f32_16x16x32_bf16 v[156:159], a[152:155], v[112:115], v[156:159]\n"
      "v_mfma_f32_16x16x32_bf16 v[156:159], a[156:159], v[116:119], v[156:159]\n"
      "v_mfma_f32_16x16x32_bf16 v[156:159], a[160:163], v[120:123], v[156:159]\n"
      "buffer_load_dword v20, v2, s[8:11], s36 offen sc1\n"
      "v_mfma_f32_16x16x32_bf16 v[156:159], a[164:167], v[124:127], v[156:159]\n"
      "v_mfma_f32_16x16x32_bf16 v[156:159], a[168:171], v[128:131], v[156:159]\n"
      "s_waitcnt lgkmcnt(0)\n"
      "v_cmp_gt_i32 vcc, v14, v15\n"
      "v_mfma_f32_16x16x32_bf16 v[156:159], a[172:175], v[132:135], v[156:159]\n"
      "buffer_load_dwordx4 v1, s[8:11], s54 offen offset:0 sc1 lds\n"
      "v_mfma_f32_16x16x32_bf16 v[156:159], a[176:179], v[136:139], v[156:159]\n"
      "s_add_u32 s58, s56, 6656\n"
      "v_mfma_f32_16x16x32_bf16 v[156:159], a[180:183], v[140:143], v[156:159]\n"
      "s_add_u32 s57, s56, 13312\n"
      "v_mfma_f32_16x16x32_bf16 v[156:159], a[184:187], v[144:147], v[156:159]\n"
      "s_waitcnt vmcnt(9)\n"
      "s_cbranch_vccnz .Lsp_dy4_11\n"
      ".Lbk_dy4_11:\n"
      "v_cmp_ne_u32 vcc, s21, v21\n"
      "v_mfma_f32_16x16x32_bf16 v[156:159], a[188:191], v[148:151], v[156:159]\n"
      ".Ld_p1:\n"
      "s_cbranch_execz .Ld_pr\n"
      "v_mfma_f32_16x16x32_bf16 v[160:163], a[192:195], v[56:59], 0\n"
      "v_mfma_f32_16x16x32_bf16 v[160:163], a[196:199], v[60:63], v[160:163]\n"
      "s_cbranch_vccnz .Lsp_df_12\n"
      ".Lbk_df_12:\n"
      "buffer_load_dwordx4 v[176:179], v0, s[8:11], s56 offen sc1\n"
      "v_mfma_f32_16x16x32_bf16 v[160:163], a[200:203], v[64:67], v[160:163]\n"
      "ds_read_b128 v[60:63], v23 offset:1024\n"
      "v_mfma_f32_16x16x32_bf16 v[160:163], a[204:207], v[68:71], v[160:163]\n"
      "ds_read_b128 v[64:67], v23 offset:2048\n"
      "v_mfma_f32_16x16x32_bf16 v[160:163], a[208:211], v[72:75], v[160:163]\n"
      "buffer_load_dwordx2 v[180:181], v13, s[8:11], s56 offen sc1\n"
      "v_mfma_f32_16x16x32_bf16 v[160:163], a[212:215], v[76:79], v[160:163]\n"
      "ds_read_b128 v[72:75], v23 offset:4096\n"
      "v_mfma_f32_16x16x32_bf16 v[160:163], a[216:219], v[80:83], v[160:163]\n"
      "ds_read_b128 v[68:71], v23 offset:3072\n"
      "s_add_u32 s61, s41, 4294967264\n"
      "v_mfma_f32_16x16x32_bf16 v[160:163], a[220:223], v[84:87], v[160:163]\n"
      "ds_read_b128 v[80:83], v23 offset:6144\n"
      "v_mfma_f32_16x16x32_bf16 v[160:163], a[224:227], v[88:91], v[160:163]\n"
      "v_cvt_pk_bf16_f32 v225, v154, v155\n"
      "v_mfma_f32_16x16x32_bf16 v[160:163], a[228:231], v[92:95], v[160:163]\n"
      "buffer_load_dwordx4 v[184:187], v0, s[8:11], s58 offen sc1\n"
      "v_mfma_f32_16x16x32_bf16 v[160:163], a[232:235], v[96:99], v[160:163]\n"
      "ds_read_b128 v[84:87], v23 offset:7168\n"
      "v_cvt_pk_bf16_f32 v226, v156, v157\n"
      "v_mfma_f32_16x16x32_bf16 v[160:163], a[236:239], v[100:103], v[160:163]\n"
      "ds_read_b128 v[76:79], v23 offset:5120\n"
      "v_mfma_f32_16x16x32_bf16 v[160:163], a[240:243], v[104:107], v[160:163]\n"
      "s_add_u32 s37, s37, 106496\n"
      "ds_read_b128 v[56:59], v23 offset:0\n"
      "v_mfma_f32_16x16x32_bf16 v[160:163], a[244:247], v[108:111], v[160:163]\n"
      "s_waitcnt vmcnt(10) lgkmcnt(4)\n"
      "buffer_store_dword v18, v4, s[8:11], s55 offen sc1\n"
      "v_mfma_f32_16x16x32_bf16 v[160:163], a[248:251], v[112:115], v[160:163]\n"
      "ds_read_b128 v[88:91], v23 offset:8192\n"
      "v_mfma_f32_16x16x32_bf16 v[160:163], a[252:255], v[116:119], v[160:163]\n"
      "ds_read_b128 v[96:99], v23 offset:10240\n"
      "v_mfma_f32_16x16x32_bf16 v[160:163], v[24:27], v[120:123], v[160:163]\n"
      "buffer_load_dwordx4 v[192:195], v0, s[8:11], s57 offen sc1\n"
      "v_mfma_f32_16x16x32_bf16 v[160:163], v[28:31], v[124:127], v[160:163]\n"
      "v_mfma_f32_16x16x32_bf16 v[160:163], v[32:35], v[128:131], v[160:163]\n"
      "buffer_load_dwordx2 v[188:189], v13, s[8:11], s58 offen sc1\n"
      "v_mfma_f32_16x16x32_bf16 v[160:163], v[36:39], v[132:135], v[160:163]\n"
      "ds_read_b128 v[100:103], v23 offset:11264\n"
      "v_mfma_f32_16x16x32_bf16 v[160:163], v[40:43], v[136:139], v[160:163]\n"
      "s_add_u32 s58, s56, 19968\n"
      "ds_read_b128 v[132:135], v23 offset:19456\n"
      "v_mfma_f32_16x16x32_bf16 v[160:163], v[44:47], v[140:143], v[160:163]\n"
      "ds_read_b128 v[104:107], v23 offset:12288\n"
      "v_mfma_f32_16x16x32_bf16 v[160:163], v[48:51], v[144:147], v[160:163]\n"
      "s_waitcnt lgkmcnt(3)\n"
      "buffer_load_dwordx2 v[196:197], v13, s[8:11], s57 offen sc1\n"
      "v_mfma_f32_16x16x32_bf16 v[160:163], v[52:55], v[148:151], v[160:163]\n"
      "s_sub_u32 s62, s62, 1\n"
      "s_cbranch_scc1 .Ld_out3\n"
      ".Ld_t4:\n"
      "v_mfma_f32_16x16x32_bf16 v[152:155], a[0:3], v[56:59], 0\n"
      "ds_read_b128 v[92:95], v23 offset:9216\n"
      "v_mfma_f32_16x16x32_bf16 v[152:155], a[4:7], v[60:63], v[152:155]\n"
      "ds_read_b128 v[108:111], v23 offset:13312\n"
      "s_add_u32 m0, s43, 0\n"
      "v_mfma_f32_16x16x32_bf16 v[152:155], a[8:11], v[64:67], v[152:155]\n"
      "ds_read_b128 v[128:131], v23 offset:18432\n"
      "v_cvt_pk_bf16_f32 v228, v160, v161\n"
      "v_mfma_f32_16x16x32_bf16 v[152:155], a[12:15], v[68:71], v[152:155]\n"
      "buffer_load_dwordx4 v[200:203], v0, s[8:11], s58 offen sc1\n"
      "v_mfma_f32_16x16x32_bf16 v[152:155], a[16:19], v[72:75], v[152:155]\n"
      "ds_read_b128 v[116:119], v23 offset:15360\n"
      "v_cvt_pk_bf16_f32 v227, v158, v159\n"
      "v_mfma_f32_16x16x32_bf16 v[152:155], a[20:23], v[76:79], v[152:155]\n"
      "ds_read_b128 v[112:115], v23 offset:14336\n"
      "v_mfma_f32_16x16x32_bf16 v[152:155], a[24:27], v[80:83], v[152:155]\n"
      "s_mov_b32 s54, s36\n"
      "ds_read_b128 v[120:123], v23 offset:16384\n"
      "v_mfma_f32_16x16x32_bf16 v[152:155], a[28:31], v[84:87], v[152:155]\n"
      "buffer_store_dwordx4 v[224:227], v0, s[8:11], s37 offen sc1\n"
      "v_mfma_f32_16x16x32_bf16 v[152:155], a[32:35], v[88:91], v[152:155]\n"
      "s_waitcnt lgkmcnt(4)\n"
      "ds_read_b128 v[140:143], v23 offset:21504\n"
      "v_mfma_f32_16x16x32_bf16 v[152:155], a[36:39], v[92:95], v[152:155]\n"
      "ds_read_b128 v[124:127], v23 offset:17408\n"
      "v_mfma_f32_16x16x32_bf16 v[152:155], a[40:43], v[96:99], v[152:155]\n"
      "ds_read_b128 v[136:139], v23 offset:20480\n"
      "v_mfma_f32_16x16x32_bf16 v[152:155], a[44:47], v[100:103], v[152:155]\n"
      "v_cvt_pk_bf16_f32 v229, v162, v163\n"
      "v_mfma_f32_16x16x32_bf16 v[152:155], a[48:51], v[104:107], v[152:155]\n"
      "buffer_store_dwordx2 v[228:229], v13, s[8:11], s37 offen sc1\n"
      "v_mfma_f32_16x16x32_bf16 v[152:155], a[52:55], v[108:111], v[152:155]\n"
      "s_waitcnt lgkmcnt(3)\n"
      "ds_read_b128 v[148:151], v23 offset:23552\n"
      "v_add_u32_e64 v14, 1, v14\n"
      "v_mfma_f32_16x16x32_bf16 v[152:155], a[56:59], v[112:115], v[152:155]\n"
      "s_add_u32 s36, s36, 165888\n"
      "s_add_u32 s37, s37, 106496\n"
      "ds_read_b128 v[144:147], v23 offset:22528\n"
      "v_mfma_f32_16x16x32_bf16 v[152:155], a[60:63], v[116:119], v[152:155]\n"
      "v_mfma_f32_16x16x32_bf16 v[152:155], a[64:67], v[120:123], v[152:155]\n"
      "s_waitcnt lgkmcnt(3)\n"
      "s_min_u32 s36, s36, s6\n"
      "v_mfma_f32_16x16x32_bf16 v[152:155], a[68:71], v[124:127], v[152:155]\n"
      "buffer_load_dwordx2 v[204:205], v13, s[8:11], s58 offen sc1\n"
      "v_mfma_f32_16x16x32_bf16 v[152:155], a[72:75], v[128:131], v[152:155]\n"
      "s_cmp_lt_u32 s41, 32\n"
      "s_cselect_b32 s59, s39, 0x40000000\n"
      "v_mfma_f32_16x16x32_bf16 v[152:155], a[76:79], v[132:135], v[152:155]\n"
      "s_waitcnt lgkmcnt(2)\n"
      "s_add_u32 s57, s56, 26624\n"
      "v_mfma_f32_16x16x32_bf16 v[152:155], a[80:83], v[136:139], v[152:155]\n"
      "s_waitcnt vmcnt(12) lgkmcnt(1)\n"
      "s_add_u32 s56, s56, 599040\n"
      "v_cmp_ne_u32 vcc, s21, v20\n"
      "v_mfma_f32_16x16x32_bf16 v[152:155], a[84:87], v[140:143], v[152:155]\n"
      "s_waitcnt lgkmcnt(0)\n"
      "buffer_load_dwordx2 v[212:213], v13, s[8:11], s57 offen sc1\n"
      "v_mfma_f32_16x16x32_bf16 v[152:155], a[88:91], v[144:147], v[152:155]\n"
      "v_mfma_f32_16x16x32_bf16 v[152:155], a[92:95], v[148:151], v[152:155]\n"
      "s_add_u32 s39, s39, 147456\n"
      "v_mfma_f32_16x16x32_bf16 v[156:159], a[96:99], v[56:59], 0\n"
      "s_waitcnt vmcnt(12)\n"
      "ds_write_b32 v16, v14\n"
      "v_mfma_f32_16x16x32_bf16 v[156:159], a[100:103], v[60:63], v[156:159]\n"
      "buffer_load_dwordx4 v[208:211], v0, s[8:11], s57 offen sc1\n"
      "v_mfma_f32_16x16x32_bf16 v[156:159], a[104:107], v[64:67], v[156:159]\n"
      "s_cmp_lt_u32 s61, 32\n"
      "s_cselect_b32 s60, s40, 0x40000000\n"
      "v_mfma_f32_16x16x32_bf16 v[156:159], a[108:111], v[68:71], v[156:159]\n"
      "s_cbranch_vccnz .Lsp_d0_13\n"
      ".Lbk_d0_13:\n"
      "v_cvt_pk_bf16_f32 v224, v152, v153\n"
      "v_mfma_f32_16x16x32_bf16 v[156:159], a[112:115], v[72:75], v[156:159]\n"
      "v_cvt_pk_bf16_f32 v225, v154, v155\n"
      "v_mfma_f32_16x16x32_bf16 v[156:159], a[116:119], v[76:79], v[156:159]\n"
      "buffer_load_dwordx4 v0, s[8:11], s54 offen offset:0 sc1 lds\n"
      "v_mfma_f32_16x16x32_bf16 v[156:159], a[120:123], v[80:83], v[156:159]\n"
      "s_waitcnt vmcnt(13)\n"
      "v_lshlrev_b32 v234, 16, v178\n"
      "v_lshlrev_b32 v233, 16, v177\n"
      "v_mfma_f32_16x16x32_bf16 v[156:159], a[124:127], v[84:87], v[156:159]\n"
      "v_lshlrev_b32 v232, 16, v176\n"
      "v_add_f32 v166, v166, v233\n"
      "v_mfma_f32_16x16x32_bf16 v[156:159], a[128:131], v[88:91], v[156:159]\n"
      "s_add_u32 s55, s55, 106496\n"
      "v_add_f32 v164, v164, v232\n"
      "v_mfma_f32_16x16x32_bf16 v[156:159], a[132:135], v[92:95], v[156:159]\n"
      "buffer_load_dwordx4 v0, s[8:11], s54 offen offset:1024 sc1 lds\n"
      "v_mfma_f32_16x16x32_bf16 v[156:159], a[136:139], v[96:99], v[156:159]\n"
      "s_waitcnt vmcnt(13) lgkmcnt(0)\n"
      "v_lshlrev_b32 v233, 16, v181\n"
      "v_lshlrev_b32 v235, 16, v179\n"
      "v_mfma_f32_16x16x32_bf16 v[156:159], a[140:143], v[100:103], v[156:159]\n"
      "v_and_b32 v176, 0xffff0000, v176\n"
      "v_lshlrev_b32 v232, 16, v180\n"
      "v_mfma_f32_16x16x32_bf16 v[156:159], a[144:147], v[104:107], v[156:159]\n"
      "v_add_f32 v172, v172, v232\n"
      "v_and_b32 v181, 0xffff0000, v181\n"
      "v_mfma_f32_16x16x32_bf16 v[156:159], a[148:151], v[108:111], v[156:159]\n"
      "v_add_f32 v168, v168, v234\n"
      "v_add_f32 v174, v174, v233\n"
      "v_mfma_f32_16x16x32_bf16 v[156:159], a[152:155], v[112:115], v[156:159]\n"
      "buffer_load_dword v20, v2, s[8:11], s36 offen sc1\n"
      "v_mfma_f32_16x16x32_bf16 v[156:159], a[156:159], v[116:119], v[156:159]\n"
      "ds_read_b32 v15, v17\n"
      "v_add_f32 v165, v165, v176\n"
      "v_mfma_f32_16x16x32_bf16 v[156:159], a[160:163], v[120:123], v[156:159]\n"
      "buffer_load_dwordx4 v0, s[8:11], s54 offen offset:2048 sc1 lds\n"
      "v_mfma_f32_16x16x32_bf16 v[156:159], a[164:167], v[124:127], v[156:159]\n"
      "s_waitcnt vmcnt(14)\n"
      "v_lshlrev_b32 v232, 16, v184\n"
      "v_and_b32 v177, 0xffff0000, v177\n"
      "v_mfma_f32_16x16x32_bf16 v[156:159], a[168:171], v[128:131], v[156:159]\n"
      "s_add_u32 s40, s40, 4294856704\n"
      "v_add_f32 v167, v167, v177\n"
      "v_and_b32 v180, 0xffff0000, v180\n"
      "v_mfma_f32_16x16x32_bf16 v[156:159], a[172:175], v[132:135], v[156:159]\n"
      "v_add_f32 v164, v164, v232\n"
      "v_lshlrev_b32 v233, 16, v185\n"
      "v_mfma_f32_16x16x32_bf16 v[156:159], a[176:179], v[136:139], v[156:159]\n"
      "buffer_load_dwordx4 v0, s[8:11], s54 offen offset:3072 sc1 lds\n"
      "v_mfma_f32_16x16x32_bf16 v[156:159], a[180:183], v[140:143], v[156:159]\n"
      "s_waitcnt lgkmcnt(0)\n"
      "v_add_f32 v166, v166, v233\n"
      "v_cmp_gt_i32 vcc, v14, v15\n"
      "v_mfma_f32_16x16x32_bf16 v[156:159], a[184:187], v[144:147], v[156:159]\n"
      "s_waitcnt vmcnt(12)\n"
      "v_lshlrev_b32 v232, 16, v188\n"
      "v_add_f32 v172, v172, v232\n"
      "v_mfma_f32_16x16x32_bf16 v[156:159], a[188:191], v[148:151], v[156:159]\n"
      "v_and_b32 v185, 0xffff0000, v185\n"
      "v_lshlrev_b32 v232, 16, v192\n"
      "v_mfma_f32_16x16x32_bf16 v[160:163], a[192:195], v[56:59], 0\n"
      "s_add_u32 m0, s43, 4096\n"
      "v_lshlrev_b32 v234, 16, v186\n"
      "v_add_f32 v164, v164, v232\n"
      "v_mfma_f32_16x16x32_bf16 v[160:163], a[196:199], v[60:63], v[160:163]\n"
      "buffer_load_dwordx4 v1, s[8:11], s54 offen offset:1024 sc1 lds\n"
      "v_mfma_f32_16x16x32_bf16 v[160:163], a[200:203], v[64:67], v[160:163]\n"
      "s_cbranch_vccnz .Lsp_dy5_14\n"
      ".Lbk_dy5_14:\n"
      "v_cvt_pk_bf16_f32 v227, v158, v159\n"
      "v_mfma_f32_16x16x32_bf16 v[160:163], a[204:207], v[68:71], v[160:163]\n"
      "ds_read_b128 v[56:59], v23 offset:24576\n"
      "v_lshlrev_b32 v233, 16, v189\n"
      "v_mfma_f32_16x16x32_bf16 v[160:163], a[208:211], v[72:75], v[160:163]\n"
      "s_waitcnt vmcnt(12)\n"
      "ds_read_b128 v[64:67], v23 offset:26624\n"
      "v_lshlrev_b32 v232, 16, v196\n"
      "v_mfma_f32_16x16x32_bf16 v[160:163], a[212:215], v[76:79], v[160:163]\n"
      "buffer_load_dwordx4 v1, s[8:11], s54 offen offset:0 sc1 lds\n"
      "v_mfma_f32_16x16x32_bf16 v[160:163], a[216:219], v[80:83], v[160:163]\n"
      "ds_read_b128 v[76:79], v23 offset:29696\n"
      "v_add_f32 v174, v174, v233\n"
      "v_mfma_f32_16x16x32_bf16 v[160:163], a[220:223], v[84:87], v[160:163]\n"
      "v_and_b32 v184, 0xffff0000, v184\n"
      "v_add_f32 v172, v172, v232\n"
      "v_mfma_f32_16x16x32_bf16 v[160:163], a[224:227], v[88:91], v[160:163]\n"
      "v_add_f32 v170, v170, v235\n"
      "v_cvt_pk_bf16_f32 v226, v156, v157\n"
      "v_mfma_f32_16x16x32_bf16 v[160:163], a[228:231], v[92:95], v[160:163]\n"
      "s_add_u32 s41, s41, 6\n"
      "ds_read_b128 v[88:91], v23 offset:32768\n"
      "v_add_f32 v165, v165, v184\n"
      "v_mfma_f32_16x16x32_bf16 v[160:163], a[232:235], v[96:99], v[160:163]\n"
      "ds_read_b128 v[68:71], v23 offset:27648\n"
      "v_mfma_f32_16x16x32_bf16 v[160:163], a[236:239], v[100:103], v[160:163]\n"
      "ds_read_b128 v[96:99], v23 offset:34816\n"
      "v_add_f32 v168, v168, v234\n"
      "v_mfma_f32_16x16x32_bf16 v[160:163], a[240:243], v[104:107], v[160:163]\n"
      "ds_read_b128 v[80:83], v23 offset:30720\n"
      "v_lshlrev_b32 v233, 16, v193\n"
      "v_mfma_f32_16x16x32_bf16 v[160:163], a[244:247], v[108:111], v[160:163]\n"
      "ds_read_b128 v[72:75], v23 offset:28672\n"
      "v_and_b32 v178, 0xffff0000, v178\n"
      "v_mfma_f32_16x16x32_bf16 v[160:163], a[248:251], v[112:115], v[160:163]\n"
      "s_waitcnt vmcnt(12) lgkmcnt(5)\n"
      "s_mov_b32 s54, s36\n"
      "ds_read_b128 v[112:115], v23 offset:38912\n"
      "v_lshlrev_b32 v232, 16, v200\n"
      "v_mfma_f32_16x16x32_bf16 v[160:163], a[252:255], v[116:119], v[160:163]\n"
      "v_add_f32 v164, v164, v232\n"
      "v_and_b32 v186, 0xffff0000, v186\n"
      "v_mfma_f32_16x16x32_bf16 v[160:163], v[24:27], v[120:123], v[160:163]\n"
      "s_waitcnt vmcnt(10) lgkmcnt(5)\n"
      "buffer_store_dword v18, v4, s[8:11], s55 offen sc1\n"
      "v_mfma_f32_16x16x32_bf16 v[160:163], v[28:31], v[124:127], v[160:163]\n"
      "ds_read_b128 v[60:63], v23 offset:25600\n"
      "v_add_f32 v169, v169, v178\n"
      "v_mfma_f32_16x16x32_bf16 v[160:163], v[32:35], v[128:131], v[160:163]\n"
      "buffer_store_dwordx4 v[224:227], v0, s[8:11], s37 offen sc1\n"
      "v_mfma_f32_16x16x32_bf16 v[160:163], v[36:39], v[132:135], v[160:163]\n"
      "ds_read_b128 v[104:107], v23 offset:36864\n"
      "v_and_b32 v200, 0xffff0000, v200\n"
      "v_mfma_f32_16x16x32_bf16 v[160:163], v[40:43], v[136:139], v[160:163]\n"
      "ds_read_b128 v[84:87], v23 offset:31744\n"
      "v_add_f32 v173, v173, v180\n"
      "v_mfma_f32_16x16x32_bf16 v[160:163], v[44:47], v[140:143], v[160:163]\n"
      "ds_read_b128 v[140:143], v23 offset:46080\n"
      "v_add_f32 v166, v166, v233\n"
      "v_mfma_f32_16x16x32_bf16 v[160:163], v[48:51], v[144:147], v[160:163]\n"
      "ds_read_b128 v[100:103], v23 offset:35840\n"
      "v_lshlrev_b32_e64 v234, 16, v194\n"
      "v_mfma_f32_16x16x32_bf16 v[160:163], v[52:55], v[148:151], v[160:163]\n"
      "s_sub_u32 s62, s62, 1\n"
      "s_cbranch_scc1 .Ld_out4\n"
      ".Ld_t5:\n"
      "v_mfma_f32_16x16x32_bf16 v[152:155], a[0:3], v[56:59], 0\n"
      "s_waitcnt lgkmcnt(4)\n"
      "ds_read_b128 v[92:95], v23 offset:33792\n"
      "ds_read_b128 v[116:119], v23 offset:39936\n"
      "v_mfma_f32_16x16x32_bf16 v[152:155], a[4:7], v[60:63], v[152:155]\n"
      "ds_read_b128 v[108:111], v23 offset:37888\n"
      "v_and_b32 v192, 0xffff0000, v192\n"
      "v_mfma_f32_16x16x32_bf16 v[152:155], a[8:11], v[64:67], v[152:155]\n"
      "ds_read_b128 v[128:131], v23 offset:43008\n"
      "v_cvt_pk_bf16_f32 v229, v162, v163\n"
      "v_mfma_f32_16x16x32_bf16 v[152:155], a[12:15], v[68:71], v[152:155]\n"
      "ds_read_b128 v[136:139], v23 offset:45056\n"
      "v_add_f32 v175, v175, v181\n"
      "v_mfma_f32_16x16x32_bf16 v[152:155], a[16:19], v[72:75], v[152:155]\n"
      "v_cvt_pk_bf16_f32 v228, v160, v161\n"
      "v_and_b32 v189, 0xffff0000, v189\n"
      "v_mfma_f32_16x16x32_bf16 v[152:155], a[20:23], v[76:79], v[152:155]\n"
      "s_waitcnt vmcnt(11) lgkmcnt(5)\n"
      "ds_read_b128 v[144:147], v23 offset:47104\n"
      "v_lshlrev_b32 v232, 16, v204\n"
      "v_mfma_f32_16x16x32_bf16 v[152:155], a[24:27], v[80:83], v[152:155]\n"
      "buffer_store_dwordx2 v[228:229], v13, s[8:11], s37 offen sc1\n"
      "v_mfma_f32_16x16x32_bf16 v[152:155], a[28:31], v[84:87], v[152:155]\n"
      "v_lshlrev_b32 v235, 16, v187\n"
      "v_add_f32 v172, v172, v232\n"
      "v_mfma_f32_16x16x32_bf16 v[152:155], a[32:35], v[88:91], v[152:155]\n"
      "s_waitcnt vmcnt(10) lgkmcnt(3)\n"
      "ds_read_b128 v[120:123], v23 offset:40960\n"
      "v_lshlrev_b32 v232, 16, v208\n"
      "v_mfma_f32_16x16x32_bf16 v[152:155], a[36:39], v[92:95], v[152:155]\n"
      "ds_read_b128 v[124:127], v23 offset:41984\n"
      "v_add_f32 v170, v170, v235\n"
      "v_mfma_f32_16x16x32_bf16 v[152:155], a[40:43], v[96:99], v[152:155]\n"
      "s_add_u32 m0, s43, 24576\n"
      "v_and_b32 v194, 0xffff0000, v194\n"
      "v_and_b32 v208, 0xffff0000, v208\n"
      "v_mfma_f32_16x16x32_bf16 v[152:155], a[44:47], v[100:103], v[152:155]\n"
      "ds_read_b128 v[132:135], v23 offset:44032\n"
      "v_add_f32 v167, v167, v185\n"
      "v_mfma_f32_16x16x32_bf16 v[152:155], a[48:51], v[104:107], v[152:155]\n"
      "v_lshlrev_b32 v233, 16, v197\n"
      "v_add_u32 v14, 1, v14\n"
      "v_mfma_f32_16x16x32_bf16 v[152:155], a[52:55], v[108:111], v[152:155]\n"
      "s_add_u32 s36, s36, 165888\n"
      "v_lshlrev_b32 v235, 16, v195\n"
      "v_add_f32 v168, v168, v234\n"
      "v_mfma_f32_16x16x32_bf16 v[152:155], a[56:59], v[112:115], v[152:155]\n"
      "buffer_load_dwordx4 v[216:219], v6, s[8:11], s38 offen sc1\n"
      "v_mfma_f32_16x16x32_bf16 v[152:155], a[60:63], v[116:119], v[152:155]\n"
      "s_waitcnt lgkmcnt(2)\n"
      "ds_read_b128 v[148:151], v23 offset:48128\n"
      "v_add_f32 v164, v164, v232\n"
      "v_mfma_f32_16x16x32_bf16 v[152:155], a[64:67], v[120:123], v[152:155]\n"
      "s_waitcnt lgkmcnt(2)\n"
      "v_add_f32 v165, v165, v192\n"
      "v_lshlrev_b32 v234, 16, v202\n"
      "v_mfma_f32_16x16x32_bf16 v[152:155], a[68:71], v[124:127], v[152:155]\n"
      "v_add_f32 v174, v174, v233\n"
      "v_lshlrev_b32 v232, 16, v212\n"
      "v_mfma_f32_16x16x32_bf16 v[152:155], a[72:75], v[128:131], v[152:155]\n"
      "s_waitcnt lgkmcnt(1)\n"
      "v_and_b32 v179, 0xffff0000, v179\n"
      "v_add_f32 v169, v169, v186\n"
      "v_mfma_f32_16x16x32_bf16 v[152:155], a[76:79], v[132:135], v[152:155]\n"
      "s_waitcnt vmcnt(8)\n"
      "v_lshlrev_b32 v233, 16, v201\n"
      "v_cmp_ne_u32 vcc, s21, v20\n"
      "v_mfma_f32_16x16x32_bf16 v[152:155], a[80:83], v[136:139], v[152:155]\n"
      "buffer_load_dwordx2 v[220:221], v7, s[8:11], s38 offen sc1\n"
      "v_mfma_f32_16x16x32_bf16 v[152:155], a[84:87], v[140:143], v[152:155]\n"
      "v_add_f32 v166, v166, v233\n"
      "v_and_b32 v193, 0xffff0000, v193\n"
      "v_mfma_f32_16x16x32_bf16 v[152:155], a[88:91], v[144:147], v[152:155]\n"
      "s_waitcnt lgkmcnt(0)\n"
      "v_and_b32 v197, 0xffff0000, v197\n"
      "v_lshlrev_b32 v233, 16, v205\n"
      "v_mfma_f32_16x16x32_bf16 v[152:155], a[92:95], v[148:151], v[152:155]\n"
      "v_add_f32 v170, v170, v235\n"
      "v_add_f32 v174, v174, v233\n"
      "v_mfma_f32_16x16x32_bf16 v[156:159], a[96:99], v[56:59], 0\n"
      "s_cbranch_vccnz .Lsp_d1_15\n"
      ".Lbk_d1_15:\n"
      "v_lshlrev_b32 v235, 16, v203\n"
      "v_mfma_f32_16x16x32_bf16 v[156:159], a[100:103], v[60:63], v[156:159]\n"
      "buffer_load_dwordx4 v0, s[8:11], s54 offen offset:0 sc1 lds\n"
      "v_mfma_f32_16x16x32_bf16 v[156:159], a[104:107], v[64:67], v[156:159]\n"
      "v_and_b32 v187, 0xffff0000, v187\n"
      "v_lshlrev_b32 v233, 16, v209\n"
      "v_mfma_f32_16x16x32_bf16 v[156:159], a[108:111], v[68:71], v[156:159]\n"
      "v_add_f32 v170, v170, v235\n"
      "v_add_f32 v166, v166, v233\n"
      "v_mfma_f32_16x16x32_bf16 v[156:159], a[112:115], v[72:75], v[156:159]\n"
      "v_and_b32 v201, 0xffff0000, v201\n"
      "v_add_f32 v165, v165, v200\n"
      "v_mfma_f32_16x16x32_bf16 v[156:159], a[116:119], v[76:79], v[156:159]\n"
      "v_and_b32 v188, 0xffff0000, v188\n"
      "v_add_f32 v165, v165, v208\n"
      "v_mfma_f32_16x16x32_bf16 v[156:159], a[120:123], v[80:83], v[156:159]\n"
      "buffer_load_dwordx4 v0, s[8:11], s54 offen offset:1024 sc1 lds\n"
      "v_mfma_f32_16x16x32_bf16 v[156:159], a[124:127], v[84:87], v[156:159]\n"
      "v_add_f32 v168, v168, v234\n"
      "v_mul_f32 v237, v164, v164\n"
      "v_mfma_f32_16x16x32_bf16 v[156:159], a[128:131], v[88:91], v[156:159]\n"
      "v_add_f32 v167, v167, v193\n"
      "v_lshlrev_b32 v233, 16, v213\n"
      "v_mfma_f32_16x16x32_bf16 v[156:159], a[132:135], v[92:95], v[156:159]\n"
      "s_waitcnt vmcnt(7)\n"
      "ds_write_b32 v16, v14\n"
      "v_mfma_f32_16x16x32_bf16 v[156:159], a[136:139], v[96:99], v[156:159]\n"
      "buffer_load_dwordx4 v0, s[8:11], s54 offen offset:2048 sc1 lds\n"
      "v_mfma_f32_16x16x32_bf16 v[156:159], a[140:143], v[100:103], v[156:159]\n"
      "v_add_f32 v171, v171, v179\n"
      "v_add_f32 v167, v167, v201\n"
      "v_mfma_f32_16x16x32_bf16 v[156:159], a[144:147], v[104:107], v[156:159]\n"
      "v_lshlrev_b32 v234, 16, v210\n"
      "v_and_b32 v213, 0xffff0000, v213\n"
      "v_mfma_f32_16x16x32_bf16 v[156:159], a[148:151], v[108:111], v[156:159]\n"
      "s_min_u32 s36, s36, s6\n"
      "v_add_f32 v173, v173, v188\n"
      "v_and_b32 v209, 0xffff0000, v209\n"
      "v_mfma_f32_16x16x32_bf16 v[156:159], a[152:155], v[112:115], v[156:159]\n"
      "buffer_load_dword v20, v2, s[8:11], s36 offen sc1\n"
      "v_mfma_f32_16x16x32_bf16 v[156:159], a[156:159], v[116:119], v[156:159]\n"
      "v_add_f32 v236, v164, v165\n"
      "v_add_f32 v175, v175, v189\n"
      "v_mfma_f32_16x16x32_bf16 v[156:159], a[160:163], v[120:123], v[156:159]\n"
      "buffer_load_dwordx4 v0, s[8:11], s54 offen offset:3072 sc1 lds\n"
      "v_mfma_f32_16x16x32_bf16 v[156:159], a[164:167], v[124:127], v[156:159]\n"
      "v_add_f32 v167, v167, v209\n"
      "v_and_b32 v203, 0xffff0000, v203\n"
      "v_mfma_f32_16x16x32_bf16 v[156:159], a[168:171], v[128:131], v[156:159]\n"
      "ds_read_b32 v15, v17\n"
      "v_fmac_f32 v237, v165, v165\n"
      "v_mfma_f32_16x16x32_bf16 v[156:159], a[172:175], v[132:135], v[156:159]\n"
      "s_add_u32 m0, s43, 28672\n"
      "v_add_f32 v175, v175, v197\n"
      "v_and_b32 v204, 0xffff0000, v204\n"
      "v_mfma_f32_16x16x32_bf16 v[156:159], a[176:179], v[136:139], v[156:159]\n"
      "v_and_b32 v195, 0xffff0000, v195\n"
      "v_add_f32 v172, v172, v232\n"
      "v_mfma_f32_16x16x32_bf16 v[156:159], a[180:183], v[140:143], v[156:159]\n"
      "buffer_load_dwordx4 v1, s[8:11], s54 offen offset:0 sc1 lds\n"
      "v_mfma_f32_16x16x32_bf16 v[156:159], a[184:187], v[144:147], v[156:159]\n"
      "v_fmac_f32 v237, v166, v166\n"
      "v_cvt_pk_bf16_f32 v225, v154, v155\n"
      "v_mfma_f32_16x16x32_bf16 v[156:159], a[188:191], v[148:151], v[156:159]\n"
      "s_waitcnt lgkmcnt(0)\n"
      "v_add_f32 v169, v169, v194\n"
      "v_cmp_gt_i32 vcc, v14, v15\n"
      "v_mfma_f32_16x16x32_bf16 v[160:163], a[192:195], v[56:59], 0\n"
      "v_and_b32 v196, 0xffff0000, v196\n"
      "v_and_b32 v212, 0xffff0000, v212\n"
      "v_mfma_f32_16x16x32_bf16 v[160:163], a[196:199], v[60:63], v[160:163]\n"
      "buffer_load_dwordx4 v1, s[8:11], s54 offen offset:1024 sc1 lds\n"
      "v_mfma_f32_16x16x32_bf16 v[160:163], a[200:203], v[64:67], v[160:163]\n"
      "s_cbranch_vccnz .Lsp_dy0_16\n"
      ".Lbk_dy0_16:\n"
      "v_mfma_f32_16x16x32_bf16 v[160:163], a[204:207], v[68:71], v[160:163]\n"
      "ds_read_b128 v[56:59], v0 offset:0\n"
      "v_cvt_pk_bf16_f32 v177, v166, v167\n"
      "v_mfma_f32_16x16x32_bf16 v[160:163], a[208:211], v[72:75], v[160:163]\n"
      "v_add_f32 v171, v171, v187\n"
      "v_and_b32 v205, 0xffff0000, v205\n"
      "v_mfma_f32_16x16x32_bf16 v[160:163], a[212:215], v[76:79], v[160:163]\n"
      "ds_read_b128 v[60:63], v0 offset:1024\n"
      "v_cvt_pk_bf16_f32 v224, v152, v153\n"
      "v_mfma_f32_16x16x32_bf16 v[160:163], a[216:219], v[80:83], v[160:163]\n"
      "s_add_u32 s55, s55, 106496\n"
      "v_and_b32 v202, 0xffff0000, v202\n"
      "v_add_f32 v169, v169, v202\n"
      "v_mfma_f32_16x16x32_bf16 v[160:163], a[220:223], v[84:87], v[160:163]\n"
      "ds_read_b128 v[84:87], v0 offset:7168\n"
      "v_add_f32 v236, v236, v166\n"
      "v_mfma_f32_16x16x32_bf16 v[160:163], a[224:227], v[88:91], v[160:163]\n"
      "ds_read_b128 v[72:75], v0 offset:4096\n"
      "v_and_b32 v210, 0xffff0000, v210\n"
      "v_mfma_f32_16x16x32_bf16 v[160:163], a[228:231], v[92:95], v[160:163]\n"
      "v_add_f32 v173, v173, v196\n"
      "v_lshlrev_b32 v235, 16, v211\n"
      "v_mfma_f32_16x16x32_bf16 v[160:163], a[232:235], v[96:99], v[160:163]\n"
      "v_add_f32 v170, v170, v235\n"
      "v_add_f32 v171, v171, v195\n"
      "v_mfma_f32_16x16x32_bf16 v[160:163], a[236:239], v[100:103], v[160:163]\n"
      "s_add_u32 s37, s37, 106496\n"
      "ds_read_b128 v[88:91], v0 offset:8192\n"
      "v_cvt_pk_bf16_f32 v227, v158, v159\n"
      "v_mfma_f32_16x16x32_bf16 v[160:163], a[240:243], v[104:107], v[160:163]\n"
      "s_waitcnt vmcnt(9) lgkmcnt(3)\n"
      "buffer_store_dword v18, v4, s[8:11], s55 offen sc1\n"
      "v_mfma_f32_16x16x32_bf16 v[160:163], a[244:247], v[108:111], v[160:163]\n"
      "ds_read_b128 v[68:71], v0 offset:3072\n"
      "v_add_f32 v174, v174, v233\n"
      "v_mfma_f32_16x16x32_bf16 v[160:163], a[248:251], v[112:115], v[160:163]\n"
      "ds_read_b128 v[104:107], v0 offset:12288\n"
      "v_add_f32 v169, v169, v210\n"
      "v_mfma_f32_16x16x32_bf16 v[160:163], a[252:255], v[116:119], v[160:163]\n"
      "ds_read_b128 v[96:99], v0 offset:10240\n"
      "v_fmac_f32 v237, v167, v167\n"
      "v_mfma_f32_16x16x32_bf16 v[160:163], v[24:27], v[120:123], v[160:163]\n"
      "ds_read_b128 v[80:83], v0 offset:6144\n"
      "v_add_f32 v175, v175, v205\n"
      "v_mfma_f32_16x16x32_bf16 v[160:163], v[28:31], v[124:127], v[160:163]\n"
      "ds_read_b128 v[64:67], v0 offset:2048\n"
      "v_add_f32 v168, v168, v234\n"
      "v_mfma_f32_16x16x32_bf16 v[160:163], v[32:35], v[128:131], v[160:163]\n"
      "v_add_f32 v236, v236, v167\n"
      "v_cvt_pk_bf16_f32 v226, v156, v157\n"
      "v_mfma_f32_16x16x32_bf16 v[160:163], v[36:39], v[132:135], v[160:163]\n"
      "ds_read_b128 v[92:95], v0 offset:9216\n"
      "v_cvt_pk_bf16_f32 v176, v164, v165\n"
      "v_mfma_f32_16x16x32_bf16 v[160:163], v[40:43], v[136:139], v[160:163]\n"
      "buffer_store_dwordx4 v[224:227], v0, s[8:11], s37 offen sc1\n"
      "ds_read_b128 v[76:79], v0 offset:5120\n"
      "v_mfma_f32_16x16x32_bf16 v[160:163], v[44:47], v[140:143], v[160:163]\n"
      "ds_read_b128 v[132:135], v0 offset:19456\n"
      "v_and_b32 v211, 0xffff0000, v211\n"
      "v_mfma_f32_16x16x32_bf16 v[160:163], v[48:51], v[144:147], v[160:163]\n"
      "ds_read_b128 v[100:103], v0 offset:11264\n"
      "v_add_f32 v171, v171, v203\n"
      "v_mfma_f32_16x16x32_bf16 v[160:163], v[52:55], v[148:151], v[160:163]\n"
      "s_sub_u32 s62, s62, 1\n"
      "s_cbranch_scc0 .Ld_t0\n"
      ".Ld_out5:\n"
      "s_nop 8\n"
      "v_cvt_pk_bf16_f32 v228, v160, v161\n"
      "v_cvt_pk_bf16_f32 v229, v162, v163\n"
      "buffer_store_dwordx2 v[228:229], v13, s[8:11], s37 offen sc1\n"
      "v_cvt_pk_bf16_f32 v178, v168, v169\n"
      "v_add_f32 v175, v175, v213\n"
      "v_add_f32 v171, v171, v211\n"
      "v_add_f32 v173, v173, v204\n"
      "v_cvt_pk_bf16_f32 v179, v170, v171\n"
      "v_add_f32 v173, v173, v212\n"
      "v_cvt_pk_bf16_f32 v180, v172, v173\n"
      "v_cvt_pk_bf16_f32 v181, v174, v175\n"
      "buffer_store_dwordx2 v[180:181], v9, s[16:19], s60 offen\n"
      "buffer_store_dwordx4 v[176:179], v8, s[16:19], s60 offen\n"
      "s_add_u32 s55, s55, 106496\n"
      "s_add_u32 s37, s37, 173056\n"
      "s_waitcnt vmcnt(2)\n"
      "buffer_store_dword v18, v4, s[8:11], s55 offen sc1\n"
      "s_add_u32 s55, s55, 173056\n"
      "s_branch .Ld_end\n"
      ".Ld_out0:\n"
      "s_nop 8\n"
      "v_add_f32 v174, v174, v162\n"
      "v_add_f32 v170, v170, v158\n"
      "v_lshlrev_b32 v172, 16, v220\n"
      "s_add_u32 s38, s38, 147456\n"
      "v_add_f32 v171, v171, v159\n"
      "v_add_f32 v169, v169, v157\n"
      "v_add_f32 v172, v172, v160\n"
      "v_add_f32 v175, v175, v163\n"
      "v_and_b32 v173, 0xffff0000, v220\n"
      "v_add_f32 v173, v173, v161\n"
      "s_add_u32 s55, s55, 173056\n"
      "s_add_u32 s57, s56, 6656\n"
      "s_add_u32 s58, s56, 13312\n"
      "s_add_u32 s62, s56, 19968\n"
      "s_add_u32 s36, s56, 26624\n"
      "s_mov_b32 s54, 0\n"
      "s_mov_b32 s22, s23\n"
      ".Ld_a0:\n"
      "buffer_load_dwordx4 v[176:179], v0, s[8:11], s56 offen sc1\n"
      "buffer_load_dwordx4 v[184:187], v0, s[8:11], s57 offen sc1\n"
      "buffer_load_dwordx4 v[192:195], v0, s[8:11], s58 offen sc1\n"
      "buffer_load_dwordx4 v[200:203], v0, s[8:11], s62 offen sc1\n"
      "buffer_load_dwordx4 v[208:211], v0, s[8:11], s36 offen sc1\n"
      "buffer_load_dword v21, v3, s[8:11], s56 offen sc1\n"
      "buffer_load_dwordx2 v[180:181], v13, s[8:11], s56 offen sc1\n"
      "buffer_load_dwordx2 v[188:189], v13, s[8:11], s57 offen sc1\n"
      "buffer_load_dwordx2 v[196:197], v13, s[8:11], s58 offen sc1\n"
      "buffer_load_dwordx2 v[204:205], v13, s[8:11], s62 offen sc1\n"
      "buffer_load_dwordx2 v[212:213], v13, s[8:11], s36 offen sc1\n"
      "s_waitcnt vmcnt(6)\n"
      "s_cmp_lg_u32 s54, 0\n"
      "s_cbranch_scc1 .Ld_b0\n"
      "v_max_u32 v232, v176, v177\n"
      "v_max3_u32 v232, v232, v178, v179\n"
      "v_max3_u32 v232, v232, v184, v185\n"
      "v_max3_u32 v232, v232, v186, v187\n"
      "v_max3_u32 v232, v232, v192, v193\n"
      "v_max3_u32 v232, v232, v194, v195\n"
      "v_max3_u32 v232, v232, v200, v201\n"
      "v_max3_u32 v232, v232, v202, v203\n"
      "v_max3_u32 v232, v232, v208, v209\n"
      "v_max3_u32 v232, v232, v210, v211\n"
      "v_cmp_eq_u32 vcc, -1, v232\n"
      "s_cbranch_vccz .Ld_b0\n"
      "s_waitcnt vmcnt(0)\n"
      "v_cmp_ne_u32 vcc, s21, v21\n"
      "s_nop 1\n"
      "s_cmp_eq_u64 vcc, 0\n"
      "s_cselect_b32 s54, 1, 0\n"
      "s_sub_u32 s22, s22, 1\n"
      "s_cbranch_scc0 .Ld_a0\n"
      ".Ld_b0:\n"
      "s_add_u32 s61, s41, 4294967264\n"
      "s_cmp_lt_u32 s41, 32\n"
      "s_cselect_b32 s59, s39, 0x40000000\n"
      "s_add_u32 s39, s39, 147456\n"
      "s_cmp_lt_u32 s61, 32\n"
      "s_cselect_b32 s60, s40, 0x40000000\n"
      "s_add_u32 s40, s40, 4294856704\n"
      "s_add_u32 s41, s41, 6\n"
      "v_lshlrev_b32 v232, 16, v176\n"
      "v_lshlrev_b32 v233, 16, v177\n"
      "v_lshlrev_b32 v234, 16, v178\n"
      "v_lshlrev_b32 v235, 16, v179\n"
      "v_and_b32 v176, 0xffff0000, v176\n"
      "v_and_b32 v177, 0xffff0000, v177\n"
      "v_and_b32 v178, 0xffff0000, v178\n"
      "v_and_b32 v179, 0xffff0000, v179\n"
      "v_add_f32 v164, v164, v232\n"
      "v_add_f32 v166, v166, v233\n"
      "v_add_f32 v168, v168, v234\n"
      "v_add_f32 v170, v170, v235\n"
      "v_add_f32 v165, v165, v176\n"
      "v_add_f32 v167, v167, v177\n"
      "v_add_f32 v169, v169, v178\n"
      "v_add_f32 v171, v171, v179\n"
      "v_lshlrev_b32 v232, 16, v184\n"
      "v_lshlrev_b32 v233, 16, v185\n"
      "v_lshlrev_b32 v234, 16, v186\n"
      "v_lshlrev_b32 v235, 16, v187\n"
      "v_and_b32 v184, 0xffff0000, v184\n"
      "v_and_b32 v185, 0xffff0000, v185\n"
      "v_and_b32 v186, 0xffff0000, v186\n"
      "v_and_b32 v187, 0xffff0000, v187\n"
      "v_add_f32 v164, v164, v232\n"
      "v_add_f32 v166, v166, v233\n"
      "v_add_f32 v168, v168, v234\n"
      "v_add_f32 v170, v170, v235\n"
      "v_add_f32 v165, v165, v184\n"
      "v_add_f32 v167, v167, v185\n"
      "v_add_f32 v169, v169, v186\n"
      "v_add_f32 v171, v171, v187\n"
      "v_lshlrev_b32 v232, 16, v192\n"
      "v_lshlrev_b32 v233, 16, v193\n"
      "v_lshlrev_b32 v234, 16, v194\n"
      "v_lshlrev_b32 v235, 16, v195\n"
      "v_and_b32 v192, 0xffff0000, v192\n"
      "v_and_b32 v193, 0xffff0000, v193\n"
      "v_and_b32 v194, 0xffff0000, v194\n"
      "v_and_b32 v195, 0xffff0000, v195\n"
      "v_add_f32 v164, v164, v232\n"
      "v_add_f32 v166, v166, v233\n"
      "v_add_f32 v168, v168, v234\n"
      "v_add_f32 v170, v170, v235\n"
      "v_add_f32 v165, v165, v192\n"
      "v_add_f32 v167, v167, v193\n"
      "v_add_f32 v169, v169, v194\n"
      "v_add_f32 v171, v171, v195\n"
      "v_lshlrev_b32 v232, 16, v200\n"
      "v_lshlrev_b32 v233, 16, v201\n"
      "v_lshlrev_b32 v234, 16, v202\n"
      "v_lshlrev_b32 v235, 16, v203\n"
      "v_and_b32 v200, 0xffff0000, v200\n"
      "v_and_b32 v201, 0xffff0000, v201\n"
      "v_and_b32 v202, 0xffff0000, v202\n"
      "v_and_b32 v203, 0xffff0000, v203\n"
      "v_add_f32 v164, v164, v232\n"
      "v_add_f32 v166, v166, v233\n"
      "v_add_f32 v168, v168, v234\n"
      "v_add_f32 v170, v170, v235\n"
      "v_add_f32 v165, v165, v200\n"
      "v_add_f32 v167, v167, v201\n"
      "v_add_f32 v169, v169, v202\n"
      "v_add_f32 v171, v171, v203\n"
      "v_lshlrev_b32 v232, 16, v208\n"
      "v_lshlrev_b32 v233, 16, v209\n"
      "v_lshlrev_b32 v234, 16, v210\n"
      "v_lshlrev_b32 v235, 16, v211\n"
      "v_and_b32 v208, 0xffff0000, v208\n"
      "v_and_b32 v209, 0xffff0000, v209\n"
      "v_and_b32 v210, 0xffff0000, v210\n"
      "v_and_b32 v211, 0xffff0000, v211\n"
      "v_add_f32 v164, v164, v232\n"
      "v_add_f32 v166, v166, v233\n"
      "v_add_f32 v168, v168, v234\n"
      "v_add_f32 v170, v170, v235\n"
      "v_add_f32 v165, v165, v208\n"
      "v_add_f32 v167, v167, v209\n"
      "v_add_f32 v169, v169, v210\n"
      "v_add_f32 v171, v171, v211\n"
      "v_cvt_pk_bf16_f32 v176, v164, v165\n"
      "v_cvt_pk_bf16_f32 v177, v166, v167\n"
      "v_cvt_pk_bf16_f32 v178, v168, v169\n"
      "v_cvt_pk_bf16_f32 v179, v170, v171\n"
      "buffer_store_dwordx4 v[176:179], v8, s[16:19], s60 offen\n"
      "s_mov_b32 s22, s23\n"
      "s_waitcnt vmcnt(1)\n"
      ".Ld_c0:\n"
      "s_cmp_lg_u32 s54, 0\n"
      "s_cbranch_scc1 .Ld_d0\n"
      "v_max_u32 v232, v180, v181\n"
      "v_max3_u32 v232, v232, v188, v189\n"
      "v_max3_u32 v232, v232, v196, v197\n"
      "v_max3_u32 v232, v232, v204, v205\n"
      "v_max3_u32 v232, v232, v212, v213\n"
      "v_cmp_eq_u32 vcc, -1, v232\n"
      "s_cbranch_vccz .Ld_d0\n"
      "v_cmp_ne_u32 vcc, s21, v21\n"
      "s_nop 1\n"
      "s_cmp_eq_u64 vcc, 0\n"
      "s_cselect_b32 s54, 1, 0\n"
      "buffer_load_dword v21, v3, s[8:11], s56 offen sc1\n"
      "buffer_load_dwordx2 v[180:181], v13, s[8:11], s56 offen sc1\n"
      "buffer_load_dwordx2 v[188:189], v13, s[8:11], s57 offen sc1\n"
      "buffer_load_dwordx2 v[196:197], v13, s[8:11], s58 offen sc1\n"
      "buffer_load_dwordx2 v[204:205], v13, s[8:11], s62 offen sc1\n"
      "buffer_load_dwordx2 v[212:213], v13, s[8:11], s36 offen sc1\n"
      "s_waitcnt vmcnt(0)\n"
      "s_sub_u32 s22, s22, 1\n"
      "s_cbranch_scc0 .Ld_c0\n"
      ".Ld_d0:\n"
      "v_lshlrev_b32 v232, 16, v180\n"
      "v_lshlrev_b32 v233, 16, v181\n"
      "v_and_b32 v180, 0xffff0000, v180\n"
      "v_and_b32 v181, 0xffff0000, v181\n"
      "v_add_f32 v172, v172, v232\n"
      "v_add_f32 v174, v174, v233\n"
      "v_add_f32 v173, v173, v180\n"
      "v_add_f32 v175, v175, v181\n"
      "v_lshlrev_b32 v232, 16, v188\n"
      "v_lshlrev_b32 v233, 16, v189\n"
      "v_and_b32 v188, 0xffff0000, v188\n"
      "v_and_b32 v189, 0xffff0000, v189\n"
      "v_add_f32 v172, v172, v232\n"
      "v_add_f32 v174, v174, v233\n"
      "v_add_f32 v173, v173, v188\n"
      "v_add_f32 v175, v175, v189\n"
      "v_lshlrev_b32 v232, 16, v196\n"
      "v_lshlrev_b32 v233, 16, v197\n"
      "v_and_b32 v196, 0xffff0000, v196\n"
      "v_and_b32 v197, 0xffff0000, v197\n"
      "v_add_f32 v172, v172, v232\n"
      "v_add_f32 v174, v174, v233\n"
      "v_add_f32 v173, v173, v196\n"
      "v_add_f32 v175, v175, v197\n"
      "v_lshlrev_b32 v232, 16, v204\n"
      "v_lshlrev_b32 v233, 16, v205\n"
      "v_and_b32 v204, 0xffff0000, v204\n"
      "v_and_b32 v205, 0xffff0000, v205\n"
      "v_add_f32 v172, v172, v232\n"
      "v_add_f32 v174, v174, v233\n"
      "v_add_f32 v173, v173, v204\n"
      "v_add_f32 v175, v175, v205\n"
      "v_lshlrev_b32 v232, 16, v212\n"
      "v_lshlrev_b32 v233, 16, v213\n"
      "v_and_b32 v212, 0xffff0000, v212\n"
      "v_and_b32 v213, 0xffff0000, v213\n"
      "v_add_f32 v172, v172, v232\n"
      "v_add_f32 v174, v174, v233\n"
      "v_add_f32 v173, v173, v212\n"
      "v_add_f32 v175, v175, v213\n"
      "v_cvt_pk_bf16_f32 v180, v172, v173\n"
      "v_cvt_pk_bf16_f32 v181, v174, v175\n"
      "buffer_store_dwordx2 v[180:181], v9, s[16:19], s60 offen\n"
      "s_branch .Ld_end\n"
      ".Ld_out1:\n"
      "s_nop 8\n"
      "v_cvt_pk_bf16_f32 v229, v162, v163\n"
      "v_cvt_pk_bf16_f32 v228, v160, v161\n"
      "buffer_store_dwordx2 v[228:229], v13, s[8:11], s37 offen sc1\n"
      "s_add_u32 s57, s56, 6656\n"
      "s_add_u32 s58, s56, 13312\n"
      "s_add_u32 s62, s56, 19968\n"
      "s_add_u32 s36, s56, 26624\n"
      "s_mov_b32 s54, 0\n"
      "s_mov_b32 s22, s23\n"
      ".Ld_a1:\n"
      "buffer_load_dwordx4 v[176:179], v0, s[8:11], s56 offen sc1\n"
      "buffer_load_dwordx4 v[184:187], v0, s[8:11], s57 offen sc1\n"
      "buffer_load_dwordx4 v[192:195], v0, s[8:11], s58 offen sc1\n"
      "buffer_load_dwordx4 v[200:203], v0, s[8:11], s62 offen sc1\n"
      "buffer_load_dwordx4 v[208:211], v0, s[8:11], s36 offen sc1\n"
      "buffer_load_dword v21, v3, s[8:11], s56 offen sc1\n"
      "buffer_load_dwordx2 v[180:181], v13, s[8:11], s56 offen sc1\n"
      "buffer_load_dwordx2 v[188:189], v13, s[8:11], s57 offen sc1\n"
      "buffer_load_dwordx2 v[196:197], v13, s[8:11], s58 offen sc1\n"
      "buffer_load_dwordx2 v[204:205], v13, s[8:11], s62 offen sc1\n"
      "buffer_load_dwordx2 v[212:213], v13, s[8:11], s36 offen sc1\n"
      "s_waitcnt vmcnt(6)\n"
      "s_cmp_lg_u32 s54, 0\n"
      "s_cbranch_scc1 .Ld_b1\n"
      "v_max_u32 v232, v176, v177\n"
      "v_max3_u32 v232, v232, v178, v179\n"
      "v_max3_u32 v232, v232, v184, v185\n"
      "v_max3_u32 v232, v232, v186, v187\n"
      "v_max3_u32 v232, v232, v192, v193\n"
      "v_max3_u32 v232, v232, v194, v195\n"
      "v_max3_u32 v232, v232, v200, v201\n"
      "v_max3_u32 v232, v232, v202, v203\n"
      "v_max3_u32 v232, v232, v208, v209\n"
      "v_max3_u32 v232, v232, v210, v211\n"
      "v_cmp_eq_u32 vcc, -1, v232\n"
      "s_cbranch_vccz .Ld_b1\n"
      "s_waitcnt vmcnt(0)\n"
      "v_cmp_ne_u32 vcc, s21, v21\n"
      "s_nop 1\n"
      "s_cmp_eq_u64 vcc, 0\n"
      "s_cselect_b32 s54, 1, 0\n"
      "s_sub_u32 s22, s22, 1\n"
      "s_cbranch_scc0 .Ld_a1\n"
      ".Ld_b1:\n"
      "buffer_store_dword v18, v4, s[8:11], s55 offen sc1\n"
      "s_add_u32 s37, s37, 106496\n"
      "s_add_u32 s55, s55, 106496\n"
      "s_add_u32 s61, s41, 4294967264\n"
      "s_cmp_lt_u32 s41, 32\n"
      "s_cselect_b32 s59, s39, 0x40000000\n"
      "s_add_u32 s39, s39, 147456\n"
      "s_cmp_lt_u32 s61, 32\n"
      "s_cselect_b32 s60, s40, 0x40000000\n"
      "s_add_u32 s40, s40, 4294856704\n"
      "s_add_u32 s41, s41, 6\n"
      "v_lshlrev_b32 v232, 16, v176\n"
      "v_lshlrev_b32 v233, 16, v177\n"
      "v_lshlrev_b32 v234, 16, v178\n"
      "v_lshlrev_b32 v235, 16, v179\n"
      "v_and_b32 v176, 0xffff0000, v176\n"
      "v_and_b32 v177, 0xffff0000, v177\n"
      "v_and_b32 v178, 0xffff0000, v178\n"
      "v_and_b32 v179, 0xffff0000, v179\n"
      "v_add_f32 v164, v164, v232\n"
      "v_add_f32 v166, v166, v233\n"
      "v_add_f32 v168, v168, v234\n"
      "v_add_f32 v170, v170, v235\n"
      "v_add_f32 v165, v165, v176\n"
      "v_add_f32 v167, v167, v177\n"
      "v_add_f32 v169, v169, v178\n"
      "v_add_f32 v171, v171, v179\n"
      "v_lshlrev_b32 v232, 16, v184\n"
      "v_lshlrev_b32 v233, 16, v185\n"
      "v_lshlrev_b32 v234, 16, v186\n"
      "v_lshlrev_b32 v235, 16, v187\n"
      "v_and_b32 v184, 0xffff0000, v184\n"
      "v_and_b32 v185, 0xffff0000, v185\n"
      "v_and_b32 v186, 0xffff0000, v186\n"
      "v_and_b32 v187, 0xffff0000, v187\n"
      "v_add_f32 v164, v164, v232\n"
      "v_add_f32 v166, v166, v233\n"
      "v_add_f32 v168, v168, v234\n"
      "v_add_f32 v170, v170, v235\n"
      "v_add_f32 v165, v165, v184\n"
      "v_add_f32 v167, v167, v185\n"
      "v_add_f32 v169, v169, v186\n"
      "v_add_f32 v171, v171, v187\n"
      "v_lshlrev_b32 v232, 16, v192\n"
      "v_lshlrev_b32 v233, 16, v193\n"
      "v_lshlrev_b32 v234, 16, v194\n"
      "v_lshlrev_b32 v235, 16, v195\n"
      "v_and_b32 v192, 0xffff0000, v192\n"
      "v_and_b32 v193, 0xffff0000, v193\n"
      "v_and_b32 v194, 0xffff0000, v194\n"
      "v_and_b32 v195, 0xffff0000, v195\n"
      "v_add_f32 v164, v164, v232\n"
      "v_add_f32 v166, v166, v233\n"
      "v_add_f32 v168, v168, v234\n"
      "v_add_f32 v170, v170, v235\n"
      "v_add_f32 v165, v165, v192\n"
      "v_add_f32 v167, v167, v193\n"
      "v_add_f32 v169, v169, v194\n"
      "v_add_f32 v171, v171, v195\n"
      "v_lshlrev_b32 v232, 16, v200\n"
      "v_lshlrev_b32 v233, 16, v201\n"
      "v_lshlrev_b32 v234, 16, v202\n"
      "v_lshlrev_b32 v235, 16, v203\n"
      "v_and_b32 v200, 0xffff0000, v200\n"
      "v_and_b32 v201, 0xffff0000, v201\n"
      "v_and_b32 v202, 0xffff0000, v202\n"
      "v_and_b32 v203, 0xffff0000, v203\n"
      "v_add_f32 v164, v164, v232\n"
      "v_add_f32 v166, v166, v233\n"
      "v_add_f32 v168, v168, v234\n"
      "v_add_f32 v170, v170, v235\n"
      "v_add_f32 v165, v165, v200\n"
      "v_add_f32 v167, v167, v201\n"
      "v_add_f32 v169, v169, v202\n"
      "v_add_f32 v171, v171, v203\n"
      "v_lshlrev_b32 v232, 16, v208\n"
      "v_lshlrev_b32 v233, 16, v209\n"
      "v_lshlrev_b32 v234, 16, v210\n"
      "v_lshlrev_b32 v235, 16, v211\n"
      "v_and_b32 v208, 0xffff0000, v208\n"
      "v_and_b32 v209, 0xffff0000, v209\n"
      "v_and_b32 v210, 0xffff0000, v210\n"
      "v_and_b32 v211, 0xffff0000, v211\n"
      "v_add_f32 v164, v164, v232\n"
      "v_add_f32 v166, v166, v233\n"
      "v_add_f32 v168, v168, v234\n"
      "v_add_f32 v170, v170, v235\n"
      "v_add_f32 v165, v165, v208\n"
      "v_add_f32 v167, v167, v209\n"
      "v_add_f32 v169, v169, v210\n"
      "v_add_f32 v171, v171, v211\n"
      "v_cvt_pk_bf16_f32 v176, v164, v165\n"
      "v_cvt_pk_bf16_f32 v177, v166, v167\n"
      "v_cvt_pk_bf16_f32 v178, v168, v169\n"
      "v_cvt_pk_bf16_f32 v179, v170, v171\n"
      "buffer_store_dwordx4 v[176:179], v8, s[16:19], s60 offen\n"
      "s_mov_b32 s22, s23\n"
      "s_waitcnt vmcnt(2)\n"
      ".Ld_c1:\n"
      "s_cmp_lg_u32 s54, 0\n"
      "s_cbranch_scc1 .Ld_d1\n"
      "v_max_u32 v232, v180, v181\n"
      "v_max3_u32 v232, v232, v188, v189\n"
      "v_max3_u32 v232, v232, v196, v197\n"
      "v_max3_u32 v232, v232, v204, v205\n"
      "v_max3_u32 v232, v232, v212, v213\n"
      "v_cmp_eq_u32 vcc, -1, v232\n"
      "s_cbranch_vccz .Ld_d1\n"
      "v_cmp_ne_u32 vcc, s21, v21\n"
      "s_nop 1\n"
      "s_cmp_eq_u64 vcc, 0\n"
      "s_cselect_b32 s54, 1, 0\n"
      "buffer_load_dword v21, v3, s[8:11], s56 offen sc1\n"
      "buffer_load_dwordx2 v[180:181], v13, s[8:11], s56 offen sc1\n"
      "buffer_load_dwordx2 v[188:189], v13, s[8:11], s57 offen sc1\n"
      "buffer_load_dwordx2 v[196:197], v13, s[8:11], s58 offen sc1\n"
      "buffer_load_dwordx2 v[204:205], v13, s[8:11], s62 offen sc1\n"
      "buffer_load_dwordx2 v[212:213], v13, s[8:11], s36 offen sc1\n"
      "s_waitcnt vmcnt(0)\n"
      "s_sub_u32 s22, s22, 1\n"
      "s_cbranch_scc0 .Ld_c1\n"
      ".Ld_d1:\n"
      "v_lshlrev_b32 v232, 16, v180\n"
      "v_lshlrev_b32 v233, 16, v181\n"
      "v_and_b32 v180, 0xffff0000, v180\n"
      "v_and_b32 v181, 0xffff0000, v181\n"
      "v_add_f32 v172, v172, v232\n"
      "v_add_f32 v174, v174, v233\n"
      "v_add_f32 v173, v173, v180\n"
      "v_add_f32 v175, v175, v181\n"
      "v_lshlrev_b32 v232, 16, v188\n"
      "v_lshlrev_b32 v233, 16, v189\n"
      "v_and_b32 v188, 0xffff0000, v188\n"
      "v_and_b32 v189, 0xffff0000, v189\n"
      "v_add_f32 v172, v172, v232\n"
      "v_add_f32 v174, v174, v233\n"
      "v_add_f32 v173, v173, v188\n"
      "v_add_f32 v175, v175, v189\n"
      "v_lshlrev_b32 v232, 16, v196\n"
      "v_lshlrev_b32 v233, 16, v197\n"
      "v_and_b32 v196, 0xffff0000, v196\n"
      "v_and_b32 v197, 0xffff0000, v197\n"
      "v_add_f32 v172, v172, v232\n"
      "v_add_f32 v174, v174, v233\n"
      "v_add_f32 v173, v173, v196\n"
      "v_add_f32 v175, v175, v197\n"
      "v_lshlrev_b32 v232, 16, v204\n"
      "v_lshlrev_b32 v233, 16, v205\n"
      "v_and_b32 v204, 0xffff0000, v204\n"
      "v_and_b32 v205, 0xffff0000, v205\n"
      "v_add_f32 v172, v172, v232\n"
      "v_add_f32 v174, v174, v233\n"
      "v_add_f32 v173, v173, v204\n"
      "v_add_f32 v175, v175, v205\n"
      "v_lshlrev_b32 v232, 16, v212\n"
      "v_lshlrev_b32 v233, 16, v213\n"
      "v_and_b32 v212, 0xffff0000, v212\n"
      "v_and_b32 v213, 0xffff0000, v213\n"
      "v_add_f32 v172, v172, v232\n"
      "v_add_f32 v174, v174, v233\n"
      "v_add_f32 v173, v173, v212\n"
      "v_add_f32 v175, v175, v213\n"
      "v_cvt_pk_bf16_f32 v180, v172, v173\n"
      "v_cvt_pk_bf16_f32 v181, v174, v175\n"
      "buffer_store_dwordx2 v[180:181], v9, s[16:19], s60 offen\n"
      "s_branch .Ld_end\n"
      ".Ld_out2:\n"
      "s_nop 8\n"
      "v_cvt_pk_bf16_f32 v229, v162, v163\n"
      "v_cvt_pk_bf16_f32 v228, v160, v161\n"
      "buffer_store_dwordx2 v[228:229], v13, s[8:11], s37 offen sc1\n"
      "s_add_u32 s57, s56, 6656\n"
      "s_add_u32 s58, s56, 13312\n"
      "s_add_u32 s62, s56, 19968\n"
      "s_add_u32 s36, s56, 26624\n"
      "s_mov_b32 s54, 0\n"
      "s_mov_b32 s22, s23\n"
      ".Ld_a2:\n"
      "buffer_load_dwordx4 v[176:179], v0, s[8:11], s56 offen sc1\n"
      "buffer_load_dwordx4 v[184:187], v0, s[8:11], s57 offen sc1\n"
      "buffer_load_dwordx4 v[192:195], v0, s[8:11], s58 offen sc1\n"
      "buffer_load_dwordx4 v[200:203], v0, s[8:11], s62 offen sc1\n"
      "buffer_load_dwordx4 v[208:211], v0, s[8:11], s36 offen sc1\n"
      "buffer_load_dword v21, v3, s[8:11], s56 offen sc1\n"
      "buffer_load_dwordx2 v[180:181], v13, s[8:11], s56 offen sc1\n"
      "buffer_load_dwordx2 v[188:189], v13, s[8:11], s57 offen sc1\n"
      "buffer_load_dwordx2 v[196:197], v13, s[8:11], s58 offen sc1\n"
      "buffer_load_dwordx2 v[204:205], v13, s[8:11], s62 offen sc1\n"
      "buffer_load_dwordx2 v[212:213], v13, s[8:11], s36 offen sc1\n"
      "s_waitcnt vmcnt(6)\n"
      "s_cmp_lg_u32 s54, 0\n"
      "s_cbranch_scc1 .Ld_b2\n"
      "v_max_u32 v232, v176, v177\n"
      "v_max3_u32 v232, v232, v178, v179\n"
      "v_max3_u32 v232, v232, v184, v185\n"
      "v_max3_u32 v232, v232, v186, v187\n"
      "v_max3_u32 v232, v232, v192, v193\n"
      "v_max3_u32 v232, v232, v194, v195\n"
      "v_max3_u32 v232, v232, v200, v201\n"
      "v_max3_u32 v232, v232, v202, v203\n"
      "v_max3_u32 v232, v232, v208, v209\n"
      "v_max3_u32 v232, v232, v210, v211\n"
      "v_cmp_eq_u32 vcc, -1, v232\n"
      "s_cbranch_vccz .Ld_b2\n"
      "s_waitcnt vmcnt(0)\n"
      "v_cmp_ne_u32 vcc, s21, v21\n"
      "s_nop 1\n"
      "s_cmp_eq_u64 vcc, 0\n"
      "s_cselect_b32 s54, 1, 0\n"
      "s_sub_u32 s22, s22, 1\n"
      "s_cbranch_scc0 .Ld_a2\n"
      ".Ld_b2:\n"
      "s_add_u32 s55, s55, 106496\n"
      "s_add_u32 s61, s41, 4294967264\n"
      "s_add_u32 s37, s37, 106496\n"
      "buffer_store_dword v18, v4, s[8:11], s55 offen sc1\n"
      "s_cmp_lt_u32 s41, 32\n"
      "s_cselect_b32 s59, s39, 0x40000000\n"
      "s_add_u32 s39, s39, 147456\n"
      "s_cmp_lt_u32 s61, 32\n"
      "s_cselect_b32 s60, s40, 0x40000000\n"
      "s_add_u32 s55, s55, 106496\n"
      "s_add_u32 s40, s40, 4294856704\n"
      "s_add_u32 s41, s41, 6\n"
      "v_lshlrev_b32 v232, 16, v176\n"
      "v_lshlrev_b32 v233, 16, v177\n"
      "v_lshlrev_b32 v234, 16, v178\n"
      "v_lshlrev_b32 v235, 16, v179\n"
      "v_and_b32 v176, 0xffff0000, v176\n"
      "v_and_b32 v177, 0xffff0000, v177\n"
      "v_and_b32 v178, 0xffff0000, v178\n"
      "v_and_b32 v179, 0xffff0000, v179\n"
      "v_add_f32 v164, v164, v232\n"
      "v_add_f32 v166, v166, v233\n"
      "v_add_f32 v168, v168, v234\n"
      "v_add_f32 v170, v170, v235\n"
      "v_add_f32 v165, v165, v176\n"
      "v_add_f32 v167, v167, v177\n"
      "v_add_f32 v169, v169, v178\n"
      "v_add_f32 v171, v171, v179\n"
      "v_lshlrev_b32 v232, 16, v184\n"
      "v_lshlrev_b32 v233, 16, v185\n"
      "v_lshlrev_b32 v234, 16, v186\n"
      "v_lshlrev_b32 v235, 16, v187\n"
      "v_and_b32 v184, 0xffff0000, v184\n"
      "v_and_b32 v185, 0xffff0000, v185\n"
      "v_and_b32 v186, 0xffff0000, v186\n"
      "v_and_b32 v187, 0xffff0000, v187\n"
      "v_add_f32 v164, v164, v232\n"
      "v_add_f32 v166, v166, v233\n"
      "v_add_f32 v168, v168, v234\n"
      "v_add_f32 v170, v170, v235\n"
      "v_add_f32 v165, v165, v184\n"
      "v_add_f32 v167, v167, v185\n"
      "v_add_f32 v169, v169, v186\n"
      "v_add_f32 v171, v171, v187\n"
      "v_lshlrev_b32 v232, 16, v192\n"
      "v_lshlrev_b32 v233, 16, v193\n"
      "v_lshlrev_b32 v234, 16, v194\n"
      "v_lshlrev_b32 v235, 16, v195\n"
      "v_and_b32 v192, 0xffff0000, v192\n"
      "v_and_b32 v193, 0xffff0000, v193\n"
      "v_and_b32 v194, 0xffff0000, v194\n"
      "v_and_b32 v195, 0xffff0000, v195\n"
      "v_add_f32 v164, v164, v232\n"
      "v_add_f32 v166, v166, v233\n"
      "v_add_f32 v168, v168, v234\n"
      "v_add_f32 v170, v170, v235\n"
      "v_add_f32 v165, v165, v192\n"
      "v_add_f32 v167, v167, v193\n"
      "v_add_f32 v169, v169, v194\n"
      "v_add_f32 v171, v171, v195\n"
      "v_lshlrev_b32 v232, 16, v200\n"
      "v_lshlrev_b32 v233, 16, v201\n"
      "v_lshlrev_b32 v234, 16, v202\n"
      "v_lshlrev_b32 v235, 16, v203\n"
      "v_and_b32 v200, 0xffff0000, v200\n"
      "v_and_b32 v201, 0xffff0000, v201\n"
      "v_and_b32 v202, 0xffff0000, v202\n"
      "v_and_b32 v203, 0xffff0000, v203\n"
      "v_add_f32 v164, v164, v232\n"
      "v_add_f32 v166, v166, v233\n"
      "v_add_f32 v168, v168, v234\n"
      "v_add_f32 v170, v170, v235\n"
      "v_add_f32 v165, v165, v200\n"
      "v_add_f32 v167, v167, v201\n"
      "v_add_f32 v169, v169, v202\n"
      "v_add_f32 v171, v171, v203\n"
      "v_lshlrev_b32 v232, 16, v208\n"
      "v_lshlrev_b32 v233, 16, v209\n"
      "v_lshlrev_b32 v234, 16, v210\n"
      "v_lshlrev_b32 v235, 16, v211\n"
      "v_and_b32 v208, 0xffff0000, v208\n"
      "v_and_b32 v209, 0xffff0000, v209\n"
      "v_and_b32 v210, 0xffff0000, v210\n"
      "v_and_b32 v211, 0xffff0000, v211\n"
      "v_add_f32 v164, v164, v232\n"
      "v_add_f32 v166, v166, v233\n"
      "v_add_f32 v168, v168, v234\n"
      "v_add_f32 v170, v170, v235\n"
      "v_add_f32 v165, v165, v208\n"
      "v_add_f32 v167, v167, v209\n"
      "v_add_f32 v169, v169, v210\n"
      "v_add_f32 v171, v171, v211\n"
      "v_cvt_pk_bf16_f32 v176, v164, v165\n"
      "v_cvt_pk_bf16_f32 v177, v166, v167\n"
      "v_cvt_pk_bf16_f32 v178, v168, v169\n"
      "v_cvt_pk_bf16_f32 v179, v170, v171\n"
      "buffer_store_dwordx4 v[176:179], v8, s[16:19], s60 offen\n"
      "s_mov_b32 s22, s23\n"
      "s_waitcnt vmcnt(2)\n"
      ".Ld_c2:\n"
      "s_cmp_lg_u32 s54, 0\n"
      "s_cbranch_scc1 .Ld_d2\n"
      "v_max_u32 v232, v180, v181\n"
      "v_max3_u32 v232, v232, v188, v189\n"
      "v_max3_u32 v232, v232, v196, v197\n"
      "v_max3_u32 v232, v232, v204, v205\n"
      "v_max3_u32 v232, v232, v212, v213\n"
      "v_cmp_eq_u32 vcc, -1, v232\n"
      "s_cbranch_vccz .Ld_d2\n"
      "v_cmp_ne_u32 vcc, s21, v21\n"
      "s_nop 1\n"
      "s_cmp_eq_u64 vcc, 0\n"
      "s_cselect_b32 s54, 1, 0\n"
      "buffer_load_dword v21, v3, s[8:11], s56 offen sc1\n"
      "buffer_load_dwordx2 v[180:181], v13, s[8:11], s56 offen sc1\n"
      "buffer_load_dwordx2 v[188:189], v13, s[8:11], s57 offen sc1\n"
      "buffer_load_dwordx2 v[196:197], v13, s[8:11], s58 offen sc1\n"
      "buffer_load_dwordx2 v[204:205], v13, s[8:11], s62 offen sc1\n"
      "buffer_load_dwordx2 v[212:213], v13, s[8:11], s36 offen sc1\n"
      "s_waitcnt vmcnt(0)\n"
      "s_sub_u32 s22, s22, 1\n"
      "s_cbranch_scc0 .Ld_c2\n"
      ".Ld_d2:\n"
      "v_lshlrev_b32 v232, 16, v180\n"
      "v_lshlrev_b32 v233, 16, v181\n"
      "v_and_b32 v180, 0xffff0000, v180\n"
      "v_and_b32 v181, 0xffff0000, v181\n"
      "v_add_f32 v172, v172, v232\n"
      "v_add_f32 v174, v174, v233\n"
      "v_add_f32 v173, v173, v180\n"
      "v_add_f32 v175, v175, v181\n"
      "v_lshlrev_b32 v232, 16, v188\n"
      "v_lshlrev_b32 v233, 16, v189\n"
      "v_and_b32 v188, 0xffff0000, v188\n"
      "v_and_b32 v189, 0xffff0000, v189\n"
      "v_add_f32 v172, v172, v232\n"
      "v_add_f32 v174, v174, v233\n"
      "v_add_f32 v173, v173, v188\n"
      "v_add_f32 v175, v175, v189\n"
      "v_lshlrev_b32 v232, 16, v196\n"
      "v_lshlrev_b32 v233, 16, v197\n"
      "v_and_b32 v196, 0xffff0000, v196\n"
      "v_and_b32 v197, 0xffff0000, v197\n"
      "v_add_f32 v172, v172, v232\n"
      "v_add_f32 v174, v174, v233\n"
      "v_add_f32 v173, v173, v196\n"
      "v_add_f32 v175, v175, v197\n"
      "v_lshlrev_b32 v232, 16, v204\n"
      "v_lshlrev_b32 v233, 16, v205\n"
      "v_and_b32 v204, 0xffff0000, v204\n"
      "v_and_b32 v205, 0xffff0000, v205\n"
      "v_add_f32 v172, v172, v232\n"
      "v_add_f32 v174, v174, v233\n"
      "v_add_f32 v173, v173, v204\n"
      "v_add_f32 v175, v175, v205\n"
      "v_lshlrev_b32 v232, 16, v212\n"
      "v_lshlrev_b32 v233, 16, v213\n"
      "v_and_b32 v212, 0xffff0000, v212\n"
      "v_and_b32 v213, 0xffff0000, v213\n"
      "v_add_f32 v172, v172, v232\n"
      "v_add_f32 v174, v174, v233\n"
      "v_add_f32 v173, v173, v212\n"
      "v_add_f32 v175, v175, v213\n"
      "v_cvt_pk_bf16_f32 v180, v172, v173\n"
      "v_cvt_pk_bf16_f32 v181, v174, v175\n"
      "buffer_store_dwordx2 v[180:181], v9, s[16:19], s60 offen\n"
      "s_branch .Ld_end\n"
      ".Ld_out3:\n"
      "s_nop 8\n"
      "v_cvt_pk_bf16_f32 v228, v160, v161\n"
      "buffer_load_dwordx4 v[200:203], v0, s[8:11], s58 offen sc1\n"
      "v_cvt_pk_bf16_f32 v227, v158, v159\n"
      "buffer_store_dwordx4 v[224:227], v0, s[8:11], s37 offen sc1\n"
      "v_cvt_pk_bf16_f32 v229, v162, v163\n"
      "buffer_store_dwordx2 v[228:229], v13, s[8:11], s37 offen sc1\n"
      "s_add_u32 s37, s37, 106496\n"
      "buffer_load_dwordx2 v[204:205], v13, s[8:11], s58 offen sc1\n"
      "s_cmp_lt_u32 s41, 32\n"
      "s_cselect_b32 s59, s39, 0x40000000\n"
      "s_add_u32 s57, s56, 26624\n"
      "s_add_u32 s56, s56, 599040\n"
      "buffer_load_dwordx2 v[212:213], v13, s[8:11], s57 offen sc1\n"
      "s_add_u32 s39, s39, 147456\n"
      "buffer_load_dwordx4 v[208:211], v0, s[8:11], s57 offen sc1\n"
      "s_cmp_lt_u32 s61, 32\n"
      "s_cselect_b32 s60, s40, 0x40000000\n"
      "s_waitcnt vmcnt(12)\n"
      "v_lshlrev_b32 v234, 16, v178\n"
      "v_lshlrev_b32 v233, 16, v177\n"
      "v_lshlrev_b32 v232, 16, v176\n"
      "v_add_f32 v166, v166, v233\n"
      "s_add_u32 s55, s55, 106496\n"
      "v_add_f32 v164, v164, v232\n"
      "s_waitcnt vmcnt(11)\n"
      "v_lshlrev_b32 v233, 16, v181\n"
      "v_lshlrev_b32 v235, 16, v179\n"
      "v_and_b32 v176, 0xffff0000, v176\n"
      "v_lshlrev_b32 v232, 16, v180\n"
      "v_add_f32 v172, v172, v232\n"
      "v_and_b32 v181, 0xffff0000, v181\n"
      "v_add_f32 v168, v168, v234\n"
      "v_add_f32 v174, v174, v233\n"
      "v_add_f32 v165, v165, v176\n"
      "s_waitcnt vmcnt(10)\n"
      "v_lshlrev_b32 v232, 16, v184\n"
      "v_and_b32 v177, 0xffff0000, v177\n"
      "s_add_u32 s40, s40, 4294856704\n"
      "v_add_f32 v167, v167, v177\n"
      "v_and_b32 v180, 0xffff0000, v180\n"
      "v_add_f32 v164, v164, v232\n"
      "v_lshlrev_b32 v233, 16, v185\n"
      "v_add_f32 v166, v166, v233\n"
      "s_waitcnt vmcnt(7)\n"
      "v_lshlrev_b32 v232, 16, v188\n"
      "v_add_f32 v172, v172, v232\n"
      "v_and_b32 v185, 0xffff0000, v185\n"
      "v_lshlrev_b32 v232, 16, v192\n"
      "v_lshlrev_b32 v234, 16, v186\n"
      "v_add_f32 v164, v164, v232\n"
      "v_lshlrev_b32 v233, 16, v189\n"
      "s_waitcnt vmcnt(6)\n"
      "v_lshlrev_b32 v232, 16, v196\n"
      "v_add_f32 v174, v174, v233\n"
      "v_and_b32 v184, 0xffff0000, v184\n"
      "v_add_f32 v172, v172, v232\n"
      "v_add_f32 v170, v170, v235\n"
      "s_add_u32 s41, s41, 6\n"
      "v_add_f32 v165, v165, v184\n"
      "v_add_f32 v168, v168, v234\n"
      "v_lshlrev_b32 v233, 16, v193\n"
      "v_and_b32 v178, 0xffff0000, v178\n"
      "s_waitcnt vmcnt(5)\n"
      "v_lshlrev_b32 v232, 16, v200\n"
      "v_add_f32 v164, v164, v232\n"
      "v_and_b32 v186, 0xffff0000, v186\n"
      "s_waitcnt vmcnt(3)\n"
      "buffer_store_dword v18, v4, s[8:11], s55 offen sc1\n"
      "v_add_f32 v169, v169, v178\n"
      "v_and_b32 v200, 0xffff0000, v200\n"
      "v_add_f32 v173, v173, v180\n"
      "v_add_f32 v166, v166, v233\n"
      "v_lshlrev_b32 v234, 16, v194\n"
      "v_and_b32 v192, 0xffff0000, v192\n"
      "v_add_f32 v175, v175, v181\n"
      "v_and_b32 v189, 0xffff0000, v189\n"
      "s_waitcnt vmcnt(3)\n"
      "v_lshlrev_b32 v232, 16, v204\n"
      "v_lshlrev_b32 v235, 16, v187\n"
      "v_add_f32 v172, v172, v232\n"
      "s_waitcnt vmcnt(1)\n"
      "v_lshlrev_b32 v232, 16, v208\n"
      "v_add_f32 v170, v170, v235\n"
      "v_and_b32 v194, 0xffff0000, v194\n"
      "v_and_b32 v208, 0xffff0000, v208\n"
      "v_add_f32 v167, v167, v185\n"
      "v_lshlrev_b32 v233, 16, v197\n"
      "v_lshlrev_b32 v235, 16, v195\n"
      "v_add_f32 v168, v168, v234\n"
      "v_add_f32 v164, v164, v232\n"
      "v_add_f32 v165, v165, v192\n"
      "v_lshlrev_b32 v234, 16, v202\n"
      "v_add_f32 v174, v174, v233\n"
      "v_lshlrev_b32 v232, 16, v212\n"
      "v_and_b32 v179, 0xffff0000, v179\n"
      "v_add_f32 v169, v169, v186\n"
      "v_lshlrev_b32 v233, 16, v201\n"
      "v_add_f32 v166, v166, v233\n"
      "v_and_b32 v193, 0xffff0000, v193\n"
      "v_and_b32 v197, 0xffff0000, v197\n"
      "v_lshlrev_b32 v233, 16, v205\n"
      "v_add_f32 v170, v170, v235\n"
      "v_add_f32 v174, v174, v233\n"
      "v_lshlrev_b32 v235, 16, v203\n"
      "v_and_b32 v187, 0xffff0000, v187\n"
      "v_lshlrev_b32 v233, 16, v209\n"
      "v_add_f32 v170, v170, v235\n"
      "v_add_f32 v166, v166, v233\n"
      "v_and_b32 v201, 0xffff0000, v201\n"
      "v_add_f32 v165, v165, v200\n"
      "v_and_b32 v188, 0xffff0000, v188\n"
      "v_add_f32 v165, v165, v208\n"
      "v_add_f32 v168, v168, v234\n"
      "v_add_f32 v167, v167, v193\n"
      "v_lshlrev_b32 v233, 16, v213\n"
      "v_add_f32 v171, v171, v179\n"
      "v_add_f32 v167, v167, v201\n"
      "v_lshlrev_b32 v234, 16, v210\n"
      "v_and_b32 v213, 0xffff0000, v213\n"
      "v_add_f32 v173, v173, v188\n"
      "v_and_b32 v209, 0xffff0000, v209\n"
      "v_add_f32 v175, v175, v189\n"
      "v_add_f32 v167, v167, v209\n"
      "v_and_b32 v203, 0xffff0000, v203\n"
      "v_add_f32 v175, v175, v197\n"
      "v_and_b32 v204, 0xffff0000, v204\n"
      "v_and_b32 v195, 0xffff0000, v195\n"
      "v_add_f32 v172, v172, v232\n"
      "v_add_f32 v169, v169, v194\n"
      "v_and_b32 v196, 0xffff0000, v196\n"
      "v_and_b32 v212, 0xffff0000, v212\n"
      "v_cvt_pk_bf16_f32 v177, v166, v167\n"
      "v_add_f32 v171, v171, v187\n"
      "v_and_b32 v205, 0xffff0000, v205\n"
      "s_add_u32 s55, s55, 106496\n"
      "v_and_b32 v202, 0xffff0000, v202\n"
      "v_add_f32 v169, v169, v202\n"
      "v_and_b32 v210, 0xffff0000, v210\n"
      "v_add_f32 v173, v173, v196\n"
      "v_lshlrev_b32 v235, 16, v211\n"
      "v_add_f32 v170, v170, v235\n"
      "v_add_f32 v171, v171, v195\n"
      "v_add_f32 v174, v174, v233\n"
      "v_add_f32 v169, v169, v210\n"
      "v_add_f32 v175, v175, v205\n"
      "v_add_f32 v168, v168, v234\n"
      "v_cvt_pk_bf16_f32 v176, v164, v165\n"
      "v_and_b32 v211, 0xffff0000, v211\n"
      "v_add_f32 v171, v171, v203\n"
      "v_cvt_pk_bf16_f32 v178, v168, v169\n"
      "v_add_f32 v175, v175, v213\n"
      "v_add_f32 v171, v171, v211\n"
      "v_add_f32 v173, v173, v204\n"
      "v_cvt_pk_bf16_f32 v179, v170, v171\n"
      "v_add_f32 v173, v173, v212\n"
      "v_cvt_pk_bf16_f32 v180, v172, v173\n"
      "v_cvt_pk_bf16_f32 v181, v174, v175\n"
      "buffer_store_dwordx2 v[180:181], v9, s[16:19], s60 offen\n"
      "buffer_store_dwordx4 v[176:179], v8, s[16:19], s60 offen\n"
      "s_branch .Ld_end\n"
      ".Ld_out4:\n"
      "v_and_b32 v192, 0xffff0000, v192\n"
      "s_nop 7\n"
      "v_cvt_pk_bf16_f32 v229, v162, v163\n"
      "v_add_f32 v175, v175, v181\n"
      "v_cvt_pk_bf16_f32 v228, v160, v161\n"
      "v_and_b32 v189, 0xffff0000, v189\n"
      "s_waitcnt vmcnt(11)\n"
      "v_lshlrev_b32 v232, 16, v204\n"
      "buffer_store_dwordx2 v[228:229], v13, s[8:11], s37 offen sc1\n"
      "v_lshlrev_b32 v235, 16, v187\n"
      "v_add_f32 v172, v172, v232\n"
      "s_waitcnt vmcnt(10)\n"
      "v_lshlrev_b32 v232, 16, v208\n"
      "v_add_f32 v170, v170, v235\n"
      "v_and_b32 v194, 0xffff0000, v194\n"
      "v_and_b32 v208, 0xffff0000, v208\n"
      "v_add_f32 v167, v167, v185\n"
      "v_lshlrev_b32 v233, 16, v197\n"
      "v_lshlrev_b32 v235, 16, v195\n"
      "v_add_f32 v168, v168, v234\n"
      "v_add_f32 v164, v164, v232\n"
      "v_add_f32 v165, v165, v192\n"
      "v_lshlrev_b32 v234, 16, v202\n"
      "v_add_f32 v174, v174, v233\n"
      "v_lshlrev_b32 v232, 16, v212\n"
      "v_and_b32 v179, 0xffff0000, v179\n"
      "v_add_f32 v169, v169, v186\n"
      "v_lshlrev_b32 v233, 16, v201\n"
      "v_add_f32 v166, v166, v233\n"
      "v_and_b32 v193, 0xffff0000, v193\n"
      "v_and_b32 v197, 0xffff0000, v197\n"
      "v_lshlrev_b32 v233, 16, v205\n"
      "v_add_f32 v170, v170, v235\n"
      "v_add_f32 v174, v174, v233\n"
      "v_lshlrev_b32 v235, 16, v203\n"
      "v_and_b32 v187, 0xffff0000, v187\n"
      "v_lshlrev_b32 v233, 16, v209\n"
      "v_add_f32 v170, v170, v235\n"
      "v_add_f32 v166, v166, v233\n"
      "v_and_b32 v201, 0xffff0000, v201\n"
      "v_add_f32 v165, v165, v200\n"
      "v_and_b32 v188, 0xffff0000, v188\n"
      "v_add_f32 v165, v165, v208\n"
      "v_add_f32 v168, v168, v234\n"
      "v_add_f32 v167, v167, v193\n"
      "v_lshlrev_b32 v233, 16, v213\n"
      "v_add_f32 v171, v171, v179\n"
      "v_add_f32 v167, v167, v201\n"
      "v_lshlrev_b32 v234, 16, v210\n"
      "v_and_b32 v213, 0xffff0000, v213\n"
      "v_add_f32 v173, v173, v188\n"
      "v_and_b32 v209, 0xffff0000, v209\n"
      "v_add_f32 v175, v175, v189\n"
      "v_add_f32 v167, v167, v209\n"
      "v_and_b32 v203, 0xffff0000, v203\n"
      "v_add_f32 v175, v175, v197\n"
      "v_and_b32 v204, 0xffff0000, v204\n"
      "v_and_b32 v195, 0xffff0000, v195\n"
      "v_add_f32 v172, v172, v232\n"
      "v_add_f32 v169, v169, v194\n"
      "v_and_b32 v196, 0xffff0000, v196\n"
      "v_and_b32 v212, 0xffff0000, v212\n"
      "v_cvt_pk_bf16_f32 v177, v166, v167\n"
      "v_add_f32 v171, v171, v187\n"
      "v_and_b32 v205, 0xffff0000, v205\n"
      "s_add_u32 s55, s55, 106496\n"
      "v_and_b32 v202, 0xffff0000, v202\n"
      "v_add_f32 v169, v169, v202\n"
      "v_and_b32 v210, 0xffff0000, v210\n"
      "v_add_f32 v173, v173, v196\n"
      "v_lshlrev_b32 v235, 16, v211\n"
      "v_add_f32 v170, v170, v235\n"
      "v_add_f32 v171, v171, v195\n"
      "s_add_u32 s37, s37, 106496\n"
      "s_waitcnt vmcnt(0)\n"
      "buffer_store_dword v18, v4, s[8:11], s55 offen sc1\n"
      "v_add_f32 v174, v174, v233\n"
      "v_add_f32 v169, v169, v210\n"
      "v_add_f32 v175, v175, v205\n"
      "v_add_f32 v168, v168, v234\n"
      "v_cvt_pk_bf16_f32 v176, v164, v165\n"
      "v_and_b32 v211, 0xffff0000, v211\n"
      "v_add_f32 v171, v171, v203\n"
      "v_cvt_pk_bf16_f32 v178, v168, v169\n"
      "v_add_f32 v175, v175, v213\n"
      "v_add_f32 v171, v171, v211\n"
      "v_add_f32 v173, v173, v204\n"
      "v_cvt_pk_bf16_f32 v179, v170, v171\n"
      "v_add_f32 v173, v173, v212\n"
      "v_cvt_pk_bf16_f32 v180, v172, v173\n"
      "v_cvt_pk_bf16_f32 v181, v174, v175\n"
      "buffer_store_dwordx2 v[180:181], v9, s[16:19], s60 offen\n"
      "buffer_store_dwordx4 v[176:179], v8, s[16:19], s60 offen\n"
      "s_add_u32 s55, s55, 106496\n"
      "s_branch .Ld_end\n"
      ".Ld_in1:\n"
      "s_add_u32 s36, s36, 165888\n"
      "s_min_u32 s36, s36, s6\n"
      "s_mov_b32 s54, s36\n"
      "s_add_u32 s36, s36, 165888\n"
      "s_min_u32 s36, s36, s6\n"
      "buffer_load_dword v20, v2, s[8:11], s36 offen sc1\n"
      "s_mov_b32 s54, s36\n"
      "s_add_u32 m0, s43, 24576\n"
      "s_add_u32 s36, s36, 165888\n"
      "s_waitcnt vmcnt(0)\n"
      "v_cmp_ne_u32 vcc, s21, v20\n"
      "s_cbranch_vccnz .Lsp_d1_17\n"
      ".Lbk_d1_17:\n"
      "buffer_load_dwordx4 v0, s[8:11], s54 offen offset:0 sc1 lds\n"
      "buffer_load_dwordx4 v0, s[8:11], s54 offen offset:1024 sc1 lds\n"
      "buffer_load_dwordx4 v0, s[8:11], s54 offen offset:2048 sc1 lds\n"
      "s_min_u32 s36, s36, s6\n"
      "buffer_load_dword v20, v2, s[8:11], s36 offen sc1\n"
      "buffer_load_dwordx4 v0, s[8:11], s54 offen offset:3072 sc1 lds\n"
      "s_add_u32 m0, s43, 28672\n"
      "s_nop 0\n"
      "buffer_load_dwordx4 v1, s[8:11], s54 offen offset:0 sc1 lds\n"
      "buffer_load_dwordx4 v1, s[8:11], s54 offen offset:1024 sc1 lds\n"
      "s_add_u32 m0, s43, 49152\n"
      "s_mov_b32 s54, s36\n"
      "v_add_u32 v14, 1, v14\n"
      "s_waitcnt vmcnt(3)\n"
      "v_cmp_ne_u32 vcc, s21, v20\n"
      "s_add_u32 s36, s36, 165888\n"
      "s_min_u32 s36, s36, s6\n"
      "s_cbranch_vccnz .Lsp_d2_18\n"
      ".Lbk_d2_18:\n"
      "s_waitcnt vmcnt(0)\n"
      "ds_write_b32 v16, v14\n"
      "buffer_load_dword v20, v2, s[8:11], s36 offen sc1\n"
      "buffer_load_dwordx4 v0, s[8:11], s54 offen offset:3072 sc1 lds\n"
      "s_add_u32 s55, s55, 106496\n"
      "s_add_u32 s37, s37, 173056\n"
      "buffer_load_dwordx4 v0, s[8:11], s54 offen offset:1024 sc1 lds\n"
      "ds_read_b32 v15, v17\n"
      "buffer_load_dwordx4 v0, s[8:11], s54 offen offset:2048 sc1 lds\n"
      "s_waitcnt lgkmcnt(0)\n"
      "v_cmp_gt_i32 vcc, v14, v15\n"
      "buffer_load_dwordx4 v0, s[8:11], s54 offen offset:0 sc1 lds\n"
      "s_add_u32 m0, s43, 53248\n"
      "s_cbranch_vccnz .Lsp_dy1_19\n"
      ".Lbk_dy1_19:\n"
      "buffer_load_dwordx4 v1, s[8:11], s54 offen offset:0 sc1 lds\n"
      "ds_read_b128 v[56:59], v0 offset:24576\n"
      "ds_read_b128 v[64:67], v0 offset:26624\n"
      "buffer_load_dwordx4 v1, s[8:11], s54 offen offset:1024 sc1 lds\n"
      "ds_read_b128 v[68:71], v0 offset:27648\n"
      "ds_read_b128 v[80:83], v0 offset:30720\n"
      "ds_read_b128 v[60:63], v0 offset:25600\n"
      "ds_read_b128 v[92:95], v0 offset:33792\n"
      "ds_read_b128 v[84:87], v0 offset:31744\n"
      "ds_read_b128 v[72:75], v0 offset:28672\n"
      "ds_read_b128 v[76:79], v0 offset:29696\n"
      "ds_read_b128 v[100:103], v0 offset:35840\n"
      "ds_read_b128 v[96:99], v0 offset:34816\n"
      "ds_read_b128 v[116:119], v0 offset:39936\n"
      "ds_read_b128 v[120:123], v0 offset:40960\n"
      "ds_read_b128 v[108:111], v0 offset:37888\n"
      "ds_read_b128 v[88:91], v0 offset:32768\n"
      "s_waitcnt vmcnt(0) lgkmcnt(0)\n"
      "s_nop 4\n"
      "s_branch .Ld_t1\n"
      ".Ld_in2:\n"
      "s_add_u32 s36, s36, 165888\n"
      "s_min_u32 s36, s36, s6\n"
      "s_mov_b32 s54, s36\n"
      "s_add_u32 s36, s36, 165888\n"
      "s_min_u32 s36, s36, s6\n"
      "s_mov_b32 s54, s36\n"
      "s_add_u32 s36, s36, 165888\n"
      "s_min_u32 s36, s36, s6\n"
      "buffer_load_dword v20, v2, s[8:11], s36 offen sc1\n"
      "s_add_u32 m0, s43, 49152\n"
      "s_mov_b32 s54, s36\n"
      "s_waitcnt vmcnt(0)\n"
      "v_cmp_ne_u32 vcc, s21, v20\n"
      "s_add_u32 s36, s36, 165888\n"
      "s_min_u32 s36, s36, s6\n"
      "s_cbranch_vccnz .Lsp_d2_20\n"
      ".Lbk_d2_20:\n"
      "buffer_load_dword v20, v2, s[8:11], s36 offen sc1\n"
      "buffer_load_dwordx4 v0, s[8:11], s54 offen offset:3072 sc1 lds\n"
      "s_add_u32 s55, s55, 106496\n"
      "s_add_u32 s37, s37, 173056\n"
      "buffer_load_dwordx4 v0, s[8:11], s54 offen offset:1024 sc1 lds\n"
      "buffer_load_dwordx4 v0, s[8:11], s54 offen offset:2048 sc1 lds\n"
      "buffer_load_dwordx4 v0, s[8:11], s54 offen offset:0 sc1 lds\n"
      "s_add_u32 m0, s43, 53248\n"
      "s_nop 0\n"
      "buffer_load_dwordx4 v1, s[8:11], s54 offen offset:0 sc1 lds\n"
      "buffer_load_dwordx4 v1, s[8:11], s54 offen offset:1024 sc1 lds\n"
      "s_mov_b32 s54, s36\n"
      "s_add_u32 s36, s36, 165888\n"
      "s_add_u32 m0, s43, 73728\n"
      "s_waitcnt vmcnt(6)\n"
      "v_cmp_ne_u32 vcc, s21, v20\n"
      "s_add_u32 s38, s38, 147456\n"
      "s_min_u32 s36, s36, s6\n"
      "s_cbranch_vccnz .Lsp_d3_21\n"
      ".Lbk_d3_21:\n"
      "buffer_load_dwordx4 v0, s[8:11], s54 offen offset:2048 sc1 lds\n"
      "v_add_u32 v14, 1, v14\n"
      "buffer_load_dwordx4 v0, s[8:11], s54 offen offset:1024 sc1 lds\n"
      "buffer_load_dword v20, v2, s[8:11], s36 offen sc1\n"
      "s_waitcnt vmcnt(3)\n"
      "ds_write_b32 v16, v14\n"
      "buffer_load_dwordx4 v0, s[8:11], s54 offen offset:0 sc1 lds\n"
      "buffer_load_dwordx4 v0, s[8:11], s54 offen offset:3072 sc1 lds\n"
      "s_add_u32 m0, s43, 77824\n"
      "ds_read_b32 v15, v17\n"
      "s_add_u32 s55, s55, 173056\n"
      "buffer_load_dwordx4 v1, s[8:11], s54 offen offset:0 sc1 lds\n"
      "s_waitcnt lgkmcnt(0)\n"
      "v_cmp_gt_i32 vcc, v14, v15\n"
      "buffer_load_dwordx4 v1, s[8:11], s54 offen offset:1024 sc1 lds\n"
      "s_cbranch_vccnz .Lsp_dy2_22\n"
      ".Lbk_dy2_22:\n"
      "ds_read_b128 v[68:71], v22 offset:3072\n"
      "ds_read_b128 v[60:63], v22 offset:1024\n"
      "ds_read_b128 v[80:83], v22 offset:6144\n"
      "ds_read_b128 v[72:75], v22 offset:4096\n"
      "ds_read_b128 v[76:79], v22 offset:5120\n"
      "ds_read_b128 v[96:99], v22 offset:10240\n"
      "ds_read_b128 v[84:87], v22 offset:7168\n"
      "ds_read_b128 v[56:59], v22 offset:0\n"
      "ds_read_b128 v[92:95], v22 offset:9216\n"
      "s_mov_b32 s54, s36\n"
      "ds_read_b128 v[64:67], v22 offset:2048\n"
      "ds_read_b128 v[100:103], v22 offset:11264\n"
      "ds_read_b128 v[132:135], v22 offset:19456\n"
      "ds_read_b128 v[112:115], v22 offset:14336\n"
      "s_add_u32 m0, s43, 98304\n"
      "s_waitcnt vmcnt(0) lgkmcnt(0)\n"
      "s_nop 4\n"
      "s_branch .Ld_t2\n"
      ".Ld_in3:\n"
      "s_add_u32 s36, s36, 165888\n"
      "s_min_u32 s36, s36, s6\n"
      "s_mov_b32 s54, s36\n"
      "s_add_u32 s36, s36, 165888\n"
      "s_min_u32 s36, s36, s6\n"
      "s_mov_b32 s54, s36\n"
      "s_add_u32 s36, s36, 165888\n"
      "s_min_u32 s36, s36, s6\n"
      "s_mov_b32 s54, s36\n"
      "s_add_u32 s36, s36, 165888\n"
      "s_min_u32 s36, s36, s6\n"
      "buffer_load_dword v20, v2, s[8:11], s36 offen sc1\n"
      "s_add_u32 s55, s55, 106496\n"
      "s_add_u32 s37, s37, 173056\n"
      "s_mov_b32 s54, s36\n"
      "s_add_u32 s36, s36, 165888\n"
      "s_add_u32 m0, s43, 73728\n"
      "s_waitcnt vmcnt(0)\n"
      "v_cmp_ne_u32 vcc, s21, v20\n"
      "s_add_u32 s38, s38, 147456\n"
      "s_min_u32 s36, s36, s6\n"
      "s_cbranch_vccnz .Lsp_d3_23\n"
      ".Lbk_d3_23:\n"
      "buffer_load_dwordx4 v0, s[8:11], s54 offen offset:2048 sc1 lds\n"
      "buffer_load_dwordx4 v0, s[8:11], s54 offen offset:1024 sc1 lds\n"
      "buffer_load_dword v20, v2, s[8:11], s36 offen sc1\n"
      "buffer_load_dwordx4 v0, s[8:11], s54 offen offset:0 sc1 lds\n"
      "buffer_load_dwordx4 v0, s[8:11], s54 offen offset:3072 sc1 lds\n"
      "s_add_u32 m0, s43, 77824\n"
      "s_add_u32 s55, s55, 173056\n"
      "buffer_load_dwordx4 v1, s[8:11], s54 offen offset:0 sc1 lds\n"
      "buffer_load_dwordx4 v1, s[8:11], s54 offen offset:1024 sc1 lds\n"
      "s_mov_b32 s54, s36\n"
      "s_add_u32 m0, s43, 98304\n"
      "s_add_u32 s36, s36, 165888\n"
      "v_add_u32 v14, 1, v14\n"
      "s_waitcnt vmcnt(4)\n"
      "v_cmp_ne_u32 vcc, s21, v20\n"
      "s_cbranch_vccnz .Lsp_d4_24\n"
      ".Lbk_d4_24:\n"
      "s_min_u32 s36, s36, s6\n"
      "buffer_load_dwordx4 v0, s[8:11], s54 offen offset:0 sc1 lds\n"
      "buffer_load_dwordx4 v0, s[8:11], s54 offen offset:2048 sc1 lds\n"
      "s_waitcnt vmcnt(2)\n"
      "ds_write_b32 v16, v14\n"
      "buffer_load_dword v20, v2, s[8:11], s36 offen sc1\n"
      "buffer_load_dwordx4 v0, s[8:11], s54 offen offset:3072 sc1 lds\n"
      "ds_read_b32 v15, v17\n"
      "buffer_load_dwordx4 v0, s[8:11], s54 offen offset:1024 sc1 lds\n"
      "s_add_u32 m0, s43, 102400\n"
      "s_nop 0\n"
      "buffer_load_dwordx4 v1, s[8:11], s54 offen offset:1024 sc1 lds\n"
      "s_waitcnt lgkmcnt(0)\n"
      "v_cmp_gt_i32 vcc, v14, v15\n"
      "buffer_load_dwordx4 v1, s[8:11], s54 offen offset:0 sc1 lds\n"
      "s_cbranch_vccnz .Lsp_dy3_25\n"
      ".Lbk_dy3_25:\n"
      "ds_read_b128 v[60:63], v22 offset:25600\n"
      "ds_read_b128 v[64:67], v22 offset:26624\n"
      "s_mov_b32 s54, s36\n"
      "ds_read_b128 v[72:75], v22 offset:28672\n"
      "ds_read_b128 v[68:71], v22 offset:27648\n"
      "ds_read_b128 v[92:95], v22 offset:33792\n"
      "ds_read_b128 v[56:59], v22 offset:24576\n"
      "ds_read_b128 v[100:103], v22 offset:35840\n"
      "ds_read_b128 v[80:83], v22 offset:30720\n"
      "ds_read_b128 v[112:115], v22 offset:38912\n"
      "s_add_u32 s37, s37, 106496\n"
      "ds_read_b128 v[88:91], v22 offset:32768\n"
      "ds_read_b128 v[96:99], v22 offset:34816\n"
      "ds_read_b128 v[76:79], v22 offset:29696\n"
      "ds_read_b128 v[116:119], v22 offset:39936\n"
      "ds_read_b128 v[84:87], v22 offset:31744\n"
      "ds_read_b128 v[104:107], v22 offset:36864\n"
      "s_waitcnt vmcnt(0) lgkmcnt(0)\n"
      "s_nop 4\n"
      "s_branch .Ld_t3\n"
      ".Ld_in4:\n"
      "s_add_u32 s36, s36, 165888\n"
      "s_min_u32 s36, s36, s6\n"
      "s_mov_b32 s54, s36\n"
      "s_add_u32 s36, s36, 165888\n"
      "s_min_u32 s36, s36, s6\n"
      "s_mov_b32 s54, s36\n"
      "s_add_u32 s36, s36, 165888\n"
      "s_min_u32 s36, s36, s6\n"
      "s_mov_b32 s54, s36\n"
      "s_add_u32 s36, s36, 165888\n"
      "s_min_u32 s36, s36, s6\n"
      "s_add_u32 s55, s55, 106496\n"
      "s_add_u32 s37, s37, 173056\n"
      "s_mov_b32 s54, s36\n"
      "s_add_u32 s36, s36, 165888\n"
      "s_add_u32 s38, s38, 147456\n"
      "s_min_u32 s36, s36, s6\n"
      "buffer_load_dword v20, v2, s[8:11], s36 offen sc1\n"
      "s_add_u32 s55, s55, 173056\n"
      "s_mov_b32 s54, s36\n"
      "s_add_u32 m0, s43, 98304\n"
      "s_add_u32 s36, s36, 165888\n"
      "s_waitcnt vmcnt(0)\n"
      "v_cmp_ne_u32 vcc, s21, v20\n"
      "s_cbranch_vccnz .Lsp_d4_26\n"
      ".Lbk_d4_26:\n"
      "s_min_u32 s36, s36, s6\n"
      "buffer_load_dwordx4 v0, s[8:11], s54 offen offset:0 sc1 lds\n"
      "buffer_load_dwordx4 v0, s[8:11], s54 offen offset:2048 sc1 lds\n"
      "buffer_load_dword v20, v2, s[8:11], s36 offen sc1\n"
      "buffer_load_dwordx4 v0, s[8:11], s54 offen offset:3072 sc1 lds\n"
      "buffer_load_dwordx4 v0, s[8:11], s54 offen offset:1024 sc1 lds\n"
      "s_add_u32 m0, s43, 102400\n"
      "s_nop 0\n"
      "buffer_load_dwordx4 v1, s[8:11], s54 offen offset:1024 sc1 lds\n"
      "buffer_load_dwordx4 v1, s[8:11], s54 offen offset:0 sc1 lds\n"
      "s_mov_b32 s54, s36\n"
      "s_add_u32 s37, s37, 106496\n"
      "s_add_u32 m0, s43, 122880\n"
      "s_waitcnt vmcnt(4)\n"
      "v_cmp_ne_u32 vcc, s21, v20\n"
      "s_add_u32 s36, s36, 165888\n"
      "s_add_u32 s55, s55, 106496\n"
      "s_cbranch_vccnz .Lsp_d5_27\n"
      ".Lbk_d5_27:\n"
      "buffer_load_dwordx4 v0, s[8:11], s54 offen offset:1024 sc1 lds\n"
      "v_add_u32 v14, 1, v14\n"
      "buffer_load_dwordx4 v0, s[8:11], s54 offen offset:0 sc1 lds\n"
      "buffer_load_dwordx4 v0, s[8:11], s54 offen offset:2048 sc1 lds\n"
      "s_waitcnt vmcnt(3)\n"
      "ds_write_b32 v16, v14\n"
      "buffer_load_dwordx4 v0, s[8:11], s54 offen offset:3072 sc1 lds\n"
      "s_min_u32 s36, s36, s6\n"
      "s_add_u32 m0, s43, 126976\n"
      "s_nop 0\n"
      "buffer_load_dwordx4 v1, s[8:11], s54 offen offset:1024 sc1 lds\n"
      "ds_read_b32 v15, v17\n"
      "buffer_load_dword v20, v2, s[8:11], s36 offen sc1\n"
      "s_waitcnt lgkmcnt(0)\n"
      "v_cmp_gt_i32 vcc, v14, v15\n"
      "buffer_load_dwordx4 v1, s[8:11], s54 offen offset:0 sc1 lds\n"
      "s_cbranch_vccnz .Lsp_dy4_28\n"
      ".Lbk_dy4_28:\n"
      "ds_read_b128 v[60:63], v23 offset:1024\n"
      "ds_read_b128 v[64:67], v23 offset:2048\n"
      "ds_read_b128 v[72:75], v23 offset:4096\n"
      "ds_read_b128 v[68:71], v23 offset:3072\n"
      "s_add_u32 s61, s41, 4294967264\n"
      "ds_read_b128 v[80:83], v23 offset:6144\n"
      "ds_read_b128 v[84:87], v23 offset:7168\n"
      "ds_read_b128 v[76:79], v23 offset:5120\n"
      "s_add_u32 s37, s37, 106496\n"
      "ds_read_b128 v[56:59], v23 offset:0\n"
      "ds_read_b128 v[88:91], v23 offset:8192\n"
      "ds_read_b128 v[96:99], v23 offset:10240\n"
      "ds_read_b128 v[100:103], v23 offset:11264\n"
      "ds_read_b128 v[132:135], v23 offset:19456\n"
      "ds_read_b128 v[104:107], v23 offset:12288\n"
      "s_waitcnt vmcnt(0) lgkmcnt(0)\n"
      "s_nop 4\n"
      "s_branch .Ld_t4\n"
      ".Ld_in5:\n"
      "s_add_u32 s36, s36, 165888\n"
      "s_min_u32 s36, s36, s6\n"
      "s_mov_b32 s54, s36\n"
      "s_add_u32 s36, s36, 165888\n"
      "s_min_u32 s36, s36, s6\n"
      "s_mov_b32 s54, s36\n"
      "s_add_u32 s36, s36, 165888\n"
      "s_min_u32 s36, s36, s6\n"
      "s_mov_b32 s54, s36\n"
      "s_add_u32 s36, s36, 165888\n"
      "s_min_u32 s36, s36, s6\n"
      "s_add_u32 s55, s55, 106496\n"
      "s_add_u32 s37, s37, 173056\n"
      "s_mov_b32 s54, s36\n"
      "s_add_u32 s36, s36, 165888\n"
      "s_add_u32 s38, s38, 147456\n"
      "s_min_u32 s36, s36, s6\n"
      "s_add_u32 s55, s55, 173056\n"
      "s_mov_b32 s54, s36\n"
      "s_add_u32 s36, s36, 165888\n"
      "s_min_u32 s36, s36, s6\n"
      "buffer_load_dword v20, v2, s[8:11], s36 offen sc1\n"
      "s_mov_b32 s54, s36\n"
      "s_add_u32 s37, s37, 106496\n"
      "s_add_u32 m0, s43, 122880\n"
      "s_waitcnt vmcnt(0)\n"
      "v_cmp_ne_u32 vcc, s21, v20\n"
      "s_add_u32 s36, s36, 165888\n"
      "s_add_u32 s55, s55, 106496\n"
      "s_cbranch_vccnz .Lsp_d5_29\n"
      ".Lbk_d5_29:\n"
      "buffer_load_dwordx4 v0, s[8:11], s54 offen offset:1024 sc1 lds\n"
      "buffer_load_dwordx4 v0, s[8:11], s54 offen offset:0 sc1 lds\n"
      "buffer_load_dwordx4 v0, s[8:11], s54 offen offset:2048 sc1 lds\n"
      "buffer_load_dwordx4 v0, s[8:11], s54 offen offset:3072 sc1 lds\n"
      "s_min_u32 s36, s36, s6\n"
      "s_add_u32 m0, s43, 126976\n"
      "s_nop 0\n"
      "buffer_load_dwordx4 v1, s[8:11], s54 offen offset:1024 sc1 lds\n"
      "buffer_load_dword v20, v2, s[8:11], s36 offen sc1\n"
      "buffer_load_dwordx4 v1, s[8:11], s54 offen offset:0 sc1 lds\n"
      "s_add_u32 s61, s41, 4294967264\n"
      "s_add_u32 s37, s37, 106496\n"
      "s_add_u32 m0, s43, 0\n"
      "s_mov_b32 s54, s36\n"
      "v_add_u32 v14, 1, v14\n"
      "s_add_u32 s36, s36, 165888\n"
      "s_add_u32 s37, s37, 106496\n"
      "s_min_u32 s36, s36, s6\n"
      "s_cmp_lt_u32 s41, 32\n"
      "s_cselect_b32 s59, s39, 0x40000000\n"
      "s_add_u32 s56, s56, 599040\n"
      "s_waitcnt vmcnt(1)\n"
      "v_cmp_ne_u32 vcc, s21, v20\n"
      "s_add_u32 s39, s39, 147456\n"
      "s_waitcnt vmcnt(0)\n"
      "ds_write_b32 v16, v14\n"
      "s_cmp_lt_u32 s61, 32\n"
      "s_cselect_b32 s60, s40, 0x40000000\n"
      "s_cbranch_vccnz .Lsp_d0_30\n"
      ".Lbk_d0_30:\n"
      "buffer_load_dwordx4 v0, s[8:11], s54 offen offset:0 sc1 lds\n"
      "s_add_u32 s55, s55, 106496\n"
      "buffer_load_dwordx4 v0, s[8:11], s54 offen offset:1024 sc1 lds\n"
      "buffer_load_dword v20, v2, s[8:11], s36 offen sc1\n"
      "ds_read_b32 v15, v17\n"
      "buffer_load_dwordx4 v0, s[8:11], s54 offen offset:2048 sc1 lds\n"
      "s_add_u32 s40, s40, 4294856704\n"
      "buffer_load_dwordx4 v0, s[8:11], s54 offen offset:3072 sc1 lds\n"
      "s_waitcnt lgkmcnt(0)\n"
      "v_cmp_gt_i32 vcc, v14, v15\n"
      "s_add_u32 m0, s43, 4096\n"
      "s_nop 0\n"
      "buffer_load_dwordx4 v1, s[8:11], s54 offen offset:1024 sc1 lds\n"
      "s_cbranch_vccnz .Lsp_dy5_31\n"
      ".Lbk_dy5_31:\n"
      "ds_read_b128 v[56:59], v23 offset:24576\n"
      "ds_read_b128 v[64:67], v23 offset:26624\n"
      "buffer_load_dwordx4 v1, s[8:11], s54 offen offset:0 sc1 lds\n"
      "ds_read_b128 v[76:79], v23 offset:29696\n"
      "s_add_u32 s41, s41, 6\n"
      "ds_read_b128 v[88:91], v23 offset:32768\n"
      "ds_read_b128 v[68:71], v23 offset:27648\n"
      "ds_read_b128 v[96:99], v23 offset:34816\n"
      "ds_read_b128 v[80:83], v23 offset:30720\n"
      "ds_read_b128 v[72:75], v23 offset:28672\n"
      "s_mov_b32 s54, s36\n"
      "ds_read_b128 v[112:115], v23 offset:38912\n"
      "ds_read_b128 v[60:63], v23 offset:25600\n"
      "ds_read_b128 v[104:107], v23 offset:36864\n"
      "ds_read_b128 v[84:87], v23 offset:31744\n"
      "ds_read_b128 v[140:143], v23 offset:46080\n"
      "ds_read_b128 v[100:103], v23 offset:35840\n"
      "s_waitcnt vmcnt(0) lgkmcnt(0)\n"
      "s_nop 4\n"
      "s_branch .Ld_t5\n"
      ".Ld_end:\n"
      "s_endpgm\n"
      ".Lsp_d0_1:\n"
      "s_mov_b32 s22, s23\n"
      ".Lsl_d0_1:\n"
      "buffer_load_dword v20, v2, s[8:11], s54 offen sc1\n"
      "s_waitcnt vmcnt(0)\n"
      "v_cmp_ne_u32 vcc, s21, v20\n"
      "s_cbranch_vccz .Lbk_d0_1\n"
      "s_sleep 6\n"
      "s_sub_u32 s22, s22, 1\n"
      "s_cbranch_scc0 .Lsl_d0_1\n"
      "s_mov_b32 s23, 0\n"
      "s_branch .Lbk_d0_1\n"
      ".Lsp_d1_2:\n"
      "s_mov_b32 s22, s23\n"
      ".Lsl_d1_2:\n"
      "buffer_load_dword v20, v2, s[8:11], s54 offen sc1\n"
      "s_waitcnt vmcnt(0)\n"
      "v_cmp_ne_u32 vcc, s21, v20\n"
      "s_cbranch_vccz .Lbk_d1_2\n"
      "s_sleep 6\n"
      "s_sub_u32 s22, s22, 1\n"
      "s_cbranch_scc0 .Lsl_d1_2\n"
      "s_mov_b32 s23, 0\n"
      "s_branch .Lbk_d1_2\n"
      ".Lsp_dy0_3:\n"
      "s_lshl_b32 s22, s23, 4\n"
      ".Lsl_dy0_3:\n"
      "ds_read_b32 v15, v17\n"
      "s_waitcnt lgkmcnt(0)\n"
      "v_cmp_gt_i32 vcc, v14, v15\n"
      "s_cbranch_vccz .Lbk_dy0_3\n"
      "s_sub_u32 s22, s22, 1\n"
      "s_cbranch_scc0 .Lsl_dy0_3\n"
      "s_mov_b32 s23, 0\n"
      "s_branch .Lbk_dy0_3\n"
      ".Lsp_d2_4:\n"
      "s_mov_b32 s22, s23\n"
      ".Lsl_d2_4:\n"
      "buffer_load_dword v20, v2, s[8:11], s54 offen sc1\n"
      "s_waitcnt vmcnt(0)\n"
      "v_cmp_ne_u32 vcc, s21, v20\n"
      "s_cbranch_vccz .Lbk_d2_4\n"
      "s_sleep 6\n"
      "s_sub_u32 s22, s22, 1\n"
      "s_cbranch_scc0 .Lsl_d2_4\n"
      "s_mov_b32 s23, 0\n"
      "s_branch .Lbk_d2_4\n"
      ".Lsp_dy1_5:\n"
      "s_lshl_b32 s22, s23, 4\n"
      ".Lsl_dy1_5:\n"
      "ds_read_b32 v15, v17\n"
      "s_waitcnt lgkmcnt(0)\n"
      "v_cmp_gt_i32 vcc, v14, v15\n"
      "s_cbranch_vccz .Lbk_dy1_5\n"
      "s_sub_u32 s22, s22, 1\n"
      "s_cbranch_scc0 .Lsl_dy1_5\n"
      "s_mov_b32 s23, 0\n"
      "s_branch .Lbk_dy1_5\n"
      ".Lsp_d3_6:\n"
      "s_mov_b32 s22, s23\n"
      ".Lsl_d3_6:\n"
      "buffer_load_dword v20, v2, s[8:11], s54 offen sc1\n"
      "s_waitcnt vmcnt(0)\n"
      "v_cmp_ne_u32 vcc, s21, v20\n"
      "s_cbranch_vccz .Lbk_d3_6\n"
      "s_sleep 6\n"
      "s_sub_u32 s22, s22, 1\n"
      "s_cbranch_scc0 .Lsl_d3_6\n"
      "s_mov_b32 s23, 0\n"
      "s_branch .Lbk_d3_6\n"
      ".Lsp_dy2_7:\n"
      "s_lshl_b32 s22, s23, 4\n"
      ".Lsl_dy2_7:\n"
      "ds_read_b32 v15, v17\n"
      "s_waitcnt lgkmcnt(0)\n"
      "v_cmp_gt_i32 vcc, v14, v15\n"
      "s_cbranch_vccz .Lbk_dy2_7\n"
      "s_sub_u32 s22, s22, 1\n"
      "s_cbranch_scc0 .Lsl_dy2_7\n"
      "s_mov_b32 s23, 0\n"
      "s_branch .Lbk_dy2_7\n"
      ".Lsp_d4_8:\n"
      "s_mov_b32 s22, s23\n"
      ".Lsl_d4_8:\n"
      "buffer_load_dword v20, v2, s[8:11], s54 offen sc1\n"
      "s_waitcnt vmcnt(0)\n"
      "v_cmp_ne_u32 vcc, s21, v20\n"
      "s_cbranch_vccz .Lbk_d4_8\n"
      "s_sleep 6\n"
      "s_sub_u32 s22, s22, 1\n"
      "s_cbranch_scc0 .Lsl_d4_8\n"
      "s_mov_b32 s23, 0\n"
      "s_branch .Lbk_d4_8\n"
      ".Lsp_dy3_9:\n"
      "s_lshl_b32 s22, s23, 4\n"
      ".Lsl_dy3_9:\n"
      "ds_read_b32 v15, v17\n"
      "s_waitcnt lgkmcnt(0)\n"
      "v_cmp_gt_i32 vcc, v14, v15\n"
      "s_cbranch_vccz .Lbk_dy3_9\n"
      "s_sub_u32 s22, s22, 1\n"
      "s_cbranch_scc0 .Lsl_dy3_9\n"
      "s_mov_b32 s23, 0\n"
      "s_branch .Lbk_dy3_9\n"
      ".Lsp_d5_10:\n"
      "s_mov_b32 s22, s23\n"
      ".Lsl_d5_10:\n"
      "buffer_load_dword v20, v2, s[8:11], s54 offen sc1\n"
      "s_waitcnt vmcnt(0)\n"
      "v_cmp_ne_u32 vcc, s21, v20\n"
      "s_cbranch_vccz .Lbk_d5_10\n"
      "s_sleep 6\n"
      "s_sub_u32 s22, s22, 1\n"
      "s_cbranch_scc0 .Lsl_d5_10\n"
      "s_mov_b32 s23, 0\n"
      "s_branch .Lbk_d5_10\n"
      ".Lsp_dy4_11:\n"
      "s_lshl_b32 s22, s23, 4\n"
      ".Lsl_dy4_11:\n"
      "ds_read_b32 v15, v17\n"
      "s_waitcnt lgkmcnt(0)\n"
      "v_cmp_gt_i32 vcc, v14, v15\n"
      "s_cbranch_vccz .Lbk_dy4_11\n"
      "s_sub_u32 s22, s22, 1\n"
      "s_cbranch_scc0 .Lsl_dy4_11\n"
      "s_mov_b32 s23, 0\n"
      "s_branch .Lbk_dy4_11\n"
      ".Lsp_df_12:\n"
      "s_mov_b32 s22, s23\n"
      ".Lsl_df_12:\n"
      "buffer_load_dword v21, v3, s[8:11], s56 offen sc1\n"
      "s_waitcnt vmcnt(0)\n"
      "v_cmp_ne_u32 vcc, s21, v21\n"
      "s_cbranch_vccz .Lbk_df_12\n"
      "s_sleep 6\n"
      "s_sub_u32 s22, s22, 1\n"
      "s_cbranch_scc0 .Lsl_df_12\n"
      "s_mov_b32 s23, 0\n"
      "s_branch .Lbk_df_12\n"
      ".Lsp_d0_13:\n"
      "s_mov_b32 s22, s23\n"
      ".Lsl_d0_13:\n"
      "buffer_load_dword v20, v2, s[8:11], s54 offen sc1\n"
      "s_waitcnt vmcnt(0)\n"
      "v_cmp_ne_u32 vcc, s21, v20\n"
      "s_cbranch_vccz .Lbk_d0_13\n"
      "s_sleep 6\n"
      "s_sub_u32 s22, s22, 1\n"
      "s_cbranch_scc0 .Lsl_d0_13\n"
      "s_mov_b32 s23, 0\n"
      "s_branch .Lbk_d0_13\n"
      ".Lsp_dy5_14:\n"
      "s_lshl_b32 s22, s23, 4\n"
      ".Lsl_dy5_14:\n"
      "ds_read_b32 v15, v17\n"
      "s_waitcnt lgkmcnt(0)\n"
      "v_cmp_gt_i32 vcc, v14, v15\n"
      "s_cbranch_vccz .Lbk_dy5_14\n"
      "s_sub_u32 s22, s22, 1\n"
      "s_cbranch_scc0 .Lsl_dy5_14\n"
      "s_mov_b32 s23, 0\n"
      "s_branch .Lbk_dy5_14\n"
      ".Lsp_d1_15:\n"
      "s_mov_b32 s22, s23\n"
      ".Lsl_d1_15:\n"
      "buffer_load_dword v20, v2, s[8:11], s54 offen sc1\n"
      "s_waitcnt vmcnt(0)\n"
      "v_cmp_ne_u32 vcc, s21, v20\n"
      "s_cbranch_vccz .Lbk_d1_15\n"
      "s_sleep 6\n"
      "s_sub_u32 s22, s22, 1\n"
      "s_cbranch_scc0 .Lsl_d1_15\n"
      "s_mov_b32 s23, 0\n"
      "s_branch .Lbk_d1_15\n"
      ".Lsp_dy0_16:\n"
      "s_lshl_b32 s22, s23, 4\n"
      ".Lsl_dy0_16:\n"
      "ds_read_b32 v15, v17\n"
      "s_waitcnt lgkmcnt(0)\n"
      "v_cmp_gt_i32 vcc, v14, v15\n"
      "s_cbranch_vccz .Lbk_dy0_16\n"
      "s_sub_u32 s22, s22, 1\n"
      "s_cbranch_scc0 .Lsl_dy0_16\n"
      "s_mov_b32 s23, 0\n"
      "s_branch .Lbk_dy0_16\n"
      ".Lsp_d1_17:\n"
      "s_mov_b32 s22, s23\n"
      ".Lsl_d1_17:\n"
      "buffer_load_dword v20, v2, s[8:11], s54 offen sc1\n"
      "s_waitcnt vmcnt(0)\n"
      "v_cmp_ne_u32 vcc, s21, v20\n"
      "s_cbranch_vccz .Lbk_d1_17\n"
      "s_sleep 6\n"
      "s_sub_u32 s22, s22, 1\n"
      "s_cbranch_scc0 .Lsl_d1_17\n"
      "s_mov_b32 s23, 0\n"
      "s_branch .Lbk_d1_17\n"
      ".Lsp_d2_18:\n"
      "s_mov_b32 s22, s23\n"
      ".Lsl_d2_18:\n"
      "buffer_load_dword v20, v2, s[8:11], s54 offen sc1\n"
      "s_waitcnt vmcnt(0)\n"
      "v_cmp_ne_u32 vcc, s21, v20\n"
      "s_cbranch_vccz .Lbk_d2_18\n"
      "s_sleep 6\n"
      "s_sub_u32 s22, s22, 1\n"
      "s_cbranch_scc0 .Lsl_d2_18\n"
      "s_mov_b32 s23, 0\n"
      "s_branch .Lbk_d2_18\n"
      ".Lsp_dy1_19:\n"
      "s_lshl_b32 s22, s23, 4\n"
      ".Lsl_dy1_19:\n"
      "ds_read_b32 v15, v17\n"
      "s_waitcnt lgkmcnt(0)\n"
      "v_cmp_gt_i32 vcc, v14, v15\n"
      "s_cbranch_vccz .Lbk_dy1_19\n"
      "s_sub_u32 s22, s22, 1\n"
      "s_cbranch_scc0 .Lsl_dy1_19\n"
      "s_mov_b32 s23, 0\n"
      "s_branch .Lbk_dy1_19\n"
      ".Lsp_d2_20:\n"
      "s_mov_b32 s22, s23\n"
      ".Lsl_d2_20:\n"
      "buffer_load_dword v20, v2, s[8:11], s54 offen sc1\n"
      "s_waitcnt vmcnt(0)\n"
      "v_cmp_ne_u32 vcc, s21, v20\n"
      "s_cbranch_vccz .Lbk_d2_20\n"
      "s_sleep 6\n"
      "s_sub_u32 s22, s22, 1\n"
      "s_cbranch_scc0 .Lsl_d2_20\n"
      "s_mov_b32 s23, 0\n"
      "s_branch .Lbk_d2_20\n"
      ".Lsp_d3_21:\n"
      "s_mov_b32 s22, s23\n"
      ".Lsl_d3_21:\n"
      "buffer_load_dword v20, v2, s[8:11], s54 offen sc1\n"
      "s_waitcnt vmcnt(0)\n"
      "v_cmp_ne_u32 vcc, s21, v20\n"
      "s_cbranch_vccz .Lbk_d3_21\n"
      "s_sleep 6\n"
      "s_sub_u32 s22, s22, 1\n"
      "s_cbranch_scc0 .Lsl_d3_21\n"
      "s_mov_b32 s23, 0\n"
      "s_branch .Lbk_d3_21\n"
      ".Lsp_dy2_22:\n"
      "s_lshl_b32 s22, s23, 4\n"
      ".Lsl_dy2_22:\n"
      "ds_read_b32 v15, v17\n"
      "s_waitcnt lgkmcnt(0)\n"
      "v_cmp_gt_i32 vcc, v14, v15\n"
      "s_cbranch_vccz .Lbk_dy2_22\n"
      "s_sub_u32 s22, s22, 1\n"
      "s_cbranch_scc0 .Lsl_dy2_22\n"
      "s_mov_b32 s23, 0\n"
      "s_branch .Lbk_dy2_22\n"
      ".Lsp_d3_23:\n"
      "s_mov_b32 s22, s23\n"
      ".Lsl_d3_23:\n"
      "buffer_load_dword v20, v2, s[8:11], s54 offen sc1\n"
      "s_waitcnt vmcnt(0)\n"
      "v_cmp_ne_u32 vcc, s21, v20\n"
      "s_cbranch_vccz .Lbk_d3_23\n"
      "s_sleep 6\n"
      "s_sub_u32 s22, s22, 1\n"
      "s_cbranch_scc0 .Lsl_d3_23\n"
      "s_mov_b32 s23, 0\n"
      "s_branch .Lbk_d3_23\n"
      ".Lsp_d4_24:\n"
      "s_mov_b32 s22, s23\n"
      ".Lsl_d4_24:\n"
      "buffer_load_dword v20, v2, s[8:11], s54 offen sc1\n"
      "s_waitcnt vmcnt(0)\n"
      "v_cmp_ne_u32 vcc, s21, v20\n"
      "s_cbranch_vccz .Lbk_d4_24\n"
      "s_sleep 6\n"
      "s_sub_u32 s22, s22, 1\n"
      "s_cbranch_scc0 .Lsl_d4_24\n"
      "s_mov_b32 s23, 0\n"
      "s_branch .Lbk_d4_24\n"
      ".Lsp_dy3_25:\n"
      "s_lshl_b32 s22, s23, 4\n"
      ".Lsl_dy3_25:\n"
      "ds_read_b32 v15, v17\n"
      "s_waitcnt lgkmcnt(0)\n"
      "v_cmp_gt_i32 vcc, v14, v15\n"
      "s_cbranch_vccz .Lbk_dy3_25\n"
      "s_sub_u32 s22, s22, 1\n"
      "s_cbranch_scc0 .Lsl_dy3_25\n"
      "s_mov_b32 s23, 0\n"
      "s_branch .Lbk_dy3_25\n"
      ".Lsp_d4_26:\n"
      "s_mov_b32 s22, s23\n"
      ".Lsl_d4_26:\n"
      "buffer_load_dword v20, v2, s[8:11], s54 offen sc1\n"
      "s_waitcnt vmcnt(0)\n"
      "v_cmp_ne_u32 vcc, s21, v20\n"
      "s_cbranch_vccz .Lbk_d4_26\n"
      "s_sleep 6\n"
      "s_sub_u32 s22, s22, 1\n"
      "s_cbranch_scc0 .Lsl_d4_26\n"
      "s_mov_b32 s23, 0\n"
      "s_branch .Lbk_d4_26\n"
      ".Lsp_d5_27:\n"
      "s_mov_b32 s22, s23\n"
      ".Lsl_d5_27:\n"
      "buffer_load_dword v20, v2, s[8:11], s54 offen sc1\n"
      "s_waitcnt vmcnt(0)\n"
      "v_cmp_ne_u32 vcc, s21, v20\n"
      "s_cbranch_vccz .Lbk_d5_27\n"
      "s_sleep 6\n"
      "s_sub_u32 s22, s22, 1\n"
      "s_cbranch_scc0 .Lsl_d5_27\n"
      "s_mov_b32 s23, 0\n"
      "s_branch .Lbk_d5_27\n"
      ".Lsp_dy4_28:\n"
      "s_lshl_b32 s22, s23, 4\n"
      ".Lsl_dy4_28:\n"
      "ds_read_b32 v15, v17\n"
      "s_waitcnt lgkmcnt(0)\n"
      "v_cmp_gt_i32 vcc, v14, v15\n"
      "s_cbranch_vccz .Lbk_dy4_28\n"
      "s_sub_u32 s22, s22, 1\n"
      "s_cbranch_scc0 .Lsl_dy4_28\n"
      "s_mov_b32 s23, 0\n"
      "s_branch .Lbk_dy4_28\n"
      ".Lsp_d5_29:\n"
      "s_mov_b32 s22, s23\n"
      ".Lsl_d5_29:\n"
      "buffer_load_dword v20, v2, s[8:11], s54 offen sc1\n"
      "s_waitcnt vmcnt(0)\n"
      "v_cmp_ne_u32 vcc, s21, v20\n"
      "s_cbranch_vccz .Lbk_d5_29\n"
      "s_sleep 6\n"
      "s_sub_u32 s22, s22, 1\n"
      "s_cbranch_scc0 .Lsl_d5_29\n"
      "s_mov_b32 s23, 0\n"
      "s_branch .Lbk_d5_29\n"
      ".Lsp_d0_30:\n"
      "s_mov_b32 s22, s23\n"
      ".Lsl_d0_30:\n"
      "buffer_load_dword v20, v2, s[8:11], s54 offen sc1\n"
      "s_waitcnt vmcnt(0)\n"
      "v_cmp_ne_u32 vcc, s21, v20\n"
      "s_cbranch_vccz .Lbk_d0_30\n"
      "s_sleep 6\n"
      "s_sub_u32 s22, s22, 1\n"
      "s_cbranch_scc0 .Lsl_d0_30\n"
      "s_mov_b32 s23, 0\n"
      "s_branch .Lbk_d0_30\n"
      ".Lsp_dy5_31:\n"
      "s_lshl_b32 s22, s23, 4\n"
      ".Lsl_dy5_31:\n"
      "ds_read_b32 v15, v17\n"
      "s_waitcnt lgkmcnt(0)\n"
      "v_cmp_gt_i32 vcc, v14, v15\n"
      "s_cbranch_vccz .Lbk_dy5_31\n"
      "s_sub_u32 s22, s22, 1\n"
      "s_cbranch_scc0 .Lsl_dy5_31\n"
      "s_mov_b32 s23, 0\n"
      "s_branch .Lbk_dy5_31\n"
      ".Ld_last:\n"
      :
      : "{s[8:11]}"(rsWS), "{s[12:15]}"(rsA), "{s[16:19]}"(rsB), "{s[92:95]}"(rsC), "{v0}"(v_l16), "{v1}"(v_l16b), "{v2}"(v_fa), "{v3}"(v_ga), "{v4}"(v_ra), "{v5}"(v_rf), "{v6}"(v_xp), "{v7}"(v_xs), "{v8}"(v_op), "{v9}"(v_os), "{v10}"(v_sw), "{v11}"(v_sr), "{v12}"(v_ss), "{v13}"(v_l8), "{v15}"(v_al), "{v16}"(v_yw), "{v17}"(v_yr), "{v19}"(v_hi), "{v176}"(v_at), "{v177}"(v_as), "{v178}"(v_vo0), "{v179}"(v_vo1), "{v180}"(v_vo2), "{v181}"(v_pw), "{v183}"(v_pu), "{s36}"(s_sh), "{s37}"(s_ps), "{s38}"(s_x1), "{s39}"(s_xr), "{s40}"(s_or), "{s41}"(s_p0), "{s42}"(s_m3), "{s43}"(s_m6), "{s44}"(s_so), "{s45}"(s_ph), "{s46}"(s_i0), "{s47}"(s_i5), "{s48}"(s_j0), "{s49}"(s_j5), "{s50}"(s_he), "{s51}"(s_pe[0]), "{s52}"(s_pe[1]), "{s53}"(s_pe[2]), "{s63}"(s_k0)
      : "v255", "a255", "s99", "vcc", "scc", "memory");

  } else {
    const unsigned e1 = (e - 1) & 3, gr = !fast ? slot & 3 : slot < 31 ? (e + slot - 27) & 3 : e1, part = !fast ? 8 * e + (slot >> 2) : slot < 31 ? 32 + e : 32 + e1;
    COMMON(112, 81920);
    const unsigned j = part, f = 4 * j + w;
    const u32x4 rsA = srd(w >> 1 ? Wu : Wg, 576 * 4608 * 2), rsC = srd(Wo, 576 * 576 * 2);
    const unsigned tw0 = !fast && slot < 4 ? ((e * 16 + slot * 4 + w) * 81 + lane) * 128 : OOB, v_tw[2] = {tw0, lane < 17 ? tw0 + 8192 : OOB};
    const unsigned v_l5 = w < 3 ? v_l16 + 16384 - w * 3072 : OOB;
    const unsigned v_fa = 19456 - w * 4096 + (lane >> 3) * 128 + (lane & 7) * 4, v_ra = up * 8 * HREC + l5 + 147456 - f * 1024 + f * 128;
    const unsigned one = q == g ? 0x3f80u << (16 * (p & 1)) : 0u, v_id[2] = {p < 2 ? one : 0u, p < 2 ? 0u : one};
    const unsigned v_st = 18432 + i16 * 32 + q * 8, v_pw = 147456 - f * 1024 + f * 128 + l8 * HREC + l7;
    const unsigned v_at = 65536 + (8 * g + q) * 256 + ((4 * w + p) ^ (4 * q)) * 16;
    const unsigned v_vo0 = (16 * (w & 1) + g) * 9216 + 256 * j + (i16 ^ (4 * g)) * 16;
    const unsigned v_vo1 = v_vo0 + 4 * 9216 - 1024, v_vo2 = v_vo1 + 4 * 9216 - 1024, v_vo3 = v_vo2 + 4 * 9216 - 1024;
    const unsigned s_sx = WS_X1 + gr * NREC * REC + w * 4096, s_sh = WS_H + gr * NREC * HREC + f * 1024;
    const unsigned s_m4 = w * 4096, s_m5 = 16384 + w * 1024;
    asm volatile(
      "s_buffer_load_dword s21, s[8:11], 0\n"
      "s_mov_b32 s23, 65536\n"
      "s_mov_b32 s31, 0x40000000\n"
      "s_mov_b32 s41, 0\n"
      "s_sleep 16\n"
      "buffer_load_dword v58, v54, s[92:95], s41 offen\n"
      "buffer_load_dword v59, v55, s[92:95], s41 offen\n"
      "s_waitcnt vmcnt(0)\n"
      "s_add_u32 m0, s38, 65536\n"
      "s_nop 0\n"
      "buffer_load_dwordx4 v6, s[12:15], s41 offen offset:0 lds\n"
      "buffer_load_dwordx4 v7, s[12:15], s41 offen offset:1024 lds\n"
      "buffer_load_dwordx4 v8, s[12:15], s41 offen offset:2048 lds\n"
      "buffer_load_dwordx4 v9, s[12:15], s41 offen offset:3072 lds\n"
      "s_add_u32 s41, s41, 294912\n"
      "s_add_u32 m0, s38, 81920\n"
      "s_nop 0\n"
      "buffer_load_dwordx4 v6, s[12:15], s41 offen offset:0 lds\n"
      "buffer_load_dwordx4 v7, s[12:15], s41 offen offset:1024 lds\n"
      "buffer_load_dwordx4 v8, s[12:15], s41 offen offset:2048 lds\n"
      "buffer_load_dwordx4 v9, s[12:15], s41 offen offset:3072 lds\n"
      "s_add_u32 s41, s41, 294912\n"
      "s_add_u32 m0, s38, 98304\n"
      "s_nop 0\n"
      "buffer_load_dwordx4 v6, s[12:15], s41 offen offset:0 lds\n"
      "buffer_load_dwordx4 v7, s[12:15], s41 offen offset:1024 lds\n"
      "buffer_load_dwordx4 v8, s[12:15], s41 offen offset:2048 lds\n"
      "buffer_load_dwordx4 v9, s[12:15], s41 offen offset:3072 lds\n"
      "s_add_u32 s41, s41, 294912\n"
      "s_add_u32 m0, s38, 114688\n"
      "s_nop 0\n"
      "buffer_load_dwordx4 v6, s[12:15], s41 offen offset:0 lds\n"
      "buffer_load_dwordx4 v7, s[12:15], s41 offen offset:1024 lds\n"
      "buffer_load_dwordx4 v8, s[12:15], s41 offen offset:2048 lds\n"
      "buffer_load_dwordx4 v9, s[12:15], s41 offen offset:3072 lds\n"
      "s_add_u32 s41, s41, 294912\n"
      "s_add_u32 m0, s38, 131072\n"
      "s_nop 0\n"
      "buffer_load_dwordx4 v6, s[12:15], s41 offen offset:0 lds\n"
      "buffer_load_dwordx4 v7, s[12:15], s41 offen offset:1024 lds\n"
      "buffer_load_dwordx4 v8, s[12:15], s41 offen offset:2048 lds\n"
      "buffer_load_dwordx4 v9, s[12:15], s41 offen offset:3072 lds\n"
      "s_add_u32 s41, s41, 294912\n"
      "v_mov_b32 v172, 0x3f803f80\n"
      "v_mov_b32 v173, 0x3f803f80\n"
      "v_mov_b32 v174, 0x3f803f80\n"
      "v_mov_b32 v175, 0x3f803f80\n"
      "v_add_u32 v19, 49152, v5\n"
      "s_mov_b64 exec, 0\n"
      "s_lshr_b32 s22, s38, 12\n"
      ".Lg_pl:\n"
      "s_cmp_eq_u32 s22, 0\n"
      "s_cbranch_scc1 .Lg_p0\n"
      "s_cmp_eq_u32 s22, 1\n"
      "s_cbranch_scc1 .Lg_p1\n"
      "s_branch .Lg_pe\n"
      ".Lg_pr:\n"
      "s_add_u32 s22, s22, 4\n"
      "s_branch .Lg_pl\n"
      ".Lg_pe:\n"
      "s_mov_b64 exec, -1\n"
      "s_waitcnt vmcnt(16)\n"
      "s_barrier\n"
      "ds_read_b64_tr_b16 a[0:1], v5 offset:0\n"
      "ds_read_b64_tr_b16 a[2:3], v5 offset:1024\n"
      "ds_read_b64_tr_b16 a[72:73], v5 offset:8192\n"
      "ds_read_b64_tr_b16 a[74:75], v5 offset:9216\n"
      "ds_read_b64_tr_b16 a[144:145], v5 offset:8\n"
      "ds_read_b64_tr_b16 a[146:147], v5 offset:1032\n"
      "ds_read_b64_tr_b16 a[216:217], v5 offset:8200\n"
      "ds_read_b64_tr_b16 a[218:219], v5 offset:9224\n"
      "s_add_u32 m0, s38, 147456\n"
      "s_nop 0\n"
      "buffer_load_dwordx4 v6, s[12:15], s41 offen offset:0 lds\n"
      "buffer_load_dwordx4 v7, s[12:15], s41 offen offset:1024 lds\n"
      "buffer_load_dwordx4 v8, s[12:15], s41 offen offset:2048 lds\n"
      "buffer_load_dwordx4 v9, s[12:15], s41 offen offset:3072 lds\n"
      "s_add_u32 s41, s41, 294912\n"
      "s_waitcnt vmcnt(16) lgkmcnt(0)\n"
      "s_barrier\n"
      "ds_read_b64_tr_b16 a[4:5], v5 offset:16384\n"
      "ds_read_b64_tr_b16 a[6:7], v5 offset:17408\n"
      "ds_read_b64_tr_b16 a[76:77], v5 offset:24576\n"
      "ds_read_b64_tr_b16 a[78:79], v5 offset:25600\n"
      "ds_read_b64_tr_b16 a[148:149], v5 offset:16392\n"
      "ds_read_b64_tr_b16 a[150:151], v5 offset:17416\n"
      "ds_read_b64_tr_b16 a[220:221], v5 offset:24584\n"
      "ds_read_b64_tr_b16 a[222:223], v5 offset:25608\n"
      "v_mfma_f32_16x16x32_bf16 v[76:79], v[172:175], a[0:3], 0\n"
      "v_mfma_f32_16x16x32_bf16 v[80:83], v[172:175], a[72:75], 0\n"
      "v_mfma_f32_16x16x32_bf16 v[84:87], v[172:175], a[144:147], 0\n"
      "v_mfma_f32_16x16x32_bf16 v[88:91], v[172:175], a[216:219], 0\n"
      "s_add_u32 m0, s38, 65536\n"
      "s_nop 0\n"
      "buffer_load_dwordx4 v6, s[12:15], s41 offen offset:0 lds\n"
      "buffer_load_dwordx4 v7, s[12:15], s41 offen offset:1024 lds\n"
      "buffer_load_dwordx4 v8, s[12:15], s41 offen offset:2048 lds\n"
      "buffer_load_dwordx4 v9, s[12:15], s41 offen offset:3072 lds\n"
      "s_add_u32 s41, s41, 294912\n"
      "s_waitcnt vmcnt(16) lgkmcnt(0)\n"
      "s_barrier\n"
      "ds_read_b64_tr_b16 a[8:9], v5 offset:32768\n"
      "ds_read_b64_tr_b16 a[10:11], v5 offset:33792\n"
      "ds_read_b64_tr_b16 a[80:81], v5 offset:40960\n"
      "ds_read_b64_tr_b16 a[82:83], v5 offset:41984\n"
      "ds_read_b64_tr_b16 a[152:153], v5 offset:32776\n"
      "ds_read_b64_tr_b16 a[154:155], v5 offset:33800\n"
      "ds_read_b64_tr_b16 a[224:225], v5 offset:40968\n"
      "ds_read_b64_tr_b16 a[226:227], v5 offset:41992\n"
      "v_mfma_f32_16x16x32_bf16 v[76:79], v[172:175], a[4:7], v[76:79]\n"
      "v_mfma_f32_16x16x32_bf16 v[80:83], v[172:175], a[76:79], v[80:83]\n"
      "v_mfma_f32_16x16x32_bf16 v[84:87], v[172:175], a[148:151], v[84:87]\n"
      "v_mfma_f32_16x16x32_bf16 v[88:91], v[172:175], a[220:223], v[88:91]\n"
      "s_add_u32 m0, s38, 81920\n"
      "s_nop 0\n"
      "buffer_load_dwordx4 v6, s[12:15], s41 offen offset:0 lds\n"
      "buffer_load_dwordx4 v7, s[12:15], s41 offen offset:1024 lds\n"
      "buffer_load_dwordx4 v8, s[12:15], s41 offen offset:2048 lds\n"
      "buffer_load_dwordx4 v9, s[12:15], s41 offen offset:3072 lds\n"
      "s_add_u32 s41, s41, 294912\n"
      "s_waitcnt vmcnt(16) lgkmcnt(0)\n"
      "s_barrier\n"
      "ds_read_b64_tr_b16 a[12:13], v19 offset:0\n"
      "ds_read_b64_tr_b16 a[14:15], v19 offset:1024\n"
      "ds_read_b64_tr_b16 a[84:85], v19 offset:8192\n"
      "ds_read_b64_tr_b16 a[86:87], v19 offset:9216\n"
      "ds_read_b64_tr_b16 a[156:157], v19 offset:8\n"
      "ds_read_b64_tr_b16 a[158:159], v19 offset:1032\n"
      "ds_read_b64_tr_b16 a[228:229], v19 offset:8200\n"
      "ds_read_b64_tr_b16 a[230:231], v19 offset:9224\n"
      "v_mfma_f32_16x16x32_bf16 v[76:79], v[172:175], a[8:11], v[76:79]\n"
      "v_mfma_f32_16x16x32_bf16 v[80:83], v[172:175], a[80:83], v[80:83]\n"
      "v_mfma_f32_16x16x32_bf16 v[84:87], v[172:175], a[152:155], v[84:87]\n"
      "v_mfma_f32_16x16x32_bf16 v[88:91], v[172:175], a[224:227], v[88:91]\n"
      "s_add_u32 m0, s38, 98304\n"
      "s_nop 0\n"
      "buffer_load_dwordx4 v6, s[12:15], s41 offen offset:0 lds\n"
      "buffer_load_dwordx4 v7, s[12:15], s41 offen offset:1024 lds\n"
      "buffer_load_dwordx4 v8, s[12:15], s41 offen offset:2048 lds\n"
      "buffer_load_dwordx4 v9, s[12:15], s41 offen offset:3072 lds\n"
      "s_add_u32 s41, s41, 294912\n"
      "s_waitcnt vmcnt(16) lgkmcnt(0)\n"
      "s_barrier\n"
      "ds_read_b64_tr_b16 a[16:17], v19 offset:16384\n"
      "ds_read_b64_tr_b16 a[18:19], v19 offset:17408\n"
      "ds_read_b64_tr_b16 a[88:89], v19 offset:24576\n"
      "ds_read_b64_tr_b16 a[90:91], v19 offset:25600\n"
      "ds_read_b64_tr_b16 a[160:161], v19 offset:16392\n"
      "ds_read_b64_tr_b16 a[162:163], v19 offset:17416\n"
      "ds_read_b64_tr_b16 a[232:233], v19 offset:24584\n"
      "ds_read_b64_tr_b16 a[234:235], v19 offset:25608\n"
      "v_mfma_f32_16x16x32_bf16 v[76:79], v[172:175], a[12:15], v[76:79]\n"
      "v_mfma_f32_16x16x32_bf16 v[80:83], v[172:175], a[84:87], v[80:83]\n"
      "v_mfma_f32_16x16x32_bf16 v[84:87], v[172:175], a[156:159], v[84:87]\n"
      "v_mfma_f32_16x16x32_bf16 v[88:91], v[172:175], a[228:231], v[88:91]\n"
      "s_add_u32 m0, s38, 114688\n"
      "s_nop 0\n"
      "buffer_load_dwordx4 v6, s[12:15], s41 offen offset:0 lds\n"
      "buffer_load_dwordx4 v7, s[12:15], s41 offen offset:1024 lds\n"
      "buffer_load_dwordx4 v8, s[12:15], s41 offen offset:2048 lds\n"
      "buffer_load_dwordx4 v9, s[12:15], s41 offen offset:3072 lds\n"
      "s_add_u32 s41, s41, 294912\n"
      "s_waitcnt vmcnt(16) lgkmcnt(0)\n"
      "s_barrier\n"
      "ds_read_b64_tr_b16 a[20:21], v19 offset:32768\n"
      "ds_read_b64_tr_b16 a[22:23], v19 offset:33792\n"
      "ds_read_b64_tr_b16 a[92:93], v19 offset:40960\n"
      "ds_read_b64_tr_b16 a[94:95], v19 offset:41984\n"
      "ds_read_b64_tr_b16 a[164:165], v19 offset:32776\n"
      "ds_read_b64_tr_b16 a[166:167], v19 offset:33800\n"
      "ds_read_b64_tr_b16 a[236:237], v19 offset:40968\n"
      "ds_read_b64_tr_b16 a[238:239], v19 offset:41992\n"
      "v_mfma_f32_16x16x32_bf16 v[76:79], v[172:175], a[16:19], v[76:79]\n"
      "v_mfma_f32_16x16x32_bf16 v[80:83], v[172:175], a[88:91], v[80:83]\n"
      "v_mfma_f32_16x16x32_bf16 v[84:87], v[172:175], a[160:163], v[84:87]\n"
      "v_mfma_f32_16x16x32_bf16 v[88:91], v[172:175], a[232:235], v[88:91]\n"
      "s_add_u32 m0, s38, 131072\n"
      "s_nop 0\n"
      "buffer_load_dwordx4 v6, s[12:15], s41 offen offset:0 lds\n"
      "buffer_load_dwordx4 v7, s[12:15], s41 offen offset:1024 lds\n"
      "buffer_load_dwordx4 v8, s[12:15], s41 offen offset:2048 lds\n"
      "buffer_load_dwordx4 v9, s[12:15], s41 offen offset:3072 lds\n"
      "s_add_u32 s41, s41, 294912\n"
      "s_waitcnt vmcnt(16) lgkmcnt(0)\n"
      "s_barrier\n"
      "ds_read_b64_tr_b16 a[24:25], v5 offset:0\n"
      "ds_read_b64_tr_b16 a[26:27], v5 offset:1024\n"
      "ds_read_b64_tr_b16 a[96:97], v5 offset:8192\n"
      "ds_read_b64_tr_b16 a[98:99], v5 offset:9216\n"
      "ds_read_b64_tr_b16 a[168:169], v5 offset:8\n"
      "ds_read_b64_tr_b16 a[170:171], v5 offset:1032\n"
      "ds_read_b64_tr_b16 a[240:241], v5 offset:8200\n"
      "ds_read_b64_tr_b16 a[242:243], v5 offset:9224\n"
      "v_mfma_f32_16x16x32_bf16 v[76:79], v[172:175], a[20:23], v[76:79]\n"
      "v_mfma_f32_16x16x32_bf16 v[80:83], v[172:175], a[92:95], v[80:83]\n"
      "v_mfma_f32_16x16x32_bf16 v[84:87], v[172:175], a[164:167], v[84:87]\n"
      "v_mfma_f32_16x16x32_bf16 v[88:91], v[172:175], a[236:239], v[88:91]\n"
      "s_add_u32 m0, s38, 147456\n"
      "s_nop 0\n"
      "buffer_load_dwordx4 v6, s[12:15], s41 offen offset:0 lds\n"
      "buffer_load_dwordx4 v7, s[12:15], s41 offen offset:1024 lds\n"
      "buffer_load_dwordx4 v8, s[12:15], s41 offen offset:2048 lds\n"
      "buffer_load_dwordx4 v9, s[12:15], s41 offen offset:3072 lds\n"
      "s_add_u32 s41, s41, 294912\n"
      "s_waitcnt vmcnt(16) lgkmcnt(0)\n"
      "s_barrier\n"
      "ds_read_b64_tr_b16 a[28:29], v5 offset:16384\n"
      "ds_read_b64_tr_b16 a[30:31], v5 offset:17408\n"
      "ds_read_b64_tr_b16 a[100:101], v5 offset:24576\n"
      "ds_read_b64_tr_b16 a[102:103], v5 offset:25600\n"
      "ds_read_b64_tr_b16 a[172:173], v5 offset:16392\n"
      "ds_read_b64_tr_b16 a[174:175], v5 offset:17416\n"
      "ds_read_b64_tr_b16 a[244:245], v5 offset:24584\n"
      "ds_read_b64_tr_b16 a[246:247], v5 offset:25608\n"
      "v_mfma_f32_16x16x32_bf16 v[76:79], v[172:175], a[24:27], v[76:79]\n"
      "v_mfma_f32_16x16x32_bf16 v[80:83], v[172:175], a[96:99], v[80:83]\n"
      "v_mfma_f32_16x16x32_bf16 v[84:87], v[172:175], a[168:171], v[84:87]\n"
      "v_mfma_f32_16x16x32_bf16 v[88:91], v[172:175], a[240:243], v[88:91]\n"
      "s_add_u32 m0, s38, 65536\n"
      "s_nop 0\n"
      "buffer_load_dwordx4 v6, s[12:15], s41 offen offset:0 lds\n"
      "buffer_load_dwordx4 v7, s[12:15], s41 offen offset:1024 lds\n"
      "buffer_load_dwordx4 v8, s[12:15], s41 offen offset:2048 lds\n"
      "buffer_load_dwordx4 v9, s[12:15], s41 offen offset:3072 lds\n"
      "s_add_u32 s41, s41, 294912\n"
      "s_waitcnt vmcnt(16) lgkmcnt(0)\n"
      "s_barrier\n"
      "ds_read_b64_tr_b16 a[32:33], v5 offset:32768\n"
      "ds_read_b64_tr_b16 a[34:35], v5 offset:33792\n"
      "ds_read_b64_tr_b16 a[104:105], v5 offset:40960\n"
      "ds_read_b64_tr_b16 a[106:107], v5 offset:41984\n"
      "ds_read_b64_tr_b16 a[176:177], v5 offset:32776\n"
      "ds_read_b64_tr_b16 a[178:179], v5 offset:33800\n"
      "ds_read_b64_tr_b16 a[248:249], v5 offset:40968\n"
      "ds_read_b64_tr_b16 a[250:251], v5 offset:41992\n"
      "v_mfma_f32_16x16x32_bf16 v[76:79], v[172:175], a[28:31], v[76:79]\n"
      "v_mfma_f32_16x16x32_bf16 v[80:83], v[172:175], a[100:103], v[80:83]\n"
      "v_mfma_f32_16x16x32_bf16 v[84:87], v[172:175], a[172:175], v[84:87]\n"
      "v_mfma_f32_16x16x32_bf16 v[88:91], v[172:175], a[244:247], v[88:91]\n"
      "s_add_u32 m0, s38, 81920\n"
      "s_nop 0\n"
      "buffer_load_dwordx4 v6, s[12:15], s41 offen offset:0 lds\n"
      "buffer_load_dwordx4 v7, s[12:15], s41 offen offset:1024 lds\n"
      "buffer_load_dwordx4 v8, s[12:15], s41 offen offset:2048 lds\n"
      "buffer_load_dwordx4 v9, s[12:15], s41 offen offset:3072 lds\n"
      "s_add_u32 s41, s41, 294912\n"
      "s_waitcnt vmcnt(16) lgkmcnt(0)\n"
      "s_barrier\n"
      "ds_read_b64_tr_b16 a[36:37], v19 offset:0\n"
      "ds_read_b64_tr_b16 a[38:39], v19 offset:1024\n"
      "ds_read_b64_tr_b16 a[108:109], v19 offset:8192\n"
      "ds_read_b64_tr_b16 a[110:111], v19 offset:9216\n"
      "ds_read_b64_tr_b16 a[180:181], v19 offset:8\n"
      "ds_read_b64_tr_b16 a[182:183], v19 offset:1032\n"
      "ds_read_b64_tr_b16 a[252:253], v19 offset:8200\n"
      "ds_read_b64_tr_b16 a[254:255], v19 offset:9224\n"
      "v_mfma_f32_16x16x32_bf16 v[76:79], v[172:175], a[32:35], v[76:79]\n"
      "v_mfma_f32_16x16x32_bf16 v[80:83], v[172:175], a[104:107], v[80:83]\n"
      "v_mfma_f32_16x16x32_bf16 v[84:87], v[172:175], a[176:179], v[84:87]\n"
      "v_mfma_f32_16x16x32_bf16 v[88:91], v[172:175], a[248:251], v[88:91]\n"
      "s_add_u32 m0, s38, 98304\n"
      "s_nop 0\n"
      "buffer_load_dwordx4 v6, s[12:15], s41 offen offset:0 lds\n"
      "buffer_load_dwordx4 v7, s[12:15], s41 offen offset:1024 lds\n"
      "buffer_load_dwordx4 v8, s[12:15], s41 offen offset:2048 lds\n"
      "buffer_load_dwordx4 v9, s[12:15], s41 offen offset:3072 lds\n"
      "s_add_u32 s41, s41, 294912\n"
      "s_waitcnt vmcnt(16) lgkmcnt(0)\n"
      "s_barrier\n"
      "ds_read_b64_tr_b16 a[40:41], v19 offset:16384\n"
      "ds_read_b64_tr_b16 a[42:43], v19 offset:17408\n"
      "ds_read_b64_tr_b16 a[112:113], v19 offset:24576\n"
      "ds_read_b64_tr_b16 a[114:115], v19 offset:25600\n"
      "ds_read_b64_tr_b16 a[184:185], v19 offset:16392\n"
      "ds_read_b64_tr_b16 a[186:187], v19 offset:17416\n"
      "ds_read_b64_tr_b16 v[20:21], v19 offset:24584\n"
      "ds_read_b64_tr_b16 v[22:23], v19 offset:25608\n"
      "v_mfma_f32_16x16x32_bf16 v[76:79], v[172:175], a[36:39], v[76:79]\n"
      "v_mfma_f32_16x16x32_bf16 v[80:83], v[172:175], a[108:111], v[80:83]\n"
      "v_mfma_f32_16x16x32_bf16 v[84:87], v[172:175], a[180:183], v[84:87]\n"
      "v_mfma_f32_16x16x32_bf16 v[88:91], v[172:175], a[252:255], v[88:91]\n"
      "s_add_u32 m0, s38, 114688\n"
      "s_nop 0\n"
      "buffer_load_dwordx4 v6, s[12:15], s41 offen offset:0 lds\n"
      "buffer_load_dwordx4 v7, s[12:15], s41 offen offset:1024 lds\n"
      "buffer_load_dwordx4 v8, s[12:15], s41 offen offset:2048 lds\n"
      "buffer_load_dwordx4 v9, s[12:15], s41 offen offset:3072 lds\n"
      "s_add_u32 s41, s41, 294912\n"
      "s_waitcnt vmcnt(16) lgkmcnt(0)\n"
      "s_barrier\n"
      "ds_read_b64_tr_b16 a[44:45], v19 offset:32768\n"
      "ds_read_b64_tr_b16 a[46:47], v19 offset:33792\n"
      "ds_read_b64_tr_b16 a[116:117], v19 offset:40960\n"
      "ds_read_b64_tr_b16 a[118:119], v19 offset:41984\n"
      "ds_read_b64_tr_b16 a[188:189], v19 offset:32776\n"
      "ds_read_b64_tr_b16 a[190:191], v19 offset:33800\n"
      "ds_read_b64_tr_b16 v[24:25], v19 offset:40968\n"
      "ds_read_b64_tr_b16 v[26:27], v19 offset:41992\n"
      "v_mfma_f32_16x16x32_bf16 v[92:95], v[172:175], a[40:43], v[76:79]\n"
      "v_mfma_f32_16x16x32_bf16 v[96:99], v[172:175], a[112:115], v[80:83]\n"
      "v_mfma_f32_16x16x32_bf16 v[100:103], v[172:175], a[184:187], v[84:87]\n"
      "v_mfma_f32_16x16x32_bf16 v[104:107], v[172:175], v[20:23], v[88:91]\n"
      "s_add_u32 m0, s38, 131072\n"
      "s_nop 0\n"
      "buffer_load_dwordx4 v6, s[12:15], s41 offen offset:0 lds\n"
      "buffer_load_dwordx4 v7, s[12:15], s41 offen offset:1024 lds\n"
      "buffer_load_dwordx4 v8, s[12:15], s41 offen offset:2048 lds\n"
      "buffer_load_dwordx4 v9, s[12:15], s41 offen offset:3072 lds\n"
      "s_add_u32 s41, s41, 294912\n"
      "s_waitcnt vmcnt(16) lgkmcnt(0)\n"
      "s_barrier\n"
      "ds_read_b64_tr_b16 a[48:49], v5 offset:0\n"
      "ds_read_b64_tr_b16 a[50:51], v5 offset:1024\n"
      "ds_read_b64_tr_b16 a[120:121], v5 offset:8192\n"
      "ds_read_b64_tr_b16 a[122:123], v5 offset:9216\n"
      "ds_read_b64_tr_b16 a[192:193], v5 offset:8\n"
      "ds_read_b64_tr_b16 a[194:195], v5 offset:1032\n"
      "ds_read_b64_tr_b16 v[28:29], v5 offset:8200\n"
      "ds_read_b64_tr_b16 v[30:31], v5 offset:9224\n"
      "v_mfma_f32_16x16x32_bf16 v[92:95], v[172:175], a[44:47], v[92:95]\n"
      "v_mfma_f32_16x16x32_bf16 v[96:99], v[172:175], a[116:119], v[96:99]\n"
      "v_mfma_f32_16x16x32_bf16 v[100:103], v[172:175], a[188:191], v[100:103]\n"
      "v_mfma_f32_16x16x32_bf16 v[104:107], v[172:175], v[24:27], v[104:107]\n"
      "s_add_u32 m0, s38, 147456\n"
      "s_nop 0\n"
      "buffer_load_dwordx4 v6, s[12:15], s41 offen offset:0 lds\n"
      "buffer_load_dwordx4 v7, s[12:15], s41 offen offset:1024 lds\n"
      "buffer_load_dwordx4 v8, s[12:15], s41 offen offset:2048 lds\n"
      "buffer_load_dwordx4 v9, s[12:15], s41 offen offset:3072 lds\n"
      "s_add_u32 s41, s41, 294912\n"
      "s_waitcnt vmcnt(16) lgkmcnt(0)\n"
      "s_barrier\n"
      "ds_read_b64_tr_b16 a[52:53], v5 offset:16384\n"
      "ds_read_b64_tr_b16 a[54:55], v5 offset:17408\n"
      "ds_read_b64_tr_b16 a[124:125], v5 offset:24576\n"
      "ds_read_b64_tr_b16 a[126:127], v5 offset:25600\n"
      "ds_read_b64_tr_b16 a[196:197], v5 offset:16392\n"
      "ds_read_b64_tr_b16 a[198:199], v5 offset:17416\n"
      "ds_read_b64_tr_b16 v[32:33], v5 offset:24584\n"
      "ds_read_b64_tr_b16 v[34:35], v5 offset:25608\n"
      "v_mfma_f32_16x16x32_bf16 v[92:95], v[172:175], a[48:51], v[92:95]\n"
      "v_mfma_f32_16x16x32_bf16 v[96:99], v[172:175], a[120:123], v[96:99]\n"
      "v_mfma_f32_16x16x32_bf16 v[100:103], v[172:175], a[192:195], v[100:103]\n"
      "v_mfma_f32_16x16x32_bf16 v[104:107], v[172:175], v[28:31], v[104:107]\n"
      "s_waitcnt vmcnt(12) lgkmcnt(0)\n"
      "s_barrier\n"
      "ds_read_b64_tr_b16 a[56:57], v5 offset:32768\n"
      "ds_read_b64_tr_b16 a[58:59], v5 offset:33792\n"
      "ds_read_b64_tr_b16 a[128:129], v5 offset:40960\n"
      "ds_read_b64_tr_b16 a[130:131], v5 offset:41984\n"
      "ds_read_b64_tr_b16 a[200:201], v5 offset:32776\n"
      "ds_read_b64_tr_b16 a[202:203], v5 offset:33800\n"
      "ds_read_b64_tr_b16 v[36:37], v5 offset:40968\n"
      "ds_read_b64_tr_b16 v[38:39], v5 offset:41992\n"
      "v_mfma_f32_16x16x32_bf16 v[92:95], v[172:175], a[52:55], v[92:95]\n"
      "v_mfma_f32_16x16x32_bf16 v[96:99], v[172:175], a[124:127], v[96:99]\n"
      "v_mfma_f32_16x16x32_bf16 v[100:103], v[172:175], a[196:199], v[100:103]\n"
      "v_mfma_f32_16x16x32_bf16 v[104:107], v[172:175], v[32:35], v[104:107]\n"
      "s_waitcnt vmcnt(8) lgkmcnt(0)\n"
      "s_barrier\n"
      "ds_read_b64_tr_b16 a[60:61], v19 offset:0\n"
      "ds_read_b64_tr_b16 a[62:63], v19 offset:1024\n"
      "ds_read_b64_tr_b16 a[132:133], v19 offset:8192\n"
      "ds_read_b64_tr_b16 a[134:135], v19 offset:9216\n"
      "ds_read_b64_tr_b16 a[204:205], v19 offset:8\n"
      "ds_read_b64_tr_b16 a[206:207], v19 offset:1032\n"
      "ds_read_b64_tr_b16 v[40:41], v19 offset:8200\n"
      "ds_read_b64_tr_b16 v[42:43], v19 offset:9224\n"
      "v_mfma_f32_16x16x32_bf16 v[92:95], v[172:175], a[56:59], v[92:95]\n"
      "v_mfma_f32_16x16x32_bf16 v[96:99], v[172:175], a[128:131], v[96:99]\n"
      "v_mfma_f32_16x16x32_bf16 v[100:103], v[172:175], a[200:203], v[100:103]\n"
      "v_mfma_f32_16x16x32_bf16 v[104:107], v[172:175], v[36:39], v[104:107]\n"
      "s_waitcnt vmcnt(4) lgkmcnt(0)\n"
      "s_barrier\n"
      "ds_read_b64_tr_b16 a[64:65], v19 offset:16384\n"
      "ds_read_b64_tr_b16 a[66:67], v19 offset:17408\n"
      "ds_read_b64_tr_b16 a[136:137], v19 offset:24576\n"
      "ds_read_b64_tr_b16 a[138:139], v19 offset:25600\n"
      "ds_read_b64_tr_b16 a[208:209], v19 offset:16392\n"
      "ds_read_b64_tr_b16 a[210:211], v19 offset:17416\n"
      "ds_read_b64_tr_b16 v[44:45], v19 offset:24584\n"
      "ds_read_b64_tr_b16 v[46:47], v19 offset:25608\n"
      "v_mfma_f32_16x16x32_bf16 v[108:111], v[172:175], a[60:63], v[92:95]\n"
      "v_mfma_f32_16x16x32_bf16 v[112:115], v[172:175], a[132:135], v[96:99]\n"
      "v_mfma_f32_16x16x32_bf16 v[116:119], v[172:175], a[204:207], v[100:103]\n"
      "v_mfma_f32_16x16x32_bf16 v[120:123], v[172:175], v[40:43], v[104:107]\n"
      "s_waitcnt vmcnt(0) lgkmcnt(0)\n"
      "s_barrier\n"
      "ds_read_b64_tr_b16 a[68:69], v19 offset:32768\n"
      "ds_read_b64_tr_b16 a[70:71], v19 offset:33792\n"
      "ds_read_b64_tr_b16 a[140:141], v19 offset:40960\n"
      "ds_read_b64_tr_b16 a[142:143], v19 offset:41984\n"
      "ds_read_b64_tr_b16 a[212:213], v19 offset:32776\n"
      "ds_read_b64_tr_b16 a[214:215], v19 offset:33800\n"
      "ds_read_b64_tr_b16 v[48:49], v19 offset:40968\n"
      "ds_read_b64_tr_b16 v[50:51], v19 offset:41992\n"
      "v_mfma_f32_16x16x32_bf16 v[108:111], v[172:175], a[64:67], v[108:111]\n"
      "v_mfma_f32_16x16x32_bf16 v[112:115], v[172:175], a[136:139], v[112:115]\n"
      "v_mfma_f32_16x16x32_bf16 v[116:119], v[172:175], a[208:211], v[116:119]\n"
      "v_mfma_f32_16x16x32_bf16 v[120:123], v[172:175], v[44:47], v[120:123]\n"
      "s_waitcnt lgkmcnt(0)\n"
      "v_mfma_f32_16x16x32_bf16 v[140:143], v[172:175], a[68:71], v[108:111]\n"
      "v_mfma_f32_16x16x32_bf16 v[144:147], v[172:175], a[140:143], v[112:115]\n"
      "v_mfma_f32_16x16x32_bf16 v[148:151], v[172:175], a[212:215], v[116:119]\n"
      "v_mfma_f32_16x16x32_bf16 v[152:155], v[172:175], v[48:51], v[120:123]\n"
      "s_getpc_b64 vcc\n"
      ".Lg_ct:\n"
      "s_add_u32 vcc_lo, vcc_lo, .Lg_p0-.Lg_ct\n"
      "s_addc_u32 vcc_hi, vcc_hi, 0\n"
      "s_lshl_b32 s30, s38, 1\n"
      "v_lshl_add_u32 v60, v0, 3, s30\n"
      "v_min_u32 v60, .Lg_last-.Lg_p0-4, v60\n"
      "global_load_dword v60, v60, vcc\n"
      "s_mul_i32 s21, s21, 1664525\n"
      "s_add_u32 s21, s21, 1013904223\n"
      "v_mov_b32 v15, s21\n"
      "buffer_store_dword v15, v12, s[8:11], 0 offen sc1\n"
      "v_not_b32 v52, v15\n"
      "v_not_b32 v53, v15\n"
      "v_not_b32 v54, v15\n"
      "v_not_b32 v55, v15\n"
      "s_add_u32 s30, s37, 0\n"
      "buffer_store_dwordx4 v[52:55], v10, s[8:11], s30 offen sc1\n"
      "v_xor_b32 v15, v15, v16\n"
      "v_mov_b32 v66, 0\n"
      "v_mov_b32 v67, 0\n"
      "v_mov_b32 v68, 0\n"
      "v_mov_b32 v69, 0\n"
      "v_mov_b32 v70, v64\n"
      "v_mov_b32 v71, v65\n"
      "v_sub_f32 v156, v140, v76\n"
      "v_sub_f32 v157, v144, v80\n"
      "v_sub_f32 v158, v148, v84\n"
      "v_sub_f32 v159, v152, v88\n"
      "v_sub_f32 v160, v140, v92\n"
      "v_sub_f32 v161, v144, v96\n"
      "v_sub_f32 v162, v148, v100\n"
      "v_sub_f32 v163, v152, v104\n"
      "v_sub_f32 v164, v140, v108\n"
      "v_sub_f32 v165, v144, v112\n"
      "v_sub_f32 v166, v148, v116\n"
      "v_sub_f32 v167, v152, v120\n"
      "v_mul_f32 v140, 0xbae38e39, v140\n"
      "v_mul_f32 v141, 0xbae38e39, v141\n"
      "v_mul_f32 v142, 0xbae38e39, v142\n"
      "v_mul_f32 v143, 0xbae38e39, v143\n"
      "v_mul_f32 v144, 0xbae38e39, v144\n"
      "v_mul_f32 v145, 0xbae38e39, v145\n"
      "v_mul_f32 v146, 0xbae38e39, v146\n"
      "v_mul_f32 v147, 0xbae38e39, v147\n"
      "v_mul_f32 v148, 0xbae38e39, v148\n"
      "v_mul_f32 v149, 0xbae38e39, v149\n"
      "v_mul_f32 v150, 0xbae38e39, v150\n"
      "v_mul_f32 v151, 0xbae38e39, v151\n"
      "v_mul_f32 v152, 0xbae38e39, v152\n"
      "v_mul_f32 v153, 0xbae38e39, v153\n"
      "v_mul_f32 v154, 0xbae38e39, v154\n"
      "v_mul_f32 v155, 0xbae38e39, v155\n"
      "v_mfma_f32_16x16x32_bf16 v[72:75], v[64:67], a[0:3], v[140:143]\n"
      "v_mfma_f32_16x16x32_bf16 v[76:79], v[68:71], a[0:3], v[140:143]\n"
      "v_mfma_f32_16x16x32_bf16 v[80:83], v[64:67], a[72:75], v[144:147]\n"
      "v_mfma_f32_16x16x32_bf16 v[84:87], v[68:71], a[72:75], v[144:147]\n"
      "v_mfma_f32_16x16x32_bf16 v[88:91], v[64:67], a[144:147], v[148:151]\n"
      "s_nop 4\n"
      "v_cvt_pk_bf16_f32 v72, v72, v73\n"
      "v_cvt_pk_bf16_f32 v73, v74, v75\n"
      "v_cvt_pk_bf16_f32 v74, v76, v77\n"
      "v_mfma_f32_16x16x32_bf16 v[92:95], v[68:71], a[144:147], v[148:151]\n"
      "v_cvt_pk_bf16_f32 v75, v78, v79\n"
      "v_accvgpr_write_b32 a0, v72\n"
      "v_accvgpr_write_b32 a1, v73\n"
      "v_accvgpr_write_b32 a2, v74\n"
      "v_accvgpr_write_b32 a3, v75\n"
      "v_mfma_f32_16x16x32_bf16 v[96:99], v[64:67], a[216:219], v[152:155]\n"
      "v_cvt_pk_bf16_f32 v80, v80, v81\n"
      "v_cvt_pk_bf16_f32 v81, v82, v83\n"
      "v_cvt_pk_bf16_f32 v82, v84, v85\n"
      "v_mfma_f32_16x16x32_bf16 v[100:103], v[68:71], a[216:219], v[152:155]\n"
      "v_cvt_pk_bf16_f32 v83, v86, v87\n"
      "v_accvgpr_write_b32 a72, v80\n"
      "v_accvgpr_write_b32 a73, v81\n"
      "v_mfma_f32_16x16x32_bf16 v[124:127], v[172:175], a[0:3], 0\n"
      "v_accvgpr_write_b32 a74, v82\n"
      "v_accvgpr_write_b32 a75, v83\n"
      "v_mfma_f32_16x16x32_bf16 v[72:75], v[64:67], a[4:7], v[140:143]\n"
      "v_cvt_pk_bf16_f32 v88, v88, v89\n"
      "v_cvt_pk_bf16_f32 v89, v90, v91\n"
      "v_cvt_pk_bf16_f32 v90, v92, v93\n"
      "v_mfma_f32_16x16x32_bf16 v[76:79], v[68:71], a[4:7], v[140:143]\n"
      "v_cvt_pk_bf16_f32 v91, v94, v95\n"
      "v_accvgpr_write_b32 a144, v88\n"
      "v_accvgpr_write_b32 a145, v89\n"
      "v_mfma_f32_16x16x32_bf16 v[128:131], v[172:175], a[72:75], 0\n"
      "v_accvgpr_write_b32 a146, v90\n"
      "v_accvgpr_write_b32 a147, v91\n"
      "v_mfma_f32_16x16x32_bf16 v[80:83], v[64:67], a[76:79], v[144:147]\n"
      "v_cvt_pk_bf16_f32 v96, v96, v97\n"
      "v_cvt_pk_bf16_f32 v97, v98, v99\n"
      "v_cvt_pk_bf16_f32 v98, v100, v101\n"
      "v_mfma_f32_16x16x32_bf16 v[84:87], v[68:71], a[76:79], v[144:147]\n"
      "v_cvt_pk_bf16_f32 v99, v102, v103\n"
      "v_accvgpr_write_b32 a216, v96\n"
      "v_accvgpr_write_b32 a217, v97\n"
      "v_mfma_f32_16x16x32_bf16 v[132:135], v[172:175], a[144:147], 0\n"
      "v_accvgpr_write_b32 a218, v98\n"
      "v_accvgpr_write_b32 a219, v99\n"
      "v_mfma_f32_16x16x32_bf16 v[88:91], v[64:67], a[148:151], v[148:151]\n"
      "v_cvt_pk_bf16_f32 v72, v72, v73\n"
      "v_cvt_pk_bf16_f32 v73, v74, v75\n"
      "v_cvt_pk_bf16_f32 v74, v76, v77\n"
      "v_mfma_f32_16x16x32_bf16 v[92:95], v[68:71], a[148:151], v[148:151]\n"
      "v_cvt_pk_bf16_f32 v75, v78, v79\n"
      "v_accvgpr_write_b32 a4, v72\n"
      "v_accvgpr_write_b32 a5, v73\n"
      "v_mfma_f32_16x16x32_bf16 v[136:139], v[172:175], a[216:219], 0\n"
      "v_accvgpr_write_b32 a6, v74\n"
      "v_accvgpr_write_b32 a7, v75\n"
      "v_mfma_f32_16x16x32_bf16 v[96:99], v[64:67], a[220:223], v[152:155]\n"
      "v_cvt_pk_bf16_f32 v80, v80, v81\n"
      "v_cvt_pk_bf16_f32 v81, v82, v83\n"
      "v_cvt_pk_bf16_f32 v82, v84, v85\n"
      "v_mfma_f32_16x16x32_bf16 v[100:103], v[68:71], a[220:223], v[152:155]\n"
      "v_cvt_pk_bf16_f32 v83, v86, v87\n"
      "v_accvgpr_write_b32 a76, v80\n"
      "v_accvgpr_write_b32 a77, v81\n"
      "v_mfma_f32_16x16x32_bf16 v[124:127], v[172:175], a[4:7], v[124:127]\n"
      "v_accvgpr_write_b32 a78, v82\n"
      "v_accvgpr_write_b32 a79, v83\n"
      "v_mfma_f32_16x16x32_bf16 v[72:75], v[64:67], a[8:11], v[140:143]\n"
      "v_cvt_pk_bf16_f32 v88, v88, v89\n"
      "v_cvt_pk_bf16_f32 v89, v90, v91\n"
      "v_cvt_pk_bf16_f32 v90, v92, v93\n"
      "v_mfma_f32_16x16x32_bf16 v[76:79], v[68:71], a[8:11], v[140:143]\n"
      "v_cvt_pk_bf16_f32 v91, v94, v95\n"
      "v_accvgpr_write_b32 a148, v88\n"
      "v_accvgpr_write_b32 a149, v89\n"
      "v_mfma_f32_16x16x32_bf16 v[128:131], v[172:175], a[76:79], v[128:131]\n"
      "v_accvgpr_write_b32 a150, v90\n"
      "v_accvgpr_write_b32 a151, v91\n"
      "v_mfma_f32_16x16x32_bf16 v[80:83], v[64:67], a[80:83], v[144:147]\n"
      "v_cvt_pk_bf16_f32 v96, v96, v97\n"
      "v_cvt_pk_bf16_f32 v97, v98, v99\n"
      "v_cvt_pk_bf16_f32 v98, v100, v101\n"
      "v_mfma_f32_16x16x32_bf16 v[84:87], v[68:71], a[80:83], v[144:147]\n"
      "v_cvt_pk_bf16_f32 v99, v102, v103\n"
      "v_accvgpr_write_b32 a220, v96\n"
      "v_accvgpr_write_b32 a221, v97\n"
      "v_mfma_f32_16x16x32_bf16 v[132:135], v[172:175], a[148:151], v[132:135]\n"
      "v_accvgpr_write_b32 a222, v98\n"
      "v_accvgpr_write_b32 a223, v99\n"
      "v_mfma_f32_16x16x32_bf16 v[88:91], v[64:67], a[152:155], v[148:151]\n"
      "v_cvt_pk_bf16_f32 v72, v72, v73\n"
      "v_cvt_pk_bf16_f32 v73, v74, v75\n"
      "v_cvt_pk_bf16_f32 v74, v76, v77\n"
      "v_mfma_f32_16x16x32_bf16 v[92:95], v[68:71], a[152:155], v[148:151]\n"
      "v_cvt_pk_bf16_f32 v75, v78, v79\n"
      "v_accvgpr_write_b32 a8, v72\n"
      "v_accvgpr_write_b32 a9, v73\n"
      "v_mfma_f32_16x16x32_bf16 v[136:139], v[172:175], a[220:223], v[136:139]\n"
      "v_accvgpr_write_b32 a10, v74\n"
      "v_accvgpr_write_b32 a11, v75\n"
      "v_mfma_f32_16x16x32_bf16 v[96:99], v[64:67], a[224:227], v[152:155]\n"
      "v_cvt_pk_bf16_f32 v80, v80, v81\n"
      "v_cvt_pk_bf16_f32 v81, v82, v83\n"
      "v_cvt_pk_bf16_f32 v82, v84, v85\n"
      "v_mfma_f32_16x16x32_bf16 v[100:103], v[68:71], a[224:227], v[152:155]\n"
      "v_cvt_pk_bf16_f32 v83, v86, v87\n"
      "v_accvgpr_write_b32 a80, v80\n"
      "v_accvgpr_write_b32 a81, v81\n"
      "v_mfma_f32_16x16x32_bf16 v[124:127], v[172:175], a[8:11], v[124:127]\n"
      "v_accvgpr_write_b32 a82, v82\n"
      "v_accvgpr_write_b32 a83, v83\n"
      "v_mfma_f32_16x16x32_bf16 v[72:75], v[64:67], a[12:15], v[140:143]\n"
      "v_cvt_pk_bf16_f32 v88, v88, v89\n"
      "v_cvt_pk_bf16_f32 v89, v90, v91\n"
      "v_cvt_pk_bf16_f32 v90, v92, v93\n"
      "v_mfma_f32_16x16x32_bf16 v[76:79], v[68:71], a[12:15], v[140:143]\n"
      "v_cvt_pk_bf16_f32 v91, v94, v95\n"
      "v_accvgpr_write_b32 a152, v88\n"
      "v_accvgpr_write_b32 a153, v89\n"
      "v_mfma_f32_16x16x32_bf16 v[128:131], v[172:175], a[80:83], v[128:131]\n"
      "v_accvgpr_write_b32 a154, v90\n"
      "v_accvgpr_write_b32 a155, v91\n"
      "v_mfma_f32_16x16x32_bf16 v[80:83], v[64:67], a[84:87], v[144:147]\n"
      "v_cvt_pk_bf16_f32 v96, v96, v97\n"
      "v_cvt_pk_bf16_f32 v97, v98, v99\n"
      "v_cvt_pk_bf16_f32 v98, v100, v101\n"
      "v_mfma_f32_16x16x32_bf16 v[84:87], v[68:71], a[84:87], v[144:147]\n"
      "v_cvt_pk_bf16_f32 v99, v102, v103\n"
      "v_accvgpr_write_b32 a224, v96\n"
      "v_accvgpr_write_b32 a225, v97\n"
      "v_mfma_f32_16x16x32_bf16 v[132:135], v[172:175], a[152:155], v[132:135]\n"
      "v_accvgpr_write_b32 a226, v98\n"
      "v_accvgpr_write_b32 a227, v99\n"
      "v_mfma_f32_16x16x32_bf16 v[88:91], v[64:67], a[156:159], v[148:151]\n"
      "v_cvt_pk_bf16_f32 v72, v72, v73\n"
      "v_cvt_pk_bf16_f32 v73, v74, v75\n"
      "v_cvt_pk_bf16_f32 v74, v76, v77\n"
      "v_mfma_f32_16x16x32_bf16 v[92:95], v[68:71], a[156:159], v[148:151]\n"
      "v_cvt_pk_bf16_f32 v75, v78, v79\n"
      "v_accvgpr_write_b32 a12, v72\n"
      "v_accvgpr_write_b32 a13, v73\n"
      "v_mfma_f32_16x16x32_bf16 v[136:139], v[172:175], a[224:227], v[136:139]\n"
      "v_accvgpr_write_b32 a14, v74\n"
      "v_accvgpr_write_b32 a15, v75\n"
      "v_mfma_f32_16x16x32_bf16 v[96:99], v[64:67], a[228:231], v[152:155]\n"
      "v_cvt_pk_bf16_f32 v80, v80, v81\n"
      "v_cvt_pk_bf16_f32 v81, v82, v83\n"
      "v_cvt_pk_bf16_f32 v82, v84, v85\n"
      "v_mfma_f32_16x16x32_bf16 v[100:103], v[68:71], a[228:231], v[152:155]\n"
      "v_cvt_pk_bf16_f32 v83, v86, v87\n"
      "v_accvgpr_write_b32 a84, v80\n"
      "v_accvgpr_write_b32 a85, v81\n"
      "v_mfma_f32_16x16x32_bf16 v[124:127], v[172:175], a[12:15], v[124:127]\n"
      "v_accvgpr_write_b32 a86, v82\n"
      "v_accvgpr_write_b32 a87, v83\n"
      "v_mfma_f32_16x16x32_bf16 v[72:75], v[64:67], a[16:19], v[140:143]\n"
      "v_cvt_pk_bf16_f32 v88, v88, v89\n"
      "v_cvt_pk_bf16_f32 v89, v90, v91\n"
      "v_cvt_pk_bf16_f32 v90, v92, v93\n"
      "v_mfma_f32_16x16x32_bf16 v[76:79], v[68:71], a[16:19], v[140:143]\n"
      "v_cvt_pk_bf16_f32 v91, v94, v95\n"
      "v_accvgpr_write_b32 a156, v88\n"
      "v_accvgpr_write_b32 a157, v89\n"
      "v_mfma_f32_16x16x32_bf16 v[128:131], v[172:175], a[84:87], v[128:131]\n"
      "v_accvgpr_write_b32 a158, v90\n"
      "v_accvgpr_write_b32 a159, v91\n"
      "v_mfma_f32_16x16x32_bf16 v[80:83], v[64:67], a[88:91], v[144:147]\n"
      "v_cvt_pk_bf16_f32 v96, v96, v97\n"
      "v_cvt_pk_bf16_f32 v97, v98, v99\n"
      "v_cvt_pk_bf16_f32 v98, v100, v101\n"
      "v_mfma_f32_16x16x32_bf16 v[84:87], v[68:71], a[88:91], v[144:147]\n"
      "v_cvt_pk_bf16_f32 v99, v102, v103\n"
      "v_accvgpr_write_b32 a228, v96\n"
      "v_accvgpr_write_b32 a229, v97\n"
      "v_mfma_f32_16x16x32_bf16 v[132:135], v[172:175], a[156:159], v[132:135]\n"
      "v_accvgpr_write_b32 a230, v98\n"
      "v_accvgpr_write_b32 a231, v99\n"
      "v_mfma_f32_16x16x32_bf16 v[88:91], v[64:67], a[160:163], v[148:151]\n"
      "v_cvt_pk_bf16_f32 v72, v72, v73\n"
      "v_cvt_pk_bf16_f32 v73, v74, v75\n"
      "v_cvt_pk_bf16_f32 v74, v76, v77\n"
      "v_mfma_f32_16x16x32_bf16 v[92:95], v[68:71], a[160:163], v[148:151]\n"
      "v_cvt_pk_bf16_f32 v75, v78, v79\n"
      "v_accvgpr_write_b32 a16, v72\n"
      "v_accvgpr_write_b32 a17, v73\n"
      "v_mfma_f32_16x16x32_bf16 v[136:139], v[172:175], a[228:231], v[136:139]\n"
      "v_accvgpr_write_b32 a18, v74\n"
      "v_accvgpr_write_b32 a19, v75\n"
      "v_mfma_f32_16x16x32_bf16 v[96:99], v[64:67], a[232:235], v[152:155]\n"
      "v_cvt_pk_bf16_f32 v80, v80, v81\n"
      "v_cvt_pk_bf16_f32 v81, v82, v83\n"
      "v_cvt_pk_bf16_f32 v82, v84, v85\n"
      "v_mfma_f32_16x16x32_bf16 v[100:103], v[68:71], a[232:235], v[152:155]\n"
      "v_cvt_pk_bf16_f32 v83, v86, v87\n"
      "v_accvgpr_write_b32 a88, v80\n"
      "v_accvgpr_write_b32 a89, v81\n"
      "v_mfma_f32_16x16x32_bf16 v[124:127], v[172:175], a[16:19], v[124:127]\n"
      "v_accvgpr_write_b32 a90, v82\n"
      "v_accvgpr_write_b32 a91, v83\n"
      "v_mfma_f32_16x16x32_bf16 v[72:75], v[64:67], a[20:23], v[140:143]\n"
      "v_cvt_pk_bf16_f32 v88, v88, v89\n"
      "v_cvt_pk_bf16_f32 v89, v90, v91\n"
      "v_cvt_pk_bf16_f32 v90, v92, v93\n"
      "v_mfma_f32_16x16x32_bf16 v[76:79], v[68:71], a[20:23], v[140:143]\n"
      "v_cvt_pk_bf16_f32 v91, v94, v95\n"
      "v_accvgpr_write_b32 a160, v88\n"
      "v_accvgpr_write_b32 a161, v89\n"
      "v_mfma_f32_16x16x32_bf16 v[128:131], v[172:175], a[88:91], v[128:131]\n"
      "v_accvgpr_write_b32 a162, v90\n"
      "v_accvgpr_write_b32 a163, v91\n"
      "v_mfma_f32_16x16x32_bf16 v[80:83], v[64:67], a[92:95], v[144:147]\n"
      "v_cvt_pk_bf16_f32 v96, v96, v97\n"
      "v_cvt_pk_bf16_f32 v97, v98, v99\n"
      "v_cvt_pk_bf16_f32 v98, v100, v101\n"
      "v_mfma_f32_16x16x32_bf16 v[84:87], v[68:71], a[92:95], v[144:147]\n"
      "v_cvt_pk_bf16_f32 v99, v102, v103\n"
      "v_accvgpr_write_b32 a232, v96\n"
      "v_accvgpr_write_b32 a233, v97\n"
      "v_mfma_f32_16x16x32_bf16 v[132:135], v[172:175], a[160:163], v[132:135]\n"
      "v_accvgpr_write_b32 a234, v98\n"
      "v_accvgpr_write_b32 a235, v99\n"
      "v_mfma_f32_16x16x32_bf16 v[88:91], v[64:67], a[164:167], v[148:151]\n"
      "v_cvt_pk_bf16_f32 v72, v72, v73\n"
      "v_cvt_pk_bf16_f32 v73, v74, v75\n"
      "v_cvt_pk_bf16_f32 v74, v76, v77\n"
      "v_mfma_f32_16x16x32_bf16 v[92:95], v[68:71], a[164:167], v[148:151]\n"
      "v_cvt_pk_bf16_f32 v75, v78, v79\n"
      "v_accvgpr_write_b32 a20, v72\n"
      "v_accvgpr_write_b32 a21, v73\n"
      "v_mfma_f32_16x16x32_bf16 v[136:139], v[172:175], a[232:235], v[136:139]\n"
      "v_accvgpr_write_b32 a22, v74\n"
      "v_accvgpr_write_b32 a23, v75\n"
      "v_mfma_f32_16x16x32_bf16 v[96:99], v[64:67], a[236:239], v[152:155]\n"
      "v_cvt_pk_bf16_f32 v80, v80, v81\n"
      "v_cvt_pk_bf16_f32 v81, v82, v83\n"
      "v_cvt_pk_bf16_f32 v82, v84, v85\n"
      "v_mfma_f32_16x16x32_bf16 v[100:103], v[68:71], a[236:239], v[152:155]\n"
      "v_cvt_pk_bf16_f32 v83, v86, v87\n"
      "v_accvgpr_write_b32 a92, v80\n"
      "v_accvgpr_write_b32 a93, v81\n"
      "v_mfma_f32_16x16x32_bf16 v[124:127], v[172:175], a[20:23], v[124:127]\n"
      "v_accvgpr_write_b32 a94, v82\n"
      "v_accvgpr_write_b32 a95, v83\n"
      "v_mfma_f32_16x16x32_bf16 v[72:75], v[64:67], a[24:27], v[140:143]\n"
      "v_cvt_pk_bf16_f32 v88, v88, v89\n"
      "v_cvt_pk_bf16_f32 v89, v90, v91\n"
      "v_cvt_pk_bf16_f32 v90, v92, v93\n"
      "v_mfma_f32_16x16x32_bf16 v[76:79], v[68:71], a[24:27], v[140:143]\n"
      "v_cvt_pk_bf16_f32 v91, v94, v95\n"
      "v_accvgpr_write_b32 a164, v88\n"
      "v_accvgpr_write_b32 a165, v89\n"
      "v_mfma_f32_16x16x32_bf16 v[128:131], v[172:175], a[92:95], v[128:131]\n"
      "v_accvgpr_write_b32 a166, v90\n"
      "v_accvgpr_write_b32 a167, v91\n"
      "v_mfma_f32_16x16x32_bf16 v[80:83], v[64:67], a[96:99], v[144:147]\n"
      "v_cvt_pk_bf16_f32 v96, v96, v97\n"
      "v_cvt_pk_bf16_f32 v97, v98, v99\n"
      "v_cvt_pk_bf16_f32 v98, v100, v101\n"
      "v_mfma_f32_16x16x32_bf16 v[84:87], v[68:71], a[96:99], v[144:147]\n"
      "v_cvt_pk_bf16_f32 v99, v102, v103\n"
      "v_accvgpr_write_b32 a236, v96\n"
      "v_accvgpr_write_b32 a237, v97\n"
      "v_mfma_f32_16x16x32_bf16 v[132:135], v[172:175], a[164:167], v[132:135]\n"
      "v_accvgpr_write_b32 a238, v98\n"
      "v_accvgpr_write_b32 a239, v99\n"
      "v_mfma_f32_16x16x32_bf16 v[88:91], v[64:67], a[168:171], v[148:151]\n"
      "v_cvt_pk_bf16_f32 v72, v72, v73\n"
      "v_cvt_pk_bf16_f32 v73, v74, v75\n"
      "v_cvt_pk_bf16_f32 v74, v76, v77\n"
      "v_mfma_f32_16x16x32_bf16 v[92:95], v[68:71], a[168:171], v[148:151]\n"
      "v_cvt_pk_bf16_f32 v75, v78, v79\n"
      "v_accvgpr_write_b32 a24, v72\n"
      "v_accvgpr_write_b32 a25, v73\n"
      "v_mfma_f32_16x16x32_bf16 v[136:139], v[172:175], a[236:239], v[136:139]\n"
      "v_accvgpr_write_b32 a26, v74\n"
      "v_accvgpr_write_b32 a27, v75\n"
      "v_mfma_f32_16x16x32_bf16 v[96:99], v[64:67], a[240:243], v[152:155]\n"
      "v_cvt_pk_bf16_f32 v80, v80, v81\n"
      "v_cvt_pk_bf16_f32 v81, v82, v83\n"
      "v_cvt_pk_bf16_f32 v82, v84, v85\n"
      "v_mfma_f32_16x16x32_bf16 v[100:103], v[68:71], a[240:243], v[152:155]\n"
      "v_cvt_pk_bf16_f32 v83, v86, v87\n"
      "v_accvgpr_write_b32 a96, v80\n"
      "v_accvgpr_write_b32 a97, v81\n"
      "v_mfma_f32_16x16x32_bf16 v[124:127], v[172:175], a[24:27], v[124:127]\n"
      "v_accvgpr_write_b32 a98, v82\n"
      "v_accvgpr_write_b32 a99, v83\n"
      "v_mfma_f32_16x16x32_bf16 v[72:75], v[64:67], a[28:31], v[140:143]\n"
      "v_cvt_pk_bf16_f32 v88, v88, v89\n"
      "v_cvt_pk_bf16_f32 v89, v90, v91\n"
      "v_cvt_pk_bf16_f32 v90, v92, v93\n"
      "v_mfma_f32_16x16x32_bf16 v[76:79], v[68:71], a[28:31], v[140:143]\n"
      "v_cvt_pk_bf16_f32 v91, v94, v95\n"
      "v_accvgpr_write_b32 a168, v88\n"
      "v_accvgpr_write_b32 a169, v89\n"
      "v_mfma_f32_16x16x32_bf16 v[128:131], v[172:175], a[96:99], v[128:131]\n"
      "v_accvgpr_write_b32 a170, v90\n"
      "v_accvgpr_write_b32 a171, v91\n"
      "v_mfma_f32_16x16x32_bf16 v[80:83], v[64:67], a[100:103], v[144:147]\n"
      "v_cvt_pk_bf16_f32 v96, v96, v97\n"
      "v_cvt_pk_bf16_f32 v97, v98, v99\n"
      "v_cvt_pk_bf16_f32 v98, v100, v101\n"
      "v_mfma_f32_16x16x32_bf16 v[84:87], v[68:71], a[100:103], v[144:147]\n"
      "v_cvt_pk_bf16_f32 v99, v102, v103\n"
      "v_accvgpr_write_b32 a240, v96\n"
      "v_accvgpr_write_b32 a241, v97\n"
      "v_mfma_f32_16x16x32_bf16 v[132:135], v[172:175], a[168:171], v[132:135]\n"
      "v_accvgpr_write_b32 a242, v98\n"
      "v_accvgpr_write_b32 a243, v99\n"
      "v_mfma_f32_16x16x32_bf16 v[88:91], v[64:67], a[172:175], v[148:151]\n"
      "v_cvt_pk_bf16_f32 v72, v72, v73\n"
      "v_cvt_pk_bf16_f32 v73, v74, v75\n"
      "v_cvt_pk_bf16_f32 v74, v76, v77\n"
      "v_mfma_f32_16x16x32_bf16 v[92:95], v[68:71], a[172:175], v[148:151]\n"
      "v_cvt_pk_bf16_f32 v75, v78, v79\n"
      "v_accvgpr_write_b32 a28, v72\n"
      "v_accvgpr_write_b32 a29, v73\n"
      "v_mfma_f32_16x16x32_bf16 v[136:139], v[172:175], a[240:243], v[136:139]\n"
      "v_accvgpr_write_b32 a30, v74\n"
      "v_accvgpr_write_b32 a31, v75\n"
      "v_mfma_f32_16x16x32_bf16 v[96:99], v[64:67], a[244:247], v[152:155]\n"
      "v_cvt_pk_bf16_f32 v80, v80, v81\n"
      "v_cvt_pk_bf16_f32 v81, v82, v83\n"
      "v_cvt_pk_bf16_f32 v82, v84, v85\n"
      "v_mfma_f32_16x16x32_bf16 v[100:103], v[68:71], a[244:247], v[152:155]\n"
      "v_cvt_pk_bf16_f32 v83, v86, v87\n"
      "v_accvgpr_write_b32 a100, v80\n"
      "v_accvgpr_write_b32 a101, v81\n"
      "v_mfma_f32_16x16x32_bf16 v[124:127], v[172:175], a[28:31], v[124:127]\n"
      "v_accvgpr_write_b32 a102, v82\n"
      "v_accvgpr_write_b32 a103, v83\n"
      "v_mfma_f32_16x16x32_bf16 v[72:75], v[64:67], a[32:35], v[140:143]\n"
      "v_cvt_pk_bf16_f32 v88, v88, v89\n"
      "v_cvt_pk_bf16_f32 v89, v90, v91\n"
      "v_cvt_pk_bf16_f32 v90, v92, v93\n"
      "v_mfma_f32_16x16x32_bf16 v[76:79], v[68:71], a[32:35], v[140:143]\n"
      "v_cvt_pk_bf16_f32 v91, v94, v95\n"
      "v_accvgpr_write_b32 a172, v88\n"
      "v_accvgpr_write_b32 a173, v89\n"
      "v_mfma_f32_16x16x32_bf16 v[128:131], v[172:175], a[100:103], v[128:131]\n"
      "v_accvgpr_write_b32 a174, v90\n"
      "v_accvgpr_write_b32 a175, v91\n"
      "v_mfma_f32_16x16x32_bf16 v[80:83], v[64:67], a[104:107], v[144:147]\n"
      "v_cvt_pk_bf16_f32 v96, v96, v97\n"
      "v_cvt_pk_bf16_f32 v97, v98, v99\n"
      "v_cvt_pk_bf16_f32 v98, v100, v101\n"
      "v_mfma_f32_16x16x32_bf16 v[84:87], v[68:71], a[104:107], v[144:147]\n"
      "v_cvt_pk_bf16_f32 v99, v102, v103\n"
      "v_accvgpr_write_b32 a244, v96\n"
      "v_accvgpr_write_b32 a245, v97\n"
      "v_mfma_f32_16x16x32_bf16 v[132:135], v[172:175], a[172:175], v[132:135]\n"
      "v_accvgpr_write_b32 a246, v98\n"
      "v_accvgpr_write_b32 a247, v99\n"
      "v_mfma_f32_16x16x32_bf16 v[88:91], v[64:67], a[176:179], v[148:151]\n"
      "v_cvt_pk_bf16_f32 v72, v72, v73\n"
      "v_cvt_pk_bf16_f32 v73, v74, v75\n"
      "v_cvt_pk_bf16_f32 v74, v76, v77\n"
      "v_mfma_f32_16x16x32_bf16 v[92:95], v[68:71], a[176:179], v[148:151]\n"
      "v_cvt_pk_bf16_f32 v75, v78, v79\n"
      "v_accvgpr_write_b32 a32, v72\n"
      "v_accvgpr_write_b32 a33, v73\n"
      "v_mfma_f32_16x16x32_bf16 v[136:139], v[172:175], a[244:247], v[136:139]\n"
      "v_accvgpr_write_b32 a34, v74\n"
      "v_accvgpr_write_b32 a35, v75\n"
      "v_mfma_f32_16x16x32_bf16 v[96:99], v[64:67], a[248:251], v[152:155]\n"
      "v_cvt_pk_bf16_f32 v80, v80, v81\n"
      "v_cvt_pk_bf16_f32 v81, v82, v83\n"
      "v_cvt_pk_bf16_f32 v82, v84, v85\n"
      "v_mfma_f32_16x16x32_bf16 v[100:103], v[68:71], a[248:251], v[152:155]\n"
      "v_cvt_pk_bf16_f32 v83, v86, v87\n"
      "v_accvgpr_write_b32 a104, v80\n"
      "v_accvgpr_write_b32 a105, v81\n"
      "v_mfma_f32_16x16x32_bf16 v[124:127], v[172:175], a[32:35], v[124:127]\n"
      "v_accvgpr_write_b32 a106, v82\n"
      "v_accvgpr_write_b32 a107, v83\n"
      "v_mfma_f32_16x16x32_bf16 v[72:75], v[64:67], a[36:39], v[140:143]\n"
      "v_cvt_pk_bf16_f32 v88, v88, v89\n"
      "v_cvt_pk_bf16_f32 v89, v90, v91\n"
      "v_cvt_pk_bf16_f32 v90, v92, v93\n"
      "v_mfma_f32_16x16x32_bf16 v[76:79], v[68:71], a[36:39], v[140:143]\n"
      "v_cvt_pk_bf16_f32 v91, v94, v95\n"
      "v_accvgpr_write_b32 a176, v88\n"
      "v_accvgpr_write_b32 a177, v89\n"
      "v_mfma_f32_16x16x32_bf16 v[128:131], v[172:175], a[104:107], v[128:131]\n"
      "v_accvgpr_write_b32 a178, v90\n"
      "v_accvgpr_write_b32 a179, v91\n"
      "v_mfma_f32_16x16x32_bf16 v[80:83], v[64:67], a[108:111], v[144:147]\n"
      "v_cvt_pk_bf16_f32 v96, v96, v97\n"
      "v_cvt_pk_bf16_f32 v97, v98, v99\n"
      "v_cvt_pk_bf16_f32 v98, v100, v101\n"
      "v_mfma_f32_16x16x32_bf16 v[84:87], v[68:71], a[108:111], v[144:147]\n"
      "v_cvt_pk_bf16_f32 v99, v102, v103\n"
      "v_accvgpr_write_b32 a248, v96\n"
      "v_accvgpr_write_b32 a249, v97\n"
      "v_mfma_f32_16x16x32_bf16 v[132:135], v[172:175], a[176:179], v[132:135]\n"
      "v_accvgpr_write_b32 a250, v98\n"
      "v_accvgpr_write_b32 a251, v99\n"
      "v_mfma_f32_16x16x32_bf16 v[88:91], v[64:67], a[180:183], v[148:151]\n"
      "v_cvt_pk_bf16_f32 v72, v72, v73\n"
      "v_cvt_pk_bf16_f32 v73, v74, v75\n"
      "v_cvt_pk_bf16_f32 v74, v76, v77\n"
      "v_mfma_f32_16x16x32_bf16 v[92:95], v[68:71], a[180:183], v[148:151]\n"
      "v_cvt_pk_bf16_f32 v75, v78, v79\n"
      "v_accvgpr_write_b32 a36, v72\n"
      "v_accvgpr_write_b32 a37, v73\n"
      "v_mfma_f32_16x16x32_bf16 v[136:139], v[172:175], a[248:251], v[136:139]\n"
      "v_accvgpr_write_b32 a38, v74\n"
      "v_accvgpr_write_b32 a39, v75\n"
      "v_mfma_f32_16x16x32_bf16 v[96:99], v[64:67], a[252:255], v[152:155]\n"
      "v_cvt_pk_bf16_f32 v80, v80, v81\n"
      "v_cvt_pk_bf16_f32 v81, v82, v83\n"
      "v_cvt_pk_bf16_f32 v82, v84, v85\n"
      "v_mfma_f32_16x16x32_bf16 v[100:103], v[68:71], a[252:255], v[152:155]\n"
      "v_cvt_pk_bf16_f32 v83, v86, v87\n"
      "v_accvgpr_write_b32 a108, v80\n"
      "v_accvgpr_write_b32 a109, v81\n"
      "v_mfma_f32_16x16x32_bf16 v[124:127], v[172:175], a[36:39], v[124:127]\n"
      "v_accvgpr_write_b32 a110, v82\n"
      "v_accvgpr_write_b32 a111, v83\n"
      "s_nop 1\n"
      "v_mfma_f32_16x16x32_bf16 v[128:131], v[172:175], a[108:111], v[128:131]\n"
      "v_cvt_pk_bf16_f32 v88, v88, v89\n"
      "v_cvt_pk_bf16_f32 v89, v90, v91\n"
      "v_cvt_pk_bf16_f32 v90, v92, v93\n"
      "v_cvt_pk_bf16_f32 v91, v94, v95\n"
      "v_accvgpr_write_b32 a180, v88\n"
      "v_accvgpr_write_b32 a181, v89\n"
      "v_accvgpr_write_b32 a182, v90\n"
      "v_accvgpr_write_b32 a183, v91\n"
      "s_nop 1\n"
      "v_mfma_f32_16x16x32_bf16 v[132:135], v[172:175], a[180:183], v[132:135]\n"
      "v_cvt_pk_bf16_f32 v96, v96, v97\n"
      "v_cvt_pk_bf16_f32 v97, v98, v99\n"
      "v_cvt_pk_bf16_f32 v98, v100, v101\n"
      "v_cvt_pk_bf16_f32 v99, v102, v103\n"
      "v_accvgpr_write_b32 a252, v96\n"
      "v_accvgpr_write_b32 a253, v97\n"
      "v_accvgpr_write_b32 a254, v98\n"
      "v_accvgpr_write_b32 a255, v99\n"
      "s_nop 1\n"
      "v_mfma_f32_16x16x32_bf16 v[136:139], v[172:175], a[252:255], v[136:139]\n"
      "v_add_f32 v168, v124, v156\n"
      "v_add_f32 v169, v128, v157\n"
      "v_add_f32 v170, v132, v158\n"
      "s_nop 5\n"
      "v_add_f32 v171, v136, v159\n"
      "v_mul_f32 v140, 0xbb800000, v168\n"
      "v_mul_f32 v141, 0xbb800000, v168\n"
      "v_mul_f32 v142, 0xbb800000, v168\n"
      "v_mul_f32 v143, 0xbb800000, v168\n"
      "v_mul_f32 v144, 0xbb800000, v169\n"
      "v_mul_f32 v145, 0xbb800000, v169\n"
      "v_mul_f32 v146, 0xbb800000, v169\n"
      "v_mul_f32 v147, 0xbb800000, v169\n"
      "v_mul_f32 v148, 0xbb800000, v170\n"
      "v_mul_f32 v149, 0xbb800000, v170\n"
      "v_mul_f32 v150, 0xbb800000, v170\n"
      "v_mul_f32 v151, 0xbb800000, v170\n"
      "v_mul_f32 v152, 0xbb800000, v171\n"
      "v_mul_f32 v153, 0xbb800000, v171\n"
      "v_mul_f32 v154, 0xbb800000, v171\n"
      "v_mul_f32 v155, 0xbb800000, v171\n"
      "v_mfma_f32_16x16x32_bf16 v[72:75], v[64:67], a[40:43], v[140:143]\n"
      "v_mfma_f32_16x16x32_bf16 v[76:79], v[68:71], a[40:43], v[140:143]\n"
      "v_mfma_f32_16x16x32_bf16 v[80:83], v[64:67], a[112:115], v[144:147]\n"
      "v_mfma_f32_16x16x32_bf16 v[84:87], v[68:71], a[112:115], v[144:147]\n"
      "v_mfma_f32_16x16x32_bf16 v[88:91], v[64:67], a[184:187], v[148:151]\n"
      "s_nop 4\n"
      "v_cvt_pk_bf16_f32 v72, v72, v73\n"
      "v_cvt_pk_bf16_f32 v73, v74, v75\n"
      "v_cvt_pk_bf16_f32 v74, v76, v77\n"
      "v_mfma_f32_16x16x32_bf16 v[92:95], v[68:71], a[184:187], v[148:151]\n"
      "v_cvt_pk_bf16_f32 v75, v78, v79\n"
      "v_accvgpr_write_b32 a40, v72\n"
      "v_accvgpr_write_b32 a41, v73\n"
      "v_accvgpr_write_b32 a42, v74\n"
      "v_accvgpr_write_b32 a43, v75\n"
      "v_mfma_f32_16x16x32_bf16 v[96:99], v[64:67], v[20:23], v[152:155]\n"
      "v_cvt_pk_bf16_f32 v80, v80, v81\n"
      "v_cvt_pk_bf16_f32 v81, v82, v83\n"
      "v_cvt_pk_bf16_f32 v82, v84, v85\n"
      "v_mfma_f32_16x16x32_bf16 v[100:103], v[68:71], v[20:23], v[152:155]\n"
      "v_cvt_pk_bf16_f32 v83, v86, v87\n"
      "v_accvgpr_write_b32 a112, v80\n"
      "v_accvgpr_write_b32 a113, v81\n"
      "v_mfma_f32_16x16x32_bf16 v[124:127], v[172:175], a[40:43], v[124:127]\n"
      "v_accvgpr_write_b32 a114, v82\n"
      "v_accvgpr_write_b32 a115, v83\n"
      "v_mfma_f32_16x16x32_bf16 v[72:75], v[64:67], a[44:47], v[140:143]\n"
      "v_cvt_pk_bf16_f32 v88, v88, v89\n"
      "v_cvt_pk_bf16_f32 v89, v90, v91\n"
      "v_cvt_pk_bf16_f32 v90, v92, v93\n"
      "v_mfma_f32_16x16x32_bf16 v[76:79], v[68:71], a[44:47], v[140:143]\n"
      "v_cvt_pk_bf16_f32 v91, v94, v95\n"
      "v_accvgpr_write_b32 a184, v88\n"
      "v_accvgpr_write_b32 a185, v89\n"
      "v_mfma_f32_16x16x32_bf16 v[128:131], v[172:175], a[112:115], v[128:131]\n"
      "v_accvgpr_write_b32 a186, v90\n"
      "v_accvgpr_write_b32 a187, v91\n"
      "v_mfma_f32_16x16x32_bf16 v[80:83], v[64:67], a[116:119], v[144:147]\n"
      "v_cvt_pk_bf16_f32 v20, v96, v97\n"
      "v_cvt_pk_bf16_f32 v21, v98, v99\n"
      "v_cvt_pk_bf16_f32 v22, v100, v101\n"
      "v_mfma_f32_16x16x32_bf16 v[84:87], v[68:71], a[116:119], v[144:147]\n"
      "v_cvt_pk_bf16_f32 v23, v102, v103\n"
      "v_mfma_f32_16x16x32_bf16 v[132:135], v[172:175], a[184:187], v[132:135]\n"
      "v_mfma_f32_16x16x32_bf16 v[88:91], v[64:67], a[188:191], v[148:151]\n"
      "v_cvt_pk_bf16_f32 v72, v72, v73\n"
      "v_cvt_pk_bf16_f32 v73, v74, v75\n"
      "v_cvt_pk_bf16_f32 v74, v76, v77\n"
      "v_mfma_f32_16x16x32_bf16 v[92:95], v[68:71], a[188:191], v[148:151]\n"
      "v_cvt_pk_bf16_f32 v75, v78, v79\n"
      "v_accvgpr_write_b32 a44, v72\n"
      "v_accvgpr_write_b32 a45, v73\n"
      "v_mfma_f32_16x16x32_bf16 v[136:139], v[172:175], v[20:23], v[136:139]\n"
      "v_accvgpr_write_b32 a46, v74\n"
      "v_accvgpr_write_b32 a47, v75\n"
      "v_mfma_f32_16x16x32_bf16 v[96:99], v[64:67], v[24:27], v[152:155]\n"
      "v_cvt_pk_bf16_f32 v80, v80, v81\n"
      "v_cvt_pk_bf16_f32 v81, v82, v83\n"
      "v_cvt_pk_bf16_f32 v82, v84, v85\n"
      "v_mfma_f32_16x16x32_bf16 v[100:103], v[68:71], v[24:27], v[152:155]\n"
      "v_cvt_pk_bf16_f32 v83, v86, v87\n"
      "v_accvgpr_write_b32 a116, v80\n"
      "v_accvgpr_write_b32 a117, v81\n"
      "v_mfma_f32_16x16x32_bf16 v[124:127], v[172:175], a[44:47], v[124:127]\n"
      "v_accvgpr_write_b32 a118, v82\n"
      "v_accvgpr_write_b32 a119, v83\n"
      "v_mfma_f32_16x16x32_bf16 v[72:75], v[64:67], a[48:51], v[140:143]\n"
      "v_cvt_pk_bf16_f32 v88, v88, v89\n"
      "v_cvt_pk_bf16_f32 v89, v90, v91\n"
      "v_cvt_pk_bf16_f32 v90, v92, v93\n"
      "v_mfma_f32_16x16x32_bf16 v[76:79], v[68:71], a[48:51], v[140:143]\n"
      "v_cvt_pk_bf16_f32 v91, v94, v95\n"
      "v_accvgpr_write_b32 a188, v88\n"
      "v_accvgpr_write_b32 a189, v89\n"
      "v_mfma_f32_16x16x32_bf16 v[128:131], v[172:175], a[116:119], v[128:131]\n"
      "v_accvgpr_write_b32 a190, v90\n"
      "v_accvgpr_write_b32 a191, v91\n"
      "v_mfma_f32_16x16x32_bf16 v[80:83], v[64:67], a[120:123], v[144:147]\n"
      "v_cvt_pk_bf16_f32 v24, v96, v97\n"
      "v_cvt_pk_bf16_f32 v25, v98, v99\n"
      "v_cvt_pk_bf16_f32 v26, v100, v101\n"
      "v_mfma_f32_16x16x32_bf16 v[84:87], v[68:71], a[120:123], v[144:147]\n"
      "v_cvt_pk_bf16_f32 v27, v102, v103\n"
      "v_mfma_f32_16x16x32_bf16 v[132:135], v[172:175], a[188:191], v[132:135]\n"
      "v_mfma_f32_16x16x32_bf16 v[88:91], v[64:67], a[192:195], v[148:151]\n"
      "v_cvt_pk_bf16_f32 v72, v72, v73\n"
      "v_cvt_pk_bf16_f32 v73, v74, v75\n"
      "v_cvt_pk_bf16_f32 v74, v76, v77\n"
      "v_mfma_f32_16x16x32_bf16 v[92:95], v[68:71], a[192:195], v[148:151]\n"
      "v_cvt_pk_bf16_f32 v75, v78, v79\n"
      "v_accvgpr_write_b32 a48, v72\n"
      "v_accvgpr_write_b32 a49, v73\n"
      "v_mfma_f32_16x16x32_bf16 v[136:139], v[172:175], v[24:27], v[136:139]\n"
      "v_accvgpr_write_b32 a50, v74\n"
      "v_accvgpr_write_b32 a51, v75\n"
      "v_mfma_f32_16x16x32_bf16 v[96:99], v[64:67], v[28:31], v[152:155]\n"
      "v_cvt_pk_bf16_f32 v80, v80, v81\n"
      "v_cvt_pk_bf16_f32 v81, v82, v83\n"
      "v_cvt_pk_bf16_f32 v82, v84, v85\n"
      "v_mfma_f32_16x16x32_bf16 v[100:103], v[68:71], v[28:31], v[152:155]\n"
      "v_cvt_pk_bf16_f32 v83, v86, v87\n"
      "v_accvgpr_write_b32 a120, v80\n"
      "v_accvgpr_write_b32 a121, v81\n"
      "v_mfma_f32_16x16x32_bf16 v[124:127], v[172:175], a[48:51], v[124:127]\n"
      "v_accvgpr_write_b32 a122, v82\n"
      "v_accvgpr_write_b32 a123, v83\n"
      "v_mfma_f32_16x16x32_bf16 v[72:75], v[64:67], a[52:55], v[140:143]\n"
      "v_cvt_pk_bf16_f32 v88, v88, v89\n"
      "v_cvt_pk_bf16_f32 v89, v90, v91\n"
      "v_cvt_pk_bf16_f32 v90, v92, v93\n"
      "v_mfma_f32_16x16x32_bf16 v[76:79], v[68:71], a[52:55], v[140:143]\n"
      "v_cvt_pk_bf16_f32 v91, v94, v95\n"
      "v_accvgpr_write_b32 a192, v88\n"
      "v_accvgpr_write_b32 a193, v89\n"
      "v_mfma_f32_16x16x32_bf16 v[128:131], v[172:175], a[120:123], v[128:131]\n"
      "v_accvgpr_write_b32 a194, v90\n"
      "v_accvgpr_write_b32 a195, v91\n"
      "v_mfma_f32_16x16x32_bf16 v[80:83], v[64:67], a[124:127], v[144:147]\n"
      "v_cvt_pk_bf16_f32 v28, v96, v97\n"
      "v_cvt_pk_bf16_f32 v29, v98, v99\n"
      "v_cvt_pk_bf16_f32 v30, v100, v101\n"
      "v_mfma_f32_16x16x32_bf16 v[84:87], v[68:71], a[124:127], v[144:147]\n"
      "v_cvt_pk_bf16_f32 v31, v102, v103\n"
      "v_mfma_f32_16x16x32_bf16 v[132:135], v[172:175], a[192:195], v[132:135]\n"
      "v_mfma_f32_16x16x32_bf16 v[88:91], v[64:67], a[196:199], v[148:151]\n"
      "v_cvt_pk_bf16_f32 v72, v72, v73\n"
      "v_cvt_pk_bf16_f32 v73, v74, v75\n"
      "v_cvt_pk_bf16_f32 v74, v76, v77\n"
      "v_mfma_f32_16x16x32_bf16 v[92:95], v[68:71], a[196:199], v[148:151]\n"
      "v_cvt_pk_bf16_f32 v75, v78, v79\n"
      "v_accvgpr_write_b32 a52, v72\n"
      "v_accvgpr_write_b32 a53, v73\n"
      "v_mfma_f32_16x16x32_bf16 v[136:139], v[172:175], v[28:31], v[136:139]\n"
      "v_accvgpr_write_b32 a54, v74\n"
      "v_accvgpr_write_b32 a55, v75\n"
      "v_mfma_f32_16x16x32_bf16 v[96:99], v[64:67], v[32:35], v[152:155]\n"
      "v_cvt_pk_bf16_f32 v80, v80, v81\n"
      "v_cvt_pk_bf16_f32 v81, v82, v83\n"
      "v_cvt_pk_bf16_f32 v82, v84, v85\n"
      "v_mfma_f32_16x16x32_bf16 v[100:103], v[68:71], v[32:35], v[152:155]\n"
      "v_cvt_pk_bf16_f32 v83, v86, v87\n"
      "v_accvgpr_write_b32 a124, v80\n"
      "v_accvgpr_write_b32 a125, v81\n"
      "v_mfma_f32_16x16x32_bf16 v[124:127], v[172:175], a[52:55], v[124:127]\n"
      "v_accvgpr_write_b32 a126, v82\n"
      "v_accvgpr_write_b32 a127, v83\n"
      "v_mfma_f32_16x16x32_bf16 v[72:75], v[64:67], a[56:59], v[140:143]\n"
      "v_cvt_pk_bf16_f32 v88, v88, v89\n"
      "v_cvt_pk_bf16_f32 v89, v90, v91\n"
      "v_cvt_pk_bf16_f32 v90, v92, v93\n"
      "v_mfma_f32_16x16x32_bf16 v[76:79], v[68:71], a[56:59], v[140:143]\n"
      "v_cvt_pk_bf16_f32 v91, v94, v95\n"
      "v_accvgpr_write_b32 a196, v88\n"
      "v_accvgpr_write_b32 a197, v89\n"
      "v_mfma_f32_16x16x32_bf16 v[128:131], v[172:175], a[124:127], v[128:131]\n"
      "v_accvgpr_write_b32 a198, v90\n"
      "v_accvgpr_write_b32 a199, v91\n"
      "v_mfma_f32_16x16x32_bf16 v[80:83], v[64:67], a[128:131], v[144:147]\n"
      "v_cvt_pk_bf16_f32 v32, v96, v97\n"
      "v_cvt_pk_bf16_f32 v33, v98, v99\n"
      "v_cvt_pk_bf16_f32 v34, v100, v101\n"
      "v_mfma_f32_16x16x32_bf16 v[84:87], v[68:71], a[128:131], v[144:147]\n"
      "v_cvt_pk_bf16_f32 v35, v102, v103\n"
      "v_mfma_f32_16x16x32_bf16 v[132:135], v[172:175], a[196:199], v[132:135]\n"
      "v_mfma_f32_16x16x32_bf16 v[88:91], v[64:67], a[200:203], v[148:151]\n"
      "v_cvt_pk_bf16_f32 v72, v72, v73\n"
      "v_cvt_pk_bf16_f32 v73, v74, v75\n"
      "v_cvt_pk_bf16_f32 v74, v76, v77\n"
      "v_mfma_f32_16x16x32_bf16 v[92:95], v[68:71], a[200:203], v[148:151]\n"
      "v_cvt_pk_bf16_f32 v75, v78, v79\n"
      "v_accvgpr_write_b32 a56, v72\n"
      "v_accvgpr_write_b32 a57, v73\n"
      "v_mfma_f32_16x16x32_bf16 v[136:139], v[172:175], v[32:35], v[136:139]\n"
      "v_accvgpr_write_b32 a58, v74\n"
      "v_accvgpr_write_b32 a59, v75\n"
      "v_mfma_f32_16x16x32_bf16 v[96:99], v[64:67], v[36:39], v[152:155]\n"
      "v_cvt_pk_bf16_f32 v80, v80, v81\n"
      "v_cvt_pk_bf16_f32 v81, v82, v83\n"
      "v_cvt_pk_bf16_f32 v82, v84, v85\n"
      "v_mfma_f32_16x16x32_bf16 v[100:103], v[68:71], v[36:39], v[152:155]\n"
      "v_cvt_pk_bf16_f32 v83, v86, v87\n"
      "v_accvgpr_write_b32 a128, v80\n"
      "v_accvgpr_write_b32 a129, v81\n"
      "v_mfma_f32_16x16x32_bf16 v[124:127], v[172:175], a[56:59], v[124:127]\n"
      "v_accvgpr_write_b32 a130, v82\n"
      "v_accvgpr_write_b32 a131, v83\n"
      "s_nop 1\n"
      "v_mfma_f32_16x16x32_bf16 v[128:131], v[172:175], a[128:131], v[128:131]\n"
      "v_cvt_pk_bf16_f32 v88, v88, v89\n"
      "v_cvt_pk_bf16_f32 v89, v90, v91\n"
      "v_cvt_pk_bf16_f32 v90, v92, v93\n"
      "v_cvt_pk_bf16_f32 v91, v94, v95\n"
      "v_accvgpr_write_b32 a200, v88\n"
      "v_accvgpr_write_b32 a201, v89\n"
      "v_accvgpr_write_b32 a202, v90\n"
      "v_accvgpr_write_b32 a203, v91\n"
      "s_nop 1\n"
      "v_mfma_f32_16x16x32_bf16 v[132:135], v[172:175], a[200:203], v[132:135]\n"
      "v_cvt_pk_bf16_f32 v36, v96, v97\n"
      "v_cvt_pk_bf16_f32 v37, v98, v99\n"
      "v_cvt_pk_bf16_f32 v38, v100, v101\n"
      "v_cvt_pk_bf16_f32 v39, v102, v103\n"
      "s_nop 1\n"
      "v_mfma_f32_16x16x32_bf16 v[136:139], v[172:175], v[36:39], v[136:139]\n"
      "v_add_f32 v168, v124, v160\n"
      "v_add_f32 v169, v128, v161\n"
      "v_add_f32 v170, v132, v162\n"
      "s_nop 5\n"
      "v_add_f32 v171, v136, v163\n"
      "v_mul_f32 v140, 0xbc2aaaab, v168\n"
      "v_mul_f32 v141, 0xbc2aaaab, v168\n"
      "v_mul_f32 v142, 0xbc2aaaab, v168\n"
      "v_mul_f32 v143, 0xbc2aaaab, v168\n"
      "v_mul_f32 v144, 0xbc2aaaab, v169\n"
      "v_mul_f32 v145, 0xbc2aaaab, v169\n"
      "v_mul_f32 v146, 0xbc2aaaab, v169\n"
      "v_mul_f32 v147, 0xbc2aaaab, v169\n"
      "v_mul_f32 v148, 0xbc2aaaab, v170\n"
      "v_mul_f32 v149, 0xbc2aaaab, v170\n"
      "v_mul_f32 v150, 0xbc2aaaab, v170\n"
      "v_mul_f32 v151, 0xbc2aaaab, v170\n"
      "v_mul_f32 v152, 0xbc2aaaab, v171\n"
      "v_mul_f32 v153, 0xbc2aaaab, v171\n"
      "v_mul_f32 v154, 0xbc2aaaab, v171\n"
      "v_mul_f32 v155, 0xbc2aaaab, v171\n"
      "v_mfma_f32_16x16x32_bf16 v[72:75], v[64:67], a[60:63], v[140:143]\n"
      "v_mfma_f32_16x16x32_bf16 v[76:79], v[68:71], a[60:63], v[140:143]\n"
      "v_mfma_f32_16x16x32_bf16 v[80:83], v[64:67], a[132:135], v[144:147]\n"
      "v_mfma_f32_16x16x32_bf16 v[84:87], v[68:71], a[132:135], v[144:147]\n"
      "v_mfma_f32_16x16x32_bf16 v[88:91], v[64:67], a[204:207], v[148:151]\n"
      "s_nop 4\n"
      "v_cvt_pk_bf16_f32 v72, v72, v73\n"
      "v_cvt_pk_bf16_f32 v73, v74, v75\n"
      "v_cvt_pk_bf16_f32 v74, v76, v77\n"
      "v_mfma_f32_16x16x32_bf16 v[92:95], v[68:71], a[204:207], v[148:151]\n"
      "v_cvt_pk_bf16_f32 v75, v78, v79\n"
      "v_accvgpr_write_b32 a60, v72\n"
      "v_accvgpr_write_b32 a61, v73\n"
      "v_accvgpr_write_b32 a62, v74\n"
      "v_accvgpr_write_b32 a63, v75\n"
      "v_mfma_f32_16x16x32_bf16 v[96:99], v[64:67], v[40:43], v[152:155]\n"
      "v_cvt_pk_bf16_f32 v80, v80, v81\n"
      "v_cvt_pk_bf16_f32 v81, v82, v83\n"
      "v_cvt_pk_bf16_f32 v82, v84, v85\n"
      "v_mfma_f32_16x16x32_bf16 v[100:103], v[68:71], v[40:43], v[152:155]\n"
      "v_cvt_pk_bf16_f32 v83, v86, v87\n"
      "v_accvgpr_write_b32 a132, v80\n"
      "v_accvgpr_write_b32 a133, v81\n"
      "v_mfma_f32_16x16x32_bf16 v[124:127], v[172:175], a[60:63], v[124:127]\n"
      "v_accvgpr_write_b32 a134, v82\n"
      "v_accvgpr_write_b32 a135, v83\n"
      "v_mfma_f32_16x16x32_bf16 v[72:75], v[64:67], a[64:67], v[140:143]\n"
      "v_cvt_pk_bf16_f32 v88, v88, v89\n"
      "v_cvt_pk_bf16_f32 v89, v90, v91\n"
      "v_cvt_pk_bf16_f32 v90, v92, v93\n"
      "v_mfma_f32_16x16x32_bf16 v[76:79], v[68:71], a[64:67], v[140:143]\n"
      "v_cvt_pk_bf16_f32 v91, v94, v95\n"
      "v_accvgpr_write_b32 a204, v88\n"
      "v_accvgpr_write_b32 a205, v89\n"
      "v_mfma_f32_16x16x32_bf16 v[128:131], v[172:175], a[132:135], v[128:131]\n"
      "v_accvgpr_write_b32 a206, v90\n"
      "v_accvgpr_write_b32 a207, v91\n"
      "v_mfma_f32_16x16x32_bf16 v[80:83], v[64:67], a[136:139], v[144:147]\n"
      "v_cvt_pk_bf16_f32 v40, v96, v97\n"
      "v_cvt_pk_bf16_f32 v41, v98, v99\n"
      "v_cvt_pk_bf16_f32 v42, v100, v101\n"
      "v_mfma_f32_16x16x32_bf16 v[84:87], v[68:71], a[136:139], v[144:147]\n"
      "v_cvt_pk_bf16_f32 v43, v102, v103\n"
      "v_mfma_f32_16x16x32_bf16 v[132:135], v[172:175], a[204:207], v[132:135]\n"
      "v_mfma_f32_16x16x32_bf16 v[88:91], v[64:67], a[208:211], v[148:151]\n"
      "v_cvt_pk_bf16_f32 v72, v72, v73\n"
      "v_cvt_pk_bf16_f32 v73, v74, v75\n"
      "v_cvt_pk_bf16_f32 v74, v76, v77\n"
      "v_mfma_f32_16x16x32_bf16 v[92:95], v[68:71], a[208:211], v[148:151]\n"
      "v_cvt_pk_bf16_f32 v75, v78, v79\n"
      "v_accvgpr_write_b32 a64, v72\n"
      "v_accvgpr_write_b32 a65, v73\n"
      "v_mfma_f32_16x16x32_bf16 v[136:139], v[172:175], v[40:43], v[136:139]\n"
      "v_accvgpr_write_b32 a66, v74\n"
      "v_accvgpr_write_b32 a67, v75\n"
      "v_mfma_f32_16x16x32_bf16 v[96:99], v[64:67], v[44:47], v[152:155]\n"
      "v_cvt_pk_bf16_f32 v80, v80, v81\n"
      "v_cvt_pk_bf16_f32 v81, v82, v83\n"
      "v_cvt_pk_bf16_f32 v82, v84, v85\n"
      "v_mfma_f32_16x16x32_bf16 v[100:103], v[68:71], v[44:47], v[152:155]\n"
      "v_cvt_pk_bf16_f32 v83, v86, v87\n"
      "v_accvgpr_write_b32 a136, v80\n"
      "v_accvgpr_write_b32 a137, v81\n"
      "v_mfma_f32_16x16x32_bf16 v[124:127], v[172:175], a[64:67], v[124:127]\n"
      "v_accvgpr_write_b32 a138, v82\n"
      "v_accvgpr_write_b32 a139, v83\n"
      "s_nop 1\n"
      "v_mfma_f32_16x16x32_bf16 v[128:131], v[172:175], a[136:139], v[128:131]\n"
      "v_cvt_pk_bf16_f32 v88, v88, v89\n"
      "v_cvt_pk_bf16_f32 v89, v90, v91\n"
      "v_cvt_pk_bf16_f32 v90, v92, v93\n"
      "v_cvt_pk_bf16_f32 v91, v94, v95\n"
      "v_accvgpr_write_b32 a208, v88\n"
      "v_accvgpr_write_b32 a209, v89\n"
      "v_accvgpr_write_b32 a210, v90\n"
      "v_accvgpr_write_b32 a211, v91\n"
      "s_nop 1\n"
      "v_mfma_f32_16x16x32_bf16 v[132:135], v[172:175], a[208:211], v[132:135]\n"
      "v_cvt_pk_bf16_f32 v44, v96, v97\n"
      "v_cvt_pk_bf16_f32 v45, v98, v99\n"
      "v_cvt_pk_bf16_f32 v46, v100, v101\n"
      "v_cvt_pk_bf16_f32 v47, v102, v103\n"
      "s_nop 1\n"
      "v_mfma_f32_16x16x32_bf16 v[136:139], v[172:175], v[44:47], v[136:139]\n"
      "v_add_f32 v168, v124, v164\n"
      "v_add_f32 v169, v128, v165\n"
      "v_add_f32 v170, v132, v166\n"
      "s_nop 5\n"
      "v_add_f32 v171, v136, v167\n"
      "v_mul_f32 v140, 0xbd000000, v168\n"
      "v_mul_f32 v141, 0xbd000000, v168\n"
      "v_mul_f32 v142, 0xbd000000, v168\n"
      "v_mul_f32 v143, 0xbd000000, v168\n"
      "v_mul_f32 v144, 0xbd000000, v169\n"
      "v_mul_f32 v145, 0xbd000000, v169\n"
      "v_mul_f32 v146, 0xbd000000, v169\n"
      "v_mul_f32 v147, 0xbd000000, v169\n"
      "v_mul_f32 v148, 0xbd000000, v170\n"
      "v_mul_f32 v149, 0xbd000000, v170\n"
      "v_mul_f32 v150, 0xbd000000, v170\n"
      "v_mul_f32 v151, 0xbd000000, v170\n"
      "v_mul_f32 v152, 0xbd000000, v171\n"
      "v_mul_f32 v153, 0xbd000000, v171\n"
      "v_mul_f32 v154, 0xbd000000, v171\n"
      "v_mul_f32 v155, 0xbd000000, v171\n"
      "v_mfma_f32_16x16x32_bf16 v[72:75], v[64:67], a[68:71], v[140:143]\n"
      "v_mfma_f32_16x16x32_bf16 v[76:79], v[68:71], a[68:71], v[140:143]\n"
      "v_mfma_f32_16x16x32_bf16 v[80:83], v[64:67], a[140:143], v[144:147]\n"
      "v_mfma_f32_16x16x32_bf16 v[84:87], v[68:71], a[140:143], v[144:147]\n"
      "v_mfma_f32_16x16x32_bf16 v[88:91], v[64:67], a[212:215], v[148:151]\n"
      "s_nop 4\n"
      "v_cvt_pk_bf16_f32 v72, v72, v73\n"
      "v_cvt_pk_bf16_f32 v73, v74, v75\n"
      "v_cvt_pk_bf16_f32 v74, v76, v77\n"
      "v_mfma_f32_16x16x32_bf16 v[92:95], v[68:71], a[212:215], v[148:151]\n"
      "v_cvt_pk_bf16_f32 v75, v78, v79\n"
      "v_accvgpr_write_b32 a68, v72\n"
      "v_accvgpr_write_b32 a69, v73\n"
      "v_accvgpr_write_b32 a70, v74\n"
      "v_accvgpr_write_b32 a71, v75\n"
      "v_mfma_f32_16x16x32_bf16 v[96:99], v[64:67], v[48:51], v[152:155]\n"
      "v_cvt_pk_bf16_f32 v80, v80, v81\n"
      "v_cvt_pk_bf16_f32 v81, v82, v83\n"
      "v_cvt_pk_bf16_f32 v82, v84, v85\n"
      "v_mfma_f32_16x16x32_bf16 v[100:103], v[68:71], v[48:51], v[152:155]\n"
      "v_cvt_pk_bf16_f32 v83, v86, v87\n"
      "v_accvgpr_write_b32 a140, v80\n"
      "v_accvgpr_write_b32 a141, v81\n"
      "v_accvgpr_write_b32 a142, v82\n"
      "v_accvgpr_write_b32 a143, v83\n"
      "v_cvt_pk_bf16_f32 v88, v88, v89\n"
      "v_cvt_pk_bf16_f32 v89, v90, v91\n"
      "v_cvt_pk_bf16_f32 v90, v92, v93\n"
      "v_cvt_pk_bf16_f32 v91, v94, v95\n"
      "v_accvgpr_write_b32 a212, v88\n"
      "v_accvgpr_write_b32 a213, v89\n"
      "v_accvgpr_write_b32 a214, v90\n"
      "v_accvgpr_write_b32 a215, v91\n"
      "v_cvt_pk_bf16_f32 v48, v96, v97\n"
      "v_cvt_pk_bf16_f32 v49, v98, v99\n"
      "v_cvt_pk_bf16_f32 v50, v100, v101\n"
      "v_cvt_pk_bf16_f32 v51, v102, v103\n"
      "v_add_u32 v17, 40960, v0\n"
      "s_barrier\n"
      "v_mov_b32 v11, 0\n"
      "ds_write_b32 v13, v11\n"
      "s_waitcnt lgkmcnt(0)\n"
      "s_barrier\n"
      "s_add_u32 s6, s36, 1548288\n"
      "s_add_u32 s36, s36, 4294942720\n"
      "s_add_u32 s42, s37, 4294635520\n"
      "s_add_u32 s37, s37, 4294635520\n"
      "s_add_u32 s36, s36, 24576\n"
      "s_min_u32 s36, s36, s6\n"
      "buffer_load_dword v18, v2, s[8:11], s36 offen sc1\n"
      "s_mov_b32 s40, s36\n"
      "s_add_u32 m0, s38, 0\n"
      "s_waitcnt vmcnt(0)\n"
      "v_cmp_ne_u32 vcc, s21, v18\n"
      "s_cbranch_vccnz .Lsp_g0_1\n"
      ".Lbk_g0_1:\n"
      "buffer_load_dwordx4 v0, s[8:11], s40 offen offset:0 sc1 lds\n"
      "s_add_u32 s36, s36, 24576\n"
      "buffer_load_dwordx4 v0, s[8:11], s40 offen offset:1024 sc1 lds\n"
      "buffer_load_dwordx4 v0, s[8:11], s40 offen offset:2048 sc1 lds\n"
      "buffer_load_dwordx4 v0, s[8:11], s40 offen offset:3072 sc1 lds\n"
      "s_add_u32 m0, s39, 0\n"
      "s_min_u32 s36, s36, s6\n"
      "buffer_load_dwordx4 v1, s[8:11], s40 offen offset:0 sc1 lds\n"
      "buffer_load_dword v18, v2, s[8:11], s36 offen sc1\n"
      "v_add_u32 v11, 1, v11\n"
      "s_mov_b32 s40, s36\n"
      "s_add_u32 m0, s38, 20480\n"
      "s_waitcnt vmcnt(1)\n"
      "ds_write_b32 v13, v11\n"
      "s_waitcnt vmcnt(0)\n"
      "v_cmp_ne_u32 vcc, s21, v18\n"
      "s_cbranch_vccnz .Lsp_g0_2\n"
      ".Lbk_g0_2:\n"
      "buffer_load_dwordx4 v0, s[8:11], s40 offen offset:0 sc1 lds\n"
      "ds_read_b32 v12, v14\n"
      "s_add_u32 s36, s36, 24576\n"
      "buffer_load_dwordx4 v0, s[8:11], s40 offen offset:1024 sc1 lds\n"
      "s_waitcnt lgkmcnt(0)\n"
      "v_cmp_gt_i32 vcc, v11, v12\n"
      "buffer_load_dwordx4 v0, s[8:11], s40 offen offset:2048 sc1 lds\n"
      "s_cbranch_vccnz .Lsp_gy0_3\n"
      ".Lbk_gy0_3:\n"
      "ds_read2st64_b64 v[168:171], v4 offset0:0 offset1:1\n"
      "ds_read_b128 v[52:55], v0 offset:0\n"
      "buffer_load_dwordx4 v0, s[8:11], s40 offen offset:3072 sc1 lds\n"
      "s_add_u32 m0, s39, 20480\n"
      "ds_read_b128 v[64:67], v0 offset:3072\n"
      "s_min_u32 s36, s36, s6\n"
      "ds_read_b128 v[56:59], v0 offset:1024\n"
      "buffer_load_dwordx4 v1, s[8:11], s40 offen offset:0 sc1 lds\n"
      "ds_read_b128 v[68:71], v0 offset:4096\n"
      "ds_read_b128 v[76:79], v0 offset:6144\n"
      "ds_read_b128 v[72:75], v0 offset:5120\n"
      "buffer_load_dword v18, v2, s[8:11], s36 offen sc1\n"
      "ds_read_b128 v[60:63], v0 offset:2048\n"
      "buffer_load_dword v16, v13, s[8:11], s31 offen\n"
      "ds_read_b128 v[84:87], v0 offset:8192\n"
      "ds_read_b128 v[80:83], v0 offset:7168\n"
      "ds_read_b128 v[96:99], v0 offset:11264\n"
      "s_mov_b32 s20, 15\n"
      "s_waitcnt lgkmcnt(0)\n"
      "s_nop 4\n"
      "s_branch .Lg_loop\n"
      ".p2align 12\n"
      ".Lg_p0:\n"
      "s_branch .Lg_pr\n"
      ".p2align 5\n"
      ".Lg_loop:\n"
      "v_mfma_f32_16x16x32_bf16 v[124:127], a[0:3], v[52:55], 0\n"
      "ds_read_b128 v[92:95], v0 offset:10240\n"
      "v_add_f32 v169, v169, v171\n"
      "v_mfma_f32_16x16x32_bf16 v[124:127], a[4:7], v[56:59], v[124:127]\n"
      "s_waitcnt lgkmcnt(4)\n"
      "ds_read_b128 v[88:91], v0 offset:9216\n"
      "v_mul_f32 v161, v133, v161\n"
      "v_mfma_f32_16x16x32_bf16 v[124:127], a[8:11], v[60:63], v[124:127]\n"
      "ds_read_b128 v[112:115], v0 offset:15360\n"
      "v_mul_f32 v163, v135, v163\n"
      "v_mfma_f32_16x16x32_bf16 v[124:127], a[12:15], v[64:67], v[124:127]\n"
      "ds_read_b128 v[100:103], v0 offset:12288\n"
      "v_mul_f32 v139, v139, v163\n"
      "v_mfma_f32_16x16x32_bf16 v[124:127], a[16:19], v[68:71], v[124:127]\n"
      "ds_read_b128 v[104:107], v0 offset:13312\n"
      "v_mul_f32 v131, v131, v159\n"
      "v_mfma_f32_16x16x32_bf16 v[124:127], a[20:23], v[72:75], v[124:127]\n"
      "ds_read_b128 v[108:111], v0 offset:14336\n"
      "v_mfma_f32_16x16x32_bf16 v[124:127], a[24:27], v[76:79], v[124:127]\n"
      "s_waitcnt lgkmcnt(6)\n"
      "ds_read_b128 v[120:123], v0 offset:17408\n"
      "v_sub_f32 v176, -v168, v170\n"
      "v_mfma_f32_16x16x32_bf16 v[124:127], a[28:31], v[80:83], v[124:127]\n"
      "v_rcp_f32 v160, v160\n"
      "v_mfma_f32_16x16x32_bf16 v[124:127], a[32:35], v[84:87], v[124:127]\n"
      "s_waitcnt lgkmcnt(5)\n"
      "ds_read_b128 v[116:119], v0 offset:16384\n"
      "v_fma_f32 v169, -v176, v176, v169\n"
      "v_mfma_f32_16x16x32_bf16 v[124:127], a[36:39], v[88:91], v[124:127]\n"
      "v_mul_f32 v160, v132, v160\n"
      "v_fma_f32 v162, v162, v177, v177\n"
      "v_mfma_f32_16x16x32_bf16 v[124:127], a[40:43], v[92:95], v[124:127]\n"
      "v_mul_f32 v130, v130, v158\n"
      "v_mul_f32 v128, v128, v156\n"
      "v_mfma_f32_16x16x32_bf16 v[124:127], a[44:47], v[96:99], v[124:127]\n"
      "s_waitcnt lgkmcnt(3)\n"
      "v_rcp_f32 v162, v162\n"
      "v_mfma_f32_16x16x32_bf16 v[124:127], a[48:51], v[100:103], v[124:127]\n"
      "v_cvt_pk_bf16_f32 v165, v130, v131\n"
      "v_mul_f32 v162, v134, v162\n"
      "v_mfma_f32_16x16x32_bf16 v[124:127], a[52:55], v[104:107], v[124:127]\n"
      "s_waitcnt lgkmcnt(1)\n"
      "v_mul_f32 v136, v136, v160\n"
      "v_mul_f32 v138, v138, v162\n"
      "v_mfma_f32_16x16x32_bf16 v[124:127], a[56:59], v[108:111], v[124:127]\n"
      "v_mul_f32 v137, v137, v161\n"
      "v_cvt_pk_bf16_f32 v167, v138, v139\n"
      "v_mfma_f32_16x16x32_bf16 v[124:127], a[60:63], v[112:115], v[124:127]\n"
      "s_waitcnt lgkmcnt(0)\n"
      "v_max_f32 v177, 0x3727c5ac, v169\n"
      "v_mfma_f32_16x16x32_bf16 v[124:127], a[64:67], v[116:119], v[124:127]\n"
      "s_add_u32 s37, s37, 165888\n"
      "v_cvt_pk_bf16_f32 v164, v128, v129\n"
      "v_mfma_f32_16x16x32_bf16 v[124:127], a[68:71], v[120:123], v[124:127]\n"
      "v_cvt_pk_bf16_f32 v166, v136, v137\n"
      "v_add_u32 v11, 1, v11\n"
      "v_mfma_f32_16x16x32_bf16 v[132:135], a[144:147], v[52:55], 0\n"
      "buffer_store_dwordx4 v[164:167], v0, s[8:11], s37 offen sc1\n"
      "v_mfma_f32_16x16x32_bf16 v[132:135], a[148:151], v[56:59], v[132:135]\n"
      "v_rsq_f32 v170, v177\n"
      "v_mfma_f32_16x16x32_bf16 v[132:135], a[152:155], v[60:63], v[132:135]\n"
      "v_mul_f32 v178, 0xc0135761, v170\n"
      "v_mul_f32 v170, v170, v170\n"
      "v_mfma_f32_16x16x32_bf16 v[132:135], a[156:159], v[64:67], v[132:135]\n"
      "v_mul_f32 v171, 0x3d372713, v178\n"
      "v_mul_f32 v157, v125, v125\n"
      "v_mfma_f32_16x16x32_bf16 v[132:135], a[160:163], v[68:71], v[132:135]\n"
      "v_mul_f32 v179, v171, v170\n"
      "v_mul_f32 v158, v126, v126\n"
      "v_mfma_f32_16x16x32_bf16 v[132:135], a[164:167], v[72:75], v[132:135]\n"
      "v_fma_f32 v157, v157, v179, v178\n"
      "s_mov_b32 s40, s36\n"
      "v_mfma_f32_16x16x32_bf16 v[132:135], a[168:171], v[76:79], v[132:135]\n"
      "v_mul_f32 v157, v125, v157\n"
      "s_add_u32 m0, s38, 40960\n"
      "v_mfma_f32_16x16x32_bf16 v[132:135], a[172:175], v[80:83], v[132:135]\n"
      "v_exp_f32 v157, v157\n"
      "v_mfma_f32_16x16x32_bf16 v[132:135], a[176:179], v[84:87], v[132:135]\n"
      "v_fma_f32 v158, v158, v179, v178\n"
      "v_fma_f32 v157, v157, v177, v177\n"
      "v_mfma_f32_16x16x32_bf16 v[132:135], a[180:183], v[88:91], v[132:135]\n"
      "v_mfma_f32_16x16x32_bf16 v[132:135], a[184:187], v[92:95], v[132:135]\n"
      "v_mul_f32 v156, v124, v124\n"
      "v_mul_f32 v158, v126, v158\n"
      "v_mfma_f32_16x16x32_bf16 v[132:135], a[188:191], v[96:99], v[132:135]\n"
      "v_mul_f32 v159, v127, v127\n"
      "v_fma_f32 v156, v156, v179, v178\n"
      "v_mfma_f32_16x16x32_bf16 v[132:135], a[192:195], v[100:103], v[132:135]\n"
      "v_fma_f32 v159, v159, v179, v178\n"
      "v_mul_f32 v156, v124, v156\n"
      "v_mfma_f32_16x16x32_bf16 v[132:135], a[196:199], v[104:107], v[132:135]\n"
      "v_exp_f32 v158, v158\n"
      "v_mfma_f32_16x16x32_bf16 v[132:135], a[200:203], v[108:111], v[132:135]\n"
      "v_fma_f32 v158, v158, v177, v177\n"
      "v_mul_f32 v159, v127, v159\n"
      "v_mfma_f32_16x16x32_bf16 v[132:135], a[204:207], v[112:115], v[132:135]\n"
      "v_exp_f32 v156, v156\n"
      "v_mfma_f32_16x16x32_bf16 v[132:135], a[208:211], v[116:119], v[132:135]\n"
      "s_add_u32 s42, s42, 165888\n"
      "v_fma_f32 v156, v156, v177, v177\n"
      "v_mfma_f32_16x16x32_bf16 v[132:135], a[212:215], v[120:123], v[132:135]\n"
      "s_waitcnt vmcnt(3)\n"
      "ds_write_b32 v13, v11\n"
      "v_mfma_f32_16x16x32_bf16 v[128:131], a[72:75], v[52:55], 0\n"
      "v_exp_f32 v159, v159\n"
      "v_mfma_f32_16x16x32_bf16 v[128:131], a[76:79], v[56:59], v[128:131]\n"
      "s_waitcnt vmcnt(2)\n"
      "v_fma_f32 v159, v159, v177, v177\n"
      "v_cmp_ne_u32 vcc, s21, v18\n"
      "v_mfma_f32_16x16x32_bf16 v[128:131], a[80:83], v[60:63], v[128:131]\n"
      "v_mul_f32 v161, v133, v133\n"
      "v_fma_f32 v161, v161, v179, v178\n"
      "v_mfma_f32_16x16x32_bf16 v[128:131], a[84:87], v[64:67], v[128:131]\n"
      "v_mul_f32 v163, v135, v135\n"
      "v_mul_f32 v161, v133, v161\n"
      "v_mfma_f32_16x16x32_bf16 v[128:131], a[88:91], v[68:71], v[128:131]\n"
      "v_mul_f32 v160, v132, v132\n"
      "v_fma_f32 v163, v163, v179, v178\n"
      "v_mfma_f32_16x16x32_bf16 v[128:131], a[92:95], v[72:75], v[128:131]\n"
      "s_cbranch_vccnz .Lsp_g0_4\n"
      ".Lbk_g0_4:\n"
      "v_rcp_f32 v156, v156\n"
      "v_mfma_f32_16x16x32_bf16 v[128:131], a[96:99], v[76:79], v[128:131]\n"
      "buffer_load_dwordx4 v0, s[8:11], s40 offen offset:0 sc1 lds\n"
      "v_mfma_f32_16x16x32_bf16 v[128:131], a[100:103], v[80:83], v[128:131]\n"
      "v_rcp_f32 v159, v159\n"
      "v_mfma_f32_16x16x32_bf16 v[128:131], a[104:107], v[84:87], v[128:131]\n"
      "ds_read_b32 v12, v14\n"
      "v_fma_f32 v160, v160, v179, v178\n"
      "v_mfma_f32_16x16x32_bf16 v[128:131], a[108:111], v[88:91], v[128:131]\n"
      "s_add_u32 s36, s36, 24576\n"
      "v_mul_f32 v162, v134, v134\n"
      "v_mul_f32 v163, v135, v163\n"
      "v_mfma_f32_16x16x32_bf16 v[128:131], a[112:115], v[92:95], v[128:131]\n"
      "v_exp_f32 v163, v163\n"
      "v_mfma_f32_16x16x32_bf16 v[128:131], a[116:119], v[96:99], v[128:131]\n"
      "buffer_load_dwordx4 v0, s[8:11], s40 offen offset:1024 sc1 lds\n"
      "v_mfma_f32_16x16x32_bf16 v[128:131], a[120:123], v[100:103], v[128:131]\n"
      "v_rcp_f32_e64 v158, v158\n"
      "v_mfma_f32_16x16x32_bf16 v[128:131], a[124:127], v[104:107], v[128:131]\n"
      "s_waitcnt lgkmcnt(0)\n"
      "v_mul_f32_e64 v160, v132, v160\n"
      "v_cmp_gt_i32 vcc, v11, v12\n"
      "v_mfma_f32_16x16x32_bf16 v[128:131], a[128:131], v[108:111], v[128:131]\n"
      "v_fma_f32 v162, v162, v179, v178\n"
      "v_mul_f32_e64 v162, v134, v162\n"
      "v_mfma_f32_16x16x32_bf16 v[128:131], a[132:135], v[112:115], v[128:131]\n"
      "buffer_load_dwordx4 v0, s[8:11], s40 offen offset:2048 sc1 lds\n"
      "v_mfma_f32_16x16x32_bf16 v[128:131], a[136:139], v[116:119], v[128:131]\n"
      "v_exp_f32_e64 v162, v162\n"
      "v_mfma_f32_16x16x32_bf16 v[128:131], a[140:143], v[120:123], v[128:131]\n"
      "s_cbranch_vccnz .Lsp_gy0_5\n"
      ".Lbk_gy0_5:\n"
      "ds_read2st64_b64 v[168:171], v4 offset0:40 offset1:41\n"
      "v_mfma_f32_16x16x32_bf16 v[136:139], a[216:219], v[52:55], 0\n"
      "ds_read_b128 v[52:55], v0 offset:20480\n"
      "v_fma_f32 v163, v163, v177, v177\n"
      "v_mfma_f32_16x16x32_bf16 v[136:139], a[220:223], v[56:59], v[136:139]\n"
      "v_rcp_f32 v157, v157\n"
      "v_mfma_f32_16x16x32_bf16 v[136:139], a[224:227], v[60:63], v[136:139]\n"
      "buffer_load_dwordx4 v0, s[8:11], s40 offen offset:3072 sc1 lds\n"
      "v_mfma_f32_16x16x32_bf16 v[136:139], a[228:231], v[64:67], v[136:139]\n"
      "s_add_u32 m0, s39, 40960\n"
      "ds_read_b128 v[64:67], v0 offset:23552\n"
      "v_mul_f32 v158, v126, v158\n"
      "v_mfma_f32_16x16x32_bf16 v[136:139], a[232:235], v[68:71], v[136:139]\n"
      "s_min_u32 s36, s36, s6\n"
      "v_rcp_f32 v163, v163\n"
      "v_mfma_f32_16x16x32_bf16 v[136:139], a[236:239], v[72:75], v[136:139]\n"
      "ds_read_b128 v[56:59], v0 offset:21504\n"
      "v_mul_f32 v157, v125, v157\n"
      "v_mfma_f32_16x16x32_bf16 v[136:139], a[240:243], v[76:79], v[136:139]\n"
      "buffer_load_dwordx4 v1, s[8:11], s40 offen offset:0 sc1 lds\n"
      "v_mfma_f32_16x16x32_bf16 v[136:139], a[244:247], v[80:83], v[136:139]\n"
      "v_exp_f32 v161, v161\n"
      "v_mfma_f32_16x16x32_bf16 v[136:139], a[248:251], v[84:87], v[136:139]\n"
      "ds_read_b128 v[68:71], v0 offset:24576\n"
      "ds_read_b128 v[76:79], v0 offset:26624\n"
      "v_mfma_f32_16x16x32_bf16 v[136:139], a[252:255], v[88:91], v[136:139]\n"
      "v_exp_f32 v160, v160\n"
      "v_mfma_f32_16x16x32_bf16 v[136:139], v[20:23], v[92:95], v[136:139]\n"
      "ds_read_b128 v[72:75], v0 offset:25600\n"
      "v_mul_f32 v129, v129, v157\n"
      "v_mfma_f32_16x16x32_bf16 v[136:139], v[24:27], v[96:99], v[136:139]\n"
      "buffer_load_dword v18, v2, s[8:11], s36 offen sc1\n"
      "v_mfma_f32_16x16x32_bf16 v[136:139], v[28:31], v[100:103], v[136:139]\n"
      "ds_read_b128 v[60:63], v0 offset:22528\n"
      "v_fma_f32 v161, v161, v177, v177\n"
      "v_mfma_f32_16x16x32_bf16 v[136:139], v[32:35], v[104:107], v[136:139]\n"
      "s_waitcnt vmcnt(6) lgkmcnt(4)\n"
      "buffer_store_dword v15, v3, s[8:11], s42 offen sc1\n"
      "v_mfma_f32_16x16x32_bf16 v[136:139], v[36:39], v[108:111], v[136:139]\n"
      "ds_read_b128 v[84:87], v0 offset:28672\n"
      "v_fma_f32 v160, v160, v177, v177\n"
      "v_mfma_f32_16x16x32_bf16 v[136:139], v[40:43], v[112:115], v[136:139]\n"
      "v_rcp_f32 v161, v161\n"
      "v_mfma_f32_16x16x32_bf16 v[136:139], v[44:47], v[116:119], v[136:139]\n"
      "ds_read_b128 v[80:83], v0 offset:27648\n"
      "v_mul_f32 v159, v127, v159\n"
      "v_mfma_f32_16x16x32_bf16 v[136:139], v[48:51], v[120:123], v[136:139]\n"
      "ds_read_b128 v[96:99], v0 offset:31744\n"
      "v_mul_f32 v156, v124, v156\n"
      "v_mfma_f32_16x16x32_bf16 v[124:127], a[0:3], v[52:55], 0\n"
      "ds_read_b128 v[92:95], v0 offset:30720\n"
      "v_add_f32 v169, v169, v171\n"
      "v_mfma_f32_16x16x32_bf16 v[124:127], a[4:7], v[56:59], v[124:127]\n"
      "s_waitcnt lgkmcnt(4)\n"
      "ds_read_b128 v[88:91], v0 offset:29696\n"
      "v_mul_f32 v161, v133, v161\n"
      "v_mfma_f32_16x16x32_bf16 v[124:127], a[8:11], v[60:63], v[124:127]\n"
      "ds_read_b128 v[112:115], v0 offset:35840\n"
      "v_mul_f32 v163, v135, v163\n"
      "v_mfma_f32_16x16x32_bf16 v[124:127], a[12:15], v[64:67], v[124:127]\n"
      "ds_read_b128 v[100:103], v0 offset:32768\n"
      "v_mul_f32 v139, v139, v163\n"
      "v_mfma_f32_16x16x32_bf16 v[124:127], a[16:19], v[68:71], v[124:127]\n"
      "ds_read_b128 v[104:107], v0 offset:33792\n"
      "v_mul_f32 v131, v131, v159\n"
      "v_mfma_f32_16x16x32_bf16 v[124:127], a[20:23], v[72:75], v[124:127]\n"
      "ds_read_b128 v[108:111], v0 offset:34816\n"
      "v_mfma_f32_16x16x32_bf16 v[124:127], a[24:27], v[76:79], v[124:127]\n"
      "s_waitcnt lgkmcnt(6)\n"
      "ds_read_b128 v[120:123], v0 offset:37888\n"
      "v_sub_f32 v176, -v168, v170\n"
      "v_mfma_f32_16x16x32_bf16 v[124:127], a[28:31], v[80:83], v[124:127]\n"
      "v_rcp_f32 v160, v160\n"
      "v_mfma_f32_16x16x32_bf16 v[124:127], a[32:35], v[84:87], v[124:127]\n"
      "s_waitcnt lgkmcnt(5)\n"
      "ds_read_b128 v[116:119], v0 offset:36864\n"
      "v_fma_f32 v169, -v176, v176, v169\n"
      "v_mfma_f32_16x16x32_bf16 v[124:127], a[36:39], v[88:91], v[124:127]\n"
      "v_mul_f32 v160, v132, v160\n"
      "v_fma_f32 v162, v162, v177, v177\n"
      "v_mfma_f32_16x16x32_bf16 v[124:127], a[40:43], v[92:95], v[124:127]\n"
      "v_mul_f32 v130, v130, v158\n"
      "v_mul_f32 v128, v128, v156\n"
      "v_mfma_f32_16x16x32_bf16 v[124:127], a[44:47], v[96:99], v[124:127]\n"
      "s_waitcnt lgkmcnt(3)\n"
      "v_rcp_f32 v162, v162\n"
      "v_mfma_f32_16x16x32_bf16 v[124:127], a[48:51], v[100:103], v[124:127]\n"
      "v_cvt_pk_bf16_f32 v165, v130, v131\n"
      "v_mul_f32 v162, v134, v162\n"
      "v_mfma_f32_16x16x32_bf16 v[124:127], a[52:55], v[104:107], v[124:127]\n"
      "s_waitcnt lgkmcnt(1)\n"
      "v_mul_f32 v136, v136, v160\n"
      "v_mul_f32 v138, v138, v162\n"
      "v_mfma_f32_16x16x32_bf16 v[124:127], a[56:59], v[108:111], v[124:127]\n"
      "v_mul_f32 v137, v137, v161\n"
      "v_cvt_pk_bf16_f32 v167, v138, v139\n"
      "v_mfma_f32_16x16x32_bf16 v[124:127], a[60:63], v[112:115], v[124:127]\n"
      "s_waitcnt lgkmcnt(0)\n"
      "v_max_f32 v177, 0x3727c5ac, v169\n"
      "v_mfma_f32_16x16x32_bf16 v[124:127], a[64:67], v[116:119], v[124:127]\n"
      "s_add_u32 s37, s37, 165888\n"
      "v_cvt_pk_bf16_f32 v164, v128, v129\n"
      "v_mfma_f32_16x16x32_bf16 v[124:127], a[68:71], v[120:123], v[124:127]\n"
      "v_cvt_pk_bf16_f32 v166, v136, v137\n"
      "v_add_u32 v11, 1, v11\n"
      "v_mfma_f32_16x16x32_bf16 v[132:135], a[144:147], v[52:55], 0\n"
      "buffer_store_dwordx4 v[164:167], v0, s[8:11], s37 offen sc1\n"
      "v_mfma_f32_16x16x32_bf16 v[132:135], a[148:151], v[56:59], v[132:135]\n"
      "v_rsq_f32 v170, v177\n"
      "v_mfma_f32_16x16x32_bf16 v[132:135], a[152:155], v[60:63], v[132:135]\n"
      "v_mul_f32 v178, 0xc0135761, v170\n"
      "v_mul_f32 v170, v170, v170\n"
      "v_mfma_f32_16x16x32_bf16 v[132:135], a[156:159], v[64:67], v[132:135]\n"
      "v_mul_f32 v171, 0x3d372713, v178\n"
      "v_mul_f32 v157, v125, v125\n"
      "v_mfma_f32_16x16x32_bf16 v[132:135], a[160:163], v[68:71], v[132:135]\n"
      "v_mul_f32 v179, v171, v170\n"
      "v_mul_f32 v158, v126, v126\n"
      "v_mfma_f32_16x16x32_bf16 v[132:135], a[164:167], v[72:75], v[132:135]\n"
      "v_fma_f32 v157, v157, v179, v178\n"
      "s_mov_b32 s40, s36\n"
      "v_mfma_f32_16x16x32_bf16 v[132:135], a[168:171], v[76:79], v[132:135]\n"
      "v_mul_f32 v157, v125, v157\n"
      "s_add_u32 m0, s38, 61440\n"
      "v_mfma_f32_16x16x32_bf16 v[132:135], a[172:175], v[80:83], v[132:135]\n"
      "v_exp_f32 v157, v157\n"
      "v_mfma_f32_16x16x32_bf16 v[132:135], a[176:179], v[84:87], v[132:135]\n"
      "v_fma_f32 v158, v158, v179, v178\n"
      "v_fma_f32 v157, v157, v177, v177\n"
      "v_mfma_f32_16x16x32_bf16 v[132:135], a[180:183], v[88:91], v[132:135]\n"
      "v_mfma_f32_16x16x32_bf16 v[132:135], a[184:187], v[92:95], v[132:135]\n"
      "v_mul_f32 v156, v124, v124\n"
      "v_mul_f32 v158, v126, v158\n"
      "v_mfma_f32_16x16x32_bf16 v[132:135], a[188:191], v[96:99], v[132:135]\n"
      "v_mul_f32 v159, v127, v127\n"
      "v_fma_f32 v156, v156, v179, v178\n"
      "v_mfma_f32_16x16x32_bf16 v[132:135], a[192:195], v[100:103], v[132:135]\n"
      "v_fma_f32 v159, v159, v179, v178\n"
      "v_mul_f32 v156, v124, v156\n"
      "v_mfma_f32_16x16x32_bf16 v[132:135], a[196:199], v[104:107], v[132:135]\n"
      "v_exp_f32 v158, v158\n"
      "v_mfma_f32_16x16x32_bf16 v[132:135], a[200:203], v[108:111], v[132:135]\n"
      "v_fma_f32 v158, v158, v177, v177\n"
      "v_mul_f32 v159, v127, v159\n"
      "v_mfma_f32_16x16x32_bf16 v[132:135], a[204:207], v[112:115], v[132:135]\n"
      "v_exp_f32 v156, v156\n"
      "v_mfma_f32_16x16x32_bf16 v[132:135], a[208:211], v[116:119], v[132:135]\n"
      "s_add_u32 s42, s42, 165888\n"
      "v_fma_f32 v156, v156, v177, v177\n"
      "v_mfma_f32_16x16x32_bf16 v[132:135], a[212:215], v[120:123], v[132:135]\n"
      "s_waitcnt vmcnt(3)\n"
      "ds_write_b32 v13, v11\n"
      "v_mfma_f32_16x16x32_bf16 v[128:131], a[72:75], v[52:55], 0\n"
      "v_exp_f32 v159, v159\n"
      "v_mfma_f32_16x16x32_bf16 v[128:131], a[76:79], v[56:59], v[128:131]\n"
      "s_waitcnt vmcnt(2)\n"
      "v_fma_f32 v159, v159, v177, v177\n"
      "v_cmp_ne_u32 vcc, s21, v18\n"
      "v_mfma_f32_16x16x32_bf16 v[128:131], a[80:83], v[60:63], v[128:131]\n"
      "v_mul_f32 v161, v133, v133\n"
      "v_fma_f32 v161, v161, v179, v178\n"
      "v_mfma_f32_16x16x32_bf16 v[128:131], a[84:87], v[64:67], v[128:131]\n"
      "v_mul_f32 v163, v135, v135\n"
      "v_mul_f32 v161, v133, v161\n"
      "v_mfma_f32_16x16x32_bf16 v[128:131], a[88:91], v[68:71], v[128:131]\n"
      "v_mul_f32 v160, v132, v132\n"
      "v_fma_f32 v163, v163, v179, v178\n"
      "v_mfma_f32_16x16x32_bf16 v[128:131], a[92:95], v[72:75], v[128:131]\n"
      "s_cbranch_vccnz .Lsp_g0_6\n"
      ".Lbk_g0_6:\n"
      "v_rcp_f32 v156, v156\n"
      "v_mfma_f32_16x16x32_bf16 v[128:131], a[96:99], v[76:79], v[128:131]\n"
      "buffer_load_dwordx4 v0, s[8:11], s40 offen offset:0 sc1 lds\n"
      "v_mfma_f32_16x16x32_bf16 v[128:131], a[100:103], v[80:83], v[128:131]\n"
      "v_rcp_f32 v159, v159\n"
      "v_mfma_f32_16x16x32_bf16 v[128:131], a[104:107], v[84:87], v[128:131]\n"
      "ds_read_b32 v12, v14\n"
      "v_fma_f32 v160, v160, v179, v178\n"
      "v_mfma_f32_16x16x32_bf16 v[128:131], a[108:111], v[88:91], v[128:131]\n"
      "s_add_u32 s36, s36, 24576\n"
      "v_mul_f32 v162, v134, v134\n"
      "v_mul_f32 v163, v135, v163\n"
      "v_mfma_f32_16x16x32_bf16 v[128:131], a[112:115], v[92:95], v[128:131]\n"
      "v_exp_f32 v163, v163\n"
      "v_mfma_f32_16x16x32_bf16 v[128:131], a[116:119], v[96:99], v[128:131]\n"
      "buffer_load_dwordx4 v0, s[8:11], s40 offen offset:1024 sc1 lds\n"
      "v_mfma_f32_16x16x32_bf16 v[128:131], a[120:123], v[100:103], v[128:131]\n"
      "v_rcp_f32 v158, v158\n"
      "v_mfma_f32_16x16x32_bf16 v[128:131], a[124:127], v[104:107], v[128:131]\n"
      "s_waitcnt lgkmcnt(0)\n"
      "v_mul_f32 v160, v132, v160\n"
      "v_cmp_gt_i32 vcc, v11, v12\n"
      "v_mfma_f32_16x16x32_bf16 v[128:131], a[128:131], v[108:111], v[128:131]\n"
      "v_fma_f32 v162, v162, v179, v178\n"
      "v_mul_f32 v162, v134, v162\n"
      "v_mfma_f32_16x16x32_bf16 v[128:131], a[132:135], v[112:115], v[128:131]\n"
      "buffer_load_dwordx4 v0, s[8:11], s40 offen offset:2048 sc1 lds\n"
      "v_mfma_f32_16x16x32_bf16 v[128:131], a[136:139], v[116:119], v[128:131]\n"
      "v_exp_f32 v162, v162\n"
      "v_mfma_f32_16x16x32_bf16 v[128:131], a[140:143], v[120:123], v[128:131]\n"
      "s_cbranch_vccnz .Lsp_gy0_7\n"
      ".Lbk_gy0_7:\n"
      "ds_read2st64_b64 v[168:171], v4 offset0:80 offset1:81\n"
      "v_mfma_f32_16x16x32_bf16 v[136:139], a[216:219], v[52:55], 0\n"
      "ds_read_b128 v[52:55], v17 offset:0\n"
      "v_fma_f32 v163, v163, v177, v177\n"
      "v_mfma_f32_16x16x32_bf16 v[136:139], a[220:223], v[56:59], v[136:139]\n"
      "v_rcp_f32 v157, v157\n"
      "v_mfma_f32_16x16x32_bf16 v[136:139], a[224:227], v[60:63], v[136:139]\n"
      "buffer_load_dwordx4 v0, s[8:11], s40 offen offset:3072 sc1 lds\n"
      "v_mfma_f32_16x16x32_bf16 v[136:139], a[228:231], v[64:67], v[136:139]\n"
      "s_add_u32 m0, s39, 61440\n"
      "ds_read_b128 v[64:67], v17 offset:3072\n"
      "v_mul_f32 v158, v126, v158\n"
      "v_mfma_f32_16x16x32_bf16 v[136:139], a[232:235], v[68:71], v[136:139]\n"
      "s_min_u32 s36, s36, s6\n"
      "v_rcp_f32 v163, v163\n"
      "v_mfma_f32_16x16x32_bf16 v[136:139], a[236:239], v[72:75], v[136:139]\n"
      "ds_read_b128 v[56:59], v17 offset:1024\n"
      "v_mul_f32 v157, v125, v157\n"
      "v_mfma_f32_16x16x32_bf16 v[136:139], a[240:243], v[76:79], v[136:139]\n"
      "buffer_load_dwordx4 v1, s[8:11], s40 offen offset:0 sc1 lds\n"
      "v_mfma_f32_16x16x32_bf16 v[136:139], a[244:247], v[80:83], v[136:139]\n"
      "v_exp_f32 v161, v161\n"
      "v_mfma_f32_16x16x32_bf16 v[136:139], a[248:251], v[84:87], v[136:139]\n"
      "ds_read_b128 v[68:71], v17 offset:4096\n"
      "ds_read_b128 v[76:79], v17 offset:6144\n"
      "v_mfma_f32_16x16x32_bf16 v[136:139], a[252:255], v[88:91], v[136:139]\n"
      "v_exp_f32 v160, v160\n"
      "v_mfma_f32_16x16x32_bf16 v[136:139], v[20:23], v[92:95], v[136:139]\n"
      "ds_read_b128 v[72:75], v17 offset:5120\n"
      "v_mul_f32 v129, v129, v157\n"
      "v_mfma_f32_16x16x32_bf16 v[136:139], v[24:27], v[96:99], v[136:139]\n"
      "buffer_load_dword v18, v2, s[8:11], s36 offen sc1\n"
      "v_mfma_f32_16x16x32_bf16 v[136:139], v[28:31], v[100:103], v[136:139]\n"
      "ds_read_b128 v[60:63], v17 offset:2048\n"
      "v_fma_f32 v161, v161, v177, v177\n"
      "v_mfma_f32_16x16x32_bf16 v[136:139], v[32:35], v[104:107], v[136:139]\n"
      "s_waitcnt vmcnt(6) lgkmcnt(4)\n"
      "buffer_store_dword v15, v3, s[8:11], s42 offen sc1\n"
      "v_mfma_f32_16x16x32_bf16 v[136:139], v[36:39], v[108:111], v[136:139]\n"
      "ds_read_b128 v[84:87], v17 offset:8192\n"
      "v_fma_f32 v160, v160, v177, v177\n"
      "v_mfma_f32_16x16x32_bf16 v[136:139], v[40:43], v[112:115], v[136:139]\n"
      "v_rcp_f32 v161, v161\n"
      "v_mfma_f32_16x16x32_bf16 v[136:139], v[44:47], v[116:119], v[136:139]\n"
      "ds_read_b128 v[80:83], v17 offset:7168\n"
      "v_mul_f32 v159, v127, v159\n"
      "v_mfma_f32_16x16x32_bf16 v[136:139], v[48:51], v[120:123], v[136:139]\n"
      "ds_read_b128 v[96:99], v17 offset:11264\n"
      "v_mul_f32 v156, v124, v156\n"
      "v_mfma_f32_16x16x32_bf16 v[124:127], a[0:3], v[52:55], 0\n"
      "ds_read_b128 v[92:95], v17 offset:10240\n"
      "v_add_f32 v169, v169, v171\n"
      "v_mfma_f32_16x16x32_bf16 v[124:127], a[4:7], v[56:59], v[124:127]\n"
      "s_waitcnt lgkmcnt(4)\n"
      "ds_read_b128 v[88:91], v17 offset:9216\n"
      "v_mul_f32 v161, v133, v161\n"
      "v_mfma_f32_16x16x32_bf16 v[124:127], a[8:11], v[60:63], v[124:127]\n"
      "ds_read_b128 v[112:115], v17 offset:15360\n"
      "v_mul_f32 v163, v135, v163\n"
      "v_mfma_f32_16x16x32_bf16 v[124:127], a[12:15], v[64:67], v[124:127]\n"
      "ds_read_b128 v[100:103], v17 offset:12288\n"
      "v_mul_f32 v139, v139, v163\n"
      "v_mfma_f32_16x16x32_bf16 v[124:127], a[16:19], v[68:71], v[124:127]\n"
      "ds_read_b128 v[104:107], v17 offset:13312\n"
      "v_mul_f32 v131, v131, v159\n"
      "v_mfma_f32_16x16x32_bf16 v[124:127], a[20:23], v[72:75], v[124:127]\n"
      "ds_read_b128 v[108:111], v17 offset:14336\n"
      "v_mfma_f32_16x16x32_bf16 v[124:127], a[24:27], v[76:79], v[124:127]\n"
      "s_waitcnt lgkmcnt(6)\n"
      "ds_read_b128 v[120:123], v17 offset:17408\n"
      "v_sub_f32 v176, -v168, v170\n"
      "v_mfma_f32_16x16x32_bf16 v[124:127], a[28:31], v[80:83], v[124:127]\n"
      "v_rcp_f32 v160, v160\n"
      "v_mfma_f32_16x16x32_bf16 v[124:127], a[32:35], v[84:87], v[124:127]\n"
      "s_waitcnt lgkmcnt(5)\n"
      "ds_read_b128 v[116:119], v17 offset:16384\n"
      "v_fma_f32 v169, -v176, v176, v169\n"
      "v_mfma_f32_16x16x32_bf16 v[124:127], a[36:39], v[88:91], v[124:127]\n"
      "v_mul_f32 v160, v132, v160\n"
      "v_fma_f32 v162, v162, v177, v177\n"
      "v_mfma_f32_16x16x32_bf16 v[124:127], a[40:43], v[92:95], v[124:127]\n"
      "v_mul_f32 v130, v130, v158\n"
      "v_mul_f32 v128, v128, v156\n"
      "v_mfma_f32_16x16x32_bf16 v[124:127], a[44:47], v[96:99], v[124:127]\n"
      "s_waitcnt lgkmcnt(3)\n"
      "v_rcp_f32 v162, v162\n"
      "v_mfma_f32_16x16x32_bf16 v[124:127], a[48:51], v[100:103], v[124:127]\n"
      "v_cvt_pk_bf16_f32 v165, v130, v131\n"
      "v_mul_f32 v162, v134, v162\n"
      "v_mfma_f32_16x16x32_bf16 v[124:127], a[52:55], v[104:107], v[124:127]\n"
      "s_waitcnt lgkmcnt(1)\n"
      "v_mul_f32 v136, v136, v160\n"
      "v_mul_f32 v138, v138, v162\n"
      "v_mfma_f32_16x16x32_bf16 v[124:127], a[56:59], v[108:111], v[124:127]\n"
      "v_mul_f32 v137, v137, v161\n"
      "v_cvt_pk_bf16_f32 v167, v138, v139\n"
      "v_mfma_f32_16x16x32_bf16 v[124:127], a[60:63], v[112:115], v[124:127]\n"
      "s_waitcnt lgkmcnt(0)\n"
      "v_max_f32 v177, 0x3727c5ac, v169\n"
      "v_mfma_f32_16x16x32_bf16 v[124:127], a[64:67], v[116:119], v[124:127]\n"
      "s_add_u32 s37, s37, 165888\n"
      "v_cvt_pk_bf16_f32 v164, v128, v129\n"
      "v_mfma_f32_16x16x32_bf16 v[124:127], a[68:71], v[120:123], v[124:127]\n"
      "v_cvt_pk_bf16_f32 v166, v136, v137\n"
      "v_add_u32 v11, 1, v11\n"
      "v_mfma_f32_16x16x32_bf16 v[132:135], a[144:147], v[52:55], 0\n"
      "buffer_store_dwordx4 v[164:167], v0, s[8:11], s37 offen sc1\n"
      "v_mfma_f32_16x16x32_bf16 v[132:135], a[148:151], v[56:59], v[132:135]\n"
      "v_rsq_f32 v170, v177\n"
      "v_mfma_f32_16x16x32_bf16 v[132:135], a[152:155], v[60:63], v[132:135]\n"
      "v_mul_f32 v178, 0xc0135761, v170\n"
      "v_mul_f32 v170, v170, v170\n"
      "v_mfma_f32_16x16x32_bf16 v[132:135], a[156:159], v[64:67], v[132:135]\n"
      "v_mul_f32 v171, 0x3d372713, v178\n"
      "v_mul_f32 v157, v125, v125\n"
      "v_mfma_f32_16x16x32_bf16 v[132:135], a[160:163], v[68:71], v[132:135]\n"
      "v_mul_f32 v179, v171, v170\n"
      "v_mul_f32 v158, v126, v126\n"
      "v_mfma_f32_16x16x32_bf16 v[132:135], a[164:167], v[72:75], v[132:135]\n"
      "v_fma_f32 v157, v157, v179, v178\n"
      "s_mov_b32 s40, s36\n"
      "v_mfma_f32_16x16x32_bf16 v[132:135], a[168:171], v[76:79], v[132:135]\n"
      "v_mul_f32 v157, v125, v157\n"
      "s_add_u32 m0, s38, 0\n"
      "v_mfma_f32_16x16x32_bf16 v[132:135], a[172:175], v[80:83], v[132:135]\n"
      "v_exp_f32 v157, v157\n"
      "v_mfma_f32_16x16x32_bf16 v[132:135], a[176:179], v[84:87], v[132:135]\n"
      "v_fma_f32 v158, v158, v179, v178\n"
      "v_fma_f32 v157, v157, v177, v177\n"
      "v_mfma_f32_16x16x32_bf16 v[132:135], a[180:183], v[88:91], v[132:135]\n"
      "v_mfma_f32_16x16x32_bf16 v[132:135], a[184:187], v[92:95], v[132:135]\n"
      "v_mul_f32 v156, v124, v124\n"
      "v_mul_f32 v158, v126, v158\n"
      "v_mfma_f32_16x16x32_bf16 v[132:135], a[188:191], v[96:99], v[132:135]\n"
      "v_mul_f32 v159, v127, v127\n"
      "v_fma_f32 v156, v156, v179, v178\n"
      "v_mfma_f32_16x16x32_bf16 v[132:135], a[192:195], v[100:103], v[132:135]\n"
      "v_fma_f32 v159, v159, v179, v178\n"
      "v_mul_f32 v156, v124, v156\n"
      "v_mfma_f32_16x16x32_bf16 v[132:135], a[196:199], v[104:107], v[132:135]\n"
      "v_exp_f32 v158, v158\n"
      "v_mfma_f32_16x16x32_bf16 v[132:135], a[200:203], v[108:111], v[132:135]\n"
      "v_fma_f32 v158, v158, v177, v177\n"
      "v_mul_f32 v159, v127, v159\n"
      "v_mfma_f32_16x16x32_bf16 v[132:135], a[204:207], v[112:115], v[132:135]\n"
      "v_exp_f32 v156, v156\n"
      "v_mfma_f32_16x16x32_bf16 v[132:135], a[208:211], v[116:119], v[132:135]\n"
      "s_add_u32 s42, s42, 165888\n"
      "v_fma_f32 v156, v156, v177, v177\n"
      "v_mfma_f32_16x16x32_bf16 v[132:135], a[212:215], v[120:123], v[132:135]\n"
      "s_waitcnt vmcnt(3)\n"
      "ds_write_b32 v13, v11\n"
      "v_mfma_f32_16x16x32_bf16 v[128:131], a[72:75], v[52:55], 0\n"
      "v_exp_f32 v159, v159\n"
      "v_mfma_f32_16x16x32_bf16 v[128:131], a[76:79], v[56:59], v[128:131]\n"
      "s_waitcnt vmcnt(2)\n"
      "v_fma_f32 v159, v159, v177, v177\n"
      "v_cmp_ne_u32 vcc, s21, v18\n"
      "v_mfma_f32_16x16x32_bf16 v[128:131], a[80:83], v[60:63], v[128:131]\n"
      "v_mul_f32 v161, v133, v133\n"
      "v_fma_f32 v161, v161, v179, v178\n"
      "v_mfma_f32_16x16x32_bf16 v[128:131], a[84:87], v[64:67], v[128:131]\n"
      "v_mul_f32 v163, v135, v135\n"
      "v_mul_f32 v161, v133, v161\n"
      "v_mfma_f32_16x16x32_bf16 v[128:131], a[88:91], v[68:71], v[128:131]\n"
      "v_mul_f32 v160, v132, v132\n"
      "v_fma_f32 v163, v163, v179, v178\n"
      "v_mfma_f32_16x16x32_bf16 v[128:131], a[92:95], v[72:75], v[128:131]\n"
      "s_cbranch_vccnz .Lsp_g0_8\n"
      ".Lbk_g0_8:\n"
      "v_rcp_f32 v156, v156\n"
      "v_mfma_f32_16x16x32_bf16 v[128:131], a[96:99], v[76:79], v[128:131]\n"
      "buffer_load_dwordx4 v0, s[8:11], s40 offen offset:0 sc1 lds\n"
      "v_mfma_f32_16x16x32_bf16 v[128:131], a[100:103], v[80:83], v[128:131]\n"
      "v_rcp_f32 v159, v159\n"
      "v_mfma_f32_16x16x32_bf16 v[128:131], a[104:107], v[84:87], v[128:131]\n"
      "ds_read_b32 v12, v14\n"
      "v_fma_f32 v160, v160, v179, v178\n"
      "v_mfma_f32_16x16x32_bf16 v[128:131], a[108:111], v[88:91], v[128:131]\n"
      "s_add_u32 s36, s36, 24576\n"
      "v_mul_f32 v162, v134, v134\n"
      "v_mul_f32 v163, v135, v163\n"
      "v_mfma_f32_16x16x32_bf16 v[128:131], a[112:115], v[92:95], v[128:131]\n"
      "v_exp_f32 v163, v163\n"
      "v_mfma_f32_16x16x32_bf16 v[128:131], a[116:119], v[96:99], v[128:131]\n"
      "buffer_load_dwordx4 v0, s[8:11], s40 offen offset:1024 sc1 lds\n"
      "v_mfma_f32_16x16x32_bf16 v[128:131], a[120:123], v[100:103], v[128:131]\n"
      "v_rcp_f32 v158, v158\n"
      "v_mfma_f32_16x16x32_bf16 v[128:131], a[124:127], v[104:107], v[128:131]\n"
      "s_waitcnt lgkmcnt(0)\n"
      "v_mul_f32_e64 v160, v132, v160\n"
      "v_cmp_gt_i32 vcc, v11, v12\n"
      "v_mfma_f32_16x16x32_bf16 v[128:131], a[128:131], v[108:111], v[128:131]\n"
      "v_fma_f32 v162, v162, v179, v178\n"
      "v_mul_f32_e64 v162, v134, v162\n"
      "v_mfma_f32_16x16x32_bf16 v[128:131], a[132:135], v[112:115], v[128:131]\n"
      "buffer_load_dwordx4 v0, s[8:11], s40 offen offset:2048 sc1 lds\n"
      "v_mfma_f32_16x16x32_bf16 v[128:131], a[136:139], v[116:119], v[128:131]\n"
      "v_exp_f32_e64 v162, v162\n"
      "v_mfma_f32_16x16x32_bf16 v[128:131], a[140:143], v[120:123], v[128:131]\n"
      "s_cbranch_vccnz .Lsp_gy0_9\n"
      ".Lbk_gy0_9:\n"
      "ds_read2st64_b64 v[168:171], v4 offset0:120 offset1:121\n"
      "v_mfma_f32_16x16x32_bf16 v[136:139], a[216:219], v[52:55], 0\n"
      "ds_read_b128 v[52:55], v17 offset:20480\n"
      "v_fma_f32 v163, v163, v177, v177\n"
      "v_mfma_f32_16x16x32_bf16 v[136:139], a[220:223], v[56:59], v[136:139]\n"
      "v_rcp_f32 v157, v157\n"
      "v_mfma_f32_16x16x32_bf16 v[136:139], a[224:227], v[60:63], v[136:139]\n"
      "buffer_load_dwordx4 v0, s[8:11], s40 offen offset:3072 sc1 lds\n"
      "v_mfma_f32_16x16x32_bf16 v[136:139], a[228:231], v[64:67], v[136:139]\n"
      "s_add_u32 m0, s39, 0\n"
      "ds_read_b128 v[64:67], v17 offset:23552\n"
      "v_mul_f32 v158, v126, v158\n"
      "v_mfma_f32_16x16x32_bf16 v[136:139], a[232:235], v[68:71], v[136:139]\n"
      "s_min_u32 s36, s36, s6\n"
      "v_rcp_f32 v163, v163\n"
      "v_mfma_f32_16x16x32_bf16 v[136:139], a[236:239], v[72:75], v[136:139]\n"
      "ds_read_b128 v[56:59], v17 offset:21504\n"
      "v_mul_f32 v157, v125, v157\n"
      "v_mfma_f32_16x16x32_bf16 v[136:139], a[240:243], v[76:79], v[136:139]\n"
      "buffer_load_dwordx4 v1, s[8:11], s40 offen offset:0 sc1 lds\n"
      "v_mfma_f32_16x16x32_bf16 v[136:139], a[244:247], v[80:83], v[136:139]\n"
      "v_exp_f32 v161, v161\n"
      "v_mfma_f32_16x16x32_bf16 v[136:139], a[248:251], v[84:87], v[136:139]\n"
      "ds_read_b128 v[68:71], v17 offset:24576\n"
      "ds_read_b128 v[76:79], v17 offset:26624\n"
      "v_mfma_f32_16x16x32_bf16 v[136:139], a[252:255], v[88:91], v[136:139]\n"
      "v_exp_f32 v160, v160\n"
      "v_mfma_f32_16x16x32_bf16 v[136:139], v[20:23], v[92:95], v[136:139]\n"
      "ds_read_b128 v[72:75], v17 offset:25600\n"
      "v_mul_f32 v129, v129, v157\n"
      "v_mfma_f32_16x16x32_bf16 v[136:139], v[24:27], v[96:99], v[136:139]\n"
      "buffer_load_dword v18, v2, s[8:11], s36 offen sc1\n"
      "v_mfma_f32_16x16x32_bf16 v[136:139], v[28:31], v[100:103], v[136:139]\n"
      "ds_read_b128 v[60:63], v17 offset:22528\n"
      "v_fma_f32 v161, v161, v177, v177\n"
      "v_mfma_f32_16x16x32_bf16 v[136:139], v[32:35], v[104:107], v[136:139]\n"
      "s_waitcnt vmcnt(6) lgkmcnt(4)\n"
      "buffer_store_dword v15, v3, s[8:11], s42 offen sc1\n"
      "v_mfma_f32_16x16x32_bf16 v[136:139], v[36:39], v[108:111], v[136:139]\n"
      "ds_read_b128 v[84:87], v17 offset:28672\n"
      "v_fma_f32 v160, v160, v177, v177\n"
      "v_mfma_f32_16x16x32_bf16 v[136:139], v[40:43], v[112:115], v[136:139]\n"
      "v_rcp_f32 v161, v161\n"
      "v_mfma_f32_16x16x32_bf16 v[136:139], v[44:47], v[116:119], v[136:139]\n"
      "ds_read_b128 v[80:83], v17 offset:27648\n"
      "v_mul_f32 v159, v127, v159\n"
      "v_mfma_f32_16x16x32_bf16 v[136:139], v[48:51], v[120:123], v[136:139]\n"
      "ds_read_b128 v[96:99], v17 offset:31744\n"
      "v_mul_f32 v156, v124, v156\n"
      "v_mfma_f32_16x16x32_bf16 v[124:127], a[0:3], v[52:55], 0\n"
      "ds_read_b128 v[92:95], v17 offset:30720\n"
      "v_add_f32 v169, v169, v171\n"
      "v_mfma_f32_16x16x32_bf16 v[124:127], a[4:7], v[56:59], v[124:127]\n"
      "s_waitcnt lgkmcnt(4)\n"
      "ds_read_b128 v[88:91], v17 offset:29696\n"
      "v_mul_f32 v161, v133, v161\n"
      "v_mfma_f32_16x16x32_bf16 v[124:127], a[8:11], v[60:63], v[124:127]\n"
      "ds_read_b128 v[112:115], v17 offset:35840\n"
      "v_mul_f32 v163, v135, v163\n"
      "v_mfma_f32_16x16x32_bf16 v[124:127], a[12:15], v[64:67], v[124:127]\n"
      ".Lg_p1:\n"
      "s_cbranch_execz .Lg_pr\n"
      "ds_read_b128 v[100:103], v17 offset:32768\n"
      "v_mul_f32 v139, v139, v163\n"
      "v_mfma_f32_16x16x32_bf16 v[124:127], a[16:19], v[68:71], v[124:127]\n"
      "ds_read_b128 v[104:107], v17 offset:33792\n"
      "v_mul_f32 v131, v131, v159\n"
      "v_mfma_f32_16x16x32_bf16 v[124:127], a[20:23], v[72:75], v[124:127]\n"
      "ds_read_b128 v[108:111], v17 offset:34816\n"
      "v_mfma_f32_16x16x32_bf16 v[124:127], a[24:27], v[76:79], v[124:127]\n"
      "s_waitcnt lgkmcnt(6)\n"
      "ds_read_b128 v[120:123], v17 offset:37888\n"
      "v_sub_f32 v176, -v168, v170\n"
      "v_mfma_f32_16x16x32_bf16 v[124:127], a[28:31], v[80:83], v[124:127]\n"
      "v_rcp_f32 v160, v160\n"
      "v_mfma_f32_16x16x32_bf16 v[124:127], a[32:35], v[84:87], v[124:127]\n"
      "s_waitcnt lgkmcnt(5)\n"
      "ds_read_b128 v[116:119], v17 offset:36864\n"
      "v_fma_f32 v169, -v176, v176, v169\n"
      "v_mfma_f32_16x16x32_bf16 v[124:127], a[36:39], v[88:91], v[124:127]\n"
      "v_mul_f32 v160, v132, v160\n"
      "v_fma_f32 v162, v162, v177, v177\n"
      "v_mfma_f32_16x16x32_bf16 v[124:127], a[40:43], v[92:95], v[124:127]\n"
      "v_mul_f32 v130, v130, v158\n"
      "v_mul_f32 v128, v128, v156\n"
      "v_mfma_f32_16x16x32_bf16 v[124:127], a[44:47], v[96:99], v[124:127]\n"
      "s_waitcnt lgkmcnt(3)\n"
      "v_rcp_f32 v162, v162\n"
      "v_mfma_f32_16x16x32_bf16 v[124:127], a[48:51], v[100:103], v[124:127]\n"
      "v_cvt_pk_bf16_f32 v165, v130, v131\n"
      "v_mul_f32 v162, v134, v162\n"
      "v_mfma_f32_16x16x32_bf16 v[124:127], a[52:55], v[104:107], v[124:127]\n"
      "s_waitcnt lgkmcnt(1)\n"
      "v_mul_f32 v136, v136, v160\n"
      "v_mul_f32 v138, v138, v162\n"
      "v_mfma_f32_16x16x32_bf16 v[124:127], a[56:59], v[108:111], v[124:127]\n"
      "v_mul_f32 v137, v137, v161\n"
      "v_cvt_pk_bf16_f32 v167, v138, v139\n"
      "v_mfma_f32_16x16x32_bf16 v[124:127], a[60:63], v[112:115], v[124:127]\n"
      "s_waitcnt lgkmcnt(0)\n"
      "v_max_f32 v177, 0x3727c5ac, v169\n"
      "v_mfma_f32_16x16x32_bf16 v[124:127], a[64:67], v[116:119], v[124:127]\n"
      "s_add_u32 s37, s37, 165888\n"
      "v_cvt_pk_bf16_f32 v164, v128, v129\n"
      "v_mfma_f32_16x16x32_bf16 v[124:127], a[68:71], v[120:123], v[124:127]\n"
      "v_cvt_pk_bf16_f32 v166, v136, v137\n"
      "v_add_u32 v11, 1, v11\n"
      "v_mfma_f32_16x16x32_bf16 v[132:135], a[144:147], v[52:55], 0\n"
      "buffer_store_dwordx4 v[164:167], v0, s[8:11], s37 offen sc1\n"
      "v_mfma_f32_16x16x32_bf16 v[132:135], a[148:151], v[56:59], v[132:135]\n"
      "v_rsq_f32 v170, v177\n"
      "v_mfma_f32_16x16x32_bf16 v[132:135], a[152:155], v[60:63], v[132:135]\n"
      "v_mul_f32 v178, 0xc0135761, v170\n"
      "v_mul_f32 v170, v170, v170\n"
      "v_mfma_f32_16x16x32_bf16 v[132:135], a[156:159], v[64:67], v[132:135]\n"
      "v_mul_f32 v171, 0x3d372713, v178\n"
      "v_mul_f32 v157, v125, v125\n"
      "v_mfma_f32_16x16x32_bf16 v[132:135], a[160:163], v[68:71], v[132:135]\n"
      "v_mul_f32 v179, v171, v170\n"
      "v_mul_f32 v158, v126, v126\n"
      "v_mfma_f32_16x16x32_bf16 v[132:135], a[164:167], v[72:75], v[132:135]\n"
      "v_fma_f32 v157, v157, v179, v178\n"
      "s_mov_b32 s40, s36\n"
      "v_mfma_f32_16x16x32_bf16 v[132:135], a[168:171], v[76:79], v[132:135]\n"
      "v_mul_f32 v157, v125, v157\n"
      "s_add_u32 m0, s38, 20480\n"
      "v_mfma_f32_16x16x32_bf16 v[132:135], a[172:175], v[80:83], v[132:135]\n"
      "v_exp_f32 v157, v157\n"
      "v_mfma_f32_16x16x32_bf16 v[132:135], a[176:179], v[84:87], v[132:135]\n"
      "v_fma_f32 v158, v158, v179, v178\n"
      "v_fma_f32 v157, v157, v177, v177\n"
      "v_mfma_f32_16x16x32_bf16 v[132:135], a[180:183], v[88:91], v[132:135]\n"
      "v_mfma_f32_16x16x32_bf16 v[132:135], a[184:187], v[92:95], v[132:135]\n"
      "v_mul_f32 v156, v124, v124\n"
      "v_mul_f32 v158, v126, v158\n"
      "v_mfma_f32_16x16x32_bf16 v[132:135], a[188:191], v[96:99], v[132:135]\n"
      "v_mul_f32 v159, v127, v127\n"
      "v_fma_f32 v156, v156, v179, v178\n"
      "v_mfma_f32_16x16x32_bf16 v[132:135], a[192:195], v[100:103], v[132:135]\n"
      "v_fma_f32 v159, v159, v179, v178\n"
      "v_mul_f32 v156, v124, v156\n"
      "v_mfma_f32_16x16x32_bf16 v[132:135], a[196:199], v[104:107], v[132:135]\n"
      "v_exp_f32 v158, v158\n"
      "v_mfma_f32_16x16x32_bf16 v[132:135], a[200:203], v[108:111], v[132:135]\n"
      "v_fma_f32 v158, v158, v177, v177\n"
      "v_mul_f32 v159, v127, v159\n"
      "v_mfma_f32_16x16x32_bf16 v[132:135], a[204:207], v[112:115], v[132:135]\n"
      "v_exp_f32 v156, v156\n"
      "v_mfma_f32_16x16x32_bf16 v[132:135], a[208:211], v[116:119], v[132:135]\n"
      "s_add_u32 s42, s42, 165888\n"
      "v_fma_f32 v156, v156, v177, v177\n"
      "v_mfma_f32_16x16x32_bf16 v[132:135], a[212:215], v[120:123], v[132:135]\n"
      "s_waitcnt vmcnt(3)\n"
      "ds_write_b32 v13, v11\n"
      "v_mfma_f32_16x16x32_bf16 v[128:131], a[72:75], v[52:55], 0\n"
      "v_exp_f32 v159, v159\n"
      "v_mfma_f32_16x16x32_bf16 v[128:131], a[76:79], v[56:59], v[128:131]\n"
      "s_waitcnt vmcnt(2)\n"
      "v_fma_f32 v159, v159, v177, v177\n"
      "v_cmp_ne_u32 vcc, s21, v18\n"
      "v_mfma_f32_16x16x32_bf16 v[128:131], a[80:83], v[60:63], v[128:131]\n"
      "v_mul_f32 v161, v133, v133\n"
      "v_fma_f32 v161, v161, v179, v178\n"
      "v_mfma_f32_16x16x32_bf16 v[128:131], a[84:87], v[64:67], v[128:131]\n"
      "v_mul_f32 v163, v135, v135\n"
      "v_mul_f32 v161, v133, v161\n"
      "v_mfma_f32_16x16x32_bf16 v[128:131], a[88:91], v[68:71], v[128:131]\n"
      "v_mul_f32 v160, v132, v132\n"
      "v_fma_f32 v163, v163, v179, v178\n"
      "v_mfma_f32_16x16x32_bf16 v[128:131], a[92:95], v[72:75], v[128:131]\n"
      "s_cbranch_vccnz .Lsp_g0_10\n"
      ".Lbk_g0_10:\n"
      "v_rcp_f32 v156, v156\n"
      "v_mfma_f32_16x16x32_bf16 v[128:131], a[96:99], v[76:79], v[128:131]\n"
      "buffer_load_dwordx4 v0, s[8:11], s40 offen offset:0 sc1 lds\n"
      "v_mfma_f32_16x16x32_bf16 v[128:131], a[100:103], v[80:83], v[128:131]\n"
      "v_rcp_f32 v159, v159\n"
      "v_mfma_f32_16x16x32_bf16 v[128:131], a[104:107], v[84:87], v[128:131]\n"
      "ds_read_b32 v12, v14\n"
      "v_fma_f32 v160, v160, v179, v178\n"
      "v_mfma_f32_16x16x32_bf16 v[128:131], a[108:111], v[88:91], v[128:131]\n"
      "s_add_u32 s36, s36, 24576\n"
      "v_mul_f32 v162, v134, v134\n"
      "v_mul_f32 v163, v135, v163\n"
      "v_mfma_f32_16x16x32_bf16 v[128:131], a[112:115], v[92:95], v[128:131]\n"
      "v_exp_f32 v163, v163\n"
      "v_mfma_f32_16x16x32_bf16 v[128:131], a[116:119], v[96:99], v[128:131]\n"
      "buffer_load_dwordx4 v0, s[8:11], s40 offen offset:1024 sc1 lds\n"
      "v_mfma_f32_16x16x32_bf16 v[128:131], a[120:123], v[100:103], v[128:131]\n"
      "v_rcp_f32 v158, v158\n"
      "v_mfma_f32_16x16x32_bf16 v[128:131], a[124:127], v[104:107], v[128:131]\n"
      "s_waitcnt lgkmcnt(0)\n"
      "v_mul_f32 v160, v132, v160\n"
      "v_cmp_gt_i32 vcc, v11, v12\n"
      "v_mfma_f32_16x16x32_bf16 v[128:131], a[128:131], v[108:111], v[128:131]\n"
      "v_fma_f32 v162, v162, v179, v178\n"
      "v_mul_f32 v162, v134, v162\n"
      "v_mfma_f32_16x16x32_bf16 v[128:131], a[132:135], v[112:115], v[128:131]\n"
      "buffer_load_dwordx4 v0, s[8:11], s40 offen offset:2048 sc1 lds\n"
      "v_mfma_f32_16x16x32_bf16 v[128:131], a[136:139], v[116:119], v[128:131]\n"
      "v_exp_f32 v162, v162\n"
      "v_mfma_f32_16x16x32_bf16 v[128:131], a[140:143], v[120:123], v[128:131]\n"
      "s_cbranch_vccnz .Lsp_gy0_11\n"
      ".Lbk_gy0_11:\n"
      "ds_read2st64_b64 v[168:171], v4 offset0:0 offset1:1\n"
      "v_mfma_f32_16x16x32_bf16 v[136:139], a[216:219], v[52:55], 0\n"
      "ds_read_b128 v[52:55], v0 offset:0\n"
      "v_fma_f32 v163, v163, v177, v177\n"
      "v_mfma_f32_16x16x32_bf16 v[136:139], a[220:223], v[56:59], v[136:139]\n"
      "v_rcp_f32 v157, v157\n"
      "v_mfma_f32_16x16x32_bf16 v[136:139], a[224:227], v[60:63], v[136:139]\n"
      "buffer_load_dwordx4 v0, s[8:11], s40 offen offset:3072 sc1 lds\n"
      "v_mfma_f32_16x16x32_bf16 v[136:139], a[228:231], v[64:67], v[136:139]\n"
      "s_add_u32 m0, s39, 20480\n"
      "ds_read_b128 v[64:67], v0 offset:3072\n"
      "v_mul_f32 v158, v126, v158\n"
      "v_mfma_f32_16x16x32_bf16 v[136:139], a[232:235], v[68:71], v[136:139]\n"
      "s_min_u32 s36, s36, s6\n"
      "v_rcp_f32 v163, v163\n"
      "v_mfma_f32_16x16x32_bf16 v[136:139], a[236:239], v[72:75], v[136:139]\n"
      "ds_read_b128 v[56:59], v0 offset:1024\n"
      "v_mul_f32 v157, v125, v157\n"
      "v_mfma_f32_16x16x32_bf16 v[136:139], a[240:243], v[76:79], v[136:139]\n"
      "buffer_load_dwordx4 v1, s[8:11], s40 offen offset:0 sc1 lds\n"
      "v_mfma_f32_16x16x32_bf16 v[136:139], a[244:247], v[80:83], v[136:139]\n"
      "v_exp_f32 v161, v161\n"
      "v_mfma_f32_16x16x32_bf16 v[136:139], a[248:251], v[84:87], v[136:139]\n"
      "ds_read_b128 v[68:71], v0 offset:4096\n"
      "ds_read_b128 v[76:79], v0 offset:6144\n"
      "v_mfma_f32_16x16x32_bf16 v[136:139], a[252:255], v[88:91], v[136:139]\n"
      "v_exp_f32 v160, v160\n"
      "v_mfma_f32_16x16x32_bf16 v[136:139], v[20:23], v[92:95], v[136:139]\n"
      "ds_read_b128 v[72:75], v0 offset:5120\n"
      "v_mul_f32 v129, v129, v157\n"
      "v_mfma_f32_16x16x32_bf16 v[136:139], v[24:27], v[96:99], v[136:139]\n"
      "buffer_load_dword v18, v2, s[8:11], s36 offen sc1\n"
      "v_mfma_f32_16x16x32_bf16 v[136:139], v[28:31], v[100:103], v[136:139]\n"
      "ds_read_b128 v[60:63], v0 offset:2048\n"
      "v_fma_f32 v161, v161, v177, v177\n"
      "v_mfma_f32_16x16x32_bf16 v[136:139], v[32:35], v[104:107], v[136:139]\n"
      "s_waitcnt vmcnt(6) lgkmcnt(4)\n"
      "buffer_store_dword v15, v3, s[8:11], s42 offen sc1\n"
      "v_mfma_f32_16x16x32_bf16 v[136:139], v[36:39], v[108:111], v[136:139]\n"
      "ds_read_b128 v[84:87], v0 offset:8192\n"
      "v_fma_f32 v160, v160, v177, v177\n"
      "v_mfma_f32_16x16x32_bf16 v[136:139], v[40:43], v[112:115], v[136:139]\n"
      "v_rcp_f32 v161, v161\n"
      "v_mfma_f32_16x16x32_bf16 v[136:139], v[44:47], v[116:119], v[136:139]\n"
      "ds_read_b128 v[80:83], v0 offset:7168\n"
      "v_mul_f32 v159, v127, v159\n"
      "v_mfma_f32_16x16x32_bf16 v[136:139], v[48:51], v[120:123], v[136:139]\n"
      "ds_read_b128 v[96:99], v0 offset:11264\n"
      "v_mul_f32 v156, v124, v156\n"
      "s_sub_u32 s20, s20, 1\n"
      "s_cbranch_scc0 .Lg_loop\n"
      ".Lg_tail:\n"
      "v_mul_f32 v161, v133, v161\n"
      "v_mul_f32 v163, v135, v163\n"
      "s_nop 4\n"
      "v_mul_f32 v139, v139, v163\n"
      "v_mul_f32 v131, v131, v159\n"
      "v_rcp_f32 v160, v160\n"
      "s_nop 0\n"
      "v_mul_f32 v160, v132, v160\n"
      "v_fma_f32 v162, v162, v177, v177\n"
      "v_mul_f32 v130, v130, v158\n"
      "v_mul_f32 v128, v128, v156\n"
      "v_rcp_f32 v162, v162\n"
      "v_cvt_pk_bf16_f32 v165, v130, v131\n"
      "v_mul_f32 v162, v134, v162\n"
      "v_mul_f32 v136, v136, v160\n"
      "v_mul_f32 v138, v138, v162\n"
      "v_mul_f32 v137, v137, v161\n"
      "v_cvt_pk_bf16_f32 v167, v138, v139\n"
      "s_add_u32 s37, s37, 165888\n"
      "v_cvt_pk_bf16_f32 v164, v128, v129\n"
      "v_cvt_pk_bf16_f32 v166, v136, v137\n"
      "buffer_store_dwordx4 v[164:167], v0, s[8:11], s37 offen sc1\n"
      "s_add_u32 s42, s42, 165888\n"
      "s_waitcnt vmcnt(0)\n"
      "buffer_store_dword v15, v3, s[8:11], s42 offen sc1\n"
      "s_add_u32 s37, s37, 165888\n"
      "s_add_u32 s42, s42, 165888\n"
      "s_endpgm\n"
      ".Lsp_g0_1:\n"
      "s_mov_b32 s22, s23\n"
      ".Lsl_g0_1:\n"
      "buffer_load_dword v18, v2, s[8:11], s40 offen sc1\n"
      "s_waitcnt vmcnt(0)\n"
      "v_cmp_ne_u32 vcc, s21, v18\n"
      "s_cbranch_vccz .Lbk_g0_1\n"
      "s_sleep 1\n"
      "s_sub_u32 s22, s22, 1\n"
      "s_cbranch_scc0 .Lsl_g0_1\n"
      "s_mov_b32 s23, 0\n"
      "s_branch .Lbk_g0_1\n"
      ".Lsp_g0_2:\n"
      "s_mov_b32 s22, s23\n"
      ".Lsl_g0_2:\n"
      "buffer_load_dword v18, v2, s[8:11], s40 offen sc1\n"
      "s_waitcnt vmcnt(0)\n"
      "v_cmp_ne_u32 vcc, s21, v18\n"
      "s_cbranch_vccz .Lbk_g0_2\n"
      "s_sleep 1\n"
      "s_sub_u32 s22, s22, 1\n"
      "s_cbranch_scc0 .Lsl_g0_2\n"
      "s_mov_b32 s23, 0\n"
      "s_branch .Lbk_g0_2\n"
      ".Lsp_gy0_3:\n"
      "s_lshl_b32 s22, s23, 4\n"
      ".Lsl_gy0_3:\n"
      "ds_read_b32 v12, v14\n"
      "s_waitcnt lgkmcnt(0)\n"
      "v_cmp_gt_i32 vcc, v11, v12\n"
      "s_cbranch_vccz .Lbk_gy0_3\n"
      "s_sub_u32 s22, s22, 1\n"
      "s_cbranch_scc0 .Lsl_gy0_3\n"
      "s_mov_b32 s23, 0\n"
      "s_branch .Lbk_gy0_3\n"
      ".Lsp_g0_4:\n"
      "s_mov_b32 s22, s23\n"
      ".Lsl_g0_4:\n"
      "buffer_load_dword v18, v2, s[8:11], s40 offen sc1\n"
      "s_waitcnt vmcnt(0)\n"
      "v_cmp_ne_u32 vcc, s21, v18\n"
      "s_cbranch_vccz .Lbk_g0_4\n"
      "s_sleep 1\n"
      "s_sub_u32 s22, s22, 1\n"
      "s_cbranch_scc0 .Lsl_g0_4\n"
      "s_mov_b32 s23, 0\n"
      "s_branch .Lbk_g0_4\n"
      ".Lsp_gy0_5:\n"
      "s_lshl_b32 s22, s23, 4\n"
      ".Lsl_gy0_5:\n"
      "ds_read_b32 v12, v14\n"
      "s_waitcnt lgkmcnt(0)\n"
      "v_cmp_gt_i32 vcc, v11, v12\n"
      "s_cbranch_vccz .Lbk_gy0_5\n"
      "s_sub_u32 s22, s22, 1\n"
      "s_cbranch_scc0 .Lsl_gy0_5\n"
      "s_mov_b32 s23, 0\n"
      "s_branch .Lbk_gy0_5\n"
      ".Lsp_g0_6:\n"
      "s_mov_b32 s22, s23\n"
      ".Lsl_g0_6:\n"
      "buffer_load_dword v18, v2, s[8:11], s40 offen sc1\n"
      "s_waitcnt vmcnt(0)\n"
      "v_cmp_ne_u32 vcc, s21, v18\n"
      "s_cbranch_vccz .Lbk_g0_6\n"
      "s_sleep 1\n"
      "s_sub_u32 s22, s22, 1\n"
      "s_cbranch_scc0 .Lsl_g0_6\n"
      "s_mov_b32 s23, 0\n"
      "s_branch .Lbk_g0_6\n"
      ".Lsp_gy0_7:\n"
      "s_lshl_b32 s22, s23, 4\n"
      ".Lsl_gy0_7:\n"
      "ds_read_b32 v12, v14\n"
      "s_waitcnt lgkmcnt(0)\n"
      "v_cmp_gt_i32 vcc, v11, v12\n"
      "s_cbranch_vccz .Lbk_gy0_7\n"
      "s_sub_u32 s22, s22, 1\n"
      "s_cbranch_scc0 .Lsl_gy0_7\n"
      "s_mov_b32 s23, 0\n"
      "s_branch .Lbk_gy0_7\n"
      ".Lsp_g0_8:\n"
      "s_mov_b32 s22, s23\n"
      ".Lsl_g0_8:\n"
      "buffer_load_dword v18, v2, s[8:11], s40 offen sc1\n"
      "s_waitcnt vmcnt(0)\n"
      "v_cmp_ne_u32 vcc, s21, v18\n"
      "s_cbranch_vccz .Lbk_g0_8\n"
      "s_sleep 1\n"
      "s_sub_u32 s22, s22, 1\n"
      "s_cbranch_scc0 .Lsl_g0_8\n"
      "s_mov_b32 s23, 0\n"
      "s_branch .Lbk_g0_8\n"
      ".Lsp_gy0_9:\n"
      "s_lshl_b32 s22, s23, 4\n"
      ".Lsl_gy0_9:\n"
      "ds_read_b32 v12, v14\n"
      "s_waitcnt lgkmcnt(0)\n"
      "v_cmp_gt_i32 vcc, v11, v12\n"
      "s_cbranch_vccz .Lbk_gy0_9\n"
      "s_sub_u32 s22, s22, 1\n"
      "s_cbranch_scc0 .Lsl_gy0_9\n"
      "s_mov_b32 s23, 0\n"
      "s_branch .Lbk_gy0_9\n"
      ".Lsp_g0_10:\n"
      "s_mov_b32 s22, s23\n"
      ".Lsl_g0_10:\n"
      "buffer_load_dword v18, v2, s[8:11], s40 offen sc1\n"
      "s_waitcnt vmcnt(0)\n"
      "v_cmp_ne_u32 vcc, s21, v18\n"
      "s_cbranch_vccz .Lbk_g0_10\n"
      "s_sleep 1\n"
      "s_sub_u32 s22, s22, 1\n"
      "s_cbranch_scc0 .Lsl_g0_10\n"
      "s_mov_b32 s23, 0\n"
      "s_branch .Lbk_g0_10\n"
      ".Lsp_gy0_11:\n"
      "s_lshl_b32 s22, s23, 4\n"
      ".Lsl_gy0_11:\n"
      "ds_read_b32 v12, v14\n"
      "s_waitcnt lgkmcnt(0)\n"
      "v_cmp_gt_i32 vcc, v11, v12\n"
      "s_cbranch_vccz .Lbk_gy0_11\n"
      "s_sub_u32 s22, s22, 1\n"
      "s_cbranch_scc0 .Lsl_gy0_11\n"
      "s_mov_b32 s23, 0\n"
      "s_branch .Lbk_gy0_11\n"
      ".Lg_last:\n"
      :
      : "{s[8:11]}"(rsWS), "{s[12:15]}"(rsA), "{s[92:95]}"(rsC), "{v0}"(v_l16), "{v1}"(v_l5), "{v2}"(v_fa), "{v3}"(v_ra), "{v4}"(v_st), "{v5}"(v_at), "{v6}"(v_vo0), "{v7}"(v_vo1), "{v8}"(v_vo2), "{v9}"(v_vo3), "{v10}"(v_pw), "{v12}"(v_al), "{v13}"(v_yw), "{v14}"(v_yr), "{v16}"(v_hi), "{v54}"(v_tw[0]), "{v55}"(v_tw[1]), "{v64}"(v_id[0]), "{v65}"(v_id[1]), "{s36}"(s_sx), "{s37}"(s_sh), "{s38}"(s_m4), "{s39}"(s_m5)
      : "v255", "a255", "s99", "vcc", "scc", "memory");

  }
}

extern "C" void kernel_launch(void* const* d_in, const int* in_sizes, int n_in, void* d_out, int out_size,
                              void* d_ws, size_t ws_size, hipStream_t stream) {
  char* ws = (char*)d_ws + ((256 - ((size_t)d_ws & 255)) & 255);
  (void)hipFuncSetAttribute((const void*)k_all, hipFuncAttributeMaxDynamicSharedMemorySize, LDS_BYTES);
  k_all<<<256, 256, LDS_BYTES, stream>>>((const u16*)d_in[0], (const u16*)d_in[1], (const u16*)d_in[2], (const u16*)d_in[4],
                                         (const u16*)d_in[5], (const u16*)d_in[6], ws, (u16*)d_out);
}
```

```cpp
#include <hip/hip_runtime.h>
#include <stdint.h>

typedef unsigned short u16;
typedef __attribute__((ext_vector_type(4))) unsigned u32x4;
constexpr unsigned WS_DBG = 65536u;
constexpr unsigned WS_XIN = 1310720u;
constexpr unsigned WS_OB = 9175040u;
constexpr unsigned WS_X1 = 17039360u;
constexpr unsigned WS_H = 26034176u;
constexpr unsigned WS_PS = 78589952u;
constexpr unsigned WS_END = 109740032u;
constexpr unsigned KSK = 0u;
constexpr unsigned ENT = 1u;
constexpr unsigned ROT = 0u;
constexpr unsigned FLIP = 0u;
constexpr unsigned PW = 1664u;
constexpr unsigned REC = 24576u;
constexpr unsigned HREC = 165888u;
constexpr unsigned PREC = 99840u;
constexpr unsigned NREC = 80u;
constexpr unsigned OOB = 0x40000000u, NEG = 0xff61b1e6u;
constexpr int LDS_BYTES = 160 * 1024;

__device__ __forceinline__ u32x4 srd(const void* p, unsigned bytes) {
  const unsigned long long a = (unsigned long long)p;
  return u32x4{(unsigned)a, (unsigned)(a >> 32) & 0xffffu, bytes, 0x00020000u};
}

extern "C" __global__ __launch_bounds__(256, 1) __attribute__((amdgpu_waves_per_eu(1, 1))) void k_all(
    const u16* X, const u16* Wqkv, const u16* Wo, const u16* Wg, const u16* Wu, const u16* Wd, char* ws, u16* out) {
  const unsigned b = blockIdx.x, xcd = b & 7, slot = b >> 3;
  const unsigned w = __builtin_amdgcn_readfirstlane(threadIdx.x >> 6), lane = threadIdx.x & 63;
  const bool fast = ((xcd ^ FLIP) & 1) == 0;
  auto qvo = [&](unsigned i, unsigned hg) {
    const unsigned c = 64 * i + lane, r = c * 2185 >> 16, cc = c - 30 * r, sc = cc * 57 >> 9;
    const unsigned o = (8 * w + r) * 3456 + sc * (1152 - 144) + 144 * hg + cc * 16 - 1024 * i;
    return o | (c < 240 && cc < 27 ? 0u : OOB);
  };
  if (ENT) {
    constexpr unsigned NL = 41, LR = 9;
    const unsigned wi = slot * 4 + w, n0 = wi * NL + lane, n = n0 < 576 * LR ? n0 : 0, r = n / LR, c = n - LR * r;
    const unsigned lx = lane - NL, m = (xcd * 128 + wi) * 4 + lx, t = m / 144;
    unsigned oc = (wi * 8 + (lane & 7)) * 128;
    asm volatile("s_getpc_b64 vcc\n.Lent_pc:\n\ts_sub_u32 vcc_lo, vcc_lo, .Lent_pc-k_all\n\ts_subb_u32 vcc_hi, vcc_hi, 0\n\ts_mov_b32 m0, %1\n\t"
                 "v_min_u32 %0, .Lw_last-k_all, %0\n\tglobal_load_lds_dword %0, vcc" : "+v"(oc) : "s"(162816 + w * 256) : "vcc", "scc", "memory");
    unsigned oq = r * 3456 + c / (LR / 3) * 1152 + ((xcd & 6) + c % (LR / 3)) * 128, ox = (32 * (t & 3) + 31 - (t >> 2)) * 18432 + (m - 144 * t) * 128;
    asm volatile("" : "+v"(oq), "+v"(ox));
    const bool isq = lane < NL && fast;
    oq = isq ? oq : lx < 4 ? ox : 0;
    asm volatile("" : "+v"(oq));
    __builtin_amdgcn_sched_barrier(0);
    const char* pa = (const char*)(isq ? Wqkv : X) + oq;
    asm volatile("s_mov_b32 m0, %1\n\ts_nop 0\n\tglobal_load_lds_dword %0, off" ::"v"(pa), "s"(162816 + w * 256) : "memory");
    __builtin_amdgcn_sched_barrier(0);
  }
  const unsigned i16 = lane & 15, g = lane >> 4, q = i16 >> 2, p = lane & 3;
  const u32x4 rsWS = srd(ws, WS_END);
  const unsigned v_l16 = lane * 16, e = xcd >> 1;
  const unsigned half = (i16 + 16 * (g >> 1)) * 16 + (g & 1) * 8;
  const unsigned line = lane < 32 ? lane * 4 : OOB, v_al = w ? OOB : line + 128 * (1 + b);
  const unsigned up = lane >> 5, l5 = (lane & 31) * 4, v_hi = 0u - up;
  const unsigned l8 = lane >> 3, l7 = (lane & 7) * 16;
#define COMMON(ID, YB) const unsigned v_dbg = WS_DBG + (((ID) + part * 4 + gr) * 4 + w) * 1024 + lane * 4, v_yw = (YB) + w * 4, v_yr = (YB) + p * 4; (void)v_dbg
  if (__builtin_expect(fast && slot < 8, 1)) {
    const unsigned gr = slot & 3, part = 2 * e + (slot >> 2);
    COMMON(0, 61440);
    const unsigned hg = part, h = 4 * hg + w, par = i16 & 1;
    const u32x4 rsA = srd(Wqkv, 576 * 1728 * 2);
    const unsigned v_l5 = w < 3 || lane < 32 ? v_l16 + 16384 - w * 3072 : OOB;
    const unsigned v_fa = 20480 - w * 4096 + (lane < 48 ? lane >> 2 : 11) * 128 + p * 4, v_ra = up * 8 * REC + l5 + 20480 + h * 128;
    const unsigned v_st = 18432 + i16 * 32 + q * 8, v_tw = 61568 + w * 128 + i16 * 8, v_tr = 61568 + w * 128 + g * 32;
    const unsigned v_om = h * 512 + half, v_oe = g ? OOB : 16384 + h * 128 + i16 * 8;
    const unsigned v_pw = 20480 + h * 128 + l8 * REC + l7;
    unsigned v_vo[4], v_bc[4], v_bp[4], v_mk[4], v_sel[4] = {0, 0, 0, 0}, v_ci[4];
#pragma unroll
    for (unsigned i = 0; i < 4; ++i) {
      const unsigned kk = 4 * g + i, ka = 4 * g + 2 * (i & 1) + par;
      v_bc[i] = (i & 1) == par && kk <= i16 ? 0u : NEG;
      v_bp[i] = (i & 1) == par && kk >= i16 + 2 ? 0u : NEG;
      v_mk[i] = (i < 2 ? ka > i16 : ka <= i16) ? 0xffffu << (16 * par) : 0u;
      v_ci[i] = p == 2 ? 0x3f800000u : 0u;
    }
    v_sel[3] = lane == 16 ? 0x3f80u : lane == 17 ? 0x3f800000u : 0u;
#pragma unroll
    for (unsigned i = 0; i < 4; ++i) v_vo[i] = qvo(i, hg);
    const unsigned rb = 65536 + g * 4224 + q * 480;
    const unsigned v_at = rb + 36 * w + 4 * (w & 1) + 8 * p, v_ax = rb + (p < 2 ? p : 2) * 144 + 72 * (w >> 1) + 32;
    const unsigned v_pm = (16 * g + 4 * (i16 >> 1 & 3) + 2 * (w & 1) + par) * 4, v_ms = i16 < 6 ? ~0u : 0u;
    const unsigned s_sx = WS_XIN + (gr * NREC + KSK) * REC + w * 4096, s_ev = s_sx + (32 - KSK) * REC, s_del = 0u - (gr ? NREC + 32 : 32) * REC;
    const unsigned s_kil = gr ? 0x7f7fffffu : 0xff7fffffu, s_so = WS_OB + (gr * NREC + KSK) * REC;
    const unsigned s_m4 = w * 4096, s_m5 = 16384 + w * 1024, s_ms = w * 4224, s_up = part + gr + w;
    asm volatile(
      "s_buffer_load_dword s21, s[8:11], 0\n"
      "s_mov_b32 s23, 65536\n"
      "s_mov_b32 s31, 0x40000000\n"
      "s_mov_b32 s51, 0\n"
      "s_add_u32 m0, s43, 65536\n"
      "s_nop 0\n"
      "buffer_load_dwordx4 v48, s[12:15], s51 offen offset:0 lds\n"
      "buffer_load_dwordx4 v49, s[12:15], s51 offen offset:1024 lds\n"
      "buffer_load_dwordx4 v50, s[12:15], s51 offen offset:2048 lds\n"
      "buffer_load_dwordx4 v51, s[12:15], s51 offen offset:3072 lds\n"
      "s_add_u32 s51, s51, 110592\n"
      "s_add_u32 m0, s43, 82432\n"
      "s_nop 0\n"
      "buffer_load_dwordx4 v48, s[12:15], s51 offen offset:0 lds\n"
      "buffer_load_dwordx4 v49, s[12:15], s51 offen offset:1024 lds\n"
      "buffer_load_dwordx4 v50, s[12:15], s51 offen offset:2048 lds\n"
      "buffer_load_dwordx4 v51, s[12:15], s51 offen offset:3072 lds\n"
      "s_add_u32 s51, s51, 110592\n"
      "s_add_u32 m0, s43, 99328\n"
      "s_nop 0\n"
      "buffer_load_dwordx4 v48, s[12:15], s51 offen offset:0 lds\n"
      "buffer_load_dwordx4 v49, s[12:15], s51 offen offset:1024 lds\n"
      "buffer_load_dwordx4 v50, s[12:15], s51 offen offset:2048 lds\n"
      "buffer_load_dwordx4 v51, s[12:15], s51 offen offset:3072 lds\n"
      "s_add_u32 s51, s51, 110592\n"
      "s_add_u32 s6, s36, 1572864\n"
      "s_add_u32 s36, s36, 4294942720\n"
      "s_add_u32 s48, s37, 811008\n"
      "s_mov_b32 s53, 0x7f7fffff\n"
      "v_mov_b32 v179, 0\n"
      "s_mov_b32 s54, 0xffff\n"
      "s_mov_b32 s55, 0\n"
      "v_mov_b32 v9, 0\n"
      "ds_write_b32 v11, v9\n"
      "v_mov_b32 v56, 0x3f803f80\n"
      "v_mov_b32 v57, 0x3f803f80\n"
      "v_mov_b32 v58, 0x3f803f80\n"
      "v_mov_b32 v59, 0x3f803f80\n"
      "s_mov_b64 exec, 0\n"
      "s_lshr_b32 s22, s41, 12\n"
      ".Lq_pl:\n"
      "s_cmp_eq_u32 s22, 0\n"
      "s_cbranch_scc1 .Lq_p0\n"
      "s_cmp_eq_u32 s22, 1\n"
      "s_cbranch_scc1 .Lq_p1\n"
      "s_cmp_eq_u32 s22, 3\n"
      "s_cbranch_scc1 .Lq_px\n"
      "s_branch .Lq_pe\n"
      ".Lq_pr:\n"
      "s_add_u32 s22, s22, 4\n"
      "s_branch .Lq_pl\n"
      ".Lq_pe:\n"
      "s_mov_b64 exec, -1\n"
      "s_waitcnt vmcnt(8) lgkmcnt(0)\n"
      "s_barrier\n"
      "ds_read_b64_tr_b16 v[60:61], v53 offset:0\n"
      "ds_read_b64_tr_b16 v[62:63], v53 offset:1920\n"
      "ds_read_b64_tr_b16 a[0:1], v52 offset:144\n"
      "ds_read_b64_tr_b16 a[2:3], v52 offset:2064\n"
      "ds_read_b64_tr_b16 a[72:73], v52 offset:288\n"
      "ds_read_b64_tr_b16 a[74:75], v52 offset:2208\n"
      "ds_read_b64_tr_b16 a[144:145], v52 offset:0\n"
      "ds_read_b64_tr_b16 a[146:147], v52 offset:1920\n"
      "s_add_u32 m0, s43, 116224\n"
      "s_nop 0\n"
      "buffer_load_dwordx4 v48, s[12:15], s51 offen offset:0 lds\n"
      "buffer_load_dwordx4 v49, s[12:15], s51 offen offset:1024 lds\n"
      "buffer_load_dwordx4 v50, s[12:15], s51 offen offset:2048 lds\n"
      "buffer_load_dwordx4 v51, s[12:15], s51 offen offset:3072 lds\n"
      "s_add_u32 s51, s51, 110592\n"
      "s_waitcnt vmcnt(8) lgkmcnt(0)\n"
      "s_barrier\n"
      "ds_read_b64_tr_b16 v[68:69], v53 offset:16896\n"
      "ds_read_b64_tr_b16 v[70:71], v53 offset:18816\n"
      "ds_read_b64_tr_b16 a[4:5], v52 offset:17040\n"
      "ds_read_b64_tr_b16 a[6:7], v52 offset:18960\n"
      "ds_read_b64_tr_b16 a[76:77], v52 offset:17184\n"
      "ds_read_b64_tr_b16 a[78:79], v52 offset:19104\n"
      "ds_read_b64_tr_b16 a[148:149], v52 offset:16896\n"
      "ds_read_b64_tr_b16 a[150:151], v52 offset:18816\n"
      "ds_bpermute_b32 v64, v54, v60\n"
      "ds_bpermute_b32 v65, v54, v61\n"
      "ds_bpermute_b32 v66, v54, v62\n"
      "ds_bpermute_b32 v67, v54, v63\n"
      "v_mfma_f32_16x16x32_bf16 v[136:139], a[0:3], v[56:59], 0\n"
      "v_mfma_f32_16x16x32_bf16 v[144:147], v[56:59], a[72:75], 0\n"
      "v_mfma_f32_16x16x32_bf16 v[148:151], a[144:147], v[56:59], 0\n"
      "s_add_u32 m0, s43, 65536\n"
      "s_nop 0\n"
      "buffer_load_dwordx4 v48, s[12:15], s51 offen offset:0 lds\n"
      "buffer_load_dwordx4 v49, s[12:15], s51 offen offset:1024 lds\n"
      "buffer_load_dwordx4 v50, s[12:15], s51 offen offset:2048 lds\n"
      "buffer_load_dwordx4 v51, s[12:15], s51 offen offset:3072 lds\n"
      "s_add_u32 s51, s51, 110592\n"
      "s_waitcnt vmcnt(8) lgkmcnt(0)\n"
      "s_barrier\n"
      "ds_read_b64_tr_b16 v[60:61], v53 offset:33792\n"
      "ds_read_b64_tr_b16 v[62:63], v53 offset:35712\n"
      "ds_read_b64_tr_b16 a[8:9], v52 offset:33936\n"
      "ds_read_b64_tr_b16 a[10:11], v52 offset:35856\n"
      "ds_read_b64_tr_b16 a[80:81], v52 offset:34080\n"
      "ds_read_b64_tr_b16 a[82:83], v52 offset:36000\n"
      "ds_read_b64_tr_b16 a[152:153], v52 offset:33792\n"
      "ds_read_b64_tr_b16 a[154:155], v52 offset:35712\n"
      "ds_bpermute_b32 v72, v54, v68\n"
      "ds_bpermute_b32 v73, v54, v69\n"
      "ds_bpermute_b32 v74, v54, v70\n"
      "ds_bpermute_b32 v75, v54, v71\n"
      "v_mfma_f32_16x16x32_bf16 v[136:139], a[4:7], v[56:59], v[136:139]\n"
      "v_mfma_f32_16x16x32_bf16 v[144:147], v[56:59], a[76:79], v[144:147]\n"
      "v_mfma_f32_16x16x32_bf16 v[148:151], a[148:151], v[56:59], v[148:151]\n"
      "s_add_u32 m0, s43, 82432\n"
      "s_nop 0\n"
      "buffer_load_dwordx4 v48, s[12:15], s51 offen offset:0 lds\n"
      "buffer_load_dwordx4 v49, s[12:15], s51 offen offset:1024 lds\n"
      "buffer_load_dwordx4 v50, s[12:15], s51 offen offset:2048 lds\n"
      "buffer_load_dwordx4 v51, s[12:15], s51 offen offset:3072 lds\n"
      "s_add_u32 s51, s51, 110592\n"
      "v_and_b32 v64, v55, v64\n"
      "v_and_b32 v65, v55, v65\n"
      "v_and_b32 v66, v55, v66\n"
      "v_and_b32 v67, v55, v67\n"
      "v_accvgpr_write_b32 a216, v64\n"
      "v_accvgpr_write_b32 a217, v65\n"
      "v_accvgpr_write_b32 a218, v66\n"
      "v_accvgpr_write_b32 a219, v67\n"
      "v_mfma_f32_16x16x32_bf16 v[140:143], v[64:67], v[56:59], 0\n"
      "s_waitcnt vmcnt(8) lgkmcnt(0)\n"
      "s_barrier\n"
      "ds_read_b64_tr_b16 v[68:69], v53 offset:50688\n"
      "ds_read_b64_tr_b16 v[70:71], v53 offset:52608\n"
      "ds_read_b64_tr_b16 a[12:13], v52 offset:50832\n"
      "ds_read_b64_tr_b16 a[14:15], v52 offset:52752\n"
      "ds_read_b64_tr_b16 a[84:85], v52 offset:50976\n"
      "ds_read_b64_tr_b16 a[86:87], v52 offset:52896\n"
      "ds_read_b64_tr_b16 a[156:157], v52 offset:50688\n"
      "ds_read_b64_tr_b16 a[158:159], v52 offset:52608\n"
      "ds_bpermute_b32 v64, v54, v60\n"
      "ds_bpermute_b32 v65, v54, v61\n"
      "ds_bpermute_b32 v66, v54, v62\n"
      "ds_bpermute_b32 v67, v54, v63\n"
      "v_mfma_f32_16x16x32_bf16 v[136:139], a[8:11], v[56:59], v[136:139]\n"
      "v_mfma_f32_16x16x32_bf16 v[144:147], v[56:59], a[80:83], v[144:147]\n"
      "v_mfma_f32_16x16x32_bf16 v[148:151], a[152:155], v[56:59], v[148:151]\n"
      "s_add_u32 m0, s43, 99328\n"
      "s_nop 0\n"
      "buffer_load_dwordx4 v48, s[12:15], s51 offen offset:0 lds\n"
      "buffer_load_dwordx4 v49, s[12:15], s51 offen offset:1024 lds\n"
      "buffer_load_dwordx4 v50, s[12:15], s51 offen offset:2048 lds\n"
      "buffer_load_dwordx4 v51, s[12:15], s51 offen offset:3072 lds\n"
      "s_add_u32 s51, s51, 110592\n"
      "v_and_b32 v72, v55, v72\n"
      "v_and_b32 v73, v55, v73\n"
      "v_and_b32 v74, v55, v74\n"
      "v_and_b32 v75, v55, v75\n"
      "v_accvgpr_write_b32 a220, v72\n"
      "v_accvgpr_write_b32 a221, v73\n"
      "v_accvgpr_write_b32 a222, v74\n"
      "v_accvgpr_write_b32 a223, v75\n"
      "v_mfma_f32_16x16x32_bf16 v[140:143], v[72:75], v[56:59], v[140:143]\n"
      "s_waitcnt vmcnt(8) lgkmcnt(0)\n"
      "s_barrier\n"
      "ds_read_b64_tr_b16 v[60:61], v53 offset:0\n"
      "ds_read_b64_tr_b16 v[62:63], v53 offset:1920\n"
      "ds_read_b64_tr_b16 a[16:17], v52 offset:144\n"
      "ds_read_b64_tr_b16 a[18:19], v52 offset:2064\n"
      "ds_read_b64_tr_b16 a[88:89], v52 offset:288\n"
      "ds_read_b64_tr_b16 a[90:91], v52 offset:2208\n"
      "ds_read_b64_tr_b16 a[160:161], v52 offset:0\n"
      "ds_read_b64_tr_b16 a[162:163], v52 offset:1920\n"
      "ds_bpermute_b32 v72, v54, v68\n"
      "ds_bpermute_b32 v73, v54, v69\n"
      "ds_bpermute_b32 v74, v54, v70\n"
      "ds_bpermute_b32 v75, v54, v71\n"
      "v_mfma_f32_16x16x32_bf16 v[136:139], a[12:15], v[56:59], v[136:139]\n"
      "v_mfma_f32_16x16x32_bf16 v[144:147], v[56:59], a[84:87], v[144:147]\n"
      "v_mfma_f32_16x16x32_bf16 v[148:151], a[156:159], v[56:59], v[148:151]\n"
      "s_add_u32 m0, s43, 116224\n"
      "s_nop 0\n"
      "buffer_load_dwordx4 v48, s[12:15], s51 offen offset:0 lds\n"
      "buffer_load_dwordx4 v49, s[12:15], s51 offen offset:1024 lds\n"
      "buffer_load_dwordx4 v50, s[12:15], s51 offen offset:2048 lds\n"
      "buffer_load_dwordx4 v51, s[12:15], s51 offen offset:3072 lds\n"
      "s_add_u32 s51, s51, 110592\n"
      "v_and_b32 v64, v55, v64\n"
      "v_and_b32 v65, v55, v65\n"
      "v_and_b32 v66, v55, v66\n"
      "v_and_b32 v67, v55, v67\n"
      "v_accvgpr_write_b32 a224, v64\n"
      "v_accvgpr_write_b32 a225, v65\n"
      "v_accvgpr_write_b32 a226, v66\n"
      "v_accvgpr_write_b32 a227, v67\n"
      "v_mfma_f32_16x16x32_bf16 v[140:143], v[64:67], v[56:59], v[140:143]\n"
      "s_waitcnt vmcnt(8) lgkmcnt(0)\n"
      "s_barrier\n"
      "ds_read_b64_tr_b16 v[68:69], v53 offset:16896\n"
      "ds_read_b64_tr_b16 v[70:71], v53 offset:18816\n"
      "ds_read_b64_tr_b16 a[20:21], v52 offset:17040\n"
      "ds_read_b64_tr_b16 a[22:23], v52 offset:18960\n"
      "ds_read_b64_tr_b16 a[92:93], v52 offset:17184\n"
      "ds_read_b64_tr_b16 a[94:95], v52 offset:19104\n"
      "ds_read_b64_tr_b16 a[164:165], v52 offset:16896\n"
      "ds_read_b64_tr_b16 a[166:167], v52 offset:18816\n"
      "ds_bpermute_b32 v64, v54, v60\n"
      "ds_bpermute_b32 v65, v54, v61\n"
      "ds_bpermute_b32 v66, v54, v62\n"
      "ds_bpermute_b32 v67, v54, v63\n"
      "v_mfma_f32_16x16x32_bf16 v[136:139], a[16:19], v[56:59], v[136:139]\n"
      "v_mfma_f32_16x16x32_bf16 v[144:147], v[56:59], a[88:91], v[144:147]\n"
      "v_mfma_f32_16x16x32_bf16 v[148:151], a[160:163], v[56:59], v[148:151]\n"
      "s_add_u32 m0, s43, 65536\n"
      "s_nop 0\n"
      "buffer_load_dwordx4 v48, s[12:15], s51 offen offset:0 lds\n"
      "buffer_load_dwordx4 v49, s[12:15], s51 offen offset:1024 lds\n"
      "buffer_load_dwordx4 v50, s[12:15], s51 offen offset:2048 lds\n"
      "buffer_load_dwordx4 v51, s[12:15], s51 offen offset:3072 lds\n"
      "s_add_u32 s51, s51, 110592\n"
      "v_and_b32 v72, v55, v72\n"
      "v_and_b32 v73, v55, v73\n"
      "v_and_b32 v74, v55, v74\n"
      "v_and_b32 v75, v55, v75\n"
      "v_accvgpr_write_b32 a228, v72\n"
      "v_accvgpr_write_b32 a229, v73\n"
      "v_accvgpr_write_b32 a230, v74\n"
      "v_accvgpr_write_b32 a231, v75\n"
      "v_mfma_f32_16x16x32_bf16 v[140:143], v[72:75], v[56:59], v[140:143]\n"
      "s_waitcnt vmcnt(8) lgkmcnt(0)\n"
      "s_barrier\n"
      "ds_read_b64_tr_b16 v[60:61], v53 offset:33792\n"
      "ds_read_b64_tr_b16 v[62:63], v53 offset:35712\n"
      "ds_read_b64_tr_b16 a[24:25], v52 offset:33936\n"
      "ds_read_b64_tr_b16 a[26:27], v52 offset:35856\n"
      "ds_read_b64_tr_b16 a[96:97], v52 offset:34080\n"
      "ds_read_b64_tr_b16 a[98:99], v52 offset:36000\n"
      "ds_read_b64_tr_b16 a[168:169], v52 offset:33792\n"
      "ds_read_b64_tr_b16 a[170:171], v52 offset:35712\n"
      "ds_bpermute_b32 v72, v54, v68\n"
      "ds_bpermute_b32 v73, v54, v69\n"
      "ds_bpermute_b32 v74, v54, v70\n"
      "ds_bpermute_b32 v75, v54, v71\n"
      "v_mfma_f32_16x16x32_bf16 v[136:139], a[20:23], v[56:59], v[136:139]\n"
      "v_mfma_f32_16x16x32_bf16 v[144:147], v[56:59], a[92:95], v[144:147]\n"
      "v_mfma_f32_16x16x32_bf16 v[148:151], a[164:167], v[56:59], v[148:151]\n"
      "s_add_u32 m0, s43, 82432\n"
      "s_nop 0\n"
      "buffer_load_dwordx4 v48, s[12:15], s51 offen offset:0 lds\n"
      "buffer_load_dwordx4 v49, s[12:15], s51 offen offset:1024 lds\n"
      "buffer_load_dwordx4 v50, s[12:15], s51 offen offset:2048 lds\n"
      "buffer_load_dwordx4 v51, s[12:15], s51 offen offset:3072 lds\n"
      "s_add_u32 s51, s51, 110592\n"
      "v_and_b32 v64, v55, v64\n"
      "v_and_b32 v65, v55, v65\n"
      "v_and_b32 v66, v55, v66\n"
      "v_and_b32 v67, v55, v67\n"
      "v_accvgpr_write_b32 a232, v64\n"
      "v_accvgpr_write_b32 a233, v65\n"
      "v_accvgpr_write_b32 a234, v66\n"
      "v_accvgpr_write_b32 a235, v67\n"
      "v_mfma_f32_16x16x32_bf16 v[140:143], v[64:67], v[56:59], v[140:143]\n"
      "s_waitcnt vmcnt(8) lgkmcnt(0)\n"
      "s_barrier\n"
      "ds_read_b64_tr_b16 v[68:69], v53 offset:50688\n"
      "ds_read_b64_tr_b16 v[70:71], v53 offset:52608\n"
      "ds_read_b64_tr_b16 a[28:29], v52 offset:50832\n"
      "ds_read_b64_tr_b16 a[30:31], v52 offset:52752\n"
      "ds_read_b64_tr_b16 a[100:101], v52 offset:50976\n"
      "ds_read_b64_tr_b16 a[102:103], v52 offset:52896\n"
      "ds_read_b64_tr_b16 a[172:173], v52 offset:50688\n"
      "ds_read_b64_tr_b16 a[174:175], v52 offset:52608\n"
      "ds_bpermute_b32 v64, v54, v60\n"
      "ds_bpermute_b32 v65, v54, v61\n"
      "ds_bpermute_b32 v66, v54, v62\n"
      "ds_bpermute_b32 v67, v54, v63\n"
      "v_mfma_f32_16x16x32_bf16 v[136:139], a[24:27], v[56:59], v[136:139]\n"
      "v_mfma_f32_16x16x32_bf16 v[144:147], v[56:59], a[96:99], v[144:147]\n"
      "v_mfma_f32_16x16x32_bf16 v[148:151], a[168:171], v[56:59], v[148:151]\n"
      "s_add_u32 m0, s43, 99328\n"
      "s_nop 0\n"
      "buffer_load_dwordx4 v48, s[12:15], s51 offen offset:0 lds\n"
      "buffer_load_dwordx4 v49, s[12:15], s51 offen offset:1024 lds\n"
      "buffer_load_dwordx4 v50, s[12:15], s51 offen offset:2048 lds\n"
      "buffer_load_dwordx4 v51, s[12:15], s51 offen offset:3072 lds\n"
      "s_add_u32 s51, s51, 110592\n"
      "v_and_b32 v72, v55, v72\n"
      "v_and_b32 v73, v55, v73\n"
      "v_and_b32 v74, v55, v74\n"
      "v_and_b32 v75, v55, v75\n"
      "v_accvgpr_write_b32 a236, v72\n"
      "v_accvgpr_write_b32 a237, v73\n"
      "v_accvgpr_write_b32 a238, v74\n"
      "v_accvgpr_write_b32 a239, v75\n"
      "v_mfma_f32_16x16x32_bf16 v[140:143], v[72:75], v[56:59], v[140:143]\n"
      "s_waitcnt vmcnt(8) lgkmcnt(0)\n"
      "s_barrier\n"
      "ds_read_b64_tr_b16 v[60:61], v53 offset:0\n"
      "ds_read_b64_tr_b16 v[62:63], v53 offset:1920\n"
      "ds_read_b64_tr_b16 a[32:33], v52 offset:144\n"
      "ds_read_b64_tr_b16 a[34:35], v52 offset:2064\n"
      "ds_read_b64_tr_b16 a[104:105], v52 offset:288\n"
      "ds_read_b64_tr_b16 a[106:107], v52 offset:2208\n"
      "ds_read_b64_tr_b16 a[176:177], v52 offset:0\n"
      "ds_read_b64_tr_b16 a[178:179], v52 offset:1920\n"
      "ds_bpermute_b32 v72, v54, v68\n"
      "ds_bpermute_b32 v73, v54, v69\n"
      "ds_bpermute_b32 v74, v54, v70\n"
      ".Lq_px:\n"
      "s_cbranch_execz .Lq_pr\n"
      "ds_bpermute_b32 v75, v54, v71\n"
      "v_mfma_f32_16x16x32_bf16 v[136:139], a[28:31], v[56:59], v[136:139]\n"
      "v_mfma_f32_16x16x32_bf16 v[144:147], v[56:59], a[100:103], v[144:147]\n"
      "v_mfma_f32_16x16x32_bf16 v[148:151], a[172:175], v[56:59], v[148:151]\n"
      "s_add_u32 m0, s43, 116224\n"
      "s_nop 0\n"
      "buffer_load_dwordx4 v48, s[12:15], s51 offen offset:0 lds\n"
      "buffer_load_dwordx4 v49, s[12:15], s51 offen offset:1024 lds\n"
      "buffer_load_dwordx4 v50, s[12:15], s51 offen offset:2048 lds\n"
      "buffer_load_dwordx4 v51, s[12:15], s51 offen offset:3072 lds\n"
      "s_add_u32 s51, s51, 110592\n"
      "v_and_b32 v64, v55, v64\n"
      "v_and_b32 v65, v55, v65\n"
      "v_and_b32 v66, v55, v66\n"
      "v_and_b32 v67, v55, v67\n"
      "v_accvgpr_write_b32 a240, v64\n"
      "v_accvgpr_write_b32 a241, v65\n"
      "v_accvgpr_write_b32 a242, v66\n"
      "v_accvgpr_write_b32 a243, v67\n"
      "v_mfma_f32_16x16x32_bf16 v[140:143], v[64:67], v[56:59], v[140:143]\n"
      "s_waitcnt vmcnt(8) lgkmcnt(0)\n"
      "s_barrier\n"
      "ds_read_b64_tr_b16 v[68:69], v53 offset:16896\n"
      "ds_read_b64_tr_b16 v[70:71], v53 offset:18816\n"
      "ds_read_b64_tr_b16 a[36:37], v52 offset:17040\n"
      "ds_read_b64_tr_b16 a[38:39], v52 offset:18960\n"
      "ds_read_b64_tr_b16 a[108:109], v52 offset:17184\n"
      "ds_read_b64_tr_b16 a[110:111], v52 offset:19104\n"
      "ds_read_b64_tr_b16 a[180:181], v52 offset:16896\n"
      "ds_read_b64_tr_b16 a[182:183], v52 offset:18816\n"
      "ds_bpermute_b32 v64, v54, v60\n"
      "ds_bpermute_b32 v65, v54, v61\n"
      "ds_bpermute_b32 v66, v54, v62\n"
      "ds_bpermute_b32 v67, v54, v63\n"
      "v_mfma_f32_16x16x32_bf16 v[136:139], a[32:35], v[56:59], v[136:139]\n"
      "v_mfma_f32_16x16x32_bf16 v[144:147], v[56:59], a[104:107], v[144:147]\n"
      "v_mfma_f32_16x16x32_bf16 v[148:151], a[176:179], v[56:59], v[148:151]\n"
      "s_add_u32 m0, s43, 65536\n"
      "s_nop 0\n"
      "buffer_load_dwordx4 v48, s[12:15], s51 offen offset:0 lds\n"
      "buffer_load_dwordx4 v49, s[12:15], s51 offen offset:1024 lds\n"
      "buffer_load_dwordx4 v50, s[12:15], s51 offen offset:2048 lds\n"
      "buffer_load_dwordx4 v51, s[12:15], s51 offen offset:3072 lds\n"
      "s_add_u32 s51, s51, 110592\n"
      "v_and_b32 v72, v55, v72\n"
      "v_and_b32 v73, v55, v73\n"
      "v_and_b32 v74, v55, v74\n"
      "v_and_b32 v75, v55, v75\n"
      "v_accvgpr_write_b32 a244, v72\n"
      "v_accvgpr_write_b32 a245, v73\n"
      "v_accvgpr_write_b32 a246, v74\n"
      "v_accvgpr_write_b32 a247, v75\n"
      "v_mfma_f32_16x16x32_bf16 v[140:143], v[72:75], v[56:59], v[140:143]\n"
      "s_waitcnt vmcnt(8) lgkmcnt(0)\n"
      "s_barrier\n"
      "ds_read_b64_tr_b16 v[60:61], v53 offset:33792\n"
      "ds_read_b64_tr_b16 v[62:63], v53 offset:35712\n"
      "ds_read_b64_tr_b16 a[40:41], v52 offset:33936\n"
      "ds_read_b64_tr_b16 a[42:43], v52 offset:35856\n"
      "ds_read_b64_tr_b16 a[112:113], v52 offset:34080\n"
      "ds_read_b64_tr_b16 a[114:115], v52 offset:36000\n"
      "ds_read_b64_tr_b16 a[184:185], v52 offset:33792\n"
      "ds_read_b64_tr_b16 a[186:187], v52 offset:35712\n"
      "ds_bpermute_b32 v72, v54, v68\n"
      "ds_bpermute_b32 v73, v54, v69\n"
      "ds_bpermute_b32 v74, v54, v70\n"
      "ds_bpermute_b32 v75, v54, v71\n"
      "v_mfma_f32_16x16x32_bf16 v[136:139], a[36:39], v[56:59], v[136:139]\n"
      "v_mfma_f32_16x16x32_bf16 v[144:147], v[56:59], a[108:111], v[144:147]\n"
      "v_mfma_f32_16x16x32_bf16 v[148:151], a[180:183], v[56:59], v[148:151]\n"
      "s_add_u32 m0, s43, 82432\n"
      "s_nop 0\n"
      "buffer_load_dwordx4 v48, s[12:15], s51 offen offset:0 lds\n"
      "buffer_load_dwordx4 v49, s[12:15], s51 offen offset:1024 lds\n"
      "buffer_load_dwordx4 v50, s[12:15], s51 offen offset:2048 lds\n"
      "buffer_load_dwordx4 v51, s[12:15], s51 offen offset:3072 lds\n"
      "s_add_u32 s51, s51, 110592\n"
      "v_and_b32 v64, v55, v64\n"
      "v_and_b32 v65, v55, v65\n"
      "v_and_b32 v66, v55, v66\n"
      "v_and_b32 v67, v55, v67\n"
      "v_accvgpr_write_b32 a248, v64\n"
      "v_accvgpr_write_b32 a249, v65\n"
      "v_accvgpr_write_b32 a250, v66\n"
      "v_accvgpr_write_b32 a251, v67\n"
      "v_mfma_f32_16x16x32_bf16 v[140:143], v[64:67], v[56:59], v[140:143]\n"
      "s_waitcnt vmcnt(8) lgkmcnt(0)\n"
      "s_barrier\n"
      "ds_read_b64_tr_b16 v[68:69], v53 offset:50688\n"
      "ds_read_b64_tr_b16 v[70:71], v53 offset:52608\n"
      "ds_read_b64_tr_b16 a[44:45], v52 offset:50832\n"
      "ds_read_b64_tr_b16 a[46:47], v52 offset:52752\n"
      "ds_read_b64_tr_b16 a[116:117], v52 offset:50976\n"
      "ds_read_b64_tr_b16 a[118:119], v52 offset:52896\n"
      "ds_read_b64_tr_b16 a[188:189], v52 offset:50688\n"
      "ds_read_b64_tr_b16 a[190:191], v52 offset:52608\n"
      "ds_bpermute_b32 v64, v54, v60\n"
      "ds_bpermute_b32 v65, v54, v61\n"
      "ds_bpermute_b32 v66, v54, v62\n"
      "ds_bpermute_b32 v67, v54, v63\n"
      "v_mfma_f32_16x16x32_bf16 v[136:139], a[40:43], v[56:59], v[136:139]\n"
      "v_mfma_f32_16x16x32_bf16 v[144:147], v[56:59], a[112:115], v[144:147]\n"
      "v_mfma_f32_16x16x32_bf16 v[148:151], a[184:187], v[56:59], v[148:151]\n"
      "s_add_u32 m0, s43, 99328\n"
      "s_nop 0\n"
      "buffer_load_dwordx4 v48, s[12:15], s51 offen offset:0 lds\n"
      "buffer_load_dwordx4 v49, s[12:15], s51 offen offset:1024 lds\n"
      "buffer_load_dwordx4 v50, s[12:15], s51 offen offset:2048 lds\n"
      "buffer_load_dwordx4 v51, s[12:15], s51 offen offset:3072 lds\n"
      "s_add_u32 s51, s51, 110592\n"
      "v_and_b32 v72, v55, v72\n"
      "v_and_b32 v73, v55, v73\n"
      "v_and_b32 v74, v55, v74\n"
      "v_and_b32 v75, v55, v75\n"
      "v_accvgpr_write_b32 a252, v72\n"
      "v_accvgpr_write_b32 a253, v73\n"
      "v_accvgpr_write_b32 a254, v74\n"
      "v_accvgpr_write_b32 a255, v75\n"
      "v_mfma_f32_16x16x32_bf16 v[140:143], v[72:75], v[56:59], v[140:143]\n"
      "s_waitcnt vmcnt(8) lgkmcnt(0)\n"
      "s_barrier\n"
      "ds_read_b64_tr_b16 v[60:61], v53 offset:0\n"
      "ds_read_b64_tr_b16 v[62:63], v53 offset:1920\n"
      "ds_read_b64_tr_b16 a[48:49], v52 offset:144\n"
      "ds_read_b64_tr_b16 a[50:51], v52 offset:2064\n"
      "ds_read_b64_tr_b16 a[120:121], v52 offset:288\n"
      "ds_read_b64_tr_b16 a[122:123], v52 offset:2208\n"
      "ds_read_b64_tr_b16 a[192:193], v52 offset:0\n"
      "ds_read_b64_tr_b16 a[194:195], v52 offset:1920\n"
      "ds_bpermute_b32 v72, v54, v68\n"
      "ds_bpermute_b32 v73, v54, v69\n"
      "ds_bpermute_b32 v74, v54, v70\n"
      "ds_bpermute_b32 v75, v54, v71\n"
      "v_mfma_f32_16x16x32_bf16 v[136:139], a[44:47], v[56:59], v[136:139]\n"
      "v_mfma_f32_16x16x32_bf16 v[144:147], v[56:59], a[116:119], v[144:147]\n"
      "v_mfma_f32_16x16x32_bf16 v[148:151], a[188:191], v[56:59], v[148:151]\n"
      "s_add_u32 m0, s43, 116224\n"
      "s_nop 0\n"
      "buffer_load_dwordx4 v48, s[12:15], s51 offen offset:0 lds\n"
      "buffer_load_dwordx4 v49, s[12:15], s51 offen offset:1024 lds\n"
      "buffer_load_dwordx4 v50, s[12:15], s51 offen offset:2048 lds\n"
      "buffer_load_dwordx4 v51, s[12:15], s51 offen offset:3072 lds\n"
      "s_add_u32 s51, s51, 110592\n"
      "v_and_b32 v64, v55, v64\n"
      "v_and_b32 v65, v55, v65\n"
      "v_and_b32 v66, v55, v66\n"
      "v_and_b32 v67, v55, v67\n"
      "v_mov_b32 v16, v64\n"
      "v_mov_b32 v17, v65\n"
      "v_mov_b32 v18, v66\n"
      "v_mov_b32 v19, v67\n"
      "v_mfma_f32_16x16x32_bf16 v[140:143], v[64:67], v[56:59], v[140:143]\n"
      "s_waitcnt vmcnt(8) lgkmcnt(0)\n"
      "s_barrier\n"
      "ds_read_b64_tr_b16 v[68:69], v53 offset:16896\n"
      "ds_read_b64_tr_b16 v[70:71], v53 offset:18816\n"
      "ds_read_b64_tr_b16 a[52:53], v52 offset:17040\n"
      "ds_read_b64_tr_b16 a[54:55], v52 offset:18960\n"
      "ds_read_b64_tr_b16 a[124:125], v52 offset:17184\n"
      "ds_read_b64_tr_b16 a[126:127], v52 offset:19104\n"
      "ds_read_b64_tr_b16 a[196:197], v52 offset:16896\n"
      "ds_read_b64_tr_b16 a[198:199], v52 offset:18816\n"
      "ds_bpermute_b32 v64, v54, v60\n"
      "ds_bpermute_b32 v65, v54, v61\n"
      "ds_bpermute_b32 v66, v54, v62\n"
      "ds_bpermute_b32 v67, v54, v63\n"
      "v_mfma_f32_16x16x32_bf16 v[136:139], a[48:51], v[56:59], v[136:139]\n"
      "v_mfma_f32_16x16x32_bf16 v[144:147], v[56:59], a[120:123], v[144:147]\n"
      "v_mfma_f32_16x16x32_bf16 v[148:151], a[192:195], v[56:59], v[148:151]\n"
      "s_add_u32 m0, s43, 65536\n"
      "s_nop 0\n"
      "buffer_load_dwordx4 v48, s[12:15], s51 offen offset:0 lds\n"
      "buffer_load_dwordx4 v49, s[12:15], s51 offen offset:1024 lds\n"
      "buffer_load_dwordx4 v50, s[12:15], s51 offen offset:2048 lds\n"
      "buffer_load_dwordx4 v51, s[12:15], s51 offen offset:3072 lds\n"
      "s_add_u32 s51, s51, 110592\n"
      "v_and_b32 v72, v55, v72\n"
      "v_and_b32 v73, v55, v73\n"
      "v_and_b32 v74, v55, v74\n"
      "v_and_b32 v75, v55, v75\n"
      "v_mov_b32 v20, v72\n"
      "v_mov_b32 v21, v73\n"
      "v_mov_b32 v22, v74\n"
      "v_mov_b32 v23, v75\n"
      "v_mfma_f32_16x16x32_bf16 v[140:143], v[72:75], v[56:59], v[140:143]\n"
      "s_add_u32 s30, s36, 24576\n"
      "buffer_load_dword v204, v2, s[8:11], s30 offen sc1\n"
      "s_add_u32 s30, s36, 49152\n"
      "buffer_load_dword v205, v2, s[8:11], s30 offen sc1\n"
      "s_waitcnt vmcnt(10) lgkmcnt(0)\n"
      "s_barrier\n"
      "ds_read_b64_tr_b16 v[60:61], v53 offset:33792\n"
      "ds_read_b64_tr_b16 v[62:63], v53 offset:35712\n"
      "ds_read_b64_tr_b16 a[56:57], v52 offset:33936\n"
      "ds_read_b64_tr_b16 a[58:59], v52 offset:35856\n"
      "ds_read_b64_tr_b16 a[128:129], v52 offset:34080\n"
      "ds_read_b64_tr_b16 a[130:131], v52 offset:36000\n"
      "ds_read_b64_tr_b16 a[200:201], v52 offset:33792\n"
      "ds_read_b64_tr_b16 a[202:203], v52 offset:35712\n"
      "ds_bpermute_b32 v72, v54, v68\n"
      "ds_bpermute_b32 v73, v54, v69\n"
      "ds_bpermute_b32 v74, v54, v70\n"
      "ds_bpermute_b32 v75, v54, v71\n"
      "v_mfma_f32_16x16x32_bf16 v[136:139], a[52:55], v[56:59], v[136:139]\n"
      "v_mfma_f32_16x16x32_bf16 v[144:147], v[56:59], a[124:127], v[144:147]\n"
      "v_mfma_f32_16x16x32_bf16 v[148:151], a[196:199], v[56:59], v[148:151]\n"
      "s_add_u32 m0, s43, 82432\n"
      "s_nop 0\n"
      "buffer_load_dwordx4 v48, s[12:15], s51 offen offset:0 lds\n"
      "buffer_load_dwordx4 v49, s[12:15], s51 offen offset:1024 lds\n"
      "buffer_load_dwordx4 v50, s[12:15], s51 offen offset:2048 lds\n"
      "buffer_load_dwordx4 v51, s[12:15], s51 offen offset:3072 lds\n"
      "s_add_u32 s51, s51, 110592\n"
      "v_and_b32 v64, v55, v64\n"
      "v_and_b32 v65, v55, v65\n"
      "v_and_b32 v66, v55, v66\n"
      "v_and_b32 v67, v55, v67\n"
      "v_mov_b32 v24, v64\n"
      "v_mov_b32 v25, v65\n"
      "v_mov_b32 v26, v66\n"
      "v_mov_b32 v27, v67\n"
      "v_mfma_f32_16x16x32_bf16 v[140:143], v[64:67], v[56:59], v[140:143]\n"
      "s_waitcnt vmcnt(10) lgkmcnt(0)\n"
      "s_barrier\n"
      "ds_read_b64_tr_b16 v[68:69], v53 offset:50688\n"
      "ds_read_b64_tr_b16 v[70:71], v53 offset:52608\n"
      "ds_read_b64_tr_b16 a[60:61], v52 offset:50832\n"
      "ds_read_b64_tr_b16 a[62:63], v52 offset:52752\n"
      "ds_read_b64_tr_b16 a[132:133], v52 offset:50976\n"
      "ds_read_b64_tr_b16 a[134:135], v52 offset:52896\n"
      "ds_read_b64_tr_b16 a[204:205], v52 offset:50688\n"
      "ds_read_b64_tr_b16 a[206:207], v52 offset:52608\n"
      "ds_bpermute_b32 v64, v54, v60\n"
      "ds_bpermute_b32 v65, v54, v61\n"
      "ds_bpermute_b32 v66, v54, v62\n"
      "ds_bpermute_b32 v67, v54, v63\n"
      "v_mfma_f32_16x16x32_bf16 v[136:139], a[56:59], v[56:59], v[136:139]\n"
      "v_mfma_f32_16x16x32_bf16 v[144:147], v[56:59], a[128:131], v[144:147]\n"
      "v_mfma_f32_16x16x32_bf16 v[148:151], a[200:203], v[56:59], v[148:151]\n"
      "v_and_b32 v72, v55, v72\n"
      "v_and_b32 v73, v55, v73\n"
      "v_and_b32 v74, v55, v74\n"
      "v_and_b32 v75, v55, v75\n"
      "v_mov_b32 v28, v72\n"
      "v_mov_b32 v29, v73\n"
      "v_mov_b32 v30, v74\n"
      "v_mov_b32 v31, v75\n"
      "v_mfma_f32_16x16x32_bf16 v[140:143], v[72:75], v[56:59], v[140:143]\n"
      "s_mul_i32 s21, s21, 1664525\n"
      "s_add_u32 s21, s21, 1013904223\n"
      "v_mov_b32 v13, s21\n"
      "buffer_store_dword v13, v10, s[8:11], 0 offen sc1\n"
      "v_not_b32 v200, v13\n"
      "v_not_b32 v201, v13\n"
      "v_not_b32 v202, v13\n"
      "v_not_b32 v203, v13\n"
      "s_add_u32 s30, s40, 0\n"
      "buffer_store_dwordx4 v[200:203], v76, s[8:11], s30 offen sc1\n"
      "v_xor_b32 v13, v13, v14\n"
      "s_add_u32 s52, s40, 4294893568\n"
      "s_add_u32 s40, s40, 4294918144\n"
      "s_add_u32 s36, s36, 24576\n"
      "s_mov_b32 s45, s36\n"
      "s_min_u32 s45, s45, s6\n"
      "s_waitcnt vmcnt(7)\n"
      "v_cmp_ne_u32 vcc, s21, v204\n"
      "s_add_u32 m0, s41, 0\n"
      "s_add_u32 s36, s36, 24576\n"
      "s_mov_b32 s46, s45\n"
      "s_cbranch_vccnz .Lsp_q0_1\n"
      ".Lbk_q0_1:\n"
      "buffer_load_dwordx4 v0, s[8:11], s46 offen offset:1024 sc1 lds\n"
      "buffer_load_dwordx4 v0, s[8:11], s46 offen offset:0 sc1 lds\n"
      "buffer_load_dwordx4 v0, s[8:11], s46 offen offset:2048 sc1 lds\n"
      "s_mov_b32 s45, s36\n"
      "s_min_u32 s45, s45, s6\n"
      "buffer_load_dwordx4 v0, s[8:11], s46 offen offset:3072 sc1 lds\n"
      "s_add_u32 m0, s42, 0\n"
      "s_nop 0\n"
      "buffer_load_dwordx4 v1, s[8:11], s46 offen offset:0 sc1 lds\n"
      "s_waitcnt lgkmcnt(0)\n"
      "s_barrier\n"
      "ds_read_b64_tr_b16 v[60:61], v53 offset:0\n"
      "ds_read_b64_tr_b16 v[62:63], v53 offset:1920\n"
      "ds_read_b64_tr_b16 a[64:65], v52 offset:144\n"
      "ds_read_b64_tr_b16 a[66:67], v52 offset:2064\n"
      "ds_read_b64_tr_b16 a[136:137], v52 offset:288\n"
      "ds_read_b64_tr_b16 a[138:139], v52 offset:2208\n"
      "ds_read_b64_tr_b16 a[208:209], v52 offset:0\n"
      "ds_read_b64_tr_b16 a[210:211], v52 offset:1920\n"
      "ds_bpermute_b32 v72, v54, v68\n"
      "ds_bpermute_b32 v73, v54, v69\n"
      "ds_bpermute_b32 v74, v54, v70\n"
      "ds_bpermute_b32 v75, v54, v71\n"
      "v_mfma_f32_16x16x32_bf16 v[136:139], a[60:63], v[56:59], v[136:139]\n"
      "v_mfma_f32_16x16x32_bf16 v[144:147], v[56:59], a[132:135], v[144:147]\n"
      "v_mfma_f32_16x16x32_bf16 v[148:151], a[204:207], v[56:59], v[148:151]\n"
      "v_and_b32 v64, v55, v64\n"
      "v_and_b32 v65, v55, v65\n"
      "v_and_b32 v66, v55, v66\n"
      "v_and_b32 v67, v55, v67\n"
      "v_mov_b32 v32, v64\n"
      "v_mov_b32 v33, v65\n"
      "v_mov_b32 v34, v66\n"
      "v_mov_b32 v35, v67\n"
      "v_mfma_f32_16x16x32_bf16 v[140:143], v[64:67], v[56:59], v[140:143]\n"
      "s_waitcnt vmcnt(7) lgkmcnt(0)\n"
      "s_barrier\n"
      "ds_read_b64_tr_b16 v[68:69], v53 offset:16896\n"
      "ds_read_b64_tr_b16 v[70:71], v53 offset:18816\n"
      "ds_read_b64_tr_b16 a[68:69], v52 offset:17040\n"
      "ds_read_b64_tr_b16 a[70:71], v52 offset:18960\n"
      "ds_read_b64_tr_b16 a[140:141], v52 offset:17184\n"
      "ds_read_b64_tr_b16 a[142:143], v52 offset:19104\n"
      "ds_read_b64_tr_b16 a[212:213], v52 offset:16896\n"
      "ds_read_b64_tr_b16 a[214:215], v52 offset:18816\n"
      "ds_bpermute_b32 v64, v54, v60\n"
      "ds_bpermute_b32 v65, v54, v61\n"
      "ds_bpermute_b32 v66, v54, v62\n"
      "ds_bpermute_b32 v67, v54, v63\n"
      "v_mfma_f32_16x16x32_bf16 v[136:139], a[64:67], v[56:59], v[136:139]\n"
      "v_mfma_f32_16x16x32_bf16 v[144:147], v[56:59], a[136:139], v[144:147]\n"
      "v_mfma_f32_16x16x32_bf16 v[148:151], a[208:211], v[56:59], v[148:151]\n"
      "v_and_b32 v72, v55, v72\n"
      "v_and_b32 v73, v55, v73\n"
      "v_and_b32 v74, v55, v74\n"
      "v_and_b32 v75, v55, v75\n"
      "v_mov_b32 v36, v72\n"
      "v_mov_b32 v37, v73\n"
      "v_mov_b32 v38, v74\n"
      "v_mov_b32 v39, v75\n"
      "v_mfma_f32_16x16x32_bf16 v[140:143], v[72:75], v[56:59], v[140:143]\n"
      "s_waitcnt lgkmcnt(0)\n"
      "ds_bpermute_b32 v72, v54, v68\n"
      "ds_bpermute_b32 v73, v54, v69\n"
      "ds_bpermute_b32 v74, v54, v70\n"
      "ds_bpermute_b32 v75, v54, v71\n"
      "v_mfma_f32_16x16x32_bf16 v[136:139], a[68:71], v[56:59], v[136:139]\n"
      "v_mfma_f32_16x16x32_bf16 v[144:147], v[56:59], a[140:143], v[144:147]\n"
      "v_mfma_f32_16x16x32_bf16 v[148:151], a[212:215], v[56:59], v[148:151]\n"
      "v_and_b32 v64, v55, v64\n"
      "v_and_b32 v65, v55, v65\n"
      "v_and_b32 v66, v55, v66\n"
      "v_and_b32 v67, v55, v67\n"
      "v_mov_b32 v40, v64\n"
      "v_mov_b32 v41, v65\n"
      "v_mov_b32 v42, v66\n"
      "v_mov_b32 v43, v67\n"
      "v_mfma_f32_16x16x32_bf16 v[140:143], v[64:67], v[56:59], v[140:143]\n"
      "s_waitcnt lgkmcnt(0)\n"
      "v_and_b32 v72, v55, v72\n"
      "v_and_b32 v73, v55, v73\n"
      "v_and_b32 v74, v55, v74\n"
      "v_and_b32 v75, v55, v75\n"
      "v_mov_b32 v44, v72\n"
      "v_mov_b32 v45, v73\n"
      "v_mov_b32 v46, v74\n"
      "v_mov_b32 v47, v75\n"
      "v_mfma_f32_16x16x32_bf16 v[140:143], v[72:75], v[56:59], v[140:143]\n"
      "s_add_u32 s36, s36, 24576\n"
      "s_cmp_eq_u32 s36, s37\n"
      "s_cselect_b32 s47, s38, 0\n"
      "s_cselect_b32 s37, s48, s37\n"
      "s_cselect_b32 s49, s39, s53\n"
      "s_add_u32 m0, s41, 20480\n"
      "s_mov_b32 s46, s45\n"
      "s_add_u32 s45, s36, s47\n"
      "v_add_u32 v9, 1, v9\n"
      "v_cmp_ne_u32 vcc, s21, v205\n"
      "s_min_u32 s45, s45, s6\n"
      "s_cbranch_vccnz .Lsp_q1_2\n"
      ".Lbk_q1_2:\n"
      "buffer_load_dwordx4 v0, s[8:11], s46 offen offset:1024 sc1 lds\n"
      "s_waitcnt vmcnt(1)\n"
      "ds_write_b32 v11, v9\n"
      "buffer_load_dwordx4 v0, s[8:11], s46 offen offset:0 sc1 lds\n"
      "buffer_load_dwordx4 v0, s[8:11], s46 offen offset:3072 sc1 lds\n"
      "buffer_load_dword v15, v2, s[8:11], s45 offen sc1\n"
      "ds_read_b32 v10, v12\n"
      "buffer_load_dwordx4 v0, s[8:11], s46 offen offset:2048 sc1 lds\n"
      "s_waitcnt lgkmcnt(0)\n"
      "v_cmp_gt_i32 vcc, v9, v10\n"
      "s_add_u32 m0, s42, 20480\n"
      "s_nop 0\n"
      "buffer_load_dwordx4 v1, s[8:11], s46 offen offset:0 sc1 lds\n"
      "s_cbranch_vccnz .Lsp_qy0_3\n"
      ".Lbk_qy0_3:\n"
      "ds_read_b128 v[52:55], v0 offset:1024\n"
      "ds_read_b128 v[48:51], v0 offset:0\n"
      "ds_read_b128 v[56:59], v0 offset:2048\n"
      "ds_read_b128 v[68:71], v0 offset:5120\n"
      "ds_read_b128 v[64:67], v0 offset:4096\n"
      "ds_read_b128 v[60:63], v0 offset:3072\n"
      "buffer_load_dword v14, v11, s[8:11], s31 offen\n"
      "ds_read2st64_b64 v[212:215], v4 offset0:0 offset1:1\n"
      "ds_read_b128 v[72:75], v0 offset:6144\n"
      "ds_read_b128 v[76:79], v0 offset:7168\n"
      "ds_read_b128 v[88:91], v0 offset:10240\n"
      "ds_read_b128 v[108:111], v0 offset:15360\n"
      "s_mov_b32 s20, 21\n"
      "s_waitcnt lgkmcnt(0)\n"
      "s_nop 4\n"
      "s_branch .Lq_loop\n"
      ".p2align 12\n"
      ".Lq_p0:\n"
      "s_branch .Lq_pr\n"
      ".p2align 5\n"
      ".Lq_loop:\n"
      "v_mfma_f32_16x16x32_bf16 v[120:123], a[0:3], v[48:51], 0\n"
      "ds_read_b128 v[80:83], v0 offset:8192\n"
      "ds_read_b128 v[84:87], v0 offset:9216\n"
      "v_mfma_f32_16x16x32_bf16 v[120:123], a[4:7], v[52:55], v[120:123]\n"
      "ds_read_b128 v[96:99], v0 offset:12288\n"
      "v_mul_f32 v232, 0x3eae1a93, v219\n"
      "v_mfma_f32_16x16x32_bf16 v[120:123], a[8:11], v[56:59], v[120:123]\n"
      "s_waitcnt lgkmcnt(7)\n"
      "ds_read_b128 v[104:107], v0 offset:14336\n"
      "v_fma_f32 v135, -v218, v151, v135\n"
      "v_mfma_f32_16x16x32_bf16 v[120:123], a[12:15], v[60:63], v[120:123]\n"
      "v_fma_f32 v133, -v218, v149, v133\n"
      "v_fma_f32 v134, -v218, v150, v134\n"
      "v_mfma_f32_16x16x32_bf16 v[120:123], a[16:19], v[64:67], v[120:123]\n"
      "ds_read_b128 v[92:95], v0 offset:11264\n"
      "v_fma_f32 v132, -v218, v148, v132\n"
      "v_mfma_f32_16x16x32_bf16 v[120:123], a[20:23], v[68:71], v[120:123]\n"
      "s_waitcnt lgkmcnt(5)\n"
      "ds_read_b64 v[216:217], v4 offset:1024\n"
      "v_cvt_pk_bf16_f32 v176, v132, v133\n"
      "v_mfma_f32_16x16x32_bf16 v[120:123], a[24:27], v[72:75], v[120:123]\n"
      "ds_read_b128 v[100:103], v0 offset:13312\n"
      "v_cvt_pk_bf16_f32 v177, v134, v135\n"
      "v_mfma_f32_16x16x32_bf16 v[120:123], a[28:31], v[76:79], v[120:123]\n"
      "s_waitcnt lgkmcnt(5)\n"
      "v_rcp_f32 v210, v210\n"
      "v_mfma_f32_16x16x32_bf16 v[120:123], a[32:35], v[80:83], v[120:123]\n"
      "v_mul_f32 v204, v204, v210\n"
      "v_add_f32 v212, v212, v214\n"
      "v_mfma_f32_16x16x32_bf16 v[188:191], v[172:175], v[176:179], v[152:155]\n"
      "ds_read_b128 v[112:115], v0 offset:16384\n"
      "v_mfma_f32_16x16x32_bf16 v[120:123], a[36:39], v[84:87], v[120:123]\n"
      "ds_read_b128 v[116:119], v0 offset:17408\n"
      "v_mul_f32 v208, v208, v210\n"
      "v_mfma_f32_16x16x32_bf16 v[120:123], a[40:43], v[88:91], v[120:123]\n"
      "s_waitcnt lgkmcnt(4)\n"
      "s_add_u32 s36, s36, 24576\n"
      "v_mul_f32 v207, v207, v210\n"
      "v_mfma_f32_16x16x32_bf16 v[120:123], a[44:47], v[92:95], v[120:123]\n"
      "v_mul_f32 v209, v209, v210\n"
      "v_cvt_pk_bf16_f32 v230, v208, v209\n"
      "v_mfma_f32_16x16x32_bf16 v[120:123], a[48:51], v[96:99], v[120:123]\n"
      "s_waitcnt lgkmcnt(2)\n"
      "buffer_store_dwordx2 v[230:231], v8, s[8:11], s40 offen sc1\n"
      "v_mfma_f32_16x16x32_bf16 v[120:123], a[52:55], v[100:103], v[120:123]\n"
      "v_mul_f32 v206, v206, v210\n"
      "v_add_f32 v218, v212, v216\n"
      "v_mfma_f32_16x16x32_bf16 v[120:123], a[56:59], v[104:107], v[120:123]\n"
      "s_mov_b32 s46, s45\n"
      "v_add_f32 v213, v213, v215\n"
      "v_mfma_f32_16x16x32_bf16 v[120:123], a[60:63], v[108:111], v[120:123]\n"
      "s_waitcnt lgkmcnt(1)\n"
      "v_cvt_pk_bf16_f32 v229, v206, v207\n"
      "v_mfma_f32_16x16x32_bf16 v[120:123], a[64:67], v[112:115], v[120:123]\n"
      "s_waitcnt lgkmcnt(0)\n"
      "v_mul_f32 v205, v205, v210\n"
      "v_cvt_pk_bf16_f32 v228, v204, v205\n"
      "v_mfma_f32_16x16x32_bf16 v[120:123], a[68:71], v[116:119], v[120:123]\n"
      "buffer_store_dwordx2 v[228:229], v7, s[8:11], s40 offen sc1\n"
      "v_mfma_f32_16x16x32_bf16 v[124:127], a[216:219], v[48:51], 0\n"
      "v_add_f32 v213, v213, v217\n"
      "v_fma_f32 v213, -v218, v218, v213\n"
      "v_mfma_f32_16x16x32_bf16 v[124:127], a[220:223], v[52:55], v[124:127]\n"
      "v_max_f32 v213, 0x3727c5ac, v213\n"
      "s_mov_b32 s45, s36\n"
      "v_mfma_f32_16x16x32_bf16 v[124:127], a[224:227], v[56:59], v[124:127]\n"
      "v_mov_b32 v187, v185\n"
      "v_mfma_f32_16x16x32_bf16 v[124:127], a[228:231], v[60:63], v[124:127]\n"
      "v_rsq_f32 v219, v213\n"
      "v_mfma_f32_16x16x32_bf16 v[124:127], a[232:235], v[64:67], v[124:127]\n"
      "s_add_u32 m0, s41, 40960\n"
      "ds_write_b64 v5, v[218:219] offset:0\n"
      "v_mfma_f32_16x16x32_bf16 v[124:127], a[236:239], v[68:71], v[124:127]\n"
      "s_waitcnt vmcnt(5)\n"
      "v_mov_b32 v186, v184\n"
      "v_cmp_ne_u32 vcc, s21, v15\n"
      "v_mfma_f32_16x16x32_bf16 v[124:127], a[240:243], v[72:75], v[124:127]\n"
      "s_min_u32 s45, s45, s6\n"
      "v_mfma_f32_16x16x32_bf16 v[124:127], a[244:247], v[76:79], v[124:127]\n"
      "v_cndmask_b32 v233, v219, 1.0, s[54:55]\n"
      "v_add_u32 v9, 1, v9\n"
      "v_mfma_f32_16x16x32_bf16 v[124:127], a[248:251], v[80:83], v[124:127]\n"
      "v_fma_f32 v123, -v218, v139, v123\n"
      "v_fma_f32 v122, -v218, v138, v122\n"
      "v_mfma_f32_16x16x32_bf16 v[124:127], a[252:255], v[84:87], v[124:127]\n"
      "v_fma_f32 v120, -v218, v136, v120\n"
      "v_fma_f32 v121, -v218, v137, v121\n"
      "v_mfma_f32_16x16x32_bf16 v[124:127], v[16:19], v[88:91], v[124:127]\n"
      "ds_read_b128 v[224:227], v6 offset:16\n"
      "v_mul_f32 v121, v219, v121\n"
      "v_mfma_f32_16x16x32_bf16 v[124:127], v[20:23], v[92:95], v[124:127]\n"
      "v_mul_f32 v120, v219, v120\n"
      "v_mfma_f32_16x16x32_bf16 v[124:127], v[24:27], v[96:99], v[124:127]\n"
      "s_cbranch_vccnz .Lsp_q2_4\n"
      ".Lbk_q2_4:\n"
      "v_mfma_f32_16x16x32_bf16 v[124:127], v[28:31], v[100:103], v[124:127]\n"
      "buffer_load_dword v15, v2, s[8:11], s45 offen sc1\n"
      "v_mfma_f32_16x16x32_bf16 v[124:127], v[32:35], v[104:107], v[124:127]\n"
      "v_mul_f32 v123, v219, v123\n"
      "v_mul_f32 v122, v219, v122\n"
      "v_mfma_f32_16x16x32_bf16 v[124:127], v[36:39], v[108:111], v[124:127]\n"
      "s_waitcnt vmcnt(4) lgkmcnt(1)\n"
      "ds_write_b32 v11, v9\n"
      "v_mfma_f32_16x16x32_bf16 v[124:127], v[40:43], v[112:115], v[124:127]\n"
      "v_mov_b32 v182, v180\n"
      "v_mfma_f32_16x16x32_bf16 v[124:127], v[44:47], v[116:119], v[124:127]\n"
      "v_cvt_pk_bf16_f32 v173, v122, v123\n"
      "v_mfma_f32_16x16x32_bf16 v[128:131], v[48:51], a[72:75], 0\n"
      "buffer_load_dwordx4 v0, s[8:11], s46 offen offset:1024 sc1 lds\n"
      "v_mfma_f32_16x16x32_bf16 v[128:131], v[52:55], a[76:79], v[128:131]\n"
      "ds_read_b128 v[220:223], v6 offset:0\n"
      "v_cvt_pk_bf16_f32 v172, v120, v121\n"
      "v_mfma_f32_16x16x32_bf16 v[128:131], v[56:59], a[80:83], v[128:131]\n"
      "v_fma_f32 v126, -v218, v142, v126\n"
      "v_fma_f32 v124, -v218, v140, v124\n"
      "v_mfma_f32_16x16x32_bf16 v[128:131], v[60:63], a[84:87], v[128:131]\n"
      "s_add_u32 s52, s52, 24576\n"
      "v_fma_f32 v125, -v218, v141, v125\n"
      "v_mul_f32 v124, v233, v124\n"
      "v_mfma_f32_16x16x32_bf16 v[128:131], v[64:67], a[88:91], v[128:131]\n"
      "v_mul_f32 v126, v219, v126\n"
      "v_mul_f32 v125, v233, v125\n"
      "v_mfma_f32_16x16x32_bf16 v[128:131], v[68:71], a[92:95], v[128:131]\n"
      "v_fma_f32 v127, -v218, v143, v127\n"
      "v_cvt_pk_bf16_f32 v175, v124, v125\n"
      "v_mfma_f32_16x16x32_bf16 v[128:131], v[72:75], a[96:99], v[128:131]\n"
      "v_mul_f32 v127, v219, v127\n"
      "v_cvt_pk_bf16_f32 v174, v126, v127\n"
      "v_mfma_f32_16x16x32_bf16 v[128:131], v[76:79], a[100:103], v[128:131]\n"
      "buffer_load_dwordx4 v0, s[8:11], s46 offen offset:2048 sc1 lds\n"
      "v_mfma_f32_16x16x32_bf16 v[192:195], v[172:175], v[176:179], v[156:159]\n"
      "ds_read_b32 v10, v12\n"
      "v_mfma_f32_16x16x32_bf16 v[196:199], v[172:175], v[164:167], v[168:171]\n"
      "v_mov_b32 v183, v181\n"
      "v_mov_b32 v178, v175\n"
      "v_mfma_f32_16x16x32_bf16 v[128:131], v[80:83], a[104:107], v[128:131]\n"
      "buffer_load_dwordx4 v0, s[8:11], s46 offen offset:0 sc1 lds\n"
      "v_mfma_f32_16x16x32_bf16 v[128:131], v[84:87], a[108:111], v[128:131]\n"
      "v_max_f32 v235, v194, v195\n"
      "v_max_f32 v234, v192, v193\n"
      "v_mfma_f32_16x16x32_bf16 v[128:131], v[88:91], a[112:115], v[128:131]\n"
      "v_max3_f32 v234, v188, v189, v234\n"
      "v_max3_f32 v235, v190, v191, v235\n"
      "v_mfma_f32_16x16x32_bf16 v[128:131], v[92:95], a[116:119], v[128:131]\n"
      "v_max_f32 v236, v234, v235\n"
      "v_mov_b32 v237, v236\n"
      "v_mfma_f32_16x16x32_bf16 v[128:131], v[96:99], a[120:123], v[128:131]\n"
      "v_permlane16_swap_b32 v236, v237\n"
      "v_mfma_f32_16x16x32_bf16 v[128:131], v[100:103], a[124:127], v[128:131]\n"
      "v_max_f32 v236, v236, v237\n"
      "v_mov_b32 v237, v236\n"
      "v_mfma_f32_16x16x32_bf16 v[128:131], v[104:107], a[128:131], v[128:131]\n"
      "s_waitcnt lgkmcnt(0)\n"
      "v_permlane32_swap_b32 v236, v237\n"
      "v_cmp_gt_i32 vcc, v9, v10\n"
      "v_mfma_f32_16x16x32_bf16 v[128:131], v[108:111], a[132:135], v[128:131]\n"
      "buffer_load_dwordx4 v0, s[8:11], s46 offen offset:3072 sc1 lds\n"
      "v_mfma_f32_16x16x32_bf16 v[128:131], v[112:115], a[136:139], v[128:131]\n"
      "s_add_u32 m0, s42, 40960\n"
      "v_max_f32 v236, v236, v237\n"
      "v_mul_f32 v236, v236, v232\n"
      "v_mfma_f32_16x16x32_bf16 v[128:131], v[116:119], a[140:143], v[128:131]\n"
      "s_add_u32 s40, s40, 24576\n"
      "v_fma_f32 v234, v234, v232, -v236\n"
      "v_fma_f32 v235, v235, v232, -v236\n"
      "v_mfma_f32_16x16x32_bf16 v[132:135], a[144:147], v[48:51], 0\n"
      "v_exp_f32 v234, v234\n"
      "v_mfma_f32_16x16x32_bf16 v[132:135], a[148:151], v[52:55], v[132:135]\n"
      "s_cbranch_vccnz .Lsp_qy1_5\n"
      ".Lbk_qy1_5:\n"
      "ds_read_b128 v[48:51], v0 offset:20480\n"
      "v_mfma_f32_16x16x32_bf16 v[132:135], a[152:155], v[56:59], v[132:135]\n"
      "v_exp_f32 v235, v235\n"
      "v_mfma_f32_16x16x32_bf16 v[132:135], a[156:159], v[60:63], v[132:135]\n"
      "ds_read_b128 v[56:59], v0 offset:22528\n"
      "v_fma_f32 v130, -v224, v147, v130\n"
      "v_mfma_f32_16x16x32_bf16 v[132:135], a[160:163], v[64:67], v[132:135]\n"
      "ds_read_b64 v[216:217], v4 offset:21504\n"
      "v_fma_f32 v129, -v222, v146, v129\n"
      "v_mfma_f32_16x16x32_bf16 v[132:135], a[164:167], v[68:71], v[132:135]\n"
      "buffer_load_dwordx4 v1, s[8:11], s46 offen offset:0 sc1 lds\n"
      "v_mfma_f32_16x16x32_bf16 v[132:135], a[168:171], v[72:75], v[132:135]\n"
      "v_cvt_pk_bf16_f32 v234, v234, v234\n"
      "v_fma_f32 v131, -v226, v144, v131\n"
      "v_mfma_f32_16x16x32_bf16 v[132:135], a[172:175], v[76:79], v[132:135]\n"
      "ds_read_b128 v[60:63], v0 offset:23552\n"
      "v_and_b32 v200, v160, v234\n"
      "v_mfma_f32_16x16x32_bf16 v[132:135], a[176:179], v[80:83], v[132:135]\n"
      "v_cvt_pk_bf16_f32 v235, v235, v235\n"
      "v_mul_f32 v129, v223, v129\n"
      "v_mfma_f32_16x16x32_bf16 v[132:135], a[180:183], v[84:87], v[132:135]\n"
      "ds_read_b128 v[52:55], v0 offset:21504\n"
      "v_mul_f32 v131, v227, v131\n"
      "v_mfma_f32_16x16x32_bf16 v[132:135], a[184:187], v[88:91], v[132:135]\n"
      "ds_read_b128 v[88:91], v0 offset:30720\n"
      "v_mul_f32 v130, v225, v130\n"
      "v_cvt_pk_bf16_f32 v185, v198, v199\n"
      "v_mfma_f32_16x16x32_bf16 v[132:135], a[188:191], v[92:95], v[132:135]\n"
      "v_cvt_pk_bf16_f32 v184, v196, v197\n"
      "v_and_b32 v201, v161, v235\n"
      "v_fma_f32 v128, -v220, v145, v128\n"
      "v_mfma_f32_16x16x32_bf16 v[132:135], a[192:195], v[96:99], v[132:135]\n"
      "ds_read_b128 v[68:71], v0 offset:25600\n"
      "v_and_b32 v203, v163, v235\n"
      "v_mfma_f32_16x16x32_bf16 v[132:135], a[196:199], v[100:103], v[132:135]\n"
      "v_mul_f32 v128, v221, v128\n"
      "v_and_b32 v202, v162, v234\n"
      "v_mfma_f32_16x16x32_bf16 v[132:135], a[200:203], v[104:107], v[132:135]\n"
      "v_cvt_pk_bf16_f32 v180, v128, v129\n"
      "v_cvt_pk_bf16_f32 v181, v130, v131\n"
      "v_mfma_f32_16x16x32_bf16 v[208:211], v[184:187], v[200:203], 0\n"
      "ds_read_b128 v[64:67], v0 offset:24576\n"
      "ds_read_b128 v[72:75], v0 offset:26624\n"
      "v_mfma_f32_16x16x32_bf16 v[204:207], v[180:183], v[200:203], 0\n"
      "s_waitcnt vmcnt(6) lgkmcnt(4)\n"
      "buffer_store_dword v13, v3, s[8:11], s52 offen sc1\n"
      "v_mfma_f32_16x16x32_bf16 v[132:135], a[204:207], v[108:111], v[132:135]\n"
      "ds_read2st64_b64 v[212:215], v4 offset0:40 offset1:41\n"
      "v_mfma_f32_16x16x32_bf16 v[132:135], a[208:211], v[112:115], v[132:135]\n"
      "v_rcp_f32 v210, v210\n"
      "v_mfma_f32_16x16x32_bf16 v[132:135], a[212:215], v[116:119], v[132:135]\n"
      "ds_read_b128 v[76:79], v0 offset:27648\n"
      "v_mul_f32 v209, v209, v210\n"
      "v_mfma_f32_16x16x32_bf16 v[120:123], a[0:3], v[48:51], 0\n"
      "ds_read_b128 v[80:83], v0 offset:28672\n"
      "ds_read_b128 v[100:103], v0 offset:33792\n"
      "v_mfma_f32_16x16x32_bf16 v[120:123], a[4:7], v[52:55], v[120:123]\n"
      "ds_read_b128 v[92:95], v0 offset:31744\n"
      "v_mul_f32 v208, v208, v210\n"
      "v_mfma_f32_16x16x32_bf16 v[120:123], a[8:11], v[56:59], v[120:123]\n"
      "ds_read_b128 v[84:87], v0 offset:29696\n"
      "v_fma_f32 v134, -v218, v150, v134\n"
      "v_mfma_f32_16x16x32_bf16 v[120:123], a[12:15], v[60:63], v[120:123]\n"
      "s_waitcnt lgkmcnt(6)\n"
      "ds_read_b128 v[104:107], v0 offset:34816\n"
      "v_fma_f32 v132, -v218, v148, v132\n"
      "v_mfma_f32_16x16x32_bf16 v[120:123], a[16:19], v[64:67], v[120:123]\n"
      "s_mov_b32 s50, s49\n"
      "ds_read_b128 v[96:99], v0 offset:32768\n"
      "v_mfma_f32_16x16x32_bf16 v[120:123], a[20:23], v[68:71], v[120:123]\n"
      "ds_read_b128 v[108:111], v0 offset:35840\n"
      "v_fma_f32 v133, -v218, v149, v133\n"
      "v_mfma_f32_16x16x32_bf16 v[120:123], a[24:27], v[72:75], v[120:123]\n"
      "s_waitcnt lgkmcnt(7)\n"
      "v_fma_f32 v135, -v218, v151, v135\n"
      "v_cvt_pk_bf16_f32 v177, v134, v135\n"
      "v_mfma_f32_16x16x32_bf16 v[120:123], a[28:31], v[76:79], v[120:123]\n"
      "s_waitcnt lgkmcnt(5)\n"
      "ds_read_b128 v[112:115], v0 offset:36864\n"
      "v_cvt_pk_bf16_f32 v176, v132, v133\n"
      "v_mfma_f32_16x16x32_bf16 v[120:123], a[32:35], v[80:83], v[120:123]\n"
      "v_mov_b32 v187, v185\n"
      "v_mul_f32 v232, 0x3eae1a93, v219\n"
      "v_mfma_f32_16x16x32_bf16 v[188:191], v[172:175], v[176:179], v[152:155]\n"
      "s_waitcnt lgkmcnt(4)\n"
      "ds_read_b128 v[116:119], v0 offset:37888\n"
      "v_mul_f32 v207, v207, v210\n"
      "v_mfma_f32_16x16x32_bf16 v[120:123], a[36:39], v[84:87], v[120:123]\n"
      "v_mul_f32 v204, v204, v210\n"
      "v_mfma_f32_16x16x32_bf16 v[120:123], a[40:43], v[88:91], v[120:123]\n"
      "v_cvt_pk_bf16_f32 v230, v208, v209\n"
      "v_add_f32 v213, v213, v215\n"
      "v_mfma_f32_16x16x32_bf16 v[120:123], a[44:47], v[92:95], v[120:123]\n"
      "s_waitcnt lgkmcnt(2)\n"
      "v_mul_f32 v205, v205, v210\n"
      "v_mfma_f32_16x16x32_bf16 v[120:123], a[48:51], v[96:99], v[120:123]\n"
      "buffer_store_dwordx2 v[230:231], v8, s[8:11], s40 offen sc1\n"
      "v_mfma_f32_16x16x32_bf16 v[120:123], a[52:55], v[100:103], v[120:123]\n"
      "v_add_f32 v212, v212, v214\n"
      "v_mfma_f32_16x16x32_bf16 v[120:123], a[56:59], v[104:107], v[120:123]\n"
      "v_cvt_pk_bf16_f32 v228, v204, v205\n"
      "v_mul_f32 v206, v206, v210\n"
      "v_mfma_f32_16x16x32_bf16 v[120:123], a[60:63], v[108:111], v[120:123]\n"
      "s_waitcnt vmcnt(7) lgkmcnt(0)\n"
      "v_cvt_pk_bf16_f32 v229, v206, v207\n"
      "v_cmp_ne_u32 vcc, s21, v15\n"
      "v_mfma_f32_16x16x32_bf16 v[120:123], a[64:67], v[112:115], v[120:123]\n"
      "s_add_u32 m0, s41, 0\n"
      "v_mov_b32 v182, v180\n"
      "v_mfma_f32_16x16x32_bf16 v[120:123], a[68:71], v[116:119], v[120:123]\n"
      "buffer_store_dwordx2 v[228:229], v7, s[8:11], s40 offen sc1\n"
      "v_mfma_f32_16x16x32_bf16 v[124:127], a[216:219], v[48:51], 0\n"
      "v_add_f32 v218, v212, v216\n"
      "v_add_f32_e64 v213, v213, v217\n"
      "v_mfma_f32_16x16x32_bf16 v[124:127], a[220:223], v[52:55], v[124:127]\n"
      "s_add_u32 s36, s36, 24576\n"
      "v_fma_f32 v213, -v218, v218, v213\n"
      "v_mfma_f32_16x16x32_bf16 v[124:127], a[224:227], v[56:59], v[124:127]\n"
      "s_mov_b32 s46, s45\n"
      "s_cbranch_vccnz .Lsp_q0_6\n"
      ".Lbk_q0_6:\n"
      "v_mfma_f32_16x16x32_bf16 v[124:127], a[228:231], v[60:63], v[124:127]\n"
      "buffer_load_dwordx4 v0, s[8:11], s46 offen offset:1024 sc1 lds\n"
      "v_mfma_f32_16x16x32_bf16 v[124:127], a[232:235], v[64:67], v[124:127]\n"
      "v_max_f32 v213, 0x3727c5ac, v213\n"
      "v_fma_f32 v120, -v218, v136, v120\n"
      "v_mfma_f32_16x16x32_bf16 v[124:127], a[236:239], v[68:71], v[124:127]\n"
      "v_mov_b32 v186, v184\n"
      "v_mfma_f32_16x16x32_bf16 v[124:127], a[240:243], v[72:75], v[124:127]\n"
      "v_add_u32 v9, 1, v9\n"
      "v_mfma_f32_16x16x32_bf16 v[124:127], a[244:247], v[76:79], v[124:127]\n"
      "s_add_u32 s40, s40, 24576\n"
      "v_fma_f32 v123, -v218, v139, v123\n"
      "v_mfma_f32_16x16x32_bf16 v[124:127], a[248:251], v[80:83], v[124:127]\n"
      "v_rsq_f32 v219, v213\n"
      "v_mfma_f32_16x16x32_bf16 v[124:127], a[252:255], v[84:87], v[124:127]\n"
      "v_mfma_f32_16x16x32_bf16 v[124:127], v[16:19], v[88:91], v[124:127]\n"
      "v_fma_f32 v121, -v218, v137, v121\n"
      "v_fma_f32 v122, -v218, v138, v122\n"
      "v_mfma_f32_16x16x32_bf16 v[124:127], v[20:23], v[92:95], v[124:127]\n"
      "buffer_load_dwordx4 v0, s[8:11], s46 offen offset:0 sc1 lds\n"
      "v_mfma_f32_16x16x32_bf16 v[124:127], v[24:27], v[96:99], v[124:127]\n"
      "v_mul_f32 v120, v219, v120\n"
      "v_mul_f32 v122, v219, v122\n"
      "v_mfma_f32_16x16x32_bf16 v[124:127], v[28:31], v[100:103], v[124:127]\n"
      "ds_write_b64 v5, v[218:219] offset:512\n"
      "v_mfma_f32_16x16x32_bf16 v[124:127], v[32:35], v[104:107], v[124:127]\n"
      "v_mul_f32 v121, v219, v121\n"
      "v_mul_f32 v123, v219, v123\n"
      "v_mfma_f32_16x16x32_bf16 v[124:127], v[36:39], v[108:111], v[124:127]\n"
      "buffer_load_dwordx4 v0, s[8:11], s46 offen offset:2048 sc1 lds\n"
      "v_mfma_f32_16x16x32_bf16 v[124:127], v[40:43], v[112:115], v[124:127]\n"
      "s_mov_b32 s45, s36\n"
      "ds_read_b128 v[224:227], v6 offset:528\n"
      "v_mfma_f32_16x16x32_bf16 v[124:127], v[44:47], v[116:119], v[124:127]\n"
      "s_min_u32 s45, s45, s6\n"
      "v_cndmask_b32 v233, v219, 1.0, s[54:55]\n"
      "v_mfma_f32_16x16x32_bf16 v[128:131], v[48:51], a[72:75], 0\n"
      "s_add_u32 s52, s52, 24576\n"
      "v_cvt_pk_bf16_f32 v172, v120, v121\n"
      "v_cvt_pk_bf16_f32 v173, v122, v123\n"
      "v_mfma_f32_16x16x32_bf16 v[128:131], v[52:55], a[76:79], v[128:131]\n"
      "buffer_load_dwordx4 v0, s[8:11], s46 offen offset:3072 sc1 lds\n"
      "v_mfma_f32_16x16x32_bf16 v[128:131], v[56:59], a[80:83], v[128:131]\n"
      "v_fma_f32 v126, -v218, v142, v126\n"
      "v_fma_f32 v124, -v218, v140, v124\n"
      "v_mul_f32 v126, v219, v126\n"
      "v_mfma_f32_16x16x32_bf16 v[128:131], v[60:63], a[84:87], v[128:131]\n"
      "v_fma_f32 v125, -v218, v141, v125\n"
      "v_mul_f32 v125, v233, v125\n"
      "v_mfma_f32_16x16x32_bf16 v[128:131], v[64:67], a[88:91], v[128:131]\n"
      "s_waitcnt vmcnt(7) lgkmcnt(1)\n"
      "ds_write_b32 v11, v9\n"
      "v_mfma_f32_16x16x32_bf16 v[128:131], v[68:71], a[92:95], v[128:131]\n"
      "v_fma_f32 v127, -v218, v143, v127\n"
      "v_mul_f32 v124, v233, v124\n"
      "v_mul_f32 v127, v219, v127\n"
      "v_mfma_f32_16x16x32_bf16 v[128:131], v[72:75], a[96:99], v[128:131]\n"
      "v_cvt_pk_bf16_f32 v174, v126, v127\n"
      "v_cvt_pk_bf16_f32 v175, v124, v125\n"
      "v_mfma_f32_16x16x32_bf16 v[128:131], v[76:79], a[100:103], v[128:131]\n"
      "buffer_load_dword v15, v2, s[8:11], s45 offen sc1\n"
      "v_mfma_f32_16x16x32_bf16 v[192:195], v[172:175], v[176:179], v[156:159]\n"
      "v_mov_b32 v183, v181\n"
      "v_mfma_f32_16x16x32_bf16 v[196:199], v[172:175], v[164:167], v[168:171]\n"
      "ds_read_b128 v[220:223], v6 offset:512\n"
      "s_add_u32 m0, s42, 0\n"
      "v_mfma_f32_16x16x32_bf16 v[128:131], v[80:83], a[104:107], v[128:131]\n"
      "buffer_load_dwordx4 v1, s[8:11], s46 offen offset:0 sc1 lds\n"
      "v_mfma_f32_16x16x32_bf16 v[128:131], v[84:87], a[108:111], v[128:131]\n"
      "v_max_f32 v235, v194, v195\n"
      "v_max3_f32 v235, v190, v191, v235\n"
      "v_mfma_f32_16x16x32_bf16 v[128:131], v[88:91], a[112:115], v[128:131]\n"
      "v_max_f32 v234, v192, v193\n"
      "v_max3_f32 v234, v188, v189, v234\n"
      "v_mfma_f32_16x16x32_bf16 v[128:131], v[92:95], a[116:119], v[128:131]\n"
      "ds_read_b32 v10, v12\n"
      "v_max_f32 v236, v234, v235\n"
      "v_mfma_f32_16x16x32_bf16 v[128:131], v[96:99], a[120:123], v[128:131]\n"
      "v_mov_b32 v237, v236\n"
      "v_cvt_pk_bf16_f32 v185, v198, v199\n"
      "v_mfma_f32_16x16x32_bf16 v[128:131], v[100:103], a[124:127], v[128:131]\n"
      "v_permlane16_swap_b32 v236, v237\n"
      "v_mfma_f32_16x16x32_bf16 v[128:131], v[104:107], a[128:131], v[128:131]\n"
      "v_max_f32 v236, v236, v237\n"
      "v_mov_b32 v237, v236\n"
      "v_mfma_f32_16x16x32_bf16 v[128:131], v[108:111], a[132:135], v[128:131]\n"
      "v_permlane32_swap_b32 v236, v237\n"
      "v_mfma_f32_16x16x32_bf16 v[128:131], v[112:115], a[136:139], v[128:131]\n"
      "s_waitcnt lgkmcnt(0)\n"
      "v_cmp_gt_i32 vcc, v9, v10\n"
      "v_max_f32 v236, v236, v237\n"
      "v_mfma_f32_16x16x32_bf16 v[128:131], v[116:119], a[140:143], v[128:131]\n"
      "v_mul_f32_e64 v236, v236, v232\n"
      "v_fma_f32 v235, v235, v232, -v236\n"
      "v_mfma_f32_16x16x32_bf16 v[132:135], a[144:147], v[48:51], 0\n"
      "v_exp_f32_e64 v235, v235\n"
      "v_mfma_f32_16x16x32_bf16 v[132:135], a[148:151], v[52:55], v[132:135]\n"
      "s_cbranch_vccnz .Lsp_qy2_7\n"
      ".Lbk_qy2_7:\n"
      "v_cvt_pk_bf16_f32 v235, v235, v235\n"
      "v_mfma_f32_16x16x32_bf16 v[132:135], a[152:155], v[56:59], v[132:135]\n"
      "v_fma_f32 v130, -v224, v147, v130\n"
      "v_mul_f32 v130, v225, v130\n"
      "v_mfma_f32_16x16x32_bf16 v[132:135], a[156:159], v[60:63], v[132:135]\n"
      "v_fma_f32 v129, -v222, v146, v129\n"
      "v_cvt_pk_bf16_f32 v184, v196, v197\n"
      "v_mfma_f32_16x16x32_bf16 v[132:135], a[160:163], v[64:67], v[132:135]\n"
      "v_fma_f32 v234, v234, v232, -v236\n"
      "v_fma_f32 v131, -v226, v144, v131\n"
      "v_mfma_f32_16x16x32_bf16 v[132:135], a[164:167], v[68:71], v[132:135]\n"
      "ds_read_b128 v[52:55], v0 offset:41984\n"
      "ds_read_b128 v[64:67], v0 offset:45056\n"
      "v_mfma_f32_16x16x32_bf16 v[132:135], a[168:171], v[72:75], v[132:135]\n"
      "ds_read_b128 v[56:59], v0 offset:43008\n"
      "v_mul_f32 v129, v223, v129\n"
      "v_mfma_f32_16x16x32_bf16 v[132:135], a[172:175], v[76:79], v[132:135]\n"
      "v_exp_f32 v234, v234\n"
      "v_mfma_f32_16x16x32_bf16 v[132:135], a[176:179], v[80:83], v[132:135]\n"
      "v_fma_f32 v128, -v220, v145, v128\n"
      "v_and_b32 v203, v163, v235\n"
      "v_mfma_f32_16x16x32_bf16 v[132:135], a[180:183], v[84:87], v[132:135]\n"
      "v_cvt_pk_bf16_f32 v234, v234, v234\n"
      "v_and_b32 v201, v161, v235\n"
      "v_mfma_f32_16x16x32_bf16 v[132:135], a[184:187], v[88:91], v[132:135]\n"
      "ds_read_b128 v[48:51], v0 offset:40960\n"
      "v_and_b32 v202, v162, v234\n"
      "v_mfma_f32_16x16x32_bf16 v[132:135], a[188:191], v[92:95], v[132:135]\n"
      "ds_read_b128 v[60:63], v0 offset:44032\n"
      "v_mul_f32 v128, v221, v128\n"
      "v_mfma_f32_16x16x32_bf16 v[132:135], a[192:195], v[96:99], v[132:135]\n"
      "ds_read_b128 v[76:79], v0 offset:48128\n"
      "v_cvt_pk_bf16_f32 v180, v128, v129\n"
      "v_mfma_f32_16x16x32_bf16 v[132:135], a[196:199], v[100:103], v[132:135]\n"
      "ds_read_b128 v[100:103], v0 offset:54272\n"
      "v_and_b32 v200, v160, v234\n"
      "v_mfma_f32_16x16x32_bf16 v[132:135], a[200:203], v[104:107], v[132:135]\n"
      "v_mul_f32 v131, v227, v131\n"
      "v_cvt_pk_bf16_f32 v181, v130, v131\n"
      "v_mfma_f32_16x16x32_bf16 v[208:211], v[184:187], v[200:203], 0\n"
      "s_waitcnt vmcnt(6) lgkmcnt(4)\n"
      "buffer_store_dword v13, v3, s[8:11], s52 offen sc1\n"
      "v_mfma_f32_16x16x32_bf16 v[204:207], v[180:183], v[200:203], 0\n"
      "ds_read_b128 v[84:87], v0 offset:50176\n"
      "ds_read_b128 v[72:75], v0 offset:47104\n"
      "v_mfma_f32_16x16x32_bf16 v[132:135], a[204:207], v[108:111], v[132:135]\n"
      "ds_read_b128 v[68:71], v0 offset:46080\n"
      "v_mov_b32 v178, v175\n"
      "v_mfma_f32_16x16x32_bf16 v[132:135], a[208:211], v[112:115], v[132:135]\n"
      "v_rcp_f32 v210, v210\n"
      "v_mfma_f32_16x16x32_bf16 v[132:135], a[212:215], v[116:119], v[132:135]\n"
      "s_waitcnt lgkmcnt(4)\n"
      "ds_read_b128 v[92:95], v0 offset:52224\n"
      "v_mul_f32 v205, v205, v210\n"
      "v_mfma_f32_16x16x32_bf16 v[120:123], a[0:3], v[48:51], 0\n"
      "s_add_u32 s36, s36, 24576\n"
      "ds_read_b128 v[80:83], v0 offset:49152\n"
      "v_mov_b32 v182, v180\n"
      "v_mfma_f32_16x16x32_bf16 v[120:123], a[4:7], v[52:55], v[120:123]\n"
      "ds_read_b128 v[96:99], v0 offset:53248\n"
      "v_mfma_f32_16x16x32_bf16 v[120:123], a[8:11], v[56:59], v[120:123]\n"
      "ds_read_b128 v[88:91], v0 offset:51200\n"
      "v_fma_f32 v132, -v218, v148, v132\n"
      "v_mfma_f32_16x16x32_bf16 v[120:123], a[12:15], v[60:63], v[120:123]\n"
      "s_cmp_eq_u32 s36, s37\n"
      "v_fma_f32 v133, -v218, v149, v133\n"
      "v_cvt_pk_bf16_f32 v176, v132, v133\n"
      "v_mfma_f32_16x16x32_bf16 v[120:123], a[16:19], v[64:67], v[120:123]\n"
      "s_waitcnt lgkmcnt(4)\n"
      "ds_read2st64_b64 v[212:215], v4 offset0:80 offset1:81\n"
      "s_cselect_b32 s47, s38, 0\n"
      "v_mfma_f32_16x16x32_bf16 v[120:123], a[20:23], v[68:71], v[120:123]\n"
      "s_cselect_b32 s37, s48, s37\n"
      "ds_read_b128 v[112:115], v0 offset:57344\n"
      "v_fma_f32 v135, -v218, v151, v135\n"
      "v_mfma_f32_16x16x32_bf16 v[120:123], a[24:27], v[72:75], v[120:123]\n"
      "s_cselect_b32 s49, s39, s53\n"
      "v_fma_f32 v134, -v218, v150, v134\n"
      "v_mov_b32 v183, v181\n"
      "v_mfma_f32_16x16x32_bf16 v[120:123], a[28:31], v[76:79], v[120:123]\n"
      "s_waitcnt lgkmcnt(4)\n"
      "ds_read_b128 v[104:107], v0 offset:55296\n"
      "v_cvt_pk_bf16_f32 v177, v134, v135\n"
      "v_mfma_f32_16x16x32_bf16 v[120:123], a[32:35], v[80:83], v[120:123]\n"
      "ds_read_b128 v[108:111], v0 offset:56320\n"
      "v_mul_f32 v206, v206, v210\n"
      "v_mfma_f32_16x16x32_bf16 v[188:191], v[172:175], v[176:179], v[152:155]\n"
      "ds_read_b64 v[216:217], v4 offset:41984\n"
      "v_mul_f32 v207, v207, v210\n"
      "v_mfma_f32_16x16x32_bf16 v[120:123], a[36:39], v[84:87], v[120:123]\n"
      "s_waitcnt lgkmcnt(5)\n"
      "s_add_u32 m0, s41, 20480\n"
      "ds_read_b128 v[116:119], v0 offset:58368\n"
      "v_mfma_f32_16x16x32_bf16 v[120:123], a[40:43], v[88:91], v[120:123]\n"
      "s_mov_b32 s46, s45\n"
      "s_add_u32 s45, s36, s47\n"
      "v_mul_f32 v209, v209, v210\n"
      "v_mfma_f32_16x16x32_bf16 v[120:123], a[44:47], v[92:95], v[120:123]\n"
      "v_cvt_pk_bf16_f32 v229, v206, v207\n"
      "v_mfma_f32_16x16x32_bf16 v[120:123], a[48:51], v[96:99], v[120:123]\n"
      "s_waitcnt lgkmcnt(4)\n"
      "v_add_f32 v213, v213, v215\n"
      "v_mfma_f32_16x16x32_bf16 v[120:123], a[52:55], v[100:103], v[120:123]\n"
      "s_waitcnt lgkmcnt(3)\n"
      "v_mul_f32 v232, 0x3eae1a93, v219\n"
      "v_mul_f32 v204, v204, v210\n"
      "v_mfma_f32_16x16x32_bf16 v[120:123], a[56:59], v[104:107], v[120:123]\n"
      "s_waitcnt lgkmcnt(2)\n"
      "v_mul_f32 v208, v208, v210\n"
      "v_cvt_pk_bf16_f32 v230, v208, v209\n"
      "v_mfma_f32_16x16x32_bf16 v[120:123], a[60:63], v[108:111], v[120:123]\n"
      "v_mfma_f32_16x16x32_bf16 v[120:123], a[64:67], v[112:115], v[120:123]\n"
      "s_waitcnt lgkmcnt(0)\n"
      "buffer_store_dwordx2 v[230:231], v8, s[8:11], s40 offen sc1\n"
      "v_mfma_f32_16x16x32_bf16 v[120:123], a[68:71], v[116:119], v[120:123]\n"
      "v_cvt_pk_bf16_f32 v228, v204, v205\n"
      "v_mfma_f32_16x16x32_bf16 v[124:127], a[216:219], v[48:51], 0\n"
      "v_add_f32 v212, v212, v214\n"
      "v_add_f32 v213, v213, v217\n"
      "v_mfma_f32_16x16x32_bf16 v[124:127], a[220:223], v[52:55], v[124:127]\n"
      "v_add_f32 v218, v212, v216\n"
      "v_fma_f32 v213, -v218, v218, v213\n"
      "v_mfma_f32_16x16x32_bf16 v[124:127], a[224:227], v[56:59], v[124:127]\n"
      "buffer_store_dwordx2 v[228:229], v7, s[8:11], s40 offen sc1\n"
      "v_mfma_f32_16x16x32_bf16 v[124:127], a[228:231], v[60:63], v[124:127]\n"
      "s_add_u32 s52, s52, 24576\n"
      "v_fma_f32 v123, -v218, v139, v123\n"
      "v_add_u32 v9, 1, v9\n"
      "v_mfma_f32_16x16x32_bf16 v[124:127], a[232:235], v[64:67], v[124:127]\n"
      "v_fma_f32 v122, -v218, v138, v122\n"
      "v_mfma_f32_16x16x32_bf16 v[124:127], a[236:239], v[68:71], v[124:127]\n"
      "s_waitcnt vmcnt(4)\n"
      "v_max_f32 v213, 0x3727c5ac, v213\n"
      "v_cmp_ne_u32 vcc, s21, v15\n"
      "v_mfma_f32_16x16x32_bf16 v[124:127], a[240:243], v[72:75], v[124:127]\n"
      "v_rsq_f32 v219, v213\n"
      "v_mfma_f32_16x16x32_bf16 v[124:127], a[244:247], v[76:79], v[124:127]\n"
      "v_mov_b32 v186, v184\n"
      "v_fma_f32 v120, -v218, v136, v120\n"
      "v_mfma_f32_16x16x32_bf16 v[124:127], a[248:251], v[80:83], v[124:127]\n"
      "s_min_u32 s45, s45, s6\n"
      "ds_write_b64 v5, v[218:219] offset:1024\n"
      "v_mfma_f32_16x16x32_bf16 v[124:127], a[252:255], v[84:87], v[124:127]\n"
      "s_cbranch_vccnz .Lsp_q1_8\n"
      ".Lbk_q1_8:\n"
      "v_mfma_f32_16x16x32_bf16 v[124:127], v[16:19], v[88:91], v[124:127]\n"
      "buffer_load_dwordx4 v0, s[8:11], s46 offen offset:1024 sc1 lds\n"
      "v_mfma_f32_16x16x32_bf16 v[124:127], v[20:23], v[92:95], v[124:127]\n"
      "v_cndmask_b32 v233, v219, 1.0, s[54:55]\n"
      "v_mul_f32 v123, v219, v123\n"
      "v_mfma_f32_16x16x32_bf16 v[124:127], v[24:27], v[96:99], v[124:127]\n"
      "ds_read_b128 v[220:223], v6 offset:1024\n"
      "v_mul_f32 v120, v219, v120\n"
      "v_mfma_f32_16x16x32_bf16 v[124:127], v[28:31], v[100:103], v[124:127]\n"
      "v_mul_f32 v122, v219, v122\n"
      "v_fma_f32 v121, -v218, v137, v121\n"
      "v_mfma_f32_16x16x32_bf16 v[124:127], v[32:35], v[104:107], v[124:127]\n"
      "s_waitcnt vmcnt(4)\n"
      "ds_write_b32 v11, v9\n"
      "v_mfma_f32_16x16x32_bf16 v[124:127], v[36:39], v[108:111], v[124:127]\n"
      "buffer_load_dwordx4 v0, s[8:11], s46 offen offset:0 sc1 lds\n"
      "v_mfma_f32_16x16x32_bf16 v[124:127], v[40:43], v[112:115], v[124:127]\n"
      "v_mfma_f32_16x16x32_bf16 v[124:127], v[44:47], v[116:119], v[124:127]\n"
      "v_cvt_pk_bf16_f32 v173, v122, v123\n"
      "v_mfma_f32_16x16x32_bf16 v[128:131], v[48:51], a[72:75], 0\n"
      "v_mul_f32 v121, v219, v121\n"
      "v_cvt_pk_bf16_f32 v172, v120, v121\n"
      "v_mfma_f32_16x16x32_bf16 v[128:131], v[52:55], a[76:79], v[128:131]\n"
      "buffer_load_dwordx4 v0, s[8:11], s46 offen offset:3072 sc1 lds\n"
      "v_mfma_f32_16x16x32_bf16 v[128:131], v[56:59], a[80:83], v[128:131]\n"
      "v_fma_f32 v125, -v218, v141, v125\n"
      "v_fma_f32 v124, -v218, v140, v124\n"
      "v_mfma_f32_16x16x32_bf16 v[128:131], v[60:63], a[84:87], v[128:131]\n"
      "v_fma_f32 v127, -v218, v143, v127\n"
      "v_mul_f32 v125, v233, v125\n"
      "v_mfma_f32_16x16x32_bf16 v[128:131], v[64:67], a[88:91], v[128:131]\n"
      "v_mul_f32 v124, v233, v124\n"
      "v_mul_f32 v127, v219, v127\n"
      "v_mfma_f32_16x16x32_bf16 v[128:131], v[68:71], a[92:95], v[128:131]\n"
      "v_fma_f32 v126, -v218, v142, v126\n"
      "v_mul_f32 v126, v219, v126\n"
      "v_mfma_f32_16x16x32_bf16 v[128:131], v[72:75], a[96:99], v[128:131]\n"
      "v_cvt_pk_bf16_f32 v175, v124, v125\n"
      "v_cvt_pk_bf16_f32 v174, v126, v127\n"
      "v_mfma_f32_16x16x32_bf16 v[128:131], v[76:79], a[100:103], v[128:131]\n"
      "buffer_load_dword v15, v2, s[8:11], s45 offen sc1\n"
      "v_mfma_f32_16x16x32_bf16 v[192:195], v[172:175], v[176:179], v[156:159]\n"
      "ds_read_b128 v[224:227], v6 offset:1040\n"
      "v_mov_b32 v187, v185\n"
      "v_mfma_f32_16x16x32_bf16 v[196:199], v[172:175], v[164:167], v[168:171]\n"
      "ds_read_b32 v10, v12\n"
      "v_mfma_f32_16x16x32_bf16 v[128:131], v[80:83], a[104:107], v[128:131]\n"
      "buffer_load_dwordx4 v0, s[8:11], s46 offen offset:2048 sc1 lds\n"
      "v_mfma_f32_16x16x32_bf16 v[128:131], v[84:87], a[108:111], v[128:131]\n"
      "v_med3_f32 v234, v192, v193, s50\n"
      "v_med3_f32 v235, v194, v195, s50\n"
      "v_mfma_f32_16x16x32_bf16 v[128:131], v[88:91], a[112:115], v[128:131]\n"
      "v_cvt_pk_bf16_f32 v185, v198, v199\n"
      "v_max3_f32 v234, v188, v189, v234\n"
      "v_mfma_f32_16x16x32_bf16 v[128:131], v[92:95], a[116:119], v[128:131]\n"
      "v_max3_f32 v235, v190, v191, v235\n"
      "v_max_f32 v236, v234, v235\n"
      "v_mfma_f32_16x16x32_bf16 v[128:131], v[96:99], a[120:123], v[128:131]\n"
      "s_waitcnt lgkmcnt(0)\n"
      "v_mov_b32 v237, v236\n"
      "v_cmp_gt_i32 vcc, v9, v10\n"
      "v_mfma_f32_16x16x32_bf16 v[128:131], v[100:103], a[124:127], v[128:131]\n"
      "s_add_u32 m0, s42, 20480\n"
      "v_permlane16_swap_b32 v236, v237\n"
      "v_mfma_f32_16x16x32_bf16 v[128:131], v[104:107], a[128:131], v[128:131]\n"
      "v_max_f32 v236, v236, v237\n"
      "v_mov_b32 v237, v236\n"
      "v_mfma_f32_16x16x32_bf16 v[128:131], v[108:111], a[132:135], v[128:131]\n"
      "buffer_load_dwordx4 v1, s[8:11], s46 offen offset:0 sc1 lds\n"
      "v_mfma_f32_16x16x32_bf16 v[128:131], v[112:115], a[136:139], v[128:131]\n"
      ".Lq_p1:\n"
      "s_cbranch_execz .Lq_pr\n"
      "v_permlane32_swap_b32 v236, v237\n"
      "v_mfma_f32_16x16x32_bf16 v[128:131], v[116:119], a[140:143], v[128:131]\n"
      "v_max_f32 v236, v236, v237\n"
      "v_mul_f32_e64 v236, v236, v232\n"
      "v_mfma_f32_16x16x32_bf16 v[132:135], a[144:147], v[48:51], 0\n"
      "s_cbranch_vccnz .Lsp_qy0_9\n"
      ".Lbk_qy0_9:\n"
      "v_fma_f32 v234, v234, v232, -v236\n"
      "v_mfma_f32_16x16x32_bf16 v[132:135], a[148:151], v[52:55], v[132:135]\n"
      "ds_read_b128 v[52:55], v0 offset:1024\n"
      "ds_read_b128 v[48:51], v0 offset:0\n"
      "v_mfma_f32_16x16x32_bf16 v[132:135], a[152:155], v[56:59], v[132:135]\n"
      "v_fma_f32 v131, -v226, v144, v131\n"
      "v_fma_f32 v235, v235, v232, -v236\n"
      "v_mfma_f32_16x16x32_bf16 v[132:135], a[156:159], v[60:63], v[132:135]\n"
      "ds_read_b128 v[56:59], v0 offset:2048\n"
      "v_fma_f32 v128, -v220, v145, v128\n"
      "v_mfma_f32_16x16x32_bf16 v[132:135], a[160:163], v[64:67], v[132:135]\n"
      "v_exp_f32 v234, v234\n"
      "v_mfma_f32_16x16x32_bf16 v[132:135], a[164:167], v[68:71], v[132:135]\n"
      "v_exp_f32 v235, v235\n"
      "v_mfma_f32_16x16x32_bf16 v[132:135], a[168:171], v[72:75], v[132:135]\n"
      "v_cvt_pk_bf16_f32 v234, v234, v234\n"
      "v_fma_f32 v129, -v222, v146, v129\n"
      "v_mfma_f32_16x16x32_bf16 v[132:135], a[172:175], v[76:79], v[132:135]\n"
      "v_mul_f32 v129, v223, v129\n"
      "v_fma_f32 v130, -v224, v147, v130\n"
      "v_mfma_f32_16x16x32_bf16 v[132:135], a[176:179], v[80:83], v[132:135]\n"
      "v_cvt_pk_bf16_f32 v235, v235, v235\n"
      "v_and_b32 v200, v160, v234\n"
      "v_mfma_f32_16x16x32_bf16 v[132:135], a[180:183], v[84:87], v[132:135]\n"
      "v_cvt_pk_bf16_f32 v184, v196, v197\n"
      "v_and_b32 v201, v161, v235\n"
      "v_mfma_f32_16x16x32_bf16 v[132:135], a[184:187], v[88:91], v[132:135]\n"
      "ds_read_b128 v[68:71], v0 offset:5120\n"
      "v_mul_f32 v130, v225, v130\n"
      "v_mfma_f32_16x16x32_bf16 v[132:135], a[188:191], v[92:95], v[132:135]\n"
      "v_and_b32 v203, v163, v235\n"
      "v_mul_f32_e64 v131, v227, v131\n"
      "v_mfma_f32_16x16x32_bf16 v[132:135], a[192:195], v[96:99], v[132:135]\n"
      "ds_read_b128 v[64:67], v0 offset:4096\n"
      "v_mul_f32_e64 v128, v221, v128\n"
      "v_mfma_f32_16x16x32_bf16 v[132:135], a[196:199], v[100:103], v[132:135]\n"
      "v_and_b32_e64 v202, v162, v234\n"
      "v_cvt_pk_bf16_f32 v181, v130, v131\n"
      "v_mfma_f32_16x16x32_bf16 v[132:135], a[200:203], v[104:107], v[132:135]\n"
      "s_add_u32 s40, s40, 24576\n"
      "ds_read_b128 v[60:63], v0 offset:3072\n"
      "v_cvt_pk_bf16_f32 v180, v128, v129\n"
      "v_mfma_f32_16x16x32_bf16 v[208:211], v[184:187], v[200:203], 0\n"
      "s_waitcnt vmcnt(6) lgkmcnt(3)\n"
      "buffer_store_dword v13, v3, s[8:11], s52 offen sc1\n"
      "ds_read2st64_b64 v[212:215], v4 offset0:0 offset1:1\n"
      "v_mfma_f32_16x16x32_bf16 v[204:207], v[180:183], v[200:203], 0\n"
      "ds_read_b128 v[72:75], v0 offset:6144\n"
      "v_mfma_f32_16x16x32_bf16 v[132:135], a[204:207], v[108:111], v[132:135]\n"
      "ds_read_b128 v[76:79], v0 offset:7168\n"
      "v_mov_b32_e64 v178, v175\n"
      "v_mfma_f32_16x16x32_bf16 v[132:135], a[208:211], v[112:115], v[132:135]\n"
      "ds_read_b128 v[88:91], v0 offset:10240\n"
      "ds_read_b128 v[108:111], v0 offset:15360\n"
      "v_mfma_f32_16x16x32_bf16 v[132:135], a[212:215], v[116:119], v[132:135]\n"
      "s_sub_u32 s20, s20, 1\n"
      "s_cbranch_scc0 .Lq_loop\n"
      ".Lq_tail:\n"
      "v_mul_f32 v232, 0x3eae1a93, v219\n"
      "s_nop 7\n"
      "v_fma_f32 v135, -v218, v151, v135\n"
      "v_fma_f32 v133, -v218, v149, v133\n"
      "v_fma_f32 v134, -v218, v150, v134\n"
      "v_fma_f32 v132, -v218, v148, v132\n"
      "v_cvt_pk_bf16_f32 v176, v132, v133\n"
      "v_cvt_pk_bf16_f32 v177, v134, v135\n"
      "v_rcp_f32 v210, v210\n"
      "s_nop 0\n"
      "v_mul_f32 v204, v204, v210\n"
      "v_mfma_f32_16x16x32_bf16 v[188:191], v[172:175], v[176:179], v[152:155]\n"
      "v_mul_f32 v208, v208, v210\n"
      "v_mul_f32 v207, v207, v210\n"
      "v_mul_f32 v209, v209, v210\n"
      "v_cvt_pk_bf16_f32 v230, v208, v209\n"
      "buffer_store_dwordx2 v[230:231], v8, s[8:11], s40 offen sc1\n"
      "v_mul_f32 v206, v206, v210\n"
      "v_cvt_pk_bf16_f32 v229, v206, v207\n"
      "v_mul_f32 v205, v205, v210\n"
      "v_cvt_pk_bf16_f32 v228, v204, v205\n"
      "buffer_store_dwordx2 v[228:229], v7, s[8:11], s40 offen sc1\n"
      "v_mov_b32 v187, v185\n"
      "v_mov_b32 v186, v184\n"
      "v_mov_b32 v182, v180\n"
      "s_add_u32 s52, s52, 24576\n"
      "v_mov_b32 v183, v181\n"
      "s_add_u32 s40, s40, 24576\n"
      "s_waitcnt vmcnt(0)\n"
      "buffer_store_dword v13, v3, s[8:11], s52 offen sc1\n"
      "s_add_u32 s52, s52, 24576\n"
      "s_cmp_lg_u32 s44, 0\n"
      "s_cbranch_scc1 .Lq_done\n"
      "v_lshlrev_b32 v48, 3, v0\n"
      "s_mov_b32 s22, s23\n"
      ".Lq_up:\n"
      "s_mov_b32 s30, 128\n"
      "buffer_load_dword v49, v48, s[8:11], s30 offen sc1\n"
      "s_mov_b32 s30, 8320\n"
      "buffer_load_dword v50, v48, s[8:11], s30 offen sc1\n"
      "s_mov_b32 s30, 16512\n"
      "buffer_load_dword v51, v48, s[8:11], s30 offen sc1\n"
      "s_mov_b32 s30, 24704\n"
      "buffer_load_dword v52, v48, s[8:11], s30 offen sc1\n"
      "s_waitcnt vmcnt(0)\n"
      "v_xor_b32 v49, s21, v49\n"
      "v_xor_b32 v50, s21, v50\n"
      "v_xor_b32 v51, s21, v51\n"
      "v_xor_b32 v52, s21, v52\n"
      "v_or3_b32 v49, v49, v50, v51\n"
      "v_or_b32 v49, v49, v52\n"
      "v_cmp_ne_u32 vcc, 0, v49\n"
      "s_cbranch_vccz .Lq_ok\n"
      "s_sleep 4\n"
      "s_sub_u32 s22, s22, 1\n"
      "s_cbranch_scc0 .Lq_up\n"
      ".Lq_ok:\n"
      "v_lshrrev_b32 v48, 2, v0\n"
      "v_and_b32 v48, 0x7c, v48\n"
      "v_mov_b32 v49, s21\n"
      "buffer_store_dword v49, v48, s[8:11], 0 offen sc1\n"
      ".Lq_done:\n"
      "s_endpgm\n"
      ".Lsp_q0_1:\n"
      "s_mov_b32 s22, s23\n"
      ".Lsl_q0_1:\n"
      "buffer_load_dword v15, v2, s[8:11], s46 offen sc1\n"
      "s_waitcnt vmcnt(0)\n"
      "v_cmp_ne_u32 vcc, s21, v15\n"
      "s_cbranch_vccz .Lbk_q0_1\n"
      "s_sleep 4\n"
      "s_sub_u32 s22, s22, 1\n"
      "s_cbranch_scc0 .Lsl_q0_1\n"
      "s_mov_b32 s23, 0\n"
      "s_branch .Lbk_q0_1\n"
      ".Lsp_q1_2:\n"
      "s_mov_b32 s22, s23\n"
      ".Lsl_q1_2:\n"
      "buffer_load_dword v15, v2, s[8:11], s46 offen sc1\n"
      "s_waitcnt vmcnt(0)\n"
      "v_cmp_ne_u32 vcc, s21, v15\n"
      "s_cbranch_vccz .Lbk_q1_2\n"
      "s_sleep 4\n"
      "s_sub_u32 s22, s22, 1\n"
      "s_cbranch_scc0 .Lsl_q1_2\n"
      "s_mov_b32 s23, 0\n"
      "s_branch .Lbk_q1_2\n"
      ".Lsp_qy0_3:\n"
      "s_lshl_b32 s22, s23, 4\n"
      ".Lsl_qy0_3:\n"
      "ds_read_b32 v10, v12\n"
      "s_waitcnt lgkmcnt(0)\n"
      "v_cmp_gt_i32 vcc, v9, v10\n"
      "s_cbranch_vccz .Lbk_qy0_3\n"
      "s_sub_u32 s22, s22, 1\n"
      "s_cbranch_scc0 .Lsl_qy0_3\n"
      "s_mov_b32 s23, 0\n"
      "s_branch .Lbk_qy0_3\n"
      ".Lsp_q2_4:\n"
      "s_mov_b32 s22, s23\n"
      ".Lsl_q2_4:\n"
      "buffer_load_dword v15, v2, s[8:11], s46 offen sc1\n"
      "s_waitcnt vmcnt(0)\n"
      "v_cmp_ne_u32 vcc, s21, v15\n"
      "s_cbranch_vccz .Lbk_q2_4\n"
      "s_sleep 4\n"
      "s_sub_u32 s22, s22, 1\n"
      "s_cbranch_scc0 .Lsl_q2_4\n"
      "s_mov_b32 s23, 0\n"
      "s_branch .Lbk_q2_4\n"
      ".Lsp_qy1_5:\n"
      "s_lshl_b32 s22, s23, 4\n"
      ".Lsl_qy1_5:\n"
      "ds_read_b32 v10, v12\n"
      "s_waitcnt lgkmcnt(0)\n"
      "v_cmp_gt_i32 vcc, v9, v10\n"
      "s_cbranch_vccz .Lbk_qy1_5\n"
      "s_sub_u32 s22, s22, 1\n"
      "s_cbranch_scc0 .Lsl_qy1_5\n"
      "s_mov_b32 s23, 0\n"
      "s_branch .Lbk_qy1_5\n"
      ".Lsp_q0_6:\n"
      "s_mov_b32 s22, s23\n"
      ".Lsl_q0_6:\n"
      "buffer_load_dword v15, v2, s[8:11], s46 offen sc1\n"
      "s_waitcnt vmcnt(0)\n"
      "v_cmp_ne_u32 vcc, s21, v15\n"
      "s_cbranch_vccz .Lbk_q0_6\n"
      "s_sleep 4\n"
      "s_sub_u32 s22, s22, 1\n"
      "s_cbranch_scc0 .Lsl_q0_6\n"
      "s_mov_b32 s23, 0\n"
      "s_branch .Lbk_q0_6\n"
      ".Lsp_qy2_7:\n"
      "s_lshl_b32 s22, s23, 4\n"
      ".Lsl_qy2_7:\n"
      "ds_read_b32 v10, v12\n"
      "s_waitcnt lgkmcnt(0)\n"
      "v_cmp_gt_i32 vcc, v9, v10\n"
      "s_cbranch_vccz .Lbk_qy2_7\n"
      "s_sub_u32 s22, s22, 1\n"
      "s_cbranch_scc0 .Lsl_qy2_7\n"
      "s_mov_b32 s23, 0\n"
      "s_branch .Lbk_qy2_7\n"
      ".Lsp_q1_8:\n"
      "s_mov_b32 s22, s23\n"
      ".Lsl_q1_8:\n"
      "buffer_load_dword v15, v2, s[8:11], s46 offen sc1\n"
      "s_waitcnt vmcnt(0)\n"
      "v_cmp_ne_u32 vcc, s21, v15\n"
      "s_cbranch_vccz .Lbk_q1_8\n"
      "s_sleep 4\n"
      "s_sub_u32 s22, s22, 1\n"
      "s_cbranch_scc0 .Lsl_q1_8\n"
      "s_mov_b32 s23, 0\n"
      "s_branch .Lbk_q1_8\n"
      ".Lsp_qy0_9:\n"
      "s_lshl_b32 s22, s23, 4\n"
      ".Lsl_qy0_9:\n"
      "ds_read_b32 v10, v12\n"
      "s_waitcnt lgkmcnt(0)\n"
      "v_cmp_gt_i32 vcc, v9, v10\n"
      "s_cbranch_vccz .Lbk_qy0_9\n"
      "s_sub_u32 s22, s22, 1\n"
      "s_cbranch_scc0 .Lsl_qy0_9\n"
      "s_mov_b32 s23, 0\n"
      "s_branch .Lbk_qy0_9\n"
      ".Lq_last:\n"
      :
      : "{s[8:11]}"(rsWS), "{s[12:15]}"(rsA), "{v0}"(v_l16), "{v1}"(v_l5), "{v2}"(v_fa), "{v3}"(v_ra), "{v4}"(v_st), "{v5}"(v_tw), "{v6}"(v_tr), "{v7}"(v_om), "{v8}"(v_oe), "{v10}"(v_al), "{v11}"(v_yw), "{v12}"(v_yr), "{v14}"(v_hi), "{v152}"(v_bc[0]), "{v153}"(v_bc[1]), "{v154}"(v_bc[2]), "{v155}"(v_bc[3]), "{v156}"(v_bp[0]), "{v157}"(v_bp[1]), "{v158}"(v_bp[2]), "{v159}"(v_bp[3]), "{v160}"(v_mk[0]), "{v161}"(v_mk[1]), "{v162}"(v_mk[2]), "{v163}"(v_mk[3]), "{v164}"(v_sel[0]), "{v165}"(v_sel[1]), "{v166}"(v_sel[2]), "{v167}"(v_sel[3]), "{v168}"(v_ci[0]), "{v169}"(v_ci[1]), "{v170}"(v_ci[2]), "{v171}"(v_ci[3]), "{v48}"(v_vo[0]), "{v49}"(v_vo[1]), "{v50}"(v_vo[2]), "{v51}"(v_vo[3]), "{v52}"(v_at), "{v53}"(v_ax), "{v54}"(v_pm), "{v55}"(v_ms), "{v76}"(v_pw), "{s36}"(s_sx), "{s37}"(s_ev), "{s38}"(s_del), "{s39}"(s_kil), "{s40}"(s_so), "{s41}"(s_m4), "{s42}"(s_m5), "{s43}"(s_ms), "{s44}"(s_up)
      : "v255", "a255", "s99", "vcc", "scc", "memory");

  } else if (fast && slot < 10) {
    const unsigned gr = e, part = slot - 8;
    COMMON(32, 81920);
    const unsigned c = part, o = w & 1;
    const u32x4 rsA = srd(Wo, 576 * 576 * 2);
    const unsigned v_l5 = v_l16 + 16384 - w * 3072, e0 = 16384 + o * 2048 + g * 512 + i16 * 8, e1 = e0 + 2048 - o * 4096;
    const unsigned v_ex[8] = {e0, e1, e0 + 20480, e1 + 20480, e0 + 40960, e1 + 40960, e0 + 61440, e1 + 61440}, v_l2 = v_l16 + 40960;
    const unsigned v_fa = 20480 - w * 4096 + (8 * w + (lane >> 3)) * 128 + (lane & 7) * 4, v_ra = up * 8 * REC + l5 + 19456 + (4 * c + w) * 128;
    const unsigned v_xp = (9 * c + 2 * w) * 1024 + v_l16, v_xs = (9 * c + 8) * 1024 + (w >> 1) * 512 + half;
    const unsigned v_rp = v_xp + WS_XIN, v_rs = v_xs + WS_XIN;
    const unsigned v_ew = 82944 + (w >> 1) * 1024 + lane * 4;
    const unsigned v_sw = 86016 + i16 * 144 + (4 * w + g) * 8, v_sr = 86016 + (8 * (w >> 1) + l8) * 144 + l7;
    const unsigned v_ss = 18432 + c * 512 + (w >> 1) * 256 + l8 * 32 + p * 8;
    const unsigned v_pw = 19456 + (4 * c + w) * 128 + l8 * REC + l7;
    unsigned v_vo[9], v_vp[9];
#pragma unroll
    for (unsigned j = 0; j < 9; ++j) {
      const unsigned ch = 64 * j + lane, r = ch / 36, x = 576 * c + ch % 36 * 16 - 1024 * (j & 3);
      v_vo[j] = (20 * o + r) * 1152 + x;
      v_vp[j] = (36 * (4 * o + (r >> 2)) + 16 + (r & 3)) * 1152 + x;
    }
    const unsigned ro = (8 * g + q) * 576;
    const unsigned v_t0 = ro + o * 18432 + 128 * w + 16 * p, v_t1 = v_t0 + 18432 - 2 * o * 18432, v_ts = ro + o * 18432 + 512 + 32 * (w >> 1) + 8 * p;
    const unsigned s_sx = WS_OB + (gr * NREC + KSK) * REC + w * 4096, s_ev = s_sx + (31 - KSK) * REC, s_sy = WS_X1 + (gr * NREC + KSK) * REC;
    const unsigned s_m4 = w * 4096, s_m5 = 16384 + w * 1024, s_m9 = w * 9216, s_so = (w >> 1) * 8 * 41472;
    const unsigned s_s8 = s_so, s_wg = o, s_rb = WS_OB + w * 4096;
    asm volatile(
      "s_buffer_load_dword s21, s[8:11], 0\n"
      "s_mov_b32 s23, 65536\n"
      "s_mov_b32 s31, 0x40000000\n"
      "s_add_u32 m0, s41, 0\n"
      "s_nop 0\n"
      "buffer_load_dwordx4 v100, s[12:15], s42 offen offset:0 lds\n"
      "buffer_load_dwordx4 v101, s[12:15], s42 offen offset:1024 lds\n"
      "buffer_load_dwordx4 v102, s[12:15], s42 offen offset:2048 lds\n"
      "buffer_load_dwordx4 v103, s[12:15], s42 offen offset:3072 lds\n"
      "s_add_u32 m0, s41, 4096\n"
      "s_nop 0\n"
      "buffer_load_dwordx4 v104, s[12:15], s42 offen offset:0 lds\n"
      "buffer_load_dwordx4 v105, s[12:15], s42 offen offset:1024 lds\n"
      "buffer_load_dwordx4 v106, s[12:15], s42 offen offset:2048 lds\n"
      "buffer_load_dwordx4 v107, s[12:15], s42 offen offset:3072 lds\n"
      "s_add_u32 m0, s41, 8192\n"
      "s_nop 0\n"
      "buffer_load_dwordx4 v108, s[12:15], s42 offen offset:0 lds\n"
      "s_add_u32 s42, s42, 41472\n"
      "s_add_u32 m0, s41, 36864\n"
      "s_nop 0\n"
      "buffer_load_dwordx4 v100, s[12:15], s42 offen offset:0 lds\n"
      "buffer_load_dwordx4 v101, s[12:15], s42 offen offset:1024 lds\n"
      "buffer_load_dwordx4 v102, s[12:15], s42 offen offset:2048 lds\n"
      "buffer_load_dwordx4 v103, s[12:15], s42 offen offset:3072 lds\n"
      "s_add_u32 m0, s41, 40960\n"
      "s_nop 0\n"
      "buffer_load_dwordx4 v104, s[12:15], s42 offen offset:0 lds\n"
      "buffer_load_dwordx4 v105, s[12:15], s42 offen offset:1024 lds\n"
      "buffer_load_dwordx4 v106, s[12:15], s42 offen offset:2048 lds\n"
      "buffer_load_dwordx4 v107, s[12:15], s42 offen offset:3072 lds\n"
      "s_add_u32 m0, s41, 45056\n"
      "s_nop 0\n"
      "buffer_load_dwordx4 v108, s[12:15], s42 offen offset:0 lds\n"
      "s_add_u32 s42, s42, 41472\n"
      "s_add_u32 m0, s41, 73728\n"
      "s_nop 0\n"
      "buffer_load_dwordx4 v100, s[12:15], s42 offen offset:0 lds\n"
      "buffer_load_dwordx4 v101, s[12:15], s42 offen offset:1024 lds\n"
      "buffer_load_dwordx4 v102, s[12:15], s42 offen offset:2048 lds\n"
      "buffer_load_dwordx4 v103, s[12:15], s42 offen offset:3072 lds\n"
      "s_add_u32 m0, s41, 77824\n"
      "s_nop 0\n"
      "buffer_load_dwordx4 v104, s[12:15], s42 offen offset:0 lds\n"
      "buffer_load_dwordx4 v105, s[12:15], s42 offen offset:1024 lds\n"
      "buffer_load_dwordx4 v106, s[12:15], s42 offen offset:2048 lds\n"
      "buffer_load_dwordx4 v107, s[12:15], s42 offen offset:3072 lds\n"
      "s_add_u32 m0, s41, 81920\n"
      "s_nop 0\n"
      "buffer_load_dwordx4 v108, s[12:15], s42 offen offset:0 lds\n"
      "s_add_u32 s42, s42, 41472\n"
      "s_waitcnt vmcnt(18)\n"
      "s_barrier\n"
      "ds_read_b64_tr_b16 v[64:65], v120 offset:0\n"
      "ds_read_b64_tr_b16 v[66:67], v120 offset:2304\n"
      "ds_read_b64_tr_b16 a[0:1], v118 offset:0\n"
      "ds_read_b64_tr_b16 a[2:3], v118 offset:2304\n"
      "ds_read_b64_tr_b16 a[72:73], v118 offset:8\n"
      "ds_read_b64_tr_b16 a[74:75], v118 offset:2312\n"
      "ds_read_b64_tr_b16 a[144:145], v118 offset:64\n"
      "ds_read_b64_tr_b16 a[146:147], v118 offset:2368\n"
      "ds_read_b64_tr_b16 a[216:217], v118 offset:72\n"
      "ds_read_b64_tr_b16 a[218:219], v118 offset:2376\n"
      "ds_read_b64_tr_b16 a[36:37], v119 offset:0\n"
      "ds_read_b64_tr_b16 a[38:39], v119 offset:2304\n"
      "ds_read_b64_tr_b16 a[108:109], v119 offset:8\n"
      "ds_read_b64_tr_b16 a[110:111], v119 offset:2312\n"
      "ds_read_b64_tr_b16 a[180:181], v119 offset:64\n"
      "ds_read_b64_tr_b16 a[182:183], v119 offset:2368\n"
      "ds_read_b64_tr_b16 a[252:253], v119 offset:72\n"
      "ds_read_b64_tr_b16 a[254:255], v119 offset:2376\n"
      "v_add_u32 v118, 36864, v118\n"
      "v_add_u32 v119, 36864, v119\n"
      "v_add_u32 v120, 36864, v120\n"
      "s_add_u32 m0, s41, 110592\n"
      "s_nop 0\n"
      "buffer_load_dwordx4 v100, s[12:15], s42 offen offset:0 lds\n"
      "buffer_load_dwordx4 v101, s[12:15], s42 offen offset:1024 lds\n"
      "buffer_load_dwordx4 v102, s[12:15], s42 offen offset:2048 lds\n"
      "buffer_load_dwordx4 v103, s[12:15], s42 offen offset:3072 lds\n"
      "s_add_u32 m0, s41, 114688\n"
      "s_nop 0\n"
      "buffer_load_dwordx4 v104, s[12:15], s42 offen offset:0 lds\n"
      "buffer_load_dwordx4 v105, s[12:15], s42 offen offset:1024 lds\n"
      "buffer_load_dwordx4 v106, s[12:15], s42 offen offset:2048 lds\n"
      "buffer_load_dwordx4 v107, s[12:15], s42 offen offset:3072 lds\n"
      "s_add_u32 m0, s41, 118784\n"
      "s_nop 0\n"
      "buffer_load_dwordx4 v108, s[12:15], s42 offen offset:0 lds\n"
      "s_add_u32 s42, s42, 41472\n"
      "s_waitcnt vmcnt(18) lgkmcnt(0)\n"
      "s_barrier\n"
      "ds_read_b64_tr_b16 v[68:69], v120 offset:0\n"
      "ds_read_b64_tr_b16 v[70:71], v120 offset:2304\n"
      "ds_read_b64_tr_b16 a[4:5], v118 offset:0\n"
      "ds_read_b64_tr_b16 a[6:7], v118 offset:2304\n"
      "ds_read_b64_tr_b16 a[76:77], v118 offset:8\n"
      "ds_read_b64_tr_b16 a[78:79], v118 offset:2312\n"
      "ds_read_b64_tr_b16 a[148:149], v118 offset:64\n"
      "ds_read_b64_tr_b16 a[150:151], v118 offset:2368\n"
      "ds_read_b64_tr_b16 a[220:221], v118 offset:72\n"
      "ds_read_b64_tr_b16 a[222:223], v118 offset:2376\n"
      "ds_read_b64_tr_b16 a[40:41], v119 offset:0\n"
      "ds_read_b64_tr_b16 a[42:43], v119 offset:2304\n"
      "ds_read_b64_tr_b16 a[112:113], v119 offset:8\n"
      "ds_read_b64_tr_b16 a[114:115], v119 offset:2312\n"
      "ds_read_b64_tr_b16 a[184:185], v119 offset:64\n"
      "ds_read_b64_tr_b16 a[186:187], v119 offset:2368\n"
      "ds_read_b64_tr_b16 v[32:33], v119 offset:72\n"
      "ds_read_b64_tr_b16 v[34:35], v119 offset:2376\n"
      "v_add_u32 v118, 36864, v118\n"
      "v_add_u32 v119, 36864, v119\n"
      "v_add_u32 v120, 36864, v120\n"
      "s_add_u32 m0, s41, 0\n"
      "s_nop 0\n"
      "buffer_load_dwordx4 v100, s[12:15], s42 offen offset:0 lds\n"
      "buffer_load_dwordx4 v101, s[12:15], s42 offen offset:1024 lds\n"
      "buffer_load_dwordx4 v102, s[12:15], s42 offen offset:2048 lds\n"
      "buffer_load_dwordx4 v103, s[12:15], s42 offen offset:3072 lds\n"
      "s_add_u32 m0, s41, 4096\n"
      "s_nop 0\n"
      "buffer_load_dwordx4 v104, s[12:15], s42 offen offset:0 lds\n"
      "buffer_load_dwordx4 v105, s[12:15], s42 offen offset:1024 lds\n"
      "buffer_load_dwordx4 v106, s[12:15], s42 offen offset:2048 lds\n"
      "buffer_load_dwordx4 v107, s[12:15], s42 offen offset:3072 lds\n"
      "s_add_u32 m0, s41, 8192\n"
      "s_nop 0\n"
      "buffer_load_dwordx4 v108, s[12:15], s42 offen offset:0 lds\n"
      "s_add_u32 s42, s42, 41472\n"
      "s_waitcnt vmcnt(18) lgkmcnt(0)\n"
      "s_barrier\n"
      "ds_read_b64_tr_b16 v[72:73], v120 offset:0\n"
      "ds_read_b64_tr_b16 v[74:75], v120 offset:2304\n"
      "ds_read_b64_tr_b16 a[8:9], v118 offset:0\n"
      "ds_read_b64_tr_b16 a[10:11], v118 offset:2304\n"
      "ds_read_b64_tr_b16 a[80:81], v118 offset:8\n"
      "ds_read_b64_tr_b16 a[82:83], v118 offset:2312\n"
      "ds_read_b64_tr_b16 a[152:153], v118 offset:64\n"
      "ds_read_b64_tr_b16 a[154:155], v118 offset:2368\n"
      "ds_read_b64_tr_b16 a[224:225], v118 offset:72\n"
      "ds_read_b64_tr_b16 a[226:227], v118 offset:2376\n"
      "ds_read_b64_tr_b16 a[44:45], v119 offset:0\n"
      "ds_read_b64_tr_b16 a[46:47], v119 offset:2304\n"
      "ds_read_b64_tr_b16 a[116:117], v119 offset:8\n"
      "ds_read_b64_tr_b16 a[118:119], v119 offset:2312\n"
      "ds_read_b64_tr_b16 a[188:189], v119 offset:64\n"
      "ds_read_b64_tr_b16 a[190:191], v119 offset:2368\n"
      "ds_read_b64_tr_b16 v[36:37], v119 offset:72\n"
      "ds_read_b64_tr_b16 v[38:39], v119 offset:2376\n"
      "v_add_u32 v118, 36864, v118\n"
      "v_add_u32 v119, 36864, v119\n"
      "v_add_u32 v120, 36864, v120\n"
      "s_add_u32 m0, s41, 36864\n"
      "s_nop 0\n"
      "buffer_load_dwordx4 v100, s[12:15], s42 offen offset:0 lds\n"
      "buffer_load_dwordx4 v101, s[12:15], s42 offen offset:1024 lds\n"
      "buffer_load_dwordx4 v102, s[12:15], s42 offen offset:2048 lds\n"
      "buffer_load_dwordx4 v103, s[12:15], s42 offen offset:3072 lds\n"
      "s_add_u32 m0, s41, 40960\n"
      "s_nop 0\n"
      "buffer_load_dwordx4 v104, s[12:15], s42 offen offset:0 lds\n"
      "buffer_load_dwordx4 v105, s[12:15], s42 offen offset:1024 lds\n"
      "buffer_load_dwordx4 v106, s[12:15], s42 offen offset:2048 lds\n"
      "buffer_load_dwordx4 v107, s[12:15], s42 offen offset:3072 lds\n"
      "s_add_u32 m0, s41, 45056\n"
      "s_nop 0\n"
      "buffer_load_dwordx4 v108, s[12:15], s42 offen offset:0 lds\n"
      "s_add_u32 s42, s42, 41472\n"
      "s_waitcnt vmcnt(18) lgkmcnt(0)\n"
      "s_barrier\n"
      "ds_read_b64_tr_b16 v[76:77], v120 offset:0\n"
      "ds_read_b64_tr_b16 v[78:79], v120 offset:2304\n"
      "ds_read_b64_tr_b16 a[12:13], v118 offset:0\n"
      "ds_read_b64_tr_b16 a[14:15], v118 offset:2304\n"
      "ds_read_b64_tr_b16 a[84:85], v118 offset:8\n"
      "ds_read_b64_tr_b16 a[86:87], v118 offset:2312\n"
      "ds_read_b64_tr_b16 a[156:157], v118 offset:64\n"
      "ds_read_b64_tr_b16 a[158:159], v118 offset:2368\n"
      "ds_read_b64_tr_b16 a[228:229], v118 offset:72\n"
      "ds_read_b64_tr_b16 a[230:231], v118 offset:2376\n"
      "ds_read_b64_tr_b16 a[48:49], v119 offset:0\n"
      "ds_read_b64_tr_b16 a[50:51], v119 offset:2304\n"
      "ds_read_b64_tr_b16 a[120:121], v119 offset:8\n"
      "ds_read_b64_tr_b16 a[122:123], v119 offset:2312\n"
      "ds_read_b64_tr_b16 a[192:193], v119 offset:64\n"
      "ds_read_b64_tr_b16 a[194:195], v119 offset:2368\n"
      "ds_read_b64_tr_b16 v[40:41], v119 offset:72\n"
      "ds_read_b64_tr_b16 v[42:43], v119 offset:2376\n"
      "v_add_u32 v118, 4294856704, v118\n"
      "v_add_u32 v119, 4294856704, v119\n"
      "v_add_u32 v120, 4294856704, v120\n"
      "s_add_u32 m0, s41, 73728\n"
      "s_nop 0\n"
      "buffer_load_dwordx4 v100, s[12:15], s42 offen offset:0 lds\n"
      "buffer_load_dwordx4 v101, s[12:15], s42 offen offset:1024 lds\n"
      "buffer_load_dwordx4 v102, s[12:15], s42 offen offset:2048 lds\n"
      "buffer_load_dwordx4 v103, s[12:15], s42 offen offset:3072 lds\n"
      "s_add_u32 m0, s41, 77824\n"
      "s_nop 0\n"
      "buffer_load_dwordx4 v104, s[12:15], s42 offen offset:0 lds\n"
      "buffer_load_dwordx4 v105, s[12:15], s42 offen offset:1024 lds\n"
      "buffer_load_dwordx4 v106, s[12:15], s42 offen offset:2048 lds\n"
      "buffer_load_dwordx4 v107, s[12:15], s42 offen offset:3072 lds\n"
      "s_add_u32 m0, s41, 81920\n"
      "s_nop 0\n"
      "buffer_load_dwordx4 v108, s[12:15], s42 offen offset:0 lds\n"
      "s_add_u32 s42, s42, 41472\n"
      "s_waitcnt vmcnt(18) lgkmcnt(0)\n"
      "s_barrier\n"
      "ds_read_b64_tr_b16 v[80:81], v120 offset:0\n"
      "ds_read_b64_tr_b16 v[82:83], v120 offset:2304\n"
      "ds_read_b64_tr_b16 a[16:17], v118 offset:0\n"
      "ds_read_b64_tr_b16 a[18:19], v118 offset:2304\n"
      "ds_read_b64_tr_b16 a[88:89], v118 offset:8\n"
      "ds_read_b64_tr_b16 a[90:91], v118 offset:2312\n"
      "ds_read_b64_tr_b16 a[160:161], v118 offset:64\n"
      "ds_read_b64_tr_b16 a[162:163], v118 offset:2368\n"
      "ds_read_b64_tr_b16 a[232:233], v118 offset:72\n"
      "ds_read_b64_tr_b16 a[234:235], v118 offset:2376\n"
      "ds_read_b64_tr_b16 a[52:53], v119 offset:0\n"
      "ds_read_b64_tr_b16 a[54:55], v119 offset:2304\n"
      "ds_read_b64_tr_b16 a[124:125], v119 offset:8\n"
      "ds_read_b64_tr_b16 a[126:127], v119 offset:2312\n"
      "ds_read_b64_tr_b16 a[196:197], v119 offset:64\n"
      "ds_read_b64_tr_b16 a[198:199], v119 offset:2368\n"
      "ds_read_b64_tr_b16 v[44:45], v119 offset:72\n"
      "ds_read_b64_tr_b16 v[46:47], v119 offset:2376\n"
      "v_add_u32 v118, 36864, v118\n"
      "v_add_u32 v119, 36864, v119\n"
      "v_add_u32 v120, 36864, v120\n"
      "s_add_u32 m0, s41, 110592\n"
      "s_nop 0\n"
      "buffer_load_dwordx4 v100, s[12:15], s42 offen offset:0 lds\n"
      "buffer_load_dwordx4 v101, s[12:15], s42 offen offset:1024 lds\n"
      "buffer_load_dwordx4 v102, s[12:15], s42 offen offset:2048 lds\n"
      "buffer_load_dwordx4 v103, s[12:15], s42 offen offset:3072 lds\n"
      "s_add_u32 m0, s41, 114688\n"
      "s_nop 0\n"
      "buffer_load_dwordx4 v104, s[12:15], s42 offen offset:0 lds\n"
      "buffer_load_dwordx4 v105, s[12:15], s42 offen offset:1024 lds\n"
      "buffer_load_dwordx4 v106, s[12:15], s42 offen offset:2048 lds\n"
      "buffer_load_dwordx4 v107, s[12:15], s42 offen offset:3072 lds\n"
      "s_add_u32 m0, s41, 118784\n"
      "s_nop 0\n"
      "buffer_load_dwordx4 v108, s[12:15], s42 offen offset:0 lds\n"
      "s_add_u32 s42, s42, 41472\n"
      "s_waitcnt vmcnt(18) lgkmcnt(0)\n"
      "s_barrier\n"
      "ds_read_b64_tr_b16 v[84:85], v120 offset:0\n"
      "ds_read_b64_tr_b16 v[86:87], v120 offset:2304\n"
      "ds_read_b64_tr_b16 a[20:21], v118 offset:0\n"
      "ds_read_b64_tr_b16 a[22:23], v118 offset:2304\n"
      "ds_read_b64_tr_b16 a[92:93], v118 offset:8\n"
      "ds_read_b64_tr_b16 a[94:95], v118 offset:2312\n"
      "ds_read_b64_tr_b16 a[164:165], v118 offset:64\n"
      "ds_read_b64_tr_b16 a[166:167], v118 offset:2368\n"
      "ds_read_b64_tr_b16 a[236:237], v118 offset:72\n"
      "ds_read_b64_tr_b16 a[238:239], v118 offset:2376\n"
      "ds_read_b64_tr_b16 a[56:57], v119 offset:0\n"
      "ds_read_b64_tr_b16 a[58:59], v119 offset:2304\n"
      "ds_read_b64_tr_b16 a[128:129], v119 offset:8\n"
      "ds_read_b64_tr_b16 a[130:131], v119 offset:2312\n"
      "ds_read_b64_tr_b16 a[200:201], v119 offset:64\n"
      "ds_read_b64_tr_b16 a[202:203], v119 offset:2368\n"
      "ds_read_b64_tr_b16 v[48:49], v119 offset:72\n"
      "ds_read_b64_tr_b16 v[50:51], v119 offset:2376\n"
      "v_add_u32 v118, 36864, v118\n"
      "v_add_u32 v119, 36864, v119\n"
      "v_add_u32 v120, 36864, v120\n"
      "s_add_u32 m0, s41, 0\n"
      "s_nop 0\n"
      "buffer_load_dwordx4 v109, s[12:15], s43 offen offset:0 lds\n"
      "buffer_load_dwordx4 v110, s[12:15], s43 offen offset:1024 lds\n"
      "buffer_load_dwordx4 v111, s[12:15], s43 offen offset:2048 lds\n"
      "buffer_load_dwordx4 v112, s[12:15], s43 offen offset:3072 lds\n"
      "s_add_u32 m0, s41, 4096\n"
      "s_nop 0\n"
      "buffer_load_dwordx4 v113, s[12:15], s43 offen offset:0 lds\n"
      "buffer_load_dwordx4 v114, s[12:15], s43 offen offset:1024 lds\n"
      "buffer_load_dwordx4 v115, s[12:15], s43 offen offset:2048 lds\n"
      "buffer_load_dwordx4 v116, s[12:15], s43 offen offset:3072 lds\n"
      "s_add_u32 m0, s41, 8192\n"
      "s_nop 0\n"
      "buffer_load_dwordx4 v117, s[12:15], s43 offen offset:0 lds\n"
      "s_add_u32 s42, s42, 41472\n"
      "s_waitcnt vmcnt(18) lgkmcnt(0)\n"
      "s_barrier\n"
      "ds_read_b64_tr_b16 v[88:89], v120 offset:0\n"
      "ds_read_b64_tr_b16 v[90:91], v120 offset:2304\n"
      "ds_read_b64_tr_b16 a[24:25], v118 offset:0\n"
      "ds_read_b64_tr_b16 a[26:27], v118 offset:2304\n"
      "ds_read_b64_tr_b16 a[96:97], v118 offset:8\n"
      "ds_read_b64_tr_b16 a[98:99], v118 offset:2312\n"
      "ds_read_b64_tr_b16 a[168:169], v118 offset:64\n"
      "ds_read_b64_tr_b16 a[170:171], v118 offset:2368\n"
      "ds_read_b64_tr_b16 a[240:241], v118 offset:72\n"
      "ds_read_b64_tr_b16 a[242:243], v118 offset:2376\n"
      "ds_read_b64_tr_b16 a[60:61], v119 offset:0\n"
      "ds_read_b64_tr_b16 a[62:63], v119 offset:2304\n"
      "ds_read_b64_tr_b16 a[132:133], v119 offset:8\n"
      "ds_read_b64_tr_b16 a[134:135], v119 offset:2312\n"
      "ds_read_b64_tr_b16 a[204:205], v119 offset:64\n"
      "ds_read_b64_tr_b16 a[206:207], v119 offset:2368\n"
      "ds_read_b64_tr_b16 v[52:53], v119 offset:72\n"
      "ds_read_b64_tr_b16 v[54:55], v119 offset:2376\n"
      "v_add_u32 v118, 36864, v118\n"
      "v_add_u32 v119, 36864, v119\n"
      "v_add_u32 v120, 36864, v120\n"
      "s_waitcnt vmcnt(9) lgkmcnt(0)\n"
      "s_barrier\n"
      "ds_read_b64_tr_b16 v[92:93], v120 offset:0\n"
      "ds_read_b64_tr_b16 v[94:95], v120 offset:2304\n"
      "ds_read_b64_tr_b16 a[28:29], v118 offset:0\n"
      "ds_read_b64_tr_b16 a[30:31], v118 offset:2304\n"
      "ds_read_b64_tr_b16 a[100:101], v118 offset:8\n"
      "ds_read_b64_tr_b16 a[102:103], v118 offset:2312\n"
      "ds_read_b64_tr_b16 a[172:173], v118 offset:64\n"
      "ds_read_b64_tr_b16 a[174:175], v118 offset:2368\n"
      "ds_read_b64_tr_b16 a[244:245], v118 offset:72\n"
      "ds_read_b64_tr_b16 a[246:247], v118 offset:2376\n"
      "ds_read_b64_tr_b16 a[64:65], v119 offset:0\n"
      "ds_read_b64_tr_b16 a[66:67], v119 offset:2304\n"
      "ds_read_b64_tr_b16 a[136:137], v119 offset:8\n"
      "ds_read_b64_tr_b16 a[138:139], v119 offset:2312\n"
      "ds_read_b64_tr_b16 a[208:209], v119 offset:64\n"
      "ds_read_b64_tr_b16 a[210:211], v119 offset:2368\n"
      "ds_read_b64_tr_b16 v[56:57], v119 offset:72\n"
      "ds_read_b64_tr_b16 v[58:59], v119 offset:2376\n"
      "v_add_u32 v118, 4294856704, v118\n"
      "v_add_u32 v119, 4294856704, v119\n"
      "v_add_u32 v120, 4294856704, v120\n"
      "s_waitcnt vmcnt(0) lgkmcnt(0)\n"
      "s_barrier\n"
      "ds_read_b64_tr_b16 v[96:97], v120 offset:0\n"
      "ds_read_b64_tr_b16 v[98:99], v120 offset:2304\n"
      "ds_read_b64_tr_b16 a[32:33], v118 offset:0\n"
      "ds_read_b64_tr_b16 a[34:35], v118 offset:2304\n"
      "ds_read_b64_tr_b16 a[104:105], v118 offset:8\n"
      "ds_read_b64_tr_b16 a[106:107], v118 offset:2312\n"
      "ds_read_b64_tr_b16 a[176:177], v118 offset:64\n"
      "ds_read_b64_tr_b16 a[178:179], v118 offset:2368\n"
      "ds_read_b64_tr_b16 a[248:249], v118 offset:72\n"
      "ds_read_b64_tr_b16 a[250:251], v118 offset:2376\n"
      "ds_read_b64_tr_b16 a[68:69], v119 offset:0\n"
      "ds_read_b64_tr_b16 a[70:71], v119 offset:2304\n"
      "ds_read_b64_tr_b16 a[140:141], v119 offset:8\n"
      "ds_read_b64_tr_b16 a[142:143], v119 offset:2312\n"
      "ds_read_b64_tr_b16 a[212:213], v119 offset:64\n"
      "ds_read_b64_tr_b16 a[214:215], v119 offset:2368\n"
      "ds_read_b64_tr_b16 v[60:61], v119 offset:72\n"
      "ds_read_b64_tr_b16 v[62:63], v119 offset:2376\n"
      "v_add_u32 v118, 36864, v118\n"
      "v_add_u32 v119, 36864, v119\n"
      "v_add_u32 v120, 36864, v120\n"
      "s_waitcnt lgkmcnt(0)\n"
      "s_getpc_b64 vcc\n"
      ".Lw_ct:\n"
      "s_add_u32 vcc_lo, vcc_lo, .Lw_p0-.Lw_ct\n"
      "s_addc_u32 vcc_hi, vcc_hi, 0\n"
      "s_lshl_b32 s30, s39, 1\n"
      "v_lshl_add_u32 v130, v0, 3, s30\n"
      "v_min_u32 v130, .Lw_last-.Lw_p0-4, v130\n"
      "global_load_dword v130, v130, vcc\n"
      "s_mul_i32 s21, s21, 1664525\n"
      "s_add_u32 s21, s21, 1013904223\n"
      "v_mov_b32 v25, s21\n"
      "buffer_store_dword v25, v22, s[8:11], 0 offen sc1\n"
      "v_not_b32 v124, v25\n"
      "v_not_b32 v125, v25\n"
      "v_not_b32 v126, v25\n"
      "v_not_b32 v127, v25\n"
      "s_add_u32 s30, s38, 0\n"
      "buffer_store_dwordx4 v[124:127], v121, s[8:11], s30 offen sc1\n"
      "v_xor_b32 v25, v25, v26\n"
      "v_mov_b32 v27, 0x3ae38e39\n"
      "s_barrier\n"
      "v_mov_b32 v21, 0\n"
      "ds_write_b32 v23, v21\n"
      "s_waitcnt lgkmcnt(0)\n"
      "s_barrier\n"
      "s_add_u32 s6, s36, 1572864\n"
      "s_add_u32 s36, s36, 4294942720\n"
      "s_mov_b32 s51, 24576\n"
      "s_waitcnt vmcnt(0) lgkmcnt(0)\n"
      "s_cmp_lg_u32 s44, 0\n"
      "s_cbranch_scc1 .Lv_start\n"
      "s_mov_b64 exec, 0\n"
      "s_lshr_b32 s22, s39, 13\n"
      ".Lw_pl:\n"
      "s_cmp_eq_u32 s22, 0\n"
      "s_cbranch_scc1 .Lw_p0\n"
      "s_cmp_eq_u32 s22, 1\n"
      "s_cbranch_scc1 .Lw_p1\n"
      "s_branch .Lw_pe\n"
      ".Lw_pr:\n"
      "s_add_u32 s22, s22, 2\n"
      "s_branch .Lw_pl\n"
      ".Lw_pe:\n"
      "s_mov_b64 exec, -1\n"
      "s_add_u32 s50, s38, 4294918144\n"
      "s_add_u32 s49, s38, 4294893568\n"
      "s_add_u32 s38, s38, 4294942720\n"
      "s_cmp_eq_u32 s36, s37\n"
      "s_cselect_b32 s48, 49152, s51\n"
      "s_add_u32 s36, s36, s48\n"
      "s_min_u32 s36, s36, s6\n"
      "buffer_load_dword v28, v11, s[8:11], s36 offen sc1\n"
      "s_mov_b32 s46, s36\n"
      "s_add_u32 s36, s36, 24576\n"
      "s_min_u32 s36, s36, s6\n"
      "s_waitcnt vmcnt(0)\n"
      "v_cmp_ne_u32 vcc, s21, v28\n"
      "s_add_u32 m0, s39, 0\n"
      "s_cbranch_vccnz .Lsp_w0_1\n"
      ".Lbk_w0_1:\n"
      "buffer_load_dword v28, v11, s[8:11], s36 offen sc1\n"
      "buffer_load_dwordx4 v0, s[8:11], s46 offen offset:0 sc1 lds\n"
      "buffer_load_dwordx4 v0, s[8:11], s46 offen offset:1024 sc1 lds\n"
      "buffer_load_dwordx4 v0, s[8:11], s46 offen offset:3072 sc1 lds\n"
      "buffer_load_dwordx4 v0, s[8:11], s46 offen offset:2048 sc1 lds\n"
      "s_add_u32 m0, s40, 0\n"
      "s_nop 0\n"
      "buffer_load_dwordx4 v1, s[8:11], s46 offen offset:0 sc1 lds\n"
      "s_sub_u32 s47, s46, s45\n"
      "buffer_load_dwordx2 v[200:201], v14, s[8:11], s47 offen sc1\n"
      "buffer_load_dwordx4 v[192:195], v13, s[8:11], s47 offen sc1\n"
      "buffer_load_dword v26, v23, s[8:11], s31 offen\n"
      "s_add_u32 m0, s39, 20480\n"
      "s_mov_b32 s46, s36\n"
      "s_waitcnt vmcnt(8)\n"
      "v_cmp_ne_u32 vcc, s21, v28\n"
      "buffer_load_dword v26, v23, s[8:11], s31 offen\n"
      "s_cmp_eq_u32 s36, s37\n"
      "s_cselect_b32 s48, 49152, s51\n"
      "s_add_u32 s36, s36, s48\n"
      "s_cbranch_vccnz .Lsp_w1_2\n"
      ".Lbk_w1_2:\n"
      "s_min_u32 s36, s36, s6\n"
      "buffer_load_dword v28, v11, s[8:11], s36 offen sc1\n"
      "buffer_load_dwordx4 v0, s[8:11], s46 offen offset:0 sc1 lds\n"
      "buffer_load_dwordx4 v0, s[8:11], s46 offen offset:1024 sc1 lds\n"
      "buffer_load_dwordx4 v0, s[8:11], s46 offen offset:3072 sc1 lds\n"
      "v_add_u32 v21, 1, v21\n"
      "s_waitcnt vmcnt(8)\n"
      "ds_write_b32 v23, v21\n"
      "buffer_load_dwordx4 v0, s[8:11], s46 offen offset:2048 sc1 lds\n"
      "s_add_u32 m0, s40, 20480\n"
      "buffer_load_dwordx4 v[196:199], v13, s[8:11], s47 offen offset:1024 sc1\n"
      "buffer_load_dwordx4 v1, s[8:11], s46 offen offset:0 sc1 lds\n"
      "ds_read_b32 v22, v24\n"
      "s_waitcnt lgkmcnt(0)\n"
      "v_cmp_gt_i32 vcc, v21, v22\n"
      "s_waitcnt vmcnt(10)\n"
      "v_and_b32 v189, 0xffff0000, v200\n"
      "v_lshlrev_b32 v188, 16, v200\n"
      "s_cbranch_vccnz .Lsp_wy0_3\n"
      ".Lbk_wy0_3:\n"
      "ds_read_b128 v[104:107], v0 offset:1024\n"
      "v_lshlrev_b32 v190, 16, v201\n"
      "ds_read_b128 v[100:103], v0 offset:0\n"
      "ds_read_b128 v[120:123], v0 offset:5120\n"
      "s_waitcnt vmcnt(9)\n"
      "v_lshlrev_b32 v178, 16, v195\n"
      "ds_read_b128 v[112:115], v0 offset:3072\n"
      "ds_read_b128 v[116:119], v0 offset:4096\n"
      "v_lshlrev_b32 v174, 16, v193\n"
      "ds_read_b128 v[124:127], v0 offset:6144\n"
      "s_sub_u32 s47, s46, s45\n"
      "ds_read_b128 v[108:111], v0 offset:2048\n"
      "v_and_b32 v191, 0xffff0000, v201\n"
      "v_and_b32 v177, 0xffff0000, v194\n"
      "ds_read_b128 v[136:139], v0 offset:8192\n"
      "v_lshlrev_b32 v176, 16, v194\n"
      "ds_read_b128 v[128:131], v0 offset:7168\n"
      "v_lshlrev_b32 v172, 16, v192\n"
      "v_and_b32 v175, 0xffff0000, v193\n"
      "buffer_load_dwordx2 v[200:201], v14, s[8:11], s47 offen sc1\n"
      "ds_read2_b32 v[132:133], v2 offset0:0 offset1:32\n"
      "v_and_b32 v173, 0xffff0000, v192\n"
      "ds_read2_b32 v[134:135], v2 offset0:64 offset1:96\n"
      "s_mov_b32 s20, 15\n"
      "s_waitcnt lgkmcnt(0)\n"
      "s_nop 4\n"
      "s_branch .Lw_loop\n"
      ".p2align 12\n"
      ".Lw_p0:\n"
      "s_branch .Lw_pr\n"
      ".p2align 5\n"
      ".Lw_loop:\n"
      "v_mfma_f32_16x16x32_bf16 v[188:191], v[64:67], v[100:103], v[188:191]\n"
      "ds_read_b128 v[140:143], v0 offset:9216\n"
      "v_and_b32 v179, 0xffff0000, v195\n"
      "v_mfma_f32_16x16x32_bf16 v[188:191], v[68:71], v[104:107], v[188:191]\n"
      "buffer_load_dwordx4 v[192:195], v13, s[8:11], s47 offen sc1\n"
      "v_mfma_f32_16x16x32_bf16 v[188:191], v[72:75], v[108:111], v[188:191]\n"
      "v_add_f32 v218, v218, v182\n"
      "v_mfma_f32_16x16x32_bf16 v[188:191], v[76:79], v[112:115], v[188:191]\n"
      "ds_read_b128 v[156:159], v0 offset:13312\n"
      "v_fmac_f32 v219, v184, v184\n"
      "v_mfma_f32_16x16x32_bf16 v[188:191], v[80:83], v[116:119], v[188:191]\n"
      "ds_read_b128 v[164:167], v0 offset:15360\n"
      "v_cvt_pk_bf16_f32 v213, v182, v183\n"
      "v_mfma_f32_16x16x32_bf16 v[188:191], v[84:87], v[120:123], v[188:191]\n"
      "ds_read_b128 v[144:147], v0 offset:10240\n"
      "v_mfma_f32_16x16x32_bf16 v[188:191], v[88:91], v[124:127], v[188:191]\n"
      "s_waitcnt lgkmcnt(4)\n"
      "v_cvt_pk_bf16_f32 v215, v186, v187\n"
      "v_add_f32 v218, v218, v183\n"
      "v_mfma_f32_16x16x32_bf16 v[188:191], v[92:95], v[128:131], v[188:191]\n"
      "buffer_store_dwordx4 v[208:211], v15, s[8:11], s38 offen sc1\n"
      "v_mfma_f32_16x16x32_bf16 v[188:191], v[96:99], v[132:135], v[188:191]\n"
      "v_cvt_pk_bf16_f32 v212, v180, v181\n"
      "v_add_f32 v218, v218, v184\n"
      "v_mfma_f32_16x16x32_bf16 v[172:175], a[0:3], v[100:103], v[172:175]\n"
      "s_add_u32 s50, s50, 24576\n"
      "v_cvt_pk_bf16_f32 v214, v184, v185\n"
      "v_add_f32 v218, v218, v185\n"
      "v_mfma_f32_16x16x32_bf16 v[172:175], a[4:7], v[104:107], v[172:175]\n"
      "s_mov_b32 s46, s36\n"
      "v_add_f32 v218, v218, v186\n"
      "v_mfma_f32_16x16x32_bf16 v[172:175], a[8:11], v[108:111], v[172:175]\n"
      "ds_read_b128 v[148:151], v0 offset:11264\n"
      "v_add_f32 v218, v218, v187\n"
      "v_mfma_f32_16x16x32_bf16 v[172:175], a[12:15], v[112:115], v[172:175]\n"
      "v_fmac_f32 v219, v185, v185\n"
      "v_add_u32 v21, 1, v21\n"
      "v_mfma_f32_16x16x32_bf16 v[172:175], a[16:19], v[116:119], v[172:175]\n"
      "s_add_u32 s36, s36, 24576\n"
      "ds_read_b128 v[152:155], v0 offset:12288\n"
      "v_fmac_f32 v219, v186, v186\n"
      "v_mfma_f32_16x16x32_bf16 v[172:175], a[20:23], v[120:123], v[172:175]\n"
      "buffer_store_dwordx2 v[216:217], v16, s[8:11], s38 offen sc1\n"
      "v_mfma_f32_16x16x32_bf16 v[172:175], a[24:27], v[124:127], v[172:175]\n"
      "ds_read_b128 v[160:163], v0 offset:14336\n"
      "v_mfma_f32_16x16x32_bf16 v[172:175], a[28:31], v[128:131], v[172:175]\n"
      "ds_read2_b32 v[170:171], v3 offset0:64 offset1:96\n"
      "v_mfma_f32_16x16x32_bf16 v[172:175], a[32:35], v[132:135], v[172:175]\n"
      "ds_read2_b32 v[168:169], v3 offset0:0 offset1:32\n"
      "s_min_u32 s36, s36, s6\n"
      "v_mfma_f32_16x16x32_bf16 v[172:175], a[36:39], v[136:139], v[172:175]\n"
      "s_waitcnt lgkmcnt(4)\n"
      "v_mfma_f32_16x16x32_bf16 v[172:175], a[40:43], v[140:143], v[172:175]\n"
      "buffer_store_dwordx4 v[212:215], v15, s[8:11], s38 offen offset:1024 sc1\n"
      "v_mfma_f32_16x16x32_bf16 v[172:175], a[44:47], v[144:147], v[172:175]\n"
      "ds_write_b32 v18, v218\n"
      "v_mfma_f32_16x16x32_bf16 v[172:175], a[48:51], v[148:151], v[172:175]\n"
      "s_waitcnt vmcnt(11) lgkmcnt(4)\n"
      "v_fmac_f32_e64 v219, v187, v187\n"
      "v_cmp_ne_u32 vcc, s21, v28\n"
      "v_mfma_f32_16x16x32_bf16 v[172:175], a[52:55], v[152:155], v[172:175]\n"
      "ds_write_b32 v18, v219 offset:4\n"
      "v_mfma_f32_16x16x32_bf16 v[172:175], a[56:59], v[156:159], v[172:175]\n"
      "s_waitcnt lgkmcnt(3)\n"
      "s_add_u32 m0, s39, 40960\n"
      "v_mfma_f32_16x16x32_bf16 v[172:175], a[60:63], v[160:163], v[172:175]\n"
      "ds_write_b32 v23, v21\n"
      "v_mfma_f32_16x16x32_bf16 v[172:175], a[64:67], v[164:167], v[172:175]\n"
      "s_waitcnt lgkmcnt(3)\n"
      "s_cbranch_vccnz .Lsp_w0_4\n"
      ".Lbk_w0_4:\n"
      "v_mfma_f32_16x16x32_bf16 v[172:175], a[68:71], v[168:171], v[172:175]\n"
      "v_mfma_f32_16x16x32_bf16 v[176:179], a[72:75], v[100:103], v[176:179]\n"
      "buffer_load_dword v28, v11, s[8:11], s36 offen sc1\n"
      "v_mfma_f32_16x16x32_bf16 v[176:179], a[76:79], v[104:107], v[176:179]\n"
      "s_waitcnt vmcnt(7) lgkmcnt(2)\n"
      "v_lshlrev_b32 v186, 16, v199\n"
      "v_lshlrev_b32 v184, 16, v198\n"
      "v_mfma_f32_16x16x32_bf16 v[176:179], a[80:83], v[108:111], v[176:179]\n"
      "v_lshlrev_b32 v180, 16, v196\n"
      "v_and_b32 v183, 0xffff0000, v197\n"
      "v_mfma_f32_16x16x32_bf16 v[176:179], a[84:87], v[112:115], v[176:179]\n"
      "buffer_store_dword v25, v12, s[8:11], s50 offen sc1\n"
      "v_mfma_f32_16x16x32_bf16 v[176:179], a[88:91], v[116:119], v[176:179]\n"
      "v_lshlrev_b32 v182, 16, v197\n"
      "v_and_b32 v181, 0xffff0000, v196\n"
      "v_mfma_f32_16x16x32_bf16 v[176:179], a[92:95], v[120:123], v[176:179]\n"
      "v_and_b32 v187, 0xffff0000, v199\n"
      "v_and_b32 v185, 0xffff0000, v198\n"
      "v_mfma_f32_16x16x32_bf16 v[176:179], a[96:99], v[124:127], v[176:179]\n"
      "buffer_load_dwordx4 v[196:199], v13, s[8:11], s47 offen offset:1024 sc1\n"
      "v_mfma_f32_16x16x32_bf16 v[176:179], a[100:103], v[128:131], v[176:179]\n"
      "ds_read_b32 v22, v24\n"
      "v_mul_f32 v219, v172, v172\n"
      "v_mfma_f32_16x16x32_bf16 v[176:179], a[104:107], v[132:135], v[176:179]\n"
      "v_mfma_f32_16x16x32_bf16 v[176:179], a[108:111], v[136:139], v[176:179]\n"
      "v_mfma_f32_16x16x32_bf16 v[176:179], a[112:115], v[140:143], v[176:179]\n"
      "buffer_load_dwordx4 v0, s[8:11], s46 offen offset:0 sc1 lds\n"
      "v_mfma_f32_16x16x32_bf16 v[176:179], a[116:119], v[144:147], v[176:179]\n"
      "v_add_f32 v218, v172, v173\n"
      "v_fmac_f32 v219, v173, v173\n"
      "v_mfma_f32_16x16x32_bf16 v[176:179], a[120:123], v[148:151], v[176:179]\n"
      "s_waitcnt lgkmcnt(0)\n"
      "v_cmp_gt_i32 vcc, v21, v22\n"
      "v_mfma_f32_16x16x32_bf16 v[176:179], a[124:127], v[152:155], v[176:179]\n"
      "v_mfma_f32_16x16x32_bf16 v[176:179], a[128:131], v[156:159], v[176:179]\n"
      "buffer_load_dwordx4 v0, s[8:11], s46 offen offset:1024 sc1 lds\n"
      "v_mfma_f32_16x16x32_bf16 v[176:179], a[132:135], v[160:163], v[176:179]\n"
      "s_cbranch_vccnz .Lsp_wz0_5\n"
      ".Lbk_wz0_5:\n"
      "ds_read2st64_b32 v[204:205], v17 offset1:1\n"
      "v_mfma_f32_16x16x32_bf16 v[176:179], a[136:139], v[164:167], v[176:179]\n"
      "ds_read2st64_b32 v[206:207], v17 offset0:2 offset1:3\n"
      "v_add_u32 v21, 1, v21\n"
      "v_mfma_f32_16x16x32_bf16 v[176:179], a[140:143], v[168:171], v[176:179]\n"
      "s_waitcnt vmcnt(10)\n"
      "ds_write_b32 v23, v21\n"
      "v_mfma_f32_16x16x32_bf16 v[180:183], a[144:147], v[100:103], v[180:183]\n"
      "buffer_load_dwordx4 v0, s[8:11], s46 offen offset:3072 sc1 lds\n"
      "v_mfma_f32_16x16x32_bf16 v[180:183], a[148:151], v[104:107], v[180:183]\n"
      "v_add_f32 v218, v218, v174\n"
      "v_fmac_f32 v219, v174, v174\n"
      "v_mfma_f32_16x16x32_bf16 v[180:183], a[152:155], v[108:111], v[180:183]\n"
      "v_fmac_f32 v219, v175, v175\n"
      "v_fmac_f32 v219, v176, v176\n"
      "v_mfma_f32_16x16x32_bf16 v[180:183], a[156:159], v[112:115], v[180:183]\n"
      "v_add_f32 v218, v218, v175\n"
      "v_add_f32 v218, v218, v176\n"
      "v_mfma_f32_16x16x32_bf16 v[180:183], a[160:163], v[116:119], v[180:183]\n"
      "buffer_load_dwordx4 v0, s[8:11], s46 offen offset:2048 sc1 lds\n"
      "v_mfma_f32_16x16x32_bf16 v[180:183], a[164:167], v[120:123], v[180:183]\n"
      "s_add_u32 m0, s40, 40960\n"
      "v_add_f32 v218, v218, v177\n"
      "v_cvt_pk_bf16_f32 v209, v174, v175\n"
      "v_mfma_f32_16x16x32_bf16 v[180:183], a[168:171], v[124:127], v[180:183]\n"
      "s_waitcnt lgkmcnt(0)\n"
      "v_add_f32 v188, v188, v204\n"
      "v_add_f32 v190, v190, v206\n"
      "v_mfma_f32_16x16x32_bf16 v[180:183], a[172:175], v[128:131], v[180:183]\n"
      "v_cvt_pk_bf16_f32 v208, v172, v173\n"
      "v_fmac_f32 v219, v177, v177\n"
      "v_mfma_f32_16x16x32_bf16 v[180:183], a[176:179], v[132:135], v[180:183]\n"
      "buffer_load_dwordx4 v1, s[8:11], s46 offen offset:0 sc1 lds\n"
      "v_mfma_f32_16x16x32_bf16 v[180:183], a[180:183], v[136:139], v[180:183]\n"
      "s_add_u32 s38, s38, 24576\n"
      "v_cvt_pk_bf16_f32 v210, v176, v177\n"
      "v_mfma_f32_16x16x32_bf16 v[180:183], a[184:187], v[140:143], v[180:183]\n"
      "ds_read_b32 v22, v24\n"
      "v_add_f32 v218, v218, v178\n"
      "v_mfma_f32_16x16x32_bf16 v[180:183], a[188:191], v[144:147], v[180:183]\n"
      "v_add_f32 v218, v218, v179\n"
      "v_add_f32 v218, v218, v188\n"
      "v_mfma_f32_16x16x32_bf16 v[180:183], a[192:195], v[148:151], v[180:183]\n"
      "s_add_u32 s50, s50, 24576\n"
      "v_fmac_f32 v219, v178, v178\n"
      "v_fmac_f32 v219, v179, v179\n"
      "v_mfma_f32_16x16x32_bf16 v[180:183], a[196:199], v[152:155], v[180:183]\n"
      "v_add_f32 v191, v191, v207\n"
      "v_cvt_pk_bf16_f32 v211, v178, v179\n"
      "v_mfma_f32_16x16x32_bf16 v[180:183], a[200:203], v[156:159], v[180:183]\n"
      "v_fmac_f32 v219, v188, v188\n"
      "v_add_f32 v189, v189, v205\n"
      "v_mfma_f32_16x16x32_bf16 v[180:183], a[204:207], v[160:163], v[180:183]\n"
      "s_waitcnt lgkmcnt(0)\n"
      "v_fmac_f32 v219, v189, v189\n"
      "v_cmp_gt_i32 vcc, v21, v22\n"
      "v_mfma_f32_16x16x32_bf16 v[180:183], a[208:211], v[164:167], v[180:183]\n"
      "v_fmac_f32 v219, v190, v190\n"
      "v_add_f32 v218, v218, v189\n"
      "v_mfma_f32_16x16x32_bf16 v[180:183], a[212:215], v[168:171], v[180:183]\n"
      "s_waitcnt vmcnt(11)\n"
      "v_lshlrev_b32 v172, 16, v192\n"
      "v_mfma_f32_16x16x32_bf16 v[184:187], a[216:219], v[100:103], v[184:187]\n"
      "v_and_b32 v175, 0xffff0000, v193\n"
      "v_and_b32 v177, 0xffff0000, v194\n"
      "v_mfma_f32_16x16x32_bf16 v[184:187], a[220:223], v[104:107], v[184:187]\n"
      "s_cbranch_vccnz .Lsp_wy1_6\n"
      ".Lbk_wy1_6:\n"
      "v_add_f32 v218, v218, v190\n"
      "v_mfma_f32_16x16x32_bf16 v[184:187], a[224:227], v[108:111], v[184:187]\n"
      "ds_read_b128 v[104:107], v0 offset:21504\n"
      "v_cvt_pk_bf16_f32 v216, v188, v189\n"
      "v_mfma_f32_16x16x32_bf16 v[184:187], a[228:231], v[112:115], v[184:187]\n"
      "v_add_f32 v218, v218, v191\n"
      "v_add_f32 v218, v218, v180\n"
      "v_mfma_f32_16x16x32_bf16 v[184:187], a[232:235], v[116:119], v[184:187]\n"
      "ds_read_b128 v[116:119], v0 offset:24576\n"
      "v_cvt_pk_bf16_f32 v217, v190, v191\n"
      "v_mfma_f32_16x16x32_bf16 v[184:187], a[236:239], v[120:123], v[184:187]\n"
      "ds_read_b128 v[108:111], v0 offset:22528\n"
      "v_lshlrev_b32 v188, 16, v200\n"
      "v_mfma_f32_16x16x32_bf16 v[184:187], a[240:243], v[124:127], v[184:187]\n"
      "ds_read_b128 v[100:103], v0 offset:20480\n"
      "v_fmac_f32 v219, v191, v191\n"
      "v_mfma_f32_16x16x32_bf16 v[184:187], a[244:247], v[128:131], v[184:187]\n"
      "v_lshlrev_b32 v190, 16, v201\n"
      "v_and_b32 v191, 0xffff0000, v201\n"
      "v_mfma_f32_16x16x32_bf16 v[184:187], a[248:251], v[132:135], v[184:187]\n"
      "ds_read2_b32 v[134:135], v4 offset0:64 offset1:96\n"
      "v_lshlrev_b32 v174, 16, v193\n"
      "v_mfma_f32_16x16x32_bf16 v[184:187], a[252:255], v[136:139], v[184:187]\n"
      "s_sub_u32 s47, s46, s45\n"
      "v_and_b32 v189, 0xffff0000, v200\n"
      "v_fmac_f32 v219, v180, v180\n"
      "v_mfma_f32_16x16x32_bf16 v[184:187], v[32:35], v[140:143], v[184:187]\n"
      "buffer_store_dwordx4 v[208:211], v15, s[8:11], s38 offen sc1\n"
      "v_mfma_f32_16x16x32_bf16 v[184:187], v[36:39], v[144:147], v[184:187]\n"
      "ds_read_b128 v[136:139], v0 offset:28672\n"
      "v_fmac_f32 v219, v181, v181\n"
      "v_mfma_f32_16x16x32_bf16 v[184:187], v[40:43], v[148:151], v[184:187]\n"
      "ds_read2_b32 v[132:133], v4 offset0:0 offset1:32\n"
      "v_lshlrev_b32 v178, 16, v195\n"
      "v_mfma_f32_16x16x32_bf16 v[184:187], v[44:47], v[152:155], v[184:187]\n"
      "ds_read_b128 v[112:115], v0 offset:23552\n"
      "v_add_f32 v218, v218, v181\n"
      "v_mfma_f32_16x16x32_bf16 v[184:187], v[48:51], v[156:159], v[184:187]\n"
      "ds_read_b128 v[120:123], v0 offset:25600\n"
      "v_and_b32 v173, 0xffff0000, v192\n"
      "v_mfma_f32_16x16x32_bf16 v[184:187], v[52:55], v[160:163], v[184:187]\n"
      "ds_read_b128 v[124:127], v0 offset:26624\n"
      "v_lshlrev_b32 v176, 16, v194\n"
      "v_mfma_f32_16x16x32_bf16 v[184:187], v[56:59], v[164:167], v[184:187]\n"
      "buffer_load_dwordx2 v[200:201], v14, s[8:11], s47 offen sc1\n"
      "v_mfma_f32_16x16x32_bf16 v[184:187], v[60:63], v[168:171], v[184:187]\n"
      "s_waitcnt lgkmcnt(5)\n"
      "ds_read_b128 v[128:131], v0 offset:27648\n"
      "v_add_f32 v218, v218, v182\n"
      "v_mfma_f32_16x16x32_bf16 v[188:191], v[64:67], v[100:103], v[188:191]\n"
      "ds_read_b128 v[140:143], v0 offset:29696\n"
      "v_cvt_pk_bf16_f32 v213, v182, v183\n"
      "v_mfma_f32_16x16x32_bf16 v[188:191], v[68:71], v[104:107], v[188:191]\n"
      "ds_read_b128 v[152:155], v0 offset:32768\n"
      "v_mfma_f32_16x16x32_bf16 v[188:191], v[72:75], v[108:111], v[188:191]\n"
      "s_waitcnt lgkmcnt(5)\n"
      "buffer_store_dwordx2 v[216:217], v16, s[8:11], s38 offen sc1\n"
      "v_mfma_f32_16x16x32_bf16 v[188:191], v[76:79], v[112:115], v[188:191]\n"
      "ds_read_b128 v[144:147], v0 offset:30720\n"
      "v_cvt_pk_bf16_f32 v212, v180, v181\n"
      "v_mfma_f32_16x16x32_bf16 v[188:191], v[80:83], v[116:119], v[188:191]\n"
      "s_waitcnt lgkmcnt(4)\n"
      "ds_read_b128 v[164:167], v0 offset:35840\n"
      "v_add_f32 v218, v218, v183\n"
      "v_mfma_f32_16x16x32_bf16 v[188:191], v[84:87], v[120:123], v[188:191]\n"
      "v_fmac_f32 v219, v182, v182\n"
      "v_add_f32 v218, v218, v184\n"
      "v_mfma_f32_16x16x32_bf16 v[188:191], v[88:91], v[124:127], v[188:191]\n"
      "s_waitcnt lgkmcnt(4)\n"
      "ds_read_b128 v[148:151], v0 offset:31744\n"
      "v_add_f32 v218, v218, v185\n"
      "v_mfma_f32_16x16x32_bf16 v[188:191], v[92:95], v[128:131], v[188:191]\n"
      "ds_read_b128 v[160:163], v0 offset:34816\n"
      "v_and_b32 v179, 0xffff0000, v195\n"
      "v_mfma_f32_16x16x32_bf16 v[188:191], v[96:99], v[132:135], v[188:191]\n"
      "buffer_load_dwordx4 v[192:195], v13, s[8:11], s47 offen sc1\n"
      "v_mfma_f32_16x16x32_bf16 v[172:175], a[0:3], v[100:103], v[172:175]\n"
      "v_cvt_pk_bf16_f32 v215, v186, v187\n"
      "v_fmac_f32 v219, v183, v183\n"
      "v_mfma_f32_16x16x32_bf16 v[172:175], a[4:7], v[104:107], v[172:175]\n"
      "v_cvt_pk_bf16_f32 v214, v184, v185\n"
      "v_mfma_f32_16x16x32_bf16 v[172:175], a[8:11], v[108:111], v[172:175]\n"
      "ds_read2_b32 v[168:169], v5 offset0:0 offset1:32\n"
      "v_mfma_f32_16x16x32_bf16 v[172:175], a[12:15], v[112:115], v[172:175]\n"
      "ds_read_b128 v[156:159], v0 offset:33792\n"
      "v_mfma_f32_16x16x32_bf16 v[172:175], a[16:19], v[116:119], v[172:175]\n"
      "buffer_store_dwordx4 v[212:215], v15, s[8:11], s38 offen offset:1024 sc1\n"
      "v_mfma_f32_16x16x32_bf16 v[172:175], a[20:23], v[120:123], v[172:175]\n"
      "v_fmac_f32 v219, v184, v184\n"
      "v_fmac_f32 v219, v185, v185\n"
      "v_mfma_f32_16x16x32_bf16 v[172:175], a[24:27], v[124:127], v[172:175]\n"
      "ds_read2_b32 v[170:171], v5 offset0:64 offset1:96\n"
      "v_add_f32 v218, v218, v186\n"
      "v_mfma_f32_16x16x32_bf16 v[172:175], a[28:31], v[128:131], v[172:175]\n"
      "v_fmac_f32 v219, v186, v186\n"
      "v_add_f32 v218, v218, v187\n"
      "v_mfma_f32_16x16x32_bf16 v[172:175], a[32:35], v[132:135], v[172:175]\n"
      "ds_write_b32 v18, v218\n"
      "v_mfma_f32_16x16x32_bf16 v[172:175], a[36:39], v[136:139], v[172:175]\n"
      "s_waitcnt lgkmcnt(3)\n"
      "s_add_u32 m0, s39, 61440\n"
      "v_add_u32 v21, 1, v21\n"
      "v_mfma_f32_16x16x32_bf16 v[172:175], a[40:43], v[140:143], v[172:175]\n"
      "s_mov_b32 s46, s36\n"
      "v_mfma_f32_16x16x32_bf16 v[172:175], a[44:47], v[144:147], v[172:175]\n"
      "v_fmac_f32 v219, v187, v187\n"
      "v_mfma_f32_16x16x32_bf16 v[172:175], a[48:51], v[148:151], v[172:175]\n"
      "ds_write_b32 v18, v219 offset:4\n"
      "v_mfma_f32_16x16x32_bf16 v[172:175], a[52:55], v[152:155], v[172:175]\n"
      "s_waitcnt vmcnt(10) lgkmcnt(2)\n"
      "v_and_b32 v181, 0xffff0000, v196\n"
      "v_mfma_f32_16x16x32_bf16 v[172:175], a[56:59], v[156:159], v[172:175]\n"
      "ds_write_b32 v23, v21\n"
      "v_mfma_f32_16x16x32_bf16 v[172:175], a[60:63], v[160:163], v[172:175]\n"
      "v_lshlrev_b32 v186, 16, v199\n"
      "v_cmp_ne_u32 vcc, s21, v28\n"
      "v_mfma_f32_16x16x32_bf16 v[172:175], a[64:67], v[164:167], v[172:175]\n"
      "buffer_store_dword v25, v12, s[8:11], s50 offen sc1\n"
      "v_mfma_f32_16x16x32_bf16 v[172:175], a[68:71], v[168:171], v[172:175]\n"
      "s_cmp_eq_u32 s36, s37\n"
      "s_cselect_b32 s48, 49152, s51\n"
      "s_add_u32 s36, s36, s48\n"
      "v_mfma_f32_16x16x32_bf16 v[176:179], a[72:75], v[100:103], v[176:179]\n"
      "s_cbranch_vccnz .Lsp_w1_7\n"
      ".Lbk_w1_7:\n"
      "s_min_u32 s36, s36, s6\n"
      "v_mfma_f32_16x16x32_bf16 v[176:179], a[76:79], v[104:107], v[176:179]\n"
      "buffer_load_dword v28, v11, s[8:11], s36 offen sc1\n"
      "v_mfma_f32_16x16x32_bf16 v[176:179], a[80:83], v[108:111], v[176:179]\n"
      "v_and_b32 v183, 0xffff0000, v197\n"
      "v_mul_f32 v219, v172, v172\n"
      "v_mfma_f32_16x16x32_bf16 v[176:179], a[84:87], v[112:115], v[176:179]\n"
      "buffer_load_dwordx4 v0, s[8:11], s46 offen offset:0 sc1 lds\n"
      "v_mfma_f32_16x16x32_bf16 v[176:179], a[88:91], v[116:119], v[176:179]\n"
      "v_mfma_f32_16x16x32_bf16 v[176:179], a[92:95], v[120:123], v[176:179]\n"
      "v_fmac_f32 v219, v173, v173\n"
      "v_mfma_f32_16x16x32_bf16 v[176:179], a[96:99], v[124:127], v[176:179]\n"
      "v_lshlrev_b32 v184, 16, v198\n"
      "v_and_b32 v185, 0xffff0000, v198\n"
      "v_mfma_f32_16x16x32_bf16 v[176:179], a[100:103], v[128:131], v[176:179]\n"
      "ds_read_b32 v22, v24\n"
      "v_fmac_f32 v219, v174, v174\n"
      "v_mfma_f32_16x16x32_bf16 v[176:179], a[104:107], v[132:135], v[176:179]\n"
      "buffer_load_dwordx4 v0, s[8:11], s46 offen offset:1024 sc1 lds\n"
      "v_mfma_f32_16x16x32_bf16 v[176:179], a[108:111], v[136:139], v[176:179]\n"
      "v_lshlrev_b32 v182, 16, v197\n"
      "v_add_f32 v218, v172, v173\n"
      "v_mfma_f32_16x16x32_bf16 v[176:179], a[112:115], v[140:143], v[176:179]\n"
      "v_add_f32 v218, v218, v174\n"
      "v_fmac_f32 v219, v175, v175\n"
      "v_mfma_f32_16x16x32_bf16 v[176:179], a[116:119], v[144:147], v[176:179]\n"
      "v_cvt_pk_bf16_f32 v208, v172, v173\n"
      "v_mfma_f32_16x16x32_bf16 v[176:179], a[120:123], v[148:151], v[176:179]\n"
      "s_waitcnt lgkmcnt(0)\n"
      "v_and_b32 v187, 0xffff0000, v199\n"
      "v_cmp_gt_i32 vcc, v21, v22\n"
      "v_mfma_f32_16x16x32_bf16 v[176:179], a[124:127], v[152:155], v[176:179]\n"
      "buffer_load_dwordx4 v0, s[8:11], s46 offen offset:3072 sc1 lds\n"
      "v_mfma_f32_16x16x32_bf16 v[176:179], a[128:131], v[156:159], v[176:179]\n"
      "v_lshlrev_b32 v180, 16, v196\n"
      "v_add_f32 v218, v218, v175\n"
      "v_mfma_f32_16x16x32_bf16 v[176:179], a[132:135], v[160:163], v[176:179]\n"
      "s_cbranch_vccnz .Lsp_wz1_8\n"
      ".Lbk_wz1_8:\n"
      "ds_read2st64_b32 v[204:205], v17 offset1:1\n"
      "v_mfma_f32_16x16x32_bf16 v[176:179], a[136:139], v[164:167], v[176:179]\n"
      "ds_read2st64_b32 v[206:207], v17 offset0:2 offset1:3\n"
      "v_add_u32 v21, 1, v21\n"
      "v_mfma_f32_16x16x32_bf16 v[176:179], a[140:143], v[168:171], v[176:179]\n"
      "s_waitcnt vmcnt(10)\n"
      "ds_write_b32 v23, v21\n"
      "v_mfma_f32_16x16x32_bf16 v[180:183], a[144:147], v[100:103], v[180:183]\n"
      "buffer_load_dwordx4 v0, s[8:11], s46 offen offset:2048 sc1 lds\n"
      "v_mfma_f32_16x16x32_bf16 v[180:183], a[148:151], v[104:107], v[180:183]\n"
      "v_mfma_f32_16x16x32_bf16 v[180:183], a[152:155], v[108:111], v[180:183]\n"
      "s_add_u32 m0, s40, 61440\n"
      "v_add_f32 v218, v218, v176\n"
      "v_mfma_f32_16x16x32_bf16 v[180:183], a[156:159], v[112:115], v[180:183]\n"
      "v_fmac_f32 v219, v176, v176\n"
      "v_cvt_pk_bf16_f32 v209, v174, v175\n"
      "v_mfma_f32_16x16x32_bf16 v[180:183], a[160:163], v[116:119], v[180:183]\n"
      "buffer_load_dwordx4 v[196:199], v13, s[8:11], s47 offen offset:1024 sc1\n"
      "v_mfma_f32_16x16x32_bf16 v[180:183], a[164:167], v[120:123], v[180:183]\n"
      "s_waitcnt lgkmcnt(1)\n"
      "v_cvt_pk_bf16_f32 v211, v178, v179\n"
      "v_add_f32 v190, v190, v206\n"
      "v_mfma_f32_16x16x32_bf16 v[180:183], a[168:171], v[124:127], v[180:183]\n"
      "v_add_f32 v218, v218, v177\n"
      "v_add_f32 v218, v218, v178\n"
      "v_mfma_f32_16x16x32_bf16 v[180:183], a[172:175], v[128:131], v[180:183]\n"
      "v_add_f32 v218, v218, v179\n"
      "v_add_f32 v189, v189, v205\n"
      "v_mfma_f32_16x16x32_bf16 v[180:183], a[176:179], v[132:135], v[180:183]\n"
      "buffer_load_dwordx4 v1, s[8:11], s46 offen offset:0 sc1 lds\n"
      "v_mfma_f32_16x16x32_bf16 v[180:183], a[180:183], v[136:139], v[180:183]\n"
      "v_add_f32 v188, v188, v204\n"
      "v_fmac_f32 v219, v177, v177\n"
      "v_mfma_f32_16x16x32_bf16 v[180:183], a[184:187], v[140:143], v[180:183]\n"
      "ds_read_b32 v22, v24\n"
      "v_fmac_f32 v219, v178, v178\n"
      "v_mfma_f32_16x16x32_bf16 v[180:183], a[188:191], v[144:147], v[180:183]\n"
      "v_cvt_pk_bf16_f32 v210, v176, v177\n"
      "v_fmac_f32 v219, v179, v179\n"
      "v_mfma_f32_16x16x32_bf16 v[180:183], a[192:195], v[148:151], v[180:183]\n"
      "v_add_f32 v191, v191, v207\n"
      "v_fmac_f32 v219, v188, v188\n"
      "v_mfma_f32_16x16x32_bf16 v[180:183], a[196:199], v[152:155], v[180:183]\n"
      "v_add_f32 v218, v218, v188\n"
      "v_mfma_f32_16x16x32_bf16 v[180:183], a[200:203], v[156:159], v[180:183]\n"
      "v_add_f32 v218, v218, v189\n"
      "v_add_f32 v218, v218, v190\n"
      "v_mfma_f32_16x16x32_bf16 v[180:183], a[204:207], v[160:163], v[180:183]\n"
      "s_waitcnt lgkmcnt(0)\n"
      "v_fmac_f32 v219, v189, v189\n"
      "v_cmp_gt_i32 vcc, v21, v22\n"
      "v_mfma_f32_16x16x32_bf16 v[180:183], a[208:211], v[164:167], v[180:183]\n"
      "v_cvt_pk_bf16_f32 v216, v188, v189\n"
      "v_fmac_f32 v219, v190, v190\n"
      "v_mfma_f32_16x16x32_bf16 v[180:183], a[212:215], v[168:171], v[180:183]\n"
      "s_waitcnt vmcnt(11)\n"
      "v_and_b32 v189, 0xffff0000, v200\n"
      "v_fmac_f32 v219, v191, v191\n"
      "v_mfma_f32_16x16x32_bf16 v[184:187], a[216:219], v[100:103], v[184:187]\n"
      "s_add_u32 s38, s38, 24576\n"
      "v_cvt_pk_bf16_f32 v217, v190, v191\n"
      "v_lshlrev_b32_e64 v188, 16, v200\n"
      "v_mfma_f32_16x16x32_bf16 v[184:187], a[220:223], v[104:107], v[184:187]\n"
      "s_cbranch_vccnz .Lsp_wy0_9\n"
      ".Lbk_wy0_9:\n"
      "v_mfma_f32_16x16x32_bf16 v[184:187], a[224:227], v[108:111], v[184:187]\n"
      "ds_read_b128 v[104:107], v10 offset:1024\n"
      "v_add_f32 v218, v218, v191\n"
      "v_mfma_f32_16x16x32_bf16 v[184:187], a[228:231], v[112:115], v[184:187]\n"
      "v_fmac_f32 v219, v180, v180\n"
      "v_lshlrev_b32 v190, 16, v201\n"
      "v_mfma_f32_16x16x32_bf16 v[184:187], a[232:235], v[116:119], v[184:187]\n"
      "ds_read_b128 v[100:103], v10 offset:0\n"
      "v_add_f32 v218, v218, v180\n"
      "v_mfma_f32_16x16x32_bf16 v[184:187], a[236:239], v[120:123], v[184:187]\n"
      "s_waitcnt vmcnt(9)\n"
      "ds_read_b128 v[120:123], v10 offset:5120\n"
      "v_lshlrev_b32 v178, 16, v195\n"
      "v_mfma_f32_16x16x32_bf16 v[184:187], a[240:243], v[124:127], v[184:187]\n"
      "ds_read_b128 v[112:115], v10 offset:3072\n"
      "v_fmac_f32 v219, v181, v181\n"
      "v_mfma_f32_16x16x32_bf16 v[184:187], a[244:247], v[128:131], v[184:187]\n"
      "ds_read_b128 v[116:119], v10 offset:4096\n"
      "v_lshlrev_b32 v174, 16, v193\n"
      "v_mfma_f32_16x16x32_bf16 v[184:187], a[248:251], v[132:135], v[184:187]\n"
      "ds_read_b128 v[124:127], v10 offset:6144\n"
      "v_fmac_f32 v219, v182, v182\n"
      "v_mfma_f32_16x16x32_bf16 v[184:187], a[252:255], v[136:139], v[184:187]\n"
      "s_sub_u32 s47, s46, s45\n"
      "ds_read_b128 v[108:111], v10 offset:2048\n"
      "v_mfma_f32_16x16x32_bf16 v[184:187], v[32:35], v[140:143], v[184:187]\n"
      "v_and_b32 v191, 0xffff0000, v201\n"
      "v_and_b32 v177, 0xffff0000, v194\n"
      "v_mfma_f32_16x16x32_bf16 v[184:187], v[36:39], v[144:147], v[184:187]\n"
      "ds_read_b128 v[136:139], v10 offset:8192\n"
      "v_lshlrev_b32 v176, 16, v194\n"
      "v_mfma_f32_16x16x32_bf16 v[184:187], v[40:43], v[148:151], v[184:187]\n"
      "ds_read_b128 v[128:131], v10 offset:7168\n"
      "v_lshlrev_b32 v172, 16, v192\n"
      "v_mfma_f32_16x16x32_bf16 v[184:187], v[44:47], v[152:155], v[184:187]\n"
      "v_fmac_f32 v219, v183, v183\n"
      "v_and_b32 v175, 0xffff0000, v193\n"
      "v_mfma_f32_16x16x32_bf16 v[184:187], v[48:51], v[156:159], v[184:187]\n"
      "buffer_load_dwordx2 v[200:201], v14, s[8:11], s47 offen sc1\n"
      "v_mfma_f32_16x16x32_bf16 v[184:187], v[52:55], v[160:163], v[184:187]\n"
      "ds_read2_b32 v[132:133], v6 offset0:0 offset1:32\n"
      "v_and_b32 v173, 0xffff0000, v192\n"
      "v_mfma_f32_16x16x32_bf16 v[184:187], v[56:59], v[164:167], v[184:187]\n"
      "s_waitcnt lgkmcnt(3)\n"
      "ds_read2_b32 v[134:135], v6 offset0:64 offset1:96\n"
      "v_add_f32 v218, v218, v181\n"
      "v_mfma_f32_16x16x32_bf16 v[184:187], v[60:63], v[168:171], v[184:187]\n"
      "v_mfma_f32_16x16x32_bf16 v[188:191], v[64:67], v[100:103], v[188:191]\n"
      "ds_read_b128 v[140:143], v10 offset:9216\n"
      "v_and_b32 v179, 0xffff0000, v195\n"
      "v_mfma_f32_16x16x32_bf16 v[188:191], v[68:71], v[104:107], v[188:191]\n"
      "buffer_load_dwordx4 v[192:195], v13, s[8:11], s47 offen sc1\n"
      "v_mfma_f32_16x16x32_bf16 v[188:191], v[72:75], v[108:111], v[188:191]\n"
      "v_add_f32 v218, v218, v182\n"
      "v_mfma_f32_16x16x32_bf16 v[188:191], v[76:79], v[112:115], v[188:191]\n"
      "ds_read_b128 v[156:159], v10 offset:13312\n"
      "v_fmac_f32 v219, v184, v184\n"
      "v_mfma_f32_16x16x32_bf16 v[188:191], v[80:83], v[116:119], v[188:191]\n"
      "ds_read_b128 v[164:167], v10 offset:15360\n"
      "v_cvt_pk_bf16_f32 v213, v182, v183\n"
      "v_mfma_f32_16x16x32_bf16 v[188:191], v[84:87], v[120:123], v[188:191]\n"
      "ds_read_b128 v[144:147], v10 offset:10240\n"
      "v_mfma_f32_16x16x32_bf16 v[188:191], v[88:91], v[124:127], v[188:191]\n"
      "s_waitcnt lgkmcnt(4)\n"
      "v_cvt_pk_bf16_f32 v215, v186, v187\n"
      "v_add_f32 v218, v218, v183\n"
      "v_mfma_f32_16x16x32_bf16 v[188:191], v[92:95], v[128:131], v[188:191]\n"
      "buffer_store_dwordx4 v[208:211], v15, s[8:11], s38 offen sc1\n"
      "v_mfma_f32_16x16x32_bf16 v[188:191], v[96:99], v[132:135], v[188:191]\n"
      "v_cvt_pk_bf16_f32 v212, v180, v181\n"
      "v_add_f32 v218, v218, v184\n"
      "v_mfma_f32_16x16x32_bf16 v[172:175], a[0:3], v[100:103], v[172:175]\n"
      "s_add_u32 s50, s50, 24576\n"
      "v_cvt_pk_bf16_f32 v214, v184, v185\n"
      "v_add_f32 v218, v218, v185\n"
      "v_mfma_f32_16x16x32_bf16 v[172:175], a[4:7], v[104:107], v[172:175]\n"
      "s_mov_b32 s46, s36\n"
      "v_add_f32 v218, v218, v186\n"
      "v_mfma_f32_16x16x32_bf16 v[172:175], a[8:11], v[108:111], v[172:175]\n"
      "ds_read_b128 v[148:151], v10 offset:11264\n"
      "v_add_f32 v218, v218, v187\n"
      "v_mfma_f32_16x16x32_bf16 v[172:175], a[12:15], v[112:115], v[172:175]\n"
      "v_fmac_f32 v219, v185, v185\n"
      "v_add_u32 v21, 1, v21\n"
      "v_mfma_f32_16x16x32_bf16 v[172:175], a[16:19], v[116:119], v[172:175]\n"
      "s_add_u32 s36, s36, 24576\n"
      "ds_read_b128 v[152:155], v10 offset:12288\n"
      "v_fmac_f32_e64 v219, v186, v186\n"
      "v_mfma_f32_16x16x32_bf16 v[172:175], a[20:23], v[120:123], v[172:175]\n"
      "buffer_store_dwordx2 v[216:217], v16, s[8:11], s38 offen sc1\n"
      "v_mfma_f32_16x16x32_bf16 v[172:175], a[24:27], v[124:127], v[172:175]\n"
      "ds_read_b128 v[160:163], v10 offset:14336\n"
      "v_mfma_f32_16x16x32_bf16 v[172:175], a[28:31], v[128:131], v[172:175]\n"
      "ds_read2_b32 v[170:171], v7 offset0:64 offset1:96\n"
      "v_mfma_f32_16x16x32_bf16 v[172:175], a[32:35], v[132:135], v[172:175]\n"
      "ds_read2_b32 v[168:169], v7 offset0:0 offset1:32\n"
      "s_min_u32 s36, s36, s6\n"
      "v_mfma_f32_16x16x32_bf16 v[172:175], a[36:39], v[136:139], v[172:175]\n"
      "s_waitcnt lgkmcnt(4)\n"
      "v_mfma_f32_16x16x32_bf16 v[172:175], a[40:43], v[140:143], v[172:175]\n"
      "buffer_store_dwordx4 v[212:215], v15, s[8:11], s38 offen offset:1024 sc1\n"
      "v_mfma_f32_16x16x32_bf16 v[172:175], a[44:47], v[144:147], v[172:175]\n"
      "ds_write_b32 v18, v218\n"
      "v_mfma_f32_16x16x32_bf16 v[172:175], a[48:51], v[148:151], v[172:175]\n"
      "s_waitcnt vmcnt(11) lgkmcnt(4)\n"
      "v_fmac_f32_e64 v219, v187, v187\n"
      "v_cmp_ne_u32 vcc, s21, v28\n"
      "v_mfma_f32_16x16x32_bf16 v[172:175], a[52:55], v[152:155], v[172:175]\n"
      "ds_write_b32 v18, v219 offset:4\n"
      "v_mfma_f32_16x16x32_bf16 v[172:175], a[56:59], v[156:159], v[172:175]\n"
      "s_waitcnt lgkmcnt(3)\n"
      "s_add_u32 m0, s39, 0\n"
      "v_mfma_f32_16x16x32_bf16 v[172:175], a[60:63], v[160:163], v[172:175]\n"
      "ds_write_b32 v23, v21\n"
      "v_mfma_f32_16x16x32_bf16 v[172:175], a[64:67], v[164:167], v[172:175]\n"
      "s_waitcnt lgkmcnt(3)\n"
      "s_cbranch_vccnz .Lsp_w0_10\n"
      ".Lbk_w0_10:\n"
      "v_mfma_f32_16x16x32_bf16 v[172:175], a[68:71], v[168:171], v[172:175]\n"
      "v_mfma_f32_16x16x32_bf16 v[176:179], a[72:75], v[100:103], v[176:179]\n"
      "buffer_load_dword v28, v11, s[8:11], s36 offen sc1\n"
      "v_mfma_f32_16x16x32_bf16 v[176:179], a[76:79], v[104:107], v[176:179]\n"
      "s_waitcnt vmcnt(7) lgkmcnt(2)\n"
      "v_lshlrev_b32 v186, 16, v199\n"
      "v_lshlrev_b32 v184, 16, v198\n"
      "v_mfma_f32_16x16x32_bf16 v[176:179], a[80:83], v[108:111], v[176:179]\n"
      "v_lshlrev_b32 v180, 16, v196\n"
      "v_and_b32 v183, 0xffff0000, v197\n"
      "v_mfma_f32_16x16x32_bf16 v[176:179], a[84:87], v[112:115], v[176:179]\n"
      "buffer_store_dword v25, v12, s[8:11], s50 offen sc1\n"
      "v_mfma_f32_16x16x32_bf16 v[176:179], a[88:91], v[116:119], v[176:179]\n"
      "v_lshlrev_b32 v182, 16, v197\n"
      "v_and_b32 v181, 0xffff0000, v196\n"
      "v_mfma_f32_16x16x32_bf16 v[176:179], a[92:95], v[120:123], v[176:179]\n"
      "v_and_b32 v187, 0xffff0000, v199\n"
      "v_and_b32 v185, 0xffff0000, v198\n"
      "v_mfma_f32_16x16x32_bf16 v[176:179], a[96:99], v[124:127], v[176:179]\n"
      "buffer_load_dwordx4 v[196:199], v13, s[8:11], s47 offen offset:1024 sc1\n"
      "v_mfma_f32_16x16x32_bf16 v[176:179], a[100:103], v[128:131], v[176:179]\n"
      "ds_read_b32 v22, v24\n"
      "v_mul_f32 v219, v172, v172\n"
      "v_mfma_f32_16x16x32_bf16 v[176:179], a[104:107], v[132:135], v[176:179]\n"
      "v_mfma_f32_16x16x32_bf16 v[176:179], a[108:111], v[136:139], v[176:179]\n"
      "v_mfma_f32_16x16x32_bf16 v[176:179], a[112:115], v[140:143], v[176:179]\n"
      "buffer_load_dwordx4 v0, s[8:11], s46 offen offset:0 sc1 lds\n"
      "v_mfma_f32_16x16x32_bf16 v[176:179], a[116:119], v[144:147], v[176:179]\n"
      "v_add_f32 v218, v172, v173\n"
      "v_fmac_f32 v219, v173, v173\n"
      "v_mfma_f32_16x16x32_bf16 v[176:179], a[120:123], v[148:151], v[176:179]\n"
      "s_waitcnt lgkmcnt(0)\n"
      "v_cmp_gt_i32 vcc, v21, v22\n"
      "v_mfma_f32_16x16x32_bf16 v[176:179], a[124:127], v[152:155], v[176:179]\n"
      "v_mfma_f32_16x16x32_bf16 v[176:179], a[128:131], v[156:159], v[176:179]\n"
      "buffer_load_dwordx4 v0, s[8:11], s46 offen offset:1024 sc1 lds\n"
      "v_mfma_f32_16x16x32_bf16 v[176:179], a[132:135], v[160:163], v[176:179]\n"
      "s_cbranch_vccnz .Lsp_wz0_11\n"
      ".Lbk_wz0_11:\n"
      "ds_read2st64_b32 v[204:205], v17 offset1:1\n"
      "v_mfma_f32_16x16x32_bf16 v[176:179], a[136:139], v[164:167], v[176:179]\n"
      "ds_read2st64_b32 v[206:207], v17 offset0:2 offset1:3\n"
      "v_add_u32 v21, 1, v21\n"
      "v_mfma_f32_16x16x32_bf16 v[176:179], a[140:143], v[168:171], v[176:179]\n"
      "s_waitcnt vmcnt(10)\n"
      "ds_write_b32 v23, v21\n"
      "v_mfma_f32_16x16x32_bf16 v[180:183], a[144:147], v[100:103], v[180:183]\n"
      "buffer_load_dwordx4 v0, s[8:11], s46 offen offset:3072 sc1 lds\n"
      "v_mfma_f32_16x16x32_bf16 v[180:183], a[148:151], v[104:107], v[180:183]\n"
      "v_add_f32 v218, v218, v174\n"
      "v_fmac_f32 v219, v174, v174\n"
      "v_mfma_f32_16x16x32_bf16 v[180:183], a[152:155], v[108:111], v[180:183]\n"
      "v_fmac_f32 v219, v175, v175\n"
      "v_fmac_f32 v219, v176, v176\n"
      "v_mfma_f32_16x16x32_bf16 v[180:183], a[156:159], v[112:115], v[180:183]\n"
      "v_add_f32 v218, v218, v175\n"
      "v_add_f32 v218, v218, v176\n"
      "v_mfma_f32_16x16x32_bf16 v[180:183], a[160:163], v[116:119], v[180:183]\n"
      "buffer_load_dwordx4 v0, s[8:11], s46 offen offset:2048 sc1 lds\n"
      "v_mfma_f32_16x16x32_bf16 v[180:183], a[164:167], v[120:123], v[180:183]\n"
      "s_add_u32 m0, s40, 0\n"
      "v_add_f32 v218, v218, v177\n"
      "v_cvt_pk_bf16_f32 v209, v174, v175\n"
      "v_mfma_f32_16x16x32_bf16 v[180:183], a[168:171], v[124:127], v[180:183]\n"
      "s_waitcnt lgkmcnt(0)\n"
      "v_add_f32 v188, v188, v204\n"
      "v_add_f32 v190, v190, v206\n"
      "v_mfma_f32_16x16x32_bf16 v[180:183], a[172:175], v[128:131], v[180:183]\n"
      "v_cvt_pk_bf16_f32 v208, v172, v173\n"
      "v_fmac_f32 v219, v177, v177\n"
      "v_mfma_f32_16x16x32_bf16 v[180:183], a[176:179], v[132:135], v[180:183]\n"
      "buffer_load_dwordx4 v1, s[8:11], s46 offen offset:0 sc1 lds\n"
      "v_mfma_f32_16x16x32_bf16 v[180:183], a[180:183], v[136:139], v[180:183]\n"
      "s_add_u32 s38, s38, 24576\n"
      "v_cvt_pk_bf16_f32 v210, v176, v177\n"
      "v_mfma_f32_16x16x32_bf16 v[180:183], a[184:187], v[140:143], v[180:183]\n"
      "ds_read_b32 v22, v24\n"
      "v_add_f32 v218, v218, v178\n"
      "v_mfma_f32_16x16x32_bf16 v[180:183], a[188:191], v[144:147], v[180:183]\n"
      "v_add_f32 v218, v218, v179\n"
      "v_add_f32 v218, v218, v188\n"
      "v_mfma_f32_16x16x32_bf16 v[180:183], a[192:195], v[148:151], v[180:183]\n"
      "s_add_u32 s50, s50, 24576\n"
      "v_fmac_f32 v219, v178, v178\n"
      "v_fmac_f32 v219, v179, v179\n"
      "v_mfma_f32_16x16x32_bf16 v[180:183], a[196:199], v[152:155], v[180:183]\n"
      "v_add_f32 v191, v191, v207\n"
      "v_cvt_pk_bf16_f32 v211, v178, v179\n"
      "v_mfma_f32_16x16x32_bf16 v[180:183], a[200:203], v[156:159], v[180:183]\n"
      "v_fmac_f32 v219, v188, v188\n"
      "v_add_f32 v189, v189, v205\n"
      "v_mfma_f32_16x16x32_bf16 v[180:183], a[204:207], v[160:163], v[180:183]\n"
      "s_waitcnt lgkmcnt(0)\n"
      "v_fmac_f32 v219, v189, v189\n"
      "v_cmp_gt_i32 vcc, v21, v22\n"
      "v_mfma_f32_16x16x32_bf16 v[180:183], a[208:211], v[164:167], v[180:183]\n"
      ".Lw_p1:\n"
      "s_cbranch_execz .Lw_pr\n"
      "v_fmac_f32 v219, v190, v190\n"
      "v_add_f32 v218, v218, v189\n"
      "v_mfma_f32_16x16x32_bf16 v[180:183], a[212:215], v[168:171], v[180:183]\n"
      "s_waitcnt vmcnt(11)\n"
      "v_lshlrev_b32 v172, 16, v192\n"
      "v_mfma_f32_16x16x32_bf16 v[184:187], a[216:219], v[100:103], v[184:187]\n"
      "v_and_b32 v175, 0xffff0000, v193\n"
      "v_and_b32 v177, 0xffff0000, v194\n"
      "v_mfma_f32_16x16x32_bf16 v[184:187], a[220:223], v[104:107], v[184:187]\n"
      "s_cbranch_vccnz .Lsp_wy1_12\n"
      ".Lbk_wy1_12:\n"
      "v_add_f32 v218, v218, v190\n"
      "v_mfma_f32_16x16x32_bf16 v[184:187], a[224:227], v[108:111], v[184:187]\n"
      "ds_read_b128 v[104:107], v10 offset:21504\n"
      "v_cvt_pk_bf16_f32 v216, v188, v189\n"
      "v_mfma_f32_16x16x32_bf16 v[184:187], a[228:231], v[112:115], v[184:187]\n"
      "v_add_f32 v218, v218, v191\n"
      "v_add_f32 v218, v218, v180\n"
      "v_mfma_f32_16x16x32_bf16 v[184:187], a[232:235], v[116:119], v[184:187]\n"
      "ds_read_b128 v[116:119], v10 offset:24576\n"
      "v_cvt_pk_bf16_f32 v217, v190, v191\n"
      "v_mfma_f32_16x16x32_bf16 v[184:187], a[236:239], v[120:123], v[184:187]\n"
      "ds_read_b128 v[108:111], v10 offset:22528\n"
      "v_lshlrev_b32 v188, 16, v200\n"
      "v_mfma_f32_16x16x32_bf16 v[184:187], a[240:243], v[124:127], v[184:187]\n"
      "ds_read_b128 v[100:103], v10 offset:20480\n"
      "v_fmac_f32 v219, v191, v191\n"
      "v_mfma_f32_16x16x32_bf16 v[184:187], a[244:247], v[128:131], v[184:187]\n"
      "v_lshlrev_b32 v190, 16, v201\n"
      "v_and_b32 v191, 0xffff0000, v201\n"
      "v_mfma_f32_16x16x32_bf16 v[184:187], a[248:251], v[132:135], v[184:187]\n"
      "ds_read2_b32 v[134:135], v8 offset0:64 offset1:96\n"
      "v_lshlrev_b32 v174, 16, v193\n"
      "v_mfma_f32_16x16x32_bf16 v[184:187], a[252:255], v[136:139], v[184:187]\n"
      "s_sub_u32 s47, s46, s45\n"
      "v_and_b32 v189, 0xffff0000, v200\n"
      "v_fmac_f32 v219, v180, v180\n"
      "v_mfma_f32_16x16x32_bf16 v[184:187], v[32:35], v[140:143], v[184:187]\n"
      "buffer_store_dwordx4 v[208:211], v15, s[8:11], s38 offen sc1\n"
      "v_mfma_f32_16x16x32_bf16 v[184:187], v[36:39], v[144:147], v[184:187]\n"
      "ds_read_b128 v[136:139], v10 offset:28672\n"
      "v_fmac_f32 v219, v181, v181\n"
      "v_mfma_f32_16x16x32_bf16 v[184:187], v[40:43], v[148:151], v[184:187]\n"
      "ds_read2_b32 v[132:133], v8 offset0:0 offset1:32\n"
      "v_lshlrev_b32 v178, 16, v195\n"
      "v_mfma_f32_16x16x32_bf16 v[184:187], v[44:47], v[152:155], v[184:187]\n"
      "ds_read_b128 v[112:115], v10 offset:23552\n"
      "v_add_f32 v218, v218, v181\n"
      "v_mfma_f32_16x16x32_bf16 v[184:187], v[48:51], v[156:159], v[184:187]\n"
      "ds_read_b128 v[120:123], v10 offset:25600\n"
      "v_and_b32 v173, 0xffff0000, v192\n"
      "v_mfma_f32_16x16x32_bf16 v[184:187], v[52:55], v[160:163], v[184:187]\n"
      "ds_read_b128 v[124:127], v10 offset:26624\n"
      "v_lshlrev_b32 v176, 16, v194\n"
      "v_mfma_f32_16x16x32_bf16 v[184:187], v[56:59], v[164:167], v[184:187]\n"
      "buffer_load_dwordx2 v[200:201], v14, s[8:11], s47 offen sc1\n"
      "v_mfma_f32_16x16x32_bf16 v[184:187], v[60:63], v[168:171], v[184:187]\n"
      "s_waitcnt lgkmcnt(5)\n"
      "ds_read_b128 v[128:131], v10 offset:27648\n"
      "v_add_f32 v218, v218, v182\n"
      "v_mfma_f32_16x16x32_bf16 v[188:191], v[64:67], v[100:103], v[188:191]\n"
      "ds_read_b128 v[140:143], v10 offset:29696\n"
      "v_cvt_pk_bf16_f32 v213, v182, v183\n"
      "v_mfma_f32_16x16x32_bf16 v[188:191], v[68:71], v[104:107], v[188:191]\n"
      "ds_read_b128 v[152:155], v10 offset:32768\n"
      "v_mfma_f32_16x16x32_bf16 v[188:191], v[72:75], v[108:111], v[188:191]\n"
      "s_waitcnt lgkmcnt(5)\n"
      "buffer_store_dwordx2 v[216:217], v16, s[8:11], s38 offen sc1\n"
      "v_mfma_f32_16x16x32_bf16 v[188:191], v[76:79], v[112:115], v[188:191]\n"
      "ds_read_b128 v[144:147], v10 offset:30720\n"
      "v_cvt_pk_bf16_f32 v212, v180, v181\n"
      "v_mfma_f32_16x16x32_bf16 v[188:191], v[80:83], v[116:119], v[188:191]\n"
      "s_waitcnt lgkmcnt(4)\n"
      "ds_read_b128 v[164:167], v10 offset:35840\n"
      "v_add_f32 v218, v218, v183\n"
      "v_mfma_f32_16x16x32_bf16 v[188:191], v[84:87], v[120:123], v[188:191]\n"
      "v_fmac_f32 v219, v182, v182\n"
      "v_add_f32 v218, v218, v184\n"
      "v_mfma_f32_16x16x32_bf16 v[188:191], v[88:91], v[124:127], v[188:191]\n"
      "s_waitcnt lgkmcnt(4)\n"
      "ds_read_b128 v[148:151], v10 offset:31744\n"
      "v_add_f32 v218, v218, v185\n"
      "v_mfma_f32_16x16x32_bf16 v[188:191], v[92:95], v[128:131], v[188:191]\n"
      "ds_read_b128 v[160:163], v10 offset:34816\n"
      "v_and_b32 v179, 0xffff0000, v195\n"
      "v_mfma_f32_16x16x32_bf16 v[188:191], v[96:99], v[132:135], v[188:191]\n"
      "buffer_load_dwordx4 v[192:195], v13, s[8:11], s47 offen sc1\n"
      "v_mfma_f32_16x16x32_bf16 v[172:175], a[0:3], v[100:103], v[172:175]\n"
      "v_cvt_pk_bf16_f32 v215, v186, v187\n"
      "v_fmac_f32 v219, v183, v183\n"
      "v_mfma_f32_16x16x32_bf16 v[172:175], a[4:7], v[104:107], v[172:175]\n"
      "v_cvt_pk_bf16_f32 v214, v184, v185\n"
      "v_mfma_f32_16x16x32_bf16 v[172:175], a[8:11], v[108:111], v[172:175]\n"
      "ds_read2_b32 v[168:169], v9 offset0:0 offset1:32\n"
      "v_mfma_f32_16x16x32_bf16 v[172:175], a[12:15], v[112:115], v[172:175]\n"
      "ds_read_b128 v[156:159], v10 offset:33792\n"
      "v_mfma_f32_16x16x32_bf16 v[172:175], a[16:19], v[116:119], v[172:175]\n"
      "buffer_store_dwordx4 v[212:215], v15, s[8:11], s38 offen offset:1024 sc1\n"
      "v_mfma_f32_16x16x32_bf16 v[172:175], a[20:23], v[120:123], v[172:175]\n"
      "v_fmac_f32 v219, v184, v184\n"
      "v_fmac_f32 v219, v185, v185\n"
      "v_mfma_f32_16x16x32_bf16 v[172:175], a[24:27], v[124:127], v[172:175]\n"
      "ds_read2_b32 v[170:171], v9 offset0:64 offset1:96\n"
      "v_add_f32 v218, v218, v186\n"
      "v_mfma_f32_16x16x32_bf16 v[172:175], a[28:31], v[128:131], v[172:175]\n"
      "v_fmac_f32 v219, v186, v186\n"
      "v_add_f32 v218, v218, v187\n"
      "v_mfma_f32_16x16x32_bf16 v[172:175], a[32:35], v[132:135], v[172:175]\n"
      "ds_write_b32 v18, v218\n"
      "v_mfma_f32_16x16x32_bf16 v[172:175], a[36:39], v[136:139], v[172:175]\n"
      "s_waitcnt lgkmcnt(3)\n"
      "s_add_u32 m0, s39, 20480\n"
      "v_add_u32 v21, 1, v21\n"
      "v_mfma_f32_16x16x32_bf16 v[172:175], a[40:43], v[140:143], v[172:175]\n"
      "s_mov_b32 s46, s36\n"
      "v_mfma_f32_16x16x32_bf16 v[172:175], a[44:47], v[144:147], v[172:175]\n"
      "v_fmac_f32 v219, v187, v187\n"
      "v_mfma_f32_16x16x32_bf16 v[172:175], a[48:51], v[148:151], v[172:175]\n"
      "ds_write_b32 v18, v219 offset:4\n"
      "v_mfma_f32_16x16x32_bf16 v[172:175], a[52:55], v[152:155], v[172:175]\n"
      "s_waitcnt vmcnt(10) lgkmcnt(2)\n"
      "v_and_b32 v181, 0xffff0000, v196\n"
      "v_mfma_f32_16x16x32_bf16 v[172:175], a[56:59], v[156:159], v[172:175]\n"
      "ds_write_b32 v23, v21\n"
      "v_mfma_f32_16x16x32_bf16 v[172:175], a[60:63], v[160:163], v[172:175]\n"
      "v_lshlrev_b32 v186, 16, v199\n"
      "v_cmp_ne_u32 vcc, s21, v28\n"
      "v_mfma_f32_16x16x32_bf16 v[172:175], a[64:67], v[164:167], v[172:175]\n"
      "buffer_store_dword v25, v12, s[8:11], s50 offen sc1\n"
      "v_mfma_f32_16x16x32_bf16 v[172:175], a[68:71], v[168:171], v[172:175]\n"
      "s_cmp_eq_u32 s36, s37\n"
      "s_cselect_b32 s48, 49152, s51\n"
      "s_add_u32 s36, s36, s48\n"
      "v_mfma_f32_16x16x32_bf16 v[176:179], a[72:75], v[100:103], v[176:179]\n"
      "s_cbranch_vccnz .Lsp_w1_13\n"
      ".Lbk_w1_13:\n"
      "s_min_u32 s36, s36, s6\n"
      "v_mfma_f32_16x16x32_bf16 v[176:179], a[76:79], v[104:107], v[176:179]\n"
      "buffer_load_dword v28, v11, s[8:11], s36 offen sc1\n"
      "v_mfma_f32_16x16x32_bf16 v[176:179], a[80:83], v[108:111], v[176:179]\n"
      "v_and_b32 v183, 0xffff0000, v197\n"
      "v_mul_f32 v219, v172, v172\n"
      "v_mfma_f32_16x16x32_bf16 v[176:179], a[84:87], v[112:115], v[176:179]\n"
      "buffer_load_dwordx4 v0, s[8:11], s46 offen offset:0 sc1 lds\n"
      "v_mfma_f32_16x16x32_bf16 v[176:179], a[88:91], v[116:119], v[176:179]\n"
      "v_mfma_f32_16x16x32_bf16 v[176:179], a[92:95], v[120:123], v[176:179]\n"
      "v_fmac_f32 v219, v173, v173\n"
      "v_mfma_f32_16x16x32_bf16 v[176:179], a[96:99], v[124:127], v[176:179]\n"
      "v_lshlrev_b32 v184, 16, v198\n"
      "v_and_b32 v185, 0xffff0000, v198\n"
      "v_mfma_f32_16x16x32_bf16 v[176:179], a[100:103], v[128:131], v[176:179]\n"
      "ds_read_b32 v22, v24\n"
      "v_fmac_f32 v219, v174, v174\n"
      "v_mfma_f32_16x16x32_bf16 v[176:179], a[104:107], v[132:135], v[176:179]\n"
      "buffer_load_dwordx4 v0, s[8:11], s46 offen offset:1024 sc1 lds\n"
      "v_mfma_f32_16x16x32_bf16 v[176:179], a[108:111], v[136:139], v[176:179]\n"
      "v_lshlrev_b32 v182, 16, v197\n"
      "v_add_f32 v218, v172, v173\n"
      "v_mfma_f32_16x16x32_bf16 v[176:179], a[112:115], v[140:143], v[176:179]\n"
      "v_add_f32 v218, v218, v174\n"
      "v_fmac_f32 v219, v175, v175\n"
      "v_mfma_f32_16x16x32_bf16 v[176:179], a[116:119], v[144:147], v[176:179]\n"
      "v_cvt_pk_bf16_f32 v208, v172, v173\n"
      "v_mfma_f32_16x16x32_bf16 v[176:179], a[120:123], v[148:151], v[176:179]\n"
      "s_waitcnt lgkmcnt(0)\n"
      "v_and_b32 v187, 0xffff0000, v199\n"
      "v_cmp_gt_i32 vcc, v21, v22\n"
      "v_mfma_f32_16x16x32_bf16 v[176:179], a[124:127], v[152:155], v[176:179]\n"
      "buffer_load_dwordx4 v0, s[8:11], s46 offen offset:3072 sc1 lds\n"
      "v_mfma_f32_16x16x32_bf16 v[176:179], a[128:131], v[156:159], v[176:179]\n"
      "v_lshlrev_b32 v180, 16, v196\n"
      "v_add_f32 v218, v218, v175\n"
      "v_mfma_f32_16x16x32_bf16 v[176:179], a[132:135], v[160:163], v[176:179]\n"
      "s_cbranch_vccnz .Lsp_wz1_14\n"
      ".Lbk_wz1_14:\n"
      "ds_read2st64_b32 v[204:205], v17 offset1:1\n"
      "v_mfma_f32_16x16x32_bf16 v[176:179], a[136:139], v[164:167], v[176:179]\n"
      "ds_read2st64_b32 v[206:207], v17 offset0:2 offset1:3\n"
      "v_add_u32 v21, 1, v21\n"
      "v_mfma_f32_16x16x32_bf16 v[176:179], a[140:143], v[168:171], v[176:179]\n"
      "s_waitcnt vmcnt(10)\n"
      "ds_write_b32 v23, v21\n"
      "v_mfma_f32_16x16x32_bf16 v[180:183], a[144:147], v[100:103], v[180:183]\n"
      "buffer_load_dwordx4 v0, s[8:11], s46 offen offset:2048 sc1 lds\n"
      "v_mfma_f32_16x16x32_bf16 v[180:183], a[148:151], v[104:107], v[180:183]\n"
      "v_mfma_f32_16x16x32_bf16 v[180:183], a[152:155], v[108:111], v[180:183]\n"
      "s_add_u32 m0, s40, 20480\n"
      "v_add_f32 v218, v218, v176\n"
      "v_mfma_f32_16x16x32_bf16 v[180:183], a[156:159], v[112:115], v[180:183]\n"
      "v_fmac_f32 v219, v176, v176\n"
      "v_cvt_pk_bf16_f32 v209, v174, v175\n"
      "v_mfma_f32_16x16x32_bf16 v[180:183], a[160:163], v[116:119], v[180:183]\n"
      "buffer_load_dwordx4 v[196:199], v13, s[8:11], s47 offen offset:1024 sc1\n"
      "v_mfma_f32_16x16x32_bf16 v[180:183], a[164:167], v[120:123], v[180:183]\n"
      "s_waitcnt lgkmcnt(1)\n"
      "v_cvt_pk_bf16_f32 v211, v178, v179\n"
      "v_add_f32 v190, v190, v206\n"
      "v_mfma_f32_16x16x32_bf16 v[180:183], a[168:171], v[124:127], v[180:183]\n"
      "v_add_f32 v218, v218, v177\n"
      "v_add_f32 v218, v218, v178\n"
      "v_mfma_f32_16x16x32_bf16 v[180:183], a[172:175], v[128:131], v[180:183]\n"
      "v_add_f32 v218, v218, v179\n"
      "v_add_f32 v189, v189, v205\n"
      "v_mfma_f32_16x16x32_bf16 v[180:183], a[176:179], v[132:135], v[180:183]\n"
      "buffer_load_dwordx4 v1, s[8:11], s46 offen offset:0 sc1 lds\n"
      "v_mfma_f32_16x16x32_bf16 v[180:183], a[180:183], v[136:139], v[180:183]\n"
      "v_add_f32 v188, v188, v204\n"
      "v_fmac_f32 v219, v177, v177\n"
      "v_mfma_f32_16x16x32_bf16 v[180:183], a[184:187], v[140:143], v[180:183]\n"
      "ds_read_b32 v22, v24\n"
      "v_fmac_f32 v219, v178, v178\n"
      "v_mfma_f32_16x16x32_bf16 v[180:183], a[188:191], v[144:147], v[180:183]\n"
      "v_cvt_pk_bf16_f32 v210, v176, v177\n"
      "v_fmac_f32 v219, v179, v179\n"
      "v_mfma_f32_16x16x32_bf16 v[180:183], a[192:195], v[148:151], v[180:183]\n"
      "v_add_f32 v191, v191, v207\n"
      "v_fmac_f32 v219, v188, v188\n"
      "v_mfma_f32_16x16x32_bf16 v[180:183], a[196:199], v[152:155], v[180:183]\n"
      "v_add_f32 v218, v218, v188\n"
      "v_mfma_f32_16x16x32_bf16 v[180:183], a[200:203], v[156:159], v[180:183]\n"
      "v_add_f32 v218, v218, v189\n"
      "v_add_f32 v218, v218, v190\n"
      "v_mfma_f32_16x16x32_bf16 v[180:183], a[204:207], v[160:163], v[180:183]\n"
      "s_waitcnt lgkmcnt(0)\n"
      "v_fmac_f32 v219, v189, v189\n"
      "v_cmp_gt_i32 vcc, v21, v22\n"
      "v_mfma_f32_16x16x32_bf16 v[180:183], a[208:211], v[164:167], v[180:183]\n"
      "v_cvt_pk_bf16_f32 v216, v188, v189\n"
      "v_fmac_f32 v219, v190, v190\n"
      "v_mfma_f32_16x16x32_bf16 v[180:183], a[212:215], v[168:171], v[180:183]\n"
      "s_waitcnt vmcnt(11)\n"
      "v_and_b32 v189, 0xffff0000, v200\n"
      "v_fmac_f32 v219, v191, v191\n"
      "v_mfma_f32_16x16x32_bf16 v[184:187], a[216:219], v[100:103], v[184:187]\n"
      "s_add_u32 s38, s38, 24576\n"
      "v_cvt_pk_bf16_f32 v217, v190, v191\n"
      "v_lshlrev_b32_e64 v188, 16, v200\n"
      "v_mfma_f32_16x16x32_bf16 v[184:187], a[220:223], v[104:107], v[184:187]\n"
      "s_cbranch_vccnz .Lsp_wy0_15\n"
      ".Lbk_wy0_15:\n"
      "v_mfma_f32_16x16x32_bf16 v[184:187], a[224:227], v[108:111], v[184:187]\n"
      "ds_read_b128 v[104:107], v0 offset:1024\n"
      "v_add_f32 v218, v218, v191\n"
      "v_mfma_f32_16x16x32_bf16 v[184:187], a[228:231], v[112:115], v[184:187]\n"
      "v_fmac_f32 v219, v180, v180\n"
      "v_lshlrev_b32 v190, 16, v201\n"
      "v_mfma_f32_16x16x32_bf16 v[184:187], a[232:235], v[116:119], v[184:187]\n"
      "ds_read_b128 v[100:103], v0 offset:0\n"
      "v_add_f32 v218, v218, v180\n"
      "v_mfma_f32_16x16x32_bf16 v[184:187], a[236:239], v[120:123], v[184:187]\n"
      "s_waitcnt vmcnt(9)\n"
      "ds_read_b128 v[120:123], v0 offset:5120\n"
      "v_lshlrev_b32 v178, 16, v195\n"
      "v_mfma_f32_16x16x32_bf16 v[184:187], a[240:243], v[124:127], v[184:187]\n"
      "ds_read_b128 v[112:115], v0 offset:3072\n"
      "v_fmac_f32 v219, v181, v181\n"
      "v_mfma_f32_16x16x32_bf16 v[184:187], a[244:247], v[128:131], v[184:187]\n"
      "ds_read_b128 v[116:119], v0 offset:4096\n"
      "v_lshlrev_b32 v174, 16, v193\n"
      "v_mfma_f32_16x16x32_bf16 v[184:187], a[248:251], v[132:135], v[184:187]\n"
      "ds_read_b128 v[124:127], v0 offset:6144\n"
      "v_fmac_f32 v219, v182, v182\n"
      "v_mfma_f32_16x16x32_bf16 v[184:187], a[252:255], v[136:139], v[184:187]\n"
      "s_sub_u32 s47, s46, s45\n"
      "ds_read_b128 v[108:111], v0 offset:2048\n"
      "v_mfma_f32_16x16x32_bf16 v[184:187], v[32:35], v[140:143], v[184:187]\n"
      "v_and_b32 v191, 0xffff0000, v201\n"
      "v_and_b32 v177, 0xffff0000, v194\n"
      "v_mfma_f32_16x16x32_bf16 v[184:187], v[36:39], v[144:147], v[184:187]\n"
      "ds_read_b128 v[136:139], v0 offset:8192\n"
      "v_lshlrev_b32 v176, 16, v194\n"
      "v_mfma_f32_16x16x32_bf16 v[184:187], v[40:43], v[148:151], v[184:187]\n"
      "ds_read_b128 v[128:131], v0 offset:7168\n"
      "v_lshlrev_b32 v172, 16, v192\n"
      "v_mfma_f32_16x16x32_bf16 v[184:187], v[44:47], v[152:155], v[184:187]\n"
      "v_fmac_f32 v219, v183, v183\n"
      "v_and_b32 v175, 0xffff0000, v193\n"
      "v_mfma_f32_16x16x32_bf16 v[184:187], v[48:51], v[156:159], v[184:187]\n"
      "buffer_load_dwordx2 v[200:201], v14, s[8:11], s47 offen sc1\n"
      "v_mfma_f32_16x16x32_bf16 v[184:187], v[52:55], v[160:163], v[184:187]\n"
      "ds_read2_b32 v[132:133], v2 offset0:0 offset1:32\n"
      "v_and_b32 v173, 0xffff0000, v192\n"
      "v_mfma_f32_16x16x32_bf16 v[184:187], v[56:59], v[164:167], v[184:187]\n"
      "s_waitcnt lgkmcnt(3)\n"
      "ds_read2_b32 v[134:135], v2 offset0:64 offset1:96\n"
      "v_add_f32 v218, v218, v181\n"
      "v_mfma_f32_16x16x32_bf16 v[184:187], v[60:63], v[168:171], v[184:187]\n"
      "s_sub_u32 s20, s20, 1\n"
      "s_cbranch_scc0 .Lw_loop\n"
      ".Lw_tail:\n"
      "v_add_f32 v218, v218, v182\n"
      "s_nop 7\n"
      "v_fmac_f32 v219, v184, v184\n"
      "v_cvt_pk_bf16_f32 v213, v182, v183\n"
      "v_cvt_pk_bf16_f32 v215, v186, v187\n"
      "v_add_f32 v218, v218, v183\n"
      "buffer_store_dwordx4 v[208:211], v15, s[8:11], s38 offen sc1\n"
      "v_cvt_pk_bf16_f32 v212, v180, v181\n"
      "v_add_f32 v218, v218, v184\n"
      "s_add_u32 s50, s50, 24576\n"
      "v_cvt_pk_bf16_f32 v214, v184, v185\n"
      "v_add_f32 v218, v218, v185\n"
      "v_add_f32 v218, v218, v186\n"
      "v_add_f32 v218, v218, v187\n"
      "v_fmac_f32 v219, v185, v185\n"
      "v_fmac_f32 v219, v186, v186\n"
      "buffer_store_dwordx2 v[216:217], v16, s[8:11], s38 offen sc1\n"
      "buffer_store_dwordx4 v[212:215], v15, s[8:11], s38 offen offset:1024 sc1\n"
      "ds_write_b32 v18, v218\n"
      "v_fmac_f32 v219, v187, v187\n"
      "ds_write_b32 v18, v219 offset:4\n"
      "s_waitcnt lgkmcnt(0)\n"
      "s_barrier\n"
      "s_waitcnt vmcnt(12)\n"
      "buffer_store_dword v25, v12, s[8:11], s50 offen sc1\n"
      "s_add_u32 s38, s38, 24576\n"
      "s_add_u32 s50, s50, 24576\n"
      "s_waitcnt vmcnt(1)\n"
      "buffer_store_dword v25, v12, s[8:11], s50 offen sc1\n"
      "s_add_u32 s50, s50, 24576\n"
      "s_endpgm\n"
      ".Lv_start:\n"
      "s_mov_b64 exec, 0\n"
      "s_lshr_b32 s22, s39, 13\n"
      ".Lv_pl:\n"
      "s_cmp_eq_u32 s22, 0\n"
      "s_cbranch_scc1 .Lv_p0\n"
      "s_cmp_eq_u32 s22, 1\n"
      "s_cbranch_scc1 .Lv_p1\n"
      "s_branch .Lv_pe\n"
      ".Lv_pr:\n"
      "s_add_u32 s22, s22, 2\n"
      "s_branch .Lv_pl\n"
      ".Lv_pe:\n"
      "s_mov_b64 exec, -1\n"
      "s_add_u32 s50, s38, 4294918144\n"
      "s_add_u32 s49, s38, 4294918144\n"
      "s_add_u32 s38, s38, 4294942720\n"
      "s_cmp_eq_u32 s36, s37\n"
      "s_cselect_b32 s48, 49152, s51\n"
      "s_add_u32 s36, s36, s48\n"
      "s_min_u32 s36, s36, s6\n"
      "buffer_load_dword v28, v11, s[8:11], s36 offen sc1\n"
      "s_mov_b32 s46, s36\n"
      "s_add_u32 s36, s36, 24576\n"
      "s_min_u32 s36, s36, s6\n"
      "s_add_u32 m0, s39, 0\n"
      "s_waitcnt vmcnt(0)\n"
      "v_cmp_ne_u32 vcc, s21, v28\n"
      "s_cbranch_vccnz .Lsp_v0_16\n"
      ".Lbk_v0_16:\n"
      "buffer_load_dword v28, v11, s[8:11], s36 offen sc1\n"
      "buffer_load_dwordx4 v0, s[8:11], s46 offen offset:3072 sc1 lds\n"
      "buffer_load_dwordx4 v0, s[8:11], s46 offen offset:0 sc1 lds\n"
      "buffer_load_dwordx4 v0, s[8:11], s46 offen offset:1024 sc1 lds\n"
      "buffer_load_dwordx4 v0, s[8:11], s46 offen offset:2048 sc1 lds\n"
      "s_add_u32 m0, s40, 0\n"
      "s_nop 0\n"
      "buffer_load_dwordx4 v1, s[8:11], s46 offen offset:0 sc1 lds\n"
      "s_sub_u32 s47, s46, s45\n"
      "buffer_load_dwordx4 v[192:195], v13, s[8:11], s47 offen sc1\n"
      "s_mov_b32 s46, s36\n"
      "s_cmp_eq_u32 s36, s37\n"
      "s_cselect_b32 s48, 49152, s51\n"
      "buffer_load_dword v26, v23, s[8:11], s31 offen\n"
      "s_add_u32 s36, s36, s48\n"
      "s_waitcnt vmcnt(7)\n"
      "v_cmp_ne_u32 vcc, s21, v28\n"
      "s_min_u32 s36, s36, s6\n"
      "s_cbranch_vccnz .Lsp_v1_17\n"
      ".Lbk_v1_17:\n"
      "buffer_load_dword v28, v11, s[8:11], s36 offen sc1\n"
      "s_add_u32 m0, s39, 20480\n"
      "s_nop 0\n"
      "buffer_load_dwordx4 v0, s[8:11], s46 offen offset:0 sc1 lds\n"
      "buffer_load_dwordx4 v0, s[8:11], s46 offen offset:1024 sc1 lds\n"
      "buffer_load_dwordx4 v0, s[8:11], s46 offen offset:2048 sc1 lds\n"
      "buffer_load_dwordx4 v[196:199], v13, s[8:11], s47 offen offset:1024 sc1\n"
      "v_add_u32 v21, 1, v21\n"
      "s_waitcnt vmcnt(7)\n"
      "ds_write_b32 v23, v21\n"
      "buffer_load_dwordx4 v0, s[8:11], s46 offen offset:3072 sc1 lds\n"
      "s_add_u32 m0, s40, 20480\n"
      "s_nop 0\n"
      "buffer_load_dwordx4 v1, s[8:11], s46 offen offset:0 sc1 lds\n"
      "ds_read_b32 v22, v24\n"
      "buffer_load_dword v26, v23, s[8:11], s31 offen\n"
      "s_waitcnt lgkmcnt(0)\n"
      "v_cmp_gt_i32 vcc, v21, v22\n"
      "buffer_load_dword v26, v23, s[8:11], s31 offen\n"
      "s_cbranch_vccnz .Lsp_vy0_18\n"
      ".Lbk_vy0_18:\n"
      "s_sub_u32 s47, s46, s45\n"
      "s_waitcnt vmcnt(10)\n"
      "v_lshlrev_b32 v174, 16, v193\n"
      "v_and_b32 v179, 0xffff0000, v195\n"
      "ds_read_b128 v[100:103], v0 offset:8192\n"
      "ds_read_b128 v[108:111], v0 offset:10240\n"
      "v_and_b32 v173, 0xffff0000, v192\n"
      "ds_read_b128 v[120:123], v0 offset:13312\n"
      "ds_read_b128 v[128:131], v0 offset:15360\n"
      "v_lshlrev_b32 v178, 16, v195\n"
      "ds_read_b128 v[104:107], v0 offset:9216\n"
      "v_lshlrev_b32 v172, 16, v192\n"
      "buffer_load_dword v26, v23, s[8:11], s31 offen\n"
      "s_mov_b32 s46, s36\n"
      "ds_read_b128 v[112:115], v0 offset:11264\n"
      "ds_read2_b32 v[134:135], v2 offset0:64 offset1:96\n"
      "ds_read2_b32 v[132:133], v2 offset0:0 offset1:32\n"
      "s_add_u32 s36, s36, 24576\n"
      "ds_read_b128 v[116:119], v0 offset:12288\n"
      "ds_read_b128 v[124:127], v0 offset:14336\n"
      "ds_read_b128 v[160:163], v0 offset:6144\n"
      "v_and_b32 v177, 0xffff0000, v194\n"
      "s_mov_b32 s20, 15\n"
      "s_waitcnt lgkmcnt(0)\n"
      "s_nop 4\n"
      "s_branch .Lv_loop\n"
      ".p2align 12\n"
      ".Lv_p0:\n"
      "s_branch .Lv_pr\n"
      ".p2align 5\n"
      ".Lv_loop:\n"
      "v_mfma_f32_16x16x32_bf16 v[188:191], v[64:67], v[100:103], 0\n"
      "ds_read_b128 v[140:143], v0 offset:1024\n"
      "v_and_b32 v175, 0xffff0000, v193\n"
      "v_mfma_f32_16x16x32_bf16 v[188:191], v[68:71], v[104:107], v[188:191]\n"
      "ds_read2_b32 v[168:169], v3 offset0:0 offset1:32\n"
      "v_lshlrev_b32 v176, 16, v194\n"
      "v_mfma_f32_16x16x32_bf16 v[188:191], v[72:75], v[108:111], v[188:191]\n"
      "s_waitcnt lgkmcnt(5)\n"
      "ds_read_b128 v[164:167], v0 offset:7168\n"
      "v_add_f32 v218, v218, v184\n"
      "v_mfma_f32_16x16x32_bf16 v[188:191], v[76:79], v[112:115], v[188:191]\n"
      "s_waitcnt lgkmcnt(5)\n"
      "ds_read_b128 v[144:147], v0 offset:2048\n"
      "v_add_f32 v218, v218, v185\n"
      "v_mfma_f32_16x16x32_bf16 v[188:191], v[80:83], v[116:119], v[188:191]\n"
      "buffer_load_dwordx4 v[192:195], v13, s[8:11], s47 offen sc1\n"
      "v_mfma_f32_16x16x32_bf16 v[188:191], v[84:87], v[120:123], v[188:191]\n"
      "s_waitcnt lgkmcnt(4)\n"
      "ds_read2_b32 v[170:171], v3 offset0:64 offset1:96\n"
      "v_fmac_f32 v219, v183, v183\n"
      "v_mfma_f32_16x16x32_bf16 v[188:191], v[88:91], v[124:127], v[188:191]\n"
      "s_min_u32 s36, s36, s6\n"
      "ds_read_b128 v[136:139], v0 offset:0\n"
      "v_fmac_f32 v219, v184, v184\n"
      "v_mfma_f32_16x16x32_bf16 v[188:191], v[92:95], v[128:131], v[188:191]\n"
      "ds_read_b128 v[148:151], v0 offset:3072\n"
      "v_mfma_f32_16x16x32_bf16 v[188:191], v[96:99], v[132:135], v[188:191]\n"
      "v_cvt_pk_bf16_f32 v215, v186, v187\n"
      "v_add_f32 v218, v218, v186\n"
      "v_mfma_f32_16x16x32_bf16 v[172:175], a[0:3], v[100:103], v[172:175]\n"
      "ds_read_b128 v[152:155], v0 offset:4096\n"
      "v_add_f32 v218, v218, v187\n"
      "v_mfma_f32_16x16x32_bf16 v[172:175], a[4:7], v[104:107], v[172:175]\n"
      "ds_read_b128 v[156:159], v0 offset:5120\n"
      "v_fmac_f32 v219, v185, v185\n"
      "v_mfma_f32_16x16x32_bf16 v[172:175], a[8:11], v[108:111], v[172:175]\n"
      "s_add_u32 m0, s39, 40960\n"
      "v_cvt_pk_bf16_f32 v214, v184, v185\n"
      "v_mfma_f32_16x16x32_bf16 v[172:175], a[12:15], v[112:115], v[172:175]\n"
      "ds_write_b32 v18, v218\n"
      "v_mfma_f32_16x16x32_bf16 v[172:175], a[16:19], v[116:119], v[172:175]\n"
      "s_waitcnt vmcnt(10) lgkmcnt(4)\n"
      "v_fmac_f32_e64 v219, v186, v186\n"
      "v_cmp_ne_u32 vcc, s21, v28\n"
      "v_mfma_f32_16x16x32_bf16 v[172:175], a[20:23], v[120:123], v[172:175]\n"
      "ds_write_b32 v17, v190 offset:512\n"
      "v_mfma_f32_16x16x32_bf16 v[172:175], a[24:27], v[124:127], v[172:175]\n"
      "buffer_store_dwordx4 v[212:215], v15, s[8:11], s38 offen offset:1024 sc1\n"
      "v_mfma_f32_16x16x32_bf16 v[172:175], a[28:31], v[128:131], v[172:175]\n"
      "ds_write_b32 v17, v188 offset:0\n"
      "v_mfma_f32_16x16x32_bf16 v[172:175], a[32:35], v[132:135], v[172:175]\n"
      "v_fmac_f32_e64 v219, v187, v187\n"
      "v_mfma_f32_16x16x32_bf16 v[172:175], a[36:39], v[136:139], v[172:175]\n"
      "ds_write_b32 v17, v191 offset:768\n"
      "v_mfma_f32_16x16x32_bf16 v[172:175], a[40:43], v[140:143], v[172:175]\n"
      "s_cbranch_vccnz .Lsp_v0_19\n"
      ".Lbk_v0_19:\n"
      "v_add_u32 v21, 1, v21\n"
      "v_mfma_f32_16x16x32_bf16 v[172:175], a[44:47], v[144:147], v[172:175]\n"
      "s_waitcnt lgkmcnt(3)\n"
      "ds_write_b32 v18, v219 offset:4\n"
      "v_mfma_f32_16x16x32_bf16 v[172:175], a[48:51], v[148:151], v[172:175]\n"
      "buffer_load_dword v28, v11, s[8:11], s36 offen sc1\n"
      "v_mfma_f32_16x16x32_bf16 v[172:175], a[52:55], v[152:155], v[172:175]\n"
      "ds_write_b32 v17, v189 offset:256\n"
      "v_mfma_f32_16x16x32_bf16 v[172:175], a[56:59], v[156:159], v[172:175]\n"
      "s_waitcnt vmcnt(8) lgkmcnt(3)\n"
      "v_lshlrev_b32 v186, 16, v199\n"
      "v_and_b32 v185, 0xffff0000, v198\n"
      "v_mfma_f32_16x16x32_bf16 v[172:175], a[60:63], v[160:163], v[172:175]\n"
      "ds_write_b32 v23, v21\n"
      "v_mfma_f32_16x16x32_bf16 v[172:175], a[64:67], v[164:167], v[172:175]\n"
      "v_and_b32 v181, 0xffff0000, v196\n"
      "v_and_b32 v187, 0xffff0000, v199\n"
      "v_mfma_f32_16x16x32_bf16 v[172:175], a[68:71], v[168:171], v[172:175]\n"
      "buffer_load_dwordx4 v0, s[8:11], s46 offen offset:3072 sc1 lds\n"
      "v_mfma_f32_16x16x32_bf16 v[176:179], a[72:75], v[100:103], v[176:179]\n"
      "v_lshlrev_b32 v182, 16, v197\n"
      "s_add_u32 s38, s38, 24576\n"
      "v_mfma_f32_16x16x32_bf16 v[176:179], a[76:79], v[104:107], v[176:179]\n"
      "v_lshlrev_b32 v184, 16, v198\n"
      "v_mfma_f32_16x16x32_bf16 v[176:179], a[80:83], v[108:111], v[176:179]\n"
      "v_cvt_pk_bf16_f32 v208, v172, v173\n"
      "v_mfma_f32_16x16x32_bf16 v[176:179], a[84:87], v[112:115], v[176:179]\n"
      "v_mul_f32 v219, v172, v172\n"
      "v_mfma_f32_16x16x32_bf16 v[176:179], a[88:91], v[116:119], v[176:179]\n"
      "v_lshlrev_b32 v180, 16, v196\n"
      "v_and_b32 v183, 0xffff0000, v197\n"
      "v_mfma_f32_16x16x32_bf16 v[176:179], a[92:95], v[120:123], v[176:179]\n"
      "v_fmac_f32 v219, v173, v173\n"
      "v_mfma_f32_16x16x32_bf16 v[176:179], a[96:99], v[124:127], v[176:179]\n"
      "buffer_load_dwordx4 v[196:199], v13, s[8:11], s47 offen offset:1024 sc1\n"
      "v_mfma_f32_16x16x32_bf16 v[176:179], a[100:103], v[128:131], v[176:179]\n"
      "ds_read_b32 v22, v24\n"
      "v_mfma_f32_16x16x32_bf16 v[176:179], a[104:107], v[132:135], v[176:179]\n"
      "v_fmac_f32 v219, v174, v174\n"
      "v_mfma_f32_16x16x32_bf16 v[176:179], a[108:111], v[136:139], v[176:179]\n"
      "v_fmac_f32 v219, v175, v175\n"
      "v_mfma_f32_16x16x32_bf16 v[176:179], a[112:115], v[140:143], v[176:179]\n"
      "buffer_load_dwordx4 v0, s[8:11], s46 offen offset:0 sc1 lds\n"
      "v_mfma_f32_16x16x32_bf16 v[176:179], a[116:119], v[144:147], v[176:179]\n"
      "v_mfma_f32_16x16x32_bf16 v[176:179], a[120:123], v[148:151], v[176:179]\n"
      "s_waitcnt lgkmcnt(0)\n"
      "v_cmp_gt_i32 vcc, v21, v22\n"
      "v_add_f32_e64 v218, v172, v173\n"
      "v_mfma_f32_16x16x32_bf16 v[176:179], a[124:127], v[152:155], v[176:179]\n"
      "v_add_f32_e64 v218, v218, v174\n"
      "v_add_f32_e64 v218, v218, v175\n"
      "v_mfma_f32_16x16x32_bf16 v[176:179], a[128:131], v[156:159], v[176:179]\n"
      "buffer_load_dwordx4 v0, s[8:11], s46 offen offset:1024 sc1 lds\n"
      "v_mfma_f32_16x16x32_bf16 v[176:179], a[132:135], v[160:163], v[176:179]\n"
      "s_cbranch_vccnz .Lsp_vz0_20\n"
      ".Lbk_vz0_20:\n"
      "ds_read_b128 v[204:207], v19\n"
      "v_mfma_f32_16x16x32_bf16 v[176:179], a[136:139], v[164:167], v[176:179]\n"
      "v_cvt_pk_bf16_f32 v209, v174, v175\n"
      "v_add_u32 v21, 1, v21\n"
      "v_mfma_f32_16x16x32_bf16 v[176:179], a[140:143], v[168:171], v[176:179]\n"
      "s_waitcnt vmcnt(10)\n"
      "ds_write_b32 v23, v21\n"
      "v_mfma_f32_16x16x32_bf16 v[180:183], a[144:147], v[100:103], v[180:183]\n"
      "v_mfma_f32_16x16x32_bf16 v[180:183], a[148:151], v[104:107], v[180:183]\n"
      "buffer_load_dwordx4 v0, s[8:11], s46 offen offset:2048 sc1 lds\n"
      "v_mfma_f32_16x16x32_bf16 v[180:183], a[152:155], v[108:111], v[180:183]\n"
      "s_add_u32 s49, s49, 24576\n"
      "s_add_u32 m0, s40, 40960\n"
      "v_mfma_f32_16x16x32_bf16 v[180:183], a[156:159], v[112:115], v[180:183]\n"
      "v_mfma_f32_16x16x32_bf16 v[180:183], a[160:163], v[116:119], v[180:183]\n"
      "s_waitcnt lgkmcnt(1)\n"
      "v_add_f32 v204, v204, v206\n"
      "v_cvt_pk_bf16_f32 v210, v176, v177\n"
      "v_mfma_f32_16x16x32_bf16 v[180:183], a[164:167], v[120:123], v[180:183]\n"
      "buffer_load_dwordx4 v1, s[8:11], s46 offen offset:0 sc1 lds\n"
      "v_mfma_f32_16x16x32_bf16 v[180:183], a[168:171], v[124:127], v[180:183]\n"
      "v_add_f32 v205, v205, v207\n"
      "v_add_f32_dpp v204, v204, v204 quad_perm:[1,0,3,2] row_mask:0xf bank_mask:0xf\n"
      "v_mfma_f32_16x16x32_bf16 v[180:183], a[172:175], v[128:131], v[180:183]\n"
      "v_fmac_f32 v219, v176, v176\n"
      "v_mfma_f32_16x16x32_bf16 v[180:183], a[176:179], v[132:135], v[180:183]\n"
      "v_add_f32_dpp v204, v204, v204 quad_perm:[2,3,0,1] row_mask:0xf bank_mask:0xf\n"
      "v_cvt_pk_bf16_f32 v211, v178, v179\n"
      "v_mfma_f32_16x16x32_bf16 v[180:183], a[180:183], v[136:139], v[180:183]\n"
      "buffer_store_dwordx4 v[208:211], v15, s[8:11], s38 offen sc1\n"
      "v_mfma_f32_16x16x32_bf16 v[180:183], a[184:187], v[140:143], v[180:183]\n"
      "ds_read_b32 v22, v24\n"
      "v_add_f32_dpp v205, v205, v205 quad_perm:[1,0,3,2] row_mask:0xf bank_mask:0xf\n"
      "v_mfma_f32_16x16x32_bf16 v[180:183], a[188:191], v[144:147], v[180:183]\n"
      "s_add_u32 s50, s50, 24576\n"
      "v_add_f32_dpp v205, v205, v205 quad_perm:[2,3,0,1] row_mask:0xf bank_mask:0xf\n"
      "v_fmac_f32 v219, v177, v177\n"
      "v_mfma_f32_16x16x32_bf16 v[180:183], a[192:195], v[148:151], v[180:183]\n"
      "v_add_f32_dpp v205, v205, v205 row_half_mirror row_mask:0xf bank_mask:0xf\n"
      "v_fmac_f32 v219, v178, v178\n"
      "v_mfma_f32_16x16x32_bf16 v[180:183], a[196:199], v[152:155], v[180:183]\n"
      "v_add_f32_dpp v204, v204, v204 row_half_mirror row_mask:0xf bank_mask:0xf\n"
      "v_mfma_f32_16x16x32_bf16 v[180:183], a[200:203], v[156:159], v[180:183]\n"
      "v_mul_f32 v204, v27, v204\n"
      "v_fmaak_f32 v205, v27, v205, 0x36a7c5ac\n"
      "v_mfma_f32_16x16x32_bf16 v[180:183], a[204:207], v[160:163], v[180:183]\n"
      "s_waitcnt vmcnt(12) lgkmcnt(1)\n"
      "buffer_store_dword v25, v12, s[8:11], s50 offen sc1\n"
      "v_mfma_f32_16x16x32_bf16 v[180:183], a[208:211], v[164:167], v[180:183]\n"
      "s_waitcnt lgkmcnt(0)\n"
      "v_cmp_gt_i32 vcc, v21, v22\n"
      "v_add_f32 v218, v218, v176\n"
      "v_mfma_f32_16x16x32_bf16 v[180:183], a[212:215], v[168:171], v[180:183]\n"
      "v_add_f32 v218, v218, v177\n"
      "v_add_f32_e64 v218, v218, v178\n"
      "v_mfma_f32_16x16x32_bf16 v[184:187], a[216:219], v[100:103], v[184:187]\n"
      "buffer_store_dwordx2 v[204:205], v20, s[8:11], s49 offen sc1\n"
      "v_mfma_f32_16x16x32_bf16 v[184:187], a[220:223], v[104:107], v[184:187]\n"
      "s_sub_u32 s47, s46, s45\n"
      "s_cbranch_vccnz .Lsp_vy1_21\n"
      ".Lbk_vy1_21:\n"
      "v_mfma_f32_16x16x32_bf16 v[184:187], a[224:227], v[108:111], v[184:187]\n"
      "s_waitcnt vmcnt(11)\n"
      "ds_read_b128 v[104:107], v0 offset:29696\n"
      "v_and_b32 v177, 0xffff0000, v194\n"
      "v_mfma_f32_16x16x32_bf16 v[184:187], a[228:231], v[112:115], v[184:187]\n"
      "v_fmac_f32 v219, v179, v179\n"
      "v_mfma_f32_16x16x32_bf16 v[184:187], a[232:235], v[116:119], v[184:187]\n"
      "v_fmac_f32 v219, v180, v180\n"
      "v_add_f32 v218, v218, v179\n"
      "v_mfma_f32_16x16x32_bf16 v[184:187], a[236:239], v[120:123], v[184:187]\n"
      "v_cvt_pk_bf16_f32 v212, v180, v181\n"
      "v_and_b32 v179, 0xffff0000, v195\n"
      "v_mfma_f32_16x16x32_bf16 v[184:187], a[240:243], v[124:127], v[184:187]\n"
      "ds_read_b128 v[120:123], v0 offset:33792\n"
      "v_lshlrev_b32 v174, 16, v193\n"
      "v_mfma_f32_16x16x32_bf16 v[184:187], a[244:247], v[128:131], v[184:187]\n"
      "ds_read_b128 v[112:115], v0 offset:31744\n"
      "v_add_f32 v218, v218, v180\n"
      "v_mfma_f32_16x16x32_bf16 v[184:187], a[248:251], v[132:135], v[184:187]\n"
      "ds_read_b128 v[100:103], v0 offset:28672\n"
      "v_add_f32 v218, v218, v181\n"
      "v_mfma_f32_16x16x32_bf16 v[184:187], a[252:255], v[136:139], v[184:187]\n"
      "ds_read_b128 v[124:127], v0 offset:34816\n"
      "v_mfma_f32_16x16x32_bf16 v[184:187], v[32:35], v[140:143], v[184:187]\n"
      "ds_read_b128 v[108:111], v0 offset:30720\n"
      "v_and_b32 v175, 0xffff0000, v193\n"
      "v_mfma_f32_16x16x32_bf16 v[184:187], v[36:39], v[144:147], v[184:187]\n"
      "ds_read_b128 v[116:119], v0 offset:32768\n"
      "v_fmac_f32 v219, v181, v181\n"
      "v_mfma_f32_16x16x32_bf16 v[184:187], v[40:43], v[148:151], v[184:187]\n"
      "v_add_f32 v218, v218, v182\n"
      "v_mfma_f32_16x16x32_bf16 v[184:187], v[44:47], v[152:155], v[184:187]\n"
      "ds_read2_b32 v[132:133], v4 offset0:0 offset1:32\n"
      "v_and_b32 v173, 0xffff0000, v192\n"
      "v_mfma_f32_16x16x32_bf16 v[184:187], v[48:51], v[156:159], v[184:187]\n"
      "ds_read_b128 v[128:131], v0 offset:35840\n"
      "v_lshlrev_b32 v178, 16, v195\n"
      "v_mfma_f32_16x16x32_bf16 v[184:187], v[52:55], v[160:163], v[184:187]\n"
      "ds_read_b128 v[156:159], v0 offset:25600\n"
      "v_lshlrev_b32 v172, 16, v192\n"
      "v_mfma_f32_16x16x32_bf16 v[184:187], v[56:59], v[164:167], v[184:187]\n"
      "ds_read_b128 v[144:147], v0 offset:22528\n"
      "v_lshlrev_b32 v176, 16, v194\n"
      "v_mfma_f32_16x16x32_bf16 v[184:187], v[60:63], v[168:171], v[184:187]\n"
      "s_waitcnt lgkmcnt(5)\n"
      "buffer_load_dwordx4 v[192:195], v13, s[8:11], s47 offen sc1\n"
      "v_mfma_f32_16x16x32_bf16 v[188:191], v[64:67], v[100:103], 0\n"
      "ds_read2_b32 v[134:135], v4 offset0:64 offset1:96\n"
      "v_fmac_f32 v219, v182, v182\n"
      "v_mfma_f32_16x16x32_bf16 v[188:191], v[68:71], v[104:107], v[188:191]\n"
      "ds_read_b128 v[148:151], v0 offset:23552\n"
      "v_add_f32 v218, v218, v183\n"
      "v_mfma_f32_16x16x32_bf16 v[188:191], v[72:75], v[108:111], v[188:191]\n"
      "s_mov_b32 s46, s36\n"
      "v_cvt_pk_bf16_f32 v213, v182, v183\n"
      "v_cvt_pk_bf16_f32 v215, v186, v187\n"
      "v_mfma_f32_16x16x32_bf16 v[188:191], v[76:79], v[112:115], v[188:191]\n"
      "s_waitcnt lgkmcnt(4)\n"
      "ds_read_b128 v[160:163], v0 offset:26624\n"
      "v_fmac_f32 v219, v183, v183\n"
      "v_mfma_f32_16x16x32_bf16 v[188:191], v[80:83], v[116:119], v[188:191]\n"
      "ds_read_b128 v[140:143], v0 offset:21504\n"
      "v_fmac_f32 v219, v184, v184\n"
      "v_mfma_f32_16x16x32_bf16 v[188:191], v[84:87], v[120:123], v[188:191]\n"
      "ds_read2_b32 v[168:169], v5 offset0:0 offset1:32\n"
      "v_add_f32 v218, v218, v184\n"
      "v_mfma_f32_16x16x32_bf16 v[188:191], v[88:91], v[124:127], v[188:191]\n"
      "ds_read_b128 v[152:155], v0 offset:24576\n"
      "v_fmac_f32 v219, v185, v185\n"
      "v_mfma_f32_16x16x32_bf16 v[188:191], v[92:95], v[128:131], v[188:191]\n"
      "s_waitcnt lgkmcnt(5)\n"
      "ds_read_b128 v[136:139], v0 offset:20480\n"
      "v_add_f32 v218, v218, v185\n"
      "v_mfma_f32_16x16x32_bf16 v[188:191], v[96:99], v[132:135], v[188:191]\n"
      "s_cmp_eq_u32 s36, s37\n"
      "s_cselect_b32 s48, 49152, s51\n"
      "ds_read_b128 v[164:167], v0 offset:27648\n"
      "v_mfma_f32_16x16x32_bf16 v[172:175], a[0:3], v[100:103], v[172:175]\n"
      "ds_read2_b32 v[170:171], v5 offset0:64 offset1:96\n"
      "v_cvt_pk_bf16_f32 v214, v184, v185\n"
      "v_mfma_f32_16x16x32_bf16 v[172:175], a[4:7], v[104:107], v[172:175]\n"
      "v_fmac_f32 v219, v186, v186\n"
      "v_add_f32 v218, v218, v186\n"
      "v_mfma_f32_16x16x32_bf16 v[172:175], a[8:11], v[108:111], v[172:175]\n"
      "ds_write_b32 v17, v189 offset:256\n"
      "v_mfma_f32_16x16x32_bf16 v[172:175], a[12:15], v[112:115], v[172:175]\n"
      "v_add_f32 v218, v218, v187\n"
      "v_add_u32 v21, 1, v21\n"
      "v_mfma_f32_16x16x32_bf16 v[172:175], a[16:19], v[116:119], v[172:175]\n"
      "buffer_store_dwordx4 v[212:215], v15, s[8:11], s38 offen offset:1024 sc1\n"
      "v_mfma_f32_16x16x32_bf16 v[172:175], a[20:23], v[120:123], v[172:175]\n"
      "ds_write_b32 v17, v190 offset:512\n"
      "v_mfma_f32_16x16x32_bf16 v[172:175], a[24:27], v[124:127], v[172:175]\n"
      "v_fmac_f32 v219, v187, v187\n"
      "v_mfma_f32_16x16x32_bf16 v[172:175], a[28:31], v[128:131], v[172:175]\n"
      "s_add_u32 s36, s36, s48\n"
      "ds_write_b32 v18, v218\n"
      "v_mfma_f32_16x16x32_bf16 v[172:175], a[32:35], v[132:135], v[172:175]\n"
      "s_waitcnt vmcnt(11) lgkmcnt(3)\n"
      "v_cmp_ne_u32 vcc, s21, v28\n"
      "v_mfma_f32_16x16x32_bf16 v[172:175], a[36:39], v[136:139], v[172:175]\n"
      "ds_write_b32 v17, v188 offset:0\n"
      "v_mfma_f32_16x16x32_bf16 v[172:175], a[40:43], v[140:143], v[172:175]\n"
      "s_waitcnt vmcnt(9) lgkmcnt(3)\n"
      "s_min_u32 s36, s36, s6\n"
      "v_lshlrev_b32 v182, 16, v197\n"
      "v_mfma_f32_16x16x32_bf16 v[172:175], a[44:47], v[144:147], v[172:175]\n"
      "ds_write_b32 v17, v191 offset:768\n"
      "v_mfma_f32_16x16x32_bf16 v[172:175], a[48:51], v[148:151], v[172:175]\n"
      "s_cbranch_vccnz .Lsp_v1_22\n"
      ".Lbk_v1_22:\n"
      "v_and_b32 v181, 0xffff0000, v196\n"
      "v_mfma_f32_16x16x32_bf16 v[172:175], a[52:55], v[152:155], v[172:175]\n"
      "ds_write_b32 v18, v219 offset:4\n"
      "v_mfma_f32_16x16x32_bf16 v[172:175], a[56:59], v[156:159], v[172:175]\n"
      "buffer_load_dword v28, v11, s[8:11], s36 offen sc1\n"
      "v_mfma_f32_16x16x32_bf16 v[172:175], a[60:63], v[160:163], v[172:175]\n"
      "s_add_u32 m0, s39, 61440\n"
      "ds_write_b32 v23, v21\n"
      "v_mfma_f32_16x16x32_bf16 v[172:175], a[64:67], v[164:167], v[172:175]\n"
      "v_and_b32 v187, 0xffff0000, v199\n"
      "v_and_b32 v183, 0xffff0000, v197\n"
      "v_mfma_f32_16x16x32_bf16 v[172:175], a[68:71], v[168:171], v[172:175]\n"
      "buffer_load_dwordx4 v0, s[8:11], s46 offen offset:0 sc1 lds\n"
      "v_mfma_f32_16x16x32_bf16 v[176:179], a[72:75], v[100:103], v[176:179]\n"
      "s_add_u32 s49, s49, 24576\n"
      "v_mfma_f32_16x16x32_bf16 v[176:179], a[76:79], v[104:107], v[176:179]\n"
      "v_mfma_f32_16x16x32_bf16 v[176:179], a[80:83], v[108:111], v[176:179]\n"
      "v_cvt_pk_bf16_f32 v209, v174, v175\n"
      "v_mfma_f32_16x16x32_bf16 v[176:179], a[84:87], v[112:115], v[176:179]\n"
      "buffer_load_dwordx4 v0, s[8:11], s46 offen offset:1024 sc1 lds\n"
      "v_mfma_f32_16x16x32_bf16 v[176:179], a[88:91], v[116:119], v[176:179]\n"
      "v_add_f32 v218, v172, v173\n"
      "v_mfma_f32_16x16x32_bf16 v[176:179], a[92:95], v[120:123], v[176:179]\n"
      "v_add_f32 v218, v218, v174\n"
      "v_lshlrev_b32 v180, 16, v196\n"
      "v_mfma_f32_16x16x32_bf16 v[176:179], a[96:99], v[124:127], v[176:179]\n"
      "ds_read_b32 v22, v24\n"
      "v_lshlrev_b32 v186, 16, v199\n"
      "v_mfma_f32_16x16x32_bf16 v[176:179], a[100:103], v[128:131], v[176:179]\n"
      "buffer_load_dwordx4 v0, s[8:11], s46 offen offset:2048 sc1 lds\n"
      "v_mfma_f32_16x16x32_bf16 v[176:179], a[104:107], v[132:135], v[176:179]\n"
      "v_mul_f32 v219, v172, v172\n"
      "v_fmac_f32 v219, v173, v173\n"
      "v_mfma_f32_16x16x32_bf16 v[176:179], a[108:111], v[136:139], v[176:179]\n"
      "v_lshlrev_b32 v184, 16, v198\n"
      "v_mfma_f32_16x16x32_bf16 v[176:179], a[112:115], v[140:143], v[176:179]\n"
      "v_fmac_f32_e64 v219, v174, v174\n"
      "v_mfma_f32_16x16x32_bf16 v[176:179], a[116:119], v[144:147], v[176:179]\n"
      "s_waitcnt lgkmcnt(0)\n"
      "v_cmp_gt_i32 vcc, v21, v22\n"
      "v_add_f32_e64 v218, v218, v175\n"
      "v_mfma_f32_16x16x32_bf16 v[176:179], a[120:123], v[148:151], v[176:179]\n"
      "v_mfma_f32_16x16x32_bf16 v[176:179], a[124:127], v[152:155], v[176:179]\n"
      "v_and_b32 v185, 0xffff0000, v198\n"
      "v_cvt_pk_bf16_f32 v208, v172, v173\n"
      "v_mfma_f32_16x16x32_bf16 v[176:179], a[128:131], v[156:159], v[176:179]\n"
      "buffer_load_dwordx4 v[196:199], v13, s[8:11], s47 offen offset:1024 sc1\n"
      "v_mfma_f32_16x16x32_bf16 v[176:179], a[132:135], v[160:163], v[176:179]\n"
      "s_cbranch_vccnz .Lsp_vz1_23\n"
      ".Lbk_vz1_23:\n"
      "ds_read_b128 v[204:207], v19\n"
      "v_mfma_f32_16x16x32_bf16 v[176:179], a[136:139], v[164:167], v[176:179]\n"
      "v_add_u32 v21, 1, v21\n"
      "v_mfma_f32_16x16x32_bf16 v[176:179], a[140:143], v[168:171], v[176:179]\n"
      "s_waitcnt vmcnt(10)\n"
      "ds_write_b32 v23, v21\n"
      "v_mfma_f32_16x16x32_bf16 v[180:183], a[144:147], v[100:103], v[180:183]\n"
      "v_fmac_f32 v219, v175, v175\n"
      "v_mfma_f32_16x16x32_bf16 v[180:183], a[148:151], v[104:107], v[180:183]\n"
      "buffer_load_dwordx4 v0, s[8:11], s46 offen offset:3072 sc1 lds\n"
      "v_mfma_f32_16x16x32_bf16 v[180:183], a[152:155], v[108:111], v[180:183]\n"
      "v_cvt_pk_bf16_f32 v210, v176, v177\n"
      "v_fmac_f32 v219, v176, v176\n"
      "v_mfma_f32_16x16x32_bf16 v[180:183], a[156:159], v[112:115], v[180:183]\n"
      "s_add_u32 m0, s40, 61440\n"
      "v_fmac_f32 v219, v177, v177\n"
      "v_mfma_f32_16x16x32_bf16 v[180:183], a[160:163], v[116:119], v[180:183]\n"
      "s_waitcnt lgkmcnt(1)\n"
      "v_add_f32 v204, v204, v206\n"
      "v_add_f32 v205, v205, v207\n"
      "v_mfma_f32_16x16x32_bf16 v[180:183], a[164:167], v[120:123], v[180:183]\n"
      "v_add_f32_dpp v204, v204, v204 quad_perm:[1,0,3,2] row_mask:0xf bank_mask:0xf\n"
      "v_fmac_f32 v219, v178, v178\n"
      "v_mfma_f32_16x16x32_bf16 v[180:183], a[168:171], v[124:127], v[180:183]\n"
      "buffer_load_dwordx4 v1, s[8:11], s46 offen offset:0 sc1 lds\n"
      "v_mfma_f32_16x16x32_bf16 v[180:183], a[172:175], v[128:131], v[180:183]\n"
      "v_add_f32_dpp v205, v205, v205 quad_perm:[1,0,3,2] row_mask:0xf bank_mask:0xf\n"
      "v_add_f32_dpp v204, v204, v204 quad_perm:[2,3,0,1] row_mask:0xf bank_mask:0xf\n"
      "v_mfma_f32_16x16x32_bf16 v[180:183], a[176:179], v[132:135], v[180:183]\n"
      "v_add_f32_dpp v204, v204, v204 row_half_mirror row_mask:0xf bank_mask:0xf\n"
      "v_cvt_pk_bf16_f32 v211, v178, v179\n"
      "v_mfma_f32_16x16x32_bf16 v[180:183], a[180:183], v[136:139], v[180:183]\n"
      "s_add_u32 s38, s38, 24576\n"
      "v_mul_f32 v204, v27, v204\n"
      "v_add_f32_dpp v205, v205, v205 quad_perm:[2,3,0,1] row_mask:0xf bank_mask:0xf\n"
      "v_mfma_f32_16x16x32_bf16 v[180:183], a[184:187], v[140:143], v[180:183]\n"
      "ds_read_b32 v22, v24\n"
      "v_add_f32 v218, v218, v176\n"
      "v_mfma_f32_16x16x32_bf16 v[180:183], a[188:191], v[144:147], v[180:183]\n"
      "v_add_f32_dpp v205, v205, v205 row_half_mirror row_mask:0xf bank_mask:0xf\n"
      "v_fmaak_f32 v205, v27, v205, 0x36a7c5ac\n"
      "v_mfma_f32_16x16x32_bf16 v[180:183], a[192:195], v[148:151], v[180:183]\n"
      "buffer_store_dwordx2 v[204:205], v20, s[8:11], s49 offen sc1\n"
      "v_mfma_f32_16x16x32_bf16 v[180:183], a[196:199], v[152:155], v[180:183]\n"
      "s_add_u32 s50, s50, 24576\n"
      "v_add_f32 v218, v218, v177\n"
      "v_mfma_f32_16x16x32_bf16 v[180:183], a[200:203], v[156:159], v[180:183]\n"
      "v_add_f32 v218, v218, v178\n"
      "v_mfma_f32_16x16x32_bf16 v[180:183], a[204:207], v[160:163], v[180:183]\n"
      "s_waitcnt lgkmcnt(0)\n"
      "v_add_f32 v218, v218, v179\n"
      "v_cmp_gt_i32 vcc, v21, v22\n"
      "v_mfma_f32_16x16x32_bf16 v[180:183], a[208:211], v[164:167], v[180:183]\n"
      "s_waitcnt vmcnt(10)\n"
      "buffer_store_dword v25, v12, s[8:11], s50 offen sc1\n"
      "v_mfma_f32_16x16x32_bf16 v[180:183], a[212:215], v[168:171], v[180:183]\n"
      "v_fmac_f32 v219, v179, v179\n"
      "v_mfma_f32_16x16x32_bf16 v[184:187], a[216:219], v[100:103], v[184:187]\n"
      "s_cbranch_vccnz .Lsp_vy0_24\n"
      ".Lbk_vy0_24:\n"
      "s_sub_u32 s47, s46, s45\n"
      "v_mfma_f32_16x16x32_bf16 v[184:187], a[220:223], v[104:107], v[184:187]\n"
      "s_waitcnt vmcnt(10)\n"
      "v_lshlrev_b32 v174, 16, v193\n"
      "v_and_b32 v179, 0xffff0000, v195\n"
      "v_mfma_f32_16x16x32_bf16 v[184:187], a[224:227], v[108:111], v[184:187]\n"
      "ds_read_b128 v[100:103], v10 offset:8192\n"
      "v_cvt_pk_bf16_f32 v213, v182, v183\n"
      "v_mfma_f32_16x16x32_bf16 v[184:187], a[228:231], v[112:115], v[184:187]\n"
      "ds_read_b128 v[108:111], v10 offset:10240\n"
      "v_and_b32 v173, 0xffff0000, v192\n"
      "v_mfma_f32_16x16x32_bf16 v[184:187], a[232:235], v[116:119], v[184:187]\n"
      "v_fmac_f32 v219, v180, v180\n"
      "v_mfma_f32_16x16x32_bf16 v[184:187], a[236:239], v[120:123], v[184:187]\n"
      "ds_read_b128 v[120:123], v10 offset:13312\n"
      "v_add_f32 v218, v218, v180\n"
      "v_mfma_f32_16x16x32_bf16 v[184:187], a[240:243], v[124:127], v[184:187]\n"
      "v_add_f32 v218, v218, v181\n"
      "v_add_f32 v218, v218, v182\n"
      "v_mfma_f32_16x16x32_bf16 v[184:187], a[244:247], v[128:131], v[184:187]\n"
      "ds_read_b128 v[128:131], v10 offset:15360\n"
      "v_lshlrev_b32 v178, 16, v195\n"
      "v_mfma_f32_16x16x32_bf16 v[184:187], a[248:251], v[132:135], v[184:187]\n"
      "v_mfma_f32_16x16x32_bf16 v[184:187], a[252:255], v[136:139], v[184:187]\n"
      "ds_read_b128 v[104:107], v10 offset:9216\n"
      "v_lshlrev_b32 v172, 16, v192\n"
      "v_mfma_f32_16x16x32_bf16 v[184:187], v[32:35], v[140:143], v[184:187]\n"
      "buffer_store_dwordx4 v[208:211], v15, s[8:11], s38 offen sc1\n"
      "v_mfma_f32_16x16x32_bf16 v[184:187], v[36:39], v[144:147], v[184:187]\n"
      "s_mov_b32 s46, s36\n"
      "ds_read_b128 v[112:115], v10 offset:11264\n"
      "v_fmac_f32 v219, v181, v181\n"
      "v_mfma_f32_16x16x32_bf16 v[184:187], v[40:43], v[148:151], v[184:187]\n"
      "ds_read2_b32 v[134:135], v6 offset0:64 offset1:96\n"
      "v_cvt_pk_bf16_f32 v212, v180, v181\n"
      "v_mfma_f32_16x16x32_bf16 v[184:187], v[44:47], v[152:155], v[184:187]\n"
      "ds_read2_b32 v[132:133], v6 offset0:0 offset1:32\n"
      "s_add_u32 s36, s36, 24576\n"
      "v_mfma_f32_16x16x32_bf16 v[184:187], v[48:51], v[156:159], v[184:187]\n"
      "ds_read_b128 v[116:119], v10 offset:12288\n"
      "v_fmac_f32 v219, v182, v182\n"
      "v_mfma_f32_16x16x32_bf16 v[184:187], v[52:55], v[160:163], v[184:187]\n"
      "ds_read_b128 v[124:127], v10 offset:14336\n"
      "v_add_f32 v218, v218, v183\n"
      "v_mfma_f32_16x16x32_bf16 v[184:187], v[56:59], v[164:167], v[184:187]\n"
      "s_waitcnt lgkmcnt(5)\n"
      "ds_read_b128 v[160:163], v10 offset:6144\n"
      "v_and_b32 v177, 0xffff0000, v194\n"
      "v_mfma_f32_16x16x32_bf16 v[184:187], v[60:63], v[168:171], v[184:187]\n"
      "v_mfma_f32_16x16x32_bf16 v[188:191], v[64:67], v[100:103], 0\n"
      "ds_read_b128 v[140:143], v10 offset:1024\n"
      "v_and_b32 v175, 0xffff0000, v193\n"
      "v_mfma_f32_16x16x32_bf16 v[188:191], v[68:71], v[104:107], v[188:191]\n"
      "ds_read2_b32 v[168:169], v7 offset0:0 offset1:32\n"
      "v_lshlrev_b32 v176, 16, v194\n"
      "v_mfma_f32_16x16x32_bf16 v[188:191], v[72:75], v[108:111], v[188:191]\n"
      "s_waitcnt lgkmcnt(5)\n"
      "ds_read_b128 v[164:167], v10 offset:7168\n"
      "v_add_f32 v218, v218, v184\n"
      "v_mfma_f32_16x16x32_bf16 v[188:191], v[76:79], v[112:115], v[188:191]\n"
      "s_waitcnt lgkmcnt(5)\n"
      "ds_read_b128 v[144:147], v10 offset:2048\n"
      "v_add_f32 v218, v218, v185\n"
      "v_mfma_f32_16x16x32_bf16 v[188:191], v[80:83], v[116:119], v[188:191]\n"
      "buffer_load_dwordx4 v[192:195], v13, s[8:11], s47 offen sc1\n"
      "v_mfma_f32_16x16x32_bf16 v[188:191], v[84:87], v[120:123], v[188:191]\n"
      "s_waitcnt lgkmcnt(4)\n"
      "ds_read2_b32 v[170:171], v7 offset0:64 offset1:96\n"
      "v_fmac_f32 v219, v183, v183\n"
      "v_mfma_f32_16x16x32_bf16 v[188:191], v[88:91], v[124:127], v[188:191]\n"
      "s_min_u32 s36, s36, s6\n"
      "ds_read_b128 v[136:139], v10 offset:0\n"
      "v_fmac_f32 v219, v184, v184\n"
      "v_mfma_f32_16x16x32_bf16 v[188:191], v[92:95], v[128:131], v[188:191]\n"
      "ds_read_b128 v[148:151], v10 offset:3072\n"
      "v_mfma_f32_16x16x32_bf16 v[188:191], v[96:99], v[132:135], v[188:191]\n"
      "v_cvt_pk_bf16_f32 v215, v186, v187\n"
      "v_add_f32 v218, v218, v186\n"
      "v_mfma_f32_16x16x32_bf16 v[172:175], a[0:3], v[100:103], v[172:175]\n"
      "ds_read_b128 v[152:155], v10 offset:4096\n"
      "v_add_f32 v218, v218, v187\n"
      "v_mfma_f32_16x16x32_bf16 v[172:175], a[4:7], v[104:107], v[172:175]\n"
      "ds_read_b128 v[156:159], v10 offset:5120\n"
      "v_fmac_f32_e64 v219, v185, v185\n"
      "v_mfma_f32_16x16x32_bf16 v[172:175], a[8:11], v[108:111], v[172:175]\n"
      "s_add_u32 m0, s39, 0\n"
      "v_cvt_pk_bf16_f32 v214, v184, v185\n"
      "v_mfma_f32_16x16x32_bf16 v[172:175], a[12:15], v[112:115], v[172:175]\n"
      "ds_write_b32 v18, v218\n"
      "v_mfma_f32_16x16x32_bf16 v[172:175], a[16:19], v[116:119], v[172:175]\n"
      "s_waitcnt vmcnt(10) lgkmcnt(4)\n"
      "v_fmac_f32_e64 v219, v186, v186\n"
      "v_cmp_ne_u32 vcc, s21, v28\n"
      "v_mfma_f32_16x16x32_bf16 v[172:175], a[20:23], v[120:123], v[172:175]\n"
      "ds_write_b32 v17, v190 offset:512\n"
      "v_mfma_f32_16x16x32_bf16 v[172:175], a[24:27], v[124:127], v[172:175]\n"
      "buffer_store_dwordx4 v[212:215], v15, s[8:11], s38 offen offset:1024 sc1\n"
      "v_mfma_f32_16x16x32_bf16 v[172:175], a[28:31], v[128:131], v[172:175]\n"
      "ds_write_b32 v17, v188 offset:0\n"
      "v_mfma_f32_16x16x32_bf16 v[172:175], a[32:35], v[132:135], v[172:175]\n"
      "v_fmac_f32_e64 v219, v187, v187\n"
      "v_mfma_f32_16x16x32_bf16 v[172:175], a[36:39], v[136:139], v[172:175]\n"
      "ds_write_b32 v17, v191 offset:768\n"
      "v_mfma_f32_16x16x32_bf16 v[172:175], a[40:43], v[140:143], v[172:175]\n"
      "s_cbranch_vccnz .Lsp_v0_25\n"
      ".Lbk_v0_25:\n"
      "v_add_u32 v21, 1, v21\n"
      "v_mfma_f32_16x16x32_bf16 v[172:175], a[44:47], v[144:147], v[172:175]\n"
      "s_waitcnt lgkmcnt(3)\n"
      "ds_write_b32 v18, v219 offset:4\n"
      "v_mfma_f32_16x16x32_bf16 v[172:175], a[48:51], v[148:151], v[172:175]\n"
      "buffer_load_dword v28, v11, s[8:11], s36 offen sc1\n"
      "v_mfma_f32_16x16x32_bf16 v[172:175], a[52:55], v[152:155], v[172:175]\n"
      "ds_write_b32 v17, v189 offset:256\n"
      "v_mfma_f32_16x16x32_bf16 v[172:175], a[56:59], v[156:159], v[172:175]\n"
      "s_waitcnt vmcnt(8) lgkmcnt(3)\n"
      "v_lshlrev_b32 v186, 16, v199\n"
      "v_and_b32 v185, 0xffff0000, v198\n"
      "v_mfma_f32_16x16x32_bf16 v[172:175], a[60:63], v[160:163], v[172:175]\n"
      "ds_write_b32 v23, v21\n"
      "v_mfma_f32_16x16x32_bf16 v[172:175], a[64:67], v[164:167], v[172:175]\n"
      "v_and_b32 v181, 0xffff0000, v196\n"
      "v_and_b32 v187, 0xffff0000, v199\n"
      "v_mfma_f32_16x16x32_bf16 v[172:175], a[68:71], v[168:171], v[172:175]\n"
      "buffer_load_dwordx4 v0, s[8:11], s46 offen offset:3072 sc1 lds\n"
      "v_mfma_f32_16x16x32_bf16 v[176:179], a[72:75], v[100:103], v[176:179]\n"
      "v_lshlrev_b32 v182, 16, v197\n"
      "s_add_u32 s38, s38, 24576\n"
      "v_mfma_f32_16x16x32_bf16 v[176:179], a[76:79], v[104:107], v[176:179]\n"
      "v_lshlrev_b32 v184, 16, v198\n"
      "v_mfma_f32_16x16x32_bf16 v[176:179], a[80:83], v[108:111], v[176:179]\n"
      "v_cvt_pk_bf16_f32 v208, v172, v173\n"
      "v_mfma_f32_16x16x32_bf16 v[176:179], a[84:87], v[112:115], v[176:179]\n"
      "v_mul_f32 v219, v172, v172\n"
      "v_mfma_f32_16x16x32_bf16 v[176:179], a[88:91], v[116:119], v[176:179]\n"
      "v_lshlrev_b32 v180, 16, v196\n"
      "v_and_b32 v183, 0xffff0000, v197\n"
      "v_mfma_f32_16x16x32_bf16 v[176:179], a[92:95], v[120:123], v[176:179]\n"
      "v_fmac_f32 v219, v173, v173\n"
      "v_mfma_f32_16x16x32_bf16 v[176:179], a[96:99], v[124:127], v[176:179]\n"
      "buffer_load_dwordx4 v[196:199], v13, s[8:11], s47 offen offset:1024 sc1\n"
      "v_mfma_f32_16x16x32_bf16 v[176:179], a[100:103], v[128:131], v[176:179]\n"
      "ds_read_b32 v22, v24\n"
      "v_mfma_f32_16x16x32_bf16 v[176:179], a[104:107], v[132:135], v[176:179]\n"
      "v_fmac_f32 v219, v174, v174\n"
      "v_mfma_f32_16x16x32_bf16 v[176:179], a[108:111], v[136:139], v[176:179]\n"
      "v_fmac_f32 v219, v175, v175\n"
      "v_mfma_f32_16x16x32_bf16 v[176:179], a[112:115], v[140:143], v[176:179]\n"
      "buffer_load_dwordx4 v0, s[8:11], s46 offen offset:0 sc1 lds\n"
      "v_mfma_f32_16x16x32_bf16 v[176:179], a[116:119], v[144:147], v[176:179]\n"
      "v_mfma_f32_16x16x32_bf16 v[176:179], a[120:123], v[148:151], v[176:179]\n"
      "s_waitcnt lgkmcnt(0)\n"
      "v_cmp_gt_i32 vcc, v21, v22\n"
      "v_add_f32_e64 v218, v172, v173\n"
      "v_mfma_f32_16x16x32_bf16 v[176:179], a[124:127], v[152:155], v[176:179]\n"
      "v_add_f32_e64 v218, v218, v174\n"
      "v_add_f32_e64 v218, v218, v175\n"
      "v_mfma_f32_16x16x32_bf16 v[176:179], a[128:131], v[156:159], v[176:179]\n"
      "buffer_load_dwordx4 v0, s[8:11], s46 offen offset:1024 sc1 lds\n"
      "v_mfma_f32_16x16x32_bf16 v[176:179], a[132:135], v[160:163], v[176:179]\n"
      "s_cbranch_vccnz .Lsp_vz0_26\n"
      ".Lbk_vz0_26:\n"
      "ds_read_b128 v[204:207], v19\n"
      "v_mfma_f32_16x16x32_bf16 v[176:179], a[136:139], v[164:167], v[176:179]\n"
      "v_cvt_pk_bf16_f32 v209, v174, v175\n"
      "v_add_u32 v21, 1, v21\n"
      "v_mfma_f32_16x16x32_bf16 v[176:179], a[140:143], v[168:171], v[176:179]\n"
      "s_waitcnt vmcnt(10)\n"
      "ds_write_b32 v23, v21\n"
      "v_mfma_f32_16x16x32_bf16 v[180:183], a[144:147], v[100:103], v[180:183]\n"
      "v_mfma_f32_16x16x32_bf16 v[180:183], a[148:151], v[104:107], v[180:183]\n"
      "buffer_load_dwordx4 v0, s[8:11], s46 offen offset:2048 sc1 lds\n"
      "v_mfma_f32_16x16x32_bf16 v[180:183], a[152:155], v[108:111], v[180:183]\n"
      "s_add_u32 s49, s49, 24576\n"
      "s_add_u32 m0, s40, 0\n"
      "v_mfma_f32_16x16x32_bf16 v[180:183], a[156:159], v[112:115], v[180:183]\n"
      "v_mfma_f32_16x16x32_bf16 v[180:183], a[160:163], v[116:119], v[180:183]\n"
      "s_waitcnt lgkmcnt(1)\n"
      "v_add_f32 v204, v204, v206\n"
      "v_cvt_pk_bf16_f32 v210, v176, v177\n"
      "v_mfma_f32_16x16x32_bf16 v[180:183], a[164:167], v[120:123], v[180:183]\n"
      "buffer_load_dwordx4 v1, s[8:11], s46 offen offset:0 sc1 lds\n"
      "v_mfma_f32_16x16x32_bf16 v[180:183], a[168:171], v[124:127], v[180:183]\n"
      "v_add_f32 v205, v205, v207\n"
      "v_add_f32_dpp v204, v204, v204 quad_perm:[1,0,3,2] row_mask:0xf bank_mask:0xf\n"
      "v_mfma_f32_16x16x32_bf16 v[180:183], a[172:175], v[128:131], v[180:183]\n"
      "v_fmac_f32 v219, v176, v176\n"
      "v_mfma_f32_16x16x32_bf16 v[180:183], a[176:179], v[132:135], v[180:183]\n"
      "v_add_f32_dpp v204, v204, v204 quad_perm:[2,3,0,1] row_mask:0xf bank_mask:0xf\n"
      "v_cvt_pk_bf16_f32 v211, v178, v179\n"
      "v_mfma_f32_16x16x32_bf16 v[180:183], a[180:183], v[136:139], v[180:183]\n"
      "buffer_store_dwordx4 v[208:211], v15, s[8:11], s38 offen sc1\n"
      "v_mfma_f32_16x16x32_bf16 v[180:183], a[184:187], v[140:143], v[180:183]\n"
      "ds_read_b32 v22, v24\n"
      "v_add_f32_dpp v205, v205, v205 quad_perm:[1,0,3,2] row_mask:0xf bank_mask:0xf\n"
      "v_mfma_f32_16x16x32_bf16 v[180:183], a[188:191], v[144:147], v[180:183]\n"
      "s_add_u32 s50, s50, 24576\n"
      "v_add_f32_dpp v205, v205, v205 quad_perm:[2,3,0,1] row_mask:0xf bank_mask:0xf\n"
      "v_fmac_f32 v219, v177, v177\n"
      "v_mfma_f32_16x16x32_bf16 v[180:183], a[192:195], v[148:151], v[180:183]\n"
      "v_add_f32_dpp v205, v205, v205 row_half_mirror row_mask:0xf bank_mask:0xf\n"
      "v_fmac_f32 v219, v178, v178\n"
      "v_mfma_f32_16x16x32_bf16 v[180:183], a[196:199], v[152:155], v[180:183]\n"
      ".Lv_p1:\n"
      "s_cbranch_execz .Lv_pr\n"
      "v_add_f32_dpp v204, v204, v204 row_half_mirror row_mask:0xf bank_mask:0xf\n"
      "v_mfma_f32_16x16x32_bf16 v[180:183], a[200:203], v[156:159], v[180:183]\n"
      "v_mul_f32 v204, v27, v204\n"
      "v_fmaak_f32 v205, v27, v205, 0x36a7c5ac\n"
      "v_mfma_f32_16x16x32_bf16 v[180:183], a[204:207], v[160:163], v[180:183]\n"
      "s_waitcnt vmcnt(12) lgkmcnt(1)\n"
      "buffer_store_dword v25, v12, s[8:11], s50 offen sc1\n"
      "v_mfma_f32_16x16x32_bf16 v[180:183], a[208:211], v[164:167], v[180:183]\n"
      "s_waitcnt lgkmcnt(0)\n"
      "v_cmp_gt_i32 vcc, v21, v22\n"
      "v_add_f32 v218, v218, v176\n"
      "v_mfma_f32_16x16x32_bf16 v[180:183], a[212:215], v[168:171], v[180:183]\n"
      "v_add_f32 v218, v218, v177\n"
      "v_add_f32_e64 v218, v218, v178\n"
      "v_mfma_f32_16x16x32_bf16 v[184:187], a[216:219], v[100:103], v[184:187]\n"
      "buffer_store_dwordx2 v[204:205], v20, s[8:11], s49 offen sc1\n"
      "v_mfma_f32_16x16x32_bf16 v[184:187], a[220:223], v[104:107], v[184:187]\n"
      "s_sub_u32 s47, s46, s45\n"
      "s_cbranch_vccnz .Lsp_vy1_27\n"
      ".Lbk_vy1_27:\n"
      "v_mfma_f32_16x16x32_bf16 v[184:187], a[224:227], v[108:111], v[184:187]\n"
      "s_waitcnt vmcnt(11)\n"
      "ds_read_b128 v[104:107], v10 offset:29696\n"
      "v_and_b32 v177, 0xffff0000, v194\n"
      "v_mfma_f32_16x16x32_bf16 v[184:187], a[228:231], v[112:115], v[184:187]\n"
      "v_fmac_f32 v219, v179, v179\n"
      "v_mfma_f32_16x16x32_bf16 v[184:187], a[232:235], v[116:119], v[184:187]\n"
      "v_fmac_f32 v219, v180, v180\n"
      "v_add_f32 v218, v218, v179\n"
      "v_mfma_f32_16x16x32_bf16 v[184:187], a[236:239], v[120:123], v[184:187]\n"
      "v_cvt_pk_bf16_f32 v212, v180, v181\n"
      "v_and_b32 v179, 0xffff0000, v195\n"
      "v_mfma_f32_16x16x32_bf16 v[184:187], a[240:243], v[124:127], v[184:187]\n"
      "ds_read_b128 v[120:123], v10 offset:33792\n"
      "v_lshlrev_b32 v174, 16, v193\n"
      "v_mfma_f32_16x16x32_bf16 v[184:187], a[244:247], v[128:131], v[184:187]\n"
      "ds_read_b128 v[112:115], v10 offset:31744\n"
      "v_add_f32 v218, v218, v180\n"
      "v_mfma_f32_16x16x32_bf16 v[184:187], a[248:251], v[132:135], v[184:187]\n"
      "ds_read_b128 v[100:103], v10 offset:28672\n"
      "v_add_f32 v218, v218, v181\n"
      "v_mfma_f32_16x16x32_bf16 v[184:187], a[252:255], v[136:139], v[184:187]\n"
      "ds_read_b128 v[124:127], v10 offset:34816\n"
      "v_mfma_f32_16x16x32_bf16 v[184:187], v[32:35], v[140:143], v[184:187]\n"
      "ds_read_b128 v[108:111], v10 offset:30720\n"
      "v_and_b32 v175, 0xffff0000, v193\n"
      "v_mfma_f32_16x16x32_bf16 v[184:187], v[36:39], v[144:147], v[184:187]\n"
      "ds_read_b128 v[116:119], v10 offset:32768\n"
      "v_fmac_f32 v219, v181, v181\n"
      "v_mfma_f32_16x16x32_bf16 v[184:187], v[40:43], v[148:151], v[184:187]\n"
      "v_add_f32 v218, v218, v182\n"
      "v_mfma_f32_16x16x32_bf16 v[184:187], v[44:47], v[152:155], v[184:187]\n"
      "ds_read2_b32 v[132:133], v8 offset0:0 offset1:32\n"
      "v_and_b32 v173, 0xffff0000, v192\n"
      "v_mfma_f32_16x16x32_bf16 v[184:187], v[48:51], v[156:159], v[184:187]\n"
      "ds_read_b128 v[128:131], v10 offset:35840\n"
      "v_lshlrev_b32 v178, 16, v195\n"
      "v_mfma_f32_16x16x32_bf16 v[184:187], v[52:55], v[160:163], v[184:187]\n"
      "ds_read_b128 v[156:159], v10 offset:25600\n"
      "v_lshlrev_b32 v172, 16, v192\n"
      "v_mfma_f32_16x16x32_bf16 v[184:187], v[56:59], v[164:167], v[184:187]\n"
      "ds_read_b128 v[144:147], v10 offset:22528\n"
      "v_lshlrev_b32 v176, 16, v194\n"
      "v_mfma_f32_16x16x32_bf16 v[184:187], v[60:63], v[168:171], v[184:187]\n"
      "s_waitcnt lgkmcnt(5)\n"
      "buffer_load_dwordx4 v[192:195], v13, s[8:11], s47 offen sc1\n"
      "v_mfma_f32_16x16x32_bf16 v[188:191], v[64:67], v[100:103], 0\n"
      "ds_read2_b32 v[134:135], v8 offset0:64 offset1:96\n"
      "v_fmac_f32 v219, v182, v182\n"
      "v_mfma_f32_16x16x32_bf16 v[188:191], v[68:71], v[104:107], v[188:191]\n"
      "ds_read_b128 v[148:151], v10 offset:23552\n"
      "v_add_f32 v218, v218, v183\n"
      "v_mfma_f32_16x16x32_bf16 v[188:191], v[72:75], v[108:111], v[188:191]\n"
      "s_mov_b32 s46, s36\n"
      "v_cvt_pk_bf16_f32 v213, v182, v183\n"
      "v_cvt_pk_bf16_f32 v215, v186, v187\n"
      "v_mfma_f32_16x16x32_bf16 v[188:191], v[76:79], v[112:115], v[188:191]\n"
      "s_waitcnt lgkmcnt(4)\n"
      "ds_read_b128 v[160:163], v10 offset:26624\n"
      "v_fmac_f32 v219, v183, v183\n"
      "v_mfma_f32_16x16x32_bf16 v[188:191], v[80:83], v[116:119], v[188:191]\n"
      "ds_read_b128 v[140:143], v10 offset:21504\n"
      "v_fmac_f32 v219, v184, v184\n"
      "v_mfma_f32_16x16x32_bf16 v[188:191], v[84:87], v[120:123], v[188:191]\n"
      "ds_read2_b32 v[168:169], v9 offset0:0 offset1:32\n"
      "v_add_f32 v218, v218, v184\n"
      "v_mfma_f32_16x16x32_bf16 v[188:191], v[88:91], v[124:127], v[188:191]\n"
      "ds_read_b128 v[152:155], v10 offset:24576\n"
      "v_fmac_f32 v219, v185, v185\n"
      "v_mfma_f32_16x16x32_bf16 v[188:191], v[92:95], v[128:131], v[188:191]\n"
      "s_waitcnt lgkmcnt(5)\n"
      "ds_read_b128 v[136:139], v10 offset:20480\n"
      "v_add_f32 v218, v218, v185\n"
      "v_mfma_f32_16x16x32_bf16 v[188:191], v[96:99], v[132:135], v[188:191]\n"
      "s_cmp_eq_u32 s36, s37\n"
      "s_cselect_b32 s48, 49152, s51\n"
      "ds_read_b128 v[164:167], v10 offset:27648\n"
      "v_mfma_f32_16x16x32_bf16 v[172:175], a[0:3], v[100:103], v[172:175]\n"
      "ds_read2_b32 v[170:171], v9 offset0:64 offset1:96\n"
      "v_cvt_pk_bf16_f32 v214, v184, v185\n"
      "v_mfma_f32_16x16x32_bf16 v[172:175], a[4:7], v[104:107], v[172:175]\n"
      "v_fmac_f32 v219, v186, v186\n"
      "v_add_f32 v218, v218, v186\n"
      "v_mfma_f32_16x16x32_bf16 v[172:175], a[8:11], v[108:111], v[172:175]\n"
      "ds_write_b32 v17, v189 offset:256\n"
      "v_mfma_f32_16x16x32_bf16 v[172:175], a[12:15], v[112:115], v[172:175]\n"
      "v_add_f32 v218, v218, v187\n"
      "v_add_u32 v21, 1, v21\n"
      "v_mfma_f32_16x16x32_bf16 v[172:175], a[16:19], v[116:119], v[172:175]\n"
      "buffer_store_dwordx4 v[212:215], v15, s[8:11], s38 offen offset:1024 sc1\n"
      "v_mfma_f32_16x16x32_bf16 v[172:175], a[20:23], v[120:123], v[172:175]\n"
      "ds_write_b32 v17, v190 offset:512\n"
      "v_mfma_f32_16x16x32_bf16 v[172:175], a[24:27], v[124:127], v[172:175]\n"
      "v_fmac_f32 v219, v187, v187\n"
      "v_mfma_f32_16x16x32_bf16 v[172:175], a[28:31], v[128:131], v[172:175]\n"
      "s_add_u32 s36, s36, s48\n"
      "ds_write_b32 v18, v218\n"
      "v_mfma_f32_16x16x32_bf16 v[172:175], a[32:35], v[132:135], v[172:175]\n"
      "s_waitcnt vmcnt(11) lgkmcnt(3)\n"
      "v_cmp_ne_u32 vcc, s21, v28\n"
      "v_mfma_f32_16x16x32_bf16 v[172:175], a[36:39], v[136:139], v[172:175]\n"
      "ds_write_b32 v17, v188 offset:0\n"
      "v_mfma_f32_16x16x32_bf16 v[172:175], a[40:43], v[140:143], v[172:175]\n"
      "s_waitcnt vmcnt(9) lgkmcnt(3)\n"
      "s_min_u32 s36, s36, s6\n"
      "v_lshlrev_b32 v182, 16, v197\n"
      "v_mfma_f32_16x16x32_bf16 v[172:175], a[44:47], v[144:147], v[172:175]\n"
      "ds_write_b32 v17, v191 offset:768\n"
      "v_mfma_f32_16x16x32_bf16 v[172:175], a[48:51], v[148:151], v[172:175]\n"
      "s_cbranch_vccnz .Lsp_v1_28\n"
      ".Lbk_v1_28:\n"
      "v_and_b32 v181, 0xffff0000, v196\n"
      "v_mfma_f32_16x16x32_bf16 v[172:175], a[52:55], v[152:155], v[172:175]\n"
      "ds_write_b32 v18, v219 offset:4\n"
      "v_mfma_f32_16x16x32_bf16 v[172:175], a[56:59], v[156:159], v[172:175]\n"
      "buffer_load_dword v28, v11, s[8:11], s36 offen sc1\n"
      "v_mfma_f32_16x16x32_bf16 v[172:175], a[60:63], v[160:163], v[172:175]\n"
      "s_add_u32 m0, s39, 20480\n"
      "ds_write_b32 v23, v21\n"
      "v_mfma_f32_16x16x32_bf16 v[172:175], a[64:67], v[164:167], v[172:175]\n"
      "v_and_b32 v187, 0xffff0000, v199\n"
      "v_and_b32 v183, 0xffff0000, v197\n"
      "v_mfma_f32_16x16x32_bf16 v[172:175], a[68:71], v[168:171], v[172:175]\n"
      "buffer_load_dwordx4 v0, s[8:11], s46 offen offset:0 sc1 lds\n"
      "v_mfma_f32_16x16x32_bf16 v[176:179], a[72:75], v[100:103], v[176:179]\n"
      "s_add_u32 s49, s49, 24576\n"
      "v_mfma_f32_16x16x32_bf16 v[176:179], a[76:79], v[104:107], v[176:179]\n"
      "v_mfma_f32_16x16x32_bf16 v[176:179], a[80:83], v[108:111], v[176:179]\n"
      "v_cvt_pk_bf16_f32 v209, v174, v175\n"
      "v_mfma_f32_16x16x32_bf16 v[176:179], a[84:87], v[112:115], v[176:179]\n"
      "buffer_load_dwordx4 v0, s[8:11], s46 offen offset:1024 sc1 lds\n"
      "v_mfma_f32_16x16x32_bf16 v[176:179], a[88:91], v[116:119], v[176:179]\n"
      "v_add_f32 v218, v172, v173\n"
      "v_mfma_f32_16x16x32_bf16 v[176:179], a[92:95], v[120:123], v[176:179]\n"
      "v_add_f32 v218, v218, v174\n"
      "v_lshlrev_b32 v180, 16, v196\n"
      "v_mfma_f32_16x16x32_bf16 v[176:179], a[96:99], v[124:127], v[176:179]\n"
      "ds_read_b32 v22, v24\n"
      "v_lshlrev_b32 v186, 16, v199\n"
      "v_mfma_f32_16x16x32_bf16 v[176:179], a[100:103], v[128:131], v[176:179]\n"
      "buffer_load_dwordx4 v0, s[8:11], s46 offen offset:2048 sc1 lds\n"
      "v_mfma_f32_16x16x32_bf16 v[176:179], a[104:107], v[132:135], v[176:179]\n"
      "v_mul_f32 v219, v172, v172\n"
      "v_fmac_f32 v219, v173, v173\n"
      "v_mfma_f32_16x16x32_bf16 v[176:179], a[108:111], v[136:139], v[176:179]\n"
      "v_lshlrev_b32 v184, 16, v198\n"
      "v_mfma_f32_16x16x32_bf16 v[176:179], a[112:115], v[140:143], v[176:179]\n"
      "v_fmac_f32_e64 v219, v174, v174\n"
      "v_mfma_f32_16x16x32_bf16 v[176:179], a[116:119], v[144:147], v[176:179]\n"
      "s_waitcnt lgkmcnt(0)\n"
      "v_cmp_gt_i32 vcc, v21, v22\n"
      "v_add_f32_e64 v218, v218, v175\n"
      "v_mfma_f32_16x16x32_bf16 v[176:179], a[120:123], v[148:151], v[176:179]\n"
      "v_mfma_f32_16x16x32_bf16 v[176:179], a[124:127], v[152:155], v[176:179]\n"
      "v_and_b32 v185, 0xffff0000, v198\n"
      "v_cvt_pk_bf16_f32 v208, v172, v173\n"
      "v_mfma_f32_16x16x32_bf16 v[176:179], a[128:131], v[156:159], v[176:179]\n"
      "buffer_load_dwordx4 v[196:199], v13, s[8:11], s47 offen offset:1024 sc1\n"
      "v_mfma_f32_16x16x32_bf16 v[176:179], a[132:135], v[160:163], v[176:179]\n"
      "s_cbranch_vccnz .Lsp_vz1_29\n"
      ".Lbk_vz1_29:\n"
      "ds_read_b128 v[204:207], v19\n"
      "v_mfma_f32_16x16x32_bf16 v[176:179], a[136:139], v[164:167], v[176:179]\n"
      "v_add_u32 v21, 1, v21\n"
      "v_mfma_f32_16x16x32_bf16 v[176:179], a[140:143], v[168:171], v[176:179]\n"
      "s_waitcnt vmcnt(10)\n"
      "ds_write_b32 v23, v21\n"
      "v_mfma_f32_16x16x32_bf16 v[180:183], a[144:147], v[100:103], v[180:183]\n"
      "v_fmac_f32 v219, v175, v175\n"
      "v_mfma_f32_16x16x32_bf16 v[180:183], a[148:151], v[104:107], v[180:183]\n"
      "buffer_load_dwordx4 v0, s[8:11], s46 offen offset:3072 sc1 lds\n"
      "v_mfma_f32_16x16x32_bf16 v[180:183], a[152:155], v[108:111], v[180:183]\n"
      "v_cvt_pk_bf16_f32 v210, v176, v177\n"
      "v_fmac_f32 v219, v176, v176\n"
      "v_mfma_f32_16x16x32_bf16 v[180:183], a[156:159], v[112:115], v[180:183]\n"
      "s_add_u32 m0, s40, 20480\n"
      "v_fmac_f32 v219, v177, v177\n"
      "v_mfma_f32_16x16x32_bf16 v[180:183], a[160:163], v[116:119], v[180:183]\n"
      "s_waitcnt lgkmcnt(1)\n"
      "v_add_f32 v204, v204, v206\n"
      "v_add_f32 v205, v205, v207\n"
      "v_mfma_f32_16x16x32_bf16 v[180:183], a[164:167], v[120:123], v[180:183]\n"
      "v_add_f32_dpp v204, v204, v204 quad_perm:[1,0,3,2] row_mask:0xf bank_mask:0xf\n"
      "v_fmac_f32 v219, v178, v178\n"
      "v_mfma_f32_16x16x32_bf16 v[180:183], a[168:171], v[124:127], v[180:183]\n"
      "buffer_load_dwordx4 v1, s[8:11], s46 offen offset:0 sc1 lds\n"
      "v_mfma_f32_16x16x32_bf16 v[180:183], a[172:175], v[128:131], v[180:183]\n"
      "v_add_f32_dpp v205, v205, v205 quad_perm:[1,0,3,2] row_mask:0xf bank_mask:0xf\n"
      "v_add_f32_dpp v204, v204, v204 quad_perm:[2,3,0,1] row_mask:0xf bank_mask:0xf\n"
      "v_mfma_f32_16x16x32_bf16 v[180:183], a[176:179], v[132:135], v[180:183]\n"
      "v_add_f32_dpp v204, v204, v204 row_half_mirror row_mask:0xf bank_mask:0xf\n"
      "v_cvt_pk_bf16_f32 v211, v178, v179\n"
      "v_mfma_f32_16x16x32_bf16 v[180:183], a[180:183], v[136:139], v[180:183]\n"
      "s_add_u32 s38, s38, 24576\n"
      "v_mul_f32 v204, v27, v204\n"
      "v_add_f32_dpp v205, v205, v205 quad_perm:[2,3,0,1] row_mask:0xf bank_mask:0xf\n"
      "v_mfma_f32_16x16x32_bf16 v[180:183], a[184:187], v[140:143], v[180:183]\n"
      "ds_read_b32 v22, v24\n"
      "v_add_f32 v218, v218, v176\n"
      "v_mfma_f32_16x16x32_bf16 v[180:183], a[188:191], v[144:147], v[180:183]\n"
      "v_add_f32_dpp v205, v205, v205 row_half_mirror row_mask:0xf bank_mask:0xf\n"
      "v_fmaak_f32 v205, v27, v205, 0x36a7c5ac\n"
      "v_mfma_f32_16x16x32_bf16 v[180:183], a[192:195], v[148:151], v[180:183]\n"
      "buffer_store_dwordx2 v[204:205], v20, s[8:11], s49 offen sc1\n"
      "v_mfma_f32_16x16x32_bf16 v[180:183], a[196:199], v[152:155], v[180:183]\n"
      "s_add_u32 s50, s50, 24576\n"
      "v_add_f32 v218, v218, v177\n"
      "v_mfma_f32_16x16x32_bf16 v[180:183], a[200:203], v[156:159], v[180:183]\n"
      "v_add_f32 v218, v218, v178\n"
      "v_mfma_f32_16x16x32_bf16 v[180:183], a[204:207], v[160:163], v[180:183]\n"
      "s_waitcnt lgkmcnt(0)\n"
      "v_add_f32 v218, v218, v179\n"
      "v_cmp_gt_i32 vcc, v21, v22\n"
      "v_mfma_f32_16x16x32_bf16 v[180:183], a[208:211], v[164:167], v[180:183]\n"
      "s_waitcnt vmcnt(10)\n"
      "buffer_store_dword v25, v12, s[8:11], s50 offen sc1\n"
      "v_mfma_f32_16x16x32_bf16 v[180:183], a[212:215], v[168:171], v[180:183]\n"
      "v_fmac_f32 v219, v179, v179\n"
      "v_mfma_f32_16x16x32_bf16 v[184:187], a[216:219], v[100:103], v[184:187]\n"
      "s_cbranch_vccnz .Lsp_vy0_30\n"
      ".Lbk_vy0_30:\n"
      "s_sub_u32 s47, s46, s45\n"
      "v_mfma_f32_16x16x32_bf16 v[184:187], a[220:223], v[104:107], v[184:187]\n"
      "s_waitcnt vmcnt(10)\n"
      "v_lshlrev_b32 v174, 16, v193\n"
      "v_and_b32 v179, 0xffff0000, v195\n"
      "v_mfma_f32_16x16x32_bf16 v[184:187], a[224:227], v[108:111], v[184:187]\n"
      "ds_read_b128 v[100:103], v0 offset:8192\n"
      "v_cvt_pk_bf16_f32 v213, v182, v183\n"
      "v_mfma_f32_16x16x32_bf16 v[184:187], a[228:231], v[112:115], v[184:187]\n"
      "ds_read_b128 v[108:111], v0 offset:10240\n"
      "v_and_b32 v173, 0xffff0000, v192\n"
      "v_mfma_f32_16x16x32_bf16 v[184:187], a[232:235], v[116:119], v[184:187]\n"
      "v_fmac_f32 v219, v180, v180\n"
      "v_mfma_f32_16x16x32_bf16 v[184:187], a[236:239], v[120:123], v[184:187]\n"
      "ds_read_b128 v[120:123], v0 offset:13312\n"
      "v_add_f32 v218, v218, v180\n"
      "v_mfma_f32_16x16x32_bf16 v[184:187], a[240:243], v[124:127], v[184:187]\n"
      "v_add_f32 v218, v218, v181\n"
      "v_add_f32 v218, v218, v182\n"
      "v_mfma_f32_16x16x32_bf16 v[184:187], a[244:247], v[128:131], v[184:187]\n"
      "ds_read_b128 v[128:131], v0 offset:15360\n"
      "v_lshlrev_b32 v178, 16, v195\n"
      "v_mfma_f32_16x16x32_bf16 v[184:187], a[248:251], v[132:135], v[184:187]\n"
      "v_mfma_f32_16x16x32_bf16 v[184:187], a[252:255], v[136:139], v[184:187]\n"
      "ds_read_b128 v[104:107], v0 offset:9216\n"
      "v_lshlrev_b32 v172, 16, v192\n"
      "v_mfma_f32_16x16x32_bf16 v[184:187], v[32:35], v[140:143], v[184:187]\n"
      "buffer_store_dwordx4 v[208:211], v15, s[8:11], s38 offen sc1\n"
      "v_mfma_f32_16x16x32_bf16 v[184:187], v[36:39], v[144:147], v[184:187]\n"
      "s_mov_b32 s46, s36\n"
      "ds_read_b128 v[112:115], v0 offset:11264\n"
      "v_fmac_f32 v219, v181, v181\n"
      "v_mfma_f32_16x16x32_bf16 v[184:187], v[40:43], v[148:151], v[184:187]\n"
      "ds_read2_b32 v[134:135], v2 offset0:64 offset1:96\n"
      "v_cvt_pk_bf16_f32 v212, v180, v181\n"
      "v_mfma_f32_16x16x32_bf16 v[184:187], v[44:47], v[152:155], v[184:187]\n"
      "ds_read2_b32 v[132:133], v2 offset0:0 offset1:32\n"
      "s_add_u32 s36, s36, 24576\n"
      "v_mfma_f32_16x16x32_bf16 v[184:187], v[48:51], v[156:159], v[184:187]\n"
      "ds_read_b128 v[116:119], v0 offset:12288\n"
      "v_fmac_f32 v219, v182, v182\n"
      "v_mfma_f32_16x16x32_bf16 v[184:187], v[52:55], v[160:163], v[184:187]\n"
      "ds_read_b128 v[124:127], v0 offset:14336\n"
      "v_add_f32 v218, v218, v183\n"
      "v_mfma_f32_16x16x32_bf16 v[184:187], v[56:59], v[164:167], v[184:187]\n"
      "s_waitcnt lgkmcnt(5)\n"
      "ds_read_b128 v[160:163], v0 offset:6144\n"
      "v_and_b32 v177, 0xffff0000, v194\n"
      "v_mfma_f32_16x16x32_bf16 v[184:187], v[60:63], v[168:171], v[184:187]\n"
      "s_sub_u32 s20, s20, 1\n"
      "s_cbranch_scc0 .Lv_loop\n"
      ".Lv_tail:\n"
      "s_nop 8\n"
      "v_add_f32 v218, v218, v184\n"
      "v_add_f32 v218, v218, v185\n"
      "v_fmac_f32 v219, v183, v183\n"
      "v_fmac_f32 v219, v184, v184\n"
      "v_cvt_pk_bf16_f32 v215, v186, v187\n"
      "v_add_f32 v218, v218, v186\n"
      "v_add_f32 v218, v218, v187\n"
      "v_fmac_f32 v219, v185, v185\n"
      "v_cvt_pk_bf16_f32 v214, v184, v185\n"
      "ds_write_b32 v18, v218\n"
      "v_fmac_f32 v219, v186, v186\n"
      "buffer_store_dwordx4 v[212:215], v15, s[8:11], s38 offen offset:1024 sc1\n"
      "v_fmac_f32 v219, v187, v187\n"
      "ds_write_b32 v18, v219 offset:4\n"
      "s_waitcnt lgkmcnt(0)\n"
      "s_barrier\n"
      "s_add_u32 s38, s38, 24576\n"
      "ds_read_b128 v[204:207], v19\n"
      "s_add_u32 s49, s49, 24576\n"
      "s_waitcnt lgkmcnt(0)\n"
      "v_add_f32 v204, v204, v206\n"
      "v_add_f32 v205, v205, v207\n"
      "s_nop 0\n"
      "v_add_f32_dpp v204, v204, v204 quad_perm:[1,0,3,2] row_mask:0xf bank_mask:0xf\n"
      "s_nop 1\n"
      "v_add_f32_dpp v204, v204, v204 quad_perm:[2,3,0,1] row_mask:0xf bank_mask:0xf\n"
      "v_add_f32_dpp v205, v205, v205 quad_perm:[1,0,3,2] row_mask:0xf bank_mask:0xf\n"
      "s_add_u32 s50, s50, 24576\n"
      "s_nop 0\n"
      "v_add_f32_dpp v205, v205, v205 quad_perm:[2,3,0,1] row_mask:0xf bank_mask:0xf\n"
      "s_nop 1\n"
      "v_add_f32_dpp v205, v205, v205 row_half_mirror row_mask:0xf bank_mask:0xf\n"
      "v_add_f32_dpp v204, v204, v204 row_half_mirror row_mask:0xf bank_mask:0xf\n"
      "v_mul_f32 v204, v27, v204\n"
      "v_fmaak_f32 v205, v27, v205, 0x36a7c5ac\n"
      "s_waitcnt vmcnt(3)\n"
      "buffer_store_dword v25, v12, s[8:11], s50 offen sc1\n"
      "buffer_store_dwordx2 v[204:205], v20, s[8:11], s49 offen sc1\n"
      "s_add_u32 s49, s49, 24576\n"
      "s_add_u32 s50, s50, 24576\n"
      "s_waitcnt vmcnt(0)\n"
      "buffer_store_dword v25, v12, s[8:11], s50 offen sc1\n"
      "s_add_u32 s50, s50, 24576\n"
      "s_endpgm\n"
      ".Lsp_w0_1:\n"
      "s_mov_b32 s22, s23\n"
      ".Lsl_w0_1:\n"
      "buffer_load_dword v28, v11, s[8:11], s46 offen sc1\n"
      "s_waitcnt vmcnt(0)\n"
      "v_cmp_ne_u32 vcc, s21, v28\n"
      "s_cbranch_vccz .Lbk_w0_1\n"
      "s_sleep 1\n"
      "s_sub_u32 s22, s22, 1\n"
      "s_cbranch_scc0 .Lsl_w0_1\n"
      "s_mov_b32 s23, 0\n"
      "s_branch .Lbk_w0_1\n"
      ".Lsp_w1_2:\n"
      "s_mov_b32 s22, s23\n"
      ".Lsl_w1_2:\n"
      "buffer_load_dword v28, v11, s[8:11], s46 offen sc1\n"
      "s_waitcnt vmcnt(0)\n"
      "v_cmp_ne_u32 vcc, s21, v28\n"
      "s_cbranch_vccz .Lbk_w1_2\n"
      "s_sleep 1\n"
      "s_sub_u32 s22, s22, 1\n"
      "s_cbranch_scc0 .Lsl_w1_2\n"
      "s_mov_b32 s23, 0\n"
      "s_branch .Lbk_w1_2\n"
      ".Lsp_wy0_3:\n"
      "s_lshl_b32 s22, s23, 4\n"
      ".Lsl_wy0_3:\n"
      "ds_read_b32 v22, v24\n"
      "s_waitcnt lgkmcnt(0)\n"
      "v_cmp_gt_i32 vcc, v21, v22\n"
      "s_cbranch_vccz .Lbk_wy0_3\n"
      "s_sub_u32 s22, s22, 1\n"
      "s_cbranch_scc0 .Lsl_wy0_3\n"
      "s_mov_b32 s23, 0\n"
      "s_branch .Lbk_wy0_3\n"
      ".Lsp_w0_4:\n"
      "s_mov_b32 s22, s23\n"
      ".Lsl_w0_4:\n"
      "buffer_load_dword v28, v11, s[8:11], s46 offen sc1\n"
      "s_waitcnt vmcnt(0)\n"
      "v_cmp_ne_u32 vcc, s21, v28\n"
      "s_cbranch_vccz .Lbk_w0_4\n"
      "s_sleep 1\n"
      "s_sub_u32 s22, s22, 1\n"
      "s_cbranch_scc0 .Lsl_w0_4\n"
      "s_mov_b32 s23, 0\n"
      "s_branch .Lbk_w0_4\n"
      ".Lsp_wz0_5:\n"
      "s_lshl_b32 s22, s23, 4\n"
      ".Lsl_wz0_5:\n"
      "ds_read_b32 v22, v24\n"
      "s_waitcnt lgkmcnt(0)\n"
      "v_cmp_gt_i32 vcc, v21, v22\n"
      "s_cbranch_vccz .Lbk_wz0_5\n"
      "s_sub_u32 s22, s22, 1\n"
      "s_cbranch_scc0 .Lsl_wz0_5\n"
      "s_mov_b32 s23, 0\n"
      "s_branch .Lbk_wz0_5\n"
      ".Lsp_wy1_6:\n"
      "s_lshl_b32 s22, s23, 4\n"
      ".Lsl_wy1_6:\n"
      "ds_read_b32 v22, v24\n"
      "s_waitcnt lgkmcnt(0)\n"
      "v_cmp_gt_i32 vcc, v21, v22\n"
      "s_cbranch_vccz .Lbk_wy1_6\n"
      "s_sub_u32 s22, s22, 1\n"
      "s_cbranch_scc0 .Lsl_wy1_6\n"
      "s_mov_b32 s23, 0\n"
      "s_branch .Lbk_wy1_6\n"
      ".Lsp_w1_7:\n"
      "s_mov_b32 s22, s23\n"
      ".Lsl_w1_7:\n"
      "buffer_load_dword v28, v11, s[8:11], s46 offen sc1\n"
      "s_waitcnt vmcnt(0)\n"
      "v_cmp_ne_u32 vcc, s21, v28\n"
      "s_cbranch_vccz .Lbk_w1_7\n"
      "s_sleep 1\n"
      "s_sub_u32 s22, s22, 1\n"
      "s_cbranch_scc0 .Lsl_w1_7\n"
      "s_mov_b32 s23, 0\n"
      "s_branch .Lbk_w1_7\n"
      ".Lsp_wz1_8:\n"
      "s_lshl_b32 s22, s23, 4\n"
      ".Lsl_wz1_8:\n"
      "ds_read_b32 v22, v24\n"
      "s_waitcnt lgkmcnt(0)\n"
      "v_cmp_gt_i32 vcc, v21, v22\n"
      "s_cbranch_vccz .Lbk_wz1_8\n"
      "s_sub_u32 s22, s22, 1\n"
      "s_cbranch_scc0 .Lsl_wz1_8\n"
      "s_mov_b32 s23, 0\n"
      "s_branch .Lbk_wz1_8\n"
      ".Lsp_wy0_9:\n"
      "s_lshl_b32 s22, s23, 4\n"
      ".Lsl_wy0_9:\n"
      "ds_read_b32 v22, v24\n"
      "s_waitcnt lgkmcnt(0)\n"
      "v_cmp_gt_i32 vcc, v21, v22\n"
      "s_cbranch_vccz .Lbk_wy0_9\n"
      "s_sub_u32 s22, s22, 1\n"
      "s_cbranch_scc0 .Lsl_wy0_9\n"
      "s_mov_b32 s23, 0\n"
      "s_branch .Lbk_wy0_9\n"
      ".Lsp_w0_10:\n"
      "s_mov_b32 s22, s23\n"
      ".Lsl_w0_10:\n"
      "buffer_load_dword v28, v11, s[8:11], s46 offen sc1\n"
      "s_waitcnt vmcnt(0)\n"
      "v_cmp_ne_u32 vcc, s21, v28\n"
      "s_cbranch_vccz .Lbk_w0_10\n"
      "s_sleep 1\n"
      "s_sub_u32 s22, s22, 1\n"
      "s_cbranch_scc0 .Lsl_w0_10\n"
      "s_mov_b32 s23, 0\n"
      "s_branch .Lbk_w0_10\n"
      ".Lsp_wz0_11:\n"
      "s_lshl_b32 s22, s23, 4\n"
      ".Lsl_wz0_11:\n"
      "ds_read_b32 v22, v24\n"
      "s_waitcnt lgkmcnt(0)\n"
      "v_cmp_gt_i32 vcc, v21, v22\n"
      "s_cbranch_vccz .Lbk_wz0_11\n"
      "s_sub_u32 s22, s22, 1\n"
      "s_cbranch_scc0 .Lsl_wz0_11\n"
      "s_mov_b32 s23, 0\n"
      "s_branch .Lbk_wz0_11\n"
      ".Lsp_wy1_12:\n"
      "s_lshl_b32 s22, s23, 4\n"
      ".Lsl_wy1_12:\n"
      "ds_read_b32 v22, v24\n"
      "s_waitcnt lgkmcnt(0)\n"
      "v_cmp_gt_i32 vcc, v21, v22\n"
      "s_cbranch_vccz .Lbk_wy1_12\n"
      "s_sub_u32 s22, s22, 1\n"
      "s_cbranch_scc0 .Lsl_wy1_12\n"
      "s_mov_b32 s23, 0\n"
      "s_branch .Lbk_wy1_12\n"
      ".Lsp_w1_13:\n"
      "s_mov_b32 s22, s23\n"
      ".Lsl_w1_13:\n"
      "buffer_load_dword v28, v11, s[8:11], s46 offen sc1\n"
      "s_waitcnt vmcnt(0)\n"
      "v_cmp_ne_u32 vcc, s21, v28\n"
      "s_cbranch_vccz .Lbk_w1_13\n"
      "s_sleep 1\n"
      "s_sub_u32 s22, s22, 1\n"
      "s_cbranch_scc0 .Lsl_w1_13\n"
      "s_mov_b32 s23, 0\n"
      "s_branch .Lbk_w1_13\n"
      ".Lsp_wz1_14:\n"
      "s_lshl_b32 s22, s23, 4\n"
      ".Lsl_wz1_14:\n"
      "ds_read_b32 v22, v24\n"
      "s_waitcnt lgkmcnt(0)\n"
      "v_cmp_gt_i32 vcc, v21, v22\n"
      "s_cbranch_vccz .Lbk_wz1_14\n"
      "s_sub_u32 s22, s22, 1\n"
      "s_cbranch_scc0 .Lsl_wz1_14\n"
      "s_mov_b32 s23, 0\n"
      "s_branch .Lbk_wz1_14\n"
      ".Lsp_wy0_15:\n"
      "s_lshl_b32 s22, s23, 4\n"
      ".Lsl_wy0_15:\n"
      "ds_read_b32 v22, v24\n"
      "s_waitcnt lgkmcnt(0)\n"
      "v_cmp_gt_i32 vcc, v21, v22\n"
      "s_cbranch_vccz .Lbk_wy0_15\n"
      "s_sub_u32 s22, s22, 1\n"
      "s_cbranch_scc0 .Lsl_wy0_15\n"
      "s_mov_b32 s23, 0\n"
      "s_branch .Lbk_wy0_15\n"
      ".Lsp_v0_16:\n"
      "s_mov_b32 s22, s23\n"
      ".Lsl_v0_16:\n"
      "buffer_load_dword v28, v11, s[8:11], s46 offen sc1\n"
      "s_waitcnt vmcnt(0)\n"
      "v_cmp_ne_u32 vcc, s21, v28\n"
      "s_cbranch_vccz .Lbk_v0_16\n"
      "s_sleep 1\n"
      "s_sub_u32 s22, s22, 1\n"
      "s_cbranch_scc0 .Lsl_v0_16\n"
      "s_mov_b32 s23, 0\n"
      "s_branch .Lbk_v0_16\n"
      ".Lsp_v1_17:\n"
      "s_mov_b32 s22, s23\n"
      ".Lsl_v1_17:\n"
      "buffer_load_dword v28, v11, s[8:11], s46 offen sc1\n"
      "s_waitcnt vmcnt(0)\n"
      "v_cmp_ne_u32 vcc, s21, v28\n"
      "s_cbranch_vccz .Lbk_v1_17\n"
      "s_sleep 1\n"
      "s_sub_u32 s22, s22, 1\n"
      "s_cbranch_scc0 .Lsl_v1_17\n"
      "s_mov_b32 s23, 0\n"
      "s_branch .Lbk_v1_17\n"
      ".Lsp_vy0_18:\n"
      "s_lshl_b32 s22, s23, 4\n"
      ".Lsl_vy0_18:\n"
      "ds_read_b32 v22, v24\n"
      "s_waitcnt lgkmcnt(0)\n"
      "v_cmp_gt_i32 vcc, v21, v22\n"
      "s_cbranch_vccz .Lbk_vy0_18\n"
      "s_sub_u32 s22, s22, 1\n"
      "s_cbranch_scc0 .Lsl_vy0_18\n"
      "s_mov_b32 s23, 0\n"
      "s_branch .Lbk_vy0_18\n"
      ".Lsp_v0_19:\n"
      "s_mov_b32 s22, s23\n"
      ".Lsl_v0_19:\n"
      "buffer_load_dword v28, v11, s[8:11], s46 offen sc1\n"
      "s_waitcnt vmcnt(0)\n"
      "v_cmp_ne_u32 vcc, s21, v28\n"
      "s_cbranch_vccz .Lbk_v0_19\n"
      "s_sleep 1\n"
      "s_sub_u32 s22, s22, 1\n"
      "s_cbranch_scc0 .Lsl_v0_19\n"
      "s_mov_b32 s23, 0\n"
      "s_branch .Lbk_v0_19\n"
      ".Lsp_vz0_20:\n"
      "s_lshl_b32 s22, s23, 4\n"
      ".Lsl_vz0_20:\n"
      "ds_read_b32 v22, v24\n"
      "s_waitcnt lgkmcnt(0)\n"
      "v_cmp_gt_i32 vcc, v21, v22\n"
      "s_cbranch_vccz .Lbk_vz0_20\n"
      "s_sub_u32 s22, s22, 1\n"
      "s_cbranch_scc0 .Lsl_vz0_20\n"
      "s_mov_b32 s23, 0\n"
      "s_branch .Lbk_vz0_20\n"
      ".Lsp_vy1_21:\n"
      "s_lshl_b32 s22, s23, 4\n"
      ".Lsl_vy1_21:\n"
      "ds_read_b32 v22, v24\n"
      "s_waitcnt lgkmcnt(0)\n"
      "v_cmp_gt_i32 vcc, v21, v22\n"
      "s_cbranch_vccz .Lbk_vy1_21\n"
      "s_sub_u32 s22, s22, 1\n"
      "s_cbranch_scc0 .Lsl_vy1_21\n"
      "s_mov_b32 s23, 0\n"
      "s_branch .Lbk_vy1_21\n"
      ".Lsp_v1_22:\n"
      "s_mov_b32 s22, s23\n"
      ".Lsl_v1_22:\n"
      "buffer_load_dword v28, v11, s[8:11], s46 offen sc1\n"
      "s_waitcnt vmcnt(0)\n"
      "v_cmp_ne_u32 vcc, s21, v28\n"
      "s_cbranch_vccz .Lbk_v1_22\n"
      "s_sleep 1\n"
      "s_sub_u32 s22, s22, 1\n"
      "s_cbranch_scc0 .Lsl_v1_22\n"
      "s_mov_b32 s23, 0\n"
      "s_branch .Lbk_v1_22\n"
      ".Lsp_vz1_23:\n"
      "s_lshl_b32 s22, s23, 4\n"
      ".Lsl_vz1_23:\n"
      "ds_read_b32 v22, v24\n"
      "s_waitcnt lgkmcnt(0)\n"
      "v_cmp_gt_i32 vcc, v21, v22\n"
      "s_cbranch_vccz .Lbk_vz1_23\n"
      "s_sub_u32 s22, s22, 1\n"
      "s_cbranch_scc0 .Lsl_vz1_23\n"
      "s_mov_b32 s23, 0\n"
      "s_branch .Lbk_vz1_23\n"
      ".Lsp_vy0_24:\n"
      "s_lshl_b32 s22, s23, 4\n"
      ".Lsl_vy0_24:\n"
      "ds_read_b32 v22, v24\n"
      "s_waitcnt lgkmcnt(0)\n"
      "v_cmp_gt_i32 vcc, v21, v22\n"
      "s_cbranch_vccz .Lbk_vy0_24\n"
      "s_sub_u32 s22, s22, 1\n"
      "s_cbranch_scc0 .Lsl_vy0_24\n"
      "s_mov_b32 s23, 0\n"
      "s_branch .Lbk_vy0_24\n"
      ".Lsp_v0_25:\n"
      "s_mov_b32 s22, s23\n"
      ".Lsl_v0_25:\n"
      "buffer_load_dword v28, v11, s[8:11], s46 offen sc1\n"
      "s_waitcnt vmcnt(0)\n"
      "v_cmp_ne_u32 vcc, s21, v28\n"
      "s_cbranch_vccz .Lbk_v0_25\n"
      "s_sleep 1\n"
      "s_sub_u32 s22, s22, 1\n"
      "s_cbranch_scc0 .Lsl_v0_25\n"
      "s_mov_b32 s23, 0\n"
      "s_branch .Lbk_v0_25\n"
      ".Lsp_vz0_26:\n"
      "s_lshl_b32 s22, s23, 4\n"
      ".Lsl_vz0_26:\n"
      "ds_read_b32 v22, v24\n"
      "s_waitcnt lgkmcnt(0)\n"
      "v_cmp_gt_i32 vcc, v21, v22\n"
      "s_cbranch_vccz .Lbk_vz0_26\n"
      "s_sub_u32 s22, s22, 1\n"
      "s_cbranch_scc0 .Lsl_vz0_26\n"
      "s_mov_b32 s23, 0\n"
      "s_branch .Lbk_vz0_26\n"
      ".Lsp_vy1_27:\n"
      "s_lshl_b32 s22, s23, 4\n"
      ".Lsl_vy1_27:\n"
      "ds_read_b32 v22, v24\n"
      "s_waitcnt lgkmcnt(0)\n"
      "v_cmp_gt_i32 vcc, v21, v22\n"
      "s_cbranch_vccz .Lbk_vy1_27\n"
      "s_sub_u32 s22, s22, 1\n"
      "s_cbranch_scc0 .Lsl_vy1_27\n"
      "s_mov_b32 s23, 0\n"
      "s_branch .Lbk_vy1_27\n"
      ".Lsp_v1_28:\n"
      "s_mov_b32 s22, s23\n"
      ".Lsl_v1_28:\n"
      "buffer_load_dword v28, v11, s[8:11], s46 offen sc1\n"
      "s_waitcnt vmcnt(0)\n"
      "v_cmp_ne_u32 vcc, s21, v28\n"
      "s_cbranch_vccz .Lbk_v1_28\n"
      "s_sleep 1\n"
      "s_sub_u32 s22, s22, 1\n"
      "s_cbranch_scc0 .Lsl_v1_28\n"
      "s_mov_b32 s23, 0\n"
      "s_branch .Lbk_v1_28\n"
      ".Lsp_vz1_29:\n"
      "s_lshl_b32 s22, s23, 4\n"
      ".Lsl_vz1_29:\n"
      "ds_read_b32 v22, v24\n"
      "s_waitcnt lgkmcnt(0)\n"
      "v_cmp_gt_i32 vcc, v21, v22\n"
      "s_cbranch_vccz .Lbk_vz1_29\n"
      "s_sub_u32 s22, s22, 1\n"
      "s_cbranch_scc0 .Lsl_vz1_29\n"
      "s_mov_b32 s23, 0\n"
      "s_branch .Lbk_vz1_29\n"
      ".Lsp_vy0_30:\n"
      "s_lshl_b32 s22, s23, 4\n"
      ".Lsl_vy0_30:\n"
      "ds_read_b32 v22, v24\n"
      "s_waitcnt lgkmcnt(0)\n"
      "v_cmp_gt_i32 vcc, v21, v22\n"
      "s_cbranch_vccz .Lbk_vy0_30\n"
      "s_sub_u32 s22, s22, 1\n"
      "s_cbranch_scc0 .Lsl_vy0_30\n"
      "s_mov_b32 s23, 0\n"
      "s_branch .Lbk_vy0_30\n"
      ".Lw_last:\n"
      :
      : "{s[8:11]}"(rsWS), "{s[12:15]}"(rsA), "{v0}"(v_l16), "{v1}"(v_l5), "{v2}"(v_ex[0]), "{v3}"(v_ex[1]), "{v4}"(v_ex[2]), "{v5}"(v_ex[3]), "{v6}"(v_ex[4]), "{v7}"(v_ex[5]), "{v8}"(v_ex[6]), "{v9}"(v_ex[7]), "{v10}"(v_l2), "{v11}"(v_fa), "{v12}"(v_ra), "{v13}"(v_rp), "{v14}"(v_rs), "{v15}"(v_xp), "{v16}"(v_xs), "{v17}"(v_ew), "{v18}"(v_sw), "{v19}"(v_sr), "{v20}"(v_ss), "{v22}"(v_al), "{v23}"(v_yw), "{v24}"(v_yr), "{v26}"(v_hi), "{v100}"(v_vo[0]), "{v101}"(v_vo[1]), "{v102}"(v_vo[2]), "{v103}"(v_vo[3]), "{v104}"(v_vo[4]), "{v105}"(v_vo[5]), "{v106}"(v_vo[6]), "{v107}"(v_vo[7]), "{v108}"(v_vo[8]), "{v109}"(v_vp[0]), "{v110}"(v_vp[1]), "{v111}"(v_vp[2]), "{v112}"(v_vp[3]), "{v113}"(v_vp[4]), "{v114}"(v_vp[5]), "{v115}"(v_vp[6]), "{v116}"(v_vp[7]), "{v117}"(v_vp[8]), "{v118}"(v_t0), "{v119}"(v_t1), "{v120}"(v_ts), "{v121}"(v_pw), "{s36}"(s_sx), "{s37}"(s_ev), "{s38}"(s_sy), "{s39}"(s_m4), "{s40}"(s_m5), "{s41}"(s_m9), "{s42}"(s_so), "{s43}"(s_s8), "{s44}"(s_wg), "{s45}"(s_rb)
      : "v255", "a255", "s99", "vcc", "scc", "memory");

  } else if (fast && slot < 28) {
    const unsigned td = (slot - 10) / 3, gr = td < 4 ? td : (e + 5 - td) & 3, part = (td < 4 ? e : td) * 3 + (slot - 10) % 3;
    COMMON(40, 149760);
    const unsigned ks = part / 3, n3 = part % 3;
    const u32x4 rsA = srd(Wd, 4608 * 576 * 2), rsB = srd(out, 2048 * 576 * 2), rsC = srd(X, 2048 * 576 * 2);
    const unsigned v_l16b = v_l16 + 4096, v_l8 = 1024 + lane * 8, f0 = 24 * ks + 6 * w;
    const unsigned v_fa = 147456 - f0 * 1024 + (f0 + (l8 < 5 ? l8 : 5)) * 128 + l7 / 4, v_ga = (l8 < 4 ? l8 : 4) * 4 * PW + 1536 + l7 / 4;
    const unsigned v_ra = up * 6 * PREC + l5 + 1536, v_rf = up * 33 * REC + l5 + 20480 + (4 * n3 + w) * 128;
    const unsigned v_pw = l8 < 5 ? (l8 + 7) * PREC + l8 * 4 * PW + 1536 + l7 : OOB, v_pu = ks && l8 + ks < 5 ? OOB : v_pw - 6 * PREC;
    const unsigned pf = 3 * (w >> 1) + 2 * (w & 1), sf = 3 * (w >> 1) + 1;
    const unsigned v_at = (8 * g + q) * 384 + 64 * pf + 16 * p, v_as = (8 * g + q) * 384 + 64 * sf + 32 * (w & 1) + 8 * p;
    auto vo = [&](unsigned i) { const unsigned c = 64 * i + lane; return (8 * w + c / 24) * 1152 + 384 * n3 + c % 24 * 16 - 1024 * i; };
    const unsigned v_vo0 = vo(0), v_vo1 = vo(1), v_vo2 = vo(2);
    const unsigned v_xp = (6 * n3 + pf) * 1024 + v_l16, v_xs = (6 * n3 + sf) * 1024 + (w & 1) * 512 + half;
    const unsigned v_op = i16 * 1152 + 384 * n3 + 64 * pf + 16 * g, v_os = i16 * 1152 + 384 * n3 + 64 * sf + 32 * (w & 1) + 8 * g;
    const unsigned v_sw = 147456 + i16 * 136 + (4 * w + g) * 8, v_sr = 147456 + (4 * w + g) * 136 + i16 * 8;
    const unsigned v_ss = 18432 + n3 * 512 + w * 128 + g * 32 + p * 8, v_s1 = n3 < 2 ? v_ss : OOB;
    const unsigned s_ph = ks ? 6 - ks : 0, s_p0 = 0u - s_ph, r0 = gr * NREC + s_p0;
    const unsigned s_sh = WS_H + r0 * HREC + f0 * 1024, s_ps = WS_PS + r0 * PREC + (n3 * 20 + w) * PW;
    const unsigned s_x1 = WS_X1 + r0 * REC, s_xr = WS_XIN + (r0 + 33) * REC, s_or = (32 * gr + 63 - s_p0) * 18432;
    const unsigned s_k0 = ROT * 6 * gr;
    const unsigned s_m3 = w * 3072, s_m6 = w * 6144, s_so = ks * 768 * 1152;
    const unsigned s_i0 = (32 * gr + 31 - ks) * 18432, s_i5 = ks < 2 ? s_i0 - 30 * 18432 : OOB;
    const unsigned s_j0 = WS_XIN + (gr * NREC + ks) * REC, s_j5 = ks < 2 ? s_j0 + 30 * REC : OOB, s_he = s_sh + (63 + s_ph) * HREC;
    auto pe = [&](unsigned i) { return ks == 3 - i ? OOB : s_ps + (63 - i + s_ph) * PREC + ((s_ph + 8 - i) % 6) * 4 * PW; };
    const unsigned s_pe[3] = {pe(0), pe(1), pe(2)};
    asm volatile(
      "s_buffer_load_dword s21, s[8:11], 0\n"
      "s_mov_b32 s23, 65536\n"
      "s_mov_b32 s31, 0x40000000\n"
      "buffer_load_dwordx4 v[56:59], v8, s[92:95], s46 offen\n"
      "buffer_load_dwordx2 v[60:61], v9, s[92:95], s46 offen\n"
      "s_waitcnt lgkmcnt(0)\n"
      "s_mul_i32 s21, s21, 1664525\n"
      "s_add_u32 s21, s21, 1013904223\n"
      "v_mov_b32 v18, s21\n"
      "v_mov_b32 v21, v18\n"
      "v_not_b32 v224, v18\n"
      "v_not_b32 v225, v18\n"
      "v_not_b32 v226, v18\n"
      "v_not_b32 v227, v18\n"
      "v_xor_b32 v18, v18, v19\n"
      "v_mov_b32 v216, -1\n"
      "v_mov_b32 v217, -1\n"
      "v_mov_b32 v218, -1\n"
      "v_mov_b32 v219, -1\n"
      "s_add_u32 s61, s48, 15728640\n"
      "buffer_store_dwordx4 v[216:219], v6, s[8:11], s61 offen sc1\n"
      "buffer_store_dwordx2 v[216:217], v7, s[8:11], s61 offen sc1\n"
      "buffer_store_dwordx2 v[216:217], v182, s[8:11], s61 offen sc1\n"
      "s_waitcnt vmcnt(3)\n"
      "buffer_store_dwordx4 v[56:59], v6, s[8:11], s48 offen sc1\n"
      "buffer_store_dwordx2 v[60:61], v7, s[8:11], s48 offen sc1\n"
      "v_lshlrev_b32 v164, 16, v56\n"
      "v_and_b32 v165, 0xffff0000, v56\n"
      "v_lshlrev_b32 v166, 16, v57\n"
      "v_and_b32 v167, 0xffff0000, v57\n"
      "v_lshlrev_b32 v168, 16, v58\n"
      "v_and_b32 v169, 0xffff0000, v58\n"
      "v_lshlrev_b32 v170, 16, v59\n"
      "v_and_b32 v171, 0xffff0000, v59\n"
      "v_lshlrev_b32 v172, 16, v60\n"
      "v_and_b32 v173, 0xffff0000, v60\n"
      "v_lshlrev_b32 v174, 16, v61\n"
      "v_and_b32 v175, 0xffff0000, v61\n"
      "v_add_f32 v62, v164, v165\n"
      "v_mul_f32 v63, v164, v164\n"
      "v_fmac_f32 v63, v165, v165\n"
      "v_add_f32 v62, v62, v166\n"
      "v_fmac_f32 v63, v166, v166\n"
      "v_add_f32 v62, v62, v167\n"
      "v_fmac_f32 v63, v167, v167\n"
      "v_add_f32 v62, v62, v168\n"
      "v_fmac_f32 v63, v168, v168\n"
      "v_add_f32 v62, v62, v169\n"
      "v_fmac_f32 v63, v169, v169\n"
      "v_add_f32 v62, v62, v170\n"
      "v_fmac_f32 v63, v170, v170\n"
      "v_add_f32 v62, v62, v171\n"
      "v_fmac_f32 v63, v171, v171\n"
      "v_add_f32 v62, v62, v172\n"
      "v_fmac_f32 v63, v172, v172\n"
      "v_add_f32 v62, v62, v173\n"
      "v_fmac_f32 v63, v173, v173\n"
      "v_add_f32 v62, v62, v174\n"
      "v_fmac_f32 v63, v174, v174\n"
      "v_add_f32 v62, v62, v175\n"
      "v_fmac_f32 v63, v175, v175\n"
      "ds_write_b64 v10, v[62:63] offset:0\n"
      "s_waitcnt lgkmcnt(0)\n"
      "s_barrier\n"
      "ds_read_b64 v[152:153], v11 offset:0\n"
      "s_waitcnt lgkmcnt(0)\n"
      "v_add_f32_dpp v152, v152, v152 row_ror:8 row_mask:0xf bank_mask:0xf\n"
      "v_add_f32_dpp v153, v153, v153 row_ror:8 row_mask:0xf bank_mask:0xf\n"
      "s_nop 0\n"
      "v_add_f32_dpp v152, v152, v152 row_ror:4 row_mask:0xf bank_mask:0xf\n"
      "v_add_f32_dpp v153, v153, v153 row_ror:4 row_mask:0xf bank_mask:0xf\n"
      "s_nop 0\n"
      "v_add_f32_dpp v152, v152, v152 row_ror:2 row_mask:0xf bank_mask:0xf\n"
      "v_add_f32_dpp v153, v153, v153 row_ror:2 row_mask:0xf bank_mask:0xf\n"
      "s_nop 0\n"
      "v_add_f32_dpp v152, v152, v152 row_ror:1 row_mask:0xf bank_mask:0xf\n"
      "v_add_f32_dpp v153, v153, v153 row_ror:1 row_mask:0xf bank_mask:0xf\n"
      "v_mul_f32 v152, 0x3ae38e39, v152\n"
      "v_add_f32 v153, 0x3afba882, v153\n"
      "v_mul_f32 v153, 0x3ae38e39, v153\n"
      "buffer_store_dwordx2 v[152:153], v12, s[8:11], s48 offen sc1\n"
      "s_add_u32 s30, s46, 4294856704\n"
      "buffer_load_dwordx4 v[64:67], v8, s[92:95], s30 offen\n"
      "buffer_load_dwordx2 v[68:69], v9, s[92:95], s30 offen\n"
      "s_add_u32 s30, s46, 4294746112\n"
      "buffer_load_dwordx4 v[72:75], v8, s[92:95], s30 offen\n"
      "buffer_load_dwordx2 v[76:77], v9, s[92:95], s30 offen\n"
      "s_waitcnt vmcnt(4)\n"
      "buffer_store_dword v18, v5, s[8:11], s48 offen sc1\n"
      "s_add_u32 s30, s46, 4294635520\n"
      "buffer_load_dwordx4 v[80:83], v8, s[92:95], s30 offen\n"
      "buffer_load_dwordx2 v[84:85], v9, s[92:95], s30 offen\n"
      "s_add_u32 s30, s46, 4294524928\n"
      "buffer_load_dwordx4 v[88:91], v8, s[92:95], s30 offen\n"
      "buffer_load_dwordx2 v[92:93], v9, s[92:95], s30 offen\n"
      "buffer_load_dwordx4 v[96:99], v8, s[92:95], s47 offen\n"
      "buffer_load_dwordx2 v[100:101], v9, s[92:95], s47 offen\n"
      "buffer_store_dword v18, v15, s[8:11], 0 offen sc1\n"
      "s_cmp_eq_u32 s45, 0\n"
      "s_cselect_b32 s30, 0x40000000, s37\n"
      "buffer_store_dword v21, v3, s[8:11], s30 offen sc1\n"
      "s_add_u32 s61, s48, 15876096\n"
      "buffer_store_dwordx4 v[216:219], v6, s[8:11], s61 offen sc1\n"
      "buffer_store_dwordx2 v[216:217], v7, s[8:11], s61 offen sc1\n"
      "buffer_store_dwordx2 v[216:217], v182, s[8:11], s61 offen sc1\n"
      "s_add_u32 s61, s48, 16023552\n"
      "buffer_store_dwordx4 v[216:219], v6, s[8:11], s61 offen sc1\n"
      "buffer_store_dwordx2 v[216:217], v7, s[8:11], s61 offen sc1\n"
      "buffer_store_dwordx2 v[216:217], v182, s[8:11], s61 offen sc1\n"
      "s_add_u32 s61, s48, 16171008\n"
      "buffer_store_dwordx4 v[216:219], v6, s[8:11], s61 offen sc1\n"
      "buffer_store_dwordx2 v[216:217], v7, s[8:11], s61 offen sc1\n"
      "buffer_store_dwordx2 v[216:217], v182, s[8:11], s61 offen sc1\n"
      "s_add_u32 s61, s48, 16318464\n"
      "buffer_store_dwordx4 v[216:219], v6, s[8:11], s61 offen sc1\n"
      "buffer_store_dwordx2 v[216:217], v7, s[8:11], s61 offen sc1\n"
      "buffer_store_dwordx2 v[216:217], v182, s[8:11], s61 offen sc1\n"
      "s_add_u32 s61, s49, 15728640\n"
      "buffer_store_dwordx4 v[216:219], v6, s[8:11], s61 offen sc1\n"
      "buffer_store_dwordx2 v[216:217], v7, s[8:11], s61 offen sc1\n"
      "buffer_store_dwordx2 v[216:217], v182, s[8:11], s61 offen sc1\n"
      "s_waitcnt vmcnt(26)\n"
      "s_add_u32 s30, s48, 147456\n"
      "buffer_store_dwordx4 v[64:67], v6, s[8:11], s30 offen sc1\n"
      "buffer_store_dwordx2 v[68:69], v7, s[8:11], s30 offen sc1\n"
      "v_lshlrev_b32 v164, 16, v64\n"
      "v_and_b32 v165, 0xffff0000, v64\n"
      "v_lshlrev_b32 v166, 16, v65\n"
      "v_and_b32 v167, 0xffff0000, v65\n"
      "v_lshlrev_b32 v168, 16, v66\n"
      "v_and_b32 v169, 0xffff0000, v66\n"
      "v_lshlrev_b32 v170, 16, v67\n"
      "v_and_b32 v171, 0xffff0000, v67\n"
      "v_lshlrev_b32 v172, 16, v68\n"
      "v_and_b32 v173, 0xffff0000, v68\n"
      "v_lshlrev_b32 v174, 16, v69\n"
      "v_and_b32 v175, 0xffff0000, v69\n"
      "v_add_f32 v70, v164, v165\n"
      "v_mul_f32 v71, v164, v164\n"
      "v_fmac_f32 v71, v165, v165\n"
      "v_add_f32 v70, v70, v166\n"
      "v_fmac_f32 v71, v166, v166\n"
      "v_add_f32 v70, v70, v167\n"
      "v_fmac_f32 v71, v167, v167\n"
      "v_add_f32 v70, v70, v168\n"
      "v_fmac_f32 v71, v168, v168\n"
      "v_add_f32 v70, v70, v169\n"
      "v_fmac_f32 v71, v169, v169\n"
      "v_add_f32 v70, v70, v170\n"
      "v_fmac_f32 v71, v170, v170\n"
      "v_add_f32 v70, v70, v171\n"
      "v_fmac_f32 v71, v171, v171\n"
      "v_add_f32 v70, v70, v172\n"
      "v_fmac_f32 v71, v172, v172\n"
      "v_add_f32 v70, v70, v173\n"
      "v_fmac_f32 v71, v173, v173\n"
      "v_add_f32 v70, v70, v174\n"
      "v_fmac_f32 v71, v174, v174\n"
      "v_add_f32 v70, v70, v175\n"
      "v_fmac_f32 v71, v175, v175\n"
      "ds_write_b64 v10, v[70:71] offset:2176\n"
      "s_waitcnt vmcnt(26)\n"
      "s_add_u32 s30, s48, 294912\n"
      "buffer_store_dwordx4 v[72:75], v6, s[8:11], s30 offen sc1\n"
      "buffer_store_dwordx2 v[76:77], v7, s[8:11], s30 offen sc1\n"
      "v_lshlrev_b32 v164, 16, v72\n"
      "v_and_b32 v165, 0xffff0000, v72\n"
      "v_lshlrev_b32 v166, 16, v73\n"
      "v_and_b32 v167, 0xffff0000, v73\n"
      "v_lshlrev_b32 v168, 16, v74\n"
      "v_and_b32 v169, 0xffff0000, v74\n"
      "v_lshlrev_b32 v170, 16, v75\n"
      "v_and_b32 v171, 0xffff0000, v75\n"
      "v_lshlrev_b32 v172, 16, v76\n"
      "v_and_b32 v173, 0xffff0000, v76\n"
      "v_lshlrev_b32 v174, 16, v77\n"
      "v_and_b32 v175, 0xffff0000, v77\n"
      "v_add_f32 v78, v164, v165\n"
      "v_mul_f32 v79, v164, v164\n"
      "v_fmac_f32 v79, v165, v165\n"
      "v_add_f32 v78, v78, v166\n"
      "v_fmac_f32 v79, v166, v166\n"
      "v_add_f32 v78, v78, v167\n"
      "v_fmac_f32 v79, v167, v167\n"
      "v_add_f32 v78, v78, v168\n"
      "v_fmac_f32 v79, v168, v168\n"
      "v_add_f32 v78, v78, v169\n"
      "v_fmac_f32 v79, v169, v169\n"
      "v_add_f32 v78, v78, v170\n"
      "v_fmac_f32 v79, v170, v170\n"
      "v_add_f32 v78, v78, v171\n"
      "v_fmac_f32 v79, v171, v171\n"
      "v_add_f32 v78, v78, v172\n"
      "v_fmac_f32 v79, v172, v172\n"
      "v_add_f32 v78, v78, v173\n"
      "v_fmac_f32 v79, v173, v173\n"
      "v_add_f32 v78, v78, v174\n"
      "v_fmac_f32 v79, v174, v174\n"
      "v_add_f32 v78, v78, v175\n"
      "v_fmac_f32 v79, v175, v175\n"
      "ds_write_b64 v10, v[78:79] offset:4352\n"
      "s_waitcnt vmcnt(25)\n"
      "s_add_u32 s30, s48, 442368\n"
      "buffer_store_dwordx4 v[80:83], v6, s[8:11], s30 offen sc1\n"
      "buffer_store_dwordx2 v[84:85], v7, s[8:11], s30 offen sc1\n"
      "v_lshlrev_b32 v164, 16, v80\n"
      "v_and_b32 v165, 0xffff0000, v80\n"
      "v_lshlrev_b32 v166, 16, v81\n"
      "v_and_b32 v167, 0xffff0000, v81\n"
      "v_lshlrev_b32 v168, 16, v82\n"
      "v_and_b32 v169, 0xffff0000, v82\n"
      "v_lshlrev_b32 v170, 16, v83\n"
      "v_and_b32 v171, 0xffff0000, v83\n"
      "v_lshlrev_b32 v172, 16, v84\n"
      "v_and_b32 v173, 0xffff0000, v84\n"
      "v_lshlrev_b32 v174, 16, v85\n"
      "v_and_b32 v175, 0xffff0000, v85\n"
      "v_add_f32 v86, v164, v165\n"
      "v_mul_f32 v87, v164, v164\n"
      "v_fmac_f32 v87, v165, v165\n"
      "v_add_f32 v86, v86, v166\n"
      "v_fmac_f32 v87, v166, v166\n"
      "v_add_f32 v86, v86, v167\n"
      "v_fmac_f32 v87, v167, v167\n"
      "v_add_f32 v86, v86, v168\n"
      "v_fmac_f32 v87, v168, v168\n"
      "v_add_f32 v86, v86, v169\n"
      "v_fmac_f32 v87, v169, v169\n"
      "v_add_f32 v86, v86, v170\n"
      "v_fmac_f32 v87, v170, v170\n"
      "v_add_f32 v86, v86, v171\n"
      "v_fmac_f32 v87, v171, v171\n"
      "v_add_f32 v86, v86, v172\n"
      "v_fmac_f32 v87, v172, v172\n"
      "v_add_f32 v86, v86, v173\n"
      "v_fmac_f32 v87, v173, v173\n"
      "v_add_f32 v86, v86, v174\n"
      "v_fmac_f32 v87, v174, v174\n"
      "v_add_f32 v86, v86, v175\n"
      "v_fmac_f32 v87, v175, v175\n"
      "ds_write_b64 v10, v[86:87] offset:6528\n"
      "s_waitcnt vmcnt(25)\n"
      "s_add_u32 s30, s48, 589824\n"
      "buffer_store_dwordx4 v[88:91], v6, s[8:11], s30 offen sc1\n"
      "buffer_store_dwordx2 v[92:93], v7, s[8:11], s30 offen sc1\n"
      "v_lshlrev_b32 v164, 16, v88\n"
      "v_and_b32 v165, 0xffff0000, v88\n"
      "v_lshlrev_b32 v166, 16, v89\n"
      "v_and_b32 v167, 0xffff0000, v89\n"
      "v_lshlrev_b32 v168, 16, v90\n"
      "v_and_b32 v169, 0xffff0000, v90\n"
      "v_lshlrev_b32 v170, 16, v91\n"
      "v_and_b32 v171, 0xffff0000, v91\n"
      "v_lshlrev_b32 v172, 16, v92\n"
      "v_and_b32 v173, 0xffff0000, v92\n"
      "v_lshlrev_b32 v174, 16, v93\n"
      "v_and_b32 v175, 0xffff0000, v93\n"
      "v_add_f32 v94, v164, v165\n"
      "v_mul_f32 v95, v164, v164\n"
      "v_fmac_f32 v95, v165, v165\n"
      "v_add_f32 v94, v94, v166\n"
      "v_fmac_f32 v95, v166, v166\n"
      "v_add_f32 v94, v94, v167\n"
      "v_fmac_f32 v95, v167, v167\n"
      "v_add_f32 v94, v94, v168\n"
      "v_fmac_f32 v95, v168, v168\n"
      "v_add_f32 v94, v94, v169\n"
      "v_fmac_f32 v95, v169, v169\n"
      "v_add_f32 v94, v94, v170\n"
      "v_fmac_f32 v95, v170, v170\n"
      "v_add_f32 v94, v94, v171\n"
      "v_fmac_f32 v95, v171, v171\n"
      "v_add_f32 v94, v94, v172\n"
      "v_fmac_f32 v95, v172, v172\n"
      "v_add_f32 v94, v94, v173\n"
      "v_fmac_f32 v95, v173, v173\n"
      "v_add_f32 v94, v94, v174\n"
      "v_fmac_f32 v95, v174, v174\n"
      "v_add_f32 v94, v94, v175\n"
      "v_fmac_f32 v95, v175, v175\n"
      "ds_write_b64 v10, v[94:95] offset:8704\n"
      "s_waitcnt vmcnt(25)\n"
      "buffer_store_dwordx4 v[96:99], v6, s[8:11], s49 offen sc1\n"
      "buffer_store_dwordx2 v[100:101], v7, s[8:11], s49 offen sc1\n"
      "v_lshlrev_b32 v164, 16, v96\n"
      "v_and_b32 v165, 0xffff0000, v96\n"
      "v_lshlrev_b32 v166, 16, v97\n"
      "v_and_b32 v167, 0xffff0000, v97\n"
      "v_lshlrev_b32 v168, 16, v98\n"
      "v_and_b32 v169, 0xffff0000, v98\n"
      "v_lshlrev_b32 v170, 16, v99\n"
      "v_and_b32 v171, 0xffff0000, v99\n"
      "v_lshlrev_b32 v172, 16, v100\n"
      "v_and_b32 v173, 0xffff0000, v100\n"
      "v_lshlrev_b32 v174, 16, v101\n"
      "v_and_b32 v175, 0xffff0000, v101\n"
      "v_add_f32 v102, v164, v165\n"
      "v_mul_f32 v103, v164, v164\n"
      "v_fmac_f32 v103, v165, v165\n"
      "v_add_f32 v102, v102, v166\n"
      "v_fmac_f32 v103, v166, v166\n"
      "v_add_f32 v102, v102, v167\n"
      "v_fmac_f32 v103, v167, v167\n"
      "v_add_f32 v102, v102, v168\n"
      "v_fmac_f32 v103, v168, v168\n"
      "v_add_f32 v102, v102, v169\n"
      "v_fmac_f32 v103, v169, v169\n"
      "v_add_f32 v102, v102, v170\n"
      "v_fmac_f32 v103, v170, v170\n"
      "v_add_f32 v102, v102, v171\n"
      "v_fmac_f32 v103, v171, v171\n"
      "v_add_f32 v102, v102, v172\n"
      "v_fmac_f32 v103, v172, v172\n"
      "v_add_f32 v102, v102, v173\n"
      "v_fmac_f32 v103, v173, v173\n"
      "v_add_f32 v102, v102, v174\n"
      "v_fmac_f32 v103, v174, v174\n"
      "v_add_f32 v102, v102, v175\n"
      "v_fmac_f32 v103, v175, v175\n"
      "ds_write_b64 v10, v[102:103] offset:10880\n"
      "s_waitcnt lgkmcnt(0)\n"
      "s_barrier\n"
      "ds_read_b64 v[154:155], v11 offset:2176\n"
      "ds_read_b64 v[156:157], v11 offset:4352\n"
      "ds_read_b64 v[158:159], v11 offset:6528\n"
      "ds_read_b64 v[160:161], v11 offset:8704\n"
      "ds_read_b64 v[162:163], v11 offset:10880\n"
      "s_waitcnt lgkmcnt(0)\n"
      "v_add_f32_dpp v154, v154, v154 row_ror:8 row_mask:0xf bank_mask:0xf\n"
      "v_add_f32_dpp v155, v155, v155 row_ror:8 row_mask:0xf bank_mask:0xf\n"
      "v_add_f32_dpp v156, v156, v156 row_ror:8 row_mask:0xf bank_mask:0xf\n"
      "v_add_f32_dpp v157, v157, v157 row_ror:8 row_mask:0xf bank_mask:0xf\n"
      "v_add_f32_dpp v158, v158, v158 row_ror:8 row_mask:0xf bank_mask:0xf\n"
      "v_add_f32_dpp v159, v159, v159 row_ror:8 row_mask:0xf bank_mask:0xf\n"
      "v_add_f32_dpp v160, v160, v160 row_ror:8 row_mask:0xf bank_mask:0xf\n"
      "v_add_f32_dpp v161, v161, v161 row_ror:8 row_mask:0xf bank_mask:0xf\n"
      "v_add_f32_dpp v162, v162, v162 row_ror:8 row_mask:0xf bank_mask:0xf\n"
      "v_add_f32_dpp v163, v163, v163 row_ror:8 row_mask:0xf bank_mask:0xf\n"
      "v_add_f32_dpp v154, v154, v154 row_ror:4 row_mask:0xf bank_mask:0xf\n"
      "v_add_f32_dpp v155, v155, v155 row_ror:4 row_mask:0xf bank_mask:0xf\n"
      "v_add_f32_dpp v156, v156, v156 row_ror:4 row_mask:0xf bank_mask:0xf\n"
      "v_add_f32_dpp v157, v157, v157 row_ror:4 row_mask:0xf bank_mask:0xf\n"
      "v_add_f32_dpp v158, v158, v158 row_ror:4 row_mask:0xf bank_mask:0xf\n"
      "v_add_f32_dpp v159, v159, v159 row_ror:4 row_mask:0xf bank_mask:0xf\n"
      "v_add_f32_dpp v160, v160, v160 row_ror:4 row_mask:0xf bank_mask:0xf\n"
      "v_add_f32_dpp v161, v161, v161 row_ror:4 row_mask:0xf bank_mask:0xf\n"
      "v_add_f32_dpp v162, v162, v162 row_ror:4 row_mask:0xf bank_mask:0xf\n"
      "v_add_f32_dpp v163, v163, v163 row_ror:4 row_mask:0xf bank_mask:0xf\n"
      "v_add_f32_dpp v154, v154, v154 row_ror:2 row_mask:0xf bank_mask:0xf\n"
      "v_add_f32_dpp v155, v155, v155 row_ror:2 row_mask:0xf bank_mask:0xf\n"
      "v_add_f32_dpp v156, v156, v156 row_ror:2 row_mask:0xf bank_mask:0xf\n"
      "v_add_f32_dpp v157, v157, v157 row_ror:2 row_mask:0xf bank_mask:0xf\n"
      "v_add_f32_dpp v158, v158, v158 row_ror:2 row_mask:0xf bank_mask:0xf\n"
      "v_add_f32_dpp v159, v159, v159 row_ror:2 row_mask:0xf bank_mask:0xf\n"
      "v_add_f32_dpp v160, v160, v160 row_ror:2 row_mask:0xf bank_mask:0xf\n"
      "v_add_f32_dpp v161, v161, v161 row_ror:2 row_mask:0xf bank_mask:0xf\n"
      "v_add_f32_dpp v162, v162, v162 row_ror:2 row_mask:0xf bank_mask:0xf\n"
      "v_add_f32_dpp v163, v163, v163 row_ror:2 row_mask:0xf bank_mask:0xf\n"
      "v_add_f32_dpp v154, v154, v154 row_ror:1 row_mask:0xf bank_mask:0xf\n"
      "v_add_f32_dpp v155, v155, v155 row_ror:1 row_mask:0xf bank_mask:0xf\n"
      "v_add_f32_dpp v156, v156, v156 row_ror:1 row_mask:0xf bank_mask:0xf\n"
      "v_add_f32_dpp v157, v157, v157 row_ror:1 row_mask:0xf bank_mask:0xf\n"
      "v_add_f32_dpp v158, v158, v158 row_ror:1 row_mask:0xf bank_mask:0xf\n"
      "v_add_f32_dpp v159, v159, v159 row_ror:1 row_mask:0xf bank_mask:0xf\n"
      "v_add_f32_dpp v160, v160, v160 row_ror:1 row_mask:0xf bank_mask:0xf\n"
      "v_add_f32_dpp v161, v161, v161 row_ror:1 row_mask:0xf bank_mask:0xf\n"
      "v_add_f32_dpp v162, v162, v162 row_ror:1 row_mask:0xf bank_mask:0xf\n"
      "v_add_f32_dpp v163, v163, v163 row_ror:1 row_mask:0xf bank_mask:0xf\n"
      "v_mul_f32 v154, 0x3ae38e39, v154\n"
      "v_add_f32 v155, 0x3afba882, v155\n"
      "v_mul_f32 v155, 0x3ae38e39, v155\n"
      "v_mul_f32 v156, 0x3ae38e39, v156\n"
      "v_add_f32 v157, 0x3afba882, v157\n"
      "v_mul_f32 v157, 0x3ae38e39, v157\n"
      "v_mul_f32 v158, 0x3ae38e39, v158\n"
      "v_add_f32 v159, 0x3afba882, v159\n"
      "v_mul_f32 v159, 0x3ae38e39, v159\n"
      "v_mul_f32 v160, 0x3ae38e39, v160\n"
      "v_add_f32 v161, 0x3afba882, v161\n"
      "v_mul_f32 v161, 0x3ae38e39, v161\n"
      "v_mul_f32 v162, 0x3ae38e39, v162\n"
      "v_add_f32 v163, 0x3afba882, v163\n"
      "v_mul_f32 v163, 0x3ae38e39, v163\n"
      "s_add_u32 s30, s48, 147456\n"
      "buffer_store_dwordx2 v[154:155], v12, s[8:11], s30 offen sc1\n"
      "s_add_u32 s30, s48, 294912\n"
      "buffer_store_dwordx2 v[156:157], v12, s[8:11], s30 offen sc1\n"
      "s_add_u32 s30, s48, 442368\n"
      "buffer_store_dwordx2 v[158:159], v12, s[8:11], s30 offen sc1\n"
      "s_add_u32 s30, s48, 589824\n"
      "buffer_store_dwordx2 v[160:161], v12, s[8:11], s30 offen sc1\n"
      "buffer_store_dwordx2 v[162:163], v12, s[8:11], s49 offen sc1\n"
      "s_waitcnt vmcnt(0)\n"
      "s_add_u32 s30, s48, 147456\n"
      "buffer_store_dword v18, v5, s[8:11], s30 offen sc1\n"
      "s_add_u32 s30, s48, 294912\n"
      "buffer_store_dword v18, v5, s[8:11], s30 offen sc1\n"
      "s_add_u32 s30, s48, 442368\n"
      "buffer_store_dword v18, v5, s[8:11], s30 offen sc1\n"
      "s_add_u32 s30, s48, 589824\n"
      "buffer_store_dword v18, v5, s[8:11], s30 offen sc1\n"
      "buffer_store_dword v18, v5, s[8:11], s49 offen sc1\n"
      "v_and_b32 v20, 0x40000000, v19\n"
      "v_or_b32 v5, v20, v5\n"
      "s_add_u32 s30, s37, 0\n"
      "buffer_store_dwordx4 v[224:227], v183, s[8:11], s30 offen sc1\n"
      "s_add_u32 s30, s37, 0\n"
      "buffer_store_dwordx4 v[224:227], v181, s[8:11], s30 offen sc1\n"
      "v_mov_b32 v216, -1\n"
      "v_mov_b32 v217, -1\n"
      "v_mov_b32 v218, -1\n"
      "v_mov_b32 v219, -1\n"
      "buffer_store_dwordx4 v[216:219], v0, s[8:11], s51 offen sc1\n"
      "buffer_store_dwordx2 v[216:217], v13, s[8:11], s51 offen sc1\n"
      "buffer_store_dwordx4 v[216:219], v0, s[8:11], s52 offen sc1\n"
      "buffer_store_dwordx2 v[216:217], v13, s[8:11], s52 offen sc1\n"
      "buffer_store_dwordx4 v[216:219], v0, s[8:11], s53 offen sc1\n"
      "buffer_store_dwordx2 v[216:217], v13, s[8:11], s53 offen sc1\n"
      "s_sleep 32\n"
      "s_waitcnt vmcnt(0) lgkmcnt(0)\n"
      "s_mov_b32 s64, s63\n"
      "s_mov_b32 s65, 6\n"
      ".Ld_r0:\n"
      "s_mul_i32 s66, s64, 36864\n"
      "s_add_u32 s66, s66, s44\n"
      "s_mul_i32 s30, s64, 8192\n"
      "s_lshr_b32 s30, s30, 16\n"
      "s_mul_i32 s30, s30, 8\n"
      "s_sub_u32 s30, s64, s30\n"
      "s_mul_i32 s30, s30, 12288\n"
      "s_add_u32 s30, s30, 0\n"
      "s_add_u32 m0, s42, s30\n"
      "s_add_u32 s64, s64, 1\n"
      "buffer_load_dwordx4 v178, s[12:15], s66 offen offset:0 lds\n"
      "buffer_load_dwordx4 v179, s[12:15], s66 offen offset:1024 lds\n"
      "buffer_load_dwordx4 v180, s[12:15], s66 offen offset:2048 lds\n"
      "s_cmp_eq_u32 s64, 24\n"
      "s_cselect_b32 s64, 0, s64\n"
      "s_sub_u32 s65, s65, 1\n"
      "s_cbranch_scc0 .Ld_r0\n"
      "v_add_u32 v20, 49152, v176\n"
      "v_add_u32 v21, 49152, v177\n"
      "v_add_u32 v22, 98304, v176\n"
      "v_add_u32 v23, 98304, v177\n"
      "s_mov_b32 s65, 23\n"
      "s_cmp_eq_u32 s63, 6\n"
      "s_cbranch_scc1 .Ld_rs6\n"
      "s_cmp_eq_u32 s63, 12\n"
      "s_cbranch_scc1 .Ld_rs12\n"
      "s_cmp_eq_u32 s63, 18\n"
      "s_cbranch_scc1 .Ld_rs18\n"
      ".Ld_rs0:\n"
      "s_add_u32 s66, s44, 258048\n"
      "s_cmp_ge_u32 s65, 7\n"
      "s_cselect_b32 s66, s66, s31\n"
      "s_add_u32 m0, s42, 86016\n"
      "s_waitcnt vmcnt(18) lgkmcnt(0)\n"
      "s_barrier\n"
      "ds_read_b64_tr_b16 a[0:1], v176 offset:0\n"
      "ds_read_b64_tr_b16 a[2:3], v176 offset:1536\n"
      "ds_read_b64_tr_b16 a[96:97], v176 offset:8\n"
      "ds_read_b64_tr_b16 a[98:99], v176 offset:1544\n"
      "ds_read_b64_tr_b16 a[192:193], v177 offset:0\n"
      "ds_read_b64_tr_b16 a[194:195], v177 offset:1536\n"
      "buffer_load_dwordx4 v178, s[12:15], s66 offen offset:0 lds\n"
      "buffer_load_dwordx4 v179, s[12:15], s66 offen offset:1024 lds\n"
      "buffer_load_dwordx4 v180, s[12:15], s66 offen offset:2048 lds\n"
      "s_sub_u32 s65, s65, 1\n"
      ".Ld_rs1:\n"
      "s_add_u32 s66, s44, 294912\n"
      "s_cmp_ge_u32 s65, 7\n"
      "s_cselect_b32 s66, s66, s31\n"
      "s_add_u32 m0, s42, 0\n"
      "s_waitcnt vmcnt(18) lgkmcnt(0)\n"
      "s_barrier\n"
      "ds_read_b64_tr_b16 a[4:5], v176 offset:12288\n"
      "ds_read_b64_tr_b16 a[6:7], v176 offset:13824\n"
      "ds_read_b64_tr_b16 a[100:101], v176 offset:12296\n"
      "ds_read_b64_tr_b16 a[102:103], v176 offset:13832\n"
      "ds_read_b64_tr_b16 a[196:197], v177 offset:12288\n"
      "ds_read_b64_tr_b16 a[198:199], v177 offset:13824\n"
      "buffer_load_dwordx4 v178, s[12:15], s66 offen offset:0 lds\n"
      "buffer_load_dwordx4 v179, s[12:15], s66 offen offset:1024 lds\n"
      "buffer_load_dwordx4 v180, s[12:15], s66 offen offset:2048 lds\n"
      "s_sub_u32 s65, s65, 1\n"
      ".Ld_rs2:\n"
      "s_add_u32 s66, s44, 331776\n"
      "s_cmp_ge_u32 s65, 7\n"
      "s_cselect_b32 s66, s66, s31\n"
      "s_add_u32 m0, s42, 12288\n"
      "s_waitcnt vmcnt(18) lgkmcnt(0)\n"
      "s_barrier\n"
      "ds_read_b64_tr_b16 a[8:9], v176 offset:24576\n"
      "ds_read_b64_tr_b16 a[10:11], v176 offset:26112\n"
      "ds_read_b64_tr_b16 a[104:105], v176 offset:24584\n"
      "ds_read_b64_tr_b16 a[106:107], v176 offset:26120\n"
      "ds_read_b64_tr_b16 a[200:201], v177 offset:24576\n"
      "ds_read_b64_tr_b16 a[202:203], v177 offset:26112\n"
      "buffer_load_dwordx4 v178, s[12:15], s66 offen offset:0 lds\n"
      "buffer_load_dwordx4 v179, s[12:15], s66 offen offset:1024 lds\n"
      "buffer_load_dwordx4 v180, s[12:15], s66 offen offset:2048 lds\n"
      "s_sub_u32 s65, s65, 1\n"
      ".Ld_rs3:\n"
      "s_add_u32 s66, s44, 368640\n"
      "s_cmp_ge_u32 s65, 7\n"
      "s_cselect_b32 s66, s66, s31\n"
      "s_add_u32 m0, s42, 24576\n"
      "s_waitcnt vmcnt(18) lgkmcnt(0)\n"
      "s_barrier\n"
      "ds_read_b64_tr_b16 a[12:13], v176 offset:36864\n"
      "ds_read_b64_tr_b16 a[14:15], v176 offset:38400\n"
      "ds_read_b64_tr_b16 a[108:109], v176 offset:36872\n"
      "ds_read_b64_tr_b16 a[110:111], v176 offset:38408\n"
      "ds_read_b64_tr_b16 a[204:205], v177 offset:36864\n"
      "ds_read_b64_tr_b16 a[206:207], v177 offset:38400\n"
      "buffer_load_dwordx4 v178, s[12:15], s66 offen offset:0 lds\n"
      "buffer_load_dwordx4 v179, s[12:15], s66 offen offset:1024 lds\n"
      "buffer_load_dwordx4 v180, s[12:15], s66 offen offset:2048 lds\n"
      "s_sub_u32 s65, s65, 1\n"
      ".Ld_rs4:\n"
      "s_add_u32 s66, s44, 405504\n"
      "s_cmp_ge_u32 s65, 7\n"
      "s_cselect_b32 s66, s66, s31\n"
      "s_add_u32 m0, s42, 36864\n"
      "s_waitcnt vmcnt(18) lgkmcnt(0)\n"
      "s_barrier\n"
      "ds_read_b64_tr_b16 a[16:17], v20 offset:0\n"
      "ds_read_b64_tr_b16 a[18:19], v20 offset:1536\n"
      "ds_read_b64_tr_b16 a[112:113], v20 offset:8\n"
      "ds_read_b64_tr_b16 a[114:115], v20 offset:1544\n"
      "ds_read_b64_tr_b16 a[208:209], v21 offset:0\n"
      "ds_read_b64_tr_b16 a[210:211], v21 offset:1536\n"
      "buffer_load_dwordx4 v178, s[12:15], s66 offen offset:0 lds\n"
      "buffer_load_dwordx4 v179, s[12:15], s66 offen offset:1024 lds\n"
      "buffer_load_dwordx4 v180, s[12:15], s66 offen offset:2048 lds\n"
      "s_sub_u32 s65, s65, 1\n"
      ".Ld_rs5:\n"
      "s_add_u32 s66, s44, 442368\n"
      "s_cmp_ge_u32 s65, 7\n"
      "s_cselect_b32 s66, s66, s31\n"
      "s_add_u32 m0, s42, 49152\n"
      "s_waitcnt vmcnt(18) lgkmcnt(0)\n"
      "s_barrier\n"
      "ds_read_b64_tr_b16 a[20:21], v20 offset:12288\n"
      "ds_read_b64_tr_b16 a[22:23], v20 offset:13824\n"
      "ds_read_b64_tr_b16 a[116:117], v20 offset:12296\n"
      "ds_read_b64_tr_b16 a[118:119], v20 offset:13832\n"
      "ds_read_b64_tr_b16 a[212:213], v21 offset:12288\n"
      "ds_read_b64_tr_b16 a[214:215], v21 offset:13824\n"
      "buffer_load_dwordx4 v178, s[12:15], s66 offen offset:0 lds\n"
      "buffer_load_dwordx4 v179, s[12:15], s66 offen offset:1024 lds\n"
      "buffer_load_dwordx4 v180, s[12:15], s66 offen offset:2048 lds\n"
      "s_sub_u32 s65, s65, 1\n"
      "s_cbranch_scc1 .Ld_re\n"
      ".Ld_rs6:\n"
      "s_add_u32 s66, s44, 479232\n"
      "s_cmp_ge_u32 s65, 7\n"
      "s_cselect_b32 s66, s66, s31\n"
      "s_add_u32 m0, s42, 61440\n"
      "s_waitcnt vmcnt(18) lgkmcnt(0)\n"
      "s_barrier\n"
      "ds_read_b64_tr_b16 a[24:25], v20 offset:24576\n"
      "ds_read_b64_tr_b16 a[26:27], v20 offset:26112\n"
      "ds_read_b64_tr_b16 a[120:121], v20 offset:24584\n"
      "ds_read_b64_tr_b16 a[122:123], v20 offset:26120\n"
      "ds_read_b64_tr_b16 a[216:217], v21 offset:24576\n"
      "ds_read_b64_tr_b16 a[218:219], v21 offset:26112\n"
      "buffer_load_dwordx4 v178, s[12:15], s66 offen offset:0 lds\n"
      "buffer_load_dwordx4 v179, s[12:15], s66 offen offset:1024 lds\n"
      "buffer_load_dwordx4 v180, s[12:15], s66 offen offset:2048 lds\n"
      "s_sub_u32 s65, s65, 1\n"
      ".Ld_rs7:\n"
      "s_add_u32 s66, s44, 516096\n"
      "s_cmp_ge_u32 s65, 7\n"
      "s_cselect_b32 s66, s66, s31\n"
      "s_add_u32 m0, s42, 73728\n"
      "s_waitcnt vmcnt(18) lgkmcnt(0)\n"
      "s_barrier\n"
      "ds_read_b64_tr_b16 a[28:29], v20 offset:36864\n"
      "ds_read_b64_tr_b16 a[30:31], v20 offset:38400\n"
      "ds_read_b64_tr_b16 a[124:125], v20 offset:36872\n"
      "ds_read_b64_tr_b16 a[126:127], v20 offset:38408\n"
      "ds_read_b64_tr_b16 a[220:221], v21 offset:36864\n"
      "ds_read_b64_tr_b16 a[222:223], v21 offset:38400\n"
      "buffer_load_dwordx4 v178, s[12:15], s66 offen offset:0 lds\n"
      "buffer_load_dwordx4 v179, s[12:15], s66 offen offset:1024 lds\n"
      "buffer_load_dwordx4 v180, s[12:15], s66 offen offset:2048 lds\n"
      "s_sub_u32 s65, s65, 1\n"
      ".Ld_rs8:\n"
      "s_add_u32 s66, s44, 552960\n"
      "s_cmp_ge_u32 s65, 7\n"
      "s_cselect_b32 s66, s66, s31\n"
      "s_add_u32 m0, s42, 86016\n"
      "s_waitcnt vmcnt(18) lgkmcnt(0)\n"
      "s_barrier\n"
      "ds_read_b64_tr_b16 a[32:33], v176 offset:0\n"
      "ds_read_b64_tr_b16 a[34:35], v176 offset:1536\n"
      "ds_read_b64_tr_b16 a[128:129], v176 offset:8\n"
      "ds_read_b64_tr_b16 a[130:131], v176 offset:1544\n"
      "ds_read_b64_tr_b16 a[224:225], v177 offset:0\n"
      "ds_read_b64_tr_b16 a[226:227], v177 offset:1536\n"
      "buffer_load_dwordx4 v178, s[12:15], s66 offen offset:0 lds\n"
      "buffer_load_dwordx4 v179, s[12:15], s66 offen offset:1024 lds\n"
      "buffer_load_dwordx4 v180, s[12:15], s66 offen offset:2048 lds\n"
      "s_sub_u32 s65, s65, 1\n"
      ".Ld_rs9:\n"
      "s_add_u32 s66, s44, 589824\n"
      "s_cmp_ge_u32 s65, 7\n"
      "s_cselect_b32 s66, s66, s31\n"
      "s_add_u32 m0, s42, 0\n"
      "s_waitcnt vmcnt(18) lgkmcnt(0)\n"
      "s_barrier\n"
      "ds_read_b64_tr_b16 a[36:37], v176 offset:12288\n"
      "ds_read_b64_tr_b16 a[38:39], v176 offset:13824\n"
      "ds_read_b64_tr_b16 a[132:133], v176 offset:12296\n"
      "ds_read_b64_tr_b16 a[134:135], v176 offset:13832\n"
      "ds_read_b64_tr_b16 a[228:229], v177 offset:12288\n"
      "ds_read_b64_tr_b16 a[230:231], v177 offset:13824\n"
      "buffer_load_dwordx4 v178, s[12:15], s66 offen offset:0 lds\n"
      "buffer_load_dwordx4 v179, s[12:15], s66 offen offset:1024 lds\n"
      "buffer_load_dwordx4 v180, s[12:15], s66 offen offset:2048 lds\n"
      "s_sub_u32 s65, s65, 1\n"
      ".Ld_rs10:\n"
      "s_add_u32 s66, s44, 626688\n"
      "s_cmp_ge_u32 s65, 7\n"
      "s_cselect_b32 s66, s66, s31\n"
      "s_add_u32 m0, s42, 12288\n"
      "s_waitcnt vmcnt(18) lgkmcnt(0)\n"
      "s_barrier\n"
      "ds_read_b64_tr_b16 a[40:41], v176 offset:24576\n"
      "ds_read_b64_tr_b16 a[42:43], v176 offset:26112\n"
      "ds_read_b64_tr_b16 a[136:137], v176 offset:24584\n"
      "ds_read_b64_tr_b16 a[138:139], v176 offset:26120\n"
      "ds_read_b64_tr_b16 a[232:233], v177 offset:24576\n"
      "ds_read_b64_tr_b16 a[234:235], v177 offset:26112\n"
      "buffer_load_dwordx4 v178, s[12:15], s66 offen offset:0 lds\n"
      "buffer_load_dwordx4 v179, s[12:15], s66 offen offset:1024 lds\n"
      "buffer_load_dwordx4 v180, s[12:15], s66 offen offset:2048 lds\n"
      "s_sub_u32 s65, s65, 1\n"
      ".Ld_rs11:\n"
      "s_add_u32 s66, s44, 663552\n"
      "s_cmp_ge_u32 s65, 7\n"
      "s_cselect_b32 s66, s66, s31\n"
      "s_add_u32 m0, s42, 24576\n"
      "s_waitcnt vmcnt(18) lgkmcnt(0)\n"
      "s_barrier\n"
      "ds_read_b64_tr_b16 a[44:45], v176 offset:36864\n"
      "ds_read_b64_tr_b16 a[46:47], v176 offset:38400\n"
      "ds_read_b64_tr_b16 a[140:141], v176 offset:36872\n"
      "ds_read_b64_tr_b16 a[142:143], v176 offset:38408\n"
      "ds_read_b64_tr_b16 a[236:237], v177 offset:36864\n"
      "ds_read_b64_tr_b16 a[238:239], v177 offset:38400\n"
      "buffer_load_dwordx4 v178, s[12:15], s66 offen offset:0 lds\n"
      "buffer_load_dwordx4 v179, s[12:15], s66 offen offset:1024 lds\n"
      "buffer_load_dwordx4 v180, s[12:15], s66 offen offset:2048 lds\n"
      "s_sub_u32 s65, s65, 1\n"
      "s_cbranch_scc1 .Ld_re\n"
      ".Ld_rs12:\n"
      "s_add_u32 s66, s44, 700416\n"
      "s_cmp_ge_u32 s65, 7\n"
      "s_cselect_b32 s66, s66, s31\n"
      "s_add_u32 m0, s42, 36864\n"
      "s_waitcnt vmcnt(18) lgkmcnt(0)\n"
      "s_barrier\n"
      "ds_read_b64_tr_b16 a[48:49], v20 offset:0\n"
      "ds_read_b64_tr_b16 a[50:51], v20 offset:1536\n"
      "ds_read_b64_tr_b16 a[144:145], v20 offset:8\n"
      "ds_read_b64_tr_b16 a[146:147], v20 offset:1544\n"
      "ds_read_b64_tr_b16 a[240:241], v21 offset:0\n"
      "ds_read_b64_tr_b16 a[242:243], v21 offset:1536\n"
      "buffer_load_dwordx4 v178, s[12:15], s66 offen offset:0 lds\n"
      "buffer_load_dwordx4 v179, s[12:15], s66 offen offset:1024 lds\n"
      "buffer_load_dwordx4 v180, s[12:15], s66 offen offset:2048 lds\n"
      "s_sub_u32 s65, s65, 1\n"
      ".Ld_rs13:\n"
      "s_add_u32 s66, s44, 737280\n"
      "s_cmp_ge_u32 s65, 7\n"
      "s_cselect_b32 s66, s66, s31\n"
      "s_add_u32 m0, s42, 49152\n"
      "s_waitcnt vmcnt(18) lgkmcnt(0)\n"
      "s_barrier\n"
      "ds_read_b64_tr_b16 a[52:53], v20 offset:12288\n"
      "ds_read_b64_tr_b16 a[54:55], v20 offset:13824\n"
      "ds_read_b64_tr_b16 a[148:149], v20 offset:12296\n"
      "ds_read_b64_tr_b16 a[150:151], v20 offset:13832\n"
      "ds_read_b64_tr_b16 a[244:245], v21 offset:12288\n"
      "ds_read_b64_tr_b16 a[246:247], v21 offset:13824\n"
      "buffer_load_dwordx4 v178, s[12:15], s66 offen offset:0 lds\n"
      "buffer_load_dwordx4 v179, s[12:15], s66 offen offset:1024 lds\n"
      "buffer_load_dwordx4 v180, s[12:15], s66 offen offset:2048 lds\n"
      "s_sub_u32 s65, s65, 1\n"
      ".Ld_rs14:\n"
      "s_add_u32 s66, s44, 774144\n"
      "s_cmp_ge_u32 s65, 7\n"
      "s_cselect_b32 s66, s66, s31\n"
      "s_add_u32 m0, s42, 61440\n"
      "s_waitcnt vmcnt(18) lgkmcnt(0)\n"
      "s_barrier\n"
      "ds_read_b64_tr_b16 a[56:57], v20 offset:24576\n"
      "ds_read_b64_tr_b16 a[58:59], v20 offset:26112\n"
      "ds_read_b64_tr_b16 a[152:153], v20 offset:24584\n"
      "ds_read_b64_tr_b16 a[154:155], v20 offset:26120\n"
      "ds_read_b64_tr_b16 a[248:249], v21 offset:24576\n"
      "ds_read_b64_tr_b16 a[250:251], v21 offset:26112\n"
      "buffer_load_dwordx4 v178, s[12:15], s66 offen offset:0 lds\n"
      "buffer_load_dwordx4 v179, s[12:15], s66 offen offset:1024 lds\n"
      "buffer_load_dwordx4 v180, s[12:15], s66 offen offset:2048 lds\n"
      "s_sub_u32 s65, s65, 1\n"
      ".Ld_rs15:\n"
      "s_add_u32 s66, s44, 811008\n"
      "s_cmp_ge_u32 s65, 7\n"
      "s_cselect_b32 s66, s66, s31\n"
      "s_add_u32 m0, s42, 73728\n"
      "s_waitcnt vmcnt(18) lgkmcnt(0)\n"
      "s_barrier\n"
      "ds_read_b64_tr_b16 a[60:61], v20 offset:36864\n"
      "ds_read_b64_tr_b16 a[62:63], v20 offset:38400\n"
      "ds_read_b64_tr_b16 a[156:157], v20 offset:36872\n"
      "ds_read_b64_tr_b16 a[158:159], v20 offset:38408\n"
      "ds_read_b64_tr_b16 a[252:253], v21 offset:36864\n"
      "ds_read_b64_tr_b16 a[254:255], v21 offset:38400\n"
      "buffer_load_dwordx4 v178, s[12:15], s66 offen offset:0 lds\n"
      "buffer_load_dwordx4 v179, s[12:15], s66 offen offset:1024 lds\n"
      "buffer_load_dwordx4 v180, s[12:15], s66 offen offset:2048 lds\n"
      "s_sub_u32 s65, s65, 1\n"
      ".Ld_rs16:\n"
      "s_add_u32 s66, s44, 847872\n"
      "s_cmp_ge_u32 s65, 7\n"
      "s_cselect_b32 s66, s66, s31\n"
      "s_add_u32 m0, s42, 86016\n"
      "s_waitcnt vmcnt(18) lgkmcnt(0)\n"
      "s_barrier\n"
      "ds_read_b64_tr_b16 a[64:65], v176 offset:0\n"
      "ds_read_b64_tr_b16 a[66:67], v176 offset:1536\n"
      "ds_read_b64_tr_b16 a[160:161], v176 offset:8\n"
      "ds_read_b64_tr_b16 a[162:163], v176 offset:1544\n"
      "ds_read_b64_tr_b16 v[24:25], v177 offset:0\n"
      "ds_read_b64_tr_b16 v[26:27], v177 offset:1536\n"
      "buffer_load_dwordx4 v178, s[12:15], s66 offen offset:0 lds\n"
      "buffer_load_dwordx4 v179, s[12:15], s66 offen offset:1024 lds\n"
      "buffer_load_dwordx4 v180, s[12:15], s66 offen offset:2048 lds\n"
      "s_sub_u32 s65, s65, 1\n"
      ".Ld_rs17:\n"
      "s_add_u32 s66, s44, 0\n"
      "s_cmp_ge_u32 s65, 7\n"
      "s_cselect_b32 s66, s66, s31\n"
      "s_add_u32 m0, s42, 0\n"
      "s_waitcnt vmcnt(18) lgkmcnt(0)\n"
      "s_barrier\n"
      "ds_read_b64_tr_b16 a[68:69], v176 offset:12288\n"
      "ds_read_b64_tr_b16 a[70:71], v176 offset:13824\n"
      "ds_read_b64_tr_b16 a[164:165], v176 offset:12296\n"
      "ds_read_b64_tr_b16 a[166:167], v176 offset:13832\n"
      "ds_read_b64_tr_b16 v[28:29], v177 offset:12288\n"
      "ds_read_b64_tr_b16 v[30:31], v177 offset:13824\n"
      "buffer_load_dwordx4 v178, s[12:15], s66 offen offset:0 lds\n"
      "buffer_load_dwordx4 v179, s[12:15], s66 offen offset:1024 lds\n"
      "buffer_load_dwordx4 v180, s[12:15], s66 offen offset:2048 lds\n"
      "s_sub_u32 s65, s65, 1\n"
      "s_cbranch_scc1 .Ld_re\n"
      ".Ld_rs18:\n"
      "s_add_u32 s66, s44, 36864\n"
      "s_cmp_ge_u32 s65, 7\n"
      "s_cselect_b32 s66, s66, s31\n"
      "s_add_u32 m0, s42, 12288\n"
      "s_waitcnt vmcnt(18) lgkmcnt(0)\n"
      "s_barrier\n"
      "ds_read_b64_tr_b16 a[72:73], v176 offset:24576\n"
      "ds_read_b64_tr_b16 a[74:75], v176 offset:26112\n"
      "ds_read_b64_tr_b16 a[168:169], v176 offset:24584\n"
      "ds_read_b64_tr_b16 a[170:171], v176 offset:26120\n"
      "ds_read_b64_tr_b16 v[32:33], v177 offset:24576\n"
      "ds_read_b64_tr_b16 v[34:35], v177 offset:26112\n"
      "buffer_load_dwordx4 v178, s[12:15], s66 offen offset:0 lds\n"
      "buffer_load_dwordx4 v179, s[12:15], s66 offen offset:1024 lds\n"
      "buffer_load_dwordx4 v180, s[12:15], s66 offen offset:2048 lds\n"
      "s_sub_u32 s65, s65, 1\n"
      ".Ld_rs19:\n"
      "s_add_u32 s66, s44, 73728\n"
      "s_cmp_ge_u32 s65, 7\n"
      "s_cselect_b32 s66, s66, s31\n"
      "s_add_u32 m0, s42, 24576\n"
      "s_waitcnt vmcnt(18) lgkmcnt(0)\n"
      "s_barrier\n"
      "ds_read_b64_tr_b16 a[76:77], v176 offset:36864\n"
      "ds_read_b64_tr_b16 a[78:79], v176 offset:38400\n"
      "ds_read_b64_tr_b16 a[172:173], v176 offset:36872\n"
      "ds_read_b64_tr_b16 a[174:175], v176 offset:38408\n"
      "ds_read_b64_tr_b16 v[36:37], v177 offset:36864\n"
      "ds_read_b64_tr_b16 v[38:39], v177 offset:38400\n"
      "buffer_load_dwordx4 v178, s[12:15], s66 offen offset:0 lds\n"
      "buffer_load_dwordx4 v179, s[12:15], s66 offen offset:1024 lds\n"
      "buffer_load_dwordx4 v180, s[12:15], s66 offen offset:2048 lds\n"
      "s_sub_u32 s65, s65, 1\n"
      ".Ld_rs20:\n"
      "s_add_u32 s66, s44, 110592\n"
      "s_cmp_ge_u32 s65, 7\n"
      "s_cselect_b32 s66, s66, s31\n"
      "s_add_u32 m0, s42, 36864\n"
      "s_waitcnt vmcnt(18) lgkmcnt(0)\n"
      "s_barrier\n"
      "ds_read_b64_tr_b16 a[80:81], v20 offset:0\n"
      "ds_read_b64_tr_b16 a[82:83], v20 offset:1536\n"
      "ds_read_b64_tr_b16 a[176:177], v20 offset:8\n"
      "ds_read_b64_tr_b16 a[178:179], v20 offset:1544\n"
      "ds_read_b64_tr_b16 v[40:41], v21 offset:0\n"
      "ds_read_b64_tr_b16 v[42:43], v21 offset:1536\n"
      "buffer_load_dwordx4 v178, s[12:15], s66 offen offset:0 lds\n"
      "buffer_load_dwordx4 v179, s[12:15], s66 offen offset:1024 lds\n"
      "buffer_load_dwordx4 v180, s[12:15], s66 offen offset:2048 lds\n"
      "s_sub_u32 s65, s65, 1\n"
      ".Ld_rs21:\n"
      "s_add_u32 s66, s44, 147456\n"
      "s_cmp_ge_u32 s65, 7\n"
      "s_cselect_b32 s66, s66, s31\n"
      "s_add_u32 m0, s42, 49152\n"
      "s_waitcnt vmcnt(18) lgkmcnt(0)\n"
      "s_barrier\n"
      "ds_read_b64_tr_b16 a[84:85], v20 offset:12288\n"
      "ds_read_b64_tr_b16 a[86:87], v20 offset:13824\n"
      "ds_read_b64_tr_b16 a[180:181], v20 offset:12296\n"
      "ds_read_b64_tr_b16 a[182:183], v20 offset:13832\n"
      "ds_read_b64_tr_b16 v[44:45], v21 offset:12288\n"
      "ds_read_b64_tr_b16 v[46:47], v21 offset:13824\n"
      "buffer_load_dwordx4 v178, s[12:15], s66 offen offset:0 lds\n"
      "buffer_load_dwordx4 v179, s[12:15], s66 offen offset:1024 lds\n"
      "buffer_load_dwordx4 v180, s[12:15], s66 offen offset:2048 lds\n"
      "s_sub_u32 s65, s65, 1\n"
      ".Ld_rs22:\n"
      "s_add_u32 s66, s44, 184320\n"
      "s_cmp_ge_u32 s65, 7\n"
      "s_cselect_b32 s66, s66, s31\n"
      "s_add_u32 m0, s42, 61440\n"
      "s_waitcnt vmcnt(18) lgkmcnt(0)\n"
      "s_barrier\n"
      "ds_read_b64_tr_b16 a[88:89], v20 offset:24576\n"
      "ds_read_b64_tr_b16 a[90:91], v20 offset:26112\n"
      "ds_read_b64_tr_b16 a[184:185], v20 offset:24584\n"
      "ds_read_b64_tr_b16 a[186:187], v20 offset:26120\n"
      "ds_read_b64_tr_b16 v[48:49], v21 offset:24576\n"
      "ds_read_b64_tr_b16 v[50:51], v21 offset:26112\n"
      "buffer_load_dwordx4 v178, s[12:15], s66 offen offset:0 lds\n"
      "buffer_load_dwordx4 v179, s[12:15], s66 offen offset:1024 lds\n"
      "buffer_load_dwordx4 v180, s[12:15], s66 offen offset:2048 lds\n"
      "s_sub_u32 s65, s65, 1\n"
      ".Ld_rs23:\n"
      "s_add_u32 s66, s44, 221184\n"
      "s_cmp_ge_u32 s65, 7\n"
      "s_cselect_b32 s66, s66, s31\n"
      "s_add_u32 m0, s42, 73728\n"
      "s_waitcnt vmcnt(18) lgkmcnt(0)\n"
      "s_barrier\n"
      "ds_read_b64_tr_b16 a[92:93], v20 offset:36864\n"
      "ds_read_b64_tr_b16 a[94:95], v20 offset:38400\n"
      "ds_read_b64_tr_b16 a[188:189], v20 offset:36872\n"
      "ds_read_b64_tr_b16 a[190:191], v20 offset:38408\n"
      "ds_read_b64_tr_b16 v[52:53], v21 offset:36864\n"
      "ds_read_b64_tr_b16 v[54:55], v21 offset:38400\n"
      "buffer_load_dwordx4 v178, s[12:15], s66 offen offset:0 lds\n"
      "buffer_load_dwordx4 v179, s[12:15], s66 offen offset:1024 lds\n"
      "buffer_load_dwordx4 v180, s[12:15], s66 offen offset:2048 lds\n"
      "s_sub_u32 s65, s65, 1\n"
      "s_cbranch_scc0 .Ld_rs0\n"
      ".Ld_re:\n"
      "s_waitcnt vmcnt(0) lgkmcnt(0)\n"
      "v_add_u32 v22, 49152, v0\n"
      "v_add_u32 v23, 98304, v0\n"
      "s_add_u32 s61, s48, 16515072\n"
      "buffer_store_dwordx4 v[216:219], v6, s[8:11], s61 offen sc1\n"
      "buffer_store_dwordx2 v[216:217], v7, s[8:11], s61 offen sc1\n"
      "buffer_store_dwordx2 v[216:217], v182, s[8:11], s61 offen sc1\n"
      "s_add_u32 s61, s48, 16662528\n"
      "buffer_store_dwordx4 v[216:219], v6, s[8:11], s61 offen sc1\n"
      "buffer_store_dwordx2 v[216:217], v7, s[8:11], s61 offen sc1\n"
      "buffer_store_dwordx2 v[216:217], v182, s[8:11], s61 offen sc1\n"
      "s_add_u32 s61, s48, 16809984\n"
      "buffer_store_dwordx4 v[216:219], v6, s[8:11], s61 offen sc1\n"
      "buffer_store_dwordx2 v[216:217], v7, s[8:11], s61 offen sc1\n"
      "buffer_store_dwordx2 v[216:217], v182, s[8:11], s61 offen sc1\n"
      "s_add_u32 s61, s48, 16957440\n"
      "buffer_store_dwordx4 v[216:219], v6, s[8:11], s61 offen sc1\n"
      "buffer_store_dwordx2 v[216:217], v7, s[8:11], s61 offen sc1\n"
      "buffer_store_dwordx2 v[216:217], v182, s[8:11], s61 offen sc1\n"
      "s_add_u32 s61, s48, 17104896\n"
      "buffer_store_dwordx4 v[216:219], v6, s[8:11], s61 offen sc1\n"
      "buffer_store_dwordx2 v[216:217], v7, s[8:11], s61 offen sc1\n"
      "buffer_store_dwordx2 v[216:217], v182, s[8:11], s61 offen sc1\n"
      "s_add_u32 s61, s49, 16515072\n"
      "buffer_store_dwordx4 v[216:219], v6, s[8:11], s61 offen sc1\n"
      "buffer_store_dwordx2 v[216:217], v7, s[8:11], s61 offen sc1\n"
      "buffer_store_dwordx2 v[216:217], v182, s[8:11], s61 offen sc1\n"
      "s_lshr_b32 s61, s42, 10\n"
      "s_mul_i32 s61, s61, 1366\n"
      "s_and_b32 s61, s61, 0x7000\n"
      "s_getpc_b64 vcc\n"
      ".Ld_ct:\n"
      "s_add_u32 vcc_lo, vcc_lo, .Ld_p0-.Ld_ct\n"
      "s_addc_u32 vcc_hi, vcc_hi, 0\n"
      "s_lshl_b32 s30, s61, 1\n"
      "v_lshl_add_u32 v56, v0, 3, s30\n"
      "v_min_u32 v56, .Ld_last-.Ld_p0-4, v56\n"
      "global_load_dword v56, v56, vcc\n"
      "s_waitcnt vmcnt(0) lgkmcnt(0)\n"
      "s_mov_b64 exec, 0\n"
      "s_lshr_b32 s22, s61, 12\n"
      ".Ld_pl:\n"
      "s_cmp_eq_u32 s22, 0\n"
      "s_cbranch_scc1 .Ld_p0\n"
      "s_cmp_eq_u32 s22, 1\n"
      "s_cbranch_scc1 .Ld_p1\n"
      "s_branch .Ld_pe\n"
      ".Ld_pr:\n"
      "s_add_u32 s22, s22, 4\n"
      "s_branch .Ld_pl\n"
      ".Ld_pe:\n"
      "s_mov_b64 exec, -1\n"
      "s_barrier\n"
      "v_mov_b32 v14, 0\n"
      "ds_write_b32 v16, v14\n"
      "s_waitcnt lgkmcnt(0)\n"
      "s_barrier\n"
      "s_mov_b32 s6, s50\n"
      "s_add_u32 s36, s36, 4294801408\n"
      "s_add_u32 s56, s37, 0\n"
      "s_add_u32 s55, s37, 4294787584\n"
      "s_add_u32 s37, s37, 4294894080\n"
      "s_mov_b32 s59, 0x40000000\n"
      "s_mov_b32 s60, 0x40000000\n"
      "s_mov_b32 s62, 63\n"
      "s_cmp_eq_u32 s45, 1\n"
      "s_cbranch_scc1 .Ld_in1\n"
      "s_cmp_eq_u32 s45, 2\n"
      "s_cbranch_scc1 .Ld_in2\n"
      "s_cmp_eq_u32 s45, 3\n"
      "s_cbranch_scc1 .Ld_in3\n"
      "s_cmp_eq_u32 s45, 4\n"
      "s_cbranch_scc1 .Ld_in4\n"
      "s_cmp_eq_u32 s45, 5\n"
      "s_cbranch_scc1 .Ld_in5\n"
      "s_add_u32 s36, s36, 165888\n"
      "s_min_u32 s36, s36, s6\n"
      "buffer_load_dword v20, v2, s[8:11], s36 offen sc1\n"
      "s_add_u32 m0, s43, 0\n"
      "s_mov_b32 s54, s36\n"
      "s_add_u32 s36, s36, 165888\n"
      "s_min_u32 s36, s36, s6\n"
      "s_waitcnt vmcnt(0)\n"
      "v_cmp_ne_u32 vcc, s21, v20\n"
      "s_cbranch_vccnz .Lsp_d0_1\n"
      ".Lbk_d0_1:\n"
      "buffer_load_dwordx4 v0, s[8:11], s54 offen offset:0 sc1 lds\n"
      "buffer_load_dwordx4 v0, s[8:11], s54 offen offset:1024 sc1 lds\n"
      "buffer_load_dword v20, v2, s[8:11], s36 offen sc1\n"
      "buffer_load_dwordx4 v0, s[8:11], s54 offen offset:2048 sc1 lds\n"
      "buffer_load_dwordx4 v0, s[8:11], s54 offen offset:3072 sc1 lds\n"
      "s_add_u32 m0, s43, 4096\n"
      "s_nop 0\n"
      "buffer_load_dwordx4 v1, s[8:11], s54 offen offset:1024 sc1 lds\n"
      "buffer_load_dwordx4 v1, s[8:11], s54 offen offset:0 sc1 lds\n"
      "s_mov_b32 s54, s36\n"
      "s_add_u32 m0, s43, 24576\n"
      "v_add_u32 v14, 1, v14\n"
      "s_add_u32 s36, s36, 165888\n"
      "buffer_load_dwordx4 v[216:219], v6, s[8:11], s38 offen sc1\n"
      "s_waitcnt vmcnt(5)\n"
      "v_cmp_ne_u32 vcc, s21, v20\n"
      "buffer_load_dwordx2 v[220:221], v7, s[8:11], s38 offen sc1\n"
      "s_cbranch_vccnz .Lsp_d1_2\n"
      ".Lbk_d1_2:\n"
      "buffer_load_dwordx4 v0, s[8:11], s54 offen offset:0 sc1 lds\n"
      "buffer_load_dwordx4 v0, s[8:11], s54 offen offset:1024 sc1 lds\n"
      "s_waitcnt vmcnt(4)\n"
      "ds_write_b32 v16, v14\n"
      "buffer_load_dwordx4 v0, s[8:11], s54 offen offset:2048 sc1 lds\n"
      "s_min_u32 s36, s36, s6\n"
      "buffer_load_dword v20, v2, s[8:11], s36 offen sc1\n"
      "buffer_load_dwordx4 v0, s[8:11], s54 offen offset:3072 sc1 lds\n"
      "ds_read_b32 v15, v17\n"
      "s_add_u32 m0, s43, 28672\n"
      "s_nop 0\n"
      "buffer_load_dwordx4 v1, s[8:11], s54 offen offset:0 sc1 lds\n"
      "s_waitcnt lgkmcnt(0)\n"
      "v_cmp_gt_i32 vcc, v14, v15\n"
      "buffer_load_dwordx4 v1, s[8:11], s54 offen offset:1024 sc1 lds\n"
      "s_cbranch_vccnz .Lsp_dy0_3\n"
      ".Lbk_dy0_3:\n"
      "ds_read_b128 v[56:59], v0 offset:0\n"
      "ds_read_b128 v[60:63], v0 offset:1024\n"
      "ds_read_b128 v[84:87], v0 offset:7168\n"
      "ds_read_b128 v[72:75], v0 offset:4096\n"
      "ds_read_b128 v[88:91], v0 offset:8192\n"
      "ds_read_b128 v[68:71], v0 offset:3072\n"
      "ds_read_b128 v[104:107], v0 offset:12288\n"
      "ds_read_b128 v[96:99], v0 offset:10240\n"
      "ds_read_b128 v[80:83], v0 offset:6144\n"
      "ds_read_b128 v[64:67], v0 offset:2048\n"
      "ds_read_b128 v[92:95], v0 offset:9216\n"
      "ds_read_b128 v[76:79], v0 offset:5120\n"
      "ds_read_b128 v[132:135], v0 offset:19456\n"
      "ds_read_b128 v[100:103], v0 offset:11264\n"
      "s_waitcnt vmcnt(0) lgkmcnt(0)\n"
      "s_nop 4\n"
      "s_branch .Ld_t0\n"
      ".p2align 12\n"
      ".Ld_p0:\n"
      "s_branch .Ld_pr\n"
      ".p2align 5\n"
      ".Ld_t0:\n"
      "v_mfma_f32_16x16x32_bf16 v[152:155], a[0:3], v[56:59], 0\n"
      "ds_read_b128 v[108:111], v0 offset:13312\n"
      "ds_read_b128 v[120:123], v0 offset:16384\n"
      "v_mfma_f32_16x16x32_bf16 v[152:155], a[4:7], v[60:63], v[152:155]\n"
      "s_waitcnt lgkmcnt(6)\n"
      "ds_read_b128 v[116:119], v0 offset:15360\n"
      "v_add_f32 v236, v236, v168\n"
      "v_mfma_f32_16x16x32_bf16 v[152:155], a[8:11], v[64:67], v[152:155]\n"
      "v_cvt_pk_bf16_f32 v228, v160, v161\n"
      "v_cvt_pk_bf16_f32 v229, v162, v163\n"
      "v_mfma_f32_16x16x32_bf16 v[152:155], a[12:15], v[68:71], v[152:155]\n"
      "buffer_store_dwordx2 v[228:229], v13, s[8:11], s37 offen sc1\n"
      "v_mfma_f32_16x16x32_bf16 v[152:155], a[16:19], v[72:75], v[152:155]\n"
      "s_waitcnt lgkmcnt(4)\n"
      "v_cvt_pk_bf16_f32 v178, v168, v169\n"
      "v_add_f32 v175, v175, v213\n"
      "v_mfma_f32_16x16x32_bf16 v[152:155], a[20:23], v[76:79], v[152:155]\n"
      "ds_read_b128 v[112:115], v0 offset:14336\n"
      "v_add_f32 v171, v171, v211\n"
      "v_mfma_f32_16x16x32_bf16 v[152:155], a[24:27], v[80:83], v[152:155]\n"
      "ds_read_b128 v[124:127], v0 offset:17408\n"
      "v_add_f32 v236, v236, v169\n"
      "v_mfma_f32_16x16x32_bf16 v[152:155], a[28:31], v[84:87], v[152:155]\n"
      "v_add_f32 v173, v173, v204\n"
      "v_cvt_pk_bf16_f32 v179, v170, v171\n"
      "v_mfma_f32_16x16x32_bf16 v[152:155], a[32:35], v[88:91], v[152:155]\n"
      "ds_read_b128 v[128:131], v0 offset:18432\n"
      "v_add_f32 v173, v173, v212\n"
      "v_mfma_f32_16x16x32_bf16 v[152:155], a[36:39], v[92:95], v[152:155]\n"
      "buffer_store_dwordx4 v[176:179], v6, s[8:11], s59 offen sc1\n"
      "v_mfma_f32_16x16x32_bf16 v[152:155], a[40:43], v[96:99], v[152:155]\n"
      "s_waitcnt lgkmcnt(3)\n"
      "ds_read_b128 v[148:151], v0 offset:23552\n"
      "v_cvt_pk_bf16_f32 v180, v172, v173\n"
      "v_mfma_f32_16x16x32_bf16 v[152:155], a[44:47], v[100:103], v[152:155]\n"
      "ds_read_b128 v[144:147], v0 offset:22528\n"
      "v_fmac_f32 v237, v168, v168\n"
      "v_mfma_f32_16x16x32_bf16 v[152:155], a[48:51], v[104:107], v[152:155]\n"
      "s_add_u32 m0, s43, 49152\n"
      "ds_read_b128 v[136:139], v0 offset:20480\n"
      "v_fmac_f32 v237, v169, v169\n"
      "v_mfma_f32_16x16x32_bf16 v[152:155], a[52:55], v[108:111], v[152:155]\n"
      "s_waitcnt lgkmcnt(4)\n"
      "s_mov_b32 s54, s36\n"
      "ds_read_b128 v[140:143], v0 offset:21504\n"
      "v_mfma_f32_16x16x32_bf16 v[152:155], a[56:59], v[112:115], v[152:155]\n"
      "v_cvt_pk_bf16_f32 v181, v174, v175\n"
      "v_fmac_f32 v237, v170, v170\n"
      "v_mfma_f32_16x16x32_bf16 v[152:155], a[60:63], v[116:119], v[152:155]\n"
      "buffer_store_dwordx2 v[180:181], v7, s[8:11], s59 offen sc1\n"
      "v_mfma_f32_16x16x32_bf16 v[152:155], a[64:67], v[120:123], v[152:155]\n"
      "v_add_u32 v14, 1, v14\n"
      "v_mfma_f32_16x16x32_bf16 v[152:155], a[68:71], v[124:127], v[152:155]\n"
      "s_waitcnt lgkmcnt(3)\n"
      "v_add_f32 v236, v236, v170\n"
      "v_fmac_f32 v237, v171, v171\n"
      "v_mfma_f32_16x16x32_bf16 v[152:155], a[72:75], v[128:131], v[152:155]\n"
      "v_fmac_f32 v237, v172, v172\n"
      "v_fmac_f32 v237, v173, v173\n"
      "v_mfma_f32_16x16x32_bf16 v[152:155], a[76:79], v[132:135], v[152:155]\n"
      "s_waitcnt lgkmcnt(1)\n"
      "buffer_store_dwordx2 v[180:181], v9, s[16:19], s60 offen\n"
      "v_mfma_f32_16x16x32_bf16 v[152:155], a[80:83], v[136:139], v[152:155]\n"
      "s_waitcnt lgkmcnt(0)\n"
      "v_add_f32 v236, v236, v171\n"
      "v_fmac_f32 v237, v174, v174\n"
      "v_mfma_f32_16x16x32_bf16 v[152:155], a[84:87], v[140:143], v[152:155]\n"
      "v_add_f32 v236, v236, v172\n"
      "v_fmac_f32 v237, v175, v175\n"
      "v_mfma_f32_16x16x32_bf16 v[152:155], a[88:91], v[144:147], v[152:155]\n"
      "v_add_f32 v236, v236, v173\n"
      "v_add_f32 v236, v236, v174\n"
      "v_mfma_f32_16x16x32_bf16 v[152:155], a[92:95], v[148:151], v[152:155]\n"
      "buffer_store_dwordx4 v[176:179], v8, s[16:19], s60 offen\n"
      "v_mfma_f32_16x16x32_bf16 v[156:159], a[96:99], v[56:59], 0\n"
      "s_waitcnt vmcnt(15)\n"
      "v_add_f32 v236, v236, v175\n"
      "v_lshlrev_b32 v166, 16, v217\n"
      "v_mfma_f32_16x16x32_bf16 v[156:159], a[100:103], v[60:63], v[156:159]\n"
      "s_waitcnt vmcnt(10)\n"
      "v_cmp_ne_u32 vcc, s21, v20\n"
      "v_mfma_f32_16x16x32_bf16 v[156:159], a[104:107], v[64:67], v[156:159]\n"
      "ds_write_b64 v10, v[236:237]\n"
      "v_mfma_f32_16x16x32_bf16 v[156:159], a[108:111], v[68:71], v[156:159]\n"
      "s_add_u32 s36, s36, 165888\n"
      "v_mfma_f32_16x16x32_bf16 v[156:159], a[112:115], v[72:75], v[156:159]\n"
      "s_min_u32 s36, s36, s6\n"
      "s_cbranch_vccnz .Lsp_d2_4\n"
      ".Lbk_d2_4:\n"
      "v_mfma_f32_16x16x32_bf16 v[156:159], a[116:119], v[76:79], v[156:159]\n"
      "s_waitcnt vmcnt(7)\n"
      "ds_write_b32 v16, v14\n"
      "v_mfma_f32_16x16x32_bf16 v[156:159], a[120:123], v[80:83], v[156:159]\n"
      "buffer_load_dword v20, v2, s[8:11], s36 offen sc1\n"
      "v_mfma_f32_16x16x32_bf16 v[156:159], a[124:127], v[84:87], v[156:159]\n"
      "v_and_b32 v167, 0xffff0000, v217\n"
      "v_lshlrev_b32 v164, 16, v216\n"
      "v_mfma_f32_16x16x32_bf16 v[156:159], a[128:131], v[88:91], v[156:159]\n"
      "v_mfma_f32_16x16x32_bf16 v[156:159], a[132:135], v[92:95], v[156:159]\n"
      "buffer_load_dwordx4 v0, s[8:11], s54 offen offset:3072 sc1 lds\n"
      "v_mfma_f32_16x16x32_bf16 v[156:159], a[136:139], v[96:99], v[156:159]\n"
      "s_add_u32 s55, s55, 106496\n"
      "v_mfma_f32_16x16x32_bf16 v[156:159], a[140:143], v[100:103], v[156:159]\n"
      "v_mfma_f32_16x16x32_bf16 v[156:159], a[144:147], v[104:107], v[156:159]\n"
      "s_add_u32 s37, s37, 173056\n"
      "v_and_b32 v165, 0xffff0000, v216\n"
      "v_mfma_f32_16x16x32_bf16 v[156:159], a[148:151], v[108:111], v[156:159]\n"
      "buffer_load_dwordx4 v0, s[8:11], s54 offen offset:1024 sc1 lds\n"
      "v_mfma_f32_16x16x32_bf16 v[156:159], a[152:155], v[112:115], v[156:159]\n"
      "ds_read_b32 v15, v17\n"
      "v_mfma_f32_16x16x32_bf16 v[156:159], a[156:159], v[116:119], v[156:159]\n"
      "v_mfma_f32_16x16x32_bf16 v[156:159], a[160:163], v[120:123], v[156:159]\n"
      "v_lshlrev_b32 v170, 16, v219\n"
      "v_mfma_f32_16x16x32_bf16 v[156:159], a[164:167], v[124:127], v[156:159]\n"
      "buffer_load_dwordx4 v0, s[8:11], s54 offen offset:2048 sc1 lds\n"
      "v_mfma_f32_16x16x32_bf16 v[156:159], a[168:171], v[128:131], v[156:159]\n"
      "v_mfma_f32_16x16x32_bf16 v[156:159], a[172:175], v[132:135], v[156:159]\n"
      "s_waitcnt lgkmcnt(0)\n"
      "v_cmp_gt_i32 vcc, v14, v15\n"
      "v_mfma_f32_16x16x32_bf16 v[156:159], a[176:179], v[136:139], v[156:159]\n"
      "v_add_f32 v165, v165, v153\n"
      "v_mfma_f32_16x16x32_bf16 v[156:159], a[180:183], v[140:143], v[156:159]\n"
      "buffer_load_dwordx4 v0, s[8:11], s54 offen offset:0 sc1 lds\n"
      "v_mfma_f32_16x16x32_bf16 v[156:159], a[184:187], v[144:147], v[156:159]\n"
      "v_add_f32 v164, v164, v152\n"
      "s_add_u32 m0, s43, 53248\n"
      "v_mfma_f32_16x16x32_bf16 v[156:159], a[188:191], v[148:151], v[156:159]\n"
      "s_cbranch_vccnz .Lsp_dy1_5\n"
      ".Lbk_dy1_5:\n"
      "ds_read_b64 v[238:239], v11\n"
      "v_mfma_f32_16x16x32_bf16 v[160:163], a[192:195], v[56:59], 0\n"
      "v_lshlrev_b32 v168, 16, v218\n"
      "v_mfma_f32_16x16x32_bf16 v[160:163], a[196:199], v[60:63], v[160:163]\n"
      "buffer_load_dwordx4 v1, s[8:11], s54 offen offset:0 sc1 lds\n"
      "v_mfma_f32_16x16x32_bf16 v[160:163], a[200:203], v[64:67], v[160:163]\n"
      "v_mfma_f32_16x16x32_bf16 v[160:163], a[204:207], v[68:71], v[160:163]\n"
      "ds_read_b128 v[56:59], v0 offset:24576\n"
      "v_mfma_f32_16x16x32_bf16 v[160:163], a[208:211], v[72:75], v[160:163]\n"
      "ds_read_b128 v[64:67], v0 offset:26624\n"
      "v_mfma_f32_16x16x32_bf16 v[160:163], a[212:215], v[76:79], v[160:163]\n"
      "buffer_load_dwordx4 v1, s[8:11], s54 offen offset:1024 sc1 lds\n"
      "v_mfma_f32_16x16x32_bf16 v[160:163], a[216:219], v[80:83], v[160:163]\n"
      "s_waitcnt lgkmcnt(2)\n"
      "ds_read_b128 v[68:71], v0 offset:27648\n"
      "v_add_f32_dpp v238, v238, v238 row_ror:8 row_mask:0xf bank_mask:0xf\n"
      "v_mfma_f32_16x16x32_bf16 v[160:163], a[220:223], v[84:87], v[160:163]\n"
      "ds_read_b128 v[80:83], v0 offset:30720\n"
      "v_add_f32_dpp v238, v238, v238 row_ror:4 row_mask:0xf bank_mask:0xf\n"
      "v_mfma_f32_16x16x32_bf16 v[160:163], a[224:227], v[88:91], v[160:163]\n"
      "ds_read_b128 v[60:63], v0 offset:25600\n"
      "v_add_f32_dpp v238, v238, v238 row_ror:2 row_mask:0xf bank_mask:0xf\n"
      "v_mfma_f32_16x16x32_bf16 v[160:163], a[228:231], v[92:95], v[160:163]\n"
      "ds_read_b128 v[92:95], v0 offset:33792\n"
      "v_add_f32_dpp v239, v239, v239 row_ror:8 row_mask:0xf bank_mask:0xf\n"
      "v_mfma_f32_16x16x32_bf16 v[160:163], a[232:235], v[96:99], v[160:163]\n"
      "ds_read_b128 v[84:87], v0 offset:31744\n"
      "v_add_f32_dpp v239, v239, v239 row_ror:4 row_mask:0xf bank_mask:0xf\n"
      "v_mfma_f32_16x16x32_bf16 v[160:163], a[236:239], v[100:103], v[160:163]\n"
      "s_waitcnt vmcnt(11) lgkmcnt(5)\n"
      "buffer_store_dword v18, v4, s[8:11], s55 offen sc1\n"
      "v_mfma_f32_16x16x32_bf16 v[160:163], a[240:243], v[104:107], v[160:163]\n"
      "ds_read_b128 v[72:75], v0 offset:28672\n"
      "v_add_f32_dpp v239, v239, v239 row_ror:2 row_mask:0xf bank_mask:0xf\n"
      "v_mfma_f32_16x16x32_bf16 v[160:163], a[244:247], v[108:111], v[160:163]\n"
      "ds_read_b128 v[76:79], v0 offset:29696\n"
      "v_add_f32_dpp v239, v239, v239 row_ror:1 row_mask:0xf bank_mask:0xf\n"
      "v_mfma_f32_16x16x32_bf16 v[160:163], a[248:251], v[112:115], v[160:163]\n"
      "v_add_f32_dpp v238, v238, v238 row_ror:1 row_mask:0xf bank_mask:0xf\n"
      "v_add_f32 v168, v168, v156\n"
      "v_mfma_f32_16x16x32_bf16 v[160:163], a[252:255], v[116:119], v[160:163]\n"
      "ds_read_b128 v[100:103], v0 offset:35840\n"
      "v_mul_f32 v238, 0x3ae38e39, v238\n"
      "v_mfma_f32_16x16x32_bf16 v[160:163], v[24:27], v[120:123], v[160:163]\n"
      "v_and_b32 v169, 0xffff0000, v218\n"
      "v_mfma_f32_16x16x32_bf16 v[160:163], v[28:31], v[124:127], v[160:163]\n"
      "ds_read_b128 v[96:99], v0 offset:34816\n"
      "v_and_b32 v175, 0xffff0000, v221\n"
      "v_mfma_f32_16x16x32_bf16 v[160:163], v[32:35], v[128:131], v[160:163]\n"
      "ds_read_b128 v[116:119], v0 offset:39936\n"
      "v_lshlrev_b32 v174, 16, v221\n"
      "v_mfma_f32_16x16x32_bf16 v[160:163], v[36:39], v[132:135], v[160:163]\n"
      "ds_read_b128 v[120:123], v0 offset:40960\n"
      "v_add_f32 v167, v167, v155\n"
      "v_mfma_f32_16x16x32_bf16 v[160:163], v[40:43], v[136:139], v[160:163]\n"
      "ds_read_b128 v[108:111], v0 offset:37888\n"
      "v_and_b32 v171, 0xffff0000, v219\n"
      "v_mfma_f32_16x16x32_bf16 v[160:163], v[44:47], v[140:143], v[160:163]\n"
      "ds_read_b128 v[88:91], v0 offset:32768\n"
      "v_add_f32 v166, v166, v154\n"
      "v_mfma_f32_16x16x32_bf16 v[160:163], v[48:51], v[144:147], v[160:163]\n"
      "v_add_f32 v239, 0x3afba882, v239\n"
      "v_mul_f32 v239, 0x3ae38e39, v239\n"
      "v_mfma_f32_16x16x32_bf16 v[160:163], v[52:55], v[148:151], v[160:163]\n"
      "s_sub_u32 s62, s62, 1\n"
      "s_cbranch_scc1 .Ld_out0\n"
      ".Ld_t1:\n"
      "v_mfma_f32_16x16x32_bf16 v[152:155], a[0:3], v[56:59], 0\n"
      "s_waitcnt lgkmcnt(4)\n"
      "buffer_store_dwordx2 v[238:239], v12, s[8:11], s59 offen sc1\n"
      "v_mfma_f32_16x16x32_bf16 v[152:155], a[4:7], v[60:63], v[152:155]\n"
      "ds_read_b128 v[104:107], v0 offset:36864\n"
      "v_mfma_f32_16x16x32_bf16 v[152:155], a[8:11], v[64:67], v[152:155]\n"
      "ds_read_b128 v[140:143], v0 offset:46080\n"
      "v_mfma_f32_16x16x32_bf16 v[152:155], a[12:15], v[68:71], v[152:155]\n"
      "ds_read_b128 v[112:115], v0 offset:38912\n"
      "v_mfma_f32_16x16x32_bf16 v[152:155], a[16:19], v[72:75], v[152:155]\n"
      "ds_read_b128 v[124:127], v0 offset:41984\n"
      "v_mfma_f32_16x16x32_bf16 v[152:155], a[20:23], v[76:79], v[152:155]\n"
      "ds_read_b128 v[132:135], v0 offset:44032\n"
      "v_mfma_f32_16x16x32_bf16 v[152:155], a[24:27], v[80:83], v[152:155]\n"
      "s_mov_b32 s54, s36\n"
      "v_add_f32 v174, v174, v162\n"
      "s_add_u32 s36, s36, 165888\n"
      "v_mfma_f32_16x16x32_bf16 v[152:155], a[28:31], v[84:87], v[152:155]\n"
      "s_waitcnt lgkmcnt(5)\n"
      "ds_read_b128 v[148:151], v0 offset:48128\n"
      "v_mfma_f32_16x16x32_bf16 v[152:155], a[32:35], v[88:91], v[152:155]\n"
      "ds_read_b128 v[128:131], v0 offset:43008\n"
      "s_add_u32 m0, s43, 73728\n"
      "v_mfma_f32_16x16x32_bf16 v[152:155], a[36:39], v[92:95], v[152:155]\n"
      "ds_read_b128 v[136:139], v0 offset:45056\n"
      "v_mfma_f32_16x16x32_bf16 v[152:155], a[40:43], v[96:99], v[152:155]\n"
      "v_mfma_f32_16x16x32_bf16 v[152:155], a[44:47], v[100:103], v[152:155]\n"
      "s_waitcnt lgkmcnt(4)\n"
      "v_add_f32 v170, v170, v158\n"
      "v_mfma_f32_16x16x32_bf16 v[152:155], a[48:51], v[104:107], v[152:155]\n"
      "v_mfma_f32_16x16x32_bf16 v[152:155], a[52:55], v[108:111], v[152:155]\n"
      "s_waitcnt vmcnt(8) lgkmcnt(3)\n"
      "v_lshlrev_b32 v172, 16, v220\n"
      "v_cmp_ne_u32 vcc, s21, v20\n"
      "v_mfma_f32_16x16x32_bf16 v[152:155], a[56:59], v[112:115], v[152:155]\n"
      "ds_read_b128 v[144:147], v0 offset:47104\n"
      "s_add_u32 s38, s38, 147456\n"
      "v_mfma_f32_16x16x32_bf16 v[152:155], a[60:63], v[116:119], v[152:155]\n"
      "s_min_u32 s36, s36, s6\n"
      "v_add_f32 v171, v171, v159\n"
      "v_mfma_f32_16x16x32_bf16 v[152:155], a[64:67], v[120:123], v[152:155]\n"
      "v_mfma_f32_16x16x32_bf16 v[152:155], a[68:71], v[124:127], v[152:155]\n"
      "s_waitcnt lgkmcnt(1)\n"
      "v_mfma_f32_16x16x32_bf16 v[152:155], a[72:75], v[128:131], v[152:155]\n"
      "v_add_f32 v169, v169, v157\n"
      "v_add_f32 v172, v172, v160\n"
      "v_mfma_f32_16x16x32_bf16 v[152:155], a[76:79], v[132:135], v[152:155]\n"
      "v_mfma_f32_16x16x32_bf16 v[152:155], a[80:83], v[136:139], v[152:155]\n"
      "s_cbranch_vccnz .Lsp_d3_6\n"
      ".Lbk_d3_6:\n"
      "v_mfma_f32_16x16x32_bf16 v[152:155], a[84:87], v[140:143], v[152:155]\n"
      "s_waitcnt lgkmcnt(0)\n"
      "v_mfma_f32_16x16x32_bf16 v[152:155], a[88:91], v[144:147], v[152:155]\n"
      "v_mfma_f32_16x16x32_bf16 v[152:155], a[92:95], v[148:151], v[152:155]\n"
      "buffer_load_dwordx4 v0, s[8:11], s54 offen offset:2048 sc1 lds\n"
      "v_mfma_f32_16x16x32_bf16 v[156:159], a[96:99], v[56:59], 0\n"
      "v_mfma_f32_16x16x32_bf16 v[156:159], a[100:103], v[60:63], v[156:159]\n"
      "v_add_u32 v14, 1, v14\n"
      "v_mfma_f32_16x16x32_bf16 v[156:159], a[104:107], v[64:67], v[156:159]\n"
      "v_cvt_pk_bf16_f32 v225, v154, v155\n"
      "v_mfma_f32_16x16x32_bf16 v[156:159], a[108:111], v[68:71], v[156:159]\n"
      "v_add_f32 v175, v175, v163\n"
      "v_mfma_f32_16x16x32_bf16 v[156:159], a[112:115], v[72:75], v[156:159]\n"
      "buffer_load_dwordx4 v0, s[8:11], s54 offen offset:1024 sc1 lds\n"
      "v_mfma_f32_16x16x32_bf16 v[156:159], a[116:119], v[76:79], v[156:159]\n"
      "v_mfma_f32_16x16x32_bf16 v[156:159], a[120:123], v[80:83], v[156:159]\n"
      "v_mfma_f32_16x16x32_bf16 v[156:159], a[124:127], v[84:87], v[156:159]\n"
      "v_and_b32 v173, 0xffff0000, v220\n"
      "v_mfma_f32_16x16x32_bf16 v[156:159], a[128:131], v[88:91], v[156:159]\n"
      "buffer_load_dword v20, v2, s[8:11], s36 offen sc1\n"
      "v_mfma_f32_16x16x32_bf16 v[156:159], a[132:135], v[92:95], v[156:159]\n"
      "s_waitcnt vmcnt(5)\n"
      "ds_write_b32 v16, v14\n"
      "v_mfma_f32_16x16x32_bf16 v[156:159], a[136:139], v[96:99], v[156:159]\n"
      "buffer_load_dwordx4 v0, s[8:11], s54 offen offset:0 sc1 lds\n"
      "v_mfma_f32_16x16x32_bf16 v[156:159], a[140:143], v[100:103], v[156:159]\n"
      "v_add_f32 v173, v173, v161\n"
      "v_mfma_f32_16x16x32_bf16 v[156:159], a[144:147], v[104:107], v[156:159]\n"
      "v_mfma_f32_16x16x32_bf16 v[156:159], a[148:151], v[108:111], v[156:159]\n"
      "v_mfma_f32_16x16x32_bf16 v[156:159], a[152:155], v[112:115], v[156:159]\n"
      "v_mfma_f32_16x16x32_bf16 v[156:159], a[156:159], v[116:119], v[156:159]\n"
      "buffer_load_dwordx4 v0, s[8:11], s54 offen offset:3072 sc1 lds\n"
      "v_mfma_f32_16x16x32_bf16 v[156:159], a[160:163], v[120:123], v[156:159]\n"
      "s_add_u32 m0, s43, 77824\n"
      "v_mfma_f32_16x16x32_bf16 v[156:159], a[164:167], v[124:127], v[156:159]\n"
      "v_mfma_f32_16x16x32_bf16 v[156:159], a[168:171], v[128:131], v[156:159]\n"
      "ds_read_b32 v15, v17\n"
      "v_mfma_f32_16x16x32_bf16 v[156:159], a[172:175], v[132:135], v[156:159]\n"
      "s_add_u32 s55, s55, 173056\n"
      "v_mfma_f32_16x16x32_bf16 v[156:159], a[176:179], v[136:139], v[156:159]\n"
      "buffer_load_dwordx4 v1, s[8:11], s54 offen offset:0 sc1 lds\n"
      "v_mfma_f32_16x16x32_bf16 v[156:159], a[180:183], v[140:143], v[156:159]\n"
      "v_mfma_f32_16x16x32_bf16 v[156:159], a[184:187], v[144:147], v[156:159]\n"
      "v_cvt_pk_bf16_f32 v224, v152, v153\n"
      "v_mfma_f32_16x16x32_bf16 v[156:159], a[188:191], v[148:151], v[156:159]\n"
      "s_waitcnt lgkmcnt(0)\n"
      "v_cmp_gt_i32 vcc, v14, v15\n"
      "v_mfma_f32_16x16x32_bf16 v[160:163], a[192:195], v[56:59], 0\n"
      "buffer_load_dwordx4 v1, s[8:11], s54 offen offset:1024 sc1 lds\n"
      "v_mfma_f32_16x16x32_bf16 v[160:163], a[196:199], v[60:63], v[160:163]\n"
      "v_mfma_f32_16x16x32_bf16 v[160:163], a[200:203], v[64:67], v[160:163]\n"
      "s_cbranch_vccnz .Lsp_dy2_7\n"
      ".Lbk_dy2_7:\n"
      "v_mfma_f32_16x16x32_bf16 v[160:163], a[204:207], v[68:71], v[160:163]\n"
      "ds_read_b128 v[68:71], v22 offset:3072\n"
      "v_mfma_f32_16x16x32_bf16 v[160:163], a[208:211], v[72:75], v[160:163]\n"
      "ds_read_b128 v[60:63], v22 offset:1024\n"
      "v_mfma_f32_16x16x32_bf16 v[160:163], a[212:215], v[76:79], v[160:163]\n"
      "s_waitcnt vmcnt(7)\n"
      "buffer_store_dword v18, v5, s[8:11], s59 offen sc1\n"
      "v_mfma_f32_16x16x32_bf16 v[160:163], a[216:219], v[80:83], v[160:163]\n"
      "v_cvt_pk_bf16_f32 v227, v158, v159\n"
      "v_mfma_f32_16x16x32_bf16 v[160:163], a[220:223], v[84:87], v[160:163]\n"
      "ds_read_b128 v[80:83], v22 offset:6144\n"
      "v_cvt_pk_bf16_f32 v226, v156, v157\n"
      "v_mfma_f32_16x16x32_bf16 v[160:163], a[224:227], v[88:91], v[160:163]\n"
      "ds_read_b128 v[72:75], v22 offset:4096\n"
      "v_mfma_f32_16x16x32_bf16 v[160:163], a[228:231], v[92:95], v[160:163]\n"
      "ds_read_b128 v[76:79], v22 offset:5120\n"
      "v_mfma_f32_16x16x32_bf16 v[160:163], a[232:235], v[96:99], v[160:163]\n"
      "ds_read_b128 v[96:99], v22 offset:10240\n"
      "v_mfma_f32_16x16x32_bf16 v[160:163], a[236:239], v[100:103], v[160:163]\n"
      "v_mfma_f32_16x16x32_bf16 v[160:163], a[240:243], v[104:107], v[160:163]\n"
      "buffer_store_dwordx4 v[224:227], v0, s[8:11], s37 offen sc1\n"
      "v_mfma_f32_16x16x32_bf16 v[160:163], a[244:247], v[108:111], v[160:163]\n"
      "v_mfma_f32_16x16x32_bf16 v[160:163], a[248:251], v[112:115], v[160:163]\n"
      "ds_read_b128 v[84:87], v22 offset:7168\n"
      "v_mfma_f32_16x16x32_bf16 v[160:163], a[252:255], v[116:119], v[160:163]\n"
      "v_mfma_f32_16x16x32_bf16 v[160:163], v[24:27], v[120:123], v[160:163]\n"
      "ds_read_b128 v[56:59], v22 offset:0\n"
      "v_mfma_f32_16x16x32_bf16 v[160:163], v[28:31], v[124:127], v[160:163]\n"
      "ds_read_b128 v[92:95], v22 offset:9216\n"
      "s_mov_b32 s54, s36\n"
      "v_mfma_f32_16x16x32_bf16 v[160:163], v[32:35], v[128:131], v[160:163]\n"
      "ds_read_b128 v[64:67], v22 offset:2048\n"
      "v_mfma_f32_16x16x32_bf16 v[160:163], v[36:39], v[132:135], v[160:163]\n"
      "ds_read_b128 v[100:103], v22 offset:11264\n"
      "v_mfma_f32_16x16x32_bf16 v[160:163], v[40:43], v[136:139], v[160:163]\n"
      "ds_read_b128 v[132:135], v22 offset:19456\n"
      "v_mfma_f32_16x16x32_bf16 v[160:163], v[44:47], v[140:143], v[160:163]\n"
      "v_mfma_f32_16x16x32_bf16 v[160:163], v[48:51], v[144:147], v[160:163]\n"
      "s_waitcnt lgkmcnt(4)\n"
      "ds_read_b128 v[112:115], v22 offset:14336\n"
      "s_add_u32 m0, s43, 98304\n"
      "v_mfma_f32_16x16x32_bf16 v[160:163], v[52:55], v[148:151], v[160:163]\n"
      "s_sub_u32 s62, s62, 1\n"
      "s_cbranch_scc1 .Ld_out1\n"
      ".Ld_t2:\n"
      "v_mfma_f32_16x16x32_bf16 v[152:155], a[0:3], v[56:59], 0\n"
      "ds_read_b128 v[88:91], v22 offset:8192\n"
      "v_mfma_f32_16x16x32_bf16 v[152:155], a[4:7], v[60:63], v[152:155]\n"
      "s_waitcnt lgkmcnt(4)\n"
      "ds_read_b128 v[120:123], v22 offset:16384\n"
      "v_mfma_f32_16x16x32_bf16 v[152:155], a[8:11], v[64:67], v[152:155]\n"
      "s_add_u32 s36, s36, 165888\n"
      "ds_read_b128 v[104:107], v22 offset:12288\n"
      "v_cvt_pk_bf16_f32 v229, v162, v163\n"
      "v_mfma_f32_16x16x32_bf16 v[152:155], a[12:15], v[68:71], v[152:155]\n"
      "ds_read_b128 v[148:151], v22 offset:23552\n"
      "v_cvt_pk_bf16_f32 v228, v160, v161\n"
      "v_mfma_f32_16x16x32_bf16 v[152:155], a[16:19], v[72:75], v[152:155]\n"
      "buffer_store_dwordx2 v[228:229], v13, s[8:11], s37 offen sc1\n"
      "v_mfma_f32_16x16x32_bf16 v[152:155], a[20:23], v[76:79], v[152:155]\n"
      "ds_read_b128 v[108:111], v22 offset:13312\n"
      "v_mfma_f32_16x16x32_bf16 v[152:155], a[24:27], v[80:83], v[152:155]\n"
      "ds_read_b128 v[116:119], v22 offset:15360\n"
      "v_mfma_f32_16x16x32_bf16 v[152:155], a[28:31], v[84:87], v[152:155]\n"
      "s_waitcnt lgkmcnt(5)\n"
      "ds_read_b128 v[124:127], v22 offset:17408\n"
      "v_mfma_f32_16x16x32_bf16 v[152:155], a[32:35], v[88:91], v[152:155]\n"
      "ds_read_b128 v[140:143], v22 offset:21504\n"
      "v_mfma_f32_16x16x32_bf16 v[152:155], a[36:39], v[92:95], v[152:155]\n"
      "ds_read_b128 v[128:131], v22 offset:18432\n"
      "v_mfma_f32_16x16x32_bf16 v[152:155], a[40:43], v[96:99], v[152:155]\n"
      "v_mfma_f32_16x16x32_bf16 v[152:155], a[44:47], v[100:103], v[152:155]\n"
      "s_waitcnt lgkmcnt(5)\n"
      "ds_read_b128 v[136:139], v22 offset:20480\n"
      "v_mfma_f32_16x16x32_bf16 v[152:155], a[48:51], v[104:107], v[152:155]\n"
      "s_waitcnt lgkmcnt(5)\n"
      "v_mfma_f32_16x16x32_bf16 v[152:155], a[52:55], v[108:111], v[152:155]\n"
      "v_mfma_f32_16x16x32_bf16 v[152:155], a[56:59], v[112:115], v[152:155]\n"
      "s_waitcnt lgkmcnt(3)\n"
      "ds_read_b128 v[144:147], v22 offset:22528\n"
      "v_mfma_f32_16x16x32_bf16 v[152:155], a[60:63], v[116:119], v[152:155]\n"
      "v_add_u32 v14, 1, v14\n"
      "v_mfma_f32_16x16x32_bf16 v[152:155], a[64:67], v[120:123], v[152:155]\n"
      "s_waitcnt vmcnt(7) lgkmcnt(2)\n"
      "v_cmp_ne_u32 vcc, s21, v20\n"
      "v_mfma_f32_16x16x32_bf16 v[152:155], a[68:71], v[124:127], v[152:155]\n"
      "v_mfma_f32_16x16x32_bf16 v[152:155], a[72:75], v[128:131], v[152:155]\n"
      "v_mfma_f32_16x16x32_bf16 v[152:155], a[76:79], v[132:135], v[152:155]\n"
      "s_waitcnt lgkmcnt(1)\n"
      "v_mfma_f32_16x16x32_bf16 v[152:155], a[80:83], v[136:139], v[152:155]\n"
      "v_mfma_f32_16x16x32_bf16 v[152:155], a[84:87], v[140:143], v[152:155]\n"
      "s_waitcnt lgkmcnt(0)\n"
      "s_cbranch_vccnz .Lsp_d4_8\n"
      ".Lbk_d4_8:\n"
      "v_mfma_f32_16x16x32_bf16 v[152:155], a[88:91], v[144:147], v[152:155]\n"
      "v_mfma_f32_16x16x32_bf16 v[152:155], a[92:95], v[148:151], v[152:155]\n"
      "s_min_u32 s36, s36, s6\n"
      "v_mfma_f32_16x16x32_bf16 v[156:159], a[96:99], v[56:59], 0\n"
      "buffer_load_dwordx4 v0, s[8:11], s54 offen offset:0 sc1 lds\n"
      "v_mfma_f32_16x16x32_bf16 v[156:159], a[100:103], v[60:63], v[156:159]\n"
      "v_mfma_f32_16x16x32_bf16 v[156:159], a[104:107], v[64:67], v[156:159]\n"
      "v_mfma_f32_16x16x32_bf16 v[156:159], a[108:111], v[68:71], v[156:159]\n"
      "v_mfma_f32_16x16x32_bf16 v[156:159], a[112:115], v[72:75], v[156:159]\n"
      "buffer_load_dwordx4 v0, s[8:11], s54 offen offset:2048 sc1 lds\n"
      "v_mfma_f32_16x16x32_bf16 v[156:159], a[116:119], v[76:79], v[156:159]\n"
      "s_waitcnt vmcnt(5)\n"
      "ds_write_b32 v16, v14\n"
      "v_mfma_f32_16x16x32_bf16 v[156:159], a[120:123], v[80:83], v[156:159]\n"
      "v_mfma_f32_16x16x32_bf16 v[156:159], a[124:127], v[84:87], v[156:159]\n"
      "v_mfma_f32_16x16x32_bf16 v[156:159], a[128:131], v[88:91], v[156:159]\n"
      "v_mfma_f32_16x16x32_bf16 v[156:159], a[132:135], v[92:95], v[156:159]\n"
      "buffer_load_dword v20, v2, s[8:11], s36 offen sc1\n"
      "v_mfma_f32_16x16x32_bf16 v[156:159], a[136:139], v[96:99], v[156:159]\n"
      "v_mfma_f32_16x16x32_bf16 v[156:159], a[140:143], v[100:103], v[156:159]\n"
      "v_cvt_pk_bf16_f32 v225, v154, v155\n"
      "v_mfma_f32_16x16x32_bf16 v[156:159], a[144:147], v[104:107], v[156:159]\n"
      "buffer_load_dwordx4 v0, s[8:11], s54 offen offset:3072 sc1 lds\n"
      "v_mfma_f32_16x16x32_bf16 v[156:159], a[148:151], v[108:111], v[156:159]\n"
      "v_mfma_f32_16x16x32_bf16 v[156:159], a[152:155], v[112:115], v[156:159]\n"
      "v_mfma_f32_16x16x32_bf16 v[156:159], a[156:159], v[116:119], v[156:159]\n"
      "ds_read_b32 v15, v17\n"
      "v_mfma_f32_16x16x32_bf16 v[156:159], a[160:163], v[120:123], v[156:159]\n"
      "buffer_load_dwordx4 v0, s[8:11], s54 offen offset:1024 sc1 lds\n"
      "v_mfma_f32_16x16x32_bf16 v[156:159], a[164:167], v[124:127], v[156:159]\n"
      "s_add_u32 m0, s43, 102400\n"
      "v_mfma_f32_16x16x32_bf16 v[156:159], a[168:171], v[128:131], v[156:159]\n"
      "v_mfma_f32_16x16x32_bf16 v[156:159], a[172:175], v[132:135], v[156:159]\n"
      "v_mfma_f32_16x16x32_bf16 v[156:159], a[176:179], v[136:139], v[156:159]\n"
      "buffer_load_dwordx4 v1, s[8:11], s54 offen offset:1024 sc1 lds\n"
      "v_mfma_f32_16x16x32_bf16 v[156:159], a[180:183], v[140:143], v[156:159]\n"
      "v_mfma_f32_16x16x32_bf16 v[156:159], a[184:187], v[144:147], v[156:159]\n"
      "s_waitcnt lgkmcnt(0)\n"
      "v_cmp_gt_i32 vcc, v14, v15\n"
      "v_mfma_f32_16x16x32_bf16 v[156:159], a[188:191], v[148:151], v[156:159]\n"
      "v_mfma_f32_16x16x32_bf16 v[160:163], a[192:195], v[56:59], 0\n"
      "buffer_load_dwordx4 v1, s[8:11], s54 offen offset:0 sc1 lds\n"
      "v_mfma_f32_16x16x32_bf16 v[160:163], a[196:199], v[60:63], v[160:163]\n"
      "s_cbranch_vccnz .Lsp_dy3_9\n"
      ".Lbk_dy3_9:\n"
      "ds_read_b128 v[60:63], v22 offset:25600\n"
      "v_mfma_f32_16x16x32_bf16 v[160:163], a[200:203], v[64:67], v[160:163]\n"
      "v_mfma_f32_16x16x32_bf16 v[160:163], a[204:207], v[68:71], v[160:163]\n"
      "v_cvt_pk_bf16_f32 v227, v158, v159\n"
      "v_mfma_f32_16x16x32_bf16 v[160:163], a[208:211], v[72:75], v[160:163]\n"
      "ds_read_b128 v[64:67], v22 offset:26624\n"
      "v_mfma_f32_16x16x32_bf16 v[160:163], a[212:215], v[76:79], v[160:163]\n"
      "s_mov_b32 s54, s36\n"
      "ds_read_b128 v[72:75], v22 offset:28672\n"
      "v_mfma_f32_16x16x32_bf16 v[160:163], a[216:219], v[80:83], v[160:163]\n"
      "ds_read_b128 v[68:71], v22 offset:27648\n"
      "v_mfma_f32_16x16x32_bf16 v[160:163], a[220:223], v[84:87], v[160:163]\n"
      "v_cvt_pk_bf16_f32 v224, v152, v153\n"
      "v_mfma_f32_16x16x32_bf16 v[160:163], a[224:227], v[88:91], v[160:163]\n"
      "s_waitcnt vmcnt(7) lgkmcnt(3)\n"
      "buffer_store_dword v18, v4, s[8:11], s55 offen sc1\n"
      "v_mfma_f32_16x16x32_bf16 v[160:163], a[228:231], v[92:95], v[160:163]\n"
      "ds_read_b128 v[92:95], v22 offset:33792\n"
      "v_mfma_f32_16x16x32_bf16 v[160:163], a[232:235], v[96:99], v[160:163]\n"
      "ds_read_b128 v[56:59], v22 offset:24576\n"
      "v_mfma_f32_16x16x32_bf16 v[160:163], a[236:239], v[100:103], v[160:163]\n"
      "buffer_load_dword v21, v3, s[8:11], s56 offen sc1\n"
      "v_mfma_f32_16x16x32_bf16 v[160:163], a[240:243], v[104:107], v[160:163]\n"
      "ds_read_b128 v[100:103], v22 offset:35840\n"
      "v_mfma_f32_16x16x32_bf16 v[160:163], a[244:247], v[108:111], v[160:163]\n"
      "ds_read_b128 v[80:83], v22 offset:30720\n"
      "v_mfma_f32_16x16x32_bf16 v[160:163], a[248:251], v[112:115], v[160:163]\n"
      "ds_read_b128 v[112:115], v22 offset:38912\n"
      "v_mfma_f32_16x16x32_bf16 v[160:163], a[252:255], v[116:119], v[160:163]\n"
      "s_add_u32 s37, s37, 106496\n"
      "ds_read_b128 v[88:91], v22 offset:32768\n"
      "v_mfma_f32_16x16x32_bf16 v[160:163], v[24:27], v[120:123], v[160:163]\n"
      "ds_read_b128 v[96:99], v22 offset:34816\n"
      "v_cvt_pk_bf16_f32 v226, v156, v157\n"
      "v_mfma_f32_16x16x32_bf16 v[160:163], v[28:31], v[124:127], v[160:163]\n"
      "ds_read_b128 v[76:79], v22 offset:29696\n"
      "v_mfma_f32_16x16x32_bf16 v[160:163], v[32:35], v[128:131], v[160:163]\n"
      "v_mfma_f32_16x16x32_bf16 v[160:163], v[36:39], v[132:135], v[160:163]\n"
      "buffer_store_dwordx4 v[224:227], v0, s[8:11], s37 offen sc1\n"
      "v_mfma_f32_16x16x32_bf16 v[160:163], v[40:43], v[136:139], v[160:163]\n"
      "ds_read_b128 v[116:119], v22 offset:39936\n"
      "v_mfma_f32_16x16x32_bf16 v[160:163], v[44:47], v[140:143], v[160:163]\n"
      "ds_read_b128 v[84:87], v22 offset:31744\n"
      "v_mfma_f32_16x16x32_bf16 v[160:163], v[48:51], v[144:147], v[160:163]\n"
      "s_waitcnt lgkmcnt(4)\n"
      "ds_read_b128 v[104:107], v22 offset:36864\n"
      "v_mfma_f32_16x16x32_bf16 v[160:163], v[52:55], v[148:151], v[160:163]\n"
      "s_sub_u32 s62, s62, 1\n"
      "s_cbranch_scc1 .Ld_out2\n"
      ".Ld_t3:\n"
      "v_mfma_f32_16x16x32_bf16 v[152:155], a[0:3], v[56:59], 0\n"
      "ds_read_b128 v[120:123], v22 offset:40960\n"
      "v_mfma_f32_16x16x32_bf16 v[152:155], a[4:7], v[60:63], v[152:155]\n"
      "ds_read_b128 v[108:111], v22 offset:37888\n"
      "v_mfma_f32_16x16x32_bf16 v[152:155], a[8:11], v[64:67], v[152:155]\n"
      "ds_read_b128 v[132:135], v22 offset:44032\n"
      "v_mfma_f32_16x16x32_bf16 v[152:155], a[12:15], v[68:71], v[152:155]\n"
      "ds_read_b128 v[136:139], v22 offset:45056\n"
      "v_cvt_pk_bf16_f32 v229, v162, v163\n"
      "v_mfma_f32_16x16x32_bf16 v[152:155], a[16:19], v[72:75], v[152:155]\n"
      "s_waitcnt lgkmcnt(5)\n"
      "v_mfma_f32_16x16x32_bf16 v[152:155], a[20:23], v[76:79], v[152:155]\n"
      "ds_read_b128 v[124:127], v22 offset:41984\n"
      "v_mfma_f32_16x16x32_bf16 v[152:155], a[24:27], v[80:83], v[152:155]\n"
      "s_add_u32 m0, s43, 122880\n"
      "v_cvt_pk_bf16_f32 v228, v160, v161\n"
      "v_mfma_f32_16x16x32_bf16 v[152:155], a[28:31], v[84:87], v[152:155]\n"
      "buffer_store_dwordx2 v[228:229], v13, s[8:11], s37 offen sc1\n"
      "v_mfma_f32_16x16x32_bf16 v[152:155], a[32:35], v[88:91], v[152:155]\n"
      "v_mfma_f32_16x16x32_bf16 v[152:155], a[36:39], v[92:95], v[152:155]\n"
      "ds_read_b128 v[128:131], v22 offset:43008\n"
      "v_mfma_f32_16x16x32_bf16 v[152:155], a[40:43], v[96:99], v[152:155]\n"
      "ds_read_b128 v[144:147], v22 offset:47104\n"
      "v_mfma_f32_16x16x32_bf16 v[152:155], a[44:47], v[100:103], v[152:155]\n"
      "s_waitcnt lgkmcnt(3)\n"
      "ds_read_b128 v[148:151], v22 offset:48128\n"
      "v_mfma_f32_16x16x32_bf16 v[152:155], a[48:51], v[104:107], v[152:155]\n"
      "ds_read_b128 v[140:143], v22 offset:46080\n"
      "v_mfma_f32_16x16x32_bf16 v[152:155], a[52:55], v[108:111], v[152:155]\n"
      "v_mfma_f32_16x16x32_bf16 v[152:155], a[56:59], v[112:115], v[152:155]\n"
      "s_waitcnt vmcnt(8) lgkmcnt(4)\n"
      "v_cmp_ne_u32 vcc, s21, v20\n"
      "v_mfma_f32_16x16x32_bf16 v[152:155], a[60:63], v[116:119], v[152:155]\n"
      "v_mfma_f32_16x16x32_bf16 v[152:155], a[64:67], v[120:123], v[152:155]\n"
      "s_add_u32 s36, s36, 165888\n"
      "s_add_u32 s55, s55, 106496\n"
      "v_mfma_f32_16x16x32_bf16 v[152:155], a[68:71], v[124:127], v[152:155]\n"
      "s_waitcnt lgkmcnt(2)\n"
      "v_mfma_f32_16x16x32_bf16 v[152:155], a[72:75], v[128:131], v[152:155]\n"
      "s_cbranch_vccnz .Lsp_d5_10\n"
      ".Lbk_d5_10:\n"
      "v_mfma_f32_16x16x32_bf16 v[152:155], a[76:79], v[132:135], v[152:155]\n"
      "buffer_load_dwordx4 v0, s[8:11], s54 offen offset:1024 sc1 lds\n"
      "v_mfma_f32_16x16x32_bf16 v[152:155], a[80:83], v[136:139], v[152:155]\n"
      "s_waitcnt lgkmcnt(0)\n"
      "v_mfma_f32_16x16x32_bf16 v[152:155], a[84:87], v[140:143], v[152:155]\n"
      "v_add_u32 v14, 1, v14\n"
      "v_mfma_f32_16x16x32_bf16 v[152:155], a[88:91], v[144:147], v[152:155]\n"
      "v_mfma_f32_16x16x32_bf16 v[152:155], a[92:95], v[148:151], v[152:155]\n"
      "buffer_load_dwordx4 v0, s[8:11], s54 offen offset:0 sc1 lds\n"
      "v_mfma_f32_16x16x32_bf16 v[156:159], a[96:99], v[56:59], 0\n"
      "v_mfma_f32_16x16x32_bf16 v[156:159], a[100:103], v[60:63], v[156:159]\n"
      "v_mfma_f32_16x16x32_bf16 v[156:159], a[104:107], v[64:67], v[156:159]\n"
      "v_mfma_f32_16x16x32_bf16 v[156:159], a[108:111], v[68:71], v[156:159]\n"
      "buffer_load_dwordx4 v0, s[8:11], s54 offen offset:2048 sc1 lds\n"
      "v_mfma_f32_16x16x32_bf16 v[156:159], a[112:115], v[72:75], v[156:159]\n"
      "s_waitcnt vmcnt(7)\n"
      "ds_write_b32 v16, v14\n"
      "v_mfma_f32_16x16x32_bf16 v[156:159], a[116:119], v[76:79], v[156:159]\n"
      "v_mfma_f32_16x16x32_bf16 v[156:159], a[120:123], v[80:83], v[156:159]\n"
      "v_mfma_f32_16x16x32_bf16 v[156:159], a[124:127], v[84:87], v[156:159]\n"
      "buffer_load_dwordx4 v0, s[8:11], s54 offen offset:3072 sc1 lds\n"
      "v_mfma_f32_16x16x32_bf16 v[156:159], a[128:131], v[88:91], v[156:159]\n"
      "v_mfma_f32_16x16x32_bf16 v[156:159], a[132:135], v[92:95], v[156:159]\n"
      "v_cvt_pk_bf16_f32 v224, v152, v153\n"
      "v_mfma_f32_16x16x32_bf16 v[156:159], a[136:139], v[96:99], v[156:159]\n"
      "v_mfma_f32_16x16x32_bf16 v[156:159], a[140:143], v[100:103], v[156:159]\n"
      "s_min_u32 s36, s36, s6\n"
      "s_add_u32 m0, s43, 126976\n"
      "v_mfma_f32_16x16x32_bf16 v[156:159], a[144:147], v[104:107], v[156:159]\n"
      "buffer_load_dwordx4 v1, s[8:11], s54 offen offset:1024 sc1 lds\n"
      "v_mfma_f32_16x16x32_bf16 v[156:159], a[148:151], v[108:111], v[156:159]\n"
      "ds_read_b32 v15, v17\n"
      "v_mfma_f32_16x16x32_bf16 v[156:159], a[152:155], v[112:115], v[156:159]\n"
      "v_mfma_f32_16x16x32_bf16 v[156:159], a[156:159], v[116:119], v[156:159]\n"
      "v_mfma_f32_16x16x32_bf16 v[156:159], a[160:163], v[120:123], v[156:159]\n"
      "buffer_load_dword v20, v2, s[8:11], s36 offen sc1\n"
      "v_mfma_f32_16x16x32_bf16 v[156:159], a[164:167], v[124:127], v[156:159]\n"
      "v_mfma_f32_16x16x32_bf16 v[156:159], a[168:171], v[128:131], v[156:159]\n"
      "s_waitcnt lgkmcnt(0)\n"
      "v_cmp_gt_i32 vcc, v14, v15\n"
      "v_mfma_f32_16x16x32_bf16 v[156:159], a[172:175], v[132:135], v[156:159]\n"
      "buffer_load_dwordx4 v1, s[8:11], s54 offen offset:0 sc1 lds\n"
      "v_mfma_f32_16x16x32_bf16 v[156:159], a[176:179], v[136:139], v[156:159]\n"
      "s_add_u32 s58, s56, 6656\n"
      "v_mfma_f32_16x16x32_bf16 v[156:159], a[180:183], v[140:143], v[156:159]\n"
      "s_add_u32 s57, s56, 13312\n"
      "v_mfma_f32_16x16x32_bf16 v[156:159], a[184:187], v[144:147], v[156:159]\n"
      "s_waitcnt vmcnt(9)\n"
      "s_cbranch_vccnz .Lsp_dy4_11\n"
      ".Lbk_dy4_11:\n"
      "v_cmp_ne_u32 vcc, s21, v21\n"
      "v_mfma_f32_16x16x32_bf16 v[156:159], a[188:191], v[148:151], v[156:159]\n"
      ".Ld_p1:\n"
      "s_cbranch_execz .Ld_pr\n"
      "v_mfma_f32_16x16x32_bf16 v[160:163], a[192:195], v[56:59], 0\n"
      "v_mfma_f32_16x16x32_bf16 v[160:163], a[196:199], v[60:63], v[160:163]\n"
      "s_cbranch_vccnz .Lsp_df_12\n"
      ".Lbk_df_12:\n"
      "buffer_load_dwordx4 v[176:179], v0, s[8:11], s56 offen sc1\n"
      "v_mfma_f32_16x16x32_bf16 v[160:163], a[200:203], v[64:67], v[160:163]\n"
      "ds_read_b128 v[60:63], v23 offset:1024\n"
      "v_mfma_f32_16x16x32_bf16 v[160:163], a[204:207], v[68:71], v[160:163]\n"
      "ds_read_b128 v[64:67], v23 offset:2048\n"
      "v_mfma_f32_16x16x32_bf16 v[160:163], a[208:211], v[72:75], v[160:163]\n"
      "buffer_load_dwordx2 v[180:181], v13, s[8:11], s56 offen sc1\n"
      "v_mfma_f32_16x16x32_bf16 v[160:163], a[212:215], v[76:79], v[160:163]\n"
      "ds_read_b128 v[72:75], v23 offset:4096\n"
      "v_mfma_f32_16x16x32_bf16 v[160:163], a[216:219], v[80:83], v[160:163]\n"
      "ds_read_b128 v[68:71], v23 offset:3072\n"
      "s_add_u32 s61, s41, 4294967264\n"
      "v_mfma_f32_16x16x32_bf16 v[160:163], a[220:223], v[84:87], v[160:163]\n"
      "ds_read_b128 v[80:83], v23 offset:6144\n"
      "v_mfma_f32_16x16x32_bf16 v[160:163], a[224:227], v[88:91], v[160:163]\n"
      "v_cvt_pk_bf16_f32 v225, v154, v155\n"
      "v_mfma_f32_16x16x32_bf16 v[160:163], a[228:231], v[92:95], v[160:163]\n"
      "buffer_load_dwordx4 v[184:187], v0, s[8:11], s58 offen sc1\n"
      "v_mfma_f32_16x16x32_bf16 v[160:163], a[232:235], v[96:99], v[160:163]\n"
      "ds_read_b128 v[84:87], v23 offset:7168\n"
      "v_cvt_pk_bf16_f32 v226, v156, v157\n"
      "v_mfma_f32_16x16x32_bf16 v[160:163], a[236:239], v[100:103], v[160:163]\n"
      "ds_read_b128 v[76:79], v23 offset:5120\n"
      "v_mfma_f32_16x16x32_bf16 v[160:163], a[240:243], v[104:107], v[160:163]\n"
      "s_add_u32 s37, s37, 106496\n"
      "ds_read_b128 v[56:59], v23 offset:0\n"
      "v_mfma_f32_16x16x32_bf16 v[160:163], a[244:247], v[108:111], v[160:163]\n"
      "s_waitcnt vmcnt(10) lgkmcnt(4)\n"
      "buffer_store_dword v18, v4, s[8:11], s55 offen sc1\n"
      "v_mfma_f32_16x16x32_bf16 v[160:163], a[248:251], v[112:115], v[160:163]\n"
      "ds_read_b128 v[88:91], v23 offset:8192\n"
      "v_mfma_f32_16x16x32_bf16 v[160:163], a[252:255], v[116:119], v[160:163]\n"
      "ds_read_b128 v[96:99], v23 offset:10240\n"
      "v_mfma_f32_16x16x32_bf16 v[160:163], v[24:27], v[120:123], v[160:163]\n"
      "buffer_load_dwordx4 v[192:195], v0, s[8:11], s57 offen sc1\n"
      "v_mfma_f32_16x16x32_bf16 v[160:163], v[28:31], v[124:127], v[160:163]\n"
      "v_mfma_f32_16x16x32_bf16 v[160:163], v[32:35], v[128:131], v[160:163]\n"
      "buffer_load_dwordx2 v[188:189], v13, s[8:11], s58 offen sc1\n"
      "v_mfma_f32_16x16x32_bf16 v[160:163], v[36:39], v[132:135], v[160:163]\n"
      "ds_read_b128 v[100:103], v23 offset:11264\n"
      "v_mfma_f32_16x16x32_bf16 v[160:163], v[40:43], v[136:139], v[160:163]\n"
      "s_add_u32 s58, s56, 19968\n"
      "ds_read_b128 v[132:135], v23 offset:19456\n"
      "v_mfma_f32_16x16x32_bf16 v[160:163], v[44:47], v[140:143], v[160:163]\n"
      "ds_read_b128 v[104:107], v23 offset:12288\n"
      "v_mfma_f32_16x16x32_bf16 v[160:163], v[48:51], v[144:147], v[160:163]\n"
      "s_waitcnt lgkmcnt(3)\n"
      "buffer_load_dwordx2 v[196:197], v13, s[8:11], s57 offen sc1\n"
      "v_mfma_f32_16x16x32_bf16 v[160:163], v[52:55], v[148:151], v[160:163]\n"
      "s_sub_u32 s62, s62, 1\n"
      "s_cbranch_scc1 .Ld_out3\n"
      ".Ld_t4:\n"
      "v_mfma_f32_16x16x32_bf16 v[152:155], a[0:3], v[56:59], 0\n"
      "ds_read_b128 v[92:95], v23 offset:9216\n"
      "v_mfma_f32_16x16x32_bf16 v[152:155], a[4:7], v[60:63], v[152:155]\n"
      "ds_read_b128 v[108:111], v23 offset:13312\n"
      "s_add_u32 m0, s43, 0\n"
      "v_mfma_f32_16x16x32_bf16 v[152:155], a[8:11], v[64:67], v[152:155]\n"
      "ds_read_b128 v[128:131], v23 offset:18432\n"
      "v_cvt_pk_bf16_f32 v228, v160, v161\n"
      "v_mfma_f32_16x16x32_bf16 v[152:155], a[12:15], v[68:71], v[152:155]\n"
      "buffer_load_dwordx4 v[200:203], v0, s[8:11], s58 offen sc1\n"
      "v_mfma_f32_16x16x32_bf16 v[152:155], a[16:19], v[72:75], v[152:155]\n"
      "ds_read_b128 v[116:119], v23 offset:15360\n"
      "v_cvt_pk_bf16_f32 v227, v158, v159\n"
      "v_mfma_f32_16x16x32_bf16 v[152:155], a[20:23], v[76:79], v[152:155]\n"
      "ds_read_b128 v[112:115], v23 offset:14336\n"
      "v_mfma_f32_16x16x32_bf16 v[152:155], a[24:27], v[80:83], v[152:155]\n"
      "s_mov_b32 s54, s36\n"
      "ds_read_b128 v[120:123], v23 offset:16384\n"
      "v_mfma_f32_16x16x32_bf16 v[152:155], a[28:31], v[84:87], v[152:155]\n"
      "buffer_store_dwordx4 v[224:227], v0, s[8:11], s37 offen sc1\n"
      "v_mfma_f32_16x16x32_bf16 v[152:155], a[32:35], v[88:91], v[152:155]\n"
      "s_waitcnt lgkmcnt(4)\n"
      "ds_read_b128 v[140:143], v23 offset:21504\n"
      "v_mfma_f32_16x16x32_bf16 v[152:155], a[36:39], v[92:95], v[152:155]\n"
      "ds_read_b128 v[124:127], v23 offset:17408\n"
      "v_mfma_f32_16x16x32_bf16 v[152:155], a[40:43], v[96:99], v[152:155]\n"
      "ds_read_b128 v[136:139], v23 offset:20480\n"
      "v_mfma_f32_16x16x32_bf16 v[152:155], a[44:47], v[100:103], v[152:155]\n"
      "v_cvt_pk_bf16_f32 v229, v162, v163\n"
      "v_mfma_f32_16x16x32_bf16 v[152:155], a[48:51], v[104:107], v[152:155]\n"
      "buffer_store_dwordx2 v[228:229], v13, s[8:11], s37 offen sc1\n"
      "v_mfma_f32_16x16x32_bf16 v[152:155], a[52:55], v[108:111], v[152:155]\n"
      "s_waitcnt lgkmcnt(3)\n"
      "ds_read_b128 v[148:151], v23 offset:23552\n"
      "v_add_u32_e64 v14, 1, v14\n"
      "v_mfma_f32_16x16x32_bf16 v[152:155], a[56:59], v[112:115], v[152:155]\n"
      "s_add_u32 s36, s36, 165888\n"
      "s_add_u32 s37, s37, 106496\n"
      "ds_read_b128 v[144:147], v23 offset:22528\n"
      "v_mfma_f32_16x16x32_bf16 v[152:155], a[60:63], v[116:119], v[152:155]\n"
      "v_mfma_f32_16x16x32_bf16 v[152:155], a[64:67], v[120:123], v[152:155]\n"
      "s_waitcnt lgkmcnt(3)\n"
      "s_min_u32 s36, s36, s6\n"
      "v_mfma_f32_16x16x32_bf16 v[152:155], a[68:71], v[124:127], v[152:155]\n"
      "buffer_load_dwordx2 v[204:205], v13, s[8:11], s58 offen sc1\n"
      "v_mfma_f32_16x16x32_bf16 v[152:155], a[72:75], v[128:131], v[152:155]\n"
      "s_cmp_lt_u32 s41, 32\n"
      "s_cselect_b32 s59, s39, 0x40000000\n"
      "v_mfma_f32_16x16x32_bf16 v[152:155], a[76:79], v[132:135], v[152:155]\n"
      "s_waitcnt lgkmcnt(2)\n"
      "s_add_u32 s57, s56, 26624\n"
      "v_mfma_f32_16x16x32_bf16 v[152:155], a[80:83], v[136:139], v[152:155]\n"
      "s_waitcnt vmcnt(12) lgkmcnt(1)\n"
      "s_add_u32 s56, s56, 599040\n"
      "v_cmp_ne_u32 vcc, s21, v20\n"
      "v_mfma_f32_16x16x32_bf16 v[152:155], a[84:87], v[140:143], v[152:155]\n"
      "s_waitcnt lgkmcnt(0)\n"
      "buffer_load_dwordx2 v[212:213], v13, s[8:11], s57 offen sc1\n"
      "v_mfma_f32_16x16x32_bf16 v[152:155], a[88:91], v[144:147], v[152:155]\n"
      "v_mfma_f32_16x16x32_bf16 v[152:155], a[92:95], v[148:151], v[152:155]\n"
      "s_add_u32 s39, s39, 147456\n"
      "v_mfma_f32_16x16x32_bf16 v[156:159], a[96:99], v[56:59], 0\n"
      "s_waitcnt vmcnt(12)\n"
      "ds_write_b32 v16, v14\n"
      "v_mfma_f32_16x16x32_bf16 v[156:159], a[100:103], v[60:63], v[156:159]\n"
      "buffer_load_dwordx4 v[208:211], v0, s[8:11], s57 offen sc1\n"
      "v_mfma_f32_16x16x32_bf16 v[156:159], a[104:107], v[64:67], v[156:159]\n"
      "s_cmp_lt_u32 s61, 32\n"
      "s_cselect_b32 s60, s40, 0x40000000\n"
      "v_mfma_f32_16x16x32_bf16 v[156:159], a[108:111], v[68:71], v[156:159]\n"
      "s_cbranch_vccnz .Lsp_d0_13\n"
      ".Lbk_d0_13:\n"
      "v_cvt_pk_bf16_f32 v224, v152, v153\n"
      "v_mfma_f32_16x16x32_bf16 v[156:159], a[112:115], v[72:75], v[156:159]\n"
      "v_cvt_pk_bf16_f32 v225, v154, v155\n"
      "v_mfma_f32_16x16x32_bf16 v[156:159], a[116:119], v[76:79], v[156:159]\n"
      "buffer_load_dwordx4 v0, s[8:11], s54 offen offset:0 sc1 lds\n"
      "v_mfma_f32_16x16x32_bf16 v[156:159], a[120:123], v[80:83], v[156:159]\n"
      "s_waitcnt vmcnt(13)\n"
      "v_lshlrev_b32 v234, 16, v178\n"
      "v_lshlrev_b32 v233, 16, v177\n"
      "v_mfma_f32_16x16x32_bf16 v[156:159], a[124:127], v[84:87], v[156:159]\n"
      "v_lshlrev_b32 v232, 16, v176\n"
      "v_add_f32 v166, v166, v233\n"
      "v_mfma_f32_16x16x32_bf16 v[156:159], a[128:131], v[88:91], v[156:159]\n"
      "s_add_u32 s55, s55, 106496\n"
      "v_add_f32 v164, v164, v232\n"
      "v_mfma_f32_16x16x32_bf16 v[156:159], a[132:135], v[92:95], v[156:159]\n"
      "buffer_load_dwordx4 v0, s[8:11], s54 offen offset:1024 sc1 lds\n"
      "v_mfma_f32_16x16x32_bf16 v[156:159], a[136:139], v[96:99], v[156:159]\n"
      "s_waitcnt vmcnt(13) lgkmcnt(0)\n"
      "v_lshlrev_b32 v233, 16, v181\n"
      "v_lshlrev_b32 v235, 16, v179\n"
      "v_mfma_f32_16x16x32_bf16 v[156:159], a[140:143], v[100:103], v[156:159]\n"
      "v_and_b32 v176, 0xffff0000, v176\n"
      "v_lshlrev_b32 v232, 16, v180\n"
      "v_mfma_f32_16x16x32_bf16 v[156:159], a[144:147], v[104:107], v[156:159]\n"
      "v_add_f32 v172, v172, v232\n"
      "v_and_b32 v181, 0xffff0000, v181\n"
      "v_mfma_f32_16x16x32_bf16 v[156:159], a[148:151], v[108:111], v[156:159]\n"
      "v_add_f32 v168, v168, v234\n"
      "v_add_f32 v174, v174, v233\n"
      "v_mfma_f32_16x16x32_bf16 v[156:159], a[152:155], v[112:115], v[156:159]\n"
      "buffer_load_dword v20, v2, s[8:11], s36 offen sc1\n"
      "v_mfma_f32_16x16x32_bf16 v[156:159], a[156:159], v[116:119], v[156:159]\n"
      "ds_read_b32 v15, v17\n"
      "v_add_f32 v165, v165, v176\n"
      "v_mfma_f32_16x16x32_bf16 v[156:159], a[160:163], v[120:123], v[156:159]\n"
      "buffer_load_dwordx4 v0, s[8:11], s54 offen offset:2048 sc1 lds\n"
      "v_mfma_f32_16x16x32_bf16 v[156:159], a[164:167], v[124:127], v[156:159]\n"
      "s_waitcnt vmcnt(14)\n"
      "v_lshlrev_b32 v232, 16, v184\n"
      "v_and_b32 v177, 0xffff0000, v177\n"
      "v_mfma_f32_16x16x32_bf16 v[156:159], a[168:171], v[128:131], v[156:159]\n"
      "s_add_u32 s40, s40, 4294856704\n"
      "v_add_f32 v167, v167, v177\n"
      "v_and_b32 v180, 0xffff0000, v180\n"
      "v_mfma_f32_16x16x32_bf16 v[156:159], a[172:175], v[132:135], v[156:159]\n"
      "v_add_f32 v164, v164, v232\n"
      "v_lshlrev_b32 v233, 16, v185\n"
      "v_mfma_f32_16x16x32_bf16 v[156:159], a[176:179], v[136:139], v[156:159]\n"
      "buffer_load_dwordx4 v0, s[8:11], s54 offen offset:3072 sc1 lds\n"
      "v_mfma_f32_16x16x32_bf16 v[156:159], a[180:183], v[140:143], v[156:159]\n"
      "s_waitcnt lgkmcnt(0)\n"
      "v_add_f32 v166, v166, v233\n"
      "v_cmp_gt_i32 vcc, v14, v15\n"
      "v_mfma_f32_16x16x32_bf16 v[156:159], a[184:187], v[144:147], v[156:159]\n"
      "s_waitcnt vmcnt(12)\n"
      "v_lshlrev_b32 v232, 16, v188\n"
      "v_add_f32 v172, v172, v232\n"
      "v_mfma_f32_16x16x32_bf16 v[156:159], a[188:191], v[148:151], v[156:159]\n"
      "v_and_b32 v185, 0xffff0000, v185\n"
      "v_lshlrev_b32 v232, 16, v192\n"
      "v_mfma_f32_16x16x32_bf16 v[160:163], a[192:195], v[56:59], 0\n"
      "s_add_u32 m0, s43, 4096\n"
      "v_lshlrev_b32 v234, 16, v186\n"
      "v_add_f32 v164, v164, v232\n"
      "v_mfma_f32_16x16x32_bf16 v[160:163], a[196:199], v[60:63], v[160:163]\n"
      "buffer_load_dwordx4 v1, s[8:11], s54 offen offset:1024 sc1 lds\n"
      "v_mfma_f32_16x16x32_bf16 v[160:163], a[200:203], v[64:67], v[160:163]\n"
      "s_cbranch_vccnz .Lsp_dy5_14\n"
      ".Lbk_dy5_14:\n"
      "v_cvt_pk_bf16_f32 v227, v158, v159\n"
      "v_mfma_f32_16x16x32_bf16 v[160:163], a[204:207], v[68:71], v[160:163]\n"
      "ds_read_b128 v[56:59], v23 offset:24576\n"
      "v_lshlrev_b32 v233, 16, v189\n"
      "v_mfma_f32_16x16x32_bf16 v[160:163], a[208:211], v[72:75], v[160:163]\n"
      "s_waitcnt vmcnt(12)\n"
      "ds_read_b128 v[64:67], v23 offset:26624\n"
      "v_lshlrev_b32 v232, 16, v196\n"
      "v_mfma_f32_16x16x32_bf16 v[160:163], a[212:215], v[76:79], v[160:163]\n"
      "buffer_load_dwordx4 v1, s[8:11], s54 offen offset:0 sc1 lds\n"
      "v_mfma_f32_16x16x32_bf16 v[160:163], a[216:219], v[80:83], v[160:163]\n"
      "ds_read_b128 v[76:79], v23 offset:29696\n"
      "v_add_f32 v174, v174, v233\n"
      "v_mfma_f32_16x16x32_bf16 v[160:163], a[220:223], v[84:87], v[160:163]\n"
      "v_and_b32 v184, 0xffff0000, v184\n"
      "v_add_f32 v172, v172, v232\n"
      "v_mfma_f32_16x16x32_bf16 v[160:163], a[224:227], v[88:91], v[160:163]\n"
      "v_add_f32 v170, v170, v235\n"
      "v_cvt_pk_bf16_f32 v226, v156, v157\n"
      "v_mfma_f32_16x16x32_bf16 v[160:163], a[228:231], v[92:95], v[160:163]\n"
      "s_add_u32 s41, s41, 6\n"
      "ds_read_b128 v[88:91], v23 offset:32768\n"
      "v_add_f32 v165, v165, v184\n"
      "v_mfma_f32_16x16x32_bf16 v[160:163], a[232:235], v[96:99], v[160:163]\n"
      "ds_read_b128 v[68:71], v23 offset:27648\n"
      "v_mfma_f32_16x16x32_bf16 v[160:163], a[236:239], v[100:103], v[160:163]\n"
      "ds_read_b128 v[96:99], v23 offset:34816\n"
      "v_add_f32 v168, v168, v234\n"
      "v_mfma_f32_16x16x32_bf16 v[160:163], a[240:243], v[104:107], v[160:163]\n"
      "ds_read_b128 v[80:83], v23 offset:30720\n"
      "v_lshlrev_b32 v233, 16, v193\n"
      "v_mfma_f32_16x16x32_bf16 v[160:163], a[244:247], v[108:111], v[160:163]\n"
      "ds_read_b128 v[72:75], v23 offset:28672\n"
      "v_and_b32 v178, 0xffff0000, v178\n"
      "v_mfma_f32_16x16x32_bf16 v[160:163], a[248:251], v[112:115], v[160:163]\n"
      "s_waitcnt vmcnt(12) lgkmcnt(5)\n"
      "s_mov_b32 s54, s36\n"
      "ds_read_b128 v[112:115], v23 offset:38912\n"
      "v_lshlrev_b32 v232, 16, v200\n"
      "v_mfma_f32_16x16x32_bf16 v[160:163], a[252:255], v[116:119], v[160:163]\n"
      "v_add_f32 v164, v164, v232\n"
      "v_and_b32 v186, 0xffff0000, v186\n"
      "v_mfma_f32_16x16x32_bf16 v[160:163], v[24:27], v[120:123], v[160:163]\n"
      "s_waitcnt vmcnt(10) lgkmcnt(5)\n"
      "buffer_store_dword v18, v4, s[8:11], s55 offen sc1\n"
      "v_mfma_f32_16x16x32_bf16 v[160:163], v[28:31], v[124:127], v[160:163]\n"
      "ds_read_b128 v[60:63], v23 offset:25600\n"
      "v_add_f32 v169, v169, v178\n"
      "v_mfma_f32_16x16x32_bf16 v[160:163], v[32:35], v[128:131], v[160:163]\n"
      "buffer_store_dwordx4 v[224:227], v0, s[8:11], s37 offen sc1\n"
      "v_mfma_f32_16x16x32_bf16 v[160:163], v[36:39], v[132:135], v[160:163]\n"
      "ds_read_b128 v[104:107], v23 offset:36864\n"
      "v_and_b32 v200, 0xffff0000, v200\n"
      "v_mfma_f32_16x16x32_bf16 v[160:163], v[40:43], v[136:139], v[160:163]\n"
      "ds_read_b128 v[84:87], v23 offset:31744\n"
      "v_add_f32 v173, v173, v180\n"
      "v_mfma_f32_16x16x32_bf16 v[160:163], v[44:47], v[140:143], v[160:163]\n"
      "ds_read_b128 v[140:143], v23 offset:46080\n"
      "v_add_f32 v166, v166, v233\n"
      "v_mfma_f32_16x16x32_bf16 v[160:163], v[48:51], v[144:147], v[160:163]\n"
      "ds_read_b128 v[100:103], v23 offset:35840\n"
      "v_lshlrev_b32_e64 v234, 16, v194\n"
      "v_mfma_f32_16x16x32_bf16 v[160:163], v[52:55], v[148:151], v[160:163]\n"
      "s_sub_u32 s62, s62, 1\n"
      "s_cbranch_scc1 .Ld_out4\n"
      ".Ld_t5:\n"
      "v_mfma_f32_16x16x32_bf16 v[152:155], a[0:3], v[56:59], 0\n"
      "s_waitcnt lgkmcnt(4)\n"
      "ds_read_b128 v[92:95], v23 offset:33792\n"
      "ds_read_b128 v[116:119], v23 offset:39936\n"
      "v_mfma_f32_16x16x32_bf16 v[152:155], a[4:7], v[60:63], v[152:155]\n"
      "ds_read_b128 v[108:111], v23 offset:37888\n"
      "v_and_b32 v192, 0xffff0000, v192\n"
      "v_mfma_f32_16x16x32_bf16 v[152:155], a[8:11], v[64:67], v[152:155]\n"
      "ds_read_b128 v[128:131], v23 offset:43008\n"
      "v_cvt_pk_bf16_f32 v229, v162, v163\n"
      "v_mfma_f32_16x16x32_bf16 v[152:155], a[12:15], v[68:71], v[152:155]\n"
      "ds_read_b128 v[136:139], v23 offset:45056\n"
      "v_add_f32 v175, v175, v181\n"
      "v_mfma_f32_16x16x32_bf16 v[152:155], a[16:19], v[72:75], v[152:155]\n"
      "v_cvt_pk_bf16_f32 v228, v160, v161\n"
      "v_and_b32 v189, 0xffff0000, v189\n"
      "v_mfma_f32_16x16x32_bf16 v[152:155], a[20:23], v[76:79], v[152:155]\n"
      "s_waitcnt vmcnt(11) lgkmcnt(5)\n"
      "ds_read_b128 v[144:147], v23 offset:47104\n"
      "v_lshlrev_b32 v232, 16, v204\n"
      "v_mfma_f32_16x16x32_bf16 v[152:155], a[24:27], v[80:83], v[152:155]\n"
      "buffer_store_dwordx2 v[228:229], v13, s[8:11], s37 offen sc1\n"
      "v_mfma_f32_16x16x32_bf16 v[152:155], a[28:31], v[84:87], v[152:155]\n"
      "v_lshlrev_b32 v235, 16, v187\n"
      "v_add_f32 v172, v172, v232\n"
      "v_mfma_f32_16x16x32_bf16 v[152:155], a[32:35], v[88:91], v[152:155]\n"
      "s_waitcnt vmcnt(10) lgkmcnt(3)\n"
      "ds_read_b128 v[120:123], v23 offset:40960\n"
      "v_lshlrev_b32 v232, 16, v208\n"
      "v_mfma_f32_16x16x32_bf16 v[152:155], a[36:39], v[92:95], v[152:155]\n"
      "ds_read_b128 v[124:127], v23 offset:41984\n"
      "v_add_f32 v170, v170, v235\n"
      "v_mfma_f32_16x16x32_bf16 v[152:155], a[40:43], v[96:99], v[152:155]\n"
      "s_add_u32 m0, s43, 24576\n"
      "v_and_b32 v194, 0xffff0000, v194\n"
      "v_and_b32 v208, 0xffff0000, v208\n"
      "v_mfma_f32_16x16x32_bf16 v[152:155], a[44:47], v[100:103], v[152:155]\n"
      "ds_read_b128 v[132:135], v23 offset:44032\n"
      "v_add_f32 v167, v167, v185\n"
      "v_mfma_f32_16x16x32_bf16 v[152:155], a[48:51], v[104:107], v[152:155]\n"
      "v_lshlrev_b32 v233, 16, v197\n"
      "v_add_u32 v14, 1, v14\n"
      "v_mfma_f32_16x16x32_bf16 v[152:155], a[52:55], v[108:111], v[152:155]\n"
      "s_add_u32 s36, s36, 165888\n"
      "v_lshlrev_b32 v235, 16, v195\n"
      "v_add_f32 v168, v168, v234\n"
      "v_mfma_f32_16x16x32_bf16 v[152:155], a[56:59], v[112:115], v[152:155]\n"
      "buffer_load_dwordx4 v[216:219], v6, s[8:11], s38 offen sc1\n"
      "v_mfma_f32_16x16x32_bf16 v[152:155], a[60:63], v[116:119], v[152:155]\n"
      "s_waitcnt lgkmcnt(2)\n"
      "ds_read_b128 v[148:151], v23 offset:48128\n"
      "v_add_f32 v164, v164, v232\n"
      "v_mfma_f32_16x16x32_bf16 v[152:155], a[64:67], v[120:123], v[152:155]\n"
      "s_waitcnt lgkmcnt(2)\n"
      "v_add_f32 v165, v165, v192\n"
      "v_lshlrev_b32 v234, 16, v202\n"
      "v_mfma_f32_16x16x32_bf16 v[152:155], a[68:71], v[124:127], v[152:155]\n"
      "v_add_f32 v174, v174, v233\n"
      "v_lshlrev_b32 v232, 16, v212\n"
      "v_mfma_f32_16x16x32_bf16 v[152:155], a[72:75], v[128:131], v[152:155]\n"
      "s_waitcnt lgkmcnt(1)\n"
      "v_and_b32 v179, 0xffff0000, v179\n"
      "v_add_f32 v169, v169, v186\n"
      "v_mfma_f32_16x16x32_bf16 v[152:155], a[76:79], v[132:135], v[152:155]\n"
      "s_waitcnt vmcnt(8)\n"
      "v_lshlrev_b32 v233, 16, v201\n"
      "v_cmp_ne_u32 vcc, s21, v20\n"
      "v_mfma_f32_16x16x32_bf16 v[152:155], a[80:83], v[136:139], v[152:155]\n"
      "buffer_load_dwordx2 v[220:221], v7, s[8:11], s38 offen sc1\n"
      "v_mfma_f32_16x16x32_bf16 v[152:155], a[84:87], v[140:143], v[152:155]\n"
      "v_add_f32 v166, v166, v233\n"
      "v_and_b32 v193, 0xffff0000, v193\n"
      "v_mfma_f32_16x16x32_bf16 v[152:155], a[88:91], v[144:147], v[152:155]\n"
      "s_waitcnt lgkmcnt(0)\n"
      "v_and_b32 v197, 0xffff0000, v197\n"
      "v_lshlrev_b32 v233, 16, v205\n"
      "v_mfma_f32_16x16x32_bf16 v[152:155], a[92:95], v[148:151], v[152:155]\n"
      "v_add_f32 v170, v170, v235\n"
      "v_add_f32 v174, v174, v233\n"
      "v_mfma_f32_16x16x32_bf16 v[156:159], a[96:99], v[56:59], 0\n"
      "s_cbranch_vccnz .Lsp_d1_15\n"
      ".Lbk_d1_15:\n"
      "v_lshlrev_b32 v235, 16, v203\n"
      "v_mfma_f32_16x16x32_bf16 v[156:159], a[100:103], v[60:63], v[156:159]\n"
      "buffer_load_dwordx4 v0, s[8:11], s54 offen offset:0 sc1 lds\n"
      "v_mfma_f32_16x16x32_bf16 v[156:159], a[104:107], v[64:67], v[156:159]\n"
      "v_and_b32 v187, 0xffff0000, v187\n"
      "v_lshlrev_b32 v233, 16, v209\n"
      "v_mfma_f32_16x16x32_bf16 v[156:159], a[108:111], v[68:71], v[156:159]\n"
      "v_add_f32 v170, v170, v235\n"
      "v_add_f32 v166, v166, v233\n"
      "v_mfma_f32_16x16x32_bf16 v[156:159], a[112:115], v[72:75], v[156:159]\n"
      "v_and_b32 v201, 0xffff0000, v201\n"
      "v_add_f32 v165, v165, v200\n"
      "v_mfma_f32_16x16x32_bf16 v[156:159], a[116:119], v[76:79], v[156:159]\n"
      "v_and_b32 v188, 0xffff0000, v188\n"
      "v_add_f32 v165, v165, v208\n"
      "v_mfma_f32_16x16x32_bf16 v[156:159], a[120:123], v[80:83], v[156:159]\n"
      "buffer_load_dwordx4 v0, s[8:11], s54 offen offset:1024 sc1 lds\n"
      "v_mfma_f32_16x16x32_bf16 v[156:159], a[124:127], v[84:87], v[156:159]\n"
      "v_add_f32 v168, v168, v234\n"
      "v_mul_f32 v237, v164, v164\n"
      "v_mfma_f32_16x16x32_bf16 v[156:159], a[128:131], v[88:91], v[156:159]\n"
      "v_add_f32 v167, v167, v193\n"
      "v_lshlrev_b32 v233, 16, v213\n"
      "v_mfma_f32_16x16x32_bf16 v[156:159], a[132:135], v[92:95], v[156:159]\n"
      "s_waitcnt vmcnt(7)\n"
      "ds_write_b32 v16, v14\n"
      "v_mfma_f32_16x16x32_bf16 v[156:159], a[136:139], v[96:99], v[156:159]\n"
      "buffer_load_dwordx4 v0, s[8:11], s54 offen offset:2048 sc1 lds\n"
      "v_mfma_f32_16x16x32_bf16 v[156:159], a[140:143], v[100:103], v[156:159]\n"
      "v_add_f32 v171, v171, v179\n"
      "v_add_f32 v167, v167, v201\n"
      "v_mfma_f32_16x16x32_bf16 v[156:159], a[144:147], v[104:107], v[156:159]\n"
      "v_lshlrev_b32 v234, 16, v210\n"
      "v_and_b32 v213, 0xffff0000, v213\n"
      "v_mfma_f32_16x16x32_bf16 v[156:159], a[148:151], v[108:111], v[156:159]\n"
      "s_min_u32 s36, s36, s6\n"
      "v_add_f32 v173, v173, v188\n"
      "v_and_b32 v209, 0xffff0000, v209\n"
      "v_mfma_f32_16x16x32_bf16 v[156:159], a[152:155], v[112:115], v[156:159]\n"
      "buffer_load_dword v20, v2, s[8:11], s36 offen sc1\n"
      "v_mfma_f32_16x16x32_bf16 v[156:159], a[156:159], v[116:119], v[156:159]\n"
      "v_add_f32 v236, v164, v165\n"
      "v_add_f32 v175, v175, v189\n"
      "v_mfma_f32_16x16x32_bf16 v[156:159], a[160:163], v[120:123], v[156:159]\n"
      "buffer_load_dwordx4 v0, s[8:11], s54 offen offset:3072 sc1 lds\n"
      "v_mfma_f32_16x16x32_bf16 v[156:159], a[164:167], v[124:127], v[156:159]\n"
      "v_add_f32 v167, v167, v209\n"
      "v_and_b32 v203, 0xffff0000, v203\n"
      "v_mfma_f32_16x16x32_bf16 v[156:159], a[168:171], v[128:131], v[156:159]\n"
      "ds_read_b32 v15, v17\n"
      "v_fmac_f32 v237, v165, v165\n"
      "v_mfma_f32_16x16x32_bf16 v[156:159], a[172:175], v[132:135], v[156:159]\n"
      "s_add_u32 m0, s43, 28672\n"
      "v_add_f32 v175, v175, v197\n"
      "v_and_b32 v204, 0xffff0000, v204\n"
      "v_mfma_f32_16x16x32_bf16 v[156:159], a[176:179], v[136:139], v[156:159]\n"
      "v_and_b32 v195, 0xffff0000, v195\n"
      "v_add_f32 v172, v172, v232\n"
      "v_mfma_f32_16x16x32_bf16 v[156:159], a[180:183], v[140:143], v[156:159]\n"
      "buffer_load_dwordx4 v1, s[8:11], s54 offen offset:0 sc1 lds\n"
      "v_mfma_f32_16x16x32_bf16 v[156:159], a[184:187], v[144:147], v[156:159]\n"
      "v_fmac_f32 v237, v166, v166\n"
      "v_cvt_pk_bf16_f32 v225, v154, v155\n"
      "v_mfma_f32_16x16x32_bf16 v[156:159], a[188:191], v[148:151], v[156:159]\n"
      "s_waitcnt lgkmcnt(0)\n"
      "v_add_f32 v169, v169, v194\n"
      "v_cmp_gt_i32 vcc, v14, v15\n"
      "v_mfma_f32_16x16x32_bf16 v[160:163], a[192:195], v[56:59], 0\n"
      "v_and_b32 v196, 0xffff0000, v196\n"
      "v_and_b32 v212, 0xffff0000, v212\n"
      "v_mfma_f32_16x16x32_bf16 v[160:163], a[196:199], v[60:63], v[160:163]\n"
      "buffer_load_dwordx4 v1, s[8:11], s54 offen offset:1024 sc1 lds\n"
      "v_mfma_f32_16x16x32_bf16 v[160:163], a[200:203], v[64:67], v[160:163]\n"
      "s_cbranch_vccnz .Lsp_dy0_16\n"
      ".Lbk_dy0_16:\n"
      "v_mfma_f32_16x16x32_bf16 v[160:163], a[204:207], v[68:71], v[160:163]\n"
      "ds_read_b128 v[56:59], v0 offset:0\n"
      "v_cvt_pk_bf16_f32 v177, v166, v167\n"
      "v_mfma_f32_16x16x32_bf16 v[160:163], a[208:211], v[72:75], v[160:163]\n"
      "v_add_f32 v171, v171, v187\n"
      "v_and_b32 v205, 0xffff0000, v205\n"
      "v_mfma_f32_16x16x32_bf16 v[160:163], a[212:215], v[76:79], v[160:163]\n"
      "ds_read_b128 v[60:63], v0 offset:1024\n"
      "v_cvt_pk_bf16_f32 v224, v152, v153\n"
      "v_mfma_f32_16x16x32_bf16 v[160:163], a[216:219], v[80:83], v[160:163]\n"
      "s_add_u32 s55, s55, 106496\n"
      "v_and_b32 v202, 0xffff0000, v202\n"
      "v_add_f32 v169, v169, v202\n"
      "v_mfma_f32_16x16x32_bf16 v[160:163], a[220:223], v[84:87], v[160:163]\n"
      "ds_read_b128 v[84:87], v0 offset:7168\n"
      "v_add_f32 v236, v236, v166\n"
      "v_mfma_f32_16x16x32_bf16 v[160:163], a[224:227], v[88:91], v[160:163]\n"
      "ds_read_b128 v[72:75], v0 offset:4096\n"
      "v_and_b32 v210, 0xffff0000, v210\n"
      "v_mfma_f32_16x16x32_bf16 v[160:163], a[228:231], v[92:95], v[160:163]\n"
      "v_add_f32 v173, v173, v196\n"
      "v_lshlrev_b32 v235, 16, v211\n"
      "v_mfma_f32_16x16x32_bf16 v[160:163], a[232:235], v[96:99], v[160:163]\n"
      "v_add_f32 v170, v170, v235\n"
      "v_add_f32 v171, v171, v195\n"
      "v_mfma_f32_16x16x32_bf16 v[160:163], a[236:239], v[100:103], v[160:163]\n"
      "s_add_u32 s37, s37, 106496\n"
      "ds_read_b128 v[88:91], v0 offset:8192\n"
      "v_cvt_pk_bf16_f32 v227, v158, v159\n"
      "v_mfma_f32_16x16x32_bf16 v[160:163], a[240:243], v[104:107], v[160:163]\n"
      "s_waitcnt vmcnt(9) lgkmcnt(3)\n"
      "buffer_store_dword v18, v4, s[8:11], s55 offen sc1\n"
      "v_mfma_f32_16x16x32_bf16 v[160:163], a[244:247], v[108:111], v[160:163]\n"
      "ds_read_b128 v[68:71], v0 offset:3072\n"
      "v_add_f32 v174, v174, v233\n"
      "v_mfma_f32_16x16x32_bf16 v[160:163], a[248:251], v[112:115], v[160:163]\n"
      "ds_read_b128 v[104:107], v0 offset:12288\n"
      "v_add_f32 v169, v169, v210\n"
      "v_mfma_f32_16x16x32_bf16 v[160:163], a[252:255], v[116:119], v[160:163]\n"
      "ds_read_b128 v[96:99], v0 offset:10240\n"
      "v_fmac_f32 v237, v167, v167\n"
      "v_mfma_f32_16x16x32_bf16 v[160:163], v[24:27], v[120:123], v[160:163]\n"
      "ds_read_b128 v[80:83], v0 offset:6144\n"
      "v_add_f32 v175, v175, v205\n"
      "v_mfma_f32_16x16x32_bf16 v[160:163], v[28:31], v[124:127], v[160:163]\n"
      "ds_read_b128 v[64:67], v0 offset:2048\n"
      "v_add_f32 v168, v168, v234\n"
      "v_mfma_f32_16x16x32_bf16 v[160:163], v[32:35], v[128:131], v[160:163]\n"
      "v_add_f32 v236, v236, v167\n"
      "v_cvt_pk_bf16_f32 v226, v156, v157\n"
      "v_mfma_f32_16x16x32_bf16 v[160:163], v[36:39], v[132:135], v[160:163]\n"
      "ds_read_b128 v[92:95], v0 offset:9216\n"
      "v_cvt_pk_bf16_f32 v176, v164, v165\n"
      "v_mfma_f32_16x16x32_bf16 v[160:163], v[40:43], v[136:139], v[160:163]\n"
      "buffer_store_dwordx4 v[224:227], v0, s[8:11], s37 offen sc1\n"
      "ds_read_b128 v[76:79], v0 offset:5120\n"
      "v_mfma_f32_16x16x32_bf16 v[160:163], v[44:47], v[140:143], v[160:163]\n"
      "ds_read_b128 v[132:135], v0 offset:19456\n"
      "v_and_b32 v211, 0xffff0000, v211\n"
      "v_mfma_f32_16x16x32_bf16 v[160:163], v[48:51], v[144:147], v[160:163]\n"
      "ds_read_b128 v[100:103], v0 offset:11264\n"
      "v_add_f32 v171, v171, v203\n"
      "v_mfma_f32_16x16x32_bf16 v[160:163], v[52:55], v[148:151], v[160:163]\n"
      "s_sub_u32 s62, s62, 1\n"
      "s_cbranch_scc0 .Ld_t0\n"
      ".Ld_out5:\n"
      "s_nop 8\n"
      "v_cvt_pk_bf16_f32 v228, v160, v161\n"
      "v_cvt_pk_bf16_f32 v229, v162, v163\n"
      "buffer_store_dwordx2 v[228:229], v13, s[8:11], s37 offen sc1\n"
      "v_cvt_pk_bf16_f32 v178, v168, v169\n"
      "v_add_f32 v175, v175, v213\n"
      "v_add_f32 v171, v171, v211\n"
      "v_add_f32 v173, v173, v204\n"
      "v_cvt_pk_bf16_f32 v179, v170, v171\n"
      "v_add_f32 v173, v173, v212\n"
      "v_cvt_pk_bf16_f32 v180, v172, v173\n"
      "v_cvt_pk_bf16_f32 v181, v174, v175\n"
      "buffer_store_dwordx2 v[180:181], v9, s[16:19], s60 offen\n"
      "buffer_store_dwordx4 v[176:179], v8, s[16:19], s60 offen\n"
      "s_add_u32 s55, s55, 106496\n"
      "s_add_u32 s37, s37, 173056\n"
      "s_waitcnt vmcnt(2)\n"
      "buffer_store_dword v18, v4, s[8:11], s55 offen sc1\n"
      "s_add_u32 s55, s55, 173056\n"
      "s_branch .Ld_end\n"
      ".Ld_out0:\n"
      "s_nop 8\n"
      "v_add_f32 v174, v174, v162\n"
      "v_add_f32 v170, v170, v158\n"
      "v_lshlrev_b32 v172, 16, v220\n"
      "s_add_u32 s38, s38, 147456\n"
      "v_add_f32 v171, v171, v159\n"
      "v_add_f32 v169, v169, v157\n"
      "v_add_f32 v172, v172, v160\n"
      "v_add_f32 v175, v175, v163\n"
      "v_and_b32 v173, 0xffff0000, v220\n"
      "v_add_f32 v173, v173, v161\n"
      "s_add_u32 s55, s55, 173056\n"
      "s_add_u32 s57, s56, 6656\n"
      "s_add_u32 s58, s56, 13312\n"
      "s_add_u32 s62, s56, 19968\n"
      "s_add_u32 s36, s56, 26624\n"
      "s_mov_b32 s54, 0\n"
      "s_mov_b32 s22, s23\n"
      ".Ld_a0:\n"
      "buffer_load_dwordx4 v[176:179], v0, s[8:11], s56 offen sc1\n"
      "buffer_load_dwordx4 v[184:187], v0, s[8:11], s57 offen sc1\n"
      "buffer_load_dwordx4 v[192:195], v0, s[8:11], s58 offen sc1\n"
      "buffer_load_dwordx4 v[200:203], v0, s[8:11], s62 offen sc1\n"
      "buffer_load_dwordx4 v[208:211], v0, s[8:11], s36 offen sc1\n"
      "buffer_load_dword v21, v3, s[8:11], s56 offen sc1\n"
      "buffer_load_dwordx2 v[180:181], v13, s[8:11], s56 offen sc1\n"
      "buffer_load_dwordx2 v[188:189], v13, s[8:11], s57 offen sc1\n"
      "buffer_load_dwordx2 v[196:197], v13, s[8:11], s58 offen sc1\n"
      "buffer_load_dwordx2 v[204:205], v13, s[8:11], s62 offen sc1\n"
      "buffer_load_dwordx2 v[212:213], v13, s[8:11], s36 offen sc1\n"
      "s_waitcnt vmcnt(6)\n"
      "s_cmp_lg_u32 s54, 0\n"
      "s_cbranch_scc1 .Ld_b0\n"
      "v_max_u32 v232, v176, v177\n"
      "v_max3_u32 v232, v232, v178, v179\n"
      "v_max3_u32 v232, v232, v184, v185\n"
      "v_max3_u32 v232, v232, v186, v187\n"
      "v_max3_u32 v232, v232, v192, v193\n"
      "v_max3_u32 v232, v232, v194, v195\n"
      "v_max3_u32 v232, v232, v200, v201\n"
      "v_max3_u32 v232, v232, v202, v203\n"
      "v_max3_u32 v232, v232, v208, v209\n"
      "v_max3_u32 v232, v232, v210, v211\n"
      "v_cmp_eq_u32 vcc, -1, v232\n"
      "s_cbranch_vccz .Ld_b0\n"
      "s_waitcnt vmcnt(0)\n"
      "v_cmp_ne_u32 vcc, s21, v21\n"
      "s_nop 1\n"
      "s_cmp_eq_u64 vcc, 0\n"
      "s_cselect_b32 s54, 1, 0\n"
      "s_sub_u32 s22, s22, 1\n"
      "s_cbranch_scc0 .Ld_a0\n"
      ".Ld_b0:\n"
      "s_add_u32 s61, s41, 4294967264\n"
      "s_cmp_lt_u32 s41, 32\n"
      "s_cselect_b32 s59, s39, 0x40000000\n"
      "s_add_u32 s39, s39, 147456\n"
      "s_cmp_lt_u32 s61, 32\n"
      "s_cselect_b32 s60, s40, 0x40000000\n"
      "s_add_u32 s40, s40, 4294856704\n"
      "s_add_u32 s41, s41, 6\n"
      "v_lshlrev_b32 v232, 16, v176\n"
      "v_lshlrev_b32 v233, 16, v177\n"
      "v_lshlrev_b32 v234, 16, v178\n"
      "v_lshlrev_b32 v235, 16, v179\n"
      "v_and_b32 v176, 0xffff0000, v176\n"
      "v_and_b32 v177, 0xffff0000, v177\n"
      "v_and_b32 v178, 0xffff0000, v178\n"
      "v_and_b32 v179, 0xffff0000, v179\n"
      "v_add_f32 v164, v164, v232\n"
      "v_add_f32 v166, v166, v233\n"
      "v_add_f32 v168, v168, v234\n"
      "v_add_f32 v170, v170, v235\n"
      "v_add_f32 v165, v165, v176\n"
      "v_add_f32 v167, v167, v177\n"
      "v_add_f32 v169, v169, v178\n"
      "v_add_f32 v171, v171, v179\n"
      "v_lshlrev_b32 v232, 16, v184\n"
      "v_lshlrev_b32 v233, 16, v185\n"
      "v_lshlrev_b32 v234, 16, v186\n"
      "v_lshlrev_b32 v235, 16, v187\n"
      "v_and_b32 v184, 0xffff0000, v184\n"
      "v_and_b32 v185, 0xffff0000, v185\n"
      "v_and_b32 v186, 0xffff0000, v186\n"
      "v_and_b32 v187, 0xffff0000, v187\n"
      "v_add_f32 v164, v164, v232\n"
      "v_add_f32 v166, v166, v233\n"
      "v_add_f32 v168, v168, v234\n"
      "v_add_f32 v170, v170, v235\n"
      "v_add_f32 v165, v165, v184\n"
      "v_add_f32 v167, v167, v185\n"
      "v_add_f32 v169, v169, v186\n"
      "v_add_f32 v171, v171, v187\n"
      "v_lshlrev_b32 v232, 16, v192\n"
      "v_lshlrev_b32 v233, 16, v193\n"
      "v_lshlrev_b32 v234, 16, v194\n"
      "v_lshlrev_b32 v235, 16, v195\n"
      "v_and_b32 v192, 0xffff0000, v192\n"
      "v_and_b32 v193, 0xffff0000, v193\n"
      "v_and_b32 v194, 0xffff0000, v194\n"
      "v_and_b32 v195, 0xffff0000, v195\n"
      "v_add_f32 v164, v164, v232\n"
      "v_add_f32 v166, v166, v233\n"
      "v_add_f32 v168, v168, v234\n"
      "v_add_f32 v170, v170, v235\n"
      "v_add_f32 v165, v165, v192\n"
      "v_add_f32 v167, v167, v193\n"
      "v_add_f32 v169, v169, v194\n"
      "v_add_f32 v171, v171, v195\n"
      "v_lshlrev_b32 v232, 16, v200\n"
      "v_lshlrev_b32 v233, 16, v201\n"
      "v_lshlrev_b32 v234, 16, v202\n"
      "v_lshlrev_b32 v235, 16, v203\n"
      "v_and_b32 v200, 0xffff0000, v200\n"
      "v_and_b32 v201, 0xffff0000, v201\n"
      "v_and_b32 v202, 0xffff0000, v202\n"
      "v_and_b32 v203, 0xffff0000, v203\n"
      "v_add_f32 v164, v164, v232\n"
      "v_add_f32 v166, v166, v233\n"
      "v_add_f32 v168, v168, v234\n"
      "v_add_f32 v170, v170, v235\n"
      "v_add_f32 v165, v165, v200\n"
      "v_add_f32 v167, v167, v201\n"
      "v_add_f32 v169, v169, v202\n"
      "v_add_f32 v171, v171, v203\n"
      "v_lshlrev_b32 v232, 16, v208\n"
      "v_lshlrev_b32 v233, 16, v209\n"
      "v_lshlrev_b32 v234, 16, v210\n"
      "v_lshlrev_b32 v235, 16, v211\n"
      "v_and_b32 v208, 0xffff0000, v208\n"
      "v_and_b32 v209, 0xffff0000, v209\n"
      "v_and_b32 v210, 0xffff0000, v210\n"
      "v_and_b32 v211, 0xffff0000, v211\n"
      "v_add_f32 v164, v164, v232\n"
      "v_add_f32 v166, v166, v233\n"
      "v_add_f32 v168, v168, v234\n"
      "v_add_f32 v170, v170, v235\n"
      "v_add_f32 v165, v165, v208\n"
      "v_add_f32 v167, v167, v209\n"
      "v_add_f32 v169, v169, v210\n"
      "v_add_f32 v171, v171, v211\n"
      "v_cvt_pk_bf16_f32 v176, v164, v165\n"
      "v_cvt_pk_bf16_f32 v177, v166, v167\n"
      "v_cvt_pk_bf16_f32 v178, v168, v169\n"
      "v_cvt_pk_bf16_f32 v179, v170, v171\n"
      "buffer_store_dwordx4 v[176:179], v8, s[16:19], s60 offen\n"
      "s_mov_b32 s22, s23\n"
      "s_waitcnt vmcnt(1)\n"
      ".Ld_c0:\n"
      "s_cmp_lg_u32 s54, 0\n"
      "s_cbranch_scc1 .Ld_d0\n"
      "v_max_u32 v232, v180, v181\n"
      "v_max3_u32 v232, v232, v188, v189\n"
      "v_max3_u32 v232, v232, v196, v197\n"
      "v_max3_u32 v232, v232, v204, v205\n"
      "v_max3_u32 v232, v232, v212, v213\n"
      "v_cmp_eq_u32 vcc, -1, v232\n"
      "s_cbranch_vccz .Ld_d0\n"
      "v_cmp_ne_u32 vcc, s21, v21\n"
      "s_nop 1\n"
      "s_cmp_eq_u64 vcc, 0\n"
      "s_cselect_b32 s54, 1, 0\n"
      "buffer_load_dword v21, v3, s[8:11], s56 offen sc1\n"
      "buffer_load_dwordx2 v[180:181], v13, s[8:11], s56 offen sc1\n"
      "buffer_load_dwordx2 v[188:189], v13, s[8:11], s57 offen sc1\n"
      "buffer_load_dwordx2 v[196:197], v13, s[8:11], s58 offen sc1\n"
      "buffer_load_dwordx2 v[204:205], v13, s[8:11], s62 offen sc1\n"
      "buffer_load_dwordx2 v[212:213], v13, s[8:11], s36 offen sc1\n"
      "s_waitcnt vmcnt(0)\n"
      "s_sub_u32 s22, s22, 1\n"
      "s_cbranch_scc0 .Ld_c0\n"
      ".Ld_d0:\n"
      "v_lshlrev_b32 v232, 16, v180\n"
      "v_lshlrev_b32 v233, 16, v181\n"
      "v_and_b32 v180, 0xffff0000, v180\n"
      "v_and_b32 v181, 0xffff0000, v181\n"
      "v_add_f32 v172, v172, v232\n"
      "v_add_f32 v174, v174, v233\n"
      "v_add_f32 v173, v173, v180\n"
      "v_add_f32 v175, v175, v181\n"
      "v_lshlrev_b32 v232, 16, v188\n"
      "v_lshlrev_b32 v233, 16, v189\n"
      "v_and_b32 v188, 0xffff0000, v188\n"
      "v_and_b32 v189, 0xffff0000, v189\n"
      "v_add_f32 v172, v172, v232\n"
      "v_add_f32 v174, v174, v233\n"
      "v_add_f32 v173, v173, v188\n"
      "v_add_f32 v175, v175, v189\n"
      "v_lshlrev_b32 v232, 16, v196\n"
      "v_lshlrev_b32 v233, 16, v197\n"
      "v_and_b32 v196, 0xffff0000, v196\n"
      "v_and_b32 v197, 0xffff0000, v197\n"
      "v_add_f32 v172, v172, v232\n"
      "v_add_f32 v174, v174, v233\n"
      "v_add_f32 v173, v173, v196\n"
      "v_add_f32 v175, v175, v197\n"
      "v_lshlrev_b32 v232, 16, v204\n"
      "v_lshlrev_b32 v233, 16, v205\n"
      "v_and_b32 v204, 0xffff0000, v204\n"
      "v_and_b32 v205, 0xffff0000, v205\n"
      "v_add_f32 v172, v172, v232\n"
      "v_add_f32 v174, v174, v233\n"
      "v_add_f32 v173, v173, v204\n"
      "v_add_f32 v175, v175, v205\n"
      "v_lshlrev_b32 v232, 16, v212\n"
      "v_lshlrev_b32 v233, 16, v213\n"
      "v_and_b32 v212, 0xffff0000, v212\n"
      "v_and_b32 v213, 0xffff0000, v213\n"
      "v_add_f32 v172, v172, v232\n"
      "v_add_f32 v174, v174, v233\n"
      "v_add_f32 v173, v173, v212\n"
      "v_add_f32 v175, v175, v213\n"
      "v_cvt_pk_bf16_f32 v180, v172, v173\n"
      "v_cvt_pk_bf16_f32 v181, v174, v175\n"
      "buffer_store_dwordx2 v[180:181], v9, s[16:19], s60 offen\n"
      "s_branch .Ld_end\n"
      ".Ld_out1:\n"
      "s_nop 8\n"
      "v_cvt_pk_bf16_f32 v229, v162, v163\n"
      "v_cvt_pk_bf16_f32 v228, v160, v161\n"
      "buffer_store_dwordx2 v[228:229], v13, s[8:11], s37 offen sc1\n"
      "s_add_u32 s57, s56, 6656\n"
      "s_add_u32 s58, s56, 13312\n"
      "s_add_u32 s62, s56, 19968\n"
      "s_add_u32 s36, s56, 26624\n"
      "s_mov_b32 s54, 0\n"
      "s_mov_b32 s22, s23\n"
      ".Ld_a1:\n"
      "buffer_load_dwordx4 v[176:179], v0, s[8:11], s56 offen sc1\n"
      "buffer_load_dwordx4 v[184:187], v0, s[8:11], s57 offen sc1\n"
      "buffer_load_dwordx4 v[192:195], v0, s[8:11], s58 offen sc1\n"
      "buffer_load_dwordx4 v[200:203], v0, s[8:11], s62 offen sc1\n"
      "buffer_load_dwordx4 v[208:211], v0, s[8:11], s36 offen sc1\n"
      "buffer_load_dword v21, v3, s[8:11], s56 offen sc1\n"
      "buffer_load_dwordx2 v[180:181], v13, s[8:11], s56 offen sc1\n"
      "buffer_load_dwordx2 v[188:189], v13, s[8:11], s57 offen sc1\n"
      "buffer_load_dwordx2 v[196:197], v13, s[8:11], s58 offen sc1\n"
      "buffer_load_dwordx2 v[204:205], v13, s[8:11], s62 offen sc1\n"
      "buffer_load_dwordx2 v[212:213], v13, s[8:11], s36 offen sc1\n"
      "s_waitcnt vmcnt(6)\n"
      "s_cmp_lg_u32 s54, 0\n"
      "s_cbranch_scc1 .Ld_b1\n"
      "v_max_u32 v232, v176, v177\n"
      "v_max3_u32 v232, v232, v178, v179\n"
      "v_max3_u32 v232, v232, v184, v185\n"
      "v_max3_u32 v232, v232, v186, v187\n"
      "v_max3_u32 v232, v232, v192, v193\n"
      "v_max3_u32 v232, v232, v194, v195\n"
      "v_max3_u32 v232, v232, v200, v201\n"
      "v_max3_u32 v232, v232, v202, v203\n"
      "v_max3_u32 v232, v232, v208, v209\n"
      "v_max3_u32 v232, v232, v210, v211\n"
      "v_cmp_eq_u32 vcc, -1, v232\n"
      "s_cbranch_vccz .Ld_b1\n"
      "s_waitcnt vmcnt(0)\n"
      "v_cmp_ne_u32 vcc, s21, v21\n"
      "s_nop 1\n"
      "s_cmp_eq_u64 vcc, 0\n"
      "s_cselect_b32 s54, 1, 0\n"
      "s_sub_u32 s22, s22, 1\n"
      "s_cbranch_scc0 .Ld_a1\n"
      ".Ld_b1:\n"
      "buffer_store_dword v18, v4, s[8:11], s55 offen sc1\n"
      "s_add_u32 s37, s37, 106496\n"
      "s_add_u32 s55, s55, 106496\n"
      "s_add_u32 s61, s41, 4294967264\n"
      "s_cmp_lt_u32 s41, 32\n"
      "s_cselect_b32 s59, s39, 0x40000000\n"
      "s_add_u32 s39, s39, 147456\n"
      "s_cmp_lt_u32 s61, 32\n"
      "s_cselect_b32 s60, s40, 0x40000000\n"
      "s_add_u32 s40, s40, 4294856704\n"
      "s_add_u32 s41, s41, 6\n"
      "v_lshlrev_b32 v232, 16, v176\n"
      "v_lshlrev_b32 v233, 16, v177\n"
      "v_lshlrev_b32 v234, 16, v178\n"
      "v_lshlrev_b32 v235, 16, v179\n"
      "v_and_b32 v176, 0xffff0000, v176\n"
      "v_and_b32 v177, 0xffff0000, v177\n"
      "v_and_b32 v178, 0xffff0000, v178\n"
      "v_and_b32 v179, 0xffff0000, v179\n"
      "v_add_f32 v164, v164, v232\n"
      "v_add_f32 v166, v166, v233\n"
      "v_add_f32 v168, v168, v234\n"
      "v_add_f32 v170, v170, v235\n"
      "v_add_f32 v165, v165, v176\n"
      "v_add_f32 v167, v167, v177\n"
      "v_add_f32 v169, v169, v178\n"
      "v_add_f32 v171, v171, v179\n"
      "v_lshlrev_b32 v232, 16, v184\n"
      "v_lshlrev_b32 v233, 16, v185\n"
      "v_lshlrev_b32 v234, 16, v186\n"
      "v_lshlrev_b32 v235, 16, v187\n"
      "v_and_b32 v184, 0xffff0000, v184\n"
      "v_and_b32 v185, 0xffff0000, v185\n"
      "v_and_b32 v186, 0xffff0000, v186\n"
      "v_and_b32 v187, 0xffff0000, v187\n"
      "v_add_f32 v164, v164, v232\n"
      "v_add_f32 v166, v166, v233\n"
      "v_add_f32 v168, v168, v234\n"
      "v_add_f32 v170, v170, v235\n"
      "v_add_f32 v165, v165, v184\n"
      "v_add_f32 v167, v167, v185\n"
      "v_add_f32 v169, v169, v186\n"
      "v_add_f32 v171, v171, v187\n"
      "v_lshlrev_b32 v232, 16, v192\n"
      "v_lshlrev_b32 v233, 16, v193\n"
      "v_lshlrev_b32 v234, 16, v194\n"
      "v_lshlrev_b32 v235, 16, v195\n"
      "v_and_b32 v192, 0xffff0000, v192\n"
      "v_and_b32 v193, 0xffff0000, v193\n"
      "v_and_b32 v194, 0xffff0000, v194\n"
      "v_and_b32 v195, 0xffff0000, v195\n"
      "v_add_f32 v164, v164, v232\n"
      "v_add_f32 v166, v166, v233\n"
      "v_add_f32 v168, v168, v234\n"
      "v_add_f32 v170, v170, v235\n"
      "v_add_f32 v165, v165, v192\n"
      "v_add_f32 v167, v167, v193\n"
      "v_add_f32 v169, v169, v194\n"
      "v_add_f32 v171, v171, v195\n"
      "v_lshlrev_b32 v232, 16, v200\n"
      "v_lshlrev_b32 v233, 16, v201\n"
      "v_lshlrev_b32 v234, 16, v202\n"
      "v_lshlrev_b32 v235, 16, v203\n"
      "v_and_b32 v200, 0xffff0000, v200\n"
      "v_and_b32 v201, 0xffff0000, v201\n"
      "v_and_b32 v202, 0xffff0000, v202\n"
      "v_and_b32 v203, 0xffff0000, v203\n"
      "v_add_f32 v164, v164, v232\n"
      "v_add_f32 v166, v166, v233\n"
      "v_add_f32 v168, v168, v234\n"
      "v_add_f32 v170, v170, v235\n"
      "v_add_f32 v165, v165, v200\n"
      "v_add_f32 v167, v167, v201\n"
      "v_add_f32 v169, v169, v202\n"
      "v_add_f32 v171, v171, v203\n"
      "v_lshlrev_b32 v232, 16, v208\n"
      "v_lshlrev_b32 v233, 16, v209\n"
      "v_lshlrev_b32 v234, 16, v210\n"
      "v_lshlrev_b32 v235, 16, v211\n"
      "v_and_b32 v208, 0xffff0000, v208\n"
      "v_and_b32 v209, 0xffff0000, v209\n"
      "v_and_b32 v210, 0xffff0000, v210\n"
      "v_and_b32 v211, 0xffff0000, v211\n"
      "v_add_f32 v164, v164, v232\n"
      "v_add_f32 v166, v166, v233\n"
      "v_add_f32 v168, v168, v234\n"
      "v_add_f32 v170, v170, v235\n"
      "v_add_f32 v165, v165, v208\n"
      "v_add_f32 v167, v167, v209\n"
      "v_add_f32 v169, v169, v210\n"
      "v_add_f32 v171, v171, v211\n"
      "v_cvt_pk_bf16_f32 v176, v164, v165\n"
      "v_cvt_pk_bf16_f32 v177, v166, v167\n"
      "v_cvt_pk_bf16_f32 v178, v168, v169\n"
      "v_cvt_pk_bf16_f32 v179, v170, v171\n"
      "buffer_store_dwordx4 v[176:179], v8, s[16:19], s60 offen\n"
      "s_mov_b32 s22, s23\n"
      "s_waitcnt vmcnt(2)\n"
      ".Ld_c1:\n"
      "s_cmp_lg_u32 s54, 0\n"
      "s_cbranch_scc1 .Ld_d1\n"
      "v_max_u32 v232, v180, v181\n"
      "v_max3_u32 v232, v232, v188, v189\n"
      "v_max3_u32 v232, v232, v196, v197\n"
      "v_max3_u32 v232, v232, v204, v205\n"
      "v_max3_u32 v232, v232, v212, v213\n"
      "v_cmp_eq_u32 vcc, -1, v232\n"
      "s_cbranch_vccz .Ld_d1\n"
      "v_cmp_ne_u32 vcc, s21, v21\n"
      "s_nop 1\n"
      "s_cmp_eq_u64 vcc, 0\n"
      "s_cselect_b32 s54, 1, 0\n"
      "buffer_load_dword v21, v3, s[8:11], s56 offen sc1\n"
      "buffer_load_dwordx2 v[180:181], v13, s[8:11], s56 offen sc1\n"
      "buffer_load_dwordx2 v[188:189], v13, s[8:11], s57 offen sc1\n"
      "buffer_load_dwordx2 v[196:197], v13, s[8:11], s58 offen sc1\n"
      "buffer_load_dwordx2 v[204:205], v13, s[8:11], s62 offen sc1\n"
      "buffer_load_dwordx2 v[212:213], v13, s[8:11], s36 offen sc1\n"
      "s_waitcnt vmcnt(0)\n"
      "s_sub_u32 s22, s22, 1\n"
      "s_cbranch_scc0 .Ld_c1\n"
      ".Ld_d1:\n"
      "v_lshlrev_b32 v232, 16, v180\n"
      "v_lshlrev_b32 v233, 16, v181\n"
      "v_and_b32 v180, 0xffff0000, v180\n"
      "v_and_b32 v181, 0xffff0000, v181\n"
      "v_add_f32 v172, v172, v232\n"
      "v_add_f32 v174, v174, v233\n"
      "v_add_f32 v173, v173, v180\n"
      "v_add_f32 v175, v175, v181\n"
      "v_lshlrev_b32 v232, 16, v188\n"
      "v_lshlrev_b32 v233, 16, v189\n"
      "v_and_b32 v188, 0xffff0000, v188\n"
      "v_and_b32 v189, 0xffff0000, v189\n"
      "v_add_f32 v172, v172, v232\n"
      "v_add_f32 v174, v174, v233\n"
      "v_add_f32 v173, v173, v188\n"
      "v_add_f32 v175, v175, v189\n"
      "v_lshlrev_b32 v232, 16, v196\n"
      "v_lshlrev_b32 v233, 16, v197\n"
      "v_and_b32 v196, 0xffff0000, v196\n"
      "v_and_b32 v197, 0xffff0000, v197\n"
      "v_add_f32 v172, v172, v232\n"
      "v_add_f32 v174, v174, v233\n"
      "v_add_f32 v173, v173, v196\n"
      "v_add_f32 v175, v175, v197\n"
      "v_lshlrev_b32 v232, 16, v204\n"
      "v_lshlrev_b32 v233, 16, v205\n"
      "v_and_b32 v204, 0xffff0000, v204\n"
      "v_and_b32 v205, 0xffff0000, v205\n"
      "v_add_f32 v172, v172, v232\n"
      "v_add_f32 v174, v174, v233\n"
      "v_add_f32 v173, v173, v204\n"
      "v_add_f32 v175, v175, v205\n"
      "v_lshlrev_b32 v232, 16, v212\n"
      "v_lshlrev_b32 v233, 16, v213\n"
      "v_and_b32 v212, 0xffff0000, v212\n"
      "v_and_b32 v213, 0xffff0000, v213\n"
      "v_add_f32 v172, v172, v232\n"
      "v_add_f32 v174, v174, v233\n"
      "v_add_f32 v173, v173, v212\n"
      "v_add_f32 v175, v175, v213\n"
      "v_cvt_pk_bf16_f32 v180, v172, v173\n"
      "v_cvt_pk_bf16_f32 v181, v174, v175\n"
      "buffer_store_dwordx2 v[180:181], v9, s[16:19], s60 offen\n"
      "s_branch .Ld_end\n"
      ".Ld_out2:\n"
      "s_nop 8\n"
      "v_cvt_pk_bf16_f32 v229, v162, v163\n"
      "v_cvt_pk_bf16_f32 v228, v160, v161\n"
      "buffer_store_dwordx2 v[228:229], v13, s[8:11], s37 offen sc1\n"
      "s_add_u32 s57, s56, 6656\n"
      "s_add_u32 s58, s56, 13312\n"
      "s_add_u32 s62, s56, 19968\n"
      "s_add_u32 s36, s56, 26624\n"
      "s_mov_b32 s54, 0\n"
      "s_mov_b32 s22, s23\n"
      ".Ld_a2:\n"
      "buffer_load_dwordx4 v[176:179], v0, s[8:11], s56 offen sc1\n"
      "buffer_load_dwordx4 v[184:187], v0, s[8:11], s57 offen sc1\n"
      "buffer_load_dwordx4 v[192:195], v0, s[8:11], s58 offen sc1\n"
      "buffer_load_dwordx4 v[200:203], v0, s[8:11], s62 offen sc1\n"
      "buffer_load_dwordx4 v[208:211], v0, s[8:11], s36 offen sc1\n"
      "buffer_load_dword v21, v3, s[8:11], s56 offen sc1\n"
      "buffer_load_dwordx2 v[180:181], v13, s[8:11], s56 offen sc1\n"
      "buffer_load_dwordx2 v[188:189], v13, s[8:11], s57 offen sc1\n"
      "buffer_load_dwordx2 v[196:197], v13, s[8:11], s58 offen sc1\n"
      "buffer_load_dwordx2 v[204:205], v13, s[8:11], s62 offen sc1\n"
      "buffer_load_dwordx2 v[212:213], v13, s[8:11], s36 offen sc1\n"
      "s_waitcnt vmcnt(6)\n"
      "s_cmp_lg_u32 s54, 0\n"
      "s_cbranch_scc1 .Ld_b2\n"
      "v_max_u32 v232, v176, v177\n"
      "v_max3_u32 v232, v232, v178, v179\n"
      "v_max3_u32 v232, v232, v184, v185\n"
      "v_max3_u32 v232, v232, v186, v187\n"
      "v_max3_u32 v232, v232, v192, v193\n"
      "v_max3_u32 v232, v232, v194, v195\n"
      "v_max3_u32 v232, v232, v200, v201\n"
      "v_max3_u32 v232, v232, v202, v203\n"
      "v_max3_u32 v232, v232, v208, v209\n"
      "v_max3_u32 v232, v232, v210, v211\n"
      "v_cmp_eq_u32 vcc, -1, v232\n"
      "s_cbranch_vccz .Ld_b2\n"
      "s_waitcnt vmcnt(0)\n"
      "v_cmp_ne_u32 vcc, s21, v21\n"
      "s_nop 1\n"
      "s_cmp_eq_u64 vcc, 0\n"
      "s_cselect_b32 s54, 1, 0\n"
      "s_sub_u32 s22, s22, 1\n"
      "s_cbranch_scc0 .Ld_a2\n"
      ".Ld_b2:\n"
      "s_add_u32 s55, s55, 106496\n"
      "s_add_u32 s61, s41, 4294967264\n"
      "s_add_u32 s37, s37, 106496\n"
      "buffer_store_dword v18, v4, s[8:11], s55 offen sc1\n"
      "s_cmp_lt_u32 s41, 32\n"
      "s_cselect_b32 s59, s39, 0x40000000\n"
      "s_add_u32 s39, s39, 147456\n"
      "s_cmp_lt_u32 s61, 32\n"
      "s_cselect_b32 s60, s40, 0x40000000\n"
      "s_add_u32 s55, s55, 106496\n"
      "s_add_u32 s40, s40, 4294856704\n"
      "s_add_u32 s41, s41, 6\n"
      "v_lshlrev_b32 v232, 16, v176\n"
      "v_lshlrev_b32 v233, 16, v177\n"
      "v_lshlrev_b32 v234, 16, v178\n"
      "v_lshlrev_b32 v235, 16, v179\n"
      "v_and_b32 v176, 0xffff0000, v176\n"
      "v_and_b32 v177, 0xffff0000, v177\n"
      "v_and_b32 v178, 0xffff0000, v178\n"
      "v_and_b32 v179, 0xffff0000, v179\n"
      "v_add_f32 v164, v164, v232\n"
      "v_add_f32 v166, v166, v233\n"
      "v_add_f32 v168, v168, v234\n"
      "v_add_f32 v170, v170, v235\n"
      "v_add_f32 v165, v165, v176\n"
      "v_add_f32 v167, v167, v177\n"
      "v_add_f32 v169, v169, v178\n"
      "v_add_f32 v171, v171, v179\n"
      "v_lshlrev_b32 v232, 16, v184\n"
      "v_lshlrev_b32 v233, 16, v185\n"
      "v_lshlrev_b32 v234, 16, v186\n"
      "v_lshlrev_b32 v235, 16, v187\n"
      "v_and_b32 v184, 0xffff0000, v184\n"
      "v_and_b32 v185, 0xffff0000, v185\n"
      "v_and_b32 v186, 0xffff0000, v186\n"
      "v_and_b32 v187, 0xffff0000, v187\n"
      "v_add_f32 v164, v164, v232\n"
      "v_add_f32 v166, v166, v233\n"
      "v_add_f32 v168, v168, v234\n"
      "v_add_f32 v170, v170, v235\n"
      "v_add_f32 v165, v165, v184\n"
      "v_add_f32 v167, v167, v185\n"
      "v_add_f32 v169, v169, v186\n"
      "v_add_f32 v171, v171, v187\n"
      "v_lshlrev_b32 v232, 16, v192\n"
      "v_lshlrev_b32 v233, 16, v193\n"
      "v_lshlrev_b32 v234, 16, v194\n"
      "v_lshlrev_b32 v235, 16, v195\n"
      "v_and_b32 v192, 0xffff0000, v192\n"
      "v_and_b32 v193, 0xffff0000, v193\n"
      "v_and_b32 v194, 0xffff0000, v194\n"
      "v_and_b32 v195, 0xffff0000, v195\n"
      "v_add_f32 v164, v164, v232\n"
      "v_add_f32 v166, v166, v233\n"
      "v_add_f32 v168, v168, v234\n"
      "v_add_f32 v170, v170, v235\n"
      "v_add_f32 v165, v165, v192\n"
      "v_add_f32 v167, v167, v193\n"
      "v_add_f32 v169, v169, v194\n"
      "v_add_f32 v171, v171, v195\n"
      "v_lshlrev_b32 v232, 16, v200\n"
      "v_lshlrev_b32 v233, 16, v201\n"
      "v_lshlrev_b32 v234, 16, v202\n"
      "v_lshlrev_b32 v235, 16, v203\n"
      "v_and_b32 v200, 0xffff0000, v200\n"
      "v_and_b32 v201, 0xffff0000, v201\n"
      "v_and_b32 v202, 0xffff0000, v202\n"
      "v_and_b32 v203, 0xffff0000, v203\n"
      "v_add_f32 v164, v164, v232\n"
      "v_add_f32 v166, v166, v233\n"
      "v_add_f32 v168, v168, v234\n"
      "v_add_f32 v170, v170, v235\n"
      "v_add_f32 v165, v165, v200\n"
      "v_add_f32 v167, v167, v201\n"
      "v_add_f32 v169, v169, v202\n"
      "v_add_f32 v171, v171, v203\n"
      "v_lshlrev_b32 v232, 16, v208\n"
      "v_lshlrev_b32 v233, 16, v209\n"
      "v_lshlrev_b32 v234, 16, v210\n"
      "v_lshlrev_b32 v235, 16, v211\n"
      "v_and_b32 v208, 0xffff0000, v208\n"
      "v_and_b32 v209, 0xffff0000, v209\n"
      "v_and_b32 v210, 0xffff0000, v210\n"
      "v_and_b32 v211, 0xffff0000, v211\n"
      "v_add_f32 v164, v164, v232\n"
      "v_add_f32 v166, v166, v233\n"
      "v_add_f32 v168, v168, v234\n"
      "v_add_f32 v170, v170, v235\n"
      "v_add_f32 v165, v165, v208\n"
      "v_add_f32 v167, v167, v209\n"
      "v_add_f32 v169, v169, v210\n"
      "v_add_f32 v171, v171, v211\n"
      "v_cvt_pk_bf16_f32 v176, v164, v165\n"
      "v_cvt_pk_bf16_f32 v177, v166, v167\n"
      "v_cvt_pk_bf16_f32 v178, v168, v169\n"
      "v_cvt_pk_bf16_f32 v179, v170, v171\n"
      "buffer_store_dwordx4 v[176:179], v8, s[16:19], s60 offen\n"
      "s_mov_b32 s22, s23\n"
      "s_waitcnt vmcnt(2)\n"
      ".Ld_c2:\n"
      "s_cmp_lg_u32 s54, 0\n"
      "s_cbranch_scc1 .Ld_d2\n"
      "v_max_u32 v232, v180, v181\n"
      "v_max3_u32 v232, v232, v188, v189\n"
      "v_max3_u32 v232, v232, v196, v197\n"
      "v_max3_u32 v232, v232, v204, v205\n"
      "v_max3_u32 v232, v232, v212, v213\n"
      "v_cmp_eq_u32 vcc, -1, v232\n"
      "s_cbranch_vccz .Ld_d2\n"
      "v_cmp_ne_u32 vcc, s21, v21\n"
      "s_nop 1\n"
      "s_cmp_eq_u64 vcc, 0\n"
      "s_cselect_b32 s54, 1, 0\n"
      "buffer_load_dword v21, v3, s[8:11], s56 offen sc1\n"
      "buffer_load_dwordx2 v[180:181], v13, s[8:11], s56 offen sc1\n"
      "buffer_load_dwordx2 v[188:189], v13, s[8:11], s57 offen sc1\n"
      "buffer_load_dwordx2 v[196:197], v13, s[8:11], s58 offen sc1\n"
      "buffer_load_dwordx2 v[204:205], v13, s[8:11], s62 offen sc1\n"
      "buffer_load_dwordx2 v[212:213], v13, s[8:11], s36 offen sc1\n"
      "s_waitcnt vmcnt(0)\n"
      "s_sub_u32 s22, s22, 1\n"
      "s_cbranch_scc0 .Ld_c2\n"
      ".Ld_d2:\n"
      "v_lshlrev_b32 v232, 16, v180\n"
      "v_lshlrev_b32 v233, 16, v181\n"
      "v_and_b32 v180, 0xffff0000, v180\n"
      "v_and_b32 v181, 0xffff0000, v181\n"
      "v_add_f32 v172, v172, v232\n"
      "v_add_f32 v174, v174, v233\n"
      "v_add_f32 v173, v173, v180\n"
      "v_add_f32 v175, v175, v181\n"
      "v_lshlrev_b32 v232, 16, v188\n"
      "v_lshlrev_b32 v233, 16, v189\n"
      "v_and_b32 v188, 0xffff0000, v188\n"
      "v_and_b32 v189, 0xffff0000, v189\n"
      "v_add_f32 v172, v172, v232\n"
      "v_add_f32 v174, v174, v233\n"
      "v_add_f32 v173, v173, v188\n"
      "v_add_f32 v175, v175, v189\n"
      "v_lshlrev_b32 v232, 16, v196\n"
      "v_lshlrev_b32 v233, 16, v197\n"
      "v_and_b32 v196, 0xffff0000, v196\n"
      "v_and_b32 v197, 0xffff0000, v197\n"
      "v_add_f32 v172, v172, v232\n"
      "v_add_f32 v174, v174, v233\n"
      "v_add_f32 v173, v173, v196\n"
      "v_add_f32 v175, v175, v197\n"
      "v_lshlrev_b32 v232, 16, v204\n"
      "v_lshlrev_b32 v233, 16, v205\n"
      "v_and_b32 v204, 0xffff0000, v204\n"
      "v_and_b32 v205, 0xffff0000, v205\n"
      "v_add_f32 v172, v172, v232\n"
      "v_add_f32 v174, v174, v233\n"
      "v_add_f32 v173, v173, v204\n"
      "v_add_f32 v175, v175, v205\n"
      "v_lshlrev_b32 v232, 16, v212\n"
      "v_lshlrev_b32 v233, 16, v213\n"
      "v_and_b32 v212, 0xffff0000, v212\n"
      "v_and_b32 v213, 0xffff0000, v213\n"
      "v_add_f32 v172, v172, v232\n"
      "v_add_f32 v174, v174, v233\n"
      "v_add_f32 v173, v173, v212\n"
      "v_add_f32 v175, v175, v213\n"
      "v_cvt_pk_bf16_f32 v180, v172, v173\n"
      "v_cvt_pk_bf16_f32 v181, v174, v175\n"
      "buffer_store_dwordx2 v[180:181], v9, s[16:19], s60 offen\n"
      "s_branch .Ld_end\n"
      ".Ld_out3:\n"
      "s_nop 8\n"
      "v_cvt_pk_bf16_f32 v228, v160, v161\n"
      "buffer_load_dwordx4 v[200:203], v0, s[8:11], s58 offen sc1\n"
      "v_cvt_pk_bf16_f32 v227, v158, v159\n"
      "buffer_store_dwordx4 v[224:227], v0, s[8:11], s37 offen sc1\n"
      "v_cvt_pk_bf16_f32 v229, v162, v163\n"
      "buffer_store_dwordx2 v[228:229], v13, s[8:11], s37 offen sc1\n"
      "s_add_u32 s37, s37, 106496\n"
      "buffer_load_dwordx2 v[204:205], v13, s[8:11], s58 offen sc1\n"
      "s_cmp_lt_u32 s41, 32\n"
      "s_cselect_b32 s59, s39, 0x40000000\n"
      "s_add_u32 s57, s56, 26624\n"
      "s_add_u32 s56, s56, 599040\n"
      "buffer_load_dwordx2 v[212:213], v13, s[8:11], s57 offen sc1\n"
      "s_add_u32 s39, s39, 147456\n"
      "buffer_load_dwordx4 v[208:211], v0, s[8:11], s57 offen sc1\n"
      "s_cmp_lt_u32 s61, 32\n"
      "s_cselect_b32 s60, s40, 0x40000000\n"
      "s_waitcnt vmcnt(12)\n"
      "v_lshlrev_b32 v234, 16, v178\n"
      "v_lshlrev_b32 v233, 16, v177\n"
      "v_lshlrev_b32 v232, 16, v176\n"
      "v_add_f32 v166, v166, v233\n"
      "s_add_u32 s55, s55, 106496\n"
      "v_add_f32 v164, v164, v232\n"
      "s_waitcnt vmcnt(11)\n"
      "v_lshlrev_b32 v233, 16, v181\n"
      "v_lshlrev_b32 v235, 16, v179\n"
      "v_and_b32 v176, 0xffff0000, v176\n"
      "v_lshlrev_b32 v232, 16, v180\n"
      "v_add_f32 v172, v172, v232\n"
      "v_and_b32 v181, 0xffff0000, v181\n"
      "v_add_f32 v168, v168, v234\n"
      "v_add_f32 v174, v174, v233\n"
      "v_add_f32 v165, v165, v176\n"
      "s_waitcnt vmcnt(10)\n"
      "v_lshlrev_b32 v232, 16, v184\n"
      "v_and_b32 v177, 0xffff0000, v177\n"
      "s_add_u32 s40, s40, 4294856704\n"
      "v_add_f32 v167, v167, v177\n"
      "v_and_b32 v180, 0xffff0000, v180\n"
      "v_add_f32 v164, v164, v232\n"
      "v_lshlrev_b32 v233, 16, v185\n"
      "v_add_f32 v166, v166, v233\n"
      "s_waitcnt vmcnt(7)\n"
      "v_lshlrev_b32 v232, 16, v188\n"
      "v_add_f32 v172, v172, v232\n"
      "v_and_b32 v185, 0xffff0000, v185\n"
      "v_lshlrev_b32 v232, 16, v192\n"
      "v_lshlrev_b32 v234, 16, v186\n"
      "v_add_f32 v164, v164, v232\n"
      "v_lshlrev_b32 v233, 16, v189\n"
      "s_waitcnt vmcnt(6)\n"
      "v_lshlrev_b32 v232, 16, v196\n"
      "v_add_f32 v174, v174, v233\n"
      "v_and_b32 v184, 0xffff0000, v184\n"
      "v_add_f32 v172, v172, v232\n"
      "v_add_f32 v170, v170, v235\n"
      "s_add_u32 s41, s41, 6\n"
      "v_add_f32 v165, v165, v184\n"
      "v_add_f32 v168, v168, v234\n"
      "v_lshlrev_b32 v233, 16, v193\n"
      "v_and_b32 v178, 0xffff0000, v178\n"
      "s_waitcnt vmcnt(5)\n"
      "v_lshlrev_b32 v232, 16, v200\n"
      "v_add_f32 v164, v164, v232\n"
      "v_and_b32 v186, 0xffff0000, v186\n"
      "s_waitcnt vmcnt(3)\n"
      "buffer_store_dword v18, v4, s[8:11], s55 offen sc1\n"
      "v_add_f32 v169, v169, v178\n"
      "v_and_b32 v200, 0xffff0000, v200\n"
      "v_add_f32 v173, v173, v180\n"
      "v_add_f32 v166, v166, v233\n"
      "v_lshlrev_b32 v234, 16, v194\n"
      "v_and_b32 v192, 0xffff0000, v192\n"
      "v_add_f32 v175, v175, v181\n"
      "v_and_b32 v189, 0xffff0000, v189\n"
      "s_waitcnt vmcnt(3)\n"
      "v_lshlrev_b32 v232, 16, v204\n"
      "v_lshlrev_b32 v235, 16, v187\n"
      "v_add_f32 v172, v172, v232\n"
      "s_waitcnt vmcnt(1)\n"
      "v_lshlrev_b32 v232, 16, v208\n"
      "v_add_f32 v170, v170, v235\n"
      "v_and_b32 v194, 0xffff0000, v194\n"
      "v_and_b32 v208, 0xffff0000, v208\n"
      "v_add_f32 v167, v167, v185\n"
      "v_lshlrev_b32 v233, 16, v197\n"
      "v_lshlrev_b32 v235, 16, v195\n"
      "v_add_f32 v168, v168, v234\n"
      "v_add_f32 v164, v164, v232\n"
      "v_add_f32 v165, v165, v192\n"
      "v_lshlrev_b32 v234, 16, v202\n"
      "v_add_f32 v174, v174, v233\n"
      "v_lshlrev_b32 v232, 16, v212\n"
      "v_and_b32 v179, 0xffff0000, v179\n"
      "v_add_f32 v169, v169, v186\n"
      "v_lshlrev_b32 v233, 16, v201\n"
      "v_add_f32 v166, v166, v233\n"
      "v_and_b32 v193, 0xffff0000, v193\n"
      "v_and_b32 v197, 0xffff0000, v197\n"
      "v_lshlrev_b32 v233, 16, v205\n"
      "v_add_f32 v170, v170, v235\n"
      "v_add_f32 v174, v174, v233\n"
      "v_lshlrev_b32 v235, 16, v203\n"
      "v_and_b32 v187, 0xffff0000, v187\n"
      "v_lshlrev_b32 v233, 16, v209\n"
      "v_add_f32 v170, v170, v235\n"
      "v_add_f32 v166, v166, v233\n"
      "v_and_b32 v201, 0xffff0000, v201\n"
      "v_add_f32 v165, v165, v200\n"
      "v_and_b32 v188, 0xffff0000, v188\n"
      "v_add_f32 v165, v165, v208\n"
      "v_add_f32 v168, v168, v234\n"
      "v_add_f32 v167, v167, v193\n"
      "v_lshlrev_b32 v233, 16, v213\n"
      "v_add_f32 v171, v171, v179\n"
      "v_add_f32 v167, v167, v201\n"
      "v_lshlrev_b32 v234, 16, v210\n"
      "v_and_b32 v213, 0xffff0000, v213\n"
      "v_add_f32 v173, v173, v188\n"
      "v_and_b32 v209, 0xffff0000, v209\n"
      "v_add_f32 v175, v175, v189\n"
      "v_add_f32 v167, v167, v209\n"
      "v_and_b32 v203, 0xffff0000, v203\n"
      "v_add_f32 v175, v175, v197\n"
      "v_and_b32 v204, 0xffff0000, v204\n"
      "v_and_b32 v195, 0xffff0000, v195\n"
      "v_add_f32 v172, v172, v232\n"
      "v_add_f32 v169, v169, v194\n"
      "v_and_b32 v196, 0xffff0000, v196\n"
      "v_and_b32 v212, 0xffff0000, v212\n"
      "v_cvt_pk_bf16_f32 v177, v166, v167\n"
      "v_add_f32 v171, v171, v187\n"
      "v_and_b32 v205, 0xffff0000, v205\n"
      "s_add_u32 s55, s55, 106496\n"
      "v_and_b32 v202, 0xffff0000, v202\n"
      "v_add_f32 v169, v169, v202\n"
      "v_and_b32 v210, 0xffff0000, v210\n"
      "v_add_f32 v173, v173, v196\n"
      "v_lshlrev_b32 v235, 16, v211\n"
      "v_add_f32 v170, v170, v235\n"
      "v_add_f32 v171, v171, v195\n"
      "v_add_f32 v174, v174, v233\n"
      "v_add_f32 v169, v169, v210\n"
      "v_add_f32 v175, v175, v205\n"
      "v_add_f32 v168, v168, v234\n"
      "v_cvt_pk_bf16_f32 v176, v164, v165\n"
      "v_and_b32 v211, 0xffff0000, v211\n"
      "v_add_f32 v171, v171, v203\n"
      "v_cvt_pk_bf16_f32 v178, v168, v169\n"
      "v_add_f32 v175, v175, v213\n"
      "v_add_f32 v171, v171, v211\n"
      "v_add_f32 v173, v173, v204\n"
      "v_cvt_pk_bf16_f32 v179, v170, v171\n"
      "v_add_f32 v173, v173, v212\n"
      "v_cvt_pk_bf16_f32 v180, v172, v173\n"
      "v_cvt_pk_bf16_f32 v181, v174, v175\n"
      "buffer_store_dwordx2 v[180:181], v9, s[16:19], s60 offen\n"
      "buffer_store_dwordx4 v[176:179], v8, s[16:19], s60 offen\n"
      "s_branch .Ld_end\n"
      ".Ld_out4:\n"
      "v_and_b32 v192, 0xffff0000, v192\n"
      "s_nop 7\n"
      "v_cvt_pk_bf16_f32 v229, v162, v163\n"
      "v_add_f32 v175, v175, v181\n"
      "v_cvt_pk_bf16_f32 v228, v160, v161\n"
      "v_and_b32 v189, 0xffff0000, v189\n"
      "s_waitcnt vmcnt(11)\n"
      "v_lshlrev_b32 v232, 16, v204\n"
      "buffer_store_dwordx2 v[228:229], v13, s[8:11], s37 offen sc1\n"
      "v_lshlrev_b32 v235, 16, v187\n"
      "v_add_f32 v172, v172, v232\n"
      "s_waitcnt vmcnt(10)\n"
      "v_lshlrev_b32 v232, 16, v208\n"
      "v_add_f32 v170, v170, v235\n"
      "v_and_b32 v194, 0xffff0000, v194\n"
      "v_and_b32 v208, 0xffff0000, v208\n"
      "v_add_f32 v167, v167, v185\n"
      "v_lshlrev_b32 v233, 16, v197\n"
      "v_lshlrev_b32 v235, 16, v195\n"
      "v_add_f32 v168, v168, v234\n"
      "v_add_f32 v164, v164, v232\n"
      "v_add_f32 v165, v165, v192\n"
      "v_lshlrev_b32 v234, 16, v202\n"
      "v_add_f32 v174, v174, v233\n"
      "v_lshlrev_b32 v232, 16, v212\n"
      "v_and_b32 v179, 0xffff0000, v179\n"
      "v_add_f32 v169, v169, v186\n"
      "v_lshlrev_b32 v233, 16, v201\n"
      "v_add_f32 v166, v166, v233\n"
      "v_and_b32 v193, 0xffff0000, v193\n"
      "v_and_b32 v197, 0xffff0000, v197\n"
      "v_lshlrev_b32 v233, 16, v205\n"
      "v_add_f32 v170, v170, v235\n"
      "v_add_f32 v174, v174, v233\n"
      "v_lshlrev_b32 v235, 16, v203\n"
      "v_and_b32 v187, 0xffff0000, v187\n"
      "v_lshlrev_b32 v233, 16, v209\n"
      "v_add_f32 v170, v170, v235\n"
      "v_add_f32 v166, v166, v233\n"
      "v_and_b32 v201, 0xffff0000, v201\n"
      "v_add_f32 v165, v165, v200\n"
      "v_and_b32 v188, 0xffff0000, v188\n"
      "v_add_f32 v165, v165, v208\n"
      "v_add_f32 v168, v168, v234\n"
      "v_add_f32 v167, v167, v193\n"
      "v_lshlrev_b32 v233, 16, v213\n"
      "v_add_f32 v171, v171, v179\n"
      "v_add_f32 v167, v167, v201\n"
      "v_lshlrev_b32 v234, 16, v210\n"
      "v_and_b32 v213, 0xffff0000, v213\n"
      "v_add_f32 v173, v173, v188\n"
      "v_and_b32 v209, 0xffff0000, v209\n"
      "v_add_f32 v175, v175, v189\n"
      "v_add_f32 v167, v167, v209\n"
      "v_and_b32 v203, 0xffff0000, v203\n"
      "v_add_f32 v175, v175, v197\n"
      "v_and_b32 v204, 0xffff0000, v204\n"
      "v_and_b32 v195, 0xffff0000, v195\n"
      "v_add_f32 v172, v172, v232\n"
      "v_add_f32 v169, v169, v194\n"
      "v_and_b32 v196, 0xffff0000, v196\n"
      "v_and_b32 v212, 0xffff0000, v212\n"
      "v_cvt_pk_bf16_f32 v177, v166, v167\n"
      "v_add_f32 v171, v171, v187\n"
      "v_and_b32 v205, 0xffff0000, v205\n"
      "s_add_u32 s55, s55, 106496\n"
      "v_and_b32 v202, 0xffff0000, v202\n"
      "v_add_f32 v169, v169, v202\n"
      "v_and_b32 v210, 0xffff0000, v210\n"
      "v_add_f32 v173, v173, v196\n"
      "v_lshlrev_b32 v235, 16, v211\n"
      "v_add_f32 v170, v170, v235\n"
      "v_add_f32 v171, v171, v195\n"
      "s_add_u32 s37, s37, 106496\n"
      "s_waitcnt vmcnt(0)\n"
      "buffer_store_dword v18, v4, s[8:11], s55 offen sc1\n"
      "v_add_f32 v174, v174, v233\n"
      "v_add_f32 v169, v169, v210\n"
      "v_add_f32 v175, v175, v205\n"
      "v_add_f32 v168, v168, v234\n"
      "v_cvt_pk_bf16_f32 v176, v164, v165\n"
      "v_and_b32 v211, 0xffff0000, v211\n"
      "v_add_f32 v171, v171, v203\n"
      "v_cvt_pk_bf16_f32 v178, v168, v169\n"
      "v_add_f32 v175, v175, v213\n"
      "v_add_f32 v171, v171, v211\n"
      "v_add_f32 v173, v173, v204\n"
      "v_cvt_pk_bf16_f32 v179, v170, v171\n"
      "v_add_f32 v173, v173, v212\n"
      "v_cvt_pk_bf16_f32 v180, v172, v173\n"
      "v_cvt_pk_bf16_f32 v181, v174, v175\n"
      "buffer_store_dwordx2 v[180:181], v9, s[16:19], s60 offen\n"
      "buffer_store_dwordx4 v[176:179], v8, s[16:19], s60 offen\n"
      "s_add_u32 s55, s55, 106496\n"
      "s_branch .Ld_end\n"
      ".Ld_in1:\n"
      "s_add_u32 s36, s36, 165888\n"
      "s_min_u32 s36, s36, s6\n"
      "s_mov_b32 s54, s36\n"
      "s_add_u32 s36, s36, 165888\n"
      "s_min_u32 s36, s36, s6\n"
      "buffer_load_dword v20, v2, s[8:11], s36 offen sc1\n"
      "s_mov_b32 s54, s36\n"
      "s_add_u32 m0, s43, 24576\n"
      "s_add_u32 s36, s36, 165888\n"
      "s_waitcnt vmcnt(0)\n"
      "v_cmp_ne_u32 vcc, s21, v20\n"
      "s_cbranch_vccnz .Lsp_d1_17\n"
      ".Lbk_d1_17:\n"
      "buffer_load_dwordx4 v0, s[8:11], s54 offen offset:0 sc1 lds\n"
      "buffer_load_dwordx4 v0, s[8:11], s54 offen offset:1024 sc1 lds\n"
      "buffer_load_dwordx4 v0, s[8:11], s54 offen offset:2048 sc1 lds\n"
      "s_min_u32 s36, s36, s6\n"
      "buffer_load_dword v20, v2, s[8:11], s36 offen sc1\n"
      "buffer_load_dwordx4 v0, s[8:11], s54 offen offset:3072 sc1 lds\n"
      "s_add_u32 m0, s43, 28672\n"
      "s_nop 0\n"
      "buffer_load_dwordx4 v1, s[8:11], s54 offen offset:0 sc1 lds\n"
      "buffer_load_dwordx4 v1, s[8:11], s54 offen offset:1024 sc1 lds\n"
      "s_add_u32 m0, s43, 49152\n"
      "s_mov_b32 s54, s36\n"
      "v_add_u32 v14, 1, v14\n"
      "s_waitcnt vmcnt(3)\n"
      "v_cmp_ne_u32 vcc, s21, v20\n"
      "s_add_u32 s36, s36, 165888\n"
      "s_min_u32 s36, s36, s6\n"
      "s_cbranch_vccnz .Lsp_d2_18\n"
      ".Lbk_d2_18:\n"
      "s_waitcnt vmcnt(0)\n"
      "ds_write_b32 v16, v14\n"
      "buffer_load_dword v20, v2, s[8:11], s36 offen sc1\n"
      "buffer_load_dwordx4 v0, s[8:11], s54 offen offset:3072 sc1 lds\n"
      "s_add_u32 s55, s55, 106496\n"
      "s_add_u32 s37, s37, 173056\n"
      "buffer_load_dwordx4 v0, s[8:11], s54 offen offset:1024 sc1 lds\n"
      "ds_read_b32 v15, v17\n"
      "buffer_load_dwordx4 v0, s[8:11], s54 offen offset:2048 sc1 lds\n"
      "s_waitcnt lgkmcnt(0)\n"
      "v_cmp_gt_i32 vcc, v14, v15\n"
      "buffer_load_dwordx4 v0, s[8:11], s54 offen offset:0 sc1 lds\n"
      "s_add_u32 m0, s43, 53248\n"
      "s_cbranch_vccnz .Lsp_dy1_19\n"
      ".Lbk_dy1_19:\n"
      "buffer_load_dwordx4 v1, s[8:11], s54 offen offset:0 sc1 lds\n"
      "ds_read_b128 v[56:59], v0 offset:24576\n"
      "ds_read_b128 v[64:67], v0 offset:26624\n"
      "buffer_load_dwordx4 v1, s[8:11], s54 offen offset:1024 sc1 lds\n"
      "ds_read_b128 v[68:71], v0 offset:27648\n"
      "ds_read_b128 v[80:83], v0 offset:30720\n"
      "ds_read_b128 v[60:63], v0 offset:25600\n"
      "ds_read_b128 v[92:95], v0 offset:33792\n"
      "ds_read_b128 v[84:87], v0 offset:31744\n"
      "ds_read_b128 v[72:75], v0 offset:28672\n"
      "ds_read_b128 v[76:79], v0 offset:29696\n"
      "ds_read_b128 v[100:103], v0 offset:35840\n"
      "ds_read_b128 v[96:99], v0 offset:34816\n"
      "ds_read_b128 v[116:119], v0 offset:39936\n"
      "ds_read_b128 v[120:123], v0 offset:40960\n"
      "ds_read_b128 v[108:111], v0 offset:37888\n"
      "ds_read_b128 v[88:91], v0 offset:32768\n"
      "s_waitcnt vmcnt(0) lgkmcnt(0)\n"
      "s_nop 4\n"
      "s_branch .Ld_t1\n"
      ".Ld_in2:\n"
      "s_add_u32 s36, s36, 165888\n"
      "s_min_u32 s36, s36, s6\n"
      "s_mov_b32 s54, s36\n"
      "s_add_u32 s36, s36, 165888\n"
      "s_min_u32 s36, s36, s6\n"
      "s_mov_b32 s54, s36\n"
      "s_add_u32 s36, s36, 165888\n"
      "s_min_u32 s36, s36, s6\n"
      "buffer_load_dword v20, v2, s[8:11], s36 offen sc1\n"
      "s_add_u32 m0, s43, 49152\n"
      "s_mov_b32 s54, s36\n"
      "s_waitcnt vmcnt(0)\n"
      "v_cmp_ne_u32 vcc, s21, v20\n"
      "s_add_u32 s36, s36, 165888\n"
      "s_min_u32 s36, s36, s6\n"
      "s_cbranch_vccnz .Lsp_d2_20\n"
      ".Lbk_d2_20:\n"
      "buffer_load_dword v20, v2, s[8:11], s36 offen sc1\n"
      "buffer_load_dwordx4 v0, s[8:11], s54 offen offset:3072 sc1 lds\n"
      "s_add_u32 s55, s55, 106496\n"
      "s_add_u32 s37, s37, 173056\n"
      "buffer_load_dwordx4 v0, s[8:11], s54 offen offset:1024 sc1 lds\n"
      "buffer_load_dwordx4 v0, s[8:11], s54 offen offset:2048 sc1 lds\n"
      "buffer_load_dwordx4 v0, s[8:11], s54 offen offset:0 sc1 lds\n"
      "s_add_u32 m0, s43, 53248\n"
      "s_nop 0\n"
      "buffer_load_dwordx4 v1, s[8:11], s54 offen offset:0 sc1 lds\n"
      "buffer_load_dwordx4 v1, s[8:11], s54 offen offset:1024 sc1 lds\n"
      "s_mov_b32 s54, s36\n"
      "s_add_u32 s36, s36, 165888\n"
      "s_add_u32 m0, s43, 73728\n"
      "s_waitcnt vmcnt(6)\n"
      "v_cmp_ne_u32 vcc, s21, v20\n"
      "s_add_u32 s38, s38, 147456\n"
      "s_min_u32 s36, s36, s6\n"
      "s_cbranch_vccnz .Lsp_d3_21\n"
      ".Lbk_d3_21:\n"
      "buffer_load_dwordx4 v0, s[8:11], s54 offen offset:2048 sc1 lds\n"
      "v_add_u32 v14, 1, v14\n"
      "buffer_load_dwordx4 v0, s[8:11], s54 offen offset:1024 sc1 lds\n"
      "buffer_load_dword v20, v2, s[8:11], s36 offen sc1\n"
      "s_waitcnt vmcnt(3)\n"
      "ds_write_b32 v16, v14\n"
      "buffer_load_dwordx4 v0, s[8:11], s54 offen offset:0 sc1 lds\n"
      "buffer_load_dwordx4 v0, s[8:11], s54 offen offset:3072 sc1 lds\n"
      "s_add_u32 m0, s43, 77824\n"
      "ds_read_b32 v15, v17\n"
      "s_add_u32 s55, s55, 173056\n"
      "buffer_load_dwordx4 v1, s[8:11], s54 offen offset:0 sc1 lds\n"
      "s_waitcnt lgkmcnt(0)\n"
      "v_cmp_gt_i32 vcc, v14, v15\n"
      "buffer_load_dwordx4 v1, s[8:11], s54 offen offset:1024 sc1 lds\n"
      "s_cbranch_vccnz .Lsp_dy2_22\n"
      ".Lbk_dy2_22:\n"
      "ds_read_b128 v[68:71], v22 offset:3072\n"
      "ds_read_b128 v[60:63], v22 offset:1024\n"
      "ds_read_b128 v[80:83], v22 offset:6144\n"
      "ds_read_b128 v[72:75], v22 offset:4096\n"
      "ds_read_b128 v[76:79], v22 offset:5120\n"
      "ds_read_b128 v[96:99], v22 offset:10240\n"
      "ds_read_b128 v[84:87], v22 offset:7168\n"
      "ds_read_b128 v[56:59], v22 offset:0\n"
      "ds_read_b128 v[92:95], v22 offset:9216\n"
      "s_mov_b32 s54, s36\n"
      "ds_read_b128 v[64:67], v22 offset:2048\n"
      "ds_read_b128 v[100:103], v22 offset:11264\n"
      "ds_read_b128 v[132:135], v22 offset:19456\n"
      "ds_read_b128 v[112:115], v22 offset:14336\n"
      "s_add_u32 m0, s43, 98304\n"
      "s_waitcnt vmcnt(0) lgkmcnt(0)\n"
      "s_nop 4\n"
      "s_branch .Ld_t2\n"
      ".Ld_in3:\n"
      "s_add_u32 s36, s36, 165888\n"
      "s_min_u32 s36, s36, s6\n"
      "s_mov_b32 s54, s36\n"
      "s_add_u32 s36, s36, 165888\n"
      "s_min_u32 s36, s36, s6\n"
      "s_mov_b32 s54, s36\n"
      "s_add_u32 s36, s36, 165888\n"
      "s_min_u32 s36, s36, s6\n"
      "s_mov_b32 s54, s36\n"
      "s_add_u32 s36, s36, 165888\n"
      "s_min_u32 s36, s36, s6\n"
      "buffer_load_dword v20, v2, s[8:11], s36 offen sc1\n"
      "s_add_u32 s55, s55, 106496\n"
      "s_add_u32 s37, s37, 173056\n"
      "s_mov_b32 s54, s36\n"
      "s_add_u32 s36, s36, 165888\n"
      "s_add_u32 m0, s43, 73728\n"
      "s_waitcnt vmcnt(0)\n"
      "v_cmp_ne_u32 vcc, s21, v20\n"
      "s_add_u32 s38, s38, 147456\n"
      "s_min_u32 s36, s36, s6\n"
      "s_cbranch_vccnz .Lsp_d3_23\n"
      ".Lbk_d3_23:\n"
      "buffer_load_dwordx4 v0, s[8:11], s54 offen offset:2048 sc1 lds\n"
      "buffer_load_dwordx4 v0, s[8:11], s54 offen offset:1024 sc1 lds\n"
      "buffer_load_dword v20, v2, s[8:11], s36 offen sc1\n"
      "buffer_load_dwordx4 v0, s[8:11], s54 offen offset:0 sc1 lds\n"
      "buffer_load_dwordx4 v0, s[8:11], s54 offen offset:3072 sc1 lds\n"
      "s_add_u32 m0, s43, 77824\n"
      "s_add_u32 s55, s55, 173056\n"
      "buffer_load_dwordx4 v1, s[8:11], s54 offen offset:0 sc1 lds\n"
      "buffer_load_dwordx4 v1, s[8:11], s54 offen offset:1024 sc1 lds\n"
      "s_mov_b32 s54, s36\n"
      "s_add_u32 m0, s43, 98304\n"
      "s_add_u32 s36, s36, 165888\n"
      "v_add_u32 v14, 1, v14\n"
      "s_waitcnt vmcnt(4)\n"
      "v_cmp_ne_u32 vcc, s21, v20\n"
      "s_cbranch_vccnz .Lsp_d4_24\n"
      ".Lbk_d4_24:\n"
      "s_min_u32 s36, s36, s6\n"
      "buffer_load_dwordx4 v0, s[8:11], s54 offen offset:0 sc1 lds\n"
      "buffer_load_dwordx4 v0, s[8:11], s54 offen offset:2048 sc1 lds\n"
      "s_waitcnt vmcnt(2)\n"
      "ds_write_b32 v16, v14\n"
      "buffer_load_dword v20, v2, s[8:11], s36 offen sc1\n"
      "buffer_load_dwordx4 v0, s[8:11], s54 offen offset:3072 sc1 lds\n"
      "ds_read_b32 v15, v17\n"
      "buffer_load_dwordx4 v0, s[8:11], s54 offen offset:1024 sc1 lds\n"
      "s_add_u32 m0, s43, 102400\n"
      "s_nop 0\n"
      "buffer_load_dwordx4 v1, s[8:11], s54 offen offset:1024 sc1 lds\n"
      "s_waitcnt lgkmcnt(0)\n"
      "v_cmp_gt_i32 vcc, v14, v15\n"
      "buffer_load_dwordx4 v1, s[8:11], s54 offen offset:0 sc1 lds\n"
      "s_cbranch_vccnz .Lsp_dy3_25\n"
      ".Lbk_dy3_25:\n"
      "ds_read_b128 v[60:63], v22 offset:25600\n"
      "ds_read_b128 v[64:67], v22 offset:26624\n"
      "s_mov_b32 s54, s36\n"
      "ds_read_b128 v[72:75], v22 offset:28672\n"
      "ds_read_b128 v[68:71], v22 offset:27648\n"
      "ds_read_b128 v[92:95], v22 offset:33792\n"
      "ds_read_b128 v[56:59], v22 offset:24576\n"
      "ds_read_b128 v[100:103], v22 offset:35840\n"
      "ds_read_b128 v[80:83], v22 offset:30720\n"
      "ds_read_b128 v[112:115], v22 offset:38912\n"
      "s_add_u32 s37, s37, 106496\n"
      "ds_read_b128 v[88:91], v22 offset:32768\n"
      "ds_read_b128 v[96:99], v22 offset:34816\n"
      "ds_read_b128 v[76:79], v22 offset:29696\n"
      "ds_read_b128 v[116:119], v22 offset:39936\n"
      "ds_read_b128 v[84:87], v22 offset:31744\n"
      "ds_read_b128 v[104:107], v22 offset:36864\n"
      "s_waitcnt vmcnt(0) lgkmcnt(0)\n"
      "s_nop 4\n"
      "s_branch .Ld_t3\n"
      ".Ld_in4:\n"
      "s_add_u32 s36, s36, 165888\n"
      "s_min_u32 s36, s36, s6\n"
      "s_mov_b32 s54, s36\n"
      "s_add_u32 s36, s36, 165888\n"
      "s_min_u32 s36, s36, s6\n"
      "s_mov_b32 s54, s36\n"
      "s_add_u32 s36, s36, 165888\n"
      "s_min_u32 s36, s36, s6\n"
      "s_mov_b32 s54, s36\n"
      "s_add_u32 s36, s36, 165888\n"
      "s_min_u32 s36, s36, s6\n"
      "s_add_u32 s55, s55, 106496\n"
      "s_add_u32 s37, s37, 173056\n"
      "s_mov_b32 s54, s36\n"
      "s_add_u32 s36, s36, 165888\n"
      "s_add_u32 s38, s38, 147456\n"
      "s_min_u32 s36, s36, s6\n"
      "buffer_load_dword v20, v2, s[8:11], s36 offen sc1\n"
      "s_add_u32 s55, s55, 173056\n"
      "s_mov_b32 s54, s36\n"
      "s_add_u32 m0, s43, 98304\n"
      "s_add_u32 s36, s36, 165888\n"
      "s_waitcnt vmcnt(0)\n"
      "v_cmp_ne_u32 vcc, s21, v20\n"
      "s_cbranch_vccnz .Lsp_d4_26\n"
      ".Lbk_d4_26:\n"
      "s_min_u32 s36, s36, s6\n"
      "buffer_load_dwordx4 v0, s[8:11], s54 offen offset:0 sc1 lds\n"
      "buffer_load_dwordx4 v0, s[8:11], s54 offen offset:2048 sc1 lds\n"
      "buffer_load_dword v20, v2, s[8:11], s36 offen sc1\n"
      "buffer_load_dwordx4 v0, s[8:11], s54 offen offset:3072 sc1 lds\n"
      "buffer_load_dwordx4 v0, s[8:11], s54 offen offset:1024 sc1 lds\n"
      "s_add_u32 m0, s43, 102400\n"
      "s_nop 0\n"
      "buffer_load_dwordx4 v1, s[8:11], s54 offen offset:1024 sc1 lds\n"
      "buffer_load_dwordx4 v1, s[8:11], s54 offen offset:0 sc1 lds\n"
      "s_mov_b32 s54, s36\n"
      "s_add_u32 s37, s37, 106496\n"
      "s_add_u32 m0, s43, 122880\n"
      "s_waitcnt vmcnt(4)\n"
      "v_cmp_ne_u32 vcc, s21, v20\n"
      "s_add_u32 s36, s36, 165888\n"
      "s_add_u32 s55, s55, 106496\n"
      "s_cbranch_vccnz .Lsp_d5_27\n"
      ".Lbk_d5_27:\n"
      "buffer_load_dwordx4 v0, s[8:11], s54 offen offset:1024 sc1 lds\n"
      "v_add_u32 v14, 1, v14\n"
      "buffer_load_dwordx4 v0, s[8:11], s54 offen offset:0 sc1 lds\n"
      "buffer_load_dwordx4 v0, s[8:11], s54 offen offset:2048 sc1 lds\n"
      "s_waitcnt vmcnt(3)\n"
      "ds_write_b32 v16, v14\n"
      "buffer_load_dwordx4 v0, s[8:11], s54 offen offset:3072 sc1 lds\n"
      "s_min_u32 s36, s36, s6\n"
      "s_add_u32 m0, s43, 126976\n"
      "s_nop 0\n"
      "buffer_load_dwordx4 v1, s[8:11], s54 offen offset:1024 sc1 lds\n"
      "ds_read_b32 v15, v17\n"
      "buffer_load_dword v20, v2, s[8:11], s36 offen sc1\n"
      "s_waitcnt lgkmcnt(0)\n"
      "v_cmp_gt_i32 vcc, v14, v15\n"
      "buffer_load_dwordx4 v1, s[8:11], s54 offen offset:0 sc1 lds\n"
      "s_cbranch_vccnz .Lsp_dy4_28\n"
      ".Lbk_dy4_28:\n"
      "ds_read_b128 v[60:63], v23 offset:1024\n"
      "ds_read_b128 v[64:67], v23 offset:2048\n"
      "ds_read_b128 v[72:75], v23 offset:4096\n"
      "ds_read_b128 v[68:71], v23 offset:3072\n"
      "s_add_u32 s61, s41, 4294967264\n"
      "ds_read_b128 v[80:83], v23 offset:6144\n"
      "ds_read_b128 v[84:87], v23 offset:7168\n"
      "ds_read_b128 v[76:79], v23 offset:5120\n"
      "s_add_u32 s37, s37, 106496\n"
      "ds_read_b128 v[56:59], v23 offset:0\n"
      "ds_read_b128 v[88:91], v23 offset:8192\n"
      "ds_read_b128 v[96:99], v23 offset:10240\n"
      "ds_read_b128 v[100:103], v23 offset:11264\n"
      "ds_read_b128 v[132:135], v23 offset:19456\n"
      "ds_read_b128 v[104:107], v23 offset:12288\n"
      "s_waitcnt vmcnt(0) lgkmcnt(0)\n"
      "s_nop 4\n"
      "s_branch .Ld_t4\n"
      ".Ld_in5:\n"
      "s_add_u32 s36, s36, 165888\n"
      "s_min_u32 s36, s36, s6\n"
      "s_mov_b32 s54, s36\n"
      "s_add_u32 s36, s36, 165888\n"
      "s_min_u32 s36, s36, s6\n"
      "s_mov_b32 s54, s36\n"
      "s_add_u32 s36, s36, 165888\n"
      "s_min_u32 s36, s36, s6\n"
      "s_mov_b32 s54, s36\n"
      "s_add_u32 s36, s36, 165888\n"
      "s_min_u32 s36, s36, s6\n"
      "s_add_u32 s55, s55, 106496\n"
      "s_add_u32 s37, s37, 173056\n"
      "s_mov_b32 s54, s36\n"
      "s_add_u32 s36, s36, 165888\n"
      "s_add_u32 s38, s38, 147456\n"
      "s_min_u32 s36, s36, s6\n"
      "s_add_u32 s55, s55, 173056\n"
      "s_mov_b32 s54, s36\n"
      "s_add_u32 s36, s36, 165888\n"
      "s_min_u32 s36, s36, s6\n"
      "buffer_load_dword v20, v2, s[8:11], s36 offen sc1\n"
      "s_mov_b32 s54, s36\n"
      "s_add_u32 s37, s37, 106496\n"
      "s_add_u32 m0, s43, 122880\n"
      "s_waitcnt vmcnt(0)\n"
      "v_cmp_ne_u32 vcc, s21, v20\n"
      "s_add_u32 s36, s36, 165888\n"
      "s_add_u32 s55, s55, 106496\n"
      "s_cbranch_vccnz .Lsp_d5_29\n"
      ".Lbk_d5_29:\n"
      "buffer_load_dwordx4 v0, s[8:11], s54 offen offset:1024 sc1 lds\n"
      "buffer_load_dwordx4 v0, s[8:11], s54 offen offset:0 sc1 lds\n"
      "buffer_load_dwordx4 v0, s[8:11], s54 offen offset:2048 sc1 lds\n"
      "buffer_load_dwordx4 v0, s[8:11], s54 offen offset:3072 sc1 lds\n"
      "s_min_u32 s36, s36, s6\n"
      "s_add_u32 m0, s43, 126976\n"
      "s_nop 0\n"
      "buffer_load_dwordx4 v1, s[8:11], s54 offen offset:1024 sc1 lds\n"
      "buffer_load_dword v20, v2, s[8:11], s36 offen sc1\n"
      "buffer_load_dwordx4 v1, s[8:11], s54 offen offset:0 sc1 lds\n"
      "s_add_u32 s61, s41, 4294967264\n"
      "s_add_u32 s37, s37, 106496\n"
      "s_add_u32 m0, s43, 0\n"
      "s_mov_b32 s54, s36\n"
      "v_add_u32 v14, 1, v14\n"
      "s_add_u32 s36, s36, 165888\n"
      "s_add_u32 s37, s37, 106496\n"
      "s_min_u32 s36, s36, s6\n"
      "s_cmp_lt_u32 s41, 32\n"
      "s_cselect_b32 s59, s39, 0x40000000\n"
      "s_add_u32 s56, s56, 599040\n"
      "s_waitcnt vmcnt(1)\n"
      "v_cmp_ne_u32 vcc, s21, v20\n"
      "s_add_u32 s39, s39, 147456\n"
      "s_waitcnt vmcnt(0)\n"
      "ds_write_b32 v16, v14\n"
      "s_cmp_lt_u32 s61, 32\n"
      "s_cselect_b32 s60, s40, 0x40000000\n"
      "s_cbranch_vccnz .Lsp_d0_30\n"
      ".Lbk_d0_30:\n"
      "buffer_load_dwordx4 v0, s[8:11], s54 offen offset:0 sc1 lds\n"
      "s_add_u32 s55, s55, 106496\n"
      "buffer_load_dwordx4 v0, s[8:11], s54 offen offset:1024 sc1 lds\n"
      "buffer_load_dword v20, v2, s[8:11], s36 offen sc1\n"
      "ds_read_b32 v15, v17\n"
      "buffer_load_dwordx4 v0, s[8:11], s54 offen offset:2048 sc1 lds\n"
      "s_add_u32 s40, s40, 4294856704\n"
      "buffer_load_dwordx4 v0, s[8:11], s54 offen offset:3072 sc1 lds\n"
      "s_waitcnt lgkmcnt(0)\n"
      "v_cmp_gt_i32 vcc, v14, v15\n"
      "s_add_u32 m0, s43, 4096\n"
      "s_nop 0\n"
      "buffer_load_dwordx4 v1, s[8:11], s54 offen offset:1024 sc1 lds\n"
      "s_cbranch_vccnz .Lsp_dy5_31\n"
      ".Lbk_dy5_31:\n"
      "ds_read_b128 v[56:59], v23 offset:24576\n"
      "ds_read_b128 v[64:67], v23 offset:26624\n"
      "buffer_load_dwordx4 v1, s[8:11], s54 offen offset:0 sc1 lds\n"
      "ds_read_b128 v[76:79], v23 offset:29696\n"
      "s_add_u32 s41, s41, 6\n"
      "ds_read_b128 v[88:91], v23 offset:32768\n"
      "ds_read_b128 v[68:71], v23 offset:27648\n"
      "ds_read_b128 v[96:99], v23 offset:34816\n"
      "ds_read_b128 v[80:83], v23 offset:30720\n"
      "ds_read_b128 v[72:75], v23 offset:28672\n"
      "s_mov_b32 s54, s36\n"
      "ds_read_b128 v[112:115], v23 offset:38912\n"
      "ds_read_b128 v[60:63], v23 offset:25600\n"
      "ds_read_b128 v[104:107], v23 offset:36864\n"
      "ds_read_b128 v[84:87], v23 offset:31744\n"
      "ds_read_b128 v[140:143], v23 offset:46080\n"
      "ds_read_b128 v[100:103], v23 offset:35840\n"
      "s_waitcnt vmcnt(0) lgkmcnt(0)\n"
      "s_nop 4\n"
      "s_branch .Ld_t5\n"
      ".Ld_end:\n"
      "s_endpgm\n"
      ".Lsp_d0_1:\n"
      "s_mov_b32 s22, s23\n"
      ".Lsl_d0_1:\n"
      "buffer_load_dword v20, v2, s[8:11], s54 offen sc1\n"
      "s_waitcnt vmcnt(0)\n"
      "v_cmp_ne_u32 vcc, s21, v20\n"
      "s_cbranch_vccz .Lbk_d0_1\n"
      "s_sleep 6\n"
      "s_sub_u32 s22, s22, 1\n"
      "s_cbranch_scc0 .Lsl_d0_1\n"
      "s_mov_b32 s23, 0\n"
      "s_branch .Lbk_d0_1\n"
      ".Lsp_d1_2:\n"
      "s_mov_b32 s22, s23\n"
      ".Lsl_d1_2:\n"
      "buffer_load_dword v20, v2, s[8:11], s54 offen sc1\n"
      "s_waitcnt vmcnt(0)\n"
      "v_cmp_ne_u32 vcc, s21, v20\n"
      "s_cbranch_vccz .Lbk_d1_2\n"
      "s_sleep 6\n"
      "s_sub_u32 s22, s22, 1\n"
      "s_cbranch_scc0 .Lsl_d1_2\n"
      "s_mov_b32 s23, 0\n"
      "s_branch .Lbk_d1_2\n"
      ".Lsp_dy0_3:\n"
      "s_lshl_b32 s22, s23, 4\n"
      ".Lsl_dy0_3:\n"
      "ds_read_b32 v15, v17\n"
      "s_waitcnt lgkmcnt(0)\n"
      "v_cmp_gt_i32 vcc, v14, v15\n"
      "s_cbranch_vccz .Lbk_dy0_3\n"
      "s_sub_u32 s22, s22, 1\n"
      "s_cbranch_scc0 .Lsl_dy0_3\n"
      "s_mov_b32 s23, 0\n"
      "s_branch .Lbk_dy0_3\n"
      ".Lsp_d2_4:\n"
      "s_mov_b32 s22, s23\n"
      ".Lsl_d2_4:\n"
      "buffer_load_dword v20, v2, s[8:11], s54 offen sc1\n"
      "s_waitcnt vmcnt(0)\n"
      "v_cmp_ne_u32 vcc, s21, v20\n"
      "s_cbranch_vccz .Lbk_d2_4\n"
      "s_sleep 6\n"
      "s_sub_u32 s22, s22, 1\n"
      "s_cbranch_scc0 .Lsl_d2_4\n"
      "s_mov_b32 s23, 0\n"
      "s_branch .Lbk_d2_4\n"
      ".Lsp_dy1_5:\n"
      "s_lshl_b32 s22, s23, 4\n"
      ".Lsl_dy1_5:\n"
      "ds_read_b32 v15, v17\n"
      "s_waitcnt lgkmcnt(0)\n"
      "v_cmp_gt_i32 vcc, v14, v15\n"
      "s_cbranch_vccz .Lbk_dy1_5\n"
      "s_sub_u32 s22, s22, 1\n"
      "s_cbranch_scc0 .Lsl_dy1_5\n"
      "s_mov_b32 s23, 0\n"
      "s_branch .Lbk_dy1_5\n"
      ".Lsp_d3_6:\n"
      "s_mov_b32 s22, s23\n"
      ".Lsl_d3_6:\n"
      "buffer_load_dword v20, v2, s[8:11], s54 offen sc1\n"
      "s_waitcnt vmcnt(0)\n"
      "v_cmp_ne_u32 vcc, s21, v20\n"
      "s_cbranch_vccz .Lbk_d3_6\n"
      "s_sleep 6\n"
      "s_sub_u32 s22, s22, 1\n"
      "s_cbranch_scc0 .Lsl_d3_6\n"
      "s_mov_b32 s23, 0\n"
      "s_branch .Lbk_d3_6\n"
      ".Lsp_dy2_7:\n"
      "s_lshl_b32 s22, s23, 4\n"
      ".Lsl_dy2_7:\n"
      "ds_read_b32 v15, v17\n"
      "s_waitcnt lgkmcnt(0)\n"
      "v_cmp_gt_i32 vcc, v14, v15\n"
      "s_cbranch_vccz .Lbk_dy2_7\n"
      "s_sub_u32 s22, s22, 1\n"
      "s_cbranch_scc0 .Lsl_dy2_7\n"
      "s_mov_b32 s23, 0\n"
      "s_branch .Lbk_dy2_7\n"
      ".Lsp_d4_8:\n"
      "s_mov_b32 s22, s23\n"
      ".Lsl_d4_8:\n"
      "buffer_load_dword v20, v2, s[8:11], s54 offen sc1\n"
      "s_waitcnt vmcnt(0)\n"
      "v_cmp_ne_u32 vcc, s21, v20\n"
      "s_cbranch_vccz .Lbk_d4_8\n"
      "s_sleep 6\n"
      "s_sub_u32 s22, s22, 1\n"
      "s_cbranch_scc0 .Lsl_d4_8\n"
      "s_mov_b32 s23, 0\n"
      "s_branch .Lbk_d4_8\n"
      ".Lsp_dy3_9:\n"
      "s_lshl_b32 s22, s23, 4\n"
      ".Lsl_dy3_9:\n"
      "ds_read_b32 v15, v17\n"
      "s_waitcnt lgkmcnt(0)\n"
      "v_cmp_gt_i32 vcc, v14, v15\n"
      "s_cbranch_vccz .Lbk_dy3_9\n"
      "s_sub_u32 s22, s22, 1\n"
      "s_cbranch_scc0 .Lsl_dy3_9\n"
      "s_mov_b32 s23, 0\n"
      "s_branch .Lbk_dy3_9\n"
      ".Lsp_d5_10:\n"
      "s_mov_b32 s22, s23\n"
      ".Lsl_d5_10:\n"
      "buffer_load_dword v20, v2, s[8:11], s54 offen sc1\n"
      "s_waitcnt vmcnt(0)\n"
      "v_cmp_ne_u32 vcc, s21, v20\n"
      "s_cbranch_vccz .Lbk_d5_10\n"
      "s_sleep 6\n"
      "s_sub_u32 s22, s22, 1\n"
      "s_cbranch_scc0 .Lsl_d5_10\n"
      "s_mov_b32 s23, 0\n"
      "s_branch .Lbk_d5_10\n"
      ".Lsp_dy4_11:\n"
      "s_lshl_b32 s22, s23, 4\n"
      ".Lsl_dy4_11:\n"
      "ds_read_b32 v15, v17\n"
      "s_waitcnt lgkmcnt(0)\n"
      "v_cmp_gt_i32 vcc, v14, v15\n"
      "s_cbranch_vccz .Lbk_dy4_11\n"
      "s_sub_u32 s22, s22, 1\n"
      "s_cbranch_scc0 .Lsl_dy4_11\n"
      "s_mov_b32 s23, 0\n"
      "s_branch .Lbk_dy4_11\n"
      ".Lsp_df_12:\n"
      "s_mov_b32 s22, s23\n"
      ".Lsl_df_12:\n"
      "buffer_load_dword v21, v3, s[8:11], s56 offen sc1\n"
      "s_waitcnt vmcnt(0)\n"
      "v_cmp_ne_u32 vcc, s21, v21\n"
      "s_cbranch_vccz .Lbk_df_12\n"
      "s_sleep 6\n"
      "s_sub_u32 s22, s22, 1\n"
      "s_cbranch_scc0 .Lsl_df_12\n"
      "s_mov_b32 s23, 0\n"
      "s_branch .Lbk_df_12\n"
      ".Lsp_d0_13:\n"
      "s_mov_b32 s22, s23\n"
      ".Lsl_d0_13:\n"
      "buffer_load_dword v20, v2, s[8:11], s54 offen sc1\n"
      "s_waitcnt vmcnt(0)\n"
      "v_cmp_ne_u32 vcc, s21, v20\n"
      "s_cbranch_vccz .Lbk_d0_13\n"
      "s_sleep 6\n"
      "s_sub_u32 s22, s22, 1\n"
      "s_cbranch_scc0 .Lsl_d0_13\n"
      "s_mov_b32 s23, 0\n"
      "s_branch .Lbk_d0_13\n"
      ".Lsp_dy5_14:\n"
      "s_lshl_b32 s22, s23, 4\n"
      ".Lsl_dy5_14:\n"
      "ds_read_b32 v15, v17\n"
      "s_waitcnt lgkmcnt(0)\n"
      "v_cmp_gt_i32 vcc, v14, v15\n"
      "s_cbranch_vccz .Lbk_dy5_14\n"
      "s_sub_u32 s22, s22, 1\n"
      "s_cbranch_scc0 .Lsl_dy5_14\n"
      "s_mov_b32 s23, 0\n"
      "s_branch .Lbk_dy5_14\n"
      ".Lsp_d1_15:\n"
      "s_mov_b32 s22, s23\n"
      ".Lsl_d1_15:\n"
      "buffer_load_dword v20, v2, s[8:11], s54 offen sc1\n"
      "s_waitcnt vmcnt(0)\n"
      "v_cmp_ne_u32 vcc, s21, v20\n"
      "s_cbranch_vccz .Lbk_d1_15\n"
      "s_sleep 6\n"
      "s_sub_u32 s22, s22, 1\n"
      "s_cbranch_scc0 .Lsl_d1_15\n"
      "s_mov_b32 s23, 0\n"
      "s_branch .Lbk_d1_15\n"
      ".Lsp_dy0_16:\n"
      "s_lshl_b32 s22, s23, 4\n"
      ".Lsl_dy0_16:\n"
      "ds_read_b32 v15, v17\n"
      "s_waitcnt lgkmcnt(0)\n"
      "v_cmp_gt_i32 vcc, v14, v15\n"
      "s_cbranch_vccz .Lbk_dy0_16\n"
      "s_sub_u32 s22, s22, 1\n"
      "s_cbranch_scc0 .Lsl_dy0_16\n"
      "s_mov_b32 s23, 0\n"
      "s_branch .Lbk_dy0_16\n"
      ".Lsp_d1_17:\n"
      "s_mov_b32 s22, s23\n"
      ".Lsl_d1_17:\n"
      "buffer_load_dword v20, v2, s[8:11], s54 offen sc1\n"
      "s_waitcnt vmcnt(0)\n"
      "v_cmp_ne_u32 vcc, s21, v20\n"
      "s_cbranch_vccz .Lbk_d1_17\n"
      "s_sleep 6\n"
      "s_sub_u32 s22, s22, 1\n"
      "s_cbranch_scc0 .Lsl_d1_17\n"
      "s_mov_b32 s23, 0\n"
      "s_branch .Lbk_d1_17\n"
      ".Lsp_d2_18:\n"
      "s_mov_b32 s22, s23\n"
      ".Lsl_d2_18:\n"
      "buffer_load_dword v20, v2, s[8:11], s54 offen sc1\n"
      "s_waitcnt vmcnt(0)\n"
      "v_cmp_ne_u32 vcc, s21, v20\n"
      "s_cbranch_vccz .Lbk_d2_18\n"
      "s_sleep 6\n"
      "s_sub_u32 s22, s22, 1\n"
      "s_cbranch_scc0 .Lsl_d2_18\n"
      "s_mov_b32 s23, 0\n"
      "s_branch .Lbk_d2_18\n"
      ".Lsp_dy1_19:\n"
      "s_lshl_b32 s22, s23, 4\n"
      ".Lsl_dy1_19:\n"
      "ds_read_b32 v15, v17\n"
      "s_waitcnt lgkmcnt(0)\n"
      "v_cmp_gt_i32 vcc, v14, v15\n"
      "s_cbranch_vccz .Lbk_dy1_19\n"
      "s_sub_u32 s22, s22, 1\n"
      "s_cbranch_scc0 .Lsl_dy1_19\n"
      "s_mov_b32 s23, 0\n"
      "s_branch .Lbk_dy1_19\n"
      ".Lsp_d2_20:\n"
      "s_mov_b32 s22, s23\n"
      ".Lsl_d2_20:\n"
      "buffer_load_dword v20, v2, s[8:11], s54 offen sc1\n"
      "s_waitcnt vmcnt(0)\n"
      "v_cmp_ne_u32 vcc, s21, v20\n"
      "s_cbranch_vccz .Lbk_d2_20\n"
      "s_sleep 6\n"
      "s_sub_u32 s22, s22, 1\n"
      "s_cbranch_scc0 .Lsl_d2_20\n"
      "s_mov_b32 s23, 0\n"
      "s_branch .Lbk_d2_20\n"
      ".Lsp_d3_21:\n"
      "s_mov_b32 s22, s23\n"
      ".Lsl_d3_21:\n"
      "buffer_load_dword v20, v2, s[8:11], s54 offen sc1\n"
      "s_waitcnt vmcnt(0)\n"
      "v_cmp_ne_u32 vcc, s21, v20\n"
      "s_cbranch_vccz .Lbk_d3_21\n"
      "s_sleep 6\n"
      "s_sub_u32 s22, s22, 1\n"
      "s_cbranch_scc0 .Lsl_d3_21\n"
      "s_mov_b32 s23, 0\n"
      "s_branch .Lbk_d3_21\n"
      ".Lsp_dy2_22:\n"
      "s_lshl_b32 s22, s23, 4\n"
      ".Lsl_dy2_22:\n"
      "ds_read_b32 v15, v17\n"
      "s_waitcnt lgkmcnt(0)\n"
      "v_cmp_gt_i32 vcc, v14, v15\n"
      "s_cbranch_vccz .Lbk_dy2_22\n"
      "s_sub_u32 s22, s22, 1\n"
      "s_cbranch_scc0 .Lsl_dy2_22\n"
      "s_mov_b32 s23, 0\n"
      "s_branch .Lbk_dy2_22\n"
      ".Lsp_d3_23:\n"
      "s_mov_b32 s22, s23\n"
      ".Lsl_d3_23:\n"
      "buffer_load_dword v20, v2, s[8:11], s54 offen sc1\n"
      "s_waitcnt vmcnt(0)\n"
      "v_cmp_ne_u32 vcc, s21, v20\n"
      "s_cbranch_vccz .Lbk_d3_23\n"
      "s_sleep 6\n"
      "s_sub_u32 s22, s22, 1\n"
      "s_cbranch_scc0 .Lsl_d3_23\n"
      "s_mov_b32 s23, 0\n"
      "s_branch .Lbk_d3_23\n"
      ".Lsp_d4_24:\n"
      "s_mov_b32 s22, s23\n"
      ".Lsl_d4_24:\n"
      "buffer_load_dword v20, v2, s[8:11], s54 offen sc1\n"
      "s_waitcnt vmcnt(0)\n"
      "v_cmp_ne_u32 vcc, s21, v20\n"
      "s_cbranch_vccz .Lbk_d4_24\n"
      "s_sleep 6\n"
      "s_sub_u32 s22, s22, 1\n"
      "s_cbranch_scc0 .Lsl_d4_24\n"
      "s_mov_b32 s23, 0\n"
      "s_branch .Lbk_d4_24\n"
      ".Lsp_dy3_25:\n"
      "s_lshl_b32 s22, s23, 4\n"
      ".Lsl_dy3_25:\n"
      "ds_read_b32 v15, v17\n"
      "s_waitcnt lgkmcnt(0)\n"
      "v_cmp_gt_i32 vcc, v14, v15\n"
      "s_cbranch_vccz .Lbk_dy3_25\n"
      "s_sub_u32 s22, s22, 1\n"
      "s_cbranch_scc0 .Lsl_dy3_25\n"
      "s_mov_b32 s23, 0\n"
      "s_branch .Lbk_dy3_25\n"
      ".Lsp_d4_26:\n"
      "s_mov_b32 s22, s23\n"
      ".Lsl_d4_26:\n"
      "buffer_load_dword v20, v2, s[8:11], s54 offen sc1\n"
      "s_waitcnt vmcnt(0)\n"
      "v_cmp_ne_u32 vcc, s21, v20\n"
      "s_cbranch_vccz .Lbk_d4_26\n"
      "s_sleep 6\n"
      "s_sub_u32 s22, s22, 1\n"
      "s_cbranch_scc0 .Lsl_d4_26\n"
      "s_mov_b32 s23, 0\n"
      "s_branch .Lbk_d4_26\n"
      ".Lsp_d5_27:\n"
      "s_mov_b32 s22, s23\n"
      ".Lsl_d5_27:\n"
      "buffer_load_dword v20, v2, s[8:11], s54 offen sc1\n"
      "s_waitcnt vmcnt(0)\n"
      "v_cmp_ne_u32 vcc, s21, v20\n"
      "s_cbranch_vccz .Lbk_d5_27\n"
      "s_sleep 6\n"
      "s_sub_u32 s22, s22, 1\n"
      "s_cbranch_scc0 .Lsl_d5_27\n"
      "s_mov_b32 s23, 0\n"
      "s_branch .Lbk_d5_27\n"
      ".Lsp_dy4_28:\n"
      "s_lshl_b32 s22, s23, 4\n"
      ".Lsl_dy4_28:\n"
      "ds_read_b32 v15, v17\n"
      "s_waitcnt lgkmcnt(0)\n"
      "v_cmp_gt_i32 vcc, v14, v15\n"
      "s_cbranch_vccz .Lbk_dy4_28\n"
      "s_sub_u32 s22, s22, 1\n"
      "s_cbranch_scc0 .Lsl_dy4_28\n"
      "s_mov_b32 s23, 0\n"
      "s_branch .Lbk_dy4_28\n"
      ".Lsp_d5_29:\n"
      "s_mov_b32 s22, s23\n"
      ".Lsl_d5_29:\n"
      "buffer_load_dword v20, v2, s[8:11], s54 offen sc1\n"
      "s_waitcnt vmcnt(0)\n"
      "v_cmp_ne_u32 vcc, s21, v20\n"
      "s_cbranch_vccz .Lbk_d5_29\n"
      "s_sleep 6\n"
      "s_sub_u32 s22, s22, 1\n"
      "s_cbranch_scc0 .Lsl_d5_29\n"
      "s_mov_b32 s23, 0\n"
      "s_branch .Lbk_d5_29\n"
      ".Lsp_d0_30:\n"
      "s_mov_b32 s22, s23\n"
      ".Lsl_d0_30:\n"
      "buffer_load_dword v20, v2, s[8:11], s54 offen sc1\n"
      "s_waitcnt vmcnt(0)\n"
      "v_cmp_ne_u32 vcc, s21, v20\n"
      "s_cbranch_vccz .Lbk_d0_30\n"
      "s_sleep 6\n"
      "s_sub_u32 s22, s22, 1\n"
      "s_cbranch_scc0 .Lsl_d0_30\n"
      "s_mov_b32 s23, 0\n"
      "s_branch .Lbk_d0_30\n"
      ".Lsp_dy5_31:\n"
      "s_lshl_b32 s22, s23, 4\n"
      ".Lsl_dy5_31:\n"
      "ds_read_b32 v15, v17\n"
      "s_waitcnt lgkmcnt(0)\n"
      "v_cmp_gt_i32 vcc, v14, v15\n"
      "s_cbranch_vccz .Lbk_dy5_31\n"
      "s_sub_u32 s22, s22, 1\n"
      "s_cbranch_scc0 .Lsl_dy5_31\n"
      "s_mov_b32 s23, 0\n"
      "s_branch .Lbk_dy5_31\n"
      ".Ld_last:\n"
      :
      : "{s[8:11]}"(rsWS), "{s[12:15]}"(rsA), "{s[16:19]}"(rsB), "{s[92:95]}"(rsC), "{v0}"(v_l16), "{v1}"(v_l16b), "{v2}"(v_fa), "{v3}"(v_ga), "{v4}"(v_ra), "{v5}"(v_rf), "{v6}"(v_xp), "{v7}"(v_xs), "{v8}"(v_op), "{v9}"(v_os), "{v10}"(v_sw), "{v11}"(v_sr), "{v12}"(v_ss), "{v13}"(v_l8), "{v15}"(v_al), "{v16}"(v_yw), "{v17}"(v_yr), "{v19}"(v_hi), "{v176}"(v_at), "{v177}"(v_as), "{v178}"(v_vo0), "{v179}"(v_vo1), "{v180}"(v_vo2), "{v181}"(v_pw), "{v183}"(v_pu), "{v182}"(v_s1), "{s36}"(s_sh), "{s37}"(s_ps), "{s38}"(s_x1), "{s39}"(s_xr), "{s40}"(s_or), "{s41}"(s_p0), "{s42}"(s_m3), "{s43}"(s_m6), "{s44}"(s_so), "{s45}"(s_ph), "{s46}"(s_i0), "{s47}"(s_i5), "{s48}"(s_j0), "{s49}"(s_j5), "{s50}"(s_he), "{s51}"(s_pe[0]), "{s52}"(s_pe[1]), "{s53}"(s_pe[2]), "{s63}"(s_k0)
      : "v255", "a255", "s99", "vcc", "scc", "memory");

  } else {
    const unsigned e1 = (e - 1) & 3, gr = !fast ? slot & 3 : slot < 31 ? (e + slot - 27) & 3 : e1, part = !fast ? 8 * e + (slot >> 2) : slot < 31 ? 32 + e : 32 + e1;
    COMMON(112, 81920);
    const unsigned j = part, f = 4 * j + w;
    const u32x4 rsA = srd(w >> 1 ? Wu : Wg, 576 * 4608 * 2), rsC = srd(Wo, 576 * 576 * 2);
    const unsigned tw0 = !fast && slot < 4 ? ((e * 16 + slot * 4 + w) * 81 + lane) * 128 : OOB, v_tw[2] = {tw0, lane < 17 ? tw0 + 8192 : OOB};
    const unsigned v_l5 = w < 3 ? v_l16 + 16384 - w * 3072 : OOB;
    const unsigned v_fa = 19456 - w * 4096 + (lane >> 3) * 128 + (lane & 7) * 4, v_ra = up * 8 * HREC + l5 + 147456 - f * 1024 + f * 128;
    const unsigned one = q == g ? 0x3f80u << (16 * (p & 1)) : 0u, v_id[2] = {p < 2 ? one : 0u, p < 2 ? 0u : one};
    const unsigned v_st = 18432 + i16 * 32 + q * 8, v_pw = 147456 - f * 1024 + f * 128 + l8 * HREC + l7;
    const unsigned v_at = 65536 + (8 * g + q) * 256 + ((4 * w + p) ^ (4 * q)) * 16;
    const unsigned v_vo0 = (16 * (w & 1) + g) * 9216 + 256 * j + (i16 ^ (4 * g)) * 16;
    const unsigned v_vo1 = v_vo0 + 4 * 9216 - 1024, v_vo2 = v_vo1 + 4 * 9216 - 1024, v_vo3 = v_vo2 + 4 * 9216 - 1024;
    const unsigned s_sx = WS_X1 + gr * NREC * REC + w * 4096, s_sh = WS_H + gr * NREC * HREC + f * 1024;
    const unsigned s_m4 = w * 4096, s_m5 = 16384 + w * 1024;
    asm volatile(
      "s_buffer_load_dword s21, s[8:11], 0\n"
      "s_mov_b32 s23, 65536\n"
      "s_mov_b32 s31, 0x40000000\n"
      "s_mov_b32 s41, 0\n"
      "s_sleep 16\n"
      "buffer_load_dword v58, v54, s[92:95], s41 offen\n"
      "buffer_load_dword v59, v55, s[92:95], s41 offen\n"
      "s_waitcnt vmcnt(0)\n"
      "s_add_u32 m0, s38, 65536\n"
      "s_nop 0\n"
      "buffer_load_dwordx4 v6, s[12:15], s41 offen offset:0 lds\n"
      "buffer_load_dwordx4 v7, s[12:15], s41 offen offset:1024 lds\n"
      "buffer_load_dwordx4 v8, s[12:15], s41 offen offset:2048 lds\n"
      "buffer_load_dwordx4 v9, s[12:15], s41 offen offset:3072 lds\n"
      "s_add_u32 s41, s41, 294912\n"
      "s_add_u32 m0, s38, 81920\n"
      "s_nop 0\n"
      "buffer_load_dwordx4 v6, s[12:15], s41 offen offset:0 lds\n"
      "buffer_load_dwordx4 v7, s[12:15], s41 offen offset:1024 lds\n"
      "buffer_load_dwordx4 v8, s[12:15], s41 offen offset:2048 lds\n"
      "buffer_load_dwordx4 v9, s[12:15], s41 offen offset:3072 lds\n"
      "s_add_u32 s41, s41, 294912\n"
      "s_add_u32 m0, s38, 98304\n"
      "s_nop 0\n"
      "buffer_load_dwordx4 v6, s[12:15], s41 offen offset:0 lds\n"
      "buffer_load_dwordx4 v7, s[12:15], s41 offen offset:1024 lds\n"
      "buffer_load_dwordx4 v8, s[12:15], s41 offen offset:2048 lds\n"
      "buffer_load_dwordx4 v9, s[12:15], s41 offen offset:3072 lds\n"
      "s_add_u32 s41, s41, 294912\n"
      "s_add_u32 m0, s38, 114688\n"
      "s_nop 0\n"
      "buffer_load_dwordx4 v6, s[12:15], s41 offen offset:0 lds\n"
      "buffer_load_dwordx4 v7, s[12:15], s41 offen offset:1024 lds\n"
      "buffer_load_dwordx4 v8, s[12:15], s41 offen offset:2048 lds\n"
      "buffer_load_dwordx4 v9, s[12:15], s41 offen offset:3072 lds\n"
      "s_add_u32 s41, s41, 294912\n"
      "s_add_u32 m0, s38, 131072\n"
      "s_nop 0\n"
      "buffer_load_dwordx4 v6, s[12:15], s41 offen offset:0 lds\n"
      "buffer_load_dwordx4 v7, s[12:15], s41 offen offset:1024 lds\n"
      "buffer_load_dwordx4 v8, s[12:15], s41 offen offset:2048 lds\n"
      "buffer_load_dwordx4 v9, s[12:15], s41 offen offset:3072 lds\n"
      "s_add_u32 s41, s41, 294912\n"
      "v_mov_b32 v172, 0x3f803f80\n"
      "v_mov_b32 v173, 0x3f803f80\n"
      "v_mov_b32 v174, 0x3f803f80\n"
      "v_mov_b32 v175, 0x3f803f80\n"
      "v_add_u32 v19, 49152, v5\n"
      "s_mov_b64 exec, 0\n"
      "s_lshr_b32 s22, s38, 12\n"
      ".Lg_pl:\n"
      "s_cmp_eq_u32 s22, 0\n"
      "s_cbranch_scc1 .Lg_p0\n"
      "s_cmp_eq_u32 s22, 1\n"
      "s_cbranch_scc1 .Lg_p1\n"
      "s_branch .Lg_pe\n"
      ".Lg_pr:\n"
      "s_add_u32 s22, s22, 4\n"
      "s_branch .Lg_pl\n"
      ".Lg_pe:\n"
      "s_mov_b64 exec, -1\n"
      "s_waitcnt vmcnt(16)\n"
      "s_barrier\n"
      "ds_read_b64_tr_b16 a[0:1], v5 offset:0\n"
      "ds_read_b64_tr_b16 a[2:3], v5 offset:1024\n"
      "ds_read_b64_tr_b16 a[72:73], v5 offset:8192\n"
      "ds_read_b64_tr_b16 a[74:75], v5 offset:9216\n"
      "ds_read_b64_tr_b16 a[144:145], v5 offset:8\n"
      "ds_read_b64_tr_b16 a[146:147], v5 offset:1032\n"
      "ds_read_b64_tr_b16 a[216:217], v5 offset:8200\n"
      "ds_read_b64_tr_b16 a[218:219], v5 offset:9224\n"
      "s_add_u32 m0, s38, 147456\n"
      "s_nop 0\n"
      "buffer_load_dwordx4 v6, s[12:15], s41 offen offset:0 lds\n"
      "buffer_load_dwordx4 v7, s[12:15], s41 offen offset:1024 lds\n"
      "buffer_load_dwordx4 v8, s[12:15], s41 offen offset:2048 lds\n"
      "buffer_load_dwordx4 v9, s[12:15], s41 offen offset:3072 lds\n"
      "s_add_u32 s41, s41, 294912\n"
      "s_waitcnt vmcnt(16) lgkmcnt(0)\n"
      "s_barrier\n"
      "ds_read_b64_tr_b16 a[4:5], v5 offset:16384\n"
      "ds_read_b64_tr_b16 a[6:7], v5 offset:17408\n"
      "ds_read_b64_tr_b16 a[76:77], v5 offset:24576\n"
      "ds_read_b64_tr_b16 a[78:79], v5 offset:25600\n"
      "ds_read_b64_tr_b16 a[148:149], v5 offset:16392\n"
      "ds_read_b64_tr_b16 a[150:151], v5 offset:17416\n"
      "ds_read_b64_tr_b16 a[220:221], v5 offset:24584\n"
      "ds_read_b64_tr_b16 a[222:223], v5 offset:25608\n"
      "v_mfma_f32_16x16x32_bf16 v[76:79], v[172:175], a[0:3], 0\n"
      "v_mfma_f32_16x16x32_bf16 v[80:83], v[172:175], a[72:75], 0\n"
      "v_mfma_f32_16x16x32_bf16 v[84:87], v[172:175], a[144:147], 0\n"
      "v_mfma_f32_16x16x32_bf16 v[88:91], v[172:175], a[216:219], 0\n"
      "s_add_u32 m0, s38, 65536\n"
      "s_nop 0\n"
      "buffer_load_dwordx4 v6, s[12:15], s41 offen offset:0 lds\n"
      "buffer_load_dwordx4 v7, s[12:15], s41 offen offset:1024 lds\n"
      "buffer_load_dwordx4 v8, s[12:15], s41 offen offset:2048 lds\n"
      "buffer_load_dwordx4 v9, s[12:15], s41 offen offset:3072 lds\n"
      "s_add_u32 s41, s41, 294912\n"
      "s_waitcnt vmcnt(16) lgkmcnt(0)\n"
      "s_barrier\n"
      "ds_read_b64_tr_b16 a[8:9], v5 offset:32768\n"
      "ds_read_b64_tr_b16 a[10:11], v5 offset:33792\n"
      "ds_read_b64_tr_b16 a[80:81], v5 offset:40960\n"
      "ds_read_b64_tr_b16 a[82:83], v5 offset:41984\n"
      "ds_read_b64_tr_b16 a[152:153], v5 offset:32776\n"
      "ds_read_b64_tr_b16 a[154:155], v5 offset:33800\n"
      "ds_read_b64_tr_b16 a[224:225], v5 offset:40968\n"
      "ds_read_b64_tr_b16 a[226:227], v5 offset:41992\n"
      "v_mfma_f32_16x16x32_bf16 v[76:79], v[172:175], a[4:7], v[76:79]\n"
      "v_mfma_f32_16x16x32_bf16 v[80:83], v[172:175], a[76:79], v[80:83]\n"
      "v_mfma_f32_16x16x32_bf16 v[84:87], v[172:175], a[148:151], v[84:87]\n"
      "v_mfma_f32_16x16x32_bf16 v[88:91], v[172:175], a[220:223], v[88:91]\n"
      "s_add_u32 m0, s38, 81920\n"
      "s_nop 0\n"
      "buffer_load_dwordx4 v6, s[12:15], s41 offen offset:0 lds\n"
      "buffer_load_dwordx4 v7, s[12:15], s41 offen offset:1024 lds\n"
      "buffer_load_dwordx4 v8, s[12:15], s41 offen offset:2048 lds\n"
      "buffer_load_dwordx4 v9, s[12:15], s41 offen offset:3072 lds\n"
      "s_add_u32 s41, s41, 294912\n"
      "s_waitcnt vmcnt(16) lgkmcnt(0)\n"
      "s_barrier\n"
      "ds_read_b64_tr_b16 a[12:13], v19 offset:0\n"
      "ds_read_b64_tr_b16 a[14:15], v19 offset:1024\n"
      "ds_read_b64_tr_b16 a[84:85], v19 offset:8192\n"
      "ds_read_b64_tr_b16 a[86:87], v19 offset:9216\n"
      "ds_read_b64_tr_b16 a[156:157], v19 offset:8\n"
      "ds_read_b64_tr_b16 a[158:159], v19 offset:1032\n"
      "ds_read_b64_tr_b16 a[228:229], v19 offset:8200\n"
      "ds_read_b64_tr_b16 a[230:231], v19 offset:9224\n"
      "v_mfma_f32_16x16x32_bf16 v[76:79], v[172:175], a[8:11], v[76:79]\n"
      "v_mfma_f32_16x16x32_bf16 v[80:83], v[172:175], a[80:83], v[80:83]\n"
      "v_mfma_f32_16x16x32_bf16 v[84:87], v[172:175], a[152:155], v[84:87]\n"
      "v_mfma_f32_16x16x32_bf16 v[88:91], v[172:175], a[224:227], v[88:91]\n"
      "s_add_u32 m0, s38, 98304\n"
      "s_nop 0\n"
      "buffer_load_dwordx4 v6, s[12:15], s41 offen offset:0 lds\n"
      "buffer_load_dwordx4 v7, s[12:15], s41 offen offset:1024 lds\n"
      "buffer_load_dwordx4 v8, s[12:15], s41 offen offset:2048 lds\n"
      "buffer_load_dwordx4 v9, s[12:15], s41 offen offset:3072 lds\n"
      "s_add_u32 s41, s41, 294912\n"
      "s_waitcnt vmcnt(16) lgkmcnt(0)\n"
      "s_barrier\n"
      "ds_read_b64_tr_b16 a[16:17], v19 offset:16384\n"
      "ds_read_b64_tr_b16 a[18:19], v19 offset:17408\n"
      "ds_read_b64_tr_b16 a[88:89], v19 offset:24576\n"
      "ds_read_b64_tr_b16 a[90:91], v19 offset:25600\n"
      "ds_read_b64_tr_b16 a[160:161], v19 offset:16392\n"
      "ds_read_b64_tr_b16 a[162:163], v19 offset:17416\n"
      "ds_read_b64_tr_b16 a[232:233], v19 offset:24584\n"
      "ds_read_b64_tr_b16 a[234:235], v19 offset:25608\n"
      "v_mfma_f32_16x16x32_bf16 v[76:79], v[172:175], a[12:15], v[76:79]\n"
      "v_mfma_f32_16x16x32_bf16 v[80:83], v[172:175], a[84:87], v[80:83]\n"
      "v_mfma_f32_16x16x32_bf16 v[84:87], v[172:175], a[156:159], v[84:87]\n"
      "v_mfma_f32_16x16x32_bf16 v[88:91], v[172:175], a[228:231], v[88:91]\n"
      "s_add_u32 m0, s38, 114688\n"
      "s_nop 0\n"
      "buffer_load_dwordx4 v6, s[12:15], s41 offen offset:0 lds\n"
      "buffer_load_dwordx4 v7, s[12:15], s41 offen offset:1024 lds\n"
      "buffer_load_dwordx4 v8, s[12:15], s41 offen offset:2048 lds\n"
      "buffer_load_dwordx4 v9, s[12:15], s41 offen offset:3072 lds\n"
      "s_add_u32 s41, s41, 294912\n"
      "s_waitcnt vmcnt(16) lgkmcnt(0)\n"
      "s_barrier\n"
      "ds_read_b64_tr_b16 a[20:21], v19 offset:32768\n"
      "ds_read_b64_tr_b16 a[22:23], v19 offset:33792\n"
      "ds_read_b64_tr_b16 a[92:93], v19 offset:40960\n"
      "ds_read_b64_tr_b16 a[94:95], v19 offset:41984\n"
      "ds_read_b64_tr_b16 a[164:165], v19 offset:32776\n"
      "ds_read_b64_tr_b16 a[166:167], v19 offset:33800\n"
      "ds_read_b64_tr_b16 a[236:237], v19 offset:40968\n"
      "ds_read_b64_tr_b16 a[238:239], v19 offset:41992\n"
      "v_mfma_f32_16x16x32_bf16 v[76:79], v[172:175], a[16:19], v[76:79]\n"
      "v_mfma_f32_16x16x32_bf16 v[80:83], v[172:175], a[88:91], v[80:83]\n"
      "v_mfma_f32_16x16x32_bf16 v[84:87], v[172:175], a[160:163], v[84:87]\n"
      "v_mfma_f32_16x16x32_bf16 v[88:91], v[172:175], a[232:235], v[88:91]\n"
      "s_add_u32 m0, s38, 131072\n"
      "s_nop 0\n"
      "buffer_load_dwordx4 v6, s[12:15], s41 offen offset:0 lds\n"
      "buffer_load_dwordx4 v7, s[12:15], s41 offen offset:1024 lds\n"
      "buffer_load_dwordx4 v8, s[12:15], s41 offen offset:2048 lds\n"
      "buffer_load_dwordx4 v9, s[12:15], s41 offen offset:3072 lds\n"
      "s_add_u32 s41, s41, 294912\n"
      "s_waitcnt vmcnt(16) lgkmcnt(0)\n"
      "s_barrier\n"
      "ds_read_b64_tr_b16 a[24:25], v5 offset:0\n"
      "ds_read_b64_tr_b16 a[26:27], v5 offset:1024\n"
      "ds_read_b64_tr_b16 a[96:97], v5 offset:8192\n"
      "ds_read_b64_tr_b16 a[98:99], v5 offset:9216\n"
      "ds_read_b64_tr_b16 a[168:169], v5 offset:8\n"
      "ds_read_b64_tr_b16 a[170:171], v5 offset:1032\n"
      "ds_read_b64_tr_b16 a[240:241], v5 offset:8200\n"
      "ds_read_b64_tr_b16 a[242:243], v5 offset:9224\n"
      "v_mfma_f32_16x16x32_bf16 v[76:79], v[172:175], a[20:23], v[76:79]\n"
      "v_mfma_f32_16x16x32_bf16 v[80:83], v[172:175], a[92:95], v[80:83]\n"
      "v_mfma_f32_16x16x32_bf16 v[84:87], v[172:175], a[164:167], v[84:87]\n"
      "v_mfma_f32_16x16x32_bf16 v[88:91], v[172:175], a[236:239], v[88:91]\n"
      "s_add_u32 m0, s38, 147456\n"
      "s_nop 0\n"
      "buffer_load_dwordx4 v6, s[12:15], s41 offen offset:0 lds\n"
      "buffer_load_dwordx4 v7, s[12:15], s41 offen offset:1024 lds\n"
      "buffer_load_dwordx4 v8, s[12:15], s41 offen offset:2048 lds\n"
      "buffer_load_dwordx4 v9, s[12:15], s41 offen offset:3072 lds\n"
      "s_add_u32 s41, s41, 294912\n"
      "s_waitcnt vmcnt(16) lgkmcnt(0)\n"
      "s_barrier\n"
      "ds_read_b64_tr_b16 a[28:29], v5 offset:16384\n"
      "ds_read_b64_tr_b16 a[30:31], v5 offset:17408\n"
      "ds_read_b64_tr_b16 a[100:101], v5 offset:24576\n"
      "ds_read_b64_tr_b16 a[102:103], v5 offset:25600\n"
      "ds_read_b64_tr_b16 a[172:173], v5 offset:16392\n"
      "ds_read_b64_tr_b16 a[174:175], v5 offset:17416\n"
      "ds_read_b64_tr_b16 a[244:245], v5 offset:24584\n"
      "ds_read_b64_tr_b16 a[246:247], v5 offset:25608\n"
      "v_mfma_f32_16x16x32_bf16 v[76:79], v[172:175], a[24:27], v[76:79]\n"
      "v_mfma_f32_16x16x32_bf16 v[80:83], v[172:175], a[96:99], v[80:83]\n"
      "v_mfma_f32_16x16x32_bf16 v[84:87], v[172:175], a[168:171], v[84:87]\n"
      "v_mfma_f32_16x16x32_bf16 v[88:91], v[172:175], a[240:243], v[88:91]\n"
      "s_add_u32 m0, s38, 65536\n"
      "s_nop 0\n"
      "buffer_load_dwordx4 v6, s[12:15], s41 offen offset:0 lds\n"
      "buffer_load_dwordx4 v7, s[12:15], s41 offen offset:1024 lds\n"
      "buffer_load_dwordx4 v8, s[12:15], s41 offen offset:2048 lds\n"
      "buffer_load_dwordx4 v9, s[12:15], s41 offen offset:3072 lds\n"
      "s_add_u32 s41, s41, 294912\n"
      "s_waitcnt vmcnt(16) lgkmcnt(0)\n"
      "s_barrier\n"
      "ds_read_b64_tr_b16 a[32:33], v5 offset:32768\n"
      "ds_read_b64_tr_b16 a[34:35], v5 offset:33792\n"
      "ds_read_b64_tr_b16 a[104:105], v5 offset:40960\n"
      "ds_read_b64_tr_b16 a[106:107], v5 offset:41984\n"
      "ds_read_b64_tr_b16 a[176:177], v5 offset:32776\n"
      "ds_read_b64_tr_b16 a[178:179], v5 offset:33800\n"
      "ds_read_b64_tr_b16 a[248:249], v5 offset:40968\n"
      "ds_read_b64_tr_b16 a[250:251], v5 offset:41992\n"
      "v_mfma_f32_16x16x32_bf16 v[76:79], v[172:175], a[28:31], v[76:79]\n"
      "v_mfma_f32_16x16x32_bf16 v[80:83], v[172:175], a[100:103], v[80:83]\n"
      "v_mfma_f32_16x16x32_bf16 v[84:87], v[172:175], a[172:175], v[84:87]\n"
      "v_mfma_f32_16x16x32_bf16 v[88:91], v[172:175], a[244:247], v[88:91]\n"
      "s_add_u32 m0, s38, 81920\n"
      "s_nop 0\n"
      "buffer_load_dwordx4 v6, s[12:15], s41 offen offset:0 lds\n"
      "buffer_load_dwordx4 v7, s[12:15], s41 offen offset:1024 lds\n"
      "buffer_load_dwordx4 v8, s[12:15], s41 offen offset:2048 lds\n"
      "buffer_load_dwordx4 v9, s[12:15], s41 offen offset:3072 lds\n"
      "s_add_u32 s41, s41, 294912\n"
      "s_waitcnt vmcnt(16) lgkmcnt(0)\n"
      "s_barrier\n"
      "ds_read_b64_tr_b16 a[36:37], v19 offset:0\n"
      "ds_read_b64_tr_b16 a[38:39], v19 offset:1024\n"
      "ds_read_b64_tr_b16 a[108:109], v19 offset:8192\n"
      "ds_read_b64_tr_b16 a[110:111], v19 offset:9216\n"
      "ds_read_b64_tr_b16 a[180:181], v19 offset:8\n"
      "ds_read_b64_tr_b16 a[182:183], v19 offset:1032\n"
      "ds_read_b64_tr_b16 a[252:253], v19 offset:8200\n"
      "ds_read_b64_tr_b16 a[254:255], v19 offset:9224\n"
      "v_mfma_f32_16x16x32_bf16 v[76:79], v[172:175], a[32:35], v[76:79]\n"
      "v_mfma_f32_16x16x32_bf16 v[80:83], v[172:175], a[104:107], v[80:83]\n"
      "v_mfma_f32_16x16x32_bf16 v[84:87], v[172:175], a[176:179], v[84:87]\n"
      "v_mfma_f32_16x16x32_bf16 v[88:91], v[172:175], a[248:251], v[88:91]\n"
      "s_add_u32 m0, s38, 98304\n"
      "s_nop 0\n"
      "buffer_load_dwordx4 v6, s[12:15], s41 offen offset:0 lds\n"
      "buffer_load_dwordx4 v7, s[12:15], s41 offen offset:1024 lds\n"
      "buffer_load_dwordx4 v8, s[12:15], s41 offen offset:2048 lds\n"
      "buffer_load_dwordx4 v9, s[12:15], s41 offen offset:3072 lds\n"
      "s_add_u32 s41, s41, 294912\n"
      "s_waitcnt vmcnt(16) lgkmcnt(0)\n"
      "s_barrier\n"
      "ds_read_b64_tr_b16 a[40:41], v19 offset:16384\n"
      "ds_read_b64_tr_b16 a[42:43], v19 offset:17408\n"
      "ds_read_b64_tr_b16 a[112:113], v19 offset:24576\n"
      "ds_read_b64_tr_b16 a[114:115], v19 offset:25600\n"
      "ds_read_b64_tr_b16 a[184:185], v19 offset:16392\n"
      "ds_read_b64_tr_b16 a[186:187], v19 offset:17416\n"
      "ds_read_b64_tr_b16 v[20:21], v19 offset:24584\n"
      "ds_read_b64_tr_b16 v[22:23], v19 offset:25608\n"
      "v_mfma_f32_16x16x32_bf16 v[76:79], v[172:175], a[36:39], v[76:79]\n"
      "v_mfma_f32_16x16x32_bf16 v[80:83], v[172:175], a[108:111], v[80:83]\n"
      "v_mfma_f32_16x16x32_bf16 v[84:87], v[172:175], a[180:183], v[84:87]\n"
      "v_mfma_f32_16x16x32_bf16 v[88:91], v[172:175], a[252:255], v[88:91]\n"
      "s_add_u32 m0, s38, 114688\n"
      "s_nop 0\n"
      "buffer_load_dwordx4 v6, s[12:15], s41 offen offset:0 lds\n"
      "buffer_load_dwordx4 v7, s[12:15], s41 offen offset:1024 lds\n"
      "buffer_load_dwordx4 v8, s[12:15], s41 offen offset:2048 lds\n"
      "buffer_load_dwordx4 v9, s[12:15], s41 offen offset:3072 lds\n"
      "s_add_u32 s41, s41, 294912\n"
      "s_waitcnt vmcnt(16) lgkmcnt(0)\n"
      "s_barrier\n"
      "ds_read_b64_tr_b16 a[44:45], v19 offset:32768\n"
      "ds_read_b64_tr_b16 a[46:47], v19 offset:33792\n"
      "ds_read_b64_tr_b16 a[116:117], v19 offset:40960\n"
      "ds_read_b64_tr_b16 a[118:119], v19 offset:41984\n"
      "ds_read_b64_tr_b16 a[188:189], v19 offset:32776\n"
      "ds_read_b64_tr_b16 a[190:191], v19 offset:33800\n"
      "ds_read_b64_tr_b16 v[24:25], v19 offset:40968\n"
      "ds_read_b64_tr_b16 v[26:27], v19 offset:41992\n"
      "v_mfma_f32_16x16x32_bf16 v[92:95], v[172:175], a[40:43], v[76:79]\n"
      "v_mfma_f32_16x16x32_bf16 v[96:99], v[172:175], a[112:115], v[80:83]\n"
      "v_mfma_f32_16x16x32_bf16 v[100:103], v[172:175], a[184:187], v[84:87]\n"
      "v_mfma_f32_16x16x32_bf16 v[104:107], v[172:175], v[20:23], v[88:91]\n"
      "s_add_u32 m0, s38, 131072\n"
      "s_nop 0\n"
      "buffer_load_dwordx4 v6, s[12:15], s41 offen offset:0 lds\n"
      "buffer_load_dwordx4 v7, s[12:15], s41 offen offset:1024 lds\n"
      "buffer_load_dwordx4 v8, s[12:15], s41 offen offset:2048 lds\n"
      "buffer_load_dwordx4 v9, s[12:15], s41 offen offset:3072 lds\n"
      "s_add_u32 s41, s41, 294912\n"
      "s_waitcnt vmcnt(16) lgkmcnt(0)\n"
      "s_barrier\n"
      "ds_read_b64_tr_b16 a[48:49], v5 offset:0\n"
      "ds_read_b64_tr_b16 a[50:51], v5 offset:1024\n"
      "ds_read_b64_tr_b16 a[120:121], v5 offset:8192\n"
      "ds_read_b64_tr_b16 a[122:123], v5 offset:9216\n"
      "ds_read_b64_tr_b16 a[192:193], v5 offset:8\n"
      "ds_read_b64_tr_b16 a[194:195], v5 offset:1032\n"
      "ds_read_b64_tr_b16 v[28:29], v5 offset:8200\n"
      "ds_read_b64_tr_b16 v[30:31], v5 offset:9224\n"
      "v_mfma_f32_16x16x32_bf16 v[92:95], v[172:175], a[44:47], v[92:95]\n"
      "v_mfma_f32_16x16x32_bf16 v[96:99], v[172:175], a[116:119], v[96:99]\n"
      "v_mfma_f32_16x16x32_bf16 v[100:103], v[172:175], a[188:191], v[100:103]\n"
      "v_mfma_f32_16x16x32_bf16 v[104:107], v[172:175], v[24:27], v[104:107]\n"
      "s_add_u32 m0, s38, 147456\n"
      "s_nop 0\n"
      "buffer_load_dwordx4 v6, s[12:15], s41 offen offset:0 lds\n"
      "buffer_load_dwordx4 v7, s[12:15], s41 offen offset:1024 lds\n"
      "buffer_load_dwordx4 v8, s[12:15], s41 offen offset:2048 lds\n"
      "buffer_load_dwordx4 v9, s[12:15], s41 offen offset:3072 lds\n"
      "s_add_u32 s41, s41, 294912\n"
      "s_waitcnt vmcnt(16) lgkmcnt(0)\n"
      "s_barrier\n"
      "ds_read_b64_tr_b16 a[52:53], v5 offset:16384\n"
      "ds_read_b64_tr_b16 a[54:55], v5 offset:17408\n"
      "ds_read_b64_tr_b16 a[124:125], v5 offset:24576\n"
      "ds_read_b64_tr_b16 a[126:127], v5 offset:25600\n"
      "ds_read_b64_tr_b16 a[196:197], v5 offset:16392\n"
      "ds_read_b64_tr_b16 a[198:199], v5 offset:17416\n"
      "ds_read_b64_tr_b16 v[32:33], v5 offset:24584\n"
      "ds_read_b64_tr_b16 v[34:35], v5 offset:25608\n"
      "v_mfma_f32_16x16x32_bf16 v[92:95], v[172:175], a[48:51], v[92:95]\n"
      "v_mfma_f32_16x16x32_bf16 v[96:99], v[172:175], a[120:123], v[96:99]\n"
      "v_mfma_f32_16x16x32_bf16 v[100:103], v[172:175], a[192:195], v[100:103]\n"
      "v_mfma_f32_16x16x32_bf16 v[104:107], v[172:175], v[28:31], v[104:107]\n"
      "s_waitcnt vmcnt(12) lgkmcnt(0)\n"
      "s_barrier\n"
      "ds_read_b64_tr_b16 a[56:57], v5 offset:32768\n"
      "ds_read_b64_tr_b16 a[58:59], v5 offset:33792\n"
      "ds_read_b64_tr_b16 a[128:129], v5 offset:40960\n"
      "ds_read_b64_tr_b16 a[130:131], v5 offset:41984\n"
      "ds_read_b64_tr_b16 a[200:201], v5 offset:32776\n"
      "ds_read_b64_tr_b16 a[202:203], v5 offset:33800\n"
      "ds_read_b64_tr_b16 v[36:37], v5 offset:40968\n"
      "ds_read_b64_tr_b16 v[38:39], v5 offset:41992\n"
      "v_mfma_f32_16x16x32_bf16 v[92:95], v[172:175], a[52:55], v[92:95]\n"
      "v_mfma_f32_16x16x32_bf16 v[96:99], v[172:175], a[124:127], v[96:99]\n"
      "v_mfma_f32_16x16x32_bf16 v[100:103], v[172:175], a[196:199], v[100:103]\n"
      "v_mfma_f32_16x16x32_bf16 v[104:107], v[172:175], v[32:35], v[104:107]\n"
      "s_waitcnt vmcnt(8) lgkmcnt(0)\n"
      "s_barrier\n"
      "ds_read_b64_tr_b16 a[60:61], v19 offset:0\n"
      "ds_read_b64_tr_b16 a[62:63], v19 offset:1024\n"
      "ds_read_b64_tr_b16 a[132:133], v19 offset:8192\n"
      "ds_read_b64_tr_b16 a[134:135], v19 offset:9216\n"
      "ds_read_b64_tr_b16 a[204:205], v19 offset:8\n"
      "ds_read_b64_tr_b16 a[206:207], v19 offset:1032\n"
      "ds_read_b64_tr_b16 v[40:41], v19 offset:8200\n"
      "ds_read_b64_tr_b16 v[42:43], v19 offset:9224\n"
      "v_mfma_f32_16x16x32_bf16 v[92:95], v[172:175], a[56:59], v[92:95]\n"
      "v_mfma_f32_16x16x32_bf16 v[96:99], v[172:175], a[128:131], v[96:99]\n"
      "v_mfma_f32_16x16x32_bf16 v[100:103], v[172:175], a[200:203], v[100:103]\n"
      "v_mfma_f32_16x16x32_bf16 v[104:107], v[172:175], v[36:39], v[104:107]\n"
      "s_waitcnt vmcnt(4) lgkmcnt(0)\n"
      "s_barrier\n"
      "ds_read_b64_tr_b16 a[64:65], v19 offset:16384\n"
      "ds_read_b64_tr_b16 a[66:67], v19 offset:17408\n"
      "ds_read_b64_tr_b16 a[136:137], v19 offset:24576\n"
      "ds_read_b64_tr_b16 a[138:139], v19 offset:25600\n"
      "ds_read_b64_tr_b16 a[208:209], v19 offset:16392\n"
      "ds_read_b64_tr_b16 a[210:211], v19 offset:17416\n"
      "ds_read_b64_tr_b16 v[44:45], v19 offset:24584\n"
      "ds_read_b64_tr_b16 v[46:47], v19 offset:25608\n"
      "v_mfma_f32_16x16x32_bf16 v[108:111], v[172:175], a[60:63], v[92:95]\n"
      "v_mfma_f32_16x16x32_bf16 v[112:115], v[172:175], a[132:135], v[96:99]\n"
      "v_mfma_f32_16x16x32_bf16 v[116:119], v[172:175], a[204:207], v[100:103]\n"
      "v_mfma_f32_16x16x32_bf16 v[120:123], v[172:175], v[40:43], v[104:107]\n"
      "s_waitcnt vmcnt(0) lgkmcnt(0)\n"
      "s_barrier\n"
      "ds_read_b64_tr_b16 a[68:69], v19 offset:32768\n"
      "ds_read_b64_tr_b16 a[70:71], v19 offset:33792\n"
      "ds_read_b64_tr_b16 a[140:141], v19 offset:40960\n"
      "ds_read_b64_tr_b16 a[142:143], v19 offset:41984\n"
      "ds_read_b64_tr_b16 a[212:213], v19 offset:32776\n"
      "ds_read_b64_tr_b16 a[214:215], v19 offset:33800\n"
      "ds_read_b64_tr_b16 v[48:49], v19 offset:40968\n"
      "ds_read_b64_tr_b16 v[50:51], v19 offset:41992\n"
      "v_mfma_f32_16x16x32_bf16 v[108:111], v[172:175], a[64:67], v[108:111]\n"
      "v_mfma_f32_16x16x32_bf16 v[112:115], v[172:175], a[136:139], v[112:115]\n"
      "v_mfma_f32_16x16x32_bf16 v[116:119], v[172:175], a[208:211], v[116:119]\n"
      "v_mfma_f32_16x16x32_bf16 v[120:123], v[172:175], v[44:47], v[120:123]\n"
      "s_waitcnt lgkmcnt(0)\n"
      "v_mfma_f32_16x16x32_bf16 v[140:143], v[172:175], a[68:71], v[108:111]\n"
      "v_mfma_f32_16x16x32_bf16 v[144:147], v[172:175], a[140:143], v[112:115]\n"
      "v_mfma_f32_16x16x32_bf16 v[148:151], v[172:175], a[212:215], v[116:119]\n"
      "v_mfma_f32_16x16x32_bf16 v[152:155], v[172:175], v[48:51], v[120:123]\n"
      "s_getpc_b64 vcc\n"
      ".Lg_ct:\n"
      "s_add_u32 vcc_lo, vcc_lo, .Lg_p0-.Lg_ct\n"
      "s_addc_u32 vcc_hi, vcc_hi, 0\n"
      "s_lshl_b32 s30, s38, 1\n"
      "v_lshl_add_u32 v60, v0, 3, s30\n"
      "v_min_u32 v60, .Lg_last-.Lg_p0-4, v60\n"
      "global_load_dword v60, v60, vcc\n"
      "s_mul_i32 s21, s21, 1664525\n"
      "s_add_u32 s21, s21, 1013904223\n"
      "v_mov_b32 v15, s21\n"
      "buffer_store_dword v15, v12, s[8:11], 0 offen sc1\n"
      "v_not_b32 v52, v15\n"
      "v_not_b32 v53, v15\n"
      "v_not_b32 v54, v15\n"
      "v_not_b32 v55, v15\n"
      "s_add_u32 s30, s37, 0\n"
      "buffer_store_dwordx4 v[52:55], v10, s[8:11], s30 offen sc1\n"
      "v_xor_b32 v15, v15, v16\n"
      "v_mov_b32 v66, 0\n"
      "v_mov_b32 v67, 0\n"
      "v_mov_b32 v68, 0\n"
      "v_mov_b32 v69, 0\n"
      "v_mov_b32 v70, v64\n"
      "v_mov_b32 v71, v65\n"
      "v_sub_f32 v156, v140, v76\n"
      "v_sub_f32 v157, v144, v80\n"
      "v_sub_f32 v158, v148, v84\n"
      "v_sub_f32 v159, v152, v88\n"
      "v_sub_f32 v160, v140, v92\n"
      "v_sub_f32 v161, v144, v96\n"
      "v_sub_f32 v162, v148, v100\n"
      "v_sub_f32 v163, v152, v104\n"
      "v_sub_f32 v164, v140, v108\n"
      "v_sub_f32 v165, v144, v112\n"
      "v_sub_f32 v166, v148, v116\n"
      "v_sub_f32 v167, v152, v120\n"
      "v_mul_f32 v140, 0xbae38e39, v140\n"
      "v_mul_f32 v141, 0xbae38e39, v141\n"
      "v_mul_f32 v142, 0xbae38e39, v142\n"
      "v_mul_f32 v143, 0xbae38e39, v143\n"
      "v_mul_f32 v144, 0xbae38e39, v144\n"
      "v_mul_f32 v145, 0xbae38e39, v145\n"
      "v_mul_f32 v146, 0xbae38e39, v146\n"
      "v_mul_f32 v147, 0xbae38e39, v147\n"
      "v_mul_f32 v148, 0xbae38e39, v148\n"
      "v_mul_f32 v149, 0xbae38e39, v149\n"
      "v_mul_f32 v150, 0xbae38e39, v150\n"
      "v_mul_f32 v151, 0xbae38e39, v151\n"
      "v_mul_f32 v152, 0xbae38e39, v152\n"
      "v_mul_f32 v153, 0xbae38e39, v153\n"
      "v_mul_f32 v154, 0xbae38e39, v154\n"
      "v_mul_f32 v155, 0xbae38e39, v155\n"
      "v_mfma_f32_16x16x32_bf16 v[72:75], v[64:67], a[0:3], v[140:143]\n"
      "v_mfma_f32_16x16x32_bf16 v[76:79], v[68:71], a[0:3], v[140:143]\n"
      "v_mfma_f32_16x16x32_bf16 v[80:83], v[64:67], a[72:75], v[144:147]\n"
      "v_mfma_f32_16x16x32_bf16 v[84:87], v[68:71], a[72:75], v[144:147]\n"
      "v_mfma_f32_16x16x32_bf16 v[88:91], v[64:67], a[144:147], v[148:151]\n"
      "s_nop 4\n"
      "v_cvt_pk_bf16_f32 v72, v72, v73\n"
      "v_cvt_pk_bf16_f32 v73, v74, v75\n"
      "v_cvt_pk_bf16_f32 v74, v76, v77\n"
      "v_mfma_f32_16x16x32_bf16 v[92:95], v[68:71], a[144:147], v[148:151]\n"
      "v_cvt_pk_bf16_f32 v75, v78, v79\n"
      "v_accvgpr_write_b32 a0, v72\n"
      "v_accvgpr_write_b32 a1, v73\n"
      "v_accvgpr_write_b32 a2, v74\n"
      "v_accvgpr_write_b32 a3, v75\n"
      "v_mfma_f32_16x16x32_bf16 v[96:99], v[64:67], a[216:219], v[152:155]\n"
      "v_cvt_pk_bf16_f32 v80, v80, v81\n"
      "v_cvt_pk_bf16_f32 v81, v82, v83\n"
      "v_cvt_pk_bf16_f32 v82, v84, v85\n"
      "v_mfma_f32_16x16x32_bf16 v[100:103], v[68:71], a[216:219], v[152:155]\n"
      "v_cvt_pk_bf16_f32 v83, v86, v87\n"
      "v_accvgpr_write_b32 a72, v80\n"
      "v_accvgpr_write_b32 a73, v81\n"
      "v_mfma_f32_16x16x32_bf16 v[124:127], v[172:175], a[0:3], 0\n"
      "v_accvgpr_write_b32 a74, v82\n"
      "v_accvgpr_write_b32 a75, v83\n"
      "v_mfma_f32_16x16x32_bf16 v[72:75], v[64:67], a[4:7], v[140:143]\n"
      "v_cvt_pk_bf16_f32 v88, v88, v89\n"
      "v_cvt_pk_bf16_f32 v89, v90, v91\n"
      "v_cvt_pk_bf16_f32 v90, v92, v93\n"
      "v_mfma_f32_16x16x32_bf16 v[76:79], v[68:71], a[4:7], v[140:143]\n"
      "v_cvt_pk_bf16_f32 v91, v94, v95\n"
      "v_accvgpr_write_b32 a144, v88\n"
      "v_accvgpr_write_b32 a145, v89\n"
      "v_mfma_f32_16x16x32_bf16 v[128:131], v[172:175], a[72:75], 0\n"
      "v_accvgpr_write_b32 a146, v90\n"
      "v_accvgpr_write_b32 a147, v91\n"
      "v_mfma_f32_16x16x32_bf16 v[80:83], v[64:67], a[76:79], v[144:147]\n"
      "v_cvt_pk_bf16_f32 v96, v96, v97\n"
      "v_cvt_pk_bf16_f32 v97, v98, v99\n"
      "v_cvt_pk_bf16_f32 v98, v100, v101\n"
      "v_mfma_f32_16x16x32_bf16 v[84:87], v[68:71], a[76:79], v[144:147]\n"
      "v_cvt_pk_bf16_f32 v99, v102, v103\n"
      "v_accvgpr_write_b32 a216, v96\n"
      "v_accvgpr_write_b32 a217, v97\n"
      "v_mfma_f32_16x16x32_bf16 v[132:135], v[172:175], a[144:147], 0\n"
      "v_accvgpr_write_b32 a218, v98\n"
      "v_accvgpr_write_b32 a219, v99\n"
      "v_mfma_f32_16x16x32_bf16 v[88:91], v[64:67], a[148:151], v[148:151]\n"
      "v_cvt_pk_bf16_f32 v72, v72, v73\n"
      "v_cvt_pk_bf16_f32 v73, v74, v75\n"
      "v_cvt_pk_bf16_f32 v74, v76, v77\n"
      "v_mfma_f32_16x16x32_bf16 v[92:95], v[68:71], a[148:151], v[148:151]\n"
      "v_cvt_pk_bf16_f32 v75, v78, v79\n"
      "v_accvgpr_write_b32 a4, v72\n"
      "v_accvgpr_write_b32 a5, v73\n"
      "v_mfma_f32_16x16x32_bf16 v[136:139], v[172:175], a[216:219], 0\n"
      "v_accvgpr_write_b32 a6, v74\n"
      "v_accvgpr_write_b32 a7, v75\n"
      "v_mfma_f32_16x16x32_bf16 v[96:99], v[64:67], a[220:223], v[152:155]\n"
      "v_cvt_pk_bf16_f32 v80, v80, v81\n"
      "v_cvt_pk_bf16_f32 v81, v82, v83\n"
      "v_cvt_pk_bf16_f32 v82, v84, v85\n"
      "v_mfma_f32_16x16x32_bf16 v[100:103], v[68:71], a[220:223], v[152:155]\n"
      "v_cvt_pk_bf16_f32 v83, v86, v87\n"
      "v_accvgpr_write_b32 a76, v80\n"
      "v_accvgpr_write_b32 a77, v81\n"
      "v_mfma_f32_16x16x32_bf16 v[124:127], v[172:175], a[4:7], v[124:127]\n"
      "v_accvgpr_write_b32 a78, v82\n"
      "v_accvgpr_write_b32 a79, v83\n"
      "v_mfma_f32_16x16x32_bf16 v[72:75], v[64:67], a[8:11], v[140:143]\n"
      "v_cvt_pk_bf16_f32 v88, v88, v89\n"
      "v_cvt_pk_bf16_f32 v89, v90, v91\n"
      "v_cvt_pk_bf16_f32 v90, v92, v93\n"
      "v_mfma_f32_16x16x32_bf16 v[76:79], v[68:71], a[8:11], v[140:143]\n"
      "v_cvt_pk_bf16_f32 v91, v94, v95\n"
      "v_accvgpr_write_b32 a148, v88\n"
      "v_accvgpr_write_b32 a149, v89\n"
      "v_mfma_f32_16x16x32_bf16 v[128:131], v[172:175], a[76:79], v[128:131]\n"
      "v_accvgpr_write_b32 a150, v90\n"
      "v_accvgpr_write_b32 a151, v91\n"
      "v_mfma_f32_16x16x32_bf16 v[80:83], v[64:67], a[80:83], v[144:147]\n"
      "v_cvt_pk_bf16_f32 v96, v96, v97\n"
      "v_cvt_pk_bf16_f32 v97, v98, v99\n"
      "v_cvt_pk_bf16_f32 v98, v100, v101\n"
      "v_mfma_f32_16x16x32_bf16 v[84:87], v[68:71], a[80:83], v[144:147]\n"
      "v_cvt_pk_bf16_f32 v99, v102, v103\n"
      "v_accvgpr_write_b32 a220, v96\n"
      "v_accvgpr_write_b32 a221, v97\n"
      "v_mfma_f32_16x16x32_bf16 v[132:135], v[172:175], a[148:151], v[132:135]\n"
      "v_accvgpr_write_b32 a222, v98\n"
      "v_accvgpr_write_b32 a223, v99\n"
      "v_mfma_f32_16x16x32_bf16 v[88:91], v[64:67], a[152:155], v[148:151]\n"
      "v_cvt_pk_bf16_f32 v72, v72, v73\n"
      "v_cvt_pk_bf16_f32 v73, v74, v75\n"
      "v_cvt_pk_bf16_f32 v74, v76, v77\n"
      "v_mfma_f32_16x16x32_bf16 v[92:95], v[68:71], a[152:155], v[148:151]\n"
      "v_cvt_pk_bf16_f32 v75, v78, v79\n"
      "v_accvgpr_write_b32 a8, v72\n"
      "v_accvgpr_write_b32 a9, v73\n"
      "v_mfma_f32_16x16x32_bf16 v[136:139], v[172:175], a[220:223], v[136:139]\n"
      "v_accvgpr_write_b32 a10, v74\n"
      "v_accvgpr_write_b32 a11, v75\n"
      "v_mfma_f32_16x16x32_bf16 v[96:99], v[64:67], a[224:227], v[152:155]\n"
      "v_cvt_pk_bf16_f32 v80, v80, v81\n"
      "v_cvt_pk_bf16_f32 v81, v82, v83\n"
      "v_cvt_pk_bf16_f32 v82, v84, v85\n"
      "v_mfma_f32_16x16x32_bf16 v[100:103], v[68:71], a[224:227], v[152:155]\n"
      "v_cvt_pk_bf16_f32 v83, v86, v87\n"
      "v_accvgpr_write_b32 a80, v80\n"
      "v_accvgpr_write_b32 a81, v81\n"
      "v_mfma_f32_16x16x32_bf16 v[124:127], v[172:175], a[8:11], v[124:127]\n"
      "v_accvgpr_write_b32 a82, v82\n"
      "v_accvgpr_write_b32 a83, v83\n"
      "v_mfma_f32_16x16x32_bf16 v[72:75], v[64:67], a[12:15], v[140:143]\n"
      "v_cvt_pk_bf16_f32 v88, v88, v89\n"
      "v_cvt_pk_bf16_f32 v89, v90, v91\n"
      "v_cvt_pk_bf16_f32 v90, v92, v93\n"
      "v_mfma_f32_16x16x32_bf16 v[76:79], v[68:71], a[12:15], v[140:143]\n"
      "v_cvt_pk_bf16_f32 v91, v94, v95\n"
      "v_accvgpr_write_b32 a152, v88\n"
      "v_accvgpr_write_b32 a153, v89\n"
      "v_mfma_f32_16x16x32_bf16 v[128:131], v[172:175], a[80:83], v[128:131]\n"
      "v_accvgpr_write_b32 a154, v90\n"
      "v_accvgpr_write_b32 a155, v91\n"
      "v_mfma_f32_16x16x32_bf16 v[80:83], v[64:67], a[84:87], v[144:147]\n"
      "v_cvt_pk_bf16_f32 v96, v96, v97\n"
      "v_cvt_pk_bf16_f32 v97, v98, v99\n"
      "v_cvt_pk_bf16_f32 v98, v100, v101\n"
      "v_mfma_f32_16x16x32_bf16 v[84:87], v[68:71], a[84:87], v[144:147]\n"
      "v_cvt_pk_bf16_f32 v99, v102, v103\n"
      "v_accvgpr_write_b32 a224, v96\n"
      "v_accvgpr_write_b32 a225, v97\n"
      "v_mfma_f32_16x16x32_bf16 v[132:135], v[172:175], a[152:155], v[132:135]\n"
      "v_accvgpr_write_b32 a226, v98\n"
      "v_accvgpr_write_b32 a227, v99\n"
      "v_mfma_f32_16x16x32_bf16 v[88:91], v[64:67], a[156:159], v[148:151]\n"
      "v_cvt_pk_bf16_f32 v72, v72, v73\n"
      "v_cvt_pk_bf16_f32 v73, v74, v75\n"
      "v_cvt_pk_bf16_f32 v74, v76, v77\n"
      "v_mfma_f32_16x16x32_bf16 v[92:95], v[68:71], a[156:159], v[148:151]\n"
      "v_cvt_pk_bf16_f32 v75, v78, v79\n"
      "v_accvgpr_write_b32 a12, v72\n"
      "v_accvgpr_write_b32 a13, v73\n"
      "v_mfma_f32_16x16x32_bf16 v[136:139], v[172:175], a[224:227], v[136:139]\n"
      "v_accvgpr_write_b32 a14, v74\n"
      "v_accvgpr_write_b32 a15, v75\n"
      "v_mfma_f32_16x16x32_bf16 v[96:99], v[64:67], a[228:231], v[152:155]\n"
      "v_cvt_pk_bf16_f32 v80, v80, v81\n"
      "v_cvt_pk_bf16_f32 v81, v82, v83\n"
      "v_cvt_pk_bf16_f32 v82, v84, v85\n"
      "v_mfma_f32_16x16x32_bf16 v[100:103], v[68:71], a[228:231], v[152:155]\n"
      "v_cvt_pk_bf16_f32 v83, v86, v87\n"
      "v_accvgpr_write_b32 a84, v80\n"
      "v_accvgpr_write_b32 a85, v81\n"
      "v_mfma_f32_16x16x32_bf16 v[124:127], v[172:175], a[12:15], v[124:127]\n"
      "v_accvgpr_write_b32 a86, v82\n"
      "v_accvgpr_write_b32 a87, v83\n"
      "v_mfma_f32_16x16x32_bf16 v[72:75], v[64:67], a[16:19], v[140:143]\n"
      "v_cvt_pk_bf16_f32 v88, v88, v89\n"
      "v_cvt_pk_bf16_f32 v89, v90, v91\n"
      "v_cvt_pk_bf16_f32 v90, v92, v93\n"
      "v_mfma_f32_16x16x32_bf16 v[76:79], v[68:71], a[16:19], v[140:143]\n"
      "v_cvt_pk_bf16_f32 v91, v94, v95\n"
      "v_accvgpr_write_b32 a156, v88\n"
      "v_accvgpr_write_b32 a157, v89\n"
      "v_mfma_f32_16x16x32_bf16 v[128:131], v[172:175], a[84:87], v[128:131]\n"
      "v_accvgpr_write_b32 a158, v90\n"
      "v_accvgpr_write_b32 a159, v91\n"
      "v_mfma_f32_16x16x32_bf16 v[80:83], v[64:67], a[88:91], v[144:147]\n"
      "v_cvt_pk_bf16_f32 v96, v96, v97\n"
      "v_cvt_pk_bf16_f32 v97, v98, v99\n"
      "v_cvt_pk_bf16_f32 v98, v100, v101\n"
      "v_mfma_f32_16x16x32_bf16 v[84:87], v[68:71], a[88:91], v[144:147]\n"
      "v_cvt_pk_bf16_f32 v99, v102, v103\n"
      "v_accvgpr_write_b32 a228, v96\n"
      "v_accvgpr_write_b32 a229, v97\n"
      "v_mfma_f32_16x16x32_bf16 v[132:135], v[172:175], a[156:159], v[132:135]\n"
      "v_accvgpr_write_b32 a230, v98\n"
      "v_accvgpr_write_b32 a231, v99\n"
      "v_mfma_f32_16x16x32_bf16 v[88:91], v[64:67], a[160:163], v[148:151]\n"
      "v_cvt_pk_bf16_f32 v72, v72, v73\n"
      "v_cvt_pk_bf16_f32 v73, v74, v75\n"
      "v_cvt_pk_bf16_f32 v74, v76, v77\n"
      "v_mfma_f32_16x16x32_bf16 v[92:95], v[68:71], a[160:163], v[148:151]\n"
      "v_cvt_pk_bf16_f32 v75, v78, v79\n"
      "v_accvgpr_write_b32 a16, v72\n"
      "v_accvgpr_write_b32 a17, v73\n"
      "v_mfma_f32_16x16x32_bf16 v[136:139], v[172:175], a[228:231], v[136:139]\n"
      "v_accvgpr_write_b32 a18, v74\n"
      "v_accvgpr_write_b32 a19, v75\n"
      "v_mfma_f32_16x16x32_bf16 v[96:99], v[64:67], a[232:235], v[152:155]\n"
      "v_cvt_pk_bf16_f32 v80, v80, v81\n"
      "v_cvt_pk_bf16_f32 v81, v82, v83\n"
      "v_cvt_pk_bf16_f32 v82, v84, v85\n"
      "v_mfma_f32_16x16x32_bf16 v[100:103], v[68:71], a[232:235], v[152:155]\n"
      "v_cvt_pk_bf16_f32 v83, v86, v87\n"
      "v_accvgpr_write_b32 a88, v80\n"
      "v_accvgpr_write_b32 a89, v81\n"
      "v_mfma_f32_16x16x32_bf16 v[124:127], v[172:175], a[16:19], v[124:127]\n"
      "v_accvgpr_write_b32 a90, v82\n"
      "v_accvgpr_write_b32 a91, v83\n"
      "v_mfma_f32_16x16x32_bf16 v[72:75], v[64:67], a[20:23], v[140:143]\n"
      "v_cvt_pk_bf16_f32 v88, v88, v89\n"
      "v_cvt_pk_bf16_f32 v89, v90, v91\n"
      "v_cvt_pk_bf16_f32 v90, v92, v93\n"
      "v_mfma_f32_16x16x32_bf16 v[76:79], v[68:71], a[20:23], v[140:143]\n"
      "v_cvt_pk_bf16_f32 v91, v94, v95\n"
      "v_accvgpr_write_b32 a160, v88\n"
      "v_accvgpr_write_b32 a161, v89\n"
      "v_mfma_f32_16x16x32_bf16 v[128:131], v[172:175], a[88:91], v[128:131]\n"
      "v_accvgpr_write_b32 a162, v90\n"
      "v_accvgpr_write_b32 a163, v91\n"
      "v_mfma_f32_16x16x32_bf16 v[80:83], v[64:67], a[92:95], v[144:147]\n"
      "v_cvt_pk_bf16_f32 v96, v96, v97\n"
      "v_cvt_pk_bf16_f32 v97, v98, v99\n"
      "v_cvt_pk_bf16_f32 v98, v100, v101\n"
      "v_mfma_f32_16x16x32_bf16 v[84:87], v[68:71], a[92:95], v[144:147]\n"
      "v_cvt_pk_bf16_f32 v99, v102, v103\n"
      "v_accvgpr_write_b32 a232, v96\n"
      "v_accvgpr_write_b32 a233, v97\n"
      "v_mfma_f32_16x16x32_bf16 v[132:135], v[172:175], a[160:163], v[132:135]\n"
      "v_accvgpr_write_b32 a234, v98\n"
      "v_accvgpr_write_b32 a235, v99\n"
      "v_mfma_f32_16x16x32_bf16 v[88:91], v[64:67], a[164:167], v[148:151]\n"
      "v_cvt_pk_bf16_f32 v72, v72, v73\n"
      "v_cvt_pk_bf16_f32 v73, v74, v75\n"
      "v_cvt_pk_bf16_f32 v74, v76, v77\n"
      "v_mfma_f32_16x16x32_bf16 v[92:95], v[68:71], a[164:167], v[148:151]\n"
      "v_cvt_pk_bf16_f32 v75, v78, v79\n"
      "v_accvgpr_write_b32 a20, v72\n"
      "v_accvgpr_write_b32 a21, v73\n"
      "v_mfma_f32_16x16x32_bf16 v[136:139], v[172:175], a[232:235], v[136:139]\n"
      "v_accvgpr_write_b32 a22, v74\n"
      "v_accvgpr_write_b32 a23, v75\n"
      "v_mfma_f32_16x16x32_bf16 v[96:99], v[64:67], a[236:239], v[152:155]\n"
      "v_cvt_pk_bf16_f32 v80, v80, v81\n"
      "v_cvt_pk_bf16_f32 v81, v82, v83\n"
      "v_cvt_pk_bf16_f32 v82, v84, v85\n"
      "v_mfma_f32_16x16x32_bf16 v[100:103], v[68:71], a[236:239], v[152:155]\n"
      "v_cvt_pk_bf16_f32 v83, v86, v87\n"
      "v_accvgpr_write_b32 a92, v80\n"
      "v_accvgpr_write_b32 a93, v81\n"
      "v_mfma_f32_16x16x32_bf16 v[124:127], v[172:175], a[20:23], v[124:127]\n"
      "v_accvgpr_write_b32 a94, v82\n"
      "v_accvgpr_write_b32 a95, v83\n"
      "v_mfma_f32_16x16x32_bf16 v[72:75], v[64:67], a[24:27], v[140:143]\n"
      "v_cvt_pk_bf16_f32 v88, v88, v89\n"
      "v_cvt_pk_bf16_f32 v89, v90, v91\n"
      "v_cvt_pk_bf16_f32 v90, v92, v93\n"
      "v_mfma_f32_16x16x32_bf16 v[76:79], v[68:71], a[24:27], v[140:143]\n"
      "v_cvt_pk_bf16_f32 v91, v94, v95\n"
      "v_accvgpr_write_b32 a164, v88\n"
      "v_accvgpr_write_b32 a165, v89\n"
      "v_mfma_f32_16x16x32_bf16 v[128:131], v[172:175], a[92:95], v[128:131]\n"
      "v_accvgpr_write_b32 a166, v90\n"
      "v_accvgpr_write_b32 a167, v91\n"
      "v_mfma_f32_16x16x32_bf16 v[80:83], v[64:67], a[96:99], v[144:147]\n"
      "v_cvt_pk_bf16_f32 v96, v96, v97\n"
      "v_cvt_pk_bf16_f32 v97, v98, v99\n"
      "v_cvt_pk_bf16_f32 v98, v100, v101\n"
      "v_mfma_f32_16x16x32_bf16 v[84:87], v[68:71], a[96:99], v[144:147]\n"
      "v_cvt_pk_bf16_f32 v99, v102, v103\n"
      "v_accvgpr_write_b32 a236, v96\n"
      "v_accvgpr_write_b32 a237, v97\n"
      "v_mfma_f32_16x16x32_bf16 v[132:135], v[172:175], a[164:167], v[132:135]\n"
      "v_accvgpr_write_b32 a238, v98\n"
      "v_accvgpr_write_b32 a239, v99\n"
      "v_mfma_f32_16x16x32_bf16 v[88:91], v[64:67], a[168:171], v[148:151]\n"
      "v_cvt_pk_bf16_f32 v72, v72, v73\n"
      "v_cvt_pk_bf16_f32 v73, v74, v75\n"
      "v_cvt_pk_bf16_f32 v74, v76, v77\n"
      "v_mfma_f32_16x16x32_bf16 v[92:95], v[68:71], a[168:171], v[148:151]\n"
      "v_cvt_pk_bf16_f32 v75, v78, v79\n"
      "v_accvgpr_write_b32 a24, v72\n"
      "v_accvgpr_write_b32 a25, v73\n"
      "v_mfma_f32_16x16x32_bf16 v[136:139], v[172:175], a[236:239], v[136:139]\n"
      "v_accvgpr_write_b32 a26, v74\n"
      "v_accvgpr_write_b32 a27, v75\n"
      "v_mfma_f32_16x16x32_bf16 v[96:99], v[64:67], a[240:243], v[152:155]\n"
      "v_cvt_pk_bf16_f32 v80, v80, v81\n"
      "v_cvt_pk_bf16_f32 v81, v82, v83\n"
      "v_cvt_pk_bf16_f32 v82, v84, v85\n"
      "v_mfma_f32_16x16x32_bf16 v[100:103], v[68:71], a[240:243], v[152:155]\n"
      "v_cvt_pk_bf16_f32 v83, v86, v87\n"
      "v_accvgpr_write_b32 a96, v80\n"
      "v_accvgpr_write_b32 a97, v81\n"
      "v_mfma_f32_16x16x32_bf16 v[124:127], v[172:175], a[24:27], v[124:127]\n"
      "v_accvgpr_write_b32 a98, v82\n"
      "v_accvgpr_write_b32 a99, v83\n"
      "v_mfma_f32_16x16x32_bf16 v[72:75], v[64:67], a[28:31], v[140:143]\n"
      "v_cvt_pk_bf16_f32 v88, v88, v89\n"
      "v_cvt_pk_bf16_f32 v89, v90, v91\n"
      "v_cvt_pk_bf16_f32 v90, v92, v93\n"
      "v_mfma_f32_16x16x32_bf16 v[76:79], v[68:71], a[28:31], v[140:143]\n"
      "v_cvt_pk_bf16_f32 v91, v94, v95\n"
      "v_accvgpr_write_b32 a168, v88\n"
      "v_accvgpr_write_b32 a169, v89\n"
      "v_mfma_f32_16x16x32_bf16 v[128:131], v[172:175], a[96:99], v[128:131]\n"
      "v_accvgpr_write_b32 a170, v90\n"
      "v_accvgpr_write_b32 a171, v91\n"
      "v_mfma_f32_16x16x32_bf16 v[80:83], v[64:67], a[100:103], v[144:147]\n"
      "v_cvt_pk_bf16_f32 v96, v96, v97\n"
      "v_cvt_pk_bf16_f32 v97, v98, v99\n"
      "v_cvt_pk_bf16_f32 v98, v100, v101\n"
      "v_mfma_f32_16x16x32_bf16 v[84:87], v[68:71], a[100:103], v[144:147]\n"
      "v_cvt_pk_bf16_f32 v99, v102, v103\n"
      "v_accvgpr_write_b32 a240, v96\n"
      "v_accvgpr_write_b32 a241, v97\n"
      "v_mfma_f32_16x16x32_bf16 v[132:135], v[172:175], a[168:171], v[132:135]\n"
      "v_accvgpr_write_b32 a242, v98\n"
      "v_accvgpr_write_b32 a243, v99\n"
      "v_mfma_f32_16x16x32_bf16 v[88:91], v[64:67], a[172:175], v[148:151]\n"
      "v_cvt_pk_bf16_f32 v72, v72, v73\n"
      "v_cvt_pk_bf16_f32 v73, v74, v75\n"
      "v_cvt_pk_bf16_f32 v74, v76, v77\n"
      "v_mfma_f32_16x16x32_bf16 v[92:95], v[68:71], a[172:175], v[148:151]\n"
      "v_cvt_pk_bf16_f32 v75, v78, v79\n"
      "v_accvgpr_write_b32 a28, v72\n"
      "v_accvgpr_write_b32 a29, v73\n"
      "v_mfma_f32_16x16x32_bf16 v[136:139], v[172:175], a[240:243], v[136:139]\n"
      "v_accvgpr_write_b32 a30, v74\n"
      "v_accvgpr_write_b32 a31, v75\n"
      "v_mfma_f32_16x16x32_bf16 v[96:99], v[64:67], a[244:247], v[152:155]\n"
      "v_cvt_pk_bf16_f32 v80, v80, v81\n"
      "v_cvt_pk_bf16_f32 v81, v82, v83\n"
      "v_cvt_pk_bf16_f32 v82, v84, v85\n"
      "v_mfma_f32_16x16x32_bf16 v[100:103], v[68:71], a[244:247], v[152:155]\n"
      "v_cvt_pk_bf16_f32 v83, v86, v87\n"
      "v_accvgpr_write_b32 a100, v80\n"
      "v_accvgpr_write_b32 a101, v81\n"
      "v_mfma_f32_16x16x32_bf16 v[124:127], v[172:175], a[28:31], v[124:127]\n"
      "v_accvgpr_write_b32 a102, v82\n"
      "v_accvgpr_write_b32 a103, v83\n"
      "v_mfma_f32_16x16x32_bf16 v[72:75], v[64:67], a[32:35], v[140:143]\n"
      "v_cvt_pk_bf16_f32 v88, v88, v89\n"
      "v_cvt_pk_bf16_f32 v89, v90, v91\n"
      "v_cvt_pk_bf16_f32 v90, v92, v93\n"
      "v_mfma_f32_16x16x32_bf16 v[76:79], v[68:71], a[32:35], v[140:143]\n"
      "v_cvt_pk_bf16_f32 v91, v94, v95\n"
      "v_accvgpr_write_b32 a172, v88\n"
      "v_accvgpr_write_b32 a173, v89\n"
      "v_mfma_f32_16x16x32_bf16 v[128:131], v[172:175], a[100:103], v[128:131]\n"
      "v_accvgpr_write_b32 a174, v90\n"
      "v_accvgpr_write_b32 a175, v91\n"
      "v_mfma_f32_16x16x32_bf16 v[80:83], v[64:67], a[104:107], v[144:147]\n"
      "v_cvt_pk_bf16_f32 v96, v96, v97\n"
      "v_cvt_pk_bf16_f32 v97, v98, v99\n"
      "v_cvt_pk_bf16_f32 v98, v100, v101\n"
      "v_mfma_f32_16x16x32_bf16 v[84:87], v[68:71], a[104:107], v[144:147]\n"
      "v_cvt_pk_bf16_f32 v99, v102, v103\n"
      "v_accvgpr_write_b32 a244, v96\n"
      "v_accvgpr_write_b32 a245, v97\n"
      "v_mfma_f32_16x16x32_bf16 v[132:135], v[172:175], a[172:175], v[132:135]\n"
      "v_accvgpr_write_b32 a246, v98\n"
      "v_accvgpr_write_b32 a247, v99\n"
      "v_mfma_f32_16x16x32_bf16 v[88:91], v[64:67], a[176:179], v[148:151]\n"
      "v_cvt_pk_bf16_f32 v72, v72, v73\n"
      "v_cvt_pk_bf16_f32 v73, v74, v75\n"
      "v_cvt_pk_bf16_f32 v74, v76, v77\n"
      "v_mfma_f32_16x16x32_bf16 v[92:95], v[68:71], a[176:179], v[148:151]\n"
      "v_cvt_pk_bf16_f32 v75, v78, v79\n"
      "v_accvgpr_write_b32 a32, v72\n"
      "v_accvgpr_write_b32 a33, v73\n"
      "v_mfma_f32_16x16x32_bf16 v[136:139], v[172:175], a[244:247], v[136:139]\n"
      "v_accvgpr_write_b32 a34, v74\n"
      "v_accvgpr_write_b32 a35, v75\n"
      "v_mfma_f32_16x16x32_bf16 v[96:99], v[64:67], a[248:251], v[152:155]\n"
      "v_cvt_pk_bf16_f32 v80, v80, v81\n"
      "v_cvt_pk_bf16_f32 v81, v82, v83\n"
      "v_cvt_pk_bf16_f32 v82, v84, v85\n"
      "v_mfma_f32_16x16x32_bf16 v[100:103], v[68:71], a[248:251], v[152:155]\n"
      "v_cvt_pk_bf16_f32 v83, v86, v87\n"
      "v_accvgpr_write_b32 a104, v80\n"
      "v_accvgpr_write_b32 a105, v81\n"
      "v_mfma_f32_16x16x32_bf16 v[124:127], v[172:175], a[32:35], v[124:127]\n"
      "v_accvgpr_write_b32 a106, v82\n"
      "v_accvgpr_write_b32 a107, v83\n"
      "v_mfma_f32_16x16x32_bf16 v[72:75], v[64:67], a[36:39], v[140:143]\n"
      "v_cvt_pk_bf16_f32 v88, v88, v89\n"
      "v_cvt_pk_bf16_f32 v89, v90, v91\n"
      "v_cvt_pk_bf16_f32 v90, v92, v93\n"
      "v_mfma_f32_16x16x32_bf16 v[76:79], v[68:71], a[36:39], v[140:143]\n"
      "v_cvt_pk_bf16_f32 v91, v94, v95\n"
      "v_accvgpr_write_b32 a176, v88\n"
      "v_accvgpr_write_b32 a177, v89\n"
      "v_mfma_f32_16x16x32_bf16 v[128:131], v[172:175], a[104:107], v[128:131]\n"
      "v_accvgpr_write_b32 a178, v90\n"
      "v_accvgpr_write_b32 a179, v91\n"
      "v_mfma_f32_16x16x32_bf16 v[80:83], v[64:67], a[108:111], v[144:147]\n"
      "v_cvt_pk_bf16_f32 v96, v96, v97\n"
      "v_cvt_pk_bf16_f32 v97, v98, v99\n"
      "v_cvt_pk_bf16_f32 v98, v100, v101\n"
      "v_mfma_f32_16x16x32_bf16 v[84:87], v[68:71], a[108:111], v[144:147]\n"
      "v_cvt_pk_bf16_f32 v99, v102, v103\n"
      "v_accvgpr_write_b32 a248, v96\n"
      "v_accvgpr_write_b32 a249, v97\n"
      "v_mfma_f32_16x16x32_bf16 v[132:135], v[172:175], a[176:179], v[132:135]\n"
      "v_accvgpr_write_b32 a250, v98\n"
      "v_accvgpr_write_b32 a251, v99\n"
      "v_mfma_f32_16x16x32_bf16 v[88:91], v[64:67], a[180:183], v[148:151]\n"
      "v_cvt_pk_bf16_f32 v72, v72, v73\n"
      "v_cvt_pk_bf16_f32 v73, v74, v75\n"
      "v_cvt_pk_bf16_f32 v74, v76, v77\n"
      "v_mfma_f32_16x16x32_bf16 v[92:95], v[68:71], a[180:183], v[148:151]\n"
      "v_cvt_pk_bf16_f32 v75, v78, v79\n"
      "v_accvgpr_write_b32 a36, v72\n"
      "v_accvgpr_write_b32 a37, v73\n"
      "v_mfma_f32_16x16x32_bf16 v[136:139], v[172:175], a[248:251], v[136:139]\n"
      "v_accvgpr_write_b32 a38, v74\n"
      "v_accvgpr_write_b32 a39, v75\n"
      "v_mfma_f32_16x16x32_bf16 v[96:99], v[64:67], a[252:255], v[152:155]\n"
      "v_cvt_pk_bf16_f32 v80, v80, v81\n"
      "v_cvt_pk_bf16_f32 v81, v82, v83\n"
      "v_cvt_pk_bf16_f32 v82, v84, v85\n"
      "v_mfma_f32_16x16x32_bf16 v[100:103], v[68:71], a[252:255], v[152:155]\n"
      "v_cvt_pk_bf16_f32 v83, v86, v87\n"
      "v_accvgpr_write_b32 a108, v80\n"
      "v_accvgpr_write_b32 a109, v81\n"
      "v_mfma_f32_16x16x32_bf16 v[124:127], v[172:175], a[36:39], v[124:127]\n"
      "v_accvgpr_write_b32 a110, v82\n"
      "v_accvgpr_write_b32 a111, v83\n"
      "s_nop 1\n"
      "v_mfma_f32_16x16x32_bf16 v[128:131], v[172:175], a[108:111], v[128:131]\n"
      "v_cvt_pk_bf16_f32 v88, v88, v89\n"
      "v_cvt_pk_bf16_f32 v89, v90, v91\n"
      "v_cvt_pk_bf16_f32 v90, v92, v93\n"
      "v_cvt_pk_bf16_f32 v91, v94, v95\n"
      "v_accvgpr_write_b32 a180, v88\n"
      "v_accvgpr_write_b32 a181, v89\n"
      "v_accvgpr_write_b32 a182, v90\n"
      "v_accvgpr_write_b32 a183, v91\n"
      "s_nop 1\n"
      "v_mfma_f32_16x16x32_bf16 v[132:135], v[172:175], a[180:183], v[132:135]\n"
      "v_cvt_pk_bf16_f32 v96, v96, v97\n"
      "v_cvt_pk_bf16_f32 v97, v98, v99\n"
      "v_cvt_pk_bf16_f32 v98, v100, v101\n"
      "v_cvt_pk_bf16_f32 v99, v102, v103\n"
      "v_accvgpr_write_b32 a252, v96\n"
      "v_accvgpr_write_b32 a253, v97\n"
      "v_accvgpr_write_b32 a254, v98\n"
      "v_accvgpr_write_b32 a255, v99\n"
      "s_nop 1\n"
      "v_mfma_f32_16x16x32_bf16 v[136:139], v[172:175], a[252:255], v[136:139]\n"
      "v_add_f32 v168, v124, v156\n"
      "v_add_f32 v169, v128, v157\n"
      "v_add_f32 v170, v132, v158\n"
      "s_nop 5\n"
      "v_add_f32 v171, v136, v159\n"
      "v_mul_f32 v140, 0xbb800000, v168\n"
      "v_mul_f32 v141, 0xbb800000, v168\n"
      "v_mul_f32 v142, 0xbb800000, v168\n"
      "v_mul_f32 v143, 0xbb800000, v168\n"
      "v_mul_f32 v144, 0xbb800000, v169\n"
      "v_mul_f32 v145, 0xbb800000, v169\n"
      "v_mul_f32 v146, 0xbb800000, v169\n"
      "v_mul_f32 v147, 0xbb800000, v169\n"
      "v_mul_f32 v148, 0xbb800000, v170\n"
      "v_mul_f32 v149, 0xbb800000, v170\n"
      "v_mul_f32 v150, 0xbb800000, v170\n"
      "v_mul_f32 v151, 0xbb800000, v170\n"
      "v_mul_f32 v152, 0xbb800000, v171\n"
      "v_mul_f32 v153, 0xbb800000, v171\n"
      "v_mul_f32 v154, 0xbb800000, v171\n"
      "v_mul_f32 v155, 0xbb800000, v171\n"
      "v_mfma_f32_16x16x32_bf16 v[72:75], v[64:67], a[40:43], v[140:143]\n"
      "v_mfma_f32_16x16x32_bf16 v[76:79], v[68:71], a[40:43], v[140:143]\n"
      "v_mfma_f32_16x16x32_bf16 v[80:83], v[64:67], a[112:115], v[144:147]\n"
      "v_mfma_f32_16x16x32_bf16 v[84:87], v[68:71], a[112:115], v[144:147]\n"
      "v_mfma_f32_16x16x32_bf16 v[88:91], v[64:67], a[184:187], v[148:151]\n"
      "s_nop 4\n"
      "v_cvt_pk_bf16_f32 v72, v72, v73\n"
      "v_cvt_pk_bf16_f32 v73, v74, v75\n"
      "v_cvt_pk_bf16_f32 v74, v76, v77\n"
      "v_mfma_f32_16x16x32_bf16 v[92:95], v[68:71], a[184:187], v[148:151]\n"
      "v_cvt_pk_bf16_f32 v75, v78, v79\n"
      "v_accvgpr_write_b32 a40, v72\n"
      "v_accvgpr_write_b32 a41, v73\n"
      "v_accvgpr_write_b32 a42, v74\n"
      "v_accvgpr_write_b32 a43, v75\n"
      "v_mfma_f32_16x16x32_bf16 v[96:99], v[64:67], v[20:23], v[152:155]\n"
      "v_cvt_pk_bf16_f32 v80, v80, v81\n"
      "v_cvt_pk_bf16_f32 v81, v82, v83\n"
      "v_cvt_pk_bf16_f32 v82, v84, v85\n"
      "v_mfma_f32_16x16x32_bf16 v[100:103], v[68:71], v[20:23], v[152:155]\n"
      "v_cvt_pk_bf16_f32 v83, v86, v87\n"
      "v_accvgpr_write_b32 a112, v80\n"
      "v_accvgpr_write_b32 a113, v81\n"
      "v_mfma_f32_16x16x32_bf16 v[124:127], v[172:175], a[40:43], v[124:127]\n"
      "v_accvgpr_write_b32 a114, v82\n"
      "v_accvgpr_write_b32 a115, v83\n"
      "v_mfma_f32_16x16x32_bf16 v[72:75], v[64:67], a[44:47], v[140:143]\n"
      "v_cvt_pk_bf16_f32 v88, v88, v89\n"
      "v_cvt_pk_bf16_f32 v89, v90, v91\n"
      "v_cvt_pk_bf16_f32 v90, v92, v93\n"
      "v_mfma_f32_16x16x32_bf16 v[76:79], v[68:71], a[44:47], v[140:143]\n"
      "v_cvt_pk_bf16_f32 v91, v94, v95\n"
      "v_accvgpr_write_b32 a184, v88\n"
      "v_accvgpr_write_b32 a185, v89\n"
      "v_mfma_f32_16x16x32_bf16 v[128:131], v[172:175], a[112:115], v[128:131]\n"
      "v_accvgpr_write_b32 a186, v90\n"
      "v_accvgpr_write_b32 a187, v91\n"
      "v_mfma_f32_16x16x32_bf16 v[80:83], v[64:67], a[116:119], v[144:147]\n"
      "v_cvt_pk_bf16_f32 v20, v96, v97\n"
      "v_cvt_pk_bf16_f32 v21, v98, v99\n"
      "v_cvt_pk_bf16_f32 v22, v100, v101\n"
      "v_mfma_f32_16x16x32_bf16 v[84:87], v[68:71], a[116:119], v[144:147]\n"
      "v_cvt_pk_bf16_f32 v23, v102, v103\n"
      "v_mfma_f32_16x16x32_bf16 v[132:135], v[172:175], a[184:187], v[132:135]\n"
      "v_mfma_f32_16x16x32_bf16 v[88:91], v[64:67], a[188:191], v[148:151]\n"
      "v_cvt_pk_bf16_f32 v72, v72, v73\n"
      "v_cvt_pk_bf16_f32 v73, v74, v75\n"
      "v_cvt_pk_bf16_f32 v74, v76, v77\n"
      "v_mfma_f32_16x16x32_bf16 v[92:95], v[68:71], a[188:191], v[148:151]\n"
      "v_cvt_pk_bf16_f32 v75, v78, v79\n"
      "v_accvgpr_write_b32 a44, v72\n"
      "v_accvgpr_write_b32 a45, v73\n"
      "v_mfma_f32_16x16x32_bf16 v[136:139], v[172:175], v[20:23], v[136:139]\n"
      "v_accvgpr_write_b32 a46, v74\n"
      "v_accvgpr_write_b32 a47, v75\n"
      "v_mfma_f32_16x16x32_bf16 v[96:99], v[64:67], v[24:27], v[152:155]\n"
      "v_cvt_pk_bf16_f32 v80, v80, v81\n"
      "v_cvt_pk_bf16_f32 v81, v82, v83\n"
      "v_cvt_pk_bf16_f32 v82, v84, v85\n"
      "v_mfma_f32_16x16x32_bf16 v[100:103], v[68:71], v[24:27], v[152:155]\n"
      "v_cvt_pk_bf16_f32 v83, v86, v87\n"
      "v_accvgpr_write_b32 a116, v80\n"
      "v_accvgpr_write_b32 a117, v81\n"
      "v_mfma_f32_16x16x32_bf16 v[124:127], v[172:175], a[44:47], v[124:127]\n"
      "v_accvgpr_write_b32 a118, v82\n"
      "v_accvgpr_write_b32 a119, v83\n"
      "v_mfma_f32_16x16x32_bf16 v[72:75], v[64:67], a[48:51], v[140:143]\n"
      "v_cvt_pk_bf16_f32 v88, v88, v89\n"
      "v_cvt_pk_bf16_f32 v89, v90, v91\n"
      "v_cvt_pk_bf16_f32 v90, v92, v93\n"
      "v_mfma_f32_16x16x32_bf16 v[76:79], v[68:71], a[48:51], v[140:143]\n"
      "v_cvt_pk_bf16_f32 v91, v94, v95\n"
      "v_accvgpr_write_b32 a188, v88\n"
      "v_accvgpr_write_b32 a189, v89\n"
      "v_mfma_f32_16x16x32_bf16 v[128:131], v[172:175], a[116:119], v[128:131]\n"
      "v_accvgpr_write_b32 a190, v90\n"
      "v_accvgpr_write_b32 a191, v91\n"
      "v_mfma_f32_16x16x32_bf16 v[80:83], v[64:67], a[120:123], v[144:147]\n"
      "v_cvt_pk_bf16_f32 v24, v96, v97\n"
      "v_cvt_pk_bf16_f32 v25, v98, v99\n"
      "v_cvt_pk_bf16_f32 v26, v100, v101\n"
      "v_mfma_f32_16x16x32_bf16 v[84:87], v[68:71], a[120:123], v[144:147]\n"
      "v_cvt_pk_bf16_f32 v27, v102, v103\n"
      "v_mfma_f32_16x16x32_bf16 v[132:135], v[172:175], a[188:191], v[132:135]\n"
      "v_mfma_f32_16x16x32_bf16 v[88:91], v[64:67], a[192:195], v[148:151]\n"
      "v_cvt_pk_bf16_f32 v72, v72, v73\n"
      "v_cvt_pk_bf16_f32 v73, v74, v75\n"
      "v_cvt_pk_bf16_f32 v74, v76, v77\n"
      "v_mfma_f32_16x16x32_bf16 v[92:95], v[68:71], a[192:195], v[148:151]\n"
      "v_cvt_pk_bf16_f32 v75, v78, v79\n"
      "v_accvgpr_write_b32 a48, v72\n"
      "v_accvgpr_write_b32 a49, v73\n"
      "v_mfma_f32_16x16x32_bf16 v[136:139], v[172:175], v[24:27], v[136:139]\n"
      "v_accvgpr_write_b32 a50, v74\n"
      "v_accvgpr_write_b32 a51, v75\n"
      "v_mfma_f32_16x16x32_bf16 v[96:99], v[64:67], v[28:31], v[152:155]\n"
      "v_cvt_pk_bf16_f32 v80, v80, v81\n"
      "v_cvt_pk_bf16_f32 v81, v82, v83\n"
      "v_cvt_pk_bf16_f32 v82, v84, v85\n"
      "v_mfma_f32_16x16x32_bf16 v[100:103], v[68:71], v[28:31], v[152:155]\n"
      "v_cvt_pk_bf16_f32 v83, v86, v87\n"
      "v_accvgpr_write_b32 a120, v80\n"
      "v_accvgpr_write_b32 a121, v81\n"
      "v_mfma_f32_16x16x32_bf16 v[124:127], v[172:175], a[48:51], v[124:127]\n"
      "v_accvgpr_write_b32 a122, v82\n"
      "v_accvgpr_write_b32 a123, v83\n"
      "v_mfma_f32_16x16x32_bf16 v[72:75], v[64:67], a[52:55], v[140:143]\n"
      "v_cvt_pk_bf16_f32 v88, v88, v89\n"
      "v_cvt_pk_bf16_f32 v89, v90, v91\n"
      "v_cvt_pk_bf16_f32 v90, v92, v93\n"
      "v_mfma_f32_16x16x32_bf16 v[76:79], v[68:71], a[52:55], v[140:143]\n"
      "v_cvt_pk_bf16_f32 v91, v94, v95\n"
      "v_accvgpr_write_b32 a192, v88\n"
      "v_accvgpr_write_b32 a193, v89\n"
      "v_mfma_f32_16x16x32_bf16 v[128:131], v[172:175], a[120:123], v[128:131]\n"
      "v_accvgpr_write_b32 a194, v90\n"
      "v_accvgpr_write_b32 a195, v91\n"
      "v_mfma_f32_16x16x32_bf16 v[80:83], v[64:67], a[124:127], v[144:147]\n"
      "v_cvt_pk_bf16_f32 v28, v96, v97\n"
      "v_cvt_pk_bf16_f32 v29, v98, v99\n"
      "v_cvt_pk_bf16_f32 v30, v100, v101\n"
      "v_mfma_f32_16x16x32_bf16 v[84:87], v[68:71], a[124:127], v[144:147]\n"
      "v_cvt_pk_bf16_f32 v31, v102, v103\n"
      "v_mfma_f32_16x16x32_bf16 v[132:135], v[172:175], a[192:195], v[132:135]\n"
      "v_mfma_f32_16x16x32_bf16 v[88:91], v[64:67], a[196:199], v[148:151]\n"
      "v_cvt_pk_bf16_f32 v72, v72, v73\n"
      "v_cvt_pk_bf16_f32 v73, v74, v75\n"
      "v_cvt_pk_bf16_f32 v74, v76, v77\n"
      "v_mfma_f32_16x16x32_bf16 v[92:95], v[68:71], a[196:199], v[148:151]\n"
      "v_cvt_pk_bf16_f32 v75, v78, v79\n"
      "v_accvgpr_write_b32 a52, v72\n"
      "v_accvgpr_write_b32 a53, v73\n"
      "v_mfma_f32_16x16x32_bf16 v[136:139], v[172:175], v[28:31], v[136:139]\n"
      "v_accvgpr_write_b32 a54, v74\n"
      "v_accvgpr_write_b32 a55, v75\n"
      "v_mfma_f32_16x16x32_bf16 v[96:99], v[64:67], v[32:35], v[152:155]\n"
      "v_cvt_pk_bf16_f32 v80, v80, v81\n"
      "v_cvt_pk_bf16_f32 v81, v82, v83\n"
      "v_cvt_pk_bf16_f32 v82, v84, v85\n"
      "v_mfma_f32_16x16x32_bf16 v[100:103], v[68:71], v[32:35], v[152:155]\n"
      "v_cvt_pk_bf16_f32 v83, v86, v87\n"
      "v_accvgpr_write_b32 a124, v80\n"
      "v_accvgpr_write_b32 a125, v81\n"
      "v_mfma_f32_16x16x32_bf16 v[124:127], v[172:175], a[52:55], v[124:127]\n"
      "v_accvgpr_write_b32 a126, v82\n"
      "v_accvgpr_write_b32 a127, v83\n"
      "v_mfma_f32_16x16x32_bf16 v[72:75], v[64:67], a[56:59], v[140:143]\n"
      "v_cvt_pk_bf16_f32 v88, v88, v89\n"
      "v_cvt_pk_bf16_f32 v89, v90, v91\n"
      "v_cvt_pk_bf16_f32 v90, v92, v93\n"
      "v_mfma_f32_16x16x32_bf16 v[76:79], v[68:71], a[56:59], v[140:143]\n"
      "v_cvt_pk_bf16_f32 v91, v94, v95\n"
      "v_accvgpr_write_b32 a196, v88\n"
      "v_accvgpr_write_b32 a197, v89\n"
      "v_mfma_f32_16x16x32_bf16 v[128:131], v[172:175], a[124:127], v[128:131]\n"
      "v_accvgpr_write_b32 a198, v90\n"
      "v_accvgpr_write_b32 a199, v91\n"
      "v_mfma_f32_16x16x32_bf16 v[80:83], v[64:67], a[128:131], v[144:147]\n"
      "v_cvt_pk_bf16_f32 v32, v96, v97\n"
      "v_cvt_pk_bf16_f32 v33, v98, v99\n"
      "v_cvt_pk_bf16_f32 v34, v100, v101\n"
      "v_mfma_f32_16x16x32_bf16 v[84:87], v[68:71], a[128:131], v[144:147]\n"
      "v_cvt_pk_bf16_f32 v35, v102, v103\n"
      "v_mfma_f32_16x16x32_bf16 v[132:135], v[172:175], a[196:199], v[132:135]\n"
      "v_mfma_f32_16x16x32_bf16 v[88:91], v[64:67], a[200:203], v[148:151]\n"
      "v_cvt_pk_bf16_f32 v72, v72, v73\n"
      "v_cvt_pk_bf16_f32 v73, v74, v75\n"
      "v_cvt_pk_bf16_f32 v74, v76, v77\n"
      "v_mfma_f32_16x16x32_bf16 v[92:95], v[68:71], a[200:203], v[148:151]\n"
      "v_cvt_pk_bf16_f32 v75, v78, v79\n"
      "v_accvgpr_write_b32 a56, v72\n"
      "v_accvgpr_write_b32 a57, v73\n"
      "v_mfma_f32_16x16x32_bf16 v[136:139], v[172:175], v[32:35], v[136:139]\n"
      "v_accvgpr_write_b32 a58, v74\n"
      "v_accvgpr_write_b32 a59, v75\n"
      "v_mfma_f32_16x16x32_bf16 v[96:99], v[64:67], v[36:39], v[152:155]\n"
      "v_cvt_pk_bf16_f32 v80, v80, v81\n"
      "v_cvt_pk_bf16_f32 v81, v82, v83\n"
      "v_cvt_pk_bf16_f32 v82, v84, v85\n"
      "v_mfma_f32_16x16x32_bf16 v[100:103], v[68:71], v[36:39], v[152:155]\n"
      "v_cvt_pk_bf16_f32 v83, v86, v87\n"
      "v_accvgpr_write_b32 a128, v80\n"
      "v_accvgpr_write_b32 a129, v81\n"
      "v_mfma_f32_16x16x32_bf16 v[124:127], v[172:175], a[56:59], v[124:127]\n"
      "v_accvgpr_write_b32 a130, v82\n"
      "v_accvgpr_write_b32 a131, v83\n"
      "s_nop 1\n"
      "v_mfma_f32_16x16x32_bf16 v[128:131], v[172:175], a[128:131], v[128:131]\n"
      "v_cvt_pk_bf16_f32 v88, v88, v89\n"
      "v_cvt_pk_bf16_f32 v89, v90, v91\n"
      "v_cvt_pk_bf16_f32 v90, v92, v93\n"
      "v_cvt_pk_bf16_f32 v91, v94, v95\n"
      "v_accvgpr_write_b32 a200, v88\n"
      "v_accvgpr_write_b32 a201, v89\n"
      "v_accvgpr_write_b32 a202, v90\n"
      "v_accvgpr_write_b32 a203, v91\n"
      "s_nop 1\n"
      "v_mfma_f32_16x16x32_bf16 v[132:135], v[172:175], a[200:203], v[132:135]\n"
      "v_cvt_pk_bf16_f32 v36, v96, v97\n"
      "v_cvt_pk_bf16_f32 v37, v98, v99\n"
      "v_cvt_pk_bf16_f32 v38, v100, v101\n"
      "v_cvt_pk_bf16_f32 v39, v102, v103\n"
      "s_nop 1\n"
      "v_mfma_f32_16x16x32_bf16 v[136:139], v[172:175], v[36:39], v[136:139]\n"
      "v_add_f32 v168, v124, v160\n"
      "v_add_f32 v169, v128, v161\n"
      "v_add_f32 v170, v132, v162\n"
      "s_nop 5\n"
      "v_add_f32 v171, v136, v163\n"
      "v_mul_f32 v140, 0xbc2aaaab, v168\n"
      "v_mul_f32 v141, 0xbc2aaaab, v168\n"
      "v_mul_f32 v142, 0xbc2aaaab, v168\n"
      "v_mul_f32 v143, 0xbc2aaaab, v168\n"
      "v_mul_f32 v144, 0xbc2aaaab, v169\n"
      "v_mul_f32 v145, 0xbc2aaaab, v169\n"
      "v_mul_f32 v146, 0xbc2aaaab, v169\n"
      "v_mul_f32 v147, 0xbc2aaaab, v169\n"
      "v_mul_f32 v148, 0xbc2aaaab, v170\n"
      "v_mul_f32 v149, 0xbc2aaaab, v170\n"
      "v_mul_f32 v150, 0xbc2aaaab, v170\n"
      "v_mul_f32 v151, 0xbc2aaaab, v170\n"
      "v_mul_f32 v152, 0xbc2aaaab, v171\n"
      "v_mul_f32 v153, 0xbc2aaaab, v171\n"
      "v_mul_f32 v154, 0xbc2aaaab, v171\n"
      "v_mul_f32 v155, 0xbc2aaaab, v171\n"
      "v_mfma_f32_16x16x32_bf16 v[72:75], v[64:67], a[60:63], v[140:143]\n"
      "v_mfma_f32_16x16x32_bf16 v[76:79], v[68:71], a[60:63], v[140:143]\n"
      "v_mfma_f32_16x16x32_bf16 v[80:83], v[64:67], a[132:135], v[144:147]\n"
      "v_mfma_f32_16x16x32_bf16 v[84:87], v[68:71], a[132:135], v[144:147]\n"
      "v_mfma_f32_16x16x32_bf16 v[88:91], v[64:67], a[204:207], v[148:151]\n"
      "s_nop 4\n"
      "v_cvt_pk_bf16_f32 v72, v72, v73\n"
      "v_cvt_pk_bf16_f32 v73, v74, v75\n"
      "v_cvt_pk_bf16_f32 v74, v76, v77\n"
      "v_mfma_f32_16x16x32_bf16 v[92:95], v[68:71], a[204:207], v[148:151]\n"
      "v_cvt_pk_bf16_f32 v75, v78, v79\n"
      "v_accvgpr_write_b32 a60, v72\n"
      "v_accvgpr_write_b32 a61, v73\n"
      "v_accvgpr_write_b32 a62, v74\n"
      "v_accvgpr_write_b32 a63, v75\n"
      "v_mfma_f32_16x16x32_bf16 v[96:99], v[64:67], v[40:43], v[152:155]\n"
      "v_cvt_pk_bf16_f32 v80, v80, v81\n"
      "v_cvt_pk_bf16_f32 v81, v82, v83\n"
      "v_cvt_pk_bf16_f32 v82, v84, v85\n"
      "v_mfma_f32_16x16x32_bf16 v[100:103], v[68:71], v[40:43], v[152:155]\n"
      "v_cvt_pk_bf16_f32 v83, v86, v87\n"
      "v_accvgpr_write_b32 a132, v80\n"
      "v_accvgpr_write_b32 a133, v81\n"
      "v_mfma_f32_16x16x32_bf16 v[124:127], v[172:175], a[60:63], v[124:127]\n"
      "v_accvgpr_write_b32 a134, v82\n"
      "v_accvgpr_write_b32 a135, v83\n"
      "v_mfma_f32_16x16x32_bf16 v[72:75], v[64:67], a[64:67], v[140:143]\n"
      "v_cvt_pk_bf16_f32 v88, v88, v89\n"
      "v_cvt_pk_bf16_f32 v89, v90, v91\n"
      "v_cvt_pk_bf16_f32 v90, v92, v93\n"
      "v_mfma_f32_16x16x32_bf16 v[76:79], v[68:71], a[64:67], v[140:143]\n"
      "v_cvt_pk_bf16_f32 v91, v94, v95\n"
      "v_accvgpr_write_b32 a204, v88\n"
      "v_accvgpr_write_b32 a205, v89\n"
      "v_mfma_f32_16x16x32_bf16 v[128:131], v[172:175], a[132:135], v[128:131]\n"
      "v_accvgpr_write_b32 a206, v90\n"
      "v_accvgpr_write_b32 a207, v91\n"
      "v_mfma_f32_16x16x32_bf16 v[80:83], v[64:67], a[136:139], v[144:147]\n"
      "v_cvt_pk_bf16_f32 v40, v96, v97\n"
      "v_cvt_pk_bf16_f32 v41, v98, v99\n"
      "v_cvt_pk_bf16_f32 v42, v100, v101\n"
      "v_mfma_f32_16x16x32_bf16 v[84:87], v[68:71], a[136:139], v[144:147]\n"
      "v_cvt_pk_bf16_f32 v43, v102, v103\n"
      "v_mfma_f32_16x16x32_bf16 v[132:135], v[172:175], a[204:207], v[132:135]\n"
      "v_mfma_f32_16x16x32_bf16 v[88:91], v[64:67], a[208:211], v[148:151]\n"
      "v_cvt_pk_bf16_f32 v72, v72, v73\n"
      "v_cvt_pk_bf16_f32 v73, v74, v75\n"
      "v_cvt_pk_bf16_f32 v74, v76, v77\n"
      "v_mfma_f32_16x16x32_bf16 v[92:95], v[68:71], a[208:211], v[148:151]\n"
      "v_cvt_pk_bf16_f32 v75, v78, v79\n"
      "v_accvgpr_write_b32 a64, v72\n"
      "v_accvgpr_write_b32 a65, v73\n"
      "v_mfma_f32_16x16x32_bf16 v[136:139], v[172:175], v[40:43], v[136:139]\n"
      "v_accvgpr_write_b32 a66, v74\n"
      "v_accvgpr_write_b32 a67, v75\n"
      "v_mfma_f32_16x16x32_bf16 v[96:99], v[64:67], v[44:47], v[152:155]\n"
      "v_cvt_pk_bf16_f32 v80, v80, v81\n"
      "v_cvt_pk_bf16_f32 v81, v82, v83\n"
      "v_cvt_pk_bf16_f32 v82, v84, v85\n"
      "v_mfma_f32_16x16x32_bf16 v[100:103], v[68:71], v[44:47], v[152:155]\n"
      "v_cvt_pk_bf16_f32 v83, v86, v87\n"
      "v_accvgpr_write_b32 a136, v80\n"
      "v_accvgpr_write_b32 a137, v81\n"
      "v_mfma_f32_16x16x32_bf16 v[124:127], v[172:175], a[64:67], v[124:127]\n"
      "v_accvgpr_write_b32 a138, v82\n"
      "v_accvgpr_write_b32 a139, v83\n"
      "s_nop 1\n"
      "v_mfma_f32_16x16x32_bf16 v[128:131], v[172:175], a[136:139], v[128:131]\n"
      "v_cvt_pk_bf16_f32 v88, v88, v89\n"
      "v_cvt_pk_bf16_f32 v89, v90, v91\n"
      "v_cvt_pk_bf16_f32 v90, v92, v93\n"
      "v_cvt_pk_bf16_f32 v91, v94, v95\n"
      "v_accvgpr_write_b32 a208, v88\n"
      "v_accvgpr_write_b32 a209, v89\n"
      "v_accvgpr_write_b32 a210, v90\n"
      "v_accvgpr_write_b32 a211, v91\n"
      "s_nop 1\n"
      "v_mfma_f32_16x16x32_bf16 v[132:135], v[172:175], a[208:211], v[132:135]\n"
      "v_cvt_pk_bf16_f32 v44, v96, v97\n"
      "v_cvt_pk_bf16_f32 v45, v98, v99\n"
      "v_cvt_pk_bf16_f32 v46, v100, v101\n"
      "v_cvt_pk_bf16_f32 v47, v102, v103\n"
      "s_nop 1\n"
      "v_mfma_f32_16x16x32_bf16 v[136:139], v[172:175], v[44:47], v[136:139]\n"
      "v_add_f32 v168, v124, v164\n"
      "v_add_f32 v169, v128, v165\n"
      "v_add_f32 v170, v132, v166\n"
      "s_nop 5\n"
      "v_add_f32 v171, v136, v167\n"
      "v_mul_f32 v140, 0xbd000000, v168\n"
      "v_mul_f32 v141, 0xbd000000, v168\n"
      "v_mul_f32 v142, 0xbd000000, v168\n"
      "v_mul_f32 v143, 0xbd000000, v168\n"
      "v_mul_f32 v144, 0xbd000000, v169\n"
      "v_mul_f32 v145, 0xbd000000, v169\n"
      "v_mul_f32 v146, 0xbd000000, v169\n"
      "v_mul_f32 v147, 0xbd000000, v169\n"
      "v_mul_f32 v148, 0xbd000000, v170\n"
      "v_mul_f32 v149, 0xbd000000, v170\n"
      "v_mul_f32 v150, 0xbd000000, v170\n"
      "v_mul_f32 v151, 0xbd000000, v170\n"
      "v_mul_f32 v152, 0xbd000000, v171\n"
      "v_mul_f32 v153, 0xbd000000, v171\n"
      "v_mul_f32 v154, 0xbd000000, v171\n"
      "v_mul_f32 v155, 0xbd000000, v171\n"
      "v_mfma_f32_16x16x32_bf16 v[72:75], v[64:67], a[68:71], v[140:143]\n"
      "v_mfma_f32_16x16x32_bf16 v[76:79], v[68:71], a[68:71], v[140:143]\n"
      "v_mfma_f32_16x16x32_bf16 v[80:83], v[64:67], a[140:143], v[144:147]\n"
      "v_mfma_f32_16x16x32_bf16 v[84:87], v[68:71], a[140:143], v[144:147]\n"
      "v_mfma_f32_16x16x32_bf16 v[88:91], v[64:67], a[212:215], v[148:151]\n"
      "s_nop 4\n"
      "v_cvt_pk_bf16_f32 v72, v72, v73\n"
      "v_cvt_pk_bf16_f32 v73, v74, v75\n"
      "v_cvt_pk_bf16_f32 v74, v76, v77\n"
      "v_mfma_f32_16x16x32_bf16 v[92:95], v[68:71], a[212:215], v[148:151]\n"
      "v_cvt_pk_bf16_f32 v75, v78, v79\n"
      "v_accvgpr_write_b32 a68, v72\n"
      "v_accvgpr_write_b32 a69, v73\n"
      "v_accvgpr_write_b32 a70, v74\n"
      "v_accvgpr_write_b32 a71, v75\n"
      "v_mfma_f32_16x16x32_bf16 v[96:99], v[64:67], v[48:51], v[152:155]\n"
      "v_cvt_pk_bf16_f32 v80, v80, v81\n"
      "v_cvt_pk_bf16_f32 v81, v82, v83\n"
      "v_cvt_pk_bf16_f32 v82, v84, v85\n"
      "v_mfma_f32_16x16x32_bf16 v[100:103], v[68:71], v[48:51], v[152:155]\n"
      "v_cvt_pk_bf16_f32 v83, v86, v87\n"
      "v_accvgpr_write_b32 a140, v80\n"
      "v_accvgpr_write_b32 a141, v81\n"
      "v_accvgpr_write_b32 a142, v82\n"
      "v_accvgpr_write_b32 a143, v83\n"
      "v_cvt_pk_bf16_f32 v88, v88, v89\n"
      "v_cvt_pk_bf16_f32 v89, v90, v91\n"
      "v_cvt_pk_bf16_f32 v90, v92, v93\n"
      "v_cvt_pk_bf16_f32 v91, v94, v95\n"
      "v_accvgpr_write_b32 a212, v88\n"
      "v_accvgpr_write_b32 a213, v89\n"
      "v_accvgpr_write_b32 a214, v90\n"
      "v_accvgpr_write_b32 a215, v91\n"
      "v_cvt_pk_bf16_f32 v48, v96, v97\n"
      "v_cvt_pk_bf16_f32 v49, v98, v99\n"
      "v_cvt_pk_bf16_f32 v50, v100, v101\n"
      "v_cvt_pk_bf16_f32 v51, v102, v103\n"
      "v_add_u32 v17, 40960, v0\n"
      "s_barrier\n"
      "v_mov_b32 v11, 0\n"
      "ds_write_b32 v13, v11\n"
      "s_waitcnt lgkmcnt(0)\n"
      "s_barrier\n"
      "s_add_u32 s6, s36, 1548288\n"
      "s_add_u32 s36, s36, 4294942720\n"
      "s_add_u32 s42, s37, 4294635520\n"
      "s_add_u32 s37, s37, 4294635520\n"
      "s_sub_u32 s43, s36, s38\n"
      "s_add_u32 s43, s43, 24576\n"
      "s_mov_b64 s[44:45], 0\n"
      "s_add_u32 s36, s36, 24576\n"
      "s_min_u32 s36, s36, s6\n"
      "buffer_load_dword v18, v2, s[8:11], s36 offen sc1\n"
      "s_mov_b32 s40, s36\n"
      "s_add_u32 s36, s36, 24576\n"
      "s_add_u32 m0, s38, 0\n"
      "s_min_u32 s36, s36, s6\n"
      "s_waitcnt vmcnt(0)\n"
      "v_cmp_ne_u32 vcc, s21, v18\n"
      "s_nop 0\n"
      "s_or_b64 vcc, vcc, s[44:45]\n"
      "s_cbranch_vccnz .Lsp_g0_1\n"
      ".Lbk_g0_1:\n"
      "buffer_load_dwordx4 v0, s[8:11], s40 offen offset:0 sc1 lds\n"
      "buffer_load_dwordx4 v0, s[8:11], s40 offen offset:1024 sc1 lds\n"
      "buffer_load_dwordx4 v0, s[8:11], s40 offen offset:2048 sc1 lds\n"
      "buffer_load_dwordx4 v0, s[8:11], s40 offen offset:3072 sc1 lds\n"
      "s_add_u32 m0, s39, 0\n"
      "s_nop 0\n"
      "buffer_load_dwordx4 v1, s[8:11], s40 offen offset:0 sc1 lds\n"
      "buffer_load_dword v18, v2, s[8:11], s36 offen sc1\n"
      "s_mov_b32 s40, s36\n"
      "s_add_u32 s36, s36, 24576\n"
      "s_add_u32 m0, s38, 20480\n"
      "s_min_u32 s36, s36, s6\n"
      "v_add_u32 v11, 1, v11\n"
      "s_waitcnt vmcnt(1)\n"
      "ds_write_b32 v13, v11\n"
      "s_waitcnt vmcnt(0)\n"
      "v_cmp_ne_u32 vcc, s21, v18\n"
      "s_nop 0\n"
      "s_or_b64 vcc, vcc, s[44:45]\n"
      "s_cbranch_vccnz .Lsp_g0_2\n"
      ".Lbk_g0_2:\n"
      "ds_read_b32 v12, v14\n"
      "buffer_load_dwordx4 v0, s[8:11], s40 offen offset:0 sc1 lds\n"
      "buffer_load_dwordx4 v0, s[8:11], s40 offen offset:1024 sc1 lds\n"
      "s_waitcnt lgkmcnt(0)\n"
      "v_cmp_gt_i32 vcc, v11, v12\n"
      "buffer_load_dwordx4 v0, s[8:11], s40 offen offset:2048 sc1 lds\n"
      "s_cbranch_vccnz .Lsp_gy0_3\n"
      ".Lbk_gy0_3:\n"
      "ds_read2st64_b64 v[168:171], v4 offset0:0 offset1:1\n"
      "ds_read_b128 v[52:55], v0 offset:0\n"
      "buffer_load_dwordx4 v0, s[8:11], s40 offen offset:3072 sc1 lds\n"
      "s_add_u32 m0, s39, 20480\n"
      "ds_read_b128 v[60:63], v0 offset:2048\n"
      "ds_read_b128 v[56:59], v0 offset:1024\n"
      "ds_read_b128 v[72:75], v0 offset:5120\n"
      "buffer_load_dwordx4 v1, s[8:11], s40 offen offset:0 sc1 lds\n"
      "ds_read_b128 v[76:79], v0 offset:6144\n"
      "s_waitcnt lgkmcnt(5)\n"
      "v_add_f32 v169, v169, v171\n"
      "ds_read_b128 v[68:71], v0 offset:4096\n"
      "ds_read_b128 v[64:67], v0 offset:3072\n"
      "buffer_load_dword v18, v2, s[8:11], s36 offen sc1\n"
      "ds_read_b128 v[100:103], v0 offset:12288\n"
      "buffer_load_dword v16, v13, s[8:11], s31 offen\n"
      "ds_read_b128 v[80:83], v0 offset:7168\n"
      "ds_read_b128 v[84:87], v0 offset:8192\n"
      "ds_read_b128 v[92:95], v0 offset:10240\n"
      "v_sub_f32 v176, -v168, v170\n"
      "ds_read_b128 v[88:91], v0 offset:9216\n"
      "s_mov_b32 s20, 15\n"
      "s_waitcnt lgkmcnt(0)\n"
      "s_nop 4\n"
      "s_branch .Lg_loop\n"
      ".p2align 12\n"
      ".Lg_p0:\n"
      "s_branch .Lg_pr\n"
      ".p2align 5\n"
      ".Lg_loop:\n"
      "v_mfma_f32_16x16x32_bf16 v[124:127], a[0:3], v[52:55], 0\n"
      "ds_read_b128 v[112:115], v0 offset:15360\n"
      "v_fma_f32 v169, -v176, v176, v169\n"
      "v_mfma_f32_16x16x32_bf16 v[124:127], a[4:7], v[56:59], v[124:127]\n"
      "ds_read_b128 v[96:99], v0 offset:11264\n"
      "v_mul_f32 v130, v130, v158\n"
      "v_mfma_f32_16x16x32_bf16 v[124:127], a[8:11], v[60:63], v[124:127]\n"
      "s_waitcnt lgkmcnt(6)\n"
      "ds_read_b128 v[104:107], v0 offset:13312\n"
      "v_mul_f32 v160, v132, v160\n"
      "v_mfma_f32_16x16x32_bf16 v[124:127], a[12:15], v[64:67], v[124:127]\n"
      "ds_read_b128 v[108:111], v0 offset:14336\n"
      "v_fma_f32 v161, v161, v177, v177\n"
      "v_mfma_f32_16x16x32_bf16 v[124:127], a[16:19], v[68:71], v[124:127]\n"
      "v_exp_f32 v160, v160\n"
      "v_mfma_f32_16x16x32_bf16 v[124:127], a[20:23], v[72:75], v[124:127]\n"
      "v_mul_f32 v131, v131, v159\n"
      "v_fma_f32 v160, v160, v177, v177\n"
      "v_mfma_f32_16x16x32_bf16 v[124:127], a[24:27], v[76:79], v[124:127]\n"
      "s_waitcnt lgkmcnt(4)\n"
      "v_rcp_f32 v161, v161\n"
      "v_mfma_f32_16x16x32_bf16 v[124:127], a[28:31], v[80:83], v[124:127]\n"
      "ds_read_b128 v[116:119], v0 offset:16384\n"
      "v_mul_f32 v161, v133, v161\n"
      "v_mfma_f32_16x16x32_bf16 v[124:127], a[32:35], v[84:87], v[124:127]\n"
      "ds_read_b128 v[120:123], v0 offset:17408\n"
      "v_cvt_pk_bf16_f32 v165, v130, v131\n"
      "v_mfma_f32_16x16x32_bf16 v[124:127], a[36:39], v[88:91], v[124:127]\n"
      "v_rcp_f32 v160, v160\n"
      "v_mfma_f32_16x16x32_bf16 v[124:127], a[40:43], v[92:95], v[124:127]\n"
      "s_waitcnt lgkmcnt(2)\n"
      "v_rcp_f32 v162, v162\n"
      "v_mfma_f32_16x16x32_bf16 v[124:127], a[44:47], v[96:99], v[124:127]\n"
      "v_mul_f32 v162, v134, v162\n"
      "v_mul_f32 v160, v132, v160\n"
      "v_mfma_f32_16x16x32_bf16 v[124:127], a[48:51], v[100:103], v[124:127]\n"
      "s_add_u32 s37, s37, 165888\n"
      "v_mul_f32 v163, v135, v163\n"
      "v_mul_f32 v139, v139, v163\n"
      "v_mfma_f32_16x16x32_bf16 v[124:127], a[52:55], v[104:107], v[124:127]\n"
      "v_mul_f32 v129, v129, v157\n"
      "v_mul_f32 v138, v138, v162\n"
      "v_mfma_f32_16x16x32_bf16 v[124:127], a[56:59], v[108:111], v[124:127]\n"
      "v_cvt_pk_bf16_f32 v167, v138, v139\n"
      "v_max_f32 v177, 0x3727c5ac, v169\n"
      "v_mfma_f32_16x16x32_bf16 v[124:127], a[60:63], v[112:115], v[124:127]\n"
      "s_waitcnt lgkmcnt(0)\n"
      "v_mul_f32 v128, v128, v156\n"
      "v_cvt_pk_bf16_f32 v164, v128, v129\n"
      "v_mfma_f32_16x16x32_bf16 v[124:127], a[64:67], v[116:119], v[124:127]\n"
      "v_mul_f32 v136, v136, v160\n"
      "v_mfma_f32_16x16x32_bf16 v[124:127], a[68:71], v[120:123], v[124:127]\n"
      "v_mul_f32 v137, v137, v161\n"
      "v_cvt_pk_bf16_f32 v166, v136, v137\n"
      "v_mfma_f32_16x16x32_bf16 v[132:135], a[144:147], v[52:55], 0\n"
      "buffer_store_dwordx4 v[164:167], v0, s[8:11], s37 offen sc1\n"
      "v_mfma_f32_16x16x32_bf16 v[132:135], a[148:151], v[56:59], v[132:135]\n"
      "s_mov_b32 s40, s36\n"
      "v_rsq_f32 v170, v177\n"
      "v_mfma_f32_16x16x32_bf16 v[132:135], a[152:155], v[60:63], v[132:135]\n"
      "s_add_u32 s36, s36, 24576\n"
      "v_mul_f32 v178, 0xc0135761, v170\n"
      "v_mul_f32 v156, v124, v124\n"
      "v_mfma_f32_16x16x32_bf16 v[132:135], a[156:159], v[64:67], v[132:135]\n"
      "s_add_u32 m0, s38, 40960\n"
      "v_mul_f32 v171, 0x3d372713, v178\n"
      "v_mfma_f32_16x16x32_bf16 v[132:135], a[160:163], v[68:71], v[132:135]\n"
      "v_mul_f32 v170, v170, v170\n"
      "v_mul_f32 v179, v171, v170\n"
      "v_mfma_f32_16x16x32_bf16 v[132:135], a[164:167], v[72:75], v[132:135]\n"
      "v_fma_f32 v156, v156, v179, v178\n"
      "v_mul_f32 v156, v124, v156\n"
      "v_mfma_f32_16x16x32_bf16 v[132:135], a[168:171], v[76:79], v[132:135]\n"
      "v_mul_f32 v157, v125, v125\n"
      "v_mul_f32 v159, v127, v127\n"
      "v_mfma_f32_16x16x32_bf16 v[132:135], a[172:175], v[80:83], v[132:135]\n"
      "s_min_u32 s36, s36, s6\n"
      "v_fma_f32 v159, v159, v179, v178\n"
      "v_mfma_f32_16x16x32_bf16 v[132:135], a[176:179], v[84:87], v[132:135]\n"
      "v_mul_f32 v159, v127, v159\n"
      "v_mfma_f32_16x16x32_bf16 v[132:135], a[180:183], v[88:91], v[132:135]\n"
      "v_fma_f32 v157, v157, v179, v178\n"
      "v_mfma_f32_16x16x32_bf16 v[132:135], a[184:187], v[92:95], v[132:135]\n"
      "v_exp_f32 v159, v159\n"
      "v_mfma_f32_16x16x32_bf16 v[132:135], a[188:191], v[96:99], v[132:135]\n"
      "v_mul_f32 v158, v126, v126\n"
      "v_fma_f32 v159, v159, v177, v177\n"
      "v_mfma_f32_16x16x32_bf16 v[132:135], a[192:195], v[100:103], v[132:135]\n"
      "v_rcp_f32 v159, v159\n"
      "v_mfma_f32_16x16x32_bf16 v[132:135], a[196:199], v[104:107], v[132:135]\n"
      "s_add_u32 s42, s42, 165888\n"
      "v_fma_f32 v158, v158, v179, v178\n"
      "v_mul_f32 v158, v126, v158\n"
      "v_mfma_f32_16x16x32_bf16 v[132:135], a[200:203], v[108:111], v[132:135]\n"
      "v_mul_f32 v159, v127, v159\n"
      "v_add_u32 v11, 1, v11\n"
      "v_mfma_f32_16x16x32_bf16 v[132:135], a[204:207], v[112:115], v[132:135]\n"
      "v_mul_f32 v157, v125, v157\n"
      "v_cmp_u_f32 s[44:45], v124, v169\n"
      "v_mfma_f32_16x16x32_bf16 v[132:135], a[208:211], v[116:119], v[132:135]\n"
      "s_waitcnt vmcnt(3)\n"
      "ds_write_b32 v13, v11\n"
      "v_mfma_f32_16x16x32_bf16 v[132:135], a[212:215], v[120:123], v[132:135]\n"
      "v_exp_f32 v158, v158\n"
      "v_mfma_f32_16x16x32_bf16 v[128:131], a[72:75], v[52:55], 0\n"
      "v_exp_f32 v156, v156\n"
      "v_mfma_f32_16x16x32_bf16 v[128:131], a[76:79], v[56:59], v[128:131]\n"
      "v_exp_f32 v157, v157\n"
      "v_mfma_f32_16x16x32_bf16 v[128:131], a[80:83], v[60:63], v[128:131]\n"
      "v_fma_f32 v158, v158, v177, v177\n"
      "v_mul_f32 v162, v134, v134\n"
      "v_mfma_f32_16x16x32_bf16 v[128:131], a[84:87], v[64:67], v[128:131]\n"
      "s_waitcnt vmcnt(2)\n"
      "v_cmp_ne_u32 vcc, s21, v18\n"
      "v_fma_f32 v156, v156, v177, v177\n"
      "v_mfma_f32_16x16x32_bf16 v[128:131], a[88:91], v[68:71], v[128:131]\n"
      "v_fma_f32 v162, v162, v179, v178\n"
      "v_mul_f32 v160, v132, v132\n"
      "v_mfma_f32_16x16x32_bf16 v[128:131], a[92:95], v[72:75], v[128:131]\n"
      "v_fma_f32 v157, v157, v177, v177\n"
      "v_mul_f32 v161, v133, v133\n"
      "v_mfma_f32_16x16x32_bf16 v[128:131], a[96:99], v[76:79], v[128:131]\n"
      "s_or_b64 vcc, vcc, s[44:45]\n"
      "v_rcp_f32 v158, v158\n"
      "v_mfma_f32_16x16x32_bf16 v[128:131], a[100:103], v[80:83], v[128:131]\n"
      "s_cbranch_vccnz .Lsp_g0_4\n"
      ".Lbk_g0_4:\n"
      "ds_read_b32 v12, v14\n"
      "v_mfma_f32_16x16x32_bf16 v[128:131], a[104:107], v[84:87], v[128:131]\n"
      "buffer_load_dwordx4 v0, s[8:11], s40 offen offset:0 sc1 lds\n"
      "v_mfma_f32_16x16x32_bf16 v[128:131], a[108:111], v[88:91], v[128:131]\n"
      "v_rcp_f32 v156, v156\n"
      "v_mfma_f32_16x16x32_bf16 v[128:131], a[112:115], v[92:95], v[128:131]\n"
      "v_rcp_f32 v157, v157\n"
      "v_mfma_f32_16x16x32_bf16 v[128:131], a[116:119], v[96:99], v[128:131]\n"
      "v_mul_f32 v163, v135, v135\n"
      "v_fma_f32 v161, v161, v179, v178\n"
      "v_mfma_f32_16x16x32_bf16 v[128:131], a[120:123], v[100:103], v[128:131]\n"
      "buffer_load_dwordx4 v0, s[8:11], s40 offen offset:1024 sc1 lds\n"
      "v_mfma_f32_16x16x32_bf16 v[128:131], a[124:127], v[104:107], v[128:131]\n"
      "v_fma_f32 v163, v163, v179, v178\n"
      "v_mul_f32 v161, v133, v161\n"
      "v_mfma_f32_16x16x32_bf16 v[128:131], a[128:131], v[108:111], v[128:131]\n"
      "s_waitcnt lgkmcnt(0)\n"
      "v_mul_f32 v163, v135, v163\n"
      "v_cmp_gt_i32 vcc, v11, v12\n"
      "v_mfma_f32_16x16x32_bf16 v[128:131], a[132:135], v[112:115], v[128:131]\n"
      "v_exp_f32 v163, v163\n"
      "v_mfma_f32_16x16x32_bf16 v[128:131], a[136:139], v[116:119], v[128:131]\n"
      "buffer_load_dwordx4 v0, s[8:11], s40 offen offset:2048 sc1 lds\n"
      "v_mfma_f32_16x16x32_bf16 v[128:131], a[140:143], v[120:123], v[128:131]\n"
      "s_cbranch_vccnz .Lsp_gy0_5\n"
      ".Lbk_gy0_5:\n"
      "ds_read2st64_b64 v[168:171], v4 offset0:40 offset1:41\n"
      "v_mfma_f32_16x16x32_bf16 v[136:139], a[216:219], v[52:55], 0\n"
      "v_exp_f32 v161, v161\n"
      "v_mfma_f32_16x16x32_bf16 v[136:139], a[220:223], v[56:59], v[136:139]\n"
      "ds_read_b128 v[52:55], v0 offset:20480\n"
      "v_mul_f32 v156, v124, v156\n"
      "v_mfma_f32_16x16x32_bf16 v[136:139], a[224:227], v[60:63], v[136:139]\n"
      "buffer_load_dwordx4 v0, s[8:11], s40 offen offset:3072 sc1 lds\n"
      "v_mfma_f32_16x16x32_bf16 v[136:139], a[228:231], v[64:67], v[136:139]\n"
      "s_add_u32 m0, s39, 40960\n"
      "ds_read_b128 v[60:63], v0 offset:22528\n"
      "v_mul_f32 v158, v126, v158\n"
      "v_mfma_f32_16x16x32_bf16 v[136:139], a[232:235], v[68:71], v[136:139]\n"
      "ds_read_b128 v[56:59], v0 offset:21504\n"
      "v_fma_f32 v163, v163, v177, v177\n"
      "v_mfma_f32_16x16x32_bf16 v[136:139], a[236:239], v[72:75], v[136:139]\n"
      "ds_read_b128 v[72:75], v0 offset:25600\n"
      "v_mul_f32 v162, v134, v162\n"
      "v_mfma_f32_16x16x32_bf16 v[136:139], a[240:243], v[76:79], v[136:139]\n"
      "buffer_load_dwordx4 v1, s[8:11], s40 offen offset:0 sc1 lds\n"
      "v_mfma_f32_16x16x32_bf16 v[136:139], a[244:247], v[80:83], v[136:139]\n"
      "s_waitcnt lgkmcnt(4)\n"
      "ds_read_b128 v[76:79], v0 offset:26624\n"
      "v_add_f32 v169, v169, v171\n"
      "v_mfma_f32_16x16x32_bf16 v[136:139], a[248:251], v[84:87], v[136:139]\n"
      "v_rcp_f32 v163, v163\n"
      "v_mfma_f32_16x16x32_bf16 v[136:139], a[252:255], v[88:91], v[136:139]\n"
      "ds_read_b128 v[68:71], v0 offset:24576\n"
      "v_mfma_f32_16x16x32_bf16 v[136:139], v[20:23], v[92:95], v[136:139]\n"
      "s_add_u32 s43, s43, 24576\n"
      "v_exp_f32 v162, v162\n"
      "v_mfma_f32_16x16x32_bf16 v[136:139], v[24:27], v[96:99], v[136:139]\n"
      "ds_read_b128 v[64:67], v0 offset:23552\n"
      "v_fma_f32 v162, v162, v177, v177\n"
      "v_mfma_f32_16x16x32_bf16 v[136:139], v[28:31], v[100:103], v[136:139]\n"
      "buffer_load_dword v18, v2, s[8:11], s36 offen sc1\n"
      "v_mfma_f32_16x16x32_bf16 v[136:139], v[32:35], v[104:107], v[136:139]\n"
      "ds_read_b128 v[100:103], v0 offset:32768\n"
      "v_fma_f32 v160, v160, v179, v178\n"
      "v_mfma_f32_16x16x32_bf16 v[136:139], v[36:39], v[108:111], v[136:139]\n"
      "s_waitcnt vmcnt(6) lgkmcnt(3)\n"
      "buffer_store_dword v15, v3, s[8:11], s42 offen sc1\n"
      "v_mfma_f32_16x16x32_bf16 v[136:139], v[40:43], v[112:115], v[136:139]\n"
      "ds_read_b128 v[80:83], v0 offset:27648\n"
      "ds_read_b128 v[84:87], v0 offset:28672\n"
      "v_mfma_f32_16x16x32_bf16 v[136:139], v[44:47], v[116:119], v[136:139]\n"
      "ds_read_b128 v[92:95], v0 offset:30720\n"
      "v_sub_f32 v176, -v168, v170\n"
      "v_mfma_f32_16x16x32_bf16 v[136:139], v[48:51], v[120:123], v[136:139]\n"
      "ds_read_b128 v[88:91], v0 offset:29696\n"
      "v_mul_f32 v157, v125, v157\n"
      "v_mfma_f32_16x16x32_bf16 v[124:127], a[0:3], v[52:55], 0\n"
      "ds_read_b128 v[112:115], v0 offset:35840\n"
      "v_fma_f32 v169, -v176, v176, v169\n"
      "v_mfma_f32_16x16x32_bf16 v[124:127], a[4:7], v[56:59], v[124:127]\n"
      "ds_read_b128 v[96:99], v0 offset:31744\n"
      "v_mul_f32 v130, v130, v158\n"
      "v_mfma_f32_16x16x32_bf16 v[124:127], a[8:11], v[60:63], v[124:127]\n"
      "s_waitcnt lgkmcnt(6)\n"
      "ds_read_b128 v[104:107], v0 offset:33792\n"
      "v_mul_f32 v160, v132, v160\n"
      "v_mfma_f32_16x16x32_bf16 v[124:127], a[12:15], v[64:67], v[124:127]\n"
      "ds_read_b128 v[108:111], v0 offset:34816\n"
      "v_fma_f32 v161, v161, v177, v177\n"
      "v_mfma_f32_16x16x32_bf16 v[124:127], a[16:19], v[68:71], v[124:127]\n"
      "v_exp_f32 v160, v160\n"
      "v_mfma_f32_16x16x32_bf16 v[124:127], a[20:23], v[72:75], v[124:127]\n"
      "v_mul_f32 v131, v131, v159\n"
      "v_fma_f32 v160, v160, v177, v177\n"
      "v_mfma_f32_16x16x32_bf16 v[124:127], a[24:27], v[76:79], v[124:127]\n"
      "s_waitcnt lgkmcnt(4)\n"
      "v_rcp_f32 v161, v161\n"
      "v_mfma_f32_16x16x32_bf16 v[124:127], a[28:31], v[80:83], v[124:127]\n"
      "ds_read_b128 v[116:119], v0 offset:36864\n"
      "v_mul_f32 v161, v133, v161\n"
      "v_mfma_f32_16x16x32_bf16 v[124:127], a[32:35], v[84:87], v[124:127]\n"
      "ds_read_b128 v[120:123], v0 offset:37888\n"
      "v_cvt_pk_bf16_f32 v165, v130, v131\n"
      "v_mfma_f32_16x16x32_bf16 v[124:127], a[36:39], v[88:91], v[124:127]\n"
      "v_rcp_f32 v160, v160\n"
      "v_mfma_f32_16x16x32_bf16 v[124:127], a[40:43], v[92:95], v[124:127]\n"
      "s_waitcnt lgkmcnt(2)\n"
      "v_rcp_f32 v162, v162\n"
      "v_mfma_f32_16x16x32_bf16 v[124:127], a[44:47], v[96:99], v[124:127]\n"
      "v_mul_f32 v162, v134, v162\n"
      "v_mul_f32 v160, v132, v160\n"
      "v_mfma_f32_16x16x32_bf16 v[124:127], a[48:51], v[100:103], v[124:127]\n"
      "s_add_u32 s37, s37, 165888\n"
      "v_mul_f32 v163, v135, v163\n"
      "v_mul_f32 v139, v139, v163\n"
      "v_mfma_f32_16x16x32_bf16 v[124:127], a[52:55], v[104:107], v[124:127]\n"
      "v_mul_f32 v129, v129, v157\n"
      "v_mul_f32 v138, v138, v162\n"
      "v_mfma_f32_16x16x32_bf16 v[124:127], a[56:59], v[108:111], v[124:127]\n"
      "v_cvt_pk_bf16_f32 v167, v138, v139\n"
      "v_max_f32 v177, 0x3727c5ac, v169\n"
      "v_mfma_f32_16x16x32_bf16 v[124:127], a[60:63], v[112:115], v[124:127]\n"
      "s_waitcnt lgkmcnt(0)\n"
      "v_mul_f32 v128, v128, v156\n"
      "v_cvt_pk_bf16_f32 v164, v128, v129\n"
      "v_mfma_f32_16x16x32_bf16 v[124:127], a[64:67], v[116:119], v[124:127]\n"
      "v_mul_f32 v136, v136, v160\n"
      "v_mfma_f32_16x16x32_bf16 v[124:127], a[68:71], v[120:123], v[124:127]\n"
      "v_mul_f32 v137, v137, v161\n"
      "v_cvt_pk_bf16_f32 v166, v136, v137\n"
      "v_mfma_f32_16x16x32_bf16 v[132:135], a[144:147], v[52:55], 0\n"
      "buffer_store_dwordx4 v[164:167], v0, s[8:11], s37 offen sc1\n"
      "v_mfma_f32_16x16x32_bf16 v[132:135], a[148:151], v[56:59], v[132:135]\n"
      "s_mov_b32 s40, s36\n"
      "v_rsq_f32 v170, v177\n"
      "v_mfma_f32_16x16x32_bf16 v[132:135], a[152:155], v[60:63], v[132:135]\n"
      "s_add_u32 s36, s36, 24576\n"
      "v_mul_f32 v178, 0xc0135761, v170\n"
      "v_mul_f32 v156, v124, v124\n"
      "v_mfma_f32_16x16x32_bf16 v[132:135], a[156:159], v[64:67], v[132:135]\n"
      "s_add_u32 m0, s38, 61440\n"
      "v_mul_f32 v171, 0x3d372713, v178\n"
      "v_mfma_f32_16x16x32_bf16 v[132:135], a[160:163], v[68:71], v[132:135]\n"
      "v_mul_f32 v170, v170, v170\n"
      "v_mul_f32 v179, v171, v170\n"
      "v_mfma_f32_16x16x32_bf16 v[132:135], a[164:167], v[72:75], v[132:135]\n"
      "v_fma_f32 v156, v156, v179, v178\n"
      "v_mul_f32 v156, v124, v156\n"
      "v_mfma_f32_16x16x32_bf16 v[132:135], a[168:171], v[76:79], v[132:135]\n"
      "v_mul_f32 v157, v125, v125\n"
      "v_mul_f32 v159, v127, v127\n"
      "v_mfma_f32_16x16x32_bf16 v[132:135], a[172:175], v[80:83], v[132:135]\n"
      "s_min_u32 s36, s36, s6\n"
      "v_fma_f32 v159, v159, v179, v178\n"
      "v_mfma_f32_16x16x32_bf16 v[132:135], a[176:179], v[84:87], v[132:135]\n"
      "v_mul_f32 v159, v127, v159\n"
      "v_mfma_f32_16x16x32_bf16 v[132:135], a[180:183], v[88:91], v[132:135]\n"
      "v_fma_f32 v157, v157, v179, v178\n"
      "v_mfma_f32_16x16x32_bf16 v[132:135], a[184:187], v[92:95], v[132:135]\n"
      "v_exp_f32 v159, v159\n"
      "v_mfma_f32_16x16x32_bf16 v[132:135], a[188:191], v[96:99], v[132:135]\n"
      "v_mul_f32 v158, v126, v126\n"
      "v_fma_f32 v159, v159, v177, v177\n"
      "v_mfma_f32_16x16x32_bf16 v[132:135], a[192:195], v[100:103], v[132:135]\n"
      "v_rcp_f32 v159, v159\n"
      "v_mfma_f32_16x16x32_bf16 v[132:135], a[196:199], v[104:107], v[132:135]\n"
      "s_add_u32 s42, s42, 165888\n"
      "v_fma_f32 v158, v158, v179, v178\n"
      "v_mul_f32 v158, v126, v158\n"
      "v_mfma_f32_16x16x32_bf16 v[132:135], a[200:203], v[108:111], v[132:135]\n"
      "v_mul_f32 v159, v127, v159\n"
      "v_add_u32 v11, 1, v11\n"
      "v_mfma_f32_16x16x32_bf16 v[132:135], a[204:207], v[112:115], v[132:135]\n"
      "v_mul_f32 v157, v125, v157\n"
      "v_cmp_u_f32 s[44:45], v124, v169\n"
      "v_mfma_f32_16x16x32_bf16 v[132:135], a[208:211], v[116:119], v[132:135]\n"
      "s_waitcnt vmcnt(3)\n"
      "ds_write_b32 v13, v11\n"
      "v_mfma_f32_16x16x32_bf16 v[132:135], a[212:215], v[120:123], v[132:135]\n"
      "v_exp_f32 v158, v158\n"
      "v_mfma_f32_16x16x32_bf16 v[128:131], a[72:75], v[52:55], 0\n"
      "v_exp_f32 v156, v156\n"
      "v_mfma_f32_16x16x32_bf16 v[128:131], a[76:79], v[56:59], v[128:131]\n"
      "v_exp_f32 v157, v157\n"
      "v_mfma_f32_16x16x32_bf16 v[128:131], a[80:83], v[60:63], v[128:131]\n"
      "v_fma_f32 v158, v158, v177, v177\n"
      "v_mul_f32 v162, v134, v134\n"
      "v_mfma_f32_16x16x32_bf16 v[128:131], a[84:87], v[64:67], v[128:131]\n"
      "s_waitcnt vmcnt(2)\n"
      "v_cmp_ne_u32 vcc, s21, v18\n"
      "v_fma_f32 v156, v156, v177, v177\n"
      "v_mfma_f32_16x16x32_bf16 v[128:131], a[88:91], v[68:71], v[128:131]\n"
      "v_fma_f32 v162, v162, v179, v178\n"
      "v_mul_f32 v160, v132, v132\n"
      "v_mfma_f32_16x16x32_bf16 v[128:131], a[92:95], v[72:75], v[128:131]\n"
      "v_fma_f32 v157, v157, v177, v177\n"
      "v_mul_f32 v161, v133, v133\n"
      "v_mfma_f32_16x16x32_bf16 v[128:131], a[96:99], v[76:79], v[128:131]\n"
      "s_or_b64 vcc, vcc, s[44:45]\n"
      "v_rcp_f32 v158, v158\n"
      "v_mfma_f32_16x16x32_bf16 v[128:131], a[100:103], v[80:83], v[128:131]\n"
      "s_cbranch_vccnz .Lsp_g0_6\n"
      ".Lbk_g0_6:\n"
      "ds_read_b32 v12, v14\n"
      "v_mfma_f32_16x16x32_bf16 v[128:131], a[104:107], v[84:87], v[128:131]\n"
      "buffer_load_dwordx4 v0, s[8:11], s40 offen offset:0 sc1 lds\n"
      "v_mfma_f32_16x16x32_bf16 v[128:131], a[108:111], v[88:91], v[128:131]\n"
      "v_rcp_f32 v156, v156\n"
      "v_mfma_f32_16x16x32_bf16 v[128:131], a[112:115], v[92:95], v[128:131]\n"
      "v_rcp_f32 v157, v157\n"
      "v_mfma_f32_16x16x32_bf16 v[128:131], a[116:119], v[96:99], v[128:131]\n"
      "v_mul_f32 v163, v135, v135\n"
      "v_fma_f32 v161, v161, v179, v178\n"
      "v_mfma_f32_16x16x32_bf16 v[128:131], a[120:123], v[100:103], v[128:131]\n"
      "buffer_load_dwordx4 v0, s[8:11], s40 offen offset:1024 sc1 lds\n"
      "v_mfma_f32_16x16x32_bf16 v[128:131], a[124:127], v[104:107], v[128:131]\n"
      "v_fma_f32 v163, v163, v179, v178\n"
      "v_mul_f32 v161, v133, v161\n"
      "v_mfma_f32_16x16x32_bf16 v[128:131], a[128:131], v[108:111], v[128:131]\n"
      "s_waitcnt lgkmcnt(0)\n"
      "v_mul_f32 v163, v135, v163\n"
      "v_cmp_gt_i32 vcc, v11, v12\n"
      "v_mfma_f32_16x16x32_bf16 v[128:131], a[132:135], v[112:115], v[128:131]\n"
      "v_exp_f32 v163, v163\n"
      "v_mfma_f32_16x16x32_bf16 v[128:131], a[136:139], v[116:119], v[128:131]\n"
      "buffer_load_dwordx4 v0, s[8:11], s40 offen offset:2048 sc1 lds\n"
      "v_mfma_f32_16x16x32_bf16 v[128:131], a[140:143], v[120:123], v[128:131]\n"
      "s_cbranch_vccnz .Lsp_gy0_7\n"
      ".Lbk_gy0_7:\n"
      "ds_read2st64_b64 v[168:171], v4 offset0:80 offset1:81\n"
      "v_mfma_f32_16x16x32_bf16 v[136:139], a[216:219], v[52:55], 0\n"
      "v_exp_f32 v161, v161\n"
      "v_mfma_f32_16x16x32_bf16 v[136:139], a[220:223], v[56:59], v[136:139]\n"
      "ds_read_b128 v[52:55], v17 offset:0\n"
      "v_mul_f32 v156, v124, v156\n"
      "v_mfma_f32_16x16x32_bf16 v[136:139], a[224:227], v[60:63], v[136:139]\n"
      "buffer_load_dwordx4 v0, s[8:11], s40 offen offset:3072 sc1 lds\n"
      "v_mfma_f32_16x16x32_bf16 v[136:139], a[228:231], v[64:67], v[136:139]\n"
      "s_add_u32 m0, s39, 61440\n"
      "ds_read_b128 v[60:63], v17 offset:2048\n"
      "v_mul_f32 v158, v126, v158\n"
      "v_mfma_f32_16x16x32_bf16 v[136:139], a[232:235], v[68:71], v[136:139]\n"
      "ds_read_b128 v[56:59], v17 offset:1024\n"
      "v_fma_f32 v163, v163, v177, v177\n"
      "v_mfma_f32_16x16x32_bf16 v[136:139], a[236:239], v[72:75], v[136:139]\n"
      "ds_read_b128 v[72:75], v17 offset:5120\n"
      "v_mul_f32 v162, v134, v162\n"
      "v_mfma_f32_16x16x32_bf16 v[136:139], a[240:243], v[76:79], v[136:139]\n"
      "buffer_load_dwordx4 v1, s[8:11], s40 offen offset:0 sc1 lds\n"
      "v_mfma_f32_16x16x32_bf16 v[136:139], a[244:247], v[80:83], v[136:139]\n"
      "s_waitcnt lgkmcnt(4)\n"
      "ds_read_b128 v[76:79], v17 offset:6144\n"
      "v_add_f32 v169, v169, v171\n"
      "v_mfma_f32_16x16x32_bf16 v[136:139], a[248:251], v[84:87], v[136:139]\n"
      "v_rcp_f32 v163, v163\n"
      "v_mfma_f32_16x16x32_bf16 v[136:139], a[252:255], v[88:91], v[136:139]\n"
      "ds_read_b128 v[68:71], v17 offset:4096\n"
      "v_mfma_f32_16x16x32_bf16 v[136:139], v[20:23], v[92:95], v[136:139]\n"
      "s_add_u32 s43, s43, 24576\n"
      "v_exp_f32 v162, v162\n"
      "v_mfma_f32_16x16x32_bf16 v[136:139], v[24:27], v[96:99], v[136:139]\n"
      "ds_read_b128 v[64:67], v17 offset:3072\n"
      "v_fma_f32 v162, v162, v177, v177\n"
      "v_mfma_f32_16x16x32_bf16 v[136:139], v[28:31], v[100:103], v[136:139]\n"
      "buffer_load_dword v18, v2, s[8:11], s36 offen sc1\n"
      "v_mfma_f32_16x16x32_bf16 v[136:139], v[32:35], v[104:107], v[136:139]\n"
      "ds_read_b128 v[100:103], v17 offset:12288\n"
      "v_fma_f32 v160, v160, v179, v178\n"
      "v_mfma_f32_16x16x32_bf16 v[136:139], v[36:39], v[108:111], v[136:139]\n"
      "s_waitcnt vmcnt(6) lgkmcnt(3)\n"
      "buffer_store_dword v15, v3, s[8:11], s42 offen sc1\n"
      "v_mfma_f32_16x16x32_bf16 v[136:139], v[40:43], v[112:115], v[136:139]\n"
      "ds_read_b128 v[80:83], v17 offset:7168\n"
      "ds_read_b128 v[84:87], v17 offset:8192\n"
      "v_mfma_f32_16x16x32_bf16 v[136:139], v[44:47], v[116:119], v[136:139]\n"
      "ds_read_b128 v[92:95], v17 offset:10240\n"
      "v_sub_f32 v176, -v168, v170\n"
      "v_mfma_f32_16x16x32_bf16 v[136:139], v[48:51], v[120:123], v[136:139]\n"
      "ds_read_b128 v[88:91], v17 offset:9216\n"
      "v_mul_f32 v157, v125, v157\n"
      "v_mfma_f32_16x16x32_bf16 v[124:127], a[0:3], v[52:55], 0\n"
      "ds_read_b128 v[112:115], v17 offset:15360\n"
      "v_fma_f32 v169, -v176, v176, v169\n"
      "v_mfma_f32_16x16x32_bf16 v[124:127], a[4:7], v[56:59], v[124:127]\n"
      "ds_read_b128 v[96:99], v17 offset:11264\n"
      "v_mul_f32 v130, v130, v158\n"
      "v_mfma_f32_16x16x32_bf16 v[124:127], a[8:11], v[60:63], v[124:127]\n"
      "s_waitcnt lgkmcnt(6)\n"
      "ds_read_b128 v[104:107], v17 offset:13312\n"
      "v_mul_f32 v160, v132, v160\n"
      "v_mfma_f32_16x16x32_bf16 v[124:127], a[12:15], v[64:67], v[124:127]\n"
      "ds_read_b128 v[108:111], v17 offset:14336\n"
      "v_fma_f32 v161, v161, v177, v177\n"
      "v_mfma_f32_16x16x32_bf16 v[124:127], a[16:19], v[68:71], v[124:127]\n"
      "v_exp_f32 v160, v160\n"
      "v_mfma_f32_16x16x32_bf16 v[124:127], a[20:23], v[72:75], v[124:127]\n"
      "v_mul_f32 v131, v131, v159\n"
      "v_fma_f32 v160, v160, v177, v177\n"
      "v_mfma_f32_16x16x32_bf16 v[124:127], a[24:27], v[76:79], v[124:127]\n"
      "s_waitcnt lgkmcnt(4)\n"
      "v_rcp_f32 v161, v161\n"
      "v_mfma_f32_16x16x32_bf16 v[124:127], a[28:31], v[80:83], v[124:127]\n"
      "ds_read_b128 v[116:119], v17 offset:16384\n"
      "v_mul_f32 v161, v133, v161\n"
      "v_mfma_f32_16x16x32_bf16 v[124:127], a[32:35], v[84:87], v[124:127]\n"
      "ds_read_b128 v[120:123], v17 offset:17408\n"
      "v_cvt_pk_bf16_f32 v165, v130, v131\n"
      "v_mfma_f32_16x16x32_bf16 v[124:127], a[36:39], v[88:91], v[124:127]\n"
      "v_rcp_f32 v160, v160\n"
      "v_mfma_f32_16x16x32_bf16 v[124:127], a[40:43], v[92:95], v[124:127]\n"
      "s_waitcnt lgkmcnt(2)\n"
      "v_rcp_f32 v162, v162\n"
      "v_mfma_f32_16x16x32_bf16 v[124:127], a[44:47], v[96:99], v[124:127]\n"
      "v_mul_f32 v162, v134, v162\n"
      "v_mul_f32 v160, v132, v160\n"
      "v_mfma_f32_16x16x32_bf16 v[124:127], a[48:51], v[100:103], v[124:127]\n"
      "s_add_u32 s37, s37, 165888\n"
      "v_mul_f32 v163, v135, v163\n"
      "v_mul_f32 v139, v139, v163\n"
      "v_mfma_f32_16x16x32_bf16 v[124:127], a[52:55], v[104:107], v[124:127]\n"
      "v_mul_f32 v129, v129, v157\n"
      "v_mul_f32 v138, v138, v162\n"
      "v_mfma_f32_16x16x32_bf16 v[124:127], a[56:59], v[108:111], v[124:127]\n"
      "v_cvt_pk_bf16_f32 v167, v138, v139\n"
      "v_max_f32 v177, 0x3727c5ac, v169\n"
      "v_mfma_f32_16x16x32_bf16 v[124:127], a[60:63], v[112:115], v[124:127]\n"
      "s_waitcnt lgkmcnt(0)\n"
      "v_mul_f32 v128, v128, v156\n"
      "v_cvt_pk_bf16_f32 v164, v128, v129\n"
      "v_mfma_f32_16x16x32_bf16 v[124:127], a[64:67], v[116:119], v[124:127]\n"
      "v_mul_f32 v136, v136, v160\n"
      "v_mfma_f32_16x16x32_bf16 v[124:127], a[68:71], v[120:123], v[124:127]\n"
      "v_mul_f32 v137, v137, v161\n"
      "v_cvt_pk_bf16_f32 v166, v136, v137\n"
      "v_mfma_f32_16x16x32_bf16 v[132:135], a[144:147], v[52:55], 0\n"
      "buffer_store_dwordx4 v[164:167], v0, s[8:11], s37 offen sc1\n"
      "v_mfma_f32_16x16x32_bf16 v[132:135], a[148:151], v[56:59], v[132:135]\n"
      "s_mov_b32 s40, s36\n"
      "v_rsq_f32 v170, v177\n"
      "v_mfma_f32_16x16x32_bf16 v[132:135], a[152:155], v[60:63], v[132:135]\n"
      "s_add_u32 s36, s36, 24576\n"
      "v_mul_f32 v178, 0xc0135761, v170\n"
      "v_mul_f32 v156, v124, v124\n"
      "v_mfma_f32_16x16x32_bf16 v[132:135], a[156:159], v[64:67], v[132:135]\n"
      "s_add_u32 m0, s38, 0\n"
      "v_mul_f32 v171, 0x3d372713, v178\n"
      "v_mfma_f32_16x16x32_bf16 v[132:135], a[160:163], v[68:71], v[132:135]\n"
      "v_mul_f32 v170, v170, v170\n"
      "v_mul_f32 v179, v171, v170\n"
      "v_mfma_f32_16x16x32_bf16 v[132:135], a[164:167], v[72:75], v[132:135]\n"
      "v_fma_f32 v156, v156, v179, v178\n"
      "v_mul_f32 v156, v124, v156\n"
      "v_mfma_f32_16x16x32_bf16 v[132:135], a[168:171], v[76:79], v[132:135]\n"
      "v_mul_f32 v157, v125, v125\n"
      "v_mul_f32 v159, v127, v127\n"
      "v_mfma_f32_16x16x32_bf16 v[132:135], a[172:175], v[80:83], v[132:135]\n"
      "s_min_u32 s36, s36, s6\n"
      "v_fma_f32 v159, v159, v179, v178\n"
      "v_mfma_f32_16x16x32_bf16 v[132:135], a[176:179], v[84:87], v[132:135]\n"
      "v_mul_f32 v159, v127, v159\n"
      "v_mfma_f32_16x16x32_bf16 v[132:135], a[180:183], v[88:91], v[132:135]\n"
      "v_fma_f32 v157, v157, v179, v178\n"
      "v_mfma_f32_16x16x32_bf16 v[132:135], a[184:187], v[92:95], v[132:135]\n"
      "v_exp_f32 v159, v159\n"
      "v_mfma_f32_16x16x32_bf16 v[132:135], a[188:191], v[96:99], v[132:135]\n"
      "v_mul_f32 v158, v126, v126\n"
      "v_fma_f32 v159, v159, v177, v177\n"
      "v_mfma_f32_16x16x32_bf16 v[132:135], a[192:195], v[100:103], v[132:135]\n"
      "v_rcp_f32 v159, v159\n"
      "v_mfma_f32_16x16x32_bf16 v[132:135], a[196:199], v[104:107], v[132:135]\n"
      "s_add_u32 s42, s42, 165888\n"
      "v_fma_f32 v158, v158, v179, v178\n"
      "v_mul_f32 v158, v126, v158\n"
      "v_mfma_f32_16x16x32_bf16 v[132:135], a[200:203], v[108:111], v[132:135]\n"
      "v_mul_f32 v159, v127, v159\n"
      "v_add_u32 v11, 1, v11\n"
      "v_mfma_f32_16x16x32_bf16 v[132:135], a[204:207], v[112:115], v[132:135]\n"
      "v_mul_f32 v157, v125, v157\n"
      "v_cmp_u_f32 s[44:45], v124, v169\n"
      "v_mfma_f32_16x16x32_bf16 v[132:135], a[208:211], v[116:119], v[132:135]\n"
      "s_waitcnt vmcnt(3)\n"
      "ds_write_b32 v13, v11\n"
      "v_mfma_f32_16x16x32_bf16 v[132:135], a[212:215], v[120:123], v[132:135]\n"
      "v_exp_f32 v158, v158\n"
      "v_mfma_f32_16x16x32_bf16 v[128:131], a[72:75], v[52:55], 0\n"
      "v_exp_f32 v156, v156\n"
      "v_mfma_f32_16x16x32_bf16 v[128:131], a[76:79], v[56:59], v[128:131]\n"
      "v_exp_f32 v157, v157\n"
      "v_mfma_f32_16x16x32_bf16 v[128:131], a[80:83], v[60:63], v[128:131]\n"
      "v_fma_f32 v158, v158, v177, v177\n"
      "v_mul_f32 v162, v134, v134\n"
      "v_mfma_f32_16x16x32_bf16 v[128:131], a[84:87], v[64:67], v[128:131]\n"
      "s_waitcnt vmcnt(2)\n"
      "v_cmp_ne_u32 vcc, s21, v18\n"
      "v_fma_f32 v156, v156, v177, v177\n"
      "v_mfma_f32_16x16x32_bf16 v[128:131], a[88:91], v[68:71], v[128:131]\n"
      "v_fma_f32 v162, v162, v179, v178\n"
      "v_mul_f32 v160, v132, v132\n"
      "v_mfma_f32_16x16x32_bf16 v[128:131], a[92:95], v[72:75], v[128:131]\n"
      "v_fma_f32 v157, v157, v177, v177\n"
      "v_mul_f32_e64 v161, v133, v133\n"
      "v_mfma_f32_16x16x32_bf16 v[128:131], a[96:99], v[76:79], v[128:131]\n"
      "s_or_b64 vcc, vcc, s[44:45]\n"
      "v_rcp_f32_e64 v158, v158\n"
      "v_mfma_f32_16x16x32_bf16 v[128:131], a[100:103], v[80:83], v[128:131]\n"
      "s_cbranch_vccnz .Lsp_g0_8\n"
      ".Lbk_g0_8:\n"
      "ds_read_b32 v12, v14\n"
      "v_mfma_f32_16x16x32_bf16 v[128:131], a[104:107], v[84:87], v[128:131]\n"
      "buffer_load_dwordx4 v0, s[8:11], s40 offen offset:0 sc1 lds\n"
      "v_mfma_f32_16x16x32_bf16 v[128:131], a[108:111], v[88:91], v[128:131]\n"
      "v_rcp_f32 v156, v156\n"
      "v_mfma_f32_16x16x32_bf16 v[128:131], a[112:115], v[92:95], v[128:131]\n"
      "v_rcp_f32 v157, v157\n"
      "v_mfma_f32_16x16x32_bf16 v[128:131], a[116:119], v[96:99], v[128:131]\n"
      "v_mul_f32 v163, v135, v135\n"
      "v_fma_f32 v161, v161, v179, v178\n"
      "v_mfma_f32_16x16x32_bf16 v[128:131], a[120:123], v[100:103], v[128:131]\n"
      "buffer_load_dwordx4 v0, s[8:11], s40 offen offset:1024 sc1 lds\n"
      "v_mfma_f32_16x16x32_bf16 v[128:131], a[124:127], v[104:107], v[128:131]\n"
      "v_fma_f32 v163, v163, v179, v178\n"
      "v_mul_f32 v161, v133, v161\n"
      "v_mfma_f32_16x16x32_bf16 v[128:131], a[128:131], v[108:111], v[128:131]\n"
      "s_waitcnt lgkmcnt(0)\n"
      "v_mul_f32 v163, v135, v163\n"
      "v_cmp_gt_i32 vcc, v11, v12\n"
      "v_mfma_f32_16x16x32_bf16 v[128:131], a[132:135], v[112:115], v[128:131]\n"
      "v_exp_f32 v163, v163\n"
      "v_mfma_f32_16x16x32_bf16 v[128:131], a[136:139], v[116:119], v[128:131]\n"
      "buffer_load_dwordx4 v0, s[8:11], s40 offen offset:2048 sc1 lds\n"
      "v_mfma_f32_16x16x32_bf16 v[128:131], a[140:143], v[120:123], v[128:131]\n"
      "s_cbranch_vccnz .Lsp_gy0_9\n"
      ".Lbk_gy0_9:\n"
      "ds_read2st64_b64 v[168:171], v4 offset0:120 offset1:121\n"
      "v_mfma_f32_16x16x32_bf16 v[136:139], a[216:219], v[52:55], 0\n"
      "v_exp_f32 v161, v161\n"
      "v_mfma_f32_16x16x32_bf16 v[136:139], a[220:223], v[56:59], v[136:139]\n"
      "ds_read_b128 v[52:55], v17 offset:20480\n"
      "v_mul_f32 v156, v124, v156\n"
      "v_mfma_f32_16x16x32_bf16 v[136:139], a[224:227], v[60:63], v[136:139]\n"
      "buffer_load_dwordx4 v0, s[8:11], s40 offen offset:3072 sc1 lds\n"
      "v_mfma_f32_16x16x32_bf16 v[136:139], a[228:231], v[64:67], v[136:139]\n"
      "s_add_u32 m0, s39, 0\n"
      "ds_read_b128 v[60:63], v17 offset:22528\n"
      "v_mul_f32 v158, v126, v158\n"
      "v_mfma_f32_16x16x32_bf16 v[136:139], a[232:235], v[68:71], v[136:139]\n"
      "ds_read_b128 v[56:59], v17 offset:21504\n"
      "v_fma_f32 v163, v163, v177, v177\n"
      "v_mfma_f32_16x16x32_bf16 v[136:139], a[236:239], v[72:75], v[136:139]\n"
      "ds_read_b128 v[72:75], v17 offset:25600\n"
      "v_mul_f32 v162, v134, v162\n"
      "v_mfma_f32_16x16x32_bf16 v[136:139], a[240:243], v[76:79], v[136:139]\n"
      "buffer_load_dwordx4 v1, s[8:11], s40 offen offset:0 sc1 lds\n"
      "v_mfma_f32_16x16x32_bf16 v[136:139], a[244:247], v[80:83], v[136:139]\n"
      "s_waitcnt lgkmcnt(4)\n"
      "ds_read_b128 v[76:79], v17 offset:26624\n"
      "v_add_f32 v169, v169, v171\n"
      "v_mfma_f32_16x16x32_bf16 v[136:139], a[248:251], v[84:87], v[136:139]\n"
      "v_rcp_f32 v163, v163\n"
      "v_mfma_f32_16x16x32_bf16 v[136:139], a[252:255], v[88:91], v[136:139]\n"
      "ds_read_b128 v[68:71], v17 offset:24576\n"
      "v_mfma_f32_16x16x32_bf16 v[136:139], v[20:23], v[92:95], v[136:139]\n"
      "s_add_u32 s43, s43, 24576\n"
      "v_exp_f32 v162, v162\n"
      "v_mfma_f32_16x16x32_bf16 v[136:139], v[24:27], v[96:99], v[136:139]\n"
      "ds_read_b128 v[64:67], v17 offset:23552\n"
      "v_fma_f32 v162, v162, v177, v177\n"
      "v_mfma_f32_16x16x32_bf16 v[136:139], v[28:31], v[100:103], v[136:139]\n"
      "buffer_load_dword v18, v2, s[8:11], s36 offen sc1\n"
      "v_mfma_f32_16x16x32_bf16 v[136:139], v[32:35], v[104:107], v[136:139]\n"
      "ds_read_b128 v[100:103], v17 offset:32768\n"
      "v_fma_f32 v160, v160, v179, v178\n"
      "v_mfma_f32_16x16x32_bf16 v[136:139], v[36:39], v[108:111], v[136:139]\n"
      "s_waitcnt vmcnt(6) lgkmcnt(3)\n"
      "buffer_store_dword v15, v3, s[8:11], s42 offen sc1\n"
      "v_mfma_f32_16x16x32_bf16 v[136:139], v[40:43], v[112:115], v[136:139]\n"
      "ds_read_b128 v[80:83], v17 offset:27648\n"
      "ds_read_b128 v[84:87], v17 offset:28672\n"
      "v_mfma_f32_16x16x32_bf16 v[136:139], v[44:47], v[116:119], v[136:139]\n"
      "ds_read_b128 v[92:95], v17 offset:30720\n"
      "v_sub_f32 v176, -v168, v170\n"
      "v_mfma_f32_16x16x32_bf16 v[136:139], v[48:51], v[120:123], v[136:139]\n"
      "ds_read_b128 v[88:91], v17 offset:29696\n"
      "v_mul_f32 v157, v125, v157\n"
      "v_mfma_f32_16x16x32_bf16 v[124:127], a[0:3], v[52:55], 0\n"
      "ds_read_b128 v[112:115], v17 offset:35840\n"
      "v_fma_f32 v169, -v176, v176, v169\n"
      "v_mfma_f32_16x16x32_bf16 v[124:127], a[4:7], v[56:59], v[124:127]\n"
      "ds_read_b128 v[96:99], v17 offset:31744\n"
      "v_mul_f32 v130, v130, v158\n"
      "v_mfma_f32_16x16x32_bf16 v[124:127], a[8:11], v[60:63], v[124:127]\n"
      "s_waitcnt lgkmcnt(6)\n"
      "ds_read_b128 v[104:107], v17 offset:33792\n"
      "v_mul_f32 v160, v132, v160\n"
      "v_mfma_f32_16x16x32_bf16 v[124:127], a[12:15], v[64:67], v[124:127]\n"
      ".Lg_p1:\n"
      "s_cbranch_execz .Lg_pr\n"
      "ds_read_b128 v[108:111], v17 offset:34816\n"
      "v_fma_f32 v161, v161, v177, v177\n"
      "v_mfma_f32_16x16x32_bf16 v[124:127], a[16:19], v[68:71], v[124:127]\n"
      "v_exp_f32 v160, v160\n"
      "v_mfma_f32_16x16x32_bf16 v[124:127], a[20:23], v[72:75], v[124:127]\n"
      "v_mul_f32 v131, v131, v159\n"
      "v_fma_f32 v160, v160, v177, v177\n"
      "v_mfma_f32_16x16x32_bf16 v[124:127], a[24:27], v[76:79], v[124:127]\n"
      "s_waitcnt lgkmcnt(4)\n"
      "v_rcp_f32 v161, v161\n"
      "v_mfma_f32_16x16x32_bf16 v[124:127], a[28:31], v[80:83], v[124:127]\n"
      "ds_read_b128 v[116:119], v17 offset:36864\n"
      "v_mul_f32 v161, v133, v161\n"
      "v_mfma_f32_16x16x32_bf16 v[124:127], a[32:35], v[84:87], v[124:127]\n"
      "ds_read_b128 v[120:123], v17 offset:37888\n"
      "v_cvt_pk_bf16_f32 v165, v130, v131\n"
      "v_mfma_f32_16x16x32_bf16 v[124:127], a[36:39], v[88:91], v[124:127]\n"
      "v_rcp_f32 v160, v160\n"
      "v_mfma_f32_16x16x32_bf16 v[124:127], a[40:43], v[92:95], v[124:127]\n"
      "s_waitcnt lgkmcnt(2)\n"
      "v_rcp_f32 v162, v162\n"
      "v_mfma_f32_16x16x32_bf16 v[124:127], a[44:47], v[96:99], v[124:127]\n"
      "v_mul_f32 v162, v134, v162\n"
      "v_mul_f32 v160, v132, v160\n"
      "v_mfma_f32_16x16x32_bf16 v[124:127], a[48:51], v[100:103], v[124:127]\n"
      "s_add_u32 s37, s37, 165888\n"
      "v_mul_f32 v163, v135, v163\n"
      "v_mul_f32 v139, v139, v163\n"
      "v_mfma_f32_16x16x32_bf16 v[124:127], a[52:55], v[104:107], v[124:127]\n"
      "v_mul_f32 v129, v129, v157\n"
      "v_mul_f32 v138, v138, v162\n"
      "v_mfma_f32_16x16x32_bf16 v[124:127], a[56:59], v[108:111], v[124:127]\n"
      "v_cvt_pk_bf16_f32 v167, v138, v139\n"
      "v_max_f32 v177, 0x3727c5ac, v169\n"
      "v_mfma_f32_16x16x32_bf16 v[124:127], a[60:63], v[112:115], v[124:127]\n"
      "s_waitcnt lgkmcnt(0)\n"
      "v_mul_f32 v128, v128, v156\n"
      "v_cvt_pk_bf16_f32 v164, v128, v129\n"
      "v_mfma_f32_16x16x32_bf16 v[124:127], a[64:67], v[116:119], v[124:127]\n"
      "v_mul_f32 v136, v136, v160\n"
      "v_mfma_f32_16x16x32_bf16 v[124:127], a[68:71], v[120:123], v[124:127]\n"
      "v_mul_f32 v137, v137, v161\n"
      "v_cvt_pk_bf16_f32 v166, v136, v137\n"
      "v_mfma_f32_16x16x32_bf16 v[132:135], a[144:147], v[52:55], 0\n"
      "buffer_store_dwordx4 v[164:167], v0, s[8:11], s37 offen sc1\n"
      "v_mfma_f32_16x16x32_bf16 v[132:135], a[148:151], v[56:59], v[132:135]\n"
      "s_mov_b32 s40, s36\n"
      "v_rsq_f32 v170, v177\n"
      "v_mfma_f32_16x16x32_bf16 v[132:135], a[152:155], v[60:63], v[132:135]\n"
      "s_add_u32 s36, s36, 24576\n"
      "v_mul_f32 v178, 0xc0135761, v170\n"
      "v_mul_f32 v156, v124, v124\n"
      "v_mfma_f32_16x16x32_bf16 v[132:135], a[156:159], v[64:67], v[132:135]\n"
      "s_add_u32 m0, s38, 20480\n"
      "v_mul_f32 v171, 0x3d372713, v178\n"
      "v_mfma_f32_16x16x32_bf16 v[132:135], a[160:163], v[68:71], v[132:135]\n"
      "v_mul_f32 v170, v170, v170\n"
      "v_mul_f32 v179, v171, v170\n"
      "v_mfma_f32_16x16x32_bf16 v[132:135], a[164:167], v[72:75], v[132:135]\n"
      "v_fma_f32 v156, v156, v179, v178\n"
      "v_mul_f32 v156, v124, v156\n"
      "v_mfma_f32_16x16x32_bf16 v[132:135], a[168:171], v[76:79], v[132:135]\n"
      "v_mul_f32 v157, v125, v125\n"
      "v_mul_f32 v159, v127, v127\n"
      "v_mfma_f32_16x16x32_bf16 v[132:135], a[172:175], v[80:83], v[132:135]\n"
      "s_min_u32 s36, s36, s6\n"
      "v_fma_f32 v159, v159, v179, v178\n"
      "v_mfma_f32_16x16x32_bf16 v[132:135], a[176:179], v[84:87], v[132:135]\n"
      "v_mul_f32 v159, v127, v159\n"
      "v_mfma_f32_16x16x32_bf16 v[132:135], a[180:183], v[88:91], v[132:135]\n"
      "v_fma_f32 v157, v157, v179, v178\n"
      "v_mfma_f32_16x16x32_bf16 v[132:135], a[184:187], v[92:95], v[132:135]\n"
      "v_exp_f32 v159, v159\n"
      "v_mfma_f32_16x16x32_bf16 v[132:135], a[188:191], v[96:99], v[132:135]\n"
      "v_mul_f32 v158, v126, v126\n"
      "v_fma_f32 v159, v159, v177, v177\n"
      "v_mfma_f32_16x16x32_bf16 v[132:135], a[192:195], v[100:103], v[132:135]\n"
      "v_rcp_f32 v159, v159\n"
      "v_mfma_f32_16x16x32_bf16 v[132:135], a[196:199], v[104:107], v[132:135]\n"
      "s_add_u32 s42, s42, 165888\n"
      "v_fma_f32 v158, v158, v179, v178\n"
      "v_mul_f32 v158, v126, v158\n"
      "v_mfma_f32_16x16x32_bf16 v[132:135], a[200:203], v[108:111], v[132:135]\n"
      "v_mul_f32 v159, v127, v159\n"
      "v_add_u32 v11, 1, v11\n"
      "v_mfma_f32_16x16x32_bf16 v[132:135], a[204:207], v[112:115], v[132:135]\n"
      "v_mul_f32 v157, v125, v157\n"
      "v_cmp_u_f32 s[44:45], v124, v169\n"
      "v_mfma_f32_16x16x32_bf16 v[132:135], a[208:211], v[116:119], v[132:135]\n"
      "s_waitcnt vmcnt(3)\n"
      "ds_write_b32 v13, v11\n"
      "v_mfma_f32_16x16x32_bf16 v[132:135], a[212:215], v[120:123], v[132:135]\n"
      "v_exp_f32 v158, v158\n"
      "v_mfma_f32_16x16x32_bf16 v[128:131], a[72:75], v[52:55], 0\n"
      "v_exp_f32 v156, v156\n"
      "v_mfma_f32_16x16x32_bf16 v[128:131], a[76:79], v[56:59], v[128:131]\n"
      "v_exp_f32 v157, v157\n"
      "v_mfma_f32_16x16x32_bf16 v[128:131], a[80:83], v[60:63], v[128:131]\n"
      "v_fma_f32 v158, v158, v177, v177\n"
      "v_mul_f32 v162, v134, v134\n"
      "v_mfma_f32_16x16x32_bf16 v[128:131], a[84:87], v[64:67], v[128:131]\n"
      "s_waitcnt vmcnt(2)\n"
      "v_cmp_ne_u32 vcc, s21, v18\n"
      "v_fma_f32 v156, v156, v177, v177\n"
      "v_mfma_f32_16x16x32_bf16 v[128:131], a[88:91], v[68:71], v[128:131]\n"
      "v_fma_f32 v162, v162, v179, v178\n"
      "v_mul_f32 v160, v132, v132\n"
      "v_mfma_f32_16x16x32_bf16 v[128:131], a[92:95], v[72:75], v[128:131]\n"
      "v_fma_f32 v157, v157, v177, v177\n"
      "v_mul_f32 v161, v133, v133\n"
      "v_mfma_f32_16x16x32_bf16 v[128:131], a[96:99], v[76:79], v[128:131]\n"
      "s_or_b64 vcc, vcc, s[44:45]\n"
      "v_rcp_f32_e64 v158, v158\n"
      "v_mfma_f32_16x16x32_bf16 v[128:131], a[100:103], v[80:83], v[128:131]\n"
      "s_cbranch_vccnz .Lsp_g0_10\n"
      ".Lbk_g0_10:\n"
      "ds_read_b32 v12, v14\n"
      "v_mfma_f32_16x16x32_bf16 v[128:131], a[104:107], v[84:87], v[128:131]\n"
      "buffer_load_dwordx4 v0, s[8:11], s40 offen offset:0 sc1 lds\n"
      "v_mfma_f32_16x16x32_bf16 v[128:131], a[108:111], v[88:91], v[128:131]\n"
      "v_rcp_f32 v156, v156\n"
      "v_mfma_f32_16x16x32_bf16 v[128:131], a[112:115], v[92:95], v[128:131]\n"
      "v_rcp_f32 v157, v157\n"
      "v_mfma_f32_16x16x32_bf16 v[128:131], a[116:119], v[96:99], v[128:131]\n"
      "v_mul_f32 v163, v135, v135\n"
      "v_fma_f32 v161, v161, v179, v178\n"
      "v_mfma_f32_16x16x32_bf16 v[128:131], a[120:123], v[100:103], v[128:131]\n"
      "buffer_load_dwordx4 v0, s[8:11], s40 offen offset:1024 sc1 lds\n"
      "v_mfma_f32_16x16x32_bf16 v[128:131], a[124:127], v[104:107], v[128:131]\n"
      "v_fma_f32 v163, v163, v179, v178\n"
      "v_mul_f32 v161, v133, v161\n"
      "v_mfma_f32_16x16x32_bf16 v[128:131], a[128:131], v[108:111], v[128:131]\n"
      "s_waitcnt lgkmcnt(0)\n"
      "v_mul_f32 v163, v135, v163\n"
      "v_cmp_gt_i32 vcc, v11, v12\n"
      "v_mfma_f32_16x16x32_bf16 v[128:131], a[132:135], v[112:115], v[128:131]\n"
      "v_exp_f32 v163, v163\n"
      "v_mfma_f32_16x16x32_bf16 v[128:131], a[136:139], v[116:119], v[128:131]\n"
      "buffer_load_dwordx4 v0, s[8:11], s40 offen offset:2048 sc1 lds\n"
      "v_mfma_f32_16x16x32_bf16 v[128:131], a[140:143], v[120:123], v[128:131]\n"
      "s_cbranch_vccnz .Lsp_gy0_11\n"
      ".Lbk_gy0_11:\n"
      "ds_read2st64_b64 v[168:171], v4 offset0:0 offset1:1\n"
      "v_mfma_f32_16x16x32_bf16 v[136:139], a[216:219], v[52:55], 0\n"
      "v_exp_f32 v161, v161\n"
      "v_mfma_f32_16x16x32_bf16 v[136:139], a[220:223], v[56:59], v[136:139]\n"
      "ds_read_b128 v[52:55], v0 offset:0\n"
      "v_mul_f32 v156, v124, v156\n"
      "v_mfma_f32_16x16x32_bf16 v[136:139], a[224:227], v[60:63], v[136:139]\n"
      "buffer_load_dwordx4 v0, s[8:11], s40 offen offset:3072 sc1 lds\n"
      "v_mfma_f32_16x16x32_bf16 v[136:139], a[228:231], v[64:67], v[136:139]\n"
      "s_add_u32 m0, s39, 20480\n"
      "ds_read_b128 v[60:63], v0 offset:2048\n"
      "v_mul_f32 v158, v126, v158\n"
      "v_mfma_f32_16x16x32_bf16 v[136:139], a[232:235], v[68:71], v[136:139]\n"
      "ds_read_b128 v[56:59], v0 offset:1024\n"
      "v_fma_f32 v163, v163, v177, v177\n"
      "v_mfma_f32_16x16x32_bf16 v[136:139], a[236:239], v[72:75], v[136:139]\n"
      "ds_read_b128 v[72:75], v0 offset:5120\n"
      "v_mul_f32 v162, v134, v162\n"
      "v_mfma_f32_16x16x32_bf16 v[136:139], a[240:243], v[76:79], v[136:139]\n"
      "buffer_load_dwordx4 v1, s[8:11], s40 offen offset:0 sc1 lds\n"
      "v_mfma_f32_16x16x32_bf16 v[136:139], a[244:247], v[80:83], v[136:139]\n"
      "s_waitcnt lgkmcnt(4)\n"
      "ds_read_b128 v[76:79], v0 offset:6144\n"
      "v_add_f32 v169, v169, v171\n"
      "v_mfma_f32_16x16x32_bf16 v[136:139], a[248:251], v[84:87], v[136:139]\n"
      "v_rcp_f32 v163, v163\n"
      "v_mfma_f32_16x16x32_bf16 v[136:139], a[252:255], v[88:91], v[136:139]\n"
      "ds_read_b128 v[68:71], v0 offset:4096\n"
      "v_mfma_f32_16x16x32_bf16 v[136:139], v[20:23], v[92:95], v[136:139]\n"
      "s_add_u32 s43, s43, 24576\n"
      "v_exp_f32 v162, v162\n"
      "v_mfma_f32_16x16x32_bf16 v[136:139], v[24:27], v[96:99], v[136:139]\n"
      "ds_read_b128 v[64:67], v0 offset:3072\n"
      "v_fma_f32 v162, v162, v177, v177\n"
      "v_mfma_f32_16x16x32_bf16 v[136:139], v[28:31], v[100:103], v[136:139]\n"
      "buffer_load_dword v18, v2, s[8:11], s36 offen sc1\n"
      "v_mfma_f32_16x16x32_bf16 v[136:139], v[32:35], v[104:107], v[136:139]\n"
      "ds_read_b128 v[100:103], v0 offset:12288\n"
      "v_fma_f32 v160, v160, v179, v178\n"
      "v_mfma_f32_16x16x32_bf16 v[136:139], v[36:39], v[108:111], v[136:139]\n"
      "s_waitcnt vmcnt(6) lgkmcnt(3)\n"
      "buffer_store_dword v15, v3, s[8:11], s42 offen sc1\n"
      "v_mfma_f32_16x16x32_bf16 v[136:139], v[40:43], v[112:115], v[136:139]\n"
      "ds_read_b128 v[80:83], v0 offset:7168\n"
      "ds_read_b128 v[84:87], v0 offset:8192\n"
      "v_mfma_f32_16x16x32_bf16 v[136:139], v[44:47], v[116:119], v[136:139]\n"
      "ds_read_b128 v[92:95], v0 offset:10240\n"
      "v_sub_f32 v176, -v168, v170\n"
      "v_mfma_f32_16x16x32_bf16 v[136:139], v[48:51], v[120:123], v[136:139]\n"
      "ds_read_b128 v[88:91], v0 offset:9216\n"
      "v_mul_f32 v157, v125, v157\n"
      "s_sub_u32 s20, s20, 1\n"
      "s_cbranch_scc0 .Lg_loop\n"
      ".Lg_tail:\n"
      "v_mul_f32 v130, v130, v158\n"
      "v_mul_f32 v160, v132, v160\n"
      "v_fma_f32 v161, v161, v177, v177\n"
      "v_exp_f32 v160, v160\n"
      "v_mul_f32 v131, v131, v159\n"
      "v_fma_f32 v160, v160, v177, v177\n"
      "v_rcp_f32 v161, v161\n"
      "s_nop 0\n"
      "v_mul_f32 v161, v133, v161\n"
      "v_cvt_pk_bf16_f32 v165, v130, v131\n"
      "v_rcp_f32 v160, v160\n"
      "v_rcp_f32 v162, v162\n"
      "s_nop 0\n"
      "v_mul_f32 v162, v134, v162\n"
      "v_mul_f32 v160, v132, v160\n"
      "s_add_u32 s37, s37, 165888\n"
      "v_mul_f32 v163, v135, v163\n"
      "v_mul_f32 v139, v139, v163\n"
      "v_mul_f32 v129, v129, v157\n"
      "v_mul_f32 v138, v138, v162\n"
      "v_cvt_pk_bf16_f32 v167, v138, v139\n"
      "v_mul_f32 v128, v128, v156\n"
      "v_cvt_pk_bf16_f32 v164, v128, v129\n"
      "v_mul_f32 v136, v136, v160\n"
      "v_mul_f32 v137, v137, v161\n"
      "v_cvt_pk_bf16_f32 v166, v136, v137\n"
      "buffer_store_dwordx4 v[164:167], v0, s[8:11], s37 offen sc1\n"
      "s_add_u32 s42, s42, 165888\n"
      "s_waitcnt vmcnt(0)\n"
      "buffer_store_dword v15, v3, s[8:11], s42 offen sc1\n"
      "s_add_u32 s37, s37, 165888\n"
      "s_add_u32 s42, s42, 165888\n"
      "s_endpgm\n"
      ".Lg_redo:\n"
      "s_add_u32 s30, s43, s38\n"
      "s_add_u32 s30, s30, 24576\n"
      "s_mov_b32 s22, s23\n"
      ".Lg_rl:\n"
      "buffer_load_dword v19, v2, s[8:11], s30 offen sc1\n"
      "s_waitcnt vmcnt(0) lgkmcnt(0)\n"
      "v_cmp_ne_u32 vcc, s21, v19\n"
      "s_cbranch_vccz .Lg_rk\n"
      "s_sleep 1\n"
      "s_sub_u32 s22, s22, 1\n"
      "s_cbranch_scc0 .Lg_rl\n"
      "s_mov_b32 s23, 0\n"
      ".Lg_rk:\n"
      "s_add_u32 s30, s43, 0\n"
      "buffer_load_dwordx4 v[52:55], v0, s[8:11], s30 offen offset:0 sc1\n"
      "buffer_load_dwordx4 v[56:59], v0, s[8:11], s30 offen offset:1024 sc1\n"
      "buffer_load_dwordx4 v[60:63], v0, s[8:11], s30 offen offset:2048 sc1\n"
      "buffer_load_dwordx4 v[64:67], v0, s[8:11], s30 offen offset:3072 sc1\n"
      "s_add_u32 s30, s43, 4096\n"
      "buffer_load_dwordx4 v[68:71], v0, s[8:11], s30 offen offset:0 sc1\n"
      "buffer_load_dwordx4 v[72:75], v0, s[8:11], s30 offen offset:1024 sc1\n"
      "buffer_load_dwordx4 v[76:79], v0, s[8:11], s30 offen offset:2048 sc1\n"
      "buffer_load_dwordx4 v[80:83], v0, s[8:11], s30 offen offset:3072 sc1\n"
      "s_add_u32 s30, s43, 8192\n"
      "buffer_load_dwordx4 v[84:87], v0, s[8:11], s30 offen offset:0 sc1\n"
      "buffer_load_dwordx4 v[88:91], v0, s[8:11], s30 offen offset:1024 sc1\n"
      "buffer_load_dwordx4 v[92:95], v0, s[8:11], s30 offen offset:2048 sc1\n"
      "buffer_load_dwordx4 v[96:99], v0, s[8:11], s30 offen offset:3072 sc1\n"
      "s_add_u32 s30, s43, 12288\n"
      "buffer_load_dwordx4 v[100:103], v0, s[8:11], s30 offen offset:0 sc1\n"
      "buffer_load_dwordx4 v[104:107], v0, s[8:11], s30 offen offset:1024 sc1\n"
      "buffer_load_dwordx4 v[108:111], v0, s[8:11], s30 offen offset:2048 sc1\n"
      "buffer_load_dwordx4 v[112:115], v0, s[8:11], s30 offen offset:3072 sc1\n"
      "s_add_u32 s30, s43, 16384\n"
      "buffer_load_dwordx4 v[116:119], v0, s[8:11], s30 offen offset:0 sc1\n"
      "buffer_load_dwordx4 v[120:123], v0, s[8:11], s30 offen offset:1024 sc1\n"
      "buffer_load_dwordx2 v[168:169], v4, s[8:11], s43 offen sc1\n"
      "buffer_load_dwordx2 v[170:171], v4, s[8:11], s43 offen offset:512 sc1\n"
      "s_waitcnt vmcnt(0)\n"
      "v_add_f32 v169, v169, v171\n"
      "v_sub_f32 v176, -v168, v170\n"
      "v_mfma_f32_16x16x32_bf16 v[124:127], a[0:3], v[52:55], 0\n"
      "v_fma_f32 v169, -v176, v176, v169\n"
      "v_mfma_f32_16x16x32_bf16 v[124:127], a[4:7], v[56:59], v[124:127]\n"
      "v_mfma_f32_16x16x32_bf16 v[124:127], a[8:11], v[60:63], v[124:127]\n"
      "v_mfma_f32_16x16x32_bf16 v[124:127], a[12:15], v[64:67], v[124:127]\n"
      "v_mfma_f32_16x16x32_bf16 v[124:127], a[16:19], v[68:71], v[124:127]\n"
      "v_mfma_f32_16x16x32_bf16 v[124:127], a[20:23], v[72:75], v[124:127]\n"
      "v_mfma_f32_16x16x32_bf16 v[124:127], a[24:27], v[76:79], v[124:127]\n"
      "v_mfma_f32_16x16x32_bf16 v[124:127], a[28:31], v[80:83], v[124:127]\n"
      "v_mfma_f32_16x16x32_bf16 v[124:127], a[32:35], v[84:87], v[124:127]\n"
      "v_mfma_f32_16x16x32_bf16 v[124:127], a[36:39], v[88:91], v[124:127]\n"
      "v_mfma_f32_16x16x32_bf16 v[124:127], a[40:43], v[92:95], v[124:127]\n"
      "v_mfma_f32_16x16x32_bf16 v[124:127], a[44:47], v[96:99], v[124:127]\n"
      "v_mfma_f32_16x16x32_bf16 v[124:127], a[48:51], v[100:103], v[124:127]\n"
      "v_mfma_f32_16x16x32_bf16 v[124:127], a[52:55], v[104:107], v[124:127]\n"
      "v_mfma_f32_16x16x32_bf16 v[124:127], a[56:59], v[108:111], v[124:127]\n"
      "v_max_f32 v177, 0x3727c5ac, v169\n"
      "v_mfma_f32_16x16x32_bf16 v[124:127], a[60:63], v[112:115], v[124:127]\n"
      "v_mfma_f32_16x16x32_bf16 v[124:127], a[64:67], v[116:119], v[124:127]\n"
      "v_mfma_f32_16x16x32_bf16 v[124:127], a[68:71], v[120:123], v[124:127]\n"
      "v_mfma_f32_16x16x32_bf16 v[132:135], a[144:147], v[52:55], 0\n"
      "v_mfma_f32_16x16x32_bf16 v[132:135], a[148:151], v[56:59], v[132:135]\n"
      "v_rsq_f32 v170, v177\n"
      "v_mfma_f32_16x16x32_bf16 v[132:135], a[152:155], v[60:63], v[132:135]\n"
      "v_mul_f32 v178, 0xc0135761, v170\n"
      "s_nop 3\n"
      "v_mul_f32 v156, v124, v124\n"
      "v_mfma_f32_16x16x32_bf16 v[132:135], a[156:159], v[64:67], v[132:135]\n"
      "v_mul_f32 v171, 0x3d372713, v178\n"
      "v_mfma_f32_16x16x32_bf16 v[132:135], a[160:163], v[68:71], v[132:135]\n"
      "v_mul_f32 v170, v170, v170\n"
      "v_mul_f32 v179, v171, v170\n"
      "v_mfma_f32_16x16x32_bf16 v[132:135], a[164:167], v[72:75], v[132:135]\n"
      "v_fma_f32 v156, v156, v179, v178\n"
      "v_mul_f32 v156, v124, v156\n"
      "v_mfma_f32_16x16x32_bf16 v[132:135], a[168:171], v[76:79], v[132:135]\n"
      "v_mul_f32 v157, v125, v125\n"
      "v_mul_f32 v159, v127, v127\n"
      "v_mfma_f32_16x16x32_bf16 v[132:135], a[172:175], v[80:83], v[132:135]\n"
      "v_fma_f32 v159, v159, v179, v178\n"
      "v_mfma_f32_16x16x32_bf16 v[132:135], a[176:179], v[84:87], v[132:135]\n"
      "v_mul_f32 v159, v127, v159\n"
      "v_mfma_f32_16x16x32_bf16 v[132:135], a[180:183], v[88:91], v[132:135]\n"
      "v_fma_f32 v157, v157, v179, v178\n"
      "v_mfma_f32_16x16x32_bf16 v[132:135], a[184:187], v[92:95], v[132:135]\n"
      "v_exp_f32 v159, v159\n"
      "v_mfma_f32_16x16x32_bf16 v[132:135], a[188:191], v[96:99], v[132:135]\n"
      "v_mul_f32 v158, v126, v126\n"
      "v_fma_f32 v159, v159, v177, v177\n"
      "v_mfma_f32_16x16x32_bf16 v[132:135], a[192:195], v[100:103], v[132:135]\n"
      "v_rcp_f32 v159, v159\n"
      "v_mfma_f32_16x16x32_bf16 v[132:135], a[196:199], v[104:107], v[132:135]\n"
      "v_fma_f32 v158, v158, v179, v178\n"
      "v_mul_f32 v158, v126, v158\n"
      "v_mfma_f32_16x16x32_bf16 v[132:135], a[200:203], v[108:111], v[132:135]\n"
      "v_mul_f32 v159, v127, v159\n"
      "v_mfma_f32_16x16x32_bf16 v[132:135], a[204:207], v[112:115], v[132:135]\n"
      "v_mul_f32 v157, v125, v157\n"
      "v_mfma_f32_16x16x32_bf16 v[132:135], a[208:211], v[116:119], v[132:135]\n"
      "v_mfma_f32_16x16x32_bf16 v[132:135], a[212:215], v[120:123], v[132:135]\n"
      "v_exp_f32 v158, v158\n"
      "v_mfma_f32_16x16x32_bf16 v[128:131], a[72:75], v[52:55], 0\n"
      "v_exp_f32 v156, v156\n"
      "v_mfma_f32_16x16x32_bf16 v[128:131], a[76:79], v[56:59], v[128:131]\n"
      "v_exp_f32 v157, v157\n"
      "v_mfma_f32_16x16x32_bf16 v[128:131], a[80:83], v[60:63], v[128:131]\n"
      "v_fma_f32 v158, v158, v177, v177\n"
      "s_nop 1\n"
      "v_mul_f32 v162, v134, v134\n"
      "v_mfma_f32_16x16x32_bf16 v[128:131], a[84:87], v[64:67], v[128:131]\n"
      "v_fma_f32 v156, v156, v177, v177\n"
      "v_mfma_f32_16x16x32_bf16 v[128:131], a[88:91], v[68:71], v[128:131]\n"
      "v_fma_f32 v162, v162, v179, v178\n"
      "v_mul_f32 v160, v132, v132\n"
      "v_mfma_f32_16x16x32_bf16 v[128:131], a[92:95], v[72:75], v[128:131]\n"
      "v_fma_f32 v157, v157, v177, v177\n"
      "v_mul_f32 v161, v133, v133\n"
      "v_mfma_f32_16x16x32_bf16 v[128:131], a[96:99], v[76:79], v[128:131]\n"
      "v_rcp_f32 v158, v158\n"
      "v_mfma_f32_16x16x32_bf16 v[128:131], a[100:103], v[80:83], v[128:131]\n"
      "s_nop 15\n"
      "s_setpc_b64 s[90:91]\n"
      ".Lsp_g0_1:\n"
      "s_cmp_eq_u64 s[44:45], 0\n"
      "s_cbranch_scc1 .Lno_g0_1\n"
      "s_call_b64 s[90:91], .Lg_redo\n"
      ".Lno_g0_1:\n"
      "s_mov_b32 s22, s23\n"
      ".Lsl_g0_1:\n"
      "buffer_load_dword v18, v2, s[8:11], s40 offen sc1\n"
      "s_waitcnt vmcnt(0)\n"
      "v_cmp_ne_u32 vcc, s21, v18\n"
      "s_cbranch_vccz .Lbk_g0_1\n"
      "s_sleep 1\n"
      "s_sub_u32 s22, s22, 1\n"
      "s_cbranch_scc0 .Lsl_g0_1\n"
      "s_mov_b32 s23, 0\n"
      "s_branch .Lbk_g0_1\n"
      ".Lsp_g0_2:\n"
      "s_cmp_eq_u64 s[44:45], 0\n"
      "s_cbranch_scc1 .Lno_g0_2\n"
      "s_call_b64 s[90:91], .Lg_redo\n"
      ".Lno_g0_2:\n"
      "s_mov_b32 s22, s23\n"
      ".Lsl_g0_2:\n"
      "buffer_load_dword v18, v2, s[8:11], s40 offen sc1\n"
      "s_waitcnt vmcnt(0)\n"
      "v_cmp_ne_u32 vcc, s21, v18\n"
      "s_cbranch_vccz .Lbk_g0_2\n"
      "s_sleep 1\n"
      "s_sub_u32 s22, s22, 1\n"
      "s_cbranch_scc0 .Lsl_g0_2\n"
      "s_mov_b32 s23, 0\n"
      "s_branch .Lbk_g0_2\n"
      ".Lsp_gy0_3:\n"
      "s_lshl_b32 s22, s23, 4\n"
      ".Lsl_gy0_3:\n"
      "ds_read_b32 v12, v14\n"
      "s_waitcnt lgkmcnt(0)\n"
      "v_cmp_gt_i32 vcc, v11, v12\n"
      "s_cbranch_vccz .Lbk_gy0_3\n"
      "s_sub_u32 s22, s22, 1\n"
      "s_cbranch_scc0 .Lsl_gy0_3\n"
      "s_mov_b32 s23, 0\n"
      "s_branch .Lbk_gy0_3\n"
      ".Lsp_g0_4:\n"
      "s_cmp_eq_u64 s[44:45], 0\n"
      "s_cbranch_scc1 .Lno_g0_4\n"
      "s_call_b64 s[90:91], .Lg_redo\n"
      ".Lno_g0_4:\n"
      "s_mov_b32 s22, s23\n"
      ".Lsl_g0_4:\n"
      "buffer_load_dword v18, v2, s[8:11], s40 offen sc1\n"
      "s_waitcnt vmcnt(0)\n"
      "v_cmp_ne_u32 vcc, s21, v18\n"
      "s_cbranch_vccz .Lbk_g0_4\n"
      "s_sleep 1\n"
      "s_sub_u32 s22, s22, 1\n"
      "s_cbranch_scc0 .Lsl_g0_4\n"
      "s_mov_b32 s23, 0\n"
      "s_branch .Lbk_g0_4\n"
      ".Lsp_gy0_5:\n"
      "s_lshl_b32 s22, s23, 4\n"
      ".Lsl_gy0_5:\n"
      "ds_read_b32 v12, v14\n"
      "s_waitcnt lgkmcnt(0)\n"
      "v_cmp_gt_i32 vcc, v11, v12\n"
      "s_cbranch_vccz .Lbk_gy0_5\n"
      "s_sub_u32 s22, s22, 1\n"
      "s_cbranch_scc0 .Lsl_gy0_5\n"
      "s_mov_b32 s23, 0\n"
      "s_branch .Lbk_gy0_5\n"
      ".Lsp_g0_6:\n"
      "s_cmp_eq_u64 s[44:45], 0\n"
      "s_cbranch_scc1 .Lno_g0_6\n"
      "s_call_b64 s[90:91], .Lg_redo\n"
      ".Lno_g0_6:\n"
      "s_mov_b32 s22, s23\n"
      ".Lsl_g0_6:\n"
      "buffer_load_dword v18, v2, s[8:11], s40 offen sc1\n"
      "s_waitcnt vmcnt(0)\n"
      "v_cmp_ne_u32 vcc, s21, v18\n"
      "s_cbranch_vccz .Lbk_g0_6\n"
      "s_sleep 1\n"
      "s_sub_u32 s22, s22, 1\n"
      "s_cbranch_scc0 .Lsl_g0_6\n"
      "s_mov_b32 s23, 0\n"
      "s_branch .Lbk_g0_6\n"
      ".Lsp_gy0_7:\n"
      "s_lshl_b32 s22, s23, 4\n"
      ".Lsl_gy0_7:\n"
      "ds_read_b32 v12, v14\n"
      "s_waitcnt lgkmcnt(0)\n"
      "v_cmp_gt_i32 vcc, v11, v12\n"
      "s_cbranch_vccz .Lbk_gy0_7\n"
      "s_sub_u32 s22, s22, 1\n"
      "s_cbranch_scc0 .Lsl_gy0_7\n"
      "s_mov_b32 s23, 0\n"
      "s_branch .Lbk_gy0_7\n"
      ".Lsp_g0_8:\n"
      "s_cmp_eq_u64 s[44:45], 0\n"
      "s_cbranch_scc1 .Lno_g0_8\n"
      "s_call_b64 s[90:91], .Lg_redo\n"
      ".Lno_g0_8:\n"
      "s_mov_b32 s22, s23\n"
      ".Lsl_g0_8:\n"
      "buffer_load_dword v18, v2, s[8:11], s40 offen sc1\n"
      "s_waitcnt vmcnt(0)\n"
      "v_cmp_ne_u32 vcc, s21, v18\n"
      "s_cbranch_vccz .Lbk_g0_8\n"
      "s_sleep 1\n"
      "s_sub_u32 s22, s22, 1\n"
      "s_cbranch_scc0 .Lsl_g0_8\n"
      "s_mov_b32 s23, 0\n"
      "s_branch .Lbk_g0_8\n"
      ".Lsp_gy0_9:\n"
      "s_lshl_b32 s22, s23, 4\n"
      ".Lsl_gy0_9:\n"
      "ds_read_b32 v12, v14\n"
      "s_waitcnt lgkmcnt(0)\n"
      "v_cmp_gt_i32 vcc, v11, v12\n"
      "s_cbranch_vccz .Lbk_gy0_9\n"
      "s_sub_u32 s22, s22, 1\n"
      "s_cbranch_scc0 .Lsl_gy0_9\n"
      "s_mov_b32 s23, 0\n"
      "s_branch .Lbk_gy0_9\n"
      ".Lsp_g0_10:\n"
      "s_cmp_eq_u64 s[44:45], 0\n"
      "s_cbranch_scc1 .Lno_g0_10\n"
      "s_call_b64 s[90:91], .Lg_redo\n"
      ".Lno_g0_10:\n"
      "s_mov_b32 s22, s23\n"
      ".Lsl_g0_10:\n"
      "buffer_load_dword v18, v2, s[8:11], s40 offen sc1\n"
      "s_waitcnt vmcnt(0)\n"
      "v_cmp_ne_u32 vcc, s21, v18\n"
      "s_cbranch_vccz .Lbk_g0_10\n"
      "s_sleep 1\n"
      "s_sub_u32 s22, s22, 1\n"
      "s_cbranch_scc0 .Lsl_g0_10\n"
      "s_mov_b32 s23, 0\n"
      "s_branch .Lbk_g0_10\n"
      ".Lsp_gy0_11:\n"
      "s_lshl_b32 s22, s23, 4\n"
      ".Lsl_gy0_11:\n"
      "ds_read_b32 v12, v14\n"
      "s_waitcnt lgkmcnt(0)\n"
      "v_cmp_gt_i32 vcc, v11, v12\n"
      "s_cbranch_vccz .Lbk_gy0_11\n"
      "s_sub_u32 s22, s22, 1\n"
      "s_cbranch_scc0 .Lsl_gy0_11\n"
      "s_mov_b32 s23, 0\n"
      "s_branch .Lbk_gy0_11\n"
      ".Lg_last:\n"
      :
      : "{s[8:11]}"(rsWS), "{s[12:15]}"(rsA), "{s[92:95]}"(rsC), "{v0}"(v_l16), "{v1}"(v_l5), "{v2}"(v_fa), "{v3}"(v_ra), "{v4}"(v_st), "{v5}"(v_at), "{v6}"(v_vo0), "{v7}"(v_vo1), "{v8}"(v_vo2), "{v9}"(v_vo3), "{v10}"(v_pw), "{v12}"(v_al), "{v13}"(v_yw), "{v14}"(v_yr), "{v16}"(v_hi), "{v54}"(v_tw[0]), "{v55}"(v_tw[1]), "{v64}"(v_id[0]), "{v65}"(v_id[1]), "{s36}"(s_sx), "{s37}"(s_sh), "{s38}"(s_m4), "{s39}"(s_m5)
      : "v255", "a255", "s99", "vcc", "scc", "memory");

  }
}

extern "C" void kernel_launch(void* const* d_in, const int* in_sizes, int n_in, void* d_out, int out_size,
                              void* d_ws, size_t ws_size, hipStream_t stream) {
  char* ws = (char*)d_ws + ((256 - ((size_t)d_ws & 255)) & 255);
  (void)hipFuncSetAttribute((const void*)k_all, hipFuncAttributeMaxDynamicSharedMemorySize, LDS_BYTES);
  k_all<<<256, 256, LDS_BYTES, stream>>>((const u16*)d_in[0], (const u16*)d_in[1], (const u16*)d_in[2], (const u16*)d_in[4],
                                         (const u16*)d_in[5], (const u16*)d_in[6], ws, (u16*)d_out);
}
```
